```python
import torch

def _softmax(x, axis=-1):
    return torch.softmax(x, dim=axis)
def _gelu(x):
    return torch.nn.functional.gelu(x, approximate="tanh")
def _silu(x):
    return torch.nn.functional.silu(x)
def _sigmoid(x):
    return torch.sigmoid(x)
def _layernorm(x, axis=-1, eps=1e-5):
    m = x.mean(dim=axis, keepdim=True)
    v = x.var(dim=axis, keepdim=True, unbiased=False)
    return (x - m) / torch.sqrt(v + eps)
def _scatter_add(tgt, idx, src, axis=0):
    return tgt.clone().index_add_(axis, idx, src)
def _segment_sum(src, seg, n):
    out = torch.zeros((n,) + src.shape[1:], dtype=src.dtype)
    return out.index_add_(0, seg, src)
def _pad(x, pw):
    flat = tuple(v for pair in reversed(pw) for v in pair)
    return torch.nn.functional.pad(x, flat)

kernel_name = 'interleave_layers__sliding_local_dilated__glu_gated_bilinear__prompt'

def setup_inputs():

    _w = (lambda a: (lambda t: t.to(torch.bfloat16) if t.is_floating_point() else t)(torch.as_tensor(a)))
    def _A_setup():
        B, S, D = 1, 2048, 576
        return {
            "x": _w((lambda *s: torch.randn(*s, dtype=torch.bfloat16) * 0.05)(B, S, D)),
            "wqkv": _w((lambda *s: torch.randn(*s, dtype=torch.bfloat16) * 0.05)(D, 3 * D)),
            "wo": _w((lambda *s: torch.randn(*s, dtype=torch.bfloat16) * 0.05)(D, D)),
        }
    _A_in = _A_setup()

    def _B_setup():
        B, S, D, F = 1, 2048, 576, 4608
        return {
            "x": _w((lambda *s: torch.randn(*s, dtype=torch.bfloat16) * 0.05)(B, S, D)),
            "w_gate": _w((lambda *s: torch.randn(*s, dtype=torch.bfloat16) * 0.8)(D, F) * (D ** -0.5)),
            "w_up": _w((lambda *s: torch.randn(*s, dtype=torch.bfloat16) * 0.8)(D, F) * (D ** -0.5)),
            "w_down": _w((lambda *s: torch.randn(*s, dtype=torch.bfloat16) * 0.8)(F, D) * (F ** -0.5)),
        }
    _B_in = _B_setup()

    return {
        "A_x": _A_in["x"],
        "A_wqkv": _A_in["wqkv"],
        "A_wo": _A_in["wo"],
        "B_x": _B_in["x"],
        "B_w_gate": _B_in["w_gate"],
        "B_w_up": _B_in["w_up"],
        "B_w_down": _B_in["w_down"],
    }

def reference(A_x, A_wqkv, A_wo, B_x, B_w_gate, B_w_up, B_w_down):
    def _A_ref(x, wqkv, wo):
        B, S, D = x.shape
        H = 32; Dh = D // H
        W, dil = 8, 2
        qkv = (lambda x, *d: x.permute(*d))((x @ wqkv).reshape(B, S, 3, H, Dh), 2, 0, 3, 1, 4)
        q, k, v = qkv[0], qkv[1], qkv[2]
        scale = Dh ** -0.5
        pos = torch.arange(S)
        off = torch.arange(W) * dil
        idx = pos[:, None] - off[None, :]
        mask = idx >= 0
        idx_c = torch.where(mask, idx, 0).reshape(-1)
        kg = k[:, :, idx_c, :].reshape(B, H, S, W, Dh)
        vg = v[:, :, idx_c, :].reshape(B, H, S, W, Dh)
        logits = torch.einsum("bhsd,bhswd->bhsw", q, kg) * scale
        logits = torch.where(mask[None, None, :, :], logits, -1e9)
        attn = _softmax(logits, axis=-1)
        o = torch.einsum("bhsw,bhswd->bhsd", attn, vg)
        return (lambda x, *d: x.permute(*d))(o, 0, 2, 1, 3).reshape(B, S, D) @ wo

    def _B_ref(x, w_gate, w_up, w_down):
        g = torch.einsum("bsd,df->bsf", x, w_gate)
        u = torch.einsum("bsd,df->bsf", x, w_up)
        h = g * u
        return torch.einsum("bsf,fd->bsd", h, w_down)

    x = A_x
    x = x + _A_ref(_layernorm(x, axis=-1), A_wqkv, A_wo)
    x = x + _B_ref(_layernorm(x, axis=-1), B_w_gate, B_w_up, B_w_down)
    x = x + _A_ref(_layernorm(x, axis=-1), A_wqkv, A_wo)
    x = x + _B_ref(_layernorm(x, axis=-1), B_w_gate, B_w_up, B_w_down)
    return x
```

```cpp
#include <hip/hip_runtime.h>
#include <hip/hip_bf16.h>
#include <stdint.h>
typedef __attribute__((ext_vector_type(32))) unsigned u32x32;
typedef __attribute__((ext_vector_type(16))) unsigned u32x16;
constexpr unsigned RP = 80u;
constexpr unsigned PAD = 12u;
constexpr unsigned XT = 18432u;
constexpr unsigned X1P = 18688u;
constexpr unsigned HT = 147456u;
constexpr unsigned PT = 110592u;
constexpr unsigned FL = 256u;
constexpr unsigned FR = 24576u;
constexpr unsigned WS_FL = 4096u;
constexpr unsigned WS_ST = 102400u;
constexpr unsigned WS_O = 1048576u;
constexpr unsigned WS_X1 = 6946816u;
constexpr unsigned WS_X2 = 13000704u;
constexpr unsigned WS_XD = 15396864u;
constexpr unsigned WS_P = 17977344u;
constexpr unsigned WS_H = 53366784u;
constexpr unsigned WS_END = 100552704u;
constexpr unsigned FO = 0u;
constexpr unsigned FX1 = 2048u;
constexpr unsigned FX2 = 16384u;
constexpr unsigned FP = 2560u;
constexpr unsigned FH = 7168u;
__device__ __forceinline__ void touch(const void* p) {
  asm volatile("s_mov_b32 m0, 0x27f00\n\ts_nop 0\n\tglobal_load_lds_dword %0, off" :: "v"(p) : "memory");
}
__global__ __launch_bounds__(256, 1) void k_main(const char* x0, const char* wqkv, const char* wo, const char* wg, const char* wu, const char* wd,
                                                 char* ws, char* out) {
  __shared__ __attribute__((aligned(1024))) char smem[163840];
  asm volatile("" :: "s"((unsigned)(size_t)(__attribute__((address_space(3))) const char*)smem) : "memory");
  const unsigned lane = threadIdx.x & 63, wave = __builtin_amdgcn_readfirstlane(threadIdx.x >> 6);
  const unsigned xc = blockIdx.x & 7, jj = blockIdx.x >> 3;
  const unsigned role = jj < 4 ? 0 : jj == 4 ? 1 : jj < 14 ? 2 : 3;
  {
    if (wave < 2) { const unsigned u = min(jj * 108 + threadIdx.x, 3455u), sg = u >> 1, k = sg / 3; touch(wqkv + k * 3456 + (sg - 3 * k) * 1152 + xc * 144 + (u & 1) * 140); }
    else if (wave == 2) { touch(ws + min(lane * 2097152u, WS_END - 4)); }
    else {
      unsigned long long pc; unsigned len;
      asm volatile("s_getpc_b64 %0\n.Lkbeg:\n\ts_mov_b32 %1, .Lkend-.Lkbeg" : "=s"(pc), "=s"(len));
      touch((const char*)pc + min((jj * 64 + lane) * 128, len));
    }
    if (wave < 2) { const unsigned u = min(jj * 90 + threadIdx.x, 2879u), k = u / 5; touch(wo + k * 1152 + (xc & 1) * 576 + min((u - 5 * k) * 128, 572u)); }
    if (jj >= 14 && wave < 2) touch(x0 + ((blockIdx.x - 112) * 128 + threadIdx.x) * 128);
  }
  const unsigned r = lane & 15, g = lane >> 4, q = r >> 2, p = lane & 3;
  u32x32 va = {}, vb = {};
  u32x16 sa = {}, sb = {};
  unsigned sv[28] = {};
  sa[0] = __builtin_amdgcn_readfirstlane(role * 4 + wave); sa[2] = (unsigned)(unsigned long long)ws; sa[3] = (unsigned)((unsigned long long)ws >> 32);
  if (role == 0) {
    const unsigned hg = xc, rho = jj, row0 = rho * RP + PAD, pc0 = wave < 2 ? wave * 5 : 2 + wave * 4, hb = wave * 36 + (wave & 1) * 4;
    auto wv = [&](unsigned j) { const unsigned c = min((wave * 7 + j) * 64 + lane, 1727u), rw = c / 27, cc = c - rw * 27, sc = cc / 9; return rw * 3456 + sc * (1152 - 144) + cc * 16 + 4096 - j * 1024; };
    auto oa = [&](unsigned b) { return 43520 + (b >> 4) * 256 + r * 16 + (b & 15); };
    const unsigned m0 = 4 * g + (r & 1), m1 = m0 + 2, hm = (r & 1) ? 0xFFFF0000u : 0xFFFFu, ninf = 0xFF800000u;
    va[0] = wv(0);
    va[1] = wv(1);
    va[2] = wv(2);
    va[3] = wv(3);
    va[4] = wv(4);
    va[5] = wv(5);
    va[6] = wv(6);
    va[7] = (8 * g + q) * 432 + hb + p * 8;
    va[8] = (8 * g + q) * 432 + 32 + (p & 1) * 72 + (wave == 0 ? (p >> 1) * 144 : 288);
    va[9] = ((rho * 32 + 31) * 16 + ((pc0 + 0) & 1) * 8 + (lane >> 3)) * 1152 + ((pc0 + 0) >> 1) * 128 + (((lane ^ (lane >> 3)) & 7) << 4) + 4096;
    va[10] = ((rho * 32 + 31) * 16 + ((pc0 + 1) & 1) * 8 + (lane >> 3)) * 1152 + ((pc0 + 1) >> 1) * 128 + (((lane ^ (lane >> 3)) & 7) << 4) + 3072;
    va[11] = ((rho * 32 + 31) * 16 + ((pc0 + 2) & 1) * 8 + (lane >> 3)) * 1152 + ((pc0 + 2) >> 1) * 128 + (((lane ^ (lane >> 3)) & 7) << 4) + 2048;
    va[12] = ((rho * 32 + 31) * 16 + ((pc0 + 3) & 1) * 8 + (lane >> 3)) * 1152 + ((pc0 + 3) >> 1) * 128 + (((lane ^ (lane >> 3)) & 7) << 4) + 1024;
    va[13] = ((rho * 32 + 31) * 16 + ((pc0 + 4) & 1) * 8 + (lane >> 3)) * 1152 + ((pc0 + 4) >> 1) * 128 + (((lane ^ (lane >> 3)) & 7) << 4) + 0;
    va[14] = WS_O + (row0 - 3) * XT + hg * 2304 + wave * 1024 + (wave < 2 ? lane : r) * 16;
    va[15] = 43520 + wave * 1024 + (wave < 2 ? lane : r) * 16;
    va[16] = oa(hb + g * 8);
    va[17] = ((g & 1) == (wave >> 1)) ? oa(32 + (wave & 1) * 4 + (wave >> 1) * 72) : 46336 + lane * 4;
    va[18] = g == 0 ? 40960 + ((wave >> 1) * 16 + r) * 8 + (wave & 1) * 4 : 46080;
    va[19] = 41216 + ((wave >> 1) * 16 + r) * 8 + (wave & 1) * 4;
    va[20] = lane * 8;
    va[21] = lane * 16;
    va[22] = 43008 + r * 4;
    va[23] = 43008 + g * 16;
    va[24] = 41984 + (q * 16 + r) * 16 + p * 4;
    va[25] = 43136 + (((m0 > r) ? 0 : 1) * 16 + m0) * 4;
    va[26] = 43136 + (((m1 > r) ? 0 : 1) * 16 + m1) * 4;
    va[27] = 43136 + (((m0 > r) ? 1 : 0) * 16 + m0) * 4;
    va[28] = 43136 + (((m1 > r) ? 1 : 0) * 16 + m1) * 4;
    va[29] = 0u;
    va[30] = (4 * g + 0 > r && !((r ^ 0) & 1)) ? 0u : ninf;
    va[31] = (4 * g + 1 > r && !((r ^ 1) & 1)) ? 0u : ninf;
    vb[0] = (4 * g + 2 > r && !((r ^ 2) & 1)) ? 0u : ninf;
    vb[1] = (4 * g + 3 > r && !((r ^ 3) & 1)) ? 0u : ninf;
    vb[2] = (4 * g + 0 <= r && !((r ^ 0) & 1)) ? 0u : ninf;
    vb[3] = (4 * g + 1 <= r && !((r ^ 1) & 1)) ? 0u : ninf;
    vb[4] = (4 * g + 2 <= r && !((r ^ 2) & 1)) ? 0u : ninf;
    vb[5] = (4 * g + 3 <= r && !((r ^ 3) & 1)) ? 0u : ninf;
    vb[6] = m0 > r ? hm : 0u;
    vb[7] = m1 > r ? hm : 0u;
    vb[8] = m0 > r ? 0u : hm;
    vb[9] = m1 > r ? 0u : hm;
    vb[10] = WS_FL + rho * FR + FX2 + (0 + min(lane, 8u)) * FL;
    vb[11] = WS_FL + rho * FR + FX2 + (9 + min(lane, 8u)) * FL;
    vb[12] = 0u - 31;
    vb[13] = WS_FL + rho * FR + FO + hg * FL;
    vb[14] = 0u - 4;
    vb[15] = WS_FL + lane * FL;
    vb[16] = 0u;
    vb[17] = r * 128 + (((0 + g) ^ (r & 7)) << 4);
    vb[18] = r * 128 + (((4 + g) ^ (r & 7)) << 4);
    sv[0] = (unsigned)(unsigned long long)(wqkv + hg * 144);
    sv[1] = (unsigned)((unsigned long long)(wqkv + hg * 144) >> 32);
    sv[2] = (unsigned)(unsigned long long)(x0 - 2048);
    sv[3] = (unsigned)((unsigned long long)(x0 - 2048) >> 32);
    sv[4] = (unsigned)(unsigned long long)((ws + WS_X2) - x0 + (rho ? 32 : 31) * (long long)XT);
    sv[5] = (unsigned)((unsigned long long)((ws + WS_X2) - x0 + (rho ? 32 : 31) * (long long)XT) >> 32);
    sv[6] = (unsigned)(unsigned long long)(-(long long)XT);
    sv[7] = (unsigned)((unsigned long long)(-(long long)XT) >> 32);
    sv[8] = 47104 + wave * 7168;
    sv[9] = pc0 * 1024 + 2048;
    sv[10] = rho ? 0u : XT;
    sv[11] = rho ? 0u - FR : 0u;
    sv[12] = rho ? 0u : ninf;
    sv[13] = blockIdx.x;
  } else if (role == 1) {
    const unsigned rho = xc >> 1, cw = xc & 1, row0 = rho * RP + PAD, pc0 = wave < 2 ? wave * 5 : 2 + wave * 4;
    auto wv = [&](unsigned j) { const unsigned b = j * 1024 + lane * 16, rw = b / 576; return rw * 576 + b + 4096 - (j & 7) * 1024; };
    va[0] = wv(0);
    va[1] = wv(1);
    va[2] = wv(2);
    va[3] = wv(3);
    va[4] = wv(4);
    va[5] = wv(5);
    va[6] = wv(6);
    va[7] = wv(7);
    va[8] = wv(8);
    va[9] = (8 * g + q) * 576 + wave * 128 + p * 16;
    va[10] = (8 * g + q) * 576 + 512 + p * 16;
    va[11] = WS_O + row0 * XT + pc0 * 1024 + 2048 + lane * 16;
    va[12] = WS_O + (row0 + 63) * XT + pc0 * 1024 + 2048 + lane * 16;
    va[13] = WS_X1 + (row0 - 3) * X1P + (cw * 9 + wave * 2) * 1024 + lane * 16;
    va[14] = WS_X1 + (row0 - 4) * X1P + XT + cw * 128 + r * 8;
    va[15] = ((rho * 32 + 31) * 16 + r) * 1152 + (cw * 9 + wave * 2) * 64 + g * 16;
    va[16] = WS_FL + rho * FR + FO + (lane & 7) * FL;
    va[17] = 0u;
    va[18] = WS_FL + rho * FR + FX1 + cw * FL;
    va[19] = 0u - 3;
    va[20] = lane * 16;
    va[21] = lane * 16;
    va[22] = lane * 8;
    sv[0] = (unsigned)(unsigned long long)(wo + wave * 16 * 1152 + cw * 576);
    sv[1] = (unsigned)((unsigned long long)(wo + wave * 16 * 1152 + cw * 576) >> 32);
    sv[2] = (unsigned)(unsigned long long)(x0);
    sv[3] = (unsigned)((unsigned long long)(x0) >> 32);
    sv[4] = (unsigned)(unsigned long long)(ws + WS_X2);
    sv[5] = (unsigned)((unsigned long long)(ws + WS_X2) >> 32);
    sv[6] = wave * 9216;
    sv[7] = pc0 * 1024 + 2048;
  } else if (role == 2) {
    const unsigned j5 = jj - 5, tri = xc * 3 + j5 / 3, nu = j5 % 3, kap = tri >> 2, rho = tri & 3, row0 = rho * RP + PAD, blk = kap >> 1, par = kap & 1;
    const unsigned r2 = wave * 4 + g, c2 = r * 4, jl = row0 - 8 + par;
    auto fa = [](unsigned rr, unsigned cc) { return (cc >> 5) * 1024 + (((cc & 31) >> 3) * 16 + rr) * 16 + ((cc >> 2) & 1) * 8; };
    auto ia = [&](unsigned pi) { const unsigned h = pi & 1; return (pi >> 1) * 2048 + (blk < 2 ? fa(h * 8 + (lane >> 3), (lane & 7) * 8) : (r >> 2) * 512 + (h * 4 + g) * 64 + p * 16); };
    auto wv = [&](unsigned j) { const unsigned b = j * 1024 + lane * 16, rw = b / 384; return rw * (1152 - 384) + b + 4096 - j * 1024; };
    va[0] = wv(0);
    va[1] = wv(1);
    va[2] = wv(2);
    va[3] = wv(3);
    va[4] = wv(4);
    va[5] = wv(5);
    va[6] = (8 * g + q) * 384 + wave * 64 + p * 16;
    va[7] = (8 * g + q) * 384 + 256 + wave * 32 + p * 8;
    va[8] = WS_H + row0 * HT + (kap * 24 + wave * 6 + 2) * 1024 + lane * 16;
    va[9] = WS_P + (row0 - 3) * PT + ((nu * 3 + (wave >> 1)) * 6 + kap) * 2048 + (wave & 1) * 1024 + lane * 16;
    va[10] = WS_P + (row0 - 3) * PT + ((nu * 3 + 2) * 6 + kap) * 2048 + wave * 512 + (r >> 1) * 64 + g * 16 + (r & 1) * 8;
    va[11] = WS_P + jl * PT + (nu * 3 + blk) * 12288 + ia(wave * 3 + 0) - 0;
    va[12] = WS_P + jl * PT + (nu * 3 + blk) * 12288 + ia(wave * 3 + 1) - 1024;
    va[13] = WS_P + jl * PT + (nu * 3 + blk) * 12288 + ia(wave * 3 + 2) - 2048;
    va[14] = WS_X1 + jl * X1P + (wave < 2 ? (nu * 3 + blk) * 2048 + fa(wave * 8 + (lane >> 3), (lane & 7) * 8) : XT + r * 16);
    va[15] = 49152 + (blk < 2 ? r2 * 128 + r * 8 : (r2 >> 1) * 256 + r * 16 + (r2 & 1) * 8);
    va[16] = 61440 + r2 * 128 + r * 8;
    va[17] = 63488 + r2 * 8;
    va[18] = ((rho * 32 + 31 + 10 - par) * 16 + r2) * 1152 + (nu * 192 + blk * 64 + c2) * 2;
    va[19] = WS_FL + rho * FR + (lane < 6 ? FH + (kap * 6 + lane) * FL : lane < 12 ? FP + (nu * 6 + lane - 6) * FL : FX1 + (lane & 1) * FL);
    va[20] = lane < 6 ? 0u : (lane < 12 ? par : par + 2) - 8;
    va[21] = WS_FL + rho * FR + FH + (kap * 6 + min(lane, 5u)) * FL;
    va[22] = 1u;
    va[23] = lane < 6 ? 0u : (lane < 12 ? par : par + 2) + 58;
    va[24] = WS_FL + rho * FR + FP + (nu * 6 + kap) * FL;
    va[25] = 0u - 4;
    va[26] = WS_FL + rho * FR + FX2 + (par * 9 + nu * 3 + blk) * FL;
    va[27] = par - 10;
    va[28] = lane * 16;
    sv[0] = (unsigned)(unsigned long long)(wd + (kap * 768 + wave * 16) * 1152 + nu * 384);
    sv[1] = (unsigned)((unsigned long long)(wd + (kap * 768 + wave * 16) * 1152 + nu * 384) >> 32);
    sv[2] = (unsigned)(unsigned long long)(out);
    sv[3] = (unsigned)((unsigned long long)(out) >> 32);
    sv[4] = 0u;
    sv[5] = 0u;
    sv[6] = (unsigned)(unsigned long long)(ws + WS_X2);
    sv[7] = (unsigned)((unsigned long long)(ws + WS_X2) >> 32);
    sv[8] = (unsigned)(unsigned long long)(ws + WS_XD);
    sv[9] = (unsigned)((unsigned long long)(ws + WS_XD) >> 32);
    sv[10] = wave * 6144;
    sv[11] = wave * 6144 + 2048;
    sv[12] = (unsigned)(unsigned long long)(ws);
    sv[13] = (unsigned)((unsigned long long)(ws) >> 32);
  } else {
    const unsigned ii = xc * 18 + (jj - 14), gam = ii >> 2, rho = ii & 3, row0 = rho * RP + PAD, pc0 = wave < 2 ? wave * 5 : 2 + wave * 4;
    va[0] = g * 9216 + ((r ^ (g << 2)) << 4) + 4096;
    va[1] = (8 * g + q) * 256 + (((4 * wave + p) ^ (q << 2)) << 4);
    va[2] = WS_X1 + row0 * X1P + pc0 * 1024 + 2048 + lane * 16;
    va[3] = WS_H + (row0 - 3) * HT + (gam * 4 + wave) * 1024 + lane * 16;
    va[4] = WS_FL + rho * FR + FX1 + (lane & 1) * FL;
    va[5] = 0u;
    va[6] = WS_FL + rho * FR + FH + gam * FL;
    va[7] = 0u - 4;
    va[8] = lane * 16;
    va[9] = 0u;
    va[10] = 0u;
    va[11] = 0u;
    va[12] = (g == q && p < 2) ? (0x3F80u << (16 * p)) : 0u;
    va[13] = (g == q && p >= 2) ? (0x3F80u << (16 * (p - 2))) : 0u;
    va[14] = 0u;
    va[15] = 0u;
    sv[0] = (unsigned)(unsigned long long)((wave < 2 ? wg : wu) + gam * 256 + (wave & 1) * (32 * 9216));
    sv[1] = (unsigned)((unsigned long long)((wave < 2 ? wg : wu) + gam * 256 + (wave & 1) * (32 * 9216)) >> 32);
    sv[2] = wave * 8192;
    sv[3] = pc0 * 1024 + 2048;
  }
#pragma unroll
  for (int i = 0; i < 28; ++i) { const unsigned u = __builtin_amdgcn_readfirstlane(sv[i]); if (i < 12) sa[i + 4] = u; else sb[i - 12] = u; }

  asm volatile(
R"(s_mov_b32 s71, 0
s_mov_b32 s74, 0
s_load_dword s37, s[38:39], 0x0
s_lshr_b32 s75, s36, 2
s_cmp_eq_u32 s75, 1
s_cbranch_scc1 .Lrole1
s_cmp_eq_u32 s75, 2
s_cbranch_scc1 .Lrole2
s_cmp_eq_u32 s75, 3
s_cbranch_scc1 .Lrole3
.Lrole0:
s_add_u32 m0, s48, 4096
s_nop 0
global_load_lds_dwordx4 v0, s[40:41] offset:-4096
global_load_lds_dwordx4 v1, s[40:41] offset:-3072
global_load_lds_dwordx4 v2, s[40:41] offset:-2048
global_load_lds_dwordx4 v3, s[40:41] offset:-1024
global_load_lds_dwordx4 v4, s[40:41] offset:0
global_load_lds_dwordx4 v5, s[40:41] offset:1024
global_load_lds_dwordx4 v6, s[40:41] offset:2048
s_add_u32 s40, s40, 221184
s_addc_u32 s41, s41, 0
s_add_u32 m0, s48, 32768
s_nop 0
global_load_lds_dwordx4 v0, s[40:41] offset:-4096
global_load_lds_dwordx4 v1, s[40:41] offset:-3072
global_load_lds_dwordx4 v2, s[40:41] offset:-2048
global_load_lds_dwordx4 v3, s[40:41] offset:-1024
global_load_lds_dwordx4 v4, s[40:41] offset:0
global_load_lds_dwordx4 v5, s[40:41] offset:1024
global_load_lds_dwordx4 v6, s[40:41] offset:2048
s_add_u32 s40, s40, 221184
s_addc_u32 s41, s41, 0
s_add_u32 m0, s48, 61440
s_nop 0
global_load_lds_dwordx4 v0, s[40:41] offset:-4096
global_load_lds_dwordx4 v1, s[40:41] offset:-3072
global_load_lds_dwordx4 v2, s[40:41] offset:-2048
global_load_lds_dwordx4 v3, s[40:41] offset:-1024
global_load_lds_dwordx4 v4, s[40:41] offset:0
global_load_lds_dwordx4 v5, s[40:41] offset:1024
global_load_lds_dwordx4 v6, s[40:41] offset:2048
s_add_u32 s40, s40, 221184
s_addc_u32 s41, s41, 0
s_add_u32 m0, s48, 90112
s_nop 0
global_load_lds_dwordx4 v0, s[40:41] offset:-4096
global_load_lds_dwordx4 v1, s[40:41] offset:-3072
global_load_lds_dwordx4 v2, s[40:41] offset:-2048
global_load_lds_dwordx4 v3, s[40:41] offset:-1024
global_load_lds_dwordx4 v4, s[40:41] offset:0
global_load_lds_dwordx4 v5, s[40:41] offset:1024
global_load_lds_dwordx4 v6, s[40:41] offset:2048
s_add_u32 s40, s40, 221184
s_addc_u32 s41, s41, 0
s_add_u32 m0, s49, 0
s_nop 0
global_load_lds_dwordx4 v9, s[42:43] offset:-2048 sc1
global_load_lds_dwordx4 v10, s[42:43] offset:-1024 sc1
global_load_lds_dwordx4 v11, s[42:43] offset:0 sc1
global_load_lds_dwordx4 v12, s[42:43] offset:1024 sc1
s_bitcmp1_b32 s36, 1
s_cbranch_scc1 .Lqa_x0
global_load_lds_dwordx4 v13, s[42:43] offset:2048 sc1
.Lqa_x0:
s_sub_u32 s42, s42, 18432
s_subb_u32 s43, s43, 0
s_add_u32 m0, s49, 18432
s_nop 0
global_load_lds_dwordx4 v9, s[42:43] offset:-2048 sc1
global_load_lds_dwordx4 v10, s[42:43] offset:-1024 sc1
global_load_lds_dwordx4 v11, s[42:43] offset:0 sc1
global_load_lds_dwordx4 v12, s[42:43] offset:1024 sc1
s_bitcmp1_b32 s36, 1
s_cbranch_scc1 .Lqa_x1
global_load_lds_dwordx4 v13, s[42:43] offset:2048 sc1
.Lqa_x1:
s_sub_u32 s42, s42, 18432
s_subb_u32 s43, s43, 0
v_add_u32 v160, 0x10000, v7
v_add_u32 v161, 0x20000, v7
v_add_u32 v162, 0x10000, v8
v_add_u32 v163, 0x20000, v8
v_mov_b32 v104, 0x3F803F80
v_mov_b32 v105, 0x3F803F80
v_mov_b32 v106, 0x3F803F80
v_mov_b32 v107, 0x3F803F80
v_mov_b32 v139, 0
v_mov_b32 v143, 0
v_mov_b32 v147, 0
v_mov_b32 v151, 0
v_mov_b32 v164, 46080
s_waitcnt lgkmcnt(0)
s_mul_i32 s37, s37, 0x9E3779B1
s_add_u32 s37, s37, 0x7F4A7C15
v_add_u32 v44, s37, v44
v_add_u32 v46, s37, v46
v_add_u32 v48, s37, v48
ds_write_b32 v164, v139
s_mov_b64 s[76:77], 0
s_mov_b32 s73, 18
s_waitcnt vmcnt(29)
s_barrier
ds_read_b64_tr_b16 a[0:1], v7 offset:47104
ds_read_b64_tr_b16 a[2:3], v7 offset:48832
ds_read_b64_tr_b16 a[4:5], v7 offset:47248
ds_read_b64_tr_b16 a[6:7], v7 offset:48976
ds_read_b64_tr_b16 a[8:9], v7 offset:47392
ds_read_b64_tr_b16 a[10:11], v7 offset:49120
ds_read_b64_tr_b16 a[216:217], v8 offset:47104
ds_read_b64_tr_b16 a[218:219], v8 offset:48832
ds_read_b64_tr_b16 a[12:13], v7 offset:60928
ds_read_b64_tr_b16 a[14:15], v7 offset:62656
ds_read_b64_tr_b16 a[16:17], v7 offset:61072
ds_read_b64_tr_b16 a[18:19], v7 offset:62800
ds_read_b64_tr_b16 a[20:21], v7 offset:61216
ds_read_b64_tr_b16 a[22:23], v7 offset:62944
ds_read_b64_tr_b16 a[220:221], v8 offset:60928
ds_read_b64_tr_b16 a[222:223], v8 offset:62656
s_waitcnt lgkmcnt(0)
s_barrier
s_add_u32 m0, s48, 4096
s_nop 0
global_load_lds_dwordx4 v0, s[40:41] offset:-4096
global_load_lds_dwordx4 v1, s[40:41] offset:-3072
global_load_lds_dwordx4 v2, s[40:41] offset:-2048
global_load_lds_dwordx4 v3, s[40:41] offset:-1024
global_load_lds_dwordx4 v4, s[40:41] offset:0
global_load_lds_dwordx4 v5, s[40:41] offset:1024
global_load_lds_dwordx4 v6, s[40:41] offset:2048
s_add_u32 s40, s40, 221184
s_addc_u32 s41, s41, 0
s_waitcnt vmcnt(29)
s_barrier
ds_read_b64_tr_b16 a[24:25], v160 offset:10240
ds_read_b64_tr_b16 a[26:27], v160 offset:11968
ds_read_b64_tr_b16 a[28:29], v160 offset:10384
ds_read_b64_tr_b16 a[30:31], v160 offset:12112
ds_read_b64_tr_b16 a[32:33], v160 offset:10528
ds_read_b64_tr_b16 a[34:35], v160 offset:12256
ds_read_b64_tr_b16 a[224:225], v162 offset:10240
ds_read_b64_tr_b16 a[226:227], v162 offset:11968
ds_read_b64_tr_b16 a[36:37], v160 offset:24064
ds_read_b64_tr_b16 a[38:39], v160 offset:25792
ds_read_b64_tr_b16 a[40:41], v160 offset:24208
ds_read_b64_tr_b16 a[42:43], v160 offset:25936
ds_read_b64_tr_b16 a[44:45], v160 offset:24352
ds_read_b64_tr_b16 a[46:47], v160 offset:26080
ds_read_b64_tr_b16 a[228:229], v162 offset:24064
ds_read_b64_tr_b16 a[230:231], v162 offset:25792
v_mfma_f32_16x16x32_bf16 v[84:87], a[0:3], v[104:107], 0
v_mfma_f32_16x16x32_bf16 v[88:91], a[4:7], v[104:107], 0
v_mfma_f32_16x16x32_bf16 v[92:95], v[104:107], a[8:11], 0
v_mfma_f32_16x16x32_bf16 v[96:99], a[216:219], v[104:107], 0
v_mfma_f32_16x16x32_bf16 v[100:103], v[104:107], a[216:219], 0
v_mfma_f32_16x16x32_bf16 v[84:87], a[12:15], v[104:107], v[84:87]
v_mfma_f32_16x16x32_bf16 v[88:91], a[16:19], v[104:107], v[88:91]
v_mfma_f32_16x16x32_bf16 v[92:95], v[104:107], a[20:23], v[92:95]
v_mfma_f32_16x16x32_bf16 v[96:99], a[220:223], v[104:107], v[96:99]
v_mfma_f32_16x16x32_bf16 v[100:103], v[104:107], a[220:223], v[100:103]
s_waitcnt lgkmcnt(0)
s_barrier
s_add_u32 m0, s48, 32768
s_nop 0
global_load_lds_dwordx4 v0, s[40:41] offset:-4096
global_load_lds_dwordx4 v1, s[40:41] offset:-3072
global_load_lds_dwordx4 v2, s[40:41] offset:-2048
global_load_lds_dwordx4 v3, s[40:41] offset:-1024
global_load_lds_dwordx4 v4, s[40:41] offset:0
global_load_lds_dwordx4 v5, s[40:41] offset:1024
global_load_lds_dwordx4 v6, s[40:41] offset:2048
s_add_u32 s40, s40, 221184
s_addc_u32 s41, s41, 0
s_waitcnt vmcnt(29)
s_barrier
ds_read_b64_tr_b16 a[48:49], v160 offset:38912
ds_read_b64_tr_b16 a[50:51], v160 offset:40640
ds_read_b64_tr_b16 a[52:53], v160 offset:39056
ds_read_b64_tr_b16 a[54:55], v160 offset:40784
ds_read_b64_tr_b16 a[56:57], v160 offset:39200
ds_read_b64_tr_b16 a[58:59], v160 offset:40928
ds_read_b64_tr_b16 a[232:233], v162 offset:38912
ds_read_b64_tr_b16 a[234:235], v162 offset:40640
ds_read_b64_tr_b16 a[60:61], v160 offset:52736
ds_read_b64_tr_b16 a[62:63], v160 offset:54464
ds_read_b64_tr_b16 a[64:65], v160 offset:52880
ds_read_b64_tr_b16 a[66:67], v160 offset:54608
ds_read_b64_tr_b16 a[68:69], v160 offset:53024
ds_read_b64_tr_b16 a[70:71], v160 offset:54752
ds_read_b64_tr_b16 a[236:237], v162 offset:52736
ds_read_b64_tr_b16 a[238:239], v162 offset:54464
v_mfma_f32_16x16x32_bf16 v[84:87], a[24:27], v[104:107], v[84:87]
v_mfma_f32_16x16x32_bf16 v[88:91], a[28:31], v[104:107], v[88:91]
v_mfma_f32_16x16x32_bf16 v[92:95], v[104:107], a[32:35], v[92:95]
v_mfma_f32_16x16x32_bf16 v[96:99], a[224:227], v[104:107], v[96:99]
v_mfma_f32_16x16x32_bf16 v[100:103], v[104:107], a[224:227], v[100:103]
v_mfma_f32_16x16x32_bf16 v[84:87], a[36:39], v[104:107], v[84:87]
v_mfma_f32_16x16x32_bf16 v[88:91], a[40:43], v[104:107], v[88:91]
v_mfma_f32_16x16x32_bf16 v[92:95], v[104:107], a[44:47], v[92:95]
v_mfma_f32_16x16x32_bf16 v[96:99], a[228:231], v[104:107], v[96:99]
v_mfma_f32_16x16x32_bf16 v[100:103], v[104:107], a[228:231], v[100:103]
s_waitcnt lgkmcnt(0)
s_barrier
s_add_u32 m0, s48, 61440
s_nop 0
global_load_lds_dwordx4 v0, s[40:41] offset:-4096
global_load_lds_dwordx4 v1, s[40:41] offset:-3072
global_load_lds_dwordx4 v2, s[40:41] offset:-2048
global_load_lds_dwordx4 v3, s[40:41] offset:-1024
global_load_lds_dwordx4 v4, s[40:41] offset:0
global_load_lds_dwordx4 v5, s[40:41] offset:1024
global_load_lds_dwordx4 v6, s[40:41] offset:2048
s_add_u32 s40, s40, 221184
s_addc_u32 s41, s41, 0
s_waitcnt vmcnt(29)
s_barrier
ds_read_b64_tr_b16 a[72:73], v161 offset:2048
ds_read_b64_tr_b16 a[74:75], v161 offset:3776
ds_read_b64_tr_b16 a[76:77], v161 offset:2192
ds_read_b64_tr_b16 a[78:79], v161 offset:3920
ds_read_b64_tr_b16 a[80:81], v161 offset:2336
ds_read_b64_tr_b16 a[82:83], v161 offset:4064
ds_read_b64_tr_b16 a[240:241], v163 offset:2048
ds_read_b64_tr_b16 a[242:243], v163 offset:3776
ds_read_b64_tr_b16 a[84:85], v161 offset:15872
ds_read_b64_tr_b16 a[86:87], v161 offset:17600
ds_read_b64_tr_b16 a[88:89], v161 offset:16016
ds_read_b64_tr_b16 a[90:91], v161 offset:17744
ds_read_b64_tr_b16 a[92:93], v161 offset:16160
ds_read_b64_tr_b16 a[94:95], v161 offset:17888
ds_read_b64_tr_b16 a[244:245], v163 offset:15872
ds_read_b64_tr_b16 a[246:247], v163 offset:17600
v_mfma_f32_16x16x32_bf16 v[84:87], a[48:51], v[104:107], v[84:87]
v_mfma_f32_16x16x32_bf16 v[88:91], a[52:55], v[104:107], v[88:91]
v_mfma_f32_16x16x32_bf16 v[92:95], v[104:107], a[56:59], v[92:95]
v_mfma_f32_16x16x32_bf16 v[96:99], a[232:235], v[104:107], v[96:99]
v_mfma_f32_16x16x32_bf16 v[100:103], v[104:107], a[232:235], v[100:103]
v_mfma_f32_16x16x32_bf16 v[84:87], a[60:63], v[104:107], v[84:87]
v_mfma_f32_16x16x32_bf16 v[88:91], a[64:67], v[104:107], v[88:91]
v_mfma_f32_16x16x32_bf16 v[92:95], v[104:107], a[68:71], v[92:95]
v_mfma_f32_16x16x32_bf16 v[96:99], a[236:239], v[104:107], v[96:99]
v_mfma_f32_16x16x32_bf16 v[100:103], v[104:107], a[236:239], v[100:103]
s_waitcnt lgkmcnt(0)
s_barrier
s_add_u32 m0, s48, 90112
s_nop 0
global_load_lds_dwordx4 v0, s[40:41] offset:-4096
global_load_lds_dwordx4 v1, s[40:41] offset:-3072
global_load_lds_dwordx4 v2, s[40:41] offset:-2048
global_load_lds_dwordx4 v3, s[40:41] offset:-1024
global_load_lds_dwordx4 v4, s[40:41] offset:0
global_load_lds_dwordx4 v5, s[40:41] offset:1024
global_load_lds_dwordx4 v6, s[40:41] offset:2048
s_add_u32 s40, s40, 221184
s_addc_u32 s41, s41, 0
s_waitcnt vmcnt(21)
s_barrier
ds_read_b64_tr_b16 a[96:97], v7 offset:47104
ds_read_b64_tr_b16 a[98:99], v7 offset:48832
ds_read_b64_tr_b16 a[100:101], v7 offset:47248
ds_read_b64_tr_b16 a[102:103], v7 offset:48976
ds_read_b64_tr_b16 a[104:105], v7 offset:47392
ds_read_b64_tr_b16 a[106:107], v7 offset:49120
ds_read_b64_tr_b16 a[248:249], v8 offset:47104
ds_read_b64_tr_b16 a[250:251], v8 offset:48832
ds_read_b64_tr_b16 a[108:109], v7 offset:60928
ds_read_b64_tr_b16 a[110:111], v7 offset:62656
ds_read_b64_tr_b16 a[112:113], v7 offset:61072
ds_read_b64_tr_b16 a[114:115], v7 offset:62800
ds_read_b64_tr_b16 a[116:117], v7 offset:61216
ds_read_b64_tr_b16 a[118:119], v7 offset:62944
ds_read_b64_tr_b16 a[252:253], v8 offset:60928
ds_read_b64_tr_b16 a[254:255], v8 offset:62656
v_mfma_f32_16x16x32_bf16 v[84:87], a[72:75], v[104:107], v[84:87]
v_mfma_f32_16x16x32_bf16 v[88:91], a[76:79], v[104:107], v[88:91]
v_mfma_f32_16x16x32_bf16 v[92:95], v[104:107], a[80:83], v[92:95]
v_mfma_f32_16x16x32_bf16 v[96:99], a[240:243], v[104:107], v[96:99]
v_mfma_f32_16x16x32_bf16 v[100:103], v[104:107], a[240:243], v[100:103]
v_mfma_f32_16x16x32_bf16 v[84:87], a[84:87], v[104:107], v[84:87]
v_mfma_f32_16x16x32_bf16 v[88:91], a[88:91], v[104:107], v[88:91]
v_mfma_f32_16x16x32_bf16 v[92:95], v[104:107], a[92:95], v[92:95]
v_mfma_f32_16x16x32_bf16 v[96:99], a[244:247], v[104:107], v[96:99]
v_mfma_f32_16x16x32_bf16 v[100:103], v[104:107], a[244:247], v[100:103]
s_waitcnt lgkmcnt(0)
s_barrier
s_add_u32 m0, s48, 4096
s_nop 0
global_load_lds_dwordx4 v0, s[40:41] offset:-4096
global_load_lds_dwordx4 v1, s[40:41] offset:-3072
global_load_lds_dwordx4 v2, s[40:41] offset:-2048
global_load_lds_dwordx4 v3, s[40:41] offset:-1024
global_load_lds_dwordx4 v4, s[40:41] offset:0
global_load_lds_dwordx4 v5, s[40:41] offset:1024
global_load_lds_dwordx4 v6, s[40:41] offset:2048
s_add_u32 s40, s40, 221184
s_addc_u32 s41, s41, 0
s_waitcnt vmcnt(21)
s_barrier
ds_read_b64_tr_b16 a[120:121], v160 offset:10240
ds_read_b64_tr_b16 a[122:123], v160 offset:11968
ds_read_b64_tr_b16 a[124:125], v160 offset:10384
ds_read_b64_tr_b16 a[126:127], v160 offset:12112
ds_read_b64_tr_b16 a[128:129], v160 offset:10528
ds_read_b64_tr_b16 a[130:131], v160 offset:12256
ds_read_b64_tr_b16 v[52:53], v162 offset:10240
ds_read_b64_tr_b16 v[54:55], v162 offset:11968
ds_read_b64_tr_b16 a[132:133], v160 offset:24064
ds_read_b64_tr_b16 a[134:135], v160 offset:25792
ds_read_b64_tr_b16 a[136:137], v160 offset:24208
ds_read_b64_tr_b16 a[138:139], v160 offset:25936
ds_read_b64_tr_b16 a[140:141], v160 offset:24352
ds_read_b64_tr_b16 a[142:143], v160 offset:26080
ds_read_b64_tr_b16 v[56:57], v162 offset:24064
ds_read_b64_tr_b16 v[58:59], v162 offset:25792
v_mfma_f32_16x16x32_bf16 v[84:87], a[96:99], v[104:107], v[84:87]
v_mfma_f32_16x16x32_bf16 v[88:91], a[100:103], v[104:107], v[88:91]
v_mfma_f32_16x16x32_bf16 v[92:95], v[104:107], a[104:107], v[92:95]
v_mfma_f32_16x16x32_bf16 v[96:99], a[248:251], v[104:107], v[96:99]
v_mfma_f32_16x16x32_bf16 v[100:103], v[104:107], a[248:251], v[100:103]
v_mfma_f32_16x16x32_bf16 v[84:87], a[108:111], v[104:107], v[84:87]
v_mfma_f32_16x16x32_bf16 v[88:91], a[112:115], v[104:107], v[88:91]
v_mfma_f32_16x16x32_bf16 v[92:95], v[104:107], a[116:119], v[92:95]
v_mfma_f32_16x16x32_bf16 v[96:99], a[252:255], v[104:107], v[96:99]
v_mfma_f32_16x16x32_bf16 v[100:103], v[104:107], a[252:255], v[100:103]
s_waitcnt vmcnt(14)
s_barrier
ds_read_b64_tr_b16 a[144:145], v160 offset:38912
ds_read_b64_tr_b16 a[146:147], v160 offset:40640
ds_read_b64_tr_b16 a[148:149], v160 offset:39056
ds_read_b64_tr_b16 a[150:151], v160 offset:40784
ds_read_b64_tr_b16 a[152:153], v160 offset:39200
ds_read_b64_tr_b16 a[154:155], v160 offset:40928
ds_read_b64_tr_b16 v[60:61], v162 offset:38912
ds_read_b64_tr_b16 v[62:63], v162 offset:40640
ds_read_b64_tr_b16 a[156:157], v160 offset:52736
ds_read_b64_tr_b16 a[158:159], v160 offset:54464
ds_read_b64_tr_b16 a[160:161], v160 offset:52880
ds_read_b64_tr_b16 a[162:163], v160 offset:54608
ds_read_b64_tr_b16 a[164:165], v160 offset:53024
ds_read_b64_tr_b16 a[166:167], v160 offset:54752
ds_read_b64_tr_b16 v[64:65], v162 offset:52736
ds_read_b64_tr_b16 v[66:67], v162 offset:54464
s_waitcnt lgkmcnt(15)
v_mfma_f32_16x16x32_bf16 v[84:87], a[120:123], v[104:107], v[84:87]
v_mfma_f32_16x16x32_bf16 v[88:91], a[124:127], v[104:107], v[88:91]
v_mfma_f32_16x16x32_bf16 v[92:95], v[104:107], a[128:131], v[92:95]
v_mfma_f32_16x16x32_bf16 v[96:99], v[52:55], v[104:107], v[96:99]
v_mfma_f32_16x16x32_bf16 v[100:103], v[104:107], v[52:55], v[100:103]
v_mfma_f32_16x16x32_bf16 v[84:87], a[132:135], v[104:107], v[84:87]
v_mfma_f32_16x16x32_bf16 v[88:91], a[136:139], v[104:107], v[88:91]
v_mfma_f32_16x16x32_bf16 v[92:95], v[104:107], a[140:143], v[92:95]
v_mfma_f32_16x16x32_bf16 v[96:99], v[56:59], v[104:107], v[96:99]
v_mfma_f32_16x16x32_bf16 v[100:103], v[104:107], v[56:59], v[100:103]
s_waitcnt vmcnt(7)
s_barrier
ds_read_b64_tr_b16 a[168:169], v161 offset:2048
ds_read_b64_tr_b16 a[170:171], v161 offset:3776
ds_read_b64_tr_b16 a[172:173], v161 offset:2192
ds_read_b64_tr_b16 a[174:175], v161 offset:3920
ds_read_b64_tr_b16 a[176:177], v161 offset:2336
ds_read_b64_tr_b16 a[178:179], v161 offset:4064
ds_read_b64_tr_b16 v[68:69], v163 offset:2048
ds_read_b64_tr_b16 v[70:71], v163 offset:3776
ds_read_b64_tr_b16 a[180:181], v161 offset:15872
ds_read_b64_tr_b16 a[182:183], v161 offset:17600
ds_read_b64_tr_b16 a[184:185], v161 offset:16016
ds_read_b64_tr_b16 a[186:187], v161 offset:17744
ds_read_b64_tr_b16 a[188:189], v161 offset:16160
ds_read_b64_tr_b16 a[190:191], v161 offset:17888
ds_read_b64_tr_b16 v[72:73], v163 offset:15872
ds_read_b64_tr_b16 v[74:75], v163 offset:17600
s_waitcnt lgkmcnt(15)
v_mfma_f32_16x16x32_bf16 v[84:87], a[144:147], v[104:107], v[84:87]
v_mfma_f32_16x16x32_bf16 v[88:91], a[148:151], v[104:107], v[88:91]
v_mfma_f32_16x16x32_bf16 v[92:95], v[104:107], a[152:155], v[92:95]
v_mfma_f32_16x16x32_bf16 v[96:99], v[60:63], v[104:107], v[96:99]
v_mfma_f32_16x16x32_bf16 v[100:103], v[104:107], v[60:63], v[100:103]
v_mfma_f32_16x16x32_bf16 v[84:87], a[156:159], v[104:107], v[84:87]
v_mfma_f32_16x16x32_bf16 v[88:91], a[160:163], v[104:107], v[88:91]
v_mfma_f32_16x16x32_bf16 v[92:95], v[104:107], a[164:167], v[92:95]
v_mfma_f32_16x16x32_bf16 v[96:99], v[64:67], v[104:107], v[96:99]
v_mfma_f32_16x16x32_bf16 v[100:103], v[104:107], v[64:67], v[100:103]
s_waitcnt vmcnt(0)
s_barrier
ds_read_b64_tr_b16 a[192:193], v7 offset:47104
ds_read_b64_tr_b16 a[194:195], v7 offset:48832
ds_read_b64_tr_b16 a[196:197], v7 offset:47248
ds_read_b64_tr_b16 a[198:199], v7 offset:48976
ds_read_b64_tr_b16 a[200:201], v7 offset:47392
ds_read_b64_tr_b16 a[202:203], v7 offset:49120
ds_read_b64_tr_b16 v[76:77], v8 offset:47104
ds_read_b64_tr_b16 v[78:79], v8 offset:48832
ds_read_b64_tr_b16 a[204:205], v7 offset:60928
ds_read_b64_tr_b16 a[206:207], v7 offset:62656
ds_read_b64_tr_b16 a[208:209], v7 offset:61072
ds_read_b64_tr_b16 a[210:211], v7 offset:62800
ds_read_b64_tr_b16 a[212:213], v7 offset:61216
ds_read_b64_tr_b16 a[214:215], v7 offset:62944
ds_read_b64_tr_b16 v[80:81], v8 offset:60928
ds_read_b64_tr_b16 v[82:83], v8 offset:62656
)"
R"(s_waitcnt lgkmcnt(15)
v_mfma_f32_16x16x32_bf16 v[84:87], a[168:171], v[104:107], v[84:87]
v_mfma_f32_16x16x32_bf16 v[88:91], a[172:175], v[104:107], v[88:91]
v_mfma_f32_16x16x32_bf16 v[92:95], v[104:107], a[176:179], v[92:95]
v_mfma_f32_16x16x32_bf16 v[96:99], v[68:71], v[104:107], v[96:99]
v_mfma_f32_16x16x32_bf16 v[100:103], v[104:107], v[68:71], v[100:103]
v_mfma_f32_16x16x32_bf16 v[84:87], a[180:183], v[104:107], v[84:87]
v_mfma_f32_16x16x32_bf16 v[88:91], a[184:187], v[104:107], v[88:91]
v_mfma_f32_16x16x32_bf16 v[92:95], v[104:107], a[188:191], v[92:95]
v_mfma_f32_16x16x32_bf16 v[96:99], v[72:75], v[104:107], v[96:99]
v_mfma_f32_16x16x32_bf16 v[100:103], v[104:107], v[72:75], v[100:103]
s_waitcnt lgkmcnt(14)
v_mfma_f32_16x16x32_bf16 v[84:87], a[192:195], v[104:107], v[84:87]
s_waitcnt lgkmcnt(12)
v_mfma_f32_16x16x32_bf16 v[88:91], a[196:199], v[104:107], v[88:91]
s_waitcnt lgkmcnt(10)
v_mfma_f32_16x16x32_bf16 v[92:95], v[104:107], a[200:203], v[92:95]
s_waitcnt lgkmcnt(8)
v_mfma_f32_16x16x32_bf16 v[96:99], v[76:79], v[104:107], v[96:99]
v_mfma_f32_16x16x32_bf16 v[100:103], v[104:107], v[76:79], v[100:103]
s_waitcnt lgkmcnt(6)
v_mfma_f32_16x16x32_bf16 v[84:87], a[204:207], v[104:107], v[84:87]
s_waitcnt lgkmcnt(4)
v_mfma_f32_16x16x32_bf16 v[88:91], a[208:211], v[104:107], v[88:91]
s_waitcnt lgkmcnt(2)
v_mfma_f32_16x16x32_bf16 v[92:95], v[104:107], a[212:215], v[92:95]
s_waitcnt lgkmcnt(0)
v_mfma_f32_16x16x32_bf16 v[96:99], v[80:83], v[104:107], v[96:99]
v_mfma_f32_16x16x32_bf16 v[100:103], v[104:107], v[80:83], v[100:103]
s_waitcnt vmcnt(0) lgkmcnt(0)
s_barrier
s_and_b32 s75, s36, 3
s_cmp_eq_u32 s75, 1
s_cbranch_scc1 .Lqa_w2
s_cmp_eq_u32 s75, 2
s_cbranch_scc1 .Lqa_w3
s_cmp_eq_u32 s75, 3
s_cbranch_scc1 .Lqa_w4
.Lqa_w1:
s_nop 7
v_mov_b32 v232, v30
v_mov_b32 v233, v31
v_mov_b32 v234, v32
v_mov_b32 v235, v33
s_mov_b32 s70, 33
s_waitcnt vmcnt(0) lgkmcnt(0)
s_nop 7
s_branch .Lqa_in5
.p2align 6
.Lqa_top14:
v_mfma_f32_16x16x32_bf16 v[132:135], a[216:219], v[42:45], 0
ds_read_b128 v[168:171], v50 offset:6144
v_max_f32 v94, v216, v217
v_mfma_f32_16x16x32_bf16 v[132:135], a[220:223], v[100:103], v[132:135]
v_max_f32 v95, v218, v219
v_mfma_f32_16x16x32_bf16 v[108:111], a[0:3], v[42:45], 0
s_waitcnt lgkmcnt(13)
ds_read_b128 v[172:175], v49 offset:8192
v_fma_f32 v236, v88, v8, v124
s_waitcnt lgkmcnt(12)
v_mfma_f32_16x16x32_bf16 v[132:135], a[224:227], v[104:107], v[132:135]
s_waitcnt lgkmcnt(10)
ds_read_b128 v[176:179], v50 offset:8192
v_fma_f32 v237, v89, v8, v125
v_mfma_f32_16x16x32_bf16 v[132:135], a[228:231], v[0:3], v[132:135]
v_fma_f32 v238, v90, v8, v126
v_fma_f32 v239, v91, v8, v127
v_mfma_f32_16x16x32_bf16 v[112:115], a[4:7], v[42:45], 0
s_waitcnt lgkmcnt(7)
ds_read_b128 v[180:183], v49 offset:10240
v_cvt_pk_bf16_f32 v148, v236, v237
v_mfma_f32_16x16x32_bf16 v[132:135], a[232:235], v[4:7], v[132:135]
s_waitcnt lgkmcnt(6)
ds_read_b128 v[184:187], v50 offset:10240
v_cvt_pk_bf16_f32 v149, v238, v239
v_mfma_f32_16x16x32_bf16 v[132:135], a[236:239], v[160:163], v[132:135]
s_nop 0
v_mfma_f32_16x16x32_bf16 v[212:215], v[148:151], v[136:139], v[30:33]
v_mul_f32 v21, 0x3eae1a93, v21
v_mul_f32 v51, v24, v21
v_mfma_f32_16x16x32_bf16 v[116:119], v[42:45], a[8:11], 0
s_waitcnt lgkmcnt(5)
ds_read_b128 v[188:191], v49 offset:12288
v_mul_f32 v93, v48, v21
v_mfma_f32_16x16x32_bf16 v[132:135], a[240:243], v[164:167], v[132:135]
s_waitcnt lgkmcnt(5)
ds_read_b128 v[192:195], v50 offset:12288
v_fma_f32 v236, v92, v224, v128
v_mfma_f32_16x16x32_bf16 v[132:135], a[244:247], v[168:171], v[132:135]
v_fma_f32 v237, v92, v225, v129
v_fma_f32 v238, v92, v226, v130
v_mfma_f32_16x16x32_bf16 v[108:111], a[12:15], v[100:103], v[108:111]
s_waitcnt lgkmcnt(5)
ds_read_b128 v[196:199], v49 offset:14336
v_max3_f32 v94, v212, v213, v94
v_mfma_f32_16x16x32_bf16 v[132:135], a[248:251], v[172:175], v[132:135]
s_xor_b32 s49, s49, 0x10000
ds_read_b128 v[200:203], v50 offset:14336
s_waitcnt lgkmcnt(6)
v_mfma_f32_16x16x32_bf16 v[132:135], a[252:255], v[176:179], v[132:135]
s_add_u32 m0, s49, 0
v_max3_f32 v95, v214, v215, v95
v_mul_f32 v94, v94, v51
v_mfma_f32_16x16x32_bf16 v[112:115], a[16:19], v[100:103], v[112:115]
s_waitcnt lgkmcnt(5)
ds_read_b128 v[204:207], v49 offset:16384
global_load_lds_dwordx4 v9, s[42:43] offset:-2048 sc1
v_mfma_f32_16x16x32_bf16 v[132:135], v[52:55], v[180:183], v[132:135]
s_waitcnt lgkmcnt(5)
ds_read_b128 v[208:211], v50 offset:16384
v_mul_f32 v95, v95, v93
v_mfma_f32_16x16x32_bf16 v[132:135], v[56:59], v[184:187], v[132:135]
v_max_f32 v240, v94, v95
v_mov_b32 v241, v240
v_mfma_f32_16x16x32_bf16 v[116:119], v[100:103], a[20:23], v[116:119]
s_waitcnt lgkmcnt(5)
v_permlane16_swap_b32 v240, v241
v_mfma_f32_16x16x32_bf16 v[132:135], v[60:63], v[188:191], v[132:135]
s_waitcnt lgkmcnt(4)
v_max_f32 v240, v240, v241
v_mov_b32 v241, v240
v_mfma_f32_16x16x32_bf16 v[132:135], v[64:67], v[192:195], v[132:135]
s_nop 0
v_permlane32_swap_b32 v240, v241
v_mfma_f32_16x16x32_bf16 v[108:111], a[24:27], v[104:107], v[108:111]
s_waitcnt lgkmcnt(3)
v_max_f32 v240, v240, v241
v_sub_f32 v94, v94, v240
v_mfma_f32_16x16x32_bf16 v[132:135], v[68:71], v[196:199], v[132:135]
s_waitcnt lgkmcnt(2)
v_sub_f32 v95, v95, v240
v_fma_f32 v239, v92, v227, v131
v_mfma_f32_16x16x32_bf16 v[132:135], v[72:75], v[200:203], v[132:135]
global_load_lds_dwordx4 v10, s[42:43] offset:-1024 sc1
v_cvt_pk_bf16_f32 v154, v236, v237
v_mfma_f32_16x16x32_bf16 v[112:115], a[28:31], v[104:107], v[112:115]
s_waitcnt lgkmcnt(1)
v_exp_f32 v242, v94
v_mfma_f32_16x16x32_bf16 v[132:135], v[76:79], v[204:207], v[132:135]
s_waitcnt lgkmcnt(0)
v_exp_f32 v243, v95
v_mfma_f32_16x16x32_bf16 v[132:135], v[80:83], v[208:211], v[132:135]
v_add_f32 v240, v242, v243
v_mov_b32 v241, v240
v_mfma_f32_16x16x32_bf16 v[116:119], v[104:107], a[32:35], v[116:119]
v_mul_f32 v242, v242, v24
v_cvt_pk_bf16_f32 v155, v238, v239
v_mfma_f32_16x16x32_bf16 v[108:111], a[36:39], v[0:3], v[108:111]
v_permlane16_swap_b32 v240, v241
v_mfma_f32_16x16x32_bf16 v[112:115], a[40:43], v[0:3], v[112:115]
v_add_f32 v240, v240, v241
v_mov_b32 v241, v240
v_mfma_f32_16x16x32_bf16 v[116:119], v[0:3], a[44:47], v[116:119]
v_mul_f32 v243, v243, v48
v_fma_f32 v236, v84, v8, v120
v_mfma_f32_16x16x32_bf16 v[108:111], a[48:51], v[4:7], v[108:111]
global_load_lds_dwordx4 v11, s[42:43] offset:0 sc1
v_fma_f32 v237, v85, v8, v121
v_mfma_f32_16x16x32_bf16 v[112:115], a[52:55], v[4:7], v[112:115]
v_permlane32_swap_b32 v240, v241
v_mfma_f32_16x16x32_bf16 v[116:119], v[4:7], a[56:59], v[116:119]
v_add_f32 v240, v240, v241
v_fma_f32 v238, v86, v8, v122
v_mfma_f32_16x16x32_bf16 v[108:111], a[60:63], v[160:163], v[108:111]
v_rcp_f32 v240, v240
v_mfma_f32_16x16x32_bf16 v[112:115], a[64:67], v[160:163], v[112:115]
v_mul_f32 v242, v242, v240
v_mul_f32 v243, v243, v240
v_mfma_f32_16x16x32_bf16 v[116:119], v[160:163], a[68:71], v[116:119]
v_cvt_pk_bf16_f32 v242, v242, v242
v_cvt_pk_bf16_f32 v243, v243, v243
v_mfma_f32_16x16x32_bf16 v[108:111], a[72:75], v[164:167], v[108:111]
v_and_b32 v222, v242, v38
v_and_b32 v223, v243, v39
v_mfma_f32_16x16x32_bf16 v[112:115], a[76:79], v[164:167], v[112:115]
v_mfma_f32_16x16x32_bf16 v[116:119], v[164:167], a[80:83], v[116:119]
s_barrier
v_mfma_f32_16x16x32_bf16 v[108:111], a[84:87], v[168:171], v[108:111]
global_load_lds_dwordx4 v12, s[42:43] offset:1024 sc1
ds_read_b32 v244, v22 offset:0
v_mfma_f32_16x16x32_bf16 v[112:115], a[88:91], v[168:171], v[112:115]
v_and_b32 v220, v242, v40
v_and_b32 v221, v243, v41
v_mfma_f32_16x16x32_bf16 v[116:119], v[168:171], a[92:95], v[116:119]
s_nop 0
v_mfma_f32_16x16x32_bf16 v[216:219], v[152:155], v[220:223], 0
v_mfma_f32_16x16x32_bf16 v[212:215], v[156:159], v[220:223], 0
v_fma_f32 v239, v87, v8, v123
v_cvt_pk_bf16_f32 v140, v236, v237
v_mfma_f32_16x16x32_bf16 v[108:111], a[96:99], v[172:175], v[108:111]
v_cvt_pk_bf16_f32 v141, v238, v239
v_mfma_f32_16x16x32_bf16 v[112:115], a[100:103], v[172:175], v[112:115]
v_mfma_f32_16x16x32_bf16 v[116:119], v[172:175], a[104:107], v[116:119]
v_mfma_f32_16x16x32_bf16 v[108:111], a[108:111], v[176:179], v[108:111]
v_cvt_pk_bf16_f32 v46, v216, v217
v_cvt_pk_bf16_f32 v47, v218, v219
v_mfma_f32_16x16x32_bf16 v[112:115], a[112:115], v[176:179], v[112:115]
s_waitcnt lgkmcnt(0)
v_fma_f32 v228, v96, v244, v132
v_fma_f32 v229, v97, v244, v133
v_mfma_f32_16x16x32_bf16 v[116:119], v[176:179], a[116:119], v[116:119]
global_load_lds_dwordx4 v13, s[42:43] offset:2048 sc1
s_add_u32 s42, s42, s46
s_addc_u32 s43, s43, s47
v_mfma_f32_16x16x32_bf16 v[108:111], a[120:123], v[180:183], v[108:111]
v_fma_f32 v230, v98, v244, v134
v_fma_f32 v231, v99, v244, v135
v_mfma_f32_16x16x32_bf16 v[112:115], a[124:127], v[180:183], v[112:115]
v_cvt_pk_bf16_f32 v228, v228, v229
v_cvt_pk_bf16_f32 v229, v230, v231
v_mfma_f32_16x16x32_bf16 v[116:119], v[180:183], a[128:131], v[116:119]
ds_write_b64 v20, v[228:229] offset:40960
v_mfma_f32_16x16x32_bf16 v[108:111], a[132:135], v[184:187], v[108:111]
ds_write_b64 v16, v[46:47]
v_mfma_f32_16x16x32_bf16 v[112:115], a[136:139], v[184:187], v[112:115]
v_cvt_pk_bf16_f32 v242, v212, v213
v_mfma_f32_16x16x32_bf16 v[116:119], v[184:187], a[140:143], v[116:119]
ds_write_b32 v17, v242
v_mfma_f32_16x16x32_bf16 v[108:111], a[144:147], v[188:191], v[108:111]
v_mfma_f32_16x16x32_bf16 v[112:115], a[148:151], v[188:191], v[112:115]
v_mfma_f32_16x16x32_bf16 v[116:119], v[188:191], a[152:155], v[116:119]
v_mfma_f32_16x16x32_bf16 v[108:111], a[156:159], v[192:195], v[108:111]
v_mfma_f32_16x16x32_bf16 v[112:115], a[160:163], v[192:195], v[112:115]
v_mfma_f32_16x16x32_bf16 v[116:119], v[192:195], a[164:167], v[116:119]
v_mfma_f32_16x16x32_bf16 v[108:111], a[168:171], v[196:199], v[108:111]
v_mfma_f32_16x16x32_bf16 v[112:115], a[172:175], v[196:199], v[112:115]
s_waitcnt vmcnt(5) lgkmcnt(0)
s_barrier
v_mfma_f32_16x16x32_bf16 v[116:119], v[196:199], a[176:179], v[116:119]
ds_read_b128 v[42:45], v49 offset:18432
v_mfma_f32_16x16x32_bf16 v[108:111], a[180:183], v[200:203], v[108:111]
ds_read_b128 v[100:103], v50 offset:18432
ds_read_b128 v[228:231], v15 offset:0
v_mfma_f32_16x16x32_bf16 v[112:115], a[184:187], v[200:203], v[112:115]
ds_read_b32 v8, v22 offset:0
ds_read_b32 v146, v19
v_mfma_f32_16x16x32_bf16 v[216:219], v[148:151], v[140:143], v[34:37]
v_mfma_f32_16x16x32_bf16 v[116:119], v[200:203], a[188:191], v[116:119]
ds_read_b128 v[104:107], v49 offset:20480
ds_read_b32 v21, v22 offset:192
v_mfma_f32_16x16x32_bf16 v[108:111], a[192:195], v[204:207], v[108:111]
ds_read_b128 v[0:3], v50 offset:20480
ds_read_b32 v24, v25
v_mfma_f32_16x16x32_bf16 v[112:115], a[196:199], v[204:207], v[112:115]
ds_read_b32 v48, v26
ds_read_b128 v[224:227], v23 offset:0
v_mfma_f32_16x16x32_bf16 v[116:119], v[204:207], a[200:203], v[116:119]
ds_read_b128 v[4:7], v49 offset:22528
ds_read_b64 v[156:157], v20 offset:41472
v_mfma_f32_16x16x32_bf16 v[108:111], a[204:207], v[208:211], v[108:111]
ds_read_b128 v[160:163], v50 offset:22528
ds_read_b32 v138, v18
v_mfma_f32_16x16x32_bf16 v[112:115], a[208:211], v[208:211], v[112:115]
s_waitcnt lgkmcnt(12)
global_store_dwordx4 v14, v[228:231], s[38:39] sc1
v_mfma_f32_16x16x32_bf16 v[116:119], v[208:211], a[212:215], v[116:119]
ds_read_b128 v[164:167], v49 offset:24576
v_add_u32 v14, 18432, v14
v_mfma_f32_16x16x32_bf16 v[132:135], a[216:219], v[42:45], 0
ds_read_b128 v[168:171], v50 offset:24576
v_max_f32 v94, v216, v217
v_mfma_f32_16x16x32_bf16 v[132:135], a[220:223], v[100:103], v[132:135]
v_max_f32 v95, v218, v219
v_mfma_f32_16x16x32_bf16 v[120:123], a[0:3], v[42:45], 0
s_waitcnt lgkmcnt(13)
ds_read_b128 v[172:175], v49 offset:26624
v_fma_f32 v236, v88, v8, v112
s_waitcnt lgkmcnt(12)
v_mfma_f32_16x16x32_bf16 v[132:135], a[224:227], v[104:107], v[132:135]
s_waitcnt lgkmcnt(10)
ds_read_b128 v[176:179], v50 offset:26624
v_fma_f32 v237, v89, v8, v113
v_mfma_f32_16x16x32_bf16 v[132:135], a[228:231], v[0:3], v[132:135]
v_fma_f32 v238, v90, v8, v114
v_fma_f32 v239, v91, v8, v115
v_mfma_f32_16x16x32_bf16 v[124:127], a[4:7], v[42:45], 0
s_waitcnt lgkmcnt(7)
ds_read_b128 v[180:183], v49 offset:28672
v_cvt_pk_bf16_f32 v144, v236, v237
v_mfma_f32_16x16x32_bf16 v[132:135], a[232:235], v[4:7], v[132:135]
s_waitcnt lgkmcnt(6)
ds_read_b128 v[184:187], v50 offset:28672
v_cvt_pk_bf16_f32 v145, v238, v239
v_mfma_f32_16x16x32_bf16 v[132:135], a[236:239], v[160:163], v[132:135]
s_nop 0
v_mfma_f32_16x16x32_bf16 v[212:215], v[144:147], v[140:143], v[232:235]
v_mul_f32 v21, 0x3eae1a93, v21
v_mul_f32 v51, v24, v21
v_mfma_f32_16x16x32_bf16 v[128:131], v[42:45], a[8:11], 0
s_waitcnt lgkmcnt(5)
ds_read_b128 v[188:191], v49 offset:30720
v_mul_f32 v93, v48, v21
v_mfma_f32_16x16x32_bf16 v[132:135], a[240:243], v[164:167], v[132:135]
s_waitcnt lgkmcnt(5)
ds_read_b128 v[192:195], v50 offset:30720
v_fma_f32 v236, v92, v224, v116
v_mfma_f32_16x16x32_bf16 v[132:135], a[244:247], v[168:171], v[132:135]
v_fma_f32 v237, v92, v225, v117
v_fma_f32 v238, v92, v226, v118
v_mfma_f32_16x16x32_bf16 v[120:123], a[12:15], v[100:103], v[120:123]
s_waitcnt lgkmcnt(5)
ds_read_b128 v[196:199], v49 offset:32768
v_max3_f32 v94, v212, v213, v94
v_mfma_f32_16x16x32_bf16 v[132:135], a[248:251], v[172:175], v[132:135]
s_add_u32 m0, s49, 18432
ds_read_b128 v[200:203], v50 offset:32768
s_waitcnt lgkmcnt(6)
v_mfma_f32_16x16x32_bf16 v[132:135], a[252:255], v[176:179], v[132:135]
global_load_lds_dwordx4 v9, s[42:43] offset:-2048 sc1
v_max3_f32 v95, v214, v215, v95
v_mfma_f32_16x16x32_bf16 v[124:127], a[16:19], v[100:103], v[124:127]
s_waitcnt lgkmcnt(5)
ds_read_b128 v[204:207], v49 offset:34816
v_mul_f32 v94, v94, v51
v_mfma_f32_16x16x32_bf16 v[132:135], v[52:55], v[180:183], v[132:135]
s_waitcnt lgkmcnt(5)
ds_read_b128 v[208:211], v50 offset:34816
v_mul_f32 v95, v95, v93
v_mfma_f32_16x16x32_bf16 v[132:135], v[56:59], v[184:187], v[132:135]
v_max_f32 v240, v94, v95
v_mov_b32 v241, v240
v_mfma_f32_16x16x32_bf16 v[128:131], v[100:103], a[20:23], v[128:131]
s_waitcnt lgkmcnt(5)
v_permlane16_swap_b32 v240, v241
v_mfma_f32_16x16x32_bf16 v[132:135], v[60:63], v[188:191], v[132:135]
s_waitcnt lgkmcnt(4)
v_max_f32 v240, v240, v241
v_mov_b32 v241, v240
v_mfma_f32_16x16x32_bf16 v[132:135], v[64:67], v[192:195], v[132:135]
s_nop 0
v_permlane32_swap_b32 v240, v241
v_mfma_f32_16x16x32_bf16 v[120:123], a[24:27], v[104:107], v[120:123]
s_waitcnt lgkmcnt(3)
v_max_f32 v240, v240, v241
v_sub_f32 v94, v94, v240
v_mfma_f32_16x16x32_bf16 v[132:135], v[68:71], v[196:199], v[132:135]
s_waitcnt lgkmcnt(2)
global_load_lds_dwordx4 v10, s[42:43] offset:-1024 sc1
v_sub_f32 v95, v95, v240
v_mfma_f32_16x16x32_bf16 v[132:135], v[72:75], v[200:203], v[132:135]
v_exp_f32 v242, v94
v_mfma_f32_16x16x32_bf16 v[124:127], a[28:31], v[104:107], v[124:127]
s_waitcnt lgkmcnt(1)
v_exp_f32 v243, v95
v_mfma_f32_16x16x32_bf16 v[132:135], v[76:79], v[204:207], v[132:135]
s_waitcnt lgkmcnt(0)
v_add_f32 v240, v242, v243
v_mov_b32 v241, v240
v_mfma_f32_16x16x32_bf16 v[132:135], v[80:83], v[208:211], v[132:135]
v_mul_f32 v242, v242, v24
v_fma_f32 v239, v92, v227, v119
v_mfma_f32_16x16x32_bf16 v[128:131], v[104:107], a[32:35], v[128:131]
v_permlane16_swap_b32 v240, v241
v_mfma_f32_16x16x32_bf16 v[120:123], a[36:39], v[0:3], v[120:123]
v_add_f32 v240, v240, v241
v_mov_b32 v241, v240
v_mfma_f32_16x16x32_bf16 v[124:127], a[40:43], v[0:3], v[124:127]
v_mul_f32 v243, v243, v48
v_cvt_pk_bf16_f32 v152, v236, v237
v_mfma_f32_16x16x32_bf16 v[128:131], v[0:3], a[44:47], v[128:131]
global_load_lds_dwordx4 v11, s[42:43] offset:0 sc1
v_cvt_pk_bf16_f32 v153, v238, v239
v_mfma_f32_16x16x32_bf16 v[120:123], a[48:51], v[4:7], v[120:123]
v_permlane32_swap_b32 v240, v241
v_mfma_f32_16x16x32_bf16 v[124:127], a[52:55], v[4:7], v[124:127]
v_add_f32 v240, v240, v241
v_fma_f32 v236, v84, v8, v108
v_mfma_f32_16x16x32_bf16 v[128:131], v[4:7], a[56:59], v[128:131]
v_rcp_f32 v240, v240
v_mfma_f32_16x16x32_bf16 v[120:123], a[60:63], v[160:163], v[120:123]
v_mul_f32 v242, v242, v240
v_mul_f32 v243, v243, v240
v_mfma_f32_16x16x32_bf16 v[124:127], a[64:67], v[160:163], v[124:127]
v_cvt_pk_bf16_f32 v242, v242, v242
v_cvt_pk_bf16_f32 v243, v243, v243
v_mfma_f32_16x16x32_bf16 v[128:131], v[160:163], a[68:71], v[128:131]
v_and_b32 v220, v242, v38
v_and_b32 v221, v243, v39
v_mfma_f32_16x16x32_bf16 v[120:123], a[72:75], v[164:167], v[120:123]
v_and_b32 v222, v242, v40
v_and_b32 v223, v243, v41
v_mfma_f32_16x16x32_bf16 v[124:127], a[76:79], v[164:167], v[124:127]
v_mfma_f32_16x16x32_bf16 v[128:131], v[164:167], a[80:83], v[128:131]
s_barrier
v_mfma_f32_16x16x32_bf16 v[120:123], a[84:87], v[168:171], v[120:123]
global_load_lds_dwordx4 v12, s[42:43] offset:1024 sc1
ds_read_b32 v244, v22 offset:64
v_mfma_f32_16x16x32_bf16 v[124:127], a[88:91], v[168:171], v[124:127]
v_mfma_f32_16x16x32_bf16 v[216:219], v[152:155], v[220:223], 0
v_mfma_f32_16x16x32_bf16 v[212:215], v[156:159], v[220:223], 0
v_fma_f32 v237, v85, v8, v109
v_fma_f32 v238, v86, v8, v110
v_mfma_f32_16x16x32_bf16 v[128:131], v[168:171], a[92:95], v[128:131]
v_fma_f32 v239, v87, v8, v111
v_cvt_pk_bf16_f32 v136, v236, v237
v_mfma_f32_16x16x32_bf16 v[120:123], a[96:99], v[172:175], v[120:123]
v_cvt_pk_bf16_f32 v137, v238, v239
v_mfma_f32_16x16x32_bf16 v[124:127], a[100:103], v[172:175], v[124:127]
v_mfma_f32_16x16x32_bf16 v[128:131], v[172:175], a[104:107], v[128:131]
v_cvt_pk_bf16_f32 v46, v216, v217
)"
R"(v_cvt_pk_bf16_f32 v47, v218, v219
v_mfma_f32_16x16x32_bf16 v[120:123], a[108:111], v[176:179], v[120:123]
ds_write_b64 v16, v[46:47]
v_mfma_f32_16x16x32_bf16 v[124:127], a[112:115], v[176:179], v[124:127]
s_waitcnt lgkmcnt(1)
v_fma_f32 v228, v96, v244, v132
v_fma_f32 v229, v97, v244, v133
v_mfma_f32_16x16x32_bf16 v[128:131], v[176:179], a[116:119], v[128:131]
global_load_lds_dwordx4 v13, s[42:43] offset:2048 sc1
s_sub_u32 s42, s42, 18432
s_subb_u32 s43, s43, 0
v_mfma_f32_16x16x32_bf16 v[120:123], a[120:123], v[180:183], v[120:123]
v_fma_f32 v230, v98, v244, v134
v_fma_f32 v231, v99, v244, v135
v_mfma_f32_16x16x32_bf16 v[124:127], a[124:127], v[180:183], v[124:127]
v_cvt_pk_bf16_f32 v228, v228, v229
v_cvt_pk_bf16_f32 v229, v230, v231
v_mfma_f32_16x16x32_bf16 v[128:131], v[180:183], a[128:131], v[128:131]
ds_write_b64 v20, v[228:229] offset:40960
v_mfma_f32_16x16x32_bf16 v[120:123], a[132:135], v[184:187], v[120:123]
v_cvt_pk_bf16_f32 v242, v212, v213
v_mfma_f32_16x16x32_bf16 v[124:127], a[136:139], v[184:187], v[124:127]
ds_write_b32 v17, v242
v_mfma_f32_16x16x32_bf16 v[128:131], v[184:187], a[140:143], v[128:131]
v_mfma_f32_16x16x32_bf16 v[120:123], a[144:147], v[188:191], v[120:123]
v_mfma_f32_16x16x32_bf16 v[124:127], a[148:151], v[188:191], v[124:127]
v_mfma_f32_16x16x32_bf16 v[128:131], v[188:191], a[152:155], v[128:131]
v_mfma_f32_16x16x32_bf16 v[120:123], a[156:159], v[192:195], v[120:123]
v_mfma_f32_16x16x32_bf16 v[124:127], a[160:163], v[192:195], v[124:127]
v_xor_b32 v49, 0x10000, v49
v_xor_b32 v50, 0x10000, v50
v_mfma_f32_16x16x32_bf16 v[128:131], v[192:195], a[164:167], v[128:131]
v_mfma_f32_16x16x32_bf16 v[120:123], a[168:171], v[196:199], v[120:123]
v_mfma_f32_16x16x32_bf16 v[124:127], a[172:175], v[196:199], v[124:127]
s_waitcnt vmcnt(5) lgkmcnt(0)
s_barrier
.Lqa_in5:
v_mfma_f32_16x16x32_bf16 v[128:131], v[196:199], a[176:179], v[128:131]
s_cmp_eq_u32 s70, s73
s_cbranch_scc1 .Lqa_hk6
.Lqa_hr7:
ds_read_b128 v[42:45], v49 offset:0
v_mfma_f32_16x16x32_bf16 v[120:123], a[180:183], v[200:203], v[120:123]
ds_read_b128 v[100:103], v50 offset:0
ds_read_b128 v[228:231], v15 offset:0
v_mfma_f32_16x16x32_bf16 v[124:127], a[184:187], v[200:203], v[124:127]
ds_read_b32 v8, v22 offset:64
ds_read_b32 v150, v19
v_mfma_f32_16x16x32_bf16 v[216:219], v[144:147], v[136:139], v[34:37]
v_mfma_f32_16x16x32_bf16 v[128:131], v[200:203], a[188:191], v[128:131]
ds_read_b128 v[104:107], v49 offset:2048
ds_read_b32 v21, v22 offset:128
v_mfma_f32_16x16x32_bf16 v[120:123], a[192:195], v[204:207], v[120:123]
ds_read_b128 v[0:3], v50 offset:2048
ds_read_b32 v24, v27
v_mfma_f32_16x16x32_bf16 v[124:127], a[196:199], v[204:207], v[124:127]
ds_read_b32 v48, v28
ds_read_b128 v[224:227], v23 offset:64
v_mfma_f32_16x16x32_bf16 v[128:131], v[204:207], a[200:203], v[128:131]
ds_read_b128 v[4:7], v49 offset:4096
ds_read_b64 v[158:159], v20 offset:41472
v_mfma_f32_16x16x32_bf16 v[120:123], a[204:207], v[208:211], v[120:123]
ds_read_b128 v[160:163], v50 offset:4096
ds_read_b32 v142, v18
v_mfma_f32_16x16x32_bf16 v[124:127], a[208:211], v[208:211], v[124:127]
s_waitcnt lgkmcnt(12)
global_store_dwordx4 v14, v[228:231], s[38:39] sc1
v_mfma_f32_16x16x32_bf16 v[128:131], v[208:211], a[212:215], v[128:131]
s_sub_u32 s70, s70, 1
ds_read_b128 v[164:167], v49 offset:6144
v_add_u32 v14, 18432, v14
s_cbranch_scc0 .Lqa_top14
s_waitcnt lgkmcnt(0)
v_max_f32 v94, v216, v217
v_max_f32 v95, v218, v219
v_fma_f32 v236, v88, v8, v124
v_fma_f32 v237, v89, v8, v125
v_fma_f32 v238, v90, v8, v126
v_fma_f32 v239, v91, v8, v127
v_cvt_pk_bf16_f32 v148, v236, v237
v_cvt_pk_bf16_f32 v149, v238, v239
s_nop 1
v_mfma_f32_16x16x32_bf16 v[212:215], v[148:151], v[136:139], v[30:33]
v_mul_f32 v21, 0x3eae1a93, v21
v_mul_f32 v51, v24, v21
v_mul_f32 v93, v48, v21
v_fma_f32 v236, v92, v224, v128
v_fma_f32 v237, v92, v225, v129
v_fma_f32 v238, v92, v226, v130
s_nop 1
v_max3_f32 v94, v212, v213, v94
v_max3_f32 v95, v214, v215, v95
v_mul_f32 v94, v94, v51
v_mul_f32 v95, v95, v93
v_max_f32 v240, v94, v95
v_mov_b32 v241, v240
s_nop 1
v_permlane16_swap_b32 v240, v241
v_max_f32 v240, v240, v241
v_mov_b32 v241, v240
s_nop 1
v_permlane32_swap_b32 v240, v241
v_max_f32 v240, v240, v241
v_sub_f32 v94, v94, v240
v_sub_f32 v95, v95, v240
v_fma_f32 v239, v92, v227, v131
v_cvt_pk_bf16_f32 v154, v236, v237
v_exp_f32 v242, v94
v_exp_f32 v243, v95
s_nop 0
v_add_f32 v240, v242, v243
v_mov_b32 v241, v240
v_mul_f32 v242, v242, v24
v_cvt_pk_bf16_f32 v155, v238, v239
v_permlane16_swap_b32 v240, v241
v_add_f32 v240, v240, v241
v_mov_b32 v241, v240
v_mul_f32 v243, v243, v48
v_fma_f32 v236, v84, v8, v120
v_fma_f32 v237, v85, v8, v121
v_permlane32_swap_b32 v240, v241
v_add_f32 v240, v240, v241
v_fma_f32 v238, v86, v8, v122
v_rcp_f32 v240, v240
s_nop 0
v_mul_f32 v242, v242, v240
v_mul_f32 v243, v243, v240
v_cvt_pk_bf16_f32 v242, v242, v242
v_cvt_pk_bf16_f32 v243, v243, v243
v_and_b32 v222, v242, v38
v_and_b32 v223, v243, v39
v_and_b32 v220, v242, v40
v_and_b32 v221, v243, v41
s_nop 1
v_mfma_f32_16x16x32_bf16 v[216:219], v[152:155], v[220:223], 0
v_mfma_f32_16x16x32_bf16 v[212:215], v[156:159], v[220:223], 0
v_fma_f32 v239, v87, v8, v123
v_cvt_pk_bf16_f32 v140, v236, v237
v_cvt_pk_bf16_f32 v141, v238, v239
s_nop 3
v_cvt_pk_bf16_f32 v46, v216, v217
v_cvt_pk_bf16_f32 v47, v218, v219
ds_write_b64 v16, v[46:47]
v_cvt_pk_bf16_f32 v242, v212, v213
ds_write_b32 v17, v242
s_waitcnt vmcnt(0)
s_waitcnt lgkmcnt(0)
s_barrier
ds_read_b128 v[228:231], v15 offset:0
s_waitcnt lgkmcnt(0)
global_store_dwordx4 v14, v[228:231], s[38:39] sc1
s_waitcnt vmcnt(0)
s_barrier
s_endpgm
.Lqa_w2:
s_nop 7
v_mov_b32 v232, v30
v_mov_b32 v233, v31
v_mov_b32 v234, v32
v_mov_b32 v235, v33
s_mov_b32 s70, 33
s_waitcnt vmcnt(0) lgkmcnt(0)
s_nop 7
s_branch .Lqa_in15
.p2align 6
.Lqa_top24:
s_waitcnt lgkmcnt(15)
v_mfma_f32_16x16x32_bf16 v[132:135], v[42:45], a[216:219], 0
s_waitcnt lgkmcnt(9)
ds_read_b128 v[168:171], v50 offset:6144
v_mul_f32 v21, 0x3eae1a93, v21
v_mfma_f32_16x16x32_bf16 v[132:135], v[94:97], a[220:223], v[132:135]
global_store_dwordx4 v14, v[228:231], s[38:39] sc1
v_mfma_f32_16x16x32_bf16 v[108:111], a[0:3], v[42:45], 0
ds_read_b128 v[172:175], v49 offset:8192
v_add_u32 v14, 18432, v14
v_mfma_f32_16x16x32_bf16 v[132:135], v[102:105], a[224:227], v[132:135]
s_waitcnt lgkmcnt(10)
ds_read_b128 v[176:179], v50 offset:8192
v_fma_f32 v236, v88, v8, v124
v_mfma_f32_16x16x32_bf16 v[132:135], v[0:3], a[228:231], v[132:135]
v_fma_f32 v237, v89, v8, v125
v_fma_f32 v238, v90, v8, v126
v_mfma_f32_16x16x32_bf16 v[112:115], a[4:7], v[42:45], 0
s_waitcnt lgkmcnt(7)
ds_read_b128 v[180:183], v49 offset:10240
v_fma_f32 v239, v91, v8, v127
v_mfma_f32_16x16x32_bf16 v[132:135], v[4:7], a[232:235], v[132:135]
s_waitcnt lgkmcnt(6)
ds_read_b128 v[184:187], v50 offset:10240
v_cvt_pk_bf16_f32 v148, v236, v237
v_mfma_f32_16x16x32_bf16 v[132:135], v[160:163], a[236:239], v[132:135]
v_cvt_pk_bf16_f32 v149, v238, v239
v_mul_f32 v93, v24, v21
v_mfma_f32_16x16x32_bf16 v[116:119], v[42:45], a[8:11], 0
ds_read_b128 v[188:191], v49 offset:12288
v_mfma_f32_16x16x32_bf16 v[212:215], v[148:151], v[136:139], v[30:33]
s_waitcnt lgkmcnt(6)
v_mul_f32 v101, v51, v21
v_fma_f32 v236, v92, v224, v128
v_mfma_f32_16x16x32_bf16 v[132:135], v[164:167], a[240:243], v[132:135]
s_waitcnt lgkmcnt(5)
ds_read_b128 v[192:195], v50 offset:12288
v_fma_f32 v237, v92, v225, v129
v_mfma_f32_16x16x32_bf16 v[132:135], v[168:171], a[244:247], v[132:135]
v_fma_f32 v238, v92, v226, v130
v_fma_f32 v239, v92, v227, v131
v_mfma_f32_16x16x32_bf16 v[108:111], a[12:15], v[94:97], v[108:111]
s_waitcnt lgkmcnt(5)
ds_read_b128 v[196:199], v49 offset:14336
v_cvt_pk_bf16_f32 v154, v236, v237
v_mfma_f32_16x16x32_bf16 v[132:135], v[172:175], a[248:251], v[132:135]
s_xor_b32 s49, s49, 0x10000
ds_read_b128 v[200:203], v50 offset:14336
s_waitcnt lgkmcnt(6)
v_mfma_f32_16x16x32_bf16 v[132:135], v[176:179], a[252:255], v[132:135]
s_add_u32 m0, s49, 0
v_max3_f32 v106, v212, v213, v106
v_max3_f32 v107, v214, v215, v107
v_mfma_f32_16x16x32_bf16 v[112:115], a[16:19], v[94:97], v[112:115]
s_waitcnt lgkmcnt(5)
ds_read_b128 v[204:207], v49 offset:16384
v_mul_f32 v106, v106, v93
v_mfma_f32_16x16x32_bf16 v[132:135], v[180:183], v[52:55], v[132:135]
s_waitcnt lgkmcnt(5)
ds_read_b128 v[208:211], v50 offset:16384
v_mul_f32 v107, v107, v101
v_mfma_f32_16x16x32_bf16 v[132:135], v[184:187], v[56:59], v[132:135]
global_load_lds_dwordx4 v9, s[42:43] offset:-2048 sc1
v_max_f32 v46, v106, v107
v_mfma_f32_16x16x32_bf16 v[116:119], v[94:97], a[20:23], v[116:119]
s_waitcnt lgkmcnt(5)
v_mov_b32 v47, v46
v_cvt_pk_bf16_f32 v155, v238, v239
v_mfma_f32_16x16x32_bf16 v[132:135], v[188:191], v[60:63], v[132:135]
s_waitcnt lgkmcnt(4)
v_permlane16_swap_b32 v46, v47
v_mfma_f32_16x16x32_bf16 v[132:135], v[192:195], v[64:67], v[132:135]
v_max_f32 v46, v46, v47
v_mov_b32 v47, v46
v_mfma_f32_16x16x32_bf16 v[108:111], a[24:27], v[102:105], v[108:111]
s_waitcnt lgkmcnt(3)
v_permlane32_swap_b32 v46, v47
v_mfma_f32_16x16x32_bf16 v[132:135], v[196:199], v[68:71], v[132:135]
s_waitcnt lgkmcnt(2)
v_max_f32 v46, v46, v47
v_sub_f32 v106, v106, v46
v_mfma_f32_16x16x32_bf16 v[132:135], v[200:203], v[72:75], v[132:135]
v_sub_f32 v107, v107, v46
v_fma_f32 v236, v84, v8, v120
v_mfma_f32_16x16x32_bf16 v[112:115], a[28:31], v[102:105], v[112:115]
s_waitcnt lgkmcnt(1)
v_exp_f32 v48, v106
v_mfma_f32_16x16x32_bf16 v[132:135], v[204:207], v[76:79], v[132:135]
s_waitcnt lgkmcnt(0)
global_load_lds_dwordx4 v10, s[42:43] offset:-1024 sc1
v_fma_f32 v237, v85, v8, v121
v_mfma_f32_16x16x32_bf16 v[132:135], v[208:211], v[80:83], v[132:135]
v_exp_f32 v240, v107
v_mfma_f32_16x16x32_bf16 v[116:119], v[102:105], a[32:35], v[116:119]
v_add_f32 v46, v48, v240
v_mov_b32 v47, v46
v_mfma_f32_16x16x32_bf16 v[108:111], a[36:39], v[0:3], v[108:111]
v_mul_f32 v48, v48, v24
v_fma_f32 v238, v86, v8, v122
v_mfma_f32_16x16x32_bf16 v[112:115], a[40:43], v[0:3], v[112:115]
v_permlane16_swap_b32 v46, v47
v_mfma_f32_16x16x32_bf16 v[116:119], v[0:3], a[44:47], v[116:119]
v_add_f32 v46, v46, v47
v_mov_b32 v47, v46
v_mfma_f32_16x16x32_bf16 v[108:111], a[48:51], v[4:7], v[108:111]
v_mul_f32 v240, v240, v51
v_fma_f32 v239, v87, v8, v123
v_mfma_f32_16x16x32_bf16 v[112:115], a[52:55], v[4:7], v[112:115]
v_permlane32_swap_b32 v46, v47
v_mfma_f32_16x16x32_bf16 v[116:119], v[4:7], a[56:59], v[116:119]
global_load_lds_dwordx4 v11, s[42:43] offset:0 sc1
v_add_f32 v46, v46, v47
v_mfma_f32_16x16x32_bf16 v[108:111], a[60:63], v[160:163], v[108:111]
v_rcp_f32 v46, v46
v_mfma_f32_16x16x32_bf16 v[112:115], a[64:67], v[160:163], v[112:115]
v_mul_f32 v48, v48, v46
v_mul_f32 v240, v240, v46
v_mfma_f32_16x16x32_bf16 v[116:119], v[160:163], a[68:71], v[116:119]
v_cvt_pk_bf16_f32 v48, v48, v48
v_cvt_pk_bf16_f32 v240, v240, v240
v_mfma_f32_16x16x32_bf16 v[108:111], a[72:75], v[164:167], v[108:111]
v_and_b32 v222, v48, v38
v_and_b32 v223, v240, v39
v_mfma_f32_16x16x32_bf16 v[112:115], a[76:79], v[164:167], v[112:115]
v_mfma_f32_16x16x32_bf16 v[116:119], v[164:167], a[80:83], v[116:119]
s_barrier
v_mfma_f32_16x16x32_bf16 v[108:111], a[84:87], v[168:171], v[108:111]
ds_read_b128 v[242:245], v23 offset:0
v_and_b32 v220, v48, v40
v_mfma_f32_16x16x32_bf16 v[112:115], a[88:91], v[168:171], v[112:115]
global_load_lds_dwordx4 v12, s[42:43] offset:1024 sc1
v_and_b32 v221, v240, v41
v_mfma_f32_16x16x32_bf16 v[116:119], v[168:171], a[92:95], v[116:119]
s_nop 0
v_mfma_f32_16x16x32_bf16 v[216:219], v[152:155], v[220:223], 0
v_mfma_f32_16x16x32_bf16 v[212:215], v[156:159], v[220:223], 0
v_cvt_pk_bf16_f32 v140, v236, v237
v_cvt_pk_bf16_f32 v141, v238, v239
v_mfma_f32_16x16x32_bf16 v[108:111], a[96:99], v[172:175], v[108:111]
v_mfma_f32_16x16x32_bf16 v[112:115], a[100:103], v[172:175], v[112:115]
v_mfma_f32_16x16x32_bf16 v[116:119], v[172:175], a[104:107], v[116:119]
v_mfma_f32_16x16x32_bf16 v[108:111], a[108:111], v[176:179], v[108:111]
s_nop 0
v_cvt_pk_bf16_f32 v98, v216, v217
v_cvt_pk_bf16_f32 v99, v218, v219
v_mfma_f32_16x16x32_bf16 v[112:115], a[112:115], v[176:179], v[112:115]
s_waitcnt lgkmcnt(0)
v_fma_f32 v228, v100, v242, v132
v_fma_f32 v229, v100, v243, v133
v_mfma_f32_16x16x32_bf16 v[116:119], v[176:179], a[116:119], v[116:119]
v_fma_f32 v230, v100, v244, v134
v_fma_f32 v231, v100, v245, v135
v_mfma_f32_16x16x32_bf16 v[108:111], a[120:123], v[180:183], v[108:111]
global_load_lds_dwordx4 v13, s[42:43] offset:2048 sc1
s_add_u32 s42, s42, s46
s_addc_u32 s43, s43, s47
v_mfma_f32_16x16x32_bf16 v[112:115], a[124:127], v[180:183], v[112:115]
v_cvt_pk_bf16_f32 v228, v228, v229
v_cvt_pk_bf16_f32 v229, v230, v231
v_mfma_f32_16x16x32_bf16 v[116:119], v[180:183], a[128:131], v[116:119]
ds_write_b64 v20, v[228:229] offset:41472
v_mfma_f32_16x16x32_bf16 v[108:111], a[132:135], v[184:187], v[108:111]
ds_write_b64 v16, v[98:99]
v_mfma_f32_16x16x32_bf16 v[112:115], a[136:139], v[184:187], v[112:115]
v_cvt_pk_bf16_f32 v48, v214, v215
v_mfma_f32_16x16x32_bf16 v[116:119], v[184:187], a[140:143], v[116:119]
ds_write_b32 v17, v48
v_mfma_f32_16x16x32_bf16 v[108:111], a[144:147], v[188:191], v[108:111]
v_mfma_f32_16x16x32_bf16 v[112:115], a[148:151], v[188:191], v[112:115]
v_mfma_f32_16x16x32_bf16 v[116:119], v[188:191], a[152:155], v[116:119]
v_mfma_f32_16x16x32_bf16 v[108:111], a[156:159], v[192:195], v[108:111]
v_mfma_f32_16x16x32_bf16 v[112:115], a[160:163], v[192:195], v[112:115]
v_mfma_f32_16x16x32_bf16 v[116:119], v[192:195], a[164:167], v[116:119]
v_mfma_f32_16x16x32_bf16 v[108:111], a[168:171], v[196:199], v[108:111]
v_mfma_f32_16x16x32_bf16 v[112:115], a[172:175], v[196:199], v[112:115]
s_waitcnt vmcnt(5) lgkmcnt(0)
s_barrier
v_mfma_f32_16x16x32_bf16 v[116:119], v[196:199], a[176:179], v[116:119]
ds_read_b128 v[42:45], v49 offset:18432
v_mfma_f32_16x16x32_bf16 v[108:111], a[180:183], v[200:203], v[108:111]
ds_read_b128 v[94:97], v50 offset:18432
ds_read_b128 v[228:231], v15 offset:0
v_mfma_f32_16x16x32_bf16 v[112:115], a[184:187], v[200:203], v[112:115]
ds_read_b32 v8, v22 offset:0
ds_read_b32 v146, v19
v_mfma_f32_16x16x32_bf16 v[216:219], v[148:151], v[140:143], v[34:37]
v_mfma_f32_16x16x32_bf16 v[116:119], v[200:203], a[188:191], v[116:119]
ds_read_b128 v[102:105], v49 offset:20480
ds_read_b32 v21, v22 offset:192
v_mfma_f32_16x16x32_bf16 v[108:111], a[192:195], v[204:207], v[108:111]
ds_read_b128 v[0:3], v50 offset:20480
ds_read_b32 v24, v25
v_mfma_f32_16x16x32_bf16 v[112:115], a[196:199], v[204:207], v[112:115]
ds_read_b32 v51, v26
ds_read_b128 v[224:227], v23 offset:0
v_mfma_f32_16x16x32_bf16 v[116:119], v[204:207], a[200:203], v[116:119]
ds_read_b128 v[4:7], v49 offset:22528
ds_read_b64 v[156:157], v20 offset:41472
v_mfma_f32_16x16x32_bf16 v[108:111], a[204:207], v[208:211], v[108:111]
ds_read_b128 v[160:163], v50 offset:22528
ds_read_b32 v138, v18
v_mfma_f32_16x16x32_bf16 v[112:115], a[208:211], v[208:211], v[112:115]
v_max_f32 v106, v216, v217
v_max_f32 v107, v218, v219
v_mfma_f32_16x16x32_bf16 v[116:119], v[208:211], a[212:215], v[116:119]
s_waitcnt lgkmcnt(14)
ds_read_b128 v[164:167], v49 offset:24576
v_mfma_f32_16x16x32_bf16 v[132:135], v[42:45], a[216:219], 0
s_waitcnt lgkmcnt(9)
ds_read_b128 v[168:171], v50 offset:24576
v_mul_f32 v21, 0x3eae1a93, v21
v_mfma_f32_16x16x32_bf16 v[132:135], v[94:97], a[220:223], v[132:135]
global_store_dwordx4 v14, v[228:231], s[38:39] sc1
v_mfma_f32_16x16x32_bf16 v[120:123], a[0:3], v[42:45], 0
ds_read_b128 v[172:175], v49 offset:26624
v_add_u32 v14, 18432, v14
v_mfma_f32_16x16x32_bf16 v[132:135], v[102:105], a[224:227], v[132:135]
s_waitcnt lgkmcnt(10)
ds_read_b128 v[176:179], v50 offset:26624
v_fma_f32 v236, v88, v8, v112
v_mfma_f32_16x16x32_bf16 v[132:135], v[0:3], a[228:231], v[132:135]
v_fma_f32 v237, v89, v8, v113
v_fma_f32 v238, v90, v8, v114
v_mfma_f32_16x16x32_bf16 v[124:127], a[4:7], v[42:45], 0
s_waitcnt lgkmcnt(7)
ds_read_b128 v[180:183], v49 offset:28672
v_fma_f32 v239, v91, v8, v115
v_mfma_f32_16x16x32_bf16 v[132:135], v[4:7], a[232:235], v[132:135]
s_waitcnt lgkmcnt(6)
ds_read_b128 v[184:187], v50 offset:28672
v_cvt_pk_bf16_f32 v144, v236, v237
v_mfma_f32_16x16x32_bf16 v[132:135], v[160:163], a[236:239], v[132:135]
v_cvt_pk_bf16_f32 v145, v238, v239
v_mul_f32 v93, v24, v21
)"
R"(v_mfma_f32_16x16x32_bf16 v[128:131], v[42:45], a[8:11], 0
ds_read_b128 v[188:191], v49 offset:30720
v_mfma_f32_16x16x32_bf16 v[212:215], v[144:147], v[140:143], v[232:235]
s_waitcnt lgkmcnt(6)
v_mul_f32 v101, v51, v21
v_fma_f32 v236, v92, v224, v116
v_mfma_f32_16x16x32_bf16 v[132:135], v[164:167], a[240:243], v[132:135]
s_waitcnt lgkmcnt(5)
ds_read_b128 v[192:195], v50 offset:30720
v_fma_f32 v237, v92, v225, v117
v_mfma_f32_16x16x32_bf16 v[132:135], v[168:171], a[244:247], v[132:135]
v_fma_f32 v238, v92, v226, v118
v_fma_f32 v239, v92, v227, v119
v_mfma_f32_16x16x32_bf16 v[120:123], a[12:15], v[94:97], v[120:123]
s_waitcnt lgkmcnt(5)
ds_read_b128 v[196:199], v49 offset:32768
v_cvt_pk_bf16_f32 v152, v236, v237
v_mfma_f32_16x16x32_bf16 v[132:135], v[172:175], a[248:251], v[132:135]
s_add_u32 m0, s49, 18432
ds_read_b128 v[200:203], v50 offset:32768
s_waitcnt lgkmcnt(6)
v_mfma_f32_16x16x32_bf16 v[132:135], v[176:179], a[252:255], v[132:135]
v_max3_f32 v106, v212, v213, v106
v_max3_f32 v107, v214, v215, v107
v_mfma_f32_16x16x32_bf16 v[124:127], a[16:19], v[94:97], v[124:127]
s_waitcnt lgkmcnt(5)
ds_read_b128 v[204:207], v49 offset:34816
v_mul_f32 v106, v106, v93
v_mfma_f32_16x16x32_bf16 v[132:135], v[180:183], v[52:55], v[132:135]
s_waitcnt lgkmcnt(5)
ds_read_b128 v[208:211], v50 offset:34816
global_load_lds_dwordx4 v9, s[42:43] offset:-2048 sc1
v_mfma_f32_16x16x32_bf16 v[132:135], v[184:187], v[56:59], v[132:135]
v_mul_f32 v107, v107, v101
v_max_f32 v46, v106, v107
v_mfma_f32_16x16x32_bf16 v[128:131], v[94:97], a[20:23], v[128:131]
s_waitcnt lgkmcnt(5)
v_mov_b32 v47, v46
v_cvt_pk_bf16_f32 v153, v238, v239
v_mfma_f32_16x16x32_bf16 v[132:135], v[188:191], v[60:63], v[132:135]
s_waitcnt lgkmcnt(4)
v_permlane16_swap_b32 v46, v47
v_mfma_f32_16x16x32_bf16 v[132:135], v[192:195], v[64:67], v[132:135]
v_max_f32 v46, v46, v47
v_mov_b32 v47, v46
v_mfma_f32_16x16x32_bf16 v[120:123], a[24:27], v[102:105], v[120:123]
s_waitcnt lgkmcnt(3)
v_permlane32_swap_b32 v46, v47
v_mfma_f32_16x16x32_bf16 v[132:135], v[196:199], v[68:71], v[132:135]
s_waitcnt lgkmcnt(2)
v_max_f32 v46, v46, v47
v_sub_f32 v106, v106, v46
v_mfma_f32_16x16x32_bf16 v[132:135], v[200:203], v[72:75], v[132:135]
v_sub_f32 v107, v107, v46
v_fma_f32 v236, v84, v8, v108
v_mfma_f32_16x16x32_bf16 v[124:127], a[28:31], v[102:105], v[124:127]
s_waitcnt lgkmcnt(1)
global_load_lds_dwordx4 v10, s[42:43] offset:-1024 sc1
v_fma_f32 v237, v85, v8, v109
v_mfma_f32_16x16x32_bf16 v[132:135], v[204:207], v[76:79], v[132:135]
s_waitcnt lgkmcnt(0)
v_exp_f32 v48, v106
v_mfma_f32_16x16x32_bf16 v[132:135], v[208:211], v[80:83], v[132:135]
v_exp_f32 v240, v107
v_mfma_f32_16x16x32_bf16 v[128:131], v[102:105], a[32:35], v[128:131]
v_add_f32 v46, v48, v240
v_mov_b32 v47, v46
v_mfma_f32_16x16x32_bf16 v[120:123], a[36:39], v[0:3], v[120:123]
v_mul_f32 v48, v48, v24
v_fma_f32 v238, v86, v8, v110
v_mfma_f32_16x16x32_bf16 v[124:127], a[40:43], v[0:3], v[124:127]
v_permlane16_swap_b32 v46, v47
v_mfma_f32_16x16x32_bf16 v[128:131], v[0:3], a[44:47], v[128:131]
v_add_f32 v46, v46, v47
v_mov_b32 v47, v46
v_mfma_f32_16x16x32_bf16 v[120:123], a[48:51], v[4:7], v[120:123]
v_mul_f32 v240, v240, v51
v_fma_f32 v239, v87, v8, v111
v_mfma_f32_16x16x32_bf16 v[124:127], a[52:55], v[4:7], v[124:127]
global_load_lds_dwordx4 v11, s[42:43] offset:0 sc1
v_cvt_pk_bf16_f32 v136, v236, v237
v_mfma_f32_16x16x32_bf16 v[128:131], v[4:7], a[56:59], v[128:131]
v_permlane32_swap_b32 v46, v47
v_mfma_f32_16x16x32_bf16 v[120:123], a[60:63], v[160:163], v[120:123]
v_add_f32 v46, v46, v47
v_cvt_pk_bf16_f32 v137, v238, v239
v_mfma_f32_16x16x32_bf16 v[124:127], a[64:67], v[160:163], v[124:127]
v_rcp_f32 v46, v46
v_mfma_f32_16x16x32_bf16 v[128:131], v[160:163], a[68:71], v[128:131]
v_mul_f32 v48, v48, v46
v_mul_f32 v240, v240, v46
v_mfma_f32_16x16x32_bf16 v[120:123], a[72:75], v[164:167], v[120:123]
v_cvt_pk_bf16_f32 v48, v48, v48
v_cvt_pk_bf16_f32 v240, v240, v240
v_mfma_f32_16x16x32_bf16 v[124:127], a[76:79], v[164:167], v[124:127]
v_mfma_f32_16x16x32_bf16 v[128:131], v[164:167], a[80:83], v[128:131]
s_barrier
v_mfma_f32_16x16x32_bf16 v[120:123], a[84:87], v[168:171], v[120:123]
global_load_lds_dwordx4 v12, s[42:43] offset:1024 sc1
ds_read_b128 v[242:245], v23 offset:64
v_mfma_f32_16x16x32_bf16 v[124:127], a[88:91], v[168:171], v[124:127]
v_and_b32 v220, v48, v38
v_and_b32 v221, v240, v39
v_mfma_f32_16x16x32_bf16 v[128:131], v[168:171], a[92:95], v[128:131]
v_and_b32 v222, v48, v40
v_and_b32 v223, v240, v41
v_mfma_f32_16x16x32_bf16 v[120:123], a[96:99], v[172:175], v[120:123]
s_nop 0
v_mfma_f32_16x16x32_bf16 v[216:219], v[152:155], v[220:223], 0
v_mfma_f32_16x16x32_bf16 v[212:215], v[156:159], v[220:223], 0
v_mfma_f32_16x16x32_bf16 v[124:127], a[100:103], v[172:175], v[124:127]
v_mfma_f32_16x16x32_bf16 v[128:131], v[172:175], a[104:107], v[128:131]
v_mfma_f32_16x16x32_bf16 v[120:123], a[108:111], v[176:179], v[120:123]
v_mfma_f32_16x16x32_bf16 v[124:127], a[112:115], v[176:179], v[124:127]
s_waitcnt lgkmcnt(0)
v_fma_f32 v228, v100, v242, v132
v_fma_f32 v229, v100, v243, v133
v_mfma_f32_16x16x32_bf16 v[128:131], v[176:179], a[116:119], v[128:131]
global_load_lds_dwordx4 v13, s[42:43] offset:2048 sc1
s_sub_u32 s42, s42, 18432
s_subb_u32 s43, s43, 0
v_mfma_f32_16x16x32_bf16 v[120:123], a[120:123], v[180:183], v[120:123]
v_fma_f32 v230, v100, v244, v134
v_fma_f32 v231, v100, v245, v135
v_mfma_f32_16x16x32_bf16 v[124:127], a[124:127], v[180:183], v[124:127]
v_cvt_pk_bf16_f32 v228, v228, v229
v_cvt_pk_bf16_f32 v229, v230, v231
v_mfma_f32_16x16x32_bf16 v[128:131], v[180:183], a[128:131], v[128:131]
ds_write_b64 v20, v[228:229] offset:41472
v_mfma_f32_16x16x32_bf16 v[120:123], a[132:135], v[184:187], v[120:123]
v_cvt_pk_bf16_f32 v98, v216, v217
v_cvt_pk_bf16_f32 v99, v218, v219
v_mfma_f32_16x16x32_bf16 v[124:127], a[136:139], v[184:187], v[124:127]
ds_write_b64 v16, v[98:99]
v_mfma_f32_16x16x32_bf16 v[128:131], v[184:187], a[140:143], v[128:131]
v_cvt_pk_bf16_f32 v48, v214, v215
v_mfma_f32_16x16x32_bf16 v[120:123], a[144:147], v[188:191], v[120:123]
ds_write_b32 v17, v48
v_mfma_f32_16x16x32_bf16 v[124:127], a[148:151], v[188:191], v[124:127]
v_mfma_f32_16x16x32_bf16 v[128:131], v[188:191], a[152:155], v[128:131]
v_mfma_f32_16x16x32_bf16 v[120:123], a[156:159], v[192:195], v[120:123]
v_mfma_f32_16x16x32_bf16 v[124:127], a[160:163], v[192:195], v[124:127]
v_xor_b32 v49, 0x10000, v49
v_xor_b32 v50, 0x10000, v50
v_mfma_f32_16x16x32_bf16 v[128:131], v[192:195], a[164:167], v[128:131]
v_mfma_f32_16x16x32_bf16 v[120:123], a[168:171], v[196:199], v[120:123]
v_mfma_f32_16x16x32_bf16 v[124:127], a[172:175], v[196:199], v[124:127]
s_waitcnt vmcnt(5) lgkmcnt(0)
s_barrier
.Lqa_in15:
v_mfma_f32_16x16x32_bf16 v[128:131], v[196:199], a[176:179], v[128:131]
s_cmp_eq_u32 s70, s73
s_cbranch_scc1 .Lqa_hk16
.Lqa_hr17:
ds_read_b128 v[42:45], v49 offset:0
v_mfma_f32_16x16x32_bf16 v[120:123], a[180:183], v[200:203], v[120:123]
ds_read_b128 v[94:97], v50 offset:0
ds_read_b128 v[228:231], v15 offset:0
v_mfma_f32_16x16x32_bf16 v[124:127], a[184:187], v[200:203], v[124:127]
ds_read_b32 v8, v22 offset:64
ds_read_b32 v150, v19
v_mfma_f32_16x16x32_bf16 v[216:219], v[144:147], v[136:139], v[34:37]
v_mfma_f32_16x16x32_bf16 v[128:131], v[200:203], a[188:191], v[128:131]
ds_read_b128 v[102:105], v49 offset:2048
ds_read_b32 v21, v22 offset:128
v_mfma_f32_16x16x32_bf16 v[120:123], a[192:195], v[204:207], v[120:123]
ds_read_b128 v[0:3], v50 offset:2048
ds_read_b32 v24, v27
v_mfma_f32_16x16x32_bf16 v[124:127], a[196:199], v[204:207], v[124:127]
ds_read_b32 v51, v28
ds_read_b128 v[224:227], v23 offset:64
v_mfma_f32_16x16x32_bf16 v[128:131], v[204:207], a[200:203], v[128:131]
ds_read_b128 v[4:7], v49 offset:4096
ds_read_b64 v[158:159], v20 offset:41472
v_mfma_f32_16x16x32_bf16 v[120:123], a[204:207], v[208:211], v[120:123]
ds_read_b128 v[160:163], v50 offset:4096
ds_read_b32 v142, v18
v_mfma_f32_16x16x32_bf16 v[124:127], a[208:211], v[208:211], v[124:127]
v_max_f32 v106, v216, v217
v_max_f32 v107, v218, v219
v_mfma_f32_16x16x32_bf16 v[128:131], v[208:211], a[212:215], v[128:131]
s_sub_u32 s70, s70, 1
ds_read_b128 v[164:167], v49 offset:6144
s_cbranch_scc0 .Lqa_top24
s_waitcnt lgkmcnt(0)
v_mul_f32 v21, 0x3eae1a93, v21
global_store_dwordx4 v14, v[228:231], s[38:39] sc1
v_add_u32 v14, 18432, v14
v_fma_f32 v236, v88, v8, v124
v_fma_f32 v237, v89, v8, v125
v_fma_f32 v238, v90, v8, v126
v_fma_f32 v239, v91, v8, v127
v_cvt_pk_bf16_f32 v148, v236, v237
v_cvt_pk_bf16_f32 v149, v238, v239
v_mul_f32 v93, v24, v21
s_nop 0
v_mfma_f32_16x16x32_bf16 v[212:215], v[148:151], v[136:139], v[30:33]
v_mul_f32 v101, v51, v21
v_fma_f32 v236, v92, v224, v128
v_fma_f32 v237, v92, v225, v129
v_fma_f32 v238, v92, v226, v130
v_fma_f32 v239, v92, v227, v131
v_cvt_pk_bf16_f32 v154, v236, v237
s_nop 1
v_max3_f32 v106, v212, v213, v106
v_max3_f32 v107, v214, v215, v107
v_mul_f32 v106, v106, v93
v_mul_f32 v107, v107, v101
v_max_f32 v46, v106, v107
v_mov_b32 v47, v46
v_cvt_pk_bf16_f32 v155, v238, v239
s_nop 0
v_permlane16_swap_b32 v46, v47
v_max_f32 v46, v46, v47
v_mov_b32 v47, v46
s_nop 1
v_permlane32_swap_b32 v46, v47
v_max_f32 v46, v46, v47
v_sub_f32 v106, v106, v46
v_sub_f32 v107, v107, v46
v_fma_f32 v236, v84, v8, v120
v_exp_f32 v48, v106
v_fma_f32 v237, v85, v8, v121
v_exp_f32 v240, v107
s_nop 0
v_add_f32 v46, v48, v240
v_mov_b32 v47, v46
v_mul_f32 v48, v48, v24
v_fma_f32 v238, v86, v8, v122
v_permlane16_swap_b32 v46, v47
v_add_f32 v46, v46, v47
v_mov_b32 v47, v46
v_mul_f32 v240, v240, v51
v_fma_f32 v239, v87, v8, v123
v_permlane32_swap_b32 v46, v47
v_add_f32 v46, v46, v47
v_rcp_f32 v46, v46
s_nop 0
v_mul_f32 v48, v48, v46
v_mul_f32 v240, v240, v46
v_cvt_pk_bf16_f32 v48, v48, v48
v_cvt_pk_bf16_f32 v240, v240, v240
v_and_b32 v222, v48, v38
v_and_b32 v223, v240, v39
v_and_b32 v220, v48, v40
v_and_b32 v221, v240, v41
s_nop 1
v_mfma_f32_16x16x32_bf16 v[216:219], v[152:155], v[220:223], 0
v_mfma_f32_16x16x32_bf16 v[212:215], v[156:159], v[220:223], 0
v_cvt_pk_bf16_f32 v140, v236, v237
v_cvt_pk_bf16_f32 v141, v238, v239
s_nop 4
v_cvt_pk_bf16_f32 v98, v216, v217
v_cvt_pk_bf16_f32 v99, v218, v219
ds_write_b64 v16, v[98:99]
v_cvt_pk_bf16_f32 v48, v214, v215
ds_write_b32 v17, v48
s_waitcnt vmcnt(0)
s_waitcnt lgkmcnt(0)
s_barrier
ds_read_b128 v[228:231], v15 offset:0
s_waitcnt lgkmcnt(0)
global_store_dwordx4 v14, v[228:231], s[38:39] sc1
s_waitcnt vmcnt(0)
s_barrier
s_endpgm
.Lqa_w3:
s_nop 7
v_mov_b32 v200, v30
v_mov_b32 v201, v31
v_mov_b32 v202, v32
v_mov_b32 v203, v33
s_mov_b32 s70, 33
s_waitcnt vmcnt(0) lgkmcnt(0)
s_nop 7
s_branch .Lqa_in25
.p2align 6
.Lqa_top38:
s_waitcnt lgkmcnt(15)
v_mfma_f32_16x16x32_bf16 v[132:135], v[0:3], v[0:3], 0
s_waitcnt lgkmcnt(9)
ds_read_b128 v[60:63], v50 offset:6144
v_mul_f32 v24, 0x3eae1a93, v24
v_mfma_f32_16x16x32_bf16 v[132:135], v[4:7], v[4:7], v[132:135]
global_store_dwordx4 v14, v[196:199], s[38:39] sc1
v_mfma_f32_16x16x32_bf16 v[108:111], a[0:3], v[0:3], 0
ds_read_b128 v[64:67], v49 offset:8192
v_add_u32 v14, 18432, v14
v_mfma_f32_16x16x32_bf16 v[132:135], v[94:97], v[94:97], v[132:135]
s_waitcnt lgkmcnt(10)
ds_read_b128 v[68:71], v50 offset:8192
v_add_u32 v44, 1, v44
v_mfma_f32_16x16x32_bf16 v[132:135], v[98:101], v[98:101], v[132:135]
global_load_dword v51, v42, s[38:39] sc1
v_fma_f32 v204, v88, v8, v124
v_mfma_f32_16x16x32_bf16 v[112:115], a[4:7], v[0:3], 0
s_waitcnt lgkmcnt(7)
ds_read_b128 v[72:75], v49 offset:10240
v_fma_f32 v205, v89, v8, v125
v_mfma_f32_16x16x32_bf16 v[132:135], v[102:105], v[102:105], v[132:135]
s_waitcnt lgkmcnt(6)
ds_read_b128 v[76:79], v50 offset:10240
v_fma_f32 v206, v90, v8, v126
v_mfma_f32_16x16x32_bf16 v[132:135], v[52:55], v[52:55], v[132:135]
v_fma_f32 v207, v91, v8, v127
v_cvt_pk_bf16_f32 v148, v204, v205
v_mfma_f32_16x16x32_bf16 v[116:119], v[0:3], a[8:11], 0
s_waitcnt lgkmcnt(5)
ds_read_b128 v[80:83], v49 offset:12288
v_cvt_pk_bf16_f32 v149, v206, v207
v_mfma_f32_16x16x32_bf16 v[132:135], v[56:59], v[56:59], v[132:135]
ds_read_b128 v[160:163], v50 offset:12288
v_mfma_f32_16x16x32_bf16 v[180:183], v[148:151], v[136:139], v[30:33]
s_waitcnt lgkmcnt(6)
v_mul_f32 v46, v93, v24
v_mul_f32 v47, v45, v24
v_mfma_f32_16x16x32_bf16 v[132:135], v[60:63], v[60:63], v[132:135]
v_fma_f32 v204, v92, v192, v128
v_fma_f32 v205, v92, v193, v129
v_mfma_f32_16x16x32_bf16 v[108:111], a[12:15], v[4:7], v[108:111]
s_waitcnt lgkmcnt(5)
ds_read_b128 v[164:167], v49 offset:14336
v_fma_f32 v206, v92, v194, v130
v_mfma_f32_16x16x32_bf16 v[132:135], v[64:67], v[64:67], v[132:135]
s_xor_b32 s49, s49, 0x10000
ds_read_b128 v[168:171], v50 offset:14336
s_waitcnt lgkmcnt(6)
v_mfma_f32_16x16x32_bf16 v[132:135], v[68:71], v[68:71], v[132:135]
s_add_u32 m0, s49, 0
v_max3_f32 v48, v180, v181, v48
v_max3_f32 v208, v182, v183, v208
v_mfma_f32_16x16x32_bf16 v[112:115], a[16:19], v[4:7], v[112:115]
s_waitcnt lgkmcnt(5)
ds_read_b128 v[172:175], v49 offset:16384
v_mul_f32 v48, v48, v46
v_mfma_f32_16x16x32_bf16 v[132:135], v[72:75], v[72:75], v[132:135]
s_waitcnt lgkmcnt(5)
ds_read_b128 v[176:179], v50 offset:16384
v_mul_f32 v208, v208, v47
v_mfma_f32_16x16x32_bf16 v[132:135], v[76:79], v[76:79], v[132:135]
v_max_f32 v209, v48, v208
v_mov_b32 v210, v209
v_mfma_f32_16x16x32_bf16 v[116:119], v[4:7], a[20:23], v[116:119]
s_waitcnt lgkmcnt(5)
v_permlane16_swap_b32 v209, v210
v_mfma_f32_16x16x32_bf16 v[132:135], v[80:83], v[80:83], v[132:135]
s_waitcnt lgkmcnt(4)
global_load_lds_dwordx4 v9, s[42:43] offset:-2048 sc1
v_max_f32 v209, v209, v210
v_mfma_f32_16x16x32_bf16 v[132:135], v[160:163], v[160:163], v[132:135]
v_mov_b32 v210, v209
v_fma_f32 v207, v92, v195, v131
v_mfma_f32_16x16x32_bf16 v[108:111], a[24:27], v[94:97], v[108:111]
s_waitcnt lgkmcnt(3)
v_permlane32_swap_b32 v209, v210
v_mfma_f32_16x16x32_bf16 v[132:135], v[164:167], v[164:167], v[132:135]
s_waitcnt lgkmcnt(2)
v_max_f32 v209, v209, v210
v_sub_f32 v48, v48, v209
v_mfma_f32_16x16x32_bf16 v[132:135], v[168:171], v[168:171], v[132:135]
v_sub_f32 v208, v208, v209
v_cvt_pk_bf16_f32 v154, v204, v205
v_mfma_f32_16x16x32_bf16 v[112:115], a[28:31], v[94:97], v[112:115]
s_waitcnt lgkmcnt(1)
v_exp_f32 v211, v48
v_mfma_f32_16x16x32_bf16 v[132:135], v[172:175], v[172:175], v[132:135]
s_waitcnt lgkmcnt(0)
v_exp_f32 v212, v208
v_mfma_f32_16x16x32_bf16 v[132:135], v[176:179], v[176:179], v[132:135]
v_add_f32 v209, v211, v212
v_mov_b32 v210, v209
v_mfma_f32_16x16x32_bf16 v[116:119], v[94:97], a[32:35], v[116:119]
global_load_lds_dwordx4 v10, s[42:43] offset:-1024 sc1
v_mul_f32 v211, v211, v93
v_mfma_f32_16x16x32_bf16 v[108:111], a[36:39], v[98:101], v[108:111]
v_permlane16_swap_b32 v209, v210
v_mfma_f32_16x16x32_bf16 v[112:115], a[40:43], v[98:101], v[112:115]
v_add_f32 v209, v209, v210
v_mov_b32 v210, v209
v_mfma_f32_16x16x32_bf16 v[116:119], v[98:101], a[44:47], v[116:119]
ds_write_b128 v21, v[132:135] offset:41984
v_mfma_f32_16x16x32_bf16 v[108:111], a[48:51], v[102:105], v[108:111]
v_mul_f32 v212, v212, v45
v_cvt_pk_bf16_f32 v155, v206, v207
v_mfma_f32_16x16x32_bf16 v[112:115], a[52:55], v[102:105], v[112:115]
v_permlane32_swap_b32 v209, v210
v_mfma_f32_16x16x32_bf16 v[116:119], v[102:105], a[56:59], v[116:119]
v_add_f32 v209, v209, v210
v_fma_f32 v204, v84, v8, v120
v_mfma_f32_16x16x32_bf16 v[108:111], a[60:63], v[52:55], v[108:111]
v_rcp_f32 v209, v209
v_mfma_f32_16x16x32_bf16 v[112:115], a[64:67], v[52:55], v[112:115]
global_load_lds_dwordx4 v11, s[42:43] offset:0 sc1
v_mul_f32 v211, v211, v209
v_mfma_f32_16x16x32_bf16 v[116:119], v[52:55], a[68:71], v[116:119]
v_mul_f32 v212, v212, v209
v_cvt_pk_bf16_f32 v211, v211, v211
v_mfma_f32_16x16x32_bf16 v[108:111], a[72:75], v[56:59], v[108:111]
v_cvt_pk_bf16_f32 v212, v212, v212
v_and_b32 v190, v211, v38
)"
R"(v_mfma_f32_16x16x32_bf16 v[112:115], a[76:79], v[56:59], v[112:115]
v_mfma_f32_16x16x32_bf16 v[116:119], v[56:59], a[80:83], v[116:119]
s_waitcnt lgkmcnt(0)
s_barrier
v_mfma_f32_16x16x32_bf16 v[108:111], a[84:87], v[60:63], v[108:111]
v_and_b32 v191, v212, v39
v_and_b32 v188, v211, v40
v_mfma_f32_16x16x32_bf16 v[112:115], a[88:91], v[60:63], v[112:115]
v_and_b32 v189, v212, v41
v_fma_f32 v205, v85, v8, v121
v_mfma_f32_16x16x32_bf16 v[116:119], v[60:63], a[92:95], v[116:119]
v_mfma_f32_16x16x32_bf16 v[184:187], v[152:155], v[188:191], 0
v_mfma_f32_16x16x32_bf16 v[180:183], v[156:159], v[188:191], 0
v_fma_f32 v206, v86, v8, v122
v_fma_f32 v207, v87, v8, v123
v_mfma_f32_16x16x32_bf16 v[108:111], a[96:99], v[64:67], v[108:111]
global_load_lds_dwordx4 v12, s[42:43] offset:1024 sc1
s_add_u32 s42, s42, s46
s_addc_u32 s43, s43, s47
v_mfma_f32_16x16x32_bf16 v[112:115], a[100:103], v[64:67], v[112:115]
v_cvt_pk_bf16_f32 v140, v204, v205
v_cvt_pk_bf16_f32 v141, v206, v207
v_mfma_f32_16x16x32_bf16 v[116:119], v[64:67], a[104:107], v[116:119]
v_mfma_f32_16x16x32_bf16 v[108:111], a[108:111], v[68:71], v[108:111]
v_cvt_pk_bf16_f32 v106, v184, v185
v_cvt_pk_bf16_f32 v107, v186, v187
v_mfma_f32_16x16x32_bf16 v[112:115], a[112:115], v[68:71], v[112:115]
ds_write_b64 v16, v[106:107]
v_mfma_f32_16x16x32_bf16 v[116:119], v[68:71], a[116:119], v[116:119]
v_cvt_pk_bf16_f32 v211, v180, v181
v_mfma_f32_16x16x32_bf16 v[108:111], a[120:123], v[72:75], v[108:111]
ds_write_b32 v17, v211
v_mfma_f32_16x16x32_bf16 v[112:115], a[124:127], v[72:75], v[112:115]
v_mfma_f32_16x16x32_bf16 v[116:119], v[72:75], a[128:131], v[116:119]
v_mfma_f32_16x16x32_bf16 v[108:111], a[132:135], v[76:79], v[108:111]
v_mfma_f32_16x16x32_bf16 v[112:115], a[136:139], v[76:79], v[112:115]
v_mfma_f32_16x16x32_bf16 v[116:119], v[76:79], a[140:143], v[116:119]
v_mfma_f32_16x16x32_bf16 v[108:111], a[144:147], v[80:83], v[108:111]
v_mfma_f32_16x16x32_bf16 v[112:115], a[148:151], v[80:83], v[112:115]
v_mfma_f32_16x16x32_bf16 v[116:119], v[80:83], a[152:155], v[116:119]
s_waitcnt vmcnt(4)
v_sub_u32 v51, v51, v44
v_cmp_lt_u32 vcc, 0x7f, v51
v_mfma_f32_16x16x32_bf16 v[108:111], a[156:159], v[160:163], v[108:111]
v_mfma_f32_16x16x32_bf16 v[112:115], a[160:163], v[160:163], v[112:115]
v_mfma_f32_16x16x32_bf16 v[116:119], v[160:163], a[164:167], v[116:119]
s_and_b64 vcc, vcc, s[76:77]
s_cbranch_vccnz .Lqa_sp28
.Lqa_rt29:
v_mfma_f32_16x16x32_bf16 v[108:111], a[168:171], v[164:167], v[108:111]
v_mfma_f32_16x16x32_bf16 v[112:115], a[172:175], v[164:167], v[112:115]
s_waitcnt lgkmcnt(0)
s_barrier
v_mfma_f32_16x16x32_bf16 v[116:119], v[164:167], a[176:179], v[116:119]
ds_read_b128 v[0:3], v49 offset:18432
v_mfma_f32_16x16x32_bf16 v[108:111], a[180:183], v[168:171], v[108:111]
ds_read_b128 v[4:7], v50 offset:18432
ds_read_b128 v[196:199], v15 offset:0
v_mfma_f32_16x16x32_bf16 v[112:115], a[184:187], v[168:171], v[112:115]
ds_read_b32 v8, v22 offset:0
ds_read_b32 v146, v19
v_mfma_f32_16x16x32_bf16 v[184:187], v[148:151], v[140:143], v[34:37]
v_mfma_f32_16x16x32_bf16 v[116:119], v[168:171], a[188:191], v[116:119]
ds_read_b128 v[94:97], v49 offset:20480
ds_read_b32 v24, v22 offset:192
v_mfma_f32_16x16x32_bf16 v[108:111], a[192:195], v[172:175], v[108:111]
ds_read_b128 v[98:101], v50 offset:20480
ds_read_b32 v93, v25
v_mfma_f32_16x16x32_bf16 v[112:115], a[196:199], v[172:175], v[112:115]
ds_read_b32 v45, v26
ds_read_b128 v[192:195], v23 offset:0
v_mfma_f32_16x16x32_bf16 v[116:119], v[172:175], a[200:203], v[116:119]
ds_read_b128 v[102:105], v49 offset:22528
ds_read_b64 v[156:157], v20 offset:41472
v_mfma_f32_16x16x32_bf16 v[108:111], a[204:207], v[176:179], v[108:111]
ds_read_b128 v[52:55], v50 offset:22528
ds_read_b32 v138, v18
v_mfma_f32_16x16x32_bf16 v[112:115], a[208:211], v[176:179], v[112:115]
v_max_f32 v48, v184, v185
v_max_f32 v208, v186, v187
v_mfma_f32_16x16x32_bf16 v[116:119], v[176:179], a[212:215], v[116:119]
s_waitcnt lgkmcnt(14)
ds_read_b128 v[56:59], v49 offset:24576
v_mfma_f32_16x16x32_bf16 v[132:135], v[0:3], v[0:3], 0
s_waitcnt lgkmcnt(9)
ds_read_b128 v[60:63], v50 offset:24576
v_mul_f32 v24, 0x3eae1a93, v24
v_mfma_f32_16x16x32_bf16 v[132:135], v[4:7], v[4:7], v[132:135]
global_store_dwordx4 v14, v[196:199], s[38:39] sc1
v_mfma_f32_16x16x32_bf16 v[120:123], a[0:3], v[0:3], 0
ds_read_b128 v[64:67], v49 offset:26624
v_add_u32 v14, 18432, v14
v_mfma_f32_16x16x32_bf16 v[132:135], v[94:97], v[94:97], v[132:135]
s_waitcnt lgkmcnt(10)
ds_read_b128 v[68:71], v50 offset:26624
v_add_u32 v44, 1, v44
v_mfma_f32_16x16x32_bf16 v[132:135], v[98:101], v[98:101], v[132:135]
global_load_dword v51, v43, s[38:39] sc1
v_fma_f32 v204, v88, v8, v112
v_mfma_f32_16x16x32_bf16 v[124:127], a[4:7], v[0:3], 0
s_waitcnt lgkmcnt(7)
ds_read_b128 v[72:75], v49 offset:28672
v_fma_f32 v205, v89, v8, v113
v_mfma_f32_16x16x32_bf16 v[132:135], v[102:105], v[102:105], v[132:135]
s_waitcnt lgkmcnt(6)
ds_read_b128 v[76:79], v50 offset:28672
v_fma_f32 v206, v90, v8, v114
v_mfma_f32_16x16x32_bf16 v[132:135], v[52:55], v[52:55], v[132:135]
v_fma_f32 v207, v91, v8, v115
v_cvt_pk_bf16_f32 v144, v204, v205
v_mfma_f32_16x16x32_bf16 v[128:131], v[0:3], a[8:11], 0
s_waitcnt lgkmcnt(5)
ds_read_b128 v[80:83], v49 offset:30720
v_cvt_pk_bf16_f32 v145, v206, v207
v_mfma_f32_16x16x32_bf16 v[132:135], v[56:59], v[56:59], v[132:135]
ds_read_b128 v[160:163], v50 offset:30720
v_mfma_f32_16x16x32_bf16 v[180:183], v[144:147], v[140:143], v[200:203]
s_waitcnt lgkmcnt(6)
v_mul_f32 v46, v93, v24
v_mul_f32 v47, v45, v24
v_mfma_f32_16x16x32_bf16 v[132:135], v[60:63], v[60:63], v[132:135]
v_fma_f32 v204, v92, v192, v116
v_fma_f32 v205, v92, v193, v117
v_mfma_f32_16x16x32_bf16 v[120:123], a[12:15], v[4:7], v[120:123]
s_waitcnt lgkmcnt(5)
ds_read_b128 v[164:167], v49 offset:32768
v_fma_f32 v206, v92, v194, v118
v_mfma_f32_16x16x32_bf16 v[132:135], v[64:67], v[64:67], v[132:135]
s_add_u32 m0, s49, 18432
ds_read_b128 v[168:171], v50 offset:32768
s_waitcnt lgkmcnt(6)
v_mfma_f32_16x16x32_bf16 v[132:135], v[68:71], v[68:71], v[132:135]
v_max3_f32 v48, v180, v181, v48
v_max3_f32 v208, v182, v183, v208
v_mfma_f32_16x16x32_bf16 v[124:127], a[16:19], v[4:7], v[124:127]
s_waitcnt lgkmcnt(5)
ds_read_b128 v[172:175], v49 offset:34816
v_mul_f32 v48, v48, v46
v_mfma_f32_16x16x32_bf16 v[132:135], v[72:75], v[72:75], v[132:135]
s_waitcnt lgkmcnt(5)
ds_read_b128 v[176:179], v50 offset:34816
v_mul_f32 v208, v208, v47
v_mfma_f32_16x16x32_bf16 v[132:135], v[76:79], v[76:79], v[132:135]
v_max_f32 v209, v48, v208
v_mov_b32 v210, v209
v_mfma_f32_16x16x32_bf16 v[128:131], v[4:7], a[20:23], v[128:131]
s_waitcnt lgkmcnt(5)
global_load_lds_dwordx4 v9, s[42:43] offset:-2048 sc1
v_fma_f32 v207, v92, v195, v119
v_mfma_f32_16x16x32_bf16 v[132:135], v[80:83], v[80:83], v[132:135]
s_waitcnt lgkmcnt(4)
v_permlane16_swap_b32 v209, v210
v_mfma_f32_16x16x32_bf16 v[132:135], v[160:163], v[160:163], v[132:135]
v_max_f32 v209, v209, v210
v_mov_b32 v210, v209
v_mfma_f32_16x16x32_bf16 v[120:123], a[24:27], v[94:97], v[120:123]
s_waitcnt lgkmcnt(3)
v_permlane32_swap_b32 v209, v210
v_mfma_f32_16x16x32_bf16 v[132:135], v[164:167], v[164:167], v[132:135]
s_waitcnt lgkmcnt(2)
v_max_f32 v209, v209, v210
v_sub_f32 v48, v48, v209
v_mfma_f32_16x16x32_bf16 v[132:135], v[168:171], v[168:171], v[132:135]
v_sub_f32 v208, v208, v209
v_cvt_pk_bf16_f32 v152, v204, v205
v_mfma_f32_16x16x32_bf16 v[124:127], a[28:31], v[94:97], v[124:127]
s_waitcnt lgkmcnt(1)
v_exp_f32 v211, v48
v_mfma_f32_16x16x32_bf16 v[132:135], v[172:175], v[172:175], v[132:135]
s_waitcnt lgkmcnt(0)
v_exp_f32 v212, v208
v_mfma_f32_16x16x32_bf16 v[132:135], v[176:179], v[176:179], v[132:135]
global_load_lds_dwordx4 v10, s[42:43] offset:-1024 sc1
v_add_f32 v209, v211, v212
v_mfma_f32_16x16x32_bf16 v[128:131], v[94:97], a[32:35], v[128:131]
v_mov_b32 v210, v209
v_mul_f32 v211, v211, v93
v_mfma_f32_16x16x32_bf16 v[120:123], a[36:39], v[98:101], v[120:123]
v_permlane16_swap_b32 v209, v210
v_mfma_f32_16x16x32_bf16 v[124:127], a[40:43], v[98:101], v[124:127]
v_add_f32 v209, v209, v210
v_mov_b32 v210, v209
v_mfma_f32_16x16x32_bf16 v[128:131], v[98:101], a[44:47], v[128:131]
ds_write_b128 v21, v[132:135] offset:41984
v_mfma_f32_16x16x32_bf16 v[120:123], a[48:51], v[102:105], v[120:123]
v_mul_f32 v212, v212, v45
v_cvt_pk_bf16_f32 v153, v206, v207
v_mfma_f32_16x16x32_bf16 v[124:127], a[52:55], v[102:105], v[124:127]
v_permlane32_swap_b32 v209, v210
v_mfma_f32_16x16x32_bf16 v[128:131], v[102:105], a[56:59], v[128:131]
v_add_f32 v209, v209, v210
v_fma_f32 v204, v84, v8, v108
v_mfma_f32_16x16x32_bf16 v[120:123], a[60:63], v[52:55], v[120:123]
global_load_lds_dwordx4 v11, s[42:43] offset:0 sc1
v_fma_f32 v205, v85, v8, v109
v_mfma_f32_16x16x32_bf16 v[124:127], a[64:67], v[52:55], v[124:127]
v_rcp_f32 v209, v209
v_mfma_f32_16x16x32_bf16 v[128:131], v[52:55], a[68:71], v[128:131]
v_mul_f32 v211, v211, v209
v_mul_f32 v212, v212, v209
v_mfma_f32_16x16x32_bf16 v[120:123], a[72:75], v[56:59], v[120:123]
v_cvt_pk_bf16_f32 v211, v211, v211
v_cvt_pk_bf16_f32 v212, v212, v212
v_mfma_f32_16x16x32_bf16 v[124:127], a[76:79], v[56:59], v[124:127]
v_mfma_f32_16x16x32_bf16 v[128:131], v[56:59], a[80:83], v[128:131]
s_waitcnt lgkmcnt(0)
s_barrier
v_mfma_f32_16x16x32_bf16 v[120:123], a[84:87], v[60:63], v[120:123]
v_and_b32 v188, v211, v38
v_and_b32 v189, v212, v39
v_mfma_f32_16x16x32_bf16 v[124:127], a[88:91], v[60:63], v[124:127]
v_and_b32 v190, v211, v40
v_and_b32 v191, v212, v41
v_mfma_f32_16x16x32_bf16 v[128:131], v[60:63], a[92:95], v[128:131]
global_load_lds_dwordx4 v12, s[42:43] offset:1024 sc1
s_sub_u32 s42, s42, 18432
s_subb_u32 s43, s43, 0
v_mfma_f32_16x16x32_bf16 v[184:187], v[152:155], v[188:191], 0
v_mfma_f32_16x16x32_bf16 v[180:183], v[156:159], v[188:191], 0
v_fma_f32 v206, v86, v8, v110
v_fma_f32 v207, v87, v8, v111
v_mfma_f32_16x16x32_bf16 v[120:123], a[96:99], v[64:67], v[120:123]
v_cvt_pk_bf16_f32 v136, v204, v205
v_cvt_pk_bf16_f32 v137, v206, v207
v_mfma_f32_16x16x32_bf16 v[124:127], a[100:103], v[64:67], v[124:127]
v_mfma_f32_16x16x32_bf16 v[128:131], v[64:67], a[104:107], v[128:131]
v_mfma_f32_16x16x32_bf16 v[120:123], a[108:111], v[68:71], v[120:123]
v_cvt_pk_bf16_f32 v106, v184, v185
v_cvt_pk_bf16_f32 v107, v186, v187
v_mfma_f32_16x16x32_bf16 v[124:127], a[112:115], v[68:71], v[124:127]
ds_write_b64 v16, v[106:107]
v_mfma_f32_16x16x32_bf16 v[128:131], v[68:71], a[116:119], v[128:131]
v_cvt_pk_bf16_f32 v211, v180, v181
v_mfma_f32_16x16x32_bf16 v[120:123], a[120:123], v[72:75], v[120:123]
ds_write_b32 v17, v211
v_mfma_f32_16x16x32_bf16 v[124:127], a[124:127], v[72:75], v[124:127]
v_mfma_f32_16x16x32_bf16 v[128:131], v[72:75], a[128:131], v[128:131]
v_mfma_f32_16x16x32_bf16 v[120:123], a[132:135], v[76:79], v[120:123]
v_mfma_f32_16x16x32_bf16 v[124:127], a[136:139], v[76:79], v[124:127]
v_mfma_f32_16x16x32_bf16 v[128:131], v[76:79], a[140:143], v[128:131]
v_mfma_f32_16x16x32_bf16 v[120:123], a[144:147], v[80:83], v[120:123]
v_mfma_f32_16x16x32_bf16 v[124:127], a[148:151], v[80:83], v[124:127]
v_mfma_f32_16x16x32_bf16 v[128:131], v[80:83], a[152:155], v[128:131]
s_waitcnt vmcnt(4)
v_sub_u32 v51, v51, v44
v_cmp_lt_u32 vcc, 0x7f, v51
v_mfma_f32_16x16x32_bf16 v[120:123], a[156:159], v[160:163], v[120:123]
v_mfma_f32_16x16x32_bf16 v[124:127], a[160:163], v[160:163], v[124:127]
v_xor_b32 v49, 0x10000, v49
v_xor_b32 v50, 0x10000, v50
v_mfma_f32_16x16x32_bf16 v[128:131], v[160:163], a[164:167], v[128:131]
s_and_b64 vcc, vcc, s[76:77]
s_cbranch_vccnz .Lqa_sp30
.Lqa_rt31:
v_mfma_f32_16x16x32_bf16 v[120:123], a[168:171], v[164:167], v[120:123]
v_mfma_f32_16x16x32_bf16 v[124:127], a[172:175], v[164:167], v[124:127]
s_waitcnt lgkmcnt(0)
s_barrier
.Lqa_in25:
v_mfma_f32_16x16x32_bf16 v[128:131], v[164:167], a[176:179], v[128:131]
s_cmp_eq_u32 s70, s73
s_cbranch_scc1 .Lqa_hk26
.Lqa_hr27:
ds_read_b128 v[0:3], v49 offset:0
v_mfma_f32_16x16x32_bf16 v[120:123], a[180:183], v[168:171], v[120:123]
ds_read_b128 v[4:7], v50 offset:0
ds_read_b128 v[196:199], v15 offset:0
v_mfma_f32_16x16x32_bf16 v[124:127], a[184:187], v[168:171], v[124:127]
ds_read_b32 v8, v22 offset:64
ds_read_b32 v150, v19
v_mfma_f32_16x16x32_bf16 v[184:187], v[144:147], v[136:139], v[34:37]
v_mfma_f32_16x16x32_bf16 v[128:131], v[168:171], a[188:191], v[128:131]
ds_read_b128 v[94:97], v49 offset:2048
ds_read_b32 v24, v22 offset:128
v_mfma_f32_16x16x32_bf16 v[120:123], a[192:195], v[172:175], v[120:123]
ds_read_b128 v[98:101], v50 offset:2048
ds_read_b32 v93, v27
v_mfma_f32_16x16x32_bf16 v[124:127], a[196:199], v[172:175], v[124:127]
ds_read_b32 v45, v28
ds_read_b128 v[192:195], v23 offset:64
v_mfma_f32_16x16x32_bf16 v[128:131], v[172:175], a[200:203], v[128:131]
ds_read_b128 v[102:105], v49 offset:4096
ds_read_b64 v[158:159], v20 offset:41472
v_mfma_f32_16x16x32_bf16 v[120:123], a[204:207], v[176:179], v[120:123]
ds_read_b128 v[52:55], v50 offset:4096
ds_read_b32 v142, v18
v_mfma_f32_16x16x32_bf16 v[124:127], a[208:211], v[176:179], v[124:127]
v_max_f32 v48, v184, v185
v_max_f32 v208, v186, v187
v_mfma_f32_16x16x32_bf16 v[128:131], v[176:179], a[212:215], v[128:131]
s_sub_u32 s70, s70, 1
ds_read_b128 v[56:59], v49 offset:6144
s_cbranch_scc0 .Lqa_top38
s_waitcnt lgkmcnt(0)
v_mul_f32 v24, 0x3eae1a93, v24
global_store_dwordx4 v14, v[196:199], s[38:39] sc1
v_add_u32 v14, 18432, v14
v_fma_f32 v204, v88, v8, v124
v_fma_f32 v205, v89, v8, v125
v_fma_f32 v206, v90, v8, v126
v_fma_f32 v207, v91, v8, v127
v_cvt_pk_bf16_f32 v148, v204, v205
v_cvt_pk_bf16_f32 v149, v206, v207
s_nop 1
v_mfma_f32_16x16x32_bf16 v[180:183], v[148:151], v[136:139], v[30:33]
v_mul_f32 v46, v93, v24
v_mul_f32 v47, v45, v24
v_fma_f32 v204, v92, v192, v128
v_fma_f32 v205, v92, v193, v129
v_fma_f32 v206, v92, v194, v130
s_nop 2
v_max3_f32 v48, v180, v181, v48
v_max3_f32 v208, v182, v183, v208
v_mul_f32 v48, v48, v46
v_mul_f32 v208, v208, v47
v_max_f32 v209, v48, v208
v_mov_b32 v210, v209
s_nop 1
v_permlane16_swap_b32 v209, v210
v_max_f32 v209, v209, v210
v_mov_b32 v210, v209
v_fma_f32 v207, v92, v195, v131
s_nop 0
v_permlane32_swap_b32 v209, v210
v_max_f32 v209, v209, v210
v_sub_f32 v48, v48, v209
v_sub_f32 v208, v208, v209
v_cvt_pk_bf16_f32 v154, v204, v205
v_exp_f32 v211, v48
v_exp_f32 v212, v208
s_nop 0
v_add_f32 v209, v211, v212
v_mov_b32 v210, v209
v_mul_f32 v211, v211, v93
s_nop 0
v_permlane16_swap_b32 v209, v210
v_add_f32 v209, v209, v210
v_mov_b32 v210, v209
v_mul_f32 v212, v212, v45
v_cvt_pk_bf16_f32 v155, v206, v207
v_permlane32_swap_b32 v209, v210
v_add_f32 v209, v209, v210
v_fma_f32 v204, v84, v8, v120
v_rcp_f32 v209, v209
s_nop 0
v_mul_f32 v211, v211, v209
v_mul_f32 v212, v212, v209
v_cvt_pk_bf16_f32 v211, v211, v211
v_cvt_pk_bf16_f32 v212, v212, v212
v_and_b32 v190, v211, v38
v_and_b32 v191, v212, v39
v_and_b32 v188, v211, v40
v_and_b32 v189, v212, v41
v_fma_f32 v205, v85, v8, v121
s_nop 0
v_mfma_f32_16x16x32_bf16 v[184:187], v[152:155], v[188:191], 0
v_mfma_f32_16x16x32_bf16 v[180:183], v[156:159], v[188:191], 0
v_fma_f32 v206, v86, v8, v122
v_fma_f32 v207, v87, v8, v123
v_cvt_pk_bf16_f32 v140, v204, v205
v_cvt_pk_bf16_f32 v141, v206, v207
s_nop 2
v_cvt_pk_bf16_f32 v106, v184, v185
v_cvt_pk_bf16_f32 v107, v186, v187
ds_write_b64 v16, v[106:107]
v_cvt_pk_bf16_f32 v211, v180, v181
ds_write_b32 v17, v211
s_waitcnt vmcnt(0)
s_waitcnt lgkmcnt(0)
s_barrier
ds_read_b128 v[196:199], v15 offset:0
s_waitcnt lgkmcnt(0)
global_store_dwordx4 v14, v[196:199], s[38:39] sc1
s_waitcnt vmcnt(0)
s_barrier
s_endpgm
.Lqa_w4:
s_nop 7
v_mov_b32 v204, v30
v_mov_b32 v205, v31
v_mov_b32 v206, v32
v_mov_b32 v207, v33
s_mov_b32 s70, 33
s_waitcnt vmcnt(0) lgkmcnt(0)
s_nop 7
s_branch .Lqa_in39
.p2align 6
.Lqa_top48:
s_waitcnt lgkmcnt(14)
v_mfma_f32_16x16x32_bf16 v[132:135], v[104:107], v[0:3], 0
s_waitcnt lgkmcnt(13)
ds_read_b128 v[64:67], v50 offset:6144
v_mfma_f32_16x16x32_bf16 v[132:135], v[104:107], v[4:7], v[132:135]
s_waitcnt lgkmcnt(9)
v_mul_f32 v21, 0x3eae1a93, v21
s_waitcnt lgkmcnt(8)
v_mul_f32 v43, v93, v21
v_mfma_f32_16x16x32_bf16 v[108:111], a[0:3], v[0:3], 0
ds_read_b128 v[68:71], v49 offset:8192
v_fma_f32 v208, v88, v8, v124
)"
R"(v_mfma_f32_16x16x32_bf16 v[132:135], v[104:107], v[94:97], v[132:135]
ds_read_b128 v[72:75], v50 offset:8192
v_fma_f32 v209, v89, v8, v125
v_mfma_f32_16x16x32_bf16 v[132:135], v[104:107], v[98:101], v[132:135]
v_fma_f32 v210, v90, v8, v126
v_fma_f32 v211, v91, v8, v127
v_mfma_f32_16x16x32_bf16 v[112:115], a[4:7], v[0:3], 0
s_waitcnt lgkmcnt(8)
ds_read_b128 v[76:79], v49 offset:10240
v_cvt_pk_bf16_f32 v148, v208, v209
v_mfma_f32_16x16x32_bf16 v[132:135], v[104:107], v[52:55], v[132:135]
s_waitcnt lgkmcnt(7)
ds_read_b128 v[80:83], v50 offset:10240
v_cvt_pk_bf16_f32 v149, v210, v211
v_mfma_f32_16x16x32_bf16 v[132:135], v[104:107], v[56:59], v[132:135]
s_nop 0
v_mfma_f32_16x16x32_bf16 v[184:187], v[148:151], v[136:139], v[30:33]
v_mul_f32 v44, v42, v21
v_fma_f32 v208, v92, v196, v128
v_mfma_f32_16x16x32_bf16 v[116:119], v[0:3], a[8:11], 0
s_waitcnt lgkmcnt(5)
ds_read_b128 v[160:163], v49 offset:12288
v_fma_f32 v209, v92, v197, v129
v_mfma_f32_16x16x32_bf16 v[132:135], v[104:107], v[60:63], v[132:135]
s_waitcnt lgkmcnt(5)
ds_read_b128 v[164:167], v50 offset:12288
v_fma_f32 v210, v92, v198, v130
v_mfma_f32_16x16x32_bf16 v[132:135], v[104:107], v[64:67], v[132:135]
v_fma_f32 v211, v92, v199, v131
v_cvt_pk_bf16_f32 v154, v208, v209
v_mfma_f32_16x16x32_bf16 v[108:111], a[12:15], v[4:7], v[108:111]
s_waitcnt lgkmcnt(5)
ds_read_b128 v[168:171], v49 offset:14336
v_max3_f32 v212, v184, v185, v212
v_mfma_f32_16x16x32_bf16 v[132:135], v[104:107], v[68:71], v[132:135]
s_xor_b32 s49, s49, 0x10000
ds_read_b128 v[172:175], v50 offset:14336
s_waitcnt lgkmcnt(6)
v_mfma_f32_16x16x32_bf16 v[132:135], v[104:107], v[72:75], v[132:135]
s_add_u32 m0, s49, 0
v_max3_f32 v213, v186, v187, v213
v_mul_f32 v212, v212, v43
v_mfma_f32_16x16x32_bf16 v[112:115], a[16:19], v[4:7], v[112:115]
s_waitcnt lgkmcnt(5)
ds_read_b128 v[176:179], v49 offset:16384
v_mul_f32 v213, v213, v44
v_mfma_f32_16x16x32_bf16 v[132:135], v[104:107], v[76:79], v[132:135]
s_waitcnt lgkmcnt(5)
ds_read_b128 v[180:183], v50 offset:16384
v_max_f32 v214, v212, v213
v_mfma_f32_16x16x32_bf16 v[132:135], v[104:107], v[80:83], v[132:135]
v_mov_b32 v215, v214
v_cvt_pk_bf16_f32 v155, v210, v211
v_mfma_f32_16x16x32_bf16 v[116:119], v[4:7], a[20:23], v[116:119]
s_waitcnt lgkmcnt(5)
v_permlane16_swap_b32 v214, v215
v_mfma_f32_16x16x32_bf16 v[132:135], v[104:107], v[160:163], v[132:135]
s_waitcnt lgkmcnt(4)
v_max_f32 v214, v214, v215
v_mov_b32 v215, v214
v_mfma_f32_16x16x32_bf16 v[132:135], v[104:107], v[164:167], v[132:135]
s_nop 0
v_permlane32_swap_b32 v214, v215
v_mfma_f32_16x16x32_bf16 v[108:111], a[24:27], v[94:97], v[108:111]
s_waitcnt lgkmcnt(3)
global_load_lds_dwordx4 v9, s[42:43] offset:-2048 sc1
v_max_f32 v214, v214, v215
v_mfma_f32_16x16x32_bf16 v[132:135], v[104:107], v[168:171], v[132:135]
s_waitcnt lgkmcnt(2)
v_sub_f32 v212, v212, v214
v_sub_f32 v213, v213, v214
v_mfma_f32_16x16x32_bf16 v[132:135], v[104:107], v[172:175], v[132:135]
v_exp_f32 v216, v212
v_mfma_f32_16x16x32_bf16 v[112:115], a[28:31], v[94:97], v[112:115]
s_waitcnt lgkmcnt(1)
v_exp_f32 v217, v213
v_mfma_f32_16x16x32_bf16 v[132:135], v[104:107], v[176:179], v[132:135]
s_waitcnt lgkmcnt(0)
v_add_f32 v214, v216, v217
v_mov_b32 v215, v214
v_mfma_f32_16x16x32_bf16 v[132:135], v[104:107], v[180:183], v[132:135]
v_mul_f32 v216, v216, v93
v_fma_f32 v208, v84, v8, v120
v_mfma_f32_16x16x32_bf16 v[116:119], v[94:97], a[32:35], v[116:119]
v_permlane16_swap_b32 v214, v215
v_mfma_f32_16x16x32_bf16 v[108:111], a[36:39], v[98:101], v[108:111]
v_add_f32 v214, v214, v215
v_mov_b32 v215, v214
v_mfma_f32_16x16x32_bf16 v[112:115], a[40:43], v[98:101], v[112:115]
v_mul_f32 v200, 0x3ae38e39, v132
v_cvt_pk_bf16_f32 v218, -v200, -v200
v_mfma_f32_16x16x32_bf16 v[116:119], v[98:101], a[44:47], v[116:119]
v_lshlrev_b32 v218, 16, v218
global_load_lds_dwordx4 v10, s[42:43] offset:-1024 sc1
v_mfma_f32_16x16x32_bf16 v[108:111], a[48:51], v[52:55], v[108:111]
ds_write_b32 v22, v218 offset:0
v_mfma_f32_16x16x32_bf16 v[112:115], a[52:55], v[52:55], v[112:115]
v_mul_f32 v217, v217, v42
v_fma_f32 v209, v85, v8, v121
v_mfma_f32_16x16x32_bf16 v[116:119], v[52:55], a[56:59], v[116:119]
v_permlane32_swap_b32 v214, v215
v_mfma_f32_16x16x32_bf16 v[108:111], a[60:63], v[56:59], v[108:111]
v_add_f32 v214, v214, v215
v_fma_f32 v210, v86, v8, v122
v_mfma_f32_16x16x32_bf16 v[112:115], a[64:67], v[56:59], v[112:115]
v_rcp_f32 v214, v214
v_mfma_f32_16x16x32_bf16 v[116:119], v[56:59], a[68:71], v[116:119]
v_mul_f32 v216, v216, v214
v_mul_f32 v217, v217, v214
v_mfma_f32_16x16x32_bf16 v[108:111], a[72:75], v[60:63], v[108:111]
v_cvt_pk_bf16_f32 v216, v216, v216
v_cvt_pk_bf16_f32 v217, v217, v217
v_mfma_f32_16x16x32_bf16 v[112:115], a[76:79], v[60:63], v[112:115]
v_mfma_f32_16x16x32_bf16 v[116:119], v[60:63], a[80:83], v[116:119]
s_waitcnt lgkmcnt(0)
s_barrier
v_mfma_f32_16x16x32_bf16 v[108:111], a[84:87], v[64:67], v[108:111]
global_load_lds_dwordx4 v11, s[42:43] offset:0 sc1
ds_read_b32 v218, v24
v_mfma_f32_16x16x32_bf16 v[112:115], a[88:91], v[64:67], v[112:115]
v_and_b32 v194, v216, v38
v_and_b32 v195, v217, v39
v_mfma_f32_16x16x32_bf16 v[116:119], v[64:67], a[92:95], v[116:119]
v_and_b32 v192, v216, v40
v_and_b32 v193, v217, v41
v_mfma_f32_16x16x32_bf16 v[108:111], a[96:99], v[68:71], v[108:111]
s_nop 0
v_mfma_f32_16x16x32_bf16 v[188:191], v[152:155], v[192:195], 0
v_mfma_f32_16x16x32_bf16 v[184:187], v[156:159], v[192:195], 0
v_fma_f32 v211, v87, v8, v123
v_cvt_pk_bf16_f32 v140, v208, v209
v_mfma_f32_16x16x32_bf16 v[112:115], a[100:103], v[68:71], v[112:115]
v_cvt_pk_bf16_f32 v141, v210, v211
v_mfma_f32_16x16x32_bf16 v[116:119], v[68:71], a[104:107], v[116:119]
v_mfma_f32_16x16x32_bf16 v[108:111], a[108:111], v[72:75], v[108:111]
s_waitcnt lgkmcnt(0)
v_mul_f32 v218, 0x3ae38e39, v218
v_fma_f32 v201, -v200, v200, v218
v_mfma_f32_16x16x32_bf16 v[112:115], a[112:115], v[72:75], v[112:115]
v_max_f32 v201, 0, v201
v_cvt_pk_bf16_f32 v201, v201, v201
v_mfma_f32_16x16x32_bf16 v[116:119], v[72:75], a[116:119], v[116:119]
global_load_lds_dwordx4 v12, s[42:43] offset:1024 sc1
s_add_u32 s42, s42, s46
s_addc_u32 s43, s43, s47
v_mfma_f32_16x16x32_bf16 v[108:111], a[120:123], v[76:79], v[108:111]
v_lshlrev_b32 v201, 16, v201
v_add_f32 v201, 0x3727c5ac, v201
v_mfma_f32_16x16x32_bf16 v[112:115], a[124:127], v[76:79], v[112:115]
v_cvt_pk_bf16_f32 v201, v201, v201
v_lshlrev_b32 v201, 16, v201
v_mfma_f32_16x16x32_bf16 v[116:119], v[76:79], a[128:131], v[116:119]
v_sqrt_f32 v201, v201
v_mfma_f32_16x16x32_bf16 v[108:111], a[132:135], v[80:83], v[108:111]
v_cvt_pk_bf16_f32 v201, v201, v201
v_lshlrev_b32 v201, 16, v201
v_mfma_f32_16x16x32_bf16 v[112:115], a[136:139], v[80:83], v[112:115]
v_rcp_f32 v218, v201
v_mfma_f32_16x16x32_bf16 v[116:119], v[80:83], a[140:143], v[116:119]
ds_write_b32 v22, v218 offset:128
v_mfma_f32_16x16x32_bf16 v[108:111], a[144:147], v[160:163], v[108:111]
v_cvt_pk_bf16_f32 v102, v188, v189
v_cvt_pk_bf16_f32 v103, v190, v191
v_mfma_f32_16x16x32_bf16 v[112:115], a[148:151], v[160:163], v[112:115]
ds_write_b64 v16, v[102:103]
v_mfma_f32_16x16x32_bf16 v[116:119], v[160:163], a[152:155], v[116:119]
v_cvt_pk_bf16_f32 v216, v186, v187
v_mfma_f32_16x16x32_bf16 v[108:111], a[156:159], v[164:167], v[108:111]
ds_write_b32 v17, v216
v_mfma_f32_16x16x32_bf16 v[112:115], a[160:163], v[164:167], v[112:115]
v_mfma_f32_16x16x32_bf16 v[116:119], v[164:167], a[164:167], v[116:119]
v_mfma_f32_16x16x32_bf16 v[108:111], a[168:171], v[168:171], v[108:111]
v_mfma_f32_16x16x32_bf16 v[112:115], a[172:175], v[168:171], v[112:115]
s_waitcnt vmcnt(5) lgkmcnt(0)
s_barrier
v_mfma_f32_16x16x32_bf16 v[116:119], v[168:171], a[176:179], v[116:119]
ds_read_b128 v[0:3], v49 offset:18432
v_mfma_f32_16x16x32_bf16 v[108:111], a[180:183], v[172:175], v[108:111]
ds_read_b128 v[4:7], v50 offset:18432
global_store_dword v45, v46, s[38:39] sc1
v_mfma_f32_16x16x32_bf16 v[112:115], a[184:187], v[172:175], v[112:115]
v_add_u32 v46, 1, v46
ds_read_b32 v8, v22 offset:0
v_mfma_f32_16x16x32_bf16 v[188:191], v[148:151], v[140:143], v[34:37]
v_mfma_f32_16x16x32_bf16 v[116:119], v[172:175], a[188:191], v[116:119]
ds_read_b128 v[94:97], v49 offset:20480
ds_read_b32 v146, v19
v_mfma_f32_16x16x32_bf16 v[108:111], a[192:195], v[176:179], v[108:111]
ds_read_b128 v[98:101], v50 offset:20480
ds_read_b32 v21, v22 offset:192
v_mfma_f32_16x16x32_bf16 v[112:115], a[196:199], v[176:179], v[112:115]
ds_read_b32 v93, v25
ds_read_b32 v42, v26
v_mfma_f32_16x16x32_bf16 v[116:119], v[176:179], a[200:203], v[116:119]
ds_read_b128 v[52:55], v49 offset:22528
ds_read_b128 v[196:199], v23 offset:0
v_mfma_f32_16x16x32_bf16 v[108:111], a[204:207], v[180:183], v[108:111]
ds_read_b128 v[56:59], v50 offset:22528
ds_read_b64 v[156:157], v20 offset:41472
v_mfma_f32_16x16x32_bf16 v[112:115], a[208:211], v[180:183], v[112:115]
ds_read_b32 v138, v18
v_max_f32 v212, v188, v189
v_mfma_f32_16x16x32_bf16 v[116:119], v[180:183], a[212:215], v[116:119]
s_waitcnt lgkmcnt(13)
ds_read_b128 v[60:63], v49 offset:24576
v_max_f32 v213, v190, v191
v_mfma_f32_16x16x32_bf16 v[132:135], v[104:107], v[0:3], 0
s_waitcnt lgkmcnt(13)
ds_read_b128 v[64:67], v50 offset:24576
v_mfma_f32_16x16x32_bf16 v[132:135], v[104:107], v[4:7], v[132:135]
s_waitcnt lgkmcnt(9)
v_mul_f32 v21, 0x3eae1a93, v21
s_waitcnt lgkmcnt(8)
v_mul_f32 v43, v93, v21
v_mfma_f32_16x16x32_bf16 v[120:123], a[0:3], v[0:3], 0
ds_read_b128 v[68:71], v49 offset:26624
v_fma_f32 v208, v88, v8, v112
v_mfma_f32_16x16x32_bf16 v[132:135], v[104:107], v[94:97], v[132:135]
ds_read_b128 v[72:75], v50 offset:26624
v_fma_f32 v209, v89, v8, v113
v_mfma_f32_16x16x32_bf16 v[132:135], v[104:107], v[98:101], v[132:135]
v_fma_f32 v210, v90, v8, v114
v_fma_f32 v211, v91, v8, v115
v_mfma_f32_16x16x32_bf16 v[124:127], a[4:7], v[0:3], 0
s_waitcnt lgkmcnt(8)
ds_read_b128 v[76:79], v49 offset:28672
v_cvt_pk_bf16_f32 v144, v208, v209
v_mfma_f32_16x16x32_bf16 v[132:135], v[104:107], v[52:55], v[132:135]
s_waitcnt lgkmcnt(7)
ds_read_b128 v[80:83], v50 offset:28672
v_cvt_pk_bf16_f32 v145, v210, v211
v_mfma_f32_16x16x32_bf16 v[132:135], v[104:107], v[56:59], v[132:135]
s_nop 0
v_mfma_f32_16x16x32_bf16 v[184:187], v[144:147], v[140:143], v[204:207]
v_mul_f32 v44, v42, v21
v_fma_f32 v208, v92, v196, v116
v_mfma_f32_16x16x32_bf16 v[128:131], v[0:3], a[8:11], 0
s_waitcnt lgkmcnt(5)
ds_read_b128 v[160:163], v49 offset:30720
v_fma_f32 v209, v92, v197, v117
v_mfma_f32_16x16x32_bf16 v[132:135], v[104:107], v[60:63], v[132:135]
s_waitcnt lgkmcnt(5)
ds_read_b128 v[164:167], v50 offset:30720
v_fma_f32 v210, v92, v198, v118
v_mfma_f32_16x16x32_bf16 v[132:135], v[104:107], v[64:67], v[132:135]
v_fma_f32 v211, v92, v199, v119
v_cvt_pk_bf16_f32 v152, v208, v209
v_mfma_f32_16x16x32_bf16 v[120:123], a[12:15], v[4:7], v[120:123]
s_waitcnt lgkmcnt(5)
ds_read_b128 v[168:171], v49 offset:32768
v_max3_f32 v212, v184, v185, v212
v_mfma_f32_16x16x32_bf16 v[132:135], v[104:107], v[68:71], v[132:135]
s_add_u32 m0, s49, 18432
ds_read_b128 v[172:175], v50 offset:32768
s_waitcnt lgkmcnt(6)
v_mfma_f32_16x16x32_bf16 v[132:135], v[104:107], v[72:75], v[132:135]
v_max3_f32 v213, v186, v187, v213
v_mul_f32 v212, v212, v43
v_mfma_f32_16x16x32_bf16 v[124:127], a[16:19], v[4:7], v[124:127]
s_waitcnt lgkmcnt(5)
ds_read_b128 v[176:179], v49 offset:34816
v_mul_f32 v213, v213, v44
v_mfma_f32_16x16x32_bf16 v[132:135], v[104:107], v[76:79], v[132:135]
s_waitcnt lgkmcnt(5)
ds_read_b128 v[180:183], v50 offset:34816
v_max_f32 v214, v212, v213
v_mfma_f32_16x16x32_bf16 v[132:135], v[104:107], v[80:83], v[132:135]
v_mov_b32 v215, v214
v_cvt_pk_bf16_f32 v153, v210, v211
v_mfma_f32_16x16x32_bf16 v[128:131], v[4:7], a[20:23], v[128:131]
s_waitcnt lgkmcnt(5)
v_permlane16_swap_b32 v214, v215
v_mfma_f32_16x16x32_bf16 v[132:135], v[104:107], v[160:163], v[132:135]
s_waitcnt lgkmcnt(4)
v_max_f32 v214, v214, v215
v_mov_b32 v215, v214
v_mfma_f32_16x16x32_bf16 v[132:135], v[104:107], v[164:167], v[132:135]
global_load_lds_dwordx4 v9, s[42:43] offset:-2048 sc1
v_fma_f32 v208, v84, v8, v108
v_mfma_f32_16x16x32_bf16 v[120:123], a[24:27], v[94:97], v[120:123]
s_waitcnt lgkmcnt(3)
v_permlane32_swap_b32 v214, v215
v_mfma_f32_16x16x32_bf16 v[132:135], v[104:107], v[168:171], v[132:135]
s_waitcnt lgkmcnt(2)
v_max_f32 v214, v214, v215
v_sub_f32 v212, v212, v214
v_mfma_f32_16x16x32_bf16 v[132:135], v[104:107], v[172:175], v[132:135]
v_sub_f32 v213, v213, v214
v_fma_f32 v209, v85, v8, v109
v_mfma_f32_16x16x32_bf16 v[124:127], a[28:31], v[94:97], v[124:127]
s_waitcnt lgkmcnt(1)
v_exp_f32 v216, v212
v_mfma_f32_16x16x32_bf16 v[132:135], v[104:107], v[176:179], v[132:135]
s_waitcnt lgkmcnt(0)
v_exp_f32 v217, v213
v_mfma_f32_16x16x32_bf16 v[132:135], v[104:107], v[180:183], v[132:135]
v_add_f32 v214, v216, v217
v_mov_b32 v215, v214
v_mfma_f32_16x16x32_bf16 v[128:131], v[94:97], a[32:35], v[128:131]
v_mul_f32 v216, v216, v93
v_fma_f32 v210, v86, v8, v110
v_mfma_f32_16x16x32_bf16 v[120:123], a[36:39], v[98:101], v[120:123]
global_load_lds_dwordx4 v10, s[42:43] offset:-1024 sc1
v_fma_f32 v211, v87, v8, v111
v_mfma_f32_16x16x32_bf16 v[124:127], a[40:43], v[98:101], v[124:127]
v_mul_f32 v200, 0x3ae38e39, v132
v_cvt_pk_bf16_f32 v218, -v200, -v200
v_mfma_f32_16x16x32_bf16 v[128:131], v[98:101], a[44:47], v[128:131]
v_lshlrev_b32 v218, 16, v218
v_cvt_pk_bf16_f32 v136, v208, v209
v_mfma_f32_16x16x32_bf16 v[120:123], a[48:51], v[52:55], v[120:123]
ds_write_b32 v22, v218 offset:64
v_mfma_f32_16x16x32_bf16 v[124:127], a[52:55], v[52:55], v[124:127]
v_permlane16_swap_b32 v214, v215
v_mfma_f32_16x16x32_bf16 v[128:131], v[52:55], a[56:59], v[128:131]
v_add_f32 v214, v214, v215
v_mov_b32 v215, v214
v_mfma_f32_16x16x32_bf16 v[120:123], a[60:63], v[56:59], v[120:123]
v_mul_f32 v217, v217, v42
v_cvt_pk_bf16_f32 v137, v210, v211
v_mfma_f32_16x16x32_bf16 v[124:127], a[64:67], v[56:59], v[124:127]
v_permlane32_swap_b32 v214, v215
v_mfma_f32_16x16x32_bf16 v[128:131], v[56:59], a[68:71], v[128:131]
global_load_lds_dwordx4 v11, s[42:43] offset:0 sc1
v_add_f32 v214, v214, v215
v_mfma_f32_16x16x32_bf16 v[120:123], a[72:75], v[60:63], v[120:123]
v_rcp_f32 v214, v214
v_mfma_f32_16x16x32_bf16 v[124:127], a[76:79], v[60:63], v[124:127]
v_mfma_f32_16x16x32_bf16 v[128:131], v[60:63], a[80:83], v[128:131]
s_waitcnt lgkmcnt(0)
s_barrier
v_mfma_f32_16x16x32_bf16 v[120:123], a[84:87], v[64:67], v[120:123]
ds_read_b32 v218, v24
v_mul_f32 v216, v216, v214
v_mfma_f32_16x16x32_bf16 v[124:127], a[88:91], v[64:67], v[124:127]
v_mul_f32 v217, v217, v214
v_cvt_pk_bf16_f32 v216, v216, v216
v_mfma_f32_16x16x32_bf16 v[128:131], v[64:67], a[92:95], v[128:131]
v_cvt_pk_bf16_f32 v217, v217, v217
v_and_b32 v192, v216, v38
v_mfma_f32_16x16x32_bf16 v[120:123], a[96:99], v[68:71], v[120:123]
v_and_b32 v193, v217, v39
v_and_b32 v194, v216, v40
v_mfma_f32_16x16x32_bf16 v[124:127], a[100:103], v[68:71], v[124:127]
global_load_lds_dwordx4 v12, s[42:43] offset:1024 sc1
s_sub_u32 s42, s42, 18432
s_subb_u32 s43, s43, 0
v_mfma_f32_16x16x32_bf16 v[128:131], v[68:71], a[104:107], v[128:131]
v_and_b32 v195, v217, v41
v_mfma_f32_16x16x32_bf16 v[120:123], a[108:111], v[72:75], v[120:123]
s_nop 0
v_mfma_f32_16x16x32_bf16 v[188:191], v[152:155], v[192:195], 0
v_mfma_f32_16x16x32_bf16 v[184:187], v[156:159], v[192:195], 0
s_waitcnt lgkmcnt(0)
v_mul_f32 v218, 0x3ae38e39, v218
v_fma_f32 v201, -v200, v200, v218
v_mfma_f32_16x16x32_bf16 v[124:127], a[112:115], v[72:75], v[124:127]
v_max_f32 v201, 0, v201
v_cvt_pk_bf16_f32 v201, v201, v201
v_mfma_f32_16x16x32_bf16 v[128:131], v[72:75], a[116:119], v[128:131]
v_lshlrev_b32 v201, 16, v201
v_add_f32 v201, 0x3727c5ac, v201
v_mfma_f32_16x16x32_bf16 v[120:123], a[120:123], v[76:79], v[120:123]
v_cvt_pk_bf16_f32 v201, v201, v201
v_lshlrev_b32 v201, 16, v201
v_mfma_f32_16x16x32_bf16 v[124:127], a[124:127], v[76:79], v[124:127]
v_sqrt_f32 v201, v201
v_mfma_f32_16x16x32_bf16 v[128:131], v[76:79], a[128:131], v[128:131]
v_cvt_pk_bf16_f32 v201, v201, v201
v_lshlrev_b32 v201, 16, v201
v_mfma_f32_16x16x32_bf16 v[120:123], a[132:135], v[80:83], v[120:123]
v_rcp_f32 v218, v201
v_mfma_f32_16x16x32_bf16 v[124:127], a[136:139], v[80:83], v[124:127]
ds_write_b32 v22, v218 offset:192
v_mfma_f32_16x16x32_bf16 v[128:131], v[80:83], a[140:143], v[128:131]
v_cvt_pk_bf16_f32 v102, v188, v189
v_cvt_pk_bf16_f32 v103, v190, v191
v_mfma_f32_16x16x32_bf16 v[120:123], a[144:147], v[160:163], v[120:123]
ds_write_b64 v16, v[102:103]
v_mfma_f32_16x16x32_bf16 v[124:127], a[148:151], v[160:163], v[124:127]
v_cvt_pk_bf16_f32 v216, v186, v187
v_mfma_f32_16x16x32_bf16 v[128:131], v[160:163], a[152:155], v[128:131]
ds_write_b32 v17, v216
v_mfma_f32_16x16x32_bf16 v[120:123], a[156:159], v[164:167], v[120:123]
v_mfma_f32_16x16x32_bf16 v[124:127], a[160:163], v[164:167], v[124:127]
v_xor_b32 v49, 0x10000, v49
v_xor_b32 v50, 0x10000, v50
v_mfma_f32_16x16x32_bf16 v[128:131], v[164:167], a[164:167], v[128:131]
v_mfma_f32_16x16x32_bf16 v[120:123], a[168:171], v[168:171], v[120:123]
v_mfma_f32_16x16x32_bf16 v[124:127], a[172:175], v[168:171], v[124:127]
s_waitcnt vmcnt(5) lgkmcnt(0)
s_barrier
.Lqa_in39:
v_mfma_f32_16x16x32_bf16 v[128:131], v[168:171], a[176:179], v[128:131]
s_cmp_eq_u32 s70, s73
s_cbranch_scc1 .Lqa_hk40
.Lqa_hr41:
ds_read_b128 v[0:3], v49 offset:0
v_mfma_f32_16x16x32_bf16 v[120:123], a[180:183], v[172:175], v[120:123]
ds_read_b128 v[4:7], v50 offset:0
)"
R"(global_store_dword v45, v46, s[38:39] sc1
v_mfma_f32_16x16x32_bf16 v[124:127], a[184:187], v[172:175], v[124:127]
v_add_u32 v46, 1, v46
ds_read_b32 v8, v22 offset:64
v_mfma_f32_16x16x32_bf16 v[188:191], v[144:147], v[136:139], v[34:37]
v_mfma_f32_16x16x32_bf16 v[128:131], v[172:175], a[188:191], v[128:131]
ds_read_b128 v[94:97], v49 offset:2048
ds_read_b32 v150, v19
v_mfma_f32_16x16x32_bf16 v[120:123], a[192:195], v[176:179], v[120:123]
ds_read_b128 v[98:101], v50 offset:2048
ds_read_b32 v21, v22 offset:128
v_mfma_f32_16x16x32_bf16 v[124:127], a[196:199], v[176:179], v[124:127]
ds_read_b32 v93, v27
ds_read_b32 v42, v28
v_mfma_f32_16x16x32_bf16 v[128:131], v[176:179], a[200:203], v[128:131]
ds_read_b128 v[52:55], v49 offset:4096
ds_read_b128 v[196:199], v23 offset:64
v_mfma_f32_16x16x32_bf16 v[120:123], a[204:207], v[180:183], v[120:123]
ds_read_b128 v[56:59], v50 offset:4096
ds_read_b64 v[158:159], v20 offset:41472
v_mfma_f32_16x16x32_bf16 v[124:127], a[208:211], v[180:183], v[124:127]
ds_read_b32 v142, v18
v_max_f32 v212, v188, v189
v_mfma_f32_16x16x32_bf16 v[128:131], v[180:183], a[212:215], v[128:131]
s_sub_u32 s70, s70, 1
ds_read_b128 v[60:63], v49 offset:6144
v_max_f32 v213, v190, v191
s_cbranch_scc0 .Lqa_top48
s_waitcnt lgkmcnt(0)
v_mul_f32 v21, 0x3eae1a93, v21
v_mul_f32 v43, v93, v21
v_fma_f32 v208, v88, v8, v124
v_fma_f32 v209, v89, v8, v125
v_fma_f32 v210, v90, v8, v126
v_fma_f32 v211, v91, v8, v127
v_cvt_pk_bf16_f32 v148, v208, v209
v_cvt_pk_bf16_f32 v149, v210, v211
s_nop 1
v_mfma_f32_16x16x32_bf16 v[184:187], v[148:151], v[136:139], v[30:33]
v_mul_f32 v44, v42, v21
v_fma_f32 v208, v92, v196, v128
v_fma_f32 v209, v92, v197, v129
v_fma_f32 v210, v92, v198, v130
v_fma_f32 v211, v92, v199, v131
v_cvt_pk_bf16_f32 v154, v208, v209
s_nop 1
v_max3_f32 v212, v184, v185, v212
v_max3_f32 v213, v186, v187, v213
v_mul_f32 v212, v212, v43
v_mul_f32 v213, v213, v44
v_max_f32 v214, v212, v213
v_mov_b32 v215, v214
v_cvt_pk_bf16_f32 v155, v210, v211
s_nop 0
v_permlane16_swap_b32 v214, v215
v_max_f32 v214, v214, v215
v_mov_b32 v215, v214
s_nop 1
v_permlane32_swap_b32 v214, v215
v_max_f32 v214, v214, v215
v_sub_f32 v212, v212, v214
v_sub_f32 v213, v213, v214
v_exp_f32 v216, v212
v_exp_f32 v217, v213
s_nop 0
v_add_f32 v214, v216, v217
v_mov_b32 v215, v214
v_mul_f32 v216, v216, v93
v_fma_f32 v208, v84, v8, v120
v_permlane16_swap_b32 v214, v215
v_add_f32 v214, v214, v215
v_mov_b32 v215, v214
v_mul_f32 v217, v217, v42
v_fma_f32 v209, v85, v8, v121
v_permlane32_swap_b32 v214, v215
v_add_f32 v214, v214, v215
v_fma_f32 v210, v86, v8, v122
v_rcp_f32 v214, v214
s_nop 0
v_mul_f32 v216, v216, v214
v_mul_f32 v217, v217, v214
v_cvt_pk_bf16_f32 v216, v216, v216
v_cvt_pk_bf16_f32 v217, v217, v217
v_and_b32 v194, v216, v38
v_and_b32 v195, v217, v39
v_and_b32 v192, v216, v40
v_and_b32 v193, v217, v41
s_nop 1
v_mfma_f32_16x16x32_bf16 v[188:191], v[152:155], v[192:195], 0
v_mfma_f32_16x16x32_bf16 v[184:187], v[156:159], v[192:195], 0
v_fma_f32 v211, v87, v8, v123
v_cvt_pk_bf16_f32 v140, v208, v209
v_cvt_pk_bf16_f32 v141, v210, v211
s_nop 3
v_cvt_pk_bf16_f32 v102, v188, v189
v_cvt_pk_bf16_f32 v103, v190, v191
ds_write_b64 v16, v[102:103]
v_cvt_pk_bf16_f32 v216, v186, v187
ds_write_b32 v17, v216
s_waitcnt vmcnt(0)
s_waitcnt lgkmcnt(0)
s_barrier
global_store_dword v45, v46, s[38:39] sc1
v_add_u32 v46, 8, v46
s_waitcnt vmcnt(1)
s_barrier
global_store_dword v45, v46, s[38:39] sc1
s_cmp_lg_u32 s53, 0
s_cbranch_scc1 .Lqa_n49
global_load_dword v51, v47, s[38:39] sc1
s_waitcnt vmcnt(0)
v_sub_u32 v51, v51, v48
v_cmp_lt_u32 vcc, 0x7f, v51
s_cbranch_vccnz .Lqa_sp50
.Lqa_rt51:
v_add_u32 v47, 24576, v47
global_load_dword v51, v47, s[38:39] sc1
s_waitcnt vmcnt(0)
v_sub_u32 v51, v51, v48
v_cmp_lt_u32 vcc, 0x7f, v51
s_cbranch_vccnz .Lqa_sp52
.Lqa_rt53:
v_add_u32 v47, 24576, v47
global_load_dword v51, v47, s[38:39] sc1
s_waitcnt vmcnt(0)
v_sub_u32 v51, v51, v48
v_cmp_lt_u32 vcc, 0x7f, v51
s_cbranch_vccnz .Lqa_sp54
.Lqa_rt55:
v_add_u32 v47, 24576, v47
global_load_dword v51, v47, s[38:39] sc1
s_waitcnt vmcnt(0)
v_sub_u32 v51, v51, v48
v_cmp_lt_u32 vcc, 0x7f, v51
s_cbranch_vccnz .Lqa_sp56
.Lqa_rt57:
v_add_u32 v47, 24576, v47
s_load_dword s75, s[38:39], 0x0
s_waitcnt lgkmcnt(0)
s_add_u32 s75, s75, 1
v_mov_b32 v51, s75
v_mov_b32 v47, 0
global_store_dword v47, v51, s[38:39] sc1
.Lqa_n49:
s_endpgm
.Lqa_hk6:
s_cmp_lg_u32 s70, 18
s_cbranch_scc1 .Lqa_e8
s_add_u32 s42, s42, s50
s_addc_u32 s43, s43, 0
s_mov_b64 s[76:77], -1
s_mov_b64 s[46:47], s[44:45]
s_mov_b32 s73, 17
s_nop 1
s_branch .Lqa_hr7
.Lqa_e8:
s_cmp_lg_u32 s70, 17
s_cbranch_scc1 .Lqa_e9
s_mov_b32 s46, -18432
s_mov_b32 s47, -1
v_or_b32 v232, s52, v232
v_or_b32 v233, s52, v233
v_or_b32 v234, s52, v234
v_or_b32 v235, s52, v235
s_mov_b32 s73, 16
s_nop 1
s_branch .Lqa_hr7
.Lqa_e9:
s_cmp_lg_u32 s70, 16
s_cbranch_scc1 .Lqa_e10
v_mov_b32 v232, v30
v_mov_b32 v233, v31
v_mov_b32 v234, v32
v_mov_b32 v235, v33
s_mov_b32 s73, 15
s_nop 1
s_branch .Lqa_hr7
.Lqa_e10:
s_cmp_lg_u32 s70, 15
s_cbranch_scc1 .Lqa_e11
v_subrev_u32 v14, 18432, v14
s_mov_b32 s73, 2
s_nop 1
s_branch .Lqa_hr7
.Lqa_e11:
s_cmp_lg_u32 s70, 2
s_cbranch_scc1 .Lqa_e12
s_add_u32 s46, s46, s50
s_addc_u32 s47, s47, 0
s_mov_b32 s73, 0
s_nop 1
s_branch .Lqa_hr7
.Lqa_e12:
s_cmp_lg_u32 s70, 0
s_cbranch_scc1 .Lqa_e13
v_or_b32 v30, s52, v30
v_or_b32 v31, s52, v31
v_or_b32 v32, s52, v32
v_or_b32 v33, s52, v33
s_mov_b32 s73, 999
s_nop 1
s_branch .Lqa_hr7
.Lqa_e13:
s_branch .Lqa_hr7
.Lqa_hk16:
s_cmp_lg_u32 s70, 18
s_cbranch_scc1 .Lqa_e18
s_add_u32 s42, s42, s50
s_addc_u32 s43, s43, 0
s_mov_b64 s[76:77], -1
s_mov_b64 s[46:47], s[44:45]
s_mov_b32 s73, 17
s_nop 1
s_branch .Lqa_hr17
.Lqa_e18:
s_cmp_lg_u32 s70, 17
s_cbranch_scc1 .Lqa_e19
s_mov_b32 s46, -18432
s_mov_b32 s47, -1
v_or_b32 v232, s52, v232
v_or_b32 v233, s52, v233
v_or_b32 v234, s52, v234
v_or_b32 v235, s52, v235
s_mov_b32 s73, 16
s_nop 1
s_branch .Lqa_hr17
.Lqa_e19:
s_cmp_lg_u32 s70, 16
s_cbranch_scc1 .Lqa_e20
v_mov_b32 v232, v30
v_mov_b32 v233, v31
v_mov_b32 v234, v32
v_mov_b32 v235, v33
s_mov_b32 s73, 15
s_nop 1
s_branch .Lqa_hr17
.Lqa_e20:
s_cmp_lg_u32 s70, 15
s_cbranch_scc1 .Lqa_e21
v_subrev_u32 v14, 18432, v14
s_mov_b32 s73, 2
s_nop 1
s_branch .Lqa_hr17
.Lqa_e21:
s_cmp_lg_u32 s70, 2
s_cbranch_scc1 .Lqa_e22
s_add_u32 s46, s46, s50
s_addc_u32 s47, s47, 0
s_mov_b32 s73, 0
s_nop 1
s_branch .Lqa_hr17
.Lqa_e22:
s_cmp_lg_u32 s70, 0
s_cbranch_scc1 .Lqa_e23
v_or_b32 v30, s52, v30
v_or_b32 v31, s52, v31
v_or_b32 v32, s52, v32
v_or_b32 v33, s52, v33
s_mov_b32 s73, 999
s_nop 1
s_branch .Lqa_hr17
.Lqa_e23:
s_branch .Lqa_hr17
.Lqa_sp28:
s_sleep 4
global_load_dword v51, v42, s[38:39] sc1
s_add_u32 s71, s71, 1
s_waitcnt vmcnt(0)
v_sub_u32 v51, v51, v44
v_cmp_lt_u32 vcc, 0x7f, v51
s_and_b64 vcc, vcc, s[76:77]
s_cbranch_vccz .Lqa_rt29
s_cmp_lt_u32 s71, 3000000
s_cbranch_scc1 .Lqa_sp28
s_branch .Lqa_rt29
.Lqa_sp30:
s_sleep 4
global_load_dword v51, v43, s[38:39] sc1
s_add_u32 s71, s71, 1
s_waitcnt vmcnt(0)
v_sub_u32 v51, v51, v44
v_cmp_lt_u32 vcc, 0x7f, v51
s_and_b64 vcc, vcc, s[76:77]
s_cbranch_vccz .Lqa_rt31
s_cmp_lt_u32 s71, 3000000
s_cbranch_scc1 .Lqa_sp30
s_branch .Lqa_rt31
.Lqa_hk26:
s_cmp_lg_u32 s70, 18
s_cbranch_scc1 .Lqa_e32
s_add_u32 s42, s42, s50
s_addc_u32 s43, s43, 0
s_mov_b64 s[76:77], -1
s_mov_b64 s[46:47], s[44:45]
s_mov_b32 s73, 17
s_nop 1
s_branch .Lqa_hr27
.Lqa_e32:
s_cmp_lg_u32 s70, 17
s_cbranch_scc1 .Lqa_e33
s_mov_b32 s46, -18432
s_mov_b32 s47, -1
v_or_b32 v200, s52, v200
v_or_b32 v201, s52, v201
v_or_b32 v202, s52, v202
v_or_b32 v203, s52, v203
s_mov_b32 s73, 16
s_nop 1
s_branch .Lqa_hr27
.Lqa_e33:
s_cmp_lg_u32 s70, 16
s_cbranch_scc1 .Lqa_e34
v_mov_b32 v200, v30
v_mov_b32 v201, v31
v_mov_b32 v202, v32
v_mov_b32 v203, v33
s_mov_b32 s73, 15
s_nop 1
s_branch .Lqa_hr27
.Lqa_e34:
s_cmp_lg_u32 s70, 15
s_cbranch_scc1 .Lqa_e35
v_subrev_u32 v14, 18432, v14
s_mov_b32 s73, 2
s_nop 1
s_branch .Lqa_hr27
.Lqa_e35:
s_cmp_lg_u32 s70, 2
s_cbranch_scc1 .Lqa_e36
v_add_u32 v42, s51, v42
v_add_u32 v44, -32, v44
s_add_u32 s46, s46, s50
s_addc_u32 s47, s47, 0
s_mov_b32 s73, 0
s_nop 1
s_branch .Lqa_hr27
.Lqa_e36:
s_cmp_lg_u32 s70, 0
s_cbranch_scc1 .Lqa_e37
v_or_b32 v30, s52, v30
v_or_b32 v31, s52, v31
v_or_b32 v32, s52, v32
v_or_b32 v33, s52, v33
s_mov_b32 s73, 999
s_nop 1
s_branch .Lqa_hr27
.Lqa_e37:
s_branch .Lqa_hr27
.Lqa_hk40:
s_cmp_lg_u32 s70, 18
s_cbranch_scc1 .Lqa_e42
s_add_u32 s42, s42, s50
s_addc_u32 s43, s43, 0
s_mov_b64 s[76:77], -1
s_mov_b64 s[46:47], s[44:45]
s_mov_b32 s73, 17
s_nop 1
s_branch .Lqa_hr41
.Lqa_e42:
s_cmp_lg_u32 s70, 17
s_cbranch_scc1 .Lqa_e43
s_mov_b32 s46, -18432
s_mov_b32 s47, -1
v_or_b32 v204, s52, v204
v_or_b32 v205, s52, v205
v_or_b32 v206, s52, v206
v_or_b32 v207, s52, v207
s_mov_b32 s73, 16
s_nop 1
s_branch .Lqa_hr41
.Lqa_e43:
s_cmp_lg_u32 s70, 16
s_cbranch_scc1 .Lqa_e44
v_mov_b32 v204, v30
v_mov_b32 v205, v31
v_mov_b32 v206, v32
v_mov_b32 v207, v33
s_mov_b32 s73, 15
s_nop 1
s_branch .Lqa_hr41
.Lqa_e44:
s_cmp_lg_u32 s70, 15
s_cbranch_scc1 .Lqa_e45
v_add_u32 v46, -1, v46
s_mov_b32 s73, 2
s_nop 1
s_branch .Lqa_hr41
.Lqa_e45:
s_cmp_lg_u32 s70, 2
s_cbranch_scc1 .Lqa_e46
s_add_u32 s46, s46, s50
s_addc_u32 s47, s47, 0
s_mov_b32 s73, 0
s_nop 1
s_branch .Lqa_hr41
.Lqa_e46:
s_cmp_lg_u32 s70, 0
s_cbranch_scc1 .Lqa_e47
v_or_b32 v30, s52, v30
v_or_b32 v31, s52, v31
)"
R"(v_or_b32 v32, s52, v32
v_or_b32 v33, s52, v33
s_mov_b32 s73, 999
s_nop 1
s_branch .Lqa_hr41
.Lqa_e47:
s_branch .Lqa_hr41
.Lqa_sp50:
s_sleep 4
global_load_dword v51, v47, s[38:39] sc1
s_add_u32 s71, s71, 1
s_waitcnt vmcnt(0)
v_sub_u32 v51, v51, v48
v_cmp_lt_u32 vcc, 0x7f, v51
s_cbranch_vccz .Lqa_rt51
s_cmp_lt_u32 s71, 3000000
s_cbranch_scc1 .Lqa_sp50
s_branch .Lqa_rt51
.Lqa_sp52:
s_sleep 4
global_load_dword v51, v47, s[38:39] sc1
s_add_u32 s71, s71, 1
s_waitcnt vmcnt(0)
v_sub_u32 v51, v51, v48
v_cmp_lt_u32 vcc, 0x7f, v51
s_cbranch_vccz .Lqa_rt53
s_cmp_lt_u32 s71, 3000000
s_cbranch_scc1 .Lqa_sp52
s_branch .Lqa_rt53
.Lqa_sp54:
s_sleep 4
global_load_dword v51, v47, s[38:39] sc1
s_add_u32 s71, s71, 1
s_waitcnt vmcnt(0)
v_sub_u32 v51, v51, v48
v_cmp_lt_u32 vcc, 0x7f, v51
s_cbranch_vccz .Lqa_rt55
s_cmp_lt_u32 s71, 3000000
s_cbranch_scc1 .Lqa_sp54
s_branch .Lqa_rt55
.Lqa_sp56:
s_sleep 4
global_load_dword v51, v47, s[38:39] sc1
s_add_u32 s71, s71, 1
s_waitcnt vmcnt(0)
v_sub_u32 v51, v51, v48
v_cmp_lt_u32 vcc, 0x7f, v51
s_cbranch_vccz .Lqa_rt57
s_cmp_lt_u32 s71, 3000000
s_cbranch_scc1 .Lqa_sp56
s_branch .Lqa_rt57
.Lrole1:
s_and_b32 s75, s36, 3
s_cmp_eq_u32 s75, 1
s_cbranch_scc1 .Lwo_w2
s_cmp_eq_u32 s75, 2
s_cbranch_scc1 .Lwo_w3
s_cmp_eq_u32 s75, 3
s_cbranch_scc1 .Lwo_w4
.Lwo_w1:
s_add_u32 m0, s46, 4096
s_nop 0
global_load_lds_dwordx4 v0, s[40:41] offset:-4096
global_load_lds_dwordx4 v1, s[40:41] offset:-3072
global_load_lds_dwordx4 v2, s[40:41] offset:-2048
global_load_lds_dwordx4 v3, s[40:41] offset:-1024
global_load_lds_dwordx4 v4, s[40:41] offset:0
global_load_lds_dwordx4 v5, s[40:41] offset:1024
global_load_lds_dwordx4 v6, s[40:41] offset:2048
global_load_lds_dwordx4 v7, s[40:41] offset:3072
s_add_u32 m0, s46, 12288
s_nop 0
global_load_lds_dwordx4 v8, s[40:41] offset:-4096
s_add_u32 s40, s40, 73728
s_addc_u32 s41, s41, 0
s_add_u32 m0, s46, 40960
s_nop 0
global_load_lds_dwordx4 v0, s[40:41] offset:-4096
global_load_lds_dwordx4 v1, s[40:41] offset:-3072
global_load_lds_dwordx4 v2, s[40:41] offset:-2048
global_load_lds_dwordx4 v3, s[40:41] offset:-1024
global_load_lds_dwordx4 v4, s[40:41] offset:0
global_load_lds_dwordx4 v5, s[40:41] offset:1024
global_load_lds_dwordx4 v6, s[40:41] offset:2048
global_load_lds_dwordx4 v7, s[40:41] offset:3072
s_add_u32 m0, s46, 49152
s_nop 0
global_load_lds_dwordx4 v8, s[40:41] offset:-4096
s_add_u32 s40, s40, 73728
s_addc_u32 s41, s41, 0
s_add_u32 m0, s46, 77824
s_nop 0
global_load_lds_dwordx4 v0, s[40:41] offset:-4096
global_load_lds_dwordx4 v1, s[40:41] offset:-3072
global_load_lds_dwordx4 v2, s[40:41] offset:-2048
global_load_lds_dwordx4 v3, s[40:41] offset:-1024
global_load_lds_dwordx4 v4, s[40:41] offset:0
global_load_lds_dwordx4 v5, s[40:41] offset:1024
global_load_lds_dwordx4 v6, s[40:41] offset:2048
global_load_lds_dwordx4 v7, s[40:41] offset:3072
s_add_u32 m0, s46, 86016
s_nop 0
global_load_lds_dwordx4 v8, s[40:41] offset:-4096
s_add_u32 s40, s40, 73728
s_addc_u32 s41, s41, 0
s_add_u32 m0, s46, 114688
s_nop 0
global_load_lds_dwordx4 v0, s[40:41] offset:-4096
global_load_lds_dwordx4 v1, s[40:41] offset:-3072
global_load_lds_dwordx4 v2, s[40:41] offset:-2048
global_load_lds_dwordx4 v3, s[40:41] offset:-1024
global_load_lds_dwordx4 v4, s[40:41] offset:0
global_load_lds_dwordx4 v5, s[40:41] offset:1024
global_load_lds_dwordx4 v6, s[40:41] offset:2048
global_load_lds_dwordx4 v7, s[40:41] offset:3072
s_add_u32 m0, s46, 122880
s_nop 0
global_load_lds_dwordx4 v8, s[40:41] offset:-4096
s_add_u32 s40, s40, 73728
s_addc_u32 s41, s41, 0
v_add_u32 v205, 0x10000, v9
v_add_u32 v206, 0x20000, v9
v_add_u32 v207, 0x10000, v10
v_add_u32 v208, 0x20000, v10
s_waitcnt lgkmcnt(0)
s_mul_i32 s37, s37, 0x9E3779B1
s_add_u32 s37, s37, 0x7F4A7C15
v_add_u32 v17, s37, v17
v_add_u32 v19, s37, v19
s_waitcnt vmcnt(27)
s_barrier
ds_read_b64_tr_b16 a[0:1], v9 offset:0
ds_read_b64_tr_b16 a[2:3], v9 offset:2304
ds_read_b64_tr_b16 a[4:5], v9 offset:8
ds_read_b64_tr_b16 a[6:7], v9 offset:2312
ds_read_b64_tr_b16 a[8:9], v9 offset:64
ds_read_b64_tr_b16 a[10:11], v9 offset:2368
ds_read_b64_tr_b16 a[12:13], v9 offset:72
ds_read_b64_tr_b16 a[14:15], v9 offset:2376
ds_read_b64_tr_b16 v[56:57], v10 offset:0
ds_read_b64_tr_b16 v[58:59], v10 offset:2304
ds_read_b64_tr_b16 v[60:61], v10 offset:8
ds_read_b64_tr_b16 v[62:63], v10 offset:2312
ds_read_b64_tr_b16 a[16:17], v9 offset:18432
ds_read_b64_tr_b16 a[18:19], v9 offset:20736
ds_read_b64_tr_b16 a[20:21], v9 offset:18440
ds_read_b64_tr_b16 a[22:23], v9 offset:20744
ds_read_b64_tr_b16 a[24:25], v9 offset:18496
ds_read_b64_tr_b16 a[26:27], v9 offset:20800
ds_read_b64_tr_b16 a[28:29], v9 offset:18504
ds_read_b64_tr_b16 a[30:31], v9 offset:20808
ds_read_b64_tr_b16 v[64:65], v10 offset:18432
ds_read_b64_tr_b16 v[66:67], v10 offset:20736
ds_read_b64_tr_b16 v[68:69], v10 offset:18440
ds_read_b64_tr_b16 v[70:71], v10 offset:20744
s_waitcnt lgkmcnt(0)
s_barrier
s_add_u32 m0, s46, 4096
s_nop 0
global_load_lds_dwordx4 v0, s[40:41] offset:-4096
global_load_lds_dwordx4 v1, s[40:41] offset:-3072
global_load_lds_dwordx4 v2, s[40:41] offset:-2048
global_load_lds_dwordx4 v3, s[40:41] offset:-1024
global_load_lds_dwordx4 v4, s[40:41] offset:0
global_load_lds_dwordx4 v5, s[40:41] offset:1024
global_load_lds_dwordx4 v6, s[40:41] offset:2048
global_load_lds_dwordx4 v7, s[40:41] offset:3072
s_add_u32 m0, s46, 12288
s_nop 0
global_load_lds_dwordx4 v8, s[40:41] offset:-4096
s_add_u32 s40, s40, 73728
s_addc_u32 s41, s41, 0
s_waitcnt vmcnt(27)
s_barrier
ds_read_b64_tr_b16 a[32:33], v9 offset:36864
ds_read_b64_tr_b16 a[34:35], v9 offset:39168
ds_read_b64_tr_b16 a[36:37], v9 offset:36872
ds_read_b64_tr_b16 a[38:39], v9 offset:39176
ds_read_b64_tr_b16 a[40:41], v9 offset:36928
ds_read_b64_tr_b16 a[42:43], v9 offset:39232
ds_read_b64_tr_b16 a[44:45], v9 offset:36936
ds_read_b64_tr_b16 a[46:47], v9 offset:39240
ds_read_b64_tr_b16 v[72:73], v10 offset:36864
ds_read_b64_tr_b16 v[74:75], v10 offset:39168
ds_read_b64_tr_b16 v[76:77], v10 offset:36872
ds_read_b64_tr_b16 v[78:79], v10 offset:39176
ds_read_b64_tr_b16 a[48:49], v9 offset:55296
ds_read_b64_tr_b16 a[50:51], v9 offset:57600
ds_read_b64_tr_b16 a[52:53], v9 offset:55304
ds_read_b64_tr_b16 a[54:55], v9 offset:57608
ds_read_b64_tr_b16 a[56:57], v9 offset:55360
ds_read_b64_tr_b16 a[58:59], v9 offset:57664
ds_read_b64_tr_b16 a[60:61], v9 offset:55368
ds_read_b64_tr_b16 a[62:63], v9 offset:57672
ds_read_b64_tr_b16 v[80:81], v10 offset:55296
ds_read_b64_tr_b16 v[82:83], v10 offset:57600
ds_read_b64_tr_b16 v[84:85], v10 offset:55304
ds_read_b64_tr_b16 v[86:87], v10 offset:57608
s_waitcnt lgkmcnt(0)
s_barrier
s_add_u32 m0, s46, 40960
s_nop 0
global_load_lds_dwordx4 v0, s[40:41] offset:-4096
global_load_lds_dwordx4 v1, s[40:41] offset:-3072
global_load_lds_dwordx4 v2, s[40:41] offset:-2048
global_load_lds_dwordx4 v3, s[40:41] offset:-1024
global_load_lds_dwordx4 v4, s[40:41] offset:0
global_load_lds_dwordx4 v5, s[40:41] offset:1024
global_load_lds_dwordx4 v6, s[40:41] offset:2048
global_load_lds_dwordx4 v7, s[40:41] offset:3072
s_add_u32 m0, s46, 49152
s_nop 0
global_load_lds_dwordx4 v8, s[40:41] offset:-4096
s_add_u32 s40, s40, 73728
s_addc_u32 s41, s41, 0
s_waitcnt vmcnt(27)
s_barrier
ds_read_b64_tr_b16 a[64:65], v205 offset:8192
ds_read_b64_tr_b16 a[66:67], v205 offset:10496
ds_read_b64_tr_b16 a[68:69], v205 offset:8200
ds_read_b64_tr_b16 a[70:71], v205 offset:10504
ds_read_b64_tr_b16 a[72:73], v205 offset:8256
ds_read_b64_tr_b16 a[74:75], v205 offset:10560
ds_read_b64_tr_b16 a[76:77], v205 offset:8264
ds_read_b64_tr_b16 a[78:79], v205 offset:10568
ds_read_b64_tr_b16 v[88:89], v207 offset:8192
ds_read_b64_tr_b16 v[90:91], v207 offset:10496
ds_read_b64_tr_b16 v[92:93], v207 offset:8200
ds_read_b64_tr_b16 v[94:95], v207 offset:10504
ds_read_b64_tr_b16 a[80:81], v205 offset:26624
ds_read_b64_tr_b16 a[82:83], v205 offset:28928
ds_read_b64_tr_b16 a[84:85], v205 offset:26632
ds_read_b64_tr_b16 a[86:87], v205 offset:28936
ds_read_b64_tr_b16 a[88:89], v205 offset:26688
ds_read_b64_tr_b16 a[90:91], v205 offset:28992
ds_read_b64_tr_b16 a[92:93], v205 offset:26696
ds_read_b64_tr_b16 a[94:95], v205 offset:29000
s_waitcnt lgkmcnt(0)
s_barrier
s_add_u32 m0, s46, 77824
s_nop 0
global_load_lds_dwordx4 v0, s[40:41] offset:-4096
global_load_lds_dwordx4 v1, s[40:41] offset:-3072
global_load_lds_dwordx4 v2, s[40:41] offset:-2048
global_load_lds_dwordx4 v3, s[40:41] offset:-1024
global_load_lds_dwordx4 v4, s[40:41] offset:0
global_load_lds_dwordx4 v5, s[40:41] offset:1024
global_load_lds_dwordx4 v6, s[40:41] offset:2048
global_load_lds_dwordx4 v7, s[40:41] offset:3072
s_add_u32 m0, s46, 86016
s_nop 0
global_load_lds_dwordx4 v8, s[40:41] offset:-4096
s_add_u32 s40, s40, 73728
s_addc_u32 s41, s41, 0
s_waitcnt vmcnt(27)
s_barrier
ds_read_b64_tr_b16 a[96:97], v205 offset:45056
ds_read_b64_tr_b16 a[98:99], v205 offset:47360
ds_read_b64_tr_b16 a[100:101], v205 offset:45064
ds_read_b64_tr_b16 a[102:103], v205 offset:47368
ds_read_b64_tr_b16 a[104:105], v205 offset:45120
ds_read_b64_tr_b16 a[106:107], v205 offset:47424
ds_read_b64_tr_b16 a[108:109], v205 offset:45128
ds_read_b64_tr_b16 a[110:111], v205 offset:47432
ds_read_b64_tr_b16 a[112:113], v205 offset:63488
ds_read_b64_tr_b16 a[114:115], v206 offset:256
ds_read_b64_tr_b16 a[116:117], v205 offset:63496
ds_read_b64_tr_b16 a[118:119], v206 offset:264
ds_read_b64_tr_b16 a[120:121], v205 offset:63552
ds_read_b64_tr_b16 a[122:123], v206 offset:320
ds_read_b64_tr_b16 a[124:125], v205 offset:63560
ds_read_b64_tr_b16 a[126:127], v206 offset:328
s_waitcnt lgkmcnt(0)
s_barrier
s_add_u32 m0, s46, 114688
s_nop 0
global_load_lds_dwordx4 v0, s[40:41] offset:-4096
global_load_lds_dwordx4 v1, s[40:41] offset:-3072
global_load_lds_dwordx4 v2, s[40:41] offset:-2048
global_load_lds_dwordx4 v3, s[40:41] offset:-1024
global_load_lds_dwordx4 v4, s[40:41] offset:0
global_load_lds_dwordx4 v5, s[40:41] offset:1024
global_load_lds_dwordx4 v6, s[40:41] offset:2048
global_load_lds_dwordx4 v7, s[40:41] offset:3072
s_add_u32 m0, s46, 122880
s_nop 0
global_load_lds_dwordx4 v8, s[40:41] offset:-4096
s_add_u32 s40, s40, 73728
s_addc_u32 s41, s41, 0
s_waitcnt vmcnt(27)
s_barrier
ds_read_b64_tr_b16 a[128:129], v9 offset:0
ds_read_b64_tr_b16 a[130:131], v9 offset:2304
ds_read_b64_tr_b16 a[132:133], v9 offset:8
ds_read_b64_tr_b16 a[134:135], v9 offset:2312
ds_read_b64_tr_b16 a[136:137], v9 offset:64
ds_read_b64_tr_b16 a[138:139], v9 offset:2368
ds_read_b64_tr_b16 a[140:141], v9 offset:72
ds_read_b64_tr_b16 a[142:143], v9 offset:2376
ds_read_b64_tr_b16 a[144:145], v9 offset:18432
ds_read_b64_tr_b16 a[146:147], v9 offset:20736
ds_read_b64_tr_b16 a[148:149], v9 offset:18440
ds_read_b64_tr_b16 a[150:151], v9 offset:20744
ds_read_b64_tr_b16 a[152:153], v9 offset:18496
ds_read_b64_tr_b16 a[154:155], v9 offset:20800
ds_read_b64_tr_b16 a[156:157], v9 offset:18504
ds_read_b64_tr_b16 a[158:159], v9 offset:20808
s_waitcnt lgkmcnt(0)
s_barrier
s_add_u32 m0, s46, 4096
s_nop 0
global_load_lds_dwordx4 v0, s[40:41] offset:-4096
global_load_lds_dwordx4 v1, s[40:41] offset:-3072
global_load_lds_dwordx4 v2, s[40:41] offset:-2048
global_load_lds_dwordx4 v3, s[40:41] offset:-1024
global_load_lds_dwordx4 v4, s[40:41] offset:0
global_load_lds_dwordx4 v5, s[40:41] offset:1024
global_load_lds_dwordx4 v6, s[40:41] offset:2048
global_load_lds_dwordx4 v7, s[40:41] offset:3072
s_add_u32 m0, s46, 12288
s_nop 0
global_load_lds_dwordx4 v8, s[40:41] offset:-4096
s_add_u32 s40, s40, 73728
s_addc_u32 s41, s41, 0
s_waitcnt vmcnt(27)
s_barrier
ds_read_b64_tr_b16 a[160:161], v9 offset:36864
ds_read_b64_tr_b16 a[162:163], v9 offset:39168
ds_read_b64_tr_b16 a[164:165], v9 offset:36872
ds_read_b64_tr_b16 a[166:167], v9 offset:39176
ds_read_b64_tr_b16 a[168:169], v9 offset:36928
ds_read_b64_tr_b16 a[170:171], v9 offset:39232
ds_read_b64_tr_b16 a[172:173], v9 offset:36936
ds_read_b64_tr_b16 a[174:175], v9 offset:39240
ds_read_b64_tr_b16 a[176:177], v9 offset:55296
ds_read_b64_tr_b16 a[178:179], v9 offset:57600
ds_read_b64_tr_b16 a[180:181], v9 offset:55304
ds_read_b64_tr_b16 a[182:183], v9 offset:57608
ds_read_b64_tr_b16 a[184:185], v9 offset:55360
ds_read_b64_tr_b16 a[186:187], v9 offset:57664
ds_read_b64_tr_b16 a[188:189], v9 offset:55368
ds_read_b64_tr_b16 a[190:191], v9 offset:57672
s_waitcnt vmcnt(18)
s_barrier
ds_read_b64_tr_b16 a[192:193], v205 offset:8192
ds_read_b64_tr_b16 a[194:195], v205 offset:10496
ds_read_b64_tr_b16 a[196:197], v205 offset:8200
ds_read_b64_tr_b16 a[198:199], v205 offset:10504
ds_read_b64_tr_b16 a[200:201], v205 offset:8256
ds_read_b64_tr_b16 a[202:203], v205 offset:10560
ds_read_b64_tr_b16 a[204:205], v205 offset:8264
ds_read_b64_tr_b16 a[206:207], v205 offset:10568
ds_read_b64_tr_b16 a[208:209], v205 offset:26624
ds_read_b64_tr_b16 a[210:211], v205 offset:28928
ds_read_b64_tr_b16 a[212:213], v205 offset:26632
ds_read_b64_tr_b16 a[214:215], v205 offset:28936
ds_read_b64_tr_b16 a[216:217], v205 offset:26688
ds_read_b64_tr_b16 a[218:219], v205 offset:28992
ds_read_b64_tr_b16 a[220:221], v205 offset:26696
ds_read_b64_tr_b16 a[222:223], v205 offset:29000
s_waitcnt vmcnt(9)
s_barrier
ds_read_b64_tr_b16 a[224:225], v205 offset:45056
ds_read_b64_tr_b16 a[226:227], v205 offset:47360
ds_read_b64_tr_b16 a[228:229], v205 offset:45064
ds_read_b64_tr_b16 a[230:231], v205 offset:47368
ds_read_b64_tr_b16 a[232:233], v205 offset:45120
ds_read_b64_tr_b16 a[234:235], v205 offset:47424
ds_read_b64_tr_b16 a[236:237], v205 offset:45128
ds_read_b64_tr_b16 a[238:239], v205 offset:47432
ds_read_b64_tr_b16 a[240:241], v205 offset:63488
ds_read_b64_tr_b16 a[242:243], v206 offset:256
ds_read_b64_tr_b16 a[244:245], v205 offset:63496
ds_read_b64_tr_b16 a[246:247], v206 offset:264
ds_read_b64_tr_b16 a[248:249], v205 offset:63552
ds_read_b64_tr_b16 a[250:251], v206 offset:320
ds_read_b64_tr_b16 a[252:253], v205 offset:63560
ds_read_b64_tr_b16 a[254:255], v206 offset:328
s_waitcnt vmcnt(0)
s_barrier
ds_read_b64_tr_b16 v[24:25], v9 offset:0
ds_read_b64_tr_b16 v[26:27], v9 offset:2304
ds_read_b64_tr_b16 v[28:29], v9 offset:8
ds_read_b64_tr_b16 v[30:31], v9 offset:2312
ds_read_b64_tr_b16 v[32:33], v9 offset:64
ds_read_b64_tr_b16 v[34:35], v9 offset:2368
ds_read_b64_tr_b16 v[36:37], v9 offset:72
ds_read_b64_tr_b16 v[38:39], v9 offset:2376
ds_read_b64_tr_b16 v[40:41], v9 offset:18432
ds_read_b64_tr_b16 v[42:43], v9 offset:20736
ds_read_b64_tr_b16 v[44:45], v9 offset:18440
ds_read_b64_tr_b16 v[46:47], v9 offset:20744
ds_read_b64_tr_b16 v[48:49], v9 offset:18496
ds_read_b64_tr_b16 v[50:51], v9 offset:20800
ds_read_b64_tr_b16 v[52:53], v9 offset:18504
ds_read_b64_tr_b16 v[54:55], v9 offset:20808
s_waitcnt lgkmcnt(0)
s_barrier
v_mov_b32 v202, v11
s_barrier
)"
R"(s_mov_b32 s70, 32
s_waitcnt vmcnt(0) lgkmcnt(0)
s_nop 7
.p2align 6
.Lwo_top7:
s_waitcnt lgkmcnt(2)
v_mfma_f32_16x16x32_bf16 v[96:99], a[0:3], v[136:139], v[96:99]
ds_read_b128 v[148:151], v20 offset:3072
v_mfma_f32_16x16x32_bf16 v[100:103], a[4:7], v[136:139], v[100:103]
s_cmp_eq_u32 s70, 16
s_cbranch_scc1 .Lwo_hk5
.Lwo_hr6:
v_mfma_f32_16x16x32_bf16 v[104:107], a[8:11], v[136:139], v[104:107]
v_mfma_f32_16x16x32_bf16 v[108:111], a[12:15], v[136:139], v[108:111]
v_cvt_pk_bf16_f32 v164, v112, v113
v_cvt_pk_bf16_f32 v165, v114, v115
v_mfma_f32_16x16x32_bf16 v[128:131], v[56:59], v[136:139], v[128:131]
v_cvt_pk_bf16_f32 v166, v116, v117
v_cvt_pk_bf16_f32 v167, v118, v119
v_mfma_f32_16x16x32_bf16 v[132:135], v[60:63], v[136:139], v[132:135]
s_waitcnt lgkmcnt(2)
v_mfma_f32_16x16x32_bf16 v[96:99], a[16:19], v[140:143], v[96:99]
ds_read_b128 v[136:139], v20 offset:4096
v_mfma_f32_16x16x32_bf16 v[100:103], a[20:23], v[140:143], v[100:103]
global_store_dwordx4 v13, v[164:167], s[38:39] offset:0 sc1
v_mfma_f32_16x16x32_bf16 v[104:107], a[24:27], v[140:143], v[104:107]
v_cvt_pk_bf16_f32 v168, v120, v121
v_cvt_pk_bf16_f32 v169, v122, v123
v_mfma_f32_16x16x32_bf16 v[108:111], a[28:31], v[140:143], v[108:111]
v_cvt_pk_bf16_f32 v170, v124, v125
v_cvt_pk_bf16_f32 v171, v126, v127
v_mfma_f32_16x16x32_bf16 v[128:131], v[64:67], v[140:143], v[128:131]
global_store_dwordx4 v13, v[168:171], s[38:39] offset:1024 sc1
v_mfma_f32_16x16x32_bf16 v[132:135], v[68:71], v[140:143], v[132:135]
s_waitcnt lgkmcnt(2)
v_add_u32 v13, 18688, v13
v_add_f32 v200, v112, v113
v_mfma_f32_16x16x32_bf16 v[96:99], a[32:35], v[144:147], v[96:99]
ds_read_b128 v[140:143], v20 offset:5120
v_mul_f32 v201, v112, v112
v_mfma_f32_16x16x32_bf16 v[100:103], a[36:39], v[144:147], v[100:103]
v_fmac_f32 v201, v113, v113
v_add_f32 v200, v200, v114
v_mfma_f32_16x16x32_bf16 v[104:107], a[40:43], v[144:147], v[104:107]
v_fmac_f32 v201, v114, v114
v_add_f32 v200, v200, v115
v_mfma_f32_16x16x32_bf16 v[108:111], a[44:47], v[144:147], v[108:111]
v_fmac_f32 v201, v115, v115
v_add_f32 v200, v200, v116
v_mfma_f32_16x16x32_bf16 v[128:131], v[72:75], v[144:147], v[128:131]
v_fmac_f32 v201, v116, v116
v_add_f32 v200, v200, v117
v_mfma_f32_16x16x32_bf16 v[132:135], v[76:79], v[144:147], v[132:135]
s_xor_b32 s47, s47, 0x10000
v_fmac_f32 v201, v117, v117
s_waitcnt lgkmcnt(2)
v_mfma_f32_16x16x32_bf16 v[96:99], a[48:51], v[148:151], v[96:99]
s_add_u32 m0, s47, 0
ds_read_b128 v[144:147], v20 offset:6144
v_add_f32 v200, v200, v118
v_mfma_f32_16x16x32_bf16 v[100:103], a[52:55], v[148:151], v[100:103]
v_fmac_f32 v201, v118, v118
v_add_f32 v200, v200, v119
v_mfma_f32_16x16x32_bf16 v[104:107], a[56:59], v[148:151], v[104:107]
global_load_lds_dwordx4 v202, s[38:39] offset:-2048 sc1
v_fmac_f32 v201, v119, v119
v_mfma_f32_16x16x32_bf16 v[108:111], a[60:63], v[148:151], v[108:111]
v_add_f32 v200, v200, v120
v_fmac_f32 v201, v120, v120
v_mfma_f32_16x16x32_bf16 v[128:131], v[80:83], v[148:151], v[128:131]
v_add_f32 v200, v200, v121
v_fmac_f32 v201, v121, v121
v_mfma_f32_16x16x32_bf16 v[132:135], v[84:87], v[148:151], v[132:135]
s_waitcnt lgkmcnt(2)
v_add_f32 v200, v200, v122
v_fmac_f32 v201, v122, v122
v_mfma_f32_16x16x32_bf16 v[96:99], a[64:67], v[136:139], v[96:99]
ds_read_b128 v[148:151], v20 offset:7168
v_add_f32 v200, v200, v123
v_mfma_f32_16x16x32_bf16 v[100:103], a[68:71], v[136:139], v[100:103]
v_fmac_f32 v201, v123, v123
v_add_f32 v200, v200, v124
v_mfma_f32_16x16x32_bf16 v[104:107], a[72:75], v[136:139], v[104:107]
v_fmac_f32 v201, v124, v124
v_add_f32 v200, v200, v125
v_mfma_f32_16x16x32_bf16 v[108:111], a[76:79], v[136:139], v[108:111]
global_load_lds_dwordx4 v202, s[38:39] offset:-1024 sc1
v_fmac_f32 v201, v125, v125
v_mfma_f32_16x16x32_bf16 v[128:131], v[88:91], v[136:139], v[128:131]
v_add_f32 v200, v200, v126
v_fmac_f32 v201, v126, v126
v_mfma_f32_16x16x32_bf16 v[132:135], v[92:95], v[136:139], v[132:135]
s_waitcnt lgkmcnt(2)
v_add_f32 v200, v200, v127
v_fmac_f32 v201, v127, v127
v_mfma_f32_16x16x32_bf16 v[96:99], a[80:83], v[140:143], v[96:99]
ds_read_b128 v[136:139], v20 offset:8192
v_mfma_f32_16x16x32_bf16 v[100:103], a[84:87], v[140:143], v[100:103]
ds_write_b64 v22, v[200:201] offset:59392
v_mfma_f32_16x16x32_bf16 v[104:107], a[88:91], v[140:143], v[104:107]
v_mfma_f32_16x16x32_bf16 v[108:111], a[92:95], v[140:143], v[108:111]
s_waitcnt lgkmcnt(3)
v_mfma_f32_16x16x32_bf16 v[96:99], a[96:99], v[144:147], v[96:99]
ds_read_b128 v[140:143], v20 offset:9216
global_load_lds_dwordx4 v202, s[38:39] offset:0 sc1
v_mfma_f32_16x16x32_bf16 v[100:103], a[100:103], v[144:147], v[100:103]
ds_write_b128 v21, v[128:131] offset:40960
v_mfma_f32_16x16x32_bf16 v[104:107], a[104:107], v[144:147], v[104:107]
ds_write_b128 v21, v[132:135] offset:41984
v_mfma_f32_16x16x32_bf16 v[108:111], a[108:111], v[144:147], v[108:111]
s_waitcnt lgkmcnt(5)
v_mfma_f32_16x16x32_bf16 v[96:99], a[112:115], v[148:151], v[96:99]
ds_read_b128 v[144:147], v20 offset:10240
v_mfma_f32_16x16x32_bf16 v[100:103], a[116:119], v[148:151], v[100:103]
v_mfma_f32_16x16x32_bf16 v[104:107], a[120:123], v[148:151], v[104:107]
v_mfma_f32_16x16x32_bf16 v[108:111], a[124:127], v[148:151], v[108:111]
s_waitcnt lgkmcnt(5)
global_load_lds_dwordx4 v202, s[38:39] offset:1024 sc1
v_mfma_f32_16x16x32_bf16 v[96:99], a[128:131], v[136:139], v[96:99]
ds_read_b128 v[148:151], v20 offset:11264
v_mfma_f32_16x16x32_bf16 v[100:103], a[132:135], v[136:139], v[100:103]
v_mfma_f32_16x16x32_bf16 v[104:107], a[136:139], v[136:139], v[104:107]
s_waitcnt vmcnt(9)
v_lshlrev_b32 v112, 16, v152
v_and_b32 v113, 0xffff0000, v152
v_mfma_f32_16x16x32_bf16 v[108:111], a[140:143], v[136:139], v[108:111]
s_waitcnt lgkmcnt(4)
v_lshlrev_b32 v114, 16, v153
v_and_b32 v115, 0xffff0000, v153
v_mfma_f32_16x16x32_bf16 v[96:99], a[144:147], v[140:143], v[96:99]
ds_read_b128 v[136:139], v20 offset:12288
v_lshlrev_b32 v116, 16, v154
v_mfma_f32_16x16x32_bf16 v[100:103], a[148:151], v[140:143], v[100:103]
v_and_b32 v117, 0xffff0000, v154
v_lshlrev_b32 v118, 16, v155
v_mfma_f32_16x16x32_bf16 v[104:107], a[152:155], v[140:143], v[104:107]
global_load_lds_dwordx4 v202, s[38:39] offset:2048 sc1
v_add_u32 v11, 18432, v11
v_mfma_f32_16x16x32_bf16 v[108:111], a[156:159], v[140:143], v[108:111]
s_waitcnt lgkmcnt(2)
v_min_u32 v202, v11, v12
v_and_b32 v119, 0xffff0000, v155
v_mfma_f32_16x16x32_bf16 v[96:99], a[160:163], v[144:147], v[96:99]
s_waitcnt vmcnt(9)
ds_read_b128 v[140:143], v20 offset:13312
v_lshlrev_b32 v120, 16, v156
v_mfma_f32_16x16x32_bf16 v[100:103], a[164:167], v[144:147], v[100:103]
v_and_b32 v121, 0xffff0000, v156
v_lshlrev_b32 v122, 16, v157
v_mfma_f32_16x16x32_bf16 v[104:107], a[168:171], v[144:147], v[104:107]
v_and_b32 v123, 0xffff0000, v157
v_lshlrev_b32 v124, 16, v158
v_mfma_f32_16x16x32_bf16 v[108:111], a[172:175], v[144:147], v[108:111]
s_waitcnt lgkmcnt(2)
v_and_b32 v125, 0xffff0000, v158
v_lshlrev_b32 v126, 16, v159
v_mfma_f32_16x16x32_bf16 v[96:99], a[176:179], v[148:151], v[96:99]
ds_read_b128 v[144:147], v20 offset:14336
v_and_b32 v127, 0xffff0000, v159
v_mfma_f32_16x16x32_bf16 v[100:103], a[180:183], v[148:151], v[100:103]
s_waitcnt vmcnt(8)
v_lshlrev_b32 v128, 16, v160
v_and_b32 v129, 0xffff0000, v160
v_mfma_f32_16x16x32_bf16 v[104:107], a[184:187], v[148:151], v[104:107]
v_lshlrev_b32 v130, 16, v161
v_and_b32 v131, 0xffff0000, v161
v_mfma_f32_16x16x32_bf16 v[108:111], a[188:191], v[148:151], v[108:111]
s_waitcnt lgkmcnt(2)
v_lshlrev_b32 v132, 16, v162
v_and_b32 v133, 0xffff0000, v162
v_mfma_f32_16x16x32_bf16 v[96:99], a[192:195], v[136:139], v[96:99]
ds_read_b128 v[148:151], v20 offset:15360
v_lshlrev_b32 v134, 16, v163
v_mfma_f32_16x16x32_bf16 v[100:103], a[196:199], v[136:139], v[100:103]
v_and_b32 v135, 0xffff0000, v163
v_mfma_f32_16x16x32_bf16 v[104:107], a[200:203], v[136:139], v[104:107]
global_load_dwordx4 v[152:155], v15, s[42:43] offset:0 sc1
v_mfma_f32_16x16x32_bf16 v[108:111], a[204:207], v[136:139], v[108:111]
s_waitcnt lgkmcnt(2)
v_mfma_f32_16x16x32_bf16 v[96:99], a[208:211], v[140:143], v[96:99]
ds_read_b128 v[136:139], v20 offset:16384
v_mfma_f32_16x16x32_bf16 v[100:103], a[212:215], v[140:143], v[100:103]
global_load_dwordx4 v[156:159], v15, s[42:43] offset:64 sc1
v_mfma_f32_16x16x32_bf16 v[104:107], a[216:219], v[140:143], v[104:107]
v_mfma_f32_16x16x32_bf16 v[108:111], a[220:223], v[140:143], v[108:111]
s_waitcnt lgkmcnt(2)
v_mfma_f32_16x16x32_bf16 v[96:99], a[224:227], v[144:147], v[96:99]
ds_read_b128 v[140:143], v20 offset:17408
global_load_dwordx4 v[160:163], v15, s[42:43] offset:512 sc1
v_mfma_f32_16x16x32_bf16 v[100:103], a[228:231], v[144:147], v[100:103]
s_sub_u32 s42, s42, 18432
s_subb_u32 s43, s43, 0
v_mfma_f32_16x16x32_bf16 v[104:107], a[232:235], v[144:147], v[104:107]
v_mfma_f32_16x16x32_bf16 v[108:111], a[236:239], v[144:147], v[108:111]
s_waitcnt lgkmcnt(2)
v_mfma_f32_16x16x32_bf16 v[96:99], a[240:243], v[148:151], v[96:99]
v_mfma_f32_16x16x32_bf16 v[100:103], a[244:247], v[148:151], v[100:103]
v_mfma_f32_16x16x32_bf16 v[104:107], a[248:251], v[148:151], v[104:107]
v_mfma_f32_16x16x32_bf16 v[108:111], a[252:255], v[148:151], v[108:111]
s_waitcnt vmcnt(8)
s_barrier
s_waitcnt lgkmcnt(1)
v_mfma_f32_16x16x32_bf16 v[96:99], v[24:27], v[136:139], v[96:99]
ds_read_b128 v[144:147], v20 offset:18432
ds_read_b128 v[148:151], v20 offset:19456
v_mfma_f32_16x16x32_bf16 v[100:103], v[28:31], v[136:139], v[100:103]
global_store_dword v18, v19, s[38:39] sc1
v_add_u32 v19, 1, v19
v_mfma_f32_16x16x32_bf16 v[104:107], v[32:35], v[136:139], v[104:107]
v_mfma_f32_16x16x32_bf16 v[108:111], v[36:39], v[136:139], v[108:111]
s_waitcnt lgkmcnt(2)
v_mfma_f32_16x16x32_bf16 v[96:99], v[40:43], v[140:143], v[96:99]
ds_read_b128 v[136:139], v20 offset:20480
v_mfma_f32_16x16x32_bf16 v[100:103], v[44:47], v[140:143], v[100:103]
v_mfma_f32_16x16x32_bf16 v[104:107], v[48:51], v[140:143], v[104:107]
v_mfma_f32_16x16x32_bf16 v[108:111], v[52:55], v[140:143], v[108:111]
s_waitcnt lgkmcnt(2)
v_mfma_f32_16x16x32_bf16 v[112:115], a[0:3], v[144:147], v[112:115]
ds_read_b128 v[140:143], v20 offset:21504
v_mfma_f32_16x16x32_bf16 v[116:119], a[4:7], v[144:147], v[116:119]
v_mfma_f32_16x16x32_bf16 v[120:123], a[8:11], v[144:147], v[120:123]
v_mfma_f32_16x16x32_bf16 v[124:127], a[12:15], v[144:147], v[124:127]
v_cvt_pk_bf16_f32 v164, v96, v97
v_cvt_pk_bf16_f32 v165, v98, v99
v_mfma_f32_16x16x32_bf16 v[128:131], v[56:59], v[144:147], v[128:131]
v_cvt_pk_bf16_f32 v166, v100, v101
v_cvt_pk_bf16_f32 v167, v102, v103
v_mfma_f32_16x16x32_bf16 v[132:135], v[60:63], v[144:147], v[132:135]
s_waitcnt lgkmcnt(2)
v_mfma_f32_16x16x32_bf16 v[112:115], a[16:19], v[148:151], v[112:115]
ds_read_b128 v[144:147], v20 offset:22528
v_mfma_f32_16x16x32_bf16 v[116:119], a[20:23], v[148:151], v[116:119]
global_store_dwordx4 v13, v[164:167], s[38:39] offset:0 sc1
v_mfma_f32_16x16x32_bf16 v[120:123], a[24:27], v[148:151], v[120:123]
v_cvt_pk_bf16_f32 v168, v104, v105
v_cvt_pk_bf16_f32 v169, v106, v107
v_mfma_f32_16x16x32_bf16 v[124:127], a[28:31], v[148:151], v[124:127]
v_cvt_pk_bf16_f32 v170, v108, v109
v_cvt_pk_bf16_f32 v171, v110, v111
v_mfma_f32_16x16x32_bf16 v[128:131], v[64:67], v[148:151], v[128:131]
global_store_dwordx4 v13, v[168:171], s[38:39] offset:1024 sc1
v_mfma_f32_16x16x32_bf16 v[132:135], v[68:71], v[148:151], v[132:135]
s_waitcnt lgkmcnt(2)
v_add_u32 v13, 18688, v13
v_add_f32 v200, v96, v97
v_mfma_f32_16x16x32_bf16 v[112:115], a[32:35], v[136:139], v[112:115]
ds_read_b128 v[148:151], v20 offset:23552
v_mul_f32 v201, v96, v96
v_mfma_f32_16x16x32_bf16 v[116:119], a[36:39], v[136:139], v[116:119]
v_fmac_f32 v201, v97, v97
v_add_f32 v200, v200, v98
v_mfma_f32_16x16x32_bf16 v[120:123], a[40:43], v[136:139], v[120:123]
v_fmac_f32 v201, v98, v98
v_add_f32 v200, v200, v99
v_mfma_f32_16x16x32_bf16 v[124:127], a[44:47], v[136:139], v[124:127]
v_fmac_f32 v201, v99, v99
v_add_f32 v200, v200, v100
v_mfma_f32_16x16x32_bf16 v[128:131], v[72:75], v[136:139], v[128:131]
v_fmac_f32 v201, v100, v100
v_add_f32 v200, v200, v101
v_mfma_f32_16x16x32_bf16 v[132:135], v[76:79], v[136:139], v[132:135]
s_waitcnt lgkmcnt(2)
v_fmac_f32 v201, v101, v101
v_add_f32 v200, v200, v102
v_mfma_f32_16x16x32_bf16 v[112:115], a[48:51], v[140:143], v[112:115]
s_add_u32 m0, s47, 18432
ds_read_b128 v[136:139], v20 offset:24576
v_fmac_f32 v201, v102, v102
v_mfma_f32_16x16x32_bf16 v[116:119], a[52:55], v[140:143], v[116:119]
v_add_f32 v200, v200, v103
v_fmac_f32 v201, v103, v103
v_mfma_f32_16x16x32_bf16 v[120:123], a[56:59], v[140:143], v[120:123]
global_load_lds_dwordx4 v202, s[38:39] offset:-2048 sc1
v_add_f32 v200, v200, v104
v_mfma_f32_16x16x32_bf16 v[124:127], a[60:63], v[140:143], v[124:127]
v_fmac_f32 v201, v104, v104
v_add_f32 v200, v200, v105
v_mfma_f32_16x16x32_bf16 v[128:131], v[80:83], v[140:143], v[128:131]
v_fmac_f32 v201, v105, v105
v_add_f32 v200, v200, v106
v_mfma_f32_16x16x32_bf16 v[132:135], v[84:87], v[140:143], v[132:135]
s_waitcnt lgkmcnt(2)
v_fmac_f32 v201, v106, v106
v_add_f32 v200, v200, v107
v_mfma_f32_16x16x32_bf16 v[112:115], a[64:67], v[144:147], v[112:115]
ds_read_b128 v[140:143], v20 offset:25600
v_fmac_f32 v201, v107, v107
v_mfma_f32_16x16x32_bf16 v[116:119], a[68:71], v[144:147], v[116:119]
v_add_f32 v200, v200, v108
v_fmac_f32 v201, v108, v108
v_mfma_f32_16x16x32_bf16 v[120:123], a[72:75], v[144:147], v[120:123]
v_add_f32 v200, v200, v109
v_fmac_f32 v201, v109, v109
v_mfma_f32_16x16x32_bf16 v[124:127], a[76:79], v[144:147], v[124:127]
global_load_lds_dwordx4 v202, s[38:39] offset:-1024 sc1
v_add_f32 v200, v200, v110
v_mfma_f32_16x16x32_bf16 v[128:131], v[88:91], v[144:147], v[128:131]
v_fmac_f32 v201, v110, v110
v_add_f32 v200, v200, v111
v_mfma_f32_16x16x32_bf16 v[132:135], v[92:95], v[144:147], v[132:135]
s_waitcnt lgkmcnt(2)
v_fmac_f32 v201, v111, v111
v_mfma_f32_16x16x32_bf16 v[112:115], a[80:83], v[148:151], v[112:115]
ds_read_b128 v[144:147], v20 offset:26624
v_mfma_f32_16x16x32_bf16 v[116:119], a[84:87], v[148:151], v[116:119]
ds_write_b64 v22, v[200:201] offset:57344
v_mfma_f32_16x16x32_bf16 v[120:123], a[88:91], v[148:151], v[120:123]
v_mfma_f32_16x16x32_bf16 v[124:127], a[92:95], v[148:151], v[124:127]
s_waitcnt lgkmcnt(3)
v_mfma_f32_16x16x32_bf16 v[112:115], a[96:99], v[136:139], v[112:115]
ds_read_b128 v[148:151], v20 offset:27648
global_load_lds_dwordx4 v202, s[38:39] offset:0 sc1
v_mfma_f32_16x16x32_bf16 v[116:119], a[100:103], v[136:139], v[116:119]
ds_write_b128 v21, v[128:131] offset:47104
v_mfma_f32_16x16x32_bf16 v[120:123], a[104:107], v[136:139], v[120:123]
ds_write_b128 v21, v[132:135] offset:48128
v_mfma_f32_16x16x32_bf16 v[124:127], a[108:111], v[136:139], v[124:127]
s_waitcnt lgkmcnt(5)
v_mfma_f32_16x16x32_bf16 v[112:115], a[112:115], v[140:143], v[112:115]
ds_read_b128 v[136:139], v20 offset:28672
v_mfma_f32_16x16x32_bf16 v[116:119], a[116:119], v[140:143], v[116:119]
v_mfma_f32_16x16x32_bf16 v[120:123], a[120:123], v[140:143], v[120:123]
v_mfma_f32_16x16x32_bf16 v[124:127], a[124:127], v[140:143], v[124:127]
s_waitcnt lgkmcnt(5)
global_load_lds_dwordx4 v202, s[38:39] offset:1024 sc1
v_mfma_f32_16x16x32_bf16 v[112:115], a[128:131], v[144:147], v[112:115]
ds_read_b128 v[140:143], v20 offset:29696
v_mfma_f32_16x16x32_bf16 v[116:119], a[132:135], v[144:147], v[116:119]
v_mfma_f32_16x16x32_bf16 v[120:123], a[136:139], v[144:147], v[120:123]
s_waitcnt vmcnt(9)
v_lshlrev_b32 v96, 16, v152
v_and_b32 v97, 0xffff0000, v152
v_mfma_f32_16x16x32_bf16 v[124:127], a[140:143], v[144:147], v[124:127]
s_waitcnt lgkmcnt(4)
v_lshlrev_b32 v98, 16, v153
v_and_b32 v99, 0xffff0000, v153
v_mfma_f32_16x16x32_bf16 v[112:115], a[144:147], v[148:151], v[112:115]
ds_read_b128 v[144:147], v20 offset:30720
v_lshlrev_b32 v100, 16, v154
v_mfma_f32_16x16x32_bf16 v[116:119], a[148:151], v[148:151], v[116:119]
v_and_b32 v101, 0xffff0000, v154
v_lshlrev_b32 v102, 16, v155
v_mfma_f32_16x16x32_bf16 v[120:123], a[152:155], v[148:151], v[120:123]
global_load_lds_dwordx4 v202, s[38:39] offset:2048 sc1
v_add_u32 v11, 18432, v11
v_mfma_f32_16x16x32_bf16 v[124:127], a[156:159], v[148:151], v[124:127]
s_waitcnt lgkmcnt(2)
v_min_u32 v202, v11, v12
v_and_b32 v103, 0xffff0000, v155
v_mfma_f32_16x16x32_bf16 v[112:115], a[160:163], v[136:139], v[112:115]
s_waitcnt vmcnt(9)
ds_read_b128 v[148:151], v20 offset:31744
v_lshlrev_b32 v104, 16, v156
v_mfma_f32_16x16x32_bf16 v[116:119], a[164:167], v[136:139], v[116:119]
v_and_b32 v105, 0xffff0000, v156
v_lshlrev_b32 v106, 16, v157
v_mfma_f32_16x16x32_bf16 v[120:123], a[168:171], v[136:139], v[120:123]
v_and_b32 v107, 0xffff0000, v157
v_lshlrev_b32 v108, 16, v158
v_mfma_f32_16x16x32_bf16 v[124:127], a[172:175], v[136:139], v[124:127]
s_waitcnt lgkmcnt(2)
v_and_b32 v109, 0xffff0000, v158
v_lshlrev_b32 v110, 16, v159
v_mfma_f32_16x16x32_bf16 v[112:115], a[176:179], v[140:143], v[112:115]
ds_read_b128 v[136:139], v20 offset:32768
v_and_b32 v111, 0xffff0000, v159
v_mfma_f32_16x16x32_bf16 v[116:119], a[180:183], v[140:143], v[116:119]
s_waitcnt vmcnt(8)
v_lshlrev_b32 v128, 16, v160
v_and_b32 v129, 0xffff0000, v160
v_mfma_f32_16x16x32_bf16 v[120:123], a[184:187], v[140:143], v[120:123]
v_lshlrev_b32 v130, 16, v161
v_and_b32 v131, 0xffff0000, v161
v_mfma_f32_16x16x32_bf16 v[124:127], a[188:191], v[140:143], v[124:127]
s_waitcnt lgkmcnt(2)
v_lshlrev_b32 v132, 16, v162
v_and_b32 v133, 0xffff0000, v162
v_mfma_f32_16x16x32_bf16 v[112:115], a[192:195], v[144:147], v[112:115]
ds_read_b128 v[140:143], v20 offset:33792
v_lshlrev_b32 v134, 16, v163
v_mfma_f32_16x16x32_bf16 v[116:119], a[196:199], v[144:147], v[116:119]
v_and_b32 v135, 0xffff0000, v163
v_mfma_f32_16x16x32_bf16 v[120:123], a[200:203], v[144:147], v[120:123]
global_load_dwordx4 v[152:155], v15, s[42:43] offset:0 sc1
v_mfma_f32_16x16x32_bf16 v[124:127], a[204:207], v[144:147], v[124:127]
s_waitcnt lgkmcnt(2)
v_mfma_f32_16x16x32_bf16 v[112:115], a[208:211], v[148:151], v[112:115]
ds_read_b128 v[144:147], v20 offset:34816
v_mfma_f32_16x16x32_bf16 v[116:119], a[212:215], v[148:151], v[116:119]
global_load_dwordx4 v[156:159], v15, s[42:43] offset:64 sc1
v_mfma_f32_16x16x32_bf16 v[120:123], a[216:219], v[148:151], v[120:123]
v_mfma_f32_16x16x32_bf16 v[124:127], a[220:223], v[148:151], v[124:127]
s_waitcnt lgkmcnt(2)
v_mfma_f32_16x16x32_bf16 v[112:115], a[224:227], v[136:139], v[112:115]
ds_read_b128 v[148:151], v20 offset:35840
global_load_dwordx4 v[160:163], v15, s[42:43] offset:512 sc1
v_mfma_f32_16x16x32_bf16 v[116:119], a[228:231], v[136:139], v[116:119]
s_sub_u32 s42, s42, 18432
s_subb_u32 s43, s43, 0
v_mfma_f32_16x16x32_bf16 v[120:123], a[232:235], v[136:139], v[120:123]
)"
R"(v_mfma_f32_16x16x32_bf16 v[124:127], a[236:239], v[136:139], v[124:127]
s_waitcnt lgkmcnt(2)
v_mfma_f32_16x16x32_bf16 v[112:115], a[240:243], v[140:143], v[112:115]
v_mfma_f32_16x16x32_bf16 v[116:119], a[244:247], v[140:143], v[116:119]
v_xor_b32 v20, 0x10000, v20
v_mfma_f32_16x16x32_bf16 v[120:123], a[248:251], v[140:143], v[120:123]
v_mfma_f32_16x16x32_bf16 v[124:127], a[252:255], v[140:143], v[124:127]
s_waitcnt vmcnt(8)
s_barrier
s_waitcnt lgkmcnt(1)
v_mfma_f32_16x16x32_bf16 v[112:115], v[24:27], v[144:147], v[112:115]
ds_read_b128 v[136:139], v20 offset:0
ds_read_b128 v[140:143], v20 offset:1024
v_mfma_f32_16x16x32_bf16 v[116:119], v[28:31], v[144:147], v[116:119]
global_store_dword v18, v19, s[38:39] sc1
v_add_u32 v19, 1, v19
v_mfma_f32_16x16x32_bf16 v[120:123], v[32:35], v[144:147], v[120:123]
v_mfma_f32_16x16x32_bf16 v[124:127], v[36:39], v[144:147], v[124:127]
s_waitcnt lgkmcnt(2)
v_mfma_f32_16x16x32_bf16 v[112:115], v[40:43], v[148:151], v[112:115]
ds_read_b128 v[144:147], v20 offset:2048
v_mfma_f32_16x16x32_bf16 v[116:119], v[44:47], v[148:151], v[116:119]
v_mfma_f32_16x16x32_bf16 v[120:123], v[48:51], v[148:151], v[120:123]
v_mfma_f32_16x16x32_bf16 v[124:127], v[52:55], v[148:151], v[124:127]
s_sub_u32 s70, s70, 1
s_cbranch_scc0 .Lwo_top7
s_waitcnt lgkmcnt(0)
s_nop 0
v_cvt_pk_bf16_f32 v164, v112, v113
v_cvt_pk_bf16_f32 v165, v114, v115
v_cvt_pk_bf16_f32 v166, v116, v117
v_cvt_pk_bf16_f32 v167, v118, v119
global_store_dwordx4 v13, v[164:167], s[38:39] offset:0 sc1
v_cvt_pk_bf16_f32 v168, v120, v121
v_cvt_pk_bf16_f32 v169, v122, v123
v_cvt_pk_bf16_f32 v170, v124, v125
v_cvt_pk_bf16_f32 v171, v126, v127
global_store_dwordx4 v13, v[168:171], s[38:39] offset:1024 sc1
v_add_u32 v13, 18688, v13
v_add_f32 v200, v112, v113
v_mul_f32 v201, v112, v112
v_fmac_f32 v201, v113, v113
v_add_f32 v200, v200, v114
v_fmac_f32 v201, v114, v114
v_add_f32 v200, v200, v115
v_fmac_f32 v201, v115, v115
v_add_f32 v200, v200, v116
v_fmac_f32 v201, v116, v116
v_add_f32 v200, v200, v117
v_fmac_f32 v201, v117, v117
v_add_f32 v200, v200, v118
v_fmac_f32 v201, v118, v118
v_add_f32 v200, v200, v119
v_fmac_f32 v201, v119, v119
v_add_f32 v200, v200, v120
v_fmac_f32 v201, v120, v120
v_add_f32 v200, v200, v121
v_fmac_f32 v201, v121, v121
v_add_f32 v200, v200, v122
v_fmac_f32 v201, v122, v122
v_add_f32 v200, v200, v123
v_fmac_f32 v201, v123, v123
v_add_f32 v200, v200, v124
v_fmac_f32 v201, v124, v124
v_add_f32 v200, v200, v125
v_fmac_f32 v201, v125, v125
v_add_f32 v200, v200, v126
v_fmac_f32 v201, v126, v126
v_add_f32 v200, v200, v127
v_fmac_f32 v201, v127, v127
ds_write_b64 v22, v[200:201] offset:59392
v_add_u32 v11, 18432, v11
v_min_u32 v202, v11, v12
s_waitcnt vmcnt(0) lgkmcnt(0)
s_barrier
global_store_dword v18, v19, s[38:39] sc1
v_add_u32 v19, 3, v19
s_waitcnt vmcnt(0)
s_barrier
global_store_dword v18, v19, s[38:39] sc1
s_endpgm
.Lwo_w2:
s_add_u32 m0, s46, 4096
s_nop 0
global_load_lds_dwordx4 v0, s[40:41] offset:-4096
global_load_lds_dwordx4 v1, s[40:41] offset:-3072
global_load_lds_dwordx4 v2, s[40:41] offset:-2048
global_load_lds_dwordx4 v3, s[40:41] offset:-1024
global_load_lds_dwordx4 v4, s[40:41] offset:0
global_load_lds_dwordx4 v5, s[40:41] offset:1024
global_load_lds_dwordx4 v6, s[40:41] offset:2048
global_load_lds_dwordx4 v7, s[40:41] offset:3072
s_add_u32 m0, s46, 12288
s_nop 0
global_load_lds_dwordx4 v8, s[40:41] offset:-4096
s_add_u32 s40, s40, 73728
s_addc_u32 s41, s41, 0
s_add_u32 m0, s46, 40960
s_nop 0
global_load_lds_dwordx4 v0, s[40:41] offset:-4096
global_load_lds_dwordx4 v1, s[40:41] offset:-3072
global_load_lds_dwordx4 v2, s[40:41] offset:-2048
global_load_lds_dwordx4 v3, s[40:41] offset:-1024
global_load_lds_dwordx4 v4, s[40:41] offset:0
global_load_lds_dwordx4 v5, s[40:41] offset:1024
global_load_lds_dwordx4 v6, s[40:41] offset:2048
global_load_lds_dwordx4 v7, s[40:41] offset:3072
s_add_u32 m0, s46, 49152
s_nop 0
global_load_lds_dwordx4 v8, s[40:41] offset:-4096
s_add_u32 s40, s40, 73728
s_addc_u32 s41, s41, 0
s_add_u32 m0, s46, 77824
s_nop 0
global_load_lds_dwordx4 v0, s[40:41] offset:-4096
global_load_lds_dwordx4 v1, s[40:41] offset:-3072
global_load_lds_dwordx4 v2, s[40:41] offset:-2048
global_load_lds_dwordx4 v3, s[40:41] offset:-1024
global_load_lds_dwordx4 v4, s[40:41] offset:0
global_load_lds_dwordx4 v5, s[40:41] offset:1024
global_load_lds_dwordx4 v6, s[40:41] offset:2048
global_load_lds_dwordx4 v7, s[40:41] offset:3072
s_add_u32 m0, s46, 86016
s_nop 0
global_load_lds_dwordx4 v8, s[40:41] offset:-4096
s_add_u32 s40, s40, 73728
s_addc_u32 s41, s41, 0
s_add_u32 m0, s46, 114688
s_nop 0
global_load_lds_dwordx4 v0, s[40:41] offset:-4096
global_load_lds_dwordx4 v1, s[40:41] offset:-3072
global_load_lds_dwordx4 v2, s[40:41] offset:-2048
global_load_lds_dwordx4 v3, s[40:41] offset:-1024
global_load_lds_dwordx4 v4, s[40:41] offset:0
global_load_lds_dwordx4 v5, s[40:41] offset:1024
global_load_lds_dwordx4 v6, s[40:41] offset:2048
global_load_lds_dwordx4 v7, s[40:41] offset:3072
s_add_u32 m0, s46, 122880
s_nop 0
global_load_lds_dwordx4 v8, s[40:41] offset:-4096
s_add_u32 s40, s40, 73728
s_addc_u32 s41, s41, 0
v_add_u32 v205, 0x10000, v9
v_add_u32 v206, 0x20000, v9
v_add_u32 v207, 0x10000, v10
v_add_u32 v208, 0x20000, v10
s_waitcnt lgkmcnt(0)
s_mul_i32 s37, s37, 0x9E3779B1
s_add_u32 s37, s37, 0x7F4A7C15
v_add_u32 v17, s37, v17
v_add_u32 v19, s37, v19
s_waitcnt vmcnt(27)
s_barrier
ds_read_b64_tr_b16 a[0:1], v9 offset:0
ds_read_b64_tr_b16 a[2:3], v9 offset:2304
ds_read_b64_tr_b16 a[4:5], v9 offset:8
ds_read_b64_tr_b16 a[6:7], v9 offset:2312
ds_read_b64_tr_b16 a[8:9], v9 offset:64
ds_read_b64_tr_b16 a[10:11], v9 offset:2368
ds_read_b64_tr_b16 a[12:13], v9 offset:72
ds_read_b64_tr_b16 a[14:15], v9 offset:2376
ds_read_b64_tr_b16 a[16:17], v9 offset:18432
ds_read_b64_tr_b16 a[18:19], v9 offset:20736
ds_read_b64_tr_b16 a[20:21], v9 offset:18440
ds_read_b64_tr_b16 a[22:23], v9 offset:20744
ds_read_b64_tr_b16 a[24:25], v9 offset:18496
ds_read_b64_tr_b16 a[26:27], v9 offset:20800
ds_read_b64_tr_b16 a[28:29], v9 offset:18504
ds_read_b64_tr_b16 a[30:31], v9 offset:20808
s_waitcnt lgkmcnt(0)
s_barrier
s_add_u32 m0, s46, 4096
s_nop 0
global_load_lds_dwordx4 v0, s[40:41] offset:-4096
global_load_lds_dwordx4 v1, s[40:41] offset:-3072
global_load_lds_dwordx4 v2, s[40:41] offset:-2048
global_load_lds_dwordx4 v3, s[40:41] offset:-1024
global_load_lds_dwordx4 v4, s[40:41] offset:0
global_load_lds_dwordx4 v5, s[40:41] offset:1024
global_load_lds_dwordx4 v6, s[40:41] offset:2048
global_load_lds_dwordx4 v7, s[40:41] offset:3072
s_add_u32 m0, s46, 12288
s_nop 0
global_load_lds_dwordx4 v8, s[40:41] offset:-4096
s_add_u32 s40, s40, 73728
s_addc_u32 s41, s41, 0
s_waitcnt vmcnt(27)
s_barrier
ds_read_b64_tr_b16 a[32:33], v9 offset:36864
ds_read_b64_tr_b16 a[34:35], v9 offset:39168
ds_read_b64_tr_b16 a[36:37], v9 offset:36872
ds_read_b64_tr_b16 a[38:39], v9 offset:39176
ds_read_b64_tr_b16 a[40:41], v9 offset:36928
ds_read_b64_tr_b16 a[42:43], v9 offset:39232
ds_read_b64_tr_b16 a[44:45], v9 offset:36936
ds_read_b64_tr_b16 a[46:47], v9 offset:39240
ds_read_b64_tr_b16 a[48:49], v9 offset:55296
ds_read_b64_tr_b16 a[50:51], v9 offset:57600
ds_read_b64_tr_b16 a[52:53], v9 offset:55304
ds_read_b64_tr_b16 a[54:55], v9 offset:57608
ds_read_b64_tr_b16 a[56:57], v9 offset:55360
ds_read_b64_tr_b16 a[58:59], v9 offset:57664
ds_read_b64_tr_b16 a[60:61], v9 offset:55368
ds_read_b64_tr_b16 a[62:63], v9 offset:57672
s_waitcnt lgkmcnt(0)
s_barrier
s_add_u32 m0, s46, 40960
s_nop 0
global_load_lds_dwordx4 v0, s[40:41] offset:-4096
global_load_lds_dwordx4 v1, s[40:41] offset:-3072
global_load_lds_dwordx4 v2, s[40:41] offset:-2048
global_load_lds_dwordx4 v3, s[40:41] offset:-1024
global_load_lds_dwordx4 v4, s[40:41] offset:0
global_load_lds_dwordx4 v5, s[40:41] offset:1024
global_load_lds_dwordx4 v6, s[40:41] offset:2048
global_load_lds_dwordx4 v7, s[40:41] offset:3072
s_add_u32 m0, s46, 49152
s_nop 0
global_load_lds_dwordx4 v8, s[40:41] offset:-4096
s_add_u32 s40, s40, 73728
s_addc_u32 s41, s41, 0
s_waitcnt vmcnt(27)
s_barrier
ds_read_b64_tr_b16 a[64:65], v205 offset:8192
ds_read_b64_tr_b16 a[66:67], v205 offset:10496
ds_read_b64_tr_b16 a[68:69], v205 offset:8200
ds_read_b64_tr_b16 a[70:71], v205 offset:10504
ds_read_b64_tr_b16 a[72:73], v205 offset:8256
ds_read_b64_tr_b16 a[74:75], v205 offset:10560
ds_read_b64_tr_b16 a[76:77], v205 offset:8264
ds_read_b64_tr_b16 a[78:79], v205 offset:10568
ds_read_b64_tr_b16 a[80:81], v205 offset:26624
ds_read_b64_tr_b16 a[82:83], v205 offset:28928
ds_read_b64_tr_b16 a[84:85], v205 offset:26632
ds_read_b64_tr_b16 a[86:87], v205 offset:28936
ds_read_b64_tr_b16 a[88:89], v205 offset:26688
ds_read_b64_tr_b16 a[90:91], v205 offset:28992
ds_read_b64_tr_b16 a[92:93], v205 offset:26696
ds_read_b64_tr_b16 a[94:95], v205 offset:29000
ds_read_b64_tr_b16 v[56:57], v207 offset:26624
ds_read_b64_tr_b16 v[58:59], v207 offset:28928
ds_read_b64_tr_b16 v[60:61], v207 offset:26632
ds_read_b64_tr_b16 v[62:63], v207 offset:28936
s_waitcnt lgkmcnt(0)
s_barrier
s_add_u32 m0, s46, 77824
s_nop 0
global_load_lds_dwordx4 v0, s[40:41] offset:-4096
global_load_lds_dwordx4 v1, s[40:41] offset:-3072
global_load_lds_dwordx4 v2, s[40:41] offset:-2048
global_load_lds_dwordx4 v3, s[40:41] offset:-1024
global_load_lds_dwordx4 v4, s[40:41] offset:0
global_load_lds_dwordx4 v5, s[40:41] offset:1024
global_load_lds_dwordx4 v6, s[40:41] offset:2048
global_load_lds_dwordx4 v7, s[40:41] offset:3072
s_add_u32 m0, s46, 86016
s_nop 0
global_load_lds_dwordx4 v8, s[40:41] offset:-4096
s_add_u32 s40, s40, 73728
s_addc_u32 s41, s41, 0
s_waitcnt vmcnt(27)
s_barrier
ds_read_b64_tr_b16 a[96:97], v205 offset:45056
ds_read_b64_tr_b16 a[98:99], v205 offset:47360
ds_read_b64_tr_b16 a[100:101], v205 offset:45064
ds_read_b64_tr_b16 a[102:103], v205 offset:47368
ds_read_b64_tr_b16 a[104:105], v205 offset:45120
ds_read_b64_tr_b16 a[106:107], v205 offset:47424
ds_read_b64_tr_b16 a[108:109], v205 offset:45128
ds_read_b64_tr_b16 a[110:111], v205 offset:47432
ds_read_b64_tr_b16 v[64:65], v207 offset:45056
ds_read_b64_tr_b16 v[66:67], v207 offset:47360
ds_read_b64_tr_b16 v[68:69], v207 offset:45064
ds_read_b64_tr_b16 v[70:71], v207 offset:47368
ds_read_b64_tr_b16 a[112:113], v205 offset:63488
ds_read_b64_tr_b16 a[114:115], v206 offset:256
ds_read_b64_tr_b16 a[116:117], v205 offset:63496
ds_read_b64_tr_b16 a[118:119], v206 offset:264
ds_read_b64_tr_b16 a[120:121], v205 offset:63552
ds_read_b64_tr_b16 a[122:123], v206 offset:320
ds_read_b64_tr_b16 a[124:125], v205 offset:63560
ds_read_b64_tr_b16 a[126:127], v206 offset:328
ds_read_b64_tr_b16 v[72:73], v207 offset:63488
ds_read_b64_tr_b16 v[74:75], v208 offset:256
ds_read_b64_tr_b16 v[76:77], v207 offset:63496
ds_read_b64_tr_b16 v[78:79], v208 offset:264
s_waitcnt lgkmcnt(0)
s_barrier
s_add_u32 m0, s46, 114688
s_nop 0
global_load_lds_dwordx4 v0, s[40:41] offset:-4096
global_load_lds_dwordx4 v1, s[40:41] offset:-3072
global_load_lds_dwordx4 v2, s[40:41] offset:-2048
global_load_lds_dwordx4 v3, s[40:41] offset:-1024
global_load_lds_dwordx4 v4, s[40:41] offset:0
global_load_lds_dwordx4 v5, s[40:41] offset:1024
global_load_lds_dwordx4 v6, s[40:41] offset:2048
global_load_lds_dwordx4 v7, s[40:41] offset:3072
s_add_u32 m0, s46, 122880
s_nop 0
global_load_lds_dwordx4 v8, s[40:41] offset:-4096
s_add_u32 s40, s40, 73728
s_addc_u32 s41, s41, 0
s_waitcnt vmcnt(27)
s_barrier
ds_read_b64_tr_b16 a[128:129], v9 offset:0
ds_read_b64_tr_b16 a[130:131], v9 offset:2304
ds_read_b64_tr_b16 a[132:133], v9 offset:8
ds_read_b64_tr_b16 a[134:135], v9 offset:2312
ds_read_b64_tr_b16 a[136:137], v9 offset:64
ds_read_b64_tr_b16 a[138:139], v9 offset:2368
ds_read_b64_tr_b16 a[140:141], v9 offset:72
ds_read_b64_tr_b16 a[142:143], v9 offset:2376
ds_read_b64_tr_b16 v[80:81], v10 offset:0
ds_read_b64_tr_b16 v[82:83], v10 offset:2304
ds_read_b64_tr_b16 v[84:85], v10 offset:8
ds_read_b64_tr_b16 v[86:87], v10 offset:2312
ds_read_b64_tr_b16 a[144:145], v9 offset:18432
ds_read_b64_tr_b16 a[146:147], v9 offset:20736
ds_read_b64_tr_b16 a[148:149], v9 offset:18440
ds_read_b64_tr_b16 a[150:151], v9 offset:20744
ds_read_b64_tr_b16 a[152:153], v9 offset:18496
ds_read_b64_tr_b16 a[154:155], v9 offset:20800
ds_read_b64_tr_b16 a[156:157], v9 offset:18504
ds_read_b64_tr_b16 a[158:159], v9 offset:20808
ds_read_b64_tr_b16 v[88:89], v10 offset:18432
ds_read_b64_tr_b16 v[90:91], v10 offset:20736
ds_read_b64_tr_b16 v[92:93], v10 offset:18440
ds_read_b64_tr_b16 v[94:95], v10 offset:20744
s_waitcnt lgkmcnt(0)
s_barrier
s_add_u32 m0, s46, 4096
s_nop 0
global_load_lds_dwordx4 v0, s[40:41] offset:-4096
global_load_lds_dwordx4 v1, s[40:41] offset:-3072
global_load_lds_dwordx4 v2, s[40:41] offset:-2048
global_load_lds_dwordx4 v3, s[40:41] offset:-1024
global_load_lds_dwordx4 v4, s[40:41] offset:0
global_load_lds_dwordx4 v5, s[40:41] offset:1024
global_load_lds_dwordx4 v6, s[40:41] offset:2048
global_load_lds_dwordx4 v7, s[40:41] offset:3072
s_add_u32 m0, s46, 12288
s_nop 0
global_load_lds_dwordx4 v8, s[40:41] offset:-4096
s_add_u32 s40, s40, 73728
s_addc_u32 s41, s41, 0
s_waitcnt vmcnt(27)
s_barrier
ds_read_b64_tr_b16 a[160:161], v9 offset:36864
ds_read_b64_tr_b16 a[162:163], v9 offset:39168
ds_read_b64_tr_b16 a[164:165], v9 offset:36872
ds_read_b64_tr_b16 a[166:167], v9 offset:39176
ds_read_b64_tr_b16 a[168:169], v9 offset:36928
ds_read_b64_tr_b16 a[170:171], v9 offset:39232
ds_read_b64_tr_b16 a[172:173], v9 offset:36936
ds_read_b64_tr_b16 a[174:175], v9 offset:39240
ds_read_b64_tr_b16 a[176:177], v9 offset:55296
ds_read_b64_tr_b16 a[178:179], v9 offset:57600
ds_read_b64_tr_b16 a[180:181], v9 offset:55304
ds_read_b64_tr_b16 a[182:183], v9 offset:57608
ds_read_b64_tr_b16 a[184:185], v9 offset:55360
ds_read_b64_tr_b16 a[186:187], v9 offset:57664
ds_read_b64_tr_b16 a[188:189], v9 offset:55368
ds_read_b64_tr_b16 a[190:191], v9 offset:57672
s_waitcnt vmcnt(18)
s_barrier
ds_read_b64_tr_b16 a[192:193], v205 offset:8192
ds_read_b64_tr_b16 a[194:195], v205 offset:10496
ds_read_b64_tr_b16 a[196:197], v205 offset:8200
ds_read_b64_tr_b16 a[198:199], v205 offset:10504
ds_read_b64_tr_b16 a[200:201], v205 offset:8256
ds_read_b64_tr_b16 a[202:203], v205 offset:10560
ds_read_b64_tr_b16 a[204:205], v205 offset:8264
ds_read_b64_tr_b16 a[206:207], v205 offset:10568
ds_read_b64_tr_b16 a[208:209], v205 offset:26624
ds_read_b64_tr_b16 a[210:211], v205 offset:28928
ds_read_b64_tr_b16 a[212:213], v205 offset:26632
ds_read_b64_tr_b16 a[214:215], v205 offset:28936
ds_read_b64_tr_b16 a[216:217], v205 offset:26688
ds_read_b64_tr_b16 a[218:219], v205 offset:28992
ds_read_b64_tr_b16 a[220:221], v205 offset:26696
ds_read_b64_tr_b16 a[222:223], v205 offset:29000
s_waitcnt vmcnt(9)
s_barrier
ds_read_b64_tr_b16 a[224:225], v205 offset:45056
ds_read_b64_tr_b16 a[226:227], v205 offset:47360
ds_read_b64_tr_b16 a[228:229], v205 offset:45064
ds_read_b64_tr_b16 a[230:231], v205 offset:47368
ds_read_b64_tr_b16 a[232:233], v205 offset:45120
ds_read_b64_tr_b16 a[234:235], v205 offset:47424
ds_read_b64_tr_b16 a[236:237], v205 offset:45128
ds_read_b64_tr_b16 a[238:239], v205 offset:47432
ds_read_b64_tr_b16 a[240:241], v205 offset:63488
ds_read_b64_tr_b16 a[242:243], v206 offset:256
ds_read_b64_tr_b16 a[244:245], v205 offset:63496
ds_read_b64_tr_b16 a[246:247], v206 offset:264
ds_read_b64_tr_b16 a[248:249], v205 offset:63552
ds_read_b64_tr_b16 a[250:251], v206 offset:320
ds_read_b64_tr_b16 a[252:253], v205 offset:63560
ds_read_b64_tr_b16 a[254:255], v206 offset:328
)"
R"(s_waitcnt vmcnt(0)
s_barrier
ds_read_b64_tr_b16 v[24:25], v9 offset:0
ds_read_b64_tr_b16 v[26:27], v9 offset:2304
ds_read_b64_tr_b16 v[28:29], v9 offset:8
ds_read_b64_tr_b16 v[30:31], v9 offset:2312
ds_read_b64_tr_b16 v[32:33], v9 offset:64
ds_read_b64_tr_b16 v[34:35], v9 offset:2368
ds_read_b64_tr_b16 v[36:37], v9 offset:72
ds_read_b64_tr_b16 v[38:39], v9 offset:2376
ds_read_b64_tr_b16 v[40:41], v9 offset:18432
ds_read_b64_tr_b16 v[42:43], v9 offset:20736
ds_read_b64_tr_b16 v[44:45], v9 offset:18440
ds_read_b64_tr_b16 v[46:47], v9 offset:20744
ds_read_b64_tr_b16 v[48:49], v9 offset:18496
ds_read_b64_tr_b16 v[50:51], v9 offset:20800
ds_read_b64_tr_b16 v[52:53], v9 offset:18504
ds_read_b64_tr_b16 v[54:55], v9 offset:20808
s_waitcnt lgkmcnt(0)
s_barrier
v_mov_b32 v202, v11
s_barrier
s_mov_b32 s70, 32
s_waitcnt vmcnt(0) lgkmcnt(0)
s_nop 7
.p2align 6
.Lwo_top10:
s_waitcnt lgkmcnt(6)
v_mfma_f32_16x16x32_bf16 v[96:99], a[80:83], v[136:139], v[96:99]
s_waitcnt lgkmcnt(3)
ds_read_b128 v[148:151], v20 offset:8192
v_add_f32 v176, v176, v180
v_mfma_f32_16x16x32_bf16 v[100:103], a[84:87], v[136:139], v[100:103]
s_cmp_eq_u32 s70, 16
s_cbranch_scc1 .Lwo_hk8
.Lwo_hr9:
v_mfma_f32_16x16x32_bf16 v[104:107], a[88:91], v[136:139], v[104:107]
s_waitcnt lgkmcnt(2)
v_add_f32 v177, v177, v181
v_add_f32 v184, v184, v188
v_mfma_f32_16x16x32_bf16 v[108:111], a[92:95], v[136:139], v[108:111]
v_cvt_pk_bf16_f32 v164, v112, v113
v_cvt_pk_bf16_f32 v165, v114, v115
v_mfma_f32_16x16x32_bf16 v[128:131], v[56:59], v[136:139], 0
v_cvt_pk_bf16_f32 v166, v116, v117
v_cvt_pk_bf16_f32 v167, v118, v119
v_mfma_f32_16x16x32_bf16 v[132:135], v[60:63], v[136:139], 0
v_add_f32 v185, v185, v189
v_add_f32 v176, v176, v184
v_mfma_f32_16x16x32_bf16 v[96:99], a[96:99], v[140:143], v[96:99]
ds_read_b128 v[136:139], v20 offset:9216
v_add_f32 v177, v177, v185
v_mfma_f32_16x16x32_bf16 v[100:103], a[100:103], v[140:143], v[100:103]
global_store_dwordx4 v13, v[164:167], s[38:39] offset:0 sc1
v_mfma_f32_16x16x32_bf16 v[104:107], a[104:107], v[140:143], v[104:107]
v_cvt_pk_bf16_f32 v168, v120, v121
v_cvt_pk_bf16_f32 v169, v122, v123
v_mfma_f32_16x16x32_bf16 v[108:111], a[108:111], v[140:143], v[108:111]
v_cvt_pk_bf16_f32 v170, v124, v125
v_cvt_pk_bf16_f32 v171, v126, v127
v_mfma_f32_16x16x32_bf16 v[128:131], v[64:67], v[140:143], v[128:131]
v_mov_b32 v203, v176
v_mov_b32 v204, v177
v_mfma_f32_16x16x32_bf16 v[132:135], v[68:71], v[140:143], v[132:135]
s_waitcnt lgkmcnt(2)
global_store_dwordx4 v13, v[168:171], s[38:39] offset:1024 sc1
v_mfma_f32_16x16x32_bf16 v[96:99], a[112:115], v[144:147], v[96:99]
ds_read_b128 v[140:143], v20 offset:0
v_add_u32 v13, 18688, v13
v_mfma_f32_16x16x32_bf16 v[100:103], a[116:119], v[144:147], v[100:103]
v_permlane16_swap_b32 v176, v203
v_mfma_f32_16x16x32_bf16 v[104:107], a[120:123], v[144:147], v[104:107]
v_permlane16_swap_b32 v177, v204
v_mfma_f32_16x16x32_bf16 v[108:111], a[124:127], v[144:147], v[108:111]
v_add_f32 v176, v176, v203
v_add_f32 v177, v177, v204
v_mfma_f32_16x16x32_bf16 v[128:131], v[72:75], v[144:147], v[128:131]
v_mov_b32 v203, v176
v_mov_b32 v204, v177
v_mfma_f32_16x16x32_bf16 v[132:135], v[76:79], v[144:147], v[132:135]
s_xor_b32 s47, s47, 0x10000
s_waitcnt lgkmcnt(2)
v_mfma_f32_16x16x32_bf16 v[96:99], a[128:131], v[148:151], v[96:99]
s_add_u32 m0, s47, 0
ds_read_b128 v[144:147], v20 offset:1024
v_mfma_f32_16x16x32_bf16 v[100:103], a[132:135], v[148:151], v[100:103]
v_permlane32_swap_b32 v176, v203
v_mfma_f32_16x16x32_bf16 v[104:107], a[136:139], v[148:151], v[104:107]
v_permlane32_swap_b32 v177, v204
v_mfma_f32_16x16x32_bf16 v[108:111], a[140:143], v[148:151], v[108:111]
v_add_f32 v176, v176, v203
v_add_f32 v177, v177, v204
v_mfma_f32_16x16x32_bf16 v[128:131], v[80:83], v[148:151], v[128:131]
global_store_dwordx2 v14, v[176:177], s[38:39] sc1
v_mfma_f32_16x16x32_bf16 v[132:135], v[84:87], v[148:151], v[132:135]
s_waitcnt lgkmcnt(2)
v_add_u32 v14, 18688, v14
global_load_lds_dwordx4 v202, s[38:39] offset:-2048 sc1
v_mfma_f32_16x16x32_bf16 v[96:99], a[144:147], v[136:139], v[96:99]
ds_read_b128 v[148:151], v20 offset:2048
v_add_f32 v200, v112, v113
v_mfma_f32_16x16x32_bf16 v[100:103], a[148:151], v[136:139], v[100:103]
v_mul_f32 v201, v112, v112
v_fmac_f32 v201, v113, v113
v_mfma_f32_16x16x32_bf16 v[104:107], a[152:155], v[136:139], v[104:107]
v_add_f32 v200, v200, v114
v_fmac_f32 v201, v114, v114
v_mfma_f32_16x16x32_bf16 v[108:111], a[156:159], v[136:139], v[108:111]
v_add_f32 v200, v200, v115
v_fmac_f32 v201, v115, v115
v_mfma_f32_16x16x32_bf16 v[128:131], v[88:91], v[136:139], v[128:131]
v_add_f32 v200, v200, v116
v_fmac_f32 v201, v116, v116
v_mfma_f32_16x16x32_bf16 v[132:135], v[92:95], v[136:139], v[132:135]
s_waitcnt lgkmcnt(2)
v_add_f32 v200, v200, v117
v_fmac_f32 v201, v117, v117
v_mfma_f32_16x16x32_bf16 v[96:99], a[0:3], v[140:143], v[96:99]
ds_read_b128 v[136:139], v20 offset:3072
global_load_lds_dwordx4 v202, s[38:39] offset:-1024 sc1
v_mfma_f32_16x16x32_bf16 v[100:103], a[4:7], v[140:143], v[100:103]
v_add_f32 v200, v200, v118
v_fmac_f32 v201, v118, v118
v_mfma_f32_16x16x32_bf16 v[104:107], a[8:11], v[140:143], v[104:107]
v_add_f32 v200, v200, v119
v_fmac_f32 v201, v119, v119
v_mfma_f32_16x16x32_bf16 v[108:111], a[12:15], v[140:143], v[108:111]
s_waitcnt lgkmcnt(2)
v_add_f32 v200, v200, v120
v_fmac_f32 v201, v120, v120
v_mfma_f32_16x16x32_bf16 v[96:99], a[16:19], v[144:147], v[96:99]
ds_read_b128 v[140:143], v20 offset:4096
v_add_f32 v200, v200, v121
v_mfma_f32_16x16x32_bf16 v[100:103], a[20:23], v[144:147], v[100:103]
v_fmac_f32 v201, v121, v121
v_add_f32 v200, v200, v122
v_mfma_f32_16x16x32_bf16 v[104:107], a[24:27], v[144:147], v[104:107]
ds_write_b128 v21, v[128:131] offset:43008
v_mfma_f32_16x16x32_bf16 v[108:111], a[28:31], v[144:147], v[108:111]
s_waitcnt lgkmcnt(3)
ds_write_b128 v21, v[132:135] offset:44032
v_mfma_f32_16x16x32_bf16 v[96:99], a[32:35], v[148:151], v[96:99]
ds_read_b128 v[144:147], v20 offset:10240
global_load_lds_dwordx4 v202, s[38:39] offset:0 sc1
v_mfma_f32_16x16x32_bf16 v[100:103], a[36:39], v[148:151], v[100:103]
v_fmac_f32 v201, v122, v122
v_add_f32 v200, v200, v123
v_mfma_f32_16x16x32_bf16 v[104:107], a[40:43], v[148:151], v[104:107]
v_fmac_f32 v201, v123, v123
v_add_f32 v200, v200, v124
v_mfma_f32_16x16x32_bf16 v[108:111], a[44:47], v[148:151], v[108:111]
s_waitcnt lgkmcnt(4)
v_fmac_f32 v201, v124, v124
v_add_f32 v200, v200, v125
v_mfma_f32_16x16x32_bf16 v[96:99], a[48:51], v[136:139], v[96:99]
ds_read_b128 v[148:151], v20 offset:11264
v_fmac_f32 v201, v125, v125
v_mfma_f32_16x16x32_bf16 v[100:103], a[52:55], v[136:139], v[100:103]
v_add_f32 v200, v200, v126
v_fmac_f32 v201, v126, v126
v_mfma_f32_16x16x32_bf16 v[104:107], a[56:59], v[136:139], v[104:107]
v_add_f32 v200, v200, v127
v_fmac_f32 v201, v127, v127
v_mfma_f32_16x16x32_bf16 v[108:111], a[60:63], v[136:139], v[108:111]
s_waitcnt lgkmcnt(4)
global_load_lds_dwordx4 v202, s[38:39] offset:1024 sc1
v_mfma_f32_16x16x32_bf16 v[96:99], a[64:67], v[140:143], v[96:99]
ds_read_b128 v[136:139], v20 offset:12288
v_mfma_f32_16x16x32_bf16 v[100:103], a[68:71], v[140:143], v[100:103]
ds_write_b64 v22, v[200:201] offset:59904
v_mfma_f32_16x16x32_bf16 v[104:107], a[72:75], v[140:143], v[104:107]
s_waitcnt vmcnt(8)
v_lshlrev_b32 v112, 16, v152
v_and_b32 v113, 0xffff0000, v152
v_mfma_f32_16x16x32_bf16 v[108:111], a[76:79], v[140:143], v[108:111]
s_waitcnt lgkmcnt(3)
v_lshlrev_b32 v114, 16, v153
v_and_b32 v115, 0xffff0000, v153
v_mfma_f32_16x16x32_bf16 v[96:99], a[160:163], v[144:147], v[96:99]
ds_read_b128 v[140:143], v20 offset:13312
v_lshlrev_b32 v116, 16, v154
v_mfma_f32_16x16x32_bf16 v[100:103], a[164:167], v[144:147], v[100:103]
v_and_b32 v117, 0xffff0000, v154
v_lshlrev_b32 v118, 16, v155
v_mfma_f32_16x16x32_bf16 v[104:107], a[168:171], v[144:147], v[104:107]
global_load_lds_dwordx4 v202, s[38:39] offset:2048 sc1
v_add_u32 v11, 18432, v11
v_mfma_f32_16x16x32_bf16 v[108:111], a[172:175], v[144:147], v[108:111]
s_waitcnt lgkmcnt(3)
v_min_u32 v202, v11, v12
v_and_b32 v119, 0xffff0000, v155
v_mfma_f32_16x16x32_bf16 v[96:99], a[176:179], v[148:151], v[96:99]
s_waitcnt vmcnt(8)
ds_read_b128 v[144:147], v20 offset:14336
v_lshlrev_b32 v120, 16, v156
v_mfma_f32_16x16x32_bf16 v[100:103], a[180:183], v[148:151], v[100:103]
v_and_b32 v121, 0xffff0000, v156
v_lshlrev_b32 v122, 16, v157
v_mfma_f32_16x16x32_bf16 v[104:107], a[184:187], v[148:151], v[104:107]
v_and_b32 v123, 0xffff0000, v157
v_lshlrev_b32 v124, 16, v158
v_mfma_f32_16x16x32_bf16 v[108:111], a[188:191], v[148:151], v[108:111]
s_waitcnt lgkmcnt(3)
v_and_b32 v125, 0xffff0000, v158
v_lshlrev_b32 v126, 16, v159
v_mfma_f32_16x16x32_bf16 v[96:99], a[192:195], v[136:139], v[96:99]
ds_read_b128 v[148:151], v20 offset:15360
v_and_b32 v127, 0xffff0000, v159
v_mfma_f32_16x16x32_bf16 v[100:103], a[196:199], v[136:139], v[100:103]
global_load_dwordx4 v[152:155], v15, s[42:43] offset:0 sc1
v_mfma_f32_16x16x32_bf16 v[104:107], a[200:203], v[136:139], v[104:107]
v_mfma_f32_16x16x32_bf16 v[108:111], a[204:207], v[136:139], v[108:111]
s_waitcnt lgkmcnt(2)
v_mfma_f32_16x16x32_bf16 v[96:99], a[208:211], v[140:143], v[96:99]
ds_read_b128 v[136:139], v20 offset:16384
global_load_dwordx4 v[156:159], v15, s[42:43] offset:64 sc1
v_mfma_f32_16x16x32_bf16 v[100:103], a[212:215], v[140:143], v[100:103]
s_sub_u32 s42, s42, 18432
s_subb_u32 s43, s43, 0
v_mfma_f32_16x16x32_bf16 v[104:107], a[216:219], v[140:143], v[104:107]
v_mfma_f32_16x16x32_bf16 v[108:111], a[220:223], v[140:143], v[108:111]
s_waitcnt lgkmcnt(2)
v_mfma_f32_16x16x32_bf16 v[96:99], a[224:227], v[144:147], v[96:99]
ds_read_b128 v[140:143], v20 offset:17408
v_mfma_f32_16x16x32_bf16 v[100:103], a[228:231], v[144:147], v[100:103]
v_mfma_f32_16x16x32_bf16 v[104:107], a[232:235], v[144:147], v[104:107]
v_mfma_f32_16x16x32_bf16 v[108:111], a[236:239], v[144:147], v[108:111]
s_waitcnt lgkmcnt(2)
v_mfma_f32_16x16x32_bf16 v[96:99], a[240:243], v[148:151], v[96:99]
v_mfma_f32_16x16x32_bf16 v[100:103], a[244:247], v[148:151], v[100:103]
v_mfma_f32_16x16x32_bf16 v[104:107], a[248:251], v[148:151], v[104:107]
v_mfma_f32_16x16x32_bf16 v[108:111], a[252:255], v[148:151], v[108:111]
s_waitcnt vmcnt(8)
s_barrier
s_waitcnt lgkmcnt(1)
v_mfma_f32_16x16x32_bf16 v[96:99], v[24:27], v[136:139], v[96:99]
ds_read_b128 v[144:147], v20 offset:23552
ds_read_b128 v[148:151], v20 offset:24576
v_mfma_f32_16x16x32_bf16 v[100:103], v[28:31], v[136:139], v[100:103]
ds_read_b64 v[176:177], v22 offset:59392
ds_read_b64 v[180:181], v22 offset:59904
v_mfma_f32_16x16x32_bf16 v[104:107], v[32:35], v[136:139], v[104:107]
ds_read_b64 v[184:185], v22 offset:60416
ds_read_b64 v[188:189], v22 offset:60928
v_mfma_f32_16x16x32_bf16 v[108:111], v[36:39], v[136:139], v[108:111]
s_waitcnt lgkmcnt(6)
v_mfma_f32_16x16x32_bf16 v[96:99], v[40:43], v[140:143], v[96:99]
ds_read_b128 v[136:139], v20 offset:25600
v_mfma_f32_16x16x32_bf16 v[100:103], v[44:47], v[140:143], v[100:103]
v_mfma_f32_16x16x32_bf16 v[104:107], v[48:51], v[140:143], v[104:107]
v_mfma_f32_16x16x32_bf16 v[108:111], v[52:55], v[140:143], v[108:111]
s_waitcnt lgkmcnt(6)
v_mfma_f32_16x16x32_bf16 v[112:115], a[80:83], v[144:147], v[112:115]
s_waitcnt lgkmcnt(3)
ds_read_b128 v[140:143], v20 offset:26624
v_add_f32 v176, v176, v180
v_mfma_f32_16x16x32_bf16 v[116:119], a[84:87], v[144:147], v[116:119]
s_waitcnt lgkmcnt(2)
v_add_f32 v177, v177, v181
v_add_f32 v184, v184, v188
v_mfma_f32_16x16x32_bf16 v[120:123], a[88:91], v[144:147], v[120:123]
v_add_f32 v185, v185, v189
v_add_f32 v176, v176, v184
v_mfma_f32_16x16x32_bf16 v[124:127], a[92:95], v[144:147], v[124:127]
v_cvt_pk_bf16_f32 v164, v96, v97
v_cvt_pk_bf16_f32 v165, v98, v99
v_mfma_f32_16x16x32_bf16 v[128:131], v[56:59], v[144:147], 0
v_cvt_pk_bf16_f32 v166, v100, v101
v_cvt_pk_bf16_f32 v167, v102, v103
v_mfma_f32_16x16x32_bf16 v[132:135], v[60:63], v[144:147], 0
v_add_f32 v177, v177, v185
v_mov_b32 v203, v176
v_mfma_f32_16x16x32_bf16 v[112:115], a[96:99], v[148:151], v[112:115]
ds_read_b128 v[144:147], v20 offset:27648
v_mov_b32 v204, v177
v_mfma_f32_16x16x32_bf16 v[116:119], a[100:103], v[148:151], v[116:119]
global_store_dwordx4 v13, v[164:167], s[38:39] offset:0 sc1
v_mfma_f32_16x16x32_bf16 v[120:123], a[104:107], v[148:151], v[120:123]
v_cvt_pk_bf16_f32 v168, v104, v105
v_cvt_pk_bf16_f32 v169, v106, v107
v_mfma_f32_16x16x32_bf16 v[124:127], a[108:111], v[148:151], v[124:127]
v_cvt_pk_bf16_f32 v170, v108, v109
v_cvt_pk_bf16_f32 v171, v110, v111
v_mfma_f32_16x16x32_bf16 v[128:131], v[64:67], v[148:151], v[128:131]
v_permlane16_swap_b32 v176, v203
v_mfma_f32_16x16x32_bf16 v[132:135], v[68:71], v[148:151], v[132:135]
s_waitcnt lgkmcnt(2)
global_store_dwordx4 v13, v[168:171], s[38:39] offset:1024 sc1
v_mfma_f32_16x16x32_bf16 v[112:115], a[112:115], v[136:139], v[112:115]
ds_read_b128 v[148:151], v20 offset:18432
v_add_u32 v13, 18688, v13
v_mfma_f32_16x16x32_bf16 v[116:119], a[116:119], v[136:139], v[116:119]
v_permlane16_swap_b32 v177, v204
v_mfma_f32_16x16x32_bf16 v[120:123], a[120:123], v[136:139], v[120:123]
v_add_f32 v176, v176, v203
v_add_f32 v177, v177, v204
v_mfma_f32_16x16x32_bf16 v[124:127], a[124:127], v[136:139], v[124:127]
v_mov_b32 v203, v176
v_mov_b32 v204, v177
v_mfma_f32_16x16x32_bf16 v[128:131], v[72:75], v[136:139], v[128:131]
v_permlane32_swap_b32 v176, v203
v_mfma_f32_16x16x32_bf16 v[132:135], v[76:79], v[136:139], v[132:135]
s_waitcnt lgkmcnt(2)
v_permlane32_swap_b32 v177, v204
v_mfma_f32_16x16x32_bf16 v[112:115], a[128:131], v[140:143], v[112:115]
s_add_u32 m0, s47, 18432
ds_read_b128 v[136:139], v20 offset:19456
v_add_f32 v176, v176, v203
v_mfma_f32_16x16x32_bf16 v[116:119], a[132:135], v[140:143], v[116:119]
v_add_f32 v177, v177, v204
v_mfma_f32_16x16x32_bf16 v[120:123], a[136:139], v[140:143], v[120:123]
global_store_dwordx2 v14, v[176:177], s[38:39] sc1
v_mfma_f32_16x16x32_bf16 v[124:127], a[140:143], v[140:143], v[124:127]
v_add_u32 v14, 18688, v14
v_mfma_f32_16x16x32_bf16 v[128:131], v[80:83], v[140:143], v[128:131]
global_load_lds_dwordx4 v202, s[38:39] offset:-2048 sc1
v_add_f32 v200, v96, v97
v_mfma_f32_16x16x32_bf16 v[132:135], v[84:87], v[140:143], v[132:135]
s_waitcnt lgkmcnt(2)
v_mul_f32 v201, v96, v96
v_fmac_f32 v201, v97, v97
v_mfma_f32_16x16x32_bf16 v[112:115], a[144:147], v[144:147], v[112:115]
ds_read_b128 v[140:143], v20 offset:20480
v_add_f32 v200, v200, v98
v_mfma_f32_16x16x32_bf16 v[116:119], a[148:151], v[144:147], v[116:119]
v_fmac_f32 v201, v98, v98
v_add_f32 v200, v200, v99
v_mfma_f32_16x16x32_bf16 v[120:123], a[152:155], v[144:147], v[120:123]
v_fmac_f32 v201, v99, v99
v_add_f32 v200, v200, v100
v_mfma_f32_16x16x32_bf16 v[124:127], a[156:159], v[144:147], v[124:127]
v_fmac_f32 v201, v100, v100
v_add_f32 v200, v200, v101
v_mfma_f32_16x16x32_bf16 v[128:131], v[88:91], v[144:147], v[128:131]
v_fmac_f32 v201, v101, v101
v_add_f32 v200, v200, v102
v_mfma_f32_16x16x32_bf16 v[132:135], v[92:95], v[144:147], v[132:135]
s_waitcnt lgkmcnt(2)
global_load_lds_dwordx4 v202, s[38:39] offset:-1024 sc1
v_fmac_f32 v201, v102, v102
v_mfma_f32_16x16x32_bf16 v[112:115], a[0:3], v[148:151], v[112:115]
ds_read_b128 v[144:147], v20 offset:21504
v_add_f32 v200, v200, v103
v_mfma_f32_16x16x32_bf16 v[116:119], a[4:7], v[148:151], v[116:119]
v_fmac_f32 v201, v103, v103
v_add_f32 v200, v200, v104
v_mfma_f32_16x16x32_bf16 v[120:123], a[8:11], v[148:151], v[120:123]
v_fmac_f32 v201, v104, v104
v_add_f32 v200, v200, v105
v_mfma_f32_16x16x32_bf16 v[124:127], a[12:15], v[148:151], v[124:127]
s_waitcnt lgkmcnt(2)
v_fmac_f32 v201, v105, v105
v_add_f32 v200, v200, v106
v_mfma_f32_16x16x32_bf16 v[112:115], a[16:19], v[136:139], v[112:115]
ds_read_b128 v[148:151], v20 offset:22528
v_fmac_f32 v201, v106, v106
v_mfma_f32_16x16x32_bf16 v[116:119], a[20:23], v[136:139], v[116:119]
v_add_f32 v200, v200, v107
v_fmac_f32 v201, v107, v107
v_mfma_f32_16x16x32_bf16 v[120:123], a[24:27], v[136:139], v[120:123]
ds_write_b128 v21, v[128:131] offset:49152
v_mfma_f32_16x16x32_bf16 v[124:127], a[28:31], v[136:139], v[124:127]
s_waitcnt lgkmcnt(3)
ds_write_b128 v21, v[132:135] offset:50176
v_mfma_f32_16x16x32_bf16 v[112:115], a[32:35], v[140:143], v[112:115]
ds_read_b128 v[136:139], v20 offset:28672
global_load_lds_dwordx4 v202, s[38:39] offset:0 sc1
v_mfma_f32_16x16x32_bf16 v[116:119], a[36:39], v[140:143], v[116:119]
v_add_f32 v200, v200, v108
v_fmac_f32 v201, v108, v108
v_mfma_f32_16x16x32_bf16 v[120:123], a[40:43], v[140:143], v[120:123]
v_add_f32 v200, v200, v109
v_fmac_f32 v201, v109, v109
v_mfma_f32_16x16x32_bf16 v[124:127], a[44:47], v[140:143], v[124:127]
s_waitcnt lgkmcnt(4)
v_add_f32 v200, v200, v110
v_fmac_f32 v201, v110, v110
v_mfma_f32_16x16x32_bf16 v[112:115], a[48:51], v[144:147], v[112:115]
ds_read_b128 v[140:143], v20 offset:29696
v_add_f32 v200, v200, v111
v_mfma_f32_16x16x32_bf16 v[116:119], a[52:55], v[144:147], v[116:119]
v_fmac_f32 v201, v111, v111
v_mfma_f32_16x16x32_bf16 v[120:123], a[56:59], v[144:147], v[120:123]
ds_write_b64 v22, v[200:201] offset:57856
v_mfma_f32_16x16x32_bf16 v[124:127], a[60:63], v[144:147], v[124:127]
s_waitcnt vmcnt(7)
global_load_lds_dwordx4 v202, s[38:39] offset:1024 sc1
v_lshlrev_b32 v96, 16, v152
s_waitcnt lgkmcnt(5)
v_mfma_f32_16x16x32_bf16 v[112:115], a[64:67], v[148:151], v[112:115]
ds_read_b128 v[144:147], v20 offset:30720
v_and_b32 v97, 0xffff0000, v152
v_mfma_f32_16x16x32_bf16 v[116:119], a[68:71], v[148:151], v[116:119]
v_lshlrev_b32 v98, 16, v153
v_and_b32 v99, 0xffff0000, v153
v_mfma_f32_16x16x32_bf16 v[120:123], a[72:75], v[148:151], v[120:123]
v_lshlrev_b32 v100, 16, v154
v_and_b32 v101, 0xffff0000, v154
v_mfma_f32_16x16x32_bf16 v[124:127], a[76:79], v[148:151], v[124:127]
s_waitcnt lgkmcnt(3)
)"
R"(v_lshlrev_b32 v102, 16, v155
v_and_b32 v103, 0xffff0000, v155
v_mfma_f32_16x16x32_bf16 v[112:115], a[160:163], v[136:139], v[112:115]
s_waitcnt vmcnt(7)
ds_read_b128 v[148:151], v20 offset:31744
v_lshlrev_b32 v104, 16, v156
v_mfma_f32_16x16x32_bf16 v[116:119], a[164:167], v[136:139], v[116:119]
v_and_b32 v105, 0xffff0000, v156
v_lshlrev_b32 v106, 16, v157
v_mfma_f32_16x16x32_bf16 v[120:123], a[168:171], v[136:139], v[120:123]
global_load_lds_dwordx4 v202, s[38:39] offset:2048 sc1
v_add_u32 v11, 18432, v11
v_mfma_f32_16x16x32_bf16 v[124:127], a[172:175], v[136:139], v[124:127]
s_waitcnt lgkmcnt(3)
v_min_u32 v202, v11, v12
v_and_b32 v107, 0xffff0000, v157
v_mfma_f32_16x16x32_bf16 v[112:115], a[176:179], v[140:143], v[112:115]
ds_read_b128 v[136:139], v20 offset:32768
v_lshlrev_b32 v108, 16, v158
v_mfma_f32_16x16x32_bf16 v[116:119], a[180:183], v[140:143], v[116:119]
v_and_b32 v109, 0xffff0000, v158
v_lshlrev_b32 v110, 16, v159
v_mfma_f32_16x16x32_bf16 v[120:123], a[184:187], v[140:143], v[120:123]
v_and_b32 v111, 0xffff0000, v159
v_mfma_f32_16x16x32_bf16 v[124:127], a[188:191], v[140:143], v[124:127]
s_waitcnt lgkmcnt(2)
global_load_dwordx4 v[152:155], v15, s[42:43] offset:0 sc1
v_mfma_f32_16x16x32_bf16 v[112:115], a[192:195], v[144:147], v[112:115]
ds_read_b128 v[140:143], v20 offset:33792
v_mfma_f32_16x16x32_bf16 v[116:119], a[196:199], v[144:147], v[116:119]
v_mfma_f32_16x16x32_bf16 v[120:123], a[200:203], v[144:147], v[120:123]
global_load_dwordx4 v[156:159], v15, s[42:43] offset:64 sc1
s_sub_u32 s42, s42, 18432
s_subb_u32 s43, s43, 0
v_mfma_f32_16x16x32_bf16 v[124:127], a[204:207], v[144:147], v[124:127]
s_waitcnt lgkmcnt(2)
v_mfma_f32_16x16x32_bf16 v[112:115], a[208:211], v[148:151], v[112:115]
ds_read_b128 v[144:147], v20 offset:34816
v_mfma_f32_16x16x32_bf16 v[116:119], a[212:215], v[148:151], v[116:119]
v_mfma_f32_16x16x32_bf16 v[120:123], a[216:219], v[148:151], v[120:123]
v_mfma_f32_16x16x32_bf16 v[124:127], a[220:223], v[148:151], v[124:127]
s_waitcnt lgkmcnt(2)
v_mfma_f32_16x16x32_bf16 v[112:115], a[224:227], v[136:139], v[112:115]
ds_read_b128 v[148:151], v20 offset:35840
v_mfma_f32_16x16x32_bf16 v[116:119], a[228:231], v[136:139], v[116:119]
v_mfma_f32_16x16x32_bf16 v[120:123], a[232:235], v[136:139], v[120:123]
v_mfma_f32_16x16x32_bf16 v[124:127], a[236:239], v[136:139], v[124:127]
s_waitcnt lgkmcnt(2)
v_mfma_f32_16x16x32_bf16 v[112:115], a[240:243], v[140:143], v[112:115]
v_mfma_f32_16x16x32_bf16 v[116:119], a[244:247], v[140:143], v[116:119]
v_xor_b32 v20, 0x10000, v20
v_mfma_f32_16x16x32_bf16 v[120:123], a[248:251], v[140:143], v[120:123]
v_mfma_f32_16x16x32_bf16 v[124:127], a[252:255], v[140:143], v[124:127]
s_waitcnt vmcnt(8)
s_barrier
s_waitcnt lgkmcnt(1)
v_mfma_f32_16x16x32_bf16 v[112:115], v[24:27], v[144:147], v[112:115]
ds_read_b128 v[136:139], v20 offset:5120
ds_read_b128 v[140:143], v20 offset:6144
v_mfma_f32_16x16x32_bf16 v[116:119], v[28:31], v[144:147], v[116:119]
ds_read_b64 v[176:177], v22 offset:57344
ds_read_b64 v[180:181], v22 offset:57856
v_mfma_f32_16x16x32_bf16 v[120:123], v[32:35], v[144:147], v[120:123]
ds_read_b64 v[184:185], v22 offset:58368
ds_read_b64 v[188:189], v22 offset:58880
v_mfma_f32_16x16x32_bf16 v[124:127], v[36:39], v[144:147], v[124:127]
s_waitcnt lgkmcnt(6)
v_mfma_f32_16x16x32_bf16 v[112:115], v[40:43], v[148:151], v[112:115]
ds_read_b128 v[144:147], v20 offset:7168
v_mfma_f32_16x16x32_bf16 v[116:119], v[44:47], v[148:151], v[116:119]
v_mfma_f32_16x16x32_bf16 v[120:123], v[48:51], v[148:151], v[120:123]
v_mfma_f32_16x16x32_bf16 v[124:127], v[52:55], v[148:151], v[124:127]
s_sub_u32 s70, s70, 1
s_cbranch_scc0 .Lwo_top10
s_waitcnt lgkmcnt(0)
v_add_f32 v176, v176, v180
v_add_f32 v177, v177, v181
v_add_f32 v184, v184, v188
v_cvt_pk_bf16_f32 v164, v112, v113
v_cvt_pk_bf16_f32 v165, v114, v115
v_cvt_pk_bf16_f32 v166, v116, v117
v_cvt_pk_bf16_f32 v167, v118, v119
v_add_f32 v185, v185, v189
v_add_f32 v176, v176, v184
v_add_f32 v177, v177, v185
global_store_dwordx4 v13, v[164:167], s[38:39] offset:0 sc1
v_cvt_pk_bf16_f32 v168, v120, v121
v_cvt_pk_bf16_f32 v169, v122, v123
v_cvt_pk_bf16_f32 v170, v124, v125
v_cvt_pk_bf16_f32 v171, v126, v127
v_mov_b32 v203, v176
v_mov_b32 v204, v177
global_store_dwordx4 v13, v[168:171], s[38:39] offset:1024 sc1
v_add_u32 v13, 18688, v13
v_permlane16_swap_b32 v176, v203
v_permlane16_swap_b32 v177, v204
v_add_f32 v176, v176, v203
v_add_f32 v177, v177, v204
v_mov_b32 v203, v176
v_mov_b32 v204, v177
s_nop 0
v_permlane32_swap_b32 v176, v203
v_permlane32_swap_b32 v177, v204
v_add_f32 v176, v176, v203
v_add_f32 v177, v177, v204
global_store_dwordx2 v14, v[176:177], s[38:39] sc1
v_add_u32 v14, 18688, v14
v_add_f32 v200, v112, v113
v_mul_f32 v201, v112, v112
v_fmac_f32 v201, v113, v113
v_add_f32 v200, v200, v114
v_fmac_f32 v201, v114, v114
v_add_f32 v200, v200, v115
v_fmac_f32 v201, v115, v115
v_add_f32 v200, v200, v116
v_fmac_f32 v201, v116, v116
v_add_f32 v200, v200, v117
v_fmac_f32 v201, v117, v117
v_add_f32 v200, v200, v118
v_fmac_f32 v201, v118, v118
v_add_f32 v200, v200, v119
v_fmac_f32 v201, v119, v119
v_add_f32 v200, v200, v120
v_fmac_f32 v201, v120, v120
v_add_f32 v200, v200, v121
v_fmac_f32 v201, v121, v121
v_add_f32 v200, v200, v122
v_fmac_f32 v201, v122, v122
v_add_f32 v200, v200, v123
v_fmac_f32 v201, v123, v123
v_add_f32 v200, v200, v124
v_fmac_f32 v201, v124, v124
v_add_f32 v200, v200, v125
v_fmac_f32 v201, v125, v125
v_add_f32 v200, v200, v126
v_fmac_f32 v201, v126, v126
v_add_f32 v200, v200, v127
v_fmac_f32 v201, v127, v127
ds_write_b64 v22, v[200:201] offset:59904
v_add_u32 v11, 18432, v11
v_min_u32 v202, v11, v12
s_waitcnt vmcnt(0) lgkmcnt(0)
s_barrier
ds_read_b64 v[176:177], v22 offset:59392
ds_read_b64 v[180:181], v22 offset:59904
ds_read_b64 v[184:185], v22 offset:60416
ds_read_b64 v[188:189], v22 offset:60928
s_waitcnt lgkmcnt(2)
v_add_f32 v176, v176, v180
v_add_f32 v177, v177, v181
s_waitcnt lgkmcnt(0)
v_add_f32 v184, v184, v188
v_add_f32 v185, v185, v189
v_add_f32 v176, v176, v184
v_add_f32 v177, v177, v185
v_mov_b32 v203, v176
v_mov_b32 v204, v177
s_nop 0
v_permlane16_swap_b32 v176, v203
v_permlane16_swap_b32 v177, v204
v_add_f32 v176, v176, v203
v_add_f32 v177, v177, v204
v_mov_b32 v203, v176
v_mov_b32 v204, v177
s_nop 0
v_permlane32_swap_b32 v176, v203
v_permlane32_swap_b32 v177, v204
v_add_f32 v176, v176, v203
v_add_f32 v177, v177, v204
global_store_dwordx2 v14, v[176:177], s[38:39] sc1
v_add_u32 v14, 18688, v14
s_waitcnt vmcnt(0)
s_barrier
s_endpgm
.Lwo_w3:
s_add_u32 m0, s46, 4096
s_nop 0
global_load_lds_dwordx4 v0, s[40:41] offset:-4096
global_load_lds_dwordx4 v1, s[40:41] offset:-3072
global_load_lds_dwordx4 v2, s[40:41] offset:-2048
global_load_lds_dwordx4 v3, s[40:41] offset:-1024
global_load_lds_dwordx4 v4, s[40:41] offset:0
global_load_lds_dwordx4 v5, s[40:41] offset:1024
global_load_lds_dwordx4 v6, s[40:41] offset:2048
global_load_lds_dwordx4 v7, s[40:41] offset:3072
s_add_u32 m0, s46, 12288
s_nop 0
global_load_lds_dwordx4 v8, s[40:41] offset:-4096
s_add_u32 s40, s40, 73728
s_addc_u32 s41, s41, 0
s_add_u32 m0, s46, 40960
s_nop 0
global_load_lds_dwordx4 v0, s[40:41] offset:-4096
global_load_lds_dwordx4 v1, s[40:41] offset:-3072
global_load_lds_dwordx4 v2, s[40:41] offset:-2048
global_load_lds_dwordx4 v3, s[40:41] offset:-1024
global_load_lds_dwordx4 v4, s[40:41] offset:0
global_load_lds_dwordx4 v5, s[40:41] offset:1024
global_load_lds_dwordx4 v6, s[40:41] offset:2048
global_load_lds_dwordx4 v7, s[40:41] offset:3072
s_add_u32 m0, s46, 49152
s_nop 0
global_load_lds_dwordx4 v8, s[40:41] offset:-4096
s_add_u32 s40, s40, 73728
s_addc_u32 s41, s41, 0
s_add_u32 m0, s46, 77824
s_nop 0
global_load_lds_dwordx4 v0, s[40:41] offset:-4096
global_load_lds_dwordx4 v1, s[40:41] offset:-3072
global_load_lds_dwordx4 v2, s[40:41] offset:-2048
global_load_lds_dwordx4 v3, s[40:41] offset:-1024
global_load_lds_dwordx4 v4, s[40:41] offset:0
global_load_lds_dwordx4 v5, s[40:41] offset:1024
global_load_lds_dwordx4 v6, s[40:41] offset:2048
global_load_lds_dwordx4 v7, s[40:41] offset:3072
s_add_u32 m0, s46, 86016
s_nop 0
global_load_lds_dwordx4 v8, s[40:41] offset:-4096
s_add_u32 s40, s40, 73728
s_addc_u32 s41, s41, 0
s_add_u32 m0, s46, 114688
s_nop 0
global_load_lds_dwordx4 v0, s[40:41] offset:-4096
global_load_lds_dwordx4 v1, s[40:41] offset:-3072
global_load_lds_dwordx4 v2, s[40:41] offset:-2048
global_load_lds_dwordx4 v3, s[40:41] offset:-1024
global_load_lds_dwordx4 v4, s[40:41] offset:0
global_load_lds_dwordx4 v5, s[40:41] offset:1024
global_load_lds_dwordx4 v6, s[40:41] offset:2048
global_load_lds_dwordx4 v7, s[40:41] offset:3072
s_add_u32 m0, s46, 122880
s_nop 0
global_load_lds_dwordx4 v8, s[40:41] offset:-4096
s_add_u32 s40, s40, 73728
s_addc_u32 s41, s41, 0
v_add_u32 v205, 0x10000, v9
v_add_u32 v206, 0x20000, v9
v_add_u32 v207, 0x10000, v10
v_add_u32 v208, 0x20000, v10
s_waitcnt lgkmcnt(0)
s_mul_i32 s37, s37, 0x9E3779B1
s_add_u32 s37, s37, 0x7F4A7C15
v_add_u32 v17, s37, v17
v_add_u32 v19, s37, v19
s_waitcnt vmcnt(27)
s_barrier
ds_read_b64_tr_b16 a[0:1], v9 offset:0
ds_read_b64_tr_b16 a[2:3], v9 offset:2304
ds_read_b64_tr_b16 a[4:5], v9 offset:8
ds_read_b64_tr_b16 a[6:7], v9 offset:2312
ds_read_b64_tr_b16 a[8:9], v9 offset:64
ds_read_b64_tr_b16 a[10:11], v9 offset:2368
ds_read_b64_tr_b16 a[12:13], v9 offset:72
ds_read_b64_tr_b16 a[14:15], v9 offset:2376
ds_read_b64_tr_b16 a[16:17], v9 offset:18432
ds_read_b64_tr_b16 a[18:19], v9 offset:20736
ds_read_b64_tr_b16 a[20:21], v9 offset:18440
ds_read_b64_tr_b16 a[22:23], v9 offset:20744
ds_read_b64_tr_b16 a[24:25], v9 offset:18496
ds_read_b64_tr_b16 a[26:27], v9 offset:20800
ds_read_b64_tr_b16 a[28:29], v9 offset:18504
ds_read_b64_tr_b16 a[30:31], v9 offset:20808
s_waitcnt lgkmcnt(0)
s_barrier
s_add_u32 m0, s46, 4096
s_nop 0
global_load_lds_dwordx4 v0, s[40:41] offset:-4096
global_load_lds_dwordx4 v1, s[40:41] offset:-3072
global_load_lds_dwordx4 v2, s[40:41] offset:-2048
global_load_lds_dwordx4 v3, s[40:41] offset:-1024
global_load_lds_dwordx4 v4, s[40:41] offset:0
global_load_lds_dwordx4 v5, s[40:41] offset:1024
global_load_lds_dwordx4 v6, s[40:41] offset:2048
global_load_lds_dwordx4 v7, s[40:41] offset:3072
s_add_u32 m0, s46, 12288
s_nop 0
global_load_lds_dwordx4 v8, s[40:41] offset:-4096
s_add_u32 s40, s40, 73728
s_addc_u32 s41, s41, 0
s_waitcnt vmcnt(27)
s_barrier
ds_read_b64_tr_b16 a[32:33], v9 offset:36864
ds_read_b64_tr_b16 a[34:35], v9 offset:39168
ds_read_b64_tr_b16 a[36:37], v9 offset:36872
ds_read_b64_tr_b16 a[38:39], v9 offset:39176
ds_read_b64_tr_b16 a[40:41], v9 offset:36928
ds_read_b64_tr_b16 a[42:43], v9 offset:39232
ds_read_b64_tr_b16 a[44:45], v9 offset:36936
ds_read_b64_tr_b16 a[46:47], v9 offset:39240
ds_read_b64_tr_b16 a[48:49], v9 offset:55296
ds_read_b64_tr_b16 a[50:51], v9 offset:57600
ds_read_b64_tr_b16 a[52:53], v9 offset:55304
ds_read_b64_tr_b16 a[54:55], v9 offset:57608
ds_read_b64_tr_b16 a[56:57], v9 offset:55360
ds_read_b64_tr_b16 a[58:59], v9 offset:57664
ds_read_b64_tr_b16 a[60:61], v9 offset:55368
ds_read_b64_tr_b16 a[62:63], v9 offset:57672
s_waitcnt lgkmcnt(0)
s_barrier
s_add_u32 m0, s46, 40960
s_nop 0
global_load_lds_dwordx4 v0, s[40:41] offset:-4096
global_load_lds_dwordx4 v1, s[40:41] offset:-3072
global_load_lds_dwordx4 v2, s[40:41] offset:-2048
global_load_lds_dwordx4 v3, s[40:41] offset:-1024
global_load_lds_dwordx4 v4, s[40:41] offset:0
global_load_lds_dwordx4 v5, s[40:41] offset:1024
global_load_lds_dwordx4 v6, s[40:41] offset:2048
global_load_lds_dwordx4 v7, s[40:41] offset:3072
s_add_u32 m0, s46, 49152
s_nop 0
global_load_lds_dwordx4 v8, s[40:41] offset:-4096
s_add_u32 s40, s40, 73728
s_addc_u32 s41, s41, 0
s_waitcnt vmcnt(27)
s_barrier
ds_read_b64_tr_b16 a[64:65], v205 offset:8192
ds_read_b64_tr_b16 a[66:67], v205 offset:10496
ds_read_b64_tr_b16 a[68:69], v205 offset:8200
ds_read_b64_tr_b16 a[70:71], v205 offset:10504
ds_read_b64_tr_b16 a[72:73], v205 offset:8256
ds_read_b64_tr_b16 a[74:75], v205 offset:10560
ds_read_b64_tr_b16 a[76:77], v205 offset:8264
ds_read_b64_tr_b16 a[78:79], v205 offset:10568
ds_read_b64_tr_b16 a[80:81], v205 offset:26624
ds_read_b64_tr_b16 a[82:83], v205 offset:28928
ds_read_b64_tr_b16 a[84:85], v205 offset:26632
ds_read_b64_tr_b16 a[86:87], v205 offset:28936
ds_read_b64_tr_b16 a[88:89], v205 offset:26688
ds_read_b64_tr_b16 a[90:91], v205 offset:28992
ds_read_b64_tr_b16 a[92:93], v205 offset:26696
ds_read_b64_tr_b16 a[94:95], v205 offset:29000
s_waitcnt lgkmcnt(0)
s_barrier
s_add_u32 m0, s46, 77824
s_nop 0
global_load_lds_dwordx4 v0, s[40:41] offset:-4096
global_load_lds_dwordx4 v1, s[40:41] offset:-3072
global_load_lds_dwordx4 v2, s[40:41] offset:-2048
global_load_lds_dwordx4 v3, s[40:41] offset:-1024
global_load_lds_dwordx4 v4, s[40:41] offset:0
global_load_lds_dwordx4 v5, s[40:41] offset:1024
global_load_lds_dwordx4 v6, s[40:41] offset:2048
global_load_lds_dwordx4 v7, s[40:41] offset:3072
s_add_u32 m0, s46, 86016
s_nop 0
global_load_lds_dwordx4 v8, s[40:41] offset:-4096
s_add_u32 s40, s40, 73728
s_addc_u32 s41, s41, 0
s_waitcnt vmcnt(27)
s_barrier
ds_read_b64_tr_b16 a[96:97], v205 offset:45056
ds_read_b64_tr_b16 a[98:99], v205 offset:47360
ds_read_b64_tr_b16 a[100:101], v205 offset:45064
ds_read_b64_tr_b16 a[102:103], v205 offset:47368
ds_read_b64_tr_b16 a[104:105], v205 offset:45120
ds_read_b64_tr_b16 a[106:107], v205 offset:47424
ds_read_b64_tr_b16 a[108:109], v205 offset:45128
ds_read_b64_tr_b16 a[110:111], v205 offset:47432
ds_read_b64_tr_b16 a[112:113], v205 offset:63488
ds_read_b64_tr_b16 a[114:115], v206 offset:256
ds_read_b64_tr_b16 a[116:117], v205 offset:63496
ds_read_b64_tr_b16 a[118:119], v206 offset:264
ds_read_b64_tr_b16 a[120:121], v205 offset:63552
ds_read_b64_tr_b16 a[122:123], v206 offset:320
ds_read_b64_tr_b16 a[124:125], v205 offset:63560
ds_read_b64_tr_b16 a[126:127], v206 offset:328
s_waitcnt lgkmcnt(0)
s_barrier
s_add_u32 m0, s46, 114688
s_nop 0
global_load_lds_dwordx4 v0, s[40:41] offset:-4096
global_load_lds_dwordx4 v1, s[40:41] offset:-3072
global_load_lds_dwordx4 v2, s[40:41] offset:-2048
global_load_lds_dwordx4 v3, s[40:41] offset:-1024
global_load_lds_dwordx4 v4, s[40:41] offset:0
global_load_lds_dwordx4 v5, s[40:41] offset:1024
global_load_lds_dwordx4 v6, s[40:41] offset:2048
global_load_lds_dwordx4 v7, s[40:41] offset:3072
s_add_u32 m0, s46, 122880
s_nop 0
global_load_lds_dwordx4 v8, s[40:41] offset:-4096
s_add_u32 s40, s40, 73728
s_addc_u32 s41, s41, 0
s_waitcnt vmcnt(27)
s_barrier
ds_read_b64_tr_b16 a[128:129], v9 offset:0
ds_read_b64_tr_b16 a[130:131], v9 offset:2304
ds_read_b64_tr_b16 a[132:133], v9 offset:8
ds_read_b64_tr_b16 a[134:135], v9 offset:2312
ds_read_b64_tr_b16 a[136:137], v9 offset:64
ds_read_b64_tr_b16 a[138:139], v9 offset:2368
ds_read_b64_tr_b16 a[140:141], v9 offset:72
ds_read_b64_tr_b16 a[142:143], v9 offset:2376
ds_read_b64_tr_b16 a[144:145], v9 offset:18432
ds_read_b64_tr_b16 a[146:147], v9 offset:20736
ds_read_b64_tr_b16 a[148:149], v9 offset:18440
ds_read_b64_tr_b16 a[150:151], v9 offset:20744
ds_read_b64_tr_b16 a[152:153], v9 offset:18496
ds_read_b64_tr_b16 a[154:155], v9 offset:20800
)"
R"(ds_read_b64_tr_b16 a[156:157], v9 offset:18504
ds_read_b64_tr_b16 a[158:159], v9 offset:20808
s_waitcnt lgkmcnt(0)
s_barrier
s_add_u32 m0, s46, 4096
s_nop 0
global_load_lds_dwordx4 v0, s[40:41] offset:-4096
global_load_lds_dwordx4 v1, s[40:41] offset:-3072
global_load_lds_dwordx4 v2, s[40:41] offset:-2048
global_load_lds_dwordx4 v3, s[40:41] offset:-1024
global_load_lds_dwordx4 v4, s[40:41] offset:0
global_load_lds_dwordx4 v5, s[40:41] offset:1024
global_load_lds_dwordx4 v6, s[40:41] offset:2048
global_load_lds_dwordx4 v7, s[40:41] offset:3072
s_add_u32 m0, s46, 12288
s_nop 0
global_load_lds_dwordx4 v8, s[40:41] offset:-4096
s_add_u32 s40, s40, 73728
s_addc_u32 s41, s41, 0
s_waitcnt vmcnt(27)
s_barrier
ds_read_b64_tr_b16 a[160:161], v9 offset:36864
ds_read_b64_tr_b16 a[162:163], v9 offset:39168
ds_read_b64_tr_b16 a[164:165], v9 offset:36872
ds_read_b64_tr_b16 a[166:167], v9 offset:39176
ds_read_b64_tr_b16 a[168:169], v9 offset:36928
ds_read_b64_tr_b16 a[170:171], v9 offset:39232
ds_read_b64_tr_b16 a[172:173], v9 offset:36936
ds_read_b64_tr_b16 a[174:175], v9 offset:39240
ds_read_b64_tr_b16 v[56:57], v10 offset:36864
ds_read_b64_tr_b16 v[58:59], v10 offset:39168
ds_read_b64_tr_b16 v[60:61], v10 offset:36872
ds_read_b64_tr_b16 v[62:63], v10 offset:39176
ds_read_b64_tr_b16 a[176:177], v9 offset:55296
ds_read_b64_tr_b16 a[178:179], v9 offset:57600
ds_read_b64_tr_b16 a[180:181], v9 offset:55304
ds_read_b64_tr_b16 a[182:183], v9 offset:57608
ds_read_b64_tr_b16 a[184:185], v9 offset:55360
ds_read_b64_tr_b16 a[186:187], v9 offset:57664
ds_read_b64_tr_b16 a[188:189], v9 offset:55368
ds_read_b64_tr_b16 a[190:191], v9 offset:57672
ds_read_b64_tr_b16 v[64:65], v10 offset:55296
ds_read_b64_tr_b16 v[66:67], v10 offset:57600
ds_read_b64_tr_b16 v[68:69], v10 offset:55304
ds_read_b64_tr_b16 v[70:71], v10 offset:57608
s_waitcnt vmcnt(18)
s_barrier
ds_read_b64_tr_b16 a[192:193], v205 offset:8192
ds_read_b64_tr_b16 a[194:195], v205 offset:10496
ds_read_b64_tr_b16 a[196:197], v205 offset:8200
ds_read_b64_tr_b16 a[198:199], v205 offset:10504
ds_read_b64_tr_b16 a[200:201], v205 offset:8256
ds_read_b64_tr_b16 a[202:203], v205 offset:10560
ds_read_b64_tr_b16 a[204:205], v205 offset:8264
ds_read_b64_tr_b16 a[206:207], v205 offset:10568
ds_read_b64_tr_b16 v[72:73], v207 offset:8192
ds_read_b64_tr_b16 v[74:75], v207 offset:10496
ds_read_b64_tr_b16 v[76:77], v207 offset:8200
ds_read_b64_tr_b16 v[78:79], v207 offset:10504
ds_read_b64_tr_b16 a[208:209], v205 offset:26624
ds_read_b64_tr_b16 a[210:211], v205 offset:28928
ds_read_b64_tr_b16 a[212:213], v205 offset:26632
ds_read_b64_tr_b16 a[214:215], v205 offset:28936
ds_read_b64_tr_b16 a[216:217], v205 offset:26688
ds_read_b64_tr_b16 a[218:219], v205 offset:28992
ds_read_b64_tr_b16 a[220:221], v205 offset:26696
ds_read_b64_tr_b16 a[222:223], v205 offset:29000
ds_read_b64_tr_b16 v[80:81], v207 offset:26624
ds_read_b64_tr_b16 v[82:83], v207 offset:28928
ds_read_b64_tr_b16 v[84:85], v207 offset:26632
ds_read_b64_tr_b16 v[86:87], v207 offset:28936
s_waitcnt vmcnt(9)
s_barrier
ds_read_b64_tr_b16 a[224:225], v205 offset:45056
ds_read_b64_tr_b16 a[226:227], v205 offset:47360
ds_read_b64_tr_b16 a[228:229], v205 offset:45064
ds_read_b64_tr_b16 a[230:231], v205 offset:47368
ds_read_b64_tr_b16 a[232:233], v205 offset:45120
ds_read_b64_tr_b16 a[234:235], v205 offset:47424
ds_read_b64_tr_b16 a[236:237], v205 offset:45128
ds_read_b64_tr_b16 a[238:239], v205 offset:47432
ds_read_b64_tr_b16 a[240:241], v205 offset:63488
ds_read_b64_tr_b16 a[242:243], v206 offset:256
ds_read_b64_tr_b16 a[244:245], v205 offset:63496
ds_read_b64_tr_b16 a[246:247], v206 offset:264
ds_read_b64_tr_b16 a[248:249], v205 offset:63552
ds_read_b64_tr_b16 a[250:251], v206 offset:320
ds_read_b64_tr_b16 a[252:253], v205 offset:63560
ds_read_b64_tr_b16 a[254:255], v206 offset:328
s_waitcnt vmcnt(0)
s_barrier
ds_read_b64_tr_b16 v[24:25], v9 offset:0
ds_read_b64_tr_b16 v[26:27], v9 offset:2304
ds_read_b64_tr_b16 v[28:29], v9 offset:8
ds_read_b64_tr_b16 v[30:31], v9 offset:2312
ds_read_b64_tr_b16 v[32:33], v9 offset:64
ds_read_b64_tr_b16 v[34:35], v9 offset:2368
ds_read_b64_tr_b16 v[36:37], v9 offset:72
ds_read_b64_tr_b16 v[38:39], v9 offset:2376
ds_read_b64_tr_b16 v[40:41], v9 offset:18432
ds_read_b64_tr_b16 v[42:43], v9 offset:20736
ds_read_b64_tr_b16 v[44:45], v9 offset:18440
ds_read_b64_tr_b16 v[46:47], v9 offset:20744
ds_read_b64_tr_b16 v[48:49], v9 offset:18496
ds_read_b64_tr_b16 v[50:51], v9 offset:20800
ds_read_b64_tr_b16 v[52:53], v9 offset:18504
ds_read_b64_tr_b16 v[54:55], v9 offset:20808
s_waitcnt lgkmcnt(0)
s_barrier
v_mov_b32 v202, v11
global_load_dword v23, v16, s[38:39] sc1
s_waitcnt vmcnt(0)
v_sub_u32 v23, v23, v17
v_cmp_lt_u32 vcc, 0x7f, v23
s_cbranch_vccnz .Lwo_sp17
.Lwo_rt18:
v_add_u32 v17, 1, v17
s_barrier
s_mov_b32 s70, 32
s_waitcnt vmcnt(0) lgkmcnt(0)
s_nop 7
.p2align 6
.Lwo_top19:
s_waitcnt lgkmcnt(2)
v_mfma_f32_16x16x32_bf16 v[96:99], a[160:163], v[136:139], v[96:99]
ds_read_b128 v[148:151], v20 offset:13312
v_mfma_f32_16x16x32_bf16 v[100:103], a[164:167], v[136:139], v[100:103]
s_cmp_eq_u32 s70, 16
s_cbranch_scc1 .Lwo_hk15
.Lwo_hr16:
v_mfma_f32_16x16x32_bf16 v[104:107], a[168:171], v[136:139], v[104:107]
v_mfma_f32_16x16x32_bf16 v[108:111], a[172:175], v[136:139], v[108:111]
v_cvt_pk_bf16_f32 v164, v112, v113
v_cvt_pk_bf16_f32 v165, v114, v115
v_mfma_f32_16x16x32_bf16 v[128:131], v[56:59], v[136:139], 0
v_cvt_pk_bf16_f32 v166, v116, v117
v_cvt_pk_bf16_f32 v167, v118, v119
v_mfma_f32_16x16x32_bf16 v[132:135], v[60:63], v[136:139], 0
s_waitcnt lgkmcnt(2)
global_load_dword v23, v16, s[38:39] sc1
v_mfma_f32_16x16x32_bf16 v[96:99], a[176:179], v[140:143], v[96:99]
ds_read_b128 v[136:139], v20 offset:0
v_mfma_f32_16x16x32_bf16 v[100:103], a[180:183], v[140:143], v[100:103]
v_mfma_f32_16x16x32_bf16 v[104:107], a[184:187], v[140:143], v[104:107]
global_store_dwordx4 v13, v[164:167], s[38:39] offset:0 sc1
v_mfma_f32_16x16x32_bf16 v[108:111], a[188:191], v[140:143], v[108:111]
v_cvt_pk_bf16_f32 v168, v120, v121
v_cvt_pk_bf16_f32 v169, v122, v123
v_mfma_f32_16x16x32_bf16 v[128:131], v[64:67], v[140:143], v[128:131]
v_cvt_pk_bf16_f32 v170, v124, v125
v_cvt_pk_bf16_f32 v171, v126, v127
v_mfma_f32_16x16x32_bf16 v[132:135], v[68:71], v[140:143], v[132:135]
s_waitcnt lgkmcnt(2)
v_mfma_f32_16x16x32_bf16 v[96:99], a[192:195], v[144:147], v[96:99]
ds_read_b128 v[140:143], v20 offset:1024
v_mfma_f32_16x16x32_bf16 v[100:103], a[196:199], v[144:147], v[100:103]
global_store_dwordx4 v13, v[168:171], s[38:39] offset:1024 sc1
v_mfma_f32_16x16x32_bf16 v[104:107], a[200:203], v[144:147], v[104:107]
v_add_u32 v13, 18688, v13
v_add_f32 v200, v112, v113
v_mfma_f32_16x16x32_bf16 v[108:111], a[204:207], v[144:147], v[108:111]
v_mul_f32 v201, v112, v112
v_fmac_f32 v201, v113, v113
v_mfma_f32_16x16x32_bf16 v[128:131], v[72:75], v[144:147], v[128:131]
v_add_f32 v200, v200, v114
v_fmac_f32 v201, v114, v114
v_mfma_f32_16x16x32_bf16 v[132:135], v[76:79], v[144:147], v[132:135]
s_xor_b32 s47, s47, 0x10000
v_add_f32 v200, v200, v115
s_waitcnt lgkmcnt(2)
v_mfma_f32_16x16x32_bf16 v[96:99], a[208:211], v[148:151], v[96:99]
s_add_u32 m0, s47, 0
ds_read_b128 v[144:147], v20 offset:2048
v_fmac_f32 v201, v115, v115
v_mfma_f32_16x16x32_bf16 v[100:103], a[212:215], v[148:151], v[100:103]
v_add_f32 v200, v200, v116
v_fmac_f32 v201, v116, v116
v_mfma_f32_16x16x32_bf16 v[104:107], a[216:219], v[148:151], v[104:107]
v_add_f32 v200, v200, v117
v_fmac_f32 v201, v117, v117
v_mfma_f32_16x16x32_bf16 v[108:111], a[220:223], v[148:151], v[108:111]
v_add_f32 v200, v200, v118
v_fmac_f32 v201, v118, v118
v_mfma_f32_16x16x32_bf16 v[128:131], v[80:83], v[148:151], v[128:131]
v_add_f32 v200, v200, v119
v_fmac_f32 v201, v119, v119
v_mfma_f32_16x16x32_bf16 v[132:135], v[84:87], v[148:151], v[132:135]
s_waitcnt lgkmcnt(2)
v_add_f32 v200, v200, v120
v_fmac_f32 v201, v120, v120
v_mfma_f32_16x16x32_bf16 v[96:99], a[0:3], v[136:139], v[96:99]
ds_read_b128 v[148:151], v20 offset:3072
global_load_lds_dwordx4 v202, s[38:39] offset:-2048 sc1
v_mfma_f32_16x16x32_bf16 v[100:103], a[4:7], v[136:139], v[100:103]
v_add_f32 v200, v200, v121
v_fmac_f32 v201, v121, v121
v_mfma_f32_16x16x32_bf16 v[104:107], a[8:11], v[136:139], v[104:107]
v_add_f32 v200, v200, v122
v_fmac_f32 v201, v122, v122
v_mfma_f32_16x16x32_bf16 v[108:111], a[12:15], v[136:139], v[108:111]
s_waitcnt lgkmcnt(2)
v_add_f32 v200, v200, v123
v_fmac_f32 v201, v123, v123
v_mfma_f32_16x16x32_bf16 v[96:99], a[16:19], v[140:143], v[96:99]
ds_read_b128 v[136:139], v20 offset:4096
v_add_f32 v200, v200, v124
v_mfma_f32_16x16x32_bf16 v[100:103], a[20:23], v[140:143], v[100:103]
v_fmac_f32 v201, v124, v124
v_add_f32 v200, v200, v125
v_mfma_f32_16x16x32_bf16 v[104:107], a[24:27], v[140:143], v[104:107]
v_fmac_f32 v201, v125, v125
v_add_f32 v200, v200, v126
v_mfma_f32_16x16x32_bf16 v[108:111], a[28:31], v[140:143], v[108:111]
s_waitcnt lgkmcnt(2)
global_load_lds_dwordx4 v202, s[38:39] offset:-1024 sc1
v_fmac_f32 v201, v126, v126
v_mfma_f32_16x16x32_bf16 v[96:99], a[32:35], v[144:147], v[96:99]
ds_read_b128 v[140:143], v20 offset:5120
v_add_f32 v200, v200, v127
v_mfma_f32_16x16x32_bf16 v[100:103], a[36:39], v[144:147], v[100:103]
ds_write_b128 v21, v[128:131] offset:45056
v_mfma_f32_16x16x32_bf16 v[104:107], a[40:43], v[144:147], v[104:107]
ds_write_b128 v21, v[132:135] offset:46080
v_mfma_f32_16x16x32_bf16 v[108:111], a[44:47], v[144:147], v[108:111]
s_waitcnt lgkmcnt(4)
v_fmac_f32 v201, v127, v127
v_mfma_f32_16x16x32_bf16 v[96:99], a[48:51], v[148:151], v[96:99]
ds_read_b128 v[144:147], v20 offset:6144
v_mfma_f32_16x16x32_bf16 v[100:103], a[52:55], v[148:151], v[100:103]
ds_write_b64 v22, v[200:201] offset:60416
v_mfma_f32_16x16x32_bf16 v[104:107], a[56:59], v[148:151], v[104:107]
global_load_lds_dwordx4 v202, s[38:39] offset:0 sc1
v_mfma_f32_16x16x32_bf16 v[108:111], a[60:63], v[148:151], v[108:111]
s_waitcnt lgkmcnt(5)
v_mfma_f32_16x16x32_bf16 v[96:99], a[64:67], v[136:139], v[96:99]
ds_read_b128 v[148:151], v20 offset:7168
v_mfma_f32_16x16x32_bf16 v[100:103], a[68:71], v[136:139], v[100:103]
v_mfma_f32_16x16x32_bf16 v[104:107], a[72:75], v[136:139], v[104:107]
v_mfma_f32_16x16x32_bf16 v[108:111], a[76:79], v[136:139], v[108:111]
s_waitcnt lgkmcnt(5)
v_mfma_f32_16x16x32_bf16 v[96:99], a[80:83], v[140:143], v[96:99]
s_waitcnt vmcnt(7)
ds_read_b128 v[136:139], v20 offset:8192
v_lshlrev_b32 v112, 16, v152
v_mfma_f32_16x16x32_bf16 v[100:103], a[84:87], v[140:143], v[100:103]
global_load_lds_dwordx4 v202, s[38:39] offset:1024 sc1
v_add_u32 v11, 18432, v11
v_mfma_f32_16x16x32_bf16 v[104:107], a[88:91], v[140:143], v[104:107]
v_min_u32 v202, v11, v12
v_and_b32 v113, 0xffff0000, v152
v_mfma_f32_16x16x32_bf16 v[108:111], a[92:95], v[140:143], v[108:111]
s_waitcnt lgkmcnt(3)
v_lshlrev_b32 v114, 16, v153
v_and_b32 v115, 0xffff0000, v153
v_mfma_f32_16x16x32_bf16 v[96:99], a[96:99], v[144:147], v[96:99]
ds_read_b128 v[140:143], v20 offset:9216
v_lshlrev_b32 v116, 16, v154
v_mfma_f32_16x16x32_bf16 v[100:103], a[100:103], v[144:147], v[100:103]
v_and_b32 v117, 0xffff0000, v154
v_lshlrev_b32 v118, 16, v155
v_mfma_f32_16x16x32_bf16 v[104:107], a[104:107], v[144:147], v[104:107]
s_waitcnt vmcnt(7)
v_and_b32 v119, 0xffff0000, v155
v_lshlrev_b32 v120, 16, v156
v_mfma_f32_16x16x32_bf16 v[108:111], a[108:111], v[144:147], v[108:111]
s_waitcnt lgkmcnt(2)
v_and_b32 v121, 0xffff0000, v156
v_lshlrev_b32 v122, 16, v157
v_mfma_f32_16x16x32_bf16 v[96:99], a[112:115], v[148:151], v[96:99]
ds_read_b128 v[144:147], v20 offset:14336
v_and_b32 v123, 0xffff0000, v157
v_mfma_f32_16x16x32_bf16 v[100:103], a[116:119], v[148:151], v[100:103]
v_lshlrev_b32 v124, 16, v158
v_and_b32 v125, 0xffff0000, v158
v_mfma_f32_16x16x32_bf16 v[104:107], a[120:123], v[148:151], v[104:107]
v_lshlrev_b32 v126, 16, v159
v_and_b32 v127, 0xffff0000, v159
v_mfma_f32_16x16x32_bf16 v[108:111], a[124:127], v[148:151], v[108:111]
s_waitcnt lgkmcnt(2)
global_load_dwordx4 v[152:155], v15, s[42:43] offset:0 sc1
v_mfma_f32_16x16x32_bf16 v[96:99], a[128:131], v[136:139], v[96:99]
ds_read_b128 v[148:151], v20 offset:15360
v_mfma_f32_16x16x32_bf16 v[100:103], a[132:135], v[136:139], v[100:103]
v_mfma_f32_16x16x32_bf16 v[104:107], a[136:139], v[136:139], v[104:107]
global_load_dwordx4 v[156:159], v15, s[42:43] offset:64 sc1
s_sub_u32 s42, s42, 18432
s_subb_u32 s43, s43, 0
v_mfma_f32_16x16x32_bf16 v[108:111], a[140:143], v[136:139], v[108:111]
s_waitcnt lgkmcnt(2)
v_mfma_f32_16x16x32_bf16 v[96:99], a[144:147], v[140:143], v[96:99]
ds_read_b128 v[136:139], v20 offset:16384
v_mfma_f32_16x16x32_bf16 v[100:103], a[148:151], v[140:143], v[100:103]
v_mfma_f32_16x16x32_bf16 v[104:107], a[152:155], v[140:143], v[104:107]
v_mfma_f32_16x16x32_bf16 v[108:111], a[156:159], v[140:143], v[108:111]
s_waitcnt lgkmcnt(2)
v_mfma_f32_16x16x32_bf16 v[96:99], a[224:227], v[144:147], v[96:99]
ds_read_b128 v[140:143], v20 offset:17408
v_mfma_f32_16x16x32_bf16 v[100:103], a[228:231], v[144:147], v[100:103]
v_mfma_f32_16x16x32_bf16 v[104:107], a[232:235], v[144:147], v[104:107]
s_waitcnt vmcnt(8)
v_sub_u32 v23, v23, v17
v_cmp_lt_u32 vcc, 0x7f, v23
v_mfma_f32_16x16x32_bf16 v[108:111], a[236:239], v[144:147], v[108:111]
s_waitcnt lgkmcnt(2)
v_mfma_f32_16x16x32_bf16 v[96:99], a[240:243], v[148:151], v[96:99]
v_mfma_f32_16x16x32_bf16 v[100:103], a[244:247], v[148:151], v[100:103]
s_cbranch_vccnz .Lwo_sp11
.Lwo_rt12:
v_mfma_f32_16x16x32_bf16 v[104:107], a[248:251], v[148:151], v[104:107]
v_mfma_f32_16x16x32_bf16 v[108:111], a[252:255], v[148:151], v[108:111]
s_waitcnt vmcnt(6)
s_barrier
s_waitcnt lgkmcnt(1)
v_mfma_f32_16x16x32_bf16 v[96:99], v[24:27], v[136:139], v[96:99]
ds_read_b128 v[144:147], v20 offset:28672
ds_read_b128 v[148:151], v20 offset:29696
v_mfma_f32_16x16x32_bf16 v[100:103], v[28:31], v[136:139], v[100:103]
v_add_u32 v17, 1, v17
v_mfma_f32_16x16x32_bf16 v[104:107], v[32:35], v[136:139], v[104:107]
v_mfma_f32_16x16x32_bf16 v[108:111], v[36:39], v[136:139], v[108:111]
s_waitcnt lgkmcnt(2)
v_mfma_f32_16x16x32_bf16 v[96:99], v[40:43], v[140:143], v[96:99]
ds_read_b128 v[136:139], v20 offset:30720
v_mfma_f32_16x16x32_bf16 v[100:103], v[44:47], v[140:143], v[100:103]
v_mfma_f32_16x16x32_bf16 v[104:107], v[48:51], v[140:143], v[104:107]
v_mfma_f32_16x16x32_bf16 v[108:111], v[52:55], v[140:143], v[108:111]
s_waitcnt lgkmcnt(2)
v_mfma_f32_16x16x32_bf16 v[112:115], a[160:163], v[144:147], v[112:115]
ds_read_b128 v[140:143], v20 offset:31744
v_mfma_f32_16x16x32_bf16 v[116:119], a[164:167], v[144:147], v[116:119]
v_mfma_f32_16x16x32_bf16 v[120:123], a[168:171], v[144:147], v[120:123]
v_mfma_f32_16x16x32_bf16 v[124:127], a[172:175], v[144:147], v[124:127]
v_cvt_pk_bf16_f32 v164, v96, v97
v_cvt_pk_bf16_f32 v165, v98, v99
v_mfma_f32_16x16x32_bf16 v[128:131], v[56:59], v[144:147], 0
v_cvt_pk_bf16_f32 v166, v100, v101
v_cvt_pk_bf16_f32 v167, v102, v103
v_mfma_f32_16x16x32_bf16 v[132:135], v[60:63], v[144:147], 0
s_waitcnt lgkmcnt(2)
global_load_dword v23, v16, s[38:39] sc1
v_mfma_f32_16x16x32_bf16 v[112:115], a[176:179], v[148:151], v[112:115]
ds_read_b128 v[144:147], v20 offset:18432
v_mfma_f32_16x16x32_bf16 v[116:119], a[180:183], v[148:151], v[116:119]
v_mfma_f32_16x16x32_bf16 v[120:123], a[184:187], v[148:151], v[120:123]
global_store_dwordx4 v13, v[164:167], s[38:39] offset:0 sc1
v_mfma_f32_16x16x32_bf16 v[124:127], a[188:191], v[148:151], v[124:127]
v_cvt_pk_bf16_f32 v168, v104, v105
v_cvt_pk_bf16_f32 v169, v106, v107
v_mfma_f32_16x16x32_bf16 v[128:131], v[64:67], v[148:151], v[128:131]
v_cvt_pk_bf16_f32 v170, v108, v109
v_cvt_pk_bf16_f32 v171, v110, v111
v_mfma_f32_16x16x32_bf16 v[132:135], v[68:71], v[148:151], v[132:135]
s_waitcnt lgkmcnt(2)
v_mfma_f32_16x16x32_bf16 v[112:115], a[192:195], v[136:139], v[112:115]
ds_read_b128 v[148:151], v20 offset:19456
v_mfma_f32_16x16x32_bf16 v[116:119], a[196:199], v[136:139], v[116:119]
global_store_dwordx4 v13, v[168:171], s[38:39] offset:1024 sc1
v_mfma_f32_16x16x32_bf16 v[120:123], a[200:203], v[136:139], v[120:123]
v_add_u32 v13, 18688, v13
v_add_f32 v200, v96, v97
v_mfma_f32_16x16x32_bf16 v[124:127], a[204:207], v[136:139], v[124:127]
v_mul_f32 v201, v96, v96
v_fmac_f32 v201, v97, v97
v_mfma_f32_16x16x32_bf16 v[128:131], v[72:75], v[136:139], v[128:131]
v_add_f32 v200, v200, v98
v_fmac_f32 v201, v98, v98
v_mfma_f32_16x16x32_bf16 v[132:135], v[76:79], v[136:139], v[132:135]
s_waitcnt lgkmcnt(2)
v_add_f32 v200, v200, v99
v_fmac_f32 v201, v99, v99
v_mfma_f32_16x16x32_bf16 v[112:115], a[208:211], v[140:143], v[112:115]
s_add_u32 m0, s47, 18432
ds_read_b128 v[136:139], v20 offset:20480
v_add_f32 v200, v200, v100
v_mfma_f32_16x16x32_bf16 v[116:119], a[212:215], v[140:143], v[116:119]
v_fmac_f32 v201, v100, v100
v_add_f32 v200, v200, v101
v_mfma_f32_16x16x32_bf16 v[120:123], a[216:219], v[140:143], v[120:123]
v_fmac_f32 v201, v101, v101
v_add_f32 v200, v200, v102
v_mfma_f32_16x16x32_bf16 v[124:127], a[220:223], v[140:143], v[124:127]
v_fmac_f32 v201, v102, v102
v_add_f32 v200, v200, v103
v_mfma_f32_16x16x32_bf16 v[128:131], v[80:83], v[140:143], v[128:131]
v_fmac_f32 v201, v103, v103
v_add_f32 v200, v200, v104
v_mfma_f32_16x16x32_bf16 v[132:135], v[84:87], v[140:143], v[132:135]
s_waitcnt lgkmcnt(2)
v_fmac_f32 v201, v104, v104
v_add_f32 v200, v200, v105
v_mfma_f32_16x16x32_bf16 v[112:115], a[0:3], v[144:147], v[112:115]
ds_read_b128 v[140:143], v20 offset:21504
global_load_lds_dwordx4 v202, s[38:39] offset:-2048 sc1
v_mfma_f32_16x16x32_bf16 v[116:119], a[4:7], v[144:147], v[116:119]
v_fmac_f32 v201, v105, v105
v_add_f32 v200, v200, v106
v_mfma_f32_16x16x32_bf16 v[120:123], a[8:11], v[144:147], v[120:123]
v_fmac_f32 v201, v106, v106
v_add_f32 v200, v200, v107
v_mfma_f32_16x16x32_bf16 v[124:127], a[12:15], v[144:147], v[124:127]
)"
R"(s_waitcnt lgkmcnt(2)
v_fmac_f32 v201, v107, v107
v_add_f32 v200, v200, v108
v_mfma_f32_16x16x32_bf16 v[112:115], a[16:19], v[148:151], v[112:115]
ds_read_b128 v[144:147], v20 offset:22528
v_fmac_f32 v201, v108, v108
v_mfma_f32_16x16x32_bf16 v[116:119], a[20:23], v[148:151], v[116:119]
v_add_f32 v200, v200, v109
v_fmac_f32 v201, v109, v109
v_mfma_f32_16x16x32_bf16 v[120:123], a[24:27], v[148:151], v[120:123]
v_add_f32 v200, v200, v110
v_fmac_f32 v201, v110, v110
v_mfma_f32_16x16x32_bf16 v[124:127], a[28:31], v[148:151], v[124:127]
s_waitcnt lgkmcnt(2)
global_load_lds_dwordx4 v202, s[38:39] offset:-1024 sc1
v_add_f32 v200, v200, v111
v_mfma_f32_16x16x32_bf16 v[112:115], a[32:35], v[136:139], v[112:115]
ds_read_b128 v[148:151], v20 offset:23552
v_fmac_f32 v201, v111, v111
v_mfma_f32_16x16x32_bf16 v[116:119], a[36:39], v[136:139], v[116:119]
ds_write_b128 v21, v[128:131] offset:51200
v_mfma_f32_16x16x32_bf16 v[120:123], a[40:43], v[136:139], v[120:123]
ds_write_b128 v21, v[132:135] offset:52224
v_mfma_f32_16x16x32_bf16 v[124:127], a[44:47], v[136:139], v[124:127]
s_waitcnt lgkmcnt(4)
ds_write_b64 v22, v[200:201] offset:58368
v_mfma_f32_16x16x32_bf16 v[112:115], a[48:51], v[140:143], v[112:115]
ds_read_b128 v[136:139], v20 offset:24576
v_mfma_f32_16x16x32_bf16 v[116:119], a[52:55], v[140:143], v[116:119]
v_mfma_f32_16x16x32_bf16 v[120:123], a[56:59], v[140:143], v[120:123]
global_load_lds_dwordx4 v202, s[38:39] offset:0 sc1
v_mfma_f32_16x16x32_bf16 v[124:127], a[60:63], v[140:143], v[124:127]
s_waitcnt lgkmcnt(5)
v_mfma_f32_16x16x32_bf16 v[112:115], a[64:67], v[144:147], v[112:115]
ds_read_b128 v[140:143], v20 offset:25600
v_mfma_f32_16x16x32_bf16 v[116:119], a[68:71], v[144:147], v[116:119]
v_mfma_f32_16x16x32_bf16 v[120:123], a[72:75], v[144:147], v[120:123]
v_mfma_f32_16x16x32_bf16 v[124:127], a[76:79], v[144:147], v[124:127]
s_waitcnt lgkmcnt(5)
v_mfma_f32_16x16x32_bf16 v[112:115], a[80:83], v[148:151], v[112:115]
s_waitcnt vmcnt(7)
ds_read_b128 v[144:147], v20 offset:26624
v_lshlrev_b32 v96, 16, v152
v_mfma_f32_16x16x32_bf16 v[116:119], a[84:87], v[148:151], v[116:119]
global_load_lds_dwordx4 v202, s[38:39] offset:1024 sc1
v_add_u32 v11, 18432, v11
v_mfma_f32_16x16x32_bf16 v[120:123], a[88:91], v[148:151], v[120:123]
v_min_u32 v202, v11, v12
v_and_b32 v97, 0xffff0000, v152
v_mfma_f32_16x16x32_bf16 v[124:127], a[92:95], v[148:151], v[124:127]
s_waitcnt lgkmcnt(2)
v_lshlrev_b32 v98, 16, v153
v_and_b32 v99, 0xffff0000, v153
v_mfma_f32_16x16x32_bf16 v[112:115], a[96:99], v[136:139], v[112:115]
ds_read_b128 v[148:151], v20 offset:27648
v_lshlrev_b32 v100, 16, v154
v_mfma_f32_16x16x32_bf16 v[116:119], a[100:103], v[136:139], v[116:119]
v_and_b32 v101, 0xffff0000, v154
v_lshlrev_b32 v102, 16, v155
v_mfma_f32_16x16x32_bf16 v[120:123], a[104:107], v[136:139], v[120:123]
s_waitcnt vmcnt(7)
v_and_b32 v103, 0xffff0000, v155
v_lshlrev_b32 v104, 16, v156
v_mfma_f32_16x16x32_bf16 v[124:127], a[108:111], v[136:139], v[124:127]
s_waitcnt lgkmcnt(2)
v_and_b32 v105, 0xffff0000, v156
v_lshlrev_b32 v106, 16, v157
v_mfma_f32_16x16x32_bf16 v[112:115], a[112:115], v[140:143], v[112:115]
ds_read_b128 v[136:139], v20 offset:32768
v_and_b32 v107, 0xffff0000, v157
v_mfma_f32_16x16x32_bf16 v[116:119], a[116:119], v[140:143], v[116:119]
v_lshlrev_b32 v108, 16, v158
v_and_b32 v109, 0xffff0000, v158
v_mfma_f32_16x16x32_bf16 v[120:123], a[120:123], v[140:143], v[120:123]
v_lshlrev_b32 v110, 16, v159
v_and_b32 v111, 0xffff0000, v159
v_mfma_f32_16x16x32_bf16 v[124:127], a[124:127], v[140:143], v[124:127]
s_waitcnt lgkmcnt(2)
global_load_dwordx4 v[152:155], v15, s[42:43] offset:0 sc1
v_mfma_f32_16x16x32_bf16 v[112:115], a[128:131], v[144:147], v[112:115]
ds_read_b128 v[140:143], v20 offset:33792
v_mfma_f32_16x16x32_bf16 v[116:119], a[132:135], v[144:147], v[116:119]
v_mfma_f32_16x16x32_bf16 v[120:123], a[136:139], v[144:147], v[120:123]
global_load_dwordx4 v[156:159], v15, s[42:43] offset:64 sc1
s_sub_u32 s42, s42, 18432
s_subb_u32 s43, s43, 0
v_mfma_f32_16x16x32_bf16 v[124:127], a[140:143], v[144:147], v[124:127]
s_waitcnt lgkmcnt(2)
v_mfma_f32_16x16x32_bf16 v[112:115], a[144:147], v[148:151], v[112:115]
ds_read_b128 v[144:147], v20 offset:34816
v_mfma_f32_16x16x32_bf16 v[116:119], a[148:151], v[148:151], v[116:119]
v_mfma_f32_16x16x32_bf16 v[120:123], a[152:155], v[148:151], v[120:123]
v_mfma_f32_16x16x32_bf16 v[124:127], a[156:159], v[148:151], v[124:127]
s_waitcnt lgkmcnt(2)
v_mfma_f32_16x16x32_bf16 v[112:115], a[224:227], v[136:139], v[112:115]
ds_read_b128 v[148:151], v20 offset:35840
v_mfma_f32_16x16x32_bf16 v[116:119], a[228:231], v[136:139], v[116:119]
v_mfma_f32_16x16x32_bf16 v[120:123], a[232:235], v[136:139], v[120:123]
s_waitcnt vmcnt(8)
v_sub_u32 v23, v23, v17
v_cmp_lt_u32 vcc, 0x7f, v23
v_mfma_f32_16x16x32_bf16 v[124:127], a[236:239], v[136:139], v[124:127]
s_waitcnt lgkmcnt(2)
v_mfma_f32_16x16x32_bf16 v[112:115], a[240:243], v[140:143], v[112:115]
v_mfma_f32_16x16x32_bf16 v[116:119], a[244:247], v[140:143], v[116:119]
s_cbranch_vccnz .Lwo_sp13
.Lwo_rt14:
v_xor_b32 v20, 0x10000, v20
v_mfma_f32_16x16x32_bf16 v[120:123], a[248:251], v[140:143], v[120:123]
v_mfma_f32_16x16x32_bf16 v[124:127], a[252:255], v[140:143], v[124:127]
s_waitcnt vmcnt(6)
s_barrier
s_waitcnt lgkmcnt(1)
v_mfma_f32_16x16x32_bf16 v[112:115], v[24:27], v[144:147], v[112:115]
ds_read_b128 v[136:139], v20 offset:10240
ds_read_b128 v[140:143], v20 offset:11264
v_mfma_f32_16x16x32_bf16 v[116:119], v[28:31], v[144:147], v[116:119]
v_add_u32 v17, 1, v17
v_mfma_f32_16x16x32_bf16 v[120:123], v[32:35], v[144:147], v[120:123]
v_mfma_f32_16x16x32_bf16 v[124:127], v[36:39], v[144:147], v[124:127]
s_waitcnt lgkmcnt(2)
v_mfma_f32_16x16x32_bf16 v[112:115], v[40:43], v[148:151], v[112:115]
ds_read_b128 v[144:147], v20 offset:12288
v_mfma_f32_16x16x32_bf16 v[116:119], v[44:47], v[148:151], v[116:119]
v_mfma_f32_16x16x32_bf16 v[120:123], v[48:51], v[148:151], v[120:123]
v_mfma_f32_16x16x32_bf16 v[124:127], v[52:55], v[148:151], v[124:127]
s_sub_u32 s70, s70, 1
s_cbranch_scc0 .Lwo_top19
s_waitcnt lgkmcnt(0)
s_nop 0
v_cvt_pk_bf16_f32 v164, v112, v113
v_cvt_pk_bf16_f32 v165, v114, v115
v_cvt_pk_bf16_f32 v166, v116, v117
v_cvt_pk_bf16_f32 v167, v118, v119
global_store_dwordx4 v13, v[164:167], s[38:39] offset:0 sc1
v_cvt_pk_bf16_f32 v168, v120, v121
v_cvt_pk_bf16_f32 v169, v122, v123
v_cvt_pk_bf16_f32 v170, v124, v125
v_cvt_pk_bf16_f32 v171, v126, v127
global_store_dwordx4 v13, v[168:171], s[38:39] offset:1024 sc1
v_add_u32 v13, 18688, v13
v_add_f32 v200, v112, v113
v_mul_f32 v201, v112, v112
v_fmac_f32 v201, v113, v113
v_add_f32 v200, v200, v114
v_fmac_f32 v201, v114, v114
v_add_f32 v200, v200, v115
v_fmac_f32 v201, v115, v115
v_add_f32 v200, v200, v116
v_fmac_f32 v201, v116, v116
v_add_f32 v200, v200, v117
v_fmac_f32 v201, v117, v117
v_add_f32 v200, v200, v118
v_fmac_f32 v201, v118, v118
v_add_f32 v200, v200, v119
v_fmac_f32 v201, v119, v119
v_add_f32 v200, v200, v120
v_fmac_f32 v201, v120, v120
v_add_f32 v200, v200, v121
v_fmac_f32 v201, v121, v121
v_add_f32 v200, v200, v122
v_fmac_f32 v201, v122, v122
v_add_f32 v200, v200, v123
v_fmac_f32 v201, v123, v123
v_add_f32 v200, v200, v124
v_fmac_f32 v201, v124, v124
v_add_f32 v200, v200, v125
v_fmac_f32 v201, v125, v125
v_add_f32 v200, v200, v126
v_fmac_f32 v201, v126, v126
v_add_f32 v200, v200, v127
v_fmac_f32 v201, v127, v127
ds_write_b64 v22, v[200:201] offset:60416
v_add_u32 v11, 18432, v11
v_min_u32 v202, v11, v12
v_sub_u32 v23, v23, v17
v_cmp_lt_u32 vcc, 0x7f, v23
s_waitcnt vmcnt(0) lgkmcnt(0)
s_barrier
s_barrier
s_endpgm
.Lwo_w4:
s_add_u32 m0, s46, 4096
s_nop 0
global_load_lds_dwordx4 v0, s[40:41] offset:-4096
global_load_lds_dwordx4 v1, s[40:41] offset:-3072
global_load_lds_dwordx4 v2, s[40:41] offset:-2048
global_load_lds_dwordx4 v3, s[40:41] offset:-1024
global_load_lds_dwordx4 v4, s[40:41] offset:0
global_load_lds_dwordx4 v5, s[40:41] offset:1024
global_load_lds_dwordx4 v6, s[40:41] offset:2048
global_load_lds_dwordx4 v7, s[40:41] offset:3072
s_add_u32 m0, s46, 12288
s_nop 0
global_load_lds_dwordx4 v8, s[40:41] offset:-4096
s_add_u32 s40, s40, 73728
s_addc_u32 s41, s41, 0
s_add_u32 m0, s46, 40960
s_nop 0
global_load_lds_dwordx4 v0, s[40:41] offset:-4096
global_load_lds_dwordx4 v1, s[40:41] offset:-3072
global_load_lds_dwordx4 v2, s[40:41] offset:-2048
global_load_lds_dwordx4 v3, s[40:41] offset:-1024
global_load_lds_dwordx4 v4, s[40:41] offset:0
global_load_lds_dwordx4 v5, s[40:41] offset:1024
global_load_lds_dwordx4 v6, s[40:41] offset:2048
global_load_lds_dwordx4 v7, s[40:41] offset:3072
s_add_u32 m0, s46, 49152
s_nop 0
global_load_lds_dwordx4 v8, s[40:41] offset:-4096
s_add_u32 s40, s40, 73728
s_addc_u32 s41, s41, 0
s_add_u32 m0, s46, 77824
s_nop 0
global_load_lds_dwordx4 v0, s[40:41] offset:-4096
global_load_lds_dwordx4 v1, s[40:41] offset:-3072
global_load_lds_dwordx4 v2, s[40:41] offset:-2048
global_load_lds_dwordx4 v3, s[40:41] offset:-1024
global_load_lds_dwordx4 v4, s[40:41] offset:0
global_load_lds_dwordx4 v5, s[40:41] offset:1024
global_load_lds_dwordx4 v6, s[40:41] offset:2048
global_load_lds_dwordx4 v7, s[40:41] offset:3072
s_add_u32 m0, s46, 86016
s_nop 0
global_load_lds_dwordx4 v8, s[40:41] offset:-4096
s_add_u32 s40, s40, 73728
s_addc_u32 s41, s41, 0
s_add_u32 m0, s46, 114688
s_nop 0
global_load_lds_dwordx4 v0, s[40:41] offset:-4096
global_load_lds_dwordx4 v1, s[40:41] offset:-3072
global_load_lds_dwordx4 v2, s[40:41] offset:-2048
global_load_lds_dwordx4 v3, s[40:41] offset:-1024
global_load_lds_dwordx4 v4, s[40:41] offset:0
global_load_lds_dwordx4 v5, s[40:41] offset:1024
global_load_lds_dwordx4 v6, s[40:41] offset:2048
global_load_lds_dwordx4 v7, s[40:41] offset:3072
s_add_u32 m0, s46, 122880
s_nop 0
global_load_lds_dwordx4 v8, s[40:41] offset:-4096
s_add_u32 s40, s40, 73728
s_addc_u32 s41, s41, 0
v_add_u32 v205, 0x10000, v9
v_add_u32 v206, 0x20000, v9
v_add_u32 v207, 0x10000, v10
v_add_u32 v208, 0x20000, v10
s_waitcnt lgkmcnt(0)
s_mul_i32 s37, s37, 0x9E3779B1
s_add_u32 s37, s37, 0x7F4A7C15
v_add_u32 v17, s37, v17
v_add_u32 v19, s37, v19
s_waitcnt vmcnt(27)
s_barrier
ds_read_b64_tr_b16 a[0:1], v9 offset:0
ds_read_b64_tr_b16 a[2:3], v9 offset:2304
ds_read_b64_tr_b16 a[4:5], v9 offset:8
ds_read_b64_tr_b16 a[6:7], v9 offset:2312
ds_read_b64_tr_b16 a[8:9], v9 offset:64
ds_read_b64_tr_b16 a[10:11], v9 offset:2368
ds_read_b64_tr_b16 a[12:13], v9 offset:72
ds_read_b64_tr_b16 a[14:15], v9 offset:2376
ds_read_b64_tr_b16 a[16:17], v9 offset:18432
ds_read_b64_tr_b16 a[18:19], v9 offset:20736
ds_read_b64_tr_b16 a[20:21], v9 offset:18440
ds_read_b64_tr_b16 a[22:23], v9 offset:20744
ds_read_b64_tr_b16 a[24:25], v9 offset:18496
ds_read_b64_tr_b16 a[26:27], v9 offset:20800
ds_read_b64_tr_b16 a[28:29], v9 offset:18504
ds_read_b64_tr_b16 a[30:31], v9 offset:20808
s_waitcnt lgkmcnt(0)
s_barrier
s_add_u32 m0, s46, 4096
s_nop 0
global_load_lds_dwordx4 v0, s[40:41] offset:-4096
global_load_lds_dwordx4 v1, s[40:41] offset:-3072
global_load_lds_dwordx4 v2, s[40:41] offset:-2048
global_load_lds_dwordx4 v3, s[40:41] offset:-1024
global_load_lds_dwordx4 v4, s[40:41] offset:0
global_load_lds_dwordx4 v5, s[40:41] offset:1024
global_load_lds_dwordx4 v6, s[40:41] offset:2048
global_load_lds_dwordx4 v7, s[40:41] offset:3072
s_add_u32 m0, s46, 12288
s_nop 0
global_load_lds_dwordx4 v8, s[40:41] offset:-4096
s_add_u32 s40, s40, 73728
s_addc_u32 s41, s41, 0
s_waitcnt vmcnt(27)
s_barrier
ds_read_b64_tr_b16 a[32:33], v9 offset:36864
ds_read_b64_tr_b16 a[34:35], v9 offset:39168
ds_read_b64_tr_b16 a[36:37], v9 offset:36872
ds_read_b64_tr_b16 a[38:39], v9 offset:39176
ds_read_b64_tr_b16 a[40:41], v9 offset:36928
ds_read_b64_tr_b16 a[42:43], v9 offset:39232
ds_read_b64_tr_b16 a[44:45], v9 offset:36936
ds_read_b64_tr_b16 a[46:47], v9 offset:39240
ds_read_b64_tr_b16 a[48:49], v9 offset:55296
ds_read_b64_tr_b16 a[50:51], v9 offset:57600
ds_read_b64_tr_b16 a[52:53], v9 offset:55304
ds_read_b64_tr_b16 a[54:55], v9 offset:57608
ds_read_b64_tr_b16 a[56:57], v9 offset:55360
ds_read_b64_tr_b16 a[58:59], v9 offset:57664
ds_read_b64_tr_b16 a[60:61], v9 offset:55368
ds_read_b64_tr_b16 a[62:63], v9 offset:57672
s_waitcnt lgkmcnt(0)
s_barrier
s_add_u32 m0, s46, 40960
s_nop 0
global_load_lds_dwordx4 v0, s[40:41] offset:-4096
global_load_lds_dwordx4 v1, s[40:41] offset:-3072
global_load_lds_dwordx4 v2, s[40:41] offset:-2048
global_load_lds_dwordx4 v3, s[40:41] offset:-1024
global_load_lds_dwordx4 v4, s[40:41] offset:0
global_load_lds_dwordx4 v5, s[40:41] offset:1024
global_load_lds_dwordx4 v6, s[40:41] offset:2048
global_load_lds_dwordx4 v7, s[40:41] offset:3072
s_add_u32 m0, s46, 49152
s_nop 0
global_load_lds_dwordx4 v8, s[40:41] offset:-4096
s_add_u32 s40, s40, 73728
s_addc_u32 s41, s41, 0
s_waitcnt vmcnt(27)
s_barrier
ds_read_b64_tr_b16 a[64:65], v205 offset:8192
ds_read_b64_tr_b16 a[66:67], v205 offset:10496
ds_read_b64_tr_b16 a[68:69], v205 offset:8200
ds_read_b64_tr_b16 a[70:71], v205 offset:10504
ds_read_b64_tr_b16 a[72:73], v205 offset:8256
ds_read_b64_tr_b16 a[74:75], v205 offset:10560
ds_read_b64_tr_b16 a[76:77], v205 offset:8264
ds_read_b64_tr_b16 a[78:79], v205 offset:10568
ds_read_b64_tr_b16 a[80:81], v205 offset:26624
ds_read_b64_tr_b16 a[82:83], v205 offset:28928
ds_read_b64_tr_b16 a[84:85], v205 offset:26632
ds_read_b64_tr_b16 a[86:87], v205 offset:28936
ds_read_b64_tr_b16 a[88:89], v205 offset:26688
ds_read_b64_tr_b16 a[90:91], v205 offset:28992
ds_read_b64_tr_b16 a[92:93], v205 offset:26696
ds_read_b64_tr_b16 a[94:95], v205 offset:29000
s_waitcnt lgkmcnt(0)
s_barrier
s_add_u32 m0, s46, 77824
s_nop 0
global_load_lds_dwordx4 v0, s[40:41] offset:-4096
global_load_lds_dwordx4 v1, s[40:41] offset:-3072
global_load_lds_dwordx4 v2, s[40:41] offset:-2048
global_load_lds_dwordx4 v3, s[40:41] offset:-1024
global_load_lds_dwordx4 v4, s[40:41] offset:0
global_load_lds_dwordx4 v5, s[40:41] offset:1024
global_load_lds_dwordx4 v6, s[40:41] offset:2048
global_load_lds_dwordx4 v7, s[40:41] offset:3072
s_add_u32 m0, s46, 86016
s_nop 0
global_load_lds_dwordx4 v8, s[40:41] offset:-4096
s_add_u32 s40, s40, 73728
s_addc_u32 s41, s41, 0
s_waitcnt vmcnt(27)
s_barrier
ds_read_b64_tr_b16 a[96:97], v205 offset:45056
ds_read_b64_tr_b16 a[98:99], v205 offset:47360
ds_read_b64_tr_b16 a[100:101], v205 offset:45064
ds_read_b64_tr_b16 a[102:103], v205 offset:47368
ds_read_b64_tr_b16 a[104:105], v205 offset:45120
ds_read_b64_tr_b16 a[106:107], v205 offset:47424
ds_read_b64_tr_b16 a[108:109], v205 offset:45128
ds_read_b64_tr_b16 a[110:111], v205 offset:47432
ds_read_b64_tr_b16 a[112:113], v205 offset:63488
ds_read_b64_tr_b16 a[114:115], v206 offset:256
ds_read_b64_tr_b16 a[116:117], v205 offset:63496
ds_read_b64_tr_b16 a[118:119], v206 offset:264
ds_read_b64_tr_b16 a[120:121], v205 offset:63552
ds_read_b64_tr_b16 a[122:123], v206 offset:320
ds_read_b64_tr_b16 a[124:125], v205 offset:63560
ds_read_b64_tr_b16 a[126:127], v206 offset:328
s_waitcnt lgkmcnt(0)
s_barrier
s_add_u32 m0, s46, 114688
s_nop 0
global_load_lds_dwordx4 v0, s[40:41] offset:-4096
global_load_lds_dwordx4 v1, s[40:41] offset:-3072
global_load_lds_dwordx4 v2, s[40:41] offset:-2048
global_load_lds_dwordx4 v3, s[40:41] offset:-1024
global_load_lds_dwordx4 v4, s[40:41] offset:0
global_load_lds_dwordx4 v5, s[40:41] offset:1024
global_load_lds_dwordx4 v6, s[40:41] offset:2048
global_load_lds_dwordx4 v7, s[40:41] offset:3072
s_add_u32 m0, s46, 122880
s_nop 0
global_load_lds_dwordx4 v8, s[40:41] offset:-4096
s_add_u32 s40, s40, 73728
s_addc_u32 s41, s41, 0
s_waitcnt vmcnt(27)
s_barrier
ds_read_b64_tr_b16 a[128:129], v9 offset:0
ds_read_b64_tr_b16 a[130:131], v9 offset:2304
ds_read_b64_tr_b16 a[132:133], v9 offset:8
ds_read_b64_tr_b16 a[134:135], v9 offset:2312
ds_read_b64_tr_b16 a[136:137], v9 offset:64
ds_read_b64_tr_b16 a[138:139], v9 offset:2368
ds_read_b64_tr_b16 a[140:141], v9 offset:72
)"
R"(ds_read_b64_tr_b16 a[142:143], v9 offset:2376
ds_read_b64_tr_b16 a[144:145], v9 offset:18432
ds_read_b64_tr_b16 a[146:147], v9 offset:20736
ds_read_b64_tr_b16 a[148:149], v9 offset:18440
ds_read_b64_tr_b16 a[150:151], v9 offset:20744
ds_read_b64_tr_b16 a[152:153], v9 offset:18496
ds_read_b64_tr_b16 a[154:155], v9 offset:20800
ds_read_b64_tr_b16 a[156:157], v9 offset:18504
ds_read_b64_tr_b16 a[158:159], v9 offset:20808
s_waitcnt lgkmcnt(0)
s_barrier
s_add_u32 m0, s46, 4096
s_nop 0
global_load_lds_dwordx4 v0, s[40:41] offset:-4096
global_load_lds_dwordx4 v1, s[40:41] offset:-3072
global_load_lds_dwordx4 v2, s[40:41] offset:-2048
global_load_lds_dwordx4 v3, s[40:41] offset:-1024
global_load_lds_dwordx4 v4, s[40:41] offset:0
global_load_lds_dwordx4 v5, s[40:41] offset:1024
global_load_lds_dwordx4 v6, s[40:41] offset:2048
global_load_lds_dwordx4 v7, s[40:41] offset:3072
s_add_u32 m0, s46, 12288
s_nop 0
global_load_lds_dwordx4 v8, s[40:41] offset:-4096
s_add_u32 s40, s40, 73728
s_addc_u32 s41, s41, 0
s_waitcnt vmcnt(27)
s_barrier
ds_read_b64_tr_b16 a[160:161], v9 offset:36864
ds_read_b64_tr_b16 a[162:163], v9 offset:39168
ds_read_b64_tr_b16 a[164:165], v9 offset:36872
ds_read_b64_tr_b16 a[166:167], v9 offset:39176
ds_read_b64_tr_b16 a[168:169], v9 offset:36928
ds_read_b64_tr_b16 a[170:171], v9 offset:39232
ds_read_b64_tr_b16 a[172:173], v9 offset:36936
ds_read_b64_tr_b16 a[174:175], v9 offset:39240
ds_read_b64_tr_b16 a[176:177], v9 offset:55296
ds_read_b64_tr_b16 a[178:179], v9 offset:57600
ds_read_b64_tr_b16 a[180:181], v9 offset:55304
ds_read_b64_tr_b16 a[182:183], v9 offset:57608
ds_read_b64_tr_b16 a[184:185], v9 offset:55360
ds_read_b64_tr_b16 a[186:187], v9 offset:57664
ds_read_b64_tr_b16 a[188:189], v9 offset:55368
ds_read_b64_tr_b16 a[190:191], v9 offset:57672
s_waitcnt vmcnt(18)
s_barrier
ds_read_b64_tr_b16 a[192:193], v205 offset:8192
ds_read_b64_tr_b16 a[194:195], v205 offset:10496
ds_read_b64_tr_b16 a[196:197], v205 offset:8200
ds_read_b64_tr_b16 a[198:199], v205 offset:10504
ds_read_b64_tr_b16 a[200:201], v205 offset:8256
ds_read_b64_tr_b16 a[202:203], v205 offset:10560
ds_read_b64_tr_b16 a[204:205], v205 offset:8264
ds_read_b64_tr_b16 a[206:207], v205 offset:10568
ds_read_b64_tr_b16 a[208:209], v205 offset:26624
ds_read_b64_tr_b16 a[210:211], v205 offset:28928
ds_read_b64_tr_b16 a[212:213], v205 offset:26632
ds_read_b64_tr_b16 a[214:215], v205 offset:28936
ds_read_b64_tr_b16 a[216:217], v205 offset:26688
ds_read_b64_tr_b16 a[218:219], v205 offset:28992
ds_read_b64_tr_b16 a[220:221], v205 offset:26696
ds_read_b64_tr_b16 a[222:223], v205 offset:29000
s_waitcnt vmcnt(9)
s_barrier
ds_read_b64_tr_b16 a[224:225], v205 offset:45056
ds_read_b64_tr_b16 a[226:227], v205 offset:47360
ds_read_b64_tr_b16 a[228:229], v205 offset:45064
ds_read_b64_tr_b16 a[230:231], v205 offset:47368
ds_read_b64_tr_b16 a[232:233], v205 offset:45120
ds_read_b64_tr_b16 a[234:235], v205 offset:47424
ds_read_b64_tr_b16 a[236:237], v205 offset:45128
ds_read_b64_tr_b16 a[238:239], v205 offset:47432
ds_read_b64_tr_b16 v[56:57], v207 offset:45056
ds_read_b64_tr_b16 v[58:59], v207 offset:47360
ds_read_b64_tr_b16 v[60:61], v207 offset:45064
ds_read_b64_tr_b16 v[62:63], v207 offset:47368
ds_read_b64_tr_b16 a[240:241], v205 offset:63488
ds_read_b64_tr_b16 a[242:243], v206 offset:256
ds_read_b64_tr_b16 a[244:245], v205 offset:63496
ds_read_b64_tr_b16 a[246:247], v206 offset:264
ds_read_b64_tr_b16 a[248:249], v205 offset:63552
ds_read_b64_tr_b16 a[250:251], v206 offset:320
ds_read_b64_tr_b16 a[252:253], v205 offset:63560
ds_read_b64_tr_b16 a[254:255], v206 offset:328
ds_read_b64_tr_b16 v[64:65], v207 offset:63488
ds_read_b64_tr_b16 v[66:67], v208 offset:256
ds_read_b64_tr_b16 v[68:69], v207 offset:63496
ds_read_b64_tr_b16 v[70:71], v208 offset:264
s_waitcnt vmcnt(0)
s_barrier
ds_read_b64_tr_b16 v[24:25], v9 offset:0
ds_read_b64_tr_b16 v[26:27], v9 offset:2304
ds_read_b64_tr_b16 v[28:29], v9 offset:8
ds_read_b64_tr_b16 v[30:31], v9 offset:2312
ds_read_b64_tr_b16 v[32:33], v9 offset:64
ds_read_b64_tr_b16 v[34:35], v9 offset:2368
ds_read_b64_tr_b16 v[36:37], v9 offset:72
ds_read_b64_tr_b16 v[38:39], v9 offset:2376
ds_read_b64_tr_b16 v[72:73], v10 offset:0
ds_read_b64_tr_b16 v[74:75], v10 offset:2304
ds_read_b64_tr_b16 v[76:77], v10 offset:8
ds_read_b64_tr_b16 v[78:79], v10 offset:2312
ds_read_b64_tr_b16 v[40:41], v9 offset:18432
ds_read_b64_tr_b16 v[42:43], v9 offset:20736
ds_read_b64_tr_b16 v[44:45], v9 offset:18440
ds_read_b64_tr_b16 v[46:47], v9 offset:20744
ds_read_b64_tr_b16 v[48:49], v9 offset:18496
ds_read_b64_tr_b16 v[50:51], v9 offset:20800
ds_read_b64_tr_b16 v[52:53], v9 offset:18504
ds_read_b64_tr_b16 v[54:55], v9 offset:20808
ds_read_b64_tr_b16 v[80:81], v10 offset:18432
ds_read_b64_tr_b16 v[82:83], v10 offset:20736
ds_read_b64_tr_b16 v[84:85], v10 offset:18440
ds_read_b64_tr_b16 v[86:87], v10 offset:20744
s_waitcnt lgkmcnt(0)
s_barrier
v_mov_b32 v202, v11
s_barrier
s_mov_b32 s70, 32
s_waitcnt vmcnt(0) lgkmcnt(0)
s_nop 7
.p2align 6
.Lwo_top22:
v_mfma_f32_16x16x32_bf16 v[96:99], a[0:3], v[136:139], v[96:99]
s_waitcnt lgkmcnt(5)
ds_read_b128 v[148:151], v20 offset:3072
v_add_f32 v132, v132, v180
v_mfma_f32_16x16x32_bf16 v[100:103], a[4:7], v[136:139], v[100:103]
s_cmp_eq_u32 s70, 16
s_cbranch_scc1 .Lwo_hk20
.Lwo_hr21:
v_mfma_f32_16x16x32_bf16 v[104:107], a[8:11], v[136:139], v[104:107]
v_add_f32 v133, v133, v181
v_add_f32 v134, v134, v182
v_mfma_f32_16x16x32_bf16 v[108:111], a[12:15], v[136:139], v[108:111]
v_cvt_pk_bf16_f32 v164, v112, v113
v_cvt_pk_bf16_f32 v165, v114, v115
v_mfma_f32_16x16x32_bf16 v[96:99], a[16:19], v[140:143], v[96:99]
ds_read_b128 v[136:139], v20 offset:4096
v_cvt_pk_bf16_f32 v166, v116, v117
v_mfma_f32_16x16x32_bf16 v[100:103], a[20:23], v[140:143], v[100:103]
v_cvt_pk_bf16_f32 v167, v118, v119
v_add_f32 v135, v135, v183
v_mfma_f32_16x16x32_bf16 v[104:107], a[24:27], v[140:143], v[104:107]
s_waitcnt lgkmcnt(6)
v_add_f32 v128, v128, v184
v_add_f32 v129, v129, v185
v_mfma_f32_16x16x32_bf16 v[108:111], a[28:31], v[140:143], v[108:111]
s_waitcnt lgkmcnt(2)
v_add_f32 v130, v130, v186
v_add_f32 v131, v131, v187
v_mfma_f32_16x16x32_bf16 v[96:99], a[32:35], v[144:147], v[96:99]
ds_read_b128 v[140:143], v20 offset:5120
v_add_f32 v132, v132, v188
v_mfma_f32_16x16x32_bf16 v[100:103], a[36:39], v[144:147], v[100:103]
global_store_dwordx4 v13, v[164:167], s[38:39] offset:0 sc1
v_mfma_f32_16x16x32_bf16 v[104:107], a[40:43], v[144:147], v[104:107]
v_cvt_pk_bf16_f32 v168, v120, v121
v_cvt_pk_bf16_f32 v169, v122, v123
v_mfma_f32_16x16x32_bf16 v[108:111], a[44:47], v[144:147], v[108:111]
s_waitcnt lgkmcnt(2)
v_cvt_pk_bf16_f32 v170, v124, v125
v_cvt_pk_bf16_f32 v171, v126, v127
v_mfma_f32_16x16x32_bf16 v[96:99], a[48:51], v[148:151], v[96:99]
ds_read_b128 v[144:147], v20 offset:6144
v_add_f32 v133, v133, v189
v_mfma_f32_16x16x32_bf16 v[100:103], a[52:55], v[148:151], v[100:103]
global_store_dwordx4 v13, v[168:171], s[38:39] offset:1024 sc1
v_mfma_f32_16x16x32_bf16 v[104:107], a[56:59], v[148:151], v[104:107]
v_add_f32 v134, v134, v190
v_add_f32 v135, v135, v191
v_mfma_f32_16x16x32_bf16 v[108:111], a[60:63], v[148:151], v[108:111]
s_waitcnt lgkmcnt(2)
v_add_f32 v128, v128, v192
v_add_f32 v129, v129, v193
v_mfma_f32_16x16x32_bf16 v[96:99], a[64:67], v[136:139], v[96:99]
ds_read_b128 v[148:151], v20 offset:7168
v_add_f32 v130, v130, v194
v_mfma_f32_16x16x32_bf16 v[100:103], a[68:71], v[136:139], v[100:103]
s_xor_b32 s47, s47, 0x10000
v_add_f32 v131, v131, v195
v_add_f32 v132, v132, v196
v_mfma_f32_16x16x32_bf16 v[104:107], a[72:75], v[136:139], v[104:107]
s_add_u32 m0, s47, 0
v_add_f32 v133, v133, v197
v_add_f32 v134, v134, v198
v_mfma_f32_16x16x32_bf16 v[108:111], a[76:79], v[136:139], v[108:111]
s_waitcnt lgkmcnt(2)
v_add_f32 v135, v135, v199
v_cvt_pk_bf16_f32 v172, v128, v129
v_mfma_f32_16x16x32_bf16 v[96:99], a[80:83], v[140:143], v[96:99]
ds_read_b128 v[136:139], v20 offset:8192
v_cvt_pk_bf16_f32 v173, v130, v131
v_mfma_f32_16x16x32_bf16 v[100:103], a[84:87], v[140:143], v[100:103]
v_cvt_pk_bf16_f32 v174, v132, v133
v_cvt_pk_bf16_f32 v175, v134, v135
v_mfma_f32_16x16x32_bf16 v[104:107], a[88:91], v[140:143], v[104:107]
global_store_dwordx4 v13, v[172:175], s[38:39] offset:2048 sc1
v_mfma_f32_16x16x32_bf16 v[108:111], a[92:95], v[140:143], v[108:111]
s_waitcnt lgkmcnt(2)
v_add_u32 v13, 18688, v13
v_add_f32 v200, v128, v129
v_mfma_f32_16x16x32_bf16 v[96:99], a[96:99], v[144:147], v[96:99]
ds_read_b128 v[140:143], v20 offset:9216
v_mul_f32 v201, v128, v128
v_mfma_f32_16x16x32_bf16 v[100:103], a[100:103], v[144:147], v[100:103]
v_fmac_f32 v201, v129, v129
v_add_f32 v200, v200, v130
v_mfma_f32_16x16x32_bf16 v[104:107], a[104:107], v[144:147], v[104:107]
global_load_lds_dwordx4 v202, s[38:39] offset:-2048 sc1
v_fmac_f32 v201, v130, v130
v_mfma_f32_16x16x32_bf16 v[108:111], a[108:111], v[144:147], v[108:111]
s_waitcnt lgkmcnt(2)
v_add_f32 v200, v200, v131
v_fmac_f32 v201, v131, v131
v_mfma_f32_16x16x32_bf16 v[96:99], a[112:115], v[148:151], v[96:99]
ds_read_b128 v[144:147], v20 offset:14336
v_add_f32 v200, v200, v132
v_mfma_f32_16x16x32_bf16 v[100:103], a[116:119], v[148:151], v[100:103]
v_fmac_f32 v201, v132, v132
v_add_f32 v200, v200, v133
v_mfma_f32_16x16x32_bf16 v[104:107], a[120:123], v[148:151], v[104:107]
v_fmac_f32 v201, v133, v133
v_add_f32 v200, v200, v134
v_mfma_f32_16x16x32_bf16 v[108:111], a[124:127], v[148:151], v[108:111]
s_waitcnt lgkmcnt(2)
v_fmac_f32 v201, v134, v134
v_add_f32 v200, v200, v135
v_mfma_f32_16x16x32_bf16 v[96:99], a[128:131], v[136:139], v[96:99]
ds_read_b128 v[148:151], v20 offset:15360
v_fmac_f32 v201, v135, v135
v_mfma_f32_16x16x32_bf16 v[100:103], a[132:135], v[136:139], v[100:103]
global_load_lds_dwordx4 v202, s[38:39] offset:-1024 sc1
v_add_f32 v200, v200, v112
v_mfma_f32_16x16x32_bf16 v[104:107], a[136:139], v[136:139], v[104:107]
v_fmac_f32 v201, v112, v112
v_add_f32 v200, v200, v113
v_mfma_f32_16x16x32_bf16 v[108:111], a[140:143], v[136:139], v[108:111]
s_waitcnt lgkmcnt(2)
v_fmac_f32 v201, v113, v113
v_add_f32 v200, v200, v114
v_mfma_f32_16x16x32_bf16 v[96:99], a[144:147], v[140:143], v[96:99]
ds_read_b128 v[136:139], v20 offset:16384
v_fmac_f32 v201, v114, v114
v_mfma_f32_16x16x32_bf16 v[100:103], a[148:151], v[140:143], v[100:103]
v_add_f32 v200, v200, v115
v_fmac_f32 v201, v115, v115
v_mfma_f32_16x16x32_bf16 v[104:107], a[152:155], v[140:143], v[104:107]
v_add_f32 v200, v200, v116
v_fmac_f32 v201, v116, v116
v_mfma_f32_16x16x32_bf16 v[108:111], a[156:159], v[140:143], v[108:111]
s_waitcnt lgkmcnt(2)
v_add_f32 v200, v200, v117
v_fmac_f32 v201, v117, v117
v_mfma_f32_16x16x32_bf16 v[96:99], a[224:227], v[144:147], v[96:99]
ds_read_b128 v[140:143], v20 offset:17408
global_load_lds_dwordx4 v202, s[38:39] offset:0 sc1
v_mfma_f32_16x16x32_bf16 v[100:103], a[228:231], v[144:147], v[100:103]
v_add_f32 v200, v200, v118
v_fmac_f32 v201, v118, v118
v_mfma_f32_16x16x32_bf16 v[104:107], a[232:235], v[144:147], v[104:107]
v_add_f32 v200, v200, v119
v_fmac_f32 v201, v119, v119
v_mfma_f32_16x16x32_bf16 v[108:111], a[236:239], v[144:147], v[108:111]
v_add_f32 v200, v200, v120
v_fmac_f32 v201, v120, v120
v_mfma_f32_16x16x32_bf16 v[128:131], v[56:59], v[144:147], 0
v_add_f32 v200, v200, v121
v_fmac_f32 v201, v121, v121
v_mfma_f32_16x16x32_bf16 v[132:135], v[60:63], v[144:147], 0
s_waitcnt lgkmcnt(2)
v_add_f32 v200, v200, v122
v_fmac_f32 v201, v122, v122
v_mfma_f32_16x16x32_bf16 v[96:99], a[240:243], v[148:151], v[96:99]
ds_read_b128 v[144:147], v20 offset:10240
v_add_f32 v200, v200, v123
v_mfma_f32_16x16x32_bf16 v[100:103], a[244:247], v[148:151], v[100:103]
global_load_lds_dwordx4 v202, s[38:39] offset:1024 sc1
v_add_u32 v11, 18432, v11
v_mfma_f32_16x16x32_bf16 v[104:107], a[248:251], v[148:151], v[104:107]
v_min_u32 v202, v11, v12
v_fmac_f32 v201, v123, v123
v_mfma_f32_16x16x32_bf16 v[108:111], a[252:255], v[148:151], v[108:111]
v_add_f32 v200, v200, v124
v_fmac_f32 v201, v124, v124
v_mfma_f32_16x16x32_bf16 v[128:131], v[64:67], v[148:151], v[128:131]
v_add_f32 v200, v200, v125
v_fmac_f32 v201, v125, v125
v_mfma_f32_16x16x32_bf16 v[132:135], v[68:71], v[148:151], v[132:135]
s_waitcnt lgkmcnt(2)
v_add_f32 v200, v200, v126
v_fmac_f32 v201, v126, v126
v_mfma_f32_16x16x32_bf16 v[96:99], v[24:27], v[136:139], v[96:99]
ds_read_b128 v[148:151], v20 offset:11264
v_add_f32 v200, v200, v127
v_mfma_f32_16x16x32_bf16 v[100:103], v[28:31], v[136:139], v[100:103]
v_fmac_f32 v201, v127, v127
v_mfma_f32_16x16x32_bf16 v[104:107], v[32:35], v[136:139], v[104:107]
ds_write_b64 v22, v[200:201] offset:60928
v_mfma_f32_16x16x32_bf16 v[108:111], v[36:39], v[136:139], v[108:111]
s_waitcnt vmcnt(8)
v_lshlrev_b32 v112, 16, v152
v_and_b32 v113, 0xffff0000, v152
v_mfma_f32_16x16x32_bf16 v[128:131], v[72:75], v[136:139], v[128:131]
v_lshlrev_b32 v114, 16, v153
v_and_b32 v115, 0xffff0000, v153
v_mfma_f32_16x16x32_bf16 v[132:135], v[76:79], v[136:139], v[132:135]
s_waitcnt lgkmcnt(3)
v_lshlrev_b32 v116, 16, v154
v_and_b32 v117, 0xffff0000, v154
v_mfma_f32_16x16x32_bf16 v[96:99], v[40:43], v[140:143], v[96:99]
ds_read_b128 v[136:139], v20 offset:12288
v_lshlrev_b32 v118, 16, v155
v_mfma_f32_16x16x32_bf16 v[100:103], v[44:47], v[140:143], v[100:103]
s_waitcnt vmcnt(7)
v_and_b32 v119, 0xffff0000, v155
v_lshlrev_b32 v120, 16, v156
v_mfma_f32_16x16x32_bf16 v[104:107], v[48:51], v[140:143], v[104:107]
v_and_b32 v121, 0xffff0000, v156
v_lshlrev_b32 v122, 16, v157
v_mfma_f32_16x16x32_bf16 v[108:111], v[52:55], v[140:143], v[108:111]
v_and_b32 v123, 0xffff0000, v157
v_lshlrev_b32 v124, 16, v158
v_mfma_f32_16x16x32_bf16 v[128:131], v[80:83], v[140:143], v[128:131]
v_and_b32 v125, 0xffff0000, v158
v_lshlrev_b32 v126, 16, v159
v_mfma_f32_16x16x32_bf16 v[132:135], v[84:87], v[140:143], v[132:135]
s_waitcnt lgkmcnt(3)
v_and_b32 v127, 0xffff0000, v159
v_mfma_f32_16x16x32_bf16 v[96:99], a[160:163], v[144:147], v[96:99]
ds_read_b128 v[140:143], v20 offset:13312
global_load_dwordx4 v[152:155], v15, s[42:43] offset:0 sc1
v_mfma_f32_16x16x32_bf16 v[100:103], a[164:167], v[144:147], v[100:103]
v_mfma_f32_16x16x32_bf16 v[104:107], a[168:171], v[144:147], v[104:107]
v_mfma_f32_16x16x32_bf16 v[108:111], a[172:175], v[144:147], v[108:111]
s_waitcnt lgkmcnt(3)
global_load_dwordx4 v[156:159], v15, s[42:43] offset:64 sc1
v_mfma_f32_16x16x32_bf16 v[96:99], a[176:179], v[148:151], v[96:99]
s_sub_u32 s42, s42, 18432
s_subb_u32 s43, s43, 0
v_mfma_f32_16x16x32_bf16 v[100:103], a[180:183], v[148:151], v[100:103]
v_mfma_f32_16x16x32_bf16 v[104:107], a[184:187], v[148:151], v[104:107]
v_mfma_f32_16x16x32_bf16 v[108:111], a[188:191], v[148:151], v[108:111]
s_waitcnt vmcnt(6) lgkmcnt(2)
s_barrier
s_waitcnt lgkmcnt(1)
v_mfma_f32_16x16x32_bf16 v[96:99], a[192:195], v[136:139], v[96:99]
ds_read_b128 v[144:147], v20 offset:18432
ds_read_b128 v[148:151], v20 offset:19456
v_mfma_f32_16x16x32_bf16 v[100:103], a[196:199], v[136:139], v[100:103]
ds_read_b128 v[176:179], v21 offset:40960
ds_read_b128 v[180:183], v21 offset:41984
v_mfma_f32_16x16x32_bf16 v[104:107], a[200:203], v[136:139], v[104:107]
ds_read_b128 v[184:187], v21 offset:43008
ds_read_b128 v[188:191], v21 offset:44032
v_mfma_f32_16x16x32_bf16 v[108:111], a[204:207], v[136:139], v[108:111]
s_waitcnt lgkmcnt(6)
ds_read_b128 v[192:195], v21 offset:45056
ds_read_b128 v[196:199], v21 offset:46080
v_mfma_f32_16x16x32_bf16 v[96:99], a[208:211], v[140:143], v[96:99]
ds_read_b128 v[136:139], v20 offset:20480
v_mfma_f32_16x16x32_bf16 v[100:103], a[212:215], v[140:143], v[100:103]
v_mfma_f32_16x16x32_bf16 v[104:107], a[216:219], v[140:143], v[104:107]
s_waitcnt lgkmcnt(6)
v_add_f32 v128, v128, v176
v_add_f32 v129, v129, v177
v_mfma_f32_16x16x32_bf16 v[108:111], a[220:223], v[140:143], v[108:111]
v_add_f32 v130, v130, v178
v_add_f32 v131, v131, v179
v_mfma_f32_16x16x32_bf16 v[112:115], a[0:3], v[144:147], v[112:115]
s_waitcnt lgkmcnt(5)
ds_read_b128 v[140:143], v20 offset:21504
v_add_f32 v132, v132, v180
v_mfma_f32_16x16x32_bf16 v[116:119], a[4:7], v[144:147], v[116:119]
v_add_f32 v133, v133, v181
v_add_f32 v134, v134, v182
v_mfma_f32_16x16x32_bf16 v[120:123], a[8:11], v[144:147], v[120:123]
s_waitcnt lgkmcnt(5)
v_add_f32 v135, v135, v183
v_add_f32 v128, v128, v184
v_mfma_f32_16x16x32_bf16 v[124:127], a[12:15], v[144:147], v[124:127]
v_cvt_pk_bf16_f32 v164, v96, v97
v_cvt_pk_bf16_f32 v165, v98, v99
v_mfma_f32_16x16x32_bf16 v[112:115], a[16:19], v[148:151], v[112:115]
ds_read_b128 v[144:147], v20 offset:22528
v_cvt_pk_bf16_f32 v166, v100, v101
v_mfma_f32_16x16x32_bf16 v[116:119], a[20:23], v[148:151], v[116:119]
v_cvt_pk_bf16_f32 v167, v102, v103
v_add_f32 v129, v129, v185
v_mfma_f32_16x16x32_bf16 v[120:123], a[24:27], v[148:151], v[120:123]
v_add_f32 v130, v130, v186
v_add_f32 v131, v131, v187
v_mfma_f32_16x16x32_bf16 v[124:127], a[28:31], v[148:151], v[124:127]
s_waitcnt lgkmcnt(5)
v_add_f32 v132, v132, v188
v_add_f32 v133, v133, v189
s_waitcnt lgkmcnt(2)
v_mfma_f32_16x16x32_bf16 v[112:115], a[32:35], v[136:139], v[112:115]
ds_read_b128 v[148:151], v20 offset:23552
v_add_f32 v134, v134, v190
)"
R"(v_mfma_f32_16x16x32_bf16 v[116:119], a[36:39], v[136:139], v[116:119]
global_store_dwordx4 v13, v[164:167], s[38:39] offset:0 sc1
v_mfma_f32_16x16x32_bf16 v[120:123], a[40:43], v[136:139], v[120:123]
v_cvt_pk_bf16_f32 v168, v104, v105
v_cvt_pk_bf16_f32 v169, v106, v107
v_mfma_f32_16x16x32_bf16 v[124:127], a[44:47], v[136:139], v[124:127]
s_waitcnt lgkmcnt(2)
v_cvt_pk_bf16_f32 v170, v108, v109
v_cvt_pk_bf16_f32 v171, v110, v111
v_mfma_f32_16x16x32_bf16 v[112:115], a[48:51], v[140:143], v[112:115]
ds_read_b128 v[136:139], v20 offset:24576
v_add_f32 v135, v135, v191
v_mfma_f32_16x16x32_bf16 v[116:119], a[52:55], v[140:143], v[116:119]
global_store_dwordx4 v13, v[168:171], s[38:39] offset:1024 sc1
v_mfma_f32_16x16x32_bf16 v[120:123], a[56:59], v[140:143], v[120:123]
v_add_f32 v128, v128, v192
v_add_f32 v129, v129, v193
v_mfma_f32_16x16x32_bf16 v[124:127], a[60:63], v[140:143], v[124:127]
s_waitcnt lgkmcnt(2)
v_add_f32 v130, v130, v194
v_add_f32 v131, v131, v195
v_mfma_f32_16x16x32_bf16 v[112:115], a[64:67], v[144:147], v[112:115]
ds_read_b128 v[140:143], v20 offset:25600
v_add_f32 v132, v132, v196
v_mfma_f32_16x16x32_bf16 v[116:119], a[68:71], v[144:147], v[116:119]
v_add_f32 v133, v133, v197
v_add_f32 v134, v134, v198
v_mfma_f32_16x16x32_bf16 v[120:123], a[72:75], v[144:147], v[120:123]
s_add_u32 m0, s47, 18432
v_add_f32 v135, v135, v199
v_cvt_pk_bf16_f32 v172, v128, v129
v_mfma_f32_16x16x32_bf16 v[124:127], a[76:79], v[144:147], v[124:127]
s_waitcnt lgkmcnt(2)
v_cvt_pk_bf16_f32 v173, v130, v131
v_cvt_pk_bf16_f32 v174, v132, v133
v_mfma_f32_16x16x32_bf16 v[112:115], a[80:83], v[148:151], v[112:115]
ds_read_b128 v[144:147], v20 offset:26624
v_cvt_pk_bf16_f32 v175, v134, v135
v_mfma_f32_16x16x32_bf16 v[116:119], a[84:87], v[148:151], v[116:119]
global_store_dwordx4 v13, v[172:175], s[38:39] offset:2048 sc1
v_mfma_f32_16x16x32_bf16 v[120:123], a[88:91], v[148:151], v[120:123]
v_add_u32 v13, 18688, v13
v_add_f32 v200, v128, v129
v_mfma_f32_16x16x32_bf16 v[124:127], a[92:95], v[148:151], v[124:127]
s_waitcnt lgkmcnt(2)
v_mul_f32 v201, v128, v128
v_fmac_f32 v201, v129, v129
v_mfma_f32_16x16x32_bf16 v[112:115], a[96:99], v[136:139], v[112:115]
ds_read_b128 v[148:151], v20 offset:27648
v_add_f32 v200, v200, v130
v_mfma_f32_16x16x32_bf16 v[116:119], a[100:103], v[136:139], v[116:119]
v_fmac_f32 v201, v130, v130
v_add_f32 v200, v200, v131
v_mfma_f32_16x16x32_bf16 v[120:123], a[104:107], v[136:139], v[120:123]
global_load_lds_dwordx4 v202, s[38:39] offset:-2048 sc1
v_fmac_f32 v201, v131, v131
v_mfma_f32_16x16x32_bf16 v[124:127], a[108:111], v[136:139], v[124:127]
s_waitcnt lgkmcnt(2)
v_add_f32 v200, v200, v132
v_fmac_f32 v201, v132, v132
v_mfma_f32_16x16x32_bf16 v[112:115], a[112:115], v[140:143], v[112:115]
ds_read_b128 v[136:139], v20 offset:32768
v_add_f32 v200, v200, v133
v_mfma_f32_16x16x32_bf16 v[116:119], a[116:119], v[140:143], v[116:119]
v_fmac_f32 v201, v133, v133
v_add_f32 v200, v200, v134
v_mfma_f32_16x16x32_bf16 v[120:123], a[120:123], v[140:143], v[120:123]
v_fmac_f32 v201, v134, v134
v_add_f32 v200, v200, v135
v_mfma_f32_16x16x32_bf16 v[124:127], a[124:127], v[140:143], v[124:127]
s_waitcnt lgkmcnt(2)
v_fmac_f32 v201, v135, v135
v_add_f32 v200, v200, v96
v_mfma_f32_16x16x32_bf16 v[112:115], a[128:131], v[144:147], v[112:115]
ds_read_b128 v[140:143], v20 offset:33792
v_fmac_f32 v201, v96, v96
v_mfma_f32_16x16x32_bf16 v[116:119], a[132:135], v[144:147], v[116:119]
global_load_lds_dwordx4 v202, s[38:39] offset:-1024 sc1
v_add_f32 v200, v200, v97
v_mfma_f32_16x16x32_bf16 v[120:123], a[136:139], v[144:147], v[120:123]
v_fmac_f32 v201, v97, v97
v_add_f32 v200, v200, v98
v_mfma_f32_16x16x32_bf16 v[124:127], a[140:143], v[144:147], v[124:127]
s_waitcnt lgkmcnt(2)
v_fmac_f32 v201, v98, v98
v_add_f32 v200, v200, v99
v_mfma_f32_16x16x32_bf16 v[112:115], a[144:147], v[148:151], v[112:115]
ds_read_b128 v[144:147], v20 offset:34816
v_fmac_f32 v201, v99, v99
v_mfma_f32_16x16x32_bf16 v[116:119], a[148:151], v[148:151], v[116:119]
v_add_f32 v200, v200, v100
v_fmac_f32 v201, v100, v100
v_mfma_f32_16x16x32_bf16 v[120:123], a[152:155], v[148:151], v[120:123]
v_add_f32 v200, v200, v101
v_fmac_f32 v201, v101, v101
v_mfma_f32_16x16x32_bf16 v[124:127], a[156:159], v[148:151], v[124:127]
s_waitcnt lgkmcnt(2)
v_add_f32 v200, v200, v102
v_fmac_f32 v201, v102, v102
v_mfma_f32_16x16x32_bf16 v[112:115], a[224:227], v[136:139], v[112:115]
ds_read_b128 v[148:151], v20 offset:35840
global_load_lds_dwordx4 v202, s[38:39] offset:0 sc1
v_mfma_f32_16x16x32_bf16 v[116:119], a[228:231], v[136:139], v[116:119]
v_add_f32 v200, v200, v103
v_fmac_f32 v201, v103, v103
v_mfma_f32_16x16x32_bf16 v[120:123], a[232:235], v[136:139], v[120:123]
v_add_f32 v200, v200, v104
v_fmac_f32 v201, v104, v104
v_mfma_f32_16x16x32_bf16 v[124:127], a[236:239], v[136:139], v[124:127]
v_add_f32 v200, v200, v105
v_fmac_f32 v201, v105, v105
v_mfma_f32_16x16x32_bf16 v[128:131], v[56:59], v[136:139], 0
v_add_f32 v200, v200, v106
v_fmac_f32 v201, v106, v106
v_mfma_f32_16x16x32_bf16 v[132:135], v[60:63], v[136:139], 0
s_waitcnt lgkmcnt(2)
v_add_f32 v200, v200, v107
v_fmac_f32 v201, v107, v107
v_mfma_f32_16x16x32_bf16 v[112:115], a[240:243], v[140:143], v[112:115]
ds_read_b128 v[136:139], v20 offset:28672
v_add_f32 v200, v200, v108
v_mfma_f32_16x16x32_bf16 v[116:119], a[244:247], v[140:143], v[116:119]
global_load_lds_dwordx4 v202, s[38:39] offset:1024 sc1
v_add_u32 v11, 18432, v11
v_mfma_f32_16x16x32_bf16 v[120:123], a[248:251], v[140:143], v[120:123]
v_min_u32 v202, v11, v12
v_fmac_f32 v201, v108, v108
v_mfma_f32_16x16x32_bf16 v[124:127], a[252:255], v[140:143], v[124:127]
v_add_f32 v200, v200, v109
v_fmac_f32 v201, v109, v109
v_mfma_f32_16x16x32_bf16 v[128:131], v[64:67], v[140:143], v[128:131]
v_add_f32 v200, v200, v110
v_fmac_f32 v201, v110, v110
v_mfma_f32_16x16x32_bf16 v[132:135], v[68:71], v[140:143], v[132:135]
s_waitcnt lgkmcnt(2)
v_add_f32 v200, v200, v111
v_fmac_f32 v201, v111, v111
v_mfma_f32_16x16x32_bf16 v[112:115], v[24:27], v[144:147], v[112:115]
ds_read_b128 v[140:143], v20 offset:29696
v_mfma_f32_16x16x32_bf16 v[116:119], v[28:31], v[144:147], v[116:119]
ds_write_b64 v22, v[200:201] offset:58880
v_mfma_f32_16x16x32_bf16 v[120:123], v[32:35], v[144:147], v[120:123]
s_waitcnt vmcnt(8)
v_lshlrev_b32 v96, 16, v152
v_and_b32 v97, 0xffff0000, v152
v_mfma_f32_16x16x32_bf16 v[124:127], v[36:39], v[144:147], v[124:127]
v_lshlrev_b32 v98, 16, v153
v_and_b32 v99, 0xffff0000, v153
v_mfma_f32_16x16x32_bf16 v[128:131], v[72:75], v[144:147], v[128:131]
v_lshlrev_b32 v100, 16, v154
v_and_b32 v101, 0xffff0000, v154
v_mfma_f32_16x16x32_bf16 v[132:135], v[76:79], v[144:147], v[132:135]
s_waitcnt lgkmcnt(3)
v_lshlrev_b32 v102, 16, v155
v_and_b32 v103, 0xffff0000, v155
v_mfma_f32_16x16x32_bf16 v[112:115], v[40:43], v[148:151], v[112:115]
s_waitcnt vmcnt(7)
ds_read_b128 v[144:147], v20 offset:30720
v_lshlrev_b32 v104, 16, v156
v_mfma_f32_16x16x32_bf16 v[116:119], v[44:47], v[148:151], v[116:119]
v_and_b32 v105, 0xffff0000, v156
v_lshlrev_b32 v106, 16, v157
v_mfma_f32_16x16x32_bf16 v[120:123], v[48:51], v[148:151], v[120:123]
v_and_b32 v107, 0xffff0000, v157
v_lshlrev_b32 v108, 16, v158
v_mfma_f32_16x16x32_bf16 v[124:127], v[52:55], v[148:151], v[124:127]
v_and_b32 v109, 0xffff0000, v158
v_lshlrev_b32 v110, 16, v159
v_mfma_f32_16x16x32_bf16 v[128:131], v[80:83], v[148:151], v[128:131]
v_and_b32 v111, 0xffff0000, v159
v_mfma_f32_16x16x32_bf16 v[132:135], v[84:87], v[148:151], v[132:135]
s_waitcnt lgkmcnt(3)
global_load_dwordx4 v[152:155], v15, s[42:43] offset:0 sc1
v_mfma_f32_16x16x32_bf16 v[112:115], a[160:163], v[136:139], v[112:115]
ds_read_b128 v[148:151], v20 offset:31744
v_mfma_f32_16x16x32_bf16 v[116:119], a[164:167], v[136:139], v[116:119]
v_mfma_f32_16x16x32_bf16 v[120:123], a[168:171], v[136:139], v[120:123]
global_load_dwordx4 v[156:159], v15, s[42:43] offset:64 sc1
s_sub_u32 s42, s42, 18432
s_subb_u32 s43, s43, 0
v_mfma_f32_16x16x32_bf16 v[124:127], a[172:175], v[136:139], v[124:127]
s_waitcnt lgkmcnt(3)
v_mfma_f32_16x16x32_bf16 v[112:115], a[176:179], v[140:143], v[112:115]
v_mfma_f32_16x16x32_bf16 v[116:119], a[180:183], v[140:143], v[116:119]
v_xor_b32 v20, 0x10000, v20
v_mfma_f32_16x16x32_bf16 v[120:123], a[184:187], v[140:143], v[120:123]
v_mfma_f32_16x16x32_bf16 v[124:127], a[188:191], v[140:143], v[124:127]
s_waitcnt vmcnt(6) lgkmcnt(2)
s_barrier
s_waitcnt lgkmcnt(1)
v_mfma_f32_16x16x32_bf16 v[112:115], a[192:195], v[144:147], v[112:115]
ds_read_b128 v[136:139], v20 offset:0
ds_read_b128 v[140:143], v20 offset:1024
v_mfma_f32_16x16x32_bf16 v[116:119], a[196:199], v[144:147], v[116:119]
ds_read_b128 v[176:179], v21 offset:47104
ds_read_b128 v[180:183], v21 offset:48128
v_mfma_f32_16x16x32_bf16 v[120:123], a[200:203], v[144:147], v[120:123]
ds_read_b128 v[184:187], v21 offset:49152
ds_read_b128 v[188:191], v21 offset:50176
v_mfma_f32_16x16x32_bf16 v[124:127], a[204:207], v[144:147], v[124:127]
s_waitcnt lgkmcnt(6)
ds_read_b128 v[192:195], v21 offset:51200
ds_read_b128 v[196:199], v21 offset:52224
v_mfma_f32_16x16x32_bf16 v[112:115], a[208:211], v[148:151], v[112:115]
ds_read_b128 v[144:147], v20 offset:2048
v_mfma_f32_16x16x32_bf16 v[116:119], a[212:215], v[148:151], v[116:119]
v_mfma_f32_16x16x32_bf16 v[120:123], a[216:219], v[148:151], v[120:123]
s_waitcnt lgkmcnt(6)
v_add_f32 v128, v128, v176
v_add_f32 v129, v129, v177
v_mfma_f32_16x16x32_bf16 v[124:127], a[220:223], v[148:151], v[124:127]
s_sub_u32 s70, s70, 1
v_add_f32 v130, v130, v178
v_add_f32 v131, v131, v179
s_cbranch_scc0 .Lwo_top22
s_waitcnt lgkmcnt(0)
v_add_f32 v132, v132, v180
v_add_f32 v133, v133, v181
v_add_f32 v134, v134, v182
v_cvt_pk_bf16_f32 v164, v112, v113
v_cvt_pk_bf16_f32 v165, v114, v115
v_cvt_pk_bf16_f32 v166, v116, v117
v_cvt_pk_bf16_f32 v167, v118, v119
v_add_f32 v135, v135, v183
v_add_f32 v128, v128, v184
v_add_f32 v129, v129, v185
v_add_f32 v130, v130, v186
v_add_f32 v131, v131, v187
v_add_f32 v132, v132, v188
global_store_dwordx4 v13, v[164:167], s[38:39] offset:0 sc1
v_cvt_pk_bf16_f32 v168, v120, v121
v_cvt_pk_bf16_f32 v169, v122, v123
v_cvt_pk_bf16_f32 v170, v124, v125
v_cvt_pk_bf16_f32 v171, v126, v127
v_add_f32 v133, v133, v189
global_store_dwordx4 v13, v[168:171], s[38:39] offset:1024 sc1
v_add_f32 v134, v134, v190
v_add_f32 v135, v135, v191
v_add_f32 v128, v128, v192
v_add_f32 v129, v129, v193
v_add_f32 v130, v130, v194
v_add_f32 v131, v131, v195
v_add_f32 v132, v132, v196
v_add_f32 v133, v133, v197
v_add_f32 v134, v134, v198
v_add_f32 v135, v135, v199
v_cvt_pk_bf16_f32 v172, v128, v129
v_cvt_pk_bf16_f32 v173, v130, v131
v_cvt_pk_bf16_f32 v174, v132, v133
v_cvt_pk_bf16_f32 v175, v134, v135
global_store_dwordx4 v13, v[172:175], s[38:39] offset:2048 sc1
v_add_u32 v13, 18688, v13
v_add_f32 v200, v128, v129
v_mul_f32 v201, v128, v128
v_fmac_f32 v201, v129, v129
v_add_f32 v200, v200, v130
v_fmac_f32 v201, v130, v130
v_add_f32 v200, v200, v131
v_fmac_f32 v201, v131, v131
v_add_f32 v200, v200, v132
v_fmac_f32 v201, v132, v132
v_add_f32 v200, v200, v133
v_fmac_f32 v201, v133, v133
v_add_f32 v200, v200, v134
v_fmac_f32 v201, v134, v134
v_add_f32 v200, v200, v135
v_fmac_f32 v201, v135, v135
v_add_f32 v200, v200, v112
v_fmac_f32 v201, v112, v112
v_add_f32 v200, v200, v113
v_fmac_f32 v201, v113, v113
v_add_f32 v200, v200, v114
v_fmac_f32 v201, v114, v114
v_add_f32 v200, v200, v115
v_fmac_f32 v201, v115, v115
v_add_f32 v200, v200, v116
v_fmac_f32 v201, v116, v116
v_add_f32 v200, v200, v117
v_fmac_f32 v201, v117, v117
v_add_f32 v200, v200, v118
v_fmac_f32 v201, v118, v118
v_add_f32 v200, v200, v119
v_fmac_f32 v201, v119, v119
v_add_f32 v200, v200, v120
v_fmac_f32 v201, v120, v120
v_add_f32 v200, v200, v121
v_fmac_f32 v201, v121, v121
v_add_f32 v200, v200, v122
v_fmac_f32 v201, v122, v122
v_add_f32 v200, v200, v123
v_add_u32 v11, 18432, v11
v_min_u32 v202, v11, v12
v_fmac_f32 v201, v123, v123
v_add_f32 v200, v200, v124
v_fmac_f32 v201, v124, v124
v_add_f32 v200, v200, v125
v_fmac_f32 v201, v125, v125
v_add_f32 v200, v200, v126
v_fmac_f32 v201, v126, v126
v_add_f32 v200, v200, v127
v_fmac_f32 v201, v127, v127
ds_write_b64 v22, v[200:201] offset:60928
s_waitcnt vmcnt(0) lgkmcnt(0)
s_barrier
s_barrier
s_endpgm
.Lwo_hk5:
s_mov_b64 s[42:43], s[44:45]
s_branch .Lwo_hr6
.Lwo_hk8:
s_mov_b64 s[42:43], s[44:45]
s_branch .Lwo_hr9
.Lwo_sp11:
s_sleep 4
global_load_dword v23, v16, s[38:39] sc1
s_add_u32 s71, s71, 1
s_waitcnt vmcnt(0)
v_sub_u32 v23, v23, v17
v_cmp_lt_u32 vcc, 0x7f, v23
s_cbranch_vccz .Lwo_rt12
s_cmp_lt_u32 s71, 3000000
s_cbranch_scc1 .Lwo_sp11
s_branch .Lwo_rt12
.Lwo_sp13:
s_sleep 4
global_load_dword v23, v16, s[38:39] sc1
s_add_u32 s71, s71, 1
s_waitcnt vmcnt(0)
v_sub_u32 v23, v23, v17
v_cmp_lt_u32 vcc, 0x7f, v23
s_cbranch_vccz .Lwo_rt14
s_cmp_lt_u32 s71, 3000000
s_cbranch_scc1 .Lwo_sp13
s_branch .Lwo_rt14
.Lwo_hk15:
s_mov_b64 s[42:43], s[44:45]
s_branch .Lwo_hr16
.Lwo_sp17:
s_sleep 4
global_load_dword v23, v16, s[38:39] sc1
s_add_u32 s71, s71, 1
s_waitcnt vmcnt(0)
v_sub_u32 v23, v23, v17
v_cmp_lt_u32 vcc, 0x7f, v23
s_cbranch_vccz .Lwo_rt18
s_cmp_lt_u32 s71, 3000000
s_cbranch_scc1 .Lwo_sp17
s_branch .Lwo_rt18
.Lwo_hk20:
s_mov_b64 s[42:43], s[44:45]
s_branch .Lwo_hr21
.Lrole2:
s_add_u32 m0, s50, 4096
s_nop 0
global_load_lds_dwordx4 v0, s[40:41] offset:-4096
global_load_lds_dwordx4 v1, s[40:41] offset:-3072
global_load_lds_dwordx4 v2, s[40:41] offset:-2048
global_load_lds_dwordx4 v3, s[40:41] offset:-1024
global_load_lds_dwordx4 v4, s[40:41] offset:0
global_load_lds_dwordx4 v5, s[40:41] offset:1024
s_add_u32 s40, s40, 73728
s_addc_u32 s41, s41, 0
s_add_u32 m0, s50, 28672
s_nop 0
global_load_lds_dwordx4 v0, s[40:41] offset:-4096
global_load_lds_dwordx4 v1, s[40:41] offset:-3072
global_load_lds_dwordx4 v2, s[40:41] offset:-2048
global_load_lds_dwordx4 v3, s[40:41] offset:-1024
global_load_lds_dwordx4 v4, s[40:41] offset:0
global_load_lds_dwordx4 v5, s[40:41] offset:1024
s_add_u32 s40, s40, 73728
s_addc_u32 s41, s41, 0
s_add_u32 m0, s50, 53248
s_nop 0
global_load_lds_dwordx4 v0, s[40:41] offset:-4096
global_load_lds_dwordx4 v1, s[40:41] offset:-3072
global_load_lds_dwordx4 v2, s[40:41] offset:-2048
global_load_lds_dwordx4 v3, s[40:41] offset:-1024
global_load_lds_dwordx4 v4, s[40:41] offset:0
global_load_lds_dwordx4 v5, s[40:41] offset:1024
s_add_u32 s40, s40, 73728
s_addc_u32 s41, s41, 0
s_add_u32 m0, s50, 77824
s_nop 0
global_load_lds_dwordx4 v0, s[40:41] offset:-4096
global_load_lds_dwordx4 v1, s[40:41] offset:-3072
global_load_lds_dwordx4 v2, s[40:41] offset:-2048
global_load_lds_dwordx4 v3, s[40:41] offset:-1024
global_load_lds_dwordx4 v4, s[40:41] offset:0
global_load_lds_dwordx4 v5, s[40:41] offset:1024
s_add_u32 s40, s40, 73728
s_addc_u32 s41, s41, 0
s_add_u32 m0, s50, 102400
s_nop 0
global_load_lds_dwordx4 v0, s[40:41] offset:-4096
global_load_lds_dwordx4 v1, s[40:41] offset:-3072
global_load_lds_dwordx4 v2, s[40:41] offset:-2048
global_load_lds_dwordx4 v3, s[40:41] offset:-1024
global_load_lds_dwordx4 v4, s[40:41] offset:0
global_load_lds_dwordx4 v5, s[40:41] offset:1024
)"
R"(s_add_u32 s40, s40, 73728
s_addc_u32 s41, s41, 0
s_add_u32 m0, s50, 126976
s_nop 0
global_load_lds_dwordx4 v0, s[40:41] offset:-4096
global_load_lds_dwordx4 v1, s[40:41] offset:-3072
global_load_lds_dwordx4 v2, s[40:41] offset:-2048
global_load_lds_dwordx4 v3, s[40:41] offset:-1024
global_load_lds_dwordx4 v4, s[40:41] offset:0
global_load_lds_dwordx4 v5, s[40:41] offset:1024
s_add_u32 s40, s40, 73728
s_addc_u32 s41, s41, 0
v_add_u32 v138, 0x10000, v6
v_add_u32 v139, 0x20000, v6
v_add_u32 v140, 0x10000, v7
v_add_u32 v141, 0x20000, v7
s_waitcnt lgkmcnt(0)
s_mul_i32 s37, s37, 0x9E3779B1
s_add_u32 s37, s37, 0x7F4A7C15
v_add_u32 v20, s37, v20
v_add_u32 v22, s37, v22
v_add_u32 v23, s37, v23
v_add_u32 v25, s37, v25
v_add_u32 v27, s37, v27
s_waitcnt vmcnt(30)
s_barrier
ds_read_b64_tr_b16 a[0:1], v6 offset:0
ds_read_b64_tr_b16 a[2:3], v6 offset:1536
ds_read_b64_tr_b16 a[4:5], v6 offset:8
ds_read_b64_tr_b16 a[6:7], v6 offset:1544
ds_read_b64_tr_b16 a[8:9], v7 offset:0
ds_read_b64_tr_b16 a[10:11], v7 offset:1536
ds_read_b64_tr_b16 a[12:13], v6 offset:12288
ds_read_b64_tr_b16 a[14:15], v6 offset:13824
ds_read_b64_tr_b16 a[16:17], v6 offset:12296
ds_read_b64_tr_b16 a[18:19], v6 offset:13832
ds_read_b64_tr_b16 a[20:21], v7 offset:12288
ds_read_b64_tr_b16 a[22:23], v7 offset:13824
s_waitcnt lgkmcnt(0)
s_barrier
s_add_u32 m0, s50, 4096
s_nop 0
global_load_lds_dwordx4 v0, s[40:41] offset:-4096
global_load_lds_dwordx4 v1, s[40:41] offset:-3072
global_load_lds_dwordx4 v2, s[40:41] offset:-2048
global_load_lds_dwordx4 v3, s[40:41] offset:-1024
global_load_lds_dwordx4 v4, s[40:41] offset:0
global_load_lds_dwordx4 v5, s[40:41] offset:1024
s_add_u32 s40, s40, 73728
s_addc_u32 s41, s41, 0
s_waitcnt vmcnt(30)
s_barrier
ds_read_b64_tr_b16 a[24:25], v6 offset:24576
ds_read_b64_tr_b16 a[26:27], v6 offset:26112
ds_read_b64_tr_b16 a[28:29], v6 offset:24584
ds_read_b64_tr_b16 a[30:31], v6 offset:26120
ds_read_b64_tr_b16 a[32:33], v7 offset:24576
ds_read_b64_tr_b16 a[34:35], v7 offset:26112
ds_read_b64_tr_b16 a[36:37], v6 offset:36864
ds_read_b64_tr_b16 a[38:39], v6 offset:38400
ds_read_b64_tr_b16 a[40:41], v6 offset:36872
ds_read_b64_tr_b16 a[42:43], v6 offset:38408
ds_read_b64_tr_b16 a[44:45], v7 offset:36864
ds_read_b64_tr_b16 a[46:47], v7 offset:38400
s_waitcnt lgkmcnt(0)
s_barrier
s_add_u32 m0, s50, 28672
s_nop 0
global_load_lds_dwordx4 v0, s[40:41] offset:-4096
global_load_lds_dwordx4 v1, s[40:41] offset:-3072
global_load_lds_dwordx4 v2, s[40:41] offset:-2048
global_load_lds_dwordx4 v3, s[40:41] offset:-1024
global_load_lds_dwordx4 v4, s[40:41] offset:0
global_load_lds_dwordx4 v5, s[40:41] offset:1024
s_add_u32 s40, s40, 73728
s_addc_u32 s41, s41, 0
s_waitcnt vmcnt(30)
s_barrier
ds_read_b64_tr_b16 a[48:49], v6 offset:49152
ds_read_b64_tr_b16 a[50:51], v6 offset:50688
ds_read_b64_tr_b16 a[52:53], v6 offset:49160
ds_read_b64_tr_b16 a[54:55], v6 offset:50696
ds_read_b64_tr_b16 a[56:57], v7 offset:49152
ds_read_b64_tr_b16 a[58:59], v7 offset:50688
ds_read_b64_tr_b16 a[60:61], v6 offset:61440
ds_read_b64_tr_b16 a[62:63], v6 offset:62976
ds_read_b64_tr_b16 a[64:65], v6 offset:61448
ds_read_b64_tr_b16 a[66:67], v6 offset:62984
ds_read_b64_tr_b16 a[68:69], v7 offset:61440
ds_read_b64_tr_b16 a[70:71], v7 offset:62976
s_waitcnt lgkmcnt(0)
s_barrier
s_add_u32 m0, s50, 53248
s_nop 0
global_load_lds_dwordx4 v0, s[40:41] offset:-4096
global_load_lds_dwordx4 v1, s[40:41] offset:-3072
global_load_lds_dwordx4 v2, s[40:41] offset:-2048
global_load_lds_dwordx4 v3, s[40:41] offset:-1024
global_load_lds_dwordx4 v4, s[40:41] offset:0
global_load_lds_dwordx4 v5, s[40:41] offset:1024
s_add_u32 s40, s40, 73728
s_addc_u32 s41, s41, 0
s_waitcnt vmcnt(30)
s_barrier
ds_read_b64_tr_b16 a[72:73], v138 offset:8192
ds_read_b64_tr_b16 a[74:75], v138 offset:9728
ds_read_b64_tr_b16 a[76:77], v138 offset:8200
ds_read_b64_tr_b16 a[78:79], v138 offset:9736
ds_read_b64_tr_b16 a[80:81], v140 offset:8192
ds_read_b64_tr_b16 a[82:83], v140 offset:9728
ds_read_b64_tr_b16 a[84:85], v138 offset:20480
ds_read_b64_tr_b16 a[86:87], v138 offset:22016
ds_read_b64_tr_b16 a[88:89], v138 offset:20488
ds_read_b64_tr_b16 a[90:91], v138 offset:22024
ds_read_b64_tr_b16 a[92:93], v140 offset:20480
ds_read_b64_tr_b16 a[94:95], v140 offset:22016
s_waitcnt lgkmcnt(0)
s_barrier
s_add_u32 m0, s50, 77824
s_nop 0
global_load_lds_dwordx4 v0, s[40:41] offset:-4096
global_load_lds_dwordx4 v1, s[40:41] offset:-3072
global_load_lds_dwordx4 v2, s[40:41] offset:-2048
global_load_lds_dwordx4 v3, s[40:41] offset:-1024
global_load_lds_dwordx4 v4, s[40:41] offset:0
global_load_lds_dwordx4 v5, s[40:41] offset:1024
s_add_u32 s40, s40, 73728
s_addc_u32 s41, s41, 0
s_waitcnt vmcnt(30)
s_barrier
ds_read_b64_tr_b16 a[96:97], v138 offset:32768
ds_read_b64_tr_b16 a[98:99], v138 offset:34304
ds_read_b64_tr_b16 a[100:101], v138 offset:32776
ds_read_b64_tr_b16 a[102:103], v138 offset:34312
ds_read_b64_tr_b16 a[104:105], v140 offset:32768
ds_read_b64_tr_b16 a[106:107], v140 offset:34304
ds_read_b64_tr_b16 a[108:109], v138 offset:45056
ds_read_b64_tr_b16 a[110:111], v138 offset:46592
ds_read_b64_tr_b16 a[112:113], v138 offset:45064
ds_read_b64_tr_b16 a[114:115], v138 offset:46600
ds_read_b64_tr_b16 a[116:117], v140 offset:45056
ds_read_b64_tr_b16 a[118:119], v140 offset:46592
s_waitcnt lgkmcnt(0)
s_barrier
s_add_u32 m0, s50, 102400
s_nop 0
global_load_lds_dwordx4 v0, s[40:41] offset:-4096
global_load_lds_dwordx4 v1, s[40:41] offset:-3072
global_load_lds_dwordx4 v2, s[40:41] offset:-2048
global_load_lds_dwordx4 v3, s[40:41] offset:-1024
global_load_lds_dwordx4 v4, s[40:41] offset:0
global_load_lds_dwordx4 v5, s[40:41] offset:1024
s_add_u32 s40, s40, 73728
s_addc_u32 s41, s41, 0
s_waitcnt vmcnt(30)
s_barrier
ds_read_b64_tr_b16 a[120:121], v138 offset:57344
ds_read_b64_tr_b16 a[122:123], v138 offset:58880
ds_read_b64_tr_b16 a[124:125], v138 offset:57352
ds_read_b64_tr_b16 a[126:127], v138 offset:58888
ds_read_b64_tr_b16 a[128:129], v140 offset:57344
ds_read_b64_tr_b16 a[130:131], v140 offset:58880
ds_read_b64_tr_b16 a[132:133], v139 offset:4096
ds_read_b64_tr_b16 a[134:135], v139 offset:5632
ds_read_b64_tr_b16 a[136:137], v139 offset:4104
ds_read_b64_tr_b16 a[138:139], v139 offset:5640
ds_read_b64_tr_b16 a[140:141], v141 offset:4096
ds_read_b64_tr_b16 a[142:143], v141 offset:5632
s_waitcnt lgkmcnt(0)
s_barrier
s_add_u32 m0, s50, 126976
s_nop 0
global_load_lds_dwordx4 v0, s[40:41] offset:-4096
global_load_lds_dwordx4 v1, s[40:41] offset:-3072
global_load_lds_dwordx4 v2, s[40:41] offset:-2048
global_load_lds_dwordx4 v3, s[40:41] offset:-1024
global_load_lds_dwordx4 v4, s[40:41] offset:0
global_load_lds_dwordx4 v5, s[40:41] offset:1024
s_add_u32 s40, s40, 73728
s_addc_u32 s41, s41, 0
s_waitcnt vmcnt(30)
s_barrier
ds_read_b64_tr_b16 a[144:145], v6 offset:0
ds_read_b64_tr_b16 a[146:147], v6 offset:1536
ds_read_b64_tr_b16 a[148:149], v6 offset:8
ds_read_b64_tr_b16 a[150:151], v6 offset:1544
ds_read_b64_tr_b16 a[152:153], v7 offset:0
ds_read_b64_tr_b16 a[154:155], v7 offset:1536
ds_read_b64_tr_b16 a[156:157], v6 offset:12288
ds_read_b64_tr_b16 a[158:159], v6 offset:13824
ds_read_b64_tr_b16 a[160:161], v6 offset:12296
ds_read_b64_tr_b16 a[162:163], v6 offset:13832
ds_read_b64_tr_b16 a[164:165], v7 offset:12288
ds_read_b64_tr_b16 a[166:167], v7 offset:13824
s_waitcnt vmcnt(24)
s_barrier
ds_read_b64_tr_b16 a[168:169], v6 offset:24576
ds_read_b64_tr_b16 a[170:171], v6 offset:26112
ds_read_b64_tr_b16 a[172:173], v6 offset:24584
ds_read_b64_tr_b16 a[174:175], v6 offset:26120
ds_read_b64_tr_b16 a[176:177], v7 offset:24576
ds_read_b64_tr_b16 a[178:179], v7 offset:26112
ds_read_b64_tr_b16 a[180:181], v6 offset:36864
ds_read_b64_tr_b16 a[182:183], v6 offset:38400
ds_read_b64_tr_b16 a[184:185], v6 offset:36872
ds_read_b64_tr_b16 a[186:187], v6 offset:38408
ds_read_b64_tr_b16 a[188:189], v7 offset:36864
ds_read_b64_tr_b16 a[190:191], v7 offset:38400
s_waitcnt vmcnt(18)
s_barrier
ds_read_b64_tr_b16 a[192:193], v6 offset:49152
ds_read_b64_tr_b16 a[194:195], v6 offset:50688
ds_read_b64_tr_b16 a[196:197], v6 offset:49160
ds_read_b64_tr_b16 a[198:199], v6 offset:50696
ds_read_b64_tr_b16 a[200:201], v7 offset:49152
ds_read_b64_tr_b16 a[202:203], v7 offset:50688
ds_read_b64_tr_b16 a[204:205], v6 offset:61440
ds_read_b64_tr_b16 a[206:207], v6 offset:62976
ds_read_b64_tr_b16 a[208:209], v6 offset:61448
ds_read_b64_tr_b16 a[210:211], v6 offset:62984
ds_read_b64_tr_b16 a[212:213], v7 offset:61440
ds_read_b64_tr_b16 a[214:215], v7 offset:62976
s_waitcnt vmcnt(12)
s_barrier
ds_read_b64_tr_b16 a[216:217], v138 offset:8192
ds_read_b64_tr_b16 a[218:219], v138 offset:9728
ds_read_b64_tr_b16 a[220:221], v138 offset:8200
ds_read_b64_tr_b16 a[222:223], v138 offset:9736
ds_read_b64_tr_b16 a[224:225], v140 offset:8192
ds_read_b64_tr_b16 a[226:227], v140 offset:9728
ds_read_b64_tr_b16 a[228:229], v138 offset:20480
ds_read_b64_tr_b16 a[230:231], v138 offset:22016
ds_read_b64_tr_b16 a[232:233], v138 offset:20488
ds_read_b64_tr_b16 a[234:235], v138 offset:22024
ds_read_b64_tr_b16 a[236:237], v140 offset:20480
ds_read_b64_tr_b16 a[238:239], v140 offset:22016
s_waitcnt vmcnt(6)
s_barrier
ds_read_b64_tr_b16 a[240:241], v138 offset:32768
ds_read_b64_tr_b16 a[242:243], v138 offset:34304
ds_read_b64_tr_b16 a[244:245], v138 offset:32776
ds_read_b64_tr_b16 a[246:247], v138 offset:34312
ds_read_b64_tr_b16 a[248:249], v140 offset:32768
ds_read_b64_tr_b16 a[250:251], v140 offset:34304
ds_read_b64_tr_b16 a[252:253], v138 offset:45056
ds_read_b64_tr_b16 a[254:255], v138 offset:46592
ds_read_b64_tr_b16 v[30:31], v138 offset:45064
ds_read_b64_tr_b16 v[32:33], v138 offset:46600
ds_read_b64_tr_b16 v[34:35], v140 offset:45056
ds_read_b64_tr_b16 v[36:37], v140 offset:46592
s_waitcnt vmcnt(0)
s_barrier
ds_read_b64_tr_b16 v[38:39], v138 offset:57344
ds_read_b64_tr_b16 v[40:41], v138 offset:58880
ds_read_b64_tr_b16 v[42:43], v138 offset:57352
ds_read_b64_tr_b16 v[44:45], v138 offset:58888
ds_read_b64_tr_b16 v[46:47], v140 offset:57344
ds_read_b64_tr_b16 v[48:49], v140 offset:58880
ds_read_b64_tr_b16 v[50:51], v139 offset:4096
ds_read_b64_tr_b16 v[52:53], v139 offset:5632
ds_read_b64_tr_b16 v[54:55], v139 offset:4104
ds_read_b64_tr_b16 v[56:57], v139 offset:5640
ds_read_b64_tr_b16 v[58:59], v141 offset:4096
ds_read_b64_tr_b16 v[60:61], v141 offset:5632
s_waitcnt lgkmcnt(0)
s_barrier
s_and_b32 s75, s36, 3
s_cmp_eq_u32 s75, 1
s_cbranch_scc1 .Ldn_w2
s_cmp_eq_u32 s75, 2
s_cbranch_scc1 .Ldn_w3
s_cmp_eq_u32 s75, 3
s_cbranch_scc1 .Ldn_w4
.Ldn_w1:
v_add_u32 v29, -1, v20
s_add_u32 m0, s51, 24576
s_mov_b32 s73, 27
s_mov_b32 s70, 32
s_waitcnt vmcnt(0) lgkmcnt(0)
s_nop 7
.p2align 6
.Ldn_top8:
s_waitcnt lgkmcnt(11)
v_mfma_f32_16x16x32_bf16 v[62:65], a[0:3], v[86:89], 0
ds_read_b128 v[98:101], v28 offset:3072
v_mfma_f32_16x16x32_bf16 v[66:69], a[4:7], v[86:89], 0
s_cmp_eq_u32 s70, s73
s_cbranch_scc1 .Ldn_hk5
.Ldn_hr6:
v_mfma_f32_16x16x32_bf16 v[70:73], a[8:11], v[86:89], 0
s_waitcnt lgkmcnt(9)
v_lshlrev_b32 v126, 16, v108
v_add_f32 v122, v122, v124
s_waitcnt lgkmcnt(7)
v_mfma_f32_16x16x32_bf16 v[62:65], a[12:15], v[90:93], v[62:65]
ds_read_b128 v[86:89], v28 offset:4096
v_cvt_pk_bf16_f32 v102, v74, v75
v_mfma_f32_16x16x32_bf16 v[66:69], a[16:19], v[90:93], v[66:69]
v_mfma_f32_16x16x32_bf16 v[70:73], a[20:23], v[90:93], v[70:73]
s_waitcnt vmcnt(6)
s_barrier
s_waitcnt lgkmcnt(2)
v_mfma_f32_16x16x32_bf16 v[62:65], a[24:27], v[94:97], v[62:65]
ds_read_b128 v[90:93], v28 offset:5120
v_cvt_pk_bf16_f32 v103, v76, v77
v_mfma_f32_16x16x32_bf16 v[66:69], a[28:31], v[94:97], v[66:69]
v_cvt_pk_bf16_f32 v104, v78, v79
v_cvt_pk_bf16_f32 v105, v80, v81
v_mfma_f32_16x16x32_bf16 v[70:73], a[32:35], v[94:97], v[70:73]
s_waitcnt lgkmcnt(2)
v_cvt_pk_bf16_f32 v106, v82, v83
v_cvt_pk_bf16_f32 v107, v84, v85
v_mfma_f32_16x16x32_bf16 v[62:65], a[36:39], v[98:101], v[62:65]
ds_read_b128 v[94:97], v28 offset:6144
v_and_b32 v127, 0xffff0000, v108
v_mfma_f32_16x16x32_bf16 v[66:69], a[40:43], v[98:101], v[66:69]
global_store_dwordx4 v9, v[102:105], s[38:39] sc1
v_mfma_f32_16x16x32_bf16 v[70:73], a[44:47], v[98:101], v[70:73]
s_waitcnt lgkmcnt(2)
v_add_u32 v9, 110592, v9
v_add_f32 v123, v123, v125
v_mfma_f32_16x16x32_bf16 v[62:65], a[48:51], v[86:89], v[62:65]
ds_read_b128 v[98:101], v28 offset:7168
v_lshlrev_b32 v128, 16, v109
v_mfma_f32_16x16x32_bf16 v[66:69], a[52:55], v[86:89], v[66:69]
global_store_dwordx2 v10, v[106:107], s[38:39] sc1
v_mfma_f32_16x16x32_bf16 v[70:73], a[56:59], v[86:89], v[70:73]
s_waitcnt lgkmcnt(2)
v_add_u32 v10, 110592, v10
v_mul_f32 v122, 0x3ae38e39, v122
v_mfma_f32_16x16x32_bf16 v[62:65], a[60:63], v[90:93], v[62:65]
ds_read_b128 v[86:89], v28 offset:8192
v_and_b32 v129, 0xffff0000, v109
v_mfma_f32_16x16x32_bf16 v[66:69], a[64:67], v[90:93], v[66:69]
v_mul_f32 v123, 0x3ae38e39, v123
v_lshlrev_b32 v130, 16, v110
v_mfma_f32_16x16x32_bf16 v[70:73], a[68:71], v[90:93], v[70:73]
s_waitcnt lgkmcnt(2)
v_fma_f32 v137, -v122, v122, v123
v_and_b32 v131, 0xffff0000, v110
v_mfma_f32_16x16x32_bf16 v[62:65], a[72:75], v[94:97], v[62:65]
ds_read_b128 v[90:93], v28 offset:9216
v_max_f32 v137, 0, v137
v_mfma_f32_16x16x32_bf16 v[66:69], a[76:79], v[94:97], v[66:69]
v_lshlrev_b32 v132, 16, v111
v_cvt_pk_bf16_f32 v137, v137, v137
v_mfma_f32_16x16x32_bf16 v[70:73], a[80:83], v[94:97], v[70:73]
s_waitcnt lgkmcnt(2)
v_and_b32 v133, 0xffff0000, v111
v_lshlrev_b32 v137, 16, v137
v_mfma_f32_16x16x32_bf16 v[62:65], a[84:87], v[98:101], v[62:65]
ds_read_b128 v[94:97], v28 offset:10240
v_add_f32 v126, v126, v130
v_mfma_f32_16x16x32_bf16 v[66:69], a[88:91], v[98:101], v[66:69]
v_add_f32 v137, 0x3727c5ac, v137
v_add_f32 v127, v127, v131
v_mfma_f32_16x16x32_bf16 v[70:73], a[92:95], v[98:101], v[70:73]
s_waitcnt lgkmcnt(2)
v_cvt_pk_bf16_f32 v137, v137, v137
v_add_f32 v128, v128, v132
v_mfma_f32_16x16x32_bf16 v[62:65], a[96:99], v[86:89], v[62:65]
ds_read_b128 v[98:101], v28 offset:11264
v_lshlrev_b32 v137, 16, v137
v_mfma_f32_16x16x32_bf16 v[66:69], a[100:103], v[86:89], v[66:69]
v_add_f32 v129, v129, v133
v_mfma_f32_16x16x32_bf16 v[70:73], a[104:107], v[86:89], v[70:73]
s_waitcnt lgkmcnt(2)
v_sqrt_f32 v137, v137
v_mfma_f32_16x16x32_bf16 v[62:65], a[108:111], v[90:93], v[62:65]
ds_read_b128 v[86:89], v28 offset:12288
v_lshlrev_b32 v130, 16, v112
v_mfma_f32_16x16x32_bf16 v[66:69], a[112:115], v[90:93], v[66:69]
v_cvt_pk_bf16_f32 v137, v137, v137
v_and_b32 v131, 0xffff0000, v112
v_mfma_f32_16x16x32_bf16 v[70:73], a[116:119], v[90:93], v[70:73]
s_waitcnt lgkmcnt(2)
v_lshlrev_b32 v137, 16, v137
v_lshlrev_b32 v132, 16, v113
v_mfma_f32_16x16x32_bf16 v[62:65], a[120:123], v[94:97], v[62:65]
ds_read_b128 v[90:93], v28 offset:13312
v_mfma_f32_16x16x32_bf16 v[66:69], a[124:127], v[94:97], v[66:69]
v_rcp_f32 v136, v137
v_mfma_f32_16x16x32_bf16 v[70:73], a[128:131], v[94:97], v[70:73]
s_waitcnt lgkmcnt(2)
v_and_b32 v133, 0xffff0000, v113
v_mul_f32 v136, v136, v136
v_mfma_f32_16x16x32_bf16 v[62:65], a[132:135], v[98:101], v[62:65]
s_xor_b32 s51, s51, 0x10000
ds_read_b128 v[94:97], v28 offset:14336
v_add_f32 v126, v126, v130
v_mfma_f32_16x16x32_bf16 v[66:69], a[136:139], v[98:101], v[66:69]
s_add_u32 m0, s51, 0
v_add_f32 v127, v127, v131
v_add_f32 v128, v128, v132
v_mfma_f32_16x16x32_bf16 v[70:73], a[140:143], v[98:101], v[70:73]
s_waitcnt lgkmcnt(2)
v_mfma_f32_16x16x32_bf16 v[62:65], a[144:147], v[86:89], v[62:65]
s_barrier
ds_read_b128 v[98:101], v28 offset:15360
v_mfma_f32_16x16x32_bf16 v[66:69], a[148:151], v[86:89], v[66:69]
)"
R"(global_load_lds_dwordx4 v8, s[38:39] offset:-2048 sc1
v_add_f32 v129, v129, v133
v_mfma_f32_16x16x32_bf16 v[70:73], a[152:155], v[86:89], v[70:73]
s_waitcnt lgkmcnt(2)
v_lshlrev_b32 v130, 16, v114
v_and_b32 v131, 0xffff0000, v114
v_mfma_f32_16x16x32_bf16 v[62:65], a[156:159], v[90:93], v[62:65]
ds_read_b128 v[86:89], v28 offset:16384
v_lshlrev_b32 v132, 16, v115
v_mfma_f32_16x16x32_bf16 v[66:69], a[160:163], v[90:93], v[66:69]
global_load_lds_dwordx4 v8, s[38:39] offset:-1024 sc1
v_and_b32 v133, 0xffff0000, v115
v_mfma_f32_16x16x32_bf16 v[70:73], a[164:167], v[90:93], v[70:73]
s_waitcnt lgkmcnt(2)
v_add_f32 v126, v126, v130
v_add_f32 v127, v127, v131
v_mfma_f32_16x16x32_bf16 v[62:65], a[168:171], v[94:97], v[62:65]
ds_read_b128 v[90:93], v28 offset:17408
v_add_f32 v128, v128, v132
v_mfma_f32_16x16x32_bf16 v[66:69], a[172:175], v[94:97], v[66:69]
global_load_lds_dwordx4 v8, s[38:39] offset:0 sc1
v_add_f32 v129, v129, v133
v_mfma_f32_16x16x32_bf16 v[70:73], a[176:179], v[94:97], v[70:73]
s_waitcnt lgkmcnt(2)
v_lshlrev_b32 v130, 16, v116
v_and_b32 v131, 0xffff0000, v116
v_mfma_f32_16x16x32_bf16 v[62:65], a[180:183], v[98:101], v[62:65]
ds_read_b128 v[94:97], v28 offset:18432
v_lshlrev_b32 v132, 16, v117
v_mfma_f32_16x16x32_bf16 v[66:69], a[184:187], v[98:101], v[66:69]
global_load_lds_dwordx4 v8, s[38:39] offset:1024 sc1
v_and_b32 v133, 0xffff0000, v117
v_mfma_f32_16x16x32_bf16 v[70:73], a[188:191], v[98:101], v[70:73]
s_waitcnt lgkmcnt(2)
v_add_f32 v126, v126, v130
v_add_f32 v127, v127, v131
v_mfma_f32_16x16x32_bf16 v[62:65], a[192:195], v[86:89], v[62:65]
ds_read_b128 v[98:101], v28 offset:19456
v_add_f32 v128, v128, v132
v_mfma_f32_16x16x32_bf16 v[66:69], a[196:199], v[86:89], v[66:69]
global_load_lds_dwordx4 v8, s[38:39] offset:2048 sc1
v_add_f32 v129, v129, v133
v_mfma_f32_16x16x32_bf16 v[70:73], a[200:203], v[86:89], v[70:73]
s_waitcnt lgkmcnt(2)
v_lshlrev_b32 v130, 16, v118
v_and_b32 v131, 0xffff0000, v118
v_mfma_f32_16x16x32_bf16 v[62:65], a[204:207], v[90:93], v[62:65]
ds_read_b128 v[86:89], v28 offset:20480
v_lshlrev_b32 v132, 16, v119
v_mfma_f32_16x16x32_bf16 v[66:69], a[208:211], v[90:93], v[66:69]
global_load_lds_dwordx4 v8, s[38:39] offset:3072 sc1
v_and_b32 v133, 0xffff0000, v119
v_mfma_f32_16x16x32_bf16 v[70:73], a[212:215], v[90:93], v[70:73]
s_waitcnt lgkmcnt(2)
v_add_u32 v8, 147456, v8
v_add_f32 v126, v126, v130
v_mfma_f32_16x16x32_bf16 v[62:65], a[216:219], v[94:97], v[62:65]
ds_read_b128 v[90:93], v28 offset:21504
v_add_f32 v127, v127, v131
v_mfma_f32_16x16x32_bf16 v[66:69], a[220:223], v[94:97], v[66:69]
s_mov_b32 m0, 49152
s_nop 0
global_load_lds_dwordx4 v11, s[52:53] offset:0 sc1
v_add_f32 v128, v128, v132
v_mfma_f32_16x16x32_bf16 v[70:73], a[224:227], v[94:97], v[70:73]
s_waitcnt lgkmcnt(2)
v_add_f32 v129, v129, v133
v_lshlrev_b32 v130, 16, v120
v_mfma_f32_16x16x32_bf16 v[62:65], a[228:231], v[98:101], v[62:65]
ds_read_b128 v[94:97], v28 offset:22528
global_load_lds_dwordx4 v12, s[52:53] offset:1024 sc1
v_mfma_f32_16x16x32_bf16 v[66:69], a[232:235], v[98:101], v[66:69]
v_and_b32 v131, 0xffff0000, v120
v_lshlrev_b32 v132, 16, v121
v_mfma_f32_16x16x32_bf16 v[70:73], a[236:239], v[98:101], v[70:73]
s_waitcnt lgkmcnt(2)
global_load_lds_dwordx4 v13, s[52:53] offset:2048 sc1
v_and_b32 v133, 0xffff0000, v121
v_mfma_f32_16x16x32_bf16 v[62:65], a[240:243], v[86:89], v[62:65]
ds_read_b128 v[98:101], v28 offset:23552
v_fma_f32 v126, v126, v136, v130
v_mfma_f32_16x16x32_bf16 v[66:69], a[244:247], v[86:89], v[66:69]
s_add_u32 s52, s52, 221184
s_addc_u32 s53, s53, 0
s_mov_b32 m0, 61440
v_mfma_f32_16x16x32_bf16 v[70:73], a[248:251], v[86:89], v[70:73]
s_waitcnt lgkmcnt(2)
v_mfma_f32_16x16x32_bf16 v[62:65], a[252:255], v[90:93], v[62:65]
s_waitcnt vmcnt(11)
s_barrier
ds_read_b128 v[86:89], v28 offset:24576
v_mfma_f32_16x16x32_bf16 v[66:69], v[30:33], v[90:93], v[66:69]
global_load_lds_dwordx4 v14, s[38:39] offset:0 sc1
v_fma_f32 v127, v127, v136, v131
v_mfma_f32_16x16x32_bf16 v[70:73], v[34:37], v[90:93], v[70:73]
s_waitcnt lgkmcnt(2)
v_fma_f32 v128, v128, v136, v132
v_fma_f32 v129, v129, v136, v133
v_mfma_f32_16x16x32_bf16 v[62:65], v[38:41], v[94:97], v[62:65]
ds_read_b128 v[90:93], v28 offset:25600
v_add_u32 v14, 37376, v14
v_mfma_f32_16x16x32_bf16 v[66:69], v[42:45], v[94:97], v[66:69]
v_cvt_pk_bf16_f32 v134, v126, v127
v_cvt_pk_bf16_f32 v135, v128, v129
v_mfma_f32_16x16x32_bf16 v[70:73], v[46:49], v[94:97], v[70:73]
s_waitcnt lgkmcnt(2)
global_store_dwordx2 v18, v[134:135], s[48:49] sc1
v_mfma_f32_16x16x32_bf16 v[62:65], v[50:53], v[98:101], v[62:65]
ds_read_b128 v[94:97], v28 offset:26624
v_subrev_u32 v18, 36864, v18
v_mfma_f32_16x16x32_bf16 v[66:69], v[54:57], v[98:101], v[66:69]
v_mfma_f32_16x16x32_bf16 v[70:73], v[58:61], v[98:101], v[70:73]
s_waitcnt lgkmcnt(2)
v_mfma_f32_16x16x32_bf16 v[74:77], a[0:3], v[86:89], 0
ds_read_b128 v[98:101], v28 offset:27648
v_mfma_f32_16x16x32_bf16 v[78:81], a[4:7], v[86:89], 0
v_mfma_f32_16x16x32_bf16 v[82:85], a[8:11], v[86:89], 0
s_waitcnt lgkmcnt(2)
v_mfma_f32_16x16x32_bf16 v[74:77], a[12:15], v[90:93], v[74:77]
ds_read_b128 v[86:89], v28 offset:28672
v_cvt_pk_bf16_f32 v102, v62, v63
v_mfma_f32_16x16x32_bf16 v[78:81], a[16:19], v[90:93], v[78:81]
v_mfma_f32_16x16x32_bf16 v[82:85], a[20:23], v[90:93], v[82:85]
s_waitcnt vmcnt(11)
s_barrier
s_waitcnt lgkmcnt(2)
v_mfma_f32_16x16x32_bf16 v[74:77], a[24:27], v[94:97], v[74:77]
ds_read_b128 v[90:93], v28 offset:29696
v_cvt_pk_bf16_f32 v103, v64, v65
v_mfma_f32_16x16x32_bf16 v[78:81], a[28:31], v[94:97], v[78:81]
v_cvt_pk_bf16_f32 v104, v66, v67
v_cvt_pk_bf16_f32 v105, v68, v69
v_mfma_f32_16x16x32_bf16 v[82:85], a[32:35], v[94:97], v[82:85]
s_waitcnt lgkmcnt(2)
v_cvt_pk_bf16_f32 v106, v70, v71
v_cvt_pk_bf16_f32 v107, v72, v73
v_mfma_f32_16x16x32_bf16 v[74:77], a[36:39], v[98:101], v[74:77]
ds_read_b128 v[94:97], v28 offset:30720
v_mfma_f32_16x16x32_bf16 v[78:81], a[40:43], v[98:101], v[78:81]
global_store_dwordx4 v9, v[102:105], s[38:39] sc1
v_mfma_f32_16x16x32_bf16 v[82:85], a[44:47], v[98:101], v[82:85]
s_waitcnt lgkmcnt(2)
v_add_u32 v9, 110592, v9
v_mfma_f32_16x16x32_bf16 v[74:77], a[48:51], v[86:89], v[74:77]
ds_read_b128 v[98:101], v28 offset:31744
v_mfma_f32_16x16x32_bf16 v[78:81], a[52:55], v[86:89], v[78:81]
global_store_dwordx2 v10, v[106:107], s[38:39] sc1
v_mfma_f32_16x16x32_bf16 v[82:85], a[56:59], v[86:89], v[82:85]
s_waitcnt lgkmcnt(2)
v_add_u32 v10, 110592, v10
v_mfma_f32_16x16x32_bf16 v[74:77], a[60:63], v[90:93], v[74:77]
ds_read_b128 v[86:89], v28 offset:32768
v_mfma_f32_16x16x32_bf16 v[78:81], a[64:67], v[90:93], v[78:81]
v_mfma_f32_16x16x32_bf16 v[82:85], a[68:71], v[90:93], v[82:85]
s_waitcnt lgkmcnt(2)
v_mfma_f32_16x16x32_bf16 v[74:77], a[72:75], v[94:97], v[74:77]
ds_read_b128 v[90:93], v28 offset:33792
v_mfma_f32_16x16x32_bf16 v[78:81], a[76:79], v[94:97], v[78:81]
v_mfma_f32_16x16x32_bf16 v[82:85], a[80:83], v[94:97], v[82:85]
s_waitcnt lgkmcnt(2)
v_mfma_f32_16x16x32_bf16 v[74:77], a[84:87], v[98:101], v[74:77]
ds_read_b128 v[94:97], v28 offset:34816
v_mfma_f32_16x16x32_bf16 v[78:81], a[88:91], v[98:101], v[78:81]
v_mfma_f32_16x16x32_bf16 v[82:85], a[92:95], v[98:101], v[82:85]
s_waitcnt lgkmcnt(2)
v_mfma_f32_16x16x32_bf16 v[74:77], a[96:99], v[86:89], v[74:77]
ds_read_b128 v[98:101], v28 offset:35840
v_mfma_f32_16x16x32_bf16 v[78:81], a[100:103], v[86:89], v[78:81]
v_mfma_f32_16x16x32_bf16 v[82:85], a[104:107], v[86:89], v[82:85]
s_waitcnt lgkmcnt(2)
v_mfma_f32_16x16x32_bf16 v[74:77], a[108:111], v[90:93], v[74:77]
ds_read_b128 v[86:89], v28 offset:36864
v_mfma_f32_16x16x32_bf16 v[78:81], a[112:115], v[90:93], v[78:81]
v_mfma_f32_16x16x32_bf16 v[82:85], a[116:119], v[90:93], v[82:85]
s_waitcnt lgkmcnt(2)
v_mfma_f32_16x16x32_bf16 v[74:77], a[120:123], v[94:97], v[74:77]
ds_read_b128 v[90:93], v28 offset:37888
v_mfma_f32_16x16x32_bf16 v[78:81], a[124:127], v[94:97], v[78:81]
v_mfma_f32_16x16x32_bf16 v[82:85], a[128:131], v[94:97], v[82:85]
s_waitcnt lgkmcnt(2)
v_mfma_f32_16x16x32_bf16 v[74:77], a[132:135], v[98:101], v[74:77]
ds_read_b128 v[94:97], v28 offset:38912
v_mfma_f32_16x16x32_bf16 v[78:81], a[136:139], v[98:101], v[78:81]
s_add_u32 m0, s51, 24576
v_mfma_f32_16x16x32_bf16 v[82:85], a[140:143], v[98:101], v[82:85]
s_waitcnt lgkmcnt(2)
v_mfma_f32_16x16x32_bf16 v[74:77], a[144:147], v[86:89], v[74:77]
s_barrier
ds_read_b128 v[98:101], v28 offset:39936
v_mfma_f32_16x16x32_bf16 v[78:81], a[148:151], v[86:89], v[78:81]
global_load_lds_dwordx4 v8, s[38:39] offset:-2048 sc1
v_mfma_f32_16x16x32_bf16 v[82:85], a[152:155], v[86:89], v[82:85]
s_waitcnt lgkmcnt(2)
v_mfma_f32_16x16x32_bf16 v[74:77], a[156:159], v[90:93], v[74:77]
ds_read_b128 v[86:89], v28 offset:40960
v_mfma_f32_16x16x32_bf16 v[78:81], a[160:163], v[90:93], v[78:81]
global_load_lds_dwordx4 v8, s[38:39] offset:-1024 sc1
v_mfma_f32_16x16x32_bf16 v[82:85], a[164:167], v[90:93], v[82:85]
s_waitcnt lgkmcnt(2)
v_mfma_f32_16x16x32_bf16 v[74:77], a[168:171], v[94:97], v[74:77]
ds_read_b128 v[90:93], v28 offset:41984
v_mfma_f32_16x16x32_bf16 v[78:81], a[172:175], v[94:97], v[78:81]
global_load_lds_dwordx4 v8, s[38:39] offset:0 sc1
v_mfma_f32_16x16x32_bf16 v[82:85], a[176:179], v[94:97], v[82:85]
s_waitcnt lgkmcnt(2)
v_mfma_f32_16x16x32_bf16 v[74:77], a[180:183], v[98:101], v[74:77]
ds_read_b128 v[94:97], v28 offset:43008
v_mfma_f32_16x16x32_bf16 v[78:81], a[184:187], v[98:101], v[78:81]
global_load_lds_dwordx4 v8, s[38:39] offset:1024 sc1
v_mfma_f32_16x16x32_bf16 v[82:85], a[188:191], v[98:101], v[82:85]
s_waitcnt lgkmcnt(2)
v_mfma_f32_16x16x32_bf16 v[74:77], a[192:195], v[86:89], v[74:77]
ds_read_b128 v[98:101], v28 offset:44032
v_mfma_f32_16x16x32_bf16 v[78:81], a[196:199], v[86:89], v[78:81]
global_load_lds_dwordx4 v8, s[38:39] offset:2048 sc1
v_mfma_f32_16x16x32_bf16 v[82:85], a[200:203], v[86:89], v[82:85]
s_waitcnt lgkmcnt(2)
v_mfma_f32_16x16x32_bf16 v[74:77], a[204:207], v[90:93], v[74:77]
ds_read_b128 v[86:89], v28 offset:45056
v_mfma_f32_16x16x32_bf16 v[78:81], a[208:211], v[90:93], v[78:81]
global_load_lds_dwordx4 v8, s[38:39] offset:3072 sc1
v_mfma_f32_16x16x32_bf16 v[82:85], a[212:215], v[90:93], v[82:85]
s_waitcnt lgkmcnt(2)
v_add_u32 v8, 147456, v8
v_mfma_f32_16x16x32_bf16 v[74:77], a[216:219], v[94:97], v[74:77]
ds_read_b128 v[90:93], v28 offset:46080
v_mfma_f32_16x16x32_bf16 v[78:81], a[220:223], v[94:97], v[78:81]
v_mfma_f32_16x16x32_bf16 v[82:85], a[224:227], v[94:97], v[82:85]
s_waitcnt lgkmcnt(2)
v_mfma_f32_16x16x32_bf16 v[74:77], a[228:231], v[98:101], v[74:77]
ds_read_b128 v[94:97], v28 offset:47104
v_mfma_f32_16x16x32_bf16 v[78:81], a[232:235], v[98:101], v[78:81]
v_mfma_f32_16x16x32_bf16 v[82:85], a[236:239], v[98:101], v[82:85]
s_waitcnt lgkmcnt(2)
v_mfma_f32_16x16x32_bf16 v[74:77], a[240:243], v[86:89], v[74:77]
ds_read_b128 v[98:101], v28 offset:48128
v_mfma_f32_16x16x32_bf16 v[78:81], a[244:247], v[86:89], v[78:81]
v_xor_b32 v28, 0x10000, v28
v_mfma_f32_16x16x32_bf16 v[82:85], a[248:251], v[86:89], v[82:85]
s_waitcnt lgkmcnt(2)
v_mfma_f32_16x16x32_bf16 v[74:77], a[252:255], v[90:93], v[74:77]
s_waitcnt vmcnt(8)
s_barrier
ds_read_b128 v[86:89], v28 offset:0
v_mfma_f32_16x16x32_bf16 v[78:81], v[30:33], v[90:93], v[78:81]
ds_read_b64 v[122:123], v17 offset:0
ds_read_b64 v[124:125], v17 offset:128
v_mfma_f32_16x16x32_bf16 v[82:85], v[34:37], v[90:93], v[82:85]
s_waitcnt lgkmcnt(4)
ds_read_b64 v[108:109], v15 offset:0
ds_read_b64 v[110:111], v15 offset:2048
v_mfma_f32_16x16x32_bf16 v[74:77], v[38:41], v[94:97], v[74:77]
ds_read_b128 v[90:93], v28 offset:1024
ds_read_b64 v[112:113], v15 offset:4096
v_mfma_f32_16x16x32_bf16 v[78:81], v[42:45], v[94:97], v[78:81]
ds_read_b64 v[114:115], v15 offset:6144
ds_read_b64 v[116:117], v15 offset:8192
v_mfma_f32_16x16x32_bf16 v[82:85], v[46:49], v[94:97], v[82:85]
s_waitcnt lgkmcnt(9)
ds_read_b64 v[118:119], v15 offset:10240
ds_read_b64 v[120:121], v16
v_mfma_f32_16x16x32_bf16 v[74:77], v[50:53], v[98:101], v[74:77]
ds_read_b128 v[94:97], v28 offset:2048
v_mfma_f32_16x16x32_bf16 v[78:81], v[54:57], v[98:101], v[78:81]
v_mfma_f32_16x16x32_bf16 v[82:85], v[58:61], v[98:101], v[82:85]
s_sub_u32 s70, s70, 1
s_cbranch_scc0 .Ldn_top8
s_waitcnt lgkmcnt(0)
ds_read_b64 v[122:123], v17 offset:0
ds_read_b64 v[124:125], v17 offset:128
ds_read_b64 v[108:109], v15 offset:0
ds_read_b64 v[110:111], v15 offset:2048
ds_read_b64 v[112:113], v15 offset:4096
ds_read_b64 v[114:115], v15 offset:6144
ds_read_b64 v[116:117], v15 offset:8192
ds_read_b64 v[118:119], v15 offset:10240
ds_read_b64 v[120:121], v16
v_cvt_pk_bf16_f32 v102, v74, v75
v_cvt_pk_bf16_f32 v103, v76, v77
v_cvt_pk_bf16_f32 v104, v78, v79
v_cvt_pk_bf16_f32 v105, v80, v81
v_cvt_pk_bf16_f32 v106, v82, v83
v_cvt_pk_bf16_f32 v107, v84, v85
global_store_dwordx4 v9, v[102:105], s[38:39] sc1
v_add_u32 v9, 110592, v9
global_store_dwordx2 v10, v[106:107], s[38:39] sc1
v_add_u32 v10, 110592, v10
s_waitcnt vmcnt(2)
s_barrier
s_waitcnt lgkmcnt(6)
v_lshlrev_b32 v126, 16, v108
v_add_f32 v122, v122, v124
v_and_b32 v127, 0xffff0000, v108
v_add_f32 v123, v123, v125
v_lshlrev_b32 v128, 16, v109
v_mul_f32 v122, 0x3ae38e39, v122
v_and_b32 v129, 0xffff0000, v109
v_mul_f32 v123, 0x3ae38e39, v123
s_waitcnt lgkmcnt(5)
v_lshlrev_b32 v130, 16, v110
v_fma_f32 v137, -v122, v122, v123
v_and_b32 v131, 0xffff0000, v110
v_max_f32 v137, 0, v137
v_lshlrev_b32 v132, 16, v111
v_cvt_pk_bf16_f32 v137, v137, v137
v_and_b32 v133, 0xffff0000, v111
v_lshlrev_b32 v137, 16, v137
v_add_f32 v126, v126, v130
v_add_f32 v137, 0x3727c5ac, v137
v_add_f32 v127, v127, v131
v_cvt_pk_bf16_f32 v137, v137, v137
v_add_f32 v128, v128, v132
v_lshlrev_b32 v137, 16, v137
v_add_f32 v129, v129, v133
v_sqrt_f32 v137, v137
s_waitcnt lgkmcnt(4)
v_lshlrev_b32 v130, 16, v112
v_cvt_pk_bf16_f32 v137, v137, v137
v_and_b32 v131, 0xffff0000, v112
v_lshlrev_b32 v137, 16, v137
v_lshlrev_b32 v132, 16, v113
v_rcp_f32 v136, v137
v_and_b32 v133, 0xffff0000, v113
v_mul_f32 v136, v136, v136
v_add_f32 v126, v126, v130
v_add_f32 v127, v127, v131
v_add_f32 v128, v128, v132
v_add_f32 v129, v129, v133
s_waitcnt lgkmcnt(3)
v_lshlrev_b32 v130, 16, v114
v_and_b32 v131, 0xffff0000, v114
v_lshlrev_b32 v132, 16, v115
v_and_b32 v133, 0xffff0000, v115
v_add_f32 v126, v126, v130
v_add_f32 v127, v127, v131
v_add_f32 v128, v128, v132
v_add_f32 v129, v129, v133
s_waitcnt lgkmcnt(2)
v_lshlrev_b32 v130, 16, v116
v_and_b32 v131, 0xffff0000, v116
v_lshlrev_b32 v132, 16, v117
v_and_b32 v133, 0xffff0000, v117
v_add_f32 v126, v126, v130
v_add_f32 v127, v127, v131
v_add_f32 v128, v128, v132
v_add_f32 v129, v129, v133
s_waitcnt lgkmcnt(1)
v_lshlrev_b32 v130, 16, v118
v_and_b32 v131, 0xffff0000, v118
v_lshlrev_b32 v132, 16, v119
v_and_b32 v133, 0xffff0000, v119
v_add_f32 v126, v126, v130
v_add_f32 v127, v127, v131
v_add_f32 v128, v128, v132
v_add_f32 v129, v129, v133
s_waitcnt lgkmcnt(0)
v_lshlrev_b32 v130, 16, v120
v_and_b32 v131, 0xffff0000, v120
v_lshlrev_b32 v132, 16, v121
v_and_b32 v133, 0xffff0000, v121
v_fma_f32 v126, v126, v136, v130
v_fma_f32 v127, v127, v136, v131
v_fma_f32 v128, v128, v136, v132
v_fma_f32 v129, v129, v136, v133
v_cvt_pk_bf16_f32 v134, v126, v127
v_cvt_pk_bf16_f32 v135, v128, v129
s_waitcnt vmcnt(0)
s_barrier
s_mov_b32 m0, 49152
s_nop 0
global_load_lds_dwordx4 v11, s[52:53] offset:0 sc1
global_load_lds_dwordx4 v12, s[52:53] offset:1024 sc1
global_load_lds_dwordx4 v13, s[52:53] offset:2048 sc1
s_add_u32 s52, s52, 221184
s_addc_u32 s53, s53, 0
s_mov_b32 m0, 61440
s_nop 0
global_load_lds_dwordx4 v14, s[38:39] offset:0 sc1
v_add_u32 v14, 37376, v14
global_store_dwordx2 v18, v[134:135], s[48:49] sc1
v_subrev_u32 v18, 36864, v18
s_waitcnt vmcnt(0)
s_barrier
ds_read_b64 v[122:123], v17 offset:0
ds_read_b64 v[124:125], v17 offset:128
ds_read_b64 v[108:109], v15 offset:0
ds_read_b64 v[110:111], v15 offset:2048
ds_read_b64 v[112:113], v15 offset:4096
ds_read_b64 v[114:115], v15 offset:6144
ds_read_b64 v[116:117], v15 offset:8192
ds_read_b64 v[118:119], v15 offset:10240
ds_read_b64 v[120:121], v16
s_waitcnt lgkmcnt(0)
s_barrier
s_mov_b32 m0, 49152
s_nop 0
global_load_lds_dwordx4 v11, s[52:53] offset:0 sc1
global_load_lds_dwordx4 v12, s[52:53] offset:1024 sc1
global_load_lds_dwordx4 v13, s[52:53] offset:2048 sc1
s_add_u32 s52, s52, 221184
)"
R"(s_addc_u32 s53, s53, 0
s_mov_b32 m0, 61440
s_nop 0
global_load_lds_dwordx4 v14, s[38:39] offset:0 sc1
v_add_u32 v14, 37376, v14
v_lshlrev_b32 v126, 16, v108
v_add_f32 v122, v122, v124
v_and_b32 v127, 0xffff0000, v108
v_add_f32 v123, v123, v125
v_lshlrev_b32 v128, 16, v109
v_mul_f32 v122, 0x3ae38e39, v122
v_and_b32 v129, 0xffff0000, v109
v_mul_f32 v123, 0x3ae38e39, v123
v_lshlrev_b32 v130, 16, v110
v_fma_f32 v137, -v122, v122, v123
v_and_b32 v131, 0xffff0000, v110
v_max_f32 v137, 0, v137
v_lshlrev_b32 v132, 16, v111
v_cvt_pk_bf16_f32 v137, v137, v137
v_and_b32 v133, 0xffff0000, v111
v_lshlrev_b32 v137, 16, v137
v_add_f32 v126, v126, v130
v_add_f32 v137, 0x3727c5ac, v137
v_add_f32 v127, v127, v131
v_cvt_pk_bf16_f32 v137, v137, v137
v_add_f32 v128, v128, v132
v_lshlrev_b32 v137, 16, v137
v_add_f32 v129, v129, v133
v_sqrt_f32 v137, v137
v_lshlrev_b32 v130, 16, v112
v_cvt_pk_bf16_f32 v137, v137, v137
v_and_b32 v131, 0xffff0000, v112
v_lshlrev_b32 v137, 16, v137
v_lshlrev_b32 v132, 16, v113
v_rcp_f32 v136, v137
v_and_b32 v133, 0xffff0000, v113
v_mul_f32 v136, v136, v136
v_add_f32 v126, v126, v130
v_add_f32 v127, v127, v131
v_add_f32 v128, v128, v132
v_add_f32 v129, v129, v133
v_lshlrev_b32 v130, 16, v114
v_and_b32 v131, 0xffff0000, v114
v_lshlrev_b32 v132, 16, v115
v_and_b32 v133, 0xffff0000, v115
v_add_f32 v126, v126, v130
v_add_f32 v127, v127, v131
v_add_f32 v128, v128, v132
v_add_f32 v129, v129, v133
v_lshlrev_b32 v130, 16, v116
v_and_b32 v131, 0xffff0000, v116
v_lshlrev_b32 v132, 16, v117
v_and_b32 v133, 0xffff0000, v117
v_add_f32 v126, v126, v130
v_add_f32 v127, v127, v131
v_add_f32 v128, v128, v132
v_add_f32 v129, v129, v133
v_lshlrev_b32 v130, 16, v118
v_and_b32 v131, 0xffff0000, v118
v_lshlrev_b32 v132, 16, v119
v_and_b32 v133, 0xffff0000, v119
v_add_f32 v126, v126, v130
v_add_f32 v127, v127, v131
v_add_f32 v128, v128, v132
v_add_f32 v129, v129, v133
v_lshlrev_b32 v130, 16, v120
v_and_b32 v131, 0xffff0000, v120
v_lshlrev_b32 v132, 16, v121
v_and_b32 v133, 0xffff0000, v121
v_fma_f32 v126, v126, v136, v130
v_fma_f32 v127, v127, v136, v131
v_fma_f32 v128, v128, v136, v132
v_fma_f32 v129, v129, v136, v133
v_cvt_pk_bf16_f32 v134, v126, v127
v_cvt_pk_bf16_f32 v135, v128, v129
global_store_dwordx2 v18, v[134:135], s[48:49] sc1
v_subrev_u32 v18, 36864, v18
s_waitcnt vmcnt(0)
s_barrier
ds_read_b64 v[122:123], v17 offset:0
ds_read_b64 v[124:125], v17 offset:128
ds_read_b64 v[108:109], v15 offset:0
ds_read_b64 v[110:111], v15 offset:2048
ds_read_b64 v[112:113], v15 offset:4096
ds_read_b64 v[114:115], v15 offset:6144
ds_read_b64 v[116:117], v15 offset:8192
ds_read_b64 v[118:119], v15 offset:10240
ds_read_b64 v[120:121], v16
s_waitcnt lgkmcnt(0)
s_barrier
s_mov_b32 m0, 49152
s_nop 0
global_load_lds_dwordx4 v11, s[52:53] offset:0 sc1
global_load_lds_dwordx4 v12, s[52:53] offset:1024 sc1
global_load_lds_dwordx4 v13, s[52:53] offset:2048 sc1
s_add_u32 s52, s52, 221184
s_addc_u32 s53, s53, 0
s_mov_b32 m0, 61440
s_nop 0
global_load_lds_dwordx4 v14, s[38:39] offset:0 sc1
v_add_u32 v14, 37376, v14
v_lshlrev_b32 v126, 16, v108
v_add_f32 v122, v122, v124
v_and_b32 v127, 0xffff0000, v108
v_add_f32 v123, v123, v125
v_lshlrev_b32 v128, 16, v109
v_mul_f32 v122, 0x3ae38e39, v122
v_and_b32 v129, 0xffff0000, v109
v_mul_f32 v123, 0x3ae38e39, v123
v_lshlrev_b32 v130, 16, v110
v_fma_f32 v137, -v122, v122, v123
v_and_b32 v131, 0xffff0000, v110
v_max_f32 v137, 0, v137
v_lshlrev_b32 v132, 16, v111
v_cvt_pk_bf16_f32 v137, v137, v137
v_and_b32 v133, 0xffff0000, v111
v_lshlrev_b32 v137, 16, v137
v_add_f32 v126, v126, v130
v_add_f32 v137, 0x3727c5ac, v137
v_add_f32 v127, v127, v131
v_cvt_pk_bf16_f32 v137, v137, v137
v_add_f32 v128, v128, v132
v_lshlrev_b32 v137, 16, v137
v_add_f32 v129, v129, v133
v_sqrt_f32 v137, v137
v_lshlrev_b32 v130, 16, v112
v_cvt_pk_bf16_f32 v137, v137, v137
v_and_b32 v131, 0xffff0000, v112
v_lshlrev_b32 v137, 16, v137
v_lshlrev_b32 v132, 16, v113
v_rcp_f32 v136, v137
v_and_b32 v133, 0xffff0000, v113
v_mul_f32 v136, v136, v136
v_add_f32 v126, v126, v130
v_add_f32 v127, v127, v131
v_add_f32 v128, v128, v132
v_add_f32 v129, v129, v133
v_lshlrev_b32 v130, 16, v114
v_and_b32 v131, 0xffff0000, v114
v_lshlrev_b32 v132, 16, v115
v_and_b32 v133, 0xffff0000, v115
v_add_f32 v126, v126, v130
v_add_f32 v127, v127, v131
v_add_f32 v128, v128, v132
v_add_f32 v129, v129, v133
v_lshlrev_b32 v130, 16, v116
v_and_b32 v131, 0xffff0000, v116
v_lshlrev_b32 v132, 16, v117
v_and_b32 v133, 0xffff0000, v117
v_add_f32 v126, v126, v130
v_add_f32 v127, v127, v131
v_add_f32 v128, v128, v132
v_add_f32 v129, v129, v133
v_lshlrev_b32 v130, 16, v118
v_and_b32 v131, 0xffff0000, v118
v_lshlrev_b32 v132, 16, v119
v_and_b32 v133, 0xffff0000, v119
v_add_f32 v126, v126, v130
v_add_f32 v127, v127, v131
v_add_f32 v128, v128, v132
v_add_f32 v129, v129, v133
v_lshlrev_b32 v130, 16, v120
v_and_b32 v131, 0xffff0000, v120
v_lshlrev_b32 v132, 16, v121
v_and_b32 v133, 0xffff0000, v121
v_fma_f32 v126, v126, v136, v130
v_fma_f32 v127, v127, v136, v131
v_fma_f32 v128, v128, v136, v132
v_fma_f32 v129, v129, v136, v133
v_cvt_pk_bf16_f32 v134, v126, v127
v_cvt_pk_bf16_f32 v135, v128, v129
global_store_dwordx2 v18, v[134:135], s[48:49] sc1
v_subrev_u32 v18, 36864, v18
s_waitcnt vmcnt(0)
s_barrier
ds_read_b64 v[122:123], v17 offset:0
ds_read_b64 v[124:125], v17 offset:128
ds_read_b64 v[108:109], v15 offset:0
ds_read_b64 v[110:111], v15 offset:2048
ds_read_b64 v[112:113], v15 offset:4096
ds_read_b64 v[114:115], v15 offset:6144
ds_read_b64 v[116:117], v15 offset:8192
ds_read_b64 v[118:119], v15 offset:10240
ds_read_b64 v[120:121], v16
s_waitcnt lgkmcnt(6)
v_lshlrev_b32 v126, 16, v108
v_add_f32 v122, v122, v124
v_and_b32 v127, 0xffff0000, v108
v_add_f32 v123, v123, v125
v_lshlrev_b32 v128, 16, v109
v_mul_f32 v122, 0x3ae38e39, v122
v_and_b32 v129, 0xffff0000, v109
v_mul_f32 v123, 0x3ae38e39, v123
s_waitcnt lgkmcnt(5)
v_lshlrev_b32 v130, 16, v110
v_fma_f32 v137, -v122, v122, v123
v_and_b32 v131, 0xffff0000, v110
v_max_f32 v137, 0, v137
v_lshlrev_b32 v132, 16, v111
v_cvt_pk_bf16_f32 v137, v137, v137
v_and_b32 v133, 0xffff0000, v111
v_lshlrev_b32 v137, 16, v137
v_add_f32 v126, v126, v130
v_add_f32 v137, 0x3727c5ac, v137
v_add_f32 v127, v127, v131
v_cvt_pk_bf16_f32 v137, v137, v137
v_add_f32 v128, v128, v132
v_lshlrev_b32 v137, 16, v137
v_add_f32 v129, v129, v133
v_sqrt_f32 v137, v137
s_waitcnt lgkmcnt(4)
v_lshlrev_b32 v130, 16, v112
v_cvt_pk_bf16_f32 v137, v137, v137
v_and_b32 v131, 0xffff0000, v112
v_lshlrev_b32 v137, 16, v137
v_lshlrev_b32 v132, 16, v113
v_rcp_f32 v136, v137
v_and_b32 v133, 0xffff0000, v113
v_mul_f32 v136, v136, v136
v_add_f32 v126, v126, v130
v_add_f32 v127, v127, v131
v_add_f32 v128, v128, v132
v_add_f32 v129, v129, v133
s_waitcnt lgkmcnt(3)
v_lshlrev_b32 v130, 16, v114
v_and_b32 v131, 0xffff0000, v114
v_lshlrev_b32 v132, 16, v115
v_and_b32 v133, 0xffff0000, v115
v_add_f32 v126, v126, v130
v_add_f32 v127, v127, v131
v_add_f32 v128, v128, v132
v_add_f32 v129, v129, v133
s_waitcnt lgkmcnt(2)
v_lshlrev_b32 v130, 16, v116
v_and_b32 v131, 0xffff0000, v116
v_lshlrev_b32 v132, 16, v117
v_and_b32 v133, 0xffff0000, v117
v_add_f32 v126, v126, v130
v_add_f32 v127, v127, v131
v_add_f32 v128, v128, v132
v_add_f32 v129, v129, v133
s_waitcnt lgkmcnt(1)
v_lshlrev_b32 v130, 16, v118
v_and_b32 v131, 0xffff0000, v118
v_lshlrev_b32 v132, 16, v119
v_and_b32 v133, 0xffff0000, v119
v_add_f32 v126, v126, v130
v_add_f32 v127, v127, v131
v_add_f32 v128, v128, v132
v_add_f32 v129, v129, v133
s_waitcnt lgkmcnt(0)
v_lshlrev_b32 v130, 16, v120
v_and_b32 v131, 0xffff0000, v120
v_lshlrev_b32 v132, 16, v121
v_and_b32 v133, 0xffff0000, v121
v_fma_f32 v126, v126, v136, v130
v_fma_f32 v127, v127, v136, v131
v_fma_f32 v128, v128, v136, v132
v_fma_f32 v129, v129, v136, v133
v_cvt_pk_bf16_f32 v134, v126, v127
v_cvt_pk_bf16_f32 v135, v128, v129
global_store_dwordx2 v18, v[134:135], s[48:49] sc1
v_subrev_u32 v18, 36864, v18
s_endpgm
.Ldn_w2:
v_add_u32 v29, -1, v20
s_add_u32 m0, s51, 24576
s_mov_b32 s73, 27
s_mov_b32 s70, 32
s_waitcnt vmcnt(0) lgkmcnt(0)
s_nop 7
.p2align 6
.Ldn_top12:
s_waitcnt lgkmcnt(11)
v_mfma_f32_16x16x32_bf16 v[62:65], a[0:3], v[86:89], 0
ds_read_b128 v[98:101], v28 offset:3072
v_mfma_f32_16x16x32_bf16 v[66:69], a[4:7], v[86:89], 0
s_cmp_eq_u32 s70, s73
s_cbranch_scc1 .Ldn_hk9
.Ldn_hr10:
v_mfma_f32_16x16x32_bf16 v[70:73], a[8:11], v[86:89], 0
s_waitcnt lgkmcnt(9)
v_lshlrev_b32 v126, 16, v108
v_add_f32 v122, v122, v124
s_waitcnt lgkmcnt(7)
v_mfma_f32_16x16x32_bf16 v[62:65], a[12:15], v[90:93], v[62:65]
ds_read_b128 v[86:89], v28 offset:4096
v_cvt_pk_bf16_f32 v102, v74, v75
v_mfma_f32_16x16x32_bf16 v[66:69], a[16:19], v[90:93], v[66:69]
v_mfma_f32_16x16x32_bf16 v[70:73], a[20:23], v[90:93], v[70:73]
s_waitcnt vmcnt(6)
s_barrier
s_waitcnt lgkmcnt(2)
v_mfma_f32_16x16x32_bf16 v[62:65], a[24:27], v[94:97], v[62:65]
ds_read_b128 v[90:93], v28 offset:5120
v_cvt_pk_bf16_f32 v103, v76, v77
v_mfma_f32_16x16x32_bf16 v[66:69], a[28:31], v[94:97], v[66:69]
v_cvt_pk_bf16_f32 v104, v78, v79
v_cvt_pk_bf16_f32 v105, v80, v81
v_mfma_f32_16x16x32_bf16 v[70:73], a[32:35], v[94:97], v[70:73]
s_waitcnt lgkmcnt(2)
v_cvt_pk_bf16_f32 v106, v82, v83
v_cvt_pk_bf16_f32 v107, v84, v85
v_mfma_f32_16x16x32_bf16 v[62:65], a[36:39], v[98:101], v[62:65]
ds_read_b128 v[94:97], v28 offset:6144
v_and_b32 v127, 0xffff0000, v108
v_mfma_f32_16x16x32_bf16 v[66:69], a[40:43], v[98:101], v[66:69]
global_store_dwordx4 v9, v[102:105], s[38:39] sc1
v_mfma_f32_16x16x32_bf16 v[70:73], a[44:47], v[98:101], v[70:73]
s_waitcnt lgkmcnt(2)
v_add_u32 v9, 110592, v9
v_add_f32 v123, v123, v125
v_mfma_f32_16x16x32_bf16 v[62:65], a[48:51], v[86:89], v[62:65]
ds_read_b128 v[98:101], v28 offset:7168
v_lshlrev_b32 v128, 16, v109
v_mfma_f32_16x16x32_bf16 v[66:69], a[52:55], v[86:89], v[66:69]
global_store_dwordx2 v10, v[106:107], s[38:39] sc1
v_mfma_f32_16x16x32_bf16 v[70:73], a[56:59], v[86:89], v[70:73]
s_waitcnt lgkmcnt(2)
v_add_u32 v10, 110592, v10
v_mul_f32 v122, 0x3ae38e39, v122
v_mfma_f32_16x16x32_bf16 v[62:65], a[60:63], v[90:93], v[62:65]
ds_read_b128 v[86:89], v28 offset:8192
v_and_b32 v129, 0xffff0000, v109
v_mfma_f32_16x16x32_bf16 v[66:69], a[64:67], v[90:93], v[66:69]
v_mul_f32 v123, 0x3ae38e39, v123
v_lshlrev_b32 v130, 16, v110
v_mfma_f32_16x16x32_bf16 v[70:73], a[68:71], v[90:93], v[70:73]
s_waitcnt lgkmcnt(2)
v_fma_f32 v137, -v122, v122, v123
v_and_b32 v131, 0xffff0000, v110
v_mfma_f32_16x16x32_bf16 v[62:65], a[72:75], v[94:97], v[62:65]
ds_read_b128 v[90:93], v28 offset:9216
v_max_f32 v137, 0, v137
v_mfma_f32_16x16x32_bf16 v[66:69], a[76:79], v[94:97], v[66:69]
v_lshlrev_b32 v132, 16, v111
v_cvt_pk_bf16_f32 v137, v137, v137
v_mfma_f32_16x16x32_bf16 v[70:73], a[80:83], v[94:97], v[70:73]
s_waitcnt lgkmcnt(2)
v_and_b32 v133, 0xffff0000, v111
v_lshlrev_b32 v137, 16, v137
v_mfma_f32_16x16x32_bf16 v[62:65], a[84:87], v[98:101], v[62:65]
ds_read_b128 v[94:97], v28 offset:10240
v_add_f32 v126, v126, v130
v_mfma_f32_16x16x32_bf16 v[66:69], a[88:91], v[98:101], v[66:69]
v_add_f32 v137, 0x3727c5ac, v137
v_add_f32 v127, v127, v131
v_mfma_f32_16x16x32_bf16 v[70:73], a[92:95], v[98:101], v[70:73]
s_waitcnt lgkmcnt(2)
v_cvt_pk_bf16_f32 v137, v137, v137
v_add_f32 v128, v128, v132
v_mfma_f32_16x16x32_bf16 v[62:65], a[96:99], v[86:89], v[62:65]
ds_read_b128 v[98:101], v28 offset:11264
v_lshlrev_b32 v137, 16, v137
v_mfma_f32_16x16x32_bf16 v[66:69], a[100:103], v[86:89], v[66:69]
v_add_f32 v129, v129, v133
v_mfma_f32_16x16x32_bf16 v[70:73], a[104:107], v[86:89], v[70:73]
s_waitcnt lgkmcnt(2)
v_sqrt_f32 v137, v137
v_mfma_f32_16x16x32_bf16 v[62:65], a[108:111], v[90:93], v[62:65]
ds_read_b128 v[86:89], v28 offset:12288
v_lshlrev_b32 v130, 16, v112
v_mfma_f32_16x16x32_bf16 v[66:69], a[112:115], v[90:93], v[66:69]
v_cvt_pk_bf16_f32 v137, v137, v137
v_and_b32 v131, 0xffff0000, v112
v_mfma_f32_16x16x32_bf16 v[70:73], a[116:119], v[90:93], v[70:73]
s_waitcnt lgkmcnt(2)
v_lshlrev_b32 v137, 16, v137
v_lshlrev_b32 v132, 16, v113
v_mfma_f32_16x16x32_bf16 v[62:65], a[120:123], v[94:97], v[62:65]
ds_read_b128 v[90:93], v28 offset:13312
v_mfma_f32_16x16x32_bf16 v[66:69], a[124:127], v[94:97], v[66:69]
v_rcp_f32 v136, v137
v_mfma_f32_16x16x32_bf16 v[70:73], a[128:131], v[94:97], v[70:73]
s_waitcnt lgkmcnt(2)
v_and_b32 v133, 0xffff0000, v113
v_mul_f32 v136, v136, v136
v_mfma_f32_16x16x32_bf16 v[62:65], a[132:135], v[98:101], v[62:65]
s_xor_b32 s51, s51, 0x10000
ds_read_b128 v[94:97], v28 offset:14336
v_add_f32 v126, v126, v130
v_mfma_f32_16x16x32_bf16 v[66:69], a[136:139], v[98:101], v[66:69]
s_add_u32 m0, s51, 0
v_add_f32 v127, v127, v131
v_add_f32 v128, v128, v132
v_mfma_f32_16x16x32_bf16 v[70:73], a[140:143], v[98:101], v[70:73]
s_waitcnt lgkmcnt(2)
v_mfma_f32_16x16x32_bf16 v[62:65], a[144:147], v[86:89], v[62:65]
s_barrier
ds_read_b128 v[98:101], v28 offset:15360
v_mfma_f32_16x16x32_bf16 v[66:69], a[148:151], v[86:89], v[66:69]
v_add_f32 v129, v129, v133
v_lshlrev_b32 v130, 16, v114
v_mfma_f32_16x16x32_bf16 v[70:73], a[152:155], v[86:89], v[70:73]
s_waitcnt lgkmcnt(2)
global_load_lds_dwordx4 v8, s[38:39] offset:-2048 sc1
v_and_b32 v131, 0xffff0000, v114
v_mfma_f32_16x16x32_bf16 v[62:65], a[156:159], v[90:93], v[62:65]
ds_read_b128 v[86:89], v28 offset:16384
v_lshlrev_b32 v132, 16, v115
)"
R"(v_mfma_f32_16x16x32_bf16 v[66:69], a[160:163], v[90:93], v[66:69]
v_and_b32 v133, 0xffff0000, v115
v_add_f32 v126, v126, v130
v_mfma_f32_16x16x32_bf16 v[70:73], a[164:167], v[90:93], v[70:73]
s_waitcnt lgkmcnt(2)
global_load_lds_dwordx4 v8, s[38:39] offset:-1024 sc1
v_add_f32 v127, v127, v131
v_mfma_f32_16x16x32_bf16 v[62:65], a[168:171], v[94:97], v[62:65]
ds_read_b128 v[90:93], v28 offset:17408
v_add_f32 v128, v128, v132
v_mfma_f32_16x16x32_bf16 v[66:69], a[172:175], v[94:97], v[66:69]
v_add_f32 v129, v129, v133
v_lshlrev_b32 v130, 16, v116
v_mfma_f32_16x16x32_bf16 v[70:73], a[176:179], v[94:97], v[70:73]
s_waitcnt lgkmcnt(2)
global_load_lds_dwordx4 v8, s[38:39] offset:0 sc1
v_and_b32 v131, 0xffff0000, v116
v_mfma_f32_16x16x32_bf16 v[62:65], a[180:183], v[98:101], v[62:65]
ds_read_b128 v[94:97], v28 offset:18432
v_lshlrev_b32 v132, 16, v117
v_mfma_f32_16x16x32_bf16 v[66:69], a[184:187], v[98:101], v[66:69]
v_and_b32 v133, 0xffff0000, v117
v_add_f32 v126, v126, v130
v_mfma_f32_16x16x32_bf16 v[70:73], a[188:191], v[98:101], v[70:73]
s_waitcnt lgkmcnt(2)
global_load_lds_dwordx4 v8, s[38:39] offset:1024 sc1
v_add_f32 v127, v127, v131
v_mfma_f32_16x16x32_bf16 v[62:65], a[192:195], v[86:89], v[62:65]
ds_read_b128 v[98:101], v28 offset:19456
v_add_f32 v128, v128, v132
v_mfma_f32_16x16x32_bf16 v[66:69], a[196:199], v[86:89], v[66:69]
v_add_f32 v129, v129, v133
v_lshlrev_b32 v130, 16, v118
v_mfma_f32_16x16x32_bf16 v[70:73], a[200:203], v[86:89], v[70:73]
s_waitcnt lgkmcnt(2)
global_load_lds_dwordx4 v8, s[38:39] offset:2048 sc1
v_and_b32 v131, 0xffff0000, v118
v_mfma_f32_16x16x32_bf16 v[62:65], a[204:207], v[90:93], v[62:65]
ds_read_b128 v[86:89], v28 offset:20480
v_lshlrev_b32 v132, 16, v119
v_mfma_f32_16x16x32_bf16 v[66:69], a[208:211], v[90:93], v[66:69]
v_and_b32 v133, 0xffff0000, v119
v_add_f32 v126, v126, v130
v_mfma_f32_16x16x32_bf16 v[70:73], a[212:215], v[90:93], v[70:73]
s_waitcnt lgkmcnt(2)
global_load_lds_dwordx4 v8, s[38:39] offset:3072 sc1
v_add_f32 v127, v127, v131
v_mfma_f32_16x16x32_bf16 v[62:65], a[216:219], v[94:97], v[62:65]
ds_read_b128 v[90:93], v28 offset:21504
v_add_u32 v8, 147456, v8
v_mfma_f32_16x16x32_bf16 v[66:69], a[220:223], v[94:97], v[66:69]
v_add_f32 v128, v128, v132
v_add_f32 v129, v129, v133
v_mfma_f32_16x16x32_bf16 v[70:73], a[224:227], v[94:97], v[70:73]
s_mov_b32 m0, 52224
s_nop 0
global_load_lds_dwordx4 v11, s[52:53] offset:0 sc1
s_waitcnt lgkmcnt(2)
v_mfma_f32_16x16x32_bf16 v[62:65], a[228:231], v[98:101], v[62:65]
ds_read_b128 v[94:97], v28 offset:22528
v_lshlrev_b32 v130, 16, v120
v_mfma_f32_16x16x32_bf16 v[66:69], a[232:235], v[98:101], v[66:69]
global_load_lds_dwordx4 v12, s[52:53] offset:1024 sc1
v_and_b32 v131, 0xffff0000, v120
v_mfma_f32_16x16x32_bf16 v[70:73], a[236:239], v[98:101], v[70:73]
s_waitcnt lgkmcnt(2)
v_lshlrev_b32 v132, 16, v121
v_and_b32 v133, 0xffff0000, v121
v_mfma_f32_16x16x32_bf16 v[62:65], a[240:243], v[86:89], v[62:65]
ds_read_b128 v[98:101], v28 offset:23552
global_load_lds_dwordx4 v13, s[52:53] offset:2048 sc1
v_mfma_f32_16x16x32_bf16 v[66:69], a[244:247], v[86:89], v[66:69]
v_fma_f32 v126, v126, v136, v130
v_fma_f32 v127, v127, v136, v131
v_mfma_f32_16x16x32_bf16 v[70:73], a[248:251], v[86:89], v[70:73]
s_add_u32 s52, s52, 221184
s_addc_u32 s53, s53, 0
s_mov_b32 m0, 62464
s_waitcnt lgkmcnt(2)
v_mfma_f32_16x16x32_bf16 v[62:65], a[252:255], v[90:93], v[62:65]
s_waitcnt vmcnt(11)
s_barrier
ds_read_b128 v[86:89], v28 offset:24576
v_mfma_f32_16x16x32_bf16 v[66:69], v[30:33], v[90:93], v[66:69]
global_load_lds_dwordx4 v14, s[38:39] offset:0 sc1
v_fma_f32 v128, v128, v136, v132
v_mfma_f32_16x16x32_bf16 v[70:73], v[34:37], v[90:93], v[70:73]
s_waitcnt lgkmcnt(2)
v_fma_f32 v129, v129, v136, v133
v_cvt_pk_bf16_f32 v134, v126, v127
v_mfma_f32_16x16x32_bf16 v[62:65], v[38:41], v[94:97], v[62:65]
ds_read_b128 v[90:93], v28 offset:25600
v_add_u32 v14, 37376, v14
v_mfma_f32_16x16x32_bf16 v[66:69], v[42:45], v[94:97], v[66:69]
v_cvt_pk_bf16_f32 v135, v128, v129
v_mfma_f32_16x16x32_bf16 v[70:73], v[46:49], v[94:97], v[70:73]
s_waitcnt lgkmcnt(2)
global_store_dwordx2 v18, v[134:135], s[48:49] sc1
v_mfma_f32_16x16x32_bf16 v[62:65], v[50:53], v[98:101], v[62:65]
ds_read_b128 v[94:97], v28 offset:26624
v_subrev_u32 v18, 36864, v18
v_mfma_f32_16x16x32_bf16 v[66:69], v[54:57], v[98:101], v[66:69]
v_mfma_f32_16x16x32_bf16 v[70:73], v[58:61], v[98:101], v[70:73]
s_waitcnt lgkmcnt(2)
v_mfma_f32_16x16x32_bf16 v[74:77], a[0:3], v[86:89], 0
ds_read_b128 v[98:101], v28 offset:27648
v_mfma_f32_16x16x32_bf16 v[78:81], a[4:7], v[86:89], 0
v_mfma_f32_16x16x32_bf16 v[82:85], a[8:11], v[86:89], 0
s_waitcnt lgkmcnt(2)
v_mfma_f32_16x16x32_bf16 v[74:77], a[12:15], v[90:93], v[74:77]
ds_read_b128 v[86:89], v28 offset:28672
v_cvt_pk_bf16_f32 v102, v62, v63
v_mfma_f32_16x16x32_bf16 v[78:81], a[16:19], v[90:93], v[78:81]
v_mfma_f32_16x16x32_bf16 v[82:85], a[20:23], v[90:93], v[82:85]
s_waitcnt vmcnt(11)
s_barrier
s_waitcnt lgkmcnt(2)
v_mfma_f32_16x16x32_bf16 v[74:77], a[24:27], v[94:97], v[74:77]
ds_read_b128 v[90:93], v28 offset:29696
v_cvt_pk_bf16_f32 v103, v64, v65
v_mfma_f32_16x16x32_bf16 v[78:81], a[28:31], v[94:97], v[78:81]
v_cvt_pk_bf16_f32 v104, v66, v67
v_cvt_pk_bf16_f32 v105, v68, v69
v_mfma_f32_16x16x32_bf16 v[82:85], a[32:35], v[94:97], v[82:85]
s_waitcnt lgkmcnt(2)
v_cvt_pk_bf16_f32 v106, v70, v71
v_cvt_pk_bf16_f32 v107, v72, v73
v_mfma_f32_16x16x32_bf16 v[74:77], a[36:39], v[98:101], v[74:77]
ds_read_b128 v[94:97], v28 offset:30720
v_mfma_f32_16x16x32_bf16 v[78:81], a[40:43], v[98:101], v[78:81]
global_store_dwordx4 v9, v[102:105], s[38:39] sc1
v_mfma_f32_16x16x32_bf16 v[82:85], a[44:47], v[98:101], v[82:85]
s_waitcnt lgkmcnt(2)
v_add_u32 v9, 110592, v9
v_mfma_f32_16x16x32_bf16 v[74:77], a[48:51], v[86:89], v[74:77]
ds_read_b128 v[98:101], v28 offset:31744
v_mfma_f32_16x16x32_bf16 v[78:81], a[52:55], v[86:89], v[78:81]
global_store_dwordx2 v10, v[106:107], s[38:39] sc1
v_mfma_f32_16x16x32_bf16 v[82:85], a[56:59], v[86:89], v[82:85]
s_waitcnt lgkmcnt(2)
v_add_u32 v10, 110592, v10
v_mfma_f32_16x16x32_bf16 v[74:77], a[60:63], v[90:93], v[74:77]
ds_read_b128 v[86:89], v28 offset:32768
v_mfma_f32_16x16x32_bf16 v[78:81], a[64:67], v[90:93], v[78:81]
v_mfma_f32_16x16x32_bf16 v[82:85], a[68:71], v[90:93], v[82:85]
s_waitcnt lgkmcnt(2)
v_mfma_f32_16x16x32_bf16 v[74:77], a[72:75], v[94:97], v[74:77]
ds_read_b128 v[90:93], v28 offset:33792
v_mfma_f32_16x16x32_bf16 v[78:81], a[76:79], v[94:97], v[78:81]
v_mfma_f32_16x16x32_bf16 v[82:85], a[80:83], v[94:97], v[82:85]
s_waitcnt lgkmcnt(2)
v_mfma_f32_16x16x32_bf16 v[74:77], a[84:87], v[98:101], v[74:77]
ds_read_b128 v[94:97], v28 offset:34816
v_mfma_f32_16x16x32_bf16 v[78:81], a[88:91], v[98:101], v[78:81]
v_mfma_f32_16x16x32_bf16 v[82:85], a[92:95], v[98:101], v[82:85]
s_waitcnt lgkmcnt(2)
v_mfma_f32_16x16x32_bf16 v[74:77], a[96:99], v[86:89], v[74:77]
ds_read_b128 v[98:101], v28 offset:35840
v_mfma_f32_16x16x32_bf16 v[78:81], a[100:103], v[86:89], v[78:81]
v_mfma_f32_16x16x32_bf16 v[82:85], a[104:107], v[86:89], v[82:85]
s_waitcnt lgkmcnt(2)
v_mfma_f32_16x16x32_bf16 v[74:77], a[108:111], v[90:93], v[74:77]
ds_read_b128 v[86:89], v28 offset:36864
v_mfma_f32_16x16x32_bf16 v[78:81], a[112:115], v[90:93], v[78:81]
v_mfma_f32_16x16x32_bf16 v[82:85], a[116:119], v[90:93], v[82:85]
s_waitcnt lgkmcnt(2)
v_mfma_f32_16x16x32_bf16 v[74:77], a[120:123], v[94:97], v[74:77]
ds_read_b128 v[90:93], v28 offset:37888
v_mfma_f32_16x16x32_bf16 v[78:81], a[124:127], v[94:97], v[78:81]
v_mfma_f32_16x16x32_bf16 v[82:85], a[128:131], v[94:97], v[82:85]
s_waitcnt lgkmcnt(2)
v_mfma_f32_16x16x32_bf16 v[74:77], a[132:135], v[98:101], v[74:77]
ds_read_b128 v[94:97], v28 offset:38912
v_mfma_f32_16x16x32_bf16 v[78:81], a[136:139], v[98:101], v[78:81]
s_add_u32 m0, s51, 24576
v_mfma_f32_16x16x32_bf16 v[82:85], a[140:143], v[98:101], v[82:85]
s_waitcnt lgkmcnt(2)
v_mfma_f32_16x16x32_bf16 v[74:77], a[144:147], v[86:89], v[74:77]
s_barrier
ds_read_b128 v[98:101], v28 offset:39936
v_mfma_f32_16x16x32_bf16 v[78:81], a[148:151], v[86:89], v[78:81]
v_mfma_f32_16x16x32_bf16 v[82:85], a[152:155], v[86:89], v[82:85]
s_waitcnt lgkmcnt(2)
global_load_lds_dwordx4 v8, s[38:39] offset:-2048 sc1
v_mfma_f32_16x16x32_bf16 v[74:77], a[156:159], v[90:93], v[74:77]
ds_read_b128 v[86:89], v28 offset:40960
v_mfma_f32_16x16x32_bf16 v[78:81], a[160:163], v[90:93], v[78:81]
v_mfma_f32_16x16x32_bf16 v[82:85], a[164:167], v[90:93], v[82:85]
s_waitcnt lgkmcnt(2)
global_load_lds_dwordx4 v8, s[38:39] offset:-1024 sc1
v_mfma_f32_16x16x32_bf16 v[74:77], a[168:171], v[94:97], v[74:77]
ds_read_b128 v[90:93], v28 offset:41984
v_mfma_f32_16x16x32_bf16 v[78:81], a[172:175], v[94:97], v[78:81]
v_mfma_f32_16x16x32_bf16 v[82:85], a[176:179], v[94:97], v[82:85]
s_waitcnt lgkmcnt(2)
global_load_lds_dwordx4 v8, s[38:39] offset:0 sc1
v_mfma_f32_16x16x32_bf16 v[74:77], a[180:183], v[98:101], v[74:77]
ds_read_b128 v[94:97], v28 offset:43008
v_mfma_f32_16x16x32_bf16 v[78:81], a[184:187], v[98:101], v[78:81]
v_mfma_f32_16x16x32_bf16 v[82:85], a[188:191], v[98:101], v[82:85]
s_waitcnt lgkmcnt(2)
global_load_lds_dwordx4 v8, s[38:39] offset:1024 sc1
v_mfma_f32_16x16x32_bf16 v[74:77], a[192:195], v[86:89], v[74:77]
ds_read_b128 v[98:101], v28 offset:44032
v_mfma_f32_16x16x32_bf16 v[78:81], a[196:199], v[86:89], v[78:81]
v_mfma_f32_16x16x32_bf16 v[82:85], a[200:203], v[86:89], v[82:85]
s_waitcnt lgkmcnt(2)
global_load_lds_dwordx4 v8, s[38:39] offset:2048 sc1
v_mfma_f32_16x16x32_bf16 v[74:77], a[204:207], v[90:93], v[74:77]
ds_read_b128 v[86:89], v28 offset:45056
v_mfma_f32_16x16x32_bf16 v[78:81], a[208:211], v[90:93], v[78:81]
v_mfma_f32_16x16x32_bf16 v[82:85], a[212:215], v[90:93], v[82:85]
s_waitcnt lgkmcnt(2)
global_load_lds_dwordx4 v8, s[38:39] offset:3072 sc1
v_mfma_f32_16x16x32_bf16 v[74:77], a[216:219], v[94:97], v[74:77]
ds_read_b128 v[90:93], v28 offset:46080
v_add_u32 v8, 147456, v8
v_mfma_f32_16x16x32_bf16 v[78:81], a[220:223], v[94:97], v[78:81]
v_mfma_f32_16x16x32_bf16 v[82:85], a[224:227], v[94:97], v[82:85]
s_waitcnt lgkmcnt(2)
v_mfma_f32_16x16x32_bf16 v[74:77], a[228:231], v[98:101], v[74:77]
ds_read_b128 v[94:97], v28 offset:47104
v_mfma_f32_16x16x32_bf16 v[78:81], a[232:235], v[98:101], v[78:81]
v_mfma_f32_16x16x32_bf16 v[82:85], a[236:239], v[98:101], v[82:85]
s_waitcnt lgkmcnt(2)
v_mfma_f32_16x16x32_bf16 v[74:77], a[240:243], v[86:89], v[74:77]
ds_read_b128 v[98:101], v28 offset:48128
v_mfma_f32_16x16x32_bf16 v[78:81], a[244:247], v[86:89], v[78:81]
v_xor_b32 v28, 0x10000, v28
v_mfma_f32_16x16x32_bf16 v[82:85], a[248:251], v[86:89], v[82:85]
s_waitcnt lgkmcnt(2)
v_mfma_f32_16x16x32_bf16 v[74:77], a[252:255], v[90:93], v[74:77]
s_waitcnt vmcnt(8)
s_barrier
ds_read_b128 v[86:89], v28 offset:0
v_mfma_f32_16x16x32_bf16 v[78:81], v[30:33], v[90:93], v[78:81]
ds_read_b64 v[122:123], v17 offset:0
ds_read_b64 v[124:125], v17 offset:128
v_mfma_f32_16x16x32_bf16 v[82:85], v[34:37], v[90:93], v[82:85]
s_waitcnt lgkmcnt(4)
ds_read_b64 v[108:109], v15 offset:0
ds_read_b64 v[110:111], v15 offset:2048
v_mfma_f32_16x16x32_bf16 v[74:77], v[38:41], v[94:97], v[74:77]
ds_read_b128 v[90:93], v28 offset:1024
ds_read_b64 v[112:113], v15 offset:4096
v_mfma_f32_16x16x32_bf16 v[78:81], v[42:45], v[94:97], v[78:81]
ds_read_b64 v[114:115], v15 offset:6144
ds_read_b64 v[116:117], v15 offset:8192
v_mfma_f32_16x16x32_bf16 v[82:85], v[46:49], v[94:97], v[82:85]
s_waitcnt lgkmcnt(9)
ds_read_b64 v[118:119], v15 offset:10240
ds_read_b64 v[120:121], v16
v_mfma_f32_16x16x32_bf16 v[74:77], v[50:53], v[98:101], v[74:77]
ds_read_b128 v[94:97], v28 offset:2048
v_mfma_f32_16x16x32_bf16 v[78:81], v[54:57], v[98:101], v[78:81]
v_mfma_f32_16x16x32_bf16 v[82:85], v[58:61], v[98:101], v[82:85]
s_sub_u32 s70, s70, 1
s_cbranch_scc0 .Ldn_top12
s_waitcnt lgkmcnt(0)
ds_read_b64 v[122:123], v17 offset:0
ds_read_b64 v[124:125], v17 offset:128
ds_read_b64 v[108:109], v15 offset:0
ds_read_b64 v[110:111], v15 offset:2048
ds_read_b64 v[112:113], v15 offset:4096
ds_read_b64 v[114:115], v15 offset:6144
ds_read_b64 v[116:117], v15 offset:8192
ds_read_b64 v[118:119], v15 offset:10240
ds_read_b64 v[120:121], v16
v_cvt_pk_bf16_f32 v102, v74, v75
v_cvt_pk_bf16_f32 v103, v76, v77
v_cvt_pk_bf16_f32 v104, v78, v79
v_cvt_pk_bf16_f32 v105, v80, v81
v_cvt_pk_bf16_f32 v106, v82, v83
v_cvt_pk_bf16_f32 v107, v84, v85
global_store_dwordx4 v9, v[102:105], s[38:39] sc1
v_add_u32 v9, 110592, v9
global_store_dwordx2 v10, v[106:107], s[38:39] sc1
v_add_u32 v10, 110592, v10
s_waitcnt vmcnt(2)
s_barrier
s_waitcnt lgkmcnt(6)
v_lshlrev_b32 v126, 16, v108
v_add_f32 v122, v122, v124
v_and_b32 v127, 0xffff0000, v108
v_add_f32 v123, v123, v125
v_lshlrev_b32 v128, 16, v109
v_mul_f32 v122, 0x3ae38e39, v122
v_and_b32 v129, 0xffff0000, v109
v_mul_f32 v123, 0x3ae38e39, v123
s_waitcnt lgkmcnt(5)
v_lshlrev_b32 v130, 16, v110
v_fma_f32 v137, -v122, v122, v123
v_and_b32 v131, 0xffff0000, v110
v_max_f32 v137, 0, v137
v_lshlrev_b32 v132, 16, v111
v_cvt_pk_bf16_f32 v137, v137, v137
v_and_b32 v133, 0xffff0000, v111
v_lshlrev_b32 v137, 16, v137
v_add_f32 v126, v126, v130
v_add_f32 v137, 0x3727c5ac, v137
v_add_f32 v127, v127, v131
v_cvt_pk_bf16_f32 v137, v137, v137
v_add_f32 v128, v128, v132
v_lshlrev_b32 v137, 16, v137
v_add_f32 v129, v129, v133
v_sqrt_f32 v137, v137
s_waitcnt lgkmcnt(4)
v_lshlrev_b32 v130, 16, v112
v_cvt_pk_bf16_f32 v137, v137, v137
v_and_b32 v131, 0xffff0000, v112
v_lshlrev_b32 v137, 16, v137
v_lshlrev_b32 v132, 16, v113
v_rcp_f32 v136, v137
v_and_b32 v133, 0xffff0000, v113
v_mul_f32 v136, v136, v136
v_add_f32 v126, v126, v130
v_add_f32 v127, v127, v131
v_add_f32 v128, v128, v132
v_add_f32 v129, v129, v133
s_waitcnt lgkmcnt(3)
v_lshlrev_b32 v130, 16, v114
v_and_b32 v131, 0xffff0000, v114
v_lshlrev_b32 v132, 16, v115
v_and_b32 v133, 0xffff0000, v115
v_add_f32 v126, v126, v130
v_add_f32 v127, v127, v131
v_add_f32 v128, v128, v132
v_add_f32 v129, v129, v133
s_waitcnt lgkmcnt(2)
v_lshlrev_b32 v130, 16, v116
v_and_b32 v131, 0xffff0000, v116
v_lshlrev_b32 v132, 16, v117
v_and_b32 v133, 0xffff0000, v117
v_add_f32 v126, v126, v130
v_add_f32 v127, v127, v131
v_add_f32 v128, v128, v132
v_add_f32 v129, v129, v133
s_waitcnt lgkmcnt(1)
v_lshlrev_b32 v130, 16, v118
v_and_b32 v131, 0xffff0000, v118
v_lshlrev_b32 v132, 16, v119
v_and_b32 v133, 0xffff0000, v119
v_add_f32 v126, v126, v130
v_add_f32 v127, v127, v131
v_add_f32 v128, v128, v132
v_add_f32 v129, v129, v133
s_waitcnt lgkmcnt(0)
v_lshlrev_b32 v130, 16, v120
v_and_b32 v131, 0xffff0000, v120
v_lshlrev_b32 v132, 16, v121
v_and_b32 v133, 0xffff0000, v121
v_fma_f32 v126, v126, v136, v130
v_fma_f32 v127, v127, v136, v131
v_fma_f32 v128, v128, v136, v132
v_fma_f32 v129, v129, v136, v133
v_cvt_pk_bf16_f32 v134, v126, v127
v_cvt_pk_bf16_f32 v135, v128, v129
s_waitcnt vmcnt(0)
s_barrier
s_mov_b32 m0, 52224
s_nop 0
global_load_lds_dwordx4 v11, s[52:53] offset:0 sc1
global_load_lds_dwordx4 v12, s[52:53] offset:1024 sc1
global_load_lds_dwordx4 v13, s[52:53] offset:2048 sc1
s_add_u32 s52, s52, 221184
s_addc_u32 s53, s53, 0
s_mov_b32 m0, 62464
s_nop 0
global_load_lds_dwordx4 v14, s[38:39] offset:0 sc1
v_add_u32 v14, 37376, v14
global_store_dwordx2 v18, v[134:135], s[48:49] sc1
v_subrev_u32 v18, 36864, v18
s_waitcnt vmcnt(0)
s_barrier
ds_read_b64 v[122:123], v17 offset:0
ds_read_b64 v[124:125], v17 offset:128
ds_read_b64 v[108:109], v15 offset:0
ds_read_b64 v[110:111], v15 offset:2048
ds_read_b64 v[112:113], v15 offset:4096
ds_read_b64 v[114:115], v15 offset:6144
ds_read_b64 v[116:117], v15 offset:8192
ds_read_b64 v[118:119], v15 offset:10240
ds_read_b64 v[120:121], v16
s_waitcnt lgkmcnt(0)
s_barrier
s_mov_b32 m0, 52224
s_nop 0
global_load_lds_dwordx4 v11, s[52:53] offset:0 sc1
global_load_lds_dwordx4 v12, s[52:53] offset:1024 sc1
global_load_lds_dwordx4 v13, s[52:53] offset:2048 sc1
s_add_u32 s52, s52, 221184
s_addc_u32 s53, s53, 0
s_mov_b32 m0, 62464
s_nop 0
global_load_lds_dwordx4 v14, s[38:39] offset:0 sc1
v_add_u32 v14, 37376, v14
v_lshlrev_b32 v126, 16, v108
v_add_f32 v122, v122, v124
v_and_b32 v127, 0xffff0000, v108
v_add_f32 v123, v123, v125
)"
R"(v_lshlrev_b32 v128, 16, v109
v_mul_f32 v122, 0x3ae38e39, v122
v_and_b32 v129, 0xffff0000, v109
v_mul_f32 v123, 0x3ae38e39, v123
v_lshlrev_b32 v130, 16, v110
v_fma_f32 v137, -v122, v122, v123
v_and_b32 v131, 0xffff0000, v110
v_max_f32 v137, 0, v137
v_lshlrev_b32 v132, 16, v111
v_cvt_pk_bf16_f32 v137, v137, v137
v_and_b32 v133, 0xffff0000, v111
v_lshlrev_b32 v137, 16, v137
v_add_f32 v126, v126, v130
v_add_f32 v137, 0x3727c5ac, v137
v_add_f32 v127, v127, v131
v_cvt_pk_bf16_f32 v137, v137, v137
v_add_f32 v128, v128, v132
v_lshlrev_b32 v137, 16, v137
v_add_f32 v129, v129, v133
v_sqrt_f32 v137, v137
v_lshlrev_b32 v130, 16, v112
v_cvt_pk_bf16_f32 v137, v137, v137
v_and_b32 v131, 0xffff0000, v112
v_lshlrev_b32 v137, 16, v137
v_lshlrev_b32 v132, 16, v113
v_rcp_f32 v136, v137
v_and_b32 v133, 0xffff0000, v113
v_mul_f32 v136, v136, v136
v_add_f32 v126, v126, v130
v_add_f32 v127, v127, v131
v_add_f32 v128, v128, v132
v_add_f32 v129, v129, v133
v_lshlrev_b32 v130, 16, v114
v_and_b32 v131, 0xffff0000, v114
v_lshlrev_b32 v132, 16, v115
v_and_b32 v133, 0xffff0000, v115
v_add_f32 v126, v126, v130
v_add_f32 v127, v127, v131
v_add_f32 v128, v128, v132
v_add_f32 v129, v129, v133
v_lshlrev_b32 v130, 16, v116
v_and_b32 v131, 0xffff0000, v116
v_lshlrev_b32 v132, 16, v117
v_and_b32 v133, 0xffff0000, v117
v_add_f32 v126, v126, v130
v_add_f32 v127, v127, v131
v_add_f32 v128, v128, v132
v_add_f32 v129, v129, v133
v_lshlrev_b32 v130, 16, v118
v_and_b32 v131, 0xffff0000, v118
v_lshlrev_b32 v132, 16, v119
v_and_b32 v133, 0xffff0000, v119
v_add_f32 v126, v126, v130
v_add_f32 v127, v127, v131
v_add_f32 v128, v128, v132
v_add_f32 v129, v129, v133
v_lshlrev_b32 v130, 16, v120
v_and_b32 v131, 0xffff0000, v120
v_lshlrev_b32 v132, 16, v121
v_and_b32 v133, 0xffff0000, v121
v_fma_f32 v126, v126, v136, v130
v_fma_f32 v127, v127, v136, v131
v_fma_f32 v128, v128, v136, v132
v_fma_f32 v129, v129, v136, v133
v_cvt_pk_bf16_f32 v134, v126, v127
v_cvt_pk_bf16_f32 v135, v128, v129
global_store_dwordx2 v18, v[134:135], s[48:49] sc1
v_subrev_u32 v18, 36864, v18
s_waitcnt vmcnt(0)
s_barrier
ds_read_b64 v[122:123], v17 offset:0
ds_read_b64 v[124:125], v17 offset:128
ds_read_b64 v[108:109], v15 offset:0
ds_read_b64 v[110:111], v15 offset:2048
ds_read_b64 v[112:113], v15 offset:4096
ds_read_b64 v[114:115], v15 offset:6144
ds_read_b64 v[116:117], v15 offset:8192
ds_read_b64 v[118:119], v15 offset:10240
ds_read_b64 v[120:121], v16
s_waitcnt lgkmcnt(0)
s_barrier
s_mov_b32 m0, 52224
s_nop 0
global_load_lds_dwordx4 v11, s[52:53] offset:0 sc1
global_load_lds_dwordx4 v12, s[52:53] offset:1024 sc1
global_load_lds_dwordx4 v13, s[52:53] offset:2048 sc1
s_add_u32 s52, s52, 221184
s_addc_u32 s53, s53, 0
s_mov_b32 m0, 62464
s_nop 0
global_load_lds_dwordx4 v14, s[38:39] offset:0 sc1
v_add_u32 v14, 37376, v14
v_lshlrev_b32 v126, 16, v108
v_add_f32 v122, v122, v124
v_and_b32 v127, 0xffff0000, v108
v_add_f32 v123, v123, v125
v_lshlrev_b32 v128, 16, v109
v_mul_f32 v122, 0x3ae38e39, v122
v_and_b32 v129, 0xffff0000, v109
v_mul_f32 v123, 0x3ae38e39, v123
v_lshlrev_b32 v130, 16, v110
v_fma_f32 v137, -v122, v122, v123
v_and_b32 v131, 0xffff0000, v110
v_max_f32 v137, 0, v137
v_lshlrev_b32 v132, 16, v111
v_cvt_pk_bf16_f32 v137, v137, v137
v_and_b32 v133, 0xffff0000, v111
v_lshlrev_b32 v137, 16, v137
v_add_f32 v126, v126, v130
v_add_f32 v137, 0x3727c5ac, v137
v_add_f32 v127, v127, v131
v_cvt_pk_bf16_f32 v137, v137, v137
v_add_f32 v128, v128, v132
v_lshlrev_b32 v137, 16, v137
v_add_f32 v129, v129, v133
v_sqrt_f32 v137, v137
v_lshlrev_b32 v130, 16, v112
v_cvt_pk_bf16_f32 v137, v137, v137
v_and_b32 v131, 0xffff0000, v112
v_lshlrev_b32 v137, 16, v137
v_lshlrev_b32 v132, 16, v113
v_rcp_f32 v136, v137
v_and_b32 v133, 0xffff0000, v113
v_mul_f32 v136, v136, v136
v_add_f32 v126, v126, v130
v_add_f32 v127, v127, v131
v_add_f32 v128, v128, v132
v_add_f32 v129, v129, v133
v_lshlrev_b32 v130, 16, v114
v_and_b32 v131, 0xffff0000, v114
v_lshlrev_b32 v132, 16, v115
v_and_b32 v133, 0xffff0000, v115
v_add_f32 v126, v126, v130
v_add_f32 v127, v127, v131
v_add_f32 v128, v128, v132
v_add_f32 v129, v129, v133
v_lshlrev_b32 v130, 16, v116
v_and_b32 v131, 0xffff0000, v116
v_lshlrev_b32 v132, 16, v117
v_and_b32 v133, 0xffff0000, v117
v_add_f32 v126, v126, v130
v_add_f32 v127, v127, v131
v_add_f32 v128, v128, v132
v_add_f32 v129, v129, v133
v_lshlrev_b32 v130, 16, v118
v_and_b32 v131, 0xffff0000, v118
v_lshlrev_b32 v132, 16, v119
v_and_b32 v133, 0xffff0000, v119
v_add_f32 v126, v126, v130
v_add_f32 v127, v127, v131
v_add_f32 v128, v128, v132
v_add_f32 v129, v129, v133
v_lshlrev_b32 v130, 16, v120
v_and_b32 v131, 0xffff0000, v120
v_lshlrev_b32 v132, 16, v121
v_and_b32 v133, 0xffff0000, v121
v_fma_f32 v126, v126, v136, v130
v_fma_f32 v127, v127, v136, v131
v_fma_f32 v128, v128, v136, v132
v_fma_f32 v129, v129, v136, v133
v_cvt_pk_bf16_f32 v134, v126, v127
v_cvt_pk_bf16_f32 v135, v128, v129
global_store_dwordx2 v18, v[134:135], s[48:49] sc1
v_subrev_u32 v18, 36864, v18
s_waitcnt vmcnt(0)
s_barrier
ds_read_b64 v[122:123], v17 offset:0
ds_read_b64 v[124:125], v17 offset:128
ds_read_b64 v[108:109], v15 offset:0
ds_read_b64 v[110:111], v15 offset:2048
ds_read_b64 v[112:113], v15 offset:4096
ds_read_b64 v[114:115], v15 offset:6144
ds_read_b64 v[116:117], v15 offset:8192
ds_read_b64 v[118:119], v15 offset:10240
ds_read_b64 v[120:121], v16
s_waitcnt lgkmcnt(6)
v_lshlrev_b32 v126, 16, v108
v_add_f32 v122, v122, v124
v_and_b32 v127, 0xffff0000, v108
v_add_f32 v123, v123, v125
v_lshlrev_b32 v128, 16, v109
v_mul_f32 v122, 0x3ae38e39, v122
v_and_b32 v129, 0xffff0000, v109
v_mul_f32 v123, 0x3ae38e39, v123
s_waitcnt lgkmcnt(5)
v_lshlrev_b32 v130, 16, v110
v_fma_f32 v137, -v122, v122, v123
v_and_b32 v131, 0xffff0000, v110
v_max_f32 v137, 0, v137
v_lshlrev_b32 v132, 16, v111
v_cvt_pk_bf16_f32 v137, v137, v137
v_and_b32 v133, 0xffff0000, v111
v_lshlrev_b32 v137, 16, v137
v_add_f32 v126, v126, v130
v_add_f32 v137, 0x3727c5ac, v137
v_add_f32 v127, v127, v131
v_cvt_pk_bf16_f32 v137, v137, v137
v_add_f32 v128, v128, v132
v_lshlrev_b32 v137, 16, v137
v_add_f32 v129, v129, v133
v_sqrt_f32 v137, v137
s_waitcnt lgkmcnt(4)
v_lshlrev_b32 v130, 16, v112
v_cvt_pk_bf16_f32 v137, v137, v137
v_and_b32 v131, 0xffff0000, v112
v_lshlrev_b32 v137, 16, v137
v_lshlrev_b32 v132, 16, v113
v_rcp_f32 v136, v137
v_and_b32 v133, 0xffff0000, v113
v_mul_f32 v136, v136, v136
v_add_f32 v126, v126, v130
v_add_f32 v127, v127, v131
v_add_f32 v128, v128, v132
v_add_f32 v129, v129, v133
s_waitcnt lgkmcnt(3)
v_lshlrev_b32 v130, 16, v114
v_and_b32 v131, 0xffff0000, v114
v_lshlrev_b32 v132, 16, v115
v_and_b32 v133, 0xffff0000, v115
v_add_f32 v126, v126, v130
v_add_f32 v127, v127, v131
v_add_f32 v128, v128, v132
v_add_f32 v129, v129, v133
s_waitcnt lgkmcnt(2)
v_lshlrev_b32 v130, 16, v116
v_and_b32 v131, 0xffff0000, v116
v_lshlrev_b32 v132, 16, v117
v_and_b32 v133, 0xffff0000, v117
v_add_f32 v126, v126, v130
v_add_f32 v127, v127, v131
v_add_f32 v128, v128, v132
v_add_f32 v129, v129, v133
s_waitcnt lgkmcnt(1)
v_lshlrev_b32 v130, 16, v118
v_and_b32 v131, 0xffff0000, v118
v_lshlrev_b32 v132, 16, v119
v_and_b32 v133, 0xffff0000, v119
v_add_f32 v126, v126, v130
v_add_f32 v127, v127, v131
v_add_f32 v128, v128, v132
v_add_f32 v129, v129, v133
s_waitcnt lgkmcnt(0)
v_lshlrev_b32 v130, 16, v120
v_and_b32 v131, 0xffff0000, v120
v_lshlrev_b32 v132, 16, v121
v_and_b32 v133, 0xffff0000, v121
v_fma_f32 v126, v126, v136, v130
v_fma_f32 v127, v127, v136, v131
v_fma_f32 v128, v128, v136, v132
v_fma_f32 v129, v129, v136, v133
v_cvt_pk_bf16_f32 v134, v126, v127
v_cvt_pk_bf16_f32 v135, v128, v129
global_store_dwordx2 v18, v[134:135], s[48:49] sc1
v_subrev_u32 v18, 36864, v18
s_endpgm
.Ldn_w3:
v_add_u32 v29, -1, v20
s_add_u32 m0, s51, 24576
s_mov_b32 s73, 27
s_mov_b32 s70, 32
s_waitcnt vmcnt(0) lgkmcnt(0)
s_nop 7
.p2align 6
.Ldn_top16:
s_waitcnt lgkmcnt(11)
v_mfma_f32_16x16x32_bf16 v[62:65], a[0:3], v[86:89], 0
ds_read_b128 v[98:101], v28 offset:3072
v_mfma_f32_16x16x32_bf16 v[66:69], a[4:7], v[86:89], 0
s_cmp_eq_u32 s70, s73
s_cbranch_scc1 .Ldn_hk13
.Ldn_hr14:
v_mfma_f32_16x16x32_bf16 v[70:73], a[8:11], v[86:89], 0
s_waitcnt lgkmcnt(9)
v_mfma_f32_16x16x32_bf16 v[62:65], a[12:15], v[90:93], v[62:65]
ds_read_b128 v[86:89], v28 offset:4096
v_cvt_pk_bf16_f32 v102, v74, v75
v_mfma_f32_16x16x32_bf16 v[66:69], a[16:19], v[90:93], v[66:69]
v_mfma_f32_16x16x32_bf16 v[70:73], a[20:23], v[90:93], v[70:73]
s_waitcnt vmcnt(7)
s_barrier
s_waitcnt lgkmcnt(4)
v_mfma_f32_16x16x32_bf16 v[62:65], a[24:27], v[94:97], v[62:65]
ds_read_b128 v[90:93], v28 offset:5120
global_store_dword v24, v25, s[38:39] sc1
v_mfma_f32_16x16x32_bf16 v[66:69], a[28:31], v[94:97], v[66:69]
v_add_u32 v25, 1, v25
v_cvt_pk_bf16_f32 v103, v76, v77
v_mfma_f32_16x16x32_bf16 v[70:73], a[32:35], v[94:97], v[70:73]
s_waitcnt lgkmcnt(2)
v_cvt_pk_bf16_f32 v104, v78, v79
v_cvt_pk_bf16_f32 v105, v80, v81
v_mfma_f32_16x16x32_bf16 v[62:65], a[36:39], v[98:101], v[62:65]
ds_read_b128 v[94:97], v28 offset:6144
v_cvt_pk_bf16_f32 v106, v82, v83
v_mfma_f32_16x16x32_bf16 v[66:69], a[40:43], v[98:101], v[66:69]
v_cvt_pk_bf16_f32 v107, v84, v85
v_lshlrev_b32 v126, 16, v108
v_mfma_f32_16x16x32_bf16 v[70:73], a[44:47], v[98:101], v[70:73]
s_waitcnt lgkmcnt(2)
v_add_f32 v122, v122, v124
v_and_b32 v127, 0xffff0000, v108
v_mfma_f32_16x16x32_bf16 v[62:65], a[48:51], v[86:89], v[62:65]
ds_read_b128 v[98:101], v28 offset:7168
v_add_f32 v123, v123, v125
v_mfma_f32_16x16x32_bf16 v[66:69], a[52:55], v[86:89], v[66:69]
global_store_dwordx4 v9, v[102:105], s[38:39] sc1
v_mfma_f32_16x16x32_bf16 v[70:73], a[56:59], v[86:89], v[70:73]
s_waitcnt lgkmcnt(2)
v_add_u32 v9, 110592, v9
v_lshlrev_b32 v128, 16, v109
v_mfma_f32_16x16x32_bf16 v[62:65], a[60:63], v[90:93], v[62:65]
ds_read_b128 v[86:89], v28 offset:8192
v_mul_f32 v122, 0x3ae38e39, v122
v_mfma_f32_16x16x32_bf16 v[66:69], a[64:67], v[90:93], v[66:69]
global_store_dwordx2 v10, v[106:107], s[38:39] sc1
v_mfma_f32_16x16x32_bf16 v[70:73], a[68:71], v[90:93], v[70:73]
s_waitcnt lgkmcnt(2)
v_add_u32 v10, 110592, v10
v_and_b32 v129, 0xffff0000, v109
v_mfma_f32_16x16x32_bf16 v[62:65], a[72:75], v[94:97], v[62:65]
ds_read_b128 v[90:93], v28 offset:9216
v_mul_f32 v123, 0x3ae38e39, v123
v_mfma_f32_16x16x32_bf16 v[66:69], a[76:79], v[94:97], v[66:69]
v_lshlrev_b32 v130, 16, v110
v_fma_f32 v137, -v122, v122, v123
v_mfma_f32_16x16x32_bf16 v[70:73], a[80:83], v[94:97], v[70:73]
s_waitcnt lgkmcnt(2)
v_and_b32 v131, 0xffff0000, v110
v_max_f32 v137, 0, v137
v_mfma_f32_16x16x32_bf16 v[62:65], a[84:87], v[98:101], v[62:65]
ds_read_b128 v[94:97], v28 offset:10240
v_lshlrev_b32 v132, 16, v111
v_mfma_f32_16x16x32_bf16 v[66:69], a[88:91], v[98:101], v[66:69]
v_cvt_pk_bf16_f32 v137, v137, v137
v_and_b32 v133, 0xffff0000, v111
v_mfma_f32_16x16x32_bf16 v[70:73], a[92:95], v[98:101], v[70:73]
s_waitcnt lgkmcnt(2)
v_lshlrev_b32 v137, 16, v137
v_add_f32 v126, v126, v130
v_mfma_f32_16x16x32_bf16 v[62:65], a[96:99], v[86:89], v[62:65]
ds_read_b128 v[98:101], v28 offset:11264
v_add_f32 v137, 0x3727c5ac, v137
v_mfma_f32_16x16x32_bf16 v[66:69], a[100:103], v[86:89], v[66:69]
v_add_f32 v127, v127, v131
v_cvt_pk_bf16_f32 v137, v137, v137
v_mfma_f32_16x16x32_bf16 v[70:73], a[104:107], v[86:89], v[70:73]
s_waitcnt lgkmcnt(2)
v_add_f32 v128, v128, v132
v_lshlrev_b32 v137, 16, v137
v_mfma_f32_16x16x32_bf16 v[62:65], a[108:111], v[90:93], v[62:65]
ds_read_b128 v[86:89], v28 offset:12288
v_add_f32 v129, v129, v133
v_mfma_f32_16x16x32_bf16 v[66:69], a[112:115], v[90:93], v[66:69]
v_sqrt_f32 v137, v137
v_mfma_f32_16x16x32_bf16 v[70:73], a[116:119], v[90:93], v[70:73]
s_waitcnt lgkmcnt(2)
v_lshlrev_b32 v130, 16, v112
v_cvt_pk_bf16_f32 v137, v137, v137
v_mfma_f32_16x16x32_bf16 v[62:65], a[120:123], v[94:97], v[62:65]
ds_read_b128 v[90:93], v28 offset:13312
v_and_b32 v131, 0xffff0000, v112
v_mfma_f32_16x16x32_bf16 v[66:69], a[124:127], v[94:97], v[66:69]
v_lshlrev_b32 v137, 16, v137
v_lshlrev_b32 v132, 16, v113
v_mfma_f32_16x16x32_bf16 v[70:73], a[128:131], v[94:97], v[70:73]
s_waitcnt lgkmcnt(2)
v_rcp_f32 v136, v137
v_mfma_f32_16x16x32_bf16 v[62:65], a[132:135], v[98:101], v[62:65]
s_xor_b32 s51, s51, 0x10000
ds_read_b128 v[94:97], v28 offset:14336
v_and_b32 v133, 0xffff0000, v113
v_mfma_f32_16x16x32_bf16 v[66:69], a[136:139], v[98:101], v[66:69]
s_add_u32 m0, s51, 0
v_mul_f32 v136, v136, v136
v_add_f32 v126, v126, v130
v_mfma_f32_16x16x32_bf16 v[70:73], a[140:143], v[98:101], v[70:73]
s_waitcnt lgkmcnt(2)
v_mfma_f32_16x16x32_bf16 v[62:65], a[144:147], v[86:89], v[62:65]
s_barrier
ds_read_b128 v[98:101], v28 offset:15360
v_mfma_f32_16x16x32_bf16 v[66:69], a[148:151], v[86:89], v[66:69]
v_add_f32 v127, v127, v131
v_add_f32 v128, v128, v132
v_mfma_f32_16x16x32_bf16 v[70:73], a[152:155], v[86:89], v[70:73]
s_waitcnt lgkmcnt(2)
v_add_f32 v129, v129, v133
v_lshlrev_b32 v130, 16, v114
v_mfma_f32_16x16x32_bf16 v[62:65], a[156:159], v[90:93], v[62:65]
ds_read_b128 v[86:89], v28 offset:16384
global_load_lds_dwordx4 v8, s[38:39] offset:-2048 sc1
v_mfma_f32_16x16x32_bf16 v[66:69], a[160:163], v[90:93], v[66:69]
v_and_b32 v131, 0xffff0000, v114
v_lshlrev_b32 v132, 16, v115
v_mfma_f32_16x16x32_bf16 v[70:73], a[164:167], v[90:93], v[70:73]
s_waitcnt lgkmcnt(2)
v_and_b32 v133, 0xffff0000, v115
v_add_f32 v126, v126, v130
v_mfma_f32_16x16x32_bf16 v[62:65], a[168:171], v[94:97], v[62:65]
ds_read_b128 v[90:93], v28 offset:17408
global_load_lds_dwordx4 v8, s[38:39] offset:-1024 sc1
)"
R"(v_mfma_f32_16x16x32_bf16 v[66:69], a[172:175], v[94:97], v[66:69]
v_add_f32 v127, v127, v131
v_add_f32 v128, v128, v132
v_mfma_f32_16x16x32_bf16 v[70:73], a[176:179], v[94:97], v[70:73]
s_waitcnt lgkmcnt(2)
v_add_f32 v129, v129, v133
v_lshlrev_b32 v130, 16, v116
v_mfma_f32_16x16x32_bf16 v[62:65], a[180:183], v[98:101], v[62:65]
ds_read_b128 v[94:97], v28 offset:18432
global_load_lds_dwordx4 v8, s[38:39] offset:0 sc1
v_mfma_f32_16x16x32_bf16 v[66:69], a[184:187], v[98:101], v[66:69]
v_and_b32 v131, 0xffff0000, v116
v_lshlrev_b32 v132, 16, v117
v_mfma_f32_16x16x32_bf16 v[70:73], a[188:191], v[98:101], v[70:73]
s_waitcnt lgkmcnt(2)
v_and_b32 v133, 0xffff0000, v117
v_add_f32 v126, v126, v130
v_mfma_f32_16x16x32_bf16 v[62:65], a[192:195], v[86:89], v[62:65]
ds_read_b128 v[98:101], v28 offset:19456
global_load_lds_dwordx4 v8, s[38:39] offset:1024 sc1
v_mfma_f32_16x16x32_bf16 v[66:69], a[196:199], v[86:89], v[66:69]
v_add_f32 v127, v127, v131
v_add_f32 v128, v128, v132
v_mfma_f32_16x16x32_bf16 v[70:73], a[200:203], v[86:89], v[70:73]
s_waitcnt lgkmcnt(2)
v_add_f32 v129, v129, v133
v_lshlrev_b32 v130, 16, v118
v_mfma_f32_16x16x32_bf16 v[62:65], a[204:207], v[90:93], v[62:65]
ds_read_b128 v[86:89], v28 offset:20480
global_load_lds_dwordx4 v8, s[38:39] offset:2048 sc1
v_mfma_f32_16x16x32_bf16 v[66:69], a[208:211], v[90:93], v[66:69]
v_and_b32 v131, 0xffff0000, v118
v_lshlrev_b32 v132, 16, v119
v_mfma_f32_16x16x32_bf16 v[70:73], a[212:215], v[90:93], v[70:73]
s_waitcnt lgkmcnt(2)
v_and_b32 v133, 0xffff0000, v119
v_add_f32 v126, v126, v130
v_mfma_f32_16x16x32_bf16 v[62:65], a[216:219], v[94:97], v[62:65]
ds_read_b128 v[90:93], v28 offset:21504
global_load_lds_dwordx4 v8, s[38:39] offset:3072 sc1
v_mfma_f32_16x16x32_bf16 v[66:69], a[220:223], v[94:97], v[66:69]
v_add_u32 v8, 147456, v8
v_add_f32 v127, v127, v131
v_mfma_f32_16x16x32_bf16 v[70:73], a[224:227], v[94:97], v[70:73]
s_waitcnt lgkmcnt(2)
v_add_f32 v128, v128, v132
v_add_f32 v129, v129, v133
v_mfma_f32_16x16x32_bf16 v[62:65], a[228:231], v[98:101], v[62:65]
s_mov_b32 m0, 55296
ds_read_b128 v[94:97], v28 offset:22528
global_load_lds_dwordx4 v11, s[52:53] offset:0 sc1
v_mfma_f32_16x16x32_bf16 v[66:69], a[232:235], v[98:101], v[66:69]
v_lshlrev_b32 v130, 16, v120
v_and_b32 v131, 0xffff0000, v120
v_mfma_f32_16x16x32_bf16 v[70:73], a[236:239], v[98:101], v[70:73]
s_waitcnt lgkmcnt(2)
global_load_lds_dwordx4 v12, s[52:53] offset:1024 sc1
v_lshlrev_b32 v132, 16, v121
v_mfma_f32_16x16x32_bf16 v[62:65], a[240:243], v[86:89], v[62:65]
ds_read_b128 v[98:101], v28 offset:23552
v_and_b32 v133, 0xffff0000, v121
v_mfma_f32_16x16x32_bf16 v[66:69], a[244:247], v[86:89], v[66:69]
global_load_lds_dwordx4 v13, s[52:53] offset:2048 sc1
v_fma_f32 v126, v126, v136, v130
v_mfma_f32_16x16x32_bf16 v[70:73], a[248:251], v[86:89], v[70:73]
s_waitcnt lgkmcnt(2)
v_mfma_f32_16x16x32_bf16 v[62:65], a[252:255], v[90:93], v[62:65]
s_waitcnt vmcnt(13)
s_barrier
ds_read_b128 v[86:89], v28 offset:24576
v_mfma_f32_16x16x32_bf16 v[66:69], v[30:33], v[90:93], v[66:69]
s_add_u32 s52, s52, 221184
s_addc_u32 s53, s53, 0
s_mov_b32 m0, 63488
v_mfma_f32_16x16x32_bf16 v[70:73], v[34:37], v[90:93], v[70:73]
s_waitcnt lgkmcnt(2)
global_load_lds_dwordx4 v14, s[38:39] offset:0 sc1
v_fma_f32 v127, v127, v136, v131
v_mfma_f32_16x16x32_bf16 v[62:65], v[38:41], v[94:97], v[62:65]
ds_read_b128 v[90:93], v28 offset:25600
v_fma_f32 v128, v128, v136, v132
v_mfma_f32_16x16x32_bf16 v[66:69], v[42:45], v[94:97], v[66:69]
v_add_u32 v14, 37376, v14
v_fma_f32 v129, v129, v136, v133
v_mfma_f32_16x16x32_bf16 v[70:73], v[46:49], v[94:97], v[70:73]
s_waitcnt lgkmcnt(2)
v_cvt_pk_bf16_f32 v134, v126, v127
v_cvt_pk_bf16_f32 v135, v128, v129
v_mfma_f32_16x16x32_bf16 v[62:65], v[50:53], v[98:101], v[62:65]
ds_read_b128 v[94:97], v28 offset:26624
v_mfma_f32_16x16x32_bf16 v[66:69], v[54:57], v[98:101], v[66:69]
global_store_dwordx2 v18, v[134:135], s[48:49] sc1
v_mfma_f32_16x16x32_bf16 v[70:73], v[58:61], v[98:101], v[70:73]
s_waitcnt lgkmcnt(2)
v_subrev_u32 v18, 36864, v18
v_mfma_f32_16x16x32_bf16 v[74:77], a[0:3], v[86:89], 0
ds_read_b128 v[98:101], v28 offset:27648
v_mfma_f32_16x16x32_bf16 v[78:81], a[4:7], v[86:89], 0
v_mfma_f32_16x16x32_bf16 v[82:85], a[8:11], v[86:89], 0
s_waitcnt lgkmcnt(2)
v_mfma_f32_16x16x32_bf16 v[74:77], a[12:15], v[90:93], v[74:77]
ds_read_b128 v[86:89], v28 offset:28672
v_cvt_pk_bf16_f32 v102, v62, v63
v_mfma_f32_16x16x32_bf16 v[78:81], a[16:19], v[90:93], v[78:81]
v_mfma_f32_16x16x32_bf16 v[82:85], a[20:23], v[90:93], v[82:85]
s_waitcnt vmcnt(11)
s_barrier
s_waitcnt lgkmcnt(2)
v_mfma_f32_16x16x32_bf16 v[74:77], a[24:27], v[94:97], v[74:77]
ds_read_b128 v[90:93], v28 offset:29696
v_cvt_pk_bf16_f32 v103, v64, v65
v_mfma_f32_16x16x32_bf16 v[78:81], a[28:31], v[94:97], v[78:81]
v_cvt_pk_bf16_f32 v104, v66, v67
v_cvt_pk_bf16_f32 v105, v68, v69
v_mfma_f32_16x16x32_bf16 v[82:85], a[32:35], v[94:97], v[82:85]
s_waitcnt lgkmcnt(2)
v_cvt_pk_bf16_f32 v106, v70, v71
v_cvt_pk_bf16_f32 v107, v72, v73
v_mfma_f32_16x16x32_bf16 v[74:77], a[36:39], v[98:101], v[74:77]
ds_read_b128 v[94:97], v28 offset:30720
global_store_dword v24, v25, s[38:39] sc1
v_mfma_f32_16x16x32_bf16 v[78:81], a[40:43], v[98:101], v[78:81]
v_add_u32 v25, 1, v25
v_mfma_f32_16x16x32_bf16 v[82:85], a[44:47], v[98:101], v[82:85]
s_waitcnt lgkmcnt(2)
global_store_dwordx4 v9, v[102:105], s[38:39] sc1
v_mfma_f32_16x16x32_bf16 v[74:77], a[48:51], v[86:89], v[74:77]
ds_read_b128 v[98:101], v28 offset:31744
v_add_u32 v9, 110592, v9
v_mfma_f32_16x16x32_bf16 v[78:81], a[52:55], v[86:89], v[78:81]
v_mfma_f32_16x16x32_bf16 v[82:85], a[56:59], v[86:89], v[82:85]
s_waitcnt lgkmcnt(2)
global_store_dwordx2 v10, v[106:107], s[38:39] sc1
v_mfma_f32_16x16x32_bf16 v[74:77], a[60:63], v[90:93], v[74:77]
ds_read_b128 v[86:89], v28 offset:32768
v_add_u32 v10, 110592, v10
v_mfma_f32_16x16x32_bf16 v[78:81], a[64:67], v[90:93], v[78:81]
v_mfma_f32_16x16x32_bf16 v[82:85], a[68:71], v[90:93], v[82:85]
s_waitcnt lgkmcnt(2)
v_mfma_f32_16x16x32_bf16 v[74:77], a[72:75], v[94:97], v[74:77]
ds_read_b128 v[90:93], v28 offset:33792
v_mfma_f32_16x16x32_bf16 v[78:81], a[76:79], v[94:97], v[78:81]
v_mfma_f32_16x16x32_bf16 v[82:85], a[80:83], v[94:97], v[82:85]
s_waitcnt lgkmcnt(2)
v_mfma_f32_16x16x32_bf16 v[74:77], a[84:87], v[98:101], v[74:77]
ds_read_b128 v[94:97], v28 offset:34816
v_mfma_f32_16x16x32_bf16 v[78:81], a[88:91], v[98:101], v[78:81]
v_mfma_f32_16x16x32_bf16 v[82:85], a[92:95], v[98:101], v[82:85]
s_waitcnt lgkmcnt(2)
v_mfma_f32_16x16x32_bf16 v[74:77], a[96:99], v[86:89], v[74:77]
ds_read_b128 v[98:101], v28 offset:35840
v_mfma_f32_16x16x32_bf16 v[78:81], a[100:103], v[86:89], v[78:81]
v_mfma_f32_16x16x32_bf16 v[82:85], a[104:107], v[86:89], v[82:85]
s_waitcnt lgkmcnt(2)
v_mfma_f32_16x16x32_bf16 v[74:77], a[108:111], v[90:93], v[74:77]
ds_read_b128 v[86:89], v28 offset:36864
v_mfma_f32_16x16x32_bf16 v[78:81], a[112:115], v[90:93], v[78:81]
v_mfma_f32_16x16x32_bf16 v[82:85], a[116:119], v[90:93], v[82:85]
s_waitcnt lgkmcnt(2)
v_mfma_f32_16x16x32_bf16 v[74:77], a[120:123], v[94:97], v[74:77]
ds_read_b128 v[90:93], v28 offset:37888
v_mfma_f32_16x16x32_bf16 v[78:81], a[124:127], v[94:97], v[78:81]
v_mfma_f32_16x16x32_bf16 v[82:85], a[128:131], v[94:97], v[82:85]
s_waitcnt lgkmcnt(2)
v_mfma_f32_16x16x32_bf16 v[74:77], a[132:135], v[98:101], v[74:77]
ds_read_b128 v[94:97], v28 offset:38912
v_mfma_f32_16x16x32_bf16 v[78:81], a[136:139], v[98:101], v[78:81]
s_add_u32 m0, s51, 24576
v_mfma_f32_16x16x32_bf16 v[82:85], a[140:143], v[98:101], v[82:85]
s_waitcnt lgkmcnt(2)
v_mfma_f32_16x16x32_bf16 v[74:77], a[144:147], v[86:89], v[74:77]
s_barrier
ds_read_b128 v[98:101], v28 offset:39936
v_mfma_f32_16x16x32_bf16 v[78:81], a[148:151], v[86:89], v[78:81]
v_mfma_f32_16x16x32_bf16 v[82:85], a[152:155], v[86:89], v[82:85]
s_waitcnt lgkmcnt(2)
v_mfma_f32_16x16x32_bf16 v[74:77], a[156:159], v[90:93], v[74:77]
ds_read_b128 v[86:89], v28 offset:40960
global_load_lds_dwordx4 v8, s[38:39] offset:-2048 sc1
v_mfma_f32_16x16x32_bf16 v[78:81], a[160:163], v[90:93], v[78:81]
v_mfma_f32_16x16x32_bf16 v[82:85], a[164:167], v[90:93], v[82:85]
s_waitcnt lgkmcnt(2)
v_mfma_f32_16x16x32_bf16 v[74:77], a[168:171], v[94:97], v[74:77]
ds_read_b128 v[90:93], v28 offset:41984
global_load_lds_dwordx4 v8, s[38:39] offset:-1024 sc1
v_mfma_f32_16x16x32_bf16 v[78:81], a[172:175], v[94:97], v[78:81]
v_mfma_f32_16x16x32_bf16 v[82:85], a[176:179], v[94:97], v[82:85]
s_waitcnt lgkmcnt(2)
v_mfma_f32_16x16x32_bf16 v[74:77], a[180:183], v[98:101], v[74:77]
ds_read_b128 v[94:97], v28 offset:43008
global_load_lds_dwordx4 v8, s[38:39] offset:0 sc1
v_mfma_f32_16x16x32_bf16 v[78:81], a[184:187], v[98:101], v[78:81]
v_mfma_f32_16x16x32_bf16 v[82:85], a[188:191], v[98:101], v[82:85]
s_waitcnt lgkmcnt(2)
v_mfma_f32_16x16x32_bf16 v[74:77], a[192:195], v[86:89], v[74:77]
ds_read_b128 v[98:101], v28 offset:44032
global_load_lds_dwordx4 v8, s[38:39] offset:1024 sc1
v_mfma_f32_16x16x32_bf16 v[78:81], a[196:199], v[86:89], v[78:81]
v_mfma_f32_16x16x32_bf16 v[82:85], a[200:203], v[86:89], v[82:85]
s_waitcnt lgkmcnt(2)
v_mfma_f32_16x16x32_bf16 v[74:77], a[204:207], v[90:93], v[74:77]
ds_read_b128 v[86:89], v28 offset:45056
global_load_lds_dwordx4 v8, s[38:39] offset:2048 sc1
v_mfma_f32_16x16x32_bf16 v[78:81], a[208:211], v[90:93], v[78:81]
v_mfma_f32_16x16x32_bf16 v[82:85], a[212:215], v[90:93], v[82:85]
s_waitcnt lgkmcnt(2)
v_mfma_f32_16x16x32_bf16 v[74:77], a[216:219], v[94:97], v[74:77]
ds_read_b128 v[90:93], v28 offset:46080
global_load_lds_dwordx4 v8, s[38:39] offset:3072 sc1
v_mfma_f32_16x16x32_bf16 v[78:81], a[220:223], v[94:97], v[78:81]
v_add_u32 v8, 147456, v8
v_mfma_f32_16x16x32_bf16 v[82:85], a[224:227], v[94:97], v[82:85]
s_waitcnt lgkmcnt(2)
v_mfma_f32_16x16x32_bf16 v[74:77], a[228:231], v[98:101], v[74:77]
ds_read_b128 v[94:97], v28 offset:47104
v_mfma_f32_16x16x32_bf16 v[78:81], a[232:235], v[98:101], v[78:81]
v_mfma_f32_16x16x32_bf16 v[82:85], a[236:239], v[98:101], v[82:85]
s_waitcnt lgkmcnt(2)
v_mfma_f32_16x16x32_bf16 v[74:77], a[240:243], v[86:89], v[74:77]
ds_read_b128 v[98:101], v28 offset:48128
v_mfma_f32_16x16x32_bf16 v[78:81], a[244:247], v[86:89], v[78:81]
v_xor_b32 v28, 0x10000, v28
v_mfma_f32_16x16x32_bf16 v[82:85], a[248:251], v[86:89], v[82:85]
s_waitcnt lgkmcnt(2)
v_mfma_f32_16x16x32_bf16 v[74:77], a[252:255], v[90:93], v[74:77]
s_waitcnt vmcnt(9)
s_barrier
ds_read_b128 v[86:89], v28 offset:0
v_mfma_f32_16x16x32_bf16 v[78:81], v[30:33], v[90:93], v[78:81]
global_store_dword v26, v27, s[38:39] sc1
v_add_u32 v27, 2, v27
v_mfma_f32_16x16x32_bf16 v[82:85], v[34:37], v[90:93], v[82:85]
s_waitcnt lgkmcnt(2)
ds_read_b64 v[122:123], v17 offset:0
ds_read_b64 v[124:125], v17 offset:128
v_mfma_f32_16x16x32_bf16 v[74:77], v[38:41], v[94:97], v[74:77]
ds_read_b128 v[90:93], v28 offset:1024
ds_read_b64 v[108:109], v15 offset:0
v_mfma_f32_16x16x32_bf16 v[78:81], v[42:45], v[94:97], v[78:81]
ds_read_b64 v[110:111], v15 offset:2048
ds_read_b64 v[112:113], v15 offset:4096
v_mfma_f32_16x16x32_bf16 v[82:85], v[46:49], v[94:97], v[82:85]
s_waitcnt lgkmcnt(7)
ds_read_b64 v[114:115], v15 offset:6144
ds_read_b64 v[116:117], v15 offset:8192
v_mfma_f32_16x16x32_bf16 v[74:77], v[50:53], v[98:101], v[74:77]
ds_read_b128 v[94:97], v28 offset:2048
ds_read_b64 v[118:119], v15 offset:10240
v_mfma_f32_16x16x32_bf16 v[78:81], v[54:57], v[98:101], v[78:81]
ds_read_b64 v[120:121], v16
v_mfma_f32_16x16x32_bf16 v[82:85], v[58:61], v[98:101], v[82:85]
s_sub_u32 s70, s70, 1
s_cbranch_scc0 .Ldn_top16
s_waitcnt lgkmcnt(0)
ds_read_b64 v[122:123], v17 offset:0
ds_read_b64 v[124:125], v17 offset:128
ds_read_b64 v[108:109], v15 offset:0
ds_read_b64 v[110:111], v15 offset:2048
ds_read_b64 v[112:113], v15 offset:4096
ds_read_b64 v[114:115], v15 offset:6144
ds_read_b64 v[116:117], v15 offset:8192
ds_read_b64 v[118:119], v15 offset:10240
ds_read_b64 v[120:121], v16
v_cvt_pk_bf16_f32 v102, v74, v75
v_cvt_pk_bf16_f32 v103, v76, v77
v_cvt_pk_bf16_f32 v104, v78, v79
v_cvt_pk_bf16_f32 v105, v80, v81
v_cvt_pk_bf16_f32 v106, v82, v83
v_cvt_pk_bf16_f32 v107, v84, v85
global_store_dwordx4 v9, v[102:105], s[38:39] sc1
v_add_u32 v9, 110592, v9
global_store_dwordx2 v10, v[106:107], s[38:39] sc1
v_add_u32 v10, 110592, v10
s_waitcnt vmcnt(2)
s_barrier
global_store_dword v24, v25, s[38:39] sc1
v_add_u32 v25, 1, v25
s_waitcnt lgkmcnt(6)
v_lshlrev_b32 v126, 16, v108
v_add_f32 v122, v122, v124
v_and_b32 v127, 0xffff0000, v108
v_add_f32 v123, v123, v125
v_lshlrev_b32 v128, 16, v109
v_mul_f32 v122, 0x3ae38e39, v122
v_and_b32 v129, 0xffff0000, v109
v_mul_f32 v123, 0x3ae38e39, v123
s_waitcnt lgkmcnt(5)
v_lshlrev_b32 v130, 16, v110
v_fma_f32 v137, -v122, v122, v123
v_and_b32 v131, 0xffff0000, v110
v_max_f32 v137, 0, v137
v_lshlrev_b32 v132, 16, v111
v_cvt_pk_bf16_f32 v137, v137, v137
v_and_b32 v133, 0xffff0000, v111
v_lshlrev_b32 v137, 16, v137
v_add_f32 v126, v126, v130
v_add_f32 v137, 0x3727c5ac, v137
v_add_f32 v127, v127, v131
v_cvt_pk_bf16_f32 v137, v137, v137
v_add_f32 v128, v128, v132
v_lshlrev_b32 v137, 16, v137
v_add_f32 v129, v129, v133
v_sqrt_f32 v137, v137
s_waitcnt lgkmcnt(4)
v_lshlrev_b32 v130, 16, v112
v_cvt_pk_bf16_f32 v137, v137, v137
v_and_b32 v131, 0xffff0000, v112
v_lshlrev_b32 v137, 16, v137
v_lshlrev_b32 v132, 16, v113
v_rcp_f32 v136, v137
v_and_b32 v133, 0xffff0000, v113
v_mul_f32 v136, v136, v136
v_add_f32 v126, v126, v130
v_add_f32 v127, v127, v131
v_add_f32 v128, v128, v132
v_add_f32 v129, v129, v133
s_waitcnt lgkmcnt(3)
v_lshlrev_b32 v130, 16, v114
v_and_b32 v131, 0xffff0000, v114
v_lshlrev_b32 v132, 16, v115
v_and_b32 v133, 0xffff0000, v115
v_add_f32 v126, v126, v130
v_add_f32 v127, v127, v131
v_add_f32 v128, v128, v132
v_add_f32 v129, v129, v133
s_waitcnt lgkmcnt(2)
v_lshlrev_b32 v130, 16, v116
v_and_b32 v131, 0xffff0000, v116
v_lshlrev_b32 v132, 16, v117
v_and_b32 v133, 0xffff0000, v117
v_add_f32 v126, v126, v130
v_add_f32 v127, v127, v131
v_add_f32 v128, v128, v132
v_add_f32 v129, v129, v133
s_waitcnt lgkmcnt(1)
v_lshlrev_b32 v130, 16, v118
v_and_b32 v131, 0xffff0000, v118
v_lshlrev_b32 v132, 16, v119
v_and_b32 v133, 0xffff0000, v119
v_add_f32 v126, v126, v130
v_add_f32 v127, v127, v131
v_add_f32 v128, v128, v132
v_add_f32 v129, v129, v133
s_waitcnt lgkmcnt(0)
v_lshlrev_b32 v130, 16, v120
v_and_b32 v131, 0xffff0000, v120
v_lshlrev_b32 v132, 16, v121
v_and_b32 v133, 0xffff0000, v121
v_fma_f32 v126, v126, v136, v130
v_fma_f32 v127, v127, v136, v131
v_fma_f32 v128, v128, v136, v132
v_fma_f32 v129, v129, v136, v133
v_cvt_pk_bf16_f32 v134, v126, v127
v_cvt_pk_bf16_f32 v135, v128, v129
s_waitcnt vmcnt(1)
s_barrier
global_store_dword v24, v25, s[38:39] sc1
s_mov_b32 m0, 55296
s_nop 0
global_load_lds_dwordx4 v11, s[52:53] offset:0 sc1
global_load_lds_dwordx4 v12, s[52:53] offset:1024 sc1
global_load_lds_dwordx4 v13, s[52:53] offset:2048 sc1
s_add_u32 s52, s52, 221184
s_addc_u32 s53, s53, 0
s_mov_b32 m0, 63488
s_nop 0
global_load_lds_dwordx4 v14, s[38:39] offset:0 sc1
v_add_u32 v14, 37376, v14
global_store_dwordx2 v18, v[134:135], s[48:49] sc1
v_subrev_u32 v18, 36864, v18
s_waitcnt vmcnt(0)
s_barrier
ds_read_b64 v[122:123], v17 offset:0
ds_read_b64 v[124:125], v17 offset:128
ds_read_b64 v[108:109], v15 offset:0
ds_read_b64 v[110:111], v15 offset:2048
ds_read_b64 v[112:113], v15 offset:4096
ds_read_b64 v[114:115], v15 offset:6144
ds_read_b64 v[116:117], v15 offset:8192
ds_read_b64 v[118:119], v15 offset:10240
ds_read_b64 v[120:121], v16
s_waitcnt lgkmcnt(0)
s_barrier
s_mov_b32 m0, 55296
s_nop 0
global_load_lds_dwordx4 v11, s[52:53] offset:0 sc1
global_load_lds_dwordx4 v12, s[52:53] offset:1024 sc1
global_load_lds_dwordx4 v13, s[52:53] offset:2048 sc1
s_add_u32 s52, s52, 221184
s_addc_u32 s53, s53, 0
s_mov_b32 m0, 63488
s_nop 0
global_load_lds_dwordx4 v14, s[38:39] offset:0 sc1
v_add_u32 v14, 37376, v14
v_lshlrev_b32 v126, 16, v108
v_add_f32 v122, v122, v124
v_and_b32 v127, 0xffff0000, v108
v_add_f32 v123, v123, v125
v_lshlrev_b32 v128, 16, v109
v_mul_f32 v122, 0x3ae38e39, v122
)"
R"(v_and_b32 v129, 0xffff0000, v109
v_mul_f32 v123, 0x3ae38e39, v123
v_lshlrev_b32 v130, 16, v110
v_fma_f32 v137, -v122, v122, v123
v_and_b32 v131, 0xffff0000, v110
v_max_f32 v137, 0, v137
v_lshlrev_b32 v132, 16, v111
v_cvt_pk_bf16_f32 v137, v137, v137
v_and_b32 v133, 0xffff0000, v111
v_lshlrev_b32 v137, 16, v137
v_add_f32 v126, v126, v130
v_add_f32 v137, 0x3727c5ac, v137
v_add_f32 v127, v127, v131
v_cvt_pk_bf16_f32 v137, v137, v137
v_add_f32 v128, v128, v132
v_lshlrev_b32 v137, 16, v137
v_add_f32 v129, v129, v133
v_sqrt_f32 v137, v137
v_lshlrev_b32 v130, 16, v112
v_cvt_pk_bf16_f32 v137, v137, v137
v_and_b32 v131, 0xffff0000, v112
v_lshlrev_b32 v137, 16, v137
v_lshlrev_b32 v132, 16, v113
v_rcp_f32 v136, v137
v_and_b32 v133, 0xffff0000, v113
v_mul_f32 v136, v136, v136
v_add_f32 v126, v126, v130
v_add_f32 v127, v127, v131
v_add_f32 v128, v128, v132
v_add_f32 v129, v129, v133
v_lshlrev_b32 v130, 16, v114
v_and_b32 v131, 0xffff0000, v114
v_lshlrev_b32 v132, 16, v115
v_and_b32 v133, 0xffff0000, v115
v_add_f32 v126, v126, v130
v_add_f32 v127, v127, v131
v_add_f32 v128, v128, v132
v_add_f32 v129, v129, v133
v_lshlrev_b32 v130, 16, v116
v_and_b32 v131, 0xffff0000, v116
v_lshlrev_b32 v132, 16, v117
v_and_b32 v133, 0xffff0000, v117
v_add_f32 v126, v126, v130
v_add_f32 v127, v127, v131
v_add_f32 v128, v128, v132
v_add_f32 v129, v129, v133
v_lshlrev_b32 v130, 16, v118
v_and_b32 v131, 0xffff0000, v118
v_lshlrev_b32 v132, 16, v119
v_and_b32 v133, 0xffff0000, v119
v_add_f32 v126, v126, v130
v_add_f32 v127, v127, v131
v_add_f32 v128, v128, v132
v_add_f32 v129, v129, v133
v_lshlrev_b32 v130, 16, v120
v_and_b32 v131, 0xffff0000, v120
v_lshlrev_b32 v132, 16, v121
v_and_b32 v133, 0xffff0000, v121
v_fma_f32 v126, v126, v136, v130
v_fma_f32 v127, v127, v136, v131
v_fma_f32 v128, v128, v136, v132
v_fma_f32 v129, v129, v136, v133
v_cvt_pk_bf16_f32 v134, v126, v127
v_cvt_pk_bf16_f32 v135, v128, v129
global_store_dwordx2 v18, v[134:135], s[48:49] sc1
v_subrev_u32 v18, 36864, v18
s_waitcnt vmcnt(0)
s_barrier
ds_read_b64 v[122:123], v17 offset:0
ds_read_b64 v[124:125], v17 offset:128
ds_read_b64 v[108:109], v15 offset:0
ds_read_b64 v[110:111], v15 offset:2048
ds_read_b64 v[112:113], v15 offset:4096
ds_read_b64 v[114:115], v15 offset:6144
ds_read_b64 v[116:117], v15 offset:8192
ds_read_b64 v[118:119], v15 offset:10240
ds_read_b64 v[120:121], v16
s_waitcnt lgkmcnt(0)
s_barrier
s_mov_b32 m0, 55296
s_nop 0
global_load_lds_dwordx4 v11, s[52:53] offset:0 sc1
global_load_lds_dwordx4 v12, s[52:53] offset:1024 sc1
global_load_lds_dwordx4 v13, s[52:53] offset:2048 sc1
s_add_u32 s52, s52, 221184
s_addc_u32 s53, s53, 0
s_mov_b32 m0, 63488
s_nop 0
global_load_lds_dwordx4 v14, s[38:39] offset:0 sc1
v_add_u32 v14, 37376, v14
v_lshlrev_b32 v126, 16, v108
v_add_f32 v122, v122, v124
v_and_b32 v127, 0xffff0000, v108
v_add_f32 v123, v123, v125
v_lshlrev_b32 v128, 16, v109
v_mul_f32 v122, 0x3ae38e39, v122
v_and_b32 v129, 0xffff0000, v109
v_mul_f32 v123, 0x3ae38e39, v123
v_lshlrev_b32 v130, 16, v110
v_fma_f32 v137, -v122, v122, v123
v_and_b32 v131, 0xffff0000, v110
v_max_f32 v137, 0, v137
v_lshlrev_b32 v132, 16, v111
v_cvt_pk_bf16_f32 v137, v137, v137
v_and_b32 v133, 0xffff0000, v111
v_lshlrev_b32 v137, 16, v137
v_add_f32 v126, v126, v130
v_add_f32 v137, 0x3727c5ac, v137
v_add_f32 v127, v127, v131
v_cvt_pk_bf16_f32 v137, v137, v137
v_add_f32 v128, v128, v132
v_lshlrev_b32 v137, 16, v137
v_add_f32 v129, v129, v133
v_sqrt_f32 v137, v137
v_lshlrev_b32 v130, 16, v112
v_cvt_pk_bf16_f32 v137, v137, v137
v_and_b32 v131, 0xffff0000, v112
v_lshlrev_b32 v137, 16, v137
v_lshlrev_b32 v132, 16, v113
v_rcp_f32 v136, v137
v_and_b32 v133, 0xffff0000, v113
v_mul_f32 v136, v136, v136
v_add_f32 v126, v126, v130
v_add_f32 v127, v127, v131
v_add_f32 v128, v128, v132
v_add_f32 v129, v129, v133
v_lshlrev_b32 v130, 16, v114
v_and_b32 v131, 0xffff0000, v114
v_lshlrev_b32 v132, 16, v115
v_and_b32 v133, 0xffff0000, v115
v_add_f32 v126, v126, v130
v_add_f32 v127, v127, v131
v_add_f32 v128, v128, v132
v_add_f32 v129, v129, v133
v_lshlrev_b32 v130, 16, v116
v_and_b32 v131, 0xffff0000, v116
v_lshlrev_b32 v132, 16, v117
v_and_b32 v133, 0xffff0000, v117
v_add_f32 v126, v126, v130
v_add_f32 v127, v127, v131
v_add_f32 v128, v128, v132
v_add_f32 v129, v129, v133
v_lshlrev_b32 v130, 16, v118
v_and_b32 v131, 0xffff0000, v118
v_lshlrev_b32 v132, 16, v119
v_and_b32 v133, 0xffff0000, v119
v_add_f32 v126, v126, v130
v_add_f32 v127, v127, v131
v_add_f32 v128, v128, v132
v_add_f32 v129, v129, v133
v_lshlrev_b32 v130, 16, v120
v_and_b32 v131, 0xffff0000, v120
v_lshlrev_b32 v132, 16, v121
v_and_b32 v133, 0xffff0000, v121
v_fma_f32 v126, v126, v136, v130
v_fma_f32 v127, v127, v136, v131
v_fma_f32 v128, v128, v136, v132
v_fma_f32 v129, v129, v136, v133
v_cvt_pk_bf16_f32 v134, v126, v127
v_cvt_pk_bf16_f32 v135, v128, v129
global_store_dwordx2 v18, v[134:135], s[48:49] sc1
v_subrev_u32 v18, 36864, v18
s_waitcnt vmcnt(0)
s_barrier
ds_read_b64 v[122:123], v17 offset:0
ds_read_b64 v[124:125], v17 offset:128
ds_read_b64 v[108:109], v15 offset:0
ds_read_b64 v[110:111], v15 offset:2048
ds_read_b64 v[112:113], v15 offset:4096
ds_read_b64 v[114:115], v15 offset:6144
ds_read_b64 v[116:117], v15 offset:8192
ds_read_b64 v[118:119], v15 offset:10240
ds_read_b64 v[120:121], v16
s_waitcnt lgkmcnt(6)
v_lshlrev_b32 v126, 16, v108
v_add_f32 v122, v122, v124
v_and_b32 v127, 0xffff0000, v108
v_add_f32 v123, v123, v125
v_lshlrev_b32 v128, 16, v109
v_mul_f32 v122, 0x3ae38e39, v122
v_and_b32 v129, 0xffff0000, v109
v_mul_f32 v123, 0x3ae38e39, v123
s_waitcnt lgkmcnt(5)
v_lshlrev_b32 v130, 16, v110
v_fma_f32 v137, -v122, v122, v123
v_and_b32 v131, 0xffff0000, v110
v_max_f32 v137, 0, v137
v_lshlrev_b32 v132, 16, v111
v_cvt_pk_bf16_f32 v137, v137, v137
v_and_b32 v133, 0xffff0000, v111
v_lshlrev_b32 v137, 16, v137
v_add_f32 v126, v126, v130
v_add_f32 v137, 0x3727c5ac, v137
v_add_f32 v127, v127, v131
v_cvt_pk_bf16_f32 v137, v137, v137
v_add_f32 v128, v128, v132
v_lshlrev_b32 v137, 16, v137
v_add_f32 v129, v129, v133
v_sqrt_f32 v137, v137
s_waitcnt lgkmcnt(4)
v_lshlrev_b32 v130, 16, v112
v_cvt_pk_bf16_f32 v137, v137, v137
v_and_b32 v131, 0xffff0000, v112
v_lshlrev_b32 v137, 16, v137
v_lshlrev_b32 v132, 16, v113
v_rcp_f32 v136, v137
v_and_b32 v133, 0xffff0000, v113
v_mul_f32 v136, v136, v136
v_add_f32 v126, v126, v130
v_add_f32 v127, v127, v131
v_add_f32 v128, v128, v132
v_add_f32 v129, v129, v133
s_waitcnt lgkmcnt(3)
v_lshlrev_b32 v130, 16, v114
v_and_b32 v131, 0xffff0000, v114
v_lshlrev_b32 v132, 16, v115
v_and_b32 v133, 0xffff0000, v115
v_add_f32 v126, v126, v130
v_add_f32 v127, v127, v131
v_add_f32 v128, v128, v132
v_add_f32 v129, v129, v133
s_waitcnt lgkmcnt(2)
v_lshlrev_b32 v130, 16, v116
v_and_b32 v131, 0xffff0000, v116
v_lshlrev_b32 v132, 16, v117
v_and_b32 v133, 0xffff0000, v117
v_add_f32 v126, v126, v130
v_add_f32 v127, v127, v131
v_add_f32 v128, v128, v132
v_add_f32 v129, v129, v133
s_waitcnt lgkmcnt(1)
v_lshlrev_b32 v130, 16, v118
v_and_b32 v131, 0xffff0000, v118
v_lshlrev_b32 v132, 16, v119
v_and_b32 v133, 0xffff0000, v119
v_add_f32 v126, v126, v130
v_add_f32 v127, v127, v131
v_add_f32 v128, v128, v132
v_add_f32 v129, v129, v133
s_waitcnt lgkmcnt(0)
v_lshlrev_b32 v130, 16, v120
v_and_b32 v131, 0xffff0000, v120
v_lshlrev_b32 v132, 16, v121
v_and_b32 v133, 0xffff0000, v121
v_fma_f32 v126, v126, v136, v130
v_fma_f32 v127, v127, v136, v131
v_fma_f32 v128, v128, v136, v132
v_fma_f32 v129, v129, v136, v133
v_cvt_pk_bf16_f32 v134, v126, v127
v_cvt_pk_bf16_f32 v135, v128, v129
global_store_dwordx2 v18, v[134:135], s[48:49] sc1
v_subrev_u32 v18, 36864, v18
s_endpgm
.Ldn_w4:
v_add_u32 v29, -1, v20
s_add_u32 m0, s51, 24576
s_mov_b32 s73, 27
s_mov_b32 s70, 32
s_waitcnt vmcnt(0) lgkmcnt(0)
s_nop 7
.p2align 6
.Ldn_top24:
s_waitcnt lgkmcnt(11)
v_mfma_f32_16x16x32_bf16 v[62:65], a[0:3], v[86:89], 0
ds_read_b128 v[98:101], v28 offset:3072
v_mfma_f32_16x16x32_bf16 v[66:69], a[4:7], v[86:89], 0
s_cmp_eq_u32 s70, s73
s_cbranch_scc1 .Ldn_hk21
.Ldn_hr22:
v_mfma_f32_16x16x32_bf16 v[70:73], a[8:11], v[86:89], 0
s_waitcnt lgkmcnt(9)
v_lshlrev_b32 v126, 16, v108
v_add_f32 v122, v122, v124
s_waitcnt lgkmcnt(7)
v_mfma_f32_16x16x32_bf16 v[62:65], a[12:15], v[90:93], v[62:65]
ds_read_b128 v[86:89], v28 offset:4096
v_cvt_pk_bf16_f32 v102, v74, v75
v_mfma_f32_16x16x32_bf16 v[66:69], a[16:19], v[90:93], v[66:69]
v_mfma_f32_16x16x32_bf16 v[70:73], a[20:23], v[90:93], v[70:73]
s_waitcnt vmcnt(7)
s_barrier
s_waitcnt lgkmcnt(2)
v_mfma_f32_16x16x32_bf16 v[62:65], a[24:27], v[94:97], v[62:65]
ds_read_b128 v[90:93], v28 offset:5120
v_cvt_pk_bf16_f32 v103, v76, v77
v_mfma_f32_16x16x32_bf16 v[66:69], a[28:31], v[94:97], v[66:69]
v_cvt_pk_bf16_f32 v104, v78, v79
v_cvt_pk_bf16_f32 v105, v80, v81
v_mfma_f32_16x16x32_bf16 v[70:73], a[32:35], v[94:97], v[70:73]
s_waitcnt lgkmcnt(2)
v_cvt_pk_bf16_f32 v106, v82, v83
v_cvt_pk_bf16_f32 v107, v84, v85
v_mfma_f32_16x16x32_bf16 v[62:65], a[36:39], v[98:101], v[62:65]
ds_read_b128 v[94:97], v28 offset:6144
v_and_b32 v127, 0xffff0000, v108
v_mfma_f32_16x16x32_bf16 v[66:69], a[40:43], v[98:101], v[66:69]
v_add_f32 v123, v123, v125
v_lshlrev_b32 v128, 16, v109
v_mfma_f32_16x16x32_bf16 v[70:73], a[44:47], v[98:101], v[70:73]
s_waitcnt lgkmcnt(2)
v_mul_f32 v122, 0x3ae38e39, v122
v_and_b32 v129, 0xffff0000, v109
v_mfma_f32_16x16x32_bf16 v[62:65], a[48:51], v[86:89], v[62:65]
ds_read_b128 v[98:101], v28 offset:7168
v_mul_f32 v123, 0x3ae38e39, v123
v_mfma_f32_16x16x32_bf16 v[66:69], a[52:55], v[86:89], v[66:69]
global_store_dwordx4 v9, v[102:105], s[38:39] sc1
v_mfma_f32_16x16x32_bf16 v[70:73], a[56:59], v[86:89], v[70:73]
s_waitcnt lgkmcnt(2)
v_add_u32 v9, 110592, v9
v_lshlrev_b32 v130, 16, v110
v_mfma_f32_16x16x32_bf16 v[62:65], a[60:63], v[90:93], v[62:65]
ds_read_b128 v[86:89], v28 offset:8192
v_fma_f32 v137, -v122, v122, v123
v_mfma_f32_16x16x32_bf16 v[66:69], a[64:67], v[90:93], v[66:69]
global_store_dwordx2 v10, v[106:107], s[38:39] sc1
v_mfma_f32_16x16x32_bf16 v[70:73], a[68:71], v[90:93], v[70:73]
s_waitcnt lgkmcnt(2)
v_add_u32 v10, 110592, v10
v_and_b32 v131, 0xffff0000, v110
v_mfma_f32_16x16x32_bf16 v[62:65], a[72:75], v[94:97], v[62:65]
ds_read_b128 v[90:93], v28 offset:9216
v_max_f32 v137, 0, v137
v_mfma_f32_16x16x32_bf16 v[66:69], a[76:79], v[94:97], v[66:69]
v_lshlrev_b32 v132, 16, v111
v_cvt_pk_bf16_f32 v137, v137, v137
v_mfma_f32_16x16x32_bf16 v[70:73], a[80:83], v[94:97], v[70:73]
s_waitcnt lgkmcnt(2)
v_and_b32 v133, 0xffff0000, v111
v_lshlrev_b32 v137, 16, v137
v_mfma_f32_16x16x32_bf16 v[62:65], a[84:87], v[98:101], v[62:65]
ds_read_b128 v[94:97], v28 offset:10240
v_add_f32 v126, v126, v130
v_mfma_f32_16x16x32_bf16 v[66:69], a[88:91], v[98:101], v[66:69]
v_add_f32 v137, 0x3727c5ac, v137
v_add_f32 v127, v127, v131
v_mfma_f32_16x16x32_bf16 v[70:73], a[92:95], v[98:101], v[70:73]
s_waitcnt lgkmcnt(2)
v_cvt_pk_bf16_f32 v137, v137, v137
v_add_f32 v128, v128, v132
v_mfma_f32_16x16x32_bf16 v[62:65], a[96:99], v[86:89], v[62:65]
ds_read_b128 v[98:101], v28 offset:11264
v_lshlrev_b32 v137, 16, v137
v_mfma_f32_16x16x32_bf16 v[66:69], a[100:103], v[86:89], v[66:69]
v_add_f32 v129, v129, v133
v_mfma_f32_16x16x32_bf16 v[70:73], a[104:107], v[86:89], v[70:73]
s_waitcnt lgkmcnt(2)
v_sqrt_f32 v137, v137
v_mfma_f32_16x16x32_bf16 v[62:65], a[108:111], v[90:93], v[62:65]
ds_read_b128 v[86:89], v28 offset:12288
v_lshlrev_b32 v130, 16, v112
v_mfma_f32_16x16x32_bf16 v[66:69], a[112:115], v[90:93], v[66:69]
v_cvt_pk_bf16_f32 v137, v137, v137
v_and_b32 v131, 0xffff0000, v112
v_mfma_f32_16x16x32_bf16 v[70:73], a[116:119], v[90:93], v[70:73]
s_waitcnt lgkmcnt(2)
v_lshlrev_b32 v137, 16, v137
v_lshlrev_b32 v132, 16, v113
v_mfma_f32_16x16x32_bf16 v[62:65], a[120:123], v[94:97], v[62:65]
ds_read_b128 v[90:93], v28 offset:13312
v_mfma_f32_16x16x32_bf16 v[66:69], a[124:127], v[94:97], v[66:69]
s_waitcnt vmcnt(6)
v_sub_u32 v29, v29, v20
v_cmp_lt_u32 vcc, 0x7f, v29
v_mfma_f32_16x16x32_bf16 v[70:73], a[128:131], v[94:97], v[70:73]
s_waitcnt lgkmcnt(2)
v_rcp_f32 v136, v137
v_mfma_f32_16x16x32_bf16 v[62:65], a[132:135], v[98:101], v[62:65]
s_xor_b32 s51, s51, 0x10000
ds_read_b128 v[94:97], v28 offset:14336
v_and_b32 v133, 0xffff0000, v113
v_mfma_f32_16x16x32_bf16 v[66:69], a[136:139], v[98:101], v[66:69]
s_cbranch_vccnz .Ldn_sp17
.Ldn_rt18:
s_add_u32 m0, s51, 0
v_mfma_f32_16x16x32_bf16 v[70:73], a[140:143], v[98:101], v[70:73]
s_waitcnt lgkmcnt(2)
v_mfma_f32_16x16x32_bf16 v[62:65], a[144:147], v[86:89], v[62:65]
s_barrier
ds_read_b128 v[98:101], v28 offset:15360
v_mfma_f32_16x16x32_bf16 v[66:69], a[148:151], v[86:89], v[66:69]
v_add_u32 v20, 2, v20
v_mul_f32 v136, v136, v136
v_mfma_f32_16x16x32_bf16 v[70:73], a[152:155], v[86:89], v[70:73]
s_waitcnt lgkmcnt(2)
v_add_f32 v126, v126, v130
v_add_f32 v127, v127, v131
v_mfma_f32_16x16x32_bf16 v[62:65], a[156:159], v[90:93], v[62:65]
ds_read_b128 v[86:89], v28 offset:16384
v_add_f32 v128, v128, v132
v_mfma_f32_16x16x32_bf16 v[66:69], a[160:163], v[90:93], v[66:69]
global_load_lds_dwordx4 v8, s[38:39] offset:-2048 sc1
v_add_f32 v129, v129, v133
v_mfma_f32_16x16x32_bf16 v[70:73], a[164:167], v[90:93], v[70:73]
s_waitcnt lgkmcnt(2)
v_lshlrev_b32 v130, 16, v114
v_and_b32 v131, 0xffff0000, v114
v_mfma_f32_16x16x32_bf16 v[62:65], a[168:171], v[94:97], v[62:65]
ds_read_b128 v[90:93], v28 offset:17408
v_lshlrev_b32 v132, 16, v115
)"
R"(v_mfma_f32_16x16x32_bf16 v[66:69], a[172:175], v[94:97], v[66:69]
global_load_lds_dwordx4 v8, s[38:39] offset:-1024 sc1
v_and_b32 v133, 0xffff0000, v115
v_mfma_f32_16x16x32_bf16 v[70:73], a[176:179], v[94:97], v[70:73]
s_waitcnt lgkmcnt(2)
global_load_dword v29, v21, s[38:39] sc1
v_add_f32 v126, v126, v130
v_mfma_f32_16x16x32_bf16 v[62:65], a[180:183], v[98:101], v[62:65]
ds_read_b128 v[94:97], v28 offset:18432
v_add_f32 v127, v127, v131
v_mfma_f32_16x16x32_bf16 v[66:69], a[184:187], v[98:101], v[66:69]
global_load_lds_dwordx4 v8, s[38:39] offset:0 sc1
v_add_f32 v128, v128, v132
v_mfma_f32_16x16x32_bf16 v[70:73], a[188:191], v[98:101], v[70:73]
s_waitcnt lgkmcnt(2)
v_add_f32 v129, v129, v133
v_lshlrev_b32 v130, 16, v116
v_mfma_f32_16x16x32_bf16 v[62:65], a[192:195], v[86:89], v[62:65]
ds_read_b128 v[98:101], v28 offset:19456
v_and_b32 v131, 0xffff0000, v116
v_mfma_f32_16x16x32_bf16 v[66:69], a[196:199], v[86:89], v[66:69]
global_load_lds_dwordx4 v8, s[38:39] offset:1024 sc1
v_lshlrev_b32 v132, 16, v117
v_mfma_f32_16x16x32_bf16 v[70:73], a[200:203], v[86:89], v[70:73]
s_waitcnt lgkmcnt(2)
v_and_b32 v133, 0xffff0000, v117
v_add_f32 v126, v126, v130
v_mfma_f32_16x16x32_bf16 v[62:65], a[204:207], v[90:93], v[62:65]
ds_read_b128 v[86:89], v28 offset:20480
v_add_f32 v127, v127, v131
v_mfma_f32_16x16x32_bf16 v[66:69], a[208:211], v[90:93], v[66:69]
global_load_lds_dwordx4 v8, s[38:39] offset:2048 sc1
v_add_f32 v128, v128, v132
v_mfma_f32_16x16x32_bf16 v[70:73], a[212:215], v[90:93], v[70:73]
s_waitcnt lgkmcnt(2)
v_add_f32 v129, v129, v133
v_lshlrev_b32 v130, 16, v118
v_mfma_f32_16x16x32_bf16 v[62:65], a[216:219], v[94:97], v[62:65]
ds_read_b128 v[90:93], v28 offset:21504
v_and_b32 v131, 0xffff0000, v118
v_mfma_f32_16x16x32_bf16 v[66:69], a[220:223], v[94:97], v[66:69]
global_load_lds_dwordx4 v8, s[38:39] offset:3072 sc1
v_lshlrev_b32 v132, 16, v119
v_mfma_f32_16x16x32_bf16 v[70:73], a[224:227], v[94:97], v[70:73]
s_waitcnt lgkmcnt(2)
v_add_u32 v8, 147456, v8
v_and_b32 v133, 0xffff0000, v119
v_mfma_f32_16x16x32_bf16 v[62:65], a[228:231], v[98:101], v[62:65]
ds_read_b128 v[94:97], v28 offset:22528
v_add_f32 v126, v126, v130
v_mfma_f32_16x16x32_bf16 v[66:69], a[232:235], v[98:101], v[66:69]
s_mov_b32 m0, 58368
s_nop 0
global_load_lds_dwordx4 v11, s[52:53] offset:0 sc1
v_add_f32 v127, v127, v131
v_mfma_f32_16x16x32_bf16 v[70:73], a[236:239], v[98:101], v[70:73]
s_waitcnt lgkmcnt(2)
v_add_f32 v128, v128, v132
v_add_f32 v129, v129, v133
v_mfma_f32_16x16x32_bf16 v[62:65], a[240:243], v[86:89], v[62:65]
ds_read_b128 v[98:101], v28 offset:23552
global_load_lds_dwordx4 v12, s[52:53] offset:1024 sc1
v_mfma_f32_16x16x32_bf16 v[66:69], a[244:247], v[86:89], v[66:69]
v_lshlrev_b32 v130, 16, v120
v_and_b32 v131, 0xffff0000, v120
v_mfma_f32_16x16x32_bf16 v[70:73], a[248:251], v[86:89], v[70:73]
s_waitcnt lgkmcnt(2)
v_mfma_f32_16x16x32_bf16 v[62:65], a[252:255], v[90:93], v[62:65]
s_waitcnt vmcnt(11)
s_barrier
ds_read_b128 v[86:89], v28 offset:24576
v_mfma_f32_16x16x32_bf16 v[66:69], v[30:33], v[90:93], v[66:69]
global_load_lds_dwordx4 v13, s[52:53] offset:2048 sc1
v_lshlrev_b32 v132, 16, v121
v_mfma_f32_16x16x32_bf16 v[70:73], v[34:37], v[90:93], v[70:73]
s_waitcnt lgkmcnt(2)
v_and_b32 v133, 0xffff0000, v121
v_fma_f32 v126, v126, v136, v130
v_mfma_f32_16x16x32_bf16 v[62:65], v[38:41], v[94:97], v[62:65]
ds_read_b128 v[90:93], v28 offset:25600
s_add_u32 s52, s52, 221184
s_addc_u32 s53, s53, 0
v_mfma_f32_16x16x32_bf16 v[66:69], v[42:45], v[94:97], v[66:69]
v_fma_f32 v127, v127, v136, v131
v_fma_f32 v128, v128, v136, v132
v_mfma_f32_16x16x32_bf16 v[70:73], v[46:49], v[94:97], v[70:73]
s_waitcnt lgkmcnt(2)
v_fma_f32 v129, v129, v136, v133
v_cvt_pk_bf16_f32 v134, v126, v127
v_mfma_f32_16x16x32_bf16 v[62:65], v[50:53], v[98:101], v[62:65]
ds_read_b128 v[94:97], v28 offset:26624
v_cvt_pk_bf16_f32 v135, v128, v129
v_mfma_f32_16x16x32_bf16 v[66:69], v[54:57], v[98:101], v[66:69]
global_store_dwordx2 v18, v[134:135], s[48:49] sc1
v_mfma_f32_16x16x32_bf16 v[70:73], v[58:61], v[98:101], v[70:73]
s_waitcnt lgkmcnt(2)
v_subrev_u32 v18, 36864, v18
v_mfma_f32_16x16x32_bf16 v[74:77], a[0:3], v[86:89], 0
ds_read_b128 v[98:101], v28 offset:27648
v_mfma_f32_16x16x32_bf16 v[78:81], a[4:7], v[86:89], 0
v_mfma_f32_16x16x32_bf16 v[82:85], a[8:11], v[86:89], 0
s_waitcnt lgkmcnt(2)
v_mfma_f32_16x16x32_bf16 v[74:77], a[12:15], v[90:93], v[74:77]
ds_read_b128 v[86:89], v28 offset:28672
v_cvt_pk_bf16_f32 v102, v62, v63
v_mfma_f32_16x16x32_bf16 v[78:81], a[16:19], v[90:93], v[78:81]
v_mfma_f32_16x16x32_bf16 v[82:85], a[20:23], v[90:93], v[82:85]
s_waitcnt vmcnt(11)
s_barrier
s_waitcnt lgkmcnt(2)
v_mfma_f32_16x16x32_bf16 v[74:77], a[24:27], v[94:97], v[74:77]
ds_read_b128 v[90:93], v28 offset:29696
v_cvt_pk_bf16_f32 v103, v64, v65
v_mfma_f32_16x16x32_bf16 v[78:81], a[28:31], v[94:97], v[78:81]
v_cvt_pk_bf16_f32 v104, v66, v67
v_cvt_pk_bf16_f32 v105, v68, v69
v_mfma_f32_16x16x32_bf16 v[82:85], a[32:35], v[94:97], v[82:85]
s_waitcnt lgkmcnt(2)
v_cvt_pk_bf16_f32 v106, v70, v71
v_cvt_pk_bf16_f32 v107, v72, v73
v_mfma_f32_16x16x32_bf16 v[74:77], a[36:39], v[98:101], v[74:77]
ds_read_b128 v[94:97], v28 offset:30720
v_mfma_f32_16x16x32_bf16 v[78:81], a[40:43], v[98:101], v[78:81]
v_mfma_f32_16x16x32_bf16 v[82:85], a[44:47], v[98:101], v[82:85]
s_waitcnt lgkmcnt(2)
v_mfma_f32_16x16x32_bf16 v[74:77], a[48:51], v[86:89], v[74:77]
ds_read_b128 v[98:101], v28 offset:31744
v_mfma_f32_16x16x32_bf16 v[78:81], a[52:55], v[86:89], v[78:81]
global_store_dwordx4 v9, v[102:105], s[38:39] sc1
v_mfma_f32_16x16x32_bf16 v[82:85], a[56:59], v[86:89], v[82:85]
s_waitcnt lgkmcnt(2)
v_add_u32 v9, 110592, v9
v_mfma_f32_16x16x32_bf16 v[74:77], a[60:63], v[90:93], v[74:77]
ds_read_b128 v[86:89], v28 offset:32768
v_mfma_f32_16x16x32_bf16 v[78:81], a[64:67], v[90:93], v[78:81]
global_store_dwordx2 v10, v[106:107], s[38:39] sc1
v_mfma_f32_16x16x32_bf16 v[82:85], a[68:71], v[90:93], v[82:85]
s_waitcnt lgkmcnt(2)
v_add_u32 v10, 110592, v10
v_mfma_f32_16x16x32_bf16 v[74:77], a[72:75], v[94:97], v[74:77]
ds_read_b128 v[90:93], v28 offset:33792
v_mfma_f32_16x16x32_bf16 v[78:81], a[76:79], v[94:97], v[78:81]
v_mfma_f32_16x16x32_bf16 v[82:85], a[80:83], v[94:97], v[82:85]
s_waitcnt lgkmcnt(2)
v_mfma_f32_16x16x32_bf16 v[74:77], a[84:87], v[98:101], v[74:77]
ds_read_b128 v[94:97], v28 offset:34816
v_mfma_f32_16x16x32_bf16 v[78:81], a[88:91], v[98:101], v[78:81]
v_mfma_f32_16x16x32_bf16 v[82:85], a[92:95], v[98:101], v[82:85]
s_waitcnt lgkmcnt(2)
v_mfma_f32_16x16x32_bf16 v[74:77], a[96:99], v[86:89], v[74:77]
ds_read_b128 v[98:101], v28 offset:35840
v_mfma_f32_16x16x32_bf16 v[78:81], a[100:103], v[86:89], v[78:81]
v_mfma_f32_16x16x32_bf16 v[82:85], a[104:107], v[86:89], v[82:85]
s_waitcnt lgkmcnt(2)
v_mfma_f32_16x16x32_bf16 v[74:77], a[108:111], v[90:93], v[74:77]
ds_read_b128 v[86:89], v28 offset:36864
v_mfma_f32_16x16x32_bf16 v[78:81], a[112:115], v[90:93], v[78:81]
v_mfma_f32_16x16x32_bf16 v[82:85], a[116:119], v[90:93], v[82:85]
s_waitcnt lgkmcnt(2)
v_mfma_f32_16x16x32_bf16 v[74:77], a[120:123], v[94:97], v[74:77]
ds_read_b128 v[90:93], v28 offset:37888
v_mfma_f32_16x16x32_bf16 v[78:81], a[124:127], v[94:97], v[78:81]
s_waitcnt vmcnt(10)
v_sub_u32 v29, v29, v22
v_cmp_lt_u32 vcc, 0x7f, v29
v_mfma_f32_16x16x32_bf16 v[82:85], a[128:131], v[94:97], v[82:85]
s_waitcnt lgkmcnt(2)
v_mfma_f32_16x16x32_bf16 v[74:77], a[132:135], v[98:101], v[74:77]
ds_read_b128 v[94:97], v28 offset:38912
v_mfma_f32_16x16x32_bf16 v[78:81], a[136:139], v[98:101], v[78:81]
s_cbranch_vccnz .Ldn_sp19
.Ldn_rt20:
s_add_u32 m0, s51, 24576
v_mfma_f32_16x16x32_bf16 v[82:85], a[140:143], v[98:101], v[82:85]
s_waitcnt lgkmcnt(2)
v_mfma_f32_16x16x32_bf16 v[74:77], a[144:147], v[86:89], v[74:77]
s_barrier
ds_read_b128 v[98:101], v28 offset:39936
v_mfma_f32_16x16x32_bf16 v[78:81], a[148:151], v[86:89], v[78:81]
v_add_u32 v22, 2, v22
v_mfma_f32_16x16x32_bf16 v[82:85], a[152:155], v[86:89], v[82:85]
s_waitcnt lgkmcnt(2)
v_mfma_f32_16x16x32_bf16 v[74:77], a[156:159], v[90:93], v[74:77]
ds_read_b128 v[86:89], v28 offset:40960
v_mfma_f32_16x16x32_bf16 v[78:81], a[160:163], v[90:93], v[78:81]
global_load_lds_dwordx4 v8, s[38:39] offset:-2048 sc1
v_mfma_f32_16x16x32_bf16 v[82:85], a[164:167], v[90:93], v[82:85]
s_waitcnt lgkmcnt(2)
v_mfma_f32_16x16x32_bf16 v[74:77], a[168:171], v[94:97], v[74:77]
ds_read_b128 v[90:93], v28 offset:41984
v_mfma_f32_16x16x32_bf16 v[78:81], a[172:175], v[94:97], v[78:81]
global_load_lds_dwordx4 v8, s[38:39] offset:-1024 sc1
v_mfma_f32_16x16x32_bf16 v[82:85], a[176:179], v[94:97], v[82:85]
s_waitcnt lgkmcnt(2)
global_load_dword v29, v19, s[38:39] sc1
v_mfma_f32_16x16x32_bf16 v[74:77], a[180:183], v[98:101], v[74:77]
ds_read_b128 v[94:97], v28 offset:43008
v_mfma_f32_16x16x32_bf16 v[78:81], a[184:187], v[98:101], v[78:81]
global_load_lds_dwordx4 v8, s[38:39] offset:0 sc1
v_mfma_f32_16x16x32_bf16 v[82:85], a[188:191], v[98:101], v[82:85]
s_waitcnt lgkmcnt(2)
v_mfma_f32_16x16x32_bf16 v[74:77], a[192:195], v[86:89], v[74:77]
ds_read_b128 v[98:101], v28 offset:44032
v_mfma_f32_16x16x32_bf16 v[78:81], a[196:199], v[86:89], v[78:81]
global_load_lds_dwordx4 v8, s[38:39] offset:1024 sc1
v_mfma_f32_16x16x32_bf16 v[82:85], a[200:203], v[86:89], v[82:85]
s_waitcnt lgkmcnt(2)
v_mfma_f32_16x16x32_bf16 v[74:77], a[204:207], v[90:93], v[74:77]
ds_read_b128 v[86:89], v28 offset:45056
v_mfma_f32_16x16x32_bf16 v[78:81], a[208:211], v[90:93], v[78:81]
global_load_lds_dwordx4 v8, s[38:39] offset:2048 sc1
v_mfma_f32_16x16x32_bf16 v[82:85], a[212:215], v[90:93], v[82:85]
s_waitcnt lgkmcnt(2)
v_mfma_f32_16x16x32_bf16 v[74:77], a[216:219], v[94:97], v[74:77]
ds_read_b128 v[90:93], v28 offset:46080
v_mfma_f32_16x16x32_bf16 v[78:81], a[220:223], v[94:97], v[78:81]
global_load_lds_dwordx4 v8, s[38:39] offset:3072 sc1
v_mfma_f32_16x16x32_bf16 v[82:85], a[224:227], v[94:97], v[82:85]
s_waitcnt lgkmcnt(2)
v_add_u32 v8, 147456, v8
v_mfma_f32_16x16x32_bf16 v[74:77], a[228:231], v[98:101], v[74:77]
ds_read_b128 v[94:97], v28 offset:47104
v_mfma_f32_16x16x32_bf16 v[78:81], a[232:235], v[98:101], v[78:81]
v_mfma_f32_16x16x32_bf16 v[82:85], a[236:239], v[98:101], v[82:85]
s_waitcnt lgkmcnt(2)
v_mfma_f32_16x16x32_bf16 v[74:77], a[240:243], v[86:89], v[74:77]
ds_read_b128 v[98:101], v28 offset:48128
v_mfma_f32_16x16x32_bf16 v[78:81], a[244:247], v[86:89], v[78:81]
v_xor_b32 v28, 0x10000, v28
v_mfma_f32_16x16x32_bf16 v[82:85], a[248:251], v[86:89], v[82:85]
s_waitcnt lgkmcnt(2)
v_mfma_f32_16x16x32_bf16 v[74:77], a[252:255], v[90:93], v[74:77]
s_waitcnt vmcnt(9)
s_barrier
ds_read_b128 v[86:89], v28 offset:0
v_mfma_f32_16x16x32_bf16 v[78:81], v[30:33], v[90:93], v[78:81]
ds_read_b64 v[122:123], v17 offset:0
ds_read_b64 v[124:125], v17 offset:128
v_mfma_f32_16x16x32_bf16 v[82:85], v[34:37], v[90:93], v[82:85]
s_waitcnt lgkmcnt(4)
ds_read_b64 v[108:109], v15 offset:0
ds_read_b64 v[110:111], v15 offset:2048
v_mfma_f32_16x16x32_bf16 v[74:77], v[38:41], v[94:97], v[74:77]
ds_read_b128 v[90:93], v28 offset:1024
ds_read_b64 v[112:113], v15 offset:4096
v_mfma_f32_16x16x32_bf16 v[78:81], v[42:45], v[94:97], v[78:81]
ds_read_b64 v[114:115], v15 offset:6144
ds_read_b64 v[116:117], v15 offset:8192
v_mfma_f32_16x16x32_bf16 v[82:85], v[46:49], v[94:97], v[82:85]
s_waitcnt lgkmcnt(9)
ds_read_b64 v[118:119], v15 offset:10240
ds_read_b64 v[120:121], v16
v_mfma_f32_16x16x32_bf16 v[74:77], v[50:53], v[98:101], v[74:77]
ds_read_b128 v[94:97], v28 offset:2048
v_mfma_f32_16x16x32_bf16 v[78:81], v[54:57], v[98:101], v[78:81]
v_mfma_f32_16x16x32_bf16 v[82:85], v[58:61], v[98:101], v[82:85]
s_sub_u32 s70, s70, 1
s_cbranch_scc0 .Ldn_top24
s_waitcnt lgkmcnt(0)
ds_read_b64 v[122:123], v17 offset:0
ds_read_b64 v[124:125], v17 offset:128
ds_read_b64 v[108:109], v15 offset:0
ds_read_b64 v[110:111], v15 offset:2048
ds_read_b64 v[112:113], v15 offset:4096
ds_read_b64 v[114:115], v15 offset:6144
ds_read_b64 v[116:117], v15 offset:8192
ds_read_b64 v[118:119], v15 offset:10240
ds_read_b64 v[120:121], v16
v_cvt_pk_bf16_f32 v102, v74, v75
v_cvt_pk_bf16_f32 v103, v76, v77
v_cvt_pk_bf16_f32 v104, v78, v79
v_cvt_pk_bf16_f32 v105, v80, v81
v_cvt_pk_bf16_f32 v106, v82, v83
v_cvt_pk_bf16_f32 v107, v84, v85
global_store_dwordx4 v9, v[102:105], s[38:39] sc1
v_add_u32 v9, 110592, v9
global_store_dwordx2 v10, v[106:107], s[38:39] sc1
v_add_u32 v10, 110592, v10
s_waitcnt vmcnt(2)
s_barrier
global_load_dword v29, v19, s[38:39] sc1
s_waitcnt vmcnt(0)
v_sub_u32 v29, v29, v23
v_cmp_lt_u32 vcc, 0x7f, v29
s_cbranch_vccnz .Ldn_sp25
.Ldn_rt26:
s_waitcnt lgkmcnt(6)
v_lshlrev_b32 v126, 16, v108
v_add_f32 v122, v122, v124
v_and_b32 v127, 0xffff0000, v108
v_add_f32 v123, v123, v125
v_lshlrev_b32 v128, 16, v109
v_mul_f32 v122, 0x3ae38e39, v122
v_and_b32 v129, 0xffff0000, v109
v_mul_f32 v123, 0x3ae38e39, v123
s_waitcnt lgkmcnt(5)
v_lshlrev_b32 v130, 16, v110
v_fma_f32 v137, -v122, v122, v123
v_and_b32 v131, 0xffff0000, v110
v_max_f32 v137, 0, v137
v_lshlrev_b32 v132, 16, v111
v_cvt_pk_bf16_f32 v137, v137, v137
v_and_b32 v133, 0xffff0000, v111
v_lshlrev_b32 v137, 16, v137
v_add_f32 v126, v126, v130
v_add_f32 v137, 0x3727c5ac, v137
v_add_f32 v127, v127, v131
v_cvt_pk_bf16_f32 v137, v137, v137
v_add_f32 v128, v128, v132
v_lshlrev_b32 v137, 16, v137
v_add_f32 v129, v129, v133
v_sqrt_f32 v137, v137
s_waitcnt lgkmcnt(4)
v_lshlrev_b32 v130, 16, v112
v_cvt_pk_bf16_f32 v137, v137, v137
v_and_b32 v131, 0xffff0000, v112
v_lshlrev_b32 v137, 16, v137
v_lshlrev_b32 v132, 16, v113
v_rcp_f32 v136, v137
v_and_b32 v133, 0xffff0000, v113
v_mul_f32 v136, v136, v136
v_add_f32 v126, v126, v130
v_add_f32 v127, v127, v131
v_add_f32 v128, v128, v132
v_add_f32 v129, v129, v133
s_waitcnt lgkmcnt(3)
v_lshlrev_b32 v130, 16, v114
v_and_b32 v131, 0xffff0000, v114
v_lshlrev_b32 v132, 16, v115
v_and_b32 v133, 0xffff0000, v115
v_add_f32 v126, v126, v130
v_add_f32 v127, v127, v131
v_add_f32 v128, v128, v132
v_add_f32 v129, v129, v133
s_waitcnt lgkmcnt(2)
v_lshlrev_b32 v130, 16, v116
v_and_b32 v131, 0xffff0000, v116
v_lshlrev_b32 v132, 16, v117
v_and_b32 v133, 0xffff0000, v117
v_add_f32 v126, v126, v130
v_add_f32 v127, v127, v131
v_add_f32 v128, v128, v132
v_add_f32 v129, v129, v133
s_waitcnt lgkmcnt(1)
v_lshlrev_b32 v130, 16, v118
v_and_b32 v131, 0xffff0000, v118
v_lshlrev_b32 v132, 16, v119
v_and_b32 v133, 0xffff0000, v119
v_add_f32 v126, v126, v130
v_add_f32 v127, v127, v131
v_add_f32 v128, v128, v132
v_add_f32 v129, v129, v133
s_waitcnt lgkmcnt(0)
v_lshlrev_b32 v130, 16, v120
v_and_b32 v131, 0xffff0000, v120
v_lshlrev_b32 v132, 16, v121
v_and_b32 v133, 0xffff0000, v121
v_fma_f32 v126, v126, v136, v130
v_fma_f32 v127, v127, v136, v131
v_fma_f32 v128, v128, v136, v132
v_fma_f32 v129, v129, v136, v133
v_cvt_pk_bf16_f32 v134, v126, v127
v_cvt_pk_bf16_f32 v135, v128, v129
s_waitcnt vmcnt(0)
s_barrier
s_mov_b32 m0, 58368
s_nop 0
global_load_lds_dwordx4 v11, s[52:53] offset:0 sc1
global_load_lds_dwordx4 v12, s[52:53] offset:1024 sc1
global_load_lds_dwordx4 v13, s[52:53] offset:2048 sc1
s_add_u32 s52, s52, 221184
s_addc_u32 s53, s53, 0
global_store_dwordx2 v18, v[134:135], s[48:49] sc1
v_subrev_u32 v18, 36864, v18
s_waitcnt vmcnt(0)
s_barrier
ds_read_b64 v[122:123], v17 offset:0
ds_read_b64 v[124:125], v17 offset:128
ds_read_b64 v[108:109], v15 offset:0
ds_read_b64 v[110:111], v15 offset:2048
ds_read_b64 v[112:113], v15 offset:4096
ds_read_b64 v[114:115], v15 offset:6144
ds_read_b64 v[116:117], v15 offset:8192
ds_read_b64 v[118:119], v15 offset:10240
ds_read_b64 v[120:121], v16
v_add_u32 v23, 2, v23
global_load_dword v29, v19, s[38:39] sc1
s_waitcnt vmcnt(0)
v_sub_u32 v29, v29, v23
v_cmp_lt_u32 vcc, 0x7f, v29
s_cbranch_vccnz .Ldn_sp27
.Ldn_rt28:
s_waitcnt lgkmcnt(0)
s_barrier
s_mov_b32 m0, 58368
s_nop 0
global_load_lds_dwordx4 v11, s[52:53] offset:0 sc1
global_load_lds_dwordx4 v12, s[52:53] offset:1024 sc1
global_load_lds_dwordx4 v13, s[52:53] offset:2048 sc1
s_add_u32 s52, s52, 221184
)"
R"(s_addc_u32 s53, s53, 0
v_lshlrev_b32 v126, 16, v108
v_add_f32 v122, v122, v124
v_and_b32 v127, 0xffff0000, v108
v_add_f32 v123, v123, v125
v_lshlrev_b32 v128, 16, v109
v_mul_f32 v122, 0x3ae38e39, v122
v_and_b32 v129, 0xffff0000, v109
v_mul_f32 v123, 0x3ae38e39, v123
v_lshlrev_b32 v130, 16, v110
v_fma_f32 v137, -v122, v122, v123
v_and_b32 v131, 0xffff0000, v110
v_max_f32 v137, 0, v137
v_lshlrev_b32 v132, 16, v111
v_cvt_pk_bf16_f32 v137, v137, v137
v_and_b32 v133, 0xffff0000, v111
v_lshlrev_b32 v137, 16, v137
v_add_f32 v126, v126, v130
v_add_f32 v137, 0x3727c5ac, v137
v_add_f32 v127, v127, v131
v_cvt_pk_bf16_f32 v137, v137, v137
v_add_f32 v128, v128, v132
v_lshlrev_b32 v137, 16, v137
v_add_f32 v129, v129, v133
v_sqrt_f32 v137, v137
v_lshlrev_b32 v130, 16, v112
v_cvt_pk_bf16_f32 v137, v137, v137
v_and_b32 v131, 0xffff0000, v112
v_lshlrev_b32 v137, 16, v137
v_lshlrev_b32 v132, 16, v113
v_rcp_f32 v136, v137
v_and_b32 v133, 0xffff0000, v113
v_mul_f32 v136, v136, v136
v_add_f32 v126, v126, v130
v_add_f32 v127, v127, v131
v_add_f32 v128, v128, v132
v_add_f32 v129, v129, v133
v_lshlrev_b32 v130, 16, v114
v_and_b32 v131, 0xffff0000, v114
v_lshlrev_b32 v132, 16, v115
v_and_b32 v133, 0xffff0000, v115
v_add_f32 v126, v126, v130
v_add_f32 v127, v127, v131
v_add_f32 v128, v128, v132
v_add_f32 v129, v129, v133
v_lshlrev_b32 v130, 16, v116
v_and_b32 v131, 0xffff0000, v116
v_lshlrev_b32 v132, 16, v117
v_and_b32 v133, 0xffff0000, v117
v_add_f32 v126, v126, v130
v_add_f32 v127, v127, v131
v_add_f32 v128, v128, v132
v_add_f32 v129, v129, v133
v_lshlrev_b32 v130, 16, v118
v_and_b32 v131, 0xffff0000, v118
v_lshlrev_b32 v132, 16, v119
v_and_b32 v133, 0xffff0000, v119
v_add_f32 v126, v126, v130
v_add_f32 v127, v127, v131
v_add_f32 v128, v128, v132
v_add_f32 v129, v129, v133
v_lshlrev_b32 v130, 16, v120
v_and_b32 v131, 0xffff0000, v120
v_lshlrev_b32 v132, 16, v121
v_and_b32 v133, 0xffff0000, v121
v_fma_f32 v126, v126, v136, v130
v_fma_f32 v127, v127, v136, v131
v_fma_f32 v128, v128, v136, v132
v_fma_f32 v129, v129, v136, v133
v_cvt_pk_bf16_f32 v134, v126, v127
v_cvt_pk_bf16_f32 v135, v128, v129
global_store_dwordx2 v18, v[134:135], s[48:49] sc1
v_subrev_u32 v18, 36864, v18
s_waitcnt vmcnt(0)
s_barrier
ds_read_b64 v[122:123], v17 offset:0
ds_read_b64 v[124:125], v17 offset:128
ds_read_b64 v[108:109], v15 offset:0
ds_read_b64 v[110:111], v15 offset:2048
ds_read_b64 v[112:113], v15 offset:4096
ds_read_b64 v[114:115], v15 offset:6144
ds_read_b64 v[116:117], v15 offset:8192
ds_read_b64 v[118:119], v15 offset:10240
ds_read_b64 v[120:121], v16
v_add_u32 v23, 2, v23
global_load_dword v29, v19, s[38:39] sc1
s_waitcnt vmcnt(0)
v_sub_u32 v29, v29, v23
v_cmp_lt_u32 vcc, 0x7f, v29
s_cbranch_vccnz .Ldn_sp29
.Ldn_rt30:
s_waitcnt lgkmcnt(0)
s_barrier
s_mov_b32 m0, 58368
s_nop 0
global_load_lds_dwordx4 v11, s[52:53] offset:0 sc1
global_load_lds_dwordx4 v12, s[52:53] offset:1024 sc1
global_load_lds_dwordx4 v13, s[52:53] offset:2048 sc1
s_add_u32 s52, s52, 221184
s_addc_u32 s53, s53, 0
v_lshlrev_b32 v126, 16, v108
v_add_f32 v122, v122, v124
v_and_b32 v127, 0xffff0000, v108
v_add_f32 v123, v123, v125
v_lshlrev_b32 v128, 16, v109
v_mul_f32 v122, 0x3ae38e39, v122
v_and_b32 v129, 0xffff0000, v109
v_mul_f32 v123, 0x3ae38e39, v123
v_lshlrev_b32 v130, 16, v110
v_fma_f32 v137, -v122, v122, v123
v_and_b32 v131, 0xffff0000, v110
v_max_f32 v137, 0, v137
v_lshlrev_b32 v132, 16, v111
v_cvt_pk_bf16_f32 v137, v137, v137
v_and_b32 v133, 0xffff0000, v111
v_lshlrev_b32 v137, 16, v137
v_add_f32 v126, v126, v130
v_add_f32 v137, 0x3727c5ac, v137
v_add_f32 v127, v127, v131
v_cvt_pk_bf16_f32 v137, v137, v137
v_add_f32 v128, v128, v132
v_lshlrev_b32 v137, 16, v137
v_add_f32 v129, v129, v133
v_sqrt_f32 v137, v137
v_lshlrev_b32 v130, 16, v112
v_cvt_pk_bf16_f32 v137, v137, v137
v_and_b32 v131, 0xffff0000, v112
v_lshlrev_b32 v137, 16, v137
v_lshlrev_b32 v132, 16, v113
v_rcp_f32 v136, v137
v_and_b32 v133, 0xffff0000, v113
v_mul_f32 v136, v136, v136
v_add_f32 v126, v126, v130
v_add_f32 v127, v127, v131
v_add_f32 v128, v128, v132
v_add_f32 v129, v129, v133
v_lshlrev_b32 v130, 16, v114
v_and_b32 v131, 0xffff0000, v114
v_lshlrev_b32 v132, 16, v115
v_and_b32 v133, 0xffff0000, v115
v_add_f32 v126, v126, v130
v_add_f32 v127, v127, v131
v_add_f32 v128, v128, v132
v_add_f32 v129, v129, v133
v_lshlrev_b32 v130, 16, v116
v_and_b32 v131, 0xffff0000, v116
v_lshlrev_b32 v132, 16, v117
v_and_b32 v133, 0xffff0000, v117
v_add_f32 v126, v126, v130
v_add_f32 v127, v127, v131
v_add_f32 v128, v128, v132
v_add_f32 v129, v129, v133
v_lshlrev_b32 v130, 16, v118
v_and_b32 v131, 0xffff0000, v118
v_lshlrev_b32 v132, 16, v119
v_and_b32 v133, 0xffff0000, v119
v_add_f32 v126, v126, v130
v_add_f32 v127, v127, v131
v_add_f32 v128, v128, v132
v_add_f32 v129, v129, v133
v_lshlrev_b32 v130, 16, v120
v_and_b32 v131, 0xffff0000, v120
v_lshlrev_b32 v132, 16, v121
v_and_b32 v133, 0xffff0000, v121
v_fma_f32 v126, v126, v136, v130
v_fma_f32 v127, v127, v136, v131
v_fma_f32 v128, v128, v136, v132
v_fma_f32 v129, v129, v136, v133
v_cvt_pk_bf16_f32 v134, v126, v127
v_cvt_pk_bf16_f32 v135, v128, v129
global_store_dwordx2 v18, v[134:135], s[48:49] sc1
v_subrev_u32 v18, 36864, v18
s_waitcnt vmcnt(0)
s_barrier
ds_read_b64 v[122:123], v17 offset:0
ds_read_b64 v[124:125], v17 offset:128
ds_read_b64 v[108:109], v15 offset:0
ds_read_b64 v[110:111], v15 offset:2048
ds_read_b64 v[112:113], v15 offset:4096
ds_read_b64 v[114:115], v15 offset:6144
ds_read_b64 v[116:117], v15 offset:8192
ds_read_b64 v[118:119], v15 offset:10240
ds_read_b64 v[120:121], v16
s_waitcnt lgkmcnt(6)
v_lshlrev_b32 v126, 16, v108
v_add_f32 v122, v122, v124
v_and_b32 v127, 0xffff0000, v108
v_add_f32 v123, v123, v125
v_lshlrev_b32 v128, 16, v109
v_mul_f32 v122, 0x3ae38e39, v122
v_and_b32 v129, 0xffff0000, v109
v_mul_f32 v123, 0x3ae38e39, v123
s_waitcnt lgkmcnt(5)
v_lshlrev_b32 v130, 16, v110
v_fma_f32 v137, -v122, v122, v123
v_and_b32 v131, 0xffff0000, v110
v_max_f32 v137, 0, v137
v_lshlrev_b32 v132, 16, v111
v_cvt_pk_bf16_f32 v137, v137, v137
v_and_b32 v133, 0xffff0000, v111
v_lshlrev_b32 v137, 16, v137
v_add_f32 v126, v126, v130
v_add_f32 v137, 0x3727c5ac, v137
v_add_f32 v127, v127, v131
v_cvt_pk_bf16_f32 v137, v137, v137
v_add_f32 v128, v128, v132
v_lshlrev_b32 v137, 16, v137
v_add_f32 v129, v129, v133
v_sqrt_f32 v137, v137
s_waitcnt lgkmcnt(4)
v_lshlrev_b32 v130, 16, v112
v_cvt_pk_bf16_f32 v137, v137, v137
v_and_b32 v131, 0xffff0000, v112
v_lshlrev_b32 v137, 16, v137
v_lshlrev_b32 v132, 16, v113
v_rcp_f32 v136, v137
v_and_b32 v133, 0xffff0000, v113
v_mul_f32 v136, v136, v136
v_add_f32 v126, v126, v130
v_add_f32 v127, v127, v131
v_add_f32 v128, v128, v132
v_add_f32 v129, v129, v133
s_waitcnt lgkmcnt(3)
v_lshlrev_b32 v130, 16, v114
v_and_b32 v131, 0xffff0000, v114
v_lshlrev_b32 v132, 16, v115
v_and_b32 v133, 0xffff0000, v115
v_add_f32 v126, v126, v130
v_add_f32 v127, v127, v131
v_add_f32 v128, v128, v132
v_add_f32 v129, v129, v133
s_waitcnt lgkmcnt(2)
v_lshlrev_b32 v130, 16, v116
v_and_b32 v131, 0xffff0000, v116
v_lshlrev_b32 v132, 16, v117
v_and_b32 v133, 0xffff0000, v117
v_add_f32 v126, v126, v130
v_add_f32 v127, v127, v131
v_add_f32 v128, v128, v132
v_add_f32 v129, v129, v133
s_waitcnt lgkmcnt(1)
v_lshlrev_b32 v130, 16, v118
v_and_b32 v131, 0xffff0000, v118
v_lshlrev_b32 v132, 16, v119
v_and_b32 v133, 0xffff0000, v119
v_add_f32 v126, v126, v130
v_add_f32 v127, v127, v131
v_add_f32 v128, v128, v132
v_add_f32 v129, v129, v133
s_waitcnt lgkmcnt(0)
v_lshlrev_b32 v130, 16, v120
v_and_b32 v131, 0xffff0000, v120
v_lshlrev_b32 v132, 16, v121
v_and_b32 v133, 0xffff0000, v121
v_fma_f32 v126, v126, v136, v130
v_fma_f32 v127, v127, v136, v131
v_fma_f32 v128, v128, v136, v132
v_fma_f32 v129, v129, v136, v133
v_cvt_pk_bf16_f32 v134, v126, v127
v_cvt_pk_bf16_f32 v135, v128, v129
global_store_dwordx2 v18, v[134:135], s[48:49] sc1
v_subrev_u32 v18, 36864, v18
s_endpgm
.Ldn_hk5:
v_add_u32 v18, s44, v18
s_mov_b32 s44, 589824
s_mov_b64 s[48:49], s[46:47]
s_mov_b64 s[46:47], s[42:43]
s_mov_b32 s73, 11
s_branch .Ldn_hr6
.Ldn_hk9:
v_add_u32 v18, s44, v18
s_mov_b32 s44, 589824
s_mov_b64 s[48:49], s[46:47]
s_mov_b64 s[46:47], s[42:43]
s_mov_b32 s73, 11
s_branch .Ldn_hr10
.Ldn_hk13:
v_add_u32 v18, s44, v18
s_mov_b32 s44, 589824
s_mov_b64 s[48:49], s[46:47]
s_mov_b64 s[46:47], s[42:43]
s_mov_b32 s73, 11
s_branch .Ldn_hr14
.Ldn_sp17:
s_sleep 4
global_load_dword v29, v19, s[38:39] sc1
s_add_u32 s71, s71, 1
s_waitcnt vmcnt(0)
v_sub_u32 v29, v29, v20
v_cmp_lt_u32 vcc, 0x7f, v29
s_cbranch_vccz .Ldn_rt18
s_cmp_lt_u32 s71, 3000000
s_cbranch_scc1 .Ldn_sp17
s_branch .Ldn_rt18
.Ldn_sp19:
s_sleep 4
global_load_dword v29, v21, s[38:39] sc1
s_add_u32 s71, s71, 1
s_waitcnt vmcnt(0)
v_sub_u32 v29, v29, v22
v_cmp_lt_u32 vcc, 0x7f, v29
s_cbranch_vccz .Ldn_rt20
s_cmp_lt_u32 s71, 3000000
s_cbranch_scc1 .Ldn_sp19
s_branch .Ldn_rt20
.Ldn_hk21:
v_add_u32 v18, s44, v18
s_mov_b32 s44, 589824
s_mov_b64 s[48:49], s[46:47]
s_mov_b64 s[46:47], s[42:43]
s_mov_b32 s73, 11
s_branch .Ldn_hr22
.Ldn_sp25:
s_sleep 4
global_load_dword v29, v19, s[38:39] sc1
s_add_u32 s71, s71, 1
s_waitcnt vmcnt(0)
v_sub_u32 v29, v29, v23
v_cmp_lt_u32 vcc, 0x7f, v29
s_cbranch_vccz .Ldn_rt26
s_cmp_lt_u32 s71, 3000000
s_cbranch_scc1 .Ldn_sp25
s_branch .Ldn_rt26
.Ldn_sp27:
s_sleep 4
global_load_dword v29, v19, s[38:39] sc1
s_add_u32 s71, s71, 1
s_waitcnt vmcnt(0)
v_sub_u32 v29, v29, v23
v_cmp_lt_u32 vcc, 0x7f, v29
s_cbranch_vccz .Ldn_rt28
s_cmp_lt_u32 s71, 3000000
s_cbranch_scc1 .Ldn_sp27
s_branch .Ldn_rt28
.Ldn_sp29:
s_sleep 4
global_load_dword v29, v19, s[38:39] sc1
s_add_u32 s71, s71, 1
s_waitcnt vmcnt(0)
v_sub_u32 v29, v29, v23
v_cmp_lt_u32 vcc, 0x7f, v29
s_cbranch_vccz .Ldn_rt30
s_cmp_lt_u32 s71, 3000000
s_cbranch_scc1 .Ldn_sp29
s_branch .Ldn_rt30
.Lrole3:
v_add_u32 v109, 35840, v0
v_add_u32 v110, 71680, v0
v_add_u32 v111, 107520, v0
v_add_u32 v112, 143360, v0
v_add_u32 v113, 179200, v0
v_add_u32 v114, 215040, v0
v_add_u32 v115, 250880, v0
s_add_u32 m0, s42, 4096
s_nop 0
global_load_lds_dwordx4 v0, s[40:41] offset:-4096
global_load_lds_dwordx4 v109, s[40:41] offset:-3072
global_load_lds_dwordx4 v110, s[40:41] offset:-2048
global_load_lds_dwordx4 v111, s[40:41] offset:-1024
global_load_lds_dwordx4 v112, s[40:41] offset:0
global_load_lds_dwordx4 v113, s[40:41] offset:1024
global_load_lds_dwordx4 v114, s[40:41] offset:2048
global_load_lds_dwordx4 v115, s[40:41] offset:3072
s_add_u32 s40, s40, 589824
s_addc_u32 s41, s41, 0
s_add_u32 m0, s42, 36864
s_nop 0
global_load_lds_dwordx4 v0, s[40:41] offset:-4096
global_load_lds_dwordx4 v109, s[40:41] offset:-3072
global_load_lds_dwordx4 v110, s[40:41] offset:-2048
global_load_lds_dwordx4 v111, s[40:41] offset:-1024
global_load_lds_dwordx4 v112, s[40:41] offset:0
global_load_lds_dwordx4 v113, s[40:41] offset:1024
global_load_lds_dwordx4 v114, s[40:41] offset:2048
global_load_lds_dwordx4 v115, s[40:41] offset:3072
s_add_u32 s40, s40, 589824
s_addc_u32 s41, s41, 0
s_add_u32 m0, s42, 69632
s_nop 0
global_load_lds_dwordx4 v0, s[40:41] offset:-4096
global_load_lds_dwordx4 v109, s[40:41] offset:-3072
global_load_lds_dwordx4 v110, s[40:41] offset:-2048
global_load_lds_dwordx4 v111, s[40:41] offset:-1024
global_load_lds_dwordx4 v112, s[40:41] offset:0
global_load_lds_dwordx4 v113, s[40:41] offset:1024
global_load_lds_dwordx4 v114, s[40:41] offset:2048
global_load_lds_dwordx4 v115, s[40:41] offset:3072
s_add_u32 s40, s40, 589824
s_addc_u32 s41, s41, 0
s_add_u32 m0, s42, 102400
s_nop 0
global_load_lds_dwordx4 v0, s[40:41] offset:-4096
global_load_lds_dwordx4 v109, s[40:41] offset:-3072
global_load_lds_dwordx4 v110, s[40:41] offset:-2048
global_load_lds_dwordx4 v111, s[40:41] offset:-1024
global_load_lds_dwordx4 v112, s[40:41] offset:0
global_load_lds_dwordx4 v113, s[40:41] offset:1024
global_load_lds_dwordx4 v114, s[40:41] offset:2048
global_load_lds_dwordx4 v115, s[40:41] offset:3072
)"
R"(s_add_u32 s40, s40, 589824
s_addc_u32 s41, s41, 0
v_add_u32 v116, 0x10000, v1
v_mov_b32 v134, 0x3F803F80
v_mov_b32 v135, 0x3F803F80
v_mov_b32 v136, 0x3F803F80
v_mov_b32 v137, 0x3F803F80
s_waitcnt lgkmcnt(0)
s_mul_i32 s37, s37, 0x9E3779B1
s_add_u32 s37, s37, 0x7F4A7C15
v_add_u32 v5, s37, v5
v_add_u32 v7, s37, v7
s_waitcnt vmcnt(24)
s_barrier
ds_read_b64_tr_b16 a[0:1], v1 offset:0
ds_read_b64_tr_b16 a[2:3], v1 offset:1024
ds_read_b64_tr_b16 a[4:5], v1 offset:8
ds_read_b64_tr_b16 a[6:7], v1 offset:1032
ds_read_b64_tr_b16 a[8:9], v1 offset:16384
ds_read_b64_tr_b16 a[10:11], v1 offset:17408
ds_read_b64_tr_b16 a[12:13], v1 offset:16392
ds_read_b64_tr_b16 a[14:15], v1 offset:17416
ds_read_b64_tr_b16 a[16:17], v1 offset:8192
ds_read_b64_tr_b16 a[18:19], v1 offset:9216
ds_read_b64_tr_b16 a[20:21], v1 offset:8200
ds_read_b64_tr_b16 a[22:23], v1 offset:9224
ds_read_b64_tr_b16 a[24:25], v1 offset:24576
ds_read_b64_tr_b16 a[26:27], v1 offset:25600
ds_read_b64_tr_b16 a[28:29], v1 offset:24584
ds_read_b64_tr_b16 a[30:31], v1 offset:25608
s_waitcnt lgkmcnt(0)
s_barrier
s_add_u32 m0, s42, 4096
s_nop 0
global_load_lds_dwordx4 v0, s[40:41] offset:-4096
global_load_lds_dwordx4 v109, s[40:41] offset:-3072
global_load_lds_dwordx4 v110, s[40:41] offset:-2048
global_load_lds_dwordx4 v111, s[40:41] offset:-1024
global_load_lds_dwordx4 v112, s[40:41] offset:0
global_load_lds_dwordx4 v113, s[40:41] offset:1024
global_load_lds_dwordx4 v114, s[40:41] offset:2048
global_load_lds_dwordx4 v115, s[40:41] offset:3072
s_add_u32 s40, s40, 589824
s_addc_u32 s41, s41, 0
s_waitcnt vmcnt(24)
s_barrier
ds_read_b64_tr_b16 a[32:33], v1 offset:32768
ds_read_b64_tr_b16 a[34:35], v1 offset:33792
ds_read_b64_tr_b16 a[36:37], v1 offset:32776
ds_read_b64_tr_b16 a[38:39], v1 offset:33800
ds_read_b64_tr_b16 a[40:41], v1 offset:49152
ds_read_b64_tr_b16 a[42:43], v1 offset:50176
ds_read_b64_tr_b16 a[44:45], v1 offset:49160
ds_read_b64_tr_b16 a[46:47], v1 offset:50184
ds_read_b64_tr_b16 a[48:49], v1 offset:40960
ds_read_b64_tr_b16 a[50:51], v1 offset:41984
ds_read_b64_tr_b16 a[52:53], v1 offset:40968
ds_read_b64_tr_b16 a[54:55], v1 offset:41992
ds_read_b64_tr_b16 a[56:57], v1 offset:57344
ds_read_b64_tr_b16 a[58:59], v1 offset:58368
ds_read_b64_tr_b16 a[60:61], v1 offset:57352
ds_read_b64_tr_b16 a[62:63], v1 offset:58376
v_mfma_f32_16x16x32_bf16 v[118:121], v[134:137], a[0:3], 0
v_mfma_f32_16x16x32_bf16 v[122:125], v[134:137], a[4:7], 0
v_mfma_f32_16x16x32_bf16 v[126:129], v[134:137], a[8:11], 0
v_mfma_f32_16x16x32_bf16 v[130:133], v[134:137], a[12:15], 0
v_mfma_f32_16x16x32_bf16 v[118:121], v[134:137], a[16:19], v[118:121]
v_mfma_f32_16x16x32_bf16 v[122:125], v[134:137], a[20:23], v[122:125]
v_mfma_f32_16x16x32_bf16 v[126:129], v[134:137], a[24:27], v[126:129]
v_mfma_f32_16x16x32_bf16 v[130:133], v[134:137], a[28:31], v[130:133]
s_waitcnt lgkmcnt(0)
s_barrier
s_add_u32 m0, s42, 36864
s_nop 0
global_load_lds_dwordx4 v0, s[40:41] offset:-4096
global_load_lds_dwordx4 v109, s[40:41] offset:-3072
global_load_lds_dwordx4 v110, s[40:41] offset:-2048
global_load_lds_dwordx4 v111, s[40:41] offset:-1024
global_load_lds_dwordx4 v112, s[40:41] offset:0
global_load_lds_dwordx4 v113, s[40:41] offset:1024
global_load_lds_dwordx4 v114, s[40:41] offset:2048
global_load_lds_dwordx4 v115, s[40:41] offset:3072
s_add_u32 s40, s40, 589824
s_addc_u32 s41, s41, 0
s_waitcnt vmcnt(24)
s_barrier
ds_read_b64_tr_b16 a[64:65], v116 offset:0
ds_read_b64_tr_b16 a[66:67], v116 offset:1024
ds_read_b64_tr_b16 a[68:69], v116 offset:8
ds_read_b64_tr_b16 a[70:71], v116 offset:1032
ds_read_b64_tr_b16 a[72:73], v116 offset:16384
ds_read_b64_tr_b16 a[74:75], v116 offset:17408
ds_read_b64_tr_b16 a[76:77], v116 offset:16392
ds_read_b64_tr_b16 a[78:79], v116 offset:17416
ds_read_b64_tr_b16 a[80:81], v116 offset:8192
ds_read_b64_tr_b16 a[82:83], v116 offset:9216
ds_read_b64_tr_b16 a[84:85], v116 offset:8200
ds_read_b64_tr_b16 a[86:87], v116 offset:9224
ds_read_b64_tr_b16 a[88:89], v116 offset:24576
ds_read_b64_tr_b16 a[90:91], v116 offset:25600
ds_read_b64_tr_b16 a[92:93], v116 offset:24584
ds_read_b64_tr_b16 a[94:95], v116 offset:25608
v_mfma_f32_16x16x32_bf16 v[118:121], v[134:137], a[32:35], v[118:121]
v_mfma_f32_16x16x32_bf16 v[122:125], v[134:137], a[36:39], v[122:125]
v_mfma_f32_16x16x32_bf16 v[126:129], v[134:137], a[40:43], v[126:129]
v_mfma_f32_16x16x32_bf16 v[130:133], v[134:137], a[44:47], v[130:133]
v_mfma_f32_16x16x32_bf16 v[118:121], v[134:137], a[48:51], v[118:121]
v_mfma_f32_16x16x32_bf16 v[122:125], v[134:137], a[52:55], v[122:125]
v_mfma_f32_16x16x32_bf16 v[126:129], v[134:137], a[56:59], v[126:129]
v_mfma_f32_16x16x32_bf16 v[130:133], v[134:137], a[60:63], v[130:133]
s_waitcnt lgkmcnt(0)
s_barrier
s_add_u32 m0, s42, 69632
s_nop 0
global_load_lds_dwordx4 v0, s[40:41] offset:-4096
global_load_lds_dwordx4 v109, s[40:41] offset:-3072
global_load_lds_dwordx4 v110, s[40:41] offset:-2048
global_load_lds_dwordx4 v111, s[40:41] offset:-1024
global_load_lds_dwordx4 v112, s[40:41] offset:0
global_load_lds_dwordx4 v113, s[40:41] offset:1024
global_load_lds_dwordx4 v114, s[40:41] offset:2048
global_load_lds_dwordx4 v115, s[40:41] offset:3072
s_add_u32 s40, s40, 589824
s_addc_u32 s41, s41, 0
s_waitcnt vmcnt(24)
s_barrier
ds_read_b64_tr_b16 a[96:97], v116 offset:32768
ds_read_b64_tr_b16 a[98:99], v116 offset:33792
ds_read_b64_tr_b16 a[100:101], v116 offset:32776
ds_read_b64_tr_b16 a[102:103], v116 offset:33800
ds_read_b64_tr_b16 a[104:105], v116 offset:49152
ds_read_b64_tr_b16 a[106:107], v116 offset:50176
ds_read_b64_tr_b16 a[108:109], v116 offset:49160
ds_read_b64_tr_b16 a[110:111], v116 offset:50184
ds_read_b64_tr_b16 a[112:113], v116 offset:40960
ds_read_b64_tr_b16 a[114:115], v116 offset:41984
ds_read_b64_tr_b16 a[116:117], v116 offset:40968
ds_read_b64_tr_b16 a[118:119], v116 offset:41992
ds_read_b64_tr_b16 a[120:121], v116 offset:57344
ds_read_b64_tr_b16 a[122:123], v116 offset:58368
ds_read_b64_tr_b16 a[124:125], v116 offset:57352
ds_read_b64_tr_b16 a[126:127], v116 offset:58376
v_mfma_f32_16x16x32_bf16 v[118:121], v[134:137], a[64:67], v[118:121]
v_mfma_f32_16x16x32_bf16 v[122:125], v[134:137], a[68:71], v[122:125]
v_mfma_f32_16x16x32_bf16 v[126:129], v[134:137], a[72:75], v[126:129]
v_mfma_f32_16x16x32_bf16 v[130:133], v[134:137], a[76:79], v[130:133]
v_mfma_f32_16x16x32_bf16 v[118:121], v[134:137], a[80:83], v[118:121]
v_mfma_f32_16x16x32_bf16 v[122:125], v[134:137], a[84:87], v[122:125]
v_mfma_f32_16x16x32_bf16 v[126:129], v[134:137], a[88:91], v[126:129]
v_mfma_f32_16x16x32_bf16 v[130:133], v[134:137], a[92:95], v[130:133]
s_waitcnt lgkmcnt(0)
s_barrier
s_add_u32 m0, s42, 102400
s_nop 0
global_load_lds_dwordx4 v0, s[40:41] offset:-4096
global_load_lds_dwordx4 v109, s[40:41] offset:-3072
global_load_lds_dwordx4 v110, s[40:41] offset:-2048
global_load_lds_dwordx4 v111, s[40:41] offset:-1024
global_load_lds_dwordx4 v112, s[40:41] offset:0
global_load_lds_dwordx4 v113, s[40:41] offset:1024
global_load_lds_dwordx4 v114, s[40:41] offset:2048
global_load_lds_dwordx4 v115, s[40:41] offset:3072
s_add_u32 s40, s40, 589824
s_addc_u32 s41, s41, 0
s_waitcnt vmcnt(24)
s_barrier
ds_read_b64_tr_b16 a[128:129], v1 offset:0
ds_read_b64_tr_b16 a[130:131], v1 offset:1024
ds_read_b64_tr_b16 a[132:133], v1 offset:8
ds_read_b64_tr_b16 a[134:135], v1 offset:1032
ds_read_b64_tr_b16 a[136:137], v1 offset:16384
ds_read_b64_tr_b16 a[138:139], v1 offset:17408
ds_read_b64_tr_b16 a[140:141], v1 offset:16392
ds_read_b64_tr_b16 a[142:143], v1 offset:17416
ds_read_b64_tr_b16 a[144:145], v1 offset:8192
ds_read_b64_tr_b16 a[146:147], v1 offset:9216
ds_read_b64_tr_b16 a[148:149], v1 offset:8200
ds_read_b64_tr_b16 a[150:151], v1 offset:9224
ds_read_b64_tr_b16 a[152:153], v1 offset:24576
ds_read_b64_tr_b16 a[154:155], v1 offset:25600
ds_read_b64_tr_b16 a[156:157], v1 offset:24584
ds_read_b64_tr_b16 a[158:159], v1 offset:25608
v_mfma_f32_16x16x32_bf16 v[118:121], v[134:137], a[96:99], v[118:121]
v_mfma_f32_16x16x32_bf16 v[122:125], v[134:137], a[100:103], v[122:125]
v_mfma_f32_16x16x32_bf16 v[126:129], v[134:137], a[104:107], v[126:129]
v_mfma_f32_16x16x32_bf16 v[130:133], v[134:137], a[108:111], v[130:133]
v_mfma_f32_16x16x32_bf16 v[118:121], v[134:137], a[112:115], v[118:121]
v_mfma_f32_16x16x32_bf16 v[122:125], v[134:137], a[116:119], v[122:125]
v_mfma_f32_16x16x32_bf16 v[126:129], v[134:137], a[120:123], v[126:129]
v_mfma_f32_16x16x32_bf16 v[130:133], v[134:137], a[124:127], v[130:133]
s_waitcnt lgkmcnt(0)
s_barrier
s_add_u32 m0, s42, 4096
s_nop 0
global_load_lds_dwordx4 v0, s[40:41] offset:-4096
global_load_lds_dwordx4 v109, s[40:41] offset:-3072
global_load_lds_dwordx4 v110, s[40:41] offset:-2048
global_load_lds_dwordx4 v111, s[40:41] offset:-1024
global_load_lds_dwordx4 v112, s[40:41] offset:0
global_load_lds_dwordx4 v113, s[40:41] offset:1024
global_load_lds_dwordx4 v114, s[40:41] offset:2048
global_load_lds_dwordx4 v115, s[40:41] offset:3072
s_add_u32 s40, s40, 589824
s_addc_u32 s41, s41, 0
s_waitcnt vmcnt(24)
s_barrier
ds_read_b64_tr_b16 a[160:161], v1 offset:32768
ds_read_b64_tr_b16 a[162:163], v1 offset:33792
ds_read_b64_tr_b16 a[164:165], v1 offset:32776
ds_read_b64_tr_b16 a[166:167], v1 offset:33800
ds_read_b64_tr_b16 a[168:169], v1 offset:49152
ds_read_b64_tr_b16 a[170:171], v1 offset:50176
ds_read_b64_tr_b16 a[172:173], v1 offset:49160
ds_read_b64_tr_b16 a[174:175], v1 offset:50184
ds_read_b64_tr_b16 a[176:177], v1 offset:40960
ds_read_b64_tr_b16 a[178:179], v1 offset:41984
ds_read_b64_tr_b16 a[180:181], v1 offset:40968
ds_read_b64_tr_b16 a[182:183], v1 offset:41992
ds_read_b64_tr_b16 a[184:185], v1 offset:57344
ds_read_b64_tr_b16 a[186:187], v1 offset:58368
ds_read_b64_tr_b16 a[188:189], v1 offset:57352
ds_read_b64_tr_b16 a[190:191], v1 offset:58376
v_mfma_f32_16x16x32_bf16 v[118:121], v[134:137], a[128:131], v[118:121]
v_mfma_f32_16x16x32_bf16 v[122:125], v[134:137], a[132:135], v[122:125]
v_mfma_f32_16x16x32_bf16 v[126:129], v[134:137], a[136:139], v[126:129]
v_mfma_f32_16x16x32_bf16 v[130:133], v[134:137], a[140:143], v[130:133]
v_mfma_f32_16x16x32_bf16 v[118:121], v[134:137], a[144:147], v[118:121]
v_mfma_f32_16x16x32_bf16 v[122:125], v[134:137], a[148:151], v[122:125]
v_mfma_f32_16x16x32_bf16 v[126:129], v[134:137], a[152:155], v[126:129]
v_mfma_f32_16x16x32_bf16 v[130:133], v[134:137], a[156:159], v[130:133]
s_waitcnt vmcnt(16)
s_barrier
ds_read_b64_tr_b16 a[192:193], v116 offset:0
ds_read_b64_tr_b16 a[194:195], v116 offset:1024
ds_read_b64_tr_b16 a[196:197], v116 offset:8
ds_read_b64_tr_b16 a[198:199], v116 offset:1032
ds_read_b64_tr_b16 a[200:201], v116 offset:16384
ds_read_b64_tr_b16 a[202:203], v116 offset:17408
ds_read_b64_tr_b16 a[204:205], v116 offset:16392
ds_read_b64_tr_b16 a[206:207], v116 offset:17416
ds_read_b64_tr_b16 a[208:209], v116 offset:8192
ds_read_b64_tr_b16 a[210:211], v116 offset:9216
ds_read_b64_tr_b16 a[212:213], v116 offset:8200
ds_read_b64_tr_b16 a[214:215], v116 offset:9224
ds_read_b64_tr_b16 a[216:217], v116 offset:24576
ds_read_b64_tr_b16 a[218:219], v116 offset:25600
ds_read_b64_tr_b16 a[220:221], v116 offset:24584
ds_read_b64_tr_b16 a[222:223], v116 offset:25608
s_waitcnt lgkmcnt(15)
v_mfma_f32_16x16x32_bf16 v[118:121], v[134:137], a[160:163], v[118:121]
v_mfma_f32_16x16x32_bf16 v[122:125], v[134:137], a[164:167], v[122:125]
v_mfma_f32_16x16x32_bf16 v[126:129], v[134:137], a[168:171], v[126:129]
v_mfma_f32_16x16x32_bf16 v[130:133], v[134:137], a[172:175], v[130:133]
v_mfma_f32_16x16x32_bf16 v[118:121], v[134:137], a[176:179], v[118:121]
v_mfma_f32_16x16x32_bf16 v[122:125], v[134:137], a[180:183], v[122:125]
v_mfma_f32_16x16x32_bf16 v[126:129], v[134:137], a[184:187], v[126:129]
v_mfma_f32_16x16x32_bf16 v[130:133], v[134:137], a[188:191], v[130:133]
s_waitcnt vmcnt(8)
s_barrier
ds_read_b64_tr_b16 a[224:225], v116 offset:32768
ds_read_b64_tr_b16 a[226:227], v116 offset:33792
ds_read_b64_tr_b16 a[228:229], v116 offset:32776
ds_read_b64_tr_b16 a[230:231], v116 offset:33800
ds_read_b64_tr_b16 a[232:233], v116 offset:49152
ds_read_b64_tr_b16 a[234:235], v116 offset:50176
ds_read_b64_tr_b16 a[236:237], v116 offset:49160
ds_read_b64_tr_b16 a[238:239], v116 offset:50184
ds_read_b64_tr_b16 a[240:241], v116 offset:40960
ds_read_b64_tr_b16 a[242:243], v116 offset:41984
ds_read_b64_tr_b16 a[244:245], v116 offset:40968
ds_read_b64_tr_b16 a[246:247], v116 offset:41992
ds_read_b64_tr_b16 a[248:249], v116 offset:57344
ds_read_b64_tr_b16 a[250:251], v116 offset:58368
ds_read_b64_tr_b16 a[252:253], v116 offset:57352
ds_read_b64_tr_b16 a[254:255], v116 offset:58376
s_waitcnt lgkmcnt(15)
v_mfma_f32_16x16x32_bf16 v[118:121], v[134:137], a[192:195], v[118:121]
v_mfma_f32_16x16x32_bf16 v[122:125], v[134:137], a[196:199], v[122:125]
v_mfma_f32_16x16x32_bf16 v[126:129], v[134:137], a[200:203], v[126:129]
v_mfma_f32_16x16x32_bf16 v[130:133], v[134:137], a[204:207], v[130:133]
v_mfma_f32_16x16x32_bf16 v[118:121], v[134:137], a[208:211], v[118:121]
v_mfma_f32_16x16x32_bf16 v[122:125], v[134:137], a[212:215], v[122:125]
v_mfma_f32_16x16x32_bf16 v[126:129], v[134:137], a[216:219], v[126:129]
v_mfma_f32_16x16x32_bf16 v[130:133], v[134:137], a[220:223], v[130:133]
s_waitcnt vmcnt(0)
s_barrier
ds_read_b64_tr_b16 v[16:17], v1 offset:0
ds_read_b64_tr_b16 v[18:19], v1 offset:1024
ds_read_b64_tr_b16 v[20:21], v1 offset:8
ds_read_b64_tr_b16 v[22:23], v1 offset:1032
ds_read_b64_tr_b16 v[24:25], v1 offset:16384
ds_read_b64_tr_b16 v[26:27], v1 offset:17408
ds_read_b64_tr_b16 v[28:29], v1 offset:16392
ds_read_b64_tr_b16 v[30:31], v1 offset:17416
ds_read_b64_tr_b16 v[32:33], v1 offset:8192
ds_read_b64_tr_b16 v[34:35], v1 offset:9216
ds_read_b64_tr_b16 v[36:37], v1 offset:8200
ds_read_b64_tr_b16 v[38:39], v1 offset:9224
ds_read_b64_tr_b16 v[40:41], v1 offset:24576
ds_read_b64_tr_b16 v[42:43], v1 offset:25600
ds_read_b64_tr_b16 v[44:45], v1 offset:24584
ds_read_b64_tr_b16 v[46:47], v1 offset:25608
s_waitcnt lgkmcnt(15)
v_mfma_f32_16x16x32_bf16 v[118:121], v[134:137], a[224:227], v[118:121]
v_mfma_f32_16x16x32_bf16 v[122:125], v[134:137], a[228:231], v[122:125]
v_mfma_f32_16x16x32_bf16 v[126:129], v[134:137], a[232:235], v[126:129]
v_mfma_f32_16x16x32_bf16 v[130:133], v[134:137], a[236:239], v[130:133]
v_mfma_f32_16x16x32_bf16 v[118:121], v[134:137], a[240:243], v[118:121]
v_mfma_f32_16x16x32_bf16 v[122:125], v[134:137], a[244:247], v[122:125]
v_mfma_f32_16x16x32_bf16 v[126:129], v[134:137], a[248:251], v[126:129]
v_mfma_f32_16x16x32_bf16 v[130:133], v[134:137], a[252:255], v[130:133]
s_waitcnt lgkmcnt(14)
v_mfma_f32_16x16x32_bf16 v[118:121], v[134:137], v[16:19], v[118:121]
s_waitcnt lgkmcnt(12)
v_mfma_f32_16x16x32_bf16 v[122:125], v[134:137], v[20:23], v[122:125]
s_waitcnt lgkmcnt(10)
v_mfma_f32_16x16x32_bf16 v[126:129], v[134:137], v[24:27], v[126:129]
s_waitcnt lgkmcnt(8)
v_mfma_f32_16x16x32_bf16 v[130:133], v[134:137], v[28:31], v[130:133]
s_waitcnt lgkmcnt(6)
v_mfma_f32_16x16x32_bf16 v[118:121], v[134:137], v[32:35], v[118:121]
s_waitcnt lgkmcnt(4)
v_mfma_f32_16x16x32_bf16 v[122:125], v[134:137], v[36:39], v[122:125]
s_waitcnt lgkmcnt(2)
v_mfma_f32_16x16x32_bf16 v[126:129], v[134:137], v[40:43], v[126:129]
s_waitcnt lgkmcnt(0)
v_mfma_f32_16x16x32_bf16 v[130:133], v[134:137], v[44:47], v[130:133]
s_nop 1
v_mul_f32 v118, 0xbae38e39, v118
v_mul_f32 v119, 0xbae38e39, v119
v_mul_f32 v120, 0xbae38e39, v120
v_mul_f32 v121, 0xbae38e39, v121
v_mul_f32 v122, 0xbae38e39, v122
v_mul_f32 v123, 0xbae38e39, v123
v_mul_f32 v124, 0xbae38e39, v124
v_mul_f32 v125, 0xbae38e39, v125
v_mul_f32 v126, 0xbae38e39, v126
v_mul_f32 v127, 0xbae38e39, v127
v_mul_f32 v128, 0xbae38e39, v128
v_mul_f32 v129, 0xbae38e39, v129
v_mul_f32 v130, 0xbae38e39, v130
v_mul_f32 v131, 0xbae38e39, v131
v_mul_f32 v132, 0xbae38e39, v132
v_mul_f32 v133, 0xbae38e39, v133
v_mfma_f32_16x16x32_bf16 v[138:141], v[12:15], a[0:3], v[118:121]
v_mfma_f32_16x16x32_bf16 v[142:145], v[10:13], a[0:3], v[118:121]
v_mfma_f32_16x16x32_bf16 v[146:149], v[12:15], a[4:7], v[122:125]
v_mfma_f32_16x16x32_bf16 v[150:153], v[10:13], a[4:7], v[122:125]
v_mfma_f32_16x16x32_bf16 v[154:157], v[12:15], a[8:11], v[126:129]
s_nop 3
v_cvt_pk_bf16_f32 v170, v138, v139
v_cvt_pk_bf16_f32 v171, v140, v141
v_cvt_pk_bf16_f32 v172, v142, v143
v_cvt_pk_bf16_f32 v173, v144, v145
v_mfma_f32_16x16x32_bf16 v[158:161], v[10:13], a[8:11], v[126:129]
v_accvgpr_write_b32 a0, v170
v_accvgpr_write_b32 a1, v171
v_accvgpr_write_b32 a2, v172
v_accvgpr_write_b32 a3, v173
v_mfma_f32_16x16x32_bf16 v[162:165], v[12:15], a[12:15], v[130:133]
v_cvt_pk_bf16_f32 v174, v146, v147
v_cvt_pk_bf16_f32 v175, v148, v149
v_cvt_pk_bf16_f32 v176, v150, v151
v_cvt_pk_bf16_f32 v177, v152, v153
v_mfma_f32_16x16x32_bf16 v[166:169], v[10:13], a[12:15], v[130:133]
v_accvgpr_write_b32 a4, v174
v_accvgpr_write_b32 a5, v175
v_accvgpr_write_b32 a6, v176
v_accvgpr_write_b32 a7, v177
v_mfma_f32_16x16x32_bf16 v[138:141], v[12:15], a[16:19], v[118:121]
v_cvt_pk_bf16_f32 v170, v154, v155
v_cvt_pk_bf16_f32 v171, v156, v157
v_cvt_pk_bf16_f32 v172, v158, v159
v_cvt_pk_bf16_f32 v173, v160, v161
v_mfma_f32_16x16x32_bf16 v[142:145], v[10:13], a[16:19], v[118:121]
v_accvgpr_write_b32 a8, v170
v_accvgpr_write_b32 a9, v171
v_accvgpr_write_b32 a10, v172
v_accvgpr_write_b32 a11, v173
v_mfma_f32_16x16x32_bf16 v[146:149], v[12:15], a[20:23], v[122:125]
v_cvt_pk_bf16_f32 v174, v162, v163
v_cvt_pk_bf16_f32 v175, v164, v165
v_cvt_pk_bf16_f32 v176, v166, v167
v_cvt_pk_bf16_f32 v177, v168, v169
v_mfma_f32_16x16x32_bf16 v[150:153], v[10:13], a[20:23], v[122:125]
v_accvgpr_write_b32 a12, v174
v_accvgpr_write_b32 a13, v175
v_accvgpr_write_b32 a14, v176
v_accvgpr_write_b32 a15, v177
v_mfma_f32_16x16x32_bf16 v[154:157], v[12:15], a[24:27], v[126:129]
v_cvt_pk_bf16_f32 v170, v138, v139
v_cvt_pk_bf16_f32 v171, v140, v141
v_cvt_pk_bf16_f32 v172, v142, v143
v_cvt_pk_bf16_f32 v173, v144, v145
v_mfma_f32_16x16x32_bf16 v[158:161], v[10:13], a[24:27], v[126:129]
v_accvgpr_write_b32 a16, v170
v_accvgpr_write_b32 a17, v171
v_accvgpr_write_b32 a18, v172
v_accvgpr_write_b32 a19, v173
v_mfma_f32_16x16x32_bf16 v[162:165], v[12:15], a[28:31], v[130:133]
)"
R"(v_cvt_pk_bf16_f32 v174, v146, v147
v_cvt_pk_bf16_f32 v175, v148, v149
v_cvt_pk_bf16_f32 v176, v150, v151
v_cvt_pk_bf16_f32 v177, v152, v153
v_mfma_f32_16x16x32_bf16 v[166:169], v[10:13], a[28:31], v[130:133]
v_accvgpr_write_b32 a20, v174
v_accvgpr_write_b32 a21, v175
v_accvgpr_write_b32 a22, v176
v_accvgpr_write_b32 a23, v177
v_mfma_f32_16x16x32_bf16 v[138:141], v[12:15], a[32:35], v[118:121]
v_cvt_pk_bf16_f32 v170, v154, v155
v_cvt_pk_bf16_f32 v171, v156, v157
v_cvt_pk_bf16_f32 v172, v158, v159
v_cvt_pk_bf16_f32 v173, v160, v161
v_mfma_f32_16x16x32_bf16 v[142:145], v[10:13], a[32:35], v[118:121]
v_accvgpr_write_b32 a24, v170
v_accvgpr_write_b32 a25, v171
v_accvgpr_write_b32 a26, v172
v_accvgpr_write_b32 a27, v173
v_mfma_f32_16x16x32_bf16 v[146:149], v[12:15], a[36:39], v[122:125]
v_cvt_pk_bf16_f32 v174, v162, v163
v_cvt_pk_bf16_f32 v175, v164, v165
v_cvt_pk_bf16_f32 v176, v166, v167
v_cvt_pk_bf16_f32 v177, v168, v169
v_mfma_f32_16x16x32_bf16 v[150:153], v[10:13], a[36:39], v[122:125]
v_accvgpr_write_b32 a28, v174
v_accvgpr_write_b32 a29, v175
v_accvgpr_write_b32 a30, v176
v_accvgpr_write_b32 a31, v177
v_mfma_f32_16x16x32_bf16 v[154:157], v[12:15], a[40:43], v[126:129]
v_cvt_pk_bf16_f32 v170, v138, v139
v_cvt_pk_bf16_f32 v171, v140, v141
v_cvt_pk_bf16_f32 v172, v142, v143
v_cvt_pk_bf16_f32 v173, v144, v145
v_mfma_f32_16x16x32_bf16 v[158:161], v[10:13], a[40:43], v[126:129]
v_accvgpr_write_b32 a32, v170
v_accvgpr_write_b32 a33, v171
v_accvgpr_write_b32 a34, v172
v_accvgpr_write_b32 a35, v173
v_mfma_f32_16x16x32_bf16 v[162:165], v[12:15], a[44:47], v[130:133]
v_cvt_pk_bf16_f32 v174, v146, v147
v_cvt_pk_bf16_f32 v175, v148, v149
v_cvt_pk_bf16_f32 v176, v150, v151
v_cvt_pk_bf16_f32 v177, v152, v153
v_mfma_f32_16x16x32_bf16 v[166:169], v[10:13], a[44:47], v[130:133]
v_accvgpr_write_b32 a36, v174
v_accvgpr_write_b32 a37, v175
v_accvgpr_write_b32 a38, v176
v_accvgpr_write_b32 a39, v177
v_mfma_f32_16x16x32_bf16 v[138:141], v[12:15], a[48:51], v[118:121]
v_cvt_pk_bf16_f32 v170, v154, v155
v_cvt_pk_bf16_f32 v171, v156, v157
v_cvt_pk_bf16_f32 v172, v158, v159
v_cvt_pk_bf16_f32 v173, v160, v161
v_mfma_f32_16x16x32_bf16 v[142:145], v[10:13], a[48:51], v[118:121]
v_accvgpr_write_b32 a40, v170
v_accvgpr_write_b32 a41, v171
v_accvgpr_write_b32 a42, v172
v_accvgpr_write_b32 a43, v173
v_mfma_f32_16x16x32_bf16 v[146:149], v[12:15], a[52:55], v[122:125]
v_cvt_pk_bf16_f32 v174, v162, v163
v_cvt_pk_bf16_f32 v175, v164, v165
v_cvt_pk_bf16_f32 v176, v166, v167
v_cvt_pk_bf16_f32 v177, v168, v169
v_mfma_f32_16x16x32_bf16 v[150:153], v[10:13], a[52:55], v[122:125]
v_accvgpr_write_b32 a44, v174
v_accvgpr_write_b32 a45, v175
v_accvgpr_write_b32 a46, v176
v_accvgpr_write_b32 a47, v177
v_mfma_f32_16x16x32_bf16 v[154:157], v[12:15], a[56:59], v[126:129]
v_cvt_pk_bf16_f32 v170, v138, v139
v_cvt_pk_bf16_f32 v171, v140, v141
v_cvt_pk_bf16_f32 v172, v142, v143
v_cvt_pk_bf16_f32 v173, v144, v145
v_mfma_f32_16x16x32_bf16 v[158:161], v[10:13], a[56:59], v[126:129]
v_accvgpr_write_b32 a48, v170
v_accvgpr_write_b32 a49, v171
v_accvgpr_write_b32 a50, v172
v_accvgpr_write_b32 a51, v173
v_mfma_f32_16x16x32_bf16 v[162:165], v[12:15], a[60:63], v[130:133]
v_cvt_pk_bf16_f32 v174, v146, v147
v_cvt_pk_bf16_f32 v175, v148, v149
v_cvt_pk_bf16_f32 v176, v150, v151
v_cvt_pk_bf16_f32 v177, v152, v153
v_mfma_f32_16x16x32_bf16 v[166:169], v[10:13], a[60:63], v[130:133]
v_accvgpr_write_b32 a52, v174
v_accvgpr_write_b32 a53, v175
v_accvgpr_write_b32 a54, v176
v_accvgpr_write_b32 a55, v177
v_mfma_f32_16x16x32_bf16 v[138:141], v[12:15], a[64:67], v[118:121]
v_cvt_pk_bf16_f32 v170, v154, v155
v_cvt_pk_bf16_f32 v171, v156, v157
v_cvt_pk_bf16_f32 v172, v158, v159
v_cvt_pk_bf16_f32 v173, v160, v161
v_mfma_f32_16x16x32_bf16 v[142:145], v[10:13], a[64:67], v[118:121]
v_accvgpr_write_b32 a56, v170
v_accvgpr_write_b32 a57, v171
v_accvgpr_write_b32 a58, v172
v_accvgpr_write_b32 a59, v173
v_mfma_f32_16x16x32_bf16 v[146:149], v[12:15], a[68:71], v[122:125]
v_cvt_pk_bf16_f32 v174, v162, v163
v_cvt_pk_bf16_f32 v175, v164, v165
v_cvt_pk_bf16_f32 v176, v166, v167
v_cvt_pk_bf16_f32 v177, v168, v169
v_mfma_f32_16x16x32_bf16 v[150:153], v[10:13], a[68:71], v[122:125]
v_accvgpr_write_b32 a60, v174
v_accvgpr_write_b32 a61, v175
v_accvgpr_write_b32 a62, v176
v_accvgpr_write_b32 a63, v177
v_mfma_f32_16x16x32_bf16 v[154:157], v[12:15], a[72:75], v[126:129]
v_cvt_pk_bf16_f32 v170, v138, v139
v_cvt_pk_bf16_f32 v171, v140, v141
v_cvt_pk_bf16_f32 v172, v142, v143
v_cvt_pk_bf16_f32 v173, v144, v145
v_mfma_f32_16x16x32_bf16 v[158:161], v[10:13], a[72:75], v[126:129]
v_accvgpr_write_b32 a64, v170
v_accvgpr_write_b32 a65, v171
v_accvgpr_write_b32 a66, v172
v_accvgpr_write_b32 a67, v173
v_mfma_f32_16x16x32_bf16 v[162:165], v[12:15], a[76:79], v[130:133]
v_cvt_pk_bf16_f32 v174, v146, v147
v_cvt_pk_bf16_f32 v175, v148, v149
v_cvt_pk_bf16_f32 v176, v150, v151
v_cvt_pk_bf16_f32 v177, v152, v153
v_mfma_f32_16x16x32_bf16 v[166:169], v[10:13], a[76:79], v[130:133]
v_accvgpr_write_b32 a68, v174
v_accvgpr_write_b32 a69, v175
v_accvgpr_write_b32 a70, v176
v_accvgpr_write_b32 a71, v177
v_mfma_f32_16x16x32_bf16 v[138:141], v[12:15], a[80:83], v[118:121]
v_cvt_pk_bf16_f32 v170, v154, v155
v_cvt_pk_bf16_f32 v171, v156, v157
v_cvt_pk_bf16_f32 v172, v158, v159
v_cvt_pk_bf16_f32 v173, v160, v161
v_mfma_f32_16x16x32_bf16 v[142:145], v[10:13], a[80:83], v[118:121]
v_accvgpr_write_b32 a72, v170
v_accvgpr_write_b32 a73, v171
v_accvgpr_write_b32 a74, v172
v_accvgpr_write_b32 a75, v173
v_mfma_f32_16x16x32_bf16 v[146:149], v[12:15], a[84:87], v[122:125]
v_cvt_pk_bf16_f32 v174, v162, v163
v_cvt_pk_bf16_f32 v175, v164, v165
v_cvt_pk_bf16_f32 v176, v166, v167
v_cvt_pk_bf16_f32 v177, v168, v169
v_mfma_f32_16x16x32_bf16 v[150:153], v[10:13], a[84:87], v[122:125]
v_accvgpr_write_b32 a76, v174
v_accvgpr_write_b32 a77, v175
v_accvgpr_write_b32 a78, v176
v_accvgpr_write_b32 a79, v177
v_mfma_f32_16x16x32_bf16 v[154:157], v[12:15], a[88:91], v[126:129]
v_cvt_pk_bf16_f32 v170, v138, v139
v_cvt_pk_bf16_f32 v171, v140, v141
v_cvt_pk_bf16_f32 v172, v142, v143
v_cvt_pk_bf16_f32 v173, v144, v145
v_mfma_f32_16x16x32_bf16 v[158:161], v[10:13], a[88:91], v[126:129]
v_accvgpr_write_b32 a80, v170
v_accvgpr_write_b32 a81, v171
v_accvgpr_write_b32 a82, v172
v_accvgpr_write_b32 a83, v173
v_mfma_f32_16x16x32_bf16 v[162:165], v[12:15], a[92:95], v[130:133]
v_cvt_pk_bf16_f32 v174, v146, v147
v_cvt_pk_bf16_f32 v175, v148, v149
v_cvt_pk_bf16_f32 v176, v150, v151
v_cvt_pk_bf16_f32 v177, v152, v153
v_mfma_f32_16x16x32_bf16 v[166:169], v[10:13], a[92:95], v[130:133]
v_accvgpr_write_b32 a84, v174
v_accvgpr_write_b32 a85, v175
v_accvgpr_write_b32 a86, v176
v_accvgpr_write_b32 a87, v177
v_mfma_f32_16x16x32_bf16 v[138:141], v[12:15], a[96:99], v[118:121]
v_cvt_pk_bf16_f32 v170, v154, v155
v_cvt_pk_bf16_f32 v171, v156, v157
v_cvt_pk_bf16_f32 v172, v158, v159
v_cvt_pk_bf16_f32 v173, v160, v161
v_mfma_f32_16x16x32_bf16 v[142:145], v[10:13], a[96:99], v[118:121]
v_accvgpr_write_b32 a88, v170
v_accvgpr_write_b32 a89, v171
v_accvgpr_write_b32 a90, v172
v_accvgpr_write_b32 a91, v173
v_mfma_f32_16x16x32_bf16 v[146:149], v[12:15], a[100:103], v[122:125]
v_cvt_pk_bf16_f32 v174, v162, v163
v_cvt_pk_bf16_f32 v175, v164, v165
v_cvt_pk_bf16_f32 v176, v166, v167
v_cvt_pk_bf16_f32 v177, v168, v169
v_mfma_f32_16x16x32_bf16 v[150:153], v[10:13], a[100:103], v[122:125]
v_accvgpr_write_b32 a92, v174
v_accvgpr_write_b32 a93, v175
v_accvgpr_write_b32 a94, v176
v_accvgpr_write_b32 a95, v177
v_mfma_f32_16x16x32_bf16 v[154:157], v[12:15], a[104:107], v[126:129]
v_cvt_pk_bf16_f32 v170, v138, v139
v_cvt_pk_bf16_f32 v171, v140, v141
v_cvt_pk_bf16_f32 v172, v142, v143
v_cvt_pk_bf16_f32 v173, v144, v145
v_mfma_f32_16x16x32_bf16 v[158:161], v[10:13], a[104:107], v[126:129]
v_accvgpr_write_b32 a96, v170
v_accvgpr_write_b32 a97, v171
v_accvgpr_write_b32 a98, v172
v_accvgpr_write_b32 a99, v173
v_mfma_f32_16x16x32_bf16 v[162:165], v[12:15], a[108:111], v[130:133]
v_cvt_pk_bf16_f32 v174, v146, v147
v_cvt_pk_bf16_f32 v175, v148, v149
v_cvt_pk_bf16_f32 v176, v150, v151
v_cvt_pk_bf16_f32 v177, v152, v153
v_mfma_f32_16x16x32_bf16 v[166:169], v[10:13], a[108:111], v[130:133]
v_accvgpr_write_b32 a100, v174
v_accvgpr_write_b32 a101, v175
v_accvgpr_write_b32 a102, v176
v_accvgpr_write_b32 a103, v177
v_mfma_f32_16x16x32_bf16 v[138:141], v[12:15], a[112:115], v[118:121]
v_cvt_pk_bf16_f32 v170, v154, v155
v_cvt_pk_bf16_f32 v171, v156, v157
v_cvt_pk_bf16_f32 v172, v158, v159
v_cvt_pk_bf16_f32 v173, v160, v161
v_mfma_f32_16x16x32_bf16 v[142:145], v[10:13], a[112:115], v[118:121]
v_accvgpr_write_b32 a104, v170
v_accvgpr_write_b32 a105, v171
v_accvgpr_write_b32 a106, v172
v_accvgpr_write_b32 a107, v173
v_mfma_f32_16x16x32_bf16 v[146:149], v[12:15], a[116:119], v[122:125]
v_cvt_pk_bf16_f32 v174, v162, v163
v_cvt_pk_bf16_f32 v175, v164, v165
v_cvt_pk_bf16_f32 v176, v166, v167
v_cvt_pk_bf16_f32 v177, v168, v169
v_mfma_f32_16x16x32_bf16 v[150:153], v[10:13], a[116:119], v[122:125]
v_accvgpr_write_b32 a108, v174
v_accvgpr_write_b32 a109, v175
v_accvgpr_write_b32 a110, v176
v_accvgpr_write_b32 a111, v177
v_mfma_f32_16x16x32_bf16 v[154:157], v[12:15], a[120:123], v[126:129]
v_cvt_pk_bf16_f32 v170, v138, v139
v_cvt_pk_bf16_f32 v171, v140, v141
v_cvt_pk_bf16_f32 v172, v142, v143
v_cvt_pk_bf16_f32 v173, v144, v145
v_mfma_f32_16x16x32_bf16 v[158:161], v[10:13], a[120:123], v[126:129]
v_accvgpr_write_b32 a112, v170
v_accvgpr_write_b32 a113, v171
v_accvgpr_write_b32 a114, v172
v_accvgpr_write_b32 a115, v173
v_mfma_f32_16x16x32_bf16 v[162:165], v[12:15], a[124:127], v[130:133]
v_cvt_pk_bf16_f32 v174, v146, v147
v_cvt_pk_bf16_f32 v175, v148, v149
v_cvt_pk_bf16_f32 v176, v150, v151
v_cvt_pk_bf16_f32 v177, v152, v153
v_mfma_f32_16x16x32_bf16 v[166:169], v[10:13], a[124:127], v[130:133]
v_accvgpr_write_b32 a116, v174
v_accvgpr_write_b32 a117, v175
v_accvgpr_write_b32 a118, v176
v_accvgpr_write_b32 a119, v177
v_mfma_f32_16x16x32_bf16 v[138:141], v[12:15], a[128:131], v[118:121]
v_cvt_pk_bf16_f32 v170, v154, v155
v_cvt_pk_bf16_f32 v171, v156, v157
v_cvt_pk_bf16_f32 v172, v158, v159
v_cvt_pk_bf16_f32 v173, v160, v161
v_mfma_f32_16x16x32_bf16 v[142:145], v[10:13], a[128:131], v[118:121]
v_accvgpr_write_b32 a120, v170
v_accvgpr_write_b32 a121, v171
v_accvgpr_write_b32 a122, v172
v_accvgpr_write_b32 a123, v173
v_mfma_f32_16x16x32_bf16 v[146:149], v[12:15], a[132:135], v[122:125]
v_cvt_pk_bf16_f32 v174, v162, v163
v_cvt_pk_bf16_f32 v175, v164, v165
v_cvt_pk_bf16_f32 v176, v166, v167
v_cvt_pk_bf16_f32 v177, v168, v169
v_mfma_f32_16x16x32_bf16 v[150:153], v[10:13], a[132:135], v[122:125]
v_accvgpr_write_b32 a124, v174
v_accvgpr_write_b32 a125, v175
v_accvgpr_write_b32 a126, v176
v_accvgpr_write_b32 a127, v177
v_mfma_f32_16x16x32_bf16 v[154:157], v[12:15], a[136:139], v[126:129]
v_cvt_pk_bf16_f32 v170, v138, v139
v_cvt_pk_bf16_f32 v171, v140, v141
v_cvt_pk_bf16_f32 v172, v142, v143
v_cvt_pk_bf16_f32 v173, v144, v145
v_mfma_f32_16x16x32_bf16 v[158:161], v[10:13], a[136:139], v[126:129]
v_accvgpr_write_b32 a128, v170
v_accvgpr_write_b32 a129, v171
v_accvgpr_write_b32 a130, v172
v_accvgpr_write_b32 a131, v173
v_mfma_f32_16x16x32_bf16 v[162:165], v[12:15], a[140:143], v[130:133]
v_cvt_pk_bf16_f32 v174, v146, v147
v_cvt_pk_bf16_f32 v175, v148, v149
v_cvt_pk_bf16_f32 v176, v150, v151
v_cvt_pk_bf16_f32 v177, v152, v153
v_mfma_f32_16x16x32_bf16 v[166:169], v[10:13], a[140:143], v[130:133]
v_accvgpr_write_b32 a132, v174
v_accvgpr_write_b32 a133, v175
v_accvgpr_write_b32 a134, v176
v_accvgpr_write_b32 a135, v177
v_mfma_f32_16x16x32_bf16 v[138:141], v[12:15], a[144:147], v[118:121]
v_cvt_pk_bf16_f32 v170, v154, v155
v_cvt_pk_bf16_f32 v171, v156, v157
v_cvt_pk_bf16_f32 v172, v158, v159
v_cvt_pk_bf16_f32 v173, v160, v161
v_mfma_f32_16x16x32_bf16 v[142:145], v[10:13], a[144:147], v[118:121]
v_accvgpr_write_b32 a136, v170
v_accvgpr_write_b32 a137, v171
v_accvgpr_write_b32 a138, v172
v_accvgpr_write_b32 a139, v173
v_mfma_f32_16x16x32_bf16 v[146:149], v[12:15], a[148:151], v[122:125]
v_cvt_pk_bf16_f32 v174, v162, v163
v_cvt_pk_bf16_f32 v175, v164, v165
v_cvt_pk_bf16_f32 v176, v166, v167
v_cvt_pk_bf16_f32 v177, v168, v169
v_mfma_f32_16x16x32_bf16 v[150:153], v[10:13], a[148:151], v[122:125]
v_accvgpr_write_b32 a140, v174
v_accvgpr_write_b32 a141, v175
v_accvgpr_write_b32 a142, v176
v_accvgpr_write_b32 a143, v177
v_mfma_f32_16x16x32_bf16 v[154:157], v[12:15], a[152:155], v[126:129]
v_cvt_pk_bf16_f32 v170, v138, v139
v_cvt_pk_bf16_f32 v171, v140, v141
v_cvt_pk_bf16_f32 v172, v142, v143
v_cvt_pk_bf16_f32 v173, v144, v145
v_mfma_f32_16x16x32_bf16 v[158:161], v[10:13], a[152:155], v[126:129]
v_accvgpr_write_b32 a144, v170
v_accvgpr_write_b32 a145, v171
v_accvgpr_write_b32 a146, v172
v_accvgpr_write_b32 a147, v173
v_mfma_f32_16x16x32_bf16 v[162:165], v[12:15], a[156:159], v[130:133]
v_cvt_pk_bf16_f32 v174, v146, v147
v_cvt_pk_bf16_f32 v175, v148, v149
v_cvt_pk_bf16_f32 v176, v150, v151
v_cvt_pk_bf16_f32 v177, v152, v153
v_mfma_f32_16x16x32_bf16 v[166:169], v[10:13], a[156:159], v[130:133]
v_accvgpr_write_b32 a148, v174
v_accvgpr_write_b32 a149, v175
v_accvgpr_write_b32 a150, v176
v_accvgpr_write_b32 a151, v177
v_mfma_f32_16x16x32_bf16 v[138:141], v[12:15], a[160:163], v[118:121]
v_cvt_pk_bf16_f32 v170, v154, v155
v_cvt_pk_bf16_f32 v171, v156, v157
v_cvt_pk_bf16_f32 v172, v158, v159
v_cvt_pk_bf16_f32 v173, v160, v161
v_mfma_f32_16x16x32_bf16 v[142:145], v[10:13], a[160:163], v[118:121]
v_accvgpr_write_b32 a152, v170
v_accvgpr_write_b32 a153, v171
v_accvgpr_write_b32 a154, v172
v_accvgpr_write_b32 a155, v173
v_mfma_f32_16x16x32_bf16 v[146:149], v[12:15], a[164:167], v[122:125]
v_cvt_pk_bf16_f32 v174, v162, v163
v_cvt_pk_bf16_f32 v175, v164, v165
v_cvt_pk_bf16_f32 v176, v166, v167
v_cvt_pk_bf16_f32 v177, v168, v169
v_mfma_f32_16x16x32_bf16 v[150:153], v[10:13], a[164:167], v[122:125]
v_accvgpr_write_b32 a156, v174
v_accvgpr_write_b32 a157, v175
v_accvgpr_write_b32 a158, v176
v_accvgpr_write_b32 a159, v177
v_mfma_f32_16x16x32_bf16 v[154:157], v[12:15], a[168:171], v[126:129]
v_cvt_pk_bf16_f32 v170, v138, v139
v_cvt_pk_bf16_f32 v171, v140, v141
v_cvt_pk_bf16_f32 v172, v142, v143
v_cvt_pk_bf16_f32 v173, v144, v145
v_mfma_f32_16x16x32_bf16 v[158:161], v[10:13], a[168:171], v[126:129]
v_accvgpr_write_b32 a160, v170
v_accvgpr_write_b32 a161, v171
v_accvgpr_write_b32 a162, v172
v_accvgpr_write_b32 a163, v173
v_mfma_f32_16x16x32_bf16 v[162:165], v[12:15], a[172:175], v[130:133]
v_cvt_pk_bf16_f32 v174, v146, v147
v_cvt_pk_bf16_f32 v175, v148, v149
v_cvt_pk_bf16_f32 v176, v150, v151
v_cvt_pk_bf16_f32 v177, v152, v153
v_mfma_f32_16x16x32_bf16 v[166:169], v[10:13], a[172:175], v[130:133]
v_accvgpr_write_b32 a164, v174
v_accvgpr_write_b32 a165, v175
v_accvgpr_write_b32 a166, v176
v_accvgpr_write_b32 a167, v177
v_mfma_f32_16x16x32_bf16 v[138:141], v[12:15], a[176:179], v[118:121]
v_cvt_pk_bf16_f32 v170, v154, v155
v_cvt_pk_bf16_f32 v171, v156, v157
v_cvt_pk_bf16_f32 v172, v158, v159
v_cvt_pk_bf16_f32 v173, v160, v161
v_mfma_f32_16x16x32_bf16 v[142:145], v[10:13], a[176:179], v[118:121]
v_accvgpr_write_b32 a168, v170
v_accvgpr_write_b32 a169, v171
v_accvgpr_write_b32 a170, v172
v_accvgpr_write_b32 a171, v173
v_mfma_f32_16x16x32_bf16 v[146:149], v[12:15], a[180:183], v[122:125]
v_cvt_pk_bf16_f32 v174, v162, v163
v_cvt_pk_bf16_f32 v175, v164, v165
v_cvt_pk_bf16_f32 v176, v166, v167
v_cvt_pk_bf16_f32 v177, v168, v169
v_mfma_f32_16x16x32_bf16 v[150:153], v[10:13], a[180:183], v[122:125]
v_accvgpr_write_b32 a172, v174
v_accvgpr_write_b32 a173, v175
v_accvgpr_write_b32 a174, v176
v_accvgpr_write_b32 a175, v177
v_mfma_f32_16x16x32_bf16 v[154:157], v[12:15], a[184:187], v[126:129]
v_cvt_pk_bf16_f32 v170, v138, v139
v_cvt_pk_bf16_f32 v171, v140, v141
v_cvt_pk_bf16_f32 v172, v142, v143
v_cvt_pk_bf16_f32 v173, v144, v145
v_mfma_f32_16x16x32_bf16 v[158:161], v[10:13], a[184:187], v[126:129]
v_accvgpr_write_b32 a176, v170
v_accvgpr_write_b32 a177, v171
v_accvgpr_write_b32 a178, v172
v_accvgpr_write_b32 a179, v173
v_mfma_f32_16x16x32_bf16 v[162:165], v[12:15], a[188:191], v[130:133]
)"
R"(v_cvt_pk_bf16_f32 v174, v146, v147
v_cvt_pk_bf16_f32 v175, v148, v149
v_cvt_pk_bf16_f32 v176, v150, v151
v_cvt_pk_bf16_f32 v177, v152, v153
v_mfma_f32_16x16x32_bf16 v[166:169], v[10:13], a[188:191], v[130:133]
v_accvgpr_write_b32 a180, v174
v_accvgpr_write_b32 a181, v175
v_accvgpr_write_b32 a182, v176
v_accvgpr_write_b32 a183, v177
v_mfma_f32_16x16x32_bf16 v[138:141], v[12:15], a[192:195], v[118:121]
v_cvt_pk_bf16_f32 v170, v154, v155
v_cvt_pk_bf16_f32 v171, v156, v157
v_cvt_pk_bf16_f32 v172, v158, v159
v_cvt_pk_bf16_f32 v173, v160, v161
v_mfma_f32_16x16x32_bf16 v[142:145], v[10:13], a[192:195], v[118:121]
v_accvgpr_write_b32 a184, v170
v_accvgpr_write_b32 a185, v171
v_accvgpr_write_b32 a186, v172
v_accvgpr_write_b32 a187, v173
v_mfma_f32_16x16x32_bf16 v[146:149], v[12:15], a[196:199], v[122:125]
v_cvt_pk_bf16_f32 v174, v162, v163
v_cvt_pk_bf16_f32 v175, v164, v165
v_cvt_pk_bf16_f32 v176, v166, v167
v_cvt_pk_bf16_f32 v177, v168, v169
v_mfma_f32_16x16x32_bf16 v[150:153], v[10:13], a[196:199], v[122:125]
v_accvgpr_write_b32 a188, v174
v_accvgpr_write_b32 a189, v175
v_accvgpr_write_b32 a190, v176
v_accvgpr_write_b32 a191, v177
v_mfma_f32_16x16x32_bf16 v[154:157], v[12:15], a[200:203], v[126:129]
v_cvt_pk_bf16_f32 v170, v138, v139
v_cvt_pk_bf16_f32 v171, v140, v141
v_cvt_pk_bf16_f32 v172, v142, v143
v_cvt_pk_bf16_f32 v173, v144, v145
v_mfma_f32_16x16x32_bf16 v[158:161], v[10:13], a[200:203], v[126:129]
v_accvgpr_write_b32 a192, v170
v_accvgpr_write_b32 a193, v171
v_accvgpr_write_b32 a194, v172
v_accvgpr_write_b32 a195, v173
v_mfma_f32_16x16x32_bf16 v[162:165], v[12:15], a[204:207], v[130:133]
v_cvt_pk_bf16_f32 v174, v146, v147
v_cvt_pk_bf16_f32 v175, v148, v149
v_cvt_pk_bf16_f32 v176, v150, v151
v_cvt_pk_bf16_f32 v177, v152, v153
v_mfma_f32_16x16x32_bf16 v[166:169], v[10:13], a[204:207], v[130:133]
v_accvgpr_write_b32 a196, v174
v_accvgpr_write_b32 a197, v175
v_accvgpr_write_b32 a198, v176
v_accvgpr_write_b32 a199, v177
v_mfma_f32_16x16x32_bf16 v[138:141], v[12:15], a[208:211], v[118:121]
v_cvt_pk_bf16_f32 v170, v154, v155
v_cvt_pk_bf16_f32 v171, v156, v157
v_cvt_pk_bf16_f32 v172, v158, v159
v_cvt_pk_bf16_f32 v173, v160, v161
v_mfma_f32_16x16x32_bf16 v[142:145], v[10:13], a[208:211], v[118:121]
v_accvgpr_write_b32 a200, v170
v_accvgpr_write_b32 a201, v171
v_accvgpr_write_b32 a202, v172
v_accvgpr_write_b32 a203, v173
v_mfma_f32_16x16x32_bf16 v[146:149], v[12:15], a[212:215], v[122:125]
v_cvt_pk_bf16_f32 v174, v162, v163
v_cvt_pk_bf16_f32 v175, v164, v165
v_cvt_pk_bf16_f32 v176, v166, v167
v_cvt_pk_bf16_f32 v177, v168, v169
v_mfma_f32_16x16x32_bf16 v[150:153], v[10:13], a[212:215], v[122:125]
v_accvgpr_write_b32 a204, v174
v_accvgpr_write_b32 a205, v175
v_accvgpr_write_b32 a206, v176
v_accvgpr_write_b32 a207, v177
v_mfma_f32_16x16x32_bf16 v[154:157], v[12:15], a[216:219], v[126:129]
v_cvt_pk_bf16_f32 v170, v138, v139
v_cvt_pk_bf16_f32 v171, v140, v141
v_cvt_pk_bf16_f32 v172, v142, v143
v_cvt_pk_bf16_f32 v173, v144, v145
v_mfma_f32_16x16x32_bf16 v[158:161], v[10:13], a[216:219], v[126:129]
v_accvgpr_write_b32 a208, v170
v_accvgpr_write_b32 a209, v171
v_accvgpr_write_b32 a210, v172
v_accvgpr_write_b32 a211, v173
v_mfma_f32_16x16x32_bf16 v[162:165], v[12:15], a[220:223], v[130:133]
v_cvt_pk_bf16_f32 v174, v146, v147
v_cvt_pk_bf16_f32 v175, v148, v149
v_cvt_pk_bf16_f32 v176, v150, v151
v_cvt_pk_bf16_f32 v177, v152, v153
v_mfma_f32_16x16x32_bf16 v[166:169], v[10:13], a[220:223], v[130:133]
v_accvgpr_write_b32 a212, v174
v_accvgpr_write_b32 a213, v175
v_accvgpr_write_b32 a214, v176
v_accvgpr_write_b32 a215, v177
v_mfma_f32_16x16x32_bf16 v[138:141], v[12:15], a[224:227], v[118:121]
v_cvt_pk_bf16_f32 v170, v154, v155
v_cvt_pk_bf16_f32 v171, v156, v157
v_cvt_pk_bf16_f32 v172, v158, v159
v_cvt_pk_bf16_f32 v173, v160, v161
v_mfma_f32_16x16x32_bf16 v[142:145], v[10:13], a[224:227], v[118:121]
v_accvgpr_write_b32 a216, v170
v_accvgpr_write_b32 a217, v171
v_accvgpr_write_b32 a218, v172
v_accvgpr_write_b32 a219, v173
v_mfma_f32_16x16x32_bf16 v[146:149], v[12:15], a[228:231], v[122:125]
v_cvt_pk_bf16_f32 v174, v162, v163
v_cvt_pk_bf16_f32 v175, v164, v165
v_cvt_pk_bf16_f32 v176, v166, v167
v_cvt_pk_bf16_f32 v177, v168, v169
v_mfma_f32_16x16x32_bf16 v[150:153], v[10:13], a[228:231], v[122:125]
v_accvgpr_write_b32 a220, v174
v_accvgpr_write_b32 a221, v175
v_accvgpr_write_b32 a222, v176
v_accvgpr_write_b32 a223, v177
v_mfma_f32_16x16x32_bf16 v[154:157], v[12:15], a[232:235], v[126:129]
v_cvt_pk_bf16_f32 v170, v138, v139
v_cvt_pk_bf16_f32 v171, v140, v141
v_cvt_pk_bf16_f32 v172, v142, v143
v_cvt_pk_bf16_f32 v173, v144, v145
v_mfma_f32_16x16x32_bf16 v[158:161], v[10:13], a[232:235], v[126:129]
v_accvgpr_write_b32 a224, v170
v_accvgpr_write_b32 a225, v171
v_accvgpr_write_b32 a226, v172
v_accvgpr_write_b32 a227, v173
v_mfma_f32_16x16x32_bf16 v[162:165], v[12:15], a[236:239], v[130:133]
v_cvt_pk_bf16_f32 v174, v146, v147
v_cvt_pk_bf16_f32 v175, v148, v149
v_cvt_pk_bf16_f32 v176, v150, v151
v_cvt_pk_bf16_f32 v177, v152, v153
v_mfma_f32_16x16x32_bf16 v[166:169], v[10:13], a[236:239], v[130:133]
v_accvgpr_write_b32 a228, v174
v_accvgpr_write_b32 a229, v175
v_accvgpr_write_b32 a230, v176
v_accvgpr_write_b32 a231, v177
v_mfma_f32_16x16x32_bf16 v[138:141], v[12:15], a[240:243], v[118:121]
v_cvt_pk_bf16_f32 v170, v154, v155
v_cvt_pk_bf16_f32 v171, v156, v157
v_cvt_pk_bf16_f32 v172, v158, v159
v_cvt_pk_bf16_f32 v173, v160, v161
v_mfma_f32_16x16x32_bf16 v[142:145], v[10:13], a[240:243], v[118:121]
v_accvgpr_write_b32 a232, v170
v_accvgpr_write_b32 a233, v171
v_accvgpr_write_b32 a234, v172
v_accvgpr_write_b32 a235, v173
v_mfma_f32_16x16x32_bf16 v[146:149], v[12:15], a[244:247], v[122:125]
v_cvt_pk_bf16_f32 v174, v162, v163
v_cvt_pk_bf16_f32 v175, v164, v165
v_cvt_pk_bf16_f32 v176, v166, v167
v_cvt_pk_bf16_f32 v177, v168, v169
v_mfma_f32_16x16x32_bf16 v[150:153], v[10:13], a[244:247], v[122:125]
v_accvgpr_write_b32 a236, v174
v_accvgpr_write_b32 a237, v175
v_accvgpr_write_b32 a238, v176
v_accvgpr_write_b32 a239, v177
v_mfma_f32_16x16x32_bf16 v[154:157], v[12:15], a[248:251], v[126:129]
v_cvt_pk_bf16_f32 v170, v138, v139
v_cvt_pk_bf16_f32 v171, v140, v141
v_cvt_pk_bf16_f32 v172, v142, v143
v_cvt_pk_bf16_f32 v173, v144, v145
v_mfma_f32_16x16x32_bf16 v[158:161], v[10:13], a[248:251], v[126:129]
v_accvgpr_write_b32 a240, v170
v_accvgpr_write_b32 a241, v171
v_accvgpr_write_b32 a242, v172
v_accvgpr_write_b32 a243, v173
v_mfma_f32_16x16x32_bf16 v[162:165], v[12:15], a[252:255], v[130:133]
v_cvt_pk_bf16_f32 v174, v146, v147
v_cvt_pk_bf16_f32 v175, v148, v149
v_cvt_pk_bf16_f32 v176, v150, v151
v_cvt_pk_bf16_f32 v177, v152, v153
v_mfma_f32_16x16x32_bf16 v[166:169], v[10:13], a[252:255], v[130:133]
v_accvgpr_write_b32 a244, v174
v_accvgpr_write_b32 a245, v175
v_accvgpr_write_b32 a246, v176
v_accvgpr_write_b32 a247, v177
v_mfma_f32_16x16x32_bf16 v[138:141], v[12:15], v[16:19], v[118:121]
v_cvt_pk_bf16_f32 v170, v154, v155
v_cvt_pk_bf16_f32 v171, v156, v157
v_cvt_pk_bf16_f32 v172, v158, v159
v_cvt_pk_bf16_f32 v173, v160, v161
v_mfma_f32_16x16x32_bf16 v[142:145], v[10:13], v[16:19], v[118:121]
v_accvgpr_write_b32 a248, v170
v_accvgpr_write_b32 a249, v171
v_accvgpr_write_b32 a250, v172
v_accvgpr_write_b32 a251, v173
v_mfma_f32_16x16x32_bf16 v[146:149], v[12:15], v[20:23], v[122:125]
v_cvt_pk_bf16_f32 v174, v162, v163
v_cvt_pk_bf16_f32 v175, v164, v165
v_cvt_pk_bf16_f32 v176, v166, v167
v_cvt_pk_bf16_f32 v177, v168, v169
v_mfma_f32_16x16x32_bf16 v[150:153], v[10:13], v[20:23], v[122:125]
v_accvgpr_write_b32 a252, v174
v_accvgpr_write_b32 a253, v175
v_accvgpr_write_b32 a254, v176
v_accvgpr_write_b32 a255, v177
v_mfma_f32_16x16x32_bf16 v[154:157], v[12:15], v[24:27], v[126:129]
v_cvt_pk_bf16_f32 v16, v138, v139
v_cvt_pk_bf16_f32 v17, v140, v141
v_cvt_pk_bf16_f32 v18, v142, v143
v_cvt_pk_bf16_f32 v19, v144, v145
v_mfma_f32_16x16x32_bf16 v[158:161], v[10:13], v[24:27], v[126:129]
v_mfma_f32_16x16x32_bf16 v[162:165], v[12:15], v[28:31], v[130:133]
v_cvt_pk_bf16_f32 v20, v146, v147
v_cvt_pk_bf16_f32 v21, v148, v149
v_cvt_pk_bf16_f32 v22, v150, v151
v_cvt_pk_bf16_f32 v23, v152, v153
v_mfma_f32_16x16x32_bf16 v[166:169], v[10:13], v[28:31], v[130:133]
v_mfma_f32_16x16x32_bf16 v[138:141], v[12:15], v[32:35], v[118:121]
v_cvt_pk_bf16_f32 v24, v154, v155
v_cvt_pk_bf16_f32 v25, v156, v157
v_cvt_pk_bf16_f32 v26, v158, v159
v_cvt_pk_bf16_f32 v27, v160, v161
v_mfma_f32_16x16x32_bf16 v[142:145], v[10:13], v[32:35], v[118:121]
v_mfma_f32_16x16x32_bf16 v[146:149], v[12:15], v[36:39], v[122:125]
v_cvt_pk_bf16_f32 v28, v162, v163
v_cvt_pk_bf16_f32 v29, v164, v165
v_cvt_pk_bf16_f32 v30, v166, v167
v_cvt_pk_bf16_f32 v31, v168, v169
v_mfma_f32_16x16x32_bf16 v[150:153], v[10:13], v[36:39], v[122:125]
v_mfma_f32_16x16x32_bf16 v[154:157], v[12:15], v[40:43], v[126:129]
v_cvt_pk_bf16_f32 v32, v138, v139
v_cvt_pk_bf16_f32 v33, v140, v141
v_cvt_pk_bf16_f32 v34, v142, v143
v_cvt_pk_bf16_f32 v35, v144, v145
v_mfma_f32_16x16x32_bf16 v[158:161], v[10:13], v[40:43], v[126:129]
v_mfma_f32_16x16x32_bf16 v[162:165], v[12:15], v[44:47], v[130:133]
v_cvt_pk_bf16_f32 v36, v146, v147
v_cvt_pk_bf16_f32 v37, v148, v149
v_cvt_pk_bf16_f32 v38, v150, v151
v_cvt_pk_bf16_f32 v39, v152, v153
v_mfma_f32_16x16x32_bf16 v[166:169], v[10:13], v[44:47], v[130:133]
v_cvt_pk_bf16_f32 v40, v154, v155
v_cvt_pk_bf16_f32 v41, v156, v157
v_cvt_pk_bf16_f32 v42, v158, v159
v_cvt_pk_bf16_f32 v43, v160, v161
v_cvt_pk_bf16_f32 v44, v162, v163
v_cvt_pk_bf16_f32 v45, v164, v165
s_nop 1
v_cvt_pk_bf16_f32 v46, v166, v167
v_cvt_pk_bf16_f32 v47, v168, v169
s_barrier
s_and_b32 s75, s36, 3
s_cmp_eq_u32 s75, 1
s_cbranch_scc1 .Lgu_w2
s_cmp_eq_u32 s75, 2
s_cbranch_scc1 .Lgu_w3
s_cmp_eq_u32 s75, 3
s_cbranch_scc1 .Lgu_w4
.Lgu_w1:
v_add_u32 v108, -1, v5
s_add_u32 m0, s43, 18432
s_mov_b32 s70, 32
s_waitcnt vmcnt(0) lgkmcnt(0)
s_nop 7
.p2align 6
.Lgu_top5:
s_waitcnt lgkmcnt(2)
v_mfma_f32_16x16x32_bf16 v[48:51], a[0:3], v[80:83], 0
ds_read_b128 v[92:95], v8 offset:3072
v_mfma_f32_16x16x32_bf16 v[52:55], a[4:7], v[80:83], 0
v_mfma_f32_16x16x32_bf16 v[56:59], a[8:11], v[80:83], 0
v_mfma_f32_16x16x32_bf16 v[60:63], a[12:15], v[80:83], 0
s_waitcnt lgkmcnt(2)
v_mul_f32 v96, v64, v72
v_mul_f32 v97, v65, v73
v_mfma_f32_16x16x32_bf16 v[48:51], a[16:19], v[84:87], v[48:51]
ds_read_b128 v[80:83], v8 offset:4096
v_mul_f32 v98, v66, v74
v_mfma_f32_16x16x32_bf16 v[52:55], a[20:23], v[84:87], v[52:55]
v_mul_f32 v99, v67, v75
v_mul_f32 v100, v68, v76
v_mfma_f32_16x16x32_bf16 v[56:59], a[24:27], v[84:87], v[56:59]
v_mul_f32 v101, v69, v77
v_mul_f32 v102, v70, v78
v_mfma_f32_16x16x32_bf16 v[60:63], a[28:31], v[84:87], v[60:63]
s_waitcnt lgkmcnt(2)
v_mfma_f32_16x16x32_bf16 v[48:51], a[32:35], v[88:91], v[48:51]
s_waitcnt vmcnt(5)
s_barrier
ds_read_b128 v[84:87], v8 offset:5120
v_mfma_f32_16x16x32_bf16 v[52:55], a[36:39], v[88:91], v[52:55]
v_mul_f32 v103, v71, v79
v_cvt_pk_bf16_f32 v104, v96, v97
v_mfma_f32_16x16x32_bf16 v[56:59], a[40:43], v[88:91], v[56:59]
v_cvt_pk_bf16_f32 v105, v98, v99
v_cvt_pk_bf16_f32 v106, v100, v101
v_mfma_f32_16x16x32_bf16 v[60:63], a[44:47], v[88:91], v[60:63]
s_waitcnt lgkmcnt(2)
v_cvt_pk_bf16_f32 v107, v102, v103
v_mfma_f32_16x16x32_bf16 v[48:51], a[48:51], v[92:95], v[48:51]
ds_read_b128 v[88:91], v8 offset:6144
v_mfma_f32_16x16x32_bf16 v[52:55], a[52:55], v[92:95], v[52:55]
global_store_dwordx4 v3, v[104:107], s[38:39] sc1
v_mfma_f32_16x16x32_bf16 v[56:59], a[56:59], v[92:95], v[56:59]
v_add_u32 v3, 147456, v3
v_mfma_f32_16x16x32_bf16 v[60:63], a[60:63], v[92:95], v[60:63]
s_waitcnt lgkmcnt(2)
v_mfma_f32_16x16x32_bf16 v[48:51], a[64:67], v[80:83], v[48:51]
ds_read_b128 v[92:95], v8 offset:7168
v_mfma_f32_16x16x32_bf16 v[52:55], a[68:71], v[80:83], v[52:55]
v_mfma_f32_16x16x32_bf16 v[56:59], a[72:75], v[80:83], v[56:59]
v_mfma_f32_16x16x32_bf16 v[60:63], a[76:79], v[80:83], v[60:63]
s_waitcnt lgkmcnt(2)
v_mfma_f32_16x16x32_bf16 v[48:51], a[80:83], v[84:87], v[48:51]
ds_read_b128 v[80:83], v8 offset:8192
v_mfma_f32_16x16x32_bf16 v[52:55], a[84:87], v[84:87], v[52:55]
v_mfma_f32_16x16x32_bf16 v[56:59], a[88:91], v[84:87], v[56:59]
v_mfma_f32_16x16x32_bf16 v[60:63], a[92:95], v[84:87], v[60:63]
s_waitcnt lgkmcnt(2)
v_mfma_f32_16x16x32_bf16 v[48:51], a[96:99], v[88:91], v[48:51]
ds_read_b128 v[84:87], v8 offset:9216
v_mfma_f32_16x16x32_bf16 v[52:55], a[100:103], v[88:91], v[52:55]
v_mfma_f32_16x16x32_bf16 v[56:59], a[104:107], v[88:91], v[56:59]
v_mfma_f32_16x16x32_bf16 v[60:63], a[108:111], v[88:91], v[60:63]
s_waitcnt lgkmcnt(2)
v_mfma_f32_16x16x32_bf16 v[48:51], a[112:115], v[92:95], v[48:51]
ds_read_b128 v[88:91], v8 offset:10240
v_mfma_f32_16x16x32_bf16 v[52:55], a[116:119], v[92:95], v[52:55]
v_mfma_f32_16x16x32_bf16 v[56:59], a[120:123], v[92:95], v[56:59]
v_mfma_f32_16x16x32_bf16 v[60:63], a[124:127], v[92:95], v[60:63]
s_waitcnt lgkmcnt(2)
v_mfma_f32_16x16x32_bf16 v[48:51], a[128:131], v[80:83], v[48:51]
ds_read_b128 v[92:95], v8 offset:11264
v_mfma_f32_16x16x32_bf16 v[52:55], a[132:135], v[80:83], v[52:55]
v_mfma_f32_16x16x32_bf16 v[56:59], a[136:139], v[80:83], v[56:59]
v_mfma_f32_16x16x32_bf16 v[60:63], a[140:143], v[80:83], v[60:63]
s_waitcnt lgkmcnt(2)
v_mfma_f32_16x16x32_bf16 v[48:51], a[144:147], v[84:87], v[48:51]
ds_read_b128 v[80:83], v8 offset:12288
v_mfma_f32_16x16x32_bf16 v[52:55], a[148:151], v[84:87], v[52:55]
v_mfma_f32_16x16x32_bf16 v[56:59], a[152:155], v[84:87], v[56:59]
v_mfma_f32_16x16x32_bf16 v[60:63], a[156:159], v[84:87], v[60:63]
s_waitcnt lgkmcnt(2)
v_mfma_f32_16x16x32_bf16 v[48:51], a[160:163], v[88:91], v[48:51]
ds_read_b128 v[84:87], v8 offset:13312
v_mfma_f32_16x16x32_bf16 v[52:55], a[164:167], v[88:91], v[52:55]
s_xor_b32 s43, s43, 0x10000
v_mfma_f32_16x16x32_bf16 v[56:59], a[168:171], v[88:91], v[56:59]
s_add_u32 m0, s43, 0
v_mfma_f32_16x16x32_bf16 v[60:63], a[172:175], v[88:91], v[60:63]
s_waitcnt lgkmcnt(2)
v_mfma_f32_16x16x32_bf16 v[48:51], a[176:179], v[92:95], v[48:51]
s_barrier
ds_read_b128 v[88:91], v8 offset:14336
v_mfma_f32_16x16x32_bf16 v[52:55], a[180:183], v[92:95], v[52:55]
global_load_lds_dwordx4 v2, s[38:39] offset:-2048 sc1
v_mfma_f32_16x16x32_bf16 v[56:59], a[184:187], v[92:95], v[56:59]
v_mfma_f32_16x16x32_bf16 v[60:63], a[188:191], v[92:95], v[60:63]
s_waitcnt lgkmcnt(2)
v_mfma_f32_16x16x32_bf16 v[48:51], a[192:195], v[80:83], v[48:51]
ds_read_b128 v[92:95], v8 offset:15360
v_mfma_f32_16x16x32_bf16 v[52:55], a[196:199], v[80:83], v[52:55]
global_load_lds_dwordx4 v2, s[38:39] offset:-1024 sc1
v_mfma_f32_16x16x32_bf16 v[56:59], a[200:203], v[80:83], v[56:59]
v_mfma_f32_16x16x32_bf16 v[60:63], a[204:207], v[80:83], v[60:63]
s_waitcnt lgkmcnt(2)
v_mfma_f32_16x16x32_bf16 v[48:51], a[208:211], v[84:87], v[48:51]
ds_read_b128 v[80:83], v8 offset:16384
v_mfma_f32_16x16x32_bf16 v[52:55], a[212:215], v[84:87], v[52:55]
global_load_lds_dwordx4 v2, s[38:39] offset:0 sc1
v_mfma_f32_16x16x32_bf16 v[56:59], a[216:219], v[84:87], v[56:59]
v_mfma_f32_16x16x32_bf16 v[60:63], a[220:223], v[84:87], v[60:63]
s_waitcnt lgkmcnt(2)
v_mfma_f32_16x16x32_bf16 v[48:51], a[224:227], v[88:91], v[48:51]
ds_read_b128 v[84:87], v8 offset:17408
v_mfma_f32_16x16x32_bf16 v[52:55], a[228:231], v[88:91], v[52:55]
global_load_lds_dwordx4 v2, s[38:39] offset:1024 sc1
v_mfma_f32_16x16x32_bf16 v[56:59], a[232:235], v[88:91], v[56:59]
v_mfma_f32_16x16x32_bf16 v[60:63], a[236:239], v[88:91], v[60:63]
s_waitcnt lgkmcnt(2)
v_mfma_f32_16x16x32_bf16 v[48:51], a[240:243], v[92:95], v[48:51]
s_waitcnt vmcnt(5)
s_barrier
ds_read_b128 v[88:91], v8 offset:18432
v_mfma_f32_16x16x32_bf16 v[52:55], a[244:247], v[92:95], v[52:55]
global_load_lds_dwordx4 v2, s[38:39] offset:2048 sc1
v_mfma_f32_16x16x32_bf16 v[56:59], a[248:251], v[92:95], v[56:59]
v_add_u32 v2, 18688, v2
v_mfma_f32_16x16x32_bf16 v[60:63], a[252:255], v[92:95], v[60:63]
s_waitcnt lgkmcnt(2)
v_mfma_f32_16x16x32_bf16 v[48:51], v[16:19], v[80:83], v[48:51]
ds_read_b128 v[92:95], v8 offset:19456
v_mfma_f32_16x16x32_bf16 v[52:55], v[20:23], v[80:83], v[52:55]
v_mfma_f32_16x16x32_bf16 v[56:59], v[24:27], v[80:83], v[56:59]
v_mfma_f32_16x16x32_bf16 v[60:63], v[28:31], v[80:83], v[60:63]
s_waitcnt lgkmcnt(2)
v_mfma_f32_16x16x32_bf16 v[48:51], v[32:35], v[84:87], v[48:51]
ds_read_b128 v[80:83], v8 offset:20480
v_mfma_f32_16x16x32_bf16 v[52:55], v[36:39], v[84:87], v[52:55]
v_mfma_f32_16x16x32_bf16 v[56:59], v[40:43], v[84:87], v[56:59]
v_mfma_f32_16x16x32_bf16 v[60:63], v[44:47], v[84:87], v[60:63]
s_waitcnt lgkmcnt(2)
v_mfma_f32_16x16x32_bf16 v[64:67], a[0:3], v[88:91], 0
ds_read_b128 v[84:87], v8 offset:21504
v_mfma_f32_16x16x32_bf16 v[68:71], a[4:7], v[88:91], 0
v_mfma_f32_16x16x32_bf16 v[72:75], a[8:11], v[88:91], 0
v_mfma_f32_16x16x32_bf16 v[76:79], a[12:15], v[88:91], 0
s_waitcnt lgkmcnt(2)
v_mul_f32 v96, v48, v56
v_mul_f32 v97, v49, v57
v_mfma_f32_16x16x32_bf16 v[64:67], a[16:19], v[92:95], v[64:67]
ds_read_b128 v[88:91], v8 offset:22528
v_mul_f32 v98, v50, v58
v_mfma_f32_16x16x32_bf16 v[68:71], a[20:23], v[92:95], v[68:71]
v_mul_f32 v99, v51, v59
v_mul_f32 v100, v52, v60
)"
R"(v_mfma_f32_16x16x32_bf16 v[72:75], a[24:27], v[92:95], v[72:75]
v_mul_f32 v101, v53, v61
v_mul_f32 v102, v54, v62
v_mfma_f32_16x16x32_bf16 v[76:79], a[28:31], v[92:95], v[76:79]
s_waitcnt lgkmcnt(2)
v_mfma_f32_16x16x32_bf16 v[64:67], a[32:35], v[80:83], v[64:67]
s_waitcnt vmcnt(5)
s_barrier
ds_read_b128 v[92:95], v8 offset:23552
v_mfma_f32_16x16x32_bf16 v[68:71], a[36:39], v[80:83], v[68:71]
v_mul_f32 v103, v55, v63
v_cvt_pk_bf16_f32 v104, v96, v97
v_mfma_f32_16x16x32_bf16 v[72:75], a[40:43], v[80:83], v[72:75]
v_cvt_pk_bf16_f32 v105, v98, v99
v_cvt_pk_bf16_f32 v106, v100, v101
v_mfma_f32_16x16x32_bf16 v[76:79], a[44:47], v[80:83], v[76:79]
s_waitcnt lgkmcnt(2)
v_cvt_pk_bf16_f32 v107, v102, v103
v_mfma_f32_16x16x32_bf16 v[64:67], a[48:51], v[84:87], v[64:67]
ds_read_b128 v[80:83], v8 offset:24576
v_mfma_f32_16x16x32_bf16 v[68:71], a[52:55], v[84:87], v[68:71]
global_store_dwordx4 v3, v[104:107], s[38:39] sc1
v_mfma_f32_16x16x32_bf16 v[72:75], a[56:59], v[84:87], v[72:75]
v_add_u32 v3, 147456, v3
v_mfma_f32_16x16x32_bf16 v[76:79], a[60:63], v[84:87], v[76:79]
s_waitcnt lgkmcnt(2)
v_mfma_f32_16x16x32_bf16 v[64:67], a[64:67], v[88:91], v[64:67]
ds_read_b128 v[84:87], v8 offset:25600
v_mfma_f32_16x16x32_bf16 v[68:71], a[68:71], v[88:91], v[68:71]
v_mfma_f32_16x16x32_bf16 v[72:75], a[72:75], v[88:91], v[72:75]
v_mfma_f32_16x16x32_bf16 v[76:79], a[76:79], v[88:91], v[76:79]
s_waitcnt lgkmcnt(2)
v_mfma_f32_16x16x32_bf16 v[64:67], a[80:83], v[92:95], v[64:67]
ds_read_b128 v[88:91], v8 offset:26624
v_mfma_f32_16x16x32_bf16 v[68:71], a[84:87], v[92:95], v[68:71]
v_mfma_f32_16x16x32_bf16 v[72:75], a[88:91], v[92:95], v[72:75]
v_mfma_f32_16x16x32_bf16 v[76:79], a[92:95], v[92:95], v[76:79]
s_waitcnt lgkmcnt(2)
v_mfma_f32_16x16x32_bf16 v[64:67], a[96:99], v[80:83], v[64:67]
ds_read_b128 v[92:95], v8 offset:27648
v_mfma_f32_16x16x32_bf16 v[68:71], a[100:103], v[80:83], v[68:71]
v_mfma_f32_16x16x32_bf16 v[72:75], a[104:107], v[80:83], v[72:75]
v_mfma_f32_16x16x32_bf16 v[76:79], a[108:111], v[80:83], v[76:79]
s_waitcnt lgkmcnt(2)
v_mfma_f32_16x16x32_bf16 v[64:67], a[112:115], v[84:87], v[64:67]
ds_read_b128 v[80:83], v8 offset:28672
v_mfma_f32_16x16x32_bf16 v[68:71], a[116:119], v[84:87], v[68:71]
v_mfma_f32_16x16x32_bf16 v[72:75], a[120:123], v[84:87], v[72:75]
v_mfma_f32_16x16x32_bf16 v[76:79], a[124:127], v[84:87], v[76:79]
s_waitcnt lgkmcnt(2)
v_mfma_f32_16x16x32_bf16 v[64:67], a[128:131], v[88:91], v[64:67]
ds_read_b128 v[84:87], v8 offset:29696
v_mfma_f32_16x16x32_bf16 v[68:71], a[132:135], v[88:91], v[68:71]
v_mfma_f32_16x16x32_bf16 v[72:75], a[136:139], v[88:91], v[72:75]
v_mfma_f32_16x16x32_bf16 v[76:79], a[140:143], v[88:91], v[76:79]
s_waitcnt lgkmcnt(2)
v_mfma_f32_16x16x32_bf16 v[64:67], a[144:147], v[92:95], v[64:67]
ds_read_b128 v[88:91], v8 offset:30720
v_mfma_f32_16x16x32_bf16 v[68:71], a[148:151], v[92:95], v[68:71]
v_mfma_f32_16x16x32_bf16 v[72:75], a[152:155], v[92:95], v[72:75]
v_mfma_f32_16x16x32_bf16 v[76:79], a[156:159], v[92:95], v[76:79]
s_waitcnt lgkmcnt(2)
v_mfma_f32_16x16x32_bf16 v[64:67], a[160:163], v[80:83], v[64:67]
ds_read_b128 v[92:95], v8 offset:31744
v_mfma_f32_16x16x32_bf16 v[68:71], a[164:167], v[80:83], v[68:71]
v_mfma_f32_16x16x32_bf16 v[72:75], a[168:171], v[80:83], v[72:75]
s_add_u32 m0, s43, 18432
v_mfma_f32_16x16x32_bf16 v[76:79], a[172:175], v[80:83], v[76:79]
s_waitcnt lgkmcnt(2)
v_mfma_f32_16x16x32_bf16 v[64:67], a[176:179], v[84:87], v[64:67]
s_barrier
ds_read_b128 v[80:83], v8 offset:32768
v_mfma_f32_16x16x32_bf16 v[68:71], a[180:183], v[84:87], v[68:71]
global_load_lds_dwordx4 v2, s[38:39] offset:-2048 sc1
v_mfma_f32_16x16x32_bf16 v[72:75], a[184:187], v[84:87], v[72:75]
v_mfma_f32_16x16x32_bf16 v[76:79], a[188:191], v[84:87], v[76:79]
s_waitcnt lgkmcnt(2)
v_mfma_f32_16x16x32_bf16 v[64:67], a[192:195], v[88:91], v[64:67]
ds_read_b128 v[84:87], v8 offset:33792
v_mfma_f32_16x16x32_bf16 v[68:71], a[196:199], v[88:91], v[68:71]
global_load_lds_dwordx4 v2, s[38:39] offset:-1024 sc1
v_mfma_f32_16x16x32_bf16 v[72:75], a[200:203], v[88:91], v[72:75]
v_mfma_f32_16x16x32_bf16 v[76:79], a[204:207], v[88:91], v[76:79]
s_waitcnt lgkmcnt(2)
v_mfma_f32_16x16x32_bf16 v[64:67], a[208:211], v[92:95], v[64:67]
ds_read_b128 v[88:91], v8 offset:34816
v_mfma_f32_16x16x32_bf16 v[68:71], a[212:215], v[92:95], v[68:71]
global_load_lds_dwordx4 v2, s[38:39] offset:0 sc1
v_mfma_f32_16x16x32_bf16 v[72:75], a[216:219], v[92:95], v[72:75]
v_mfma_f32_16x16x32_bf16 v[76:79], a[220:223], v[92:95], v[76:79]
s_waitcnt lgkmcnt(2)
v_mfma_f32_16x16x32_bf16 v[64:67], a[224:227], v[80:83], v[64:67]
ds_read_b128 v[92:95], v8 offset:35840
v_mfma_f32_16x16x32_bf16 v[68:71], a[228:231], v[80:83], v[68:71]
global_load_lds_dwordx4 v2, s[38:39] offset:1024 sc1
v_mfma_f32_16x16x32_bf16 v[72:75], a[232:235], v[80:83], v[72:75]
v_xor_b32 v8, 0x10000, v8
v_mfma_f32_16x16x32_bf16 v[76:79], a[236:239], v[80:83], v[76:79]
s_waitcnt lgkmcnt(2)
v_mfma_f32_16x16x32_bf16 v[64:67], a[240:243], v[84:87], v[64:67]
s_waitcnt vmcnt(5)
s_barrier
ds_read_b128 v[80:83], v8 offset:0
v_mfma_f32_16x16x32_bf16 v[68:71], a[244:247], v[84:87], v[68:71]
global_load_lds_dwordx4 v2, s[38:39] offset:2048 sc1
v_mfma_f32_16x16x32_bf16 v[72:75], a[248:251], v[84:87], v[72:75]
v_add_u32 v2, 18688, v2
v_mfma_f32_16x16x32_bf16 v[76:79], a[252:255], v[84:87], v[76:79]
s_waitcnt lgkmcnt(2)
v_mfma_f32_16x16x32_bf16 v[64:67], v[16:19], v[88:91], v[64:67]
ds_read_b128 v[84:87], v8 offset:1024
v_mfma_f32_16x16x32_bf16 v[68:71], v[20:23], v[88:91], v[68:71]
v_mfma_f32_16x16x32_bf16 v[72:75], v[24:27], v[88:91], v[72:75]
v_mfma_f32_16x16x32_bf16 v[76:79], v[28:31], v[88:91], v[76:79]
s_waitcnt lgkmcnt(2)
v_mfma_f32_16x16x32_bf16 v[64:67], v[32:35], v[92:95], v[64:67]
ds_read_b128 v[88:91], v8 offset:2048
v_mfma_f32_16x16x32_bf16 v[68:71], v[36:39], v[92:95], v[68:71]
v_mfma_f32_16x16x32_bf16 v[72:75], v[40:43], v[92:95], v[72:75]
v_mfma_f32_16x16x32_bf16 v[76:79], v[44:47], v[92:95], v[76:79]
s_sub_u32 s70, s70, 1
s_cbranch_scc0 .Lgu_top5
s_nop 4
v_mul_f32 v96, v64, v72
v_mul_f32 v97, v65, v73
v_mul_f32 v98, v66, v74
v_mul_f32 v99, v67, v75
v_mul_f32 v100, v68, v76
v_mul_f32 v101, v69, v77
v_mul_f32 v102, v70, v78
v_mul_f32 v103, v71, v79
v_cvt_pk_bf16_f32 v104, v96, v97
v_cvt_pk_bf16_f32 v105, v98, v99
v_cvt_pk_bf16_f32 v106, v100, v101
v_cvt_pk_bf16_f32 v107, v102, v103
global_store_dwordx4 v3, v[104:107], s[38:39] sc1
s_waitcnt vmcnt(1)
s_barrier
s_waitcnt vmcnt(0)
s_barrier
s_endpgm
.Lgu_w2:
v_add_u32 v108, -1, v5
s_add_u32 m0, s43, 18432
s_mov_b32 s70, 32
s_waitcnt vmcnt(0) lgkmcnt(0)
s_nop 7
.p2align 6
.Lgu_top6:
s_waitcnt lgkmcnt(2)
v_mfma_f32_16x16x32_bf16 v[48:51], a[0:3], v[80:83], 0
ds_read_b128 v[92:95], v8 offset:3072
v_mfma_f32_16x16x32_bf16 v[52:55], a[4:7], v[80:83], 0
v_mfma_f32_16x16x32_bf16 v[56:59], a[8:11], v[80:83], 0
v_mfma_f32_16x16x32_bf16 v[60:63], a[12:15], v[80:83], 0
s_waitcnt lgkmcnt(2)
v_mul_f32 v96, v64, v72
v_mul_f32 v97, v65, v73
v_mfma_f32_16x16x32_bf16 v[48:51], a[16:19], v[84:87], v[48:51]
ds_read_b128 v[80:83], v8 offset:4096
v_mul_f32 v98, v66, v74
v_mfma_f32_16x16x32_bf16 v[52:55], a[20:23], v[84:87], v[52:55]
v_mul_f32 v99, v67, v75
v_mul_f32 v100, v68, v76
v_mfma_f32_16x16x32_bf16 v[56:59], a[24:27], v[84:87], v[56:59]
v_mul_f32 v101, v69, v77
v_mul_f32 v102, v70, v78
v_mfma_f32_16x16x32_bf16 v[60:63], a[28:31], v[84:87], v[60:63]
s_waitcnt lgkmcnt(2)
v_mfma_f32_16x16x32_bf16 v[48:51], a[32:35], v[88:91], v[48:51]
s_waitcnt vmcnt(5)
s_barrier
ds_read_b128 v[84:87], v8 offset:5120
v_mfma_f32_16x16x32_bf16 v[52:55], a[36:39], v[88:91], v[52:55]
v_mul_f32 v103, v71, v79
v_cvt_pk_bf16_f32 v104, v96, v97
v_mfma_f32_16x16x32_bf16 v[56:59], a[40:43], v[88:91], v[56:59]
v_cvt_pk_bf16_f32 v105, v98, v99
v_cvt_pk_bf16_f32 v106, v100, v101
v_mfma_f32_16x16x32_bf16 v[60:63], a[44:47], v[88:91], v[60:63]
s_waitcnt lgkmcnt(2)
v_cvt_pk_bf16_f32 v107, v102, v103
v_mfma_f32_16x16x32_bf16 v[48:51], a[48:51], v[92:95], v[48:51]
ds_read_b128 v[88:91], v8 offset:6144
v_mfma_f32_16x16x32_bf16 v[52:55], a[52:55], v[92:95], v[52:55]
global_store_dwordx4 v3, v[104:107], s[38:39] sc1
v_mfma_f32_16x16x32_bf16 v[56:59], a[56:59], v[92:95], v[56:59]
v_add_u32 v3, 147456, v3
v_mfma_f32_16x16x32_bf16 v[60:63], a[60:63], v[92:95], v[60:63]
s_waitcnt lgkmcnt(2)
v_mfma_f32_16x16x32_bf16 v[48:51], a[64:67], v[80:83], v[48:51]
ds_read_b128 v[92:95], v8 offset:7168
v_mfma_f32_16x16x32_bf16 v[52:55], a[68:71], v[80:83], v[52:55]
v_mfma_f32_16x16x32_bf16 v[56:59], a[72:75], v[80:83], v[56:59]
v_mfma_f32_16x16x32_bf16 v[60:63], a[76:79], v[80:83], v[60:63]
s_waitcnt lgkmcnt(2)
v_mfma_f32_16x16x32_bf16 v[48:51], a[80:83], v[84:87], v[48:51]
ds_read_b128 v[80:83], v8 offset:8192
v_mfma_f32_16x16x32_bf16 v[52:55], a[84:87], v[84:87], v[52:55]
v_mfma_f32_16x16x32_bf16 v[56:59], a[88:91], v[84:87], v[56:59]
v_mfma_f32_16x16x32_bf16 v[60:63], a[92:95], v[84:87], v[60:63]
s_waitcnt lgkmcnt(2)
v_mfma_f32_16x16x32_bf16 v[48:51], a[96:99], v[88:91], v[48:51]
ds_read_b128 v[84:87], v8 offset:9216
v_mfma_f32_16x16x32_bf16 v[52:55], a[100:103], v[88:91], v[52:55]
v_mfma_f32_16x16x32_bf16 v[56:59], a[104:107], v[88:91], v[56:59]
v_mfma_f32_16x16x32_bf16 v[60:63], a[108:111], v[88:91], v[60:63]
s_waitcnt lgkmcnt(2)
v_mfma_f32_16x16x32_bf16 v[48:51], a[112:115], v[92:95], v[48:51]
ds_read_b128 v[88:91], v8 offset:10240
v_mfma_f32_16x16x32_bf16 v[52:55], a[116:119], v[92:95], v[52:55]
v_mfma_f32_16x16x32_bf16 v[56:59], a[120:123], v[92:95], v[56:59]
v_mfma_f32_16x16x32_bf16 v[60:63], a[124:127], v[92:95], v[60:63]
s_waitcnt lgkmcnt(2)
v_mfma_f32_16x16x32_bf16 v[48:51], a[128:131], v[80:83], v[48:51]
ds_read_b128 v[92:95], v8 offset:11264
v_mfma_f32_16x16x32_bf16 v[52:55], a[132:135], v[80:83], v[52:55]
v_mfma_f32_16x16x32_bf16 v[56:59], a[136:139], v[80:83], v[56:59]
v_mfma_f32_16x16x32_bf16 v[60:63], a[140:143], v[80:83], v[60:63]
s_waitcnt lgkmcnt(2)
v_mfma_f32_16x16x32_bf16 v[48:51], a[144:147], v[84:87], v[48:51]
ds_read_b128 v[80:83], v8 offset:12288
v_mfma_f32_16x16x32_bf16 v[52:55], a[148:151], v[84:87], v[52:55]
v_mfma_f32_16x16x32_bf16 v[56:59], a[152:155], v[84:87], v[56:59]
v_mfma_f32_16x16x32_bf16 v[60:63], a[156:159], v[84:87], v[60:63]
s_waitcnt lgkmcnt(2)
v_mfma_f32_16x16x32_bf16 v[48:51], a[160:163], v[88:91], v[48:51]
ds_read_b128 v[84:87], v8 offset:13312
v_mfma_f32_16x16x32_bf16 v[52:55], a[164:167], v[88:91], v[52:55]
s_xor_b32 s43, s43, 0x10000
v_mfma_f32_16x16x32_bf16 v[56:59], a[168:171], v[88:91], v[56:59]
s_add_u32 m0, s43, 0
v_mfma_f32_16x16x32_bf16 v[60:63], a[172:175], v[88:91], v[60:63]
s_waitcnt lgkmcnt(2)
v_mfma_f32_16x16x32_bf16 v[48:51], a[176:179], v[92:95], v[48:51]
s_barrier
ds_read_b128 v[88:91], v8 offset:14336
v_mfma_f32_16x16x32_bf16 v[52:55], a[180:183], v[92:95], v[52:55]
v_mfma_f32_16x16x32_bf16 v[56:59], a[184:187], v[92:95], v[56:59]
global_load_lds_dwordx4 v2, s[38:39] offset:-2048 sc1
v_mfma_f32_16x16x32_bf16 v[60:63], a[188:191], v[92:95], v[60:63]
s_waitcnt lgkmcnt(2)
v_mfma_f32_16x16x32_bf16 v[48:51], a[192:195], v[80:83], v[48:51]
ds_read_b128 v[92:95], v8 offset:15360
v_mfma_f32_16x16x32_bf16 v[52:55], a[196:199], v[80:83], v[52:55]
v_mfma_f32_16x16x32_bf16 v[56:59], a[200:203], v[80:83], v[56:59]
global_load_lds_dwordx4 v2, s[38:39] offset:-1024 sc1
v_mfma_f32_16x16x32_bf16 v[60:63], a[204:207], v[80:83], v[60:63]
s_waitcnt lgkmcnt(2)
v_mfma_f32_16x16x32_bf16 v[48:51], a[208:211], v[84:87], v[48:51]
ds_read_b128 v[80:83], v8 offset:16384
v_mfma_f32_16x16x32_bf16 v[52:55], a[212:215], v[84:87], v[52:55]
v_mfma_f32_16x16x32_bf16 v[56:59], a[216:219], v[84:87], v[56:59]
global_load_lds_dwordx4 v2, s[38:39] offset:0 sc1
v_mfma_f32_16x16x32_bf16 v[60:63], a[220:223], v[84:87], v[60:63]
s_waitcnt lgkmcnt(2)
v_mfma_f32_16x16x32_bf16 v[48:51], a[224:227], v[88:91], v[48:51]
ds_read_b128 v[84:87], v8 offset:17408
v_mfma_f32_16x16x32_bf16 v[52:55], a[228:231], v[88:91], v[52:55]
v_mfma_f32_16x16x32_bf16 v[56:59], a[232:235], v[88:91], v[56:59]
global_load_lds_dwordx4 v2, s[38:39] offset:1024 sc1
v_mfma_f32_16x16x32_bf16 v[60:63], a[236:239], v[88:91], v[60:63]
s_waitcnt lgkmcnt(2)
v_mfma_f32_16x16x32_bf16 v[48:51], a[240:243], v[92:95], v[48:51]
s_waitcnt vmcnt(5)
s_barrier
ds_read_b128 v[88:91], v8 offset:18432
v_mfma_f32_16x16x32_bf16 v[52:55], a[244:247], v[92:95], v[52:55]
v_mfma_f32_16x16x32_bf16 v[56:59], a[248:251], v[92:95], v[56:59]
global_load_lds_dwordx4 v2, s[38:39] offset:2048 sc1
v_mfma_f32_16x16x32_bf16 v[60:63], a[252:255], v[92:95], v[60:63]
s_waitcnt lgkmcnt(2)
v_add_u32 v2, 18688, v2
v_mfma_f32_16x16x32_bf16 v[48:51], v[16:19], v[80:83], v[48:51]
ds_read_b128 v[92:95], v8 offset:19456
v_mfma_f32_16x16x32_bf16 v[52:55], v[20:23], v[80:83], v[52:55]
v_mfma_f32_16x16x32_bf16 v[56:59], v[24:27], v[80:83], v[56:59]
v_mfma_f32_16x16x32_bf16 v[60:63], v[28:31], v[80:83], v[60:63]
s_waitcnt lgkmcnt(2)
v_mfma_f32_16x16x32_bf16 v[48:51], v[32:35], v[84:87], v[48:51]
ds_read_b128 v[80:83], v8 offset:20480
v_mfma_f32_16x16x32_bf16 v[52:55], v[36:39], v[84:87], v[52:55]
v_mfma_f32_16x16x32_bf16 v[56:59], v[40:43], v[84:87], v[56:59]
v_mfma_f32_16x16x32_bf16 v[60:63], v[44:47], v[84:87], v[60:63]
s_waitcnt lgkmcnt(2)
v_mfma_f32_16x16x32_bf16 v[64:67], a[0:3], v[88:91], 0
ds_read_b128 v[84:87], v8 offset:21504
v_mfma_f32_16x16x32_bf16 v[68:71], a[4:7], v[88:91], 0
v_mfma_f32_16x16x32_bf16 v[72:75], a[8:11], v[88:91], 0
v_mfma_f32_16x16x32_bf16 v[76:79], a[12:15], v[88:91], 0
s_waitcnt lgkmcnt(2)
v_mul_f32 v96, v48, v56
v_mul_f32 v97, v49, v57
v_mfma_f32_16x16x32_bf16 v[64:67], a[16:19], v[92:95], v[64:67]
ds_read_b128 v[88:91], v8 offset:22528
v_mul_f32 v98, v50, v58
v_mfma_f32_16x16x32_bf16 v[68:71], a[20:23], v[92:95], v[68:71]
v_mul_f32 v99, v51, v59
v_mul_f32 v100, v52, v60
v_mfma_f32_16x16x32_bf16 v[72:75], a[24:27], v[92:95], v[72:75]
v_mul_f32 v101, v53, v61
v_mul_f32 v102, v54, v62
v_mfma_f32_16x16x32_bf16 v[76:79], a[28:31], v[92:95], v[76:79]
s_waitcnt lgkmcnt(2)
v_mfma_f32_16x16x32_bf16 v[64:67], a[32:35], v[80:83], v[64:67]
s_waitcnt vmcnt(5)
s_barrier
ds_read_b128 v[92:95], v8 offset:23552
v_mfma_f32_16x16x32_bf16 v[68:71], a[36:39], v[80:83], v[68:71]
v_mul_f32 v103, v55, v63
v_cvt_pk_bf16_f32 v104, v96, v97
v_mfma_f32_16x16x32_bf16 v[72:75], a[40:43], v[80:83], v[72:75]
v_cvt_pk_bf16_f32 v105, v98, v99
v_cvt_pk_bf16_f32 v106, v100, v101
v_mfma_f32_16x16x32_bf16 v[76:79], a[44:47], v[80:83], v[76:79]
s_waitcnt lgkmcnt(2)
v_cvt_pk_bf16_f32 v107, v102, v103
v_mfma_f32_16x16x32_bf16 v[64:67], a[48:51], v[84:87], v[64:67]
ds_read_b128 v[80:83], v8 offset:24576
v_mfma_f32_16x16x32_bf16 v[68:71], a[52:55], v[84:87], v[68:71]
global_store_dwordx4 v3, v[104:107], s[38:39] sc1
v_mfma_f32_16x16x32_bf16 v[72:75], a[56:59], v[84:87], v[72:75]
v_add_u32 v3, 147456, v3
v_mfma_f32_16x16x32_bf16 v[76:79], a[60:63], v[84:87], v[76:79]
s_waitcnt lgkmcnt(2)
v_mfma_f32_16x16x32_bf16 v[64:67], a[64:67], v[88:91], v[64:67]
ds_read_b128 v[84:87], v8 offset:25600
v_mfma_f32_16x16x32_bf16 v[68:71], a[68:71], v[88:91], v[68:71]
v_mfma_f32_16x16x32_bf16 v[72:75], a[72:75], v[88:91], v[72:75]
v_mfma_f32_16x16x32_bf16 v[76:79], a[76:79], v[88:91], v[76:79]
s_waitcnt lgkmcnt(2)
v_mfma_f32_16x16x32_bf16 v[64:67], a[80:83], v[92:95], v[64:67]
ds_read_b128 v[88:91], v8 offset:26624
v_mfma_f32_16x16x32_bf16 v[68:71], a[84:87], v[92:95], v[68:71]
v_mfma_f32_16x16x32_bf16 v[72:75], a[88:91], v[92:95], v[72:75]
v_mfma_f32_16x16x32_bf16 v[76:79], a[92:95], v[92:95], v[76:79]
s_waitcnt lgkmcnt(2)
v_mfma_f32_16x16x32_bf16 v[64:67], a[96:99], v[80:83], v[64:67]
ds_read_b128 v[92:95], v8 offset:27648
v_mfma_f32_16x16x32_bf16 v[68:71], a[100:103], v[80:83], v[68:71]
v_mfma_f32_16x16x32_bf16 v[72:75], a[104:107], v[80:83], v[72:75]
v_mfma_f32_16x16x32_bf16 v[76:79], a[108:111], v[80:83], v[76:79]
s_waitcnt lgkmcnt(2)
v_mfma_f32_16x16x32_bf16 v[64:67], a[112:115], v[84:87], v[64:67]
ds_read_b128 v[80:83], v8 offset:28672
v_mfma_f32_16x16x32_bf16 v[68:71], a[116:119], v[84:87], v[68:71]
v_mfma_f32_16x16x32_bf16 v[72:75], a[120:123], v[84:87], v[72:75]
v_mfma_f32_16x16x32_bf16 v[76:79], a[124:127], v[84:87], v[76:79]
s_waitcnt lgkmcnt(2)
v_mfma_f32_16x16x32_bf16 v[64:67], a[128:131], v[88:91], v[64:67]
ds_read_b128 v[84:87], v8 offset:29696
v_mfma_f32_16x16x32_bf16 v[68:71], a[132:135], v[88:91], v[68:71]
v_mfma_f32_16x16x32_bf16 v[72:75], a[136:139], v[88:91], v[72:75]
v_mfma_f32_16x16x32_bf16 v[76:79], a[140:143], v[88:91], v[76:79]
s_waitcnt lgkmcnt(2)
v_mfma_f32_16x16x32_bf16 v[64:67], a[144:147], v[92:95], v[64:67]
ds_read_b128 v[88:91], v8 offset:30720
v_mfma_f32_16x16x32_bf16 v[68:71], a[148:151], v[92:95], v[68:71]
v_mfma_f32_16x16x32_bf16 v[72:75], a[152:155], v[92:95], v[72:75]
v_mfma_f32_16x16x32_bf16 v[76:79], a[156:159], v[92:95], v[76:79]
s_waitcnt lgkmcnt(2)
v_mfma_f32_16x16x32_bf16 v[64:67], a[160:163], v[80:83], v[64:67]
ds_read_b128 v[92:95], v8 offset:31744
v_mfma_f32_16x16x32_bf16 v[68:71], a[164:167], v[80:83], v[68:71]
v_mfma_f32_16x16x32_bf16 v[72:75], a[168:171], v[80:83], v[72:75]
s_add_u32 m0, s43, 18432
v_mfma_f32_16x16x32_bf16 v[76:79], a[172:175], v[80:83], v[76:79]
s_waitcnt lgkmcnt(2)
v_mfma_f32_16x16x32_bf16 v[64:67], a[176:179], v[84:87], v[64:67]
s_barrier
ds_read_b128 v[80:83], v8 offset:32768
v_mfma_f32_16x16x32_bf16 v[68:71], a[180:183], v[84:87], v[68:71]
v_mfma_f32_16x16x32_bf16 v[72:75], a[184:187], v[84:87], v[72:75]
global_load_lds_dwordx4 v2, s[38:39] offset:-2048 sc1
v_mfma_f32_16x16x32_bf16 v[76:79], a[188:191], v[84:87], v[76:79]
s_waitcnt lgkmcnt(2)
v_mfma_f32_16x16x32_bf16 v[64:67], a[192:195], v[88:91], v[64:67]
ds_read_b128 v[84:87], v8 offset:33792
v_mfma_f32_16x16x32_bf16 v[68:71], a[196:199], v[88:91], v[68:71]
v_mfma_f32_16x16x32_bf16 v[72:75], a[200:203], v[88:91], v[72:75]
global_load_lds_dwordx4 v2, s[38:39] offset:-1024 sc1
v_mfma_f32_16x16x32_bf16 v[76:79], a[204:207], v[88:91], v[76:79]
s_waitcnt lgkmcnt(2)
v_mfma_f32_16x16x32_bf16 v[64:67], a[208:211], v[92:95], v[64:67]
ds_read_b128 v[88:91], v8 offset:34816
v_mfma_f32_16x16x32_bf16 v[68:71], a[212:215], v[92:95], v[68:71]
v_mfma_f32_16x16x32_bf16 v[72:75], a[216:219], v[92:95], v[72:75]
global_load_lds_dwordx4 v2, s[38:39] offset:0 sc1
v_mfma_f32_16x16x32_bf16 v[76:79], a[220:223], v[92:95], v[76:79]
s_waitcnt lgkmcnt(2)
v_mfma_f32_16x16x32_bf16 v[64:67], a[224:227], v[80:83], v[64:67]
ds_read_b128 v[92:95], v8 offset:35840
v_mfma_f32_16x16x32_bf16 v[68:71], a[228:231], v[80:83], v[68:71]
v_mfma_f32_16x16x32_bf16 v[72:75], a[232:235], v[80:83], v[72:75]
v_xor_b32 v8, 0x10000, v8
global_load_lds_dwordx4 v2, s[38:39] offset:1024 sc1
v_mfma_f32_16x16x32_bf16 v[76:79], a[236:239], v[80:83], v[76:79]
s_waitcnt lgkmcnt(2)
v_mfma_f32_16x16x32_bf16 v[64:67], a[240:243], v[84:87], v[64:67]
s_waitcnt vmcnt(5)
)"
R"(s_barrier
ds_read_b128 v[80:83], v8 offset:0
v_mfma_f32_16x16x32_bf16 v[68:71], a[244:247], v[84:87], v[68:71]
v_mfma_f32_16x16x32_bf16 v[72:75], a[248:251], v[84:87], v[72:75]
global_load_lds_dwordx4 v2, s[38:39] offset:2048 sc1
v_mfma_f32_16x16x32_bf16 v[76:79], a[252:255], v[84:87], v[76:79]
s_waitcnt lgkmcnt(2)
v_add_u32 v2, 18688, v2
v_mfma_f32_16x16x32_bf16 v[64:67], v[16:19], v[88:91], v[64:67]
ds_read_b128 v[84:87], v8 offset:1024
v_mfma_f32_16x16x32_bf16 v[68:71], v[20:23], v[88:91], v[68:71]
v_mfma_f32_16x16x32_bf16 v[72:75], v[24:27], v[88:91], v[72:75]
v_mfma_f32_16x16x32_bf16 v[76:79], v[28:31], v[88:91], v[76:79]
s_waitcnt lgkmcnt(2)
v_mfma_f32_16x16x32_bf16 v[64:67], v[32:35], v[92:95], v[64:67]
ds_read_b128 v[88:91], v8 offset:2048
v_mfma_f32_16x16x32_bf16 v[68:71], v[36:39], v[92:95], v[68:71]
v_mfma_f32_16x16x32_bf16 v[72:75], v[40:43], v[92:95], v[72:75]
v_mfma_f32_16x16x32_bf16 v[76:79], v[44:47], v[92:95], v[76:79]
s_sub_u32 s70, s70, 1
s_cbranch_scc0 .Lgu_top6
s_nop 4
v_mul_f32 v96, v64, v72
v_mul_f32 v97, v65, v73
v_mul_f32 v98, v66, v74
v_mul_f32 v99, v67, v75
v_mul_f32 v100, v68, v76
v_mul_f32 v101, v69, v77
v_mul_f32 v102, v70, v78
v_mul_f32 v103, v71, v79
v_cvt_pk_bf16_f32 v104, v96, v97
v_cvt_pk_bf16_f32 v105, v98, v99
v_cvt_pk_bf16_f32 v106, v100, v101
v_cvt_pk_bf16_f32 v107, v102, v103
global_store_dwordx4 v3, v[104:107], s[38:39] sc1
s_waitcnt vmcnt(1)
s_barrier
s_waitcnt vmcnt(0)
s_barrier
s_endpgm
.Lgu_w3:
v_add_u32 v108, -1, v5
s_add_u32 m0, s43, 18432
s_mov_b32 s70, 32
s_waitcnt vmcnt(0) lgkmcnt(0)
s_nop 7
.p2align 6
.Lgu_top7:
s_waitcnt lgkmcnt(2)
v_mfma_f32_16x16x32_bf16 v[48:51], a[0:3], v[80:83], 0
ds_read_b128 v[92:95], v8 offset:3072
v_mfma_f32_16x16x32_bf16 v[52:55], a[4:7], v[80:83], 0
v_mfma_f32_16x16x32_bf16 v[56:59], a[8:11], v[80:83], 0
v_mfma_f32_16x16x32_bf16 v[60:63], a[12:15], v[80:83], 0
s_waitcnt lgkmcnt(2)
v_mul_f32 v96, v64, v72
v_mul_f32 v97, v65, v73
v_mfma_f32_16x16x32_bf16 v[48:51], a[16:19], v[84:87], v[48:51]
ds_read_b128 v[80:83], v8 offset:4096
v_mul_f32 v98, v66, v74
v_mfma_f32_16x16x32_bf16 v[52:55], a[20:23], v[84:87], v[52:55]
v_mul_f32 v99, v67, v75
v_mul_f32 v100, v68, v76
v_mfma_f32_16x16x32_bf16 v[56:59], a[24:27], v[84:87], v[56:59]
v_mul_f32 v101, v69, v77
v_mul_f32 v102, v70, v78
v_mfma_f32_16x16x32_bf16 v[60:63], a[28:31], v[84:87], v[60:63]
s_waitcnt lgkmcnt(2)
v_mfma_f32_16x16x32_bf16 v[48:51], a[32:35], v[88:91], v[48:51]
s_waitcnt vmcnt(4)
s_barrier
ds_read_b128 v[84:87], v8 offset:5120
v_mfma_f32_16x16x32_bf16 v[52:55], a[36:39], v[88:91], v[52:55]
global_store_dword v6, v7, s[38:39] sc1
v_add_u32 v7, 1, v7
v_mfma_f32_16x16x32_bf16 v[56:59], a[40:43], v[88:91], v[56:59]
v_mul_f32 v103, v71, v79
v_cvt_pk_bf16_f32 v104, v96, v97
v_mfma_f32_16x16x32_bf16 v[60:63], a[44:47], v[88:91], v[60:63]
s_waitcnt lgkmcnt(2)
v_cvt_pk_bf16_f32 v105, v98, v99
v_cvt_pk_bf16_f32 v106, v100, v101
v_mfma_f32_16x16x32_bf16 v[48:51], a[48:51], v[92:95], v[48:51]
ds_read_b128 v[88:91], v8 offset:6144
v_cvt_pk_bf16_f32 v107, v102, v103
v_mfma_f32_16x16x32_bf16 v[52:55], a[52:55], v[92:95], v[52:55]
v_mfma_f32_16x16x32_bf16 v[56:59], a[56:59], v[92:95], v[56:59]
v_mfma_f32_16x16x32_bf16 v[60:63], a[60:63], v[92:95], v[60:63]
s_waitcnt lgkmcnt(2)
global_store_dwordx4 v3, v[104:107], s[38:39] sc1
v_mfma_f32_16x16x32_bf16 v[48:51], a[64:67], v[80:83], v[48:51]
ds_read_b128 v[92:95], v8 offset:7168
v_add_u32 v3, 147456, v3
v_mfma_f32_16x16x32_bf16 v[52:55], a[68:71], v[80:83], v[52:55]
v_mfma_f32_16x16x32_bf16 v[56:59], a[72:75], v[80:83], v[56:59]
v_mfma_f32_16x16x32_bf16 v[60:63], a[76:79], v[80:83], v[60:63]
s_waitcnt lgkmcnt(2)
v_mfma_f32_16x16x32_bf16 v[48:51], a[80:83], v[84:87], v[48:51]
ds_read_b128 v[80:83], v8 offset:8192
v_mfma_f32_16x16x32_bf16 v[52:55], a[84:87], v[84:87], v[52:55]
v_mfma_f32_16x16x32_bf16 v[56:59], a[88:91], v[84:87], v[56:59]
v_mfma_f32_16x16x32_bf16 v[60:63], a[92:95], v[84:87], v[60:63]
s_waitcnt lgkmcnt(2)
v_mfma_f32_16x16x32_bf16 v[48:51], a[96:99], v[88:91], v[48:51]
ds_read_b128 v[84:87], v8 offset:9216
v_mfma_f32_16x16x32_bf16 v[52:55], a[100:103], v[88:91], v[52:55]
v_mfma_f32_16x16x32_bf16 v[56:59], a[104:107], v[88:91], v[56:59]
v_mfma_f32_16x16x32_bf16 v[60:63], a[108:111], v[88:91], v[60:63]
s_waitcnt lgkmcnt(2)
v_mfma_f32_16x16x32_bf16 v[48:51], a[112:115], v[92:95], v[48:51]
ds_read_b128 v[88:91], v8 offset:10240
v_mfma_f32_16x16x32_bf16 v[52:55], a[116:119], v[92:95], v[52:55]
v_mfma_f32_16x16x32_bf16 v[56:59], a[120:123], v[92:95], v[56:59]
v_mfma_f32_16x16x32_bf16 v[60:63], a[124:127], v[92:95], v[60:63]
s_waitcnt lgkmcnt(2)
v_mfma_f32_16x16x32_bf16 v[48:51], a[128:131], v[80:83], v[48:51]
ds_read_b128 v[92:95], v8 offset:11264
v_mfma_f32_16x16x32_bf16 v[52:55], a[132:135], v[80:83], v[52:55]
v_mfma_f32_16x16x32_bf16 v[56:59], a[136:139], v[80:83], v[56:59]
v_mfma_f32_16x16x32_bf16 v[60:63], a[140:143], v[80:83], v[60:63]
s_waitcnt lgkmcnt(2)
v_mfma_f32_16x16x32_bf16 v[48:51], a[144:147], v[84:87], v[48:51]
ds_read_b128 v[80:83], v8 offset:12288
v_mfma_f32_16x16x32_bf16 v[52:55], a[148:151], v[84:87], v[52:55]
v_mfma_f32_16x16x32_bf16 v[56:59], a[152:155], v[84:87], v[56:59]
v_mfma_f32_16x16x32_bf16 v[60:63], a[156:159], v[84:87], v[60:63]
s_waitcnt lgkmcnt(2)
v_mfma_f32_16x16x32_bf16 v[48:51], a[160:163], v[88:91], v[48:51]
ds_read_b128 v[84:87], v8 offset:13312
v_mfma_f32_16x16x32_bf16 v[52:55], a[164:167], v[88:91], v[52:55]
s_xor_b32 s43, s43, 0x10000
v_mfma_f32_16x16x32_bf16 v[56:59], a[168:171], v[88:91], v[56:59]
s_add_u32 m0, s43, 0
v_mfma_f32_16x16x32_bf16 v[60:63], a[172:175], v[88:91], v[60:63]
s_waitcnt lgkmcnt(2)
v_mfma_f32_16x16x32_bf16 v[48:51], a[176:179], v[92:95], v[48:51]
s_barrier
ds_read_b128 v[88:91], v8 offset:14336
v_mfma_f32_16x16x32_bf16 v[52:55], a[180:183], v[92:95], v[52:55]
v_mfma_f32_16x16x32_bf16 v[56:59], a[184:187], v[92:95], v[56:59]
v_mfma_f32_16x16x32_bf16 v[60:63], a[188:191], v[92:95], v[60:63]
s_waitcnt lgkmcnt(2)
global_load_lds_dwordx4 v2, s[38:39] offset:-2048 sc1
v_mfma_f32_16x16x32_bf16 v[48:51], a[192:195], v[80:83], v[48:51]
ds_read_b128 v[92:95], v8 offset:15360
v_mfma_f32_16x16x32_bf16 v[52:55], a[196:199], v[80:83], v[52:55]
v_mfma_f32_16x16x32_bf16 v[56:59], a[200:203], v[80:83], v[56:59]
v_mfma_f32_16x16x32_bf16 v[60:63], a[204:207], v[80:83], v[60:63]
s_waitcnt lgkmcnt(2)
global_load_lds_dwordx4 v2, s[38:39] offset:-1024 sc1
v_mfma_f32_16x16x32_bf16 v[48:51], a[208:211], v[84:87], v[48:51]
ds_read_b128 v[80:83], v8 offset:16384
v_mfma_f32_16x16x32_bf16 v[52:55], a[212:215], v[84:87], v[52:55]
v_mfma_f32_16x16x32_bf16 v[56:59], a[216:219], v[84:87], v[56:59]
v_mfma_f32_16x16x32_bf16 v[60:63], a[220:223], v[84:87], v[60:63]
s_waitcnt lgkmcnt(2)
global_load_lds_dwordx4 v2, s[38:39] offset:0 sc1
v_mfma_f32_16x16x32_bf16 v[48:51], a[224:227], v[88:91], v[48:51]
ds_read_b128 v[84:87], v8 offset:17408
v_mfma_f32_16x16x32_bf16 v[52:55], a[228:231], v[88:91], v[52:55]
v_mfma_f32_16x16x32_bf16 v[56:59], a[232:235], v[88:91], v[56:59]
v_mfma_f32_16x16x32_bf16 v[60:63], a[236:239], v[88:91], v[60:63]
s_waitcnt lgkmcnt(2)
v_mfma_f32_16x16x32_bf16 v[48:51], a[240:243], v[92:95], v[48:51]
s_waitcnt vmcnt(5)
s_barrier
ds_read_b128 v[88:91], v8 offset:18432
v_mfma_f32_16x16x32_bf16 v[52:55], a[244:247], v[92:95], v[52:55]
global_load_lds_dwordx4 v2, s[38:39] offset:1024 sc1
v_mfma_f32_16x16x32_bf16 v[56:59], a[248:251], v[92:95], v[56:59]
v_add_u32 v2, 18688, v2
v_mfma_f32_16x16x32_bf16 v[60:63], a[252:255], v[92:95], v[60:63]
s_waitcnt lgkmcnt(2)
v_mfma_f32_16x16x32_bf16 v[48:51], v[16:19], v[80:83], v[48:51]
ds_read_b128 v[92:95], v8 offset:19456
v_mfma_f32_16x16x32_bf16 v[52:55], v[20:23], v[80:83], v[52:55]
v_mfma_f32_16x16x32_bf16 v[56:59], v[24:27], v[80:83], v[56:59]
v_mfma_f32_16x16x32_bf16 v[60:63], v[28:31], v[80:83], v[60:63]
s_waitcnt lgkmcnt(2)
v_mfma_f32_16x16x32_bf16 v[48:51], v[32:35], v[84:87], v[48:51]
ds_read_b128 v[80:83], v8 offset:20480
v_mfma_f32_16x16x32_bf16 v[52:55], v[36:39], v[84:87], v[52:55]
v_mfma_f32_16x16x32_bf16 v[56:59], v[40:43], v[84:87], v[56:59]
v_mfma_f32_16x16x32_bf16 v[60:63], v[44:47], v[84:87], v[60:63]
s_waitcnt lgkmcnt(2)
v_mfma_f32_16x16x32_bf16 v[64:67], a[0:3], v[88:91], 0
ds_read_b128 v[84:87], v8 offset:21504
v_mfma_f32_16x16x32_bf16 v[68:71], a[4:7], v[88:91], 0
v_mfma_f32_16x16x32_bf16 v[72:75], a[8:11], v[88:91], 0
v_mfma_f32_16x16x32_bf16 v[76:79], a[12:15], v[88:91], 0
s_waitcnt lgkmcnt(2)
v_mul_f32 v96, v48, v56
v_mul_f32 v97, v49, v57
v_mfma_f32_16x16x32_bf16 v[64:67], a[16:19], v[92:95], v[64:67]
ds_read_b128 v[88:91], v8 offset:22528
v_mul_f32 v98, v50, v58
v_mfma_f32_16x16x32_bf16 v[68:71], a[20:23], v[92:95], v[68:71]
v_mul_f32 v99, v51, v59
v_mul_f32 v100, v52, v60
v_mfma_f32_16x16x32_bf16 v[72:75], a[24:27], v[92:95], v[72:75]
v_mul_f32 v101, v53, v61
v_mul_f32 v102, v54, v62
v_mfma_f32_16x16x32_bf16 v[76:79], a[28:31], v[92:95], v[76:79]
s_waitcnt lgkmcnt(2)
v_mfma_f32_16x16x32_bf16 v[64:67], a[32:35], v[80:83], v[64:67]
s_waitcnt vmcnt(4)
s_barrier
ds_read_b128 v[92:95], v8 offset:23552
v_mfma_f32_16x16x32_bf16 v[68:71], a[36:39], v[80:83], v[68:71]
v_mul_f32 v103, v55, v63
v_cvt_pk_bf16_f32 v104, v96, v97
v_mfma_f32_16x16x32_bf16 v[72:75], a[40:43], v[80:83], v[72:75]
v_cvt_pk_bf16_f32 v105, v98, v99
v_cvt_pk_bf16_f32 v106, v100, v101
v_mfma_f32_16x16x32_bf16 v[76:79], a[44:47], v[80:83], v[76:79]
s_waitcnt lgkmcnt(2)
v_cvt_pk_bf16_f32 v107, v102, v103
global_store_dword v6, v7, s[38:39] sc1
v_mfma_f32_16x16x32_bf16 v[64:67], a[48:51], v[84:87], v[64:67]
ds_read_b128 v[80:83], v8 offset:24576
v_add_u32 v7, 1, v7
v_mfma_f32_16x16x32_bf16 v[68:71], a[52:55], v[84:87], v[68:71]
v_mfma_f32_16x16x32_bf16 v[72:75], a[56:59], v[84:87], v[72:75]
global_store_dwordx4 v3, v[104:107], s[38:39] sc1
v_mfma_f32_16x16x32_bf16 v[76:79], a[60:63], v[84:87], v[76:79]
s_waitcnt lgkmcnt(2)
v_add_u32 v3, 147456, v3
v_mfma_f32_16x16x32_bf16 v[64:67], a[64:67], v[88:91], v[64:67]
ds_read_b128 v[84:87], v8 offset:25600
v_mfma_f32_16x16x32_bf16 v[68:71], a[68:71], v[88:91], v[68:71]
v_mfma_f32_16x16x32_bf16 v[72:75], a[72:75], v[88:91], v[72:75]
v_mfma_f32_16x16x32_bf16 v[76:79], a[76:79], v[88:91], v[76:79]
s_waitcnt lgkmcnt(2)
v_mfma_f32_16x16x32_bf16 v[64:67], a[80:83], v[92:95], v[64:67]
ds_read_b128 v[88:91], v8 offset:26624
v_mfma_f32_16x16x32_bf16 v[68:71], a[84:87], v[92:95], v[68:71]
v_mfma_f32_16x16x32_bf16 v[72:75], a[88:91], v[92:95], v[72:75]
v_mfma_f32_16x16x32_bf16 v[76:79], a[92:95], v[92:95], v[76:79]
s_waitcnt lgkmcnt(2)
v_mfma_f32_16x16x32_bf16 v[64:67], a[96:99], v[80:83], v[64:67]
ds_read_b128 v[92:95], v8 offset:27648
v_mfma_f32_16x16x32_bf16 v[68:71], a[100:103], v[80:83], v[68:71]
v_mfma_f32_16x16x32_bf16 v[72:75], a[104:107], v[80:83], v[72:75]
v_mfma_f32_16x16x32_bf16 v[76:79], a[108:111], v[80:83], v[76:79]
s_waitcnt lgkmcnt(2)
v_mfma_f32_16x16x32_bf16 v[64:67], a[112:115], v[84:87], v[64:67]
ds_read_b128 v[80:83], v8 offset:28672
v_mfma_f32_16x16x32_bf16 v[68:71], a[116:119], v[84:87], v[68:71]
v_mfma_f32_16x16x32_bf16 v[72:75], a[120:123], v[84:87], v[72:75]
v_mfma_f32_16x16x32_bf16 v[76:79], a[124:127], v[84:87], v[76:79]
s_waitcnt lgkmcnt(2)
v_mfma_f32_16x16x32_bf16 v[64:67], a[128:131], v[88:91], v[64:67]
ds_read_b128 v[84:87], v8 offset:29696
v_mfma_f32_16x16x32_bf16 v[68:71], a[132:135], v[88:91], v[68:71]
v_mfma_f32_16x16x32_bf16 v[72:75], a[136:139], v[88:91], v[72:75]
v_mfma_f32_16x16x32_bf16 v[76:79], a[140:143], v[88:91], v[76:79]
s_waitcnt lgkmcnt(2)
v_mfma_f32_16x16x32_bf16 v[64:67], a[144:147], v[92:95], v[64:67]
ds_read_b128 v[88:91], v8 offset:30720
v_mfma_f32_16x16x32_bf16 v[68:71], a[148:151], v[92:95], v[68:71]
v_mfma_f32_16x16x32_bf16 v[72:75], a[152:155], v[92:95], v[72:75]
v_mfma_f32_16x16x32_bf16 v[76:79], a[156:159], v[92:95], v[76:79]
s_waitcnt lgkmcnt(2)
v_mfma_f32_16x16x32_bf16 v[64:67], a[160:163], v[80:83], v[64:67]
ds_read_b128 v[92:95], v8 offset:31744
v_mfma_f32_16x16x32_bf16 v[68:71], a[164:167], v[80:83], v[68:71]
v_mfma_f32_16x16x32_bf16 v[72:75], a[168:171], v[80:83], v[72:75]
s_add_u32 m0, s43, 18432
v_mfma_f32_16x16x32_bf16 v[76:79], a[172:175], v[80:83], v[76:79]
s_waitcnt lgkmcnt(2)
v_mfma_f32_16x16x32_bf16 v[64:67], a[176:179], v[84:87], v[64:67]
s_barrier
ds_read_b128 v[80:83], v8 offset:32768
v_mfma_f32_16x16x32_bf16 v[68:71], a[180:183], v[84:87], v[68:71]
v_mfma_f32_16x16x32_bf16 v[72:75], a[184:187], v[84:87], v[72:75]
v_mfma_f32_16x16x32_bf16 v[76:79], a[188:191], v[84:87], v[76:79]
s_waitcnt lgkmcnt(2)
global_load_lds_dwordx4 v2, s[38:39] offset:-2048 sc1
v_mfma_f32_16x16x32_bf16 v[64:67], a[192:195], v[88:91], v[64:67]
ds_read_b128 v[84:87], v8 offset:33792
v_mfma_f32_16x16x32_bf16 v[68:71], a[196:199], v[88:91], v[68:71]
v_mfma_f32_16x16x32_bf16 v[72:75], a[200:203], v[88:91], v[72:75]
v_mfma_f32_16x16x32_bf16 v[76:79], a[204:207], v[88:91], v[76:79]
s_waitcnt lgkmcnt(2)
global_load_lds_dwordx4 v2, s[38:39] offset:-1024 sc1
v_mfma_f32_16x16x32_bf16 v[64:67], a[208:211], v[92:95], v[64:67]
ds_read_b128 v[88:91], v8 offset:34816
v_mfma_f32_16x16x32_bf16 v[68:71], a[212:215], v[92:95], v[68:71]
v_mfma_f32_16x16x32_bf16 v[72:75], a[216:219], v[92:95], v[72:75]
v_mfma_f32_16x16x32_bf16 v[76:79], a[220:223], v[92:95], v[76:79]
s_waitcnt lgkmcnt(2)
global_load_lds_dwordx4 v2, s[38:39] offset:0 sc1
v_mfma_f32_16x16x32_bf16 v[64:67], a[224:227], v[80:83], v[64:67]
ds_read_b128 v[92:95], v8 offset:35840
v_mfma_f32_16x16x32_bf16 v[68:71], a[228:231], v[80:83], v[68:71]
v_mfma_f32_16x16x32_bf16 v[72:75], a[232:235], v[80:83], v[72:75]
v_xor_b32 v8, 0x10000, v8
v_mfma_f32_16x16x32_bf16 v[76:79], a[236:239], v[80:83], v[76:79]
s_waitcnt lgkmcnt(2)
v_mfma_f32_16x16x32_bf16 v[64:67], a[240:243], v[84:87], v[64:67]
s_waitcnt vmcnt(5)
s_barrier
ds_read_b128 v[80:83], v8 offset:0
v_mfma_f32_16x16x32_bf16 v[68:71], a[244:247], v[84:87], v[68:71]
global_load_lds_dwordx4 v2, s[38:39] offset:1024 sc1
v_mfma_f32_16x16x32_bf16 v[72:75], a[248:251], v[84:87], v[72:75]
v_add_u32 v2, 18688, v2
v_mfma_f32_16x16x32_bf16 v[76:79], a[252:255], v[84:87], v[76:79]
s_waitcnt lgkmcnt(2)
v_mfma_f32_16x16x32_bf16 v[64:67], v[16:19], v[88:91], v[64:67]
ds_read_b128 v[84:87], v8 offset:1024
v_mfma_f32_16x16x32_bf16 v[68:71], v[20:23], v[88:91], v[68:71]
v_mfma_f32_16x16x32_bf16 v[72:75], v[24:27], v[88:91], v[72:75]
v_mfma_f32_16x16x32_bf16 v[76:79], v[28:31], v[88:91], v[76:79]
s_waitcnt lgkmcnt(2)
v_mfma_f32_16x16x32_bf16 v[64:67], v[32:35], v[92:95], v[64:67]
ds_read_b128 v[88:91], v8 offset:2048
v_mfma_f32_16x16x32_bf16 v[68:71], v[36:39], v[92:95], v[68:71]
v_mfma_f32_16x16x32_bf16 v[72:75], v[40:43], v[92:95], v[72:75]
v_mfma_f32_16x16x32_bf16 v[76:79], v[44:47], v[92:95], v[76:79]
s_sub_u32 s70, s70, 1
s_cbranch_scc0 .Lgu_top7
s_nop 4
v_mul_f32 v96, v64, v72
v_mul_f32 v97, v65, v73
v_mul_f32 v98, v66, v74
v_mul_f32 v99, v67, v75
v_mul_f32 v100, v68, v76
v_mul_f32 v101, v69, v77
v_mul_f32 v102, v70, v78
v_mul_f32 v103, v71, v79
v_cvt_pk_bf16_f32 v104, v96, v97
v_cvt_pk_bf16_f32 v105, v98, v99
v_cvt_pk_bf16_f32 v106, v100, v101
v_cvt_pk_bf16_f32 v107, v102, v103
global_store_dwordx4 v3, v[104:107], s[38:39] sc1
s_waitcnt vmcnt(1)
s_barrier
global_store_dword v6, v7, s[38:39] sc1
v_add_u32 v7, 3, v7
s_waitcnt vmcnt(1)
s_barrier
global_store_dword v6, v7, s[38:39] sc1
s_endpgm
.Lgu_w4:
v_add_u32 v108, -1, v5
s_add_u32 m0, s43, 18432
s_mov_b32 s70, 32
s_waitcnt vmcnt(0) lgkmcnt(0)
s_nop 7
.p2align 6
.Lgu_top12:
s_waitcnt lgkmcnt(2)
v_mfma_f32_16x16x32_bf16 v[48:51], a[0:3], v[80:83], 0
ds_read_b128 v[92:95], v8 offset:3072
v_mfma_f32_16x16x32_bf16 v[52:55], a[4:7], v[80:83], 0
v_mfma_f32_16x16x32_bf16 v[56:59], a[8:11], v[80:83], 0
v_mfma_f32_16x16x32_bf16 v[60:63], a[12:15], v[80:83], 0
s_waitcnt lgkmcnt(2)
v_mul_f32 v96, v64, v72
v_mul_f32 v97, v65, v73
v_mfma_f32_16x16x32_bf16 v[48:51], a[16:19], v[84:87], v[48:51]
ds_read_b128 v[80:83], v8 offset:4096
v_mul_f32 v98, v66, v74
v_mfma_f32_16x16x32_bf16 v[52:55], a[20:23], v[84:87], v[52:55]
v_mul_f32 v99, v67, v75
v_mul_f32 v100, v68, v76
v_mfma_f32_16x16x32_bf16 v[56:59], a[24:27], v[84:87], v[56:59]
v_mul_f32 v101, v69, v77
v_mul_f32 v102, v70, v78
v_mfma_f32_16x16x32_bf16 v[60:63], a[28:31], v[84:87], v[60:63]
s_waitcnt lgkmcnt(2)
v_mfma_f32_16x16x32_bf16 v[48:51], a[32:35], v[88:91], v[48:51]
s_waitcnt vmcnt(5)
s_barrier
ds_read_b128 v[84:87], v8 offset:5120
v_mfma_f32_16x16x32_bf16 v[52:55], a[36:39], v[88:91], v[52:55]
v_mul_f32 v103, v71, v79
v_cvt_pk_bf16_f32 v104, v96, v97
v_mfma_f32_16x16x32_bf16 v[56:59], a[40:43], v[88:91], v[56:59]
v_cvt_pk_bf16_f32 v105, v98, v99
v_cvt_pk_bf16_f32 v106, v100, v101
v_mfma_f32_16x16x32_bf16 v[60:63], a[44:47], v[88:91], v[60:63]
s_waitcnt lgkmcnt(2)
v_cvt_pk_bf16_f32 v107, v102, v103
v_mfma_f32_16x16x32_bf16 v[48:51], a[48:51], v[92:95], v[48:51]
ds_read_b128 v[88:91], v8 offset:6144
v_mfma_f32_16x16x32_bf16 v[52:55], a[52:55], v[92:95], v[52:55]
v_mfma_f32_16x16x32_bf16 v[56:59], a[56:59], v[92:95], v[56:59]
v_mfma_f32_16x16x32_bf16 v[60:63], a[60:63], v[92:95], v[60:63]
s_waitcnt lgkmcnt(2)
global_store_dwordx4 v3, v[104:107], s[38:39] sc1
v_mfma_f32_16x16x32_bf16 v[48:51], a[64:67], v[80:83], v[48:51]
ds_read_b128 v[92:95], v8 offset:7168
v_add_u32 v3, 147456, v3
v_mfma_f32_16x16x32_bf16 v[52:55], a[68:71], v[80:83], v[52:55]
v_mfma_f32_16x16x32_bf16 v[56:59], a[72:75], v[80:83], v[56:59]
v_mfma_f32_16x16x32_bf16 v[60:63], a[76:79], v[80:83], v[60:63]
s_waitcnt lgkmcnt(2)
v_mfma_f32_16x16x32_bf16 v[48:51], a[80:83], v[84:87], v[48:51]
)"
R"(ds_read_b128 v[80:83], v8 offset:8192
v_mfma_f32_16x16x32_bf16 v[52:55], a[84:87], v[84:87], v[52:55]
v_mfma_f32_16x16x32_bf16 v[56:59], a[88:91], v[84:87], v[56:59]
v_mfma_f32_16x16x32_bf16 v[60:63], a[92:95], v[84:87], v[60:63]
s_waitcnt lgkmcnt(2)
v_mfma_f32_16x16x32_bf16 v[48:51], a[96:99], v[88:91], v[48:51]
ds_read_b128 v[84:87], v8 offset:9216
v_mfma_f32_16x16x32_bf16 v[52:55], a[100:103], v[88:91], v[52:55]
v_mfma_f32_16x16x32_bf16 v[56:59], a[104:107], v[88:91], v[56:59]
v_mfma_f32_16x16x32_bf16 v[60:63], a[108:111], v[88:91], v[60:63]
s_waitcnt lgkmcnt(2)
v_mfma_f32_16x16x32_bf16 v[48:51], a[112:115], v[92:95], v[48:51]
ds_read_b128 v[88:91], v8 offset:10240
v_mfma_f32_16x16x32_bf16 v[52:55], a[116:119], v[92:95], v[52:55]
v_mfma_f32_16x16x32_bf16 v[56:59], a[120:123], v[92:95], v[56:59]
v_mfma_f32_16x16x32_bf16 v[60:63], a[124:127], v[92:95], v[60:63]
s_waitcnt lgkmcnt(2)
v_mfma_f32_16x16x32_bf16 v[48:51], a[128:131], v[80:83], v[48:51]
ds_read_b128 v[92:95], v8 offset:11264
v_mfma_f32_16x16x32_bf16 v[52:55], a[132:135], v[80:83], v[52:55]
v_mfma_f32_16x16x32_bf16 v[56:59], a[136:139], v[80:83], v[56:59]
v_mfma_f32_16x16x32_bf16 v[60:63], a[140:143], v[80:83], v[60:63]
s_waitcnt lgkmcnt(2)
v_mfma_f32_16x16x32_bf16 v[48:51], a[144:147], v[84:87], v[48:51]
ds_read_b128 v[80:83], v8 offset:12288
v_mfma_f32_16x16x32_bf16 v[52:55], a[148:151], v[84:87], v[52:55]
v_mfma_f32_16x16x32_bf16 v[56:59], a[152:155], v[84:87], v[56:59]
v_mfma_f32_16x16x32_bf16 v[60:63], a[156:159], v[84:87], v[60:63]
s_waitcnt vmcnt(3)
v_sub_u32 v108, v108, v5
v_cmp_lt_u32 vcc, 0x7f, v108
s_waitcnt lgkmcnt(2)
v_mfma_f32_16x16x32_bf16 v[48:51], a[160:163], v[88:91], v[48:51]
ds_read_b128 v[84:87], v8 offset:13312
v_mfma_f32_16x16x32_bf16 v[52:55], a[164:167], v[88:91], v[52:55]
s_xor_b32 s43, s43, 0x10000
v_mfma_f32_16x16x32_bf16 v[56:59], a[168:171], v[88:91], v[56:59]
s_cbranch_vccnz .Lgu_sp8
.Lgu_rt9:
s_add_u32 m0, s43, 0
v_mfma_f32_16x16x32_bf16 v[60:63], a[172:175], v[88:91], v[60:63]
s_waitcnt lgkmcnt(2)
v_mfma_f32_16x16x32_bf16 v[48:51], a[176:179], v[92:95], v[48:51]
s_barrier
ds_read_b128 v[88:91], v8 offset:14336
v_mfma_f32_16x16x32_bf16 v[52:55], a[180:183], v[92:95], v[52:55]
v_add_u32 v5, 1, v5
v_mfma_f32_16x16x32_bf16 v[56:59], a[184:187], v[92:95], v[56:59]
v_mfma_f32_16x16x32_bf16 v[60:63], a[188:191], v[92:95], v[60:63]
s_waitcnt lgkmcnt(2)
v_mfma_f32_16x16x32_bf16 v[48:51], a[192:195], v[80:83], v[48:51]
ds_read_b128 v[92:95], v8 offset:15360
global_load_lds_dwordx4 v2, s[38:39] offset:-2048 sc1
v_mfma_f32_16x16x32_bf16 v[52:55], a[196:199], v[80:83], v[52:55]
v_mfma_f32_16x16x32_bf16 v[56:59], a[200:203], v[80:83], v[56:59]
v_mfma_f32_16x16x32_bf16 v[60:63], a[204:207], v[80:83], v[60:63]
s_waitcnt lgkmcnt(2)
v_mfma_f32_16x16x32_bf16 v[48:51], a[208:211], v[84:87], v[48:51]
ds_read_b128 v[80:83], v8 offset:16384
global_load_lds_dwordx4 v2, s[38:39] offset:-1024 sc1
v_mfma_f32_16x16x32_bf16 v[52:55], a[212:215], v[84:87], v[52:55]
global_load_dword v108, v4, s[38:39] sc1
v_mfma_f32_16x16x32_bf16 v[56:59], a[216:219], v[84:87], v[56:59]
v_mfma_f32_16x16x32_bf16 v[60:63], a[220:223], v[84:87], v[60:63]
s_waitcnt lgkmcnt(2)
v_mfma_f32_16x16x32_bf16 v[48:51], a[224:227], v[88:91], v[48:51]
ds_read_b128 v[84:87], v8 offset:17408
global_load_lds_dwordx4 v2, s[38:39] offset:0 sc1
v_mfma_f32_16x16x32_bf16 v[52:55], a[228:231], v[88:91], v[52:55]
v_mfma_f32_16x16x32_bf16 v[56:59], a[232:235], v[88:91], v[56:59]
v_mfma_f32_16x16x32_bf16 v[60:63], a[236:239], v[88:91], v[60:63]
s_waitcnt lgkmcnt(2)
v_mfma_f32_16x16x32_bf16 v[48:51], a[240:243], v[92:95], v[48:51]
s_waitcnt vmcnt(5)
s_barrier
ds_read_b128 v[88:91], v8 offset:18432
v_mfma_f32_16x16x32_bf16 v[52:55], a[244:247], v[92:95], v[52:55]
global_load_lds_dwordx4 v2, s[38:39] offset:1024 sc1
v_mfma_f32_16x16x32_bf16 v[56:59], a[248:251], v[92:95], v[56:59]
v_add_u32 v2, 18688, v2
v_mfma_f32_16x16x32_bf16 v[60:63], a[252:255], v[92:95], v[60:63]
s_waitcnt lgkmcnt(2)
v_mfma_f32_16x16x32_bf16 v[48:51], v[16:19], v[80:83], v[48:51]
ds_read_b128 v[92:95], v8 offset:19456
v_mfma_f32_16x16x32_bf16 v[52:55], v[20:23], v[80:83], v[52:55]
v_mfma_f32_16x16x32_bf16 v[56:59], v[24:27], v[80:83], v[56:59]
v_mfma_f32_16x16x32_bf16 v[60:63], v[28:31], v[80:83], v[60:63]
s_waitcnt lgkmcnt(2)
v_mfma_f32_16x16x32_bf16 v[48:51], v[32:35], v[84:87], v[48:51]
ds_read_b128 v[80:83], v8 offset:20480
v_mfma_f32_16x16x32_bf16 v[52:55], v[36:39], v[84:87], v[52:55]
v_mfma_f32_16x16x32_bf16 v[56:59], v[40:43], v[84:87], v[56:59]
v_mfma_f32_16x16x32_bf16 v[60:63], v[44:47], v[84:87], v[60:63]
s_waitcnt lgkmcnt(2)
v_mfma_f32_16x16x32_bf16 v[64:67], a[0:3], v[88:91], 0
ds_read_b128 v[84:87], v8 offset:21504
v_mfma_f32_16x16x32_bf16 v[68:71], a[4:7], v[88:91], 0
v_mfma_f32_16x16x32_bf16 v[72:75], a[8:11], v[88:91], 0
v_mfma_f32_16x16x32_bf16 v[76:79], a[12:15], v[88:91], 0
s_waitcnt lgkmcnt(2)
v_mul_f32 v96, v48, v56
v_mul_f32 v97, v49, v57
v_mfma_f32_16x16x32_bf16 v[64:67], a[16:19], v[92:95], v[64:67]
ds_read_b128 v[88:91], v8 offset:22528
v_mul_f32 v98, v50, v58
v_mfma_f32_16x16x32_bf16 v[68:71], a[20:23], v[92:95], v[68:71]
v_mul_f32 v99, v51, v59
v_mul_f32 v100, v52, v60
v_mfma_f32_16x16x32_bf16 v[72:75], a[24:27], v[92:95], v[72:75]
v_mul_f32 v101, v53, v61
v_mul_f32 v102, v54, v62
v_mfma_f32_16x16x32_bf16 v[76:79], a[28:31], v[92:95], v[76:79]
s_waitcnt lgkmcnt(2)
v_mfma_f32_16x16x32_bf16 v[64:67], a[32:35], v[80:83], v[64:67]
s_waitcnt vmcnt(5)
s_barrier
ds_read_b128 v[92:95], v8 offset:23552
v_mfma_f32_16x16x32_bf16 v[68:71], a[36:39], v[80:83], v[68:71]
v_mul_f32 v103, v55, v63
v_cvt_pk_bf16_f32 v104, v96, v97
v_mfma_f32_16x16x32_bf16 v[72:75], a[40:43], v[80:83], v[72:75]
v_cvt_pk_bf16_f32 v105, v98, v99
v_cvt_pk_bf16_f32 v106, v100, v101
v_mfma_f32_16x16x32_bf16 v[76:79], a[44:47], v[80:83], v[76:79]
s_waitcnt lgkmcnt(2)
v_cvt_pk_bf16_f32 v107, v102, v103
v_mfma_f32_16x16x32_bf16 v[64:67], a[48:51], v[84:87], v[64:67]
ds_read_b128 v[80:83], v8 offset:24576
v_mfma_f32_16x16x32_bf16 v[68:71], a[52:55], v[84:87], v[68:71]
v_mfma_f32_16x16x32_bf16 v[72:75], a[56:59], v[84:87], v[72:75]
v_mfma_f32_16x16x32_bf16 v[76:79], a[60:63], v[84:87], v[76:79]
s_waitcnt lgkmcnt(2)
global_store_dwordx4 v3, v[104:107], s[38:39] sc1
v_mfma_f32_16x16x32_bf16 v[64:67], a[64:67], v[88:91], v[64:67]
ds_read_b128 v[84:87], v8 offset:25600
v_add_u32 v3, 147456, v3
v_mfma_f32_16x16x32_bf16 v[68:71], a[68:71], v[88:91], v[68:71]
v_mfma_f32_16x16x32_bf16 v[72:75], a[72:75], v[88:91], v[72:75]
v_mfma_f32_16x16x32_bf16 v[76:79], a[76:79], v[88:91], v[76:79]
s_waitcnt lgkmcnt(2)
v_mfma_f32_16x16x32_bf16 v[64:67], a[80:83], v[92:95], v[64:67]
ds_read_b128 v[88:91], v8 offset:26624
v_mfma_f32_16x16x32_bf16 v[68:71], a[84:87], v[92:95], v[68:71]
v_mfma_f32_16x16x32_bf16 v[72:75], a[88:91], v[92:95], v[72:75]
v_mfma_f32_16x16x32_bf16 v[76:79], a[92:95], v[92:95], v[76:79]
s_waitcnt lgkmcnt(2)
v_mfma_f32_16x16x32_bf16 v[64:67], a[96:99], v[80:83], v[64:67]
ds_read_b128 v[92:95], v8 offset:27648
v_mfma_f32_16x16x32_bf16 v[68:71], a[100:103], v[80:83], v[68:71]
v_mfma_f32_16x16x32_bf16 v[72:75], a[104:107], v[80:83], v[72:75]
v_mfma_f32_16x16x32_bf16 v[76:79], a[108:111], v[80:83], v[76:79]
s_waitcnt lgkmcnt(2)
v_mfma_f32_16x16x32_bf16 v[64:67], a[112:115], v[84:87], v[64:67]
ds_read_b128 v[80:83], v8 offset:28672
v_mfma_f32_16x16x32_bf16 v[68:71], a[116:119], v[84:87], v[68:71]
v_mfma_f32_16x16x32_bf16 v[72:75], a[120:123], v[84:87], v[72:75]
v_mfma_f32_16x16x32_bf16 v[76:79], a[124:127], v[84:87], v[76:79]
s_waitcnt lgkmcnt(2)
v_mfma_f32_16x16x32_bf16 v[64:67], a[128:131], v[88:91], v[64:67]
ds_read_b128 v[84:87], v8 offset:29696
v_mfma_f32_16x16x32_bf16 v[68:71], a[132:135], v[88:91], v[68:71]
v_mfma_f32_16x16x32_bf16 v[72:75], a[136:139], v[88:91], v[72:75]
v_mfma_f32_16x16x32_bf16 v[76:79], a[140:143], v[88:91], v[76:79]
s_waitcnt lgkmcnt(2)
v_mfma_f32_16x16x32_bf16 v[64:67], a[144:147], v[92:95], v[64:67]
ds_read_b128 v[88:91], v8 offset:30720
v_mfma_f32_16x16x32_bf16 v[68:71], a[148:151], v[92:95], v[68:71]
v_mfma_f32_16x16x32_bf16 v[72:75], a[152:155], v[92:95], v[72:75]
v_mfma_f32_16x16x32_bf16 v[76:79], a[156:159], v[92:95], v[76:79]
s_waitcnt vmcnt(3)
v_sub_u32 v108, v108, v5
v_cmp_lt_u32 vcc, 0x7f, v108
s_waitcnt lgkmcnt(2)
v_mfma_f32_16x16x32_bf16 v[64:67], a[160:163], v[80:83], v[64:67]
ds_read_b128 v[92:95], v8 offset:31744
v_mfma_f32_16x16x32_bf16 v[68:71], a[164:167], v[80:83], v[68:71]
v_mfma_f32_16x16x32_bf16 v[72:75], a[168:171], v[80:83], v[72:75]
s_cbranch_vccnz .Lgu_sp10
.Lgu_rt11:
s_add_u32 m0, s43, 18432
v_mfma_f32_16x16x32_bf16 v[76:79], a[172:175], v[80:83], v[76:79]
s_waitcnt lgkmcnt(2)
v_mfma_f32_16x16x32_bf16 v[64:67], a[176:179], v[84:87], v[64:67]
s_barrier
ds_read_b128 v[80:83], v8 offset:32768
v_mfma_f32_16x16x32_bf16 v[68:71], a[180:183], v[84:87], v[68:71]
v_add_u32 v5, 1, v5
v_mfma_f32_16x16x32_bf16 v[72:75], a[184:187], v[84:87], v[72:75]
v_mfma_f32_16x16x32_bf16 v[76:79], a[188:191], v[84:87], v[76:79]
s_waitcnt lgkmcnt(2)
v_mfma_f32_16x16x32_bf16 v[64:67], a[192:195], v[88:91], v[64:67]
ds_read_b128 v[84:87], v8 offset:33792
global_load_lds_dwordx4 v2, s[38:39] offset:-2048 sc1
v_mfma_f32_16x16x32_bf16 v[68:71], a[196:199], v[88:91], v[68:71]
v_mfma_f32_16x16x32_bf16 v[72:75], a[200:203], v[88:91], v[72:75]
v_mfma_f32_16x16x32_bf16 v[76:79], a[204:207], v[88:91], v[76:79]
s_waitcnt lgkmcnt(2)
v_mfma_f32_16x16x32_bf16 v[64:67], a[208:211], v[92:95], v[64:67]
ds_read_b128 v[88:91], v8 offset:34816
global_load_lds_dwordx4 v2, s[38:39] offset:-1024 sc1
v_mfma_f32_16x16x32_bf16 v[68:71], a[212:215], v[92:95], v[68:71]
global_load_dword v108, v4, s[38:39] sc1
v_mfma_f32_16x16x32_bf16 v[72:75], a[216:219], v[92:95], v[72:75]
v_mfma_f32_16x16x32_bf16 v[76:79], a[220:223], v[92:95], v[76:79]
s_waitcnt lgkmcnt(2)
v_mfma_f32_16x16x32_bf16 v[64:67], a[224:227], v[80:83], v[64:67]
ds_read_b128 v[92:95], v8 offset:35840
global_load_lds_dwordx4 v2, s[38:39] offset:0 sc1
v_mfma_f32_16x16x32_bf16 v[68:71], a[228:231], v[80:83], v[68:71]
v_mfma_f32_16x16x32_bf16 v[72:75], a[232:235], v[80:83], v[72:75]
v_xor_b32 v8, 0x10000, v8
v_mfma_f32_16x16x32_bf16 v[76:79], a[236:239], v[80:83], v[76:79]
s_waitcnt lgkmcnt(2)
v_mfma_f32_16x16x32_bf16 v[64:67], a[240:243], v[84:87], v[64:67]
s_waitcnt vmcnt(5)
s_barrier
ds_read_b128 v[80:83], v8 offset:0
v_mfma_f32_16x16x32_bf16 v[68:71], a[244:247], v[84:87], v[68:71]
global_load_lds_dwordx4 v2, s[38:39] offset:1024 sc1
v_mfma_f32_16x16x32_bf16 v[72:75], a[248:251], v[84:87], v[72:75]
v_add_u32 v2, 18688, v2
v_mfma_f32_16x16x32_bf16 v[76:79], a[252:255], v[84:87], v[76:79]
s_waitcnt lgkmcnt(2)
v_mfma_f32_16x16x32_bf16 v[64:67], v[16:19], v[88:91], v[64:67]
ds_read_b128 v[84:87], v8 offset:1024
v_mfma_f32_16x16x32_bf16 v[68:71], v[20:23], v[88:91], v[68:71]
v_mfma_f32_16x16x32_bf16 v[72:75], v[24:27], v[88:91], v[72:75]
v_mfma_f32_16x16x32_bf16 v[76:79], v[28:31], v[88:91], v[76:79]
s_waitcnt lgkmcnt(2)
v_mfma_f32_16x16x32_bf16 v[64:67], v[32:35], v[92:95], v[64:67]
ds_read_b128 v[88:91], v8 offset:2048
v_mfma_f32_16x16x32_bf16 v[68:71], v[36:39], v[92:95], v[68:71]
v_mfma_f32_16x16x32_bf16 v[72:75], v[40:43], v[92:95], v[72:75]
v_mfma_f32_16x16x32_bf16 v[76:79], v[44:47], v[92:95], v[76:79]
s_sub_u32 s70, s70, 1
s_cbranch_scc0 .Lgu_top12
s_nop 4
v_mul_f32 v96, v64, v72
v_mul_f32 v97, v65, v73
v_mul_f32 v98, v66, v74
v_mul_f32 v99, v67, v75
v_mul_f32 v100, v68, v76
v_mul_f32 v101, v69, v77
v_mul_f32 v102, v70, v78
v_mul_f32 v103, v71, v79
v_cvt_pk_bf16_f32 v104, v96, v97
v_cvt_pk_bf16_f32 v105, v98, v99
v_cvt_pk_bf16_f32 v106, v100, v101
v_cvt_pk_bf16_f32 v107, v102, v103
global_store_dwordx4 v3, v[104:107], s[38:39] sc1
s_waitcnt vmcnt(1)
s_barrier
s_waitcnt vmcnt(0)
s_barrier
s_endpgm
.Lgu_sp8:
s_sleep 4
global_load_dword v108, v4, s[38:39] sc1
s_add_u32 s71, s71, 1
s_waitcnt vmcnt(0)
v_sub_u32 v108, v108, v5
v_cmp_lt_u32 vcc, 0x7f, v108
s_cbranch_vccz .Lgu_rt9
s_cmp_lt_u32 s71, 3000000
s_cbranch_scc1 .Lgu_sp8
s_branch .Lgu_rt9
.Lgu_sp10:
s_sleep 4
global_load_dword v108, v4, s[38:39] sc1
s_add_u32 s71, s71, 1
s_waitcnt vmcnt(0)
v_sub_u32 v108, v108, v5
v_cmp_lt_u32 vcc, 0x7f, v108
s_cbranch_vccz .Lgu_rt11
s_cmp_lt_u32 s71, 3000000
s_cbranch_scc1 .Lgu_sp10
s_branch .Lgu_rt11
.Lkend:
)"
  :: "{v[0:31]}"(va), "{v[32:63]}"(vb), "{s[36:51]}"(sa), "{s[52:67]}"(sb) : "v255", "a255", "s99", "vcc", "memory");
}

extern "C" void kernel_launch(void* const* d_in, const int* in_sizes, int n_in,
                              void* d_out, int out_size, void* d_ws, size_t ws_size,
                              hipStream_t stream) {
  k_main<<<256, 256, 0, stream>>>((const char*)d_in[0], (const char*)d_in[1], (const char*)d_in[2], (const char*)d_in[4],
                                  (const char*)d_in[5], (const char*)d_in[6], (char*)d_ws, (char*)d_out);
}
```

```cpp
#include <hip/hip_runtime.h>
#include <hip/hip_bf16.h>
#include <stdint.h>
typedef __attribute__((ext_vector_type(32))) unsigned u32x32;
typedef __attribute__((ext_vector_type(16))) unsigned u32x16;
constexpr unsigned RP = 80u;
constexpr unsigned PAD = 12u;
constexpr unsigned XT = 18432u;
constexpr unsigned X1P = 18688u;
constexpr unsigned HT = 147456u;
constexpr unsigned PT = 110592u;
constexpr unsigned FL = 1024u;
constexpr unsigned FR = 102400u;
constexpr unsigned WS_FL = 4096u;
constexpr unsigned WS_ST = 413696u;
constexpr unsigned WS_O = 1048576u;
constexpr unsigned WS_X1 = 6946816u;
constexpr unsigned WS_X2 = 13000704u;
constexpr unsigned WS_XD = 15396864u;
constexpr unsigned WS_P = 17977344u;
constexpr unsigned WS_H = 53366784u;
constexpr unsigned WS_END = 100552704u;
constexpr unsigned FO = 0u;
constexpr unsigned FX1 = 8192u;
constexpr unsigned FX2 = 65536u;
constexpr unsigned FP = 10240u;
constexpr unsigned FH = 28672u;
__device__ __forceinline__ void touch(const void* p) {
  asm volatile("s_mov_b32 m0, 0x27f00\n\ts_nop 0\n\tglobal_load_lds_dword %0, off" :: "v"(p) : "memory");
}
__global__ __launch_bounds__(256, 1) void k_main(const char* x0, const char* wqkv, const char* wo, const char* wg, const char* wu, const char* wd,
                                                 char* ws, char* out) {
  __shared__ __attribute__((aligned(1024))) char smem[163840];
  asm volatile("" :: "s"((unsigned)(size_t)(__attribute__((address_space(3))) const char*)smem) : "memory");
  const unsigned lane = threadIdx.x & 63, wave = __builtin_amdgcn_readfirstlane(threadIdx.x >> 6);
  const unsigned xc = blockIdx.x & 7, jj = blockIdx.x >> 3;
  const unsigned role = jj < 4 ? 0 : jj == 4 ? 1 : jj < 14 ? 2 : 3;
  const unsigned qa_hg = xc, qa_rho = jj, wo_rho = xc >> 1, wo_cw = xc & 1, dn_i = xc * 9 + jj - 5, gu_i = xc * 18 + jj - 14;
  {
    if (wave < 2) { const unsigned u = min(jj * 108 + threadIdx.x, 3455u), sg = u >> 1, k = sg / 3; touch(wqkv + k * 3456 + (sg - 3 * k) * 1152 + xc * 144 + (u & 1) * 140); }
    else if (wave == 2) { touch(ws + min(lane * 2097152u, WS_END - 4)); }
    else {
      unsigned long long pc; unsigned len;
      asm volatile("s_getpc_b64 %0\n.Lkbeg:\n\ts_mov_b32 %1, .Lkend-.Lkbeg" : "=s"(pc), "=s"(len));
      touch((const char*)pc + min((jj * 64 + lane) * 128, len));
    }
    if (wave < 2) { const unsigned u = min(jj * 90 + threadIdx.x, 2879u), k = u / 5; touch(wo + k * 1152 + (xc & 1) * 576 + min((u - 5 * k) * 128, 572u)); }
    if (jj >= 14 && wave < 2) touch(x0 + ((blockIdx.x - 112) * 128 + threadIdx.x) * 128);
  }

  const unsigned r = lane & 15, g = lane >> 4, q = r >> 2, p = lane & 3;
  u32x32 va = {}, vb = {};
  u32x16 sa = {}, sb = {};
  unsigned sv[28] = {};
  sa[0] = __builtin_amdgcn_readfirstlane(role * 4 + wave); sa[2] = (unsigned)(unsigned long long)ws; sa[3] = (unsigned)((unsigned long long)ws >> 32);
  if (role == 0) {
    const unsigned hg = qa_hg, rho = qa_rho, row0 = rho * RP + PAD, pc0 = wave < 2 ? wave * 5 : 2 + wave * 4, hb = wave * 36 + (wave & 1) * 4;
    auto wv = [&](unsigned j) { const unsigned c = min((wave * 7 + j) * 64 + lane, 1727u), rw = c / 27, cc = c - rw * 27, sc = cc / 9; return rw * 3456 + sc * (1152 - 144) + cc * 16 + 4096 - j * 1024; };
    auto oa = [&](unsigned b) { return 43520 + (b >> 4) * 256 + r * 16 + (b & 15); };
    const unsigned m0 = 4 * g + (r & 1), m1 = m0 + 2, hm = (r & 1) ? 0xFFFF0000u : 0xFFFFu, ninf = 0xFF800000u;
    va[0] = wv(0);
    va[1] = wv(1);
    va[2] = wv(2);
    va[3] = wv(3);
    va[4] = wv(4);
    va[5] = wv(5);
    va[6] = wv(6);
    va[7] = (8 * g + q) * 432 + hb + p * 8;
    va[8] = (8 * g + q) * 432 + 32 + (p & 1) * 72 + (wave == 0 ? (p >> 1) * 144 : 288);
    va[9] = ((rho * 32 + 31) * 16 + ((pc0 + 0) & 1) * 8 + (lane >> 3)) * 1152 + ((pc0 + 0) >> 1) * 128 + (((lane ^ (lane >> 3)) & 7) << 4) + 4096;
    va[10] = ((rho * 32 + 31) * 16 + ((pc0 + 1) & 1) * 8 + (lane >> 3)) * 1152 + ((pc0 + 1) >> 1) * 128 + (((lane ^ (lane >> 3)) & 7) << 4) + 3072;
    va[11] = ((rho * 32 + 31) * 16 + ((pc0 + 2) & 1) * 8 + (lane >> 3)) * 1152 + ((pc0 + 2) >> 1) * 128 + (((lane ^ (lane >> 3)) & 7) << 4) + 2048;
    va[12] = ((rho * 32 + 31) * 16 + ((pc0 + 3) & 1) * 8 + (lane >> 3)) * 1152 + ((pc0 + 3) >> 1) * 128 + (((lane ^ (lane >> 3)) & 7) << 4) + 1024;
    va[13] = ((rho * 32 + 31) * 16 + ((pc0 + 4) & 1) * 8 + (lane >> 3)) * 1152 + ((pc0 + 4) >> 1) * 128 + (((lane ^ (lane >> 3)) & 7) << 4) + 0;
    va[14] = WS_O + (row0 - 3) * XT + hg * 2304 + wave * 1024 + (wave < 2 ? lane : r) * 16;
    va[15] = 43520 + wave * 1024 + (wave < 2 ? lane : r) * 16;
    va[16] = oa(hb + g * 8);
    va[17] = ((g & 1) == (wave >> 1)) ? oa(32 + (wave & 1) * 4 + (wave >> 1) * 72) : 46336 + lane * 4;
    va[18] = g == 0 ? 40960 + ((wave >> 1) * 16 + r) * 8 + (wave & 1) * 4 : 46080;
    va[19] = 41216 + ((wave >> 1) * 16 + r) * 8 + (wave & 1) * 4;
    va[20] = lane * 8;
    va[21] = lane * 16;
    va[22] = 43008 + r * 4;
    va[23] = 43008 + g * 16;
    va[24] = 41984 + (q * 16 + r) * 16 + p * 4;
    va[25] = 43136 + (((m0 > r) ? 0 : 1) * 16 + m0) * 4;
    va[26] = 43136 + (((m1 > r) ? 0 : 1) * 16 + m1) * 4;
    va[27] = 43136 + (((m0 > r) ? 1 : 0) * 16 + m0) * 4;
    va[28] = 43136 + (((m1 > r) ? 1 : 0) * 16 + m1) * 4;
    va[29] = 0u;
    va[30] = (4 * g + 0 > r && !((r ^ 0) & 1)) ? 0u : ninf;
    va[31] = (4 * g + 1 > r && !((r ^ 1) & 1)) ? 0u : ninf;
    vb[0] = (4 * g + 2 > r && !((r ^ 2) & 1)) ? 0u : ninf;
    vb[1] = (4 * g + 3 > r && !((r ^ 3) & 1)) ? 0u : ninf;
    vb[2] = (4 * g + 0 <= r && !((r ^ 0) & 1)) ? 0u : ninf;
    vb[3] = (4 * g + 1 <= r && !((r ^ 1) & 1)) ? 0u : ninf;
    vb[4] = (4 * g + 2 <= r && !((r ^ 2) & 1)) ? 0u : ninf;
    vb[5] = (4 * g + 3 <= r && !((r ^ 3) & 1)) ? 0u : ninf;
    vb[6] = m0 > r ? hm : 0u;
    vb[7] = m1 > r ? hm : 0u;
    vb[8] = m0 > r ? 0u : hm;
    vb[9] = m1 > r ? 0u : hm;
    vb[10] = WS_FL + rho * FR + FX2 + min(lane, 35u) * FL;
    vb[11] = WS_FL + rho * FR + FX2 + min(lane, 35u) * FL;
    vb[12] = 0u - 31;
    vb[13] = WS_FL + rho * FR + FO + hg * FL;
    vb[14] = 0u - 4;
    vb[15] = WS_FL + lane * FL;
    vb[16] = 0u;
    vb[17] = r * 128 + (((0 + g) ^ (r & 7)) << 4);
    vb[18] = r * 128 + (((4 + g) ^ (r & 7)) << 4);
    sv[0] = (unsigned)(unsigned long long)(wqkv + hg * 144);
    sv[1] = (unsigned)((unsigned long long)(wqkv + hg * 144) >> 32);
    sv[2] = (unsigned)(unsigned long long)(x0 - 2048);
    sv[3] = (unsigned)((unsigned long long)(x0 - 2048) >> 32);
    sv[4] = (unsigned)(unsigned long long)((ws + WS_X2) - x0 + (rho ? 32 : 31) * (long long)XT);
    sv[5] = (unsigned)((unsigned long long)((ws + WS_X2) - x0 + (rho ? 32 : 31) * (long long)XT) >> 32);
    sv[6] = (unsigned)(unsigned long long)(-(long long)XT);
    sv[7] = (unsigned)((unsigned long long)(-(long long)XT) >> 32);
    sv[8] = 47104 + wave * 7168;
    sv[9] = pc0 * 1024 + 2048;
    sv[10] = rho ? 0u : XT;
    sv[11] = rho ? 0u - FR : 0u;
    sv[12] = rho ? 0u : ninf;
    sv[13] = hg * 4 + rho;
  } else if (role == 1) {
    const unsigned rho = wo_rho, cw = wo_cw, row0 = rho * RP + PAD, pc0 = wave * 5;
    auto wv = [&](unsigned j) { const unsigned b = j * 1024 + lane * 16, rw = b / 576; return rw * 576 + b + 4096 - (j & 7) * 1024; };
    va[0] = wv(0);
    va[1] = wv(1);
    va[2] = wv(2);
    va[3] = wv(3);
    va[4] = wv(4);
    va[5] = wv(5);
    va[6] = wv(6);
    va[7] = wv(7);
    va[8] = wv(8);
    va[9] = (8 * g + q) * 576 + cw * 64 + wave * 128 + p * 16;
    va[10] = (8 * g + q) * 576 + (cw ? 0 : 512) + p * 16 + (wave >> 1) * 8;
    va[11] = WS_O + (row0 - 2) * XT + pc0 * 1024 + 2048 + lane * 16;
    va[12] = WS_O + (row0 + 63) * XT + pc0 * 1024 + 2048 + lane * 16;
    va[13] = WS_X1 + (row0 - 5) * X1P + (cw * 10 + wave * 2) * 1024 + lane * 16;
    va[14] = WS_X1 + (row0 - 4) * X1P + (8 + cw) * 1024 + lane * 16;
    va[15] = WS_X1 + (row0 - 6) * X1P + XT + cw * 128 + r * 8;
    va[16] = ((rho * 32 + 31) * 16 + 0 + (lane >> 3)) * 1152 + (cw * 5 + wave) * 128 + (((lane ^ (lane >> 3)) & 7) << 4) - 0;
    va[17] = ((rho * 32 + 31) * 16 + 8 + (lane >> 3)) * 1152 + (cw * 5 + wave) * 128 + (((lane ^ (lane >> 3)) & 7) << 4) - 1024;
    va[18] = ((rho * 32 + 31) * 16 + r) * 1152 + (8 + cw) * 64 + g * 16;
    va[19] = 47104 + wave * 2048 + r * 128 + (((0 + g) ^ (r & 7)) << 4);
    va[20] = 47104 + wave * 2048 + r * 128 + (((4 + g) ^ (r & 7)) << 4);
    va[21] = WS_FL + rho * FR + FO + (lane & 7) * FL;
    va[22] = 0u - 2;
    va[23] = WS_FL + rho * FR + FX1 + cw * FL;
    va[24] = 0u - 5;
    va[25] = lane * 16;
    va[26] = lane * 16;
    va[27] = lane * 8;
    sv[0] = (unsigned)(unsigned long long)(wo + wave * 16 * 1152 + cw * 576);
    sv[1] = (unsigned)((unsigned long long)(wo + wave * 16 * 1152 + cw * 576) >> 32);
    sv[2] = (unsigned)(unsigned long long)(x0);
    sv[3] = (unsigned)((unsigned long long)(x0) >> 32);
    sv[4] = (unsigned)(unsigned long long)(x0);
    sv[5] = (unsigned)((unsigned long long)(x0) >> 32);
    sv[6] = (unsigned)(unsigned long long)(ws + WS_X2);
    sv[7] = (unsigned)((unsigned long long)(ws + WS_X2) >> 32);
    sv[8] = wave * 9216;
    sv[9] = pc0 * 1024 + 2048;
  } else if (role == 2) {
    const unsigned tri = dn_i / 3, nu = dn_i % 3, kap = tri >> 2, rho = tri & 3, row0 = rho * RP + PAD;
    auto wv = [&](unsigned j) { const unsigned b = j * 1024 + lane * 16, rw = b / 384; return rw * (1152 - 384) + b + 4096 - j * 1024; };
    va[0] = wv(0);
    va[1] = wv(1);
    va[2] = wv(2);
    va[3] = wv(3);
    va[4] = wv(4);
    va[5] = wv(5);
    va[6] = (8 * g + q) * 384 + wave * 64 + p * 16 + (kap & 1) * 8;
    va[7] = (8 * g + q) * 384 + wave * 64 + p * 16 + 8 - (kap & 1) * 8;
    va[8] = (8 * g + q) * 384 + 256 + wave * 32 + (p ^ (kap & 1)) * 8;
    va[9] = WS_H + row0 * HT + (kap * 24 + wave * 6 + 2) * 1024 + lane * 16;
    va[10] = WS_H + (row0 + 63) * HT + (kap * 24 + wave * 6 + 2) * 1024 + lane * 16;
    va[11] = WS_P + (row0 - 3) * PT + (((nu * 3 + (wave >> 1)) * 4 + q) * 6 + kap) * 512 + (wave & 1) * 256 + g * 64 + p * 16;
    va[12] = WS_P + (row0 - 3) * PT + (((nu * 3 + 2) * 4 + q) * 6 + kap) * 512 + wave * 128 + (g >> 1) * 64 + p * 16;
    va[13] = WS_FL + rho * FR + FH + (kap * 6 + min(lane, 5u)) * FL;
    va[14] = 0u;
    va[15] = WS_FL + rho * FR + FP + (nu * 6 + kap) * FL;
    va[16] = 0u - 4;
    va[17] = lane * 16;
    sv[0] = (unsigned)(unsigned long long)(wd + (kap * 768 + wave * 16) * 1152 + nu * 384);
    sv[1] = (unsigned)((unsigned long long)(wd + (kap * 768 + wave * 16) * 1152 + nu * 384) >> 32);
    sv[2] = wave * 6144;
    sv[3] = wave * 6144 + 2048;
  } else {
    const unsigned ii = gu_i, gam = ii >> 2, rho = ii & 3, row0 = rho * RP + PAD, pc0 = wave * 6;
    const unsigned bk = gam >> 2, qd = gam & 3, od = (lane >> 3) & 1, un = g * 8 + (lane & 7), jr = qd * 4 + p, jc = (un >> 2) * 8 + od * 4;
    va[0] = g * 9216 + ((r ^ (g << 2)) << 4) + 4096;
    va[1] = (8 * g + q) * 256 + (((4 * wave + p) ^ (q << 2)) << 4);
    va[2] = WS_X1 + row0 * X1P + pc0 * 1024 + 2048 + lane * 16;
    va[3] = WS_H + (row0 - 3) * HT + (gam * 4 + wave) * 1024 + lane * 16;
    va[4] = WS_FL + rho * FR + FX1 + (lane & 1) * FL;
    va[5] = 0u;
    va[6] = WS_FL + rho * FR + FH + gam * FL;
    va[7] = 0u - 4;
    va[8] = lane * 16;
    va[9] = WS_X1 + (row0 + 63) * X1P + pc0 * 1024 + 2048 + lane * 16;
    va[10] = WS_P + row0 * PT + gam * 3072 + od * 512 + un * 16;
    va[11] = WS_X1 + row0 * X1P + bk * 2048 + (un >> 2) * 256 + jr * 16 + od * 8;
    va[12] = WS_X1 + row0 * X1P + XT + jr * 8;
    va[13] = ((rho * 32 + 31) * 16 + qd * 4 + g) * 1152 + bk * 128 + r * 8;
    va[14] = 49152 + wave * 1024 + p * 144 + jc * 2;
    va[15] = 49152 + wave * 1024 + g * 144 + r * 8;
    va[16] = WS_FL + rho * FR + FP + (bk / 3 * 6 + min(lane, 5u)) * FL;
    va[17] = 0u;
    va[18] = WS_FL + rho * FR + FX2 + gam * FL;
    va[19] = (rho * 144 + (wave & 1) * 40 + (lane >> 1)) * 9216 + (lane & 1) * 128;
    va[20] = 0u;
    va[21] = 0u;
    va[22] = (g == q && p < 2) ? (0x3F80u << (16 * p)) : 0u;
    va[23] = (g == q && p >= 2) ? (0x3F80u << (16 * (p - 2))) : 0u;
    va[24] = 0u;
    va[25] = 0u;
    sv[0] = (unsigned)(unsigned long long)((wave < 2 ? wg : wu) + gam * 256 + (wave & 1) * (32 * 9216));
    sv[1] = (unsigned)((unsigned long long)((wave < 2 ? wg : wu) + gam * 256 + (wave & 1) * (32 * 9216)) >> 32);
    sv[2] = wave * 8192;
    sv[3] = pc0 * 1024 + 2048;
    sv[4] = (unsigned)(unsigned long long)(ws + WS_X2);
    sv[5] = (unsigned)((unsigned long long)(ws + WS_X2) >> 32);
    sv[6] = (unsigned)(unsigned long long)(out + 32 * XT);
    sv[7] = (unsigned)((unsigned long long)(out + 32 * XT) >> 32);
    sv[8] = (unsigned)(unsigned long long)(ws + WS_XD);
    sv[9] = (unsigned)((unsigned long long)(ws + WS_XD) >> 32);
    sv[10] = (unsigned)(unsigned long long)(ws - PT);
    sv[11] = (unsigned)((unsigned long long)(ws - PT) >> 32);
    sv[12] = (unsigned)(unsigned long long)(ws - X1P);
    sv[13] = (unsigned)((unsigned long long)(ws - X1P) >> 32);
    sv[14] = (unsigned)(unsigned long long)(ws + WS_X2);
    sv[15] = (unsigned)((unsigned long long)(ws + WS_X2) >> 32);
    sv[16] = (unsigned)(unsigned long long)(ws + WS_XD);
    sv[17] = (unsigned)((unsigned long long)(ws + WS_XD) >> 32);
    sv[18] = (unsigned)(unsigned long long)(ws + WS_XD);
    sv[19] = (unsigned)((unsigned long long)(ws + WS_XD) >> 32);
    sv[20] = 0u;
  }
#pragma unroll
  for (int i = 0; i < 28; ++i) { const unsigned u = __builtin_amdgcn_readfirstlane(sv[i]); if (i < 12) sa[i + 4] = u; else sb[i - 12] = u; }

  asm volatile(
R"(s_mov_b32 s71, 0
s_mov_b32 s74, 0
s_load_dword s37, s[38:39], 0x0
s_lshr_b32 s75, s36, 2
s_cmp_eq_u32 s75, 1
s_cbranch_scc1 .Lrole1
s_cmp_eq_u32 s75, 2
s_cbranch_scc1 .Lrole2
s_cmp_eq_u32 s75, 3
s_cbranch_scc1 .Lrole3
.Lrole0:
s_add_u32 m0, s48, 4096
s_nop 0
global_load_lds_dwordx4 v0, s[40:41] offset:-4096
global_load_lds_dwordx4 v1, s[40:41] offset:-3072
global_load_lds_dwordx4 v2, s[40:41] offset:-2048
global_load_lds_dwordx4 v3, s[40:41] offset:-1024
global_load_lds_dwordx4 v4, s[40:41] offset:0
global_load_lds_dwordx4 v5, s[40:41] offset:1024
global_load_lds_dwordx4 v6, s[40:41] offset:2048
s_add_u32 s40, s40, 221184
s_addc_u32 s41, s41, 0
s_add_u32 m0, s48, 32768
s_nop 0
global_load_lds_dwordx4 v0, s[40:41] offset:-4096
global_load_lds_dwordx4 v1, s[40:41] offset:-3072
global_load_lds_dwordx4 v2, s[40:41] offset:-2048
global_load_lds_dwordx4 v3, s[40:41] offset:-1024
global_load_lds_dwordx4 v4, s[40:41] offset:0
global_load_lds_dwordx4 v5, s[40:41] offset:1024
global_load_lds_dwordx4 v6, s[40:41] offset:2048
s_add_u32 s40, s40, 221184
s_addc_u32 s41, s41, 0
s_add_u32 m0, s48, 61440
s_nop 0
global_load_lds_dwordx4 v0, s[40:41] offset:-4096
global_load_lds_dwordx4 v1, s[40:41] offset:-3072
global_load_lds_dwordx4 v2, s[40:41] offset:-2048
global_load_lds_dwordx4 v3, s[40:41] offset:-1024
global_load_lds_dwordx4 v4, s[40:41] offset:0
global_load_lds_dwordx4 v5, s[40:41] offset:1024
global_load_lds_dwordx4 v6, s[40:41] offset:2048
s_add_u32 s40, s40, 221184
s_addc_u32 s41, s41, 0
s_add_u32 m0, s48, 90112
s_nop 0
global_load_lds_dwordx4 v0, s[40:41] offset:-4096
global_load_lds_dwordx4 v1, s[40:41] offset:-3072
global_load_lds_dwordx4 v2, s[40:41] offset:-2048
global_load_lds_dwordx4 v3, s[40:41] offset:-1024
global_load_lds_dwordx4 v4, s[40:41] offset:0
global_load_lds_dwordx4 v5, s[40:41] offset:1024
global_load_lds_dwordx4 v6, s[40:41] offset:2048
s_add_u32 s40, s40, 221184
s_addc_u32 s41, s41, 0
s_add_u32 m0, s49, 0
s_nop 0
global_load_lds_dwordx4 v9, s[42:43] offset:-2048 sc1
global_load_lds_dwordx4 v10, s[42:43] offset:-1024 sc1
global_load_lds_dwordx4 v11, s[42:43] offset:0 sc1
global_load_lds_dwordx4 v12, s[42:43] offset:1024 sc1
s_bitcmp1_b32 s36, 1
s_cbranch_scc1 .Lqa_x0
global_load_lds_dwordx4 v13, s[42:43] offset:2048 sc1
.Lqa_x0:
s_sub_u32 s42, s42, 18432
s_subb_u32 s43, s43, 0
s_add_u32 m0, s49, 18432
s_nop 0
global_load_lds_dwordx4 v9, s[42:43] offset:-2048 sc1
global_load_lds_dwordx4 v10, s[42:43] offset:-1024 sc1
global_load_lds_dwordx4 v11, s[42:43] offset:0 sc1
global_load_lds_dwordx4 v12, s[42:43] offset:1024 sc1
s_bitcmp1_b32 s36, 1
s_cbranch_scc1 .Lqa_x1
global_load_lds_dwordx4 v13, s[42:43] offset:2048 sc1
.Lqa_x1:
s_sub_u32 s42, s42, 18432
s_subb_u32 s43, s43, 0
v_add_u32 v255, 0x10000, v7
v_add_u32 v160, 0x20000, v7
v_add_u32 v161, 0x10000, v8
v_add_u32 v162, 0x20000, v8
v_mov_b32 v104, 0x3F803F80
v_mov_b32 v105, 0x3F803F80
v_mov_b32 v106, 0x3F803F80
v_mov_b32 v107, 0x3F803F80
v_mov_b32 v139, 0
v_mov_b32 v143, 0
v_mov_b32 v147, 0
v_mov_b32 v151, 0
v_mov_b32 v163, 46080
s_waitcnt lgkmcnt(0)
s_mul_i32 s37, s37, 0x9E3779B1
s_add_u32 s37, s37, 0x7F4A7C15
v_add_u32 v44, s37, v44
v_add_u32 v46, s37, v46
v_add_u32 v48, s37, v48
ds_write_b32 v163, v139
s_mov_b64 s[76:77], 0
s_mov_b32 s73, 18
s_waitcnt vmcnt(29)
s_barrier
ds_read_b64_tr_b16 a[0:1], v7 offset:47104
ds_read_b64_tr_b16 a[2:3], v7 offset:48832
ds_read_b64_tr_b16 a[4:5], v7 offset:47248
ds_read_b64_tr_b16 a[6:7], v7 offset:48976
ds_read_b64_tr_b16 a[8:9], v7 offset:47392
ds_read_b64_tr_b16 a[10:11], v7 offset:49120
ds_read_b64_tr_b16 a[216:217], v8 offset:47104
ds_read_b64_tr_b16 a[218:219], v8 offset:48832
ds_read_b64_tr_b16 a[12:13], v7 offset:60928
ds_read_b64_tr_b16 a[14:15], v7 offset:62656
ds_read_b64_tr_b16 a[16:17], v7 offset:61072
ds_read_b64_tr_b16 a[18:19], v7 offset:62800
ds_read_b64_tr_b16 a[20:21], v7 offset:61216
ds_read_b64_tr_b16 a[22:23], v7 offset:62944
ds_read_b64_tr_b16 a[220:221], v8 offset:60928
ds_read_b64_tr_b16 a[222:223], v8 offset:62656
s_waitcnt lgkmcnt(0)
s_barrier
s_add_u32 m0, s48, 4096
s_nop 0
global_load_lds_dwordx4 v0, s[40:41] offset:-4096
global_load_lds_dwordx4 v1, s[40:41] offset:-3072
global_load_lds_dwordx4 v2, s[40:41] offset:-2048
global_load_lds_dwordx4 v3, s[40:41] offset:-1024
global_load_lds_dwordx4 v4, s[40:41] offset:0
global_load_lds_dwordx4 v5, s[40:41] offset:1024
global_load_lds_dwordx4 v6, s[40:41] offset:2048
s_add_u32 s40, s40, 221184
s_addc_u32 s41, s41, 0
s_waitcnt vmcnt(29)
s_barrier
ds_read_b64_tr_b16 a[24:25], v255 offset:10240
ds_read_b64_tr_b16 a[26:27], v255 offset:11968
ds_read_b64_tr_b16 a[28:29], v255 offset:10384
ds_read_b64_tr_b16 a[30:31], v255 offset:12112
ds_read_b64_tr_b16 a[32:33], v255 offset:10528
ds_read_b64_tr_b16 a[34:35], v255 offset:12256
ds_read_b64_tr_b16 a[224:225], v161 offset:10240
ds_read_b64_tr_b16 a[226:227], v161 offset:11968
ds_read_b64_tr_b16 a[36:37], v255 offset:24064
ds_read_b64_tr_b16 a[38:39], v255 offset:25792
ds_read_b64_tr_b16 a[40:41], v255 offset:24208
ds_read_b64_tr_b16 a[42:43], v255 offset:25936
ds_read_b64_tr_b16 a[44:45], v255 offset:24352
ds_read_b64_tr_b16 a[46:47], v255 offset:26080
ds_read_b64_tr_b16 a[228:229], v161 offset:24064
ds_read_b64_tr_b16 a[230:231], v161 offset:25792
v_mfma_f32_16x16x32_bf16 v[84:87], a[0:3], v[104:107], 0
v_mfma_f32_16x16x32_bf16 v[88:91], a[4:7], v[104:107], 0
v_mfma_f32_16x16x32_bf16 v[92:95], v[104:107], a[8:11], 0
v_mfma_f32_16x16x32_bf16 v[96:99], a[216:219], v[104:107], 0
v_mfma_f32_16x16x32_bf16 v[100:103], v[104:107], a[216:219], 0
v_mfma_f32_16x16x32_bf16 v[84:87], a[12:15], v[104:107], v[84:87]
v_mfma_f32_16x16x32_bf16 v[88:91], a[16:19], v[104:107], v[88:91]
v_mfma_f32_16x16x32_bf16 v[92:95], v[104:107], a[20:23], v[92:95]
v_mfma_f32_16x16x32_bf16 v[96:99], a[220:223], v[104:107], v[96:99]
v_mfma_f32_16x16x32_bf16 v[100:103], v[104:107], a[220:223], v[100:103]
s_waitcnt lgkmcnt(0)
s_barrier
s_add_u32 m0, s48, 32768
s_nop 0
global_load_lds_dwordx4 v0, s[40:41] offset:-4096
global_load_lds_dwordx4 v1, s[40:41] offset:-3072
global_load_lds_dwordx4 v2, s[40:41] offset:-2048
global_load_lds_dwordx4 v3, s[40:41] offset:-1024
global_load_lds_dwordx4 v4, s[40:41] offset:0
global_load_lds_dwordx4 v5, s[40:41] offset:1024
global_load_lds_dwordx4 v6, s[40:41] offset:2048
s_add_u32 s40, s40, 221184
s_addc_u32 s41, s41, 0
s_waitcnt vmcnt(29)
s_barrier
ds_read_b64_tr_b16 a[48:49], v255 offset:38912
ds_read_b64_tr_b16 a[50:51], v255 offset:40640
ds_read_b64_tr_b16 a[52:53], v255 offset:39056
ds_read_b64_tr_b16 a[54:55], v255 offset:40784
ds_read_b64_tr_b16 a[56:57], v255 offset:39200
ds_read_b64_tr_b16 a[58:59], v255 offset:40928
ds_read_b64_tr_b16 a[232:233], v161 offset:38912
ds_read_b64_tr_b16 a[234:235], v161 offset:40640
ds_read_b64_tr_b16 a[60:61], v255 offset:52736
ds_read_b64_tr_b16 a[62:63], v255 offset:54464
ds_read_b64_tr_b16 a[64:65], v255 offset:52880
ds_read_b64_tr_b16 a[66:67], v255 offset:54608
ds_read_b64_tr_b16 a[68:69], v255 offset:53024
ds_read_b64_tr_b16 a[70:71], v255 offset:54752
ds_read_b64_tr_b16 a[236:237], v161 offset:52736
ds_read_b64_tr_b16 a[238:239], v161 offset:54464
v_mfma_f32_16x16x32_bf16 v[84:87], a[24:27], v[104:107], v[84:87]
v_mfma_f32_16x16x32_bf16 v[88:91], a[28:31], v[104:107], v[88:91]
v_mfma_f32_16x16x32_bf16 v[92:95], v[104:107], a[32:35], v[92:95]
v_mfma_f32_16x16x32_bf16 v[96:99], a[224:227], v[104:107], v[96:99]
v_mfma_f32_16x16x32_bf16 v[100:103], v[104:107], a[224:227], v[100:103]
v_mfma_f32_16x16x32_bf16 v[84:87], a[36:39], v[104:107], v[84:87]
v_mfma_f32_16x16x32_bf16 v[88:91], a[40:43], v[104:107], v[88:91]
v_mfma_f32_16x16x32_bf16 v[92:95], v[104:107], a[44:47], v[92:95]
v_mfma_f32_16x16x32_bf16 v[96:99], a[228:231], v[104:107], v[96:99]
v_mfma_f32_16x16x32_bf16 v[100:103], v[104:107], a[228:231], v[100:103]
s_waitcnt lgkmcnt(0)
s_barrier
s_add_u32 m0, s48, 61440
s_nop 0
global_load_lds_dwordx4 v0, s[40:41] offset:-4096
global_load_lds_dwordx4 v1, s[40:41] offset:-3072
global_load_lds_dwordx4 v2, s[40:41] offset:-2048
global_load_lds_dwordx4 v3, s[40:41] offset:-1024
global_load_lds_dwordx4 v4, s[40:41] offset:0
global_load_lds_dwordx4 v5, s[40:41] offset:1024
global_load_lds_dwordx4 v6, s[40:41] offset:2048
s_add_u32 s40, s40, 221184
s_addc_u32 s41, s41, 0
s_waitcnt vmcnt(29)
s_barrier
ds_read_b64_tr_b16 a[72:73], v160 offset:2048
ds_read_b64_tr_b16 a[74:75], v160 offset:3776
ds_read_b64_tr_b16 a[76:77], v160 offset:2192
ds_read_b64_tr_b16 a[78:79], v160 offset:3920
ds_read_b64_tr_b16 a[80:81], v160 offset:2336
ds_read_b64_tr_b16 a[82:83], v160 offset:4064
ds_read_b64_tr_b16 a[240:241], v162 offset:2048
ds_read_b64_tr_b16 a[242:243], v162 offset:3776
ds_read_b64_tr_b16 a[84:85], v160 offset:15872
ds_read_b64_tr_b16 a[86:87], v160 offset:17600
ds_read_b64_tr_b16 a[88:89], v160 offset:16016
ds_read_b64_tr_b16 a[90:91], v160 offset:17744
ds_read_b64_tr_b16 a[92:93], v160 offset:16160
ds_read_b64_tr_b16 a[94:95], v160 offset:17888
ds_read_b64_tr_b16 a[244:245], v162 offset:15872
ds_read_b64_tr_b16 a[246:247], v162 offset:17600
v_mfma_f32_16x16x32_bf16 v[84:87], a[48:51], v[104:107], v[84:87]
v_mfma_f32_16x16x32_bf16 v[88:91], a[52:55], v[104:107], v[88:91]
v_mfma_f32_16x16x32_bf16 v[92:95], v[104:107], a[56:59], v[92:95]
v_mfma_f32_16x16x32_bf16 v[96:99], a[232:235], v[104:107], v[96:99]
v_mfma_f32_16x16x32_bf16 v[100:103], v[104:107], a[232:235], v[100:103]
v_mfma_f32_16x16x32_bf16 v[84:87], a[60:63], v[104:107], v[84:87]
v_mfma_f32_16x16x32_bf16 v[88:91], a[64:67], v[104:107], v[88:91]
v_mfma_f32_16x16x32_bf16 v[92:95], v[104:107], a[68:71], v[92:95]
v_mfma_f32_16x16x32_bf16 v[96:99], a[236:239], v[104:107], v[96:99]
v_mfma_f32_16x16x32_bf16 v[100:103], v[104:107], a[236:239], v[100:103]
s_waitcnt lgkmcnt(0)
s_barrier
s_add_u32 m0, s48, 90112
s_nop 0
global_load_lds_dwordx4 v0, s[40:41] offset:-4096
global_load_lds_dwordx4 v1, s[40:41] offset:-3072
global_load_lds_dwordx4 v2, s[40:41] offset:-2048
global_load_lds_dwordx4 v3, s[40:41] offset:-1024
global_load_lds_dwordx4 v4, s[40:41] offset:0
global_load_lds_dwordx4 v5, s[40:41] offset:1024
global_load_lds_dwordx4 v6, s[40:41] offset:2048
s_add_u32 s40, s40, 221184
s_addc_u32 s41, s41, 0
s_waitcnt vmcnt(21)
s_barrier
ds_read_b64_tr_b16 a[96:97], v7 offset:47104
ds_read_b64_tr_b16 a[98:99], v7 offset:48832
ds_read_b64_tr_b16 a[100:101], v7 offset:47248
ds_read_b64_tr_b16 a[102:103], v7 offset:48976
ds_read_b64_tr_b16 a[104:105], v7 offset:47392
ds_read_b64_tr_b16 a[106:107], v7 offset:49120
ds_read_b64_tr_b16 a[248:249], v8 offset:47104
ds_read_b64_tr_b16 a[250:251], v8 offset:48832
ds_read_b64_tr_b16 a[108:109], v7 offset:60928
ds_read_b64_tr_b16 a[110:111], v7 offset:62656
ds_read_b64_tr_b16 a[112:113], v7 offset:61072
ds_read_b64_tr_b16 a[114:115], v7 offset:62800
ds_read_b64_tr_b16 a[116:117], v7 offset:61216
ds_read_b64_tr_b16 a[118:119], v7 offset:62944
ds_read_b64_tr_b16 a[252:253], v8 offset:60928
ds_read_b64_tr_b16 a[254:255], v8 offset:62656
v_mfma_f32_16x16x32_bf16 v[84:87], a[72:75], v[104:107], v[84:87]
v_mfma_f32_16x16x32_bf16 v[88:91], a[76:79], v[104:107], v[88:91]
v_mfma_f32_16x16x32_bf16 v[92:95], v[104:107], a[80:83], v[92:95]
v_mfma_f32_16x16x32_bf16 v[96:99], a[240:243], v[104:107], v[96:99]
v_mfma_f32_16x16x32_bf16 v[100:103], v[104:107], a[240:243], v[100:103]
v_mfma_f32_16x16x32_bf16 v[84:87], a[84:87], v[104:107], v[84:87]
v_mfma_f32_16x16x32_bf16 v[88:91], a[88:91], v[104:107], v[88:91]
v_mfma_f32_16x16x32_bf16 v[92:95], v[104:107], a[92:95], v[92:95]
v_mfma_f32_16x16x32_bf16 v[96:99], a[244:247], v[104:107], v[96:99]
v_mfma_f32_16x16x32_bf16 v[100:103], v[104:107], a[244:247], v[100:103]
s_waitcnt lgkmcnt(0)
s_barrier
s_add_u32 m0, s48, 4096
s_nop 0
global_load_lds_dwordx4 v0, s[40:41] offset:-4096
global_load_lds_dwordx4 v1, s[40:41] offset:-3072
global_load_lds_dwordx4 v2, s[40:41] offset:-2048
global_load_lds_dwordx4 v3, s[40:41] offset:-1024
global_load_lds_dwordx4 v4, s[40:41] offset:0
global_load_lds_dwordx4 v5, s[40:41] offset:1024
global_load_lds_dwordx4 v6, s[40:41] offset:2048
s_add_u32 s40, s40, 221184
s_addc_u32 s41, s41, 0
s_waitcnt vmcnt(21)
s_barrier
ds_read_b64_tr_b16 a[120:121], v255 offset:10240
ds_read_b64_tr_b16 a[122:123], v255 offset:11968
ds_read_b64_tr_b16 a[124:125], v255 offset:10384
ds_read_b64_tr_b16 a[126:127], v255 offset:12112
ds_read_b64_tr_b16 a[128:129], v255 offset:10528
ds_read_b64_tr_b16 a[130:131], v255 offset:12256
ds_read_b64_tr_b16 v[52:53], v161 offset:10240
ds_read_b64_tr_b16 v[54:55], v161 offset:11968
ds_read_b64_tr_b16 a[132:133], v255 offset:24064
ds_read_b64_tr_b16 a[134:135], v255 offset:25792
ds_read_b64_tr_b16 a[136:137], v255 offset:24208
ds_read_b64_tr_b16 a[138:139], v255 offset:25936
ds_read_b64_tr_b16 a[140:141], v255 offset:24352
ds_read_b64_tr_b16 a[142:143], v255 offset:26080
ds_read_b64_tr_b16 v[56:57], v161 offset:24064
ds_read_b64_tr_b16 v[58:59], v161 offset:25792
v_mfma_f32_16x16x32_bf16 v[84:87], a[96:99], v[104:107], v[84:87]
v_mfma_f32_16x16x32_bf16 v[88:91], a[100:103], v[104:107], v[88:91]
v_mfma_f32_16x16x32_bf16 v[92:95], v[104:107], a[104:107], v[92:95]
v_mfma_f32_16x16x32_bf16 v[96:99], a[248:251], v[104:107], v[96:99]
v_mfma_f32_16x16x32_bf16 v[100:103], v[104:107], a[248:251], v[100:103]
v_mfma_f32_16x16x32_bf16 v[84:87], a[108:111], v[104:107], v[84:87]
v_mfma_f32_16x16x32_bf16 v[88:91], a[112:115], v[104:107], v[88:91]
v_mfma_f32_16x16x32_bf16 v[92:95], v[104:107], a[116:119], v[92:95]
v_mfma_f32_16x16x32_bf16 v[96:99], a[252:255], v[104:107], v[96:99]
v_mfma_f32_16x16x32_bf16 v[100:103], v[104:107], a[252:255], v[100:103]
s_waitcnt vmcnt(14)
s_barrier
ds_read_b64_tr_b16 a[144:145], v255 offset:38912
ds_read_b64_tr_b16 a[146:147], v255 offset:40640
ds_read_b64_tr_b16 a[148:149], v255 offset:39056
ds_read_b64_tr_b16 a[150:151], v255 offset:40784
ds_read_b64_tr_b16 a[152:153], v255 offset:39200
ds_read_b64_tr_b16 a[154:155], v255 offset:40928
ds_read_b64_tr_b16 v[60:61], v161 offset:38912
ds_read_b64_tr_b16 v[62:63], v161 offset:40640
ds_read_b64_tr_b16 a[156:157], v255 offset:52736
ds_read_b64_tr_b16 a[158:159], v255 offset:54464
ds_read_b64_tr_b16 a[160:161], v255 offset:52880
ds_read_b64_tr_b16 a[162:163], v255 offset:54608
ds_read_b64_tr_b16 a[164:165], v255 offset:53024
ds_read_b64_tr_b16 a[166:167], v255 offset:54752
ds_read_b64_tr_b16 v[64:65], v161 offset:52736
ds_read_b64_tr_b16 v[66:67], v161 offset:54464
s_waitcnt lgkmcnt(15)
v_mfma_f32_16x16x32_bf16 v[84:87], a[120:123], v[104:107], v[84:87]
v_mfma_f32_16x16x32_bf16 v[88:91], a[124:127], v[104:107], v[88:91]
v_mfma_f32_16x16x32_bf16 v[92:95], v[104:107], a[128:131], v[92:95]
v_mfma_f32_16x16x32_bf16 v[96:99], v[52:55], v[104:107], v[96:99]
v_mfma_f32_16x16x32_bf16 v[100:103], v[104:107], v[52:55], v[100:103]
v_mfma_f32_16x16x32_bf16 v[84:87], a[132:135], v[104:107], v[84:87]
v_mfma_f32_16x16x32_bf16 v[88:91], a[136:139], v[104:107], v[88:91]
v_mfma_f32_16x16x32_bf16 v[92:95], v[104:107], a[140:143], v[92:95]
v_mfma_f32_16x16x32_bf16 v[96:99], v[56:59], v[104:107], v[96:99]
v_mfma_f32_16x16x32_bf16 v[100:103], v[104:107], v[56:59], v[100:103]
s_waitcnt vmcnt(7)
s_barrier
ds_read_b64_tr_b16 a[168:169], v160 offset:2048
ds_read_b64_tr_b16 a[170:171], v160 offset:3776
ds_read_b64_tr_b16 a[172:173], v160 offset:2192
ds_read_b64_tr_b16 a[174:175], v160 offset:3920
ds_read_b64_tr_b16 a[176:177], v160 offset:2336
ds_read_b64_tr_b16 a[178:179], v160 offset:4064
ds_read_b64_tr_b16 v[68:69], v162 offset:2048
ds_read_b64_tr_b16 v[70:71], v162 offset:3776
ds_read_b64_tr_b16 a[180:181], v160 offset:15872
ds_read_b64_tr_b16 a[182:183], v160 offset:17600
ds_read_b64_tr_b16 a[184:185], v160 offset:16016
ds_read_b64_tr_b16 a[186:187], v160 offset:17744
ds_read_b64_tr_b16 a[188:189], v160 offset:16160
ds_read_b64_tr_b16 a[190:191], v160 offset:17888
ds_read_b64_tr_b16 v[72:73], v162 offset:15872
ds_read_b64_tr_b16 v[74:75], v162 offset:17600
s_waitcnt lgkmcnt(15)
v_mfma_f32_16x16x32_bf16 v[84:87], a[144:147], v[104:107], v[84:87]
v_mfma_f32_16x16x32_bf16 v[88:91], a[148:151], v[104:107], v[88:91]
v_mfma_f32_16x16x32_bf16 v[92:95], v[104:107], a[152:155], v[92:95]
v_mfma_f32_16x16x32_bf16 v[96:99], v[60:63], v[104:107], v[96:99]
v_mfma_f32_16x16x32_bf16 v[100:103], v[104:107], v[60:63], v[100:103]
v_mfma_f32_16x16x32_bf16 v[84:87], a[156:159], v[104:107], v[84:87]
v_mfma_f32_16x16x32_bf16 v[88:91], a[160:163], v[104:107], v[88:91]
v_mfma_f32_16x16x32_bf16 v[92:95], v[104:107], a[164:167], v[92:95]
v_mfma_f32_16x16x32_bf16 v[96:99], v[64:67], v[104:107], v[96:99]
v_mfma_f32_16x16x32_bf16 v[100:103], v[104:107], v[64:67], v[100:103]
s_waitcnt vmcnt(0)
s_barrier
ds_read_b64_tr_b16 a[192:193], v7 offset:47104
ds_read_b64_tr_b16 a[194:195], v7 offset:48832
ds_read_b64_tr_b16 a[196:197], v7 offset:47248
ds_read_b64_tr_b16 a[198:199], v7 offset:48976
ds_read_b64_tr_b16 a[200:201], v7 offset:47392
ds_read_b64_tr_b16 a[202:203], v7 offset:49120
ds_read_b64_tr_b16 v[76:77], v8 offset:47104
ds_read_b64_tr_b16 v[78:79], v8 offset:48832
ds_read_b64_tr_b16 a[204:205], v7 offset:60928
ds_read_b64_tr_b16 a[206:207], v7 offset:62656
ds_read_b64_tr_b16 a[208:209], v7 offset:61072
ds_read_b64_tr_b16 a[210:211], v7 offset:62800
ds_read_b64_tr_b16 a[212:213], v7 offset:61216
ds_read_b64_tr_b16 a[214:215], v7 offset:62944
ds_read_b64_tr_b16 v[80:81], v8 offset:60928
ds_read_b64_tr_b16 v[82:83], v8 offset:62656
)"
R"(s_waitcnt lgkmcnt(15)
v_mfma_f32_16x16x32_bf16 v[84:87], a[168:171], v[104:107], v[84:87]
v_mfma_f32_16x16x32_bf16 v[88:91], a[172:175], v[104:107], v[88:91]
v_mfma_f32_16x16x32_bf16 v[92:95], v[104:107], a[176:179], v[92:95]
v_mfma_f32_16x16x32_bf16 v[96:99], v[68:71], v[104:107], v[96:99]
v_mfma_f32_16x16x32_bf16 v[100:103], v[104:107], v[68:71], v[100:103]
v_mfma_f32_16x16x32_bf16 v[84:87], a[180:183], v[104:107], v[84:87]
v_mfma_f32_16x16x32_bf16 v[88:91], a[184:187], v[104:107], v[88:91]
v_mfma_f32_16x16x32_bf16 v[92:95], v[104:107], a[188:191], v[92:95]
v_mfma_f32_16x16x32_bf16 v[96:99], v[72:75], v[104:107], v[96:99]
v_mfma_f32_16x16x32_bf16 v[100:103], v[104:107], v[72:75], v[100:103]
s_waitcnt lgkmcnt(14)
v_mfma_f32_16x16x32_bf16 v[84:87], a[192:195], v[104:107], v[84:87]
s_waitcnt lgkmcnt(12)
v_mfma_f32_16x16x32_bf16 v[88:91], a[196:199], v[104:107], v[88:91]
s_waitcnt lgkmcnt(10)
v_mfma_f32_16x16x32_bf16 v[92:95], v[104:107], a[200:203], v[92:95]
s_waitcnt lgkmcnt(8)
v_mfma_f32_16x16x32_bf16 v[96:99], v[76:79], v[104:107], v[96:99]
v_mfma_f32_16x16x32_bf16 v[100:103], v[104:107], v[76:79], v[100:103]
s_waitcnt lgkmcnt(6)
v_mfma_f32_16x16x32_bf16 v[84:87], a[204:207], v[104:107], v[84:87]
s_waitcnt lgkmcnt(4)
v_mfma_f32_16x16x32_bf16 v[88:91], a[208:211], v[104:107], v[88:91]
s_waitcnt lgkmcnt(2)
v_mfma_f32_16x16x32_bf16 v[92:95], v[104:107], a[212:215], v[92:95]
s_waitcnt lgkmcnt(0)
v_mfma_f32_16x16x32_bf16 v[96:99], v[80:83], v[104:107], v[96:99]
v_mfma_f32_16x16x32_bf16 v[100:103], v[104:107], v[80:83], v[100:103]
s_waitcnt vmcnt(0) lgkmcnt(0)
s_barrier
s_and_b32 s75, s36, 3
s_cmp_eq_u32 s75, 1
s_cbranch_scc1 .Lqa_w2
s_cmp_eq_u32 s75, 2
s_cbranch_scc1 .Lqa_w3
s_cmp_eq_u32 s75, 3
s_cbranch_scc1 .Lqa_w4
.Lqa_w1:
s_nop 7
v_mov_b32 v232, v30
v_mov_b32 v233, v31
v_mov_b32 v234, v32
v_mov_b32 v235, v33
s_mov_b32 s70, 33
s_waitcnt vmcnt(0) lgkmcnt(0)
s_nop 7
s_branch .Lqa_in5
.p2align 6
.Lqa_top15:
s_waitcnt lgkmcnt(15)
v_mfma_f32_16x16x32_bf16 v[132:135], a[216:219], v[42:45], 0
s_waitcnt lgkmcnt(14)
ds_read_b128 v[168:171], v50 offset:6144
v_max_f32 v48, v216, v217
v_mfma_f32_16x16x32_bf16 v[132:135], a[220:223], v[100:103], v[132:135]
s_waitcnt lgkmcnt(14)
global_store_dwordx4 v14, v[228:231], s[38:39] sc1
v_mfma_f32_16x16x32_bf16 v[108:111], a[0:3], v[42:45], 0
s_waitcnt lgkmcnt(11)
ds_read_b128 v[172:175], v49 offset:8192
v_add_u32 v14, 18432, v14
v_mfma_f32_16x16x32_bf16 v[132:135], a[224:227], v[104:107], v[132:135]
s_waitcnt lgkmcnt(10)
ds_read_b128 v[176:179], v50 offset:8192
v_fma_f32 v236, v88, v8, v124
v_mfma_f32_16x16x32_bf16 v[132:135], a[228:231], v[0:3], v[132:135]
v_fma_f32 v237, v89, v8, v125
v_fma_f32 v238, v90, v8, v126
v_mfma_f32_16x16x32_bf16 v[112:115], a[4:7], v[42:45], 0
s_waitcnt lgkmcnt(7)
ds_read_b128 v[180:183], v49 offset:10240
v_fma_f32 v239, v91, v8, v127
v_mfma_f32_16x16x32_bf16 v[132:135], a[232:235], v[4:7], v[132:135]
s_waitcnt lgkmcnt(6)
ds_read_b128 v[184:187], v50 offset:10240
v_cvt_pk_bf16_f32 v148, v236, v237
v_mfma_f32_16x16x32_bf16 v[132:135], a[236:239], v[160:163], v[132:135]
v_cvt_pk_bf16_f32 v149, v238, v239
v_max_f32 v93, v218, v219
v_mfma_f32_16x16x32_bf16 v[116:119], v[42:45], a[8:11], 0
ds_read_b128 v[188:191], v49 offset:12288
v_mfma_f32_16x16x32_bf16 v[212:215], v[148:151], v[136:139], v[30:33]
s_waitcnt lgkmcnt(6)
v_fma_f32 v236, v92, v224, v128
v_fma_f32 v237, v92, v225, v129
v_mfma_f32_16x16x32_bf16 v[132:135], a[240:243], v[164:167], v[132:135]
s_waitcnt lgkmcnt(5)
ds_read_b128 v[192:195], v50 offset:12288
v_mul_f32 v21, 0x3eae1a93, v21
v_mfma_f32_16x16x32_bf16 v[132:135], a[244:247], v[168:171], v[132:135]
v_mul_f32 v46, v24, v21
v_mul_f32 v47, v51, v21
v_mfma_f32_16x16x32_bf16 v[108:111], a[12:15], v[100:103], v[108:111]
s_waitcnt lgkmcnt(5)
ds_read_b128 v[196:199], v49 offset:14336
v_fma_f32 v238, v92, v226, v130
v_mfma_f32_16x16x32_bf16 v[132:135], a[248:251], v[172:175], v[132:135]
s_xor_b32 s49, s49, 0x10000
ds_read_b128 v[200:203], v50 offset:14336
s_waitcnt lgkmcnt(6)
v_mfma_f32_16x16x32_bf16 v[132:135], a[252:255], v[176:179], v[132:135]
s_add_u32 m0, s49, 0
v_max3_f32 v48, v212, v213, v48
v_max3_f32 v93, v214, v215, v93
v_mfma_f32_16x16x32_bf16 v[112:115], a[16:19], v[100:103], v[112:115]
s_waitcnt lgkmcnt(5)
ds_read_b128 v[204:207], v49 offset:16384
global_load_lds_dwordx4 v9, s[42:43] offset:-2048 sc1
v_mfma_f32_16x16x32_bf16 v[132:135], v[52:55], v[180:183], v[132:135]
s_waitcnt lgkmcnt(5)
ds_read_b128 v[208:211], v50 offset:16384
v_mul_f32 v48, v48, v46
v_mfma_f32_16x16x32_bf16 v[132:135], v[56:59], v[184:187], v[132:135]
v_mul_f32 v93, v93, v47
v_max_f32 v94, v48, v93
v_mfma_f32_16x16x32_bf16 v[116:119], v[100:103], a[20:23], v[116:119]
s_waitcnt lgkmcnt(5)
v_mov_b32 v95, v94
v_fma_f32 v239, v92, v227, v131
v_mfma_f32_16x16x32_bf16 v[132:135], v[60:63], v[188:191], v[132:135]
s_waitcnt lgkmcnt(4)
v_permlane16_swap_b32 v94, v95
v_mfma_f32_16x16x32_bf16 v[132:135], v[64:67], v[192:195], v[132:135]
v_max_f32 v94, v94, v95
v_mov_b32 v95, v94
v_mfma_f32_16x16x32_bf16 v[108:111], a[24:27], v[104:107], v[108:111]
s_waitcnt lgkmcnt(3)
v_permlane32_swap_b32 v94, v95
v_mfma_f32_16x16x32_bf16 v[132:135], v[68:71], v[196:199], v[132:135]
s_waitcnt lgkmcnt(2)
v_max_f32 v94, v94, v95
v_sub_f32 v48, v48, v94
v_mfma_f32_16x16x32_bf16 v[132:135], v[72:75], v[200:203], v[132:135]
global_load_lds_dwordx4 v10, s[42:43] offset:-1024 sc1
v_sub_f32 v93, v93, v94
v_mfma_f32_16x16x32_bf16 v[112:115], a[28:31], v[104:107], v[112:115]
s_waitcnt lgkmcnt(1)
v_exp_f32 v240, v48
v_mfma_f32_16x16x32_bf16 v[132:135], v[76:79], v[204:207], v[132:135]
s_waitcnt lgkmcnt(0)
v_exp_f32 v241, v93
v_mfma_f32_16x16x32_bf16 v[132:135], v[80:83], v[208:211], v[132:135]
v_add_f32 v94, v240, v241
v_mov_b32 v95, v94
v_mfma_f32_16x16x32_bf16 v[116:119], v[104:107], a[32:35], v[116:119]
v_mul_f32 v240, v240, v24
v_cvt_pk_bf16_f32 v154, v236, v237
v_mfma_f32_16x16x32_bf16 v[108:111], a[36:39], v[0:3], v[108:111]
v_permlane16_swap_b32 v94, v95
v_mfma_f32_16x16x32_bf16 v[112:115], a[40:43], v[0:3], v[112:115]
v_add_f32 v94, v94, v95
v_mov_b32 v95, v94
v_mfma_f32_16x16x32_bf16 v[116:119], v[0:3], a[44:47], v[116:119]
v_mul_f32 v241, v241, v51
v_cvt_pk_bf16_f32 v155, v238, v239
v_mfma_f32_16x16x32_bf16 v[108:111], a[48:51], v[4:7], v[108:111]
global_load_lds_dwordx4 v11, s[42:43] offset:0 sc1
v_fma_f32 v236, v84, v8, v120
v_mfma_f32_16x16x32_bf16 v[112:115], a[52:55], v[4:7], v[112:115]
v_permlane32_swap_b32 v94, v95
v_mfma_f32_16x16x32_bf16 v[116:119], v[4:7], a[56:59], v[116:119]
v_add_f32 v94, v94, v95
v_fma_f32 v237, v85, v8, v121
v_mfma_f32_16x16x32_bf16 v[108:111], a[60:63], v[160:163], v[108:111]
v_rcp_f32 v94, v94
v_mfma_f32_16x16x32_bf16 v[112:115], a[64:67], v[160:163], v[112:115]
v_mul_f32 v240, v240, v94
v_mul_f32 v241, v241, v94
v_mfma_f32_16x16x32_bf16 v[116:119], v[160:163], a[68:71], v[116:119]
v_cvt_pk_bf16_f32 v240, v240, v240
v_cvt_pk_bf16_f32 v241, v241, v241
v_mfma_f32_16x16x32_bf16 v[108:111], a[72:75], v[164:167], v[108:111]
v_and_b32 v222, v240, v38
v_and_b32 v223, v241, v39
v_mfma_f32_16x16x32_bf16 v[112:115], a[76:79], v[164:167], v[112:115]
v_mfma_f32_16x16x32_bf16 v[116:119], v[164:167], a[80:83], v[116:119]
s_barrier
v_mfma_f32_16x16x32_bf16 v[108:111], a[84:87], v[168:171], v[108:111]
global_load_lds_dwordx4 v12, s[42:43] offset:1024 sc1
ds_read_b32 v242, v22 offset:0
v_mfma_f32_16x16x32_bf16 v[112:115], a[88:91], v[168:171], v[112:115]
v_and_b32 v220, v240, v40
v_and_b32 v221, v241, v41
v_mfma_f32_16x16x32_bf16 v[116:119], v[168:171], a[92:95], v[116:119]
s_nop 0
v_mfma_f32_16x16x32_bf16 v[216:219], v[152:155], v[220:223], 0
v_mfma_f32_16x16x32_bf16 v[212:215], v[156:159], v[220:223], 0
v_fma_f32 v238, v86, v8, v122
v_fma_f32 v239, v87, v8, v123
v_mfma_f32_16x16x32_bf16 v[108:111], a[96:99], v[172:175], v[108:111]
v_cvt_pk_bf16_f32 v140, v236, v237
v_cvt_pk_bf16_f32 v141, v238, v239
v_mfma_f32_16x16x32_bf16 v[112:115], a[100:103], v[172:175], v[112:115]
v_mfma_f32_16x16x32_bf16 v[116:119], v[172:175], a[104:107], v[116:119]
v_mfma_f32_16x16x32_bf16 v[108:111], a[108:111], v[176:179], v[108:111]
v_cvt_pk_bf16_f32 v254, v216, v217
v_cvt_pk_bf16_f32 v255, v218, v219
v_mfma_f32_16x16x32_bf16 v[112:115], a[112:115], v[176:179], v[112:115]
s_waitcnt lgkmcnt(0)
v_fma_f32 v228, v96, v242, v132
v_fma_f32 v229, v97, v242, v133
v_mfma_f32_16x16x32_bf16 v[116:119], v[176:179], a[116:119], v[116:119]
global_load_lds_dwordx4 v13, s[42:43] offset:2048 sc1
s_add_u32 s42, s42, s46
s_addc_u32 s43, s43, s47
v_mfma_f32_16x16x32_bf16 v[108:111], a[120:123], v[180:183], v[108:111]
v_fma_f32 v230, v98, v242, v134
v_fma_f32 v231, v99, v242, v135
v_mfma_f32_16x16x32_bf16 v[112:115], a[124:127], v[180:183], v[112:115]
v_cvt_pk_bf16_f32 v228, v228, v229
v_cvt_pk_bf16_f32 v229, v230, v231
v_mfma_f32_16x16x32_bf16 v[116:119], v[180:183], a[128:131], v[116:119]
ds_write_b64 v20, v[228:229] offset:40960
v_mfma_f32_16x16x32_bf16 v[108:111], a[132:135], v[184:187], v[108:111]
ds_write_b64 v16, v[254:255]
v_mfma_f32_16x16x32_bf16 v[112:115], a[136:139], v[184:187], v[112:115]
v_cvt_pk_bf16_f32 v240, v212, v213
v_mfma_f32_16x16x32_bf16 v[116:119], v[184:187], a[140:143], v[116:119]
ds_write_b32 v17, v240
v_mfma_f32_16x16x32_bf16 v[108:111], a[144:147], v[188:191], v[108:111]
v_mfma_f32_16x16x32_bf16 v[112:115], a[148:151], v[188:191], v[112:115]
v_mfma_f32_16x16x32_bf16 v[116:119], v[188:191], a[152:155], v[116:119]
v_mfma_f32_16x16x32_bf16 v[108:111], a[156:159], v[192:195], v[108:111]
v_mfma_f32_16x16x32_bf16 v[112:115], a[160:163], v[192:195], v[112:115]
v_mfma_f32_16x16x32_bf16 v[116:119], v[192:195], a[164:167], v[116:119]
v_mfma_f32_16x16x32_bf16 v[108:111], a[168:171], v[196:199], v[108:111]
v_mfma_f32_16x16x32_bf16 v[112:115], a[172:175], v[196:199], v[112:115]
s_waitcnt vmcnt(5) lgkmcnt(0)
s_barrier
v_mfma_f32_16x16x32_bf16 v[116:119], v[196:199], a[176:179], v[116:119]
ds_read_b128 v[42:45], v49 offset:18432
v_mfma_f32_16x16x32_bf16 v[108:111], a[180:183], v[200:203], v[108:111]
ds_read_b128 v[100:103], v50 offset:18432
ds_read_b128 v[228:231], v15 offset:0
v_mfma_f32_16x16x32_bf16 v[112:115], a[184:187], v[200:203], v[112:115]
ds_read_b32 v8, v22 offset:0
ds_read_b32 v146, v19
v_mfma_f32_16x16x32_bf16 v[216:219], v[148:151], v[140:143], v[34:37]
v_mfma_f32_16x16x32_bf16 v[116:119], v[200:203], a[188:191], v[116:119]
ds_read_b128 v[104:107], v49 offset:20480
ds_read_b32 v21, v22 offset:192
v_mfma_f32_16x16x32_bf16 v[108:111], a[192:195], v[204:207], v[108:111]
ds_read_b128 v[0:3], v50 offset:20480
ds_read_b32 v24, v25
v_mfma_f32_16x16x32_bf16 v[112:115], a[196:199], v[204:207], v[112:115]
ds_read_b32 v51, v26
ds_read_b128 v[224:227], v23 offset:0
v_mfma_f32_16x16x32_bf16 v[116:119], v[204:207], a[200:203], v[116:119]
ds_read_b128 v[4:7], v49 offset:22528
ds_read_b64 v[156:157], v20 offset:41472
v_mfma_f32_16x16x32_bf16 v[108:111], a[204:207], v[208:211], v[108:111]
ds_read_b128 v[160:163], v50 offset:22528
ds_read_b32 v138, v18
v_mfma_f32_16x16x32_bf16 v[112:115], a[208:211], v[208:211], v[112:115]
s_waitcnt lgkmcnt(12)
global_store_dwordx4 v14, v[228:231], s[38:39] sc1
v_mfma_f32_16x16x32_bf16 v[116:119], v[208:211], a[212:215], v[116:119]
ds_read_b128 v[164:167], v49 offset:24576
v_add_u32 v14, 18432, v14
v_mfma_f32_16x16x32_bf16 v[132:135], a[216:219], v[42:45], 0
ds_read_b128 v[168:171], v50 offset:24576
v_max_f32 v48, v216, v217
v_mfma_f32_16x16x32_bf16 v[132:135], a[220:223], v[100:103], v[132:135]
v_max_f32 v93, v218, v219
v_mfma_f32_16x16x32_bf16 v[120:123], a[0:3], v[42:45], 0
s_waitcnt lgkmcnt(13)
ds_read_b128 v[172:175], v49 offset:26624
v_fma_f32 v236, v88, v8, v112
s_waitcnt lgkmcnt(12)
v_mfma_f32_16x16x32_bf16 v[132:135], a[224:227], v[104:107], v[132:135]
s_waitcnt lgkmcnt(10)
ds_read_b128 v[176:179], v50 offset:26624
v_fma_f32 v237, v89, v8, v113
v_mfma_f32_16x16x32_bf16 v[132:135], a[228:231], v[0:3], v[132:135]
v_fma_f32 v238, v90, v8, v114
v_fma_f32 v239, v91, v8, v115
v_mfma_f32_16x16x32_bf16 v[124:127], a[4:7], v[42:45], 0
s_waitcnt lgkmcnt(7)
ds_read_b128 v[180:183], v49 offset:28672
v_cvt_pk_bf16_f32 v144, v236, v237
v_mfma_f32_16x16x32_bf16 v[132:135], a[232:235], v[4:7], v[132:135]
s_waitcnt lgkmcnt(6)
ds_read_b128 v[184:187], v50 offset:28672
v_cvt_pk_bf16_f32 v145, v238, v239
v_mfma_f32_16x16x32_bf16 v[132:135], a[236:239], v[160:163], v[132:135]
s_nop 0
v_mfma_f32_16x16x32_bf16 v[212:215], v[144:147], v[140:143], v[232:235]
v_mul_f32 v21, 0x3eae1a93, v21
v_mul_f32 v46, v24, v21
v_mfma_f32_16x16x32_bf16 v[128:131], v[42:45], a[8:11], 0
s_waitcnt lgkmcnt(5)
ds_read_b128 v[188:191], v49 offset:30720
v_mul_f32 v47, v51, v21
v_mfma_f32_16x16x32_bf16 v[132:135], a[240:243], v[164:167], v[132:135]
s_waitcnt lgkmcnt(5)
ds_read_b128 v[192:195], v50 offset:30720
v_fma_f32 v236, v92, v224, v116
v_mfma_f32_16x16x32_bf16 v[132:135], a[244:247], v[168:171], v[132:135]
v_fma_f32 v237, v92, v225, v117
v_fma_f32 v238, v92, v226, v118
v_mfma_f32_16x16x32_bf16 v[120:123], a[12:15], v[100:103], v[120:123]
s_waitcnt lgkmcnt(5)
ds_read_b128 v[196:199], v49 offset:32768
v_max3_f32 v48, v212, v213, v48
v_mfma_f32_16x16x32_bf16 v[132:135], a[248:251], v[172:175], v[132:135]
s_add_u32 m0, s49, 18432
ds_read_b128 v[200:203], v50 offset:32768
s_waitcnt lgkmcnt(6)
v_mfma_f32_16x16x32_bf16 v[132:135], a[252:255], v[176:179], v[132:135]
global_load_lds_dwordx4 v9, s[42:43] offset:-2048 sc1
v_max3_f32 v93, v214, v215, v93
v_mfma_f32_16x16x32_bf16 v[124:127], a[16:19], v[100:103], v[124:127]
s_waitcnt lgkmcnt(5)
ds_read_b128 v[204:207], v49 offset:34816
v_mul_f32 v48, v48, v46
v_mfma_f32_16x16x32_bf16 v[132:135], v[52:55], v[180:183], v[132:135]
s_waitcnt lgkmcnt(5)
ds_read_b128 v[208:211], v50 offset:34816
v_mul_f32 v93, v93, v47
v_mfma_f32_16x16x32_bf16 v[132:135], v[56:59], v[184:187], v[132:135]
v_max_f32 v94, v48, v93
v_mov_b32 v95, v94
v_mfma_f32_16x16x32_bf16 v[128:131], v[100:103], a[20:23], v[128:131]
s_waitcnt lgkmcnt(5)
v_permlane16_swap_b32 v94, v95
v_mfma_f32_16x16x32_bf16 v[132:135], v[60:63], v[188:191], v[132:135]
s_waitcnt lgkmcnt(4)
v_max_f32 v94, v94, v95
v_mov_b32 v95, v94
v_mfma_f32_16x16x32_bf16 v[132:135], v[64:67], v[192:195], v[132:135]
s_nop 0
v_permlane32_swap_b32 v94, v95
v_mfma_f32_16x16x32_bf16 v[120:123], a[24:27], v[104:107], v[120:123]
s_waitcnt lgkmcnt(3)
v_max_f32 v94, v94, v95
v_sub_f32 v48, v48, v94
v_mfma_f32_16x16x32_bf16 v[132:135], v[68:71], v[196:199], v[132:135]
s_waitcnt lgkmcnt(2)
global_load_lds_dwordx4 v10, s[42:43] offset:-1024 sc1
v_sub_f32 v93, v93, v94
v_mfma_f32_16x16x32_bf16 v[132:135], v[72:75], v[200:203], v[132:135]
v_exp_f32 v240, v48
v_mfma_f32_16x16x32_bf16 v[124:127], a[28:31], v[104:107], v[124:127]
s_waitcnt lgkmcnt(1)
v_exp_f32 v241, v93
v_mfma_f32_16x16x32_bf16 v[132:135], v[76:79], v[204:207], v[132:135]
s_waitcnt lgkmcnt(0)
v_add_f32 v94, v240, v241
v_mov_b32 v95, v94
v_mfma_f32_16x16x32_bf16 v[132:135], v[80:83], v[208:211], v[132:135]
v_mul_f32 v240, v240, v24
v_fma_f32 v239, v92, v227, v119
v_mfma_f32_16x16x32_bf16 v[128:131], v[104:107], a[32:35], v[128:131]
v_permlane16_swap_b32 v94, v95
v_mfma_f32_16x16x32_bf16 v[120:123], a[36:39], v[0:3], v[120:123]
v_add_f32 v94, v94, v95
v_mov_b32 v95, v94
v_mfma_f32_16x16x32_bf16 v[124:127], a[40:43], v[0:3], v[124:127]
v_mul_f32 v241, v241, v51
v_cvt_pk_bf16_f32 v152, v236, v237
v_mfma_f32_16x16x32_bf16 v[128:131], v[0:3], a[44:47], v[128:131]
global_load_lds_dwordx4 v11, s[42:43] offset:0 sc1
v_cvt_pk_bf16_f32 v153, v238, v239
v_mfma_f32_16x16x32_bf16 v[120:123], a[48:51], v[4:7], v[120:123]
v_permlane32_swap_b32 v94, v95
v_mfma_f32_16x16x32_bf16 v[124:127], a[52:55], v[4:7], v[124:127]
v_add_f32 v94, v94, v95
v_fma_f32 v236, v84, v8, v108
v_mfma_f32_16x16x32_bf16 v[128:131], v[4:7], a[56:59], v[128:131]
v_rcp_f32 v94, v94
v_mfma_f32_16x16x32_bf16 v[120:123], a[60:63], v[160:163], v[120:123]
v_mul_f32 v240, v240, v94
v_mul_f32 v241, v241, v94
v_mfma_f32_16x16x32_bf16 v[124:127], a[64:67], v[160:163], v[124:127]
v_cvt_pk_bf16_f32 v240, v240, v240
v_cvt_pk_bf16_f32 v241, v241, v241
v_mfma_f32_16x16x32_bf16 v[128:131], v[160:163], a[68:71], v[128:131]
v_and_b32 v220, v240, v38
v_and_b32 v221, v241, v39
v_mfma_f32_16x16x32_bf16 v[120:123], a[72:75], v[164:167], v[120:123]
v_and_b32 v222, v240, v40
v_and_b32 v223, v241, v41
v_mfma_f32_16x16x32_bf16 v[124:127], a[76:79], v[164:167], v[124:127]
v_mfma_f32_16x16x32_bf16 v[128:131], v[164:167], a[80:83], v[128:131]
s_barrier
v_mfma_f32_16x16x32_bf16 v[120:123], a[84:87], v[168:171], v[120:123]
global_load_lds_dwordx4 v12, s[42:43] offset:1024 sc1
ds_read_b32 v242, v22 offset:64
v_mfma_f32_16x16x32_bf16 v[124:127], a[88:91], v[168:171], v[124:127]
v_mfma_f32_16x16x32_bf16 v[216:219], v[152:155], v[220:223], 0
v_mfma_f32_16x16x32_bf16 v[212:215], v[156:159], v[220:223], 0
v_fma_f32 v237, v85, v8, v109
v_fma_f32 v238, v86, v8, v110
v_mfma_f32_16x16x32_bf16 v[128:131], v[168:171], a[92:95], v[128:131]
v_fma_f32 v239, v87, v8, v111
v_cvt_pk_bf16_f32 v136, v236, v237
v_mfma_f32_16x16x32_bf16 v[120:123], a[96:99], v[172:175], v[120:123]
v_cvt_pk_bf16_f32 v137, v238, v239
v_mfma_f32_16x16x32_bf16 v[124:127], a[100:103], v[172:175], v[124:127]
)"
R"(v_mfma_f32_16x16x32_bf16 v[128:131], v[172:175], a[104:107], v[128:131]
v_cvt_pk_bf16_f32 v254, v216, v217
v_cvt_pk_bf16_f32 v255, v218, v219
v_mfma_f32_16x16x32_bf16 v[120:123], a[108:111], v[176:179], v[120:123]
ds_write_b64 v16, v[254:255]
v_mfma_f32_16x16x32_bf16 v[124:127], a[112:115], v[176:179], v[124:127]
s_waitcnt lgkmcnt(1)
v_fma_f32 v228, v96, v242, v132
v_fma_f32 v229, v97, v242, v133
v_mfma_f32_16x16x32_bf16 v[128:131], v[176:179], a[116:119], v[128:131]
global_load_lds_dwordx4 v13, s[42:43] offset:2048 sc1
s_sub_u32 s42, s42, 18432
s_subb_u32 s43, s43, 0
v_mfma_f32_16x16x32_bf16 v[120:123], a[120:123], v[180:183], v[120:123]
v_fma_f32 v230, v98, v242, v134
v_fma_f32 v231, v99, v242, v135
v_mfma_f32_16x16x32_bf16 v[124:127], a[124:127], v[180:183], v[124:127]
v_cvt_pk_bf16_f32 v228, v228, v229
v_cvt_pk_bf16_f32 v229, v230, v231
v_mfma_f32_16x16x32_bf16 v[128:131], v[180:183], a[128:131], v[128:131]
ds_write_b64 v20, v[228:229] offset:40960
v_mfma_f32_16x16x32_bf16 v[120:123], a[132:135], v[184:187], v[120:123]
v_cvt_pk_bf16_f32 v240, v212, v213
v_mfma_f32_16x16x32_bf16 v[124:127], a[136:139], v[184:187], v[124:127]
ds_write_b32 v17, v240
v_mfma_f32_16x16x32_bf16 v[128:131], v[184:187], a[140:143], v[128:131]
v_mfma_f32_16x16x32_bf16 v[120:123], a[144:147], v[188:191], v[120:123]
v_mfma_f32_16x16x32_bf16 v[124:127], a[148:151], v[188:191], v[124:127]
v_mfma_f32_16x16x32_bf16 v[128:131], v[188:191], a[152:155], v[128:131]
v_mfma_f32_16x16x32_bf16 v[120:123], a[156:159], v[192:195], v[120:123]
v_mfma_f32_16x16x32_bf16 v[124:127], a[160:163], v[192:195], v[124:127]
v_xor_b32 v49, 0x10000, v49
v_xor_b32 v50, 0x10000, v50
v_mfma_f32_16x16x32_bf16 v[128:131], v[192:195], a[164:167], v[128:131]
v_mfma_f32_16x16x32_bf16 v[120:123], a[168:171], v[196:199], v[120:123]
v_mfma_f32_16x16x32_bf16 v[124:127], a[172:175], v[196:199], v[124:127]
s_waitcnt vmcnt(5) lgkmcnt(0)
s_barrier
.Lqa_in5:
v_mfma_f32_16x16x32_bf16 v[128:131], v[196:199], a[176:179], v[128:131]
s_cmp_eq_u32 s70, s73
s_cbranch_scc1 .Lqa_hk6
.Lqa_hr7:
ds_read_b128 v[42:45], v49 offset:0
v_mfma_f32_16x16x32_bf16 v[120:123], a[180:183], v[200:203], v[120:123]
ds_read_b128 v[100:103], v50 offset:0
ds_read_b128 v[228:231], v15 offset:0
v_mfma_f32_16x16x32_bf16 v[124:127], a[184:187], v[200:203], v[124:127]
ds_read_b32 v8, v22 offset:64
ds_read_b32 v150, v19
v_mfma_f32_16x16x32_bf16 v[216:219], v[144:147], v[136:139], v[34:37]
v_mfma_f32_16x16x32_bf16 v[128:131], v[200:203], a[188:191], v[128:131]
ds_read_b128 v[104:107], v49 offset:2048
ds_read_b32 v21, v22 offset:128
v_mfma_f32_16x16x32_bf16 v[120:123], a[192:195], v[204:207], v[120:123]
ds_read_b128 v[0:3], v50 offset:2048
ds_read_b32 v24, v27
v_mfma_f32_16x16x32_bf16 v[124:127], a[196:199], v[204:207], v[124:127]
ds_read_b32 v51, v28
ds_read_b128 v[224:227], v23 offset:64
v_mfma_f32_16x16x32_bf16 v[128:131], v[204:207], a[200:203], v[128:131]
ds_read_b128 v[4:7], v49 offset:4096
ds_read_b64 v[158:159], v20 offset:41472
v_mfma_f32_16x16x32_bf16 v[120:123], a[204:207], v[208:211], v[120:123]
ds_read_b128 v[160:163], v50 offset:4096
ds_read_b32 v142, v18
v_mfma_f32_16x16x32_bf16 v[124:127], a[208:211], v[208:211], v[124:127]
v_mfma_f32_16x16x32_bf16 v[128:131], v[208:211], a[212:215], v[128:131]
s_sub_u32 s70, s70, 1
ds_read_b128 v[164:167], v49 offset:6144
s_cbranch_scc0 .Lqa_top15
s_waitcnt lgkmcnt(0)
v_max_f32 v48, v216, v217
global_store_dwordx4 v14, v[228:231], s[38:39] sc1
v_add_u32 v14, 18432, v14
v_fma_f32 v236, v88, v8, v124
v_fma_f32 v237, v89, v8, v125
v_fma_f32 v238, v90, v8, v126
v_fma_f32 v239, v91, v8, v127
v_cvt_pk_bf16_f32 v148, v236, v237
v_cvt_pk_bf16_f32 v149, v238, v239
v_max_f32 v93, v218, v219
s_nop 0
v_mfma_f32_16x16x32_bf16 v[212:215], v[148:151], v[136:139], v[30:33]
v_fma_f32 v236, v92, v224, v128
v_fma_f32 v237, v92, v225, v129
v_mul_f32 v21, 0x3eae1a93, v21
v_mul_f32 v46, v24, v21
v_mul_f32 v47, v51, v21
v_fma_f32 v238, v92, v226, v130
s_nop 1
v_max3_f32 v48, v212, v213, v48
v_max3_f32 v93, v214, v215, v93
v_mul_f32 v48, v48, v46
v_mul_f32 v93, v93, v47
v_max_f32 v94, v48, v93
v_mov_b32 v95, v94
v_fma_f32 v239, v92, v227, v131
s_nop 0
v_permlane16_swap_b32 v94, v95
v_max_f32 v94, v94, v95
v_mov_b32 v95, v94
s_nop 1
v_permlane32_swap_b32 v94, v95
v_max_f32 v94, v94, v95
v_sub_f32 v48, v48, v94
v_sub_f32 v93, v93, v94
v_exp_f32 v240, v48
v_exp_f32 v241, v93
s_nop 0
v_add_f32 v94, v240, v241
v_mov_b32 v95, v94
v_mul_f32 v240, v240, v24
v_cvt_pk_bf16_f32 v154, v236, v237
v_permlane16_swap_b32 v94, v95
v_add_f32 v94, v94, v95
v_mov_b32 v95, v94
v_mul_f32 v241, v241, v51
v_cvt_pk_bf16_f32 v155, v238, v239
v_fma_f32 v236, v84, v8, v120
v_permlane32_swap_b32 v94, v95
v_add_f32 v94, v94, v95
v_fma_f32 v237, v85, v8, v121
v_rcp_f32 v94, v94
s_nop 0
v_mul_f32 v240, v240, v94
v_mul_f32 v241, v241, v94
v_cvt_pk_bf16_f32 v240, v240, v240
v_cvt_pk_bf16_f32 v241, v241, v241
v_and_b32 v222, v240, v38
v_and_b32 v223, v241, v39
s_barrier
v_and_b32 v220, v240, v40
v_and_b32 v221, v241, v41
s_nop 1
v_mfma_f32_16x16x32_bf16 v[216:219], v[152:155], v[220:223], 0
v_mfma_f32_16x16x32_bf16 v[212:215], v[156:159], v[220:223], 0
v_fma_f32 v238, v86, v8, v122
v_fma_f32 v239, v87, v8, v123
v_cvt_pk_bf16_f32 v140, v236, v237
v_cvt_pk_bf16_f32 v141, v238, v239
s_nop 2
v_cvt_pk_bf16_f32 v254, v216, v217
v_cvt_pk_bf16_f32 v255, v218, v219
ds_write_b64 v16, v[254:255]
v_cvt_pk_bf16_f32 v240, v212, v213
ds_write_b32 v17, v240
s_waitcnt vmcnt(0)
s_waitcnt lgkmcnt(0)
s_barrier
ds_read_b128 v[228:231], v15 offset:0
s_waitcnt lgkmcnt(0)
global_store_dwordx4 v14, v[228:231], s[38:39] sc1
s_waitcnt vmcnt(0)
s_barrier
s_endpgm
.Lqa_w2:
s_nop 7
v_mov_b32 v232, v30
v_mov_b32 v233, v31
v_mov_b32 v234, v32
v_mov_b32 v235, v33
s_mov_b32 s70, 33
s_waitcnt vmcnt(0) lgkmcnt(0)
s_nop 7
s_branch .Lqa_in16
.p2align 6
.Lqa_top26:
s_waitcnt lgkmcnt(15)
v_mfma_f32_16x16x32_bf16 v[132:135], v[42:45], a[216:219], 0
s_waitcnt lgkmcnt(14)
ds_read_b128 v[168:171], v50 offset:6144
v_max_f32 v106, v216, v217
v_mfma_f32_16x16x32_bf16 v[132:135], v[94:97], a[220:223], v[132:135]
s_waitcnt lgkmcnt(14)
global_store_dwordx4 v14, v[228:231], s[38:39] sc1
v_mfma_f32_16x16x32_bf16 v[108:111], a[0:3], v[42:45], 0
s_waitcnt lgkmcnt(11)
ds_read_b128 v[172:175], v49 offset:8192
v_add_u32 v14, 18432, v14
v_mfma_f32_16x16x32_bf16 v[132:135], v[102:105], a[224:227], v[132:135]
s_waitcnt lgkmcnt(10)
ds_read_b128 v[176:179], v50 offset:8192
v_fma_f32 v236, v88, v8, v124
v_mfma_f32_16x16x32_bf16 v[132:135], v[0:3], a[228:231], v[132:135]
v_fma_f32 v237, v89, v8, v125
v_fma_f32 v238, v90, v8, v126
v_mfma_f32_16x16x32_bf16 v[112:115], a[4:7], v[42:45], 0
s_waitcnt lgkmcnt(7)
ds_read_b128 v[180:183], v49 offset:10240
v_fma_f32 v239, v91, v8, v127
v_mfma_f32_16x16x32_bf16 v[132:135], v[4:7], a[232:235], v[132:135]
s_waitcnt lgkmcnt(6)
ds_read_b128 v[184:187], v50 offset:10240
v_cvt_pk_bf16_f32 v148, v236, v237
v_mfma_f32_16x16x32_bf16 v[132:135], v[160:163], a[236:239], v[132:135]
v_cvt_pk_bf16_f32 v149, v238, v239
v_max_f32 v107, v218, v219
v_mfma_f32_16x16x32_bf16 v[116:119], v[42:45], a[8:11], 0
ds_read_b128 v[188:191], v49 offset:12288
v_mfma_f32_16x16x32_bf16 v[212:215], v[148:151], v[136:139], v[30:33]
s_waitcnt lgkmcnt(6)
v_fma_f32 v236, v92, v224, v128
v_fma_f32 v237, v92, v225, v129
v_mfma_f32_16x16x32_bf16 v[132:135], v[164:167], a[240:243], v[132:135]
s_waitcnt lgkmcnt(5)
ds_read_b128 v[192:195], v50 offset:12288
v_mul_f32 v21, 0x3eae1a93, v21
v_mfma_f32_16x16x32_bf16 v[132:135], v[168:171], a[244:247], v[132:135]
v_mul_f32 v93, v24, v21
v_mul_f32 v101, v51, v21
v_mfma_f32_16x16x32_bf16 v[108:111], a[12:15], v[94:97], v[108:111]
s_waitcnt lgkmcnt(5)
ds_read_b128 v[196:199], v49 offset:14336
v_fma_f32 v238, v92, v226, v130
v_mfma_f32_16x16x32_bf16 v[132:135], v[172:175], a[248:251], v[132:135]
s_xor_b32 s49, s49, 0x10000
ds_read_b128 v[200:203], v50 offset:14336
s_waitcnt lgkmcnt(6)
v_mfma_f32_16x16x32_bf16 v[132:135], v[176:179], a[252:255], v[132:135]
s_add_u32 m0, s49, 0
v_max3_f32 v106, v212, v213, v106
v_max3_f32 v107, v214, v215, v107
v_mfma_f32_16x16x32_bf16 v[112:115], a[16:19], v[94:97], v[112:115]
s_waitcnt lgkmcnt(5)
ds_read_b128 v[204:207], v49 offset:16384
v_mul_f32 v106, v106, v93
v_mfma_f32_16x16x32_bf16 v[132:135], v[180:183], v[52:55], v[132:135]
s_waitcnt lgkmcnt(5)
ds_read_b128 v[208:211], v50 offset:16384
v_mul_f32 v107, v107, v101
v_mfma_f32_16x16x32_bf16 v[132:135], v[184:187], v[56:59], v[132:135]
global_load_lds_dwordx4 v9, s[42:43] offset:-2048 sc1
v_max_f32 v254, v106, v107
v_mfma_f32_16x16x32_bf16 v[116:119], v[94:97], a[20:23], v[116:119]
s_waitcnt lgkmcnt(5)
v_mov_b32 v255, v254
v_fma_f32 v239, v92, v227, v131
v_mfma_f32_16x16x32_bf16 v[132:135], v[188:191], v[60:63], v[132:135]
s_waitcnt lgkmcnt(4)
v_permlane16_swap_b32 v254, v255
v_mfma_f32_16x16x32_bf16 v[132:135], v[192:195], v[64:67], v[132:135]
v_max_f32 v254, v254, v255
v_mov_b32 v255, v254
v_mfma_f32_16x16x32_bf16 v[108:111], a[24:27], v[102:105], v[108:111]
s_waitcnt lgkmcnt(3)
v_permlane32_swap_b32 v254, v255
v_mfma_f32_16x16x32_bf16 v[132:135], v[196:199], v[68:71], v[132:135]
s_waitcnt lgkmcnt(2)
v_max_f32 v254, v254, v255
v_sub_f32 v106, v106, v254
v_mfma_f32_16x16x32_bf16 v[132:135], v[200:203], v[72:75], v[132:135]
v_sub_f32 v107, v107, v254
v_cvt_pk_bf16_f32 v154, v236, v237
v_mfma_f32_16x16x32_bf16 v[112:115], a[28:31], v[102:105], v[112:115]
s_waitcnt lgkmcnt(1)
v_exp_f32 v46, v106
v_mfma_f32_16x16x32_bf16 v[132:135], v[204:207], v[76:79], v[132:135]
s_waitcnt lgkmcnt(0)
global_load_lds_dwordx4 v10, s[42:43] offset:-1024 sc1
v_cvt_pk_bf16_f32 v155, v238, v239
v_mfma_f32_16x16x32_bf16 v[132:135], v[208:211], v[80:83], v[132:135]
v_exp_f32 v47, v107
v_mfma_f32_16x16x32_bf16 v[116:119], v[102:105], a[32:35], v[116:119]
v_add_f32 v254, v46, v47
v_mov_b32 v255, v254
v_mfma_f32_16x16x32_bf16 v[108:111], a[36:39], v[0:3], v[108:111]
v_mul_f32 v46, v46, v24
v_fma_f32 v236, v84, v8, v120
v_mfma_f32_16x16x32_bf16 v[112:115], a[40:43], v[0:3], v[112:115]
v_permlane16_swap_b32 v254, v255
v_mfma_f32_16x16x32_bf16 v[116:119], v[0:3], a[44:47], v[116:119]
v_add_f32 v254, v254, v255
v_mov_b32 v255, v254
v_mfma_f32_16x16x32_bf16 v[108:111], a[48:51], v[4:7], v[108:111]
v_mul_f32 v47, v47, v51
v_fma_f32 v237, v85, v8, v121
v_mfma_f32_16x16x32_bf16 v[112:115], a[52:55], v[4:7], v[112:115]
v_permlane32_swap_b32 v254, v255
v_mfma_f32_16x16x32_bf16 v[116:119], v[4:7], a[56:59], v[116:119]
global_load_lds_dwordx4 v11, s[42:43] offset:0 sc1
v_add_f32 v254, v254, v255
v_mfma_f32_16x16x32_bf16 v[108:111], a[60:63], v[160:163], v[108:111]
v_rcp_f32 v254, v254
v_mfma_f32_16x16x32_bf16 v[112:115], a[64:67], v[160:163], v[112:115]
v_mul_f32 v46, v46, v254
v_mul_f32 v47, v47, v254
v_mfma_f32_16x16x32_bf16 v[116:119], v[160:163], a[68:71], v[116:119]
v_cvt_pk_bf16_f32 v46, v46, v46
v_cvt_pk_bf16_f32 v47, v47, v47
v_mfma_f32_16x16x32_bf16 v[108:111], a[72:75], v[164:167], v[108:111]
v_and_b32 v222, v46, v38
v_and_b32 v223, v47, v39
v_mfma_f32_16x16x32_bf16 v[112:115], a[76:79], v[164:167], v[112:115]
v_mfma_f32_16x16x32_bf16 v[116:119], v[164:167], a[80:83], v[116:119]
s_barrier
v_mfma_f32_16x16x32_bf16 v[108:111], a[84:87], v[168:171], v[108:111]
ds_read_b128 v[240:243], v23 offset:0
v_and_b32 v220, v46, v40
v_mfma_f32_16x16x32_bf16 v[112:115], a[88:91], v[168:171], v[112:115]
global_load_lds_dwordx4 v12, s[42:43] offset:1024 sc1
v_and_b32 v221, v47, v41
v_mfma_f32_16x16x32_bf16 v[116:119], v[168:171], a[92:95], v[116:119]
s_nop 0
v_mfma_f32_16x16x32_bf16 v[216:219], v[152:155], v[220:223], 0
v_mfma_f32_16x16x32_bf16 v[212:215], v[156:159], v[220:223], 0
v_fma_f32 v238, v86, v8, v122
v_fma_f32 v239, v87, v8, v123
v_mfma_f32_16x16x32_bf16 v[108:111], a[96:99], v[172:175], v[108:111]
v_cvt_pk_bf16_f32 v140, v236, v237
v_cvt_pk_bf16_f32 v141, v238, v239
v_mfma_f32_16x16x32_bf16 v[112:115], a[100:103], v[172:175], v[112:115]
v_mfma_f32_16x16x32_bf16 v[116:119], v[172:175], a[104:107], v[116:119]
v_mfma_f32_16x16x32_bf16 v[108:111], a[108:111], v[176:179], v[108:111]
v_cvt_pk_bf16_f32 v98, v216, v217
v_cvt_pk_bf16_f32 v99, v218, v219
v_mfma_f32_16x16x32_bf16 v[112:115], a[112:115], v[176:179], v[112:115]
s_waitcnt lgkmcnt(0)
v_fma_f32 v228, v100, v240, v132
v_fma_f32 v229, v100, v241, v133
v_mfma_f32_16x16x32_bf16 v[116:119], v[176:179], a[116:119], v[116:119]
v_fma_f32 v230, v100, v242, v134
v_fma_f32 v231, v100, v243, v135
v_mfma_f32_16x16x32_bf16 v[108:111], a[120:123], v[180:183], v[108:111]
global_load_lds_dwordx4 v13, s[42:43] offset:2048 sc1
s_add_u32 s42, s42, s46
s_addc_u32 s43, s43, s47
v_mfma_f32_16x16x32_bf16 v[112:115], a[124:127], v[180:183], v[112:115]
v_cvt_pk_bf16_f32 v228, v228, v229
v_cvt_pk_bf16_f32 v229, v230, v231
v_mfma_f32_16x16x32_bf16 v[116:119], v[180:183], a[128:131], v[116:119]
ds_write_b64 v20, v[228:229] offset:41472
v_mfma_f32_16x16x32_bf16 v[108:111], a[132:135], v[184:187], v[108:111]
ds_write_b64 v16, v[98:99]
v_mfma_f32_16x16x32_bf16 v[112:115], a[136:139], v[184:187], v[112:115]
v_cvt_pk_bf16_f32 v46, v214, v215
v_mfma_f32_16x16x32_bf16 v[116:119], v[184:187], a[140:143], v[116:119]
ds_write_b32 v17, v46
v_mfma_f32_16x16x32_bf16 v[108:111], a[144:147], v[188:191], v[108:111]
v_mfma_f32_16x16x32_bf16 v[112:115], a[148:151], v[188:191], v[112:115]
v_mfma_f32_16x16x32_bf16 v[116:119], v[188:191], a[152:155], v[116:119]
v_mfma_f32_16x16x32_bf16 v[108:111], a[156:159], v[192:195], v[108:111]
v_mfma_f32_16x16x32_bf16 v[112:115], a[160:163], v[192:195], v[112:115]
v_mfma_f32_16x16x32_bf16 v[116:119], v[192:195], a[164:167], v[116:119]
v_mfma_f32_16x16x32_bf16 v[108:111], a[168:171], v[196:199], v[108:111]
v_mfma_f32_16x16x32_bf16 v[112:115], a[172:175], v[196:199], v[112:115]
s_waitcnt vmcnt(5) lgkmcnt(0)
s_barrier
v_mfma_f32_16x16x32_bf16 v[116:119], v[196:199], a[176:179], v[116:119]
ds_read_b128 v[42:45], v49 offset:18432
v_mfma_f32_16x16x32_bf16 v[108:111], a[180:183], v[200:203], v[108:111]
ds_read_b128 v[94:97], v50 offset:18432
ds_read_b128 v[228:231], v15 offset:0
v_mfma_f32_16x16x32_bf16 v[112:115], a[184:187], v[200:203], v[112:115]
ds_read_b32 v8, v22 offset:0
ds_read_b32 v146, v19
v_mfma_f32_16x16x32_bf16 v[216:219], v[148:151], v[140:143], v[34:37]
v_mfma_f32_16x16x32_bf16 v[116:119], v[200:203], a[188:191], v[116:119]
ds_read_b128 v[102:105], v49 offset:20480
ds_read_b32 v21, v22 offset:192
v_mfma_f32_16x16x32_bf16 v[108:111], a[192:195], v[204:207], v[108:111]
ds_read_b128 v[0:3], v50 offset:20480
ds_read_b32 v24, v25
v_mfma_f32_16x16x32_bf16 v[112:115], a[196:199], v[204:207], v[112:115]
ds_read_b32 v51, v26
ds_read_b128 v[224:227], v23 offset:0
v_mfma_f32_16x16x32_bf16 v[116:119], v[204:207], a[200:203], v[116:119]
ds_read_b128 v[4:7], v49 offset:22528
ds_read_b64 v[156:157], v20 offset:41472
v_mfma_f32_16x16x32_bf16 v[108:111], a[204:207], v[208:211], v[108:111]
ds_read_b128 v[160:163], v50 offset:22528
ds_read_b32 v138, v18
v_mfma_f32_16x16x32_bf16 v[112:115], a[208:211], v[208:211], v[112:115]
v_max_f32 v106, v216, v217
v_max_f32 v107, v218, v219
v_mfma_f32_16x16x32_bf16 v[116:119], v[208:211], a[212:215], v[116:119]
s_waitcnt lgkmcnt(14)
ds_read_b128 v[164:167], v49 offset:24576
v_mfma_f32_16x16x32_bf16 v[132:135], v[42:45], a[216:219], 0
s_waitcnt lgkmcnt(9)
ds_read_b128 v[168:171], v50 offset:24576
v_mul_f32 v21, 0x3eae1a93, v21
v_mfma_f32_16x16x32_bf16 v[132:135], v[94:97], a[220:223], v[132:135]
global_store_dwordx4 v14, v[228:231], s[38:39] sc1
v_mfma_f32_16x16x32_bf16 v[120:123], a[0:3], v[42:45], 0
ds_read_b128 v[172:175], v49 offset:26624
v_add_u32 v14, 18432, v14
v_mfma_f32_16x16x32_bf16 v[132:135], v[102:105], a[224:227], v[132:135]
s_waitcnt lgkmcnt(10)
ds_read_b128 v[176:179], v50 offset:26624
v_fma_f32 v236, v88, v8, v112
v_mfma_f32_16x16x32_bf16 v[132:135], v[0:3], a[228:231], v[132:135]
v_fma_f32 v237, v89, v8, v113
v_fma_f32 v238, v90, v8, v114
v_mfma_f32_16x16x32_bf16 v[124:127], a[4:7], v[42:45], 0
s_waitcnt lgkmcnt(7)
ds_read_b128 v[180:183], v49 offset:28672
v_fma_f32 v239, v91, v8, v115
v_mfma_f32_16x16x32_bf16 v[132:135], v[4:7], a[232:235], v[132:135]
s_waitcnt lgkmcnt(6)
)"
R"(ds_read_b128 v[184:187], v50 offset:28672
v_cvt_pk_bf16_f32 v144, v236, v237
v_mfma_f32_16x16x32_bf16 v[132:135], v[160:163], a[236:239], v[132:135]
v_cvt_pk_bf16_f32 v145, v238, v239
v_mul_f32 v93, v24, v21
v_mfma_f32_16x16x32_bf16 v[128:131], v[42:45], a[8:11], 0
ds_read_b128 v[188:191], v49 offset:30720
v_mfma_f32_16x16x32_bf16 v[212:215], v[144:147], v[140:143], v[232:235]
s_waitcnt lgkmcnt(6)
v_mul_f32 v101, v51, v21
v_fma_f32 v236, v92, v224, v116
v_mfma_f32_16x16x32_bf16 v[132:135], v[164:167], a[240:243], v[132:135]
s_waitcnt lgkmcnt(5)
ds_read_b128 v[192:195], v50 offset:30720
v_fma_f32 v237, v92, v225, v117
v_mfma_f32_16x16x32_bf16 v[132:135], v[168:171], a[244:247], v[132:135]
v_fma_f32 v238, v92, v226, v118
v_fma_f32 v239, v92, v227, v119
v_mfma_f32_16x16x32_bf16 v[120:123], a[12:15], v[94:97], v[120:123]
s_waitcnt lgkmcnt(5)
ds_read_b128 v[196:199], v49 offset:32768
v_cvt_pk_bf16_f32 v152, v236, v237
v_mfma_f32_16x16x32_bf16 v[132:135], v[172:175], a[248:251], v[132:135]
s_add_u32 m0, s49, 18432
ds_read_b128 v[200:203], v50 offset:32768
s_waitcnt lgkmcnt(6)
v_mfma_f32_16x16x32_bf16 v[132:135], v[176:179], a[252:255], v[132:135]
v_max3_f32 v106, v212, v213, v106
v_max3_f32 v107, v214, v215, v107
v_mfma_f32_16x16x32_bf16 v[124:127], a[16:19], v[94:97], v[124:127]
s_waitcnt lgkmcnt(5)
ds_read_b128 v[204:207], v49 offset:34816
v_mul_f32 v106, v106, v93
v_mfma_f32_16x16x32_bf16 v[132:135], v[180:183], v[52:55], v[132:135]
s_waitcnt lgkmcnt(5)
ds_read_b128 v[208:211], v50 offset:34816
global_load_lds_dwordx4 v9, s[42:43] offset:-2048 sc1
v_mfma_f32_16x16x32_bf16 v[132:135], v[184:187], v[56:59], v[132:135]
v_mul_f32 v107, v107, v101
v_max_f32 v254, v106, v107
v_mfma_f32_16x16x32_bf16 v[128:131], v[94:97], a[20:23], v[128:131]
s_waitcnt lgkmcnt(5)
v_mov_b32 v255, v254
v_cvt_pk_bf16_f32 v153, v238, v239
v_mfma_f32_16x16x32_bf16 v[132:135], v[188:191], v[60:63], v[132:135]
s_waitcnt lgkmcnt(4)
v_permlane16_swap_b32 v254, v255
v_mfma_f32_16x16x32_bf16 v[132:135], v[192:195], v[64:67], v[132:135]
v_max_f32 v254, v254, v255
v_mov_b32 v255, v254
v_mfma_f32_16x16x32_bf16 v[120:123], a[24:27], v[102:105], v[120:123]
s_waitcnt lgkmcnt(3)
v_permlane32_swap_b32 v254, v255
v_mfma_f32_16x16x32_bf16 v[132:135], v[196:199], v[68:71], v[132:135]
s_waitcnt lgkmcnt(2)
v_max_f32 v254, v254, v255
v_sub_f32 v106, v106, v254
v_mfma_f32_16x16x32_bf16 v[132:135], v[200:203], v[72:75], v[132:135]
v_sub_f32 v107, v107, v254
v_fma_f32 v236, v84, v8, v108
v_mfma_f32_16x16x32_bf16 v[124:127], a[28:31], v[102:105], v[124:127]
s_waitcnt lgkmcnt(1)
global_load_lds_dwordx4 v10, s[42:43] offset:-1024 sc1
v_fma_f32 v237, v85, v8, v109
v_mfma_f32_16x16x32_bf16 v[132:135], v[204:207], v[76:79], v[132:135]
s_waitcnt lgkmcnt(0)
v_exp_f32 v46, v106
v_mfma_f32_16x16x32_bf16 v[132:135], v[208:211], v[80:83], v[132:135]
v_exp_f32 v47, v107
v_mfma_f32_16x16x32_bf16 v[128:131], v[102:105], a[32:35], v[128:131]
v_add_f32 v254, v46, v47
v_mov_b32 v255, v254
v_mfma_f32_16x16x32_bf16 v[120:123], a[36:39], v[0:3], v[120:123]
v_mul_f32 v46, v46, v24
v_fma_f32 v238, v86, v8, v110
v_mfma_f32_16x16x32_bf16 v[124:127], a[40:43], v[0:3], v[124:127]
v_permlane16_swap_b32 v254, v255
v_mfma_f32_16x16x32_bf16 v[128:131], v[0:3], a[44:47], v[128:131]
v_add_f32 v254, v254, v255
v_mov_b32 v255, v254
v_mfma_f32_16x16x32_bf16 v[120:123], a[48:51], v[4:7], v[120:123]
v_mul_f32 v47, v47, v51
v_fma_f32 v239, v87, v8, v111
v_mfma_f32_16x16x32_bf16 v[124:127], a[52:55], v[4:7], v[124:127]
global_load_lds_dwordx4 v11, s[42:43] offset:0 sc1
v_cvt_pk_bf16_f32 v136, v236, v237
v_mfma_f32_16x16x32_bf16 v[128:131], v[4:7], a[56:59], v[128:131]
v_permlane32_swap_b32 v254, v255
v_mfma_f32_16x16x32_bf16 v[120:123], a[60:63], v[160:163], v[120:123]
v_add_f32 v254, v254, v255
v_cvt_pk_bf16_f32 v137, v238, v239
v_mfma_f32_16x16x32_bf16 v[124:127], a[64:67], v[160:163], v[124:127]
v_rcp_f32 v254, v254
v_mfma_f32_16x16x32_bf16 v[128:131], v[160:163], a[68:71], v[128:131]
v_mul_f32 v46, v46, v254
v_mul_f32 v47, v47, v254
v_mfma_f32_16x16x32_bf16 v[120:123], a[72:75], v[164:167], v[120:123]
v_cvt_pk_bf16_f32 v46, v46, v46
v_cvt_pk_bf16_f32 v47, v47, v47
v_mfma_f32_16x16x32_bf16 v[124:127], a[76:79], v[164:167], v[124:127]
v_mfma_f32_16x16x32_bf16 v[128:131], v[164:167], a[80:83], v[128:131]
s_barrier
v_mfma_f32_16x16x32_bf16 v[120:123], a[84:87], v[168:171], v[120:123]
global_load_lds_dwordx4 v12, s[42:43] offset:1024 sc1
ds_read_b128 v[240:243], v23 offset:64
v_mfma_f32_16x16x32_bf16 v[124:127], a[88:91], v[168:171], v[124:127]
v_and_b32 v220, v46, v38
v_and_b32 v221, v47, v39
v_mfma_f32_16x16x32_bf16 v[128:131], v[168:171], a[92:95], v[128:131]
v_and_b32 v222, v46, v40
v_and_b32 v223, v47, v41
v_mfma_f32_16x16x32_bf16 v[120:123], a[96:99], v[172:175], v[120:123]
s_nop 0
v_mfma_f32_16x16x32_bf16 v[216:219], v[152:155], v[220:223], 0
v_mfma_f32_16x16x32_bf16 v[212:215], v[156:159], v[220:223], 0
v_mfma_f32_16x16x32_bf16 v[124:127], a[100:103], v[172:175], v[124:127]
v_mfma_f32_16x16x32_bf16 v[128:131], v[172:175], a[104:107], v[128:131]
v_mfma_f32_16x16x32_bf16 v[120:123], a[108:111], v[176:179], v[120:123]
v_mfma_f32_16x16x32_bf16 v[124:127], a[112:115], v[176:179], v[124:127]
s_waitcnt lgkmcnt(0)
v_fma_f32 v228, v100, v240, v132
v_fma_f32 v229, v100, v241, v133
v_mfma_f32_16x16x32_bf16 v[128:131], v[176:179], a[116:119], v[128:131]
global_load_lds_dwordx4 v13, s[42:43] offset:2048 sc1
s_sub_u32 s42, s42, 18432
s_subb_u32 s43, s43, 0
v_mfma_f32_16x16x32_bf16 v[120:123], a[120:123], v[180:183], v[120:123]
v_fma_f32 v230, v100, v242, v134
v_fma_f32 v231, v100, v243, v135
v_mfma_f32_16x16x32_bf16 v[124:127], a[124:127], v[180:183], v[124:127]
v_cvt_pk_bf16_f32 v228, v228, v229
v_cvt_pk_bf16_f32 v229, v230, v231
v_mfma_f32_16x16x32_bf16 v[128:131], v[180:183], a[128:131], v[128:131]
ds_write_b64 v20, v[228:229] offset:41472
v_mfma_f32_16x16x32_bf16 v[120:123], a[132:135], v[184:187], v[120:123]
v_cvt_pk_bf16_f32 v98, v216, v217
v_cvt_pk_bf16_f32 v99, v218, v219
v_mfma_f32_16x16x32_bf16 v[124:127], a[136:139], v[184:187], v[124:127]
ds_write_b64 v16, v[98:99]
v_mfma_f32_16x16x32_bf16 v[128:131], v[184:187], a[140:143], v[128:131]
v_cvt_pk_bf16_f32 v46, v214, v215
v_mfma_f32_16x16x32_bf16 v[120:123], a[144:147], v[188:191], v[120:123]
ds_write_b32 v17, v46
v_mfma_f32_16x16x32_bf16 v[124:127], a[148:151], v[188:191], v[124:127]
v_mfma_f32_16x16x32_bf16 v[128:131], v[188:191], a[152:155], v[128:131]
v_mfma_f32_16x16x32_bf16 v[120:123], a[156:159], v[192:195], v[120:123]
v_mfma_f32_16x16x32_bf16 v[124:127], a[160:163], v[192:195], v[124:127]
v_xor_b32 v49, 0x10000, v49
v_xor_b32 v50, 0x10000, v50
v_mfma_f32_16x16x32_bf16 v[128:131], v[192:195], a[164:167], v[128:131]
v_mfma_f32_16x16x32_bf16 v[120:123], a[168:171], v[196:199], v[120:123]
v_mfma_f32_16x16x32_bf16 v[124:127], a[172:175], v[196:199], v[124:127]
s_waitcnt vmcnt(5) lgkmcnt(0)
s_barrier
.Lqa_in16:
v_mfma_f32_16x16x32_bf16 v[128:131], v[196:199], a[176:179], v[128:131]
s_cmp_eq_u32 s70, s73
s_cbranch_scc1 .Lqa_hk17
.Lqa_hr18:
ds_read_b128 v[42:45], v49 offset:0
v_mfma_f32_16x16x32_bf16 v[120:123], a[180:183], v[200:203], v[120:123]
ds_read_b128 v[94:97], v50 offset:0
ds_read_b128 v[228:231], v15 offset:0
v_mfma_f32_16x16x32_bf16 v[124:127], a[184:187], v[200:203], v[124:127]
ds_read_b32 v8, v22 offset:64
ds_read_b32 v150, v19
v_mfma_f32_16x16x32_bf16 v[216:219], v[144:147], v[136:139], v[34:37]
v_mfma_f32_16x16x32_bf16 v[128:131], v[200:203], a[188:191], v[128:131]
ds_read_b128 v[102:105], v49 offset:2048
ds_read_b32 v21, v22 offset:128
v_mfma_f32_16x16x32_bf16 v[120:123], a[192:195], v[204:207], v[120:123]
ds_read_b128 v[0:3], v50 offset:2048
ds_read_b32 v24, v27
v_mfma_f32_16x16x32_bf16 v[124:127], a[196:199], v[204:207], v[124:127]
ds_read_b32 v51, v28
ds_read_b128 v[224:227], v23 offset:64
v_mfma_f32_16x16x32_bf16 v[128:131], v[204:207], a[200:203], v[128:131]
ds_read_b128 v[4:7], v49 offset:4096
ds_read_b64 v[158:159], v20 offset:41472
v_mfma_f32_16x16x32_bf16 v[120:123], a[204:207], v[208:211], v[120:123]
ds_read_b128 v[160:163], v50 offset:4096
ds_read_b32 v142, v18
v_mfma_f32_16x16x32_bf16 v[124:127], a[208:211], v[208:211], v[124:127]
v_mfma_f32_16x16x32_bf16 v[128:131], v[208:211], a[212:215], v[128:131]
s_sub_u32 s70, s70, 1
ds_read_b128 v[164:167], v49 offset:6144
s_cbranch_scc0 .Lqa_top26
s_waitcnt lgkmcnt(0)
v_max_f32 v106, v216, v217
global_store_dwordx4 v14, v[228:231], s[38:39] sc1
v_add_u32 v14, 18432, v14
v_fma_f32 v236, v88, v8, v124
v_fma_f32 v237, v89, v8, v125
v_fma_f32 v238, v90, v8, v126
v_fma_f32 v239, v91, v8, v127
v_cvt_pk_bf16_f32 v148, v236, v237
v_cvt_pk_bf16_f32 v149, v238, v239
v_max_f32 v107, v218, v219
s_nop 0
v_mfma_f32_16x16x32_bf16 v[212:215], v[148:151], v[136:139], v[30:33]
v_fma_f32 v236, v92, v224, v128
v_fma_f32 v237, v92, v225, v129
v_mul_f32 v21, 0x3eae1a93, v21
v_mul_f32 v93, v24, v21
v_mul_f32 v101, v51, v21
v_fma_f32 v238, v92, v226, v130
s_nop 1
v_max3_f32 v106, v212, v213, v106
v_max3_f32 v107, v214, v215, v107
v_mul_f32 v106, v106, v93
v_mul_f32 v107, v107, v101
v_max_f32 v254, v106, v107
v_mov_b32 v255, v254
v_fma_f32 v239, v92, v227, v131
s_nop 0
v_permlane16_swap_b32 v254, v255
v_max_f32 v254, v254, v255
v_mov_b32 v255, v254
s_nop 1
v_permlane32_swap_b32 v254, v255
v_max_f32 v254, v254, v255
v_sub_f32 v106, v106, v254
v_sub_f32 v107, v107, v254
v_cvt_pk_bf16_f32 v154, v236, v237
v_exp_f32 v46, v106
v_cvt_pk_bf16_f32 v155, v238, v239
v_exp_f32 v47, v107
s_nop 0
v_add_f32 v254, v46, v47
v_mov_b32 v255, v254
v_mul_f32 v46, v46, v24
v_fma_f32 v236, v84, v8, v120
v_permlane16_swap_b32 v254, v255
v_add_f32 v254, v254, v255
v_mov_b32 v255, v254
v_mul_f32 v47, v47, v51
v_fma_f32 v237, v85, v8, v121
v_permlane32_swap_b32 v254, v255
v_add_f32 v254, v254, v255
v_rcp_f32 v254, v254
s_nop 0
v_mul_f32 v46, v46, v254
v_mul_f32 v47, v47, v254
v_cvt_pk_bf16_f32 v46, v46, v46
v_cvt_pk_bf16_f32 v47, v47, v47
v_and_b32 v222, v46, v38
v_and_b32 v223, v47, v39
s_barrier
v_and_b32 v220, v46, v40
v_and_b32 v221, v47, v41
s_nop 1
v_mfma_f32_16x16x32_bf16 v[216:219], v[152:155], v[220:223], 0
v_mfma_f32_16x16x32_bf16 v[212:215], v[156:159], v[220:223], 0
v_fma_f32 v238, v86, v8, v122
v_fma_f32 v239, v87, v8, v123
v_cvt_pk_bf16_f32 v140, v236, v237
v_cvt_pk_bf16_f32 v141, v238, v239
s_nop 2
v_cvt_pk_bf16_f32 v98, v216, v217
v_cvt_pk_bf16_f32 v99, v218, v219
ds_write_b64 v16, v[98:99]
v_cvt_pk_bf16_f32 v46, v214, v215
ds_write_b32 v17, v46
s_waitcnt vmcnt(0)
s_waitcnt lgkmcnt(0)
s_barrier
ds_read_b128 v[228:231], v15 offset:0
s_waitcnt lgkmcnt(0)
global_store_dwordx4 v14, v[228:231], s[38:39] sc1
s_waitcnt vmcnt(0)
s_barrier
s_endpgm
.Lqa_w3:
s_nop 7
v_mov_b32 v200, v30
v_mov_b32 v201, v31
v_mov_b32 v202, v32
v_mov_b32 v203, v33
s_mov_b32 s70, 33
s_waitcnt vmcnt(0) lgkmcnt(0)
s_nop 7
s_branch .Lqa_in27
.p2align 6
.Lqa_top41:
s_waitcnt lgkmcnt(15)
v_mfma_f32_16x16x32_bf16 v[132:135], v[0:3], v[0:3], 0
s_waitcnt lgkmcnt(14)
ds_read_b128 v[60:63], v50 offset:6144
v_add_u32 v44, 1, v44
v_mfma_f32_16x16x32_bf16 v[132:135], v[4:7], v[4:7], v[132:135]
s_waitcnt lgkmcnt(14)
s_mov_b64 exec, 65535
global_store_dwordx4 v14, v[196:199], s[38:39] sc1
s_mov_b64 exec, -1
v_mfma_f32_16x16x32_bf16 v[108:111], a[0:3], v[0:3], 0
s_waitcnt lgkmcnt(11)
ds_read_b128 v[64:67], v49 offset:8192
v_add_u32 v14, 18432, v14
v_mfma_f32_16x16x32_bf16 v[132:135], v[94:97], v[94:97], v[132:135]
s_waitcnt lgkmcnt(10)
ds_read_b128 v[68:71], v50 offset:8192
v_fma_f32 v204, v88, v8, v124
v_mfma_f32_16x16x32_bf16 v[132:135], v[98:101], v[98:101], v[132:135]
v_fma_f32 v205, v89, v8, v125
v_fma_f32 v206, v90, v8, v126
v_mfma_f32_16x16x32_bf16 v[112:115], a[4:7], v[0:3], 0
s_waitcnt lgkmcnt(7)
ds_read_b128 v[72:75], v49 offset:10240
v_fma_f32 v207, v91, v8, v127
v_mfma_f32_16x16x32_bf16 v[132:135], v[102:105], v[102:105], v[132:135]
s_waitcnt lgkmcnt(6)
ds_read_b128 v[76:79], v50 offset:10240
v_cvt_pk_bf16_f32 v148, v204, v205
v_mfma_f32_16x16x32_bf16 v[132:135], v[52:55], v[52:55], v[132:135]
v_cvt_pk_bf16_f32 v149, v206, v207
v_max_f32 v47, v184, v185
v_mfma_f32_16x16x32_bf16 v[116:119], v[0:3], a[8:11], 0
ds_read_b128 v[80:83], v49 offset:12288
v_mfma_f32_16x16x32_bf16 v[180:183], v[148:151], v[136:139], v[30:33]
s_waitcnt lgkmcnt(6)
v_max_f32 v48, v186, v187
v_fma_f32 v204, v92, v192, v128
v_mfma_f32_16x16x32_bf16 v[132:135], v[56:59], v[56:59], v[132:135]
s_waitcnt lgkmcnt(5)
ds_read_b128 v[160:163], v50 offset:12288
v_fma_f32 v205, v92, v193, v129
v_mfma_f32_16x16x32_bf16 v[132:135], v[60:63], v[60:63], v[132:135]
v_mul_f32 v24, 0x3eae1a93, v24
v_mul_f32 v45, v93, v24
v_mfma_f32_16x16x32_bf16 v[108:111], a[12:15], v[4:7], v[108:111]
s_waitcnt lgkmcnt(5)
ds_read_b128 v[164:167], v49 offset:14336
v_mul_f32 v46, v255, v24
v_mfma_f32_16x16x32_bf16 v[132:135], v[64:67], v[64:67], v[132:135]
s_xor_b32 s49, s49, 0x10000
ds_read_b128 v[168:171], v50 offset:14336
s_waitcnt lgkmcnt(6)
v_mfma_f32_16x16x32_bf16 v[132:135], v[68:71], v[68:71], v[132:135]
s_add_u32 m0, s49, 0
v_max3_f32 v47, v180, v181, v47
v_max3_f32 v48, v182, v183, v48
v_mfma_f32_16x16x32_bf16 v[112:115], a[16:19], v[4:7], v[112:115]
s_waitcnt lgkmcnt(5)
ds_read_b128 v[172:175], v49 offset:16384
v_mul_f32 v47, v47, v45
v_mfma_f32_16x16x32_bf16 v[132:135], v[72:75], v[72:75], v[132:135]
s_waitcnt lgkmcnt(5)
ds_read_b128 v[176:179], v50 offset:16384
v_mul_f32 v48, v48, v46
v_mfma_f32_16x16x32_bf16 v[132:135], v[76:79], v[76:79], v[132:135]
v_max_f32 v208, v47, v48
v_mov_b32 v209, v208
v_mfma_f32_16x16x32_bf16 v[116:119], v[4:7], a[20:23], v[116:119]
s_waitcnt lgkmcnt(5)
v_permlane16_swap_b32 v208, v209
v_mfma_f32_16x16x32_bf16 v[132:135], v[80:83], v[80:83], v[132:135]
s_waitcnt lgkmcnt(4)
global_load_lds_dwordx4 v9, s[42:43] offset:-2048 sc1
v_max_f32 v208, v208, v209
v_mfma_f32_16x16x32_bf16 v[132:135], v[160:163], v[160:163], v[132:135]
v_mov_b32 v209, v208
v_fma_f32 v206, v92, v194, v130
v_mfma_f32_16x16x32_bf16 v[108:111], a[24:27], v[94:97], v[108:111]
s_waitcnt lgkmcnt(3)
v_permlane32_swap_b32 v208, v209
v_mfma_f32_16x16x32_bf16 v[132:135], v[164:167], v[164:167], v[132:135]
s_waitcnt lgkmcnt(2)
v_max_f32 v208, v208, v209
v_sub_f32 v47, v47, v208
v_mfma_f32_16x16x32_bf16 v[132:135], v[168:171], v[168:171], v[132:135]
v_sub_f32 v48, v48, v208
v_fma_f32 v207, v92, v195, v131
v_mfma_f32_16x16x32_bf16 v[112:115], a[28:31], v[94:97], v[112:115]
s_waitcnt lgkmcnt(1)
v_exp_f32 v210, v47
v_mfma_f32_16x16x32_bf16 v[132:135], v[172:175], v[172:175], v[132:135]
s_waitcnt lgkmcnt(0)
v_exp_f32 v211, v48
v_mfma_f32_16x16x32_bf16 v[132:135], v[176:179], v[176:179], v[132:135]
v_add_f32 v208, v210, v211
v_mov_b32 v209, v208
v_mfma_f32_16x16x32_bf16 v[116:119], v[94:97], a[32:35], v[116:119]
global_load_lds_dwordx4 v10, s[42:43] offset:-1024 sc1
v_mul_f32 v210, v210, v93
v_mfma_f32_16x16x32_bf16 v[108:111], a[36:39], v[98:101], v[108:111]
v_permlane16_swap_b32 v208, v209
v_mfma_f32_16x16x32_bf16 v[112:115], a[40:43], v[98:101], v[112:115]
v_add_f32 v208, v208, v209
v_mov_b32 v209, v208
v_mfma_f32_16x16x32_bf16 v[116:119], v[98:101], a[44:47], v[116:119]
ds_write_b128 v21, v[132:135] offset:41984
v_mfma_f32_16x16x32_bf16 v[108:111], a[48:51], v[102:105], v[108:111]
v_mul_f32 v211, v211, v255
v_cvt_pk_bf16_f32 v154, v204, v205
v_mfma_f32_16x16x32_bf16 v[112:115], a[52:55], v[102:105], v[112:115]
v_permlane32_swap_b32 v208, v209
v_mfma_f32_16x16x32_bf16 v[116:119], v[102:105], a[56:59], v[116:119]
v_add_f32 v208, v208, v209
v_cvt_pk_bf16_f32 v155, v206, v207
v_mfma_f32_16x16x32_bf16 v[108:111], a[60:63], v[52:55], v[108:111]
)"
R"(v_rcp_f32 v208, v208
v_mfma_f32_16x16x32_bf16 v[112:115], a[64:67], v[52:55], v[112:115]
global_load_lds_dwordx4 v11, s[42:43] offset:0 sc1
v_mul_f32 v210, v210, v208
v_mfma_f32_16x16x32_bf16 v[116:119], v[52:55], a[68:71], v[116:119]
v_mul_f32 v211, v211, v208
v_cvt_pk_bf16_f32 v210, v210, v210
v_mfma_f32_16x16x32_bf16 v[108:111], a[72:75], v[56:59], v[108:111]
v_cvt_pk_bf16_f32 v211, v211, v211
v_and_b32 v190, v210, v38
v_mfma_f32_16x16x32_bf16 v[112:115], a[76:79], v[56:59], v[112:115]
v_mfma_f32_16x16x32_bf16 v[116:119], v[56:59], a[80:83], v[116:119]
s_waitcnt lgkmcnt(0)
s_barrier
v_mfma_f32_16x16x32_bf16 v[108:111], a[84:87], v[60:63], v[108:111]
v_and_b32 v191, v211, v39
v_and_b32 v188, v210, v40
v_mfma_f32_16x16x32_bf16 v[112:115], a[88:91], v[60:63], v[112:115]
v_and_b32 v189, v211, v41
v_fma_f32 v204, v84, v8, v120
v_mfma_f32_16x16x32_bf16 v[116:119], v[60:63], a[92:95], v[116:119]
v_mfma_f32_16x16x32_bf16 v[184:187], v[152:155], v[188:191], 0
v_mfma_f32_16x16x32_bf16 v[180:183], v[156:159], v[188:191], 0
v_fma_f32 v205, v85, v8, v121
v_fma_f32 v206, v86, v8, v122
v_mfma_f32_16x16x32_bf16 v[108:111], a[96:99], v[64:67], v[108:111]
global_load_lds_dwordx4 v12, s[42:43] offset:1024 sc1
s_add_u32 s42, s42, s46
s_addc_u32 s43, s43, s47
v_mfma_f32_16x16x32_bf16 v[112:115], a[100:103], v[64:67], v[112:115]
v_fma_f32 v207, v87, v8, v123
v_cvt_pk_bf16_f32 v140, v204, v205
v_mfma_f32_16x16x32_bf16 v[116:119], v[64:67], a[104:107], v[116:119]
v_cvt_pk_bf16_f32 v141, v206, v207
v_mfma_f32_16x16x32_bf16 v[108:111], a[108:111], v[68:71], v[108:111]
v_cvt_pk_bf16_f32 v106, v184, v185
v_cvt_pk_bf16_f32 v107, v186, v187
v_mfma_f32_16x16x32_bf16 v[112:115], a[112:115], v[68:71], v[112:115]
ds_write_b64 v16, v[106:107]
v_mfma_f32_16x16x32_bf16 v[116:119], v[68:71], a[116:119], v[116:119]
v_cvt_pk_bf16_f32 v210, v180, v181
v_mfma_f32_16x16x32_bf16 v[108:111], a[120:123], v[72:75], v[108:111]
ds_write_b32 v17, v210
v_mfma_f32_16x16x32_bf16 v[112:115], a[124:127], v[72:75], v[112:115]
v_mfma_f32_16x16x32_bf16 v[116:119], v[72:75], a[128:131], v[116:119]
v_mfma_f32_16x16x32_bf16 v[108:111], a[132:135], v[76:79], v[108:111]
v_mfma_f32_16x16x32_bf16 v[112:115], a[136:139], v[76:79], v[112:115]
v_mfma_f32_16x16x32_bf16 v[116:119], v[76:79], a[140:143], v[116:119]
v_mfma_f32_16x16x32_bf16 v[108:111], a[144:147], v[80:83], v[108:111]
v_mfma_f32_16x16x32_bf16 v[112:115], a[148:151], v[80:83], v[112:115]
v_mfma_f32_16x16x32_bf16 v[116:119], v[80:83], a[152:155], v[116:119]
s_waitcnt vmcnt(5)
v_sub_u32 v51, v51, v44
v_cmp_lt_u32 vcc, 0x7f, v51
v_mfma_f32_16x16x32_bf16 v[108:111], a[156:159], v[160:163], v[108:111]
global_load_dword v254, v42, s[38:39] sc1
v_mfma_f32_16x16x32_bf16 v[112:115], a[160:163], v[160:163], v[112:115]
v_mfma_f32_16x16x32_bf16 v[116:119], v[160:163], a[164:167], v[116:119]
s_and_b64 vcc, vcc, s[76:77]
s_cbranch_vccnz .Lqa_sp30
.Lqa_rt31:
v_mfma_f32_16x16x32_bf16 v[108:111], a[168:171], v[164:167], v[108:111]
v_mfma_f32_16x16x32_bf16 v[112:115], a[172:175], v[164:167], v[112:115]
s_waitcnt vmcnt(5) lgkmcnt(0)
s_barrier
v_mfma_f32_16x16x32_bf16 v[116:119], v[164:167], a[176:179], v[116:119]
ds_read_b128 v[0:3], v49 offset:18432
v_mfma_f32_16x16x32_bf16 v[108:111], a[180:183], v[168:171], v[108:111]
ds_read_b128 v[4:7], v50 offset:18432
ds_read_b128 v[196:199], v15 offset:0
v_mfma_f32_16x16x32_bf16 v[112:115], a[184:187], v[168:171], v[112:115]
ds_read_b32 v8, v22 offset:0
ds_read_b32 v146, v19
v_mfma_f32_16x16x32_bf16 v[184:187], v[148:151], v[140:143], v[34:37]
v_mfma_f32_16x16x32_bf16 v[116:119], v[168:171], a[188:191], v[116:119]
ds_read_b128 v[94:97], v49 offset:20480
ds_read_b32 v24, v22 offset:192
v_mfma_f32_16x16x32_bf16 v[108:111], a[192:195], v[172:175], v[108:111]
ds_read_b128 v[98:101], v50 offset:20480
ds_read_b32 v93, v25
v_mfma_f32_16x16x32_bf16 v[112:115], a[196:199], v[172:175], v[112:115]
ds_read_b32 v255, v26
ds_read_b128 v[192:195], v23 offset:0
v_mfma_f32_16x16x32_bf16 v[116:119], v[172:175], a[200:203], v[116:119]
ds_read_b128 v[102:105], v49 offset:22528
ds_read_b64 v[156:157], v20 offset:41472
v_mfma_f32_16x16x32_bf16 v[108:111], a[204:207], v[176:179], v[108:111]
ds_read_b128 v[52:55], v50 offset:22528
ds_read_b32 v138, v18
v_mfma_f32_16x16x32_bf16 v[112:115], a[208:211], v[176:179], v[112:115]
v_add_u32 v44, 1, v44
v_max_f32 v47, v184, v185
v_mfma_f32_16x16x32_bf16 v[116:119], v[176:179], a[212:215], v[116:119]
s_waitcnt lgkmcnt(14)
ds_read_b128 v[56:59], v49 offset:24576
v_max_f32 v48, v186, v187
v_mfma_f32_16x16x32_bf16 v[132:135], v[0:3], v[0:3], 0
s_waitcnt lgkmcnt(14)
ds_read_b128 v[60:63], v50 offset:24576
v_mfma_f32_16x16x32_bf16 v[132:135], v[4:7], v[4:7], v[132:135]
s_waitcnt lgkmcnt(14)
s_mov_b64 exec, 65535
global_store_dwordx4 v14, v[196:199], s[38:39] sc1
s_mov_b64 exec, -1
v_mfma_f32_16x16x32_bf16 v[120:123], a[0:3], v[0:3], 0
s_waitcnt lgkmcnt(11)
ds_read_b128 v[64:67], v49 offset:26624
v_add_u32 v14, 18432, v14
v_mfma_f32_16x16x32_bf16 v[132:135], v[94:97], v[94:97], v[132:135]
s_waitcnt lgkmcnt(10)
ds_read_b128 v[68:71], v50 offset:26624
v_fma_f32 v204, v88, v8, v112
v_mfma_f32_16x16x32_bf16 v[132:135], v[98:101], v[98:101], v[132:135]
v_fma_f32 v205, v89, v8, v113
v_fma_f32 v206, v90, v8, v114
v_mfma_f32_16x16x32_bf16 v[124:127], a[4:7], v[0:3], 0
s_waitcnt lgkmcnt(7)
ds_read_b128 v[72:75], v49 offset:28672
v_fma_f32 v207, v91, v8, v115
v_mfma_f32_16x16x32_bf16 v[132:135], v[102:105], v[102:105], v[132:135]
s_waitcnt lgkmcnt(6)
ds_read_b128 v[76:79], v50 offset:28672
v_cvt_pk_bf16_f32 v144, v204, v205
v_mfma_f32_16x16x32_bf16 v[132:135], v[52:55], v[52:55], v[132:135]
v_cvt_pk_bf16_f32 v145, v206, v207
v_mul_f32 v24, 0x3eae1a93, v24
v_mfma_f32_16x16x32_bf16 v[128:131], v[0:3], a[8:11], 0
ds_read_b128 v[80:83], v49 offset:30720
v_mfma_f32_16x16x32_bf16 v[180:183], v[144:147], v[140:143], v[200:203]
s_waitcnt lgkmcnt(6)
v_mul_f32 v45, v93, v24
v_mul_f32 v46, v255, v24
v_mfma_f32_16x16x32_bf16 v[132:135], v[56:59], v[56:59], v[132:135]
s_waitcnt lgkmcnt(5)
ds_read_b128 v[160:163], v50 offset:30720
v_fma_f32 v204, v92, v192, v116
v_mfma_f32_16x16x32_bf16 v[132:135], v[60:63], v[60:63], v[132:135]
v_fma_f32 v205, v92, v193, v117
v_fma_f32 v206, v92, v194, v118
v_mfma_f32_16x16x32_bf16 v[120:123], a[12:15], v[4:7], v[120:123]
s_waitcnt lgkmcnt(5)
ds_read_b128 v[164:167], v49 offset:32768
v_fma_f32 v207, v92, v195, v119
v_mfma_f32_16x16x32_bf16 v[132:135], v[64:67], v[64:67], v[132:135]
s_add_u32 m0, s49, 18432
ds_read_b128 v[168:171], v50 offset:32768
s_waitcnt lgkmcnt(6)
v_mfma_f32_16x16x32_bf16 v[132:135], v[68:71], v[68:71], v[132:135]
v_max3_f32 v47, v180, v181, v47
v_max3_f32 v48, v182, v183, v48
v_mfma_f32_16x16x32_bf16 v[124:127], a[16:19], v[4:7], v[124:127]
s_waitcnt lgkmcnt(5)
ds_read_b128 v[172:175], v49 offset:34816
v_mul_f32 v47, v47, v45
v_mfma_f32_16x16x32_bf16 v[132:135], v[72:75], v[72:75], v[132:135]
s_waitcnt lgkmcnt(5)
ds_read_b128 v[176:179], v50 offset:34816
v_mul_f32 v48, v48, v46
v_mfma_f32_16x16x32_bf16 v[132:135], v[76:79], v[76:79], v[132:135]
v_max_f32 v208, v47, v48
v_mov_b32 v209, v208
v_mfma_f32_16x16x32_bf16 v[128:131], v[4:7], a[20:23], v[128:131]
s_waitcnt lgkmcnt(5)
global_load_lds_dwordx4 v9, s[42:43] offset:-2048 sc1
v_cvt_pk_bf16_f32 v152, v204, v205
v_mfma_f32_16x16x32_bf16 v[132:135], v[80:83], v[80:83], v[132:135]
s_waitcnt lgkmcnt(4)
v_permlane16_swap_b32 v208, v209
v_mfma_f32_16x16x32_bf16 v[132:135], v[160:163], v[160:163], v[132:135]
v_max_f32 v208, v208, v209
v_mov_b32 v209, v208
v_mfma_f32_16x16x32_bf16 v[120:123], a[24:27], v[94:97], v[120:123]
s_waitcnt lgkmcnt(3)
v_permlane32_swap_b32 v208, v209
v_mfma_f32_16x16x32_bf16 v[132:135], v[164:167], v[164:167], v[132:135]
s_waitcnt lgkmcnt(2)
v_max_f32 v208, v208, v209
v_sub_f32 v47, v47, v208
v_mfma_f32_16x16x32_bf16 v[132:135], v[168:171], v[168:171], v[132:135]
v_sub_f32 v48, v48, v208
v_cvt_pk_bf16_f32 v153, v206, v207
v_mfma_f32_16x16x32_bf16 v[124:127], a[28:31], v[94:97], v[124:127]
s_waitcnt lgkmcnt(1)
v_exp_f32 v210, v47
v_mfma_f32_16x16x32_bf16 v[132:135], v[172:175], v[172:175], v[132:135]
s_waitcnt lgkmcnt(0)
v_exp_f32 v211, v48
v_mfma_f32_16x16x32_bf16 v[132:135], v[176:179], v[176:179], v[132:135]
global_load_lds_dwordx4 v10, s[42:43] offset:-1024 sc1
v_add_f32 v208, v210, v211
v_mfma_f32_16x16x32_bf16 v[128:131], v[94:97], a[32:35], v[128:131]
v_mov_b32 v209, v208
v_mul_f32 v210, v210, v93
v_mfma_f32_16x16x32_bf16 v[120:123], a[36:39], v[98:101], v[120:123]
v_permlane16_swap_b32 v208, v209
v_mfma_f32_16x16x32_bf16 v[124:127], a[40:43], v[98:101], v[124:127]
v_add_f32 v208, v208, v209
v_mov_b32 v209, v208
v_mfma_f32_16x16x32_bf16 v[128:131], v[98:101], a[44:47], v[128:131]
ds_write_b128 v21, v[132:135] offset:41984
v_mfma_f32_16x16x32_bf16 v[120:123], a[48:51], v[102:105], v[120:123]
v_mul_f32 v211, v211, v255
v_fma_f32 v204, v84, v8, v108
v_mfma_f32_16x16x32_bf16 v[124:127], a[52:55], v[102:105], v[124:127]
v_permlane32_swap_b32 v208, v209
v_mfma_f32_16x16x32_bf16 v[128:131], v[102:105], a[56:59], v[128:131]
v_add_f32 v208, v208, v209
v_fma_f32 v205, v85, v8, v109
v_mfma_f32_16x16x32_bf16 v[120:123], a[60:63], v[52:55], v[120:123]
global_load_lds_dwordx4 v11, s[42:43] offset:0 sc1
v_fma_f32 v206, v86, v8, v110
v_mfma_f32_16x16x32_bf16 v[124:127], a[64:67], v[52:55], v[124:127]
v_rcp_f32 v208, v208
v_mfma_f32_16x16x32_bf16 v[128:131], v[52:55], a[68:71], v[128:131]
v_mul_f32 v210, v210, v208
v_mul_f32 v211, v211, v208
v_mfma_f32_16x16x32_bf16 v[120:123], a[72:75], v[56:59], v[120:123]
v_cvt_pk_bf16_f32 v210, v210, v210
v_cvt_pk_bf16_f32 v211, v211, v211
v_mfma_f32_16x16x32_bf16 v[124:127], a[76:79], v[56:59], v[124:127]
v_mfma_f32_16x16x32_bf16 v[128:131], v[56:59], a[80:83], v[128:131]
s_waitcnt lgkmcnt(0)
s_barrier
v_mfma_f32_16x16x32_bf16 v[120:123], a[84:87], v[60:63], v[120:123]
v_and_b32 v188, v210, v38
v_and_b32 v189, v211, v39
v_mfma_f32_16x16x32_bf16 v[124:127], a[88:91], v[60:63], v[124:127]
v_and_b32 v190, v210, v40
v_and_b32 v191, v211, v41
v_mfma_f32_16x16x32_bf16 v[128:131], v[60:63], a[92:95], v[128:131]
global_load_lds_dwordx4 v12, s[42:43] offset:1024 sc1
s_sub_u32 s42, s42, 18432
s_subb_u32 s43, s43, 0
v_mfma_f32_16x16x32_bf16 v[184:187], v[152:155], v[188:191], 0
v_mfma_f32_16x16x32_bf16 v[180:183], v[156:159], v[188:191], 0
v_fma_f32 v207, v87, v8, v111
v_cvt_pk_bf16_f32 v136, v204, v205
v_mfma_f32_16x16x32_bf16 v[120:123], a[96:99], v[64:67], v[120:123]
v_cvt_pk_bf16_f32 v137, v206, v207
v_mfma_f32_16x16x32_bf16 v[124:127], a[100:103], v[64:67], v[124:127]
v_mfma_f32_16x16x32_bf16 v[128:131], v[64:67], a[104:107], v[128:131]
v_mfma_f32_16x16x32_bf16 v[120:123], a[108:111], v[68:71], v[120:123]
v_cvt_pk_bf16_f32 v106, v184, v185
v_cvt_pk_bf16_f32 v107, v186, v187
v_mfma_f32_16x16x32_bf16 v[124:127], a[112:115], v[68:71], v[124:127]
ds_write_b64 v16, v[106:107]
v_mfma_f32_16x16x32_bf16 v[128:131], v[68:71], a[116:119], v[128:131]
v_cvt_pk_bf16_f32 v210, v180, v181
v_mfma_f32_16x16x32_bf16 v[120:123], a[120:123], v[72:75], v[120:123]
ds_write_b32 v17, v210
v_mfma_f32_16x16x32_bf16 v[124:127], a[124:127], v[72:75], v[124:127]
v_mfma_f32_16x16x32_bf16 v[128:131], v[72:75], a[128:131], v[128:131]
v_mfma_f32_16x16x32_bf16 v[120:123], a[132:135], v[76:79], v[120:123]
v_mfma_f32_16x16x32_bf16 v[124:127], a[136:139], v[76:79], v[124:127]
v_mfma_f32_16x16x32_bf16 v[128:131], v[76:79], a[140:143], v[128:131]
v_mfma_f32_16x16x32_bf16 v[120:123], a[144:147], v[80:83], v[120:123]
v_mfma_f32_16x16x32_bf16 v[124:127], a[148:151], v[80:83], v[124:127]
v_mfma_f32_16x16x32_bf16 v[128:131], v[80:83], a[152:155], v[128:131]
s_waitcnt vmcnt(5)
v_sub_u32 v254, v254, v44
v_cmp_lt_u32 vcc, 0x7f, v254
v_mfma_f32_16x16x32_bf16 v[120:123], a[156:159], v[160:163], v[120:123]
global_load_dword v51, v43, s[38:39] sc1
v_mfma_f32_16x16x32_bf16 v[124:127], a[160:163], v[160:163], v[124:127]
v_xor_b32 v49, 0x10000, v49
v_xor_b32 v50, 0x10000, v50
v_mfma_f32_16x16x32_bf16 v[128:131], v[160:163], a[164:167], v[128:131]
s_and_b64 vcc, vcc, s[76:77]
s_cbranch_vccnz .Lqa_sp32
.Lqa_rt33:
v_mfma_f32_16x16x32_bf16 v[120:123], a[168:171], v[164:167], v[120:123]
v_mfma_f32_16x16x32_bf16 v[124:127], a[172:175], v[164:167], v[124:127]
s_waitcnt vmcnt(5) lgkmcnt(0)
s_barrier
.Lqa_in27:
v_mfma_f32_16x16x32_bf16 v[128:131], v[164:167], a[176:179], v[128:131]
s_cmp_eq_u32 s70, s73
s_cbranch_scc1 .Lqa_hk28
.Lqa_hr29:
ds_read_b128 v[0:3], v49 offset:0
v_mfma_f32_16x16x32_bf16 v[120:123], a[180:183], v[168:171], v[120:123]
ds_read_b128 v[4:7], v50 offset:0
ds_read_b128 v[196:199], v15 offset:0
v_mfma_f32_16x16x32_bf16 v[124:127], a[184:187], v[168:171], v[124:127]
ds_read_b32 v8, v22 offset:64
ds_read_b32 v150, v19
v_mfma_f32_16x16x32_bf16 v[184:187], v[144:147], v[136:139], v[34:37]
v_mfma_f32_16x16x32_bf16 v[128:131], v[168:171], a[188:191], v[128:131]
ds_read_b128 v[94:97], v49 offset:2048
ds_read_b32 v24, v22 offset:128
v_mfma_f32_16x16x32_bf16 v[120:123], a[192:195], v[172:175], v[120:123]
ds_read_b128 v[98:101], v50 offset:2048
ds_read_b32 v93, v27
v_mfma_f32_16x16x32_bf16 v[124:127], a[196:199], v[172:175], v[124:127]
ds_read_b32 v255, v28
ds_read_b128 v[192:195], v23 offset:64
v_mfma_f32_16x16x32_bf16 v[128:131], v[172:175], a[200:203], v[128:131]
ds_read_b128 v[102:105], v49 offset:4096
ds_read_b64 v[158:159], v20 offset:41472
v_mfma_f32_16x16x32_bf16 v[120:123], a[204:207], v[176:179], v[120:123]
ds_read_b128 v[52:55], v50 offset:4096
ds_read_b32 v142, v18
v_mfma_f32_16x16x32_bf16 v[124:127], a[208:211], v[176:179], v[124:127]
v_mfma_f32_16x16x32_bf16 v[128:131], v[176:179], a[212:215], v[128:131]
s_sub_u32 s70, s70, 1
ds_read_b128 v[56:59], v49 offset:6144
s_cbranch_scc0 .Lqa_top41
s_waitcnt lgkmcnt(0)
s_mov_b64 exec, 65535
global_store_dwordx4 v14, v[196:199], s[38:39] sc1
s_mov_b64 exec, -1
v_add_u32 v14, 18432, v14
v_fma_f32 v204, v88, v8, v124
v_fma_f32 v205, v89, v8, v125
v_fma_f32 v206, v90, v8, v126
v_fma_f32 v207, v91, v8, v127
v_cvt_pk_bf16_f32 v148, v204, v205
v_cvt_pk_bf16_f32 v149, v206, v207
v_max_f32 v47, v184, v185
s_nop 0
v_mfma_f32_16x16x32_bf16 v[180:183], v[148:151], v[136:139], v[30:33]
v_max_f32 v48, v186, v187
v_fma_f32 v204, v92, v192, v128
v_fma_f32 v205, v92, v193, v129
v_mul_f32 v24, 0x3eae1a93, v24
v_mul_f32 v45, v93, v24
v_mul_f32 v46, v255, v24
s_nop 1
v_max3_f32 v47, v180, v181, v47
v_max3_f32 v48, v182, v183, v48
v_mul_f32 v47, v47, v45
v_mul_f32 v48, v48, v46
v_max_f32 v208, v47, v48
v_mov_b32 v209, v208
s_nop 1
v_permlane16_swap_b32 v208, v209
v_max_f32 v208, v208, v209
v_mov_b32 v209, v208
v_fma_f32 v206, v92, v194, v130
s_nop 0
v_permlane32_swap_b32 v208, v209
v_max_f32 v208, v208, v209
v_sub_f32 v47, v47, v208
v_sub_f32 v48, v48, v208
v_fma_f32 v207, v92, v195, v131
v_exp_f32 v210, v47
v_exp_f32 v211, v48
s_nop 0
v_add_f32 v208, v210, v211
v_mov_b32 v209, v208
v_mul_f32 v210, v210, v93
s_nop 0
v_permlane16_swap_b32 v208, v209
v_add_f32 v208, v208, v209
v_mov_b32 v209, v208
v_mul_f32 v211, v211, v255
v_cvt_pk_bf16_f32 v154, v204, v205
v_permlane32_swap_b32 v208, v209
v_add_f32 v208, v208, v209
v_cvt_pk_bf16_f32 v155, v206, v207
v_rcp_f32 v208, v208
s_nop 0
v_mul_f32 v210, v210, v208
v_mul_f32 v211, v211, v208
v_cvt_pk_bf16_f32 v210, v210, v210
v_cvt_pk_bf16_f32 v211, v211, v211
v_and_b32 v190, v210, v38
s_barrier
v_and_b32 v191, v211, v39
v_and_b32 v188, v210, v40
v_and_b32 v189, v211, v41
v_fma_f32 v204, v84, v8, v120
s_nop 0
v_mfma_f32_16x16x32_bf16 v[184:187], v[152:155], v[188:191], 0
v_mfma_f32_16x16x32_bf16 v[180:183], v[156:159], v[188:191], 0
v_fma_f32 v205, v85, v8, v121
v_fma_f32 v206, v86, v8, v122
v_fma_f32 v207, v87, v8, v123
v_cvt_pk_bf16_f32 v140, v204, v205
v_cvt_pk_bf16_f32 v141, v206, v207
s_nop 1
v_cvt_pk_bf16_f32 v106, v184, v185
v_cvt_pk_bf16_f32 v107, v186, v187
ds_write_b64 v16, v[106:107]
v_cvt_pk_bf16_f32 v210, v180, v181
ds_write_b32 v17, v210
s_waitcnt vmcnt(0)
s_waitcnt lgkmcnt(0)
s_barrier
ds_read_b128 v[196:199], v15 offset:0
s_waitcnt lgkmcnt(0)
global_store_dwordx4 v14, v[196:199], s[38:39] sc1
s_waitcnt vmcnt(0)
s_barrier
s_endpgm
.Lqa_w4:
s_nop 7
v_mov_b32 v204, v30
v_mov_b32 v205, v31
v_mov_b32 v206, v32
)"
R"(v_mov_b32 v207, v33
s_mov_b32 s70, 33
s_waitcnt vmcnt(0) lgkmcnt(0)
s_nop 7
s_branch .Lqa_in42
.p2align 6
.Lqa_top52:
s_waitcnt lgkmcnt(12)
v_mfma_f32_16x16x32_bf16 v[132:135], v[104:107], v[0:3], 0
s_waitcnt lgkmcnt(11)
ds_read_b128 v[64:67], v50 offset:6144
ds_read_b64 v[158:159], v20 offset:41472
v_mfma_f32_16x16x32_bf16 v[132:135], v[104:107], v[4:7], v[132:135]
ds_read_b32 v142, v18
v_max_f32 v43, v188, v189
v_mfma_f32_16x16x32_bf16 v[108:111], a[0:3], v[0:3], 0
s_waitcnt lgkmcnt(13)
ds_read_b128 v[68:71], v49 offset:8192
v_fma_f32 v208, v88, v8, v124
s_waitcnt lgkmcnt(13)
v_mfma_f32_16x16x32_bf16 v[132:135], v[104:107], v[94:97], v[132:135]
s_waitcnt lgkmcnt(12)
ds_read_b128 v[72:75], v50 offset:8192
v_fma_f32 v209, v89, v8, v125
v_mfma_f32_16x16x32_bf16 v[132:135], v[104:107], v[98:101], v[132:135]
v_fma_f32 v210, v90, v8, v126
v_fma_f32 v211, v91, v8, v127
v_mfma_f32_16x16x32_bf16 v[112:115], a[4:7], v[0:3], 0
s_waitcnt lgkmcnt(9)
ds_read_b128 v[76:79], v49 offset:10240
v_cvt_pk_bf16_f32 v148, v208, v209
v_mfma_f32_16x16x32_bf16 v[132:135], v[104:107], v[52:55], v[132:135]
s_waitcnt lgkmcnt(8)
ds_read_b128 v[80:83], v50 offset:10240
v_cvt_pk_bf16_f32 v149, v210, v211
v_mfma_f32_16x16x32_bf16 v[132:135], v[104:107], v[56:59], v[132:135]
s_nop 0
v_mfma_f32_16x16x32_bf16 v[184:187], v[148:151], v[136:139], v[30:33]
s_waitcnt lgkmcnt(8)
v_max_f32 v44, v190, v191
v_fma_f32 v208, v92, v196, v128
v_mfma_f32_16x16x32_bf16 v[116:119], v[0:3], a[8:11], 0
s_waitcnt lgkmcnt(7)
ds_read_b128 v[160:163], v49 offset:12288
v_fma_f32 v209, v92, v197, v129
v_mfma_f32_16x16x32_bf16 v[132:135], v[104:107], v[60:63], v[132:135]
s_waitcnt lgkmcnt(7)
ds_read_b128 v[164:167], v50 offset:12288
v_mul_f32 v21, 0x3eae1a93, v21
v_mfma_f32_16x16x32_bf16 v[132:135], v[104:107], v[64:67], v[132:135]
v_mul_f32 v255, v93, v21
v_mul_f32 v42, v254, v21
v_mfma_f32_16x16x32_bf16 v[108:111], a[12:15], v[4:7], v[108:111]
s_waitcnt lgkmcnt(5)
ds_read_b128 v[168:171], v49 offset:14336
v_max3_f32 v43, v184, v185, v43
v_mfma_f32_16x16x32_bf16 v[132:135], v[104:107], v[68:71], v[132:135]
s_xor_b32 s49, s49, 0x10000
ds_read_b128 v[172:175], v50 offset:14336
s_waitcnt lgkmcnt(6)
v_mfma_f32_16x16x32_bf16 v[132:135], v[104:107], v[72:75], v[132:135]
s_add_u32 m0, s49, 0
v_max3_f32 v44, v186, v187, v44
v_mul_f32 v43, v43, v255
v_mfma_f32_16x16x32_bf16 v[112:115], a[16:19], v[4:7], v[112:115]
s_waitcnt lgkmcnt(5)
ds_read_b128 v[176:179], v49 offset:16384
v_mul_f32 v44, v44, v42
v_mfma_f32_16x16x32_bf16 v[132:135], v[104:107], v[76:79], v[132:135]
s_waitcnt lgkmcnt(5)
ds_read_b128 v[180:183], v50 offset:16384
v_max_f32 v212, v43, v44
v_mfma_f32_16x16x32_bf16 v[132:135], v[104:107], v[80:83], v[132:135]
v_mov_b32 v213, v212
v_fma_f32 v210, v92, v198, v130
v_mfma_f32_16x16x32_bf16 v[116:119], v[4:7], a[20:23], v[116:119]
s_waitcnt lgkmcnt(5)
v_permlane16_swap_b32 v212, v213
v_mfma_f32_16x16x32_bf16 v[132:135], v[104:107], v[160:163], v[132:135]
s_waitcnt lgkmcnt(4)
v_max_f32 v212, v212, v213
v_mov_b32 v213, v212
v_mfma_f32_16x16x32_bf16 v[132:135], v[104:107], v[164:167], v[132:135]
s_nop 0
v_permlane32_swap_b32 v212, v213
v_mfma_f32_16x16x32_bf16 v[108:111], a[24:27], v[94:97], v[108:111]
s_waitcnt lgkmcnt(3)
global_load_lds_dwordx4 v9, s[42:43] offset:-2048 sc1
v_max_f32 v212, v212, v213
v_mfma_f32_16x16x32_bf16 v[132:135], v[104:107], v[168:171], v[132:135]
s_waitcnt lgkmcnt(2)
v_sub_f32 v43, v43, v212
v_sub_f32 v44, v44, v212
v_mfma_f32_16x16x32_bf16 v[132:135], v[104:107], v[172:175], v[132:135]
v_exp_f32 v214, v43
v_mfma_f32_16x16x32_bf16 v[112:115], a[28:31], v[94:97], v[112:115]
s_waitcnt lgkmcnt(1)
v_exp_f32 v215, v44
v_mfma_f32_16x16x32_bf16 v[132:135], v[104:107], v[176:179], v[132:135]
s_waitcnt lgkmcnt(0)
v_add_f32 v212, v214, v215
v_mov_b32 v213, v212
v_mfma_f32_16x16x32_bf16 v[132:135], v[104:107], v[180:183], v[132:135]
v_mul_f32 v214, v214, v93
v_fma_f32 v211, v92, v199, v131
v_mfma_f32_16x16x32_bf16 v[116:119], v[94:97], a[32:35], v[116:119]
v_permlane16_swap_b32 v212, v213
v_mfma_f32_16x16x32_bf16 v[108:111], a[36:39], v[98:101], v[108:111]
v_add_f32 v212, v212, v213
v_mov_b32 v213, v212
v_mfma_f32_16x16x32_bf16 v[112:115], a[40:43], v[98:101], v[112:115]
v_mul_f32 v200, 0x3ae38e39, v132
v_cvt_pk_bf16_f32 v216, -v200, -v200
v_mfma_f32_16x16x32_bf16 v[116:119], v[98:101], a[44:47], v[116:119]
v_lshlrev_b32 v216, 16, v216
global_load_lds_dwordx4 v10, s[42:43] offset:-1024 sc1
v_mfma_f32_16x16x32_bf16 v[108:111], a[48:51], v[52:55], v[108:111]
ds_write_b32 v22, v216 offset:0
v_mfma_f32_16x16x32_bf16 v[112:115], a[52:55], v[52:55], v[112:115]
v_mul_f32 v215, v215, v254
v_cvt_pk_bf16_f32 v154, v208, v209
v_mfma_f32_16x16x32_bf16 v[116:119], v[52:55], a[56:59], v[116:119]
v_permlane32_swap_b32 v212, v213
v_mfma_f32_16x16x32_bf16 v[108:111], a[60:63], v[56:59], v[108:111]
v_add_f32 v212, v212, v213
v_cvt_pk_bf16_f32 v155, v210, v211
v_mfma_f32_16x16x32_bf16 v[112:115], a[64:67], v[56:59], v[112:115]
v_rcp_f32 v212, v212
v_mfma_f32_16x16x32_bf16 v[116:119], v[56:59], a[68:71], v[116:119]
v_mul_f32 v214, v214, v212
v_mul_f32 v215, v215, v212
v_mfma_f32_16x16x32_bf16 v[108:111], a[72:75], v[60:63], v[108:111]
v_cvt_pk_bf16_f32 v214, v214, v214
v_cvt_pk_bf16_f32 v215, v215, v215
v_mfma_f32_16x16x32_bf16 v[112:115], a[76:79], v[60:63], v[112:115]
v_mfma_f32_16x16x32_bf16 v[116:119], v[60:63], a[80:83], v[116:119]
s_waitcnt lgkmcnt(0)
s_barrier
v_mfma_f32_16x16x32_bf16 v[108:111], a[84:87], v[64:67], v[108:111]
global_load_lds_dwordx4 v11, s[42:43] offset:0 sc1
ds_read_b32 v216, v24
v_mfma_f32_16x16x32_bf16 v[112:115], a[88:91], v[64:67], v[112:115]
v_and_b32 v194, v214, v38
v_and_b32 v195, v215, v39
v_mfma_f32_16x16x32_bf16 v[116:119], v[64:67], a[92:95], v[116:119]
v_and_b32 v192, v214, v40
v_and_b32 v193, v215, v41
v_mfma_f32_16x16x32_bf16 v[108:111], a[96:99], v[68:71], v[108:111]
s_nop 0
v_mfma_f32_16x16x32_bf16 v[188:191], v[152:155], v[192:195], 0
v_mfma_f32_16x16x32_bf16 v[184:187], v[156:159], v[192:195], 0
v_fma_f32 v208, v84, v8, v120
v_fma_f32 v209, v85, v8, v121
v_mfma_f32_16x16x32_bf16 v[112:115], a[100:103], v[68:71], v[112:115]
v_fma_f32 v210, v86, v8, v122
v_fma_f32 v211, v87, v8, v123
v_mfma_f32_16x16x32_bf16 v[116:119], v[68:71], a[104:107], v[116:119]
v_cvt_pk_bf16_f32 v140, v208, v209
v_cvt_pk_bf16_f32 v141, v210, v211
v_mfma_f32_16x16x32_bf16 v[108:111], a[108:111], v[72:75], v[108:111]
s_waitcnt lgkmcnt(0)
v_mul_f32 v216, 0x3ae38e39, v216
v_fma_f32 v201, -v200, v200, v216
v_mfma_f32_16x16x32_bf16 v[112:115], a[112:115], v[72:75], v[112:115]
v_max_f32 v201, 0, v201
v_cvt_pk_bf16_f32 v201, v201, v201
v_mfma_f32_16x16x32_bf16 v[116:119], v[72:75], a[116:119], v[116:119]
global_load_lds_dwordx4 v12, s[42:43] offset:1024 sc1
s_add_u32 s42, s42, s46
s_addc_u32 s43, s43, s47
v_mfma_f32_16x16x32_bf16 v[108:111], a[120:123], v[76:79], v[108:111]
v_lshlrev_b32 v201, 16, v201
v_add_f32 v201, 0x3727c5ac, v201
v_mfma_f32_16x16x32_bf16 v[112:115], a[124:127], v[76:79], v[112:115]
v_cvt_pk_bf16_f32 v201, v201, v201
v_lshlrev_b32 v201, 16, v201
v_mfma_f32_16x16x32_bf16 v[116:119], v[76:79], a[128:131], v[116:119]
v_sqrt_f32 v201, v201
v_mfma_f32_16x16x32_bf16 v[108:111], a[132:135], v[80:83], v[108:111]
v_cvt_pk_bf16_f32 v201, v201, v201
v_lshlrev_b32 v201, 16, v201
v_mfma_f32_16x16x32_bf16 v[112:115], a[136:139], v[80:83], v[112:115]
v_rcp_f32 v216, v201
v_mfma_f32_16x16x32_bf16 v[116:119], v[80:83], a[140:143], v[116:119]
ds_write_b32 v22, v216 offset:128
v_mfma_f32_16x16x32_bf16 v[108:111], a[144:147], v[160:163], v[108:111]
v_cvt_pk_bf16_f32 v102, v188, v189
v_cvt_pk_bf16_f32 v103, v190, v191
v_mfma_f32_16x16x32_bf16 v[112:115], a[148:151], v[160:163], v[112:115]
ds_write_b64 v16, v[102:103]
v_mfma_f32_16x16x32_bf16 v[116:119], v[160:163], a[152:155], v[116:119]
v_cvt_pk_bf16_f32 v214, v186, v187
v_mfma_f32_16x16x32_bf16 v[108:111], a[156:159], v[164:167], v[108:111]
ds_write_b32 v17, v214
v_mfma_f32_16x16x32_bf16 v[112:115], a[160:163], v[164:167], v[112:115]
v_mfma_f32_16x16x32_bf16 v[116:119], v[164:167], a[164:167], v[116:119]
v_mfma_f32_16x16x32_bf16 v[108:111], a[168:171], v[168:171], v[108:111]
v_mfma_f32_16x16x32_bf16 v[112:115], a[172:175], v[168:171], v[112:115]
s_waitcnt vmcnt(5) lgkmcnt(0)
s_barrier
v_mfma_f32_16x16x32_bf16 v[116:119], v[168:171], a[176:179], v[116:119]
ds_read_b128 v[0:3], v49 offset:18432
v_mfma_f32_16x16x32_bf16 v[108:111], a[180:183], v[172:175], v[108:111]
ds_read_b128 v[4:7], v50 offset:18432
ds_read_b32 v8, v22 offset:0
v_mfma_f32_16x16x32_bf16 v[112:115], a[184:187], v[172:175], v[112:115]
s_mov_b64 exec, 1
global_store_dword v45, v46, s[38:39] sc1
s_mov_b64 exec, -1
v_mfma_f32_16x16x32_bf16 v[188:191], v[148:151], v[140:143], v[34:37]
v_mfma_f32_16x16x32_bf16 v[116:119], v[172:175], a[188:191], v[116:119]
ds_read_b128 v[94:97], v49 offset:20480
v_add_u32 v46, 1, v46
v_mfma_f32_16x16x32_bf16 v[108:111], a[192:195], v[176:179], v[108:111]
ds_read_b128 v[98:101], v50 offset:20480
ds_read_b32 v146, v19
v_mfma_f32_16x16x32_bf16 v[112:115], a[196:199], v[176:179], v[112:115]
ds_read_b32 v21, v22 offset:192
ds_read_b32 v93, v25
v_mfma_f32_16x16x32_bf16 v[116:119], v[176:179], a[200:203], v[116:119]
ds_read_b128 v[52:55], v49 offset:22528
ds_read_b32 v254, v26
v_mfma_f32_16x16x32_bf16 v[108:111], a[204:207], v[180:183], v[108:111]
ds_read_b128 v[56:59], v50 offset:22528
ds_read_b128 v[196:199], v23 offset:0
v_mfma_f32_16x16x32_bf16 v[112:115], a[208:211], v[180:183], v[112:115]
ds_read_b64 v[156:157], v20 offset:41472
ds_read_b32 v138, v18
v_mfma_f32_16x16x32_bf16 v[116:119], v[180:183], a[212:215], v[116:119]
s_waitcnt lgkmcnt(13)
ds_read_b128 v[60:63], v49 offset:24576
v_max_f32 v43, v188, v189
v_mfma_f32_16x16x32_bf16 v[132:135], v[104:107], v[0:3], 0
s_waitcnt lgkmcnt(13)
ds_read_b128 v[64:67], v50 offset:24576
v_max_f32 v44, v190, v191
v_mfma_f32_16x16x32_bf16 v[132:135], v[104:107], v[4:7], v[132:135]
v_mfma_f32_16x16x32_bf16 v[120:123], a[0:3], v[0:3], 0
s_waitcnt lgkmcnt(13)
ds_read_b128 v[68:71], v49 offset:26624
v_fma_f32 v208, v88, v8, v112
s_waitcnt lgkmcnt(13)
v_mfma_f32_16x16x32_bf16 v[132:135], v[104:107], v[94:97], v[132:135]
s_waitcnt lgkmcnt(12)
ds_read_b128 v[72:75], v50 offset:26624
v_fma_f32 v209, v89, v8, v113
v_mfma_f32_16x16x32_bf16 v[132:135], v[104:107], v[98:101], v[132:135]
v_fma_f32 v210, v90, v8, v114
v_fma_f32 v211, v91, v8, v115
v_mfma_f32_16x16x32_bf16 v[124:127], a[4:7], v[0:3], 0
s_waitcnt lgkmcnt(9)
ds_read_b128 v[76:79], v49 offset:28672
v_cvt_pk_bf16_f32 v144, v208, v209
v_mfma_f32_16x16x32_bf16 v[132:135], v[104:107], v[52:55], v[132:135]
s_waitcnt lgkmcnt(8)
ds_read_b128 v[80:83], v50 offset:28672
v_cvt_pk_bf16_f32 v145, v210, v211
v_mfma_f32_16x16x32_bf16 v[132:135], v[104:107], v[56:59], v[132:135]
s_nop 0
v_mfma_f32_16x16x32_bf16 v[184:187], v[144:147], v[140:143], v[204:207]
v_mul_f32 v21, 0x3eae1a93, v21
v_mul_f32 v255, v93, v21
v_mfma_f32_16x16x32_bf16 v[128:131], v[0:3], a[8:11], 0
s_waitcnt lgkmcnt(5)
ds_read_b128 v[160:163], v49 offset:30720
v_mul_f32 v42, v254, v21
v_mfma_f32_16x16x32_bf16 v[132:135], v[104:107], v[60:63], v[132:135]
s_waitcnt lgkmcnt(5)
ds_read_b128 v[164:167], v50 offset:30720
v_fma_f32 v208, v92, v196, v116
v_mfma_f32_16x16x32_bf16 v[132:135], v[104:107], v[64:67], v[132:135]
v_fma_f32 v209, v92, v197, v117
v_fma_f32 v210, v92, v198, v118
v_mfma_f32_16x16x32_bf16 v[120:123], a[12:15], v[4:7], v[120:123]
s_waitcnt lgkmcnt(5)
ds_read_b128 v[168:171], v49 offset:32768
v_max3_f32 v43, v184, v185, v43
v_mfma_f32_16x16x32_bf16 v[132:135], v[104:107], v[68:71], v[132:135]
s_add_u32 m0, s49, 18432
ds_read_b128 v[172:175], v50 offset:32768
s_waitcnt lgkmcnt(6)
v_mfma_f32_16x16x32_bf16 v[132:135], v[104:107], v[72:75], v[132:135]
v_max3_f32 v44, v186, v187, v44
v_mul_f32 v43, v43, v255
v_mfma_f32_16x16x32_bf16 v[124:127], a[16:19], v[4:7], v[124:127]
s_waitcnt lgkmcnt(5)
ds_read_b128 v[176:179], v49 offset:34816
v_mul_f32 v44, v44, v42
v_mfma_f32_16x16x32_bf16 v[132:135], v[104:107], v[76:79], v[132:135]
s_waitcnt lgkmcnt(5)
ds_read_b128 v[180:183], v50 offset:34816
v_max_f32 v212, v43, v44
v_mfma_f32_16x16x32_bf16 v[132:135], v[104:107], v[80:83], v[132:135]
v_mov_b32 v213, v212
v_fma_f32 v211, v92, v199, v119
v_mfma_f32_16x16x32_bf16 v[128:131], v[4:7], a[20:23], v[128:131]
s_waitcnt lgkmcnt(5)
v_permlane16_swap_b32 v212, v213
v_mfma_f32_16x16x32_bf16 v[132:135], v[104:107], v[160:163], v[132:135]
s_waitcnt lgkmcnt(4)
v_max_f32 v212, v212, v213
v_mov_b32 v213, v212
v_mfma_f32_16x16x32_bf16 v[132:135], v[104:107], v[164:167], v[132:135]
global_load_lds_dwordx4 v9, s[42:43] offset:-2048 sc1
v_cvt_pk_bf16_f32 v152, v208, v209
v_mfma_f32_16x16x32_bf16 v[120:123], a[24:27], v[94:97], v[120:123]
s_waitcnt lgkmcnt(3)
v_permlane32_swap_b32 v212, v213
v_mfma_f32_16x16x32_bf16 v[132:135], v[104:107], v[168:171], v[132:135]
s_waitcnt lgkmcnt(2)
v_max_f32 v212, v212, v213
v_sub_f32 v43, v43, v212
v_mfma_f32_16x16x32_bf16 v[132:135], v[104:107], v[172:175], v[132:135]
v_sub_f32 v44, v44, v212
v_cvt_pk_bf16_f32 v153, v210, v211
v_mfma_f32_16x16x32_bf16 v[124:127], a[28:31], v[94:97], v[124:127]
s_waitcnt lgkmcnt(1)
v_exp_f32 v214, v43
v_mfma_f32_16x16x32_bf16 v[132:135], v[104:107], v[176:179], v[132:135]
s_waitcnt lgkmcnt(0)
v_exp_f32 v215, v44
v_mfma_f32_16x16x32_bf16 v[132:135], v[104:107], v[180:183], v[132:135]
v_add_f32 v212, v214, v215
v_mov_b32 v213, v212
v_mfma_f32_16x16x32_bf16 v[128:131], v[94:97], a[32:35], v[128:131]
v_mul_f32 v214, v214, v93
v_fma_f32 v208, v84, v8, v108
v_mfma_f32_16x16x32_bf16 v[120:123], a[36:39], v[98:101], v[120:123]
global_load_lds_dwordx4 v10, s[42:43] offset:-1024 sc1
v_fma_f32 v209, v85, v8, v109
v_mfma_f32_16x16x32_bf16 v[124:127], a[40:43], v[98:101], v[124:127]
v_mul_f32 v200, 0x3ae38e39, v132
v_cvt_pk_bf16_f32 v216, -v200, -v200
v_mfma_f32_16x16x32_bf16 v[128:131], v[98:101], a[44:47], v[128:131]
v_lshlrev_b32 v216, 16, v216
v_fma_f32 v210, v86, v8, v110
v_mfma_f32_16x16x32_bf16 v[120:123], a[48:51], v[52:55], v[120:123]
ds_write_b32 v22, v216 offset:64
v_mfma_f32_16x16x32_bf16 v[124:127], a[52:55], v[52:55], v[124:127]
v_permlane16_swap_b32 v212, v213
v_mfma_f32_16x16x32_bf16 v[128:131], v[52:55], a[56:59], v[128:131]
v_add_f32 v212, v212, v213
v_mov_b32 v213, v212
v_mfma_f32_16x16x32_bf16 v[120:123], a[60:63], v[56:59], v[120:123]
v_mul_f32 v215, v215, v254
v_fma_f32 v211, v87, v8, v111
v_mfma_f32_16x16x32_bf16 v[124:127], a[64:67], v[56:59], v[124:127]
v_permlane32_swap_b32 v212, v213
v_mfma_f32_16x16x32_bf16 v[128:131], v[56:59], a[68:71], v[128:131]
global_load_lds_dwordx4 v11, s[42:43] offset:0 sc1
v_add_f32 v212, v212, v213
v_mfma_f32_16x16x32_bf16 v[120:123], a[72:75], v[60:63], v[120:123]
v_rcp_f32 v212, v212
v_mfma_f32_16x16x32_bf16 v[124:127], a[76:79], v[60:63], v[124:127]
v_mfma_f32_16x16x32_bf16 v[128:131], v[60:63], a[80:83], v[128:131]
s_waitcnt lgkmcnt(0)
s_barrier
v_mfma_f32_16x16x32_bf16 v[120:123], a[84:87], v[64:67], v[120:123]
ds_read_b32 v216, v24
v_mul_f32 v214, v214, v212
v_mfma_f32_16x16x32_bf16 v[124:127], a[88:91], v[64:67], v[124:127]
v_mul_f32 v215, v215, v212
v_cvt_pk_bf16_f32 v214, v214, v214
v_mfma_f32_16x16x32_bf16 v[128:131], v[64:67], a[92:95], v[128:131]
v_cvt_pk_bf16_f32 v215, v215, v215
v_and_b32 v192, v214, v38
v_mfma_f32_16x16x32_bf16 v[120:123], a[96:99], v[68:71], v[120:123]
v_and_b32 v193, v215, v39
v_and_b32 v194, v214, v40
v_mfma_f32_16x16x32_bf16 v[124:127], a[100:103], v[68:71], v[124:127]
global_load_lds_dwordx4 v12, s[42:43] offset:1024 sc1
s_sub_u32 s42, s42, 18432
s_subb_u32 s43, s43, 0
v_mfma_f32_16x16x32_bf16 v[128:131], v[68:71], a[104:107], v[128:131]
v_and_b32 v195, v215, v41
v_cvt_pk_bf16_f32 v136, v208, v209
v_mfma_f32_16x16x32_bf16 v[120:123], a[108:111], v[72:75], v[120:123]
v_mfma_f32_16x16x32_bf16 v[188:191], v[152:155], v[192:195], 0
v_mfma_f32_16x16x32_bf16 v[184:187], v[156:159], v[192:195], 0
s_waitcnt lgkmcnt(0)
v_mul_f32 v216, 0x3ae38e39, v216
v_fma_f32 v201, -v200, v200, v216
v_mfma_f32_16x16x32_bf16 v[124:127], a[112:115], v[72:75], v[124:127]
v_max_f32 v201, 0, v201
v_cvt_pk_bf16_f32 v201, v201, v201
v_mfma_f32_16x16x32_bf16 v[128:131], v[72:75], a[116:119], v[128:131]
v_lshlrev_b32 v201, 16, v201
v_add_f32 v201, 0x3727c5ac, v201
v_mfma_f32_16x16x32_bf16 v[120:123], a[120:123], v[76:79], v[120:123]
v_cvt_pk_bf16_f32 v201, v201, v201
v_lshlrev_b32 v201, 16, v201
v_mfma_f32_16x16x32_bf16 v[124:127], a[124:127], v[76:79], v[124:127]
v_sqrt_f32 v201, v201
v_mfma_f32_16x16x32_bf16 v[128:131], v[76:79], a[128:131], v[128:131]
v_cvt_pk_bf16_f32 v201, v201, v201
v_lshlrev_b32 v201, 16, v201
v_mfma_f32_16x16x32_bf16 v[120:123], a[132:135], v[80:83], v[120:123]
v_rcp_f32 v216, v201
v_mfma_f32_16x16x32_bf16 v[124:127], a[136:139], v[80:83], v[124:127]
ds_write_b32 v22, v216 offset:192
)"
R"(v_mfma_f32_16x16x32_bf16 v[128:131], v[80:83], a[140:143], v[128:131]
v_cvt_pk_bf16_f32 v102, v188, v189
v_cvt_pk_bf16_f32 v103, v190, v191
v_mfma_f32_16x16x32_bf16 v[120:123], a[144:147], v[160:163], v[120:123]
ds_write_b64 v16, v[102:103]
v_mfma_f32_16x16x32_bf16 v[124:127], a[148:151], v[160:163], v[124:127]
v_cvt_pk_bf16_f32 v214, v186, v187
v_cvt_pk_bf16_f32 v137, v210, v211
v_mfma_f32_16x16x32_bf16 v[128:131], v[160:163], a[152:155], v[128:131]
ds_write_b32 v17, v214
v_mfma_f32_16x16x32_bf16 v[120:123], a[156:159], v[164:167], v[120:123]
v_mfma_f32_16x16x32_bf16 v[124:127], a[160:163], v[164:167], v[124:127]
v_xor_b32 v49, 0x10000, v49
v_xor_b32 v50, 0x10000, v50
v_mfma_f32_16x16x32_bf16 v[128:131], v[164:167], a[164:167], v[128:131]
v_mfma_f32_16x16x32_bf16 v[120:123], a[168:171], v[168:171], v[120:123]
v_mfma_f32_16x16x32_bf16 v[124:127], a[172:175], v[168:171], v[124:127]
s_waitcnt vmcnt(5) lgkmcnt(0)
s_barrier
.Lqa_in42:
v_mfma_f32_16x16x32_bf16 v[128:131], v[168:171], a[176:179], v[128:131]
s_cmp_eq_u32 s70, s73
s_cbranch_scc1 .Lqa_hk43
.Lqa_hr44:
ds_read_b128 v[0:3], v49 offset:0
v_mfma_f32_16x16x32_bf16 v[120:123], a[180:183], v[172:175], v[120:123]
ds_read_b128 v[4:7], v50 offset:0
ds_read_b32 v8, v22 offset:64
v_mfma_f32_16x16x32_bf16 v[124:127], a[184:187], v[172:175], v[124:127]
s_mov_b64 exec, 1
global_store_dword v45, v46, s[38:39] sc1
s_mov_b64 exec, -1
v_mfma_f32_16x16x32_bf16 v[188:191], v[144:147], v[136:139], v[34:37]
v_mfma_f32_16x16x32_bf16 v[128:131], v[172:175], a[188:191], v[128:131]
ds_read_b128 v[94:97], v49 offset:2048
v_add_u32 v46, 1, v46
v_mfma_f32_16x16x32_bf16 v[120:123], a[192:195], v[176:179], v[120:123]
ds_read_b128 v[98:101], v50 offset:2048
ds_read_b32 v150, v19
v_mfma_f32_16x16x32_bf16 v[124:127], a[196:199], v[176:179], v[124:127]
ds_read_b32 v21, v22 offset:128
ds_read_b32 v93, v27
v_mfma_f32_16x16x32_bf16 v[128:131], v[176:179], a[200:203], v[128:131]
ds_read_b128 v[52:55], v49 offset:4096
ds_read_b32 v254, v28
v_mfma_f32_16x16x32_bf16 v[120:123], a[204:207], v[180:183], v[120:123]
ds_read_b128 v[56:59], v50 offset:4096
ds_read_b128 v[196:199], v23 offset:64
v_mfma_f32_16x16x32_bf16 v[124:127], a[208:211], v[180:183], v[124:127]
v_mfma_f32_16x16x32_bf16 v[128:131], v[180:183], a[212:215], v[128:131]
s_sub_u32 s70, s70, 1
ds_read_b128 v[60:63], v49 offset:6144
s_cbranch_scc0 .Lqa_top52
s_waitcnt lgkmcnt(0)
ds_read_b64 v[158:159], v20 offset:41472
ds_read_b32 v142, v18
v_max_f32 v43, v188, v189
v_fma_f32 v208, v88, v8, v124
v_fma_f32 v209, v89, v8, v125
v_fma_f32 v210, v90, v8, v126
v_fma_f32 v211, v91, v8, v127
v_cvt_pk_bf16_f32 v148, v208, v209
v_cvt_pk_bf16_f32 v149, v210, v211
s_nop 1
v_mfma_f32_16x16x32_bf16 v[184:187], v[148:151], v[136:139], v[30:33]
s_waitcnt lgkmcnt(0)
v_max_f32 v44, v190, v191
v_fma_f32 v208, v92, v196, v128
v_fma_f32 v209, v92, v197, v129
v_mul_f32 v21, 0x3eae1a93, v21
v_mul_f32 v255, v93, v21
v_mul_f32 v42, v254, v21
s_nop 0
v_max3_f32 v43, v184, v185, v43
v_max3_f32 v44, v186, v187, v44
v_mul_f32 v43, v43, v255
v_mul_f32 v44, v44, v42
v_max_f32 v212, v43, v44
v_mov_b32 v213, v212
v_fma_f32 v210, v92, v198, v130
s_nop 0
v_permlane16_swap_b32 v212, v213
v_max_f32 v212, v212, v213
v_mov_b32 v213, v212
s_nop 1
v_permlane32_swap_b32 v212, v213
v_max_f32 v212, v212, v213
v_sub_f32 v43, v43, v212
v_sub_f32 v44, v44, v212
v_exp_f32 v214, v43
v_exp_f32 v215, v44
s_nop 0
v_add_f32 v212, v214, v215
v_mov_b32 v213, v212
v_mul_f32 v214, v214, v93
v_fma_f32 v211, v92, v199, v131
v_permlane16_swap_b32 v212, v213
v_add_f32 v212, v212, v213
v_mov_b32 v213, v212
v_mul_f32 v215, v215, v254
v_cvt_pk_bf16_f32 v154, v208, v209
v_permlane32_swap_b32 v212, v213
v_add_f32 v212, v212, v213
v_cvt_pk_bf16_f32 v155, v210, v211
v_rcp_f32 v212, v212
s_nop 0
v_mul_f32 v214, v214, v212
v_mul_f32 v215, v215, v212
v_cvt_pk_bf16_f32 v214, v214, v214
v_cvt_pk_bf16_f32 v215, v215, v215
s_barrier
v_and_b32 v194, v214, v38
v_and_b32 v195, v215, v39
v_and_b32 v192, v214, v40
v_and_b32 v193, v215, v41
s_nop 1
v_mfma_f32_16x16x32_bf16 v[188:191], v[152:155], v[192:195], 0
v_mfma_f32_16x16x32_bf16 v[184:187], v[156:159], v[192:195], 0
v_fma_f32 v208, v84, v8, v120
v_fma_f32 v209, v85, v8, v121
v_fma_f32 v210, v86, v8, v122
v_fma_f32 v211, v87, v8, v123
v_cvt_pk_bf16_f32 v140, v208, v209
v_cvt_pk_bf16_f32 v141, v210, v211
s_nop 0
v_cvt_pk_bf16_f32 v102, v188, v189
v_cvt_pk_bf16_f32 v103, v190, v191
ds_write_b64 v16, v[102:103]
v_cvt_pk_bf16_f32 v214, v186, v187
ds_write_b32 v17, v214
s_waitcnt vmcnt(0)
s_waitcnt lgkmcnt(0)
s_barrier
s_mov_b64 exec, 1
global_store_dword v45, v46, s[38:39] sc1
s_mov_b64 exec, -1
v_add_u32 v46, 8, v46
s_waitcnt vmcnt(1)
s_barrier
s_mov_b64 exec, 1
global_store_dword v45, v46, s[38:39] sc1
s_mov_b64 exec, -1
s_cmp_lg_u32 s53, 0
s_cbranch_scc1 .Lqa_n53
global_load_dword v51, v47, s[38:39] sc1
s_waitcnt vmcnt(0)
v_sub_u32 v51, v51, v48
v_cmp_lt_u32 vcc, 0x7f, v51
s_cbranch_vccnz .Lqa_sp54
.Lqa_rt55:
v_add_u32 v47, 102400, v47
global_load_dword v51, v47, s[38:39] sc1
s_waitcnt vmcnt(0)
v_sub_u32 v51, v51, v48
v_cmp_lt_u32 vcc, 0x7f, v51
s_cbranch_vccnz .Lqa_sp56
.Lqa_rt57:
v_add_u32 v47, 102400, v47
global_load_dword v51, v47, s[38:39] sc1
s_waitcnt vmcnt(0)
v_sub_u32 v51, v51, v48
v_cmp_lt_u32 vcc, 0x7f, v51
s_cbranch_vccnz .Lqa_sp58
.Lqa_rt59:
v_add_u32 v47, 102400, v47
global_load_dword v51, v47, s[38:39] sc1
s_waitcnt vmcnt(0)
v_sub_u32 v51, v51, v48
v_cmp_lt_u32 vcc, 0x7f, v51
s_cbranch_vccnz .Lqa_sp60
.Lqa_rt61:
v_add_u32 v47, 102400, v47
s_load_dword s75, s[38:39], 0x0
s_waitcnt vmcnt(0) lgkmcnt(0)
s_add_u32 s75, s75, 1
v_mov_b32 v51, s75
v_mov_b32 v47, 0
global_store_dword v47, v51, s[38:39] sc1
.Lqa_n53:
s_endpgm
.Lqa_hk6:
s_cmp_lg_u32 s70, 18
s_cbranch_scc1 .Lqa_e8
s_add_u32 s42, s42, s50
s_addc_u32 s43, s43, 0
s_mov_b64 s[76:77], -1
s_mov_b64 s[46:47], s[44:45]
s_mov_b32 s73, 17
s_nop 1
s_branch .Lqa_hr7
.Lqa_e8:
s_cmp_lg_u32 s70, 17
s_cbranch_scc1 .Lqa_e9
s_mov_b32 s46, -18432
s_mov_b32 s47, -1
v_or_b32 v232, s52, v232
v_or_b32 v233, s52, v233
v_or_b32 v234, s52, v234
v_or_b32 v235, s52, v235
s_mov_b32 s73, 16
s_nop 1
s_branch .Lqa_hr7
.Lqa_e9:
s_cmp_lg_u32 s70, 16
s_cbranch_scc1 .Lqa_e10
v_mov_b32 v232, v30
v_mov_b32 v233, v31
v_mov_b32 v234, v32
v_mov_b32 v235, v33
s_mov_b32 s73, 15
s_nop 1
s_branch .Lqa_hr7
.Lqa_e10:
s_cmp_lg_u32 s70, 15
s_cbranch_scc1 .Lqa_e11
v_subrev_u32 v14, 18432, v14
s_mov_b32 s73, 3
s_nop 1
s_branch .Lqa_hr7
.Lqa_e11:
s_cmp_lg_u32 s70, 3
s_cbranch_scc1 .Lqa_e12
s_mov_b32 s73, 2
s_nop 1
s_branch .Lqa_hr7
.Lqa_e12:
s_cmp_lg_u32 s70, 2
s_cbranch_scc1 .Lqa_e13
s_add_u32 s46, s46, s50
s_addc_u32 s47, s47, 0
s_mov_b32 s73, 0
s_nop 1
s_branch .Lqa_hr7
.Lqa_e13:
s_cmp_lg_u32 s70, 0
s_cbranch_scc1 .Lqa_e14
v_or_b32 v30, s52, v30
v_or_b32 v31, s52, v31
v_or_b32 v32, s52, v32
v_or_b32 v33, s52, v33
s_mov_b32 s73, 999
s_nop 1
s_branch .Lqa_hr7
.Lqa_e14:
s_branch .Lqa_hr7
.Lqa_hk17:
s_cmp_lg_u32 s70, 18
s_cbranch_scc1 .Lqa_e19
s_add_u32 s42, s42, s50
s_addc_u32 s43, s43, 0
s_mov_b64 s[76:77], -1
s_mov_b64 s[46:47], s[44:45]
s_mov_b32 s73, 17
s_nop 1
s_branch .Lqa_hr18
.Lqa_e19:
s_cmp_lg_u32 s70, 17
s_cbranch_scc1 .Lqa_e20
s_mov_b32 s46, -18432
s_mov_b32 s47, -1
v_or_b32 v232, s52, v232
v_or_b32 v233, s52, v233
v_or_b32 v234, s52, v234
v_or_b32 v235, s52, v235
s_mov_b32 s73, 16
s_nop 1
s_branch .Lqa_hr18
.Lqa_e20:
s_cmp_lg_u32 s70, 16
s_cbranch_scc1 .Lqa_e21
v_mov_b32 v232, v30
v_mov_b32 v233, v31
v_mov_b32 v234, v32
v_mov_b32 v235, v33
s_mov_b32 s73, 15
s_nop 1
s_branch .Lqa_hr18
.Lqa_e21:
s_cmp_lg_u32 s70, 15
s_cbranch_scc1 .Lqa_e22
v_subrev_u32 v14, 18432, v14
s_mov_b32 s73, 3
s_nop 1
s_branch .Lqa_hr18
.Lqa_e22:
s_cmp_lg_u32 s70, 3
s_cbranch_scc1 .Lqa_e23
s_mov_b32 s73, 2
s_nop 1
s_branch .Lqa_hr18
.Lqa_e23:
s_cmp_lg_u32 s70, 2
s_cbranch_scc1 .Lqa_e24
s_add_u32 s46, s46, s50
s_addc_u32 s47, s47, 0
s_mov_b32 s73, 0
s_nop 1
s_branch .Lqa_hr18
.Lqa_e24:
s_cmp_lg_u32 s70, 0
s_cbranch_scc1 .Lqa_e25
v_or_b32 v30, s52, v30
v_or_b32 v31, s52, v31
v_or_b32 v32, s52, v32
v_or_b32 v33, s52, v33
s_mov_b32 s73, 999
s_nop 1
s_branch .Lqa_hr18
.Lqa_e25:
s_branch .Lqa_hr18
.Lqa_sp30:
s_sleep 1
global_load_dword v51, v42, s[38:39] sc1
s_add_u32 s71, s71, 1
s_waitcnt vmcnt(0)
v_sub_u32 v51, v51, v44
v_cmp_lt_u32 vcc, 0x7f, v51
s_and_b64 vcc, vcc, s[76:77]
s_cbranch_vccz .Lqa_rt31
s_cmp_lt_u32 s71, 3000000
s_cbranch_scc1 .Lqa_sp30
s_branch .Lqa_rt31
.Lqa_sp32:
s_sleep 1
global_load_dword v254, v42, s[38:39] sc1
s_add_u32 s71, s71, 1
s_waitcnt vmcnt(0)
v_sub_u32 v254, v254, v44
v_cmp_lt_u32 vcc, 0x7f, v254
s_and_b64 vcc, vcc, s[76:77]
s_cbranch_vccz .Lqa_rt33
s_cmp_lt_u32 s71, 3000000
s_cbranch_scc1 .Lqa_sp32
s_branch .Lqa_rt33
.Lqa_hk28:
s_cmp_lg_u32 s70, 18
s_cbranch_scc1 .Lqa_e34
s_add_u32 s42, s42, s50
s_addc_u32 s43, s43, 0
s_mov_b64 s[76:77], -1
s_mov_b64 s[46:47], s[44:45]
s_mov_b32 s73, 17
s_nop 1
s_branch .Lqa_hr29
.Lqa_e34:
s_cmp_lg_u32 s70, 17
s_cbranch_scc1 .Lqa_e35
s_mov_b32 s46, -18432
s_mov_b32 s47, -1
v_or_b32 v200, s52, v200
v_or_b32 v201, s52, v201
v_or_b32 v202, s52, v202
v_or_b32 v203, s52, v203
s_mov_b32 s73, 16
s_nop 1
s_branch .Lqa_hr29
.Lqa_e35:
s_cmp_lg_u32 s70, 16
s_cbranch_scc1 .Lqa_e36
v_mov_b32 v200, v30
v_mov_b32 v201, v31
v_mov_b32 v202, v32
v_mov_b32 v203, v33
s_mov_b32 s73, 15
s_nop 1
s_branch .Lqa_hr29
.Lqa_e36:
s_cmp_lg_u32 s70, 15
s_cbranch_scc1 .Lqa_e37
v_subrev_u32 v14, 18432, v14
s_mov_b32 s73, 3
s_nop 1
s_branch .Lqa_hr29
.Lqa_e37:
s_cmp_lg_u32 s70, 3
s_cbranch_scc1 .Lqa_e38
v_add_u32 v43, s51, v43
s_mov_b32 s73, 2
s_nop 1
s_branch .Lqa_hr29
.Lqa_e38:
s_cmp_lg_u32 s70, 2
s_cbranch_scc1 .Lqa_e39
v_add_u32 v42, s51, v42
v_add_u32 v44, -32, v44
s_add_u32 s46, s46, s50
s_addc_u32 s47, s47, 0
s_mov_b32 s73, 0
s_nop 1
s_branch .Lqa_hr29
.Lqa_e39:
s_cmp_lg_u32 s70, 0
s_cbranch_scc1 .Lqa_e40
v_or_b32 v30, s52, v30
v_or_b32 v31, s52, v31
v_or_b32 v32, s52, v32
v_or_b32 v33, s52, v33
s_mov_b32 s73, 999
s_nop 1
s_branch .Lqa_hr29
)"
R"(.Lqa_e40:
s_branch .Lqa_hr29
.Lqa_hk43:
s_cmp_lg_u32 s70, 18
s_cbranch_scc1 .Lqa_e45
s_add_u32 s42, s42, s50
s_addc_u32 s43, s43, 0
s_mov_b64 s[76:77], -1
s_mov_b64 s[46:47], s[44:45]
s_mov_b32 s73, 17
s_nop 1
s_branch .Lqa_hr44
.Lqa_e45:
s_cmp_lg_u32 s70, 17
s_cbranch_scc1 .Lqa_e46
s_mov_b32 s46, -18432
s_mov_b32 s47, -1
v_or_b32 v204, s52, v204
v_or_b32 v205, s52, v205
v_or_b32 v206, s52, v206
v_or_b32 v207, s52, v207
s_mov_b32 s73, 16
s_nop 1
s_branch .Lqa_hr44
.Lqa_e46:
s_cmp_lg_u32 s70, 16
s_cbranch_scc1 .Lqa_e47
v_mov_b32 v204, v30
v_mov_b32 v205, v31
v_mov_b32 v206, v32
v_mov_b32 v207, v33
s_mov_b32 s73, 15
s_nop 1
s_branch .Lqa_hr44
.Lqa_e47:
s_cmp_lg_u32 s70, 15
s_cbranch_scc1 .Lqa_e48
v_add_u32 v46, -1, v46
s_mov_b32 s73, 3
s_nop 1
s_branch .Lqa_hr44
.Lqa_e48:
s_cmp_lg_u32 s70, 3
s_cbranch_scc1 .Lqa_e49
s_mov_b32 s73, 2
s_nop 1
s_branch .Lqa_hr44
.Lqa_e49:
s_cmp_lg_u32 s70, 2
s_cbranch_scc1 .Lqa_e50
s_add_u32 s46, s46, s50
s_addc_u32 s47, s47, 0
s_mov_b32 s73, 0
s_nop 1
s_branch .Lqa_hr44
.Lqa_e50:
s_cmp_lg_u32 s70, 0
s_cbranch_scc1 .Lqa_e51
v_or_b32 v30, s52, v30
v_or_b32 v31, s52, v31
v_or_b32 v32, s52, v32
v_or_b32 v33, s52, v33
s_mov_b32 s73, 999
s_nop 1
s_branch .Lqa_hr44
.Lqa_e51:
s_branch .Lqa_hr44
.Lqa_sp54:
s_sleep 1
global_load_dword v51, v47, s[38:39] sc1
s_add_u32 s71, s71, 1
s_waitcnt vmcnt(0)
v_sub_u32 v51, v51, v48
v_cmp_lt_u32 vcc, 0x7f, v51
s_cbranch_vccz .Lqa_rt55
s_cmp_lt_u32 s71, 3000000
s_cbranch_scc1 .Lqa_sp54
s_branch .Lqa_rt55
.Lqa_sp56:
s_sleep 1
global_load_dword v51, v47, s[38:39] sc1
s_add_u32 s71, s71, 1
s_waitcnt vmcnt(0)
v_sub_u32 v51, v51, v48
v_cmp_lt_u32 vcc, 0x7f, v51
s_cbranch_vccz .Lqa_rt57
s_cmp_lt_u32 s71, 3000000
s_cbranch_scc1 .Lqa_sp56
s_branch .Lqa_rt57
.Lqa_sp58:
s_sleep 1
global_load_dword v51, v47, s[38:39] sc1
s_add_u32 s71, s71, 1
s_waitcnt vmcnt(0)
v_sub_u32 v51, v51, v48
v_cmp_lt_u32 vcc, 0x7f, v51
s_cbranch_vccz .Lqa_rt59
s_cmp_lt_u32 s71, 3000000
s_cbranch_scc1 .Lqa_sp58
s_branch .Lqa_rt59
.Lqa_sp60:
s_sleep 1
global_load_dword v51, v47, s[38:39] sc1
s_add_u32 s71, s71, 1
s_waitcnt vmcnt(0)
v_sub_u32 v51, v51, v48
v_cmp_lt_u32 vcc, 0x7f, v51
s_cbranch_vccz .Lqa_rt61
s_cmp_lt_u32 s71, 3000000
s_cbranch_scc1 .Lqa_sp60
s_branch .Lqa_rt61
.Lrole1:
s_and_b32 s75, s36, 3
s_cmp_eq_u32 s75, 1
s_cbranch_scc1 .Lwo_w2
s_cmp_eq_u32 s75, 2
s_cbranch_scc1 .Lwo_w3
s_cmp_eq_u32 s75, 3
s_cbranch_scc1 .Lwo_w4
.Lwo_w1:
s_add_u32 m0, s48, 4096
s_nop 0
global_load_lds_dwordx4 v0, s[40:41] offset:-4096
global_load_lds_dwordx4 v1, s[40:41] offset:-3072
global_load_lds_dwordx4 v2, s[40:41] offset:-2048
global_load_lds_dwordx4 v3, s[40:41] offset:-1024
global_load_lds_dwordx4 v4, s[40:41] offset:0
global_load_lds_dwordx4 v5, s[40:41] offset:1024
global_load_lds_dwordx4 v6, s[40:41] offset:2048
global_load_lds_dwordx4 v7, s[40:41] offset:3072
s_add_u32 m0, s48, 12288
s_nop 0
global_load_lds_dwordx4 v8, s[40:41] offset:-4096
s_add_u32 s40, s40, 73728
s_addc_u32 s41, s41, 0
s_add_u32 m0, s48, 40960
s_nop 0
global_load_lds_dwordx4 v0, s[40:41] offset:-4096
global_load_lds_dwordx4 v1, s[40:41] offset:-3072
global_load_lds_dwordx4 v2, s[40:41] offset:-2048
global_load_lds_dwordx4 v3, s[40:41] offset:-1024
global_load_lds_dwordx4 v4, s[40:41] offset:0
global_load_lds_dwordx4 v5, s[40:41] offset:1024
global_load_lds_dwordx4 v6, s[40:41] offset:2048
global_load_lds_dwordx4 v7, s[40:41] offset:3072
s_add_u32 m0, s48, 49152
s_nop 0
global_load_lds_dwordx4 v8, s[40:41] offset:-4096
s_add_u32 s40, s40, 73728
s_addc_u32 s41, s41, 0
s_add_u32 m0, s48, 77824
s_nop 0
global_load_lds_dwordx4 v0, s[40:41] offset:-4096
global_load_lds_dwordx4 v1, s[40:41] offset:-3072
global_load_lds_dwordx4 v2, s[40:41] offset:-2048
global_load_lds_dwordx4 v3, s[40:41] offset:-1024
global_load_lds_dwordx4 v4, s[40:41] offset:0
global_load_lds_dwordx4 v5, s[40:41] offset:1024
global_load_lds_dwordx4 v6, s[40:41] offset:2048
global_load_lds_dwordx4 v7, s[40:41] offset:3072
s_add_u32 m0, s48, 86016
s_nop 0
global_load_lds_dwordx4 v8, s[40:41] offset:-4096
s_add_u32 s40, s40, 73728
s_addc_u32 s41, s41, 0
s_add_u32 m0, s48, 114688
s_nop 0
global_load_lds_dwordx4 v0, s[40:41] offset:-4096
global_load_lds_dwordx4 v1, s[40:41] offset:-3072
global_load_lds_dwordx4 v2, s[40:41] offset:-2048
global_load_lds_dwordx4 v3, s[40:41] offset:-1024
global_load_lds_dwordx4 v4, s[40:41] offset:0
global_load_lds_dwordx4 v5, s[40:41] offset:1024
global_load_lds_dwordx4 v6, s[40:41] offset:2048
global_load_lds_dwordx4 v7, s[40:41] offset:3072
s_add_u32 m0, s48, 122880
s_nop 0
global_load_lds_dwordx4 v8, s[40:41] offset:-4096
s_add_u32 s40, s40, 73728
s_addc_u32 s41, s41, 0
v_add_u32 v204, 0x10000, v9
v_add_u32 v205, 0x20000, v9
v_add_u32 v206, 0x10000, v10
v_add_u32 v207, 0x20000, v10
s_waitcnt lgkmcnt(0)
s_mul_i32 s37, s37, 0x9E3779B1
s_add_u32 s37, s37, 0x7F4A7C15
v_add_u32 v22, s37, v22
v_add_u32 v24, s37, v24
s_waitcnt vmcnt(27)
s_barrier
ds_read_b64_tr_b16 a[0:1], v9 offset:0
ds_read_b64_tr_b16 a[2:3], v9 offset:2304
ds_read_b64_tr_b16 a[4:5], v9 offset:8
ds_read_b64_tr_b16 a[6:7], v9 offset:2312
ds_read_b64_tr_b16 a[8:9], v9 offset:64
ds_read_b64_tr_b16 a[10:11], v9 offset:2368
ds_read_b64_tr_b16 a[12:13], v9 offset:72
ds_read_b64_tr_b16 a[14:15], v9 offset:2376
ds_read_b64_tr_b16 v[60:61], v10 offset:0
ds_read_b64_tr_b16 v[62:63], v10 offset:2304
ds_read_b64_tr_b16 a[16:17], v9 offset:18432
ds_read_b64_tr_b16 a[18:19], v9 offset:20736
ds_read_b64_tr_b16 a[20:21], v9 offset:18440
ds_read_b64_tr_b16 a[22:23], v9 offset:20744
ds_read_b64_tr_b16 a[24:25], v9 offset:18496
ds_read_b64_tr_b16 a[26:27], v9 offset:20800
ds_read_b64_tr_b16 a[28:29], v9 offset:18504
ds_read_b64_tr_b16 a[30:31], v9 offset:20808
ds_read_b64_tr_b16 v[64:65], v10 offset:18432
ds_read_b64_tr_b16 v[66:67], v10 offset:20736
s_waitcnt lgkmcnt(0)
s_barrier
s_add_u32 m0, s48, 4096
s_nop 0
global_load_lds_dwordx4 v0, s[40:41] offset:-4096
global_load_lds_dwordx4 v1, s[40:41] offset:-3072
global_load_lds_dwordx4 v2, s[40:41] offset:-2048
global_load_lds_dwordx4 v3, s[40:41] offset:-1024
global_load_lds_dwordx4 v4, s[40:41] offset:0
global_load_lds_dwordx4 v5, s[40:41] offset:1024
global_load_lds_dwordx4 v6, s[40:41] offset:2048
global_load_lds_dwordx4 v7, s[40:41] offset:3072
s_add_u32 m0, s48, 12288
s_nop 0
global_load_lds_dwordx4 v8, s[40:41] offset:-4096
s_add_u32 s40, s40, 73728
s_addc_u32 s41, s41, 0
s_waitcnt vmcnt(27)
s_barrier
ds_read_b64_tr_b16 a[32:33], v9 offset:36864
ds_read_b64_tr_b16 a[34:35], v9 offset:39168
ds_read_b64_tr_b16 a[36:37], v9 offset:36872
ds_read_b64_tr_b16 a[38:39], v9 offset:39176
ds_read_b64_tr_b16 a[40:41], v9 offset:36928
ds_read_b64_tr_b16 a[42:43], v9 offset:39232
ds_read_b64_tr_b16 a[44:45], v9 offset:36936
ds_read_b64_tr_b16 a[46:47], v9 offset:39240
ds_read_b64_tr_b16 v[68:69], v10 offset:36864
ds_read_b64_tr_b16 v[70:71], v10 offset:39168
ds_read_b64_tr_b16 a[48:49], v9 offset:55296
ds_read_b64_tr_b16 a[50:51], v9 offset:57600
ds_read_b64_tr_b16 a[52:53], v9 offset:55304
ds_read_b64_tr_b16 a[54:55], v9 offset:57608
ds_read_b64_tr_b16 a[56:57], v9 offset:55360
ds_read_b64_tr_b16 a[58:59], v9 offset:57664
ds_read_b64_tr_b16 a[60:61], v9 offset:55368
ds_read_b64_tr_b16 a[62:63], v9 offset:57672
ds_read_b64_tr_b16 v[72:73], v10 offset:55296
ds_read_b64_tr_b16 v[74:75], v10 offset:57600
s_waitcnt lgkmcnt(0)
s_barrier
s_add_u32 m0, s48, 40960
s_nop 0
global_load_lds_dwordx4 v0, s[40:41] offset:-4096
global_load_lds_dwordx4 v1, s[40:41] offset:-3072
global_load_lds_dwordx4 v2, s[40:41] offset:-2048
global_load_lds_dwordx4 v3, s[40:41] offset:-1024
global_load_lds_dwordx4 v4, s[40:41] offset:0
global_load_lds_dwordx4 v5, s[40:41] offset:1024
global_load_lds_dwordx4 v6, s[40:41] offset:2048
global_load_lds_dwordx4 v7, s[40:41] offset:3072
s_add_u32 m0, s48, 49152
s_nop 0
global_load_lds_dwordx4 v8, s[40:41] offset:-4096
s_add_u32 s40, s40, 73728
s_addc_u32 s41, s41, 0
s_waitcnt vmcnt(27)
s_barrier
ds_read_b64_tr_b16 a[64:65], v204 offset:8192
ds_read_b64_tr_b16 a[66:67], v204 offset:10496
ds_read_b64_tr_b16 a[68:69], v204 offset:8200
ds_read_b64_tr_b16 a[70:71], v204 offset:10504
ds_read_b64_tr_b16 a[72:73], v204 offset:8256
ds_read_b64_tr_b16 a[74:75], v204 offset:10560
ds_read_b64_tr_b16 a[76:77], v204 offset:8264
ds_read_b64_tr_b16 a[78:79], v204 offset:10568
ds_read_b64_tr_b16 v[76:77], v206 offset:8192
ds_read_b64_tr_b16 v[78:79], v206 offset:10496
ds_read_b64_tr_b16 a[80:81], v204 offset:26624
ds_read_b64_tr_b16 a[82:83], v204 offset:28928
ds_read_b64_tr_b16 a[84:85], v204 offset:26632
ds_read_b64_tr_b16 a[86:87], v204 offset:28936
ds_read_b64_tr_b16 a[88:89], v204 offset:26688
ds_read_b64_tr_b16 a[90:91], v204 offset:28992
ds_read_b64_tr_b16 a[92:93], v204 offset:26696
ds_read_b64_tr_b16 a[94:95], v204 offset:29000
ds_read_b64_tr_b16 v[80:81], v206 offset:26624
ds_read_b64_tr_b16 v[82:83], v206 offset:28928
s_waitcnt lgkmcnt(0)
s_barrier
s_add_u32 m0, s48, 77824
s_nop 0
global_load_lds_dwordx4 v0, s[40:41] offset:-4096
global_load_lds_dwordx4 v1, s[40:41] offset:-3072
global_load_lds_dwordx4 v2, s[40:41] offset:-2048
global_load_lds_dwordx4 v3, s[40:41] offset:-1024
global_load_lds_dwordx4 v4, s[40:41] offset:0
global_load_lds_dwordx4 v5, s[40:41] offset:1024
global_load_lds_dwordx4 v6, s[40:41] offset:2048
global_load_lds_dwordx4 v7, s[40:41] offset:3072
s_add_u32 m0, s48, 86016
s_nop 0
global_load_lds_dwordx4 v8, s[40:41] offset:-4096
s_add_u32 s40, s40, 73728
s_addc_u32 s41, s41, 0
s_waitcnt vmcnt(27)
s_barrier
ds_read_b64_tr_b16 a[96:97], v204 offset:45056
ds_read_b64_tr_b16 a[98:99], v204 offset:47360
ds_read_b64_tr_b16 a[100:101], v204 offset:45064
ds_read_b64_tr_b16 a[102:103], v204 offset:47368
ds_read_b64_tr_b16 a[104:105], v204 offset:45120
ds_read_b64_tr_b16 a[106:107], v204 offset:47424
ds_read_b64_tr_b16 a[108:109], v204 offset:45128
ds_read_b64_tr_b16 a[110:111], v204 offset:47432
ds_read_b64_tr_b16 v[84:85], v206 offset:45056
ds_read_b64_tr_b16 v[86:87], v206 offset:47360
ds_read_b64_tr_b16 a[112:113], v204 offset:63488
ds_read_b64_tr_b16 a[114:115], v205 offset:256
ds_read_b64_tr_b16 a[116:117], v204 offset:63496
ds_read_b64_tr_b16 a[118:119], v205 offset:264
ds_read_b64_tr_b16 a[120:121], v204 offset:63552
ds_read_b64_tr_b16 a[122:123], v205 offset:320
ds_read_b64_tr_b16 a[124:125], v204 offset:63560
ds_read_b64_tr_b16 a[126:127], v205 offset:328
ds_read_b64_tr_b16 v[88:89], v206 offset:63488
ds_read_b64_tr_b16 v[90:91], v207 offset:256
s_waitcnt lgkmcnt(0)
s_barrier
s_add_u32 m0, s48, 114688
s_nop 0
global_load_lds_dwordx4 v0, s[40:41] offset:-4096
global_load_lds_dwordx4 v1, s[40:41] offset:-3072
global_load_lds_dwordx4 v2, s[40:41] offset:-2048
global_load_lds_dwordx4 v3, s[40:41] offset:-1024
global_load_lds_dwordx4 v4, s[40:41] offset:0
global_load_lds_dwordx4 v5, s[40:41] offset:1024
global_load_lds_dwordx4 v6, s[40:41] offset:2048
global_load_lds_dwordx4 v7, s[40:41] offset:3072
s_add_u32 m0, s48, 122880
s_nop 0
global_load_lds_dwordx4 v8, s[40:41] offset:-4096
s_add_u32 s40, s40, 73728
s_addc_u32 s41, s41, 0
s_waitcnt vmcnt(27)
s_barrier
ds_read_b64_tr_b16 a[128:129], v9 offset:0
ds_read_b64_tr_b16 a[130:131], v9 offset:2304
ds_read_b64_tr_b16 a[132:133], v9 offset:8
ds_read_b64_tr_b16 a[134:135], v9 offset:2312
ds_read_b64_tr_b16 a[136:137], v9 offset:64
ds_read_b64_tr_b16 a[138:139], v9 offset:2368
ds_read_b64_tr_b16 a[140:141], v9 offset:72
ds_read_b64_tr_b16 a[142:143], v9 offset:2376
ds_read_b64_tr_b16 v[92:93], v10 offset:0
ds_read_b64_tr_b16 v[94:95], v10 offset:2304
ds_read_b64_tr_b16 a[144:145], v9 offset:18432
ds_read_b64_tr_b16 a[146:147], v9 offset:20736
ds_read_b64_tr_b16 a[148:149], v9 offset:18440
ds_read_b64_tr_b16 a[150:151], v9 offset:20744
ds_read_b64_tr_b16 a[152:153], v9 offset:18496
ds_read_b64_tr_b16 a[154:155], v9 offset:20800
ds_read_b64_tr_b16 a[156:157], v9 offset:18504
ds_read_b64_tr_b16 a[158:159], v9 offset:20808
s_waitcnt lgkmcnt(0)
s_barrier
s_add_u32 m0, s48, 4096
s_nop 0
global_load_lds_dwordx4 v0, s[40:41] offset:-4096
global_load_lds_dwordx4 v1, s[40:41] offset:-3072
global_load_lds_dwordx4 v2, s[40:41] offset:-2048
global_load_lds_dwordx4 v3, s[40:41] offset:-1024
global_load_lds_dwordx4 v4, s[40:41] offset:0
global_load_lds_dwordx4 v5, s[40:41] offset:1024
global_load_lds_dwordx4 v6, s[40:41] offset:2048
global_load_lds_dwordx4 v7, s[40:41] offset:3072
s_add_u32 m0, s48, 12288
s_nop 0
global_load_lds_dwordx4 v8, s[40:41] offset:-4096
s_add_u32 s40, s40, 73728
s_addc_u32 s41, s41, 0
s_waitcnt vmcnt(27)
s_barrier
ds_read_b64_tr_b16 a[160:161], v9 offset:36864
ds_read_b64_tr_b16 a[162:163], v9 offset:39168
ds_read_b64_tr_b16 a[164:165], v9 offset:36872
ds_read_b64_tr_b16 a[166:167], v9 offset:39176
ds_read_b64_tr_b16 a[168:169], v9 offset:36928
ds_read_b64_tr_b16 a[170:171], v9 offset:39232
ds_read_b64_tr_b16 a[172:173], v9 offset:36936
ds_read_b64_tr_b16 a[174:175], v9 offset:39240
ds_read_b64_tr_b16 a[176:177], v9 offset:55296
ds_read_b64_tr_b16 a[178:179], v9 offset:57600
ds_read_b64_tr_b16 a[180:181], v9 offset:55304
ds_read_b64_tr_b16 a[182:183], v9 offset:57608
ds_read_b64_tr_b16 a[184:185], v9 offset:55360
ds_read_b64_tr_b16 a[186:187], v9 offset:57664
ds_read_b64_tr_b16 a[188:189], v9 offset:55368
ds_read_b64_tr_b16 a[190:191], v9 offset:57672
)"
R"(s_waitcnt vmcnt(18)
s_barrier
ds_read_b64_tr_b16 a[192:193], v204 offset:8192
ds_read_b64_tr_b16 a[194:195], v204 offset:10496
ds_read_b64_tr_b16 a[196:197], v204 offset:8200
ds_read_b64_tr_b16 a[198:199], v204 offset:10504
ds_read_b64_tr_b16 a[200:201], v204 offset:8256
ds_read_b64_tr_b16 a[202:203], v204 offset:10560
ds_read_b64_tr_b16 a[204:205], v204 offset:8264
ds_read_b64_tr_b16 a[206:207], v204 offset:10568
ds_read_b64_tr_b16 a[208:209], v204 offset:26624
ds_read_b64_tr_b16 a[210:211], v204 offset:28928
ds_read_b64_tr_b16 a[212:213], v204 offset:26632
ds_read_b64_tr_b16 a[214:215], v204 offset:28936
ds_read_b64_tr_b16 a[216:217], v204 offset:26688
ds_read_b64_tr_b16 a[218:219], v204 offset:28992
ds_read_b64_tr_b16 a[220:221], v204 offset:26696
ds_read_b64_tr_b16 a[222:223], v204 offset:29000
s_waitcnt vmcnt(9)
s_barrier
ds_read_b64_tr_b16 a[224:225], v204 offset:45056
ds_read_b64_tr_b16 a[226:227], v204 offset:47360
ds_read_b64_tr_b16 a[228:229], v204 offset:45064
ds_read_b64_tr_b16 a[230:231], v204 offset:47368
ds_read_b64_tr_b16 a[232:233], v204 offset:45120
ds_read_b64_tr_b16 a[234:235], v204 offset:47424
ds_read_b64_tr_b16 a[236:237], v204 offset:45128
ds_read_b64_tr_b16 a[238:239], v204 offset:47432
ds_read_b64_tr_b16 a[240:241], v204 offset:63488
ds_read_b64_tr_b16 a[242:243], v205 offset:256
ds_read_b64_tr_b16 a[244:245], v204 offset:63496
ds_read_b64_tr_b16 a[246:247], v205 offset:264
ds_read_b64_tr_b16 a[248:249], v204 offset:63552
ds_read_b64_tr_b16 a[250:251], v205 offset:320
ds_read_b64_tr_b16 a[252:253], v204 offset:63560
ds_read_b64_tr_b16 a[254:255], v205 offset:328
s_waitcnt vmcnt(0)
s_barrier
ds_read_b64_tr_b16 v[28:29], v9 offset:0
ds_read_b64_tr_b16 v[30:31], v9 offset:2304
ds_read_b64_tr_b16 v[32:33], v9 offset:8
ds_read_b64_tr_b16 v[34:35], v9 offset:2312
ds_read_b64_tr_b16 v[36:37], v9 offset:64
ds_read_b64_tr_b16 v[38:39], v9 offset:2368
ds_read_b64_tr_b16 v[40:41], v9 offset:72
ds_read_b64_tr_b16 v[42:43], v9 offset:2376
ds_read_b64_tr_b16 v[44:45], v9 offset:18432
ds_read_b64_tr_b16 v[46:47], v9 offset:20736
ds_read_b64_tr_b16 v[48:49], v9 offset:18440
ds_read_b64_tr_b16 v[50:51], v9 offset:20744
ds_read_b64_tr_b16 v[52:53], v9 offset:18496
ds_read_b64_tr_b16 v[54:55], v9 offset:20800
ds_read_b64_tr_b16 v[56:57], v9 offset:18504
ds_read_b64_tr_b16 v[58:59], v9 offset:20808
s_waitcnt lgkmcnt(0)
s_barrier
s_mov_b64 s[76:77], 0
s_mov_b32 s70, 33
s_waitcnt vmcnt(0) lgkmcnt(0)
s_nop 7
.p2align 6
.Lwo_top5:
s_waitcnt lgkmcnt(2)
v_mfma_f32_16x16x32_bf16 v[96:99], a[0:3], v[132:135], v[96:99]
ds_read_b128 v[144:147], v25 offset:3072
v_mfma_f32_16x16x32_bf16 v[100:103], a[4:7], v[132:135], v[100:103]
v_mfma_f32_16x16x32_bf16 v[104:107], a[8:11], v[132:135], v[104:107]
v_mfma_f32_16x16x32_bf16 v[108:111], a[12:15], v[132:135], v[108:111]
v_cvt_pk_bf16_f32 v164, v112, v113
v_cvt_pk_bf16_f32 v165, v114, v115
v_mfma_f32_16x16x32_bf16 v[128:131], v[60:63], v[132:135], 0
s_waitcnt lgkmcnt(2)
v_cvt_pk_bf16_f32 v166, v116, v117
v_cvt_pk_bf16_f32 v167, v118, v119
v_mfma_f32_16x16x32_bf16 v[96:99], a[16:19], v[136:139], v[96:99]
ds_read_b128 v[132:135], v25 offset:4096
v_add_f32 v254, v112, v113
v_mfma_f32_16x16x32_bf16 v[100:103], a[20:23], v[136:139], v[100:103]
global_store_dwordx4 v13, v[164:167], s[38:39] offset:0 sc1
v_mfma_f32_16x16x32_bf16 v[104:107], a[24:27], v[136:139], v[104:107]
v_cvt_pk_bf16_f32 v168, v120, v121
v_cvt_pk_bf16_f32 v169, v122, v123
v_mfma_f32_16x16x32_bf16 v[108:111], a[28:31], v[136:139], v[108:111]
v_cvt_pk_bf16_f32 v170, v124, v125
v_cvt_pk_bf16_f32 v171, v126, v127
v_mfma_f32_16x16x32_bf16 v[128:131], v[64:67], v[136:139], v[128:131]
s_waitcnt lgkmcnt(2)
global_store_dwordx4 v13, v[168:171], s[38:39] offset:1024 sc1
v_mfma_f32_16x16x32_bf16 v[96:99], a[32:35], v[140:143], v[96:99]
s_cmp_eq_u32 s70, 32
ds_read_b128 v[136:139], v25 offset:5120
v_add_u32 v13, 18688, v13
v_mfma_f32_16x16x32_bf16 v[100:103], a[36:39], v[140:143], v[100:103]
s_cselect_b64 s[42:43], s[44:45], s[42:43]
v_mul_f32 v255, v112, v112
v_fmac_f32 v255, v113, v113
v_mfma_f32_16x16x32_bf16 v[104:107], a[40:43], v[140:143], v[104:107]
s_cmp_eq_u32 s70, 16
v_add_f32 v254, v254, v114
v_fmac_f32 v255, v114, v114
v_mfma_f32_16x16x32_bf16 v[108:111], a[44:47], v[140:143], v[108:111]
s_cselect_b64 s[42:43], s[46:47], s[42:43]
v_add_f32 v254, v254, v115
v_fmac_f32 v255, v115, v115
v_mfma_f32_16x16x32_bf16 v[128:131], v[68:71], v[140:143], v[128:131]
s_waitcnt lgkmcnt(2)
v_add_f32 v254, v254, v116
v_fmac_f32 v255, v116, v116
v_mfma_f32_16x16x32_bf16 v[96:99], a[48:51], v[144:147], v[96:99]
s_xor_b32 s49, s49, 0x10000
ds_read_b128 v[140:143], v25 offset:6144
v_add_f32 v254, v254, v117
v_mfma_f32_16x16x32_bf16 v[100:103], a[52:55], v[144:147], v[100:103]
s_mov_b32 m0, 47104
v_fmac_f32 v255, v117, v117
v_add_f32 v254, v254, v118
v_mfma_f32_16x16x32_bf16 v[104:107], a[56:59], v[144:147], v[104:107]
v_fmac_f32 v255, v118, v118
v_add_f32 v254, v254, v119
v_mfma_f32_16x16x32_bf16 v[108:111], a[60:63], v[144:147], v[108:111]
global_load_lds_dwordx4 v16, s[42:43] offset:0 sc1
v_fmac_f32 v255, v119, v119
v_mfma_f32_16x16x32_bf16 v[128:131], v[72:75], v[144:147], v[128:131]
s_waitcnt lgkmcnt(2)
v_add_f32 v254, v254, v120
v_fmac_f32 v255, v120, v120
v_mfma_f32_16x16x32_bf16 v[96:99], a[64:67], v[132:135], v[96:99]
ds_read_b128 v[144:147], v25 offset:7168
v_add_f32 v254, v254, v121
v_mfma_f32_16x16x32_bf16 v[100:103], a[68:71], v[132:135], v[100:103]
v_fmac_f32 v255, v121, v121
v_add_f32 v254, v254, v122
v_mfma_f32_16x16x32_bf16 v[104:107], a[72:75], v[132:135], v[104:107]
s_waitcnt vmcnt(9)
ds_read_b128 v[148:151], v19 offset:8192
ds_read_b128 v[152:155], v20 offset:8192
v_mfma_f32_16x16x32_bf16 v[108:111], a[76:79], v[132:135], v[108:111]
v_fmac_f32 v255, v122, v122
v_add_f32 v254, v254, v123
v_mfma_f32_16x16x32_bf16 v[128:131], v[76:79], v[132:135], v[128:131]
s_waitcnt lgkmcnt(4)
v_fmac_f32 v255, v123, v123
v_add_f32 v254, v254, v124
v_mfma_f32_16x16x32_bf16 v[96:99], a[80:83], v[136:139], v[96:99]
ds_read_b128 v[132:135], v25 offset:8192
global_load_lds_dwordx4 v17, s[42:43] offset:1024 sc1
v_mfma_f32_16x16x32_bf16 v[100:103], a[84:87], v[136:139], v[100:103]
v_fmac_f32 v255, v124, v124
v_add_f32 v254, v254, v125
v_mfma_f32_16x16x32_bf16 v[104:107], a[88:91], v[136:139], v[104:107]
v_fmac_f32 v255, v125, v125
v_add_f32 v254, v254, v126
v_mfma_f32_16x16x32_bf16 v[108:111], a[92:95], v[136:139], v[108:111]
v_fmac_f32 v255, v126, v126
v_add_f32 v254, v254, v127
v_mfma_f32_16x16x32_bf16 v[128:131], v[80:83], v[136:139], v[128:131]
s_waitcnt lgkmcnt(4)
v_fmac_f32 v255, v127, v127
v_mfma_f32_16x16x32_bf16 v[96:99], a[96:99], v[140:143], v[96:99]
s_add_u32 m0, s49, 0
ds_read_b128 v[136:139], v25 offset:9216
v_mfma_f32_16x16x32_bf16 v[100:103], a[100:103], v[140:143], v[100:103]
ds_write_b64 v27, v[254:255] offset:45056
v_mfma_f32_16x16x32_bf16 v[104:107], a[104:107], v[140:143], v[104:107]
s_waitcnt lgkmcnt(4)
v_lshlrev_b32 v112, 16, v148
v_and_b32 v113, 0xffff0000, v148
v_mfma_f32_16x16x32_bf16 v[108:111], a[108:111], v[140:143], v[108:111]
s_sub_u32 s42, s42, 18432
v_lshlrev_b32 v114, 16, v149
v_and_b32 v115, 0xffff0000, v149
v_mfma_f32_16x16x32_bf16 v[128:131], v[84:87], v[140:143], v[128:131]
v_lshlrev_b32 v116, 16, v150
v_and_b32 v117, 0xffff0000, v150
v_mfma_f32_16x16x32_bf16 v[96:99], a[112:115], v[144:147], v[96:99]
s_subb_u32 s43, s43, 0
ds_read_b128 v[140:143], v25 offset:10240
v_lshlrev_b32 v118, 16, v151
v_mfma_f32_16x16x32_bf16 v[100:103], a[116:119], v[144:147], v[100:103]
s_waitcnt lgkmcnt(4)
v_and_b32 v119, 0xffff0000, v151
v_lshlrev_b32 v120, 16, v152
v_mfma_f32_16x16x32_bf16 v[104:107], a[120:123], v[144:147], v[104:107]
v_and_b32 v121, 0xffff0000, v152
v_lshlrev_b32 v122, 16, v153
v_mfma_f32_16x16x32_bf16 v[108:111], a[124:127], v[144:147], v[108:111]
v_and_b32 v123, 0xffff0000, v153
v_lshlrev_b32 v124, 16, v154
v_mfma_f32_16x16x32_bf16 v[128:131], v[88:91], v[144:147], v[128:131]
s_waitcnt lgkmcnt(3)
v_and_b32 v125, 0xffff0000, v154
v_lshlrev_b32 v126, 16, v155
v_mfma_f32_16x16x32_bf16 v[96:99], a[128:131], v[132:135], v[96:99]
ds_read_b128 v[144:147], v25 offset:11264
v_and_b32 v127, 0xffff0000, v155
v_mfma_f32_16x16x32_bf16 v[100:103], a[132:135], v[132:135], v[100:103]
v_mfma_f32_16x16x32_bf16 v[104:107], a[136:139], v[132:135], v[104:107]
v_mfma_f32_16x16x32_bf16 v[108:111], a[140:143], v[132:135], v[108:111]
v_mfma_f32_16x16x32_bf16 v[128:131], v[92:95], v[132:135], v[128:131]
s_waitcnt lgkmcnt(3)
v_mfma_f32_16x16x32_bf16 v[96:99], a[144:147], v[136:139], v[96:99]
ds_read_b128 v[132:135], v25 offset:12288
v_mfma_f32_16x16x32_bf16 v[100:103], a[148:151], v[136:139], v[100:103]
v_mfma_f32_16x16x32_bf16 v[104:107], a[152:155], v[136:139], v[104:107]
s_nop 2
ds_write_b128 v26, v[128:131] offset:36864
v_mfma_f32_16x16x32_bf16 v[108:111], a[156:159], v[136:139], v[108:111]
s_waitcnt lgkmcnt(3)
v_mfma_f32_16x16x32_bf16 v[96:99], a[160:163], v[140:143], v[96:99]
ds_read_b128 v[136:139], v25 offset:13312
v_mfma_f32_16x16x32_bf16 v[100:103], a[164:167], v[140:143], v[100:103]
v_mfma_f32_16x16x32_bf16 v[104:107], a[168:171], v[140:143], v[104:107]
v_mfma_f32_16x16x32_bf16 v[108:111], a[172:175], v[140:143], v[108:111]
s_waitcnt lgkmcnt(3)
v_mfma_f32_16x16x32_bf16 v[96:99], a[176:179], v[144:147], v[96:99]
ds_read_b128 v[140:143], v25 offset:14336
v_mfma_f32_16x16x32_bf16 v[100:103], a[180:183], v[144:147], v[100:103]
v_mfma_f32_16x16x32_bf16 v[104:107], a[184:187], v[144:147], v[104:107]
s_waitcnt lgkmcnt(2)
s_barrier
v_mfma_f32_16x16x32_bf16 v[108:111], a[188:191], v[144:147], v[108:111]
global_load_lds_dwordx4 v11, s[38:39] offset:-2048 sc1
v_mfma_f32_16x16x32_bf16 v[96:99], a[192:195], v[132:135], v[96:99]
ds_read_b128 v[144:147], v25 offset:15360
v_mfma_f32_16x16x32_bf16 v[100:103], a[196:199], v[132:135], v[100:103]
v_mfma_f32_16x16x32_bf16 v[104:107], a[200:203], v[132:135], v[104:107]
global_load_lds_dwordx4 v11, s[38:39] offset:-1024 sc1
v_mfma_f32_16x16x32_bf16 v[108:111], a[204:207], v[132:135], v[108:111]
s_waitcnt lgkmcnt(2)
v_mfma_f32_16x16x32_bf16 v[96:99], a[208:211], v[136:139], v[96:99]
ds_read_b128 v[132:135], v25 offset:16384
v_mfma_f32_16x16x32_bf16 v[100:103], a[212:215], v[136:139], v[100:103]
global_load_lds_dwordx4 v11, s[38:39] offset:0 sc1
v_mfma_f32_16x16x32_bf16 v[104:107], a[216:219], v[136:139], v[104:107]
v_mfma_f32_16x16x32_bf16 v[108:111], a[220:223], v[136:139], v[108:111]
s_waitcnt lgkmcnt(2)
v_mfma_f32_16x16x32_bf16 v[96:99], a[224:227], v[140:143], v[96:99]
ds_read_b128 v[136:139], v25 offset:17408
global_load_lds_dwordx4 v11, s[38:39] offset:1024 sc1
v_mfma_f32_16x16x32_bf16 v[100:103], a[228:231], v[140:143], v[100:103]
v_mfma_f32_16x16x32_bf16 v[104:107], a[232:235], v[140:143], v[104:107]
v_mfma_f32_16x16x32_bf16 v[108:111], a[236:239], v[140:143], v[108:111]
s_waitcnt lgkmcnt(2)
global_load_lds_dwordx4 v11, s[38:39] offset:2048 sc1
v_add_u32 v11, 18432, v11
v_mfma_f32_16x16x32_bf16 v[96:99], a[240:243], v[144:147], v[96:99]
v_min_u32 v11, v11, v12
v_mfma_f32_16x16x32_bf16 v[100:103], a[244:247], v[144:147], v[100:103]
v_mfma_f32_16x16x32_bf16 v[104:107], a[248:251], v[144:147], v[104:107]
v_mfma_f32_16x16x32_bf16 v[108:111], a[252:255], v[144:147], v[108:111]
s_waitcnt vmcnt(7)
s_barrier
s_waitcnt lgkmcnt(1)
v_mfma_f32_16x16x32_bf16 v[96:99], v[28:31], v[132:135], v[96:99]
ds_read_b128 v[140:143], v25 offset:18432
ds_read_b128 v[144:147], v25 offset:19456
v_mfma_f32_16x16x32_bf16 v[100:103], v[32:35], v[132:135], v[100:103]
s_mov_b64 exec, 1
global_store_dword v23, v24, s[38:39] sc1
s_mov_b64 exec, -1
v_mfma_f32_16x16x32_bf16 v[104:107], v[36:39], v[132:135], v[104:107]
v_add_u32 v24, 1, v24
v_mfma_f32_16x16x32_bf16 v[108:111], v[40:43], v[132:135], v[108:111]
s_waitcnt lgkmcnt(2)
v_mfma_f32_16x16x32_bf16 v[96:99], v[44:47], v[136:139], v[96:99]
ds_read_b128 v[132:135], v25 offset:20480
v_mfma_f32_16x16x32_bf16 v[100:103], v[48:51], v[136:139], v[100:103]
v_mfma_f32_16x16x32_bf16 v[104:107], v[52:55], v[136:139], v[104:107]
v_mfma_f32_16x16x32_bf16 v[108:111], v[56:59], v[136:139], v[108:111]
s_waitcnt lgkmcnt(2)
v_mfma_f32_16x16x32_bf16 v[112:115], a[0:3], v[140:143], v[112:115]
ds_read_b128 v[136:139], v25 offset:21504
v_mfma_f32_16x16x32_bf16 v[116:119], a[4:7], v[140:143], v[116:119]
v_mfma_f32_16x16x32_bf16 v[120:123], a[8:11], v[140:143], v[120:123]
v_mfma_f32_16x16x32_bf16 v[124:127], a[12:15], v[140:143], v[124:127]
v_cvt_pk_bf16_f32 v164, v96, v97
v_cvt_pk_bf16_f32 v165, v98, v99
v_mfma_f32_16x16x32_bf16 v[128:131], v[60:63], v[140:143], 0
s_waitcnt lgkmcnt(2)
v_cvt_pk_bf16_f32 v166, v100, v101
v_cvt_pk_bf16_f32 v167, v102, v103
v_mfma_f32_16x16x32_bf16 v[112:115], a[16:19], v[144:147], v[112:115]
ds_read_b128 v[140:143], v25 offset:22528
v_add_f32 v254, v96, v97
v_mfma_f32_16x16x32_bf16 v[116:119], a[20:23], v[144:147], v[116:119]
global_store_dwordx4 v13, v[164:167], s[38:39] offset:0 sc1
v_mfma_f32_16x16x32_bf16 v[120:123], a[24:27], v[144:147], v[120:123]
v_cvt_pk_bf16_f32 v168, v104, v105
v_cvt_pk_bf16_f32 v169, v106, v107
v_mfma_f32_16x16x32_bf16 v[124:127], a[28:31], v[144:147], v[124:127]
v_cvt_pk_bf16_f32 v170, v108, v109
v_cvt_pk_bf16_f32 v171, v110, v111
v_mfma_f32_16x16x32_bf16 v[128:131], v[64:67], v[144:147], v[128:131]
s_waitcnt lgkmcnt(2)
global_store_dwordx4 v13, v[168:171], s[38:39] offset:1024 sc1
v_mfma_f32_16x16x32_bf16 v[112:115], a[32:35], v[132:135], v[112:115]
s_mov_b32 m0, 55296
ds_read_b128 v[144:147], v25 offset:23552
v_add_u32 v13, 18688, v13
v_mfma_f32_16x16x32_bf16 v[116:119], a[36:39], v[132:135], v[116:119]
v_mul_f32 v255, v96, v96
v_fmac_f32 v255, v97, v97
v_mfma_f32_16x16x32_bf16 v[120:123], a[40:43], v[132:135], v[120:123]
v_add_f32 v254, v254, v98
v_fmac_f32 v255, v98, v98
v_mfma_f32_16x16x32_bf16 v[124:127], a[44:47], v[132:135], v[124:127]
v_add_f32 v254, v254, v99
v_fmac_f32 v255, v99, v99
v_mfma_f32_16x16x32_bf16 v[128:131], v[68:71], v[132:135], v[128:131]
s_waitcnt lgkmcnt(2)
v_add_f32 v254, v254, v100
v_fmac_f32 v255, v100, v100
v_mfma_f32_16x16x32_bf16 v[112:115], a[48:51], v[136:139], v[112:115]
ds_read_b128 v[132:135], v25 offset:24576
v_add_f32 v254, v254, v101
v_mfma_f32_16x16x32_bf16 v[116:119], a[52:55], v[136:139], v[116:119]
v_fmac_f32 v255, v101, v101
v_add_f32 v254, v254, v102
v_mfma_f32_16x16x32_bf16 v[120:123], a[56:59], v[136:139], v[120:123]
v_fmac_f32 v255, v102, v102
v_add_f32 v254, v254, v103
v_mfma_f32_16x16x32_bf16 v[124:127], a[60:63], v[136:139], v[124:127]
global_load_lds_dwordx4 v16, s[42:43] offset:0 sc1
v_fmac_f32 v255, v103, v103
v_mfma_f32_16x16x32_bf16 v[128:131], v[72:75], v[136:139], v[128:131]
s_waitcnt lgkmcnt(2)
v_add_f32 v254, v254, v104
v_fmac_f32 v255, v104, v104
v_mfma_f32_16x16x32_bf16 v[112:115], a[64:67], v[140:143], v[112:115]
ds_read_b128 v[136:139], v25 offset:25600
v_add_f32 v254, v254, v105
v_mfma_f32_16x16x32_bf16 v[116:119], a[68:71], v[140:143], v[116:119]
v_fmac_f32 v255, v105, v105
v_add_f32 v254, v254, v106
v_mfma_f32_16x16x32_bf16 v[120:123], a[72:75], v[140:143], v[120:123]
s_waitcnt vmcnt(9)
ds_read_b128 v[148:151], v19 offset:0
ds_read_b128 v[152:155], v20 offset:0
v_mfma_f32_16x16x32_bf16 v[124:127], a[76:79], v[140:143], v[124:127]
v_fmac_f32 v255, v106, v106
v_add_f32 v254, v254, v107
v_mfma_f32_16x16x32_bf16 v[128:131], v[76:79], v[140:143], v[128:131]
s_waitcnt lgkmcnt(4)
v_fmac_f32 v255, v107, v107
v_add_f32 v254, v254, v108
v_mfma_f32_16x16x32_bf16 v[112:115], a[80:83], v[144:147], v[112:115]
ds_read_b128 v[140:143], v25 offset:26624
global_load_lds_dwordx4 v17, s[42:43] offset:1024 sc1
v_mfma_f32_16x16x32_bf16 v[116:119], a[84:87], v[144:147], v[116:119]
v_fmac_f32 v255, v108, v108
v_add_f32 v254, v254, v109
v_mfma_f32_16x16x32_bf16 v[120:123], a[88:91], v[144:147], v[120:123]
v_fmac_f32 v255, v109, v109
v_add_f32 v254, v254, v110
v_mfma_f32_16x16x32_bf16 v[124:127], a[92:95], v[144:147], v[124:127]
v_fmac_f32 v255, v110, v110
v_add_f32 v254, v254, v111
v_mfma_f32_16x16x32_bf16 v[128:131], v[80:83], v[144:147], v[128:131]
s_waitcnt lgkmcnt(4)
v_fmac_f32 v255, v111, v111
v_mfma_f32_16x16x32_bf16 v[112:115], a[96:99], v[132:135], v[112:115]
s_add_u32 m0, s49, 18432
ds_read_b128 v[144:147], v25 offset:27648
v_mfma_f32_16x16x32_bf16 v[116:119], a[100:103], v[132:135], v[116:119]
ds_write_b64 v27, v[254:255] offset:43008
v_mfma_f32_16x16x32_bf16 v[120:123], a[104:107], v[132:135], v[120:123]
s_waitcnt lgkmcnt(4)
v_lshlrev_b32 v96, 16, v148
v_and_b32 v97, 0xffff0000, v148
v_mfma_f32_16x16x32_bf16 v[124:127], a[108:111], v[132:135], v[124:127]
s_sub_u32 s42, s42, 18432
v_lshlrev_b32 v98, 16, v149
v_and_b32 v99, 0xffff0000, v149
v_mfma_f32_16x16x32_bf16 v[128:131], v[84:87], v[132:135], v[128:131]
v_lshlrev_b32 v100, 16, v150
v_and_b32 v101, 0xffff0000, v150
v_mfma_f32_16x16x32_bf16 v[112:115], a[112:115], v[136:139], v[112:115]
s_subb_u32 s43, s43, 0
ds_read_b128 v[132:135], v25 offset:28672
v_lshlrev_b32 v102, 16, v151
v_mfma_f32_16x16x32_bf16 v[116:119], a[116:119], v[136:139], v[116:119]
s_waitcnt lgkmcnt(4)
v_and_b32 v103, 0xffff0000, v151
v_lshlrev_b32 v104, 16, v152
v_mfma_f32_16x16x32_bf16 v[120:123], a[120:123], v[136:139], v[120:123]
v_and_b32 v105, 0xffff0000, v152
v_lshlrev_b32 v106, 16, v153
v_mfma_f32_16x16x32_bf16 v[124:127], a[124:127], v[136:139], v[124:127]
v_and_b32 v107, 0xffff0000, v153
v_lshlrev_b32 v108, 16, v154
v_mfma_f32_16x16x32_bf16 v[128:131], v[88:91], v[136:139], v[128:131]
s_waitcnt lgkmcnt(3)
v_and_b32 v109, 0xffff0000, v154
v_lshlrev_b32 v110, 16, v155
v_mfma_f32_16x16x32_bf16 v[112:115], a[128:131], v[140:143], v[112:115]
ds_read_b128 v[136:139], v25 offset:29696
v_and_b32 v111, 0xffff0000, v155
v_mfma_f32_16x16x32_bf16 v[116:119], a[132:135], v[140:143], v[116:119]
v_mfma_f32_16x16x32_bf16 v[120:123], a[136:139], v[140:143], v[120:123]
v_mfma_f32_16x16x32_bf16 v[124:127], a[140:143], v[140:143], v[124:127]
)"
R"(v_mfma_f32_16x16x32_bf16 v[128:131], v[92:95], v[140:143], v[128:131]
s_waitcnt lgkmcnt(3)
v_mfma_f32_16x16x32_bf16 v[112:115], a[144:147], v[144:147], v[112:115]
ds_read_b128 v[140:143], v25 offset:30720
v_mfma_f32_16x16x32_bf16 v[116:119], a[148:151], v[144:147], v[116:119]
v_mfma_f32_16x16x32_bf16 v[120:123], a[152:155], v[144:147], v[120:123]
s_nop 2
ds_write_b128 v26, v[128:131] offset:36864
v_mfma_f32_16x16x32_bf16 v[124:127], a[156:159], v[144:147], v[124:127]
s_waitcnt lgkmcnt(3)
v_mfma_f32_16x16x32_bf16 v[112:115], a[160:163], v[132:135], v[112:115]
ds_read_b128 v[144:147], v25 offset:31744
v_mfma_f32_16x16x32_bf16 v[116:119], a[164:167], v[132:135], v[116:119]
v_mfma_f32_16x16x32_bf16 v[120:123], a[168:171], v[132:135], v[120:123]
v_mfma_f32_16x16x32_bf16 v[124:127], a[172:175], v[132:135], v[124:127]
s_waitcnt lgkmcnt(3)
v_mfma_f32_16x16x32_bf16 v[112:115], a[176:179], v[136:139], v[112:115]
ds_read_b128 v[132:135], v25 offset:32768
v_mfma_f32_16x16x32_bf16 v[116:119], a[180:183], v[136:139], v[116:119]
v_mfma_f32_16x16x32_bf16 v[120:123], a[184:187], v[136:139], v[120:123]
s_waitcnt lgkmcnt(2)
s_barrier
v_mfma_f32_16x16x32_bf16 v[124:127], a[188:191], v[136:139], v[124:127]
global_load_lds_dwordx4 v11, s[38:39] offset:-2048 sc1
v_mfma_f32_16x16x32_bf16 v[112:115], a[192:195], v[140:143], v[112:115]
s_mov_b64 s[76:77], -1
ds_read_b128 v[136:139], v25 offset:33792
v_mfma_f32_16x16x32_bf16 v[116:119], a[196:199], v[140:143], v[116:119]
v_mfma_f32_16x16x32_bf16 v[120:123], a[200:203], v[140:143], v[120:123]
global_load_lds_dwordx4 v11, s[38:39] offset:-1024 sc1
v_mfma_f32_16x16x32_bf16 v[124:127], a[204:207], v[140:143], v[124:127]
s_waitcnt lgkmcnt(2)
v_mfma_f32_16x16x32_bf16 v[112:115], a[208:211], v[144:147], v[112:115]
ds_read_b128 v[140:143], v25 offset:34816
v_mfma_f32_16x16x32_bf16 v[116:119], a[212:215], v[144:147], v[116:119]
global_load_lds_dwordx4 v11, s[38:39] offset:0 sc1
v_mfma_f32_16x16x32_bf16 v[120:123], a[216:219], v[144:147], v[120:123]
v_mfma_f32_16x16x32_bf16 v[124:127], a[220:223], v[144:147], v[124:127]
s_waitcnt lgkmcnt(2)
v_mfma_f32_16x16x32_bf16 v[112:115], a[224:227], v[132:135], v[112:115]
ds_read_b128 v[144:147], v25 offset:35840
global_load_lds_dwordx4 v11, s[38:39] offset:1024 sc1
v_mfma_f32_16x16x32_bf16 v[116:119], a[228:231], v[132:135], v[116:119]
v_mfma_f32_16x16x32_bf16 v[120:123], a[232:235], v[132:135], v[120:123]
v_mfma_f32_16x16x32_bf16 v[124:127], a[236:239], v[132:135], v[124:127]
s_waitcnt lgkmcnt(2)
global_load_lds_dwordx4 v11, s[38:39] offset:2048 sc1
v_add_u32 v11, 18432, v11
v_mfma_f32_16x16x32_bf16 v[112:115], a[240:243], v[136:139], v[112:115]
v_min_u32 v11, v11, v12
v_mfma_f32_16x16x32_bf16 v[116:119], a[244:247], v[136:139], v[116:119]
v_xor_b32 v25, 0x10000, v25
v_mfma_f32_16x16x32_bf16 v[120:123], a[248:251], v[136:139], v[120:123]
v_mfma_f32_16x16x32_bf16 v[124:127], a[252:255], v[136:139], v[124:127]
s_waitcnt vmcnt(7)
s_barrier
s_waitcnt lgkmcnt(1)
v_mfma_f32_16x16x32_bf16 v[112:115], v[28:31], v[140:143], v[112:115]
ds_read_b128 v[132:135], v25 offset:0
ds_read_b128 v[136:139], v25 offset:1024
v_mfma_f32_16x16x32_bf16 v[116:119], v[32:35], v[140:143], v[116:119]
s_mov_b64 exec, 1
global_store_dword v23, v24, s[38:39] sc1
s_mov_b64 exec, -1
v_mfma_f32_16x16x32_bf16 v[120:123], v[36:39], v[140:143], v[120:123]
v_add_u32 v24, 1, v24
v_mfma_f32_16x16x32_bf16 v[124:127], v[40:43], v[140:143], v[124:127]
s_waitcnt lgkmcnt(2)
v_mfma_f32_16x16x32_bf16 v[112:115], v[44:47], v[144:147], v[112:115]
ds_read_b128 v[140:143], v25 offset:2048
v_mfma_f32_16x16x32_bf16 v[116:119], v[48:51], v[144:147], v[116:119]
v_mfma_f32_16x16x32_bf16 v[120:123], v[52:55], v[144:147], v[120:123]
v_mfma_f32_16x16x32_bf16 v[124:127], v[56:59], v[144:147], v[124:127]
s_sub_u32 s70, s70, 1
s_cbranch_scc0 .Lwo_top5
s_nop 1
v_cvt_pk_bf16_f32 v164, v112, v113
v_cvt_pk_bf16_f32 v165, v114, v115
v_cvt_pk_bf16_f32 v166, v116, v117
v_cvt_pk_bf16_f32 v167, v118, v119
global_store_dwordx4 v13, v[164:167], s[38:39] offset:0 sc1
v_cvt_pk_bf16_f32 v168, v120, v121
v_cvt_pk_bf16_f32 v169, v122, v123
v_cvt_pk_bf16_f32 v170, v124, v125
v_cvt_pk_bf16_f32 v171, v126, v127
global_store_dwordx4 v13, v[168:171], s[38:39] offset:1024 sc1
v_add_f32 v254, v112, v113
v_mul_f32 v255, v112, v112
v_fmac_f32 v255, v113, v113
v_add_f32 v254, v254, v114
v_fmac_f32 v255, v114, v114
v_add_f32 v254, v254, v115
v_fmac_f32 v255, v115, v115
v_add_f32 v254, v254, v116
v_fmac_f32 v255, v116, v116
v_add_f32 v254, v254, v117
v_fmac_f32 v255, v117, v117
v_add_f32 v254, v254, v118
v_fmac_f32 v255, v118, v118
v_add_f32 v254, v254, v119
v_fmac_f32 v255, v119, v119
v_add_f32 v254, v254, v120
v_fmac_f32 v255, v120, v120
v_add_f32 v254, v254, v121
v_fmac_f32 v255, v121, v121
v_add_f32 v254, v254, v122
v_fmac_f32 v255, v122, v122
v_add_f32 v254, v254, v123
v_fmac_f32 v255, v123, v123
v_add_f32 v254, v254, v124
v_fmac_f32 v255, v124, v124
v_add_f32 v254, v254, v125
v_fmac_f32 v255, v125, v125
v_add_f32 v254, v254, v126
v_fmac_f32 v255, v126, v126
v_add_f32 v254, v254, v127
v_fmac_f32 v255, v127, v127
ds_write_b64 v27, v[254:255] offset:45056
s_waitcnt vmcnt(0) lgkmcnt(0)
s_barrier
s_mov_b64 exec, 1
global_store_dword v23, v24, s[38:39] sc1
s_mov_b64 exec, -1
v_add_u32 v24, 3, v24
s_waitcnt vmcnt(0)
s_barrier
s_mov_b64 exec, 1
global_store_dword v23, v24, s[38:39] sc1
s_mov_b64 exec, -1
s_endpgm
.Lwo_w2:
s_add_u32 m0, s48, 4096
s_nop 0
global_load_lds_dwordx4 v0, s[40:41] offset:-4096
global_load_lds_dwordx4 v1, s[40:41] offset:-3072
global_load_lds_dwordx4 v2, s[40:41] offset:-2048
global_load_lds_dwordx4 v3, s[40:41] offset:-1024
global_load_lds_dwordx4 v4, s[40:41] offset:0
global_load_lds_dwordx4 v5, s[40:41] offset:1024
global_load_lds_dwordx4 v6, s[40:41] offset:2048
global_load_lds_dwordx4 v7, s[40:41] offset:3072
s_add_u32 m0, s48, 12288
s_nop 0
global_load_lds_dwordx4 v8, s[40:41] offset:-4096
s_add_u32 s40, s40, 73728
s_addc_u32 s41, s41, 0
s_add_u32 m0, s48, 40960
s_nop 0
global_load_lds_dwordx4 v0, s[40:41] offset:-4096
global_load_lds_dwordx4 v1, s[40:41] offset:-3072
global_load_lds_dwordx4 v2, s[40:41] offset:-2048
global_load_lds_dwordx4 v3, s[40:41] offset:-1024
global_load_lds_dwordx4 v4, s[40:41] offset:0
global_load_lds_dwordx4 v5, s[40:41] offset:1024
global_load_lds_dwordx4 v6, s[40:41] offset:2048
global_load_lds_dwordx4 v7, s[40:41] offset:3072
s_add_u32 m0, s48, 49152
s_nop 0
global_load_lds_dwordx4 v8, s[40:41] offset:-4096
s_add_u32 s40, s40, 73728
s_addc_u32 s41, s41, 0
s_add_u32 m0, s48, 77824
s_nop 0
global_load_lds_dwordx4 v0, s[40:41] offset:-4096
global_load_lds_dwordx4 v1, s[40:41] offset:-3072
global_load_lds_dwordx4 v2, s[40:41] offset:-2048
global_load_lds_dwordx4 v3, s[40:41] offset:-1024
global_load_lds_dwordx4 v4, s[40:41] offset:0
global_load_lds_dwordx4 v5, s[40:41] offset:1024
global_load_lds_dwordx4 v6, s[40:41] offset:2048
global_load_lds_dwordx4 v7, s[40:41] offset:3072
s_add_u32 m0, s48, 86016
s_nop 0
global_load_lds_dwordx4 v8, s[40:41] offset:-4096
s_add_u32 s40, s40, 73728
s_addc_u32 s41, s41, 0
s_add_u32 m0, s48, 114688
s_nop 0
global_load_lds_dwordx4 v0, s[40:41] offset:-4096
global_load_lds_dwordx4 v1, s[40:41] offset:-3072
global_load_lds_dwordx4 v2, s[40:41] offset:-2048
global_load_lds_dwordx4 v3, s[40:41] offset:-1024
global_load_lds_dwordx4 v4, s[40:41] offset:0
global_load_lds_dwordx4 v5, s[40:41] offset:1024
global_load_lds_dwordx4 v6, s[40:41] offset:2048
global_load_lds_dwordx4 v7, s[40:41] offset:3072
s_add_u32 m0, s48, 122880
s_nop 0
global_load_lds_dwordx4 v8, s[40:41] offset:-4096
s_add_u32 s40, s40, 73728
s_addc_u32 s41, s41, 0
v_add_u32 v204, 0x10000, v9
v_add_u32 v205, 0x20000, v9
v_add_u32 v206, 0x10000, v10
v_add_u32 v207, 0x20000, v10
s_waitcnt lgkmcnt(0)
s_mul_i32 s37, s37, 0x9E3779B1
s_add_u32 s37, s37, 0x7F4A7C15
v_add_u32 v22, s37, v22
v_add_u32 v24, s37, v24
s_waitcnt vmcnt(27)
s_barrier
ds_read_b64_tr_b16 a[0:1], v9 offset:0
ds_read_b64_tr_b16 a[2:3], v9 offset:2304
ds_read_b64_tr_b16 a[4:5], v9 offset:8
ds_read_b64_tr_b16 a[6:7], v9 offset:2312
ds_read_b64_tr_b16 a[8:9], v9 offset:64
ds_read_b64_tr_b16 a[10:11], v9 offset:2368
ds_read_b64_tr_b16 a[12:13], v9 offset:72
ds_read_b64_tr_b16 a[14:15], v9 offset:2376
ds_read_b64_tr_b16 a[16:17], v9 offset:18432
ds_read_b64_tr_b16 a[18:19], v9 offset:20736
ds_read_b64_tr_b16 a[20:21], v9 offset:18440
ds_read_b64_tr_b16 a[22:23], v9 offset:20744
ds_read_b64_tr_b16 a[24:25], v9 offset:18496
ds_read_b64_tr_b16 a[26:27], v9 offset:20800
ds_read_b64_tr_b16 a[28:29], v9 offset:18504
ds_read_b64_tr_b16 a[30:31], v9 offset:20808
s_waitcnt lgkmcnt(0)
s_barrier
s_add_u32 m0, s48, 4096
s_nop 0
global_load_lds_dwordx4 v0, s[40:41] offset:-4096
global_load_lds_dwordx4 v1, s[40:41] offset:-3072
global_load_lds_dwordx4 v2, s[40:41] offset:-2048
global_load_lds_dwordx4 v3, s[40:41] offset:-1024
global_load_lds_dwordx4 v4, s[40:41] offset:0
global_load_lds_dwordx4 v5, s[40:41] offset:1024
global_load_lds_dwordx4 v6, s[40:41] offset:2048
global_load_lds_dwordx4 v7, s[40:41] offset:3072
s_add_u32 m0, s48, 12288
s_nop 0
global_load_lds_dwordx4 v8, s[40:41] offset:-4096
s_add_u32 s40, s40, 73728
s_addc_u32 s41, s41, 0
s_waitcnt vmcnt(27)
s_barrier
ds_read_b64_tr_b16 a[32:33], v9 offset:36864
ds_read_b64_tr_b16 a[34:35], v9 offset:39168
ds_read_b64_tr_b16 a[36:37], v9 offset:36872
ds_read_b64_tr_b16 a[38:39], v9 offset:39176
ds_read_b64_tr_b16 a[40:41], v9 offset:36928
ds_read_b64_tr_b16 a[42:43], v9 offset:39232
ds_read_b64_tr_b16 a[44:45], v9 offset:36936
ds_read_b64_tr_b16 a[46:47], v9 offset:39240
ds_read_b64_tr_b16 a[48:49], v9 offset:55296
ds_read_b64_tr_b16 a[50:51], v9 offset:57600
ds_read_b64_tr_b16 a[52:53], v9 offset:55304
ds_read_b64_tr_b16 a[54:55], v9 offset:57608
ds_read_b64_tr_b16 a[56:57], v9 offset:55360
ds_read_b64_tr_b16 a[58:59], v9 offset:57664
ds_read_b64_tr_b16 a[60:61], v9 offset:55368
ds_read_b64_tr_b16 a[62:63], v9 offset:57672
s_waitcnt lgkmcnt(0)
s_barrier
s_add_u32 m0, s48, 40960
s_nop 0
global_load_lds_dwordx4 v0, s[40:41] offset:-4096
global_load_lds_dwordx4 v1, s[40:41] offset:-3072
global_load_lds_dwordx4 v2, s[40:41] offset:-2048
global_load_lds_dwordx4 v3, s[40:41] offset:-1024
global_load_lds_dwordx4 v4, s[40:41] offset:0
global_load_lds_dwordx4 v5, s[40:41] offset:1024
global_load_lds_dwordx4 v6, s[40:41] offset:2048
global_load_lds_dwordx4 v7, s[40:41] offset:3072
s_add_u32 m0, s48, 49152
s_nop 0
global_load_lds_dwordx4 v8, s[40:41] offset:-4096
s_add_u32 s40, s40, 73728
s_addc_u32 s41, s41, 0
s_waitcnt vmcnt(27)
s_barrier
ds_read_b64_tr_b16 a[64:65], v204 offset:8192
ds_read_b64_tr_b16 a[66:67], v204 offset:10496
ds_read_b64_tr_b16 a[68:69], v204 offset:8200
ds_read_b64_tr_b16 a[70:71], v204 offset:10504
ds_read_b64_tr_b16 a[72:73], v204 offset:8256
ds_read_b64_tr_b16 a[74:75], v204 offset:10560
ds_read_b64_tr_b16 a[76:77], v204 offset:8264
ds_read_b64_tr_b16 a[78:79], v204 offset:10568
ds_read_b64_tr_b16 a[80:81], v204 offset:26624
ds_read_b64_tr_b16 a[82:83], v204 offset:28928
ds_read_b64_tr_b16 a[84:85], v204 offset:26632
ds_read_b64_tr_b16 a[86:87], v204 offset:28936
ds_read_b64_tr_b16 a[88:89], v204 offset:26688
ds_read_b64_tr_b16 a[90:91], v204 offset:28992
ds_read_b64_tr_b16 a[92:93], v204 offset:26696
ds_read_b64_tr_b16 a[94:95], v204 offset:29000
s_waitcnt lgkmcnt(0)
s_barrier
s_add_u32 m0, s48, 77824
s_nop 0
global_load_lds_dwordx4 v0, s[40:41] offset:-4096
global_load_lds_dwordx4 v1, s[40:41] offset:-3072
global_load_lds_dwordx4 v2, s[40:41] offset:-2048
global_load_lds_dwordx4 v3, s[40:41] offset:-1024
global_load_lds_dwordx4 v4, s[40:41] offset:0
global_load_lds_dwordx4 v5, s[40:41] offset:1024
global_load_lds_dwordx4 v6, s[40:41] offset:2048
global_load_lds_dwordx4 v7, s[40:41] offset:3072
s_add_u32 m0, s48, 86016
s_nop 0
global_load_lds_dwordx4 v8, s[40:41] offset:-4096
s_add_u32 s40, s40, 73728
s_addc_u32 s41, s41, 0
s_waitcnt vmcnt(27)
s_barrier
ds_read_b64_tr_b16 a[96:97], v204 offset:45056
ds_read_b64_tr_b16 a[98:99], v204 offset:47360
ds_read_b64_tr_b16 a[100:101], v204 offset:45064
ds_read_b64_tr_b16 a[102:103], v204 offset:47368
ds_read_b64_tr_b16 a[104:105], v204 offset:45120
ds_read_b64_tr_b16 a[106:107], v204 offset:47424
ds_read_b64_tr_b16 a[108:109], v204 offset:45128
ds_read_b64_tr_b16 a[110:111], v204 offset:47432
ds_read_b64_tr_b16 a[112:113], v204 offset:63488
ds_read_b64_tr_b16 a[114:115], v205 offset:256
ds_read_b64_tr_b16 a[116:117], v204 offset:63496
ds_read_b64_tr_b16 a[118:119], v205 offset:264
ds_read_b64_tr_b16 a[120:121], v204 offset:63552
ds_read_b64_tr_b16 a[122:123], v205 offset:320
ds_read_b64_tr_b16 a[124:125], v204 offset:63560
ds_read_b64_tr_b16 a[126:127], v205 offset:328
s_waitcnt lgkmcnt(0)
s_barrier
s_add_u32 m0, s48, 114688
s_nop 0
global_load_lds_dwordx4 v0, s[40:41] offset:-4096
global_load_lds_dwordx4 v1, s[40:41] offset:-3072
global_load_lds_dwordx4 v2, s[40:41] offset:-2048
global_load_lds_dwordx4 v3, s[40:41] offset:-1024
global_load_lds_dwordx4 v4, s[40:41] offset:0
global_load_lds_dwordx4 v5, s[40:41] offset:1024
global_load_lds_dwordx4 v6, s[40:41] offset:2048
global_load_lds_dwordx4 v7, s[40:41] offset:3072
s_add_u32 m0, s48, 122880
s_nop 0
global_load_lds_dwordx4 v8, s[40:41] offset:-4096
s_add_u32 s40, s40, 73728
s_addc_u32 s41, s41, 0
s_waitcnt vmcnt(27)
s_barrier
ds_read_b64_tr_b16 a[128:129], v9 offset:0
ds_read_b64_tr_b16 a[130:131], v9 offset:2304
ds_read_b64_tr_b16 a[132:133], v9 offset:8
ds_read_b64_tr_b16 a[134:135], v9 offset:2312
ds_read_b64_tr_b16 a[136:137], v9 offset:64
ds_read_b64_tr_b16 a[138:139], v9 offset:2368
ds_read_b64_tr_b16 a[140:141], v9 offset:72
ds_read_b64_tr_b16 a[142:143], v9 offset:2376
ds_read_b64_tr_b16 a[144:145], v9 offset:18432
ds_read_b64_tr_b16 a[146:147], v9 offset:20736
ds_read_b64_tr_b16 a[148:149], v9 offset:18440
ds_read_b64_tr_b16 a[150:151], v9 offset:20744
ds_read_b64_tr_b16 a[152:153], v9 offset:18496
ds_read_b64_tr_b16 a[154:155], v9 offset:20800
ds_read_b64_tr_b16 a[156:157], v9 offset:18504
ds_read_b64_tr_b16 a[158:159], v9 offset:20808
ds_read_b64_tr_b16 v[60:61], v10 offset:18432
ds_read_b64_tr_b16 v[62:63], v10 offset:20736
s_waitcnt lgkmcnt(0)
s_barrier
s_add_u32 m0, s48, 4096
s_nop 0
global_load_lds_dwordx4 v0, s[40:41] offset:-4096
global_load_lds_dwordx4 v1, s[40:41] offset:-3072
global_load_lds_dwordx4 v2, s[40:41] offset:-2048
global_load_lds_dwordx4 v3, s[40:41] offset:-1024
global_load_lds_dwordx4 v4, s[40:41] offset:0
global_load_lds_dwordx4 v5, s[40:41] offset:1024
global_load_lds_dwordx4 v6, s[40:41] offset:2048
global_load_lds_dwordx4 v7, s[40:41] offset:3072
s_add_u32 m0, s48, 12288
s_nop 0
global_load_lds_dwordx4 v8, s[40:41] offset:-4096
s_add_u32 s40, s40, 73728
s_addc_u32 s41, s41, 0
s_waitcnt vmcnt(27)
s_barrier
ds_read_b64_tr_b16 a[160:161], v9 offset:36864
ds_read_b64_tr_b16 a[162:163], v9 offset:39168
ds_read_b64_tr_b16 a[164:165], v9 offset:36872
ds_read_b64_tr_b16 a[166:167], v9 offset:39176
ds_read_b64_tr_b16 a[168:169], v9 offset:36928
ds_read_b64_tr_b16 a[170:171], v9 offset:39232
ds_read_b64_tr_b16 a[172:173], v9 offset:36936
ds_read_b64_tr_b16 a[174:175], v9 offset:39240
ds_read_b64_tr_b16 v[64:65], v10 offset:36864
ds_read_b64_tr_b16 v[66:67], v10 offset:39168
ds_read_b64_tr_b16 a[176:177], v9 offset:55296
ds_read_b64_tr_b16 a[178:179], v9 offset:57600
ds_read_b64_tr_b16 a[180:181], v9 offset:55304
ds_read_b64_tr_b16 a[182:183], v9 offset:57608
ds_read_b64_tr_b16 a[184:185], v9 offset:55360
ds_read_b64_tr_b16 a[186:187], v9 offset:57664
ds_read_b64_tr_b16 a[188:189], v9 offset:55368
ds_read_b64_tr_b16 a[190:191], v9 offset:57672
ds_read_b64_tr_b16 v[68:69], v10 offset:55296
ds_read_b64_tr_b16 v[70:71], v10 offset:57600
s_waitcnt vmcnt(18)
)"
R"(s_barrier
ds_read_b64_tr_b16 a[192:193], v204 offset:8192
ds_read_b64_tr_b16 a[194:195], v204 offset:10496
ds_read_b64_tr_b16 a[196:197], v204 offset:8200
ds_read_b64_tr_b16 a[198:199], v204 offset:10504
ds_read_b64_tr_b16 a[200:201], v204 offset:8256
ds_read_b64_tr_b16 a[202:203], v204 offset:10560
ds_read_b64_tr_b16 a[204:205], v204 offset:8264
ds_read_b64_tr_b16 a[206:207], v204 offset:10568
ds_read_b64_tr_b16 v[72:73], v206 offset:8192
ds_read_b64_tr_b16 v[74:75], v206 offset:10496
ds_read_b64_tr_b16 a[208:209], v204 offset:26624
ds_read_b64_tr_b16 a[210:211], v204 offset:28928
ds_read_b64_tr_b16 a[212:213], v204 offset:26632
ds_read_b64_tr_b16 a[214:215], v204 offset:28936
ds_read_b64_tr_b16 a[216:217], v204 offset:26688
ds_read_b64_tr_b16 a[218:219], v204 offset:28992
ds_read_b64_tr_b16 a[220:221], v204 offset:26696
ds_read_b64_tr_b16 a[222:223], v204 offset:29000
ds_read_b64_tr_b16 v[76:77], v206 offset:26624
ds_read_b64_tr_b16 v[78:79], v206 offset:28928
s_waitcnt vmcnt(9)
s_barrier
ds_read_b64_tr_b16 a[224:225], v204 offset:45056
ds_read_b64_tr_b16 a[226:227], v204 offset:47360
ds_read_b64_tr_b16 a[228:229], v204 offset:45064
ds_read_b64_tr_b16 a[230:231], v204 offset:47368
ds_read_b64_tr_b16 a[232:233], v204 offset:45120
ds_read_b64_tr_b16 a[234:235], v204 offset:47424
ds_read_b64_tr_b16 a[236:237], v204 offset:45128
ds_read_b64_tr_b16 a[238:239], v204 offset:47432
ds_read_b64_tr_b16 v[80:81], v206 offset:45056
ds_read_b64_tr_b16 v[82:83], v206 offset:47360
ds_read_b64_tr_b16 a[240:241], v204 offset:63488
ds_read_b64_tr_b16 a[242:243], v205 offset:256
ds_read_b64_tr_b16 a[244:245], v204 offset:63496
ds_read_b64_tr_b16 a[246:247], v205 offset:264
ds_read_b64_tr_b16 a[248:249], v204 offset:63552
ds_read_b64_tr_b16 a[250:251], v205 offset:320
ds_read_b64_tr_b16 a[252:253], v204 offset:63560
ds_read_b64_tr_b16 a[254:255], v205 offset:328
ds_read_b64_tr_b16 v[84:85], v206 offset:63488
ds_read_b64_tr_b16 v[86:87], v207 offset:256
s_waitcnt vmcnt(0)
s_barrier
ds_read_b64_tr_b16 v[28:29], v9 offset:0
ds_read_b64_tr_b16 v[30:31], v9 offset:2304
ds_read_b64_tr_b16 v[32:33], v9 offset:8
ds_read_b64_tr_b16 v[34:35], v9 offset:2312
ds_read_b64_tr_b16 v[36:37], v9 offset:64
ds_read_b64_tr_b16 v[38:39], v9 offset:2368
ds_read_b64_tr_b16 v[40:41], v9 offset:72
ds_read_b64_tr_b16 v[42:43], v9 offset:2376
ds_read_b64_tr_b16 v[88:89], v10 offset:0
ds_read_b64_tr_b16 v[90:91], v10 offset:2304
ds_read_b64_tr_b16 v[44:45], v9 offset:18432
ds_read_b64_tr_b16 v[46:47], v9 offset:20736
ds_read_b64_tr_b16 v[48:49], v9 offset:18440
ds_read_b64_tr_b16 v[50:51], v9 offset:20744
ds_read_b64_tr_b16 v[52:53], v9 offset:18496
ds_read_b64_tr_b16 v[54:55], v9 offset:20800
ds_read_b64_tr_b16 v[56:57], v9 offset:18504
ds_read_b64_tr_b16 v[58:59], v9 offset:20808
ds_read_b64_tr_b16 v[92:93], v10 offset:18432
ds_read_b64_tr_b16 v[94:95], v10 offset:20736
s_waitcnt lgkmcnt(0)
s_barrier
s_mov_b64 s[76:77], 0
s_mov_b32 s70, 33
s_waitcnt vmcnt(0) lgkmcnt(0)
s_nop 7
.p2align 6
.Lwo_top12:
s_waitcnt lgkmcnt(2)
v_mfma_f32_16x16x32_bf16 v[96:99], a[144:147], v[132:135], v[96:99]
ds_read_b128 v[144:147], v25 offset:12288
v_mfma_f32_16x16x32_bf16 v[100:103], a[148:151], v[132:135], v[100:103]
v_mfma_f32_16x16x32_bf16 v[104:107], a[152:155], v[132:135], v[104:107]
v_mfma_f32_16x16x32_bf16 v[108:111], a[156:159], v[132:135], v[108:111]
v_cvt_pk_bf16_f32 v164, v112, v113
v_cvt_pk_bf16_f32 v165, v114, v115
v_mfma_f32_16x16x32_bf16 v[128:131], v[60:63], v[132:135], 0
s_waitcnt lgkmcnt(2)
v_cvt_pk_bf16_f32 v166, v116, v117
v_cvt_pk_bf16_f32 v167, v118, v119
v_mfma_f32_16x16x32_bf16 v[96:99], a[160:163], v[136:139], v[96:99]
ds_read_b128 v[132:135], v25 offset:13312
v_add_f32 v254, v112, v113
v_mfma_f32_16x16x32_bf16 v[100:103], a[164:167], v[136:139], v[100:103]
global_store_dwordx4 v13, v[164:167], s[38:39] offset:0 sc1
v_mfma_f32_16x16x32_bf16 v[104:107], a[168:171], v[136:139], v[104:107]
v_cvt_pk_bf16_f32 v168, v120, v121
v_cvt_pk_bf16_f32 v169, v122, v123
v_mfma_f32_16x16x32_bf16 v[108:111], a[172:175], v[136:139], v[108:111]
v_cvt_pk_bf16_f32 v170, v124, v125
v_cvt_pk_bf16_f32 v171, v126, v127
v_mfma_f32_16x16x32_bf16 v[128:131], v[64:67], v[136:139], v[128:131]
s_waitcnt lgkmcnt(2)
v_mul_f32 v255, v112, v112
v_fmac_f32 v255, v113, v113
v_mfma_f32_16x16x32_bf16 v[96:99], a[176:179], v[140:143], v[96:99]
s_cmp_eq_u32 s70, 32
ds_read_b128 v[136:139], v25 offset:14336
v_add_f32 v254, v254, v114
v_mfma_f32_16x16x32_bf16 v[100:103], a[180:183], v[140:143], v[100:103]
s_cselect_b64 s[42:43], s[44:45], s[42:43]
global_store_dwordx4 v13, v[168:171], s[38:39] offset:1024 sc1
v_mfma_f32_16x16x32_bf16 v[104:107], a[184:187], v[140:143], v[104:107]
s_cmp_eq_u32 s70, 16
v_add_u32 v13, 18688, v13
v_fmac_f32 v255, v114, v114
v_mfma_f32_16x16x32_bf16 v[108:111], a[188:191], v[140:143], v[108:111]
s_cselect_b64 s[42:43], s[46:47], s[42:43]
v_add_f32 v254, v254, v115
v_fmac_f32 v255, v115, v115
v_mfma_f32_16x16x32_bf16 v[128:131], v[68:71], v[140:143], v[128:131]
s_waitcnt lgkmcnt(2)
v_add_f32 v254, v254, v116
v_fmac_f32 v255, v116, v116
v_mfma_f32_16x16x32_bf16 v[96:99], a[192:195], v[144:147], v[96:99]
s_xor_b32 s49, s49, 0x10000
ds_read_b128 v[140:143], v25 offset:15360
v_add_f32 v254, v254, v117
v_mfma_f32_16x16x32_bf16 v[100:103], a[196:199], v[144:147], v[100:103]
s_mov_b32 m0, 49152
v_fmac_f32 v255, v117, v117
v_add_f32 v254, v254, v118
v_mfma_f32_16x16x32_bf16 v[104:107], a[200:203], v[144:147], v[104:107]
v_fmac_f32 v255, v118, v118
v_add_f32 v254, v254, v119
v_mfma_f32_16x16x32_bf16 v[108:111], a[204:207], v[144:147], v[108:111]
v_fmac_f32 v255, v119, v119
v_add_f32 v254, v254, v120
v_mfma_f32_16x16x32_bf16 v[128:131], v[72:75], v[144:147], v[128:131]
s_waitcnt lgkmcnt(2)
v_fmac_f32 v255, v120, v120
v_add_f32 v254, v254, v121
v_mfma_f32_16x16x32_bf16 v[96:99], a[208:211], v[132:135], v[96:99]
ds_read_b128 v[144:147], v25 offset:16384
global_load_lds_dwordx4 v16, s[42:43] offset:0 sc1
v_mfma_f32_16x16x32_bf16 v[100:103], a[212:215], v[132:135], v[100:103]
v_fmac_f32 v255, v121, v121
v_add_f32 v254, v254, v122
v_mfma_f32_16x16x32_bf16 v[104:107], a[216:219], v[132:135], v[104:107]
s_waitcnt vmcnt(9)
ds_read_b128 v[148:151], v19 offset:8192
ds_read_b128 v[152:155], v20 offset:8192
v_mfma_f32_16x16x32_bf16 v[108:111], a[220:223], v[132:135], v[108:111]
v_fmac_f32 v255, v122, v122
v_add_f32 v254, v254, v123
v_mfma_f32_16x16x32_bf16 v[128:131], v[76:79], v[132:135], v[128:131]
s_waitcnt lgkmcnt(4)
v_fmac_f32 v255, v123, v123
v_add_f32 v254, v254, v124
v_mfma_f32_16x16x32_bf16 v[96:99], a[224:227], v[136:139], v[96:99]
ds_read_b128 v[132:135], v25 offset:17408
v_fmac_f32 v255, v124, v124
v_mfma_f32_16x16x32_bf16 v[100:103], a[228:231], v[136:139], v[100:103]
v_add_f32 v254, v254, v125
v_fmac_f32 v255, v125, v125
v_mfma_f32_16x16x32_bf16 v[104:107], a[232:235], v[136:139], v[104:107]
global_load_lds_dwordx4 v17, s[42:43] offset:1024 sc1
v_add_f32 v254, v254, v126
v_mfma_f32_16x16x32_bf16 v[108:111], a[236:239], v[136:139], v[108:111]
v_fmac_f32 v255, v126, v126
v_add_f32 v254, v254, v127
v_mfma_f32_16x16x32_bf16 v[128:131], v[80:83], v[136:139], v[128:131]
s_waitcnt lgkmcnt(4)
v_fmac_f32 v255, v127, v127
v_mfma_f32_16x16x32_bf16 v[96:99], a[240:243], v[140:143], v[96:99]
ds_read_b128 v[136:139], v25 offset:0
v_mfma_f32_16x16x32_bf16 v[100:103], a[244:247], v[140:143], v[100:103]
ds_write_b64 v27, v[254:255] offset:45568
v_mfma_f32_16x16x32_bf16 v[104:107], a[248:251], v[140:143], v[104:107]
s_add_u32 m0, s49, 0
s_waitcnt lgkmcnt(4)
v_lshlrev_b32 v112, 16, v148
v_and_b32 v113, 0xffff0000, v148
v_mfma_f32_16x16x32_bf16 v[108:111], a[252:255], v[140:143], v[108:111]
v_lshlrev_b32 v114, 16, v149
v_and_b32 v115, 0xffff0000, v149
v_mfma_f32_16x16x32_bf16 v[128:131], v[84:87], v[140:143], v[128:131]
v_lshlrev_b32 v116, 16, v150
v_and_b32 v117, 0xffff0000, v150
v_mfma_f32_16x16x32_bf16 v[96:99], v[28:31], v[144:147], v[96:99]
s_sub_u32 s42, s42, 18432
ds_read_b128 v[140:143], v25 offset:1024
v_lshlrev_b32 v118, 16, v151
v_mfma_f32_16x16x32_bf16 v[100:103], v[32:35], v[144:147], v[100:103]
s_subb_u32 s43, s43, 0
v_and_b32 v119, 0xffff0000, v151
s_waitcnt lgkmcnt(4)
v_lshlrev_b32 v120, 16, v152
v_mfma_f32_16x16x32_bf16 v[104:107], v[36:39], v[144:147], v[104:107]
v_and_b32 v121, 0xffff0000, v152
v_lshlrev_b32 v122, 16, v153
v_mfma_f32_16x16x32_bf16 v[108:111], v[40:43], v[144:147], v[108:111]
v_and_b32 v123, 0xffff0000, v153
v_lshlrev_b32 v124, 16, v154
v_mfma_f32_16x16x32_bf16 v[128:131], v[88:91], v[144:147], v[128:131]
s_waitcnt lgkmcnt(3)
v_and_b32 v125, 0xffff0000, v154
v_lshlrev_b32 v126, 16, v155
v_mfma_f32_16x16x32_bf16 v[96:99], v[44:47], v[132:135], v[96:99]
ds_read_b128 v[144:147], v25 offset:2048
v_and_b32 v127, 0xffff0000, v155
v_mfma_f32_16x16x32_bf16 v[100:103], v[48:51], v[132:135], v[100:103]
v_mfma_f32_16x16x32_bf16 v[104:107], v[52:55], v[132:135], v[104:107]
v_mfma_f32_16x16x32_bf16 v[108:111], v[56:59], v[132:135], v[108:111]
v_mfma_f32_16x16x32_bf16 v[128:131], v[92:95], v[132:135], v[128:131]
s_waitcnt lgkmcnt(3)
v_mfma_f32_16x16x32_bf16 v[96:99], a[0:3], v[136:139], v[96:99]
ds_read_b128 v[132:135], v25 offset:3072
v_mfma_f32_16x16x32_bf16 v[100:103], a[4:7], v[136:139], v[100:103]
v_mfma_f32_16x16x32_bf16 v[104:107], a[8:11], v[136:139], v[104:107]
v_mfma_f32_16x16x32_bf16 v[108:111], a[12:15], v[136:139], v[108:111]
s_waitcnt lgkmcnt(2)
s_nop 0
ds_write_b128 v26, v[128:131] offset:37888
v_mfma_f32_16x16x32_bf16 v[96:99], a[16:19], v[140:143], v[96:99]
ds_read_b128 v[136:139], v25 offset:4096
v_mfma_f32_16x16x32_bf16 v[100:103], a[20:23], v[140:143], v[100:103]
v_sub_u32 v201, v201, v22
v_cmp_lt_u32 vcc, 0x7f, v201
v_mfma_f32_16x16x32_bf16 v[104:107], a[24:27], v[140:143], v[104:107]
v_mfma_f32_16x16x32_bf16 v[108:111], a[28:31], v[140:143], v[108:111]
s_waitcnt lgkmcnt(3)
v_mfma_f32_16x16x32_bf16 v[96:99], a[32:35], v[144:147], v[96:99]
s_and_b64 vcc, vcc, s[76:77]
s_cbranch_vccnz .Lwo_fr8
.Lwo_rt7:
ds_read_b128 v[140:143], v25 offset:5120
v_mfma_f32_16x16x32_bf16 v[100:103], a[36:39], v[144:147], v[100:103]
v_mfma_f32_16x16x32_bf16 v[104:107], a[40:43], v[144:147], v[104:107]
s_waitcnt lgkmcnt(2)
s_barrier
v_mfma_f32_16x16x32_bf16 v[108:111], a[44:47], v[144:147], v[108:111]
v_add_u32 v22, 1, v22
v_mfma_f32_16x16x32_bf16 v[96:99], a[48:51], v[132:135], v[96:99]
ds_read_b128 v[144:147], v25 offset:6144
global_load_lds_dwordx4 v11, s[38:39] offset:-2048 sc1
v_mfma_f32_16x16x32_bf16 v[100:103], a[52:55], v[132:135], v[100:103]
v_mfma_f32_16x16x32_bf16 v[104:107], a[56:59], v[132:135], v[104:107]
v_mfma_f32_16x16x32_bf16 v[108:111], a[60:63], v[132:135], v[108:111]
s_waitcnt lgkmcnt(2)
global_load_lds_dwordx4 v11, s[38:39] offset:-1024 sc1
v_mfma_f32_16x16x32_bf16 v[96:99], a[64:67], v[136:139], v[96:99]
ds_read_b128 v[132:135], v25 offset:7168
v_mfma_f32_16x16x32_bf16 v[100:103], a[68:71], v[136:139], v[100:103]
v_mfma_f32_16x16x32_bf16 v[104:107], a[72:75], v[136:139], v[104:107]
global_load_lds_dwordx4 v11, s[38:39] offset:0 sc1
v_mfma_f32_16x16x32_bf16 v[108:111], a[76:79], v[136:139], v[108:111]
s_waitcnt lgkmcnt(2)
v_mfma_f32_16x16x32_bf16 v[96:99], a[80:83], v[140:143], v[96:99]
ds_read_b128 v[136:139], v25 offset:8192
v_mfma_f32_16x16x32_bf16 v[100:103], a[84:87], v[140:143], v[100:103]
global_load_lds_dwordx4 v11, s[38:39] offset:1024 sc1
v_mfma_f32_16x16x32_bf16 v[104:107], a[88:91], v[140:143], v[104:107]
v_mfma_f32_16x16x32_bf16 v[108:111], a[92:95], v[140:143], v[108:111]
s_waitcnt lgkmcnt(2)
global_load_dword v201, v21, s[38:39] sc1
v_mfma_f32_16x16x32_bf16 v[96:99], a[96:99], v[144:147], v[96:99]
global_load_lds_dwordx4 v11, s[38:39] offset:2048 sc1
v_add_u32 v11, 18432, v11
v_mfma_f32_16x16x32_bf16 v[100:103], a[100:103], v[144:147], v[100:103]
v_min_u32 v11, v11, v12
v_mfma_f32_16x16x32_bf16 v[104:107], a[104:107], v[144:147], v[104:107]
v_mfma_f32_16x16x32_bf16 v[108:111], a[108:111], v[144:147], v[108:111]
s_waitcnt vmcnt(8)
s_barrier
s_waitcnt lgkmcnt(1)
v_mfma_f32_16x16x32_bf16 v[96:99], a[112:115], v[132:135], v[96:99]
ds_read_b128 v[140:143], v25 offset:27648
ds_read_b128 v[144:147], v25 offset:28672
v_mfma_f32_16x16x32_bf16 v[100:103], a[116:119], v[132:135], v[100:103]
v_mfma_f32_16x16x32_bf16 v[104:107], a[120:123], v[132:135], v[104:107]
v_mfma_f32_16x16x32_bf16 v[108:111], a[124:127], v[132:135], v[108:111]
s_waitcnt lgkmcnt(2)
v_mfma_f32_16x16x32_bf16 v[96:99], a[128:131], v[136:139], v[96:99]
ds_read_b128 v[132:135], v25 offset:29696
v_mfma_f32_16x16x32_bf16 v[100:103], a[132:135], v[136:139], v[100:103]
v_mfma_f32_16x16x32_bf16 v[104:107], a[136:139], v[136:139], v[104:107]
v_mfma_f32_16x16x32_bf16 v[108:111], a[140:143], v[136:139], v[108:111]
s_waitcnt lgkmcnt(2)
v_mfma_f32_16x16x32_bf16 v[112:115], a[144:147], v[140:143], v[112:115]
ds_read_b128 v[136:139], v25 offset:30720
v_mfma_f32_16x16x32_bf16 v[116:119], a[148:151], v[140:143], v[116:119]
v_mfma_f32_16x16x32_bf16 v[120:123], a[152:155], v[140:143], v[120:123]
v_mfma_f32_16x16x32_bf16 v[124:127], a[156:159], v[140:143], v[124:127]
v_cvt_pk_bf16_f32 v164, v96, v97
v_cvt_pk_bf16_f32 v165, v98, v99
v_mfma_f32_16x16x32_bf16 v[128:131], v[60:63], v[140:143], 0
s_waitcnt lgkmcnt(2)
v_cvt_pk_bf16_f32 v166, v100, v101
v_cvt_pk_bf16_f32 v167, v102, v103
v_mfma_f32_16x16x32_bf16 v[112:115], a[160:163], v[144:147], v[112:115]
ds_read_b128 v[140:143], v25 offset:31744
v_add_f32 v254, v96, v97
v_mfma_f32_16x16x32_bf16 v[116:119], a[164:167], v[144:147], v[116:119]
global_store_dwordx4 v13, v[164:167], s[38:39] offset:0 sc1
v_mfma_f32_16x16x32_bf16 v[120:123], a[168:171], v[144:147], v[120:123]
v_cvt_pk_bf16_f32 v168, v104, v105
v_cvt_pk_bf16_f32 v169, v106, v107
v_mfma_f32_16x16x32_bf16 v[124:127], a[172:175], v[144:147], v[124:127]
v_cvt_pk_bf16_f32 v170, v108, v109
v_cvt_pk_bf16_f32 v171, v110, v111
v_mfma_f32_16x16x32_bf16 v[128:131], v[64:67], v[144:147], v[128:131]
s_waitcnt lgkmcnt(2)
v_mul_f32 v255, v96, v96
v_fmac_f32 v255, v97, v97
v_mfma_f32_16x16x32_bf16 v[112:115], a[176:179], v[132:135], v[112:115]
s_mov_b32 m0, 57344
ds_read_b128 v[144:147], v25 offset:32768
v_add_f32 v254, v254, v98
v_mfma_f32_16x16x32_bf16 v[116:119], a[180:183], v[132:135], v[116:119]
global_store_dwordx4 v13, v[168:171], s[38:39] offset:1024 sc1
v_mfma_f32_16x16x32_bf16 v[120:123], a[184:187], v[132:135], v[120:123]
v_add_u32 v13, 18688, v13
v_fmac_f32 v255, v98, v98
v_mfma_f32_16x16x32_bf16 v[124:127], a[188:191], v[132:135], v[124:127]
v_add_f32 v254, v254, v99
v_fmac_f32 v255, v99, v99
v_mfma_f32_16x16x32_bf16 v[128:131], v[68:71], v[132:135], v[128:131]
s_waitcnt lgkmcnt(2)
v_add_f32 v254, v254, v100
v_fmac_f32 v255, v100, v100
v_mfma_f32_16x16x32_bf16 v[112:115], a[192:195], v[136:139], v[112:115]
ds_read_b128 v[132:135], v25 offset:33792
v_add_f32 v254, v254, v101
v_mfma_f32_16x16x32_bf16 v[116:119], a[196:199], v[136:139], v[116:119]
v_fmac_f32 v255, v101, v101
v_add_f32 v254, v254, v102
v_mfma_f32_16x16x32_bf16 v[120:123], a[200:203], v[136:139], v[120:123]
v_fmac_f32 v255, v102, v102
v_add_f32 v254, v254, v103
v_mfma_f32_16x16x32_bf16 v[124:127], a[204:207], v[136:139], v[124:127]
v_fmac_f32 v255, v103, v103
v_add_f32 v254, v254, v104
v_mfma_f32_16x16x32_bf16 v[128:131], v[72:75], v[136:139], v[128:131]
s_waitcnt lgkmcnt(2)
v_fmac_f32 v255, v104, v104
v_add_f32 v254, v254, v105
v_mfma_f32_16x16x32_bf16 v[112:115], a[208:211], v[140:143], v[112:115]
ds_read_b128 v[136:139], v25 offset:34816
global_load_lds_dwordx4 v16, s[42:43] offset:0 sc1
v_mfma_f32_16x16x32_bf16 v[116:119], a[212:215], v[140:143], v[116:119]
v_fmac_f32 v255, v105, v105
v_add_f32 v254, v254, v106
v_mfma_f32_16x16x32_bf16 v[120:123], a[216:219], v[140:143], v[120:123]
s_waitcnt vmcnt(9)
ds_read_b128 v[148:151], v19 offset:0
ds_read_b128 v[152:155], v20 offset:0
v_mfma_f32_16x16x32_bf16 v[124:127], a[220:223], v[140:143], v[124:127]
v_fmac_f32 v255, v106, v106
v_add_f32 v254, v254, v107
v_mfma_f32_16x16x32_bf16 v[128:131], v[76:79], v[140:143], v[128:131]
s_waitcnt lgkmcnt(4)
v_fmac_f32 v255, v107, v107
v_add_f32 v254, v254, v108
v_mfma_f32_16x16x32_bf16 v[112:115], a[224:227], v[144:147], v[112:115]
ds_read_b128 v[140:143], v25 offset:35840
v_fmac_f32 v255, v108, v108
v_mfma_f32_16x16x32_bf16 v[116:119], a[228:231], v[144:147], v[116:119]
v_add_f32 v254, v254, v109
v_fmac_f32 v255, v109, v109
v_mfma_f32_16x16x32_bf16 v[120:123], a[232:235], v[144:147], v[120:123]
global_load_lds_dwordx4 v17, s[42:43] offset:1024 sc1
v_add_f32 v254, v254, v110
v_mfma_f32_16x16x32_bf16 v[124:127], a[236:239], v[144:147], v[124:127]
v_fmac_f32 v255, v110, v110
v_add_f32 v254, v254, v111
v_mfma_f32_16x16x32_bf16 v[128:131], v[80:83], v[144:147], v[128:131]
s_waitcnt lgkmcnt(4)
v_fmac_f32 v255, v111, v111
v_mfma_f32_16x16x32_bf16 v[112:115], a[240:243], v[132:135], v[112:115]
ds_read_b128 v[144:147], v25 offset:18432
v_mfma_f32_16x16x32_bf16 v[116:119], a[244:247], v[132:135], v[116:119]
ds_write_b64 v27, v[254:255] offset:43520
v_mfma_f32_16x16x32_bf16 v[120:123], a[248:251], v[132:135], v[120:123]
s_add_u32 m0, s49, 18432
s_waitcnt lgkmcnt(4)
v_lshlrev_b32 v96, 16, v148
v_and_b32 v97, 0xffff0000, v148
v_mfma_f32_16x16x32_bf16 v[124:127], a[252:255], v[132:135], v[124:127]
v_lshlrev_b32 v98, 16, v149
v_and_b32 v99, 0xffff0000, v149
v_mfma_f32_16x16x32_bf16 v[128:131], v[84:87], v[132:135], v[128:131]
v_lshlrev_b32 v100, 16, v150
v_and_b32 v101, 0xffff0000, v150
v_mfma_f32_16x16x32_bf16 v[112:115], v[28:31], v[136:139], v[112:115]
s_sub_u32 s42, s42, 18432
ds_read_b128 v[132:135], v25 offset:19456
v_lshlrev_b32 v102, 16, v151
v_mfma_f32_16x16x32_bf16 v[116:119], v[32:35], v[136:139], v[116:119]
s_subb_u32 s43, s43, 0
v_and_b32 v103, 0xffff0000, v151
s_waitcnt lgkmcnt(4)
v_lshlrev_b32 v104, 16, v152
v_mfma_f32_16x16x32_bf16 v[120:123], v[36:39], v[136:139], v[120:123]
v_and_b32 v105, 0xffff0000, v152
)"
R"(v_lshlrev_b32 v106, 16, v153
v_mfma_f32_16x16x32_bf16 v[124:127], v[40:43], v[136:139], v[124:127]
v_and_b32 v107, 0xffff0000, v153
v_lshlrev_b32 v108, 16, v154
v_mfma_f32_16x16x32_bf16 v[128:131], v[88:91], v[136:139], v[128:131]
s_waitcnt lgkmcnt(3)
v_and_b32 v109, 0xffff0000, v154
v_lshlrev_b32 v110, 16, v155
v_mfma_f32_16x16x32_bf16 v[112:115], v[44:47], v[140:143], v[112:115]
ds_read_b128 v[136:139], v25 offset:20480
v_and_b32 v111, 0xffff0000, v155
v_mfma_f32_16x16x32_bf16 v[116:119], v[48:51], v[140:143], v[116:119]
v_mfma_f32_16x16x32_bf16 v[120:123], v[52:55], v[140:143], v[120:123]
v_mfma_f32_16x16x32_bf16 v[124:127], v[56:59], v[140:143], v[124:127]
v_mfma_f32_16x16x32_bf16 v[128:131], v[92:95], v[140:143], v[128:131]
s_waitcnt lgkmcnt(3)
v_mfma_f32_16x16x32_bf16 v[112:115], a[0:3], v[144:147], v[112:115]
ds_read_b128 v[140:143], v25 offset:21504
v_mfma_f32_16x16x32_bf16 v[116:119], a[4:7], v[144:147], v[116:119]
v_mfma_f32_16x16x32_bf16 v[120:123], a[8:11], v[144:147], v[120:123]
v_mfma_f32_16x16x32_bf16 v[124:127], a[12:15], v[144:147], v[124:127]
s_waitcnt lgkmcnt(2)
s_nop 0
ds_write_b128 v26, v[128:131] offset:37888
v_mfma_f32_16x16x32_bf16 v[112:115], a[16:19], v[132:135], v[112:115]
ds_read_b128 v[144:147], v25 offset:22528
v_mfma_f32_16x16x32_bf16 v[116:119], a[20:23], v[132:135], v[116:119]
v_sub_u32 v200, v200, v22
v_cmp_lt_u32 vcc, 0x7f, v200
v_mfma_f32_16x16x32_bf16 v[120:123], a[24:27], v[132:135], v[120:123]
v_mfma_f32_16x16x32_bf16 v[124:127], a[28:31], v[132:135], v[124:127]
s_waitcnt lgkmcnt(3)
v_mfma_f32_16x16x32_bf16 v[112:115], a[32:35], v[136:139], v[112:115]
s_and_b64 vcc, vcc, s[76:77]
s_cbranch_vccnz .Lwo_fr11
.Lwo_rt10:
ds_read_b128 v[132:135], v25 offset:23552
v_mfma_f32_16x16x32_bf16 v[116:119], a[36:39], v[136:139], v[116:119]
v_mfma_f32_16x16x32_bf16 v[120:123], a[40:43], v[136:139], v[120:123]
s_waitcnt lgkmcnt(2)
s_barrier
v_mfma_f32_16x16x32_bf16 v[124:127], a[44:47], v[136:139], v[124:127]
v_add_u32 v22, 1, v22
v_mfma_f32_16x16x32_bf16 v[112:115], a[48:51], v[140:143], v[112:115]
s_mov_b64 s[76:77], -1
ds_read_b128 v[136:139], v25 offset:24576
global_load_lds_dwordx4 v11, s[38:39] offset:-2048 sc1
v_mfma_f32_16x16x32_bf16 v[116:119], a[52:55], v[140:143], v[116:119]
v_mfma_f32_16x16x32_bf16 v[120:123], a[56:59], v[140:143], v[120:123]
v_mfma_f32_16x16x32_bf16 v[124:127], a[60:63], v[140:143], v[124:127]
s_waitcnt lgkmcnt(2)
global_load_lds_dwordx4 v11, s[38:39] offset:-1024 sc1
v_mfma_f32_16x16x32_bf16 v[112:115], a[64:67], v[144:147], v[112:115]
ds_read_b128 v[140:143], v25 offset:25600
v_mfma_f32_16x16x32_bf16 v[116:119], a[68:71], v[144:147], v[116:119]
v_mfma_f32_16x16x32_bf16 v[120:123], a[72:75], v[144:147], v[120:123]
global_load_lds_dwordx4 v11, s[38:39] offset:0 sc1
v_mfma_f32_16x16x32_bf16 v[124:127], a[76:79], v[144:147], v[124:127]
s_waitcnt lgkmcnt(2)
v_mfma_f32_16x16x32_bf16 v[112:115], a[80:83], v[132:135], v[112:115]
ds_read_b128 v[144:147], v25 offset:26624
v_mfma_f32_16x16x32_bf16 v[116:119], a[84:87], v[132:135], v[116:119]
global_load_lds_dwordx4 v11, s[38:39] offset:1024 sc1
v_mfma_f32_16x16x32_bf16 v[120:123], a[88:91], v[132:135], v[120:123]
v_mfma_f32_16x16x32_bf16 v[124:127], a[92:95], v[132:135], v[124:127]
s_waitcnt lgkmcnt(2)
global_load_dword v200, v21, s[38:39] sc1
v_mfma_f32_16x16x32_bf16 v[112:115], a[96:99], v[136:139], v[112:115]
global_load_lds_dwordx4 v11, s[38:39] offset:2048 sc1
v_add_u32 v11, 18432, v11
v_mfma_f32_16x16x32_bf16 v[116:119], a[100:103], v[136:139], v[116:119]
v_xor_b32 v25, 0x10000, v25
v_min_u32 v11, v11, v12
v_mfma_f32_16x16x32_bf16 v[120:123], a[104:107], v[136:139], v[120:123]
v_mfma_f32_16x16x32_bf16 v[124:127], a[108:111], v[136:139], v[124:127]
s_waitcnt vmcnt(8)
s_barrier
s_waitcnt lgkmcnt(1)
v_mfma_f32_16x16x32_bf16 v[112:115], a[112:115], v[140:143], v[112:115]
ds_read_b128 v[132:135], v25 offset:9216
ds_read_b128 v[136:139], v25 offset:10240
v_mfma_f32_16x16x32_bf16 v[116:119], a[116:119], v[140:143], v[116:119]
v_mfma_f32_16x16x32_bf16 v[120:123], a[120:123], v[140:143], v[120:123]
v_mfma_f32_16x16x32_bf16 v[124:127], a[124:127], v[140:143], v[124:127]
s_waitcnt lgkmcnt(2)
v_mfma_f32_16x16x32_bf16 v[112:115], a[128:131], v[144:147], v[112:115]
ds_read_b128 v[140:143], v25 offset:11264
v_mfma_f32_16x16x32_bf16 v[116:119], a[132:135], v[144:147], v[116:119]
v_mfma_f32_16x16x32_bf16 v[120:123], a[136:139], v[144:147], v[120:123]
v_mfma_f32_16x16x32_bf16 v[124:127], a[140:143], v[144:147], v[124:127]
s_sub_u32 s70, s70, 1
s_cbranch_scc0 .Lwo_top12
s_nop 1
v_cvt_pk_bf16_f32 v164, v112, v113
v_cvt_pk_bf16_f32 v165, v114, v115
v_cvt_pk_bf16_f32 v166, v116, v117
v_cvt_pk_bf16_f32 v167, v118, v119
global_store_dwordx4 v13, v[164:167], s[38:39] offset:0 sc1
v_cvt_pk_bf16_f32 v168, v120, v121
v_cvt_pk_bf16_f32 v169, v122, v123
v_cvt_pk_bf16_f32 v170, v124, v125
v_cvt_pk_bf16_f32 v171, v126, v127
global_store_dwordx4 v13, v[168:171], s[38:39] offset:1024 sc1
v_add_f32 v254, v112, v113
v_mul_f32 v255, v112, v112
v_fmac_f32 v255, v113, v113
v_add_f32 v254, v254, v114
v_fmac_f32 v255, v114, v114
v_add_f32 v254, v254, v115
v_fmac_f32 v255, v115, v115
v_add_f32 v254, v254, v116
v_fmac_f32 v255, v116, v116
v_add_f32 v254, v254, v117
v_fmac_f32 v255, v117, v117
v_add_f32 v254, v254, v118
v_fmac_f32 v255, v118, v118
v_add_f32 v254, v254, v119
v_fmac_f32 v255, v119, v119
v_add_f32 v254, v254, v120
v_fmac_f32 v255, v120, v120
v_add_f32 v254, v254, v121
v_fmac_f32 v255, v121, v121
v_add_f32 v254, v254, v122
v_fmac_f32 v255, v122, v122
v_add_f32 v254, v254, v123
v_fmac_f32 v255, v123, v123
v_add_f32 v254, v254, v124
v_fmac_f32 v255, v124, v124
v_add_f32 v254, v254, v125
v_fmac_f32 v255, v125, v125
v_add_f32 v254, v254, v126
v_fmac_f32 v255, v126, v126
v_add_f32 v254, v254, v127
v_fmac_f32 v255, v127, v127
ds_write_b64 v27, v[254:255] offset:45568
s_waitcnt vmcnt(0) lgkmcnt(0)
s_barrier
s_barrier
s_endpgm
.Lwo_w3:
s_add_u32 m0, s48, 4096
s_nop 0
global_load_lds_dwordx4 v0, s[40:41] offset:-4096
global_load_lds_dwordx4 v1, s[40:41] offset:-3072
global_load_lds_dwordx4 v2, s[40:41] offset:-2048
global_load_lds_dwordx4 v3, s[40:41] offset:-1024
global_load_lds_dwordx4 v4, s[40:41] offset:0
global_load_lds_dwordx4 v5, s[40:41] offset:1024
global_load_lds_dwordx4 v6, s[40:41] offset:2048
global_load_lds_dwordx4 v7, s[40:41] offset:3072
s_add_u32 m0, s48, 12288
s_nop 0
global_load_lds_dwordx4 v8, s[40:41] offset:-4096
s_add_u32 s40, s40, 73728
s_addc_u32 s41, s41, 0
s_add_u32 m0, s48, 40960
s_nop 0
global_load_lds_dwordx4 v0, s[40:41] offset:-4096
global_load_lds_dwordx4 v1, s[40:41] offset:-3072
global_load_lds_dwordx4 v2, s[40:41] offset:-2048
global_load_lds_dwordx4 v3, s[40:41] offset:-1024
global_load_lds_dwordx4 v4, s[40:41] offset:0
global_load_lds_dwordx4 v5, s[40:41] offset:1024
global_load_lds_dwordx4 v6, s[40:41] offset:2048
global_load_lds_dwordx4 v7, s[40:41] offset:3072
s_add_u32 m0, s48, 49152
s_nop 0
global_load_lds_dwordx4 v8, s[40:41] offset:-4096
s_add_u32 s40, s40, 73728
s_addc_u32 s41, s41, 0
s_add_u32 m0, s48, 77824
s_nop 0
global_load_lds_dwordx4 v0, s[40:41] offset:-4096
global_load_lds_dwordx4 v1, s[40:41] offset:-3072
global_load_lds_dwordx4 v2, s[40:41] offset:-2048
global_load_lds_dwordx4 v3, s[40:41] offset:-1024
global_load_lds_dwordx4 v4, s[40:41] offset:0
global_load_lds_dwordx4 v5, s[40:41] offset:1024
global_load_lds_dwordx4 v6, s[40:41] offset:2048
global_load_lds_dwordx4 v7, s[40:41] offset:3072
s_add_u32 m0, s48, 86016
s_nop 0
global_load_lds_dwordx4 v8, s[40:41] offset:-4096
s_add_u32 s40, s40, 73728
s_addc_u32 s41, s41, 0
s_add_u32 m0, s48, 114688
s_nop 0
global_load_lds_dwordx4 v0, s[40:41] offset:-4096
global_load_lds_dwordx4 v1, s[40:41] offset:-3072
global_load_lds_dwordx4 v2, s[40:41] offset:-2048
global_load_lds_dwordx4 v3, s[40:41] offset:-1024
global_load_lds_dwordx4 v4, s[40:41] offset:0
global_load_lds_dwordx4 v5, s[40:41] offset:1024
global_load_lds_dwordx4 v6, s[40:41] offset:2048
global_load_lds_dwordx4 v7, s[40:41] offset:3072
s_add_u32 m0, s48, 122880
s_nop 0
global_load_lds_dwordx4 v8, s[40:41] offset:-4096
s_add_u32 s40, s40, 73728
s_addc_u32 s41, s41, 0
v_add_u32 v204, 0x10000, v9
v_add_u32 v205, 0x20000, v9
v_add_u32 v206, 0x10000, v10
v_add_u32 v207, 0x20000, v10
s_waitcnt lgkmcnt(0)
s_mul_i32 s37, s37, 0x9E3779B1
s_add_u32 s37, s37, 0x7F4A7C15
v_add_u32 v22, s37, v22
v_add_u32 v24, s37, v24
s_waitcnt vmcnt(27)
s_barrier
ds_read_b64_tr_b16 a[0:1], v9 offset:0
ds_read_b64_tr_b16 a[2:3], v9 offset:2304
ds_read_b64_tr_b16 a[4:5], v9 offset:8
ds_read_b64_tr_b16 a[6:7], v9 offset:2312
ds_read_b64_tr_b16 a[8:9], v9 offset:64
ds_read_b64_tr_b16 a[10:11], v9 offset:2368
ds_read_b64_tr_b16 a[12:13], v9 offset:72
ds_read_b64_tr_b16 a[14:15], v9 offset:2376
ds_read_b64_tr_b16 v[60:61], v10 offset:0
ds_read_b64_tr_b16 v[62:63], v10 offset:2304
ds_read_b64_tr_b16 a[16:17], v9 offset:18432
ds_read_b64_tr_b16 a[18:19], v9 offset:20736
ds_read_b64_tr_b16 a[20:21], v9 offset:18440
ds_read_b64_tr_b16 a[22:23], v9 offset:20744
ds_read_b64_tr_b16 a[24:25], v9 offset:18496
ds_read_b64_tr_b16 a[26:27], v9 offset:20800
ds_read_b64_tr_b16 a[28:29], v9 offset:18504
ds_read_b64_tr_b16 a[30:31], v9 offset:20808
ds_read_b64_tr_b16 v[64:65], v10 offset:18432
ds_read_b64_tr_b16 v[66:67], v10 offset:20736
s_waitcnt lgkmcnt(0)
s_barrier
s_add_u32 m0, s48, 4096
s_nop 0
global_load_lds_dwordx4 v0, s[40:41] offset:-4096
global_load_lds_dwordx4 v1, s[40:41] offset:-3072
global_load_lds_dwordx4 v2, s[40:41] offset:-2048
global_load_lds_dwordx4 v3, s[40:41] offset:-1024
global_load_lds_dwordx4 v4, s[40:41] offset:0
global_load_lds_dwordx4 v5, s[40:41] offset:1024
global_load_lds_dwordx4 v6, s[40:41] offset:2048
global_load_lds_dwordx4 v7, s[40:41] offset:3072
s_add_u32 m0, s48, 12288
s_nop 0
global_load_lds_dwordx4 v8, s[40:41] offset:-4096
s_add_u32 s40, s40, 73728
s_addc_u32 s41, s41, 0
s_waitcnt vmcnt(27)
s_barrier
ds_read_b64_tr_b16 a[32:33], v9 offset:36864
ds_read_b64_tr_b16 a[34:35], v9 offset:39168
ds_read_b64_tr_b16 a[36:37], v9 offset:36872
ds_read_b64_tr_b16 a[38:39], v9 offset:39176
ds_read_b64_tr_b16 a[40:41], v9 offset:36928
ds_read_b64_tr_b16 a[42:43], v9 offset:39232
ds_read_b64_tr_b16 a[44:45], v9 offset:36936
ds_read_b64_tr_b16 a[46:47], v9 offset:39240
ds_read_b64_tr_b16 v[68:69], v10 offset:36864
ds_read_b64_tr_b16 v[70:71], v10 offset:39168
ds_read_b64_tr_b16 a[48:49], v9 offset:55296
ds_read_b64_tr_b16 a[50:51], v9 offset:57600
ds_read_b64_tr_b16 a[52:53], v9 offset:55304
ds_read_b64_tr_b16 a[54:55], v9 offset:57608
ds_read_b64_tr_b16 a[56:57], v9 offset:55360
ds_read_b64_tr_b16 a[58:59], v9 offset:57664
ds_read_b64_tr_b16 a[60:61], v9 offset:55368
ds_read_b64_tr_b16 a[62:63], v9 offset:57672
ds_read_b64_tr_b16 v[72:73], v10 offset:55296
ds_read_b64_tr_b16 v[74:75], v10 offset:57600
s_waitcnt lgkmcnt(0)
s_barrier
s_add_u32 m0, s48, 40960
s_nop 0
global_load_lds_dwordx4 v0, s[40:41] offset:-4096
global_load_lds_dwordx4 v1, s[40:41] offset:-3072
global_load_lds_dwordx4 v2, s[40:41] offset:-2048
global_load_lds_dwordx4 v3, s[40:41] offset:-1024
global_load_lds_dwordx4 v4, s[40:41] offset:0
global_load_lds_dwordx4 v5, s[40:41] offset:1024
global_load_lds_dwordx4 v6, s[40:41] offset:2048
global_load_lds_dwordx4 v7, s[40:41] offset:3072
s_add_u32 m0, s48, 49152
s_nop 0
global_load_lds_dwordx4 v8, s[40:41] offset:-4096
s_add_u32 s40, s40, 73728
s_addc_u32 s41, s41, 0
s_waitcnt vmcnt(27)
s_barrier
ds_read_b64_tr_b16 a[64:65], v204 offset:8192
ds_read_b64_tr_b16 a[66:67], v204 offset:10496
ds_read_b64_tr_b16 a[68:69], v204 offset:8200
ds_read_b64_tr_b16 a[70:71], v204 offset:10504
ds_read_b64_tr_b16 a[72:73], v204 offset:8256
ds_read_b64_tr_b16 a[74:75], v204 offset:10560
ds_read_b64_tr_b16 a[76:77], v204 offset:8264
ds_read_b64_tr_b16 a[78:79], v204 offset:10568
ds_read_b64_tr_b16 v[76:77], v206 offset:8192
ds_read_b64_tr_b16 v[78:79], v206 offset:10496
ds_read_b64_tr_b16 a[80:81], v204 offset:26624
ds_read_b64_tr_b16 a[82:83], v204 offset:28928
ds_read_b64_tr_b16 a[84:85], v204 offset:26632
ds_read_b64_tr_b16 a[86:87], v204 offset:28936
ds_read_b64_tr_b16 a[88:89], v204 offset:26688
ds_read_b64_tr_b16 a[90:91], v204 offset:28992
ds_read_b64_tr_b16 a[92:93], v204 offset:26696
ds_read_b64_tr_b16 a[94:95], v204 offset:29000
ds_read_b64_tr_b16 v[80:81], v206 offset:26624
ds_read_b64_tr_b16 v[82:83], v206 offset:28928
s_waitcnt lgkmcnt(0)
s_barrier
s_add_u32 m0, s48, 77824
s_nop 0
global_load_lds_dwordx4 v0, s[40:41] offset:-4096
global_load_lds_dwordx4 v1, s[40:41] offset:-3072
global_load_lds_dwordx4 v2, s[40:41] offset:-2048
global_load_lds_dwordx4 v3, s[40:41] offset:-1024
global_load_lds_dwordx4 v4, s[40:41] offset:0
global_load_lds_dwordx4 v5, s[40:41] offset:1024
global_load_lds_dwordx4 v6, s[40:41] offset:2048
global_load_lds_dwordx4 v7, s[40:41] offset:3072
s_add_u32 m0, s48, 86016
s_nop 0
global_load_lds_dwordx4 v8, s[40:41] offset:-4096
s_add_u32 s40, s40, 73728
s_addc_u32 s41, s41, 0
s_waitcnt vmcnt(27)
s_barrier
ds_read_b64_tr_b16 a[96:97], v204 offset:45056
ds_read_b64_tr_b16 a[98:99], v204 offset:47360
ds_read_b64_tr_b16 a[100:101], v204 offset:45064
ds_read_b64_tr_b16 a[102:103], v204 offset:47368
ds_read_b64_tr_b16 a[104:105], v204 offset:45120
ds_read_b64_tr_b16 a[106:107], v204 offset:47424
ds_read_b64_tr_b16 a[108:109], v204 offset:45128
ds_read_b64_tr_b16 a[110:111], v204 offset:47432
ds_read_b64_tr_b16 v[84:85], v206 offset:45056
ds_read_b64_tr_b16 v[86:87], v206 offset:47360
ds_read_b64_tr_b16 a[112:113], v204 offset:63488
ds_read_b64_tr_b16 a[114:115], v205 offset:256
ds_read_b64_tr_b16 a[116:117], v204 offset:63496
ds_read_b64_tr_b16 a[118:119], v205 offset:264
ds_read_b64_tr_b16 a[120:121], v204 offset:63552
ds_read_b64_tr_b16 a[122:123], v205 offset:320
ds_read_b64_tr_b16 a[124:125], v204 offset:63560
ds_read_b64_tr_b16 a[126:127], v205 offset:328
ds_read_b64_tr_b16 v[88:89], v206 offset:63488
ds_read_b64_tr_b16 v[90:91], v207 offset:256
s_waitcnt lgkmcnt(0)
s_barrier
s_add_u32 m0, s48, 114688
s_nop 0
global_load_lds_dwordx4 v0, s[40:41] offset:-4096
global_load_lds_dwordx4 v1, s[40:41] offset:-3072
global_load_lds_dwordx4 v2, s[40:41] offset:-2048
global_load_lds_dwordx4 v3, s[40:41] offset:-1024
global_load_lds_dwordx4 v4, s[40:41] offset:0
global_load_lds_dwordx4 v5, s[40:41] offset:1024
global_load_lds_dwordx4 v6, s[40:41] offset:2048
global_load_lds_dwordx4 v7, s[40:41] offset:3072
s_add_u32 m0, s48, 122880
s_nop 0
global_load_lds_dwordx4 v8, s[40:41] offset:-4096
s_add_u32 s40, s40, 73728
s_addc_u32 s41, s41, 0
s_waitcnt vmcnt(27)
s_barrier
ds_read_b64_tr_b16 a[128:129], v9 offset:0
ds_read_b64_tr_b16 a[130:131], v9 offset:2304
ds_read_b64_tr_b16 a[132:133], v9 offset:8
ds_read_b64_tr_b16 a[134:135], v9 offset:2312
ds_read_b64_tr_b16 a[136:137], v9 offset:64
ds_read_b64_tr_b16 a[138:139], v9 offset:2368
ds_read_b64_tr_b16 a[140:141], v9 offset:72
ds_read_b64_tr_b16 a[142:143], v9 offset:2376
ds_read_b64_tr_b16 v[92:93], v10 offset:0
ds_read_b64_tr_b16 v[94:95], v10 offset:2304
ds_read_b64_tr_b16 a[144:145], v9 offset:18432
ds_read_b64_tr_b16 a[146:147], v9 offset:20736
ds_read_b64_tr_b16 a[148:149], v9 offset:18440
ds_read_b64_tr_b16 a[150:151], v9 offset:20744
ds_read_b64_tr_b16 a[152:153], v9 offset:18496
ds_read_b64_tr_b16 a[154:155], v9 offset:20800
ds_read_b64_tr_b16 a[156:157], v9 offset:18504
ds_read_b64_tr_b16 a[158:159], v9 offset:20808
s_waitcnt lgkmcnt(0)
s_barrier
s_add_u32 m0, s48, 4096
s_nop 0
global_load_lds_dwordx4 v0, s[40:41] offset:-4096
global_load_lds_dwordx4 v1, s[40:41] offset:-3072
global_load_lds_dwordx4 v2, s[40:41] offset:-2048
global_load_lds_dwordx4 v3, s[40:41] offset:-1024
global_load_lds_dwordx4 v4, s[40:41] offset:0
global_load_lds_dwordx4 v5, s[40:41] offset:1024
global_load_lds_dwordx4 v6, s[40:41] offset:2048
global_load_lds_dwordx4 v7, s[40:41] offset:3072
s_add_u32 m0, s48, 12288
s_nop 0
global_load_lds_dwordx4 v8, s[40:41] offset:-4096
)"
R"(s_add_u32 s40, s40, 73728
s_addc_u32 s41, s41, 0
s_waitcnt vmcnt(27)
s_barrier
ds_read_b64_tr_b16 a[160:161], v9 offset:36864
ds_read_b64_tr_b16 a[162:163], v9 offset:39168
ds_read_b64_tr_b16 a[164:165], v9 offset:36872
ds_read_b64_tr_b16 a[166:167], v9 offset:39176
ds_read_b64_tr_b16 a[168:169], v9 offset:36928
ds_read_b64_tr_b16 a[170:171], v9 offset:39232
ds_read_b64_tr_b16 a[172:173], v9 offset:36936
ds_read_b64_tr_b16 a[174:175], v9 offset:39240
ds_read_b64_tr_b16 a[176:177], v9 offset:55296
ds_read_b64_tr_b16 a[178:179], v9 offset:57600
ds_read_b64_tr_b16 a[180:181], v9 offset:55304
ds_read_b64_tr_b16 a[182:183], v9 offset:57608
ds_read_b64_tr_b16 a[184:185], v9 offset:55360
ds_read_b64_tr_b16 a[186:187], v9 offset:57664
ds_read_b64_tr_b16 a[188:189], v9 offset:55368
ds_read_b64_tr_b16 a[190:191], v9 offset:57672
s_waitcnt vmcnt(18)
s_barrier
ds_read_b64_tr_b16 a[192:193], v204 offset:8192
ds_read_b64_tr_b16 a[194:195], v204 offset:10496
ds_read_b64_tr_b16 a[196:197], v204 offset:8200
ds_read_b64_tr_b16 a[198:199], v204 offset:10504
ds_read_b64_tr_b16 a[200:201], v204 offset:8256
ds_read_b64_tr_b16 a[202:203], v204 offset:10560
ds_read_b64_tr_b16 a[204:205], v204 offset:8264
ds_read_b64_tr_b16 a[206:207], v204 offset:10568
ds_read_b64_tr_b16 a[208:209], v204 offset:26624
ds_read_b64_tr_b16 a[210:211], v204 offset:28928
ds_read_b64_tr_b16 a[212:213], v204 offset:26632
ds_read_b64_tr_b16 a[214:215], v204 offset:28936
ds_read_b64_tr_b16 a[216:217], v204 offset:26688
ds_read_b64_tr_b16 a[218:219], v204 offset:28992
ds_read_b64_tr_b16 a[220:221], v204 offset:26696
ds_read_b64_tr_b16 a[222:223], v204 offset:29000
s_waitcnt vmcnt(9)
s_barrier
ds_read_b64_tr_b16 a[224:225], v204 offset:45056
ds_read_b64_tr_b16 a[226:227], v204 offset:47360
ds_read_b64_tr_b16 a[228:229], v204 offset:45064
ds_read_b64_tr_b16 a[230:231], v204 offset:47368
ds_read_b64_tr_b16 a[232:233], v204 offset:45120
ds_read_b64_tr_b16 a[234:235], v204 offset:47424
ds_read_b64_tr_b16 a[236:237], v204 offset:45128
ds_read_b64_tr_b16 a[238:239], v204 offset:47432
ds_read_b64_tr_b16 a[240:241], v204 offset:63488
ds_read_b64_tr_b16 a[242:243], v205 offset:256
ds_read_b64_tr_b16 a[244:245], v204 offset:63496
ds_read_b64_tr_b16 a[246:247], v205 offset:264
ds_read_b64_tr_b16 a[248:249], v204 offset:63552
ds_read_b64_tr_b16 a[250:251], v205 offset:320
ds_read_b64_tr_b16 a[252:253], v204 offset:63560
ds_read_b64_tr_b16 a[254:255], v205 offset:328
s_waitcnt vmcnt(0)
s_barrier
ds_read_b64_tr_b16 v[28:29], v9 offset:0
ds_read_b64_tr_b16 v[30:31], v9 offset:2304
ds_read_b64_tr_b16 v[32:33], v9 offset:8
ds_read_b64_tr_b16 v[34:35], v9 offset:2312
ds_read_b64_tr_b16 v[36:37], v9 offset:64
ds_read_b64_tr_b16 v[38:39], v9 offset:2368
ds_read_b64_tr_b16 v[40:41], v9 offset:72
ds_read_b64_tr_b16 v[42:43], v9 offset:2376
ds_read_b64_tr_b16 v[44:45], v9 offset:18432
ds_read_b64_tr_b16 v[46:47], v9 offset:20736
ds_read_b64_tr_b16 v[48:49], v9 offset:18440
ds_read_b64_tr_b16 v[50:51], v9 offset:20744
ds_read_b64_tr_b16 v[52:53], v9 offset:18496
ds_read_b64_tr_b16 v[54:55], v9 offset:20800
ds_read_b64_tr_b16 v[56:57], v9 offset:18504
ds_read_b64_tr_b16 v[58:59], v9 offset:20808
s_waitcnt lgkmcnt(0)
s_barrier
s_mov_b64 s[76:77], 0
s_mov_b32 s70, 33
s_waitcnt vmcnt(0) lgkmcnt(0)
s_nop 7
.p2align 6
.Lwo_top13:
s_waitcnt lgkmcnt(2)
v_mfma_f32_16x16x32_bf16 v[96:99], a[0:3], v[132:135], v[96:99]
ds_read_b128 v[144:147], v25 offset:3072
v_mfma_f32_16x16x32_bf16 v[100:103], a[4:7], v[132:135], v[100:103]
v_mfma_f32_16x16x32_bf16 v[104:107], a[8:11], v[132:135], v[104:107]
v_mfma_f32_16x16x32_bf16 v[108:111], a[12:15], v[132:135], v[108:111]
v_cvt_pk_bf16_f32 v164, v112, v113
v_cvt_pk_bf16_f32 v165, v114, v115
v_mfma_f32_16x16x32_bf16 v[128:131], v[60:63], v[132:135], 0
s_waitcnt lgkmcnt(2)
v_cvt_pk_bf16_f32 v166, v116, v117
v_cvt_pk_bf16_f32 v167, v118, v119
v_mfma_f32_16x16x32_bf16 v[96:99], a[16:19], v[136:139], v[96:99]
ds_read_b128 v[132:135], v25 offset:4096
v_add_f32 v254, v112, v113
v_mfma_f32_16x16x32_bf16 v[100:103], a[20:23], v[136:139], v[100:103]
v_mul_f32 v255, v112, v112
v_fmac_f32 v255, v113, v113
v_mfma_f32_16x16x32_bf16 v[104:107], a[24:27], v[136:139], v[104:107]
global_store_dwordx4 v13, v[164:167], s[38:39] offset:0 sc1
v_mfma_f32_16x16x32_bf16 v[108:111], a[28:31], v[136:139], v[108:111]
v_cvt_pk_bf16_f32 v168, v120, v121
v_cvt_pk_bf16_f32 v169, v122, v123
v_mfma_f32_16x16x32_bf16 v[128:131], v[64:67], v[136:139], v[128:131]
s_waitcnt lgkmcnt(2)
v_cvt_pk_bf16_f32 v170, v124, v125
v_cvt_pk_bf16_f32 v171, v126, v127
v_mfma_f32_16x16x32_bf16 v[96:99], a[32:35], v[140:143], v[96:99]
s_cmp_eq_u32 s70, 32
ds_read_b128 v[136:139], v25 offset:5120
v_add_f32 v254, v254, v114
v_mfma_f32_16x16x32_bf16 v[100:103], a[36:39], v[140:143], v[100:103]
s_cselect_b64 s[42:43], s[44:45], s[42:43]
global_store_dwordx4 v13, v[168:171], s[38:39] offset:1024 sc1
v_mfma_f32_16x16x32_bf16 v[104:107], a[40:43], v[140:143], v[104:107]
s_cmp_eq_u32 s70, 16
v_add_u32 v13, 18688, v13
v_fmac_f32 v255, v114, v114
v_mfma_f32_16x16x32_bf16 v[108:111], a[44:47], v[140:143], v[108:111]
s_cselect_b64 s[42:43], s[46:47], s[42:43]
v_add_f32 v254, v254, v115
v_fmac_f32 v255, v115, v115
v_mfma_f32_16x16x32_bf16 v[128:131], v[68:71], v[140:143], v[128:131]
s_waitcnt lgkmcnt(2)
v_add_f32 v254, v254, v116
v_fmac_f32 v255, v116, v116
v_mfma_f32_16x16x32_bf16 v[96:99], a[48:51], v[144:147], v[96:99]
s_xor_b32 s49, s49, 0x10000
ds_read_b128 v[140:143], v25 offset:6144
v_add_f32 v254, v254, v117
v_mfma_f32_16x16x32_bf16 v[100:103], a[52:55], v[144:147], v[100:103]
s_mov_b32 m0, 51200
v_fmac_f32 v255, v117, v117
v_add_f32 v254, v254, v118
v_mfma_f32_16x16x32_bf16 v[104:107], a[56:59], v[144:147], v[104:107]
v_fmac_f32 v255, v118, v118
v_add_f32 v254, v254, v119
v_mfma_f32_16x16x32_bf16 v[108:111], a[60:63], v[144:147], v[108:111]
v_fmac_f32 v255, v119, v119
v_add_f32 v254, v254, v120
v_mfma_f32_16x16x32_bf16 v[128:131], v[72:75], v[144:147], v[128:131]
s_waitcnt lgkmcnt(2)
v_fmac_f32 v255, v120, v120
v_add_f32 v254, v254, v121
v_mfma_f32_16x16x32_bf16 v[96:99], a[64:67], v[132:135], v[96:99]
ds_read_b128 v[144:147], v25 offset:7168
v_fmac_f32 v255, v121, v121
v_mfma_f32_16x16x32_bf16 v[100:103], a[68:71], v[132:135], v[100:103]
v_add_f32 v254, v254, v122
v_fmac_f32 v255, v122, v122
v_mfma_f32_16x16x32_bf16 v[104:107], a[72:75], v[132:135], v[104:107]
s_waitcnt vmcnt(8)
global_load_lds_dwordx4 v16, s[42:43] offset:0 sc1
ds_read_b128 v[148:151], v19 offset:8192
v_mfma_f32_16x16x32_bf16 v[108:111], a[76:79], v[132:135], v[108:111]
ds_read_b128 v[152:155], v20 offset:8192
v_add_f32 v254, v254, v123
v_mfma_f32_16x16x32_bf16 v[128:131], v[76:79], v[132:135], v[128:131]
s_waitcnt lgkmcnt(4)
v_fmac_f32 v255, v123, v123
v_add_f32 v254, v254, v124
v_mfma_f32_16x16x32_bf16 v[96:99], a[80:83], v[136:139], v[96:99]
ds_read_b128 v[132:135], v25 offset:8192
v_fmac_f32 v255, v124, v124
v_mfma_f32_16x16x32_bf16 v[100:103], a[84:87], v[136:139], v[100:103]
v_add_f32 v254, v254, v125
v_fmac_f32 v255, v125, v125
v_mfma_f32_16x16x32_bf16 v[104:107], a[88:91], v[136:139], v[104:107]
v_add_f32 v254, v254, v126
v_fmac_f32 v255, v126, v126
v_mfma_f32_16x16x32_bf16 v[108:111], a[92:95], v[136:139], v[108:111]
v_add_f32 v254, v254, v127
v_fmac_f32 v255, v127, v127
v_mfma_f32_16x16x32_bf16 v[128:131], v[80:83], v[136:139], v[128:131]
s_waitcnt lgkmcnt(4)
global_load_lds_dwordx4 v17, s[42:43] offset:1024 sc1
v_mfma_f32_16x16x32_bf16 v[96:99], a[96:99], v[140:143], v[96:99]
ds_read_b128 v[136:139], v25 offset:9216
v_mfma_f32_16x16x32_bf16 v[100:103], a[100:103], v[140:143], v[100:103]
ds_write_b64 v27, v[254:255] offset:46080
v_mfma_f32_16x16x32_bf16 v[104:107], a[104:107], v[140:143], v[104:107]
s_waitcnt lgkmcnt(4)
v_lshlrev_b32 v112, 16, v148
v_and_b32 v113, 0xffff0000, v148
v_mfma_f32_16x16x32_bf16 v[108:111], a[108:111], v[140:143], v[108:111]
v_lshlrev_b32 v114, 16, v149
v_and_b32 v115, 0xffff0000, v149
v_mfma_f32_16x16x32_bf16 v[128:131], v[84:87], v[140:143], v[128:131]
v_lshlrev_b32 v116, 16, v150
v_and_b32 v117, 0xffff0000, v150
v_mfma_f32_16x16x32_bf16 v[96:99], a[112:115], v[144:147], v[96:99]
s_add_u32 m0, s49, 0
ds_read_b128 v[140:143], v25 offset:10240
v_lshlrev_b32 v118, 16, v151
v_mfma_f32_16x16x32_bf16 v[100:103], a[116:119], v[144:147], v[100:103]
s_waitcnt lgkmcnt(4)
v_and_b32 v119, 0xffff0000, v151
v_lshlrev_b32 v120, 16, v152
v_mfma_f32_16x16x32_bf16 v[104:107], a[120:123], v[144:147], v[104:107]
s_sub_u32 s42, s42, 18432
v_and_b32 v121, 0xffff0000, v152
v_lshlrev_b32 v122, 16, v153
v_mfma_f32_16x16x32_bf16 v[108:111], a[124:127], v[144:147], v[108:111]
s_subb_u32 s43, s43, 0
v_and_b32 v123, 0xffff0000, v153
v_lshlrev_b32 v124, 16, v154
v_mfma_f32_16x16x32_bf16 v[128:131], v[88:91], v[144:147], v[128:131]
s_waitcnt lgkmcnt(3)
v_and_b32 v125, 0xffff0000, v154
v_lshlrev_b32 v126, 16, v155
v_mfma_f32_16x16x32_bf16 v[96:99], a[128:131], v[132:135], v[96:99]
ds_read_b128 v[144:147], v25 offset:11264
v_and_b32 v127, 0xffff0000, v155
v_mfma_f32_16x16x32_bf16 v[100:103], a[132:135], v[132:135], v[100:103]
v_mfma_f32_16x16x32_bf16 v[104:107], a[136:139], v[132:135], v[104:107]
v_mfma_f32_16x16x32_bf16 v[108:111], a[140:143], v[132:135], v[108:111]
v_mfma_f32_16x16x32_bf16 v[128:131], v[92:95], v[132:135], v[128:131]
s_waitcnt lgkmcnt(3)
ds_read_b64 v[176:177], v27 offset:43008
ds_read_b64 v[180:181], v27 offset:43520
v_mfma_f32_16x16x32_bf16 v[96:99], a[144:147], v[136:139], v[96:99]
ds_read_b128 v[132:135], v25 offset:12288
ds_read_b64 v[184:185], v27 offset:44032
v_mfma_f32_16x16x32_bf16 v[100:103], a[148:151], v[136:139], v[100:103]
ds_read_b64 v[188:189], v27 offset:44544
v_mfma_f32_16x16x32_bf16 v[104:107], a[152:155], v[136:139], v[104:107]
v_mfma_f32_16x16x32_bf16 v[108:111], a[156:159], v[136:139], v[108:111]
s_waitcnt lgkmcnt(6)
v_mfma_f32_16x16x32_bf16 v[96:99], a[160:163], v[140:143], v[96:99]
ds_read_b128 v[136:139], v25 offset:13312
v_mfma_f32_16x16x32_bf16 v[100:103], a[164:167], v[140:143], v[100:103]
ds_write_b128 v26, v[128:131] offset:38912
v_mfma_f32_16x16x32_bf16 v[104:107], a[168:171], v[140:143], v[104:107]
v_mfma_f32_16x16x32_bf16 v[108:111], a[172:175], v[140:143], v[108:111]
s_waitcnt lgkmcnt(5)
v_add_f32 v176, v176, v180
v_add_f32 v177, v177, v181
v_mfma_f32_16x16x32_bf16 v[96:99], a[176:179], v[144:147], v[96:99]
s_waitcnt lgkmcnt(2)
ds_read_b128 v[140:143], v25 offset:14336
v_add_f32 v184, v184, v188
v_mfma_f32_16x16x32_bf16 v[100:103], a[180:183], v[144:147], v[100:103]
v_mfma_f32_16x16x32_bf16 v[104:107], a[184:187], v[144:147], v[104:107]
s_waitcnt lgkmcnt(1)
s_barrier
v_mfma_f32_16x16x32_bf16 v[108:111], a[188:191], v[144:147], v[108:111]
v_add_f32 v185, v185, v189
v_add_f32 v176, v176, v184
v_mfma_f32_16x16x32_bf16 v[96:99], a[192:195], v[132:135], v[96:99]
ds_read_b128 v[144:147], v25 offset:15360
v_add_f32 v177, v177, v185
v_mfma_f32_16x16x32_bf16 v[100:103], a[196:199], v[132:135], v[100:103]
global_load_lds_dwordx4 v11, s[38:39] offset:-2048 sc1
v_mov_b32 v202, v176
v_mfma_f32_16x16x32_bf16 v[104:107], a[200:203], v[132:135], v[104:107]
v_mov_b32 v203, v177
v_mfma_f32_16x16x32_bf16 v[108:111], a[204:207], v[132:135], v[108:111]
v_permlane16_swap_b32 v176, v202
v_mfma_f32_16x16x32_bf16 v[96:99], a[208:211], v[136:139], v[96:99]
ds_read_b128 v[132:135], v25 offset:16384
global_load_lds_dwordx4 v11, s[38:39] offset:-1024 sc1
v_mfma_f32_16x16x32_bf16 v[100:103], a[212:215], v[136:139], v[100:103]
v_permlane16_swap_b32 v177, v203
v_mfma_f32_16x16x32_bf16 v[104:107], a[216:219], v[136:139], v[104:107]
v_add_f32 v176, v176, v202
v_add_f32 v177, v177, v203
v_mfma_f32_16x16x32_bf16 v[108:111], a[220:223], v[136:139], v[108:111]
s_waitcnt lgkmcnt(2)
global_load_lds_dwordx4 v11, s[38:39] offset:0 sc1
v_mov_b32 v202, v176
v_mfma_f32_16x16x32_bf16 v[96:99], a[224:227], v[140:143], v[96:99]
ds_read_b128 v[136:139], v25 offset:17408
v_mov_b32 v203, v177
v_mfma_f32_16x16x32_bf16 v[100:103], a[228:231], v[140:143], v[100:103]
v_permlane32_swap_b32 v176, v202
v_mfma_f32_16x16x32_bf16 v[104:107], a[232:235], v[140:143], v[104:107]
global_load_lds_dwordx4 v11, s[38:39] offset:1024 sc1
v_mfma_f32_16x16x32_bf16 v[108:111], a[236:239], v[140:143], v[108:111]
s_waitcnt lgkmcnt(2)
v_permlane32_swap_b32 v177, v203
v_mfma_f32_16x16x32_bf16 v[96:99], a[240:243], v[144:147], v[96:99]
v_add_f32 v176, v176, v202
v_add_f32 v177, v177, v203
v_mfma_f32_16x16x32_bf16 v[100:103], a[244:247], v[144:147], v[100:103]
global_load_lds_dwordx4 v11, s[38:39] offset:2048 sc1
v_add_u32 v11, 18432, v11
v_mfma_f32_16x16x32_bf16 v[104:107], a[248:251], v[144:147], v[104:107]
v_mfma_f32_16x16x32_bf16 v[108:111], a[252:255], v[144:147], v[108:111]
s_waitcnt vmcnt(7)
s_barrier
s_waitcnt lgkmcnt(1)
v_mfma_f32_16x16x32_bf16 v[96:99], v[28:31], v[132:135], v[96:99]
ds_read_b128 v[140:143], v25 offset:18432
ds_read_b128 v[144:147], v25 offset:19456
v_mfma_f32_16x16x32_bf16 v[100:103], v[32:35], v[132:135], v[100:103]
v_min_u32 v11, v11, v12
s_mov_b64 exec, 65535
global_store_dwordx2 v15, v[176:177], s[38:39] sc1
s_mov_b64 exec, -1
v_mfma_f32_16x16x32_bf16 v[104:107], v[36:39], v[132:135], v[104:107]
v_add_u32 v15, 18688, v15
v_mfma_f32_16x16x32_bf16 v[108:111], v[40:43], v[132:135], v[108:111]
s_waitcnt lgkmcnt(2)
v_mfma_f32_16x16x32_bf16 v[96:99], v[44:47], v[136:139], v[96:99]
ds_read_b128 v[132:135], v25 offset:20480
v_mfma_f32_16x16x32_bf16 v[100:103], v[48:51], v[136:139], v[100:103]
v_mfma_f32_16x16x32_bf16 v[104:107], v[52:55], v[136:139], v[104:107]
v_mfma_f32_16x16x32_bf16 v[108:111], v[56:59], v[136:139], v[108:111]
s_waitcnt lgkmcnt(2)
v_mfma_f32_16x16x32_bf16 v[112:115], a[0:3], v[140:143], v[112:115]
ds_read_b128 v[136:139], v25 offset:21504
v_mfma_f32_16x16x32_bf16 v[116:119], a[4:7], v[140:143], v[116:119]
v_mfma_f32_16x16x32_bf16 v[120:123], a[8:11], v[140:143], v[120:123]
v_mfma_f32_16x16x32_bf16 v[124:127], a[12:15], v[140:143], v[124:127]
v_cvt_pk_bf16_f32 v164, v96, v97
v_cvt_pk_bf16_f32 v165, v98, v99
v_mfma_f32_16x16x32_bf16 v[128:131], v[60:63], v[140:143], 0
s_waitcnt lgkmcnt(2)
v_cvt_pk_bf16_f32 v166, v100, v101
v_cvt_pk_bf16_f32 v167, v102, v103
v_mfma_f32_16x16x32_bf16 v[112:115], a[16:19], v[144:147], v[112:115]
ds_read_b128 v[140:143], v25 offset:22528
v_add_f32 v254, v96, v97
v_mfma_f32_16x16x32_bf16 v[116:119], a[20:23], v[144:147], v[116:119]
v_mul_f32 v255, v96, v96
v_fmac_f32 v255, v97, v97
v_mfma_f32_16x16x32_bf16 v[120:123], a[24:27], v[144:147], v[120:123]
global_store_dwordx4 v13, v[164:167], s[38:39] offset:0 sc1
v_mfma_f32_16x16x32_bf16 v[124:127], a[28:31], v[144:147], v[124:127]
v_cvt_pk_bf16_f32 v168, v104, v105
v_cvt_pk_bf16_f32 v169, v106, v107
v_mfma_f32_16x16x32_bf16 v[128:131], v[64:67], v[144:147], v[128:131]
s_waitcnt lgkmcnt(2)
v_cvt_pk_bf16_f32 v170, v108, v109
v_cvt_pk_bf16_f32 v171, v110, v111
v_mfma_f32_16x16x32_bf16 v[112:115], a[32:35], v[132:135], v[112:115]
s_mov_b32 m0, 59392
ds_read_b128 v[144:147], v25 offset:23552
v_add_f32 v254, v254, v98
v_mfma_f32_16x16x32_bf16 v[116:119], a[36:39], v[132:135], v[116:119]
global_store_dwordx4 v13, v[168:171], s[38:39] offset:1024 sc1
v_mfma_f32_16x16x32_bf16 v[120:123], a[40:43], v[132:135], v[120:123]
v_add_u32 v13, 18688, v13
v_fmac_f32 v255, v98, v98
v_mfma_f32_16x16x32_bf16 v[124:127], a[44:47], v[132:135], v[124:127]
v_add_f32 v254, v254, v99
v_fmac_f32 v255, v99, v99
v_mfma_f32_16x16x32_bf16 v[128:131], v[68:71], v[132:135], v[128:131]
s_waitcnt lgkmcnt(2)
v_add_f32 v254, v254, v100
v_fmac_f32 v255, v100, v100
v_mfma_f32_16x16x32_bf16 v[112:115], a[48:51], v[136:139], v[112:115]
ds_read_b128 v[132:135], v25 offset:24576
v_add_f32 v254, v254, v101
v_mfma_f32_16x16x32_bf16 v[116:119], a[52:55], v[136:139], v[116:119]
v_fmac_f32 v255, v101, v101
v_add_f32 v254, v254, v102
v_mfma_f32_16x16x32_bf16 v[120:123], a[56:59], v[136:139], v[120:123]
v_fmac_f32 v255, v102, v102
v_add_f32 v254, v254, v103
v_mfma_f32_16x16x32_bf16 v[124:127], a[60:63], v[136:139], v[124:127]
v_fmac_f32 v255, v103, v103
v_add_f32 v254, v254, v104
v_mfma_f32_16x16x32_bf16 v[128:131], v[72:75], v[136:139], v[128:131]
s_waitcnt lgkmcnt(2)
v_fmac_f32 v255, v104, v104
v_add_f32 v254, v254, v105
v_mfma_f32_16x16x32_bf16 v[112:115], a[64:67], v[140:143], v[112:115]
ds_read_b128 v[136:139], v25 offset:25600
v_fmac_f32 v255, v105, v105
v_mfma_f32_16x16x32_bf16 v[116:119], a[68:71], v[140:143], v[116:119]
v_add_f32 v254, v254, v106
v_fmac_f32 v255, v106, v106
v_mfma_f32_16x16x32_bf16 v[120:123], a[72:75], v[140:143], v[120:123]
s_waitcnt vmcnt(8)
global_load_lds_dwordx4 v16, s[42:43] offset:0 sc1
ds_read_b128 v[148:151], v19 offset:0
v_mfma_f32_16x16x32_bf16 v[124:127], a[76:79], v[140:143], v[124:127]
ds_read_b128 v[152:155], v20 offset:0
v_add_f32 v254, v254, v107
v_mfma_f32_16x16x32_bf16 v[128:131], v[76:79], v[140:143], v[128:131]
s_waitcnt lgkmcnt(4)
v_fmac_f32 v255, v107, v107
v_add_f32 v254, v254, v108
v_mfma_f32_16x16x32_bf16 v[112:115], a[80:83], v[144:147], v[112:115]
ds_read_b128 v[140:143], v25 offset:26624
v_fmac_f32 v255, v108, v108
v_mfma_f32_16x16x32_bf16 v[116:119], a[84:87], v[144:147], v[116:119]
v_add_f32 v254, v254, v109
v_fmac_f32 v255, v109, v109
v_mfma_f32_16x16x32_bf16 v[120:123], a[88:91], v[144:147], v[120:123]
v_add_f32 v254, v254, v110
v_fmac_f32 v255, v110, v110
v_mfma_f32_16x16x32_bf16 v[124:127], a[92:95], v[144:147], v[124:127]
v_add_f32 v254, v254, v111
v_fmac_f32 v255, v111, v111
v_mfma_f32_16x16x32_bf16 v[128:131], v[80:83], v[144:147], v[128:131]
s_waitcnt lgkmcnt(4)
)"
R"(global_load_lds_dwordx4 v17, s[42:43] offset:1024 sc1
v_mfma_f32_16x16x32_bf16 v[112:115], a[96:99], v[132:135], v[112:115]
ds_read_b128 v[144:147], v25 offset:27648
v_mfma_f32_16x16x32_bf16 v[116:119], a[100:103], v[132:135], v[116:119]
ds_write_b64 v27, v[254:255] offset:44032
v_mfma_f32_16x16x32_bf16 v[120:123], a[104:107], v[132:135], v[120:123]
s_waitcnt lgkmcnt(4)
v_lshlrev_b32 v96, 16, v148
v_and_b32 v97, 0xffff0000, v148
v_mfma_f32_16x16x32_bf16 v[124:127], a[108:111], v[132:135], v[124:127]
v_lshlrev_b32 v98, 16, v149
v_and_b32 v99, 0xffff0000, v149
v_mfma_f32_16x16x32_bf16 v[128:131], v[84:87], v[132:135], v[128:131]
v_lshlrev_b32 v100, 16, v150
v_and_b32 v101, 0xffff0000, v150
v_mfma_f32_16x16x32_bf16 v[112:115], a[112:115], v[136:139], v[112:115]
s_add_u32 m0, s49, 18432
ds_read_b128 v[132:135], v25 offset:28672
v_lshlrev_b32 v102, 16, v151
v_mfma_f32_16x16x32_bf16 v[116:119], a[116:119], v[136:139], v[116:119]
s_waitcnt lgkmcnt(4)
v_and_b32 v103, 0xffff0000, v151
v_lshlrev_b32 v104, 16, v152
v_mfma_f32_16x16x32_bf16 v[120:123], a[120:123], v[136:139], v[120:123]
s_sub_u32 s42, s42, 18432
v_and_b32 v105, 0xffff0000, v152
v_lshlrev_b32 v106, 16, v153
v_mfma_f32_16x16x32_bf16 v[124:127], a[124:127], v[136:139], v[124:127]
s_subb_u32 s43, s43, 0
v_and_b32 v107, 0xffff0000, v153
v_lshlrev_b32 v108, 16, v154
v_mfma_f32_16x16x32_bf16 v[128:131], v[88:91], v[136:139], v[128:131]
s_waitcnt lgkmcnt(3)
v_and_b32 v109, 0xffff0000, v154
v_lshlrev_b32 v110, 16, v155
v_mfma_f32_16x16x32_bf16 v[112:115], a[128:131], v[140:143], v[112:115]
ds_read_b128 v[136:139], v25 offset:29696
v_and_b32 v111, 0xffff0000, v155
v_mfma_f32_16x16x32_bf16 v[116:119], a[132:135], v[140:143], v[116:119]
v_mfma_f32_16x16x32_bf16 v[120:123], a[136:139], v[140:143], v[120:123]
v_mfma_f32_16x16x32_bf16 v[124:127], a[140:143], v[140:143], v[124:127]
v_mfma_f32_16x16x32_bf16 v[128:131], v[92:95], v[140:143], v[128:131]
s_waitcnt lgkmcnt(3)
ds_read_b64 v[176:177], v27 offset:45056
ds_read_b64 v[180:181], v27 offset:45568
v_mfma_f32_16x16x32_bf16 v[112:115], a[144:147], v[144:147], v[112:115]
ds_read_b128 v[140:143], v25 offset:30720
ds_read_b64 v[184:185], v27 offset:46080
v_mfma_f32_16x16x32_bf16 v[116:119], a[148:151], v[144:147], v[116:119]
ds_read_b64 v[188:189], v27 offset:46592
v_mfma_f32_16x16x32_bf16 v[120:123], a[152:155], v[144:147], v[120:123]
v_mfma_f32_16x16x32_bf16 v[124:127], a[156:159], v[144:147], v[124:127]
s_waitcnt lgkmcnt(6)
v_mfma_f32_16x16x32_bf16 v[112:115], a[160:163], v[132:135], v[112:115]
ds_read_b128 v[144:147], v25 offset:31744
v_mfma_f32_16x16x32_bf16 v[116:119], a[164:167], v[132:135], v[116:119]
ds_write_b128 v26, v[128:131] offset:38912
v_mfma_f32_16x16x32_bf16 v[120:123], a[168:171], v[132:135], v[120:123]
v_mfma_f32_16x16x32_bf16 v[124:127], a[172:175], v[132:135], v[124:127]
s_waitcnt lgkmcnt(5)
v_add_f32 v176, v176, v180
v_add_f32 v177, v177, v181
v_mfma_f32_16x16x32_bf16 v[112:115], a[176:179], v[136:139], v[112:115]
s_waitcnt lgkmcnt(2)
ds_read_b128 v[132:135], v25 offset:32768
v_add_f32 v184, v184, v188
v_mfma_f32_16x16x32_bf16 v[116:119], a[180:183], v[136:139], v[116:119]
v_mfma_f32_16x16x32_bf16 v[120:123], a[184:187], v[136:139], v[120:123]
s_waitcnt lgkmcnt(1)
s_barrier
v_mfma_f32_16x16x32_bf16 v[124:127], a[188:191], v[136:139], v[124:127]
v_add_f32 v185, v185, v189
v_add_f32 v176, v176, v184
v_mfma_f32_16x16x32_bf16 v[112:115], a[192:195], v[140:143], v[112:115]
s_mov_b64 s[76:77], -1
ds_read_b128 v[136:139], v25 offset:33792
v_add_f32 v177, v177, v185
v_mfma_f32_16x16x32_bf16 v[116:119], a[196:199], v[140:143], v[116:119]
global_load_lds_dwordx4 v11, s[38:39] offset:-2048 sc1
v_mov_b32 v202, v176
v_mfma_f32_16x16x32_bf16 v[120:123], a[200:203], v[140:143], v[120:123]
v_mov_b32 v203, v177
v_mfma_f32_16x16x32_bf16 v[124:127], a[204:207], v[140:143], v[124:127]
v_permlane16_swap_b32 v176, v202
v_mfma_f32_16x16x32_bf16 v[112:115], a[208:211], v[144:147], v[112:115]
ds_read_b128 v[140:143], v25 offset:34816
global_load_lds_dwordx4 v11, s[38:39] offset:-1024 sc1
v_mfma_f32_16x16x32_bf16 v[116:119], a[212:215], v[144:147], v[116:119]
v_permlane16_swap_b32 v177, v203
v_mfma_f32_16x16x32_bf16 v[120:123], a[216:219], v[144:147], v[120:123]
v_add_f32 v176, v176, v202
v_add_f32 v177, v177, v203
v_mfma_f32_16x16x32_bf16 v[124:127], a[220:223], v[144:147], v[124:127]
s_waitcnt lgkmcnt(2)
global_load_lds_dwordx4 v11, s[38:39] offset:0 sc1
v_mov_b32 v202, v176
v_mfma_f32_16x16x32_bf16 v[112:115], a[224:227], v[132:135], v[112:115]
ds_read_b128 v[144:147], v25 offset:35840
v_mov_b32 v203, v177
v_mfma_f32_16x16x32_bf16 v[116:119], a[228:231], v[132:135], v[116:119]
v_permlane32_swap_b32 v176, v202
v_mfma_f32_16x16x32_bf16 v[120:123], a[232:235], v[132:135], v[120:123]
global_load_lds_dwordx4 v11, s[38:39] offset:1024 sc1
v_mfma_f32_16x16x32_bf16 v[124:127], a[236:239], v[132:135], v[124:127]
s_waitcnt lgkmcnt(2)
v_permlane32_swap_b32 v177, v203
v_mfma_f32_16x16x32_bf16 v[112:115], a[240:243], v[136:139], v[112:115]
v_add_f32 v176, v176, v202
v_add_f32 v177, v177, v203
v_mfma_f32_16x16x32_bf16 v[116:119], a[244:247], v[136:139], v[116:119]
v_xor_b32 v25, 0x10000, v25
global_load_lds_dwordx4 v11, s[38:39] offset:2048 sc1
v_mfma_f32_16x16x32_bf16 v[120:123], a[248:251], v[136:139], v[120:123]
v_mfma_f32_16x16x32_bf16 v[124:127], a[252:255], v[136:139], v[124:127]
s_waitcnt vmcnt(7)
s_barrier
s_waitcnt lgkmcnt(1)
v_mfma_f32_16x16x32_bf16 v[112:115], v[28:31], v[140:143], v[112:115]
ds_read_b128 v[132:135], v25 offset:0
ds_read_b128 v[136:139], v25 offset:1024
v_mfma_f32_16x16x32_bf16 v[116:119], v[32:35], v[140:143], v[116:119]
v_add_u32 v11, 18432, v11
v_min_u32 v11, v11, v12
v_mfma_f32_16x16x32_bf16 v[120:123], v[36:39], v[140:143], v[120:123]
s_mov_b64 exec, 65535
global_store_dwordx2 v15, v[176:177], s[38:39] sc1
s_mov_b64 exec, -1
v_add_u32 v15, 18688, v15
v_mfma_f32_16x16x32_bf16 v[124:127], v[40:43], v[140:143], v[124:127]
s_waitcnt lgkmcnt(2)
v_mfma_f32_16x16x32_bf16 v[112:115], v[44:47], v[144:147], v[112:115]
ds_read_b128 v[140:143], v25 offset:2048
v_mfma_f32_16x16x32_bf16 v[116:119], v[48:51], v[144:147], v[116:119]
v_mfma_f32_16x16x32_bf16 v[120:123], v[52:55], v[144:147], v[120:123]
v_mfma_f32_16x16x32_bf16 v[124:127], v[56:59], v[144:147], v[124:127]
s_sub_u32 s70, s70, 1
s_cbranch_scc0 .Lwo_top13
s_nop 1
v_cvt_pk_bf16_f32 v164, v112, v113
v_cvt_pk_bf16_f32 v165, v114, v115
v_cvt_pk_bf16_f32 v166, v116, v117
v_cvt_pk_bf16_f32 v167, v118, v119
global_store_dwordx4 v13, v[164:167], s[38:39] offset:0 sc1
v_cvt_pk_bf16_f32 v168, v120, v121
v_cvt_pk_bf16_f32 v169, v122, v123
v_cvt_pk_bf16_f32 v170, v124, v125
v_cvt_pk_bf16_f32 v171, v126, v127
global_store_dwordx4 v13, v[168:171], s[38:39] offset:1024 sc1
v_add_f32 v254, v112, v113
v_mul_f32 v255, v112, v112
v_fmac_f32 v255, v113, v113
v_add_f32 v254, v254, v114
v_fmac_f32 v255, v114, v114
v_add_f32 v254, v254, v115
v_fmac_f32 v255, v115, v115
v_add_f32 v254, v254, v116
v_fmac_f32 v255, v116, v116
v_add_f32 v254, v254, v117
v_fmac_f32 v255, v117, v117
v_add_f32 v254, v254, v118
v_fmac_f32 v255, v118, v118
v_add_f32 v254, v254, v119
v_fmac_f32 v255, v119, v119
v_add_f32 v254, v254, v120
v_fmac_f32 v255, v120, v120
v_add_f32 v254, v254, v121
v_fmac_f32 v255, v121, v121
v_add_f32 v254, v254, v122
v_fmac_f32 v255, v122, v122
v_add_f32 v254, v254, v123
v_fmac_f32 v255, v123, v123
v_add_f32 v254, v254, v124
v_fmac_f32 v255, v124, v124
v_add_f32 v254, v254, v125
v_fmac_f32 v255, v125, v125
v_add_f32 v254, v254, v126
v_fmac_f32 v255, v126, v126
v_add_f32 v254, v254, v127
v_fmac_f32 v255, v127, v127
ds_write_b64 v27, v[254:255] offset:46080
s_waitcnt vmcnt(0) lgkmcnt(0)
s_barrier
ds_read_b64 v[176:177], v27 offset:43008
ds_read_b64 v[180:181], v27 offset:43520
ds_read_b64 v[184:185], v27 offset:44032
ds_read_b64 v[188:189], v27 offset:44544
s_waitcnt lgkmcnt(2)
v_add_f32 v176, v176, v180
v_add_f32 v177, v177, v181
s_waitcnt lgkmcnt(0)
v_add_f32 v184, v184, v188
v_add_f32 v185, v185, v189
v_add_f32 v176, v176, v184
v_add_f32 v177, v177, v185
v_mov_b32 v202, v176
v_mov_b32 v203, v177
s_nop 0
v_permlane16_swap_b32 v176, v202
v_permlane16_swap_b32 v177, v203
v_add_f32 v176, v176, v202
v_add_f32 v177, v177, v203
v_mov_b32 v202, v176
v_mov_b32 v203, v177
s_nop 0
v_permlane32_swap_b32 v176, v202
v_permlane32_swap_b32 v177, v203
v_add_f32 v176, v176, v202
v_add_f32 v177, v177, v203
s_mov_b64 exec, 65535
global_store_dwordx2 v15, v[176:177], s[38:39] sc1
s_mov_b64 exec, -1
v_add_u32 v15, 18688, v15
ds_read_b64 v[176:177], v27 offset:45056
ds_read_b64 v[180:181], v27 offset:45568
ds_read_b64 v[184:185], v27 offset:46080
ds_read_b64 v[188:189], v27 offset:46592
s_waitcnt lgkmcnt(2)
v_add_f32 v176, v176, v180
v_add_f32 v177, v177, v181
s_waitcnt lgkmcnt(0)
v_add_f32 v184, v184, v188
v_add_f32 v185, v185, v189
v_add_f32 v176, v176, v184
v_add_f32 v177, v177, v185
v_mov_b32 v202, v176
v_mov_b32 v203, v177
s_nop 0
v_permlane16_swap_b32 v176, v202
v_permlane16_swap_b32 v177, v203
v_add_f32 v176, v176, v202
v_add_f32 v177, v177, v203
v_mov_b32 v202, v176
v_mov_b32 v203, v177
s_nop 0
v_permlane32_swap_b32 v176, v202
v_permlane32_swap_b32 v177, v203
v_add_f32 v176, v176, v202
v_add_f32 v177, v177, v203
s_mov_b64 exec, 65535
global_store_dwordx2 v15, v[176:177], s[38:39] sc1
s_mov_b64 exec, -1
v_add_u32 v15, 18688, v15
s_waitcnt vmcnt(0)
s_barrier
s_endpgm
.Lwo_w4:
s_add_u32 m0, s48, 4096
s_nop 0
global_load_lds_dwordx4 v0, s[40:41] offset:-4096
global_load_lds_dwordx4 v1, s[40:41] offset:-3072
global_load_lds_dwordx4 v2, s[40:41] offset:-2048
global_load_lds_dwordx4 v3, s[40:41] offset:-1024
global_load_lds_dwordx4 v4, s[40:41] offset:0
global_load_lds_dwordx4 v5, s[40:41] offset:1024
global_load_lds_dwordx4 v6, s[40:41] offset:2048
global_load_lds_dwordx4 v7, s[40:41] offset:3072
s_add_u32 m0, s48, 12288
s_nop 0
global_load_lds_dwordx4 v8, s[40:41] offset:-4096
s_add_u32 s40, s40, 73728
s_addc_u32 s41, s41, 0
s_add_u32 m0, s48, 40960
s_nop 0
global_load_lds_dwordx4 v0, s[40:41] offset:-4096
global_load_lds_dwordx4 v1, s[40:41] offset:-3072
global_load_lds_dwordx4 v2, s[40:41] offset:-2048
global_load_lds_dwordx4 v3, s[40:41] offset:-1024
global_load_lds_dwordx4 v4, s[40:41] offset:0
global_load_lds_dwordx4 v5, s[40:41] offset:1024
global_load_lds_dwordx4 v6, s[40:41] offset:2048
global_load_lds_dwordx4 v7, s[40:41] offset:3072
s_add_u32 m0, s48, 49152
s_nop 0
global_load_lds_dwordx4 v8, s[40:41] offset:-4096
s_add_u32 s40, s40, 73728
s_addc_u32 s41, s41, 0
s_add_u32 m0, s48, 77824
s_nop 0
global_load_lds_dwordx4 v0, s[40:41] offset:-4096
global_load_lds_dwordx4 v1, s[40:41] offset:-3072
global_load_lds_dwordx4 v2, s[40:41] offset:-2048
global_load_lds_dwordx4 v3, s[40:41] offset:-1024
global_load_lds_dwordx4 v4, s[40:41] offset:0
global_load_lds_dwordx4 v5, s[40:41] offset:1024
global_load_lds_dwordx4 v6, s[40:41] offset:2048
global_load_lds_dwordx4 v7, s[40:41] offset:3072
s_add_u32 m0, s48, 86016
s_nop 0
global_load_lds_dwordx4 v8, s[40:41] offset:-4096
s_add_u32 s40, s40, 73728
s_addc_u32 s41, s41, 0
s_add_u32 m0, s48, 114688
s_nop 0
global_load_lds_dwordx4 v0, s[40:41] offset:-4096
global_load_lds_dwordx4 v1, s[40:41] offset:-3072
global_load_lds_dwordx4 v2, s[40:41] offset:-2048
global_load_lds_dwordx4 v3, s[40:41] offset:-1024
global_load_lds_dwordx4 v4, s[40:41] offset:0
global_load_lds_dwordx4 v5, s[40:41] offset:1024
global_load_lds_dwordx4 v6, s[40:41] offset:2048
global_load_lds_dwordx4 v7, s[40:41] offset:3072
s_add_u32 m0, s48, 122880
s_nop 0
global_load_lds_dwordx4 v8, s[40:41] offset:-4096
s_add_u32 s40, s40, 73728
s_addc_u32 s41, s41, 0
v_add_u32 v204, 0x10000, v9
v_add_u32 v205, 0x20000, v9
v_add_u32 v206, 0x10000, v10
v_add_u32 v207, 0x20000, v10
s_waitcnt lgkmcnt(0)
s_mul_i32 s37, s37, 0x9E3779B1
s_add_u32 s37, s37, 0x7F4A7C15
v_add_u32 v22, s37, v22
v_add_u32 v24, s37, v24
s_waitcnt vmcnt(27)
s_barrier
ds_read_b64_tr_b16 a[0:1], v9 offset:0
ds_read_b64_tr_b16 a[2:3], v9 offset:2304
ds_read_b64_tr_b16 a[4:5], v9 offset:8
ds_read_b64_tr_b16 a[6:7], v9 offset:2312
ds_read_b64_tr_b16 a[8:9], v9 offset:64
ds_read_b64_tr_b16 a[10:11], v9 offset:2368
ds_read_b64_tr_b16 a[12:13], v9 offset:72
ds_read_b64_tr_b16 a[14:15], v9 offset:2376
ds_read_b64_tr_b16 a[16:17], v9 offset:18432
ds_read_b64_tr_b16 a[18:19], v9 offset:20736
ds_read_b64_tr_b16 a[20:21], v9 offset:18440
ds_read_b64_tr_b16 a[22:23], v9 offset:20744
ds_read_b64_tr_b16 a[24:25], v9 offset:18496
ds_read_b64_tr_b16 a[26:27], v9 offset:20800
ds_read_b64_tr_b16 a[28:29], v9 offset:18504
ds_read_b64_tr_b16 a[30:31], v9 offset:20808
s_waitcnt lgkmcnt(0)
s_barrier
s_add_u32 m0, s48, 4096
s_nop 0
global_load_lds_dwordx4 v0, s[40:41] offset:-4096
global_load_lds_dwordx4 v1, s[40:41] offset:-3072
global_load_lds_dwordx4 v2, s[40:41] offset:-2048
global_load_lds_dwordx4 v3, s[40:41] offset:-1024
global_load_lds_dwordx4 v4, s[40:41] offset:0
global_load_lds_dwordx4 v5, s[40:41] offset:1024
global_load_lds_dwordx4 v6, s[40:41] offset:2048
global_load_lds_dwordx4 v7, s[40:41] offset:3072
s_add_u32 m0, s48, 12288
s_nop 0
global_load_lds_dwordx4 v8, s[40:41] offset:-4096
s_add_u32 s40, s40, 73728
s_addc_u32 s41, s41, 0
s_waitcnt vmcnt(27)
s_barrier
ds_read_b64_tr_b16 a[32:33], v9 offset:36864
ds_read_b64_tr_b16 a[34:35], v9 offset:39168
ds_read_b64_tr_b16 a[36:37], v9 offset:36872
ds_read_b64_tr_b16 a[38:39], v9 offset:39176
ds_read_b64_tr_b16 a[40:41], v9 offset:36928
ds_read_b64_tr_b16 a[42:43], v9 offset:39232
ds_read_b64_tr_b16 a[44:45], v9 offset:36936
ds_read_b64_tr_b16 a[46:47], v9 offset:39240
ds_read_b64_tr_b16 a[48:49], v9 offset:55296
ds_read_b64_tr_b16 a[50:51], v9 offset:57600
ds_read_b64_tr_b16 a[52:53], v9 offset:55304
ds_read_b64_tr_b16 a[54:55], v9 offset:57608
ds_read_b64_tr_b16 a[56:57], v9 offset:55360
ds_read_b64_tr_b16 a[58:59], v9 offset:57664
ds_read_b64_tr_b16 a[60:61], v9 offset:55368
ds_read_b64_tr_b16 a[62:63], v9 offset:57672
s_waitcnt lgkmcnt(0)
s_barrier
s_add_u32 m0, s48, 40960
s_nop 0
global_load_lds_dwordx4 v0, s[40:41] offset:-4096
global_load_lds_dwordx4 v1, s[40:41] offset:-3072
global_load_lds_dwordx4 v2, s[40:41] offset:-2048
global_load_lds_dwordx4 v3, s[40:41] offset:-1024
global_load_lds_dwordx4 v4, s[40:41] offset:0
global_load_lds_dwordx4 v5, s[40:41] offset:1024
global_load_lds_dwordx4 v6, s[40:41] offset:2048
global_load_lds_dwordx4 v7, s[40:41] offset:3072
s_add_u32 m0, s48, 49152
s_nop 0
global_load_lds_dwordx4 v8, s[40:41] offset:-4096
s_add_u32 s40, s40, 73728
s_addc_u32 s41, s41, 0
s_waitcnt vmcnt(27)
s_barrier
ds_read_b64_tr_b16 a[64:65], v204 offset:8192
ds_read_b64_tr_b16 a[66:67], v204 offset:10496
ds_read_b64_tr_b16 a[68:69], v204 offset:8200
ds_read_b64_tr_b16 a[70:71], v204 offset:10504
ds_read_b64_tr_b16 a[72:73], v204 offset:8256
ds_read_b64_tr_b16 a[74:75], v204 offset:10560
ds_read_b64_tr_b16 a[76:77], v204 offset:8264
ds_read_b64_tr_b16 a[78:79], v204 offset:10568
ds_read_b64_tr_b16 a[80:81], v204 offset:26624
ds_read_b64_tr_b16 a[82:83], v204 offset:28928
ds_read_b64_tr_b16 a[84:85], v204 offset:26632
ds_read_b64_tr_b16 a[86:87], v204 offset:28936
ds_read_b64_tr_b16 a[88:89], v204 offset:26688
)"
R"(ds_read_b64_tr_b16 a[90:91], v204 offset:28992
ds_read_b64_tr_b16 a[92:93], v204 offset:26696
ds_read_b64_tr_b16 a[94:95], v204 offset:29000
s_waitcnt lgkmcnt(0)
s_barrier
s_add_u32 m0, s48, 77824
s_nop 0
global_load_lds_dwordx4 v0, s[40:41] offset:-4096
global_load_lds_dwordx4 v1, s[40:41] offset:-3072
global_load_lds_dwordx4 v2, s[40:41] offset:-2048
global_load_lds_dwordx4 v3, s[40:41] offset:-1024
global_load_lds_dwordx4 v4, s[40:41] offset:0
global_load_lds_dwordx4 v5, s[40:41] offset:1024
global_load_lds_dwordx4 v6, s[40:41] offset:2048
global_load_lds_dwordx4 v7, s[40:41] offset:3072
s_add_u32 m0, s48, 86016
s_nop 0
global_load_lds_dwordx4 v8, s[40:41] offset:-4096
s_add_u32 s40, s40, 73728
s_addc_u32 s41, s41, 0
s_waitcnt vmcnt(27)
s_barrier
ds_read_b64_tr_b16 a[96:97], v204 offset:45056
ds_read_b64_tr_b16 a[98:99], v204 offset:47360
ds_read_b64_tr_b16 a[100:101], v204 offset:45064
ds_read_b64_tr_b16 a[102:103], v204 offset:47368
ds_read_b64_tr_b16 a[104:105], v204 offset:45120
ds_read_b64_tr_b16 a[106:107], v204 offset:47424
ds_read_b64_tr_b16 a[108:109], v204 offset:45128
ds_read_b64_tr_b16 a[110:111], v204 offset:47432
ds_read_b64_tr_b16 a[112:113], v204 offset:63488
ds_read_b64_tr_b16 a[114:115], v205 offset:256
ds_read_b64_tr_b16 a[116:117], v204 offset:63496
ds_read_b64_tr_b16 a[118:119], v205 offset:264
ds_read_b64_tr_b16 a[120:121], v204 offset:63552
ds_read_b64_tr_b16 a[122:123], v205 offset:320
ds_read_b64_tr_b16 a[124:125], v204 offset:63560
ds_read_b64_tr_b16 a[126:127], v205 offset:328
s_waitcnt lgkmcnt(0)
s_barrier
s_add_u32 m0, s48, 114688
s_nop 0
global_load_lds_dwordx4 v0, s[40:41] offset:-4096
global_load_lds_dwordx4 v1, s[40:41] offset:-3072
global_load_lds_dwordx4 v2, s[40:41] offset:-2048
global_load_lds_dwordx4 v3, s[40:41] offset:-1024
global_load_lds_dwordx4 v4, s[40:41] offset:0
global_load_lds_dwordx4 v5, s[40:41] offset:1024
global_load_lds_dwordx4 v6, s[40:41] offset:2048
global_load_lds_dwordx4 v7, s[40:41] offset:3072
s_add_u32 m0, s48, 122880
s_nop 0
global_load_lds_dwordx4 v8, s[40:41] offset:-4096
s_add_u32 s40, s40, 73728
s_addc_u32 s41, s41, 0
s_waitcnt vmcnt(27)
s_barrier
ds_read_b64_tr_b16 a[128:129], v9 offset:0
ds_read_b64_tr_b16 a[130:131], v9 offset:2304
ds_read_b64_tr_b16 a[132:133], v9 offset:8
ds_read_b64_tr_b16 a[134:135], v9 offset:2312
ds_read_b64_tr_b16 a[136:137], v9 offset:64
ds_read_b64_tr_b16 a[138:139], v9 offset:2368
ds_read_b64_tr_b16 a[140:141], v9 offset:72
ds_read_b64_tr_b16 a[142:143], v9 offset:2376
ds_read_b64_tr_b16 a[144:145], v9 offset:18432
ds_read_b64_tr_b16 a[146:147], v9 offset:20736
ds_read_b64_tr_b16 a[148:149], v9 offset:18440
ds_read_b64_tr_b16 a[150:151], v9 offset:20744
ds_read_b64_tr_b16 a[152:153], v9 offset:18496
ds_read_b64_tr_b16 a[154:155], v9 offset:20800
ds_read_b64_tr_b16 a[156:157], v9 offset:18504
ds_read_b64_tr_b16 a[158:159], v9 offset:20808
ds_read_b64_tr_b16 v[60:61], v10 offset:18432
ds_read_b64_tr_b16 v[62:63], v10 offset:20736
s_waitcnt lgkmcnt(0)
s_barrier
s_add_u32 m0, s48, 4096
s_nop 0
global_load_lds_dwordx4 v0, s[40:41] offset:-4096
global_load_lds_dwordx4 v1, s[40:41] offset:-3072
global_load_lds_dwordx4 v2, s[40:41] offset:-2048
global_load_lds_dwordx4 v3, s[40:41] offset:-1024
global_load_lds_dwordx4 v4, s[40:41] offset:0
global_load_lds_dwordx4 v5, s[40:41] offset:1024
global_load_lds_dwordx4 v6, s[40:41] offset:2048
global_load_lds_dwordx4 v7, s[40:41] offset:3072
s_add_u32 m0, s48, 12288
s_nop 0
global_load_lds_dwordx4 v8, s[40:41] offset:-4096
s_add_u32 s40, s40, 73728
s_addc_u32 s41, s41, 0
s_waitcnt vmcnt(27)
s_barrier
ds_read_b64_tr_b16 a[160:161], v9 offset:36864
ds_read_b64_tr_b16 a[162:163], v9 offset:39168
ds_read_b64_tr_b16 a[164:165], v9 offset:36872
ds_read_b64_tr_b16 a[166:167], v9 offset:39176
ds_read_b64_tr_b16 a[168:169], v9 offset:36928
ds_read_b64_tr_b16 a[170:171], v9 offset:39232
ds_read_b64_tr_b16 a[172:173], v9 offset:36936
ds_read_b64_tr_b16 a[174:175], v9 offset:39240
ds_read_b64_tr_b16 v[64:65], v10 offset:36864
ds_read_b64_tr_b16 v[66:67], v10 offset:39168
ds_read_b64_tr_b16 a[176:177], v9 offset:55296
ds_read_b64_tr_b16 a[178:179], v9 offset:57600
ds_read_b64_tr_b16 a[180:181], v9 offset:55304
ds_read_b64_tr_b16 a[182:183], v9 offset:57608
ds_read_b64_tr_b16 a[184:185], v9 offset:55360
ds_read_b64_tr_b16 a[186:187], v9 offset:57664
ds_read_b64_tr_b16 a[188:189], v9 offset:55368
ds_read_b64_tr_b16 a[190:191], v9 offset:57672
ds_read_b64_tr_b16 v[68:69], v10 offset:55296
ds_read_b64_tr_b16 v[70:71], v10 offset:57600
s_waitcnt vmcnt(18)
s_barrier
ds_read_b64_tr_b16 a[192:193], v204 offset:8192
ds_read_b64_tr_b16 a[194:195], v204 offset:10496
ds_read_b64_tr_b16 a[196:197], v204 offset:8200
ds_read_b64_tr_b16 a[198:199], v204 offset:10504
ds_read_b64_tr_b16 a[200:201], v204 offset:8256
ds_read_b64_tr_b16 a[202:203], v204 offset:10560
ds_read_b64_tr_b16 a[204:205], v204 offset:8264
ds_read_b64_tr_b16 a[206:207], v204 offset:10568
ds_read_b64_tr_b16 v[72:73], v206 offset:8192
ds_read_b64_tr_b16 v[74:75], v206 offset:10496
ds_read_b64_tr_b16 a[208:209], v204 offset:26624
ds_read_b64_tr_b16 a[210:211], v204 offset:28928
ds_read_b64_tr_b16 a[212:213], v204 offset:26632
ds_read_b64_tr_b16 a[214:215], v204 offset:28936
ds_read_b64_tr_b16 a[216:217], v204 offset:26688
ds_read_b64_tr_b16 a[218:219], v204 offset:28992
ds_read_b64_tr_b16 a[220:221], v204 offset:26696
ds_read_b64_tr_b16 a[222:223], v204 offset:29000
ds_read_b64_tr_b16 v[76:77], v206 offset:26624
ds_read_b64_tr_b16 v[78:79], v206 offset:28928
s_waitcnt vmcnt(9)
s_barrier
ds_read_b64_tr_b16 a[224:225], v204 offset:45056
ds_read_b64_tr_b16 a[226:227], v204 offset:47360
ds_read_b64_tr_b16 a[228:229], v204 offset:45064
ds_read_b64_tr_b16 a[230:231], v204 offset:47368
ds_read_b64_tr_b16 a[232:233], v204 offset:45120
ds_read_b64_tr_b16 a[234:235], v204 offset:47424
ds_read_b64_tr_b16 a[236:237], v204 offset:45128
ds_read_b64_tr_b16 a[238:239], v204 offset:47432
ds_read_b64_tr_b16 v[80:81], v206 offset:45056
ds_read_b64_tr_b16 v[82:83], v206 offset:47360
ds_read_b64_tr_b16 a[240:241], v204 offset:63488
ds_read_b64_tr_b16 a[242:243], v205 offset:256
ds_read_b64_tr_b16 a[244:245], v204 offset:63496
ds_read_b64_tr_b16 a[246:247], v205 offset:264
ds_read_b64_tr_b16 a[248:249], v204 offset:63552
ds_read_b64_tr_b16 a[250:251], v205 offset:320
ds_read_b64_tr_b16 a[252:253], v204 offset:63560
ds_read_b64_tr_b16 a[254:255], v205 offset:328
ds_read_b64_tr_b16 v[84:85], v206 offset:63488
ds_read_b64_tr_b16 v[86:87], v207 offset:256
s_waitcnt vmcnt(0)
s_barrier
ds_read_b64_tr_b16 v[28:29], v9 offset:0
ds_read_b64_tr_b16 v[30:31], v9 offset:2304
ds_read_b64_tr_b16 v[32:33], v9 offset:8
ds_read_b64_tr_b16 v[34:35], v9 offset:2312
ds_read_b64_tr_b16 v[36:37], v9 offset:64
ds_read_b64_tr_b16 v[38:39], v9 offset:2368
ds_read_b64_tr_b16 v[40:41], v9 offset:72
ds_read_b64_tr_b16 v[42:43], v9 offset:2376
ds_read_b64_tr_b16 v[88:89], v10 offset:0
ds_read_b64_tr_b16 v[90:91], v10 offset:2304
ds_read_b64_tr_b16 v[44:45], v9 offset:18432
ds_read_b64_tr_b16 v[46:47], v9 offset:20736
ds_read_b64_tr_b16 v[48:49], v9 offset:18440
ds_read_b64_tr_b16 v[50:51], v9 offset:20744
ds_read_b64_tr_b16 v[52:53], v9 offset:18496
ds_read_b64_tr_b16 v[54:55], v9 offset:20800
ds_read_b64_tr_b16 v[56:57], v9 offset:18504
ds_read_b64_tr_b16 v[58:59], v9 offset:20808
ds_read_b64_tr_b16 v[92:93], v10 offset:18432
ds_read_b64_tr_b16 v[94:95], v10 offset:20736
s_waitcnt lgkmcnt(0)
s_barrier
s_mov_b64 s[76:77], 0
s_mov_b32 s70, 33
s_waitcnt vmcnt(0) lgkmcnt(0)
s_nop 7
.p2align 6
.Lwo_top14:
s_waitcnt lgkmcnt(2)
v_mfma_f32_16x16x32_bf16 v[96:99], a[144:147], v[132:135], v[96:99]
ds_read_b128 v[144:147], v25 offset:12288
v_add_f32 v254, v176, v177
v_mfma_f32_16x16x32_bf16 v[100:103], a[148:151], v[132:135], v[100:103]
v_mul_f32 v255, v176, v176
v_fmac_f32 v255, v177, v177
v_mfma_f32_16x16x32_bf16 v[104:107], a[152:155], v[132:135], v[104:107]
v_add_f32 v254, v254, v178
v_fmac_f32 v255, v178, v178
v_mfma_f32_16x16x32_bf16 v[108:111], a[156:159], v[132:135], v[108:111]
v_cvt_pk_bf16_f32 v164, v112, v113
v_cvt_pk_bf16_f32 v165, v114, v115
v_mfma_f32_16x16x32_bf16 v[128:131], v[60:63], v[132:135], 0
s_waitcnt lgkmcnt(2)
v_cvt_pk_bf16_f32 v166, v116, v117
v_cvt_pk_bf16_f32 v167, v118, v119
v_mfma_f32_16x16x32_bf16 v[96:99], a[160:163], v[136:139], v[96:99]
ds_read_b128 v[132:135], v25 offset:13312
v_add_f32 v254, v254, v179
v_mfma_f32_16x16x32_bf16 v[100:103], a[164:167], v[136:139], v[100:103]
v_fmac_f32 v255, v179, v179
v_add_f32 v254, v254, v180
v_mfma_f32_16x16x32_bf16 v[104:107], a[168:171], v[136:139], v[104:107]
v_fmac_f32 v255, v180, v180
v_add_f32 v254, v254, v181
v_mfma_f32_16x16x32_bf16 v[108:111], a[172:175], v[136:139], v[108:111]
global_store_dwordx4 v13, v[164:167], s[38:39] offset:0 sc1
v_mfma_f32_16x16x32_bf16 v[128:131], v[64:67], v[136:139], v[128:131]
s_waitcnt lgkmcnt(2)
v_cvt_pk_bf16_f32 v168, v120, v121
v_cvt_pk_bf16_f32 v169, v122, v123
v_mfma_f32_16x16x32_bf16 v[96:99], a[176:179], v[140:143], v[96:99]
s_cmp_eq_u32 s70, 32
ds_read_b128 v[136:139], v25 offset:14336
v_cvt_pk_bf16_f32 v170, v124, v125
v_mfma_f32_16x16x32_bf16 v[100:103], a[180:183], v[140:143], v[100:103]
s_cselect_b64 s[42:43], s[44:45], s[42:43]
v_cvt_pk_bf16_f32 v171, v126, v127
v_fmac_f32 v255, v181, v181
v_mfma_f32_16x16x32_bf16 v[104:107], a[184:187], v[140:143], v[104:107]
s_cmp_eq_u32 s70, 16
global_store_dwordx4 v13, v[168:171], s[38:39] offset:1024 sc1
v_mfma_f32_16x16x32_bf16 v[108:111], a[188:191], v[140:143], v[108:111]
s_cselect_b64 s[42:43], s[46:47], s[42:43]
v_add_u32 v13, 18688, v13
v_add_f32 v254, v254, v182
v_mfma_f32_16x16x32_bf16 v[128:131], v[68:71], v[140:143], v[128:131]
s_waitcnt lgkmcnt(2)
v_fmac_f32 v255, v182, v182
v_add_f32 v254, v254, v183
v_mfma_f32_16x16x32_bf16 v[96:99], a[192:195], v[144:147], v[96:99]
s_xor_b32 s49, s49, 0x10000
ds_read_b128 v[140:143], v25 offset:15360
v_fmac_f32 v255, v183, v183
v_mfma_f32_16x16x32_bf16 v[100:103], a[196:199], v[144:147], v[100:103]
s_mov_b32 m0, 53248
v_add_f32 v254, v254, v112
v_fmac_f32 v255, v112, v112
v_mfma_f32_16x16x32_bf16 v[104:107], a[200:203], v[144:147], v[104:107]
v_add_f32 v254, v254, v113
v_fmac_f32 v255, v113, v113
v_mfma_f32_16x16x32_bf16 v[108:111], a[204:207], v[144:147], v[108:111]
v_add_f32 v254, v254, v114
v_fmac_f32 v255, v114, v114
v_mfma_f32_16x16x32_bf16 v[128:131], v[72:75], v[144:147], v[128:131]
s_waitcnt lgkmcnt(2)
v_add_f32 v254, v254, v115
v_fmac_f32 v255, v115, v115
v_mfma_f32_16x16x32_bf16 v[96:99], a[208:211], v[132:135], v[96:99]
ds_read_b128 v[144:147], v25 offset:16384
v_add_f32 v254, v254, v116
v_mfma_f32_16x16x32_bf16 v[100:103], a[212:215], v[132:135], v[100:103]
v_fmac_f32 v255, v116, v116
v_add_f32 v254, v254, v117
v_mfma_f32_16x16x32_bf16 v[104:107], a[216:219], v[132:135], v[104:107]
s_waitcnt vmcnt(7)
ds_read_b128 v[148:151], v19 offset:8192
ds_read_b128 v[152:155], v20 offset:8192
v_mfma_f32_16x16x32_bf16 v[108:111], a[220:223], v[132:135], v[108:111]
v_fmac_f32 v255, v117, v117
v_add_f32 v254, v254, v118
v_mfma_f32_16x16x32_bf16 v[128:131], v[76:79], v[132:135], v[128:131]
s_waitcnt lgkmcnt(4)
global_load_lds_dwordx4 v16, s[42:43] offset:0 sc1
v_fmac_f32 v255, v118, v118
v_mfma_f32_16x16x32_bf16 v[96:99], a[224:227], v[136:139], v[96:99]
ds_read_b128 v[132:135], v25 offset:17408
v_add_f32 v254, v254, v119
v_mfma_f32_16x16x32_bf16 v[100:103], a[228:231], v[136:139], v[100:103]
v_fmac_f32 v255, v119, v119
v_add_f32 v254, v254, v120
v_mfma_f32_16x16x32_bf16 v[104:107], a[232:235], v[136:139], v[104:107]
v_fmac_f32 v255, v120, v120
v_add_f32 v254, v254, v121
v_mfma_f32_16x16x32_bf16 v[108:111], a[236:239], v[136:139], v[108:111]
v_fmac_f32 v255, v121, v121
v_add_f32 v254, v254, v122
v_mfma_f32_16x16x32_bf16 v[128:131], v[80:83], v[136:139], v[128:131]
s_waitcnt lgkmcnt(4)
v_fmac_f32 v255, v122, v122
v_add_f32 v254, v254, v123
v_mfma_f32_16x16x32_bf16 v[96:99], a[240:243], v[140:143], v[96:99]
ds_read_b128 v[136:139], v25 offset:0
v_fmac_f32 v255, v123, v123
v_mfma_f32_16x16x32_bf16 v[100:103], a[244:247], v[140:143], v[100:103]
global_load_lds_dwordx4 v17, s[42:43] offset:1024 sc1
v_add_f32 v254, v254, v124
v_mfma_f32_16x16x32_bf16 v[104:107], a[248:251], v[140:143], v[104:107]
v_fmac_f32 v255, v124, v124
v_add_f32 v254, v254, v125
v_mfma_f32_16x16x32_bf16 v[108:111], a[252:255], v[140:143], v[108:111]
v_fmac_f32 v255, v125, v125
v_add_f32 v254, v254, v126
v_mfma_f32_16x16x32_bf16 v[128:131], v[84:87], v[140:143], v[128:131]
s_waitcnt lgkmcnt(4)
v_fmac_f32 v255, v126, v126
v_add_f32 v254, v254, v127
v_mfma_f32_16x16x32_bf16 v[96:99], v[28:31], v[144:147], v[96:99]
ds_read_b128 v[140:143], v25 offset:1024
v_fmac_f32 v255, v127, v127
v_mfma_f32_16x16x32_bf16 v[100:103], v[32:35], v[144:147], v[100:103]
s_add_u32 m0, s49, 0
ds_write_b64 v27, v[254:255] offset:46592
v_mfma_f32_16x16x32_bf16 v[104:107], v[36:39], v[144:147], v[104:107]
s_waitcnt lgkmcnt(5)
v_lshlrev_b32 v112, 16, v148
v_and_b32 v113, 0xffff0000, v148
v_mfma_f32_16x16x32_bf16 v[108:111], v[40:43], v[144:147], v[108:111]
v_lshlrev_b32 v114, 16, v149
v_and_b32 v115, 0xffff0000, v149
v_mfma_f32_16x16x32_bf16 v[128:131], v[88:91], v[144:147], v[128:131]
s_waitcnt lgkmcnt(3)
v_lshlrev_b32 v116, 16, v150
v_and_b32 v117, 0xffff0000, v150
v_mfma_f32_16x16x32_bf16 v[96:99], v[44:47], v[132:135], v[96:99]
ds_read_b128 v[144:147], v25 offset:2048
v_lshlrev_b32 v118, 16, v151
v_mfma_f32_16x16x32_bf16 v[100:103], v[48:51], v[132:135], v[100:103]
v_and_b32 v119, 0xffff0000, v151
v_lshlrev_b32 v120, 16, v152
v_mfma_f32_16x16x32_bf16 v[104:107], v[52:55], v[132:135], v[104:107]
v_and_b32 v121, 0xffff0000, v152
v_lshlrev_b32 v122, 16, v153
v_mfma_f32_16x16x32_bf16 v[108:111], v[56:59], v[132:135], v[108:111]
v_and_b32 v123, 0xffff0000, v153
v_lshlrev_b32 v124, 16, v154
v_mfma_f32_16x16x32_bf16 v[128:131], v[92:95], v[132:135], v[128:131]
s_waitcnt lgkmcnt(3)
v_lshlrev_b32 v176, 16, v156
v_and_b32 v177, 0xffff0000, v156
v_mfma_f32_16x16x32_bf16 v[96:99], a[0:3], v[136:139], v[96:99]
ds_read_b128 v[132:135], v25 offset:3072
v_lshlrev_b32 v178, 16, v157
v_mfma_f32_16x16x32_bf16 v[100:103], a[4:7], v[136:139], v[100:103]
v_and_b32 v179, 0xffff0000, v157
v_lshlrev_b32 v180, 16, v158
v_mfma_f32_16x16x32_bf16 v[104:107], a[8:11], v[136:139], v[104:107]
v_and_b32 v181, 0xffff0000, v158
v_lshlrev_b32 v182, 16, v159
v_mfma_f32_16x16x32_bf16 v[108:111], a[12:15], v[136:139], v[108:111]
s_waitcnt lgkmcnt(3)
v_and_b32 v183, 0xffff0000, v159
v_add_f32 v180, v180, v128
v_mfma_f32_16x16x32_bf16 v[96:99], a[16:19], v[140:143], v[96:99]
ds_read_b128 v[136:139], v25 offset:4096
v_add_f32 v181, v181, v129
v_mfma_f32_16x16x32_bf16 v[100:103], a[20:23], v[140:143], v[100:103]
v_add_f32 v182, v182, v130
v_add_f32 v183, v183, v131
v_mfma_f32_16x16x32_bf16 v[104:107], a[24:27], v[140:143], v[104:107]
global_load_dwordx4 v[156:159], v18, s[42:43] sc1
v_and_b32 v125, 0xffff0000, v154
v_mfma_f32_16x16x32_bf16 v[108:111], a[28:31], v[140:143], v[108:111]
s_waitcnt lgkmcnt(2)
v_lshlrev_b32 v126, 16, v155
v_and_b32 v127, 0xffff0000, v155
v_mfma_f32_16x16x32_bf16 v[96:99], a[32:35], v[144:147], v[96:99]
s_sub_u32 s42, s42, 18432
ds_read_b128 v[140:143], v25 offset:5120
v_mfma_f32_16x16x32_bf16 v[100:103], a[36:39], v[144:147], v[100:103]
v_mfma_f32_16x16x32_bf16 v[104:107], a[40:43], v[144:147], v[104:107]
s_barrier
v_mfma_f32_16x16x32_bf16 v[108:111], a[44:47], v[144:147], v[108:111]
s_waitcnt lgkmcnt(2)
ds_read_b128 v[184:187], v26 offset:36864
ds_read_b128 v[188:191], v26 offset:37888
v_mfma_f32_16x16x32_bf16 v[96:99], a[48:51], v[132:135], v[96:99]
s_subb_u32 s43, s43, 0
ds_read_b128 v[144:147], v25 offset:6144
ds_read_b128 v[192:195], v26 offset:38912
v_mfma_f32_16x16x32_bf16 v[100:103], a[52:55], v[132:135], v[100:103]
v_mfma_f32_16x16x32_bf16 v[104:107], a[56:59], v[132:135], v[104:107]
global_load_lds_dwordx4 v11, s[38:39] offset:-2048 sc1
v_mfma_f32_16x16x32_bf16 v[108:111], a[60:63], v[132:135], v[108:111]
s_waitcnt lgkmcnt(5)
v_mfma_f32_16x16x32_bf16 v[96:99], a[64:67], v[136:139], v[96:99]
ds_read_b128 v[132:135], v25 offset:7168
v_mfma_f32_16x16x32_bf16 v[100:103], a[68:71], v[136:139], v[100:103]
s_waitcnt lgkmcnt(4)
global_load_lds_dwordx4 v11, s[38:39] offset:-1024 sc1
v_add_f32 v176, v176, v184
v_mfma_f32_16x16x32_bf16 v[104:107], a[72:75], v[136:139], v[104:107]
v_add_f32 v177, v177, v185
v_add_f32 v178, v178, v186
v_mfma_f32_16x16x32_bf16 v[108:111], a[76:79], v[136:139], v[108:111]
s_waitcnt lgkmcnt(3)
v_add_f32 v179, v179, v187
v_add_f32 v176, v176, v188
v_mfma_f32_16x16x32_bf16 v[96:99], a[80:83], v[140:143], v[96:99]
ds_read_b128 v[136:139], v25 offset:8192
global_load_lds_dwordx4 v11, s[38:39] offset:0 sc1
)"
R"(v_mfma_f32_16x16x32_bf16 v[100:103], a[84:87], v[140:143], v[100:103]
v_add_u32 v11, 18432, v11
v_min_u32 v11, v11, v12
v_mfma_f32_16x16x32_bf16 v[104:107], a[88:91], v[140:143], v[104:107]
v_add_f32 v177, v177, v189
v_add_f32 v178, v178, v190
v_mfma_f32_16x16x32_bf16 v[108:111], a[92:95], v[140:143], v[108:111]
s_waitcnt lgkmcnt(2)
v_add_f32 v179, v179, v191
v_add_f32 v180, v180, v192
v_mfma_f32_16x16x32_bf16 v[96:99], a[96:99], v[144:147], v[96:99]
v_add_f32 v181, v181, v193
v_add_f32 v182, v182, v194
v_mfma_f32_16x16x32_bf16 v[100:103], a[100:103], v[144:147], v[100:103]
v_add_f32 v183, v183, v195
v_cvt_pk_bf16_f32 v172, v176, v177
v_mfma_f32_16x16x32_bf16 v[104:107], a[104:107], v[144:147], v[104:107]
v_mfma_f32_16x16x32_bf16 v[108:111], a[108:111], v[144:147], v[108:111]
s_waitcnt vmcnt(6)
s_barrier
s_waitcnt lgkmcnt(1)
v_mfma_f32_16x16x32_bf16 v[96:99], a[112:115], v[132:135], v[96:99]
ds_read_b128 v[140:143], v25 offset:27648
ds_read_b128 v[144:147], v25 offset:28672
v_mfma_f32_16x16x32_bf16 v[100:103], a[116:119], v[132:135], v[100:103]
v_cvt_pk_bf16_f32 v173, v178, v179
v_cvt_pk_bf16_f32 v174, v180, v181
v_mfma_f32_16x16x32_bf16 v[104:107], a[120:123], v[132:135], v[104:107]
v_cvt_pk_bf16_f32 v175, v182, v183
v_mfma_f32_16x16x32_bf16 v[108:111], a[124:127], v[132:135], v[108:111]
s_waitcnt lgkmcnt(2)
global_store_dwordx4 v14, v[172:175], s[38:39] sc1
v_add_u32 v14, 18688, v14
v_mfma_f32_16x16x32_bf16 v[96:99], a[128:131], v[136:139], v[96:99]
ds_read_b128 v[132:135], v25 offset:29696
v_mfma_f32_16x16x32_bf16 v[100:103], a[132:135], v[136:139], v[100:103]
v_mfma_f32_16x16x32_bf16 v[104:107], a[136:139], v[136:139], v[104:107]
v_mfma_f32_16x16x32_bf16 v[108:111], a[140:143], v[136:139], v[108:111]
s_waitcnt lgkmcnt(2)
v_mfma_f32_16x16x32_bf16 v[112:115], a[144:147], v[140:143], v[112:115]
ds_read_b128 v[136:139], v25 offset:30720
v_add_f32 v254, v176, v177
v_mfma_f32_16x16x32_bf16 v[116:119], a[148:151], v[140:143], v[116:119]
v_mul_f32 v255, v176, v176
v_fmac_f32 v255, v177, v177
v_mfma_f32_16x16x32_bf16 v[120:123], a[152:155], v[140:143], v[120:123]
v_add_f32 v254, v254, v178
v_fmac_f32 v255, v178, v178
v_mfma_f32_16x16x32_bf16 v[124:127], a[156:159], v[140:143], v[124:127]
v_cvt_pk_bf16_f32 v164, v96, v97
v_cvt_pk_bf16_f32 v165, v98, v99
v_mfma_f32_16x16x32_bf16 v[128:131], v[60:63], v[140:143], 0
s_waitcnt lgkmcnt(2)
v_cvt_pk_bf16_f32 v166, v100, v101
v_cvt_pk_bf16_f32 v167, v102, v103
v_mfma_f32_16x16x32_bf16 v[112:115], a[160:163], v[144:147], v[112:115]
ds_read_b128 v[140:143], v25 offset:31744
v_add_f32 v254, v254, v179
v_mfma_f32_16x16x32_bf16 v[116:119], a[164:167], v[144:147], v[116:119]
v_fmac_f32 v255, v179, v179
v_add_f32 v254, v254, v180
v_mfma_f32_16x16x32_bf16 v[120:123], a[168:171], v[144:147], v[120:123]
v_fmac_f32 v255, v180, v180
v_add_f32 v254, v254, v181
v_mfma_f32_16x16x32_bf16 v[124:127], a[172:175], v[144:147], v[124:127]
global_store_dwordx4 v13, v[164:167], s[38:39] offset:0 sc1
v_mfma_f32_16x16x32_bf16 v[128:131], v[64:67], v[144:147], v[128:131]
s_waitcnt lgkmcnt(2)
v_cvt_pk_bf16_f32 v168, v104, v105
v_cvt_pk_bf16_f32 v169, v106, v107
v_mfma_f32_16x16x32_bf16 v[112:115], a[176:179], v[132:135], v[112:115]
s_mov_b32 m0, 61440
ds_read_b128 v[144:147], v25 offset:32768
v_cvt_pk_bf16_f32 v170, v108, v109
v_mfma_f32_16x16x32_bf16 v[116:119], a[180:183], v[132:135], v[116:119]
v_cvt_pk_bf16_f32 v171, v110, v111
v_fmac_f32 v255, v181, v181
v_mfma_f32_16x16x32_bf16 v[120:123], a[184:187], v[132:135], v[120:123]
global_store_dwordx4 v13, v[168:171], s[38:39] offset:1024 sc1
v_mfma_f32_16x16x32_bf16 v[124:127], a[188:191], v[132:135], v[124:127]
v_add_u32 v13, 18688, v13
v_add_f32 v254, v254, v182
v_mfma_f32_16x16x32_bf16 v[128:131], v[68:71], v[132:135], v[128:131]
s_waitcnt lgkmcnt(2)
v_fmac_f32 v255, v182, v182
v_add_f32 v254, v254, v183
v_mfma_f32_16x16x32_bf16 v[112:115], a[192:195], v[136:139], v[112:115]
ds_read_b128 v[132:135], v25 offset:33792
v_fmac_f32 v255, v183, v183
v_mfma_f32_16x16x32_bf16 v[116:119], a[196:199], v[136:139], v[116:119]
v_add_f32 v254, v254, v96
v_fmac_f32 v255, v96, v96
v_mfma_f32_16x16x32_bf16 v[120:123], a[200:203], v[136:139], v[120:123]
v_add_f32 v254, v254, v97
v_fmac_f32 v255, v97, v97
v_mfma_f32_16x16x32_bf16 v[124:127], a[204:207], v[136:139], v[124:127]
v_add_f32 v254, v254, v98
v_fmac_f32 v255, v98, v98
v_mfma_f32_16x16x32_bf16 v[128:131], v[72:75], v[136:139], v[128:131]
s_waitcnt lgkmcnt(2)
v_add_f32 v254, v254, v99
v_fmac_f32 v255, v99, v99
v_mfma_f32_16x16x32_bf16 v[112:115], a[208:211], v[140:143], v[112:115]
ds_read_b128 v[136:139], v25 offset:34816
v_add_f32 v254, v254, v100
v_mfma_f32_16x16x32_bf16 v[116:119], a[212:215], v[140:143], v[116:119]
v_fmac_f32 v255, v100, v100
v_add_f32 v254, v254, v101
v_mfma_f32_16x16x32_bf16 v[120:123], a[216:219], v[140:143], v[120:123]
s_waitcnt vmcnt(7)
ds_read_b128 v[148:151], v19 offset:0
ds_read_b128 v[152:155], v20 offset:0
v_mfma_f32_16x16x32_bf16 v[124:127], a[220:223], v[140:143], v[124:127]
v_fmac_f32 v255, v101, v101
v_add_f32 v254, v254, v102
v_mfma_f32_16x16x32_bf16 v[128:131], v[76:79], v[140:143], v[128:131]
s_waitcnt lgkmcnt(4)
global_load_lds_dwordx4 v16, s[42:43] offset:0 sc1
v_fmac_f32 v255, v102, v102
v_mfma_f32_16x16x32_bf16 v[112:115], a[224:227], v[144:147], v[112:115]
ds_read_b128 v[140:143], v25 offset:35840
v_add_f32 v254, v254, v103
v_mfma_f32_16x16x32_bf16 v[116:119], a[228:231], v[144:147], v[116:119]
v_fmac_f32 v255, v103, v103
v_add_f32 v254, v254, v104
v_mfma_f32_16x16x32_bf16 v[120:123], a[232:235], v[144:147], v[120:123]
v_fmac_f32 v255, v104, v104
v_add_f32 v254, v254, v105
v_mfma_f32_16x16x32_bf16 v[124:127], a[236:239], v[144:147], v[124:127]
v_fmac_f32 v255, v105, v105
v_add_f32 v254, v254, v106
v_mfma_f32_16x16x32_bf16 v[128:131], v[80:83], v[144:147], v[128:131]
s_waitcnt lgkmcnt(4)
v_fmac_f32 v255, v106, v106
v_add_f32 v254, v254, v107
v_mfma_f32_16x16x32_bf16 v[112:115], a[240:243], v[132:135], v[112:115]
ds_read_b128 v[144:147], v25 offset:18432
v_fmac_f32 v255, v107, v107
v_mfma_f32_16x16x32_bf16 v[116:119], a[244:247], v[132:135], v[116:119]
global_load_lds_dwordx4 v17, s[42:43] offset:1024 sc1
v_add_f32 v254, v254, v108
v_mfma_f32_16x16x32_bf16 v[120:123], a[248:251], v[132:135], v[120:123]
v_fmac_f32 v255, v108, v108
v_add_f32 v254, v254, v109
v_mfma_f32_16x16x32_bf16 v[124:127], a[252:255], v[132:135], v[124:127]
v_fmac_f32 v255, v109, v109
v_add_f32 v254, v254, v110
v_mfma_f32_16x16x32_bf16 v[128:131], v[84:87], v[132:135], v[128:131]
s_waitcnt lgkmcnt(4)
v_fmac_f32 v255, v110, v110
v_add_f32 v254, v254, v111
v_mfma_f32_16x16x32_bf16 v[112:115], v[28:31], v[136:139], v[112:115]
ds_read_b128 v[132:135], v25 offset:19456
v_fmac_f32 v255, v111, v111
v_mfma_f32_16x16x32_bf16 v[116:119], v[32:35], v[136:139], v[116:119]
s_add_u32 m0, s49, 18432
ds_write_b64 v27, v[254:255] offset:44544
v_mfma_f32_16x16x32_bf16 v[120:123], v[36:39], v[136:139], v[120:123]
s_waitcnt lgkmcnt(5)
v_lshlrev_b32 v96, 16, v148
v_and_b32 v97, 0xffff0000, v148
v_mfma_f32_16x16x32_bf16 v[124:127], v[40:43], v[136:139], v[124:127]
v_lshlrev_b32 v98, 16, v149
v_and_b32 v99, 0xffff0000, v149
v_mfma_f32_16x16x32_bf16 v[128:131], v[88:91], v[136:139], v[128:131]
s_waitcnt lgkmcnt(3)
v_lshlrev_b32 v100, 16, v150
v_and_b32 v101, 0xffff0000, v150
v_mfma_f32_16x16x32_bf16 v[112:115], v[44:47], v[140:143], v[112:115]
ds_read_b128 v[136:139], v25 offset:20480
v_lshlrev_b32 v102, 16, v151
v_mfma_f32_16x16x32_bf16 v[116:119], v[48:51], v[140:143], v[116:119]
v_and_b32 v103, 0xffff0000, v151
v_lshlrev_b32 v104, 16, v152
v_mfma_f32_16x16x32_bf16 v[120:123], v[52:55], v[140:143], v[120:123]
v_and_b32 v105, 0xffff0000, v152
v_lshlrev_b32 v106, 16, v153
v_mfma_f32_16x16x32_bf16 v[124:127], v[56:59], v[140:143], v[124:127]
v_and_b32 v107, 0xffff0000, v153
v_lshlrev_b32 v108, 16, v154
v_mfma_f32_16x16x32_bf16 v[128:131], v[92:95], v[140:143], v[128:131]
s_waitcnt lgkmcnt(3)
v_lshlrev_b32 v176, 16, v160
v_and_b32 v177, 0xffff0000, v160
v_mfma_f32_16x16x32_bf16 v[112:115], a[0:3], v[144:147], v[112:115]
ds_read_b128 v[140:143], v25 offset:21504
v_lshlrev_b32 v178, 16, v161
v_mfma_f32_16x16x32_bf16 v[116:119], a[4:7], v[144:147], v[116:119]
v_and_b32 v179, 0xffff0000, v161
v_lshlrev_b32 v180, 16, v162
v_mfma_f32_16x16x32_bf16 v[120:123], a[8:11], v[144:147], v[120:123]
v_and_b32 v181, 0xffff0000, v162
v_lshlrev_b32 v182, 16, v163
v_mfma_f32_16x16x32_bf16 v[124:127], a[12:15], v[144:147], v[124:127]
s_waitcnt lgkmcnt(3)
v_and_b32 v183, 0xffff0000, v163
v_add_f32 v180, v180, v128
v_mfma_f32_16x16x32_bf16 v[112:115], a[16:19], v[132:135], v[112:115]
ds_read_b128 v[144:147], v25 offset:22528
v_add_f32 v181, v181, v129
v_mfma_f32_16x16x32_bf16 v[116:119], a[20:23], v[132:135], v[116:119]
v_add_f32 v182, v182, v130
v_add_f32 v183, v183, v131
v_mfma_f32_16x16x32_bf16 v[120:123], a[24:27], v[132:135], v[120:123]
global_load_dwordx4 v[160:163], v18, s[42:43] sc1
v_and_b32 v109, 0xffff0000, v154
v_mfma_f32_16x16x32_bf16 v[124:127], a[28:31], v[132:135], v[124:127]
s_waitcnt lgkmcnt(2)
v_lshlrev_b32 v110, 16, v155
v_and_b32 v111, 0xffff0000, v155
v_mfma_f32_16x16x32_bf16 v[112:115], a[32:35], v[136:139], v[112:115]
s_sub_u32 s42, s42, 18432
ds_read_b128 v[132:135], v25 offset:23552
v_mfma_f32_16x16x32_bf16 v[116:119], a[36:39], v[136:139], v[116:119]
v_mfma_f32_16x16x32_bf16 v[120:123], a[40:43], v[136:139], v[120:123]
s_barrier
v_mfma_f32_16x16x32_bf16 v[124:127], a[44:47], v[136:139], v[124:127]
s_waitcnt lgkmcnt(2)
ds_read_b128 v[184:187], v26 offset:36864
ds_read_b128 v[188:191], v26 offset:37888
v_mfma_f32_16x16x32_bf16 v[112:115], a[48:51], v[140:143], v[112:115]
s_mov_b64 s[76:77], -1
ds_read_b128 v[136:139], v25 offset:24576
ds_read_b128 v[192:195], v26 offset:38912
v_mfma_f32_16x16x32_bf16 v[116:119], a[52:55], v[140:143], v[116:119]
s_subb_u32 s43, s43, 0
v_mfma_f32_16x16x32_bf16 v[120:123], a[56:59], v[140:143], v[120:123]
global_load_lds_dwordx4 v11, s[38:39] offset:-2048 sc1
v_mfma_f32_16x16x32_bf16 v[124:127], a[60:63], v[140:143], v[124:127]
s_waitcnt lgkmcnt(5)
v_mfma_f32_16x16x32_bf16 v[112:115], a[64:67], v[144:147], v[112:115]
ds_read_b128 v[140:143], v25 offset:25600
v_mfma_f32_16x16x32_bf16 v[116:119], a[68:71], v[144:147], v[116:119]
s_waitcnt lgkmcnt(4)
global_load_lds_dwordx4 v11, s[38:39] offset:-1024 sc1
v_add_f32 v176, v176, v184
v_mfma_f32_16x16x32_bf16 v[120:123], a[72:75], v[144:147], v[120:123]
v_add_f32 v177, v177, v185
v_add_f32 v178, v178, v186
v_mfma_f32_16x16x32_bf16 v[124:127], a[76:79], v[144:147], v[124:127]
s_waitcnt lgkmcnt(3)
v_add_f32 v179, v179, v187
v_add_f32 v176, v176, v188
v_mfma_f32_16x16x32_bf16 v[112:115], a[80:83], v[132:135], v[112:115]
ds_read_b128 v[144:147], v25 offset:26624
global_load_lds_dwordx4 v11, s[38:39] offset:0 sc1
v_mfma_f32_16x16x32_bf16 v[116:119], a[84:87], v[132:135], v[116:119]
v_add_u32 v11, 18432, v11
v_min_u32 v11, v11, v12
v_mfma_f32_16x16x32_bf16 v[120:123], a[88:91], v[132:135], v[120:123]
v_add_f32 v177, v177, v189
v_add_f32 v178, v178, v190
v_mfma_f32_16x16x32_bf16 v[124:127], a[92:95], v[132:135], v[124:127]
s_waitcnt lgkmcnt(2)
v_add_f32 v179, v179, v191
v_add_f32 v180, v180, v192
v_mfma_f32_16x16x32_bf16 v[112:115], a[96:99], v[136:139], v[112:115]
v_add_f32 v181, v181, v193
v_add_f32 v182, v182, v194
v_mfma_f32_16x16x32_bf16 v[116:119], a[100:103], v[136:139], v[116:119]
v_xor_b32 v25, 0x10000, v25
v_add_f32 v183, v183, v195
v_mfma_f32_16x16x32_bf16 v[120:123], a[104:107], v[136:139], v[120:123]
v_mfma_f32_16x16x32_bf16 v[124:127], a[108:111], v[136:139], v[124:127]
s_waitcnt vmcnt(6)
s_barrier
s_waitcnt lgkmcnt(1)
v_mfma_f32_16x16x32_bf16 v[112:115], a[112:115], v[140:143], v[112:115]
ds_read_b128 v[132:135], v25 offset:9216
ds_read_b128 v[136:139], v25 offset:10240
v_mfma_f32_16x16x32_bf16 v[116:119], a[116:119], v[140:143], v[116:119]
v_cvt_pk_bf16_f32 v172, v176, v177
v_cvt_pk_bf16_f32 v173, v178, v179
v_mfma_f32_16x16x32_bf16 v[120:123], a[120:123], v[140:143], v[120:123]
v_cvt_pk_bf16_f32 v174, v180, v181
v_cvt_pk_bf16_f32 v175, v182, v183
v_mfma_f32_16x16x32_bf16 v[124:127], a[124:127], v[140:143], v[124:127]
s_waitcnt lgkmcnt(2)
global_store_dwordx4 v14, v[172:175], s[38:39] sc1
v_add_u32 v14, 18688, v14
v_mfma_f32_16x16x32_bf16 v[112:115], a[128:131], v[144:147], v[112:115]
ds_read_b128 v[140:143], v25 offset:11264
v_mfma_f32_16x16x32_bf16 v[116:119], a[132:135], v[144:147], v[116:119]
v_mfma_f32_16x16x32_bf16 v[120:123], a[136:139], v[144:147], v[120:123]
v_mfma_f32_16x16x32_bf16 v[124:127], a[140:143], v[144:147], v[124:127]
s_sub_u32 s70, s70, 1
s_cbranch_scc0 .Lwo_top14
s_nop 1
v_cvt_pk_bf16_f32 v164, v112, v113
v_cvt_pk_bf16_f32 v165, v114, v115
v_cvt_pk_bf16_f32 v166, v116, v117
v_cvt_pk_bf16_f32 v167, v118, v119
global_store_dwordx4 v13, v[164:167], s[38:39] offset:0 sc1
v_cvt_pk_bf16_f32 v168, v120, v121
v_cvt_pk_bf16_f32 v169, v122, v123
v_cvt_pk_bf16_f32 v170, v124, v125
v_cvt_pk_bf16_f32 v171, v126, v127
global_store_dwordx4 v13, v[168:171], s[38:39] offset:1024 sc1
v_add_f32 v254, v176, v177
v_mul_f32 v255, v176, v176
v_fmac_f32 v255, v177, v177
v_add_f32 v254, v254, v178
v_fmac_f32 v255, v178, v178
v_add_f32 v254, v254, v179
v_fmac_f32 v255, v179, v179
v_add_f32 v254, v254, v180
v_fmac_f32 v255, v180, v180
v_add_f32 v254, v254, v181
v_fmac_f32 v255, v181, v181
v_add_f32 v254, v254, v182
v_fmac_f32 v255, v182, v182
v_add_f32 v254, v254, v183
v_fmac_f32 v255, v183, v183
v_add_f32 v254, v254, v112
v_fmac_f32 v255, v112, v112
v_add_f32 v254, v254, v113
v_fmac_f32 v255, v113, v113
v_add_f32 v254, v254, v114
v_fmac_f32 v255, v114, v114
v_add_f32 v254, v254, v115
v_fmac_f32 v255, v115, v115
v_add_f32 v254, v254, v116
v_fmac_f32 v255, v116, v116
v_add_f32 v254, v254, v117
v_fmac_f32 v255, v117, v117
v_add_f32 v254, v254, v118
v_fmac_f32 v255, v118, v118
v_add_f32 v254, v254, v119
v_fmac_f32 v255, v119, v119
v_add_f32 v254, v254, v120
v_fmac_f32 v255, v120, v120
v_add_f32 v254, v254, v121
v_fmac_f32 v255, v121, v121
v_add_f32 v254, v254, v122
v_fmac_f32 v255, v122, v122
v_add_f32 v254, v254, v123
v_fmac_f32 v255, v123, v123
v_add_f32 v254, v254, v124
v_fmac_f32 v255, v124, v124
v_add_f32 v254, v254, v125
v_fmac_f32 v255, v125, v125
v_add_f32 v254, v254, v126
v_fmac_f32 v255, v126, v126
v_add_f32 v254, v254, v127
v_fmac_f32 v255, v127, v127
ds_write_b64 v27, v[254:255] offset:46592
s_waitcnt vmcnt(0) lgkmcnt(0)
s_barrier
s_barrier
s_endpgm
.Lwo_sp6:
s_sleep 1
global_load_dword v201, v21, s[38:39] sc1
s_add_u32 s71, s71, 1
s_waitcnt vmcnt(0)
v_sub_u32 v201, v201, v22
v_cmp_lt_u32 vcc, 0x7f, v201
s_and_b64 vcc, vcc, s[76:77]
s_cbranch_vccz .Lwo_rt7
s_cmp_lt_u32 s71, 3000000
s_cbranch_scc1 .Lwo_sp6
s_branch .Lwo_rt7
.Lwo_fr8:
s_waitcnt vmcnt(5)
v_mov_b32 v201, v200
v_sub_u32 v201, v201, v22
v_cmp_lt_u32 vcc, 0x7f, v201
s_and_b64 vcc, vcc, s[76:77]
s_cbranch_vccz .Lwo_rt7
s_branch .Lwo_sp6
.Lwo_sp9:
s_sleep 1
global_load_dword v200, v21, s[38:39] sc1
s_add_u32 s71, s71, 1
s_waitcnt vmcnt(0)
v_sub_u32 v200, v200, v22
v_cmp_lt_u32 vcc, 0x7f, v200
s_and_b64 vcc, vcc, s[76:77]
s_cbranch_vccz .Lwo_rt10
s_cmp_lt_u32 s71, 3000000
s_cbranch_scc1 .Lwo_sp9
s_branch .Lwo_rt10
.Lwo_fr11:
s_waitcnt vmcnt(5)
v_mov_b32 v200, v201
v_sub_u32 v200, v200, v22
v_cmp_lt_u32 vcc, 0x7f, v200
s_and_b64 vcc, vcc, s[76:77]
s_cbranch_vccz .Lwo_rt10
s_branch .Lwo_sp9
.Lrole2:
s_sleep 127
s_sleep 73
s_add_u32 m0, s42, 4096
s_nop 0
global_load_lds_dwordx4 v0, s[40:41] offset:-4096 nt
global_load_lds_dwordx4 v1, s[40:41] offset:-3072 nt
global_load_lds_dwordx4 v2, s[40:41] offset:-2048 nt
global_load_lds_dwordx4 v3, s[40:41] offset:-1024 nt
global_load_lds_dwordx4 v4, s[40:41] offset:0 nt
)"
R"(global_load_lds_dwordx4 v5, s[40:41] offset:1024 nt
s_add_u32 s40, s40, 73728
s_addc_u32 s41, s41, 0
s_add_u32 m0, s42, 28672
s_nop 0
global_load_lds_dwordx4 v0, s[40:41] offset:-4096 nt
global_load_lds_dwordx4 v1, s[40:41] offset:-3072 nt
global_load_lds_dwordx4 v2, s[40:41] offset:-2048 nt
global_load_lds_dwordx4 v3, s[40:41] offset:-1024 nt
global_load_lds_dwordx4 v4, s[40:41] offset:0 nt
global_load_lds_dwordx4 v5, s[40:41] offset:1024 nt
s_add_u32 s40, s40, 73728
s_addc_u32 s41, s41, 0
s_add_u32 m0, s42, 53248
s_nop 0
global_load_lds_dwordx4 v0, s[40:41] offset:-4096 nt
global_load_lds_dwordx4 v1, s[40:41] offset:-3072 nt
global_load_lds_dwordx4 v2, s[40:41] offset:-2048 nt
global_load_lds_dwordx4 v3, s[40:41] offset:-1024 nt
global_load_lds_dwordx4 v4, s[40:41] offset:0 nt
global_load_lds_dwordx4 v5, s[40:41] offset:1024 nt
s_add_u32 s40, s40, 73728
s_addc_u32 s41, s41, 0
s_add_u32 m0, s42, 77824
s_nop 0
global_load_lds_dwordx4 v0, s[40:41] offset:-4096 nt
global_load_lds_dwordx4 v1, s[40:41] offset:-3072 nt
global_load_lds_dwordx4 v2, s[40:41] offset:-2048 nt
global_load_lds_dwordx4 v3, s[40:41] offset:-1024 nt
global_load_lds_dwordx4 v4, s[40:41] offset:0 nt
global_load_lds_dwordx4 v5, s[40:41] offset:1024 nt
s_add_u32 s40, s40, 73728
s_addc_u32 s41, s41, 0
s_add_u32 m0, s42, 102400
s_nop 0
global_load_lds_dwordx4 v0, s[40:41] offset:-4096 nt
global_load_lds_dwordx4 v1, s[40:41] offset:-3072 nt
global_load_lds_dwordx4 v2, s[40:41] offset:-2048 nt
global_load_lds_dwordx4 v3, s[40:41] offset:-1024 nt
global_load_lds_dwordx4 v4, s[40:41] offset:0 nt
global_load_lds_dwordx4 v5, s[40:41] offset:1024 nt
s_add_u32 s40, s40, 73728
s_addc_u32 s41, s41, 0
s_add_u32 m0, s42, 126976
s_nop 0
global_load_lds_dwordx4 v0, s[40:41] offset:-4096 nt
global_load_lds_dwordx4 v1, s[40:41] offset:-3072 nt
global_load_lds_dwordx4 v2, s[40:41] offset:-2048 nt
global_load_lds_dwordx4 v3, s[40:41] offset:-1024 nt
global_load_lds_dwordx4 v4, s[40:41] offset:0 nt
global_load_lds_dwordx4 v5, s[40:41] offset:1024 nt
s_add_u32 s40, s40, 73728
s_addc_u32 s41, s41, 0
v_add_u32 v255, 0x10000, v6
v_add_u32 v98, 0x20000, v6
v_add_u32 v99, 0x10000, v7
v_add_u32 v100, 0x20000, v7
v_add_u32 v101, 0x10000, v8
v_add_u32 v102, 0x20000, v8
s_waitcnt lgkmcnt(0)
s_mul_i32 s37, s37, 0x9E3779B1
s_add_u32 s37, s37, 0x7F4A7C15
v_add_u32 v14, s37, v14
v_add_u32 v16, s37, v16
s_waitcnt vmcnt(30)
s_barrier
ds_read_b64_tr_b16 a[0:1], v6 offset:0
ds_read_b64_tr_b16 a[2:3], v6 offset:1536
ds_read_b64_tr_b16 a[4:5], v7 offset:0
ds_read_b64_tr_b16 a[6:7], v7 offset:1536
ds_read_b64_tr_b16 a[8:9], v8 offset:0
ds_read_b64_tr_b16 a[10:11], v8 offset:1536
ds_read_b64_tr_b16 a[12:13], v6 offset:12288
ds_read_b64_tr_b16 a[14:15], v6 offset:13824
ds_read_b64_tr_b16 a[16:17], v7 offset:12288
ds_read_b64_tr_b16 a[18:19], v7 offset:13824
ds_read_b64_tr_b16 a[20:21], v8 offset:12288
ds_read_b64_tr_b16 a[22:23], v8 offset:13824
s_waitcnt lgkmcnt(0)
s_barrier
s_add_u32 m0, s42, 4096
s_nop 0
global_load_lds_dwordx4 v0, s[40:41] offset:-4096 nt
global_load_lds_dwordx4 v1, s[40:41] offset:-3072 nt
global_load_lds_dwordx4 v2, s[40:41] offset:-2048 nt
global_load_lds_dwordx4 v3, s[40:41] offset:-1024 nt
global_load_lds_dwordx4 v4, s[40:41] offset:0 nt
global_load_lds_dwordx4 v5, s[40:41] offset:1024 nt
s_add_u32 s40, s40, 73728
s_addc_u32 s41, s41, 0
s_waitcnt vmcnt(30)
s_barrier
ds_read_b64_tr_b16 a[24:25], v6 offset:24576
ds_read_b64_tr_b16 a[26:27], v6 offset:26112
ds_read_b64_tr_b16 a[28:29], v7 offset:24576
ds_read_b64_tr_b16 a[30:31], v7 offset:26112
ds_read_b64_tr_b16 a[32:33], v8 offset:24576
ds_read_b64_tr_b16 a[34:35], v8 offset:26112
ds_read_b64_tr_b16 a[36:37], v6 offset:36864
ds_read_b64_tr_b16 a[38:39], v6 offset:38400
ds_read_b64_tr_b16 a[40:41], v7 offset:36864
ds_read_b64_tr_b16 a[42:43], v7 offset:38400
ds_read_b64_tr_b16 a[44:45], v8 offset:36864
ds_read_b64_tr_b16 a[46:47], v8 offset:38400
s_waitcnt lgkmcnt(0)
s_barrier
s_add_u32 m0, s42, 28672
s_nop 0
global_load_lds_dwordx4 v0, s[40:41] offset:-4096 nt
global_load_lds_dwordx4 v1, s[40:41] offset:-3072 nt
global_load_lds_dwordx4 v2, s[40:41] offset:-2048 nt
global_load_lds_dwordx4 v3, s[40:41] offset:-1024 nt
global_load_lds_dwordx4 v4, s[40:41] offset:0 nt
global_load_lds_dwordx4 v5, s[40:41] offset:1024 nt
s_add_u32 s40, s40, 73728
s_addc_u32 s41, s41, 0
s_waitcnt vmcnt(30)
s_barrier
ds_read_b64_tr_b16 a[48:49], v6 offset:49152
ds_read_b64_tr_b16 a[50:51], v6 offset:50688
ds_read_b64_tr_b16 a[52:53], v7 offset:49152
ds_read_b64_tr_b16 a[54:55], v7 offset:50688
ds_read_b64_tr_b16 a[56:57], v8 offset:49152
ds_read_b64_tr_b16 a[58:59], v8 offset:50688
ds_read_b64_tr_b16 a[60:61], v6 offset:61440
ds_read_b64_tr_b16 a[62:63], v6 offset:62976
ds_read_b64_tr_b16 a[64:65], v7 offset:61440
ds_read_b64_tr_b16 a[66:67], v7 offset:62976
ds_read_b64_tr_b16 a[68:69], v8 offset:61440
ds_read_b64_tr_b16 a[70:71], v8 offset:62976
s_waitcnt lgkmcnt(0)
s_barrier
s_add_u32 m0, s42, 53248
s_nop 0
global_load_lds_dwordx4 v0, s[40:41] offset:-4096 nt
global_load_lds_dwordx4 v1, s[40:41] offset:-3072 nt
global_load_lds_dwordx4 v2, s[40:41] offset:-2048 nt
global_load_lds_dwordx4 v3, s[40:41] offset:-1024 nt
global_load_lds_dwordx4 v4, s[40:41] offset:0 nt
global_load_lds_dwordx4 v5, s[40:41] offset:1024 nt
s_add_u32 s40, s40, 73728
s_addc_u32 s41, s41, 0
s_waitcnt vmcnt(30)
s_barrier
ds_read_b64_tr_b16 a[72:73], v255 offset:8192
ds_read_b64_tr_b16 a[74:75], v255 offset:9728
ds_read_b64_tr_b16 a[76:77], v99 offset:8192
ds_read_b64_tr_b16 a[78:79], v99 offset:9728
ds_read_b64_tr_b16 a[80:81], v101 offset:8192
ds_read_b64_tr_b16 a[82:83], v101 offset:9728
ds_read_b64_tr_b16 a[84:85], v255 offset:20480
ds_read_b64_tr_b16 a[86:87], v255 offset:22016
ds_read_b64_tr_b16 a[88:89], v99 offset:20480
ds_read_b64_tr_b16 a[90:91], v99 offset:22016
ds_read_b64_tr_b16 a[92:93], v101 offset:20480
ds_read_b64_tr_b16 a[94:95], v101 offset:22016
s_waitcnt lgkmcnt(0)
s_barrier
s_add_u32 m0, s42, 77824
s_nop 0
global_load_lds_dwordx4 v0, s[40:41] offset:-4096 nt
global_load_lds_dwordx4 v1, s[40:41] offset:-3072 nt
global_load_lds_dwordx4 v2, s[40:41] offset:-2048 nt
global_load_lds_dwordx4 v3, s[40:41] offset:-1024 nt
global_load_lds_dwordx4 v4, s[40:41] offset:0 nt
global_load_lds_dwordx4 v5, s[40:41] offset:1024 nt
s_add_u32 s40, s40, 73728
s_addc_u32 s41, s41, 0
s_waitcnt vmcnt(30)
s_barrier
ds_read_b64_tr_b16 a[96:97], v255 offset:32768
ds_read_b64_tr_b16 a[98:99], v255 offset:34304
ds_read_b64_tr_b16 a[100:101], v99 offset:32768
ds_read_b64_tr_b16 a[102:103], v99 offset:34304
ds_read_b64_tr_b16 a[104:105], v101 offset:32768
ds_read_b64_tr_b16 a[106:107], v101 offset:34304
ds_read_b64_tr_b16 a[108:109], v255 offset:45056
ds_read_b64_tr_b16 a[110:111], v255 offset:46592
ds_read_b64_tr_b16 a[112:113], v99 offset:45056
ds_read_b64_tr_b16 a[114:115], v99 offset:46592
ds_read_b64_tr_b16 a[116:117], v101 offset:45056
ds_read_b64_tr_b16 a[118:119], v101 offset:46592
s_waitcnt lgkmcnt(0)
s_barrier
s_add_u32 m0, s42, 102400
s_nop 0
global_load_lds_dwordx4 v0, s[40:41] offset:-4096 nt
global_load_lds_dwordx4 v1, s[40:41] offset:-3072 nt
global_load_lds_dwordx4 v2, s[40:41] offset:-2048 nt
global_load_lds_dwordx4 v3, s[40:41] offset:-1024 nt
global_load_lds_dwordx4 v4, s[40:41] offset:0 nt
global_load_lds_dwordx4 v5, s[40:41] offset:1024 nt
s_add_u32 s40, s40, 73728
s_addc_u32 s41, s41, 0
s_waitcnt vmcnt(30)
s_barrier
ds_read_b64_tr_b16 a[120:121], v255 offset:57344
ds_read_b64_tr_b16 a[122:123], v255 offset:58880
ds_read_b64_tr_b16 a[124:125], v99 offset:57344
ds_read_b64_tr_b16 a[126:127], v99 offset:58880
ds_read_b64_tr_b16 a[128:129], v101 offset:57344
ds_read_b64_tr_b16 a[130:131], v101 offset:58880
ds_read_b64_tr_b16 a[132:133], v98 offset:4096
ds_read_b64_tr_b16 a[134:135], v98 offset:5632
ds_read_b64_tr_b16 a[136:137], v100 offset:4096
ds_read_b64_tr_b16 a[138:139], v100 offset:5632
ds_read_b64_tr_b16 a[140:141], v102 offset:4096
ds_read_b64_tr_b16 a[142:143], v102 offset:5632
s_waitcnt lgkmcnt(0)
s_barrier
s_add_u32 m0, s42, 126976
s_nop 0
global_load_lds_dwordx4 v0, s[40:41] offset:-4096 nt
global_load_lds_dwordx4 v1, s[40:41] offset:-3072 nt
global_load_lds_dwordx4 v2, s[40:41] offset:-2048 nt
global_load_lds_dwordx4 v3, s[40:41] offset:-1024 nt
global_load_lds_dwordx4 v4, s[40:41] offset:0 nt
global_load_lds_dwordx4 v5, s[40:41] offset:1024 nt
s_add_u32 s40, s40, 73728
s_addc_u32 s41, s41, 0
s_waitcnt vmcnt(30)
s_barrier
ds_read_b64_tr_b16 a[144:145], v6 offset:0
ds_read_b64_tr_b16 a[146:147], v6 offset:1536
ds_read_b64_tr_b16 a[148:149], v7 offset:0
ds_read_b64_tr_b16 a[150:151], v7 offset:1536
ds_read_b64_tr_b16 a[152:153], v8 offset:0
ds_read_b64_tr_b16 a[154:155], v8 offset:1536
ds_read_b64_tr_b16 a[156:157], v6 offset:12288
ds_read_b64_tr_b16 a[158:159], v6 offset:13824
ds_read_b64_tr_b16 a[160:161], v7 offset:12288
ds_read_b64_tr_b16 a[162:163], v7 offset:13824
ds_read_b64_tr_b16 a[164:165], v8 offset:12288
ds_read_b64_tr_b16 a[166:167], v8 offset:13824
s_waitcnt vmcnt(24)
s_barrier
ds_read_b64_tr_b16 a[168:169], v6 offset:24576
ds_read_b64_tr_b16 a[170:171], v6 offset:26112
ds_read_b64_tr_b16 a[172:173], v7 offset:24576
ds_read_b64_tr_b16 a[174:175], v7 offset:26112
ds_read_b64_tr_b16 a[176:177], v8 offset:24576
ds_read_b64_tr_b16 a[178:179], v8 offset:26112
ds_read_b64_tr_b16 a[180:181], v6 offset:36864
ds_read_b64_tr_b16 a[182:183], v6 offset:38400
ds_read_b64_tr_b16 a[184:185], v7 offset:36864
ds_read_b64_tr_b16 a[186:187], v7 offset:38400
ds_read_b64_tr_b16 a[188:189], v8 offset:36864
ds_read_b64_tr_b16 a[190:191], v8 offset:38400
s_waitcnt vmcnt(18)
s_barrier
ds_read_b64_tr_b16 a[192:193], v6 offset:49152
ds_read_b64_tr_b16 a[194:195], v6 offset:50688
ds_read_b64_tr_b16 a[196:197], v7 offset:49152
ds_read_b64_tr_b16 a[198:199], v7 offset:50688
ds_read_b64_tr_b16 a[200:201], v8 offset:49152
ds_read_b64_tr_b16 a[202:203], v8 offset:50688
ds_read_b64_tr_b16 a[204:205], v6 offset:61440
ds_read_b64_tr_b16 a[206:207], v6 offset:62976
ds_read_b64_tr_b16 a[208:209], v7 offset:61440
ds_read_b64_tr_b16 a[210:211], v7 offset:62976
ds_read_b64_tr_b16 a[212:213], v8 offset:61440
ds_read_b64_tr_b16 a[214:215], v8 offset:62976
s_waitcnt vmcnt(12)
s_barrier
ds_read_b64_tr_b16 a[216:217], v255 offset:8192
ds_read_b64_tr_b16 a[218:219], v255 offset:9728
ds_read_b64_tr_b16 a[220:221], v99 offset:8192
ds_read_b64_tr_b16 a[222:223], v99 offset:9728
ds_read_b64_tr_b16 a[224:225], v101 offset:8192
ds_read_b64_tr_b16 a[226:227], v101 offset:9728
ds_read_b64_tr_b16 a[228:229], v255 offset:20480
ds_read_b64_tr_b16 a[230:231], v255 offset:22016
ds_read_b64_tr_b16 a[232:233], v99 offset:20480
ds_read_b64_tr_b16 a[234:235], v99 offset:22016
ds_read_b64_tr_b16 a[236:237], v101 offset:20480
ds_read_b64_tr_b16 a[238:239], v101 offset:22016
s_waitcnt vmcnt(6)
s_barrier
ds_read_b64_tr_b16 a[240:241], v255 offset:32768
ds_read_b64_tr_b16 a[242:243], v255 offset:34304
ds_read_b64_tr_b16 a[244:245], v99 offset:32768
ds_read_b64_tr_b16 a[246:247], v99 offset:34304
ds_read_b64_tr_b16 a[248:249], v101 offset:32768
ds_read_b64_tr_b16 a[250:251], v101 offset:34304
ds_read_b64_tr_b16 a[252:253], v255 offset:45056
ds_read_b64_tr_b16 a[254:255], v255 offset:46592
ds_read_b64_tr_b16 v[18:19], v99 offset:45056
ds_read_b64_tr_b16 v[20:21], v99 offset:46592
ds_read_b64_tr_b16 v[22:23], v101 offset:45056
ds_read_b64_tr_b16 v[24:25], v101 offset:46592
s_waitcnt vmcnt(0)
s_barrier
ds_read_b64_tr_b16 v[26:27], v255 offset:57344
ds_read_b64_tr_b16 v[28:29], v255 offset:58880
ds_read_b64_tr_b16 v[30:31], v99 offset:57344
ds_read_b64_tr_b16 v[32:33], v99 offset:58880
ds_read_b64_tr_b16 v[34:35], v101 offset:57344
ds_read_b64_tr_b16 v[36:37], v101 offset:58880
ds_read_b64_tr_b16 v[38:39], v98 offset:4096
ds_read_b64_tr_b16 v[40:41], v98 offset:5632
ds_read_b64_tr_b16 v[42:43], v100 offset:4096
ds_read_b64_tr_b16 v[44:45], v100 offset:5632
ds_read_b64_tr_b16 v[46:47], v102 offset:4096
ds_read_b64_tr_b16 v[48:49], v102 offset:5632
s_waitcnt lgkmcnt(0)
s_barrier
s_and_b32 s75, s36, 3
s_cmp_eq_u32 s75, 1
s_cbranch_scc1 .Ldn_w2
s_cmp_eq_u32 s75, 2
s_cbranch_scc1 .Ldn_w3
s_cmp_eq_u32 s75, 3
s_cbranch_scc1 .Ldn_w4
.Ldn_w1:
v_add_u32 v254, -1, v14
s_mov_b32 s76, 0xffff
s_mov_b32 s77, 0xffff
s_mov_b32 s70, 32
s_waitcnt vmcnt(0) lgkmcnt(0)
s_nop 7
.p2align 6
.Ldn_top5:
s_waitcnt lgkmcnt(2)
v_mfma_f32_16x16x32_bf16 v[50:53], a[0:3], v[74:77], 0
ds_read_b128 v[86:89], v17 offset:3072
v_mfma_f32_16x16x32_bf16 v[54:57], a[4:7], v[74:77], 0
v_mfma_f32_16x16x32_bf16 v[58:61], a[8:11], v[74:77], 0
s_waitcnt lgkmcnt(2)
v_cvt_pk_bf16_f32 v90, v62, v63
v_cvt_pk_bf16_f32 v91, v64, v65
v_mfma_f32_16x16x32_bf16 v[50:53], a[12:15], v[78:81], v[50:53]
ds_read_b128 v[74:77], v17 offset:4096
v_cvt_pk_bf16_f32 v92, v66, v67
v_mfma_f32_16x16x32_bf16 v[54:57], a[16:19], v[78:81], v[54:57]
v_mfma_f32_16x16x32_bf16 v[58:61], a[20:23], v[78:81], v[58:61]
s_waitcnt vmcnt(6)
s_barrier
s_waitcnt lgkmcnt(2)
v_mfma_f32_16x16x32_bf16 v[50:53], a[24:27], v[82:85], v[50:53]
ds_read_b128 v[78:81], v17 offset:5120
v_cvt_pk_bf16_f32 v93, v68, v69
v_mfma_f32_16x16x32_bf16 v[54:57], a[28:31], v[82:85], v[54:57]
global_store_dwordx4 v11, v[90:93], s[38:39] sc1
v_mfma_f32_16x16x32_bf16 v[58:61], a[32:35], v[82:85], v[58:61]
s_waitcnt lgkmcnt(2)
v_add_u32 v11, 110592, v11
v_cvt_pk_bf16_f32 v94, v70, v71
v_mfma_f32_16x16x32_bf16 v[50:53], a[36:39], v[86:89], v[50:53]
ds_read_b128 v[82:85], v17 offset:6144
v_cvt_pk_bf16_f32 v96, v70, v71
v_mfma_f32_16x16x32_bf16 v[54:57], a[40:43], v[86:89], v[54:57]
v_cvt_pk_bf16_f32 v95, v72, v73
v_cvt_pk_bf16_f32 v97, v72, v73
v_mfma_f32_16x16x32_bf16 v[58:61], a[44:47], v[86:89], v[58:61]
s_waitcnt lgkmcnt(2)
v_permlane16_swap_b32 v94, v96
v_mfma_f32_16x16x32_bf16 v[50:53], a[48:51], v[74:77], v[50:53]
ds_read_b128 v[86:89], v17 offset:7168
v_mfma_f32_16x16x32_bf16 v[54:57], a[52:55], v[74:77], v[54:57]
v_permlane16_swap_b32 v95, v97
v_mfma_f32_16x16x32_bf16 v[58:61], a[56:59], v[74:77], v[58:61]
s_waitcnt lgkmcnt(2)
s_mov_b64 exec, s[76:77]
global_store_dwordx4 v12, v[94:97], s[38:39] sc1
s_mov_b64 exec, -1
v_mfma_f32_16x16x32_bf16 v[50:53], a[60:63], v[78:81], v[50:53]
ds_read_b128 v[74:77], v17 offset:8192
v_add_u32 v12, 110592, v12
v_mfma_f32_16x16x32_bf16 v[54:57], a[64:67], v[78:81], v[54:57]
v_mfma_f32_16x16x32_bf16 v[58:61], a[68:71], v[78:81], v[58:61]
s_waitcnt lgkmcnt(2)
v_mfma_f32_16x16x32_bf16 v[50:53], a[72:75], v[82:85], v[50:53]
ds_read_b128 v[78:81], v17 offset:9216
v_mfma_f32_16x16x32_bf16 v[54:57], a[76:79], v[82:85], v[54:57]
v_mfma_f32_16x16x32_bf16 v[58:61], a[80:83], v[82:85], v[58:61]
s_waitcnt lgkmcnt(2)
v_mfma_f32_16x16x32_bf16 v[50:53], a[84:87], v[86:89], v[50:53]
ds_read_b128 v[82:85], v17 offset:10240
v_mfma_f32_16x16x32_bf16 v[54:57], a[88:91], v[86:89], v[54:57]
v_mfma_f32_16x16x32_bf16 v[58:61], a[92:95], v[86:89], v[58:61]
s_waitcnt lgkmcnt(2)
v_mfma_f32_16x16x32_bf16 v[50:53], a[96:99], v[74:77], v[50:53]
ds_read_b128 v[86:89], v17 offset:11264
v_mfma_f32_16x16x32_bf16 v[54:57], a[100:103], v[74:77], v[54:57]
v_mfma_f32_16x16x32_bf16 v[58:61], a[104:107], v[74:77], v[58:61]
s_waitcnt lgkmcnt(2)
v_mfma_f32_16x16x32_bf16 v[50:53], a[108:111], v[78:81], v[50:53]
ds_read_b128 v[74:77], v17 offset:12288
v_mfma_f32_16x16x32_bf16 v[54:57], a[112:115], v[78:81], v[54:57]
v_mfma_f32_16x16x32_bf16 v[58:61], a[116:119], v[78:81], v[58:61]
s_waitcnt lgkmcnt(2)
v_mfma_f32_16x16x32_bf16 v[50:53], a[120:123], v[82:85], v[50:53]
ds_read_b128 v[78:81], v17 offset:13312
v_mfma_f32_16x16x32_bf16 v[54:57], a[124:127], v[82:85], v[54:57]
v_mfma_f32_16x16x32_bf16 v[58:61], a[128:131], v[82:85], v[58:61]
s_waitcnt lgkmcnt(2)
v_mfma_f32_16x16x32_bf16 v[50:53], a[132:135], v[86:89], v[50:53]
ds_read_b128 v[82:85], v17 offset:14336
v_mfma_f32_16x16x32_bf16 v[54:57], a[136:139], v[86:89], v[54:57]
v_mfma_f32_16x16x32_bf16 v[58:61], a[140:143], v[86:89], v[58:61]
)"
R"(s_waitcnt lgkmcnt(2)
v_mfma_f32_16x16x32_bf16 v[50:53], a[144:147], v[74:77], v[50:53]
ds_read_b128 v[86:89], v17 offset:15360
v_mfma_f32_16x16x32_bf16 v[54:57], a[148:151], v[74:77], v[54:57]
v_mfma_f32_16x16x32_bf16 v[58:61], a[152:155], v[74:77], v[58:61]
s_waitcnt lgkmcnt(2)
v_mfma_f32_16x16x32_bf16 v[50:53], a[156:159], v[78:81], v[50:53]
ds_read_b128 v[74:77], v17 offset:16384
v_mfma_f32_16x16x32_bf16 v[54:57], a[160:163], v[78:81], v[54:57]
s_xor_b32 s43, s43, 0x10000
v_mfma_f32_16x16x32_bf16 v[58:61], a[164:167], v[78:81], v[58:61]
s_add_u32 m0, s43, 0
s_waitcnt lgkmcnt(2)
v_mfma_f32_16x16x32_bf16 v[50:53], a[168:171], v[82:85], v[50:53]
ds_read_b128 v[78:81], v17 offset:17408
v_mfma_f32_16x16x32_bf16 v[54:57], a[172:175], v[82:85], v[54:57]
v_mfma_f32_16x16x32_bf16 v[58:61], a[176:179], v[82:85], v[58:61]
s_barrier
s_waitcnt lgkmcnt(2)
v_mfma_f32_16x16x32_bf16 v[50:53], a[180:183], v[86:89], v[50:53]
ds_read_b128 v[82:85], v17 offset:18432
global_load_lds_dwordx4 v9, s[38:39] offset:-2048 sc1
v_mfma_f32_16x16x32_bf16 v[54:57], a[184:187], v[86:89], v[54:57]
v_mfma_f32_16x16x32_bf16 v[58:61], a[188:191], v[86:89], v[58:61]
s_waitcnt lgkmcnt(2)
v_mfma_f32_16x16x32_bf16 v[50:53], a[192:195], v[74:77], v[50:53]
ds_read_b128 v[86:89], v17 offset:19456
global_load_lds_dwordx4 v9, s[38:39] offset:-1024 sc1
v_mfma_f32_16x16x32_bf16 v[54:57], a[196:199], v[74:77], v[54:57]
v_mfma_f32_16x16x32_bf16 v[58:61], a[200:203], v[74:77], v[58:61]
s_waitcnt lgkmcnt(2)
v_mfma_f32_16x16x32_bf16 v[50:53], a[204:207], v[78:81], v[50:53]
ds_read_b128 v[74:77], v17 offset:20480
global_load_lds_dwordx4 v9, s[38:39] offset:0 sc1
v_mfma_f32_16x16x32_bf16 v[54:57], a[208:211], v[78:81], v[54:57]
v_mfma_f32_16x16x32_bf16 v[58:61], a[212:215], v[78:81], v[58:61]
s_waitcnt lgkmcnt(2)
v_mfma_f32_16x16x32_bf16 v[50:53], a[216:219], v[82:85], v[50:53]
ds_read_b128 v[78:81], v17 offset:21504
global_load_lds_dwordx4 v9, s[38:39] offset:1024 sc1
v_mfma_f32_16x16x32_bf16 v[54:57], a[220:223], v[82:85], v[54:57]
v_mfma_f32_16x16x32_bf16 v[58:61], a[224:227], v[82:85], v[58:61]
s_waitcnt lgkmcnt(2)
v_mfma_f32_16x16x32_bf16 v[50:53], a[228:231], v[86:89], v[50:53]
ds_read_b128 v[82:85], v17 offset:22528
global_load_lds_dwordx4 v9, s[38:39] offset:2048 sc1
v_mfma_f32_16x16x32_bf16 v[54:57], a[232:235], v[86:89], v[54:57]
v_mfma_f32_16x16x32_bf16 v[58:61], a[236:239], v[86:89], v[58:61]
s_waitcnt lgkmcnt(2)
v_mfma_f32_16x16x32_bf16 v[50:53], a[240:243], v[74:77], v[50:53]
ds_read_b128 v[86:89], v17 offset:23552
global_load_lds_dwordx4 v9, s[38:39] offset:3072 sc1
v_mfma_f32_16x16x32_bf16 v[54:57], a[244:247], v[74:77], v[54:57]
v_mfma_f32_16x16x32_bf16 v[58:61], a[248:251], v[74:77], v[58:61]
s_waitcnt vmcnt(8)
s_barrier
s_waitcnt lgkmcnt(2)
v_mfma_f32_16x16x32_bf16 v[50:53], a[252:255], v[78:81], v[50:53]
ds_read_b128 v[74:77], v17 offset:24576
v_add_u32 v9, 147456, v9
v_mfma_f32_16x16x32_bf16 v[54:57], v[18:21], v[78:81], v[54:57]
v_min_u32 v9, v9, v10
v_mfma_f32_16x16x32_bf16 v[58:61], v[22:25], v[78:81], v[58:61]
s_waitcnt lgkmcnt(2)
v_mfma_f32_16x16x32_bf16 v[50:53], v[26:29], v[82:85], v[50:53]
ds_read_b128 v[78:81], v17 offset:25600
v_mfma_f32_16x16x32_bf16 v[54:57], v[30:33], v[82:85], v[54:57]
v_mfma_f32_16x16x32_bf16 v[58:61], v[34:37], v[82:85], v[58:61]
s_waitcnt lgkmcnt(2)
v_mfma_f32_16x16x32_bf16 v[50:53], v[38:41], v[86:89], v[50:53]
ds_read_b128 v[82:85], v17 offset:26624
v_mfma_f32_16x16x32_bf16 v[54:57], v[42:45], v[86:89], v[54:57]
v_mfma_f32_16x16x32_bf16 v[58:61], v[46:49], v[86:89], v[58:61]
s_waitcnt lgkmcnt(2)
v_mfma_f32_16x16x32_bf16 v[62:65], a[0:3], v[74:77], 0
ds_read_b128 v[86:89], v17 offset:27648
v_mfma_f32_16x16x32_bf16 v[66:69], a[4:7], v[74:77], 0
v_mfma_f32_16x16x32_bf16 v[70:73], a[8:11], v[74:77], 0
s_waitcnt lgkmcnt(2)
v_cvt_pk_bf16_f32 v90, v50, v51
v_cvt_pk_bf16_f32 v91, v52, v53
v_mfma_f32_16x16x32_bf16 v[62:65], a[12:15], v[78:81], v[62:65]
ds_read_b128 v[74:77], v17 offset:28672
v_cvt_pk_bf16_f32 v92, v54, v55
v_mfma_f32_16x16x32_bf16 v[66:69], a[16:19], v[78:81], v[66:69]
v_mfma_f32_16x16x32_bf16 v[70:73], a[20:23], v[78:81], v[70:73]
s_waitcnt vmcnt(6)
s_barrier
s_waitcnt lgkmcnt(2)
v_mfma_f32_16x16x32_bf16 v[62:65], a[24:27], v[82:85], v[62:65]
ds_read_b128 v[78:81], v17 offset:29696
v_cvt_pk_bf16_f32 v93, v56, v57
v_mfma_f32_16x16x32_bf16 v[66:69], a[28:31], v[82:85], v[66:69]
global_store_dwordx4 v11, v[90:93], s[38:39] sc1
v_mfma_f32_16x16x32_bf16 v[70:73], a[32:35], v[82:85], v[70:73]
s_waitcnt lgkmcnt(2)
v_add_u32 v11, 110592, v11
v_cvt_pk_bf16_f32 v94, v58, v59
v_mfma_f32_16x16x32_bf16 v[62:65], a[36:39], v[86:89], v[62:65]
ds_read_b128 v[82:85], v17 offset:30720
v_cvt_pk_bf16_f32 v96, v58, v59
v_mfma_f32_16x16x32_bf16 v[66:69], a[40:43], v[86:89], v[66:69]
v_cvt_pk_bf16_f32 v95, v60, v61
v_cvt_pk_bf16_f32 v97, v60, v61
v_mfma_f32_16x16x32_bf16 v[70:73], a[44:47], v[86:89], v[70:73]
s_waitcnt lgkmcnt(2)
v_permlane16_swap_b32 v94, v96
v_mfma_f32_16x16x32_bf16 v[62:65], a[48:51], v[74:77], v[62:65]
ds_read_b128 v[86:89], v17 offset:31744
v_mfma_f32_16x16x32_bf16 v[66:69], a[52:55], v[74:77], v[66:69]
v_permlane16_swap_b32 v95, v97
v_mfma_f32_16x16x32_bf16 v[70:73], a[56:59], v[74:77], v[70:73]
s_waitcnt lgkmcnt(2)
s_mov_b64 exec, s[76:77]
global_store_dwordx4 v12, v[94:97], s[38:39] sc1
s_mov_b64 exec, -1
v_mfma_f32_16x16x32_bf16 v[62:65], a[60:63], v[78:81], v[62:65]
ds_read_b128 v[74:77], v17 offset:32768
v_add_u32 v12, 110592, v12
v_mfma_f32_16x16x32_bf16 v[66:69], a[64:67], v[78:81], v[66:69]
v_mfma_f32_16x16x32_bf16 v[70:73], a[68:71], v[78:81], v[70:73]
s_waitcnt lgkmcnt(2)
v_mfma_f32_16x16x32_bf16 v[62:65], a[72:75], v[82:85], v[62:65]
ds_read_b128 v[78:81], v17 offset:33792
v_mfma_f32_16x16x32_bf16 v[66:69], a[76:79], v[82:85], v[66:69]
v_mfma_f32_16x16x32_bf16 v[70:73], a[80:83], v[82:85], v[70:73]
s_waitcnt lgkmcnt(2)
v_mfma_f32_16x16x32_bf16 v[62:65], a[84:87], v[86:89], v[62:65]
ds_read_b128 v[82:85], v17 offset:34816
v_mfma_f32_16x16x32_bf16 v[66:69], a[88:91], v[86:89], v[66:69]
v_mfma_f32_16x16x32_bf16 v[70:73], a[92:95], v[86:89], v[70:73]
s_waitcnt lgkmcnt(2)
v_mfma_f32_16x16x32_bf16 v[62:65], a[96:99], v[74:77], v[62:65]
ds_read_b128 v[86:89], v17 offset:35840
v_mfma_f32_16x16x32_bf16 v[66:69], a[100:103], v[74:77], v[66:69]
v_mfma_f32_16x16x32_bf16 v[70:73], a[104:107], v[74:77], v[70:73]
s_waitcnt lgkmcnt(2)
v_mfma_f32_16x16x32_bf16 v[62:65], a[108:111], v[78:81], v[62:65]
ds_read_b128 v[74:77], v17 offset:36864
v_mfma_f32_16x16x32_bf16 v[66:69], a[112:115], v[78:81], v[66:69]
v_mfma_f32_16x16x32_bf16 v[70:73], a[116:119], v[78:81], v[70:73]
s_waitcnt lgkmcnt(2)
v_mfma_f32_16x16x32_bf16 v[62:65], a[120:123], v[82:85], v[62:65]
ds_read_b128 v[78:81], v17 offset:37888
v_mfma_f32_16x16x32_bf16 v[66:69], a[124:127], v[82:85], v[66:69]
v_mfma_f32_16x16x32_bf16 v[70:73], a[128:131], v[82:85], v[70:73]
s_waitcnt lgkmcnt(2)
v_mfma_f32_16x16x32_bf16 v[62:65], a[132:135], v[86:89], v[62:65]
ds_read_b128 v[82:85], v17 offset:38912
v_mfma_f32_16x16x32_bf16 v[66:69], a[136:139], v[86:89], v[66:69]
v_mfma_f32_16x16x32_bf16 v[70:73], a[140:143], v[86:89], v[70:73]
s_waitcnt lgkmcnt(2)
v_mfma_f32_16x16x32_bf16 v[62:65], a[144:147], v[74:77], v[62:65]
ds_read_b128 v[86:89], v17 offset:39936
v_mfma_f32_16x16x32_bf16 v[66:69], a[148:151], v[74:77], v[66:69]
v_mfma_f32_16x16x32_bf16 v[70:73], a[152:155], v[74:77], v[70:73]
s_waitcnt lgkmcnt(2)
v_mfma_f32_16x16x32_bf16 v[62:65], a[156:159], v[78:81], v[62:65]
ds_read_b128 v[74:77], v17 offset:40960
v_mfma_f32_16x16x32_bf16 v[66:69], a[160:163], v[78:81], v[66:69]
v_mfma_f32_16x16x32_bf16 v[70:73], a[164:167], v[78:81], v[70:73]
s_add_u32 m0, s43, 24576
s_waitcnt lgkmcnt(2)
v_mfma_f32_16x16x32_bf16 v[62:65], a[168:171], v[82:85], v[62:65]
ds_read_b128 v[78:81], v17 offset:41984
v_mfma_f32_16x16x32_bf16 v[66:69], a[172:175], v[82:85], v[66:69]
v_mfma_f32_16x16x32_bf16 v[70:73], a[176:179], v[82:85], v[70:73]
s_barrier
s_waitcnt lgkmcnt(2)
v_mfma_f32_16x16x32_bf16 v[62:65], a[180:183], v[86:89], v[62:65]
ds_read_b128 v[82:85], v17 offset:43008
global_load_lds_dwordx4 v9, s[38:39] offset:-2048 sc1
v_mfma_f32_16x16x32_bf16 v[66:69], a[184:187], v[86:89], v[66:69]
v_mfma_f32_16x16x32_bf16 v[70:73], a[188:191], v[86:89], v[70:73]
s_waitcnt lgkmcnt(2)
v_mfma_f32_16x16x32_bf16 v[62:65], a[192:195], v[74:77], v[62:65]
ds_read_b128 v[86:89], v17 offset:44032
global_load_lds_dwordx4 v9, s[38:39] offset:-1024 sc1
v_mfma_f32_16x16x32_bf16 v[66:69], a[196:199], v[74:77], v[66:69]
v_mfma_f32_16x16x32_bf16 v[70:73], a[200:203], v[74:77], v[70:73]
s_waitcnt lgkmcnt(2)
v_mfma_f32_16x16x32_bf16 v[62:65], a[204:207], v[78:81], v[62:65]
ds_read_b128 v[74:77], v17 offset:45056
global_load_lds_dwordx4 v9, s[38:39] offset:0 sc1
v_mfma_f32_16x16x32_bf16 v[66:69], a[208:211], v[78:81], v[66:69]
v_mfma_f32_16x16x32_bf16 v[70:73], a[212:215], v[78:81], v[70:73]
s_waitcnt lgkmcnt(2)
v_mfma_f32_16x16x32_bf16 v[62:65], a[216:219], v[82:85], v[62:65]
ds_read_b128 v[78:81], v17 offset:46080
global_load_lds_dwordx4 v9, s[38:39] offset:1024 sc1
v_mfma_f32_16x16x32_bf16 v[66:69], a[220:223], v[82:85], v[66:69]
v_mfma_f32_16x16x32_bf16 v[70:73], a[224:227], v[82:85], v[70:73]
s_waitcnt lgkmcnt(2)
v_mfma_f32_16x16x32_bf16 v[62:65], a[228:231], v[86:89], v[62:65]
ds_read_b128 v[82:85], v17 offset:47104
global_load_lds_dwordx4 v9, s[38:39] offset:2048 sc1
v_mfma_f32_16x16x32_bf16 v[66:69], a[232:235], v[86:89], v[66:69]
v_mfma_f32_16x16x32_bf16 v[70:73], a[236:239], v[86:89], v[70:73]
s_waitcnt lgkmcnt(2)
v_mfma_f32_16x16x32_bf16 v[62:65], a[240:243], v[74:77], v[62:65]
ds_read_b128 v[86:89], v17 offset:48128
v_xor_b32 v17, 0x10000, v17
v_mfma_f32_16x16x32_bf16 v[66:69], a[244:247], v[74:77], v[66:69]
v_mfma_f32_16x16x32_bf16 v[70:73], a[248:251], v[74:77], v[70:73]
s_waitcnt vmcnt(7)
s_barrier
s_waitcnt lgkmcnt(2)
v_mfma_f32_16x16x32_bf16 v[62:65], a[252:255], v[78:81], v[62:65]
ds_read_b128 v[74:77], v17 offset:0
global_load_lds_dwordx4 v9, s[38:39] offset:3072 sc1
v_mfma_f32_16x16x32_bf16 v[66:69], v[18:21], v[78:81], v[66:69]
v_add_u32 v9, 147456, v9
v_min_u32 v9, v9, v10
v_mfma_f32_16x16x32_bf16 v[70:73], v[22:25], v[78:81], v[70:73]
s_waitcnt lgkmcnt(2)
v_mfma_f32_16x16x32_bf16 v[62:65], v[26:29], v[82:85], v[62:65]
ds_read_b128 v[78:81], v17 offset:1024
v_mfma_f32_16x16x32_bf16 v[66:69], v[30:33], v[82:85], v[66:69]
v_mfma_f32_16x16x32_bf16 v[70:73], v[34:37], v[82:85], v[70:73]
s_waitcnt lgkmcnt(2)
v_mfma_f32_16x16x32_bf16 v[62:65], v[38:41], v[86:89], v[62:65]
ds_read_b128 v[82:85], v17 offset:2048
v_mfma_f32_16x16x32_bf16 v[66:69], v[42:45], v[86:89], v[66:69]
v_mfma_f32_16x16x32_bf16 v[70:73], v[46:49], v[86:89], v[70:73]
s_sub_u32 s70, s70, 1
s_cbranch_scc0 .Ldn_top5
s_nop 2
v_cvt_pk_bf16_f32 v90, v62, v63
v_cvt_pk_bf16_f32 v91, v64, v65
v_cvt_pk_bf16_f32 v92, v66, v67
v_cvt_pk_bf16_f32 v93, v68, v69
global_store_dwordx4 v11, v[90:93], s[38:39] sc1
v_add_u32 v11, 110592, v11
v_cvt_pk_bf16_f32 v94, v70, v71
v_cvt_pk_bf16_f32 v96, v70, v71
v_cvt_pk_bf16_f32 v95, v72, v73
v_cvt_pk_bf16_f32 v97, v72, v73
v_permlane16_swap_b32 v94, v96
s_nop 0
v_permlane16_swap_b32 v95, v97
s_mov_b64 exec, s[76:77]
global_store_dwordx4 v12, v[94:97], s[38:39] sc1
s_mov_b64 exec, -1
v_add_u32 v12, 110592, v12
s_waitcnt vmcnt(2)
s_barrier
s_waitcnt vmcnt(0)
s_barrier
s_endpgm
.Ldn_w2:
v_add_u32 v254, -1, v14
s_mov_b32 s76, 0xffff
s_mov_b32 s77, 0xffff
s_mov_b32 s70, 32
s_waitcnt vmcnt(0) lgkmcnt(0)
s_nop 7
.p2align 6
.Ldn_top6:
s_waitcnt lgkmcnt(2)
v_mfma_f32_16x16x32_bf16 v[50:53], a[0:3], v[74:77], 0
ds_read_b128 v[86:89], v17 offset:3072
v_mfma_f32_16x16x32_bf16 v[54:57], a[4:7], v[74:77], 0
v_mfma_f32_16x16x32_bf16 v[58:61], a[8:11], v[74:77], 0
s_waitcnt lgkmcnt(2)
v_cvt_pk_bf16_f32 v90, v62, v63
v_cvt_pk_bf16_f32 v91, v64, v65
v_mfma_f32_16x16x32_bf16 v[50:53], a[12:15], v[78:81], v[50:53]
ds_read_b128 v[74:77], v17 offset:4096
v_cvt_pk_bf16_f32 v92, v66, v67
v_mfma_f32_16x16x32_bf16 v[54:57], a[16:19], v[78:81], v[54:57]
v_mfma_f32_16x16x32_bf16 v[58:61], a[20:23], v[78:81], v[58:61]
s_waitcnt vmcnt(6)
s_barrier
s_waitcnt lgkmcnt(2)
v_mfma_f32_16x16x32_bf16 v[50:53], a[24:27], v[82:85], v[50:53]
ds_read_b128 v[78:81], v17 offset:5120
v_cvt_pk_bf16_f32 v93, v68, v69
v_mfma_f32_16x16x32_bf16 v[54:57], a[28:31], v[82:85], v[54:57]
v_cvt_pk_bf16_f32 v94, v70, v71
v_cvt_pk_bf16_f32 v96, v70, v71
v_mfma_f32_16x16x32_bf16 v[58:61], a[32:35], v[82:85], v[58:61]
s_waitcnt lgkmcnt(2)
global_store_dwordx4 v11, v[90:93], s[38:39] sc1
v_mfma_f32_16x16x32_bf16 v[50:53], a[36:39], v[86:89], v[50:53]
ds_read_b128 v[82:85], v17 offset:6144
v_add_u32 v11, 110592, v11
v_mfma_f32_16x16x32_bf16 v[54:57], a[40:43], v[86:89], v[54:57]
v_cvt_pk_bf16_f32 v95, v72, v73
v_cvt_pk_bf16_f32 v97, v72, v73
v_mfma_f32_16x16x32_bf16 v[58:61], a[44:47], v[86:89], v[58:61]
s_waitcnt lgkmcnt(2)
v_permlane16_swap_b32 v94, v96
v_mfma_f32_16x16x32_bf16 v[50:53], a[48:51], v[74:77], v[50:53]
ds_read_b128 v[86:89], v17 offset:7168
v_mfma_f32_16x16x32_bf16 v[54:57], a[52:55], v[74:77], v[54:57]
v_permlane16_swap_b32 v95, v97
v_mfma_f32_16x16x32_bf16 v[58:61], a[56:59], v[74:77], v[58:61]
s_waitcnt lgkmcnt(2)
s_mov_b64 exec, s[76:77]
global_store_dwordx4 v12, v[94:97], s[38:39] sc1
s_mov_b64 exec, -1
v_mfma_f32_16x16x32_bf16 v[50:53], a[60:63], v[78:81], v[50:53]
ds_read_b128 v[74:77], v17 offset:8192
v_add_u32 v12, 110592, v12
v_mfma_f32_16x16x32_bf16 v[54:57], a[64:67], v[78:81], v[54:57]
v_mfma_f32_16x16x32_bf16 v[58:61], a[68:71], v[78:81], v[58:61]
s_waitcnt lgkmcnt(2)
v_mfma_f32_16x16x32_bf16 v[50:53], a[72:75], v[82:85], v[50:53]
ds_read_b128 v[78:81], v17 offset:9216
v_mfma_f32_16x16x32_bf16 v[54:57], a[76:79], v[82:85], v[54:57]
v_mfma_f32_16x16x32_bf16 v[58:61], a[80:83], v[82:85], v[58:61]
s_waitcnt lgkmcnt(2)
v_mfma_f32_16x16x32_bf16 v[50:53], a[84:87], v[86:89], v[50:53]
ds_read_b128 v[82:85], v17 offset:10240
v_mfma_f32_16x16x32_bf16 v[54:57], a[88:91], v[86:89], v[54:57]
v_mfma_f32_16x16x32_bf16 v[58:61], a[92:95], v[86:89], v[58:61]
s_waitcnt lgkmcnt(2)
v_mfma_f32_16x16x32_bf16 v[50:53], a[96:99], v[74:77], v[50:53]
ds_read_b128 v[86:89], v17 offset:11264
v_mfma_f32_16x16x32_bf16 v[54:57], a[100:103], v[74:77], v[54:57]
v_mfma_f32_16x16x32_bf16 v[58:61], a[104:107], v[74:77], v[58:61]
s_waitcnt lgkmcnt(2)
v_mfma_f32_16x16x32_bf16 v[50:53], a[108:111], v[78:81], v[50:53]
ds_read_b128 v[74:77], v17 offset:12288
v_mfma_f32_16x16x32_bf16 v[54:57], a[112:115], v[78:81], v[54:57]
v_mfma_f32_16x16x32_bf16 v[58:61], a[116:119], v[78:81], v[58:61]
s_waitcnt lgkmcnt(2)
v_mfma_f32_16x16x32_bf16 v[50:53], a[120:123], v[82:85], v[50:53]
ds_read_b128 v[78:81], v17 offset:13312
v_mfma_f32_16x16x32_bf16 v[54:57], a[124:127], v[82:85], v[54:57]
v_mfma_f32_16x16x32_bf16 v[58:61], a[128:131], v[82:85], v[58:61]
s_waitcnt lgkmcnt(2)
v_mfma_f32_16x16x32_bf16 v[50:53], a[132:135], v[86:89], v[50:53]
ds_read_b128 v[82:85], v17 offset:14336
v_mfma_f32_16x16x32_bf16 v[54:57], a[136:139], v[86:89], v[54:57]
v_mfma_f32_16x16x32_bf16 v[58:61], a[140:143], v[86:89], v[58:61]
s_waitcnt lgkmcnt(2)
v_mfma_f32_16x16x32_bf16 v[50:53], a[144:147], v[74:77], v[50:53]
ds_read_b128 v[86:89], v17 offset:15360
v_mfma_f32_16x16x32_bf16 v[54:57], a[148:151], v[74:77], v[54:57]
v_mfma_f32_16x16x32_bf16 v[58:61], a[152:155], v[74:77], v[58:61]
s_waitcnt lgkmcnt(2)
v_mfma_f32_16x16x32_bf16 v[50:53], a[156:159], v[78:81], v[50:53]
ds_read_b128 v[74:77], v17 offset:16384
v_mfma_f32_16x16x32_bf16 v[54:57], a[160:163], v[78:81], v[54:57]
s_xor_b32 s43, s43, 0x10000
v_mfma_f32_16x16x32_bf16 v[58:61], a[164:167], v[78:81], v[58:61]
s_add_u32 m0, s43, 0
s_waitcnt lgkmcnt(2)
v_mfma_f32_16x16x32_bf16 v[50:53], a[168:171], v[82:85], v[50:53]
ds_read_b128 v[78:81], v17 offset:17408
v_mfma_f32_16x16x32_bf16 v[54:57], a[172:175], v[82:85], v[54:57]
v_mfma_f32_16x16x32_bf16 v[58:61], a[176:179], v[82:85], v[58:61]
s_barrier
s_waitcnt lgkmcnt(2)
v_mfma_f32_16x16x32_bf16 v[50:53], a[180:183], v[86:89], v[50:53]
ds_read_b128 v[82:85], v17 offset:18432
v_mfma_f32_16x16x32_bf16 v[54:57], a[184:187], v[86:89], v[54:57]
global_load_lds_dwordx4 v9, s[38:39] offset:-2048 sc1
v_mfma_f32_16x16x32_bf16 v[58:61], a[188:191], v[86:89], v[58:61]
s_waitcnt lgkmcnt(2)
v_mfma_f32_16x16x32_bf16 v[50:53], a[192:195], v[74:77], v[50:53]
ds_read_b128 v[86:89], v17 offset:19456
v_mfma_f32_16x16x32_bf16 v[54:57], a[196:199], v[74:77], v[54:57]
global_load_lds_dwordx4 v9, s[38:39] offset:-1024 sc1
v_mfma_f32_16x16x32_bf16 v[58:61], a[200:203], v[74:77], v[58:61]
s_waitcnt lgkmcnt(2)
v_mfma_f32_16x16x32_bf16 v[50:53], a[204:207], v[78:81], v[50:53]
ds_read_b128 v[74:77], v17 offset:20480
v_mfma_f32_16x16x32_bf16 v[54:57], a[208:211], v[78:81], v[54:57]
global_load_lds_dwordx4 v9, s[38:39] offset:0 sc1
v_mfma_f32_16x16x32_bf16 v[58:61], a[212:215], v[78:81], v[58:61]
s_waitcnt lgkmcnt(2)
v_mfma_f32_16x16x32_bf16 v[50:53], a[216:219], v[82:85], v[50:53]
ds_read_b128 v[78:81], v17 offset:21504
v_mfma_f32_16x16x32_bf16 v[54:57], a[220:223], v[82:85], v[54:57]
global_load_lds_dwordx4 v9, s[38:39] offset:1024 sc1
v_mfma_f32_16x16x32_bf16 v[58:61], a[224:227], v[82:85], v[58:61]
s_waitcnt lgkmcnt(2)
v_mfma_f32_16x16x32_bf16 v[50:53], a[228:231], v[86:89], v[50:53]
ds_read_b128 v[82:85], v17 offset:22528
v_mfma_f32_16x16x32_bf16 v[54:57], a[232:235], v[86:89], v[54:57]
global_load_lds_dwordx4 v9, s[38:39] offset:2048 sc1
v_mfma_f32_16x16x32_bf16 v[58:61], a[236:239], v[86:89], v[58:61]
s_waitcnt lgkmcnt(2)
v_mfma_f32_16x16x32_bf16 v[50:53], a[240:243], v[74:77], v[50:53]
ds_read_b128 v[86:89], v17 offset:23552
v_mfma_f32_16x16x32_bf16 v[54:57], a[244:247], v[74:77], v[54:57]
v_mfma_f32_16x16x32_bf16 v[58:61], a[248:251], v[74:77], v[58:61]
s_waitcnt vmcnt(7)
s_barrier
s_waitcnt lgkmcnt(2)
v_mfma_f32_16x16x32_bf16 v[50:53], a[252:255], v[78:81], v[50:53]
ds_read_b128 v[74:77], v17 offset:24576
global_load_lds_dwordx4 v9, s[38:39] offset:3072 sc1
v_mfma_f32_16x16x32_bf16 v[54:57], v[18:21], v[78:81], v[54:57]
v_add_u32 v9, 147456, v9
v_min_u32 v9, v9, v10
v_mfma_f32_16x16x32_bf16 v[58:61], v[22:25], v[78:81], v[58:61]
s_waitcnt lgkmcnt(2)
)"
R"(v_mfma_f32_16x16x32_bf16 v[50:53], v[26:29], v[82:85], v[50:53]
ds_read_b128 v[78:81], v17 offset:25600
v_mfma_f32_16x16x32_bf16 v[54:57], v[30:33], v[82:85], v[54:57]
v_mfma_f32_16x16x32_bf16 v[58:61], v[34:37], v[82:85], v[58:61]
s_waitcnt lgkmcnt(2)
v_mfma_f32_16x16x32_bf16 v[50:53], v[38:41], v[86:89], v[50:53]
ds_read_b128 v[82:85], v17 offset:26624
v_mfma_f32_16x16x32_bf16 v[54:57], v[42:45], v[86:89], v[54:57]
v_mfma_f32_16x16x32_bf16 v[58:61], v[46:49], v[86:89], v[58:61]
s_waitcnt lgkmcnt(2)
v_mfma_f32_16x16x32_bf16 v[62:65], a[0:3], v[74:77], 0
ds_read_b128 v[86:89], v17 offset:27648
v_mfma_f32_16x16x32_bf16 v[66:69], a[4:7], v[74:77], 0
v_mfma_f32_16x16x32_bf16 v[70:73], a[8:11], v[74:77], 0
s_waitcnt lgkmcnt(2)
v_cvt_pk_bf16_f32 v90, v50, v51
v_cvt_pk_bf16_f32 v91, v52, v53
v_mfma_f32_16x16x32_bf16 v[62:65], a[12:15], v[78:81], v[62:65]
ds_read_b128 v[74:77], v17 offset:28672
v_cvt_pk_bf16_f32 v92, v54, v55
v_mfma_f32_16x16x32_bf16 v[66:69], a[16:19], v[78:81], v[66:69]
v_mfma_f32_16x16x32_bf16 v[70:73], a[20:23], v[78:81], v[70:73]
s_waitcnt vmcnt(6)
s_barrier
s_waitcnt lgkmcnt(2)
v_mfma_f32_16x16x32_bf16 v[62:65], a[24:27], v[82:85], v[62:65]
ds_read_b128 v[78:81], v17 offset:29696
v_cvt_pk_bf16_f32 v93, v56, v57
v_mfma_f32_16x16x32_bf16 v[66:69], a[28:31], v[82:85], v[66:69]
v_cvt_pk_bf16_f32 v94, v58, v59
v_cvt_pk_bf16_f32 v96, v58, v59
v_mfma_f32_16x16x32_bf16 v[70:73], a[32:35], v[82:85], v[70:73]
s_waitcnt lgkmcnt(2)
global_store_dwordx4 v11, v[90:93], s[38:39] sc1
v_mfma_f32_16x16x32_bf16 v[62:65], a[36:39], v[86:89], v[62:65]
ds_read_b128 v[82:85], v17 offset:30720
v_add_u32 v11, 110592, v11
v_mfma_f32_16x16x32_bf16 v[66:69], a[40:43], v[86:89], v[66:69]
v_cvt_pk_bf16_f32 v95, v60, v61
v_cvt_pk_bf16_f32 v97, v60, v61
v_mfma_f32_16x16x32_bf16 v[70:73], a[44:47], v[86:89], v[70:73]
s_waitcnt lgkmcnt(2)
v_permlane16_swap_b32 v94, v96
v_mfma_f32_16x16x32_bf16 v[62:65], a[48:51], v[74:77], v[62:65]
ds_read_b128 v[86:89], v17 offset:31744
v_mfma_f32_16x16x32_bf16 v[66:69], a[52:55], v[74:77], v[66:69]
v_permlane16_swap_b32 v95, v97
v_mfma_f32_16x16x32_bf16 v[70:73], a[56:59], v[74:77], v[70:73]
s_waitcnt lgkmcnt(2)
s_mov_b64 exec, s[76:77]
global_store_dwordx4 v12, v[94:97], s[38:39] sc1
s_mov_b64 exec, -1
v_mfma_f32_16x16x32_bf16 v[62:65], a[60:63], v[78:81], v[62:65]
ds_read_b128 v[74:77], v17 offset:32768
v_add_u32 v12, 110592, v12
v_mfma_f32_16x16x32_bf16 v[66:69], a[64:67], v[78:81], v[66:69]
v_mfma_f32_16x16x32_bf16 v[70:73], a[68:71], v[78:81], v[70:73]
s_waitcnt lgkmcnt(2)
v_mfma_f32_16x16x32_bf16 v[62:65], a[72:75], v[82:85], v[62:65]
ds_read_b128 v[78:81], v17 offset:33792
v_mfma_f32_16x16x32_bf16 v[66:69], a[76:79], v[82:85], v[66:69]
v_mfma_f32_16x16x32_bf16 v[70:73], a[80:83], v[82:85], v[70:73]
s_waitcnt lgkmcnt(2)
v_mfma_f32_16x16x32_bf16 v[62:65], a[84:87], v[86:89], v[62:65]
ds_read_b128 v[82:85], v17 offset:34816
v_mfma_f32_16x16x32_bf16 v[66:69], a[88:91], v[86:89], v[66:69]
v_mfma_f32_16x16x32_bf16 v[70:73], a[92:95], v[86:89], v[70:73]
s_waitcnt lgkmcnt(2)
v_mfma_f32_16x16x32_bf16 v[62:65], a[96:99], v[74:77], v[62:65]
ds_read_b128 v[86:89], v17 offset:35840
v_mfma_f32_16x16x32_bf16 v[66:69], a[100:103], v[74:77], v[66:69]
v_mfma_f32_16x16x32_bf16 v[70:73], a[104:107], v[74:77], v[70:73]
s_waitcnt lgkmcnt(2)
v_mfma_f32_16x16x32_bf16 v[62:65], a[108:111], v[78:81], v[62:65]
ds_read_b128 v[74:77], v17 offset:36864
v_mfma_f32_16x16x32_bf16 v[66:69], a[112:115], v[78:81], v[66:69]
v_mfma_f32_16x16x32_bf16 v[70:73], a[116:119], v[78:81], v[70:73]
s_waitcnt lgkmcnt(2)
v_mfma_f32_16x16x32_bf16 v[62:65], a[120:123], v[82:85], v[62:65]
ds_read_b128 v[78:81], v17 offset:37888
v_mfma_f32_16x16x32_bf16 v[66:69], a[124:127], v[82:85], v[66:69]
v_mfma_f32_16x16x32_bf16 v[70:73], a[128:131], v[82:85], v[70:73]
s_waitcnt lgkmcnt(2)
v_mfma_f32_16x16x32_bf16 v[62:65], a[132:135], v[86:89], v[62:65]
ds_read_b128 v[82:85], v17 offset:38912
v_mfma_f32_16x16x32_bf16 v[66:69], a[136:139], v[86:89], v[66:69]
v_mfma_f32_16x16x32_bf16 v[70:73], a[140:143], v[86:89], v[70:73]
s_waitcnt lgkmcnt(2)
v_mfma_f32_16x16x32_bf16 v[62:65], a[144:147], v[74:77], v[62:65]
ds_read_b128 v[86:89], v17 offset:39936
v_mfma_f32_16x16x32_bf16 v[66:69], a[148:151], v[74:77], v[66:69]
v_mfma_f32_16x16x32_bf16 v[70:73], a[152:155], v[74:77], v[70:73]
s_waitcnt lgkmcnt(2)
v_mfma_f32_16x16x32_bf16 v[62:65], a[156:159], v[78:81], v[62:65]
ds_read_b128 v[74:77], v17 offset:40960
v_mfma_f32_16x16x32_bf16 v[66:69], a[160:163], v[78:81], v[66:69]
v_mfma_f32_16x16x32_bf16 v[70:73], a[164:167], v[78:81], v[70:73]
s_add_u32 m0, s43, 24576
s_waitcnt lgkmcnt(2)
v_mfma_f32_16x16x32_bf16 v[62:65], a[168:171], v[82:85], v[62:65]
ds_read_b128 v[78:81], v17 offset:41984
v_mfma_f32_16x16x32_bf16 v[66:69], a[172:175], v[82:85], v[66:69]
v_mfma_f32_16x16x32_bf16 v[70:73], a[176:179], v[82:85], v[70:73]
s_barrier
s_waitcnt lgkmcnt(2)
v_mfma_f32_16x16x32_bf16 v[62:65], a[180:183], v[86:89], v[62:65]
ds_read_b128 v[82:85], v17 offset:43008
v_mfma_f32_16x16x32_bf16 v[66:69], a[184:187], v[86:89], v[66:69]
global_load_lds_dwordx4 v9, s[38:39] offset:-2048 sc1
v_mfma_f32_16x16x32_bf16 v[70:73], a[188:191], v[86:89], v[70:73]
s_waitcnt lgkmcnt(2)
v_mfma_f32_16x16x32_bf16 v[62:65], a[192:195], v[74:77], v[62:65]
ds_read_b128 v[86:89], v17 offset:44032
v_mfma_f32_16x16x32_bf16 v[66:69], a[196:199], v[74:77], v[66:69]
global_load_lds_dwordx4 v9, s[38:39] offset:-1024 sc1
v_mfma_f32_16x16x32_bf16 v[70:73], a[200:203], v[74:77], v[70:73]
s_waitcnt lgkmcnt(2)
v_mfma_f32_16x16x32_bf16 v[62:65], a[204:207], v[78:81], v[62:65]
ds_read_b128 v[74:77], v17 offset:45056
v_mfma_f32_16x16x32_bf16 v[66:69], a[208:211], v[78:81], v[66:69]
global_load_lds_dwordx4 v9, s[38:39] offset:0 sc1
v_mfma_f32_16x16x32_bf16 v[70:73], a[212:215], v[78:81], v[70:73]
s_waitcnt lgkmcnt(2)
v_mfma_f32_16x16x32_bf16 v[62:65], a[216:219], v[82:85], v[62:65]
ds_read_b128 v[78:81], v17 offset:46080
v_mfma_f32_16x16x32_bf16 v[66:69], a[220:223], v[82:85], v[66:69]
global_load_lds_dwordx4 v9, s[38:39] offset:1024 sc1
v_mfma_f32_16x16x32_bf16 v[70:73], a[224:227], v[82:85], v[70:73]
s_waitcnt lgkmcnt(2)
v_mfma_f32_16x16x32_bf16 v[62:65], a[228:231], v[86:89], v[62:65]
ds_read_b128 v[82:85], v17 offset:47104
v_mfma_f32_16x16x32_bf16 v[66:69], a[232:235], v[86:89], v[66:69]
global_load_lds_dwordx4 v9, s[38:39] offset:2048 sc1
v_mfma_f32_16x16x32_bf16 v[70:73], a[236:239], v[86:89], v[70:73]
s_waitcnt lgkmcnt(2)
v_mfma_f32_16x16x32_bf16 v[62:65], a[240:243], v[74:77], v[62:65]
ds_read_b128 v[86:89], v17 offset:48128
v_xor_b32 v17, 0x10000, v17
v_mfma_f32_16x16x32_bf16 v[66:69], a[244:247], v[74:77], v[66:69]
v_mfma_f32_16x16x32_bf16 v[70:73], a[248:251], v[74:77], v[70:73]
s_waitcnt vmcnt(7)
s_barrier
s_waitcnt lgkmcnt(2)
v_mfma_f32_16x16x32_bf16 v[62:65], a[252:255], v[78:81], v[62:65]
ds_read_b128 v[74:77], v17 offset:0
global_load_lds_dwordx4 v9, s[38:39] offset:3072 sc1
v_mfma_f32_16x16x32_bf16 v[66:69], v[18:21], v[78:81], v[66:69]
v_add_u32 v9, 147456, v9
v_min_u32 v9, v9, v10
v_mfma_f32_16x16x32_bf16 v[70:73], v[22:25], v[78:81], v[70:73]
s_waitcnt lgkmcnt(2)
v_mfma_f32_16x16x32_bf16 v[62:65], v[26:29], v[82:85], v[62:65]
ds_read_b128 v[78:81], v17 offset:1024
v_mfma_f32_16x16x32_bf16 v[66:69], v[30:33], v[82:85], v[66:69]
v_mfma_f32_16x16x32_bf16 v[70:73], v[34:37], v[82:85], v[70:73]
s_waitcnt lgkmcnt(2)
v_mfma_f32_16x16x32_bf16 v[62:65], v[38:41], v[86:89], v[62:65]
ds_read_b128 v[82:85], v17 offset:2048
v_mfma_f32_16x16x32_bf16 v[66:69], v[42:45], v[86:89], v[66:69]
v_mfma_f32_16x16x32_bf16 v[70:73], v[46:49], v[86:89], v[70:73]
s_sub_u32 s70, s70, 1
s_cbranch_scc0 .Ldn_top6
s_nop 2
v_cvt_pk_bf16_f32 v90, v62, v63
v_cvt_pk_bf16_f32 v91, v64, v65
v_cvt_pk_bf16_f32 v92, v66, v67
v_cvt_pk_bf16_f32 v93, v68, v69
global_store_dwordx4 v11, v[90:93], s[38:39] sc1
v_add_u32 v11, 110592, v11
v_cvt_pk_bf16_f32 v94, v70, v71
v_cvt_pk_bf16_f32 v96, v70, v71
v_cvt_pk_bf16_f32 v95, v72, v73
v_cvt_pk_bf16_f32 v97, v72, v73
v_permlane16_swap_b32 v94, v96
s_nop 0
v_permlane16_swap_b32 v95, v97
s_mov_b64 exec, s[76:77]
global_store_dwordx4 v12, v[94:97], s[38:39] sc1
s_mov_b64 exec, -1
v_add_u32 v12, 110592, v12
s_waitcnt vmcnt(2)
s_barrier
s_waitcnt vmcnt(0)
s_barrier
s_endpgm
.Ldn_w3:
v_add_u32 v254, -1, v14
s_mov_b32 s76, 0xffff
s_mov_b32 s77, 0xffff
s_mov_b32 s70, 32
s_waitcnt vmcnt(0) lgkmcnt(0)
s_nop 7
.p2align 6
.Ldn_top7:
s_waitcnt lgkmcnt(2)
v_mfma_f32_16x16x32_bf16 v[50:53], a[0:3], v[74:77], 0
ds_read_b128 v[86:89], v17 offset:3072
v_mfma_f32_16x16x32_bf16 v[54:57], a[4:7], v[74:77], 0
v_mfma_f32_16x16x32_bf16 v[58:61], a[8:11], v[74:77], 0
s_waitcnt lgkmcnt(2)
v_cvt_pk_bf16_f32 v90, v62, v63
v_cvt_pk_bf16_f32 v91, v64, v65
v_mfma_f32_16x16x32_bf16 v[50:53], a[12:15], v[78:81], v[50:53]
ds_read_b128 v[74:77], v17 offset:4096
v_cvt_pk_bf16_f32 v92, v66, v67
v_mfma_f32_16x16x32_bf16 v[54:57], a[16:19], v[78:81], v[54:57]
v_mfma_f32_16x16x32_bf16 v[58:61], a[20:23], v[78:81], v[58:61]
s_waitcnt vmcnt(7)
s_barrier
s_waitcnt lgkmcnt(2)
v_mfma_f32_16x16x32_bf16 v[50:53], a[24:27], v[82:85], v[50:53]
ds_read_b128 v[78:81], v17 offset:5120
v_cvt_pk_bf16_f32 v93, v68, v69
v_mfma_f32_16x16x32_bf16 v[54:57], a[28:31], v[82:85], v[54:57]
s_mov_b64 exec, 1
global_store_dword v15, v16, s[38:39] sc1
s_mov_b64 exec, -1
v_mfma_f32_16x16x32_bf16 v[58:61], a[32:35], v[82:85], v[58:61]
s_waitcnt lgkmcnt(2)
v_add_u32 v16, 1, v16
v_cvt_pk_bf16_f32 v94, v70, v71
v_mfma_f32_16x16x32_bf16 v[50:53], a[36:39], v[86:89], v[50:53]
ds_read_b128 v[82:85], v17 offset:6144
v_cvt_pk_bf16_f32 v96, v70, v71
v_mfma_f32_16x16x32_bf16 v[54:57], a[40:43], v[86:89], v[54:57]
global_store_dwordx4 v11, v[90:93], s[38:39] sc1
v_mfma_f32_16x16x32_bf16 v[58:61], a[44:47], v[86:89], v[58:61]
s_waitcnt lgkmcnt(2)
v_add_u32 v11, 110592, v11
v_cvt_pk_bf16_f32 v95, v72, v73
v_mfma_f32_16x16x32_bf16 v[50:53], a[48:51], v[74:77], v[50:53]
ds_read_b128 v[86:89], v17 offset:7168
v_cvt_pk_bf16_f32 v97, v72, v73
v_mfma_f32_16x16x32_bf16 v[54:57], a[52:55], v[74:77], v[54:57]
v_permlane16_swap_b32 v94, v96
v_mfma_f32_16x16x32_bf16 v[58:61], a[56:59], v[74:77], v[58:61]
s_waitcnt lgkmcnt(2)
v_permlane16_swap_b32 v95, v97
v_mfma_f32_16x16x32_bf16 v[50:53], a[60:63], v[78:81], v[50:53]
ds_read_b128 v[74:77], v17 offset:8192
v_mfma_f32_16x16x32_bf16 v[54:57], a[64:67], v[78:81], v[54:57]
s_mov_b64 exec, s[76:77]
global_store_dwordx4 v12, v[94:97], s[38:39] sc1
s_mov_b64 exec, -1
v_mfma_f32_16x16x32_bf16 v[58:61], a[68:71], v[78:81], v[58:61]
s_waitcnt lgkmcnt(2)
v_add_u32 v12, 110592, v12
v_mfma_f32_16x16x32_bf16 v[50:53], a[72:75], v[82:85], v[50:53]
ds_read_b128 v[78:81], v17 offset:9216
v_mfma_f32_16x16x32_bf16 v[54:57], a[76:79], v[82:85], v[54:57]
v_mfma_f32_16x16x32_bf16 v[58:61], a[80:83], v[82:85], v[58:61]
s_waitcnt lgkmcnt(2)
v_mfma_f32_16x16x32_bf16 v[50:53], a[84:87], v[86:89], v[50:53]
ds_read_b128 v[82:85], v17 offset:10240
v_mfma_f32_16x16x32_bf16 v[54:57], a[88:91], v[86:89], v[54:57]
v_mfma_f32_16x16x32_bf16 v[58:61], a[92:95], v[86:89], v[58:61]
s_waitcnt lgkmcnt(2)
v_mfma_f32_16x16x32_bf16 v[50:53], a[96:99], v[74:77], v[50:53]
ds_read_b128 v[86:89], v17 offset:11264
v_mfma_f32_16x16x32_bf16 v[54:57], a[100:103], v[74:77], v[54:57]
v_mfma_f32_16x16x32_bf16 v[58:61], a[104:107], v[74:77], v[58:61]
s_waitcnt lgkmcnt(2)
v_mfma_f32_16x16x32_bf16 v[50:53], a[108:111], v[78:81], v[50:53]
ds_read_b128 v[74:77], v17 offset:12288
v_mfma_f32_16x16x32_bf16 v[54:57], a[112:115], v[78:81], v[54:57]
v_mfma_f32_16x16x32_bf16 v[58:61], a[116:119], v[78:81], v[58:61]
s_waitcnt lgkmcnt(2)
v_mfma_f32_16x16x32_bf16 v[50:53], a[120:123], v[82:85], v[50:53]
ds_read_b128 v[78:81], v17 offset:13312
v_mfma_f32_16x16x32_bf16 v[54:57], a[124:127], v[82:85], v[54:57]
v_mfma_f32_16x16x32_bf16 v[58:61], a[128:131], v[82:85], v[58:61]
s_waitcnt lgkmcnt(2)
v_mfma_f32_16x16x32_bf16 v[50:53], a[132:135], v[86:89], v[50:53]
ds_read_b128 v[82:85], v17 offset:14336
v_mfma_f32_16x16x32_bf16 v[54:57], a[136:139], v[86:89], v[54:57]
v_mfma_f32_16x16x32_bf16 v[58:61], a[140:143], v[86:89], v[58:61]
s_waitcnt lgkmcnt(2)
v_mfma_f32_16x16x32_bf16 v[50:53], a[144:147], v[74:77], v[50:53]
ds_read_b128 v[86:89], v17 offset:15360
v_mfma_f32_16x16x32_bf16 v[54:57], a[148:151], v[74:77], v[54:57]
v_mfma_f32_16x16x32_bf16 v[58:61], a[152:155], v[74:77], v[58:61]
s_waitcnt lgkmcnt(2)
v_mfma_f32_16x16x32_bf16 v[50:53], a[156:159], v[78:81], v[50:53]
ds_read_b128 v[74:77], v17 offset:16384
v_mfma_f32_16x16x32_bf16 v[54:57], a[160:163], v[78:81], v[54:57]
s_xor_b32 s43, s43, 0x10000
v_mfma_f32_16x16x32_bf16 v[58:61], a[164:167], v[78:81], v[58:61]
s_add_u32 m0, s43, 0
s_waitcnt lgkmcnt(2)
v_mfma_f32_16x16x32_bf16 v[50:53], a[168:171], v[82:85], v[50:53]
ds_read_b128 v[78:81], v17 offset:17408
v_mfma_f32_16x16x32_bf16 v[54:57], a[172:175], v[82:85], v[54:57]
v_mfma_f32_16x16x32_bf16 v[58:61], a[176:179], v[82:85], v[58:61]
s_barrier
s_waitcnt lgkmcnt(2)
v_mfma_f32_16x16x32_bf16 v[50:53], a[180:183], v[86:89], v[50:53]
ds_read_b128 v[82:85], v17 offset:18432
v_mfma_f32_16x16x32_bf16 v[54:57], a[184:187], v[86:89], v[54:57]
v_mfma_f32_16x16x32_bf16 v[58:61], a[188:191], v[86:89], v[58:61]
s_waitcnt lgkmcnt(2)
global_load_lds_dwordx4 v9, s[38:39] offset:-2048 sc1
v_mfma_f32_16x16x32_bf16 v[50:53], a[192:195], v[74:77], v[50:53]
ds_read_b128 v[86:89], v17 offset:19456
v_mfma_f32_16x16x32_bf16 v[54:57], a[196:199], v[74:77], v[54:57]
v_mfma_f32_16x16x32_bf16 v[58:61], a[200:203], v[74:77], v[58:61]
s_waitcnt lgkmcnt(2)
global_load_lds_dwordx4 v9, s[38:39] offset:-1024 sc1
v_mfma_f32_16x16x32_bf16 v[50:53], a[204:207], v[78:81], v[50:53]
ds_read_b128 v[74:77], v17 offset:20480
v_mfma_f32_16x16x32_bf16 v[54:57], a[208:211], v[78:81], v[54:57]
v_mfma_f32_16x16x32_bf16 v[58:61], a[212:215], v[78:81], v[58:61]
s_waitcnt lgkmcnt(2)
global_load_lds_dwordx4 v9, s[38:39] offset:0 sc1
v_mfma_f32_16x16x32_bf16 v[50:53], a[216:219], v[82:85], v[50:53]
ds_read_b128 v[78:81], v17 offset:21504
v_mfma_f32_16x16x32_bf16 v[54:57], a[220:223], v[82:85], v[54:57]
v_mfma_f32_16x16x32_bf16 v[58:61], a[224:227], v[82:85], v[58:61]
s_waitcnt lgkmcnt(2)
global_load_lds_dwordx4 v9, s[38:39] offset:1024 sc1
v_mfma_f32_16x16x32_bf16 v[50:53], a[228:231], v[86:89], v[50:53]
ds_read_b128 v[82:85], v17 offset:22528
v_mfma_f32_16x16x32_bf16 v[54:57], a[232:235], v[86:89], v[54:57]
v_mfma_f32_16x16x32_bf16 v[58:61], a[236:239], v[86:89], v[58:61]
s_waitcnt lgkmcnt(2)
global_load_lds_dwordx4 v9, s[38:39] offset:2048 sc1
v_mfma_f32_16x16x32_bf16 v[50:53], a[240:243], v[74:77], v[50:53]
ds_read_b128 v[86:89], v17 offset:23552
v_mfma_f32_16x16x32_bf16 v[54:57], a[244:247], v[74:77], v[54:57]
v_mfma_f32_16x16x32_bf16 v[58:61], a[248:251], v[74:77], v[58:61]
s_waitcnt vmcnt(8)
s_barrier
s_waitcnt lgkmcnt(2)
v_mfma_f32_16x16x32_bf16 v[50:53], a[252:255], v[78:81], v[50:53]
ds_read_b128 v[74:77], v17 offset:24576
global_load_lds_dwordx4 v9, s[38:39] offset:3072 sc1
v_mfma_f32_16x16x32_bf16 v[54:57], v[18:21], v[78:81], v[54:57]
v_add_u32 v9, 147456, v9
v_min_u32 v9, v9, v10
v_mfma_f32_16x16x32_bf16 v[58:61], v[22:25], v[78:81], v[58:61]
s_waitcnt lgkmcnt(2)
v_mfma_f32_16x16x32_bf16 v[50:53], v[26:29], v[82:85], v[50:53]
ds_read_b128 v[78:81], v17 offset:25600
v_mfma_f32_16x16x32_bf16 v[54:57], v[30:33], v[82:85], v[54:57]
v_mfma_f32_16x16x32_bf16 v[58:61], v[34:37], v[82:85], v[58:61]
s_waitcnt lgkmcnt(2)
v_mfma_f32_16x16x32_bf16 v[50:53], v[38:41], v[86:89], v[50:53]
ds_read_b128 v[82:85], v17 offset:26624
v_mfma_f32_16x16x32_bf16 v[54:57], v[42:45], v[86:89], v[54:57]
v_mfma_f32_16x16x32_bf16 v[58:61], v[46:49], v[86:89], v[58:61]
s_waitcnt lgkmcnt(2)
v_mfma_f32_16x16x32_bf16 v[62:65], a[0:3], v[74:77], 0
ds_read_b128 v[86:89], v17 offset:27648
v_mfma_f32_16x16x32_bf16 v[66:69], a[4:7], v[74:77], 0
v_mfma_f32_16x16x32_bf16 v[70:73], a[8:11], v[74:77], 0
s_waitcnt lgkmcnt(2)
v_cvt_pk_bf16_f32 v90, v50, v51
v_cvt_pk_bf16_f32 v91, v52, v53
v_mfma_f32_16x16x32_bf16 v[62:65], a[12:15], v[78:81], v[62:65]
ds_read_b128 v[74:77], v17 offset:28672
v_cvt_pk_bf16_f32 v92, v54, v55
v_mfma_f32_16x16x32_bf16 v[66:69], a[16:19], v[78:81], v[66:69]
v_mfma_f32_16x16x32_bf16 v[70:73], a[20:23], v[78:81], v[70:73]
s_waitcnt vmcnt(6)
s_barrier
s_waitcnt lgkmcnt(2)
v_mfma_f32_16x16x32_bf16 v[62:65], a[24:27], v[82:85], v[62:65]
ds_read_b128 v[78:81], v17 offset:29696
v_cvt_pk_bf16_f32 v93, v56, v57
v_mfma_f32_16x16x32_bf16 v[66:69], a[28:31], v[82:85], v[66:69]
v_cvt_pk_bf16_f32 v94, v58, v59
v_cvt_pk_bf16_f32 v96, v58, v59
v_mfma_f32_16x16x32_bf16 v[70:73], a[32:35], v[82:85], v[70:73]
s_waitcnt lgkmcnt(2)
v_cvt_pk_bf16_f32 v95, v60, v61
v_cvt_pk_bf16_f32 v97, v60, v61
v_mfma_f32_16x16x32_bf16 v[62:65], a[36:39], v[86:89], v[62:65]
ds_read_b128 v[82:85], v17 offset:30720
v_mfma_f32_16x16x32_bf16 v[66:69], a[40:43], v[86:89], v[66:69]
global_store_dwordx4 v11, v[90:93], s[38:39] sc1
v_mfma_f32_16x16x32_bf16 v[70:73], a[44:47], v[86:89], v[70:73]
s_waitcnt lgkmcnt(2)
v_add_u32 v11, 110592, v11
v_mfma_f32_16x16x32_bf16 v[62:65], a[48:51], v[74:77], v[62:65]
ds_read_b128 v[86:89], v17 offset:31744
v_mfma_f32_16x16x32_bf16 v[66:69], a[52:55], v[74:77], v[66:69]
v_permlane16_swap_b32 v94, v96
v_mfma_f32_16x16x32_bf16 v[70:73], a[56:59], v[74:77], v[70:73]
s_waitcnt lgkmcnt(2)
v_permlane16_swap_b32 v95, v97
v_mfma_f32_16x16x32_bf16 v[62:65], a[60:63], v[78:81], v[62:65]
ds_read_b128 v[74:77], v17 offset:32768
v_mfma_f32_16x16x32_bf16 v[66:69], a[64:67], v[78:81], v[66:69]
s_mov_b64 exec, s[76:77]
global_store_dwordx4 v12, v[94:97], s[38:39] sc1
s_mov_b64 exec, -1
v_mfma_f32_16x16x32_bf16 v[70:73], a[68:71], v[78:81], v[70:73]
s_waitcnt lgkmcnt(2)
v_add_u32 v12, 110592, v12
v_mfma_f32_16x16x32_bf16 v[62:65], a[72:75], v[82:85], v[62:65]
ds_read_b128 v[78:81], v17 offset:33792
)"
R"(v_mfma_f32_16x16x32_bf16 v[66:69], a[76:79], v[82:85], v[66:69]
s_mov_b64 exec, 1
global_store_dword v15, v16, s[38:39] sc1
s_mov_b64 exec, -1
v_mfma_f32_16x16x32_bf16 v[70:73], a[80:83], v[82:85], v[70:73]
s_waitcnt lgkmcnt(2)
v_add_u32 v16, 1, v16
v_mfma_f32_16x16x32_bf16 v[62:65], a[84:87], v[86:89], v[62:65]
ds_read_b128 v[82:85], v17 offset:34816
v_mfma_f32_16x16x32_bf16 v[66:69], a[88:91], v[86:89], v[66:69]
v_mfma_f32_16x16x32_bf16 v[70:73], a[92:95], v[86:89], v[70:73]
s_waitcnt lgkmcnt(2)
v_mfma_f32_16x16x32_bf16 v[62:65], a[96:99], v[74:77], v[62:65]
ds_read_b128 v[86:89], v17 offset:35840
v_mfma_f32_16x16x32_bf16 v[66:69], a[100:103], v[74:77], v[66:69]
v_mfma_f32_16x16x32_bf16 v[70:73], a[104:107], v[74:77], v[70:73]
s_waitcnt lgkmcnt(2)
v_mfma_f32_16x16x32_bf16 v[62:65], a[108:111], v[78:81], v[62:65]
ds_read_b128 v[74:77], v17 offset:36864
v_mfma_f32_16x16x32_bf16 v[66:69], a[112:115], v[78:81], v[66:69]
v_mfma_f32_16x16x32_bf16 v[70:73], a[116:119], v[78:81], v[70:73]
s_waitcnt lgkmcnt(2)
v_mfma_f32_16x16x32_bf16 v[62:65], a[120:123], v[82:85], v[62:65]
ds_read_b128 v[78:81], v17 offset:37888
v_mfma_f32_16x16x32_bf16 v[66:69], a[124:127], v[82:85], v[66:69]
v_mfma_f32_16x16x32_bf16 v[70:73], a[128:131], v[82:85], v[70:73]
s_waitcnt lgkmcnt(2)
v_mfma_f32_16x16x32_bf16 v[62:65], a[132:135], v[86:89], v[62:65]
ds_read_b128 v[82:85], v17 offset:38912
v_mfma_f32_16x16x32_bf16 v[66:69], a[136:139], v[86:89], v[66:69]
v_mfma_f32_16x16x32_bf16 v[70:73], a[140:143], v[86:89], v[70:73]
s_waitcnt lgkmcnt(2)
v_mfma_f32_16x16x32_bf16 v[62:65], a[144:147], v[74:77], v[62:65]
ds_read_b128 v[86:89], v17 offset:39936
v_mfma_f32_16x16x32_bf16 v[66:69], a[148:151], v[74:77], v[66:69]
v_mfma_f32_16x16x32_bf16 v[70:73], a[152:155], v[74:77], v[70:73]
s_waitcnt lgkmcnt(2)
v_mfma_f32_16x16x32_bf16 v[62:65], a[156:159], v[78:81], v[62:65]
ds_read_b128 v[74:77], v17 offset:40960
v_mfma_f32_16x16x32_bf16 v[66:69], a[160:163], v[78:81], v[66:69]
v_mfma_f32_16x16x32_bf16 v[70:73], a[164:167], v[78:81], v[70:73]
s_add_u32 m0, s43, 24576
s_waitcnt lgkmcnt(2)
v_mfma_f32_16x16x32_bf16 v[62:65], a[168:171], v[82:85], v[62:65]
ds_read_b128 v[78:81], v17 offset:41984
v_mfma_f32_16x16x32_bf16 v[66:69], a[172:175], v[82:85], v[66:69]
v_mfma_f32_16x16x32_bf16 v[70:73], a[176:179], v[82:85], v[70:73]
s_barrier
s_waitcnt lgkmcnt(2)
v_mfma_f32_16x16x32_bf16 v[62:65], a[180:183], v[86:89], v[62:65]
ds_read_b128 v[82:85], v17 offset:43008
v_mfma_f32_16x16x32_bf16 v[66:69], a[184:187], v[86:89], v[66:69]
v_mfma_f32_16x16x32_bf16 v[70:73], a[188:191], v[86:89], v[70:73]
s_waitcnt lgkmcnt(2)
global_load_lds_dwordx4 v9, s[38:39] offset:-2048 sc1
v_mfma_f32_16x16x32_bf16 v[62:65], a[192:195], v[74:77], v[62:65]
ds_read_b128 v[86:89], v17 offset:44032
v_mfma_f32_16x16x32_bf16 v[66:69], a[196:199], v[74:77], v[66:69]
v_mfma_f32_16x16x32_bf16 v[70:73], a[200:203], v[74:77], v[70:73]
s_waitcnt lgkmcnt(2)
global_load_lds_dwordx4 v9, s[38:39] offset:-1024 sc1
v_mfma_f32_16x16x32_bf16 v[62:65], a[204:207], v[78:81], v[62:65]
ds_read_b128 v[74:77], v17 offset:45056
v_mfma_f32_16x16x32_bf16 v[66:69], a[208:211], v[78:81], v[66:69]
v_mfma_f32_16x16x32_bf16 v[70:73], a[212:215], v[78:81], v[70:73]
s_waitcnt lgkmcnt(2)
global_load_lds_dwordx4 v9, s[38:39] offset:0 sc1
v_mfma_f32_16x16x32_bf16 v[62:65], a[216:219], v[82:85], v[62:65]
ds_read_b128 v[78:81], v17 offset:46080
v_mfma_f32_16x16x32_bf16 v[66:69], a[220:223], v[82:85], v[66:69]
v_mfma_f32_16x16x32_bf16 v[70:73], a[224:227], v[82:85], v[70:73]
s_waitcnt lgkmcnt(2)
global_load_lds_dwordx4 v9, s[38:39] offset:1024 sc1
v_mfma_f32_16x16x32_bf16 v[62:65], a[228:231], v[86:89], v[62:65]
ds_read_b128 v[82:85], v17 offset:47104
v_mfma_f32_16x16x32_bf16 v[66:69], a[232:235], v[86:89], v[66:69]
v_mfma_f32_16x16x32_bf16 v[70:73], a[236:239], v[86:89], v[70:73]
s_waitcnt lgkmcnt(2)
global_load_lds_dwordx4 v9, s[38:39] offset:2048 sc1
v_mfma_f32_16x16x32_bf16 v[62:65], a[240:243], v[74:77], v[62:65]
ds_read_b128 v[86:89], v17 offset:48128
v_xor_b32 v17, 0x10000, v17
v_mfma_f32_16x16x32_bf16 v[66:69], a[244:247], v[74:77], v[66:69]
v_mfma_f32_16x16x32_bf16 v[70:73], a[248:251], v[74:77], v[70:73]
s_waitcnt vmcnt(8)
s_barrier
s_waitcnt lgkmcnt(2)
v_mfma_f32_16x16x32_bf16 v[62:65], a[252:255], v[78:81], v[62:65]
ds_read_b128 v[74:77], v17 offset:0
global_load_lds_dwordx4 v9, s[38:39] offset:3072 sc1
v_mfma_f32_16x16x32_bf16 v[66:69], v[18:21], v[78:81], v[66:69]
v_add_u32 v9, 147456, v9
v_min_u32 v9, v9, v10
v_mfma_f32_16x16x32_bf16 v[70:73], v[22:25], v[78:81], v[70:73]
s_waitcnt lgkmcnt(2)
v_mfma_f32_16x16x32_bf16 v[62:65], v[26:29], v[82:85], v[62:65]
ds_read_b128 v[78:81], v17 offset:1024
v_mfma_f32_16x16x32_bf16 v[66:69], v[30:33], v[82:85], v[66:69]
v_mfma_f32_16x16x32_bf16 v[70:73], v[34:37], v[82:85], v[70:73]
s_waitcnt lgkmcnt(2)
v_mfma_f32_16x16x32_bf16 v[62:65], v[38:41], v[86:89], v[62:65]
ds_read_b128 v[82:85], v17 offset:2048
v_mfma_f32_16x16x32_bf16 v[66:69], v[42:45], v[86:89], v[66:69]
v_mfma_f32_16x16x32_bf16 v[70:73], v[46:49], v[86:89], v[70:73]
s_sub_u32 s70, s70, 1
s_cbranch_scc0 .Ldn_top7
s_nop 2
v_cvt_pk_bf16_f32 v90, v62, v63
v_cvt_pk_bf16_f32 v91, v64, v65
v_cvt_pk_bf16_f32 v92, v66, v67
v_cvt_pk_bf16_f32 v93, v68, v69
global_store_dwordx4 v11, v[90:93], s[38:39] sc1
v_add_u32 v11, 110592, v11
v_cvt_pk_bf16_f32 v94, v70, v71
v_cvt_pk_bf16_f32 v96, v70, v71
v_cvt_pk_bf16_f32 v95, v72, v73
v_cvt_pk_bf16_f32 v97, v72, v73
v_permlane16_swap_b32 v94, v96
s_nop 0
v_permlane16_swap_b32 v95, v97
s_mov_b64 exec, s[76:77]
global_store_dwordx4 v12, v[94:97], s[38:39] sc1
s_mov_b64 exec, -1
v_add_u32 v12, 110592, v12
s_waitcnt vmcnt(2)
s_barrier
s_mov_b64 exec, 1
global_store_dword v15, v16, s[38:39] sc1
s_mov_b64 exec, -1
v_add_u32 v16, 1, v16
s_waitcnt vmcnt(1)
s_barrier
s_mov_b64 exec, 1
global_store_dword v15, v16, s[38:39] sc1
s_mov_b64 exec, -1
s_endpgm
.Ldn_w4:
v_add_u32 v254, -1, v14
s_mov_b32 s76, 0xffff
s_mov_b32 s77, 0xffff
s_mov_b32 s70, 32
s_waitcnt vmcnt(0) lgkmcnt(0)
s_nop 7
.p2align 6
.Ldn_top12:
s_waitcnt lgkmcnt(2)
v_mfma_f32_16x16x32_bf16 v[50:53], a[0:3], v[74:77], 0
ds_read_b128 v[86:89], v17 offset:3072
v_mfma_f32_16x16x32_bf16 v[54:57], a[4:7], v[74:77], 0
global_load_dword v254, v13, s[38:39] sc1
v_mfma_f32_16x16x32_bf16 v[58:61], a[8:11], v[74:77], 0
s_waitcnt lgkmcnt(2)
v_cvt_pk_bf16_f32 v90, v62, v63
v_cvt_pk_bf16_f32 v91, v64, v65
v_mfma_f32_16x16x32_bf16 v[50:53], a[12:15], v[78:81], v[50:53]
ds_read_b128 v[74:77], v17 offset:4096
v_cvt_pk_bf16_f32 v92, v66, v67
v_mfma_f32_16x16x32_bf16 v[54:57], a[16:19], v[78:81], v[54:57]
v_mfma_f32_16x16x32_bf16 v[58:61], a[20:23], v[78:81], v[58:61]
s_waitcnt vmcnt(7)
s_barrier
s_waitcnt lgkmcnt(2)
v_mfma_f32_16x16x32_bf16 v[50:53], a[24:27], v[82:85], v[50:53]
ds_read_b128 v[78:81], v17 offset:5120
v_cvt_pk_bf16_f32 v93, v68, v69
v_mfma_f32_16x16x32_bf16 v[54:57], a[28:31], v[82:85], v[54:57]
v_cvt_pk_bf16_f32 v94, v70, v71
v_cvt_pk_bf16_f32 v96, v70, v71
v_mfma_f32_16x16x32_bf16 v[58:61], a[32:35], v[82:85], v[58:61]
s_waitcnt lgkmcnt(2)
v_cvt_pk_bf16_f32 v95, v72, v73
v_cvt_pk_bf16_f32 v97, v72, v73
v_mfma_f32_16x16x32_bf16 v[50:53], a[36:39], v[86:89], v[50:53]
ds_read_b128 v[82:85], v17 offset:6144
v_mfma_f32_16x16x32_bf16 v[54:57], a[40:43], v[86:89], v[54:57]
global_store_dwordx4 v11, v[90:93], s[38:39] sc1
v_mfma_f32_16x16x32_bf16 v[58:61], a[44:47], v[86:89], v[58:61]
s_waitcnt lgkmcnt(2)
v_add_u32 v11, 110592, v11
v_mfma_f32_16x16x32_bf16 v[50:53], a[48:51], v[74:77], v[50:53]
ds_read_b128 v[86:89], v17 offset:7168
v_mfma_f32_16x16x32_bf16 v[54:57], a[52:55], v[74:77], v[54:57]
v_permlane16_swap_b32 v94, v96
v_mfma_f32_16x16x32_bf16 v[58:61], a[56:59], v[74:77], v[58:61]
s_waitcnt lgkmcnt(2)
v_permlane16_swap_b32 v95, v97
v_mfma_f32_16x16x32_bf16 v[50:53], a[60:63], v[78:81], v[50:53]
ds_read_b128 v[74:77], v17 offset:8192
v_mfma_f32_16x16x32_bf16 v[54:57], a[64:67], v[78:81], v[54:57]
s_mov_b64 exec, s[76:77]
global_store_dwordx4 v12, v[94:97], s[38:39] sc1
s_mov_b64 exec, -1
v_mfma_f32_16x16x32_bf16 v[58:61], a[68:71], v[78:81], v[58:61]
s_waitcnt lgkmcnt(2)
v_add_u32 v12, 110592, v12
v_mfma_f32_16x16x32_bf16 v[50:53], a[72:75], v[82:85], v[50:53]
ds_read_b128 v[78:81], v17 offset:9216
v_mfma_f32_16x16x32_bf16 v[54:57], a[76:79], v[82:85], v[54:57]
v_mfma_f32_16x16x32_bf16 v[58:61], a[80:83], v[82:85], v[58:61]
s_waitcnt lgkmcnt(2)
v_mfma_f32_16x16x32_bf16 v[50:53], a[84:87], v[86:89], v[50:53]
ds_read_b128 v[82:85], v17 offset:10240
v_mfma_f32_16x16x32_bf16 v[54:57], a[88:91], v[86:89], v[54:57]
v_mfma_f32_16x16x32_bf16 v[58:61], a[92:95], v[86:89], v[58:61]
s_waitcnt lgkmcnt(2)
v_mfma_f32_16x16x32_bf16 v[50:53], a[96:99], v[74:77], v[50:53]
ds_read_b128 v[86:89], v17 offset:11264
v_mfma_f32_16x16x32_bf16 v[54:57], a[100:103], v[74:77], v[54:57]
v_mfma_f32_16x16x32_bf16 v[58:61], a[104:107], v[74:77], v[58:61]
s_waitcnt lgkmcnt(2)
v_mfma_f32_16x16x32_bf16 v[50:53], a[108:111], v[78:81], v[50:53]
ds_read_b128 v[74:77], v17 offset:12288
v_mfma_f32_16x16x32_bf16 v[54:57], a[112:115], v[78:81], v[54:57]
v_mfma_f32_16x16x32_bf16 v[58:61], a[116:119], v[78:81], v[58:61]
s_waitcnt lgkmcnt(2)
v_mfma_f32_16x16x32_bf16 v[50:53], a[120:123], v[82:85], v[50:53]
ds_read_b128 v[78:81], v17 offset:13312
v_mfma_f32_16x16x32_bf16 v[54:57], a[124:127], v[82:85], v[54:57]
v_mfma_f32_16x16x32_bf16 v[58:61], a[128:131], v[82:85], v[58:61]
s_waitcnt lgkmcnt(2)
v_mfma_f32_16x16x32_bf16 v[50:53], a[132:135], v[86:89], v[50:53]
ds_read_b128 v[82:85], v17 offset:14336
v_mfma_f32_16x16x32_bf16 v[54:57], a[136:139], v[86:89], v[54:57]
v_mfma_f32_16x16x32_bf16 v[58:61], a[140:143], v[86:89], v[58:61]
s_waitcnt lgkmcnt(2)
v_mfma_f32_16x16x32_bf16 v[50:53], a[144:147], v[74:77], v[50:53]
ds_read_b128 v[86:89], v17 offset:15360
v_mfma_f32_16x16x32_bf16 v[54:57], a[148:151], v[74:77], v[54:57]
v_mfma_f32_16x16x32_bf16 v[58:61], a[152:155], v[74:77], v[58:61]
s_waitcnt lgkmcnt(2)
v_mfma_f32_16x16x32_bf16 v[50:53], a[156:159], v[78:81], v[50:53]
s_waitcnt vmcnt(2)
ds_read_b128 v[74:77], v17 offset:16384
v_sub_u32 v254, v254, v14
v_cmp_lt_u32 vcc, 0x7f, v254
v_mfma_f32_16x16x32_bf16 v[54:57], a[160:163], v[78:81], v[54:57]
s_xor_b32 s43, s43, 0x10000
v_mfma_f32_16x16x32_bf16 v[58:61], a[164:167], v[78:81], v[58:61]
s_add_u32 m0, s43, 0
s_waitcnt lgkmcnt(2)
v_mfma_f32_16x16x32_bf16 v[50:53], a[168:171], v[82:85], v[50:53]
ds_read_b128 v[78:81], v17 offset:17408
s_cbranch_vccnz .Ldn_sp8
.Ldn_rt9:
v_mfma_f32_16x16x32_bf16 v[54:57], a[172:175], v[82:85], v[54:57]
v_mfma_f32_16x16x32_bf16 v[58:61], a[176:179], v[82:85], v[58:61]
s_barrier
s_waitcnt lgkmcnt(2)
v_mfma_f32_16x16x32_bf16 v[50:53], a[180:183], v[86:89], v[50:53]
ds_read_b128 v[82:85], v17 offset:18432
v_add_u32 v14, 1, v14
v_mfma_f32_16x16x32_bf16 v[54:57], a[184:187], v[86:89], v[54:57]
v_mfma_f32_16x16x32_bf16 v[58:61], a[188:191], v[86:89], v[58:61]
s_waitcnt lgkmcnt(2)
v_mfma_f32_16x16x32_bf16 v[50:53], a[192:195], v[74:77], v[50:53]
ds_read_b128 v[86:89], v17 offset:19456
global_load_lds_dwordx4 v9, s[38:39] offset:-2048 sc1
v_mfma_f32_16x16x32_bf16 v[54:57], a[196:199], v[74:77], v[54:57]
v_mfma_f32_16x16x32_bf16 v[58:61], a[200:203], v[74:77], v[58:61]
s_waitcnt lgkmcnt(2)
v_mfma_f32_16x16x32_bf16 v[50:53], a[204:207], v[78:81], v[50:53]
ds_read_b128 v[74:77], v17 offset:20480
global_load_lds_dwordx4 v9, s[38:39] offset:-1024 sc1
v_mfma_f32_16x16x32_bf16 v[54:57], a[208:211], v[78:81], v[54:57]
v_mfma_f32_16x16x32_bf16 v[58:61], a[212:215], v[78:81], v[58:61]
s_waitcnt lgkmcnt(2)
v_mfma_f32_16x16x32_bf16 v[50:53], a[216:219], v[82:85], v[50:53]
ds_read_b128 v[78:81], v17 offset:21504
global_load_lds_dwordx4 v9, s[38:39] offset:0 sc1
v_mfma_f32_16x16x32_bf16 v[54:57], a[220:223], v[82:85], v[54:57]
v_mfma_f32_16x16x32_bf16 v[58:61], a[224:227], v[82:85], v[58:61]
s_waitcnt lgkmcnt(2)
v_mfma_f32_16x16x32_bf16 v[50:53], a[228:231], v[86:89], v[50:53]
ds_read_b128 v[82:85], v17 offset:22528
global_load_lds_dwordx4 v9, s[38:39] offset:1024 sc1
v_mfma_f32_16x16x32_bf16 v[54:57], a[232:235], v[86:89], v[54:57]
v_mfma_f32_16x16x32_bf16 v[58:61], a[236:239], v[86:89], v[58:61]
s_waitcnt lgkmcnt(2)
v_mfma_f32_16x16x32_bf16 v[50:53], a[240:243], v[74:77], v[50:53]
ds_read_b128 v[86:89], v17 offset:23552
global_load_lds_dwordx4 v9, s[38:39] offset:2048 sc1
v_mfma_f32_16x16x32_bf16 v[54:57], a[244:247], v[74:77], v[54:57]
v_mfma_f32_16x16x32_bf16 v[58:61], a[248:251], v[74:77], v[58:61]
s_barrier
s_waitcnt lgkmcnt(2)
v_mfma_f32_16x16x32_bf16 v[50:53], a[252:255], v[78:81], v[50:53]
ds_read_b128 v[74:77], v17 offset:24576
global_load_lds_dwordx4 v9, s[38:39] offset:3072 sc1
v_mfma_f32_16x16x32_bf16 v[54:57], v[18:21], v[78:81], v[54:57]
v_add_u32 v9, 147456, v9
v_min_u32 v9, v9, v10
v_mfma_f32_16x16x32_bf16 v[58:61], v[22:25], v[78:81], v[58:61]
s_waitcnt lgkmcnt(2)
v_mfma_f32_16x16x32_bf16 v[50:53], v[26:29], v[82:85], v[50:53]
ds_read_b128 v[78:81], v17 offset:25600
v_mfma_f32_16x16x32_bf16 v[54:57], v[30:33], v[82:85], v[54:57]
v_mfma_f32_16x16x32_bf16 v[58:61], v[34:37], v[82:85], v[58:61]
s_waitcnt lgkmcnt(2)
v_mfma_f32_16x16x32_bf16 v[50:53], v[38:41], v[86:89], v[50:53]
ds_read_b128 v[82:85], v17 offset:26624
v_mfma_f32_16x16x32_bf16 v[54:57], v[42:45], v[86:89], v[54:57]
v_mfma_f32_16x16x32_bf16 v[58:61], v[46:49], v[86:89], v[58:61]
s_waitcnt lgkmcnt(2)
v_mfma_f32_16x16x32_bf16 v[62:65], a[0:3], v[74:77], 0
ds_read_b128 v[86:89], v17 offset:27648
v_mfma_f32_16x16x32_bf16 v[66:69], a[4:7], v[74:77], 0
global_load_dword v254, v13, s[38:39] sc1
v_mfma_f32_16x16x32_bf16 v[70:73], a[8:11], v[74:77], 0
s_waitcnt lgkmcnt(2)
v_cvt_pk_bf16_f32 v90, v50, v51
v_cvt_pk_bf16_f32 v91, v52, v53
v_mfma_f32_16x16x32_bf16 v[62:65], a[12:15], v[78:81], v[62:65]
ds_read_b128 v[74:77], v17 offset:28672
v_cvt_pk_bf16_f32 v92, v54, v55
v_mfma_f32_16x16x32_bf16 v[66:69], a[16:19], v[78:81], v[66:69]
v_mfma_f32_16x16x32_bf16 v[70:73], a[20:23], v[78:81], v[70:73]
s_waitcnt vmcnt(7)
s_barrier
s_waitcnt lgkmcnt(2)
v_mfma_f32_16x16x32_bf16 v[62:65], a[24:27], v[82:85], v[62:65]
ds_read_b128 v[78:81], v17 offset:29696
v_cvt_pk_bf16_f32 v93, v56, v57
v_mfma_f32_16x16x32_bf16 v[66:69], a[28:31], v[82:85], v[66:69]
v_cvt_pk_bf16_f32 v94, v58, v59
v_cvt_pk_bf16_f32 v96, v58, v59
v_mfma_f32_16x16x32_bf16 v[70:73], a[32:35], v[82:85], v[70:73]
s_waitcnt lgkmcnt(2)
v_cvt_pk_bf16_f32 v95, v60, v61
v_cvt_pk_bf16_f32 v97, v60, v61
v_mfma_f32_16x16x32_bf16 v[62:65], a[36:39], v[86:89], v[62:65]
ds_read_b128 v[82:85], v17 offset:30720
v_mfma_f32_16x16x32_bf16 v[66:69], a[40:43], v[86:89], v[66:69]
global_store_dwordx4 v11, v[90:93], s[38:39] sc1
v_mfma_f32_16x16x32_bf16 v[70:73], a[44:47], v[86:89], v[70:73]
s_waitcnt lgkmcnt(2)
v_add_u32 v11, 110592, v11
v_mfma_f32_16x16x32_bf16 v[62:65], a[48:51], v[74:77], v[62:65]
ds_read_b128 v[86:89], v17 offset:31744
v_mfma_f32_16x16x32_bf16 v[66:69], a[52:55], v[74:77], v[66:69]
v_permlane16_swap_b32 v94, v96
v_mfma_f32_16x16x32_bf16 v[70:73], a[56:59], v[74:77], v[70:73]
s_waitcnt lgkmcnt(2)
v_permlane16_swap_b32 v95, v97
v_mfma_f32_16x16x32_bf16 v[62:65], a[60:63], v[78:81], v[62:65]
ds_read_b128 v[74:77], v17 offset:32768
v_mfma_f32_16x16x32_bf16 v[66:69], a[64:67], v[78:81], v[66:69]
s_mov_b64 exec, s[76:77]
global_store_dwordx4 v12, v[94:97], s[38:39] sc1
s_mov_b64 exec, -1
v_mfma_f32_16x16x32_bf16 v[70:73], a[68:71], v[78:81], v[70:73]
s_waitcnt lgkmcnt(2)
v_add_u32 v12, 110592, v12
v_mfma_f32_16x16x32_bf16 v[62:65], a[72:75], v[82:85], v[62:65]
ds_read_b128 v[78:81], v17 offset:33792
v_mfma_f32_16x16x32_bf16 v[66:69], a[76:79], v[82:85], v[66:69]
v_mfma_f32_16x16x32_bf16 v[70:73], a[80:83], v[82:85], v[70:73]
s_waitcnt lgkmcnt(2)
v_mfma_f32_16x16x32_bf16 v[62:65], a[84:87], v[86:89], v[62:65]
ds_read_b128 v[82:85], v17 offset:34816
v_mfma_f32_16x16x32_bf16 v[66:69], a[88:91], v[86:89], v[66:69]
v_mfma_f32_16x16x32_bf16 v[70:73], a[92:95], v[86:89], v[70:73]
s_waitcnt lgkmcnt(2)
v_mfma_f32_16x16x32_bf16 v[62:65], a[96:99], v[74:77], v[62:65]
ds_read_b128 v[86:89], v17 offset:35840
v_mfma_f32_16x16x32_bf16 v[66:69], a[100:103], v[74:77], v[66:69]
v_mfma_f32_16x16x32_bf16 v[70:73], a[104:107], v[74:77], v[70:73]
s_waitcnt lgkmcnt(2)
v_mfma_f32_16x16x32_bf16 v[62:65], a[108:111], v[78:81], v[62:65]
ds_read_b128 v[74:77], v17 offset:36864
v_mfma_f32_16x16x32_bf16 v[66:69], a[112:115], v[78:81], v[66:69]
v_mfma_f32_16x16x32_bf16 v[70:73], a[116:119], v[78:81], v[70:73]
s_waitcnt lgkmcnt(2)
v_mfma_f32_16x16x32_bf16 v[62:65], a[120:123], v[82:85], v[62:65]
ds_read_b128 v[78:81], v17 offset:37888
v_mfma_f32_16x16x32_bf16 v[66:69], a[124:127], v[82:85], v[66:69]
v_mfma_f32_16x16x32_bf16 v[70:73], a[128:131], v[82:85], v[70:73]
s_waitcnt lgkmcnt(2)
v_mfma_f32_16x16x32_bf16 v[62:65], a[132:135], v[86:89], v[62:65]
ds_read_b128 v[82:85], v17 offset:38912
v_mfma_f32_16x16x32_bf16 v[66:69], a[136:139], v[86:89], v[66:69]
v_mfma_f32_16x16x32_bf16 v[70:73], a[140:143], v[86:89], v[70:73]
s_waitcnt lgkmcnt(2)
v_mfma_f32_16x16x32_bf16 v[62:65], a[144:147], v[74:77], v[62:65]
ds_read_b128 v[86:89], v17 offset:39936
v_mfma_f32_16x16x32_bf16 v[66:69], a[148:151], v[74:77], v[66:69]
v_mfma_f32_16x16x32_bf16 v[70:73], a[152:155], v[74:77], v[70:73]
s_waitcnt lgkmcnt(2)
v_mfma_f32_16x16x32_bf16 v[62:65], a[156:159], v[78:81], v[62:65]
s_waitcnt vmcnt(2)
ds_read_b128 v[74:77], v17 offset:40960
v_sub_u32 v254, v254, v14
v_cmp_lt_u32 vcc, 0x7f, v254
v_mfma_f32_16x16x32_bf16 v[66:69], a[160:163], v[78:81], v[66:69]
v_mfma_f32_16x16x32_bf16 v[70:73], a[164:167], v[78:81], v[70:73]
s_add_u32 m0, s43, 24576
s_waitcnt lgkmcnt(2)
v_mfma_f32_16x16x32_bf16 v[62:65], a[168:171], v[82:85], v[62:65]
ds_read_b128 v[78:81], v17 offset:41984
s_cbranch_vccnz .Ldn_sp10
.Ldn_rt11:
)"
R"(v_mfma_f32_16x16x32_bf16 v[66:69], a[172:175], v[82:85], v[66:69]
v_mfma_f32_16x16x32_bf16 v[70:73], a[176:179], v[82:85], v[70:73]
s_barrier
s_waitcnt lgkmcnt(2)
v_mfma_f32_16x16x32_bf16 v[62:65], a[180:183], v[86:89], v[62:65]
ds_read_b128 v[82:85], v17 offset:43008
v_add_u32 v14, 1, v14
v_mfma_f32_16x16x32_bf16 v[66:69], a[184:187], v[86:89], v[66:69]
v_mfma_f32_16x16x32_bf16 v[70:73], a[188:191], v[86:89], v[70:73]
s_waitcnt lgkmcnt(2)
v_mfma_f32_16x16x32_bf16 v[62:65], a[192:195], v[74:77], v[62:65]
ds_read_b128 v[86:89], v17 offset:44032
global_load_lds_dwordx4 v9, s[38:39] offset:-2048 sc1
v_mfma_f32_16x16x32_bf16 v[66:69], a[196:199], v[74:77], v[66:69]
v_mfma_f32_16x16x32_bf16 v[70:73], a[200:203], v[74:77], v[70:73]
s_waitcnt lgkmcnt(2)
v_mfma_f32_16x16x32_bf16 v[62:65], a[204:207], v[78:81], v[62:65]
ds_read_b128 v[74:77], v17 offset:45056
global_load_lds_dwordx4 v9, s[38:39] offset:-1024 sc1
v_mfma_f32_16x16x32_bf16 v[66:69], a[208:211], v[78:81], v[66:69]
v_mfma_f32_16x16x32_bf16 v[70:73], a[212:215], v[78:81], v[70:73]
s_waitcnt lgkmcnt(2)
v_mfma_f32_16x16x32_bf16 v[62:65], a[216:219], v[82:85], v[62:65]
ds_read_b128 v[78:81], v17 offset:46080
global_load_lds_dwordx4 v9, s[38:39] offset:0 sc1
v_mfma_f32_16x16x32_bf16 v[66:69], a[220:223], v[82:85], v[66:69]
v_mfma_f32_16x16x32_bf16 v[70:73], a[224:227], v[82:85], v[70:73]
s_waitcnt lgkmcnt(2)
v_mfma_f32_16x16x32_bf16 v[62:65], a[228:231], v[86:89], v[62:65]
ds_read_b128 v[82:85], v17 offset:47104
global_load_lds_dwordx4 v9, s[38:39] offset:1024 sc1
v_mfma_f32_16x16x32_bf16 v[66:69], a[232:235], v[86:89], v[66:69]
v_mfma_f32_16x16x32_bf16 v[70:73], a[236:239], v[86:89], v[70:73]
s_waitcnt lgkmcnt(2)
v_mfma_f32_16x16x32_bf16 v[62:65], a[240:243], v[74:77], v[62:65]
ds_read_b128 v[86:89], v17 offset:48128
v_xor_b32 v17, 0x10000, v17
v_mfma_f32_16x16x32_bf16 v[66:69], a[244:247], v[74:77], v[66:69]
v_mfma_f32_16x16x32_bf16 v[70:73], a[248:251], v[74:77], v[70:73]
s_barrier
s_waitcnt lgkmcnt(2)
v_mfma_f32_16x16x32_bf16 v[62:65], a[252:255], v[78:81], v[62:65]
ds_read_b128 v[74:77], v17 offset:0
global_load_lds_dwordx4 v9, s[38:39] offset:2048 sc1
v_mfma_f32_16x16x32_bf16 v[66:69], v[18:21], v[78:81], v[66:69]
v_mfma_f32_16x16x32_bf16 v[70:73], v[22:25], v[78:81], v[70:73]
s_waitcnt lgkmcnt(2)
v_mfma_f32_16x16x32_bf16 v[62:65], v[26:29], v[82:85], v[62:65]
ds_read_b128 v[78:81], v17 offset:1024
global_load_lds_dwordx4 v9, s[38:39] offset:3072 sc1
v_mfma_f32_16x16x32_bf16 v[66:69], v[30:33], v[82:85], v[66:69]
v_add_u32 v9, 147456, v9
v_min_u32 v9, v9, v10
v_mfma_f32_16x16x32_bf16 v[70:73], v[34:37], v[82:85], v[70:73]
s_waitcnt lgkmcnt(2)
v_mfma_f32_16x16x32_bf16 v[62:65], v[38:41], v[86:89], v[62:65]
ds_read_b128 v[82:85], v17 offset:2048
v_mfma_f32_16x16x32_bf16 v[66:69], v[42:45], v[86:89], v[66:69]
v_mfma_f32_16x16x32_bf16 v[70:73], v[46:49], v[86:89], v[70:73]
s_sub_u32 s70, s70, 1
s_cbranch_scc0 .Ldn_top12
s_nop 2
v_cvt_pk_bf16_f32 v90, v62, v63
v_cvt_pk_bf16_f32 v91, v64, v65
v_cvt_pk_bf16_f32 v92, v66, v67
v_cvt_pk_bf16_f32 v93, v68, v69
global_store_dwordx4 v11, v[90:93], s[38:39] sc1
v_add_u32 v11, 110592, v11
v_cvt_pk_bf16_f32 v94, v70, v71
v_cvt_pk_bf16_f32 v96, v70, v71
v_cvt_pk_bf16_f32 v95, v72, v73
v_cvt_pk_bf16_f32 v97, v72, v73
v_permlane16_swap_b32 v94, v96
s_nop 0
v_permlane16_swap_b32 v95, v97
s_mov_b64 exec, s[76:77]
global_store_dwordx4 v12, v[94:97], s[38:39] sc1
s_mov_b64 exec, -1
v_add_u32 v12, 110592, v12
s_waitcnt vmcnt(2)
s_barrier
s_waitcnt vmcnt(0)
s_barrier
s_endpgm
.Ldn_sp8:
s_sleep 1
global_load_dword v254, v13, s[38:39] sc1
s_add_u32 s71, s71, 1
s_waitcnt vmcnt(0)
v_sub_u32 v254, v254, v14
v_cmp_lt_u32 vcc, 0x7f, v254
s_cbranch_vccz .Ldn_rt9
s_cmp_lt_u32 s71, 3000000
s_cbranch_scc1 .Ldn_sp8
s_branch .Ldn_rt9
.Ldn_sp10:
s_sleep 1
global_load_dword v254, v13, s[38:39] sc1
s_add_u32 s71, s71, 1
s_waitcnt vmcnt(0)
v_sub_u32 v254, v254, v14
v_cmp_lt_u32 vcc, 0x7f, v254
s_cbranch_vccz .Ldn_rt11
s_cmp_lt_u32 s71, 3000000
s_cbranch_scc1 .Ldn_sp10
s_branch .Ldn_rt11
.Lrole3:
v_add_u32 v255, 35840, v0
v_add_u32 v118, 71680, v0
v_add_u32 v119, 107520, v0
v_add_u32 v120, 143360, v0
v_add_u32 v121, 179200, v0
v_add_u32 v122, 215040, v0
v_add_u32 v123, 250880, v0
s_sleep 40
s_add_u32 m0, s42, 4096
s_nop 0
global_load_lds_dwordx4 v0, s[40:41] offset:-4096 nt
global_load_lds_dwordx4 v255, s[40:41] offset:-3072 nt
global_load_lds_dwordx4 v118, s[40:41] offset:-2048 nt
global_load_lds_dwordx4 v119, s[40:41] offset:-1024 nt
global_load_lds_dwordx4 v120, s[40:41] offset:0 nt
global_load_lds_dwordx4 v121, s[40:41] offset:1024 nt
global_load_lds_dwordx4 v122, s[40:41] offset:2048 nt
global_load_lds_dwordx4 v123, s[40:41] offset:3072 nt
s_add_u32 s40, s40, 589824
s_addc_u32 s41, s41, 0
s_add_u32 m0, s42, 36864
s_nop 0
global_load_lds_dwordx4 v0, s[40:41] offset:-4096 nt
global_load_lds_dwordx4 v255, s[40:41] offset:-3072 nt
global_load_lds_dwordx4 v118, s[40:41] offset:-2048 nt
global_load_lds_dwordx4 v119, s[40:41] offset:-1024 nt
global_load_lds_dwordx4 v120, s[40:41] offset:0 nt
global_load_lds_dwordx4 v121, s[40:41] offset:1024 nt
global_load_lds_dwordx4 v122, s[40:41] offset:2048 nt
global_load_lds_dwordx4 v123, s[40:41] offset:3072 nt
s_add_u32 s40, s40, 589824
s_addc_u32 s41, s41, 0
s_add_u32 m0, s42, 69632
s_nop 0
global_load_lds_dwordx4 v0, s[40:41] offset:-4096 nt
global_load_lds_dwordx4 v255, s[40:41] offset:-3072 nt
global_load_lds_dwordx4 v118, s[40:41] offset:-2048 nt
global_load_lds_dwordx4 v119, s[40:41] offset:-1024 nt
global_load_lds_dwordx4 v120, s[40:41] offset:0 nt
global_load_lds_dwordx4 v121, s[40:41] offset:1024 nt
global_load_lds_dwordx4 v122, s[40:41] offset:2048 nt
global_load_lds_dwordx4 v123, s[40:41] offset:3072 nt
s_add_u32 s40, s40, 589824
s_addc_u32 s41, s41, 0
s_add_u32 m0, s42, 102400
s_nop 0
global_load_lds_dwordx4 v0, s[40:41] offset:-4096 nt
global_load_lds_dwordx4 v255, s[40:41] offset:-3072 nt
global_load_lds_dwordx4 v118, s[40:41] offset:-2048 nt
global_load_lds_dwordx4 v119, s[40:41] offset:-1024 nt
global_load_lds_dwordx4 v120, s[40:41] offset:0 nt
global_load_lds_dwordx4 v121, s[40:41] offset:1024 nt
global_load_lds_dwordx4 v122, s[40:41] offset:2048 nt
global_load_lds_dwordx4 v123, s[40:41] offset:3072 nt
s_add_u32 s40, s40, 589824
s_addc_u32 s41, s41, 0
v_add_u32 v124, 0x10000, v1
v_mov_b32 v142, 0x3F803F80
v_mov_b32 v143, 0x3F803F80
v_mov_b32 v144, 0x3F803F80
v_mov_b32 v145, 0x3F803F80
s_waitcnt lgkmcnt(0)
s_mul_i32 s37, s37, 0x9E3779B1
s_add_u32 s37, s37, 0x7F4A7C15
v_add_u32 v5, s37, v5
v_add_u32 v7, s37, v7
v_add_u32 v17, s37, v17
s_waitcnt vmcnt(24)
s_barrier
ds_read_b64_tr_b16 a[0:1], v1 offset:0
ds_read_b64_tr_b16 a[2:3], v1 offset:1024
ds_read_b64_tr_b16 a[4:5], v1 offset:8
ds_read_b64_tr_b16 a[6:7], v1 offset:1032
ds_read_b64_tr_b16 a[8:9], v1 offset:16384
ds_read_b64_tr_b16 a[10:11], v1 offset:17408
ds_read_b64_tr_b16 a[12:13], v1 offset:16392
ds_read_b64_tr_b16 a[14:15], v1 offset:17416
ds_read_b64_tr_b16 a[16:17], v1 offset:8192
ds_read_b64_tr_b16 a[18:19], v1 offset:9216
ds_read_b64_tr_b16 a[20:21], v1 offset:8200
ds_read_b64_tr_b16 a[22:23], v1 offset:9224
ds_read_b64_tr_b16 a[24:25], v1 offset:24576
ds_read_b64_tr_b16 a[26:27], v1 offset:25600
ds_read_b64_tr_b16 a[28:29], v1 offset:24584
ds_read_b64_tr_b16 a[30:31], v1 offset:25608
s_waitcnt lgkmcnt(0)
s_barrier
s_add_u32 m0, s42, 4096
s_nop 0
global_load_lds_dwordx4 v0, s[40:41] offset:-4096 nt
global_load_lds_dwordx4 v255, s[40:41] offset:-3072 nt
global_load_lds_dwordx4 v118, s[40:41] offset:-2048 nt
global_load_lds_dwordx4 v119, s[40:41] offset:-1024 nt
global_load_lds_dwordx4 v120, s[40:41] offset:0 nt
global_load_lds_dwordx4 v121, s[40:41] offset:1024 nt
global_load_lds_dwordx4 v122, s[40:41] offset:2048 nt
global_load_lds_dwordx4 v123, s[40:41] offset:3072 nt
s_add_u32 s40, s40, 589824
s_addc_u32 s41, s41, 0
s_waitcnt vmcnt(24)
s_barrier
ds_read_b64_tr_b16 a[32:33], v1 offset:32768
ds_read_b64_tr_b16 a[34:35], v1 offset:33792
ds_read_b64_tr_b16 a[36:37], v1 offset:32776
ds_read_b64_tr_b16 a[38:39], v1 offset:33800
ds_read_b64_tr_b16 a[40:41], v1 offset:49152
ds_read_b64_tr_b16 a[42:43], v1 offset:50176
ds_read_b64_tr_b16 a[44:45], v1 offset:49160
ds_read_b64_tr_b16 a[46:47], v1 offset:50184
ds_read_b64_tr_b16 a[48:49], v1 offset:40960
ds_read_b64_tr_b16 a[50:51], v1 offset:41984
ds_read_b64_tr_b16 a[52:53], v1 offset:40968
ds_read_b64_tr_b16 a[54:55], v1 offset:41992
ds_read_b64_tr_b16 a[56:57], v1 offset:57344
ds_read_b64_tr_b16 a[58:59], v1 offset:58368
ds_read_b64_tr_b16 a[60:61], v1 offset:57352
ds_read_b64_tr_b16 a[62:63], v1 offset:58376
v_mfma_f32_16x16x32_bf16 v[126:129], v[142:145], a[0:3], 0
v_mfma_f32_16x16x32_bf16 v[130:133], v[142:145], a[4:7], 0
v_mfma_f32_16x16x32_bf16 v[134:137], v[142:145], a[8:11], 0
v_mfma_f32_16x16x32_bf16 v[138:141], v[142:145], a[12:15], 0
v_mfma_f32_16x16x32_bf16 v[126:129], v[142:145], a[16:19], v[126:129]
v_mfma_f32_16x16x32_bf16 v[130:133], v[142:145], a[20:23], v[130:133]
v_mfma_f32_16x16x32_bf16 v[134:137], v[142:145], a[24:27], v[134:137]
v_mfma_f32_16x16x32_bf16 v[138:141], v[142:145], a[28:31], v[138:141]
s_waitcnt lgkmcnt(0)
s_barrier
s_add_u32 m0, s42, 36864
s_nop 0
global_load_lds_dwordx4 v0, s[40:41] offset:-4096 nt
global_load_lds_dwordx4 v255, s[40:41] offset:-3072 nt
global_load_lds_dwordx4 v118, s[40:41] offset:-2048 nt
global_load_lds_dwordx4 v119, s[40:41] offset:-1024 nt
global_load_lds_dwordx4 v120, s[40:41] offset:0 nt
global_load_lds_dwordx4 v121, s[40:41] offset:1024 nt
global_load_lds_dwordx4 v122, s[40:41] offset:2048 nt
global_load_lds_dwordx4 v123, s[40:41] offset:3072 nt
s_add_u32 s40, s40, 589824
s_addc_u32 s41, s41, 0
s_waitcnt vmcnt(24)
s_barrier
ds_read_b64_tr_b16 a[64:65], v124 offset:0
ds_read_b64_tr_b16 a[66:67], v124 offset:1024
ds_read_b64_tr_b16 a[68:69], v124 offset:8
ds_read_b64_tr_b16 a[70:71], v124 offset:1032
ds_read_b64_tr_b16 a[72:73], v124 offset:16384
ds_read_b64_tr_b16 a[74:75], v124 offset:17408
ds_read_b64_tr_b16 a[76:77], v124 offset:16392
ds_read_b64_tr_b16 a[78:79], v124 offset:17416
ds_read_b64_tr_b16 a[80:81], v124 offset:8192
ds_read_b64_tr_b16 a[82:83], v124 offset:9216
ds_read_b64_tr_b16 a[84:85], v124 offset:8200
ds_read_b64_tr_b16 a[86:87], v124 offset:9224
ds_read_b64_tr_b16 a[88:89], v124 offset:24576
ds_read_b64_tr_b16 a[90:91], v124 offset:25600
ds_read_b64_tr_b16 a[92:93], v124 offset:24584
ds_read_b64_tr_b16 a[94:95], v124 offset:25608
v_mfma_f32_16x16x32_bf16 v[126:129], v[142:145], a[32:35], v[126:129]
v_mfma_f32_16x16x32_bf16 v[130:133], v[142:145], a[36:39], v[130:133]
v_mfma_f32_16x16x32_bf16 v[134:137], v[142:145], a[40:43], v[134:137]
v_mfma_f32_16x16x32_bf16 v[138:141], v[142:145], a[44:47], v[138:141]
v_mfma_f32_16x16x32_bf16 v[126:129], v[142:145], a[48:51], v[126:129]
v_mfma_f32_16x16x32_bf16 v[130:133], v[142:145], a[52:55], v[130:133]
v_mfma_f32_16x16x32_bf16 v[134:137], v[142:145], a[56:59], v[134:137]
v_mfma_f32_16x16x32_bf16 v[138:141], v[142:145], a[60:63], v[138:141]
s_waitcnt lgkmcnt(0)
s_barrier
s_add_u32 m0, s42, 69632
s_nop 0
global_load_lds_dwordx4 v0, s[40:41] offset:-4096 nt
global_load_lds_dwordx4 v255, s[40:41] offset:-3072 nt
global_load_lds_dwordx4 v118, s[40:41] offset:-2048 nt
global_load_lds_dwordx4 v119, s[40:41] offset:-1024 nt
global_load_lds_dwordx4 v120, s[40:41] offset:0 nt
global_load_lds_dwordx4 v121, s[40:41] offset:1024 nt
global_load_lds_dwordx4 v122, s[40:41] offset:2048 nt
global_load_lds_dwordx4 v123, s[40:41] offset:3072 nt
s_add_u32 s40, s40, 589824
s_addc_u32 s41, s41, 0
s_waitcnt vmcnt(24)
s_barrier
ds_read_b64_tr_b16 a[96:97], v124 offset:32768
ds_read_b64_tr_b16 a[98:99], v124 offset:33792
ds_read_b64_tr_b16 a[100:101], v124 offset:32776
ds_read_b64_tr_b16 a[102:103], v124 offset:33800
ds_read_b64_tr_b16 a[104:105], v124 offset:49152
ds_read_b64_tr_b16 a[106:107], v124 offset:50176
ds_read_b64_tr_b16 a[108:109], v124 offset:49160
ds_read_b64_tr_b16 a[110:111], v124 offset:50184
ds_read_b64_tr_b16 a[112:113], v124 offset:40960
ds_read_b64_tr_b16 a[114:115], v124 offset:41984
ds_read_b64_tr_b16 a[116:117], v124 offset:40968
ds_read_b64_tr_b16 a[118:119], v124 offset:41992
ds_read_b64_tr_b16 a[120:121], v124 offset:57344
ds_read_b64_tr_b16 a[122:123], v124 offset:58368
ds_read_b64_tr_b16 a[124:125], v124 offset:57352
ds_read_b64_tr_b16 a[126:127], v124 offset:58376
v_mfma_f32_16x16x32_bf16 v[126:129], v[142:145], a[64:67], v[126:129]
v_mfma_f32_16x16x32_bf16 v[130:133], v[142:145], a[68:71], v[130:133]
v_mfma_f32_16x16x32_bf16 v[134:137], v[142:145], a[72:75], v[134:137]
v_mfma_f32_16x16x32_bf16 v[138:141], v[142:145], a[76:79], v[138:141]
v_mfma_f32_16x16x32_bf16 v[126:129], v[142:145], a[80:83], v[126:129]
v_mfma_f32_16x16x32_bf16 v[130:133], v[142:145], a[84:87], v[130:133]
v_mfma_f32_16x16x32_bf16 v[134:137], v[142:145], a[88:91], v[134:137]
v_mfma_f32_16x16x32_bf16 v[138:141], v[142:145], a[92:95], v[138:141]
s_waitcnt lgkmcnt(0)
s_barrier
s_add_u32 m0, s42, 102400
s_nop 0
global_load_lds_dwordx4 v0, s[40:41] offset:-4096 nt
global_load_lds_dwordx4 v255, s[40:41] offset:-3072 nt
global_load_lds_dwordx4 v118, s[40:41] offset:-2048 nt
global_load_lds_dwordx4 v119, s[40:41] offset:-1024 nt
global_load_lds_dwordx4 v120, s[40:41] offset:0 nt
global_load_lds_dwordx4 v121, s[40:41] offset:1024 nt
global_load_lds_dwordx4 v122, s[40:41] offset:2048 nt
global_load_lds_dwordx4 v123, s[40:41] offset:3072 nt
s_add_u32 s40, s40, 589824
s_addc_u32 s41, s41, 0
s_waitcnt vmcnt(24)
s_barrier
ds_read_b64_tr_b16 a[128:129], v1 offset:0
ds_read_b64_tr_b16 a[130:131], v1 offset:1024
ds_read_b64_tr_b16 a[132:133], v1 offset:8
ds_read_b64_tr_b16 a[134:135], v1 offset:1032
ds_read_b64_tr_b16 a[136:137], v1 offset:16384
ds_read_b64_tr_b16 a[138:139], v1 offset:17408
ds_read_b64_tr_b16 a[140:141], v1 offset:16392
ds_read_b64_tr_b16 a[142:143], v1 offset:17416
ds_read_b64_tr_b16 a[144:145], v1 offset:8192
ds_read_b64_tr_b16 a[146:147], v1 offset:9216
ds_read_b64_tr_b16 a[148:149], v1 offset:8200
ds_read_b64_tr_b16 a[150:151], v1 offset:9224
ds_read_b64_tr_b16 a[152:153], v1 offset:24576
ds_read_b64_tr_b16 a[154:155], v1 offset:25600
ds_read_b64_tr_b16 a[156:157], v1 offset:24584
ds_read_b64_tr_b16 a[158:159], v1 offset:25608
v_mfma_f32_16x16x32_bf16 v[126:129], v[142:145], a[96:99], v[126:129]
v_mfma_f32_16x16x32_bf16 v[130:133], v[142:145], a[100:103], v[130:133]
v_mfma_f32_16x16x32_bf16 v[134:137], v[142:145], a[104:107], v[134:137]
v_mfma_f32_16x16x32_bf16 v[138:141], v[142:145], a[108:111], v[138:141]
v_mfma_f32_16x16x32_bf16 v[126:129], v[142:145], a[112:115], v[126:129]
v_mfma_f32_16x16x32_bf16 v[130:133], v[142:145], a[116:119], v[130:133]
v_mfma_f32_16x16x32_bf16 v[134:137], v[142:145], a[120:123], v[134:137]
v_mfma_f32_16x16x32_bf16 v[138:141], v[142:145], a[124:127], v[138:141]
s_waitcnt lgkmcnt(0)
s_barrier
s_add_u32 m0, s42, 4096
s_nop 0
global_load_lds_dwordx4 v0, s[40:41] offset:-4096 nt
global_load_lds_dwordx4 v255, s[40:41] offset:-3072 nt
global_load_lds_dwordx4 v118, s[40:41] offset:-2048 nt
global_load_lds_dwordx4 v119, s[40:41] offset:-1024 nt
global_load_lds_dwordx4 v120, s[40:41] offset:0 nt
global_load_lds_dwordx4 v121, s[40:41] offset:1024 nt
global_load_lds_dwordx4 v122, s[40:41] offset:2048 nt
global_load_lds_dwordx4 v123, s[40:41] offset:3072 nt
s_add_u32 s40, s40, 589824
s_addc_u32 s41, s41, 0
s_waitcnt vmcnt(24)
s_barrier
ds_read_b64_tr_b16 a[160:161], v1 offset:32768
ds_read_b64_tr_b16 a[162:163], v1 offset:33792
ds_read_b64_tr_b16 a[164:165], v1 offset:32776
ds_read_b64_tr_b16 a[166:167], v1 offset:33800
ds_read_b64_tr_b16 a[168:169], v1 offset:49152
ds_read_b64_tr_b16 a[170:171], v1 offset:50176
ds_read_b64_tr_b16 a[172:173], v1 offset:49160
ds_read_b64_tr_b16 a[174:175], v1 offset:50184
ds_read_b64_tr_b16 a[176:177], v1 offset:40960
ds_read_b64_tr_b16 a[178:179], v1 offset:41984
ds_read_b64_tr_b16 a[180:181], v1 offset:40968
ds_read_b64_tr_b16 a[182:183], v1 offset:41992
ds_read_b64_tr_b16 a[184:185], v1 offset:57344
ds_read_b64_tr_b16 a[186:187], v1 offset:58368
ds_read_b64_tr_b16 a[188:189], v1 offset:57352
ds_read_b64_tr_b16 a[190:191], v1 offset:58376
v_mfma_f32_16x16x32_bf16 v[126:129], v[142:145], a[128:131], v[126:129]
v_mfma_f32_16x16x32_bf16 v[130:133], v[142:145], a[132:135], v[130:133]
v_mfma_f32_16x16x32_bf16 v[134:137], v[142:145], a[136:139], v[134:137]
v_mfma_f32_16x16x32_bf16 v[138:141], v[142:145], a[140:143], v[138:141]
v_mfma_f32_16x16x32_bf16 v[126:129], v[142:145], a[144:147], v[126:129]
v_mfma_f32_16x16x32_bf16 v[130:133], v[142:145], a[148:151], v[130:133]
v_mfma_f32_16x16x32_bf16 v[134:137], v[142:145], a[152:155], v[134:137]
v_mfma_f32_16x16x32_bf16 v[138:141], v[142:145], a[156:159], v[138:141]
s_waitcnt vmcnt(16)
s_barrier
ds_read_b64_tr_b16 a[192:193], v124 offset:0
ds_read_b64_tr_b16 a[194:195], v124 offset:1024
ds_read_b64_tr_b16 a[196:197], v124 offset:8
ds_read_b64_tr_b16 a[198:199], v124 offset:1032
ds_read_b64_tr_b16 a[200:201], v124 offset:16384
ds_read_b64_tr_b16 a[202:203], v124 offset:17408
)"
R"(ds_read_b64_tr_b16 a[204:205], v124 offset:16392
ds_read_b64_tr_b16 a[206:207], v124 offset:17416
ds_read_b64_tr_b16 a[208:209], v124 offset:8192
ds_read_b64_tr_b16 a[210:211], v124 offset:9216
ds_read_b64_tr_b16 a[212:213], v124 offset:8200
ds_read_b64_tr_b16 a[214:215], v124 offset:9224
ds_read_b64_tr_b16 a[216:217], v124 offset:24576
ds_read_b64_tr_b16 a[218:219], v124 offset:25600
ds_read_b64_tr_b16 a[220:221], v124 offset:24584
ds_read_b64_tr_b16 a[222:223], v124 offset:25608
s_waitcnt lgkmcnt(15)
v_mfma_f32_16x16x32_bf16 v[126:129], v[142:145], a[160:163], v[126:129]
v_mfma_f32_16x16x32_bf16 v[130:133], v[142:145], a[164:167], v[130:133]
v_mfma_f32_16x16x32_bf16 v[134:137], v[142:145], a[168:171], v[134:137]
v_mfma_f32_16x16x32_bf16 v[138:141], v[142:145], a[172:175], v[138:141]
v_mfma_f32_16x16x32_bf16 v[126:129], v[142:145], a[176:179], v[126:129]
v_mfma_f32_16x16x32_bf16 v[130:133], v[142:145], a[180:183], v[130:133]
v_mfma_f32_16x16x32_bf16 v[134:137], v[142:145], a[184:187], v[134:137]
v_mfma_f32_16x16x32_bf16 v[138:141], v[142:145], a[188:191], v[138:141]
s_waitcnt vmcnt(8)
s_barrier
ds_read_b64_tr_b16 a[224:225], v124 offset:32768
ds_read_b64_tr_b16 a[226:227], v124 offset:33792
ds_read_b64_tr_b16 a[228:229], v124 offset:32776
ds_read_b64_tr_b16 a[230:231], v124 offset:33800
ds_read_b64_tr_b16 a[232:233], v124 offset:49152
ds_read_b64_tr_b16 a[234:235], v124 offset:50176
ds_read_b64_tr_b16 a[236:237], v124 offset:49160
ds_read_b64_tr_b16 a[238:239], v124 offset:50184
ds_read_b64_tr_b16 a[240:241], v124 offset:40960
ds_read_b64_tr_b16 a[242:243], v124 offset:41984
ds_read_b64_tr_b16 a[244:245], v124 offset:40968
ds_read_b64_tr_b16 a[246:247], v124 offset:41992
ds_read_b64_tr_b16 a[248:249], v124 offset:57344
ds_read_b64_tr_b16 a[250:251], v124 offset:58368
ds_read_b64_tr_b16 a[252:253], v124 offset:57352
ds_read_b64_tr_b16 a[254:255], v124 offset:58376
s_waitcnt lgkmcnt(15)
v_mfma_f32_16x16x32_bf16 v[126:129], v[142:145], a[192:195], v[126:129]
v_mfma_f32_16x16x32_bf16 v[130:133], v[142:145], a[196:199], v[130:133]
v_mfma_f32_16x16x32_bf16 v[134:137], v[142:145], a[200:203], v[134:137]
v_mfma_f32_16x16x32_bf16 v[138:141], v[142:145], a[204:207], v[138:141]
v_mfma_f32_16x16x32_bf16 v[126:129], v[142:145], a[208:211], v[126:129]
v_mfma_f32_16x16x32_bf16 v[130:133], v[142:145], a[212:215], v[130:133]
v_mfma_f32_16x16x32_bf16 v[134:137], v[142:145], a[216:219], v[134:137]
v_mfma_f32_16x16x32_bf16 v[138:141], v[142:145], a[220:223], v[138:141]
s_waitcnt vmcnt(0)
s_barrier
ds_read_b64_tr_b16 v[26:27], v1 offset:0
ds_read_b64_tr_b16 v[28:29], v1 offset:1024
ds_read_b64_tr_b16 v[30:31], v1 offset:8
ds_read_b64_tr_b16 v[32:33], v1 offset:1032
ds_read_b64_tr_b16 v[34:35], v1 offset:16384
ds_read_b64_tr_b16 v[36:37], v1 offset:17408
ds_read_b64_tr_b16 v[38:39], v1 offset:16392
ds_read_b64_tr_b16 v[40:41], v1 offset:17416
ds_read_b64_tr_b16 v[42:43], v1 offset:8192
ds_read_b64_tr_b16 v[44:45], v1 offset:9216
ds_read_b64_tr_b16 v[46:47], v1 offset:8200
ds_read_b64_tr_b16 v[48:49], v1 offset:9224
ds_read_b64_tr_b16 v[50:51], v1 offset:24576
ds_read_b64_tr_b16 v[52:53], v1 offset:25600
ds_read_b64_tr_b16 v[54:55], v1 offset:24584
ds_read_b64_tr_b16 v[56:57], v1 offset:25608
s_waitcnt lgkmcnt(15)
v_mfma_f32_16x16x32_bf16 v[126:129], v[142:145], a[224:227], v[126:129]
v_mfma_f32_16x16x32_bf16 v[130:133], v[142:145], a[228:231], v[130:133]
v_mfma_f32_16x16x32_bf16 v[134:137], v[142:145], a[232:235], v[134:137]
v_mfma_f32_16x16x32_bf16 v[138:141], v[142:145], a[236:239], v[138:141]
v_mfma_f32_16x16x32_bf16 v[126:129], v[142:145], a[240:243], v[126:129]
v_mfma_f32_16x16x32_bf16 v[130:133], v[142:145], a[244:247], v[130:133]
v_mfma_f32_16x16x32_bf16 v[134:137], v[142:145], a[248:251], v[134:137]
v_mfma_f32_16x16x32_bf16 v[138:141], v[142:145], a[252:255], v[138:141]
s_waitcnt lgkmcnt(14)
v_mfma_f32_16x16x32_bf16 v[126:129], v[142:145], v[26:29], v[126:129]
s_waitcnt lgkmcnt(12)
v_mfma_f32_16x16x32_bf16 v[130:133], v[142:145], v[30:33], v[130:133]
s_waitcnt lgkmcnt(10)
v_mfma_f32_16x16x32_bf16 v[134:137], v[142:145], v[34:37], v[134:137]
s_waitcnt lgkmcnt(8)
v_mfma_f32_16x16x32_bf16 v[138:141], v[142:145], v[38:41], v[138:141]
s_waitcnt lgkmcnt(6)
v_mfma_f32_16x16x32_bf16 v[126:129], v[142:145], v[42:45], v[126:129]
s_waitcnt lgkmcnt(4)
v_mfma_f32_16x16x32_bf16 v[130:133], v[142:145], v[46:49], v[130:133]
s_waitcnt lgkmcnt(2)
v_mfma_f32_16x16x32_bf16 v[134:137], v[142:145], v[50:53], v[134:137]
s_waitcnt lgkmcnt(0)
v_mfma_f32_16x16x32_bf16 v[138:141], v[142:145], v[54:57], v[138:141]
s_nop 1
v_mul_f32 v126, 0xbae38e39, v126
v_mul_f32 v127, 0xbae38e39, v127
v_mul_f32 v128, 0xbae38e39, v128
v_mul_f32 v129, 0xbae38e39, v129
v_mul_f32 v130, 0xbae38e39, v130
v_mul_f32 v131, 0xbae38e39, v131
v_mul_f32 v132, 0xbae38e39, v132
v_mul_f32 v133, 0xbae38e39, v133
v_mul_f32 v134, 0xbae38e39, v134
v_mul_f32 v135, 0xbae38e39, v135
v_mul_f32 v136, 0xbae38e39, v136
v_mul_f32 v137, 0xbae38e39, v137
v_mul_f32 v138, 0xbae38e39, v138
v_mul_f32 v139, 0xbae38e39, v139
v_mul_f32 v140, 0xbae38e39, v140
v_mul_f32 v141, 0xbae38e39, v141
v_mfma_f32_16x16x32_bf16 v[146:149], v[22:25], a[0:3], v[126:129]
v_mfma_f32_16x16x32_bf16 v[150:153], v[20:23], a[0:3], v[126:129]
v_mfma_f32_16x16x32_bf16 v[154:157], v[22:25], a[4:7], v[130:133]
v_mfma_f32_16x16x32_bf16 v[158:161], v[20:23], a[4:7], v[130:133]
v_mfma_f32_16x16x32_bf16 v[162:165], v[22:25], a[8:11], v[134:137]
s_nop 3
v_cvt_pk_bf16_f32 v178, v146, v147
v_cvt_pk_bf16_f32 v179, v148, v149
v_cvt_pk_bf16_f32 v180, v150, v151
v_cvt_pk_bf16_f32 v181, v152, v153
v_mfma_f32_16x16x32_bf16 v[166:169], v[20:23], a[8:11], v[134:137]
v_accvgpr_write_b32 a0, v178
v_accvgpr_write_b32 a1, v179
v_accvgpr_write_b32 a2, v180
v_accvgpr_write_b32 a3, v181
v_mfma_f32_16x16x32_bf16 v[170:173], v[22:25], a[12:15], v[138:141]
v_cvt_pk_bf16_f32 v182, v154, v155
v_cvt_pk_bf16_f32 v183, v156, v157
v_cvt_pk_bf16_f32 v184, v158, v159
v_cvt_pk_bf16_f32 v185, v160, v161
v_mfma_f32_16x16x32_bf16 v[174:177], v[20:23], a[12:15], v[138:141]
v_accvgpr_write_b32 a4, v182
v_accvgpr_write_b32 a5, v183
v_accvgpr_write_b32 a6, v184
v_accvgpr_write_b32 a7, v185
v_mfma_f32_16x16x32_bf16 v[146:149], v[22:25], a[16:19], v[126:129]
v_cvt_pk_bf16_f32 v178, v162, v163
v_cvt_pk_bf16_f32 v179, v164, v165
v_cvt_pk_bf16_f32 v180, v166, v167
v_cvt_pk_bf16_f32 v181, v168, v169
v_mfma_f32_16x16x32_bf16 v[150:153], v[20:23], a[16:19], v[126:129]
v_accvgpr_write_b32 a8, v178
v_accvgpr_write_b32 a9, v179
v_accvgpr_write_b32 a10, v180
v_accvgpr_write_b32 a11, v181
v_mfma_f32_16x16x32_bf16 v[154:157], v[22:25], a[20:23], v[130:133]
v_cvt_pk_bf16_f32 v182, v170, v171
v_cvt_pk_bf16_f32 v183, v172, v173
v_cvt_pk_bf16_f32 v184, v174, v175
v_cvt_pk_bf16_f32 v185, v176, v177
v_mfma_f32_16x16x32_bf16 v[158:161], v[20:23], a[20:23], v[130:133]
v_accvgpr_write_b32 a12, v182
v_accvgpr_write_b32 a13, v183
v_accvgpr_write_b32 a14, v184
v_accvgpr_write_b32 a15, v185
v_mfma_f32_16x16x32_bf16 v[162:165], v[22:25], a[24:27], v[134:137]
v_cvt_pk_bf16_f32 v178, v146, v147
v_cvt_pk_bf16_f32 v179, v148, v149
v_cvt_pk_bf16_f32 v180, v150, v151
v_cvt_pk_bf16_f32 v181, v152, v153
v_mfma_f32_16x16x32_bf16 v[166:169], v[20:23], a[24:27], v[134:137]
v_accvgpr_write_b32 a16, v178
v_accvgpr_write_b32 a17, v179
v_accvgpr_write_b32 a18, v180
v_accvgpr_write_b32 a19, v181
v_mfma_f32_16x16x32_bf16 v[170:173], v[22:25], a[28:31], v[138:141]
v_cvt_pk_bf16_f32 v182, v154, v155
v_cvt_pk_bf16_f32 v183, v156, v157
v_cvt_pk_bf16_f32 v184, v158, v159
v_cvt_pk_bf16_f32 v185, v160, v161
v_mfma_f32_16x16x32_bf16 v[174:177], v[20:23], a[28:31], v[138:141]
v_accvgpr_write_b32 a20, v182
v_accvgpr_write_b32 a21, v183
v_accvgpr_write_b32 a22, v184
v_accvgpr_write_b32 a23, v185
v_mfma_f32_16x16x32_bf16 v[146:149], v[22:25], a[32:35], v[126:129]
v_cvt_pk_bf16_f32 v178, v162, v163
v_cvt_pk_bf16_f32 v179, v164, v165
v_cvt_pk_bf16_f32 v180, v166, v167
v_cvt_pk_bf16_f32 v181, v168, v169
v_mfma_f32_16x16x32_bf16 v[150:153], v[20:23], a[32:35], v[126:129]
v_accvgpr_write_b32 a24, v178
v_accvgpr_write_b32 a25, v179
v_accvgpr_write_b32 a26, v180
v_accvgpr_write_b32 a27, v181
v_mfma_f32_16x16x32_bf16 v[154:157], v[22:25], a[36:39], v[130:133]
v_cvt_pk_bf16_f32 v182, v170, v171
v_cvt_pk_bf16_f32 v183, v172, v173
v_cvt_pk_bf16_f32 v184, v174, v175
v_cvt_pk_bf16_f32 v185, v176, v177
v_mfma_f32_16x16x32_bf16 v[158:161], v[20:23], a[36:39], v[130:133]
v_accvgpr_write_b32 a28, v182
v_accvgpr_write_b32 a29, v183
v_accvgpr_write_b32 a30, v184
v_accvgpr_write_b32 a31, v185
v_mfma_f32_16x16x32_bf16 v[162:165], v[22:25], a[40:43], v[134:137]
v_cvt_pk_bf16_f32 v178, v146, v147
v_cvt_pk_bf16_f32 v179, v148, v149
v_cvt_pk_bf16_f32 v180, v150, v151
v_cvt_pk_bf16_f32 v181, v152, v153
v_mfma_f32_16x16x32_bf16 v[166:169], v[20:23], a[40:43], v[134:137]
v_accvgpr_write_b32 a32, v178
v_accvgpr_write_b32 a33, v179
v_accvgpr_write_b32 a34, v180
v_accvgpr_write_b32 a35, v181
v_mfma_f32_16x16x32_bf16 v[170:173], v[22:25], a[44:47], v[138:141]
v_cvt_pk_bf16_f32 v182, v154, v155
v_cvt_pk_bf16_f32 v183, v156, v157
v_cvt_pk_bf16_f32 v184, v158, v159
v_cvt_pk_bf16_f32 v185, v160, v161
v_mfma_f32_16x16x32_bf16 v[174:177], v[20:23], a[44:47], v[138:141]
v_accvgpr_write_b32 a36, v182
v_accvgpr_write_b32 a37, v183
v_accvgpr_write_b32 a38, v184
v_accvgpr_write_b32 a39, v185
v_mfma_f32_16x16x32_bf16 v[146:149], v[22:25], a[48:51], v[126:129]
v_cvt_pk_bf16_f32 v178, v162, v163
v_cvt_pk_bf16_f32 v179, v164, v165
v_cvt_pk_bf16_f32 v180, v166, v167
v_cvt_pk_bf16_f32 v181, v168, v169
v_mfma_f32_16x16x32_bf16 v[150:153], v[20:23], a[48:51], v[126:129]
v_accvgpr_write_b32 a40, v178
v_accvgpr_write_b32 a41, v179
v_accvgpr_write_b32 a42, v180
v_accvgpr_write_b32 a43, v181
v_mfma_f32_16x16x32_bf16 v[154:157], v[22:25], a[52:55], v[130:133]
v_cvt_pk_bf16_f32 v182, v170, v171
v_cvt_pk_bf16_f32 v183, v172, v173
v_cvt_pk_bf16_f32 v184, v174, v175
v_cvt_pk_bf16_f32 v185, v176, v177
v_mfma_f32_16x16x32_bf16 v[158:161], v[20:23], a[52:55], v[130:133]
v_accvgpr_write_b32 a44, v182
v_accvgpr_write_b32 a45, v183
v_accvgpr_write_b32 a46, v184
v_accvgpr_write_b32 a47, v185
v_mfma_f32_16x16x32_bf16 v[162:165], v[22:25], a[56:59], v[134:137]
v_cvt_pk_bf16_f32 v178, v146, v147
v_cvt_pk_bf16_f32 v179, v148, v149
v_cvt_pk_bf16_f32 v180, v150, v151
v_cvt_pk_bf16_f32 v181, v152, v153
v_mfma_f32_16x16x32_bf16 v[166:169], v[20:23], a[56:59], v[134:137]
v_accvgpr_write_b32 a48, v178
v_accvgpr_write_b32 a49, v179
v_accvgpr_write_b32 a50, v180
v_accvgpr_write_b32 a51, v181
v_mfma_f32_16x16x32_bf16 v[170:173], v[22:25], a[60:63], v[138:141]
v_cvt_pk_bf16_f32 v182, v154, v155
v_cvt_pk_bf16_f32 v183, v156, v157
v_cvt_pk_bf16_f32 v184, v158, v159
v_cvt_pk_bf16_f32 v185, v160, v161
v_mfma_f32_16x16x32_bf16 v[174:177], v[20:23], a[60:63], v[138:141]
v_accvgpr_write_b32 a52, v182
v_accvgpr_write_b32 a53, v183
v_accvgpr_write_b32 a54, v184
v_accvgpr_write_b32 a55, v185
v_mfma_f32_16x16x32_bf16 v[146:149], v[22:25], a[64:67], v[126:129]
v_cvt_pk_bf16_f32 v178, v162, v163
v_cvt_pk_bf16_f32 v179, v164, v165
v_cvt_pk_bf16_f32 v180, v166, v167
v_cvt_pk_bf16_f32 v181, v168, v169
v_mfma_f32_16x16x32_bf16 v[150:153], v[20:23], a[64:67], v[126:129]
v_accvgpr_write_b32 a56, v178
v_accvgpr_write_b32 a57, v179
v_accvgpr_write_b32 a58, v180
v_accvgpr_write_b32 a59, v181
v_mfma_f32_16x16x32_bf16 v[154:157], v[22:25], a[68:71], v[130:133]
v_cvt_pk_bf16_f32 v182, v170, v171
v_cvt_pk_bf16_f32 v183, v172, v173
v_cvt_pk_bf16_f32 v184, v174, v175
v_cvt_pk_bf16_f32 v185, v176, v177
v_mfma_f32_16x16x32_bf16 v[158:161], v[20:23], a[68:71], v[130:133]
v_accvgpr_write_b32 a60, v182
v_accvgpr_write_b32 a61, v183
v_accvgpr_write_b32 a62, v184
v_accvgpr_write_b32 a63, v185
v_mfma_f32_16x16x32_bf16 v[162:165], v[22:25], a[72:75], v[134:137]
v_cvt_pk_bf16_f32 v178, v146, v147
v_cvt_pk_bf16_f32 v179, v148, v149
v_cvt_pk_bf16_f32 v180, v150, v151
v_cvt_pk_bf16_f32 v181, v152, v153
v_mfma_f32_16x16x32_bf16 v[166:169], v[20:23], a[72:75], v[134:137]
v_accvgpr_write_b32 a64, v178
v_accvgpr_write_b32 a65, v179
v_accvgpr_write_b32 a66, v180
v_accvgpr_write_b32 a67, v181
v_mfma_f32_16x16x32_bf16 v[170:173], v[22:25], a[76:79], v[138:141]
v_cvt_pk_bf16_f32 v182, v154, v155
v_cvt_pk_bf16_f32 v183, v156, v157
v_cvt_pk_bf16_f32 v184, v158, v159
v_cvt_pk_bf16_f32 v185, v160, v161
v_mfma_f32_16x16x32_bf16 v[174:177], v[20:23], a[76:79], v[138:141]
v_accvgpr_write_b32 a68, v182
v_accvgpr_write_b32 a69, v183
v_accvgpr_write_b32 a70, v184
v_accvgpr_write_b32 a71, v185
v_mfma_f32_16x16x32_bf16 v[146:149], v[22:25], a[80:83], v[126:129]
v_cvt_pk_bf16_f32 v178, v162, v163
v_cvt_pk_bf16_f32 v179, v164, v165
v_cvt_pk_bf16_f32 v180, v166, v167
v_cvt_pk_bf16_f32 v181, v168, v169
v_mfma_f32_16x16x32_bf16 v[150:153], v[20:23], a[80:83], v[126:129]
v_accvgpr_write_b32 a72, v178
v_accvgpr_write_b32 a73, v179
v_accvgpr_write_b32 a74, v180
v_accvgpr_write_b32 a75, v181
v_mfma_f32_16x16x32_bf16 v[154:157], v[22:25], a[84:87], v[130:133]
v_cvt_pk_bf16_f32 v182, v170, v171
v_cvt_pk_bf16_f32 v183, v172, v173
v_cvt_pk_bf16_f32 v184, v174, v175
v_cvt_pk_bf16_f32 v185, v176, v177
v_mfma_f32_16x16x32_bf16 v[158:161], v[20:23], a[84:87], v[130:133]
v_accvgpr_write_b32 a76, v182
v_accvgpr_write_b32 a77, v183
v_accvgpr_write_b32 a78, v184
v_accvgpr_write_b32 a79, v185
v_mfma_f32_16x16x32_bf16 v[162:165], v[22:25], a[88:91], v[134:137]
v_cvt_pk_bf16_f32 v178, v146, v147
v_cvt_pk_bf16_f32 v179, v148, v149
v_cvt_pk_bf16_f32 v180, v150, v151
v_cvt_pk_bf16_f32 v181, v152, v153
v_mfma_f32_16x16x32_bf16 v[166:169], v[20:23], a[88:91], v[134:137]
v_accvgpr_write_b32 a80, v178
v_accvgpr_write_b32 a81, v179
v_accvgpr_write_b32 a82, v180
v_accvgpr_write_b32 a83, v181
v_mfma_f32_16x16x32_bf16 v[170:173], v[22:25], a[92:95], v[138:141]
v_cvt_pk_bf16_f32 v182, v154, v155
v_cvt_pk_bf16_f32 v183, v156, v157
v_cvt_pk_bf16_f32 v184, v158, v159
v_cvt_pk_bf16_f32 v185, v160, v161
v_mfma_f32_16x16x32_bf16 v[174:177], v[20:23], a[92:95], v[138:141]
v_accvgpr_write_b32 a84, v182
v_accvgpr_write_b32 a85, v183
v_accvgpr_write_b32 a86, v184
v_accvgpr_write_b32 a87, v185
v_mfma_f32_16x16x32_bf16 v[146:149], v[22:25], a[96:99], v[126:129]
v_cvt_pk_bf16_f32 v178, v162, v163
v_cvt_pk_bf16_f32 v179, v164, v165
v_cvt_pk_bf16_f32 v180, v166, v167
v_cvt_pk_bf16_f32 v181, v168, v169
v_mfma_f32_16x16x32_bf16 v[150:153], v[20:23], a[96:99], v[126:129]
v_accvgpr_write_b32 a88, v178
v_accvgpr_write_b32 a89, v179
v_accvgpr_write_b32 a90, v180
v_accvgpr_write_b32 a91, v181
v_mfma_f32_16x16x32_bf16 v[154:157], v[22:25], a[100:103], v[130:133]
v_cvt_pk_bf16_f32 v182, v170, v171
v_cvt_pk_bf16_f32 v183, v172, v173
v_cvt_pk_bf16_f32 v184, v174, v175
v_cvt_pk_bf16_f32 v185, v176, v177
v_mfma_f32_16x16x32_bf16 v[158:161], v[20:23], a[100:103], v[130:133]
v_accvgpr_write_b32 a92, v182
v_accvgpr_write_b32 a93, v183
v_accvgpr_write_b32 a94, v184
v_accvgpr_write_b32 a95, v185
v_mfma_f32_16x16x32_bf16 v[162:165], v[22:25], a[104:107], v[134:137]
v_cvt_pk_bf16_f32 v178, v146, v147
v_cvt_pk_bf16_f32 v179, v148, v149
v_cvt_pk_bf16_f32 v180, v150, v151
v_cvt_pk_bf16_f32 v181, v152, v153
v_mfma_f32_16x16x32_bf16 v[166:169], v[20:23], a[104:107], v[134:137]
v_accvgpr_write_b32 a96, v178
v_accvgpr_write_b32 a97, v179
v_accvgpr_write_b32 a98, v180
v_accvgpr_write_b32 a99, v181
v_mfma_f32_16x16x32_bf16 v[170:173], v[22:25], a[108:111], v[138:141]
v_cvt_pk_bf16_f32 v182, v154, v155
v_cvt_pk_bf16_f32 v183, v156, v157
v_cvt_pk_bf16_f32 v184, v158, v159
v_cvt_pk_bf16_f32 v185, v160, v161
v_mfma_f32_16x16x32_bf16 v[174:177], v[20:23], a[108:111], v[138:141]
v_accvgpr_write_b32 a100, v182
v_accvgpr_write_b32 a101, v183
v_accvgpr_write_b32 a102, v184
v_accvgpr_write_b32 a103, v185
v_mfma_f32_16x16x32_bf16 v[146:149], v[22:25], a[112:115], v[126:129]
v_cvt_pk_bf16_f32 v178, v162, v163
v_cvt_pk_bf16_f32 v179, v164, v165
v_cvt_pk_bf16_f32 v180, v166, v167
v_cvt_pk_bf16_f32 v181, v168, v169
v_mfma_f32_16x16x32_bf16 v[150:153], v[20:23], a[112:115], v[126:129]
v_accvgpr_write_b32 a104, v178
v_accvgpr_write_b32 a105, v179
v_accvgpr_write_b32 a106, v180
v_accvgpr_write_b32 a107, v181
v_mfma_f32_16x16x32_bf16 v[154:157], v[22:25], a[116:119], v[130:133]
v_cvt_pk_bf16_f32 v182, v170, v171
v_cvt_pk_bf16_f32 v183, v172, v173
v_cvt_pk_bf16_f32 v184, v174, v175
v_cvt_pk_bf16_f32 v185, v176, v177
v_mfma_f32_16x16x32_bf16 v[158:161], v[20:23], a[116:119], v[130:133]
v_accvgpr_write_b32 a108, v182
v_accvgpr_write_b32 a109, v183
v_accvgpr_write_b32 a110, v184
v_accvgpr_write_b32 a111, v185
v_mfma_f32_16x16x32_bf16 v[162:165], v[22:25], a[120:123], v[134:137]
v_cvt_pk_bf16_f32 v178, v146, v147
v_cvt_pk_bf16_f32 v179, v148, v149
v_cvt_pk_bf16_f32 v180, v150, v151
v_cvt_pk_bf16_f32 v181, v152, v153
v_mfma_f32_16x16x32_bf16 v[166:169], v[20:23], a[120:123], v[134:137]
v_accvgpr_write_b32 a112, v178
v_accvgpr_write_b32 a113, v179
v_accvgpr_write_b32 a114, v180
)"
R"(v_accvgpr_write_b32 a115, v181
v_mfma_f32_16x16x32_bf16 v[170:173], v[22:25], a[124:127], v[138:141]
v_cvt_pk_bf16_f32 v182, v154, v155
v_cvt_pk_bf16_f32 v183, v156, v157
v_cvt_pk_bf16_f32 v184, v158, v159
v_cvt_pk_bf16_f32 v185, v160, v161
v_mfma_f32_16x16x32_bf16 v[174:177], v[20:23], a[124:127], v[138:141]
v_accvgpr_write_b32 a116, v182
v_accvgpr_write_b32 a117, v183
v_accvgpr_write_b32 a118, v184
v_accvgpr_write_b32 a119, v185
v_mfma_f32_16x16x32_bf16 v[146:149], v[22:25], a[128:131], v[126:129]
v_cvt_pk_bf16_f32 v178, v162, v163
v_cvt_pk_bf16_f32 v179, v164, v165
v_cvt_pk_bf16_f32 v180, v166, v167
v_cvt_pk_bf16_f32 v181, v168, v169
v_mfma_f32_16x16x32_bf16 v[150:153], v[20:23], a[128:131], v[126:129]
v_accvgpr_write_b32 a120, v178
v_accvgpr_write_b32 a121, v179
v_accvgpr_write_b32 a122, v180
v_accvgpr_write_b32 a123, v181
v_mfma_f32_16x16x32_bf16 v[154:157], v[22:25], a[132:135], v[130:133]
v_cvt_pk_bf16_f32 v182, v170, v171
v_cvt_pk_bf16_f32 v183, v172, v173
v_cvt_pk_bf16_f32 v184, v174, v175
v_cvt_pk_bf16_f32 v185, v176, v177
v_mfma_f32_16x16x32_bf16 v[158:161], v[20:23], a[132:135], v[130:133]
v_accvgpr_write_b32 a124, v182
v_accvgpr_write_b32 a125, v183
v_accvgpr_write_b32 a126, v184
v_accvgpr_write_b32 a127, v185
v_mfma_f32_16x16x32_bf16 v[162:165], v[22:25], a[136:139], v[134:137]
v_cvt_pk_bf16_f32 v178, v146, v147
v_cvt_pk_bf16_f32 v179, v148, v149
v_cvt_pk_bf16_f32 v180, v150, v151
v_cvt_pk_bf16_f32 v181, v152, v153
v_mfma_f32_16x16x32_bf16 v[166:169], v[20:23], a[136:139], v[134:137]
v_accvgpr_write_b32 a128, v178
v_accvgpr_write_b32 a129, v179
v_accvgpr_write_b32 a130, v180
v_accvgpr_write_b32 a131, v181
v_mfma_f32_16x16x32_bf16 v[170:173], v[22:25], a[140:143], v[138:141]
v_cvt_pk_bf16_f32 v182, v154, v155
v_cvt_pk_bf16_f32 v183, v156, v157
v_cvt_pk_bf16_f32 v184, v158, v159
v_cvt_pk_bf16_f32 v185, v160, v161
v_mfma_f32_16x16x32_bf16 v[174:177], v[20:23], a[140:143], v[138:141]
v_accvgpr_write_b32 a132, v182
v_accvgpr_write_b32 a133, v183
v_accvgpr_write_b32 a134, v184
v_accvgpr_write_b32 a135, v185
v_mfma_f32_16x16x32_bf16 v[146:149], v[22:25], a[144:147], v[126:129]
v_cvt_pk_bf16_f32 v178, v162, v163
v_cvt_pk_bf16_f32 v179, v164, v165
v_cvt_pk_bf16_f32 v180, v166, v167
v_cvt_pk_bf16_f32 v181, v168, v169
v_mfma_f32_16x16x32_bf16 v[150:153], v[20:23], a[144:147], v[126:129]
v_accvgpr_write_b32 a136, v178
v_accvgpr_write_b32 a137, v179
v_accvgpr_write_b32 a138, v180
v_accvgpr_write_b32 a139, v181
v_mfma_f32_16x16x32_bf16 v[154:157], v[22:25], a[148:151], v[130:133]
v_cvt_pk_bf16_f32 v182, v170, v171
v_cvt_pk_bf16_f32 v183, v172, v173
v_cvt_pk_bf16_f32 v184, v174, v175
v_cvt_pk_bf16_f32 v185, v176, v177
v_mfma_f32_16x16x32_bf16 v[158:161], v[20:23], a[148:151], v[130:133]
v_accvgpr_write_b32 a140, v182
v_accvgpr_write_b32 a141, v183
v_accvgpr_write_b32 a142, v184
v_accvgpr_write_b32 a143, v185
v_mfma_f32_16x16x32_bf16 v[162:165], v[22:25], a[152:155], v[134:137]
v_cvt_pk_bf16_f32 v178, v146, v147
v_cvt_pk_bf16_f32 v179, v148, v149
v_cvt_pk_bf16_f32 v180, v150, v151
v_cvt_pk_bf16_f32 v181, v152, v153
v_mfma_f32_16x16x32_bf16 v[166:169], v[20:23], a[152:155], v[134:137]
v_accvgpr_write_b32 a144, v178
v_accvgpr_write_b32 a145, v179
v_accvgpr_write_b32 a146, v180
v_accvgpr_write_b32 a147, v181
v_mfma_f32_16x16x32_bf16 v[170:173], v[22:25], a[156:159], v[138:141]
v_cvt_pk_bf16_f32 v182, v154, v155
v_cvt_pk_bf16_f32 v183, v156, v157
v_cvt_pk_bf16_f32 v184, v158, v159
v_cvt_pk_bf16_f32 v185, v160, v161
v_mfma_f32_16x16x32_bf16 v[174:177], v[20:23], a[156:159], v[138:141]
v_accvgpr_write_b32 a148, v182
v_accvgpr_write_b32 a149, v183
v_accvgpr_write_b32 a150, v184
v_accvgpr_write_b32 a151, v185
v_mfma_f32_16x16x32_bf16 v[146:149], v[22:25], a[160:163], v[126:129]
v_cvt_pk_bf16_f32 v178, v162, v163
v_cvt_pk_bf16_f32 v179, v164, v165
v_cvt_pk_bf16_f32 v180, v166, v167
v_cvt_pk_bf16_f32 v181, v168, v169
v_mfma_f32_16x16x32_bf16 v[150:153], v[20:23], a[160:163], v[126:129]
v_accvgpr_write_b32 a152, v178
v_accvgpr_write_b32 a153, v179
v_accvgpr_write_b32 a154, v180
v_accvgpr_write_b32 a155, v181
v_mfma_f32_16x16x32_bf16 v[154:157], v[22:25], a[164:167], v[130:133]
v_cvt_pk_bf16_f32 v182, v170, v171
v_cvt_pk_bf16_f32 v183, v172, v173
v_cvt_pk_bf16_f32 v184, v174, v175
v_cvt_pk_bf16_f32 v185, v176, v177
v_mfma_f32_16x16x32_bf16 v[158:161], v[20:23], a[164:167], v[130:133]
v_accvgpr_write_b32 a156, v182
v_accvgpr_write_b32 a157, v183
v_accvgpr_write_b32 a158, v184
v_accvgpr_write_b32 a159, v185
v_mfma_f32_16x16x32_bf16 v[162:165], v[22:25], a[168:171], v[134:137]
v_cvt_pk_bf16_f32 v178, v146, v147
v_cvt_pk_bf16_f32 v179, v148, v149
v_cvt_pk_bf16_f32 v180, v150, v151
v_cvt_pk_bf16_f32 v181, v152, v153
v_mfma_f32_16x16x32_bf16 v[166:169], v[20:23], a[168:171], v[134:137]
v_accvgpr_write_b32 a160, v178
v_accvgpr_write_b32 a161, v179
v_accvgpr_write_b32 a162, v180
v_accvgpr_write_b32 a163, v181
v_mfma_f32_16x16x32_bf16 v[170:173], v[22:25], a[172:175], v[138:141]
v_cvt_pk_bf16_f32 v182, v154, v155
v_cvt_pk_bf16_f32 v183, v156, v157
v_cvt_pk_bf16_f32 v184, v158, v159
v_cvt_pk_bf16_f32 v185, v160, v161
v_mfma_f32_16x16x32_bf16 v[174:177], v[20:23], a[172:175], v[138:141]
v_accvgpr_write_b32 a164, v182
v_accvgpr_write_b32 a165, v183
v_accvgpr_write_b32 a166, v184
v_accvgpr_write_b32 a167, v185
v_mfma_f32_16x16x32_bf16 v[146:149], v[22:25], a[176:179], v[126:129]
v_cvt_pk_bf16_f32 v178, v162, v163
v_cvt_pk_bf16_f32 v179, v164, v165
v_cvt_pk_bf16_f32 v180, v166, v167
v_cvt_pk_bf16_f32 v181, v168, v169
v_mfma_f32_16x16x32_bf16 v[150:153], v[20:23], a[176:179], v[126:129]
v_accvgpr_write_b32 a168, v178
v_accvgpr_write_b32 a169, v179
v_accvgpr_write_b32 a170, v180
v_accvgpr_write_b32 a171, v181
v_mfma_f32_16x16x32_bf16 v[154:157], v[22:25], a[180:183], v[130:133]
v_cvt_pk_bf16_f32 v182, v170, v171
v_cvt_pk_bf16_f32 v183, v172, v173
v_cvt_pk_bf16_f32 v184, v174, v175
v_cvt_pk_bf16_f32 v185, v176, v177
v_mfma_f32_16x16x32_bf16 v[158:161], v[20:23], a[180:183], v[130:133]
v_accvgpr_write_b32 a172, v182
v_accvgpr_write_b32 a173, v183
v_accvgpr_write_b32 a174, v184
v_accvgpr_write_b32 a175, v185
v_mfma_f32_16x16x32_bf16 v[162:165], v[22:25], a[184:187], v[134:137]
v_cvt_pk_bf16_f32 v178, v146, v147
v_cvt_pk_bf16_f32 v179, v148, v149
v_cvt_pk_bf16_f32 v180, v150, v151
v_cvt_pk_bf16_f32 v181, v152, v153
v_mfma_f32_16x16x32_bf16 v[166:169], v[20:23], a[184:187], v[134:137]
v_accvgpr_write_b32 a176, v178
v_accvgpr_write_b32 a177, v179
v_accvgpr_write_b32 a178, v180
v_accvgpr_write_b32 a179, v181
v_mfma_f32_16x16x32_bf16 v[170:173], v[22:25], a[188:191], v[138:141]
v_cvt_pk_bf16_f32 v182, v154, v155
v_cvt_pk_bf16_f32 v183, v156, v157
v_cvt_pk_bf16_f32 v184, v158, v159
v_cvt_pk_bf16_f32 v185, v160, v161
v_mfma_f32_16x16x32_bf16 v[174:177], v[20:23], a[188:191], v[138:141]
v_accvgpr_write_b32 a180, v182
v_accvgpr_write_b32 a181, v183
v_accvgpr_write_b32 a182, v184
v_accvgpr_write_b32 a183, v185
v_mfma_f32_16x16x32_bf16 v[146:149], v[22:25], a[192:195], v[126:129]
v_cvt_pk_bf16_f32 v178, v162, v163
v_cvt_pk_bf16_f32 v179, v164, v165
v_cvt_pk_bf16_f32 v180, v166, v167
v_cvt_pk_bf16_f32 v181, v168, v169
v_mfma_f32_16x16x32_bf16 v[150:153], v[20:23], a[192:195], v[126:129]
v_accvgpr_write_b32 a184, v178
v_accvgpr_write_b32 a185, v179
v_accvgpr_write_b32 a186, v180
v_accvgpr_write_b32 a187, v181
v_mfma_f32_16x16x32_bf16 v[154:157], v[22:25], a[196:199], v[130:133]
v_cvt_pk_bf16_f32 v182, v170, v171
v_cvt_pk_bf16_f32 v183, v172, v173
v_cvt_pk_bf16_f32 v184, v174, v175
v_cvt_pk_bf16_f32 v185, v176, v177
v_mfma_f32_16x16x32_bf16 v[158:161], v[20:23], a[196:199], v[130:133]
v_accvgpr_write_b32 a188, v182
v_accvgpr_write_b32 a189, v183
v_accvgpr_write_b32 a190, v184
v_accvgpr_write_b32 a191, v185
v_mfma_f32_16x16x32_bf16 v[162:165], v[22:25], a[200:203], v[134:137]
v_cvt_pk_bf16_f32 v178, v146, v147
v_cvt_pk_bf16_f32 v179, v148, v149
v_cvt_pk_bf16_f32 v180, v150, v151
v_cvt_pk_bf16_f32 v181, v152, v153
v_mfma_f32_16x16x32_bf16 v[166:169], v[20:23], a[200:203], v[134:137]
v_accvgpr_write_b32 a192, v178
v_accvgpr_write_b32 a193, v179
v_accvgpr_write_b32 a194, v180
v_accvgpr_write_b32 a195, v181
v_mfma_f32_16x16x32_bf16 v[170:173], v[22:25], a[204:207], v[138:141]
v_cvt_pk_bf16_f32 v182, v154, v155
v_cvt_pk_bf16_f32 v183, v156, v157
v_cvt_pk_bf16_f32 v184, v158, v159
v_cvt_pk_bf16_f32 v185, v160, v161
v_mfma_f32_16x16x32_bf16 v[174:177], v[20:23], a[204:207], v[138:141]
v_accvgpr_write_b32 a196, v182
v_accvgpr_write_b32 a197, v183
v_accvgpr_write_b32 a198, v184
v_accvgpr_write_b32 a199, v185
v_mfma_f32_16x16x32_bf16 v[146:149], v[22:25], a[208:211], v[126:129]
v_cvt_pk_bf16_f32 v178, v162, v163
v_cvt_pk_bf16_f32 v179, v164, v165
v_cvt_pk_bf16_f32 v180, v166, v167
v_cvt_pk_bf16_f32 v181, v168, v169
v_mfma_f32_16x16x32_bf16 v[150:153], v[20:23], a[208:211], v[126:129]
v_accvgpr_write_b32 a200, v178
v_accvgpr_write_b32 a201, v179
v_accvgpr_write_b32 a202, v180
v_accvgpr_write_b32 a203, v181
v_mfma_f32_16x16x32_bf16 v[154:157], v[22:25], a[212:215], v[130:133]
v_cvt_pk_bf16_f32 v182, v170, v171
v_cvt_pk_bf16_f32 v183, v172, v173
v_cvt_pk_bf16_f32 v184, v174, v175
v_cvt_pk_bf16_f32 v185, v176, v177
v_mfma_f32_16x16x32_bf16 v[158:161], v[20:23], a[212:215], v[130:133]
v_accvgpr_write_b32 a204, v182
v_accvgpr_write_b32 a205, v183
v_accvgpr_write_b32 a206, v184
v_accvgpr_write_b32 a207, v185
v_mfma_f32_16x16x32_bf16 v[162:165], v[22:25], a[216:219], v[134:137]
v_cvt_pk_bf16_f32 v178, v146, v147
v_cvt_pk_bf16_f32 v179, v148, v149
v_cvt_pk_bf16_f32 v180, v150, v151
v_cvt_pk_bf16_f32 v181, v152, v153
v_mfma_f32_16x16x32_bf16 v[166:169], v[20:23], a[216:219], v[134:137]
v_accvgpr_write_b32 a208, v178
v_accvgpr_write_b32 a209, v179
v_accvgpr_write_b32 a210, v180
v_accvgpr_write_b32 a211, v181
v_mfma_f32_16x16x32_bf16 v[170:173], v[22:25], a[220:223], v[138:141]
v_cvt_pk_bf16_f32 v182, v154, v155
v_cvt_pk_bf16_f32 v183, v156, v157
v_cvt_pk_bf16_f32 v184, v158, v159
v_cvt_pk_bf16_f32 v185, v160, v161
v_mfma_f32_16x16x32_bf16 v[174:177], v[20:23], a[220:223], v[138:141]
v_accvgpr_write_b32 a212, v182
v_accvgpr_write_b32 a213, v183
v_accvgpr_write_b32 a214, v184
v_accvgpr_write_b32 a215, v185
v_mfma_f32_16x16x32_bf16 v[146:149], v[22:25], a[224:227], v[126:129]
v_cvt_pk_bf16_f32 v178, v162, v163
v_cvt_pk_bf16_f32 v179, v164, v165
v_cvt_pk_bf16_f32 v180, v166, v167
v_cvt_pk_bf16_f32 v181, v168, v169
v_mfma_f32_16x16x32_bf16 v[150:153], v[20:23], a[224:227], v[126:129]
v_accvgpr_write_b32 a216, v178
v_accvgpr_write_b32 a217, v179
v_accvgpr_write_b32 a218, v180
v_accvgpr_write_b32 a219, v181
v_mfma_f32_16x16x32_bf16 v[154:157], v[22:25], a[228:231], v[130:133]
v_cvt_pk_bf16_f32 v182, v170, v171
v_cvt_pk_bf16_f32 v183, v172, v173
v_cvt_pk_bf16_f32 v184, v174, v175
v_cvt_pk_bf16_f32 v185, v176, v177
v_mfma_f32_16x16x32_bf16 v[158:161], v[20:23], a[228:231], v[130:133]
v_accvgpr_write_b32 a220, v182
v_accvgpr_write_b32 a221, v183
v_accvgpr_write_b32 a222, v184
v_accvgpr_write_b32 a223, v185
v_mfma_f32_16x16x32_bf16 v[162:165], v[22:25], a[232:235], v[134:137]
v_cvt_pk_bf16_f32 v178, v146, v147
v_cvt_pk_bf16_f32 v179, v148, v149
v_cvt_pk_bf16_f32 v180, v150, v151
v_cvt_pk_bf16_f32 v181, v152, v153
v_mfma_f32_16x16x32_bf16 v[166:169], v[20:23], a[232:235], v[134:137]
v_accvgpr_write_b32 a224, v178
v_accvgpr_write_b32 a225, v179
v_accvgpr_write_b32 a226, v180
v_accvgpr_write_b32 a227, v181
v_mfma_f32_16x16x32_bf16 v[170:173], v[22:25], a[236:239], v[138:141]
v_cvt_pk_bf16_f32 v182, v154, v155
v_cvt_pk_bf16_f32 v183, v156, v157
v_cvt_pk_bf16_f32 v184, v158, v159
v_cvt_pk_bf16_f32 v185, v160, v161
v_mfma_f32_16x16x32_bf16 v[174:177], v[20:23], a[236:239], v[138:141]
v_accvgpr_write_b32 a228, v182
v_accvgpr_write_b32 a229, v183
v_accvgpr_write_b32 a230, v184
v_accvgpr_write_b32 a231, v185
v_mfma_f32_16x16x32_bf16 v[146:149], v[22:25], a[240:243], v[126:129]
v_cvt_pk_bf16_f32 v178, v162, v163
v_cvt_pk_bf16_f32 v179, v164, v165
v_cvt_pk_bf16_f32 v180, v166, v167
v_cvt_pk_bf16_f32 v181, v168, v169
v_mfma_f32_16x16x32_bf16 v[150:153], v[20:23], a[240:243], v[126:129]
v_accvgpr_write_b32 a232, v178
v_accvgpr_write_b32 a233, v179
v_accvgpr_write_b32 a234, v180
v_accvgpr_write_b32 a235, v181
v_mfma_f32_16x16x32_bf16 v[154:157], v[22:25], a[244:247], v[130:133]
v_cvt_pk_bf16_f32 v182, v170, v171
v_cvt_pk_bf16_f32 v183, v172, v173
v_cvt_pk_bf16_f32 v184, v174, v175
v_cvt_pk_bf16_f32 v185, v176, v177
v_mfma_f32_16x16x32_bf16 v[158:161], v[20:23], a[244:247], v[130:133]
v_accvgpr_write_b32 a236, v182
v_accvgpr_write_b32 a237, v183
v_accvgpr_write_b32 a238, v184
v_accvgpr_write_b32 a239, v185
v_mfma_f32_16x16x32_bf16 v[162:165], v[22:25], a[248:251], v[134:137]
v_cvt_pk_bf16_f32 v178, v146, v147
v_cvt_pk_bf16_f32 v179, v148, v149
v_cvt_pk_bf16_f32 v180, v150, v151
v_cvt_pk_bf16_f32 v181, v152, v153
v_mfma_f32_16x16x32_bf16 v[166:169], v[20:23], a[248:251], v[134:137]
v_accvgpr_write_b32 a240, v178
v_accvgpr_write_b32 a241, v179
v_accvgpr_write_b32 a242, v180
v_accvgpr_write_b32 a243, v181
v_mfma_f32_16x16x32_bf16 v[170:173], v[22:25], a[252:255], v[138:141]
v_cvt_pk_bf16_f32 v182, v154, v155
v_cvt_pk_bf16_f32 v183, v156, v157
v_cvt_pk_bf16_f32 v184, v158, v159
v_cvt_pk_bf16_f32 v185, v160, v161
v_mfma_f32_16x16x32_bf16 v[174:177], v[20:23], a[252:255], v[138:141]
v_accvgpr_write_b32 a244, v182
v_accvgpr_write_b32 a245, v183
v_accvgpr_write_b32 a246, v184
v_accvgpr_write_b32 a247, v185
v_mfma_f32_16x16x32_bf16 v[146:149], v[22:25], v[26:29], v[126:129]
v_cvt_pk_bf16_f32 v178, v162, v163
v_cvt_pk_bf16_f32 v179, v164, v165
v_cvt_pk_bf16_f32 v180, v166, v167
v_cvt_pk_bf16_f32 v181, v168, v169
v_mfma_f32_16x16x32_bf16 v[150:153], v[20:23], v[26:29], v[126:129]
v_accvgpr_write_b32 a248, v178
v_accvgpr_write_b32 a249, v179
v_accvgpr_write_b32 a250, v180
v_accvgpr_write_b32 a251, v181
v_mfma_f32_16x16x32_bf16 v[154:157], v[22:25], v[30:33], v[130:133]
v_cvt_pk_bf16_f32 v182, v170, v171
v_cvt_pk_bf16_f32 v183, v172, v173
v_cvt_pk_bf16_f32 v184, v174, v175
v_cvt_pk_bf16_f32 v185, v176, v177
v_mfma_f32_16x16x32_bf16 v[158:161], v[20:23], v[30:33], v[130:133]
v_accvgpr_write_b32 a252, v182
v_accvgpr_write_b32 a253, v183
v_accvgpr_write_b32 a254, v184
v_accvgpr_write_b32 a255, v185
v_mfma_f32_16x16x32_bf16 v[162:165], v[22:25], v[34:37], v[134:137]
v_cvt_pk_bf16_f32 v26, v146, v147
v_cvt_pk_bf16_f32 v27, v148, v149
v_cvt_pk_bf16_f32 v28, v150, v151
v_cvt_pk_bf16_f32 v29, v152, v153
v_mfma_f32_16x16x32_bf16 v[166:169], v[20:23], v[34:37], v[134:137]
v_mfma_f32_16x16x32_bf16 v[170:173], v[22:25], v[38:41], v[138:141]
v_cvt_pk_bf16_f32 v30, v154, v155
v_cvt_pk_bf16_f32 v31, v156, v157
v_cvt_pk_bf16_f32 v32, v158, v159
v_cvt_pk_bf16_f32 v33, v160, v161
v_mfma_f32_16x16x32_bf16 v[174:177], v[20:23], v[38:41], v[138:141]
v_mfma_f32_16x16x32_bf16 v[146:149], v[22:25], v[42:45], v[126:129]
v_cvt_pk_bf16_f32 v34, v162, v163
v_cvt_pk_bf16_f32 v35, v164, v165
v_cvt_pk_bf16_f32 v36, v166, v167
v_cvt_pk_bf16_f32 v37, v168, v169
v_mfma_f32_16x16x32_bf16 v[150:153], v[20:23], v[42:45], v[126:129]
v_mfma_f32_16x16x32_bf16 v[154:157], v[22:25], v[46:49], v[130:133]
v_cvt_pk_bf16_f32 v38, v170, v171
v_cvt_pk_bf16_f32 v39, v172, v173
v_cvt_pk_bf16_f32 v40, v174, v175
v_cvt_pk_bf16_f32 v41, v176, v177
v_mfma_f32_16x16x32_bf16 v[158:161], v[20:23], v[46:49], v[130:133]
v_mfma_f32_16x16x32_bf16 v[162:165], v[22:25], v[50:53], v[134:137]
v_cvt_pk_bf16_f32 v42, v146, v147
v_cvt_pk_bf16_f32 v43, v148, v149
v_cvt_pk_bf16_f32 v44, v150, v151
v_cvt_pk_bf16_f32 v45, v152, v153
v_mfma_f32_16x16x32_bf16 v[166:169], v[20:23], v[50:53], v[134:137]
v_mfma_f32_16x16x32_bf16 v[170:173], v[22:25], v[54:57], v[138:141]
v_cvt_pk_bf16_f32 v46, v154, v155
v_cvt_pk_bf16_f32 v47, v156, v157
v_cvt_pk_bf16_f32 v48, v158, v159
v_cvt_pk_bf16_f32 v49, v160, v161
v_mfma_f32_16x16x32_bf16 v[174:177], v[20:23], v[54:57], v[138:141]
v_cvt_pk_bf16_f32 v50, v162, v163
v_cvt_pk_bf16_f32 v51, v164, v165
v_cvt_pk_bf16_f32 v52, v166, v167
v_cvt_pk_bf16_f32 v53, v168, v169
v_cvt_pk_bf16_f32 v54, v170, v171
v_cvt_pk_bf16_f32 v55, v172, v173
s_nop 1
v_cvt_pk_bf16_f32 v56, v174, v175
v_cvt_pk_bf16_f32 v57, v176, v177
s_barrier
s_and_b32 s75, s36, 3
s_cmp_eq_u32 s75, 1
s_cbranch_scc1 .Lgu_w2
)"
R"(s_cmp_eq_u32 s75, 2
s_cbranch_scc1 .Lgu_w3
s_cmp_eq_u32 s75, 3
s_cbranch_scc1 .Lgu_w4
.Lgu_w1:
v_add_u32 v254, -1, v5
s_mov_b32 s70, 32
s_waitcnt vmcnt(0) lgkmcnt(0)
s_nop 7
.p2align 6
.Lgu_top5:
s_waitcnt lgkmcnt(2)
v_mfma_f32_16x16x32_bf16 v[58:61], a[0:3], v[90:93], 0
ds_read_b128 v[102:105], v8 offset:3072
v_mfma_f32_16x16x32_bf16 v[62:65], a[4:7], v[90:93], 0
v_mfma_f32_16x16x32_bf16 v[66:69], a[8:11], v[90:93], 0
v_mfma_f32_16x16x32_bf16 v[70:73], a[12:15], v[90:93], 0
s_waitcnt lgkmcnt(2)
v_mul_f32 v106, v74, v82
v_mul_f32 v107, v75, v83
v_mfma_f32_16x16x32_bf16 v[58:61], a[16:19], v[94:97], v[58:61]
ds_read_b128 v[90:93], v8 offset:4096
v_mul_f32 v108, v76, v84
v_mfma_f32_16x16x32_bf16 v[62:65], a[20:23], v[94:97], v[62:65]
v_mul_f32 v109, v77, v85
v_mul_f32 v110, v78, v86
v_mfma_f32_16x16x32_bf16 v[66:69], a[24:27], v[94:97], v[66:69]
v_mul_f32 v111, v79, v87
v_mul_f32 v112, v80, v88
v_mfma_f32_16x16x32_bf16 v[70:73], a[28:31], v[94:97], v[70:73]
s_waitcnt lgkmcnt(2)
v_mfma_f32_16x16x32_bf16 v[58:61], a[32:35], v[98:101], v[58:61]
s_waitcnt vmcnt(6)
s_barrier
ds_read_b128 v[94:97], v8 offset:5120
v_mfma_f32_16x16x32_bf16 v[62:65], a[36:39], v[98:101], v[62:65]
v_mul_f32 v113, v81, v89
v_cvt_pk_bf16_f32 v114, v106, v107
v_mfma_f32_16x16x32_bf16 v[66:69], a[40:43], v[98:101], v[66:69]
v_cvt_pk_bf16_f32 v115, v108, v109
v_cvt_pk_bf16_f32 v116, v110, v111
v_mfma_f32_16x16x32_bf16 v[70:73], a[44:47], v[98:101], v[70:73]
s_waitcnt lgkmcnt(2)
v_cvt_pk_bf16_f32 v117, v112, v113
v_mfma_f32_16x16x32_bf16 v[58:61], a[48:51], v[102:105], v[58:61]
ds_read_b128 v[98:101], v8 offset:6144
v_mfma_f32_16x16x32_bf16 v[62:65], a[52:55], v[102:105], v[62:65]
global_store_dwordx4 v3, v[114:117], s[38:39] sc1
v_mfma_f32_16x16x32_bf16 v[66:69], a[56:59], v[102:105], v[66:69]
v_add_u32 v3, 147456, v3
v_mfma_f32_16x16x32_bf16 v[70:73], a[60:63], v[102:105], v[70:73]
s_waitcnt lgkmcnt(2)
v_mfma_f32_16x16x32_bf16 v[58:61], a[64:67], v[90:93], v[58:61]
ds_read_b128 v[102:105], v8 offset:7168
v_mfma_f32_16x16x32_bf16 v[62:65], a[68:71], v[90:93], v[62:65]
v_mfma_f32_16x16x32_bf16 v[66:69], a[72:75], v[90:93], v[66:69]
v_mfma_f32_16x16x32_bf16 v[70:73], a[76:79], v[90:93], v[70:73]
s_waitcnt lgkmcnt(2)
v_mfma_f32_16x16x32_bf16 v[58:61], a[80:83], v[94:97], v[58:61]
ds_read_b128 v[90:93], v8 offset:8192
v_mfma_f32_16x16x32_bf16 v[62:65], a[84:87], v[94:97], v[62:65]
v_mfma_f32_16x16x32_bf16 v[66:69], a[88:91], v[94:97], v[66:69]
v_mfma_f32_16x16x32_bf16 v[70:73], a[92:95], v[94:97], v[70:73]
s_waitcnt lgkmcnt(2)
v_mfma_f32_16x16x32_bf16 v[58:61], a[96:99], v[98:101], v[58:61]
ds_read_b128 v[94:97], v8 offset:9216
v_mfma_f32_16x16x32_bf16 v[62:65], a[100:103], v[98:101], v[62:65]
v_mfma_f32_16x16x32_bf16 v[66:69], a[104:107], v[98:101], v[66:69]
v_mfma_f32_16x16x32_bf16 v[70:73], a[108:111], v[98:101], v[70:73]
s_waitcnt lgkmcnt(2)
v_mfma_f32_16x16x32_bf16 v[58:61], a[112:115], v[102:105], v[58:61]
ds_read_b128 v[98:101], v8 offset:10240
v_mfma_f32_16x16x32_bf16 v[62:65], a[116:119], v[102:105], v[62:65]
v_mfma_f32_16x16x32_bf16 v[66:69], a[120:123], v[102:105], v[66:69]
v_mfma_f32_16x16x32_bf16 v[70:73], a[124:127], v[102:105], v[70:73]
s_waitcnt lgkmcnt(2)
v_mfma_f32_16x16x32_bf16 v[58:61], a[128:131], v[90:93], v[58:61]
ds_read_b128 v[102:105], v8 offset:11264
v_mfma_f32_16x16x32_bf16 v[62:65], a[132:135], v[90:93], v[62:65]
v_mfma_f32_16x16x32_bf16 v[66:69], a[136:139], v[90:93], v[66:69]
v_mfma_f32_16x16x32_bf16 v[70:73], a[140:143], v[90:93], v[70:73]
s_waitcnt lgkmcnt(2)
v_mfma_f32_16x16x32_bf16 v[58:61], a[144:147], v[94:97], v[58:61]
ds_read_b128 v[90:93], v8 offset:12288
v_mfma_f32_16x16x32_bf16 v[62:65], a[148:151], v[94:97], v[62:65]
v_mfma_f32_16x16x32_bf16 v[66:69], a[152:155], v[94:97], v[66:69]
v_mfma_f32_16x16x32_bf16 v[70:73], a[156:159], v[94:97], v[70:73]
s_waitcnt lgkmcnt(2)
v_mfma_f32_16x16x32_bf16 v[58:61], a[160:163], v[98:101], v[58:61]
ds_read_b128 v[94:97], v8 offset:13312
v_mfma_f32_16x16x32_bf16 v[62:65], a[164:167], v[98:101], v[62:65]
v_mfma_f32_16x16x32_bf16 v[66:69], a[168:171], v[98:101], v[66:69]
v_mfma_f32_16x16x32_bf16 v[70:73], a[172:175], v[98:101], v[70:73]
s_waitcnt lgkmcnt(2)
v_mfma_f32_16x16x32_bf16 v[58:61], a[176:179], v[102:105], v[58:61]
ds_read_b128 v[98:101], v8 offset:14336
v_mfma_f32_16x16x32_bf16 v[62:65], a[180:183], v[102:105], v[62:65]
s_xor_b32 s43, s43, 0x10000
v_mfma_f32_16x16x32_bf16 v[66:69], a[184:187], v[102:105], v[66:69]
s_add_u32 m0, s43, 0
v_mfma_f32_16x16x32_bf16 v[70:73], a[188:191], v[102:105], v[70:73]
s_waitcnt lgkmcnt(2)
v_mfma_f32_16x16x32_bf16 v[58:61], a[192:195], v[90:93], v[58:61]
s_barrier
ds_read_b128 v[102:105], v8 offset:15360
v_mfma_f32_16x16x32_bf16 v[62:65], a[196:199], v[90:93], v[62:65]
global_load_lds_dwordx4 v2, s[38:39] offset:-2048 sc1
v_mfma_f32_16x16x32_bf16 v[66:69], a[200:203], v[90:93], v[66:69]
v_mfma_f32_16x16x32_bf16 v[70:73], a[204:207], v[90:93], v[70:73]
s_waitcnt lgkmcnt(2)
v_mfma_f32_16x16x32_bf16 v[58:61], a[208:211], v[94:97], v[58:61]
ds_read_b128 v[90:93], v8 offset:16384
global_load_lds_dwordx4 v2, s[38:39] offset:-1024 sc1
v_mfma_f32_16x16x32_bf16 v[62:65], a[212:215], v[94:97], v[62:65]
v_mfma_f32_16x16x32_bf16 v[66:69], a[216:219], v[94:97], v[66:69]
v_mfma_f32_16x16x32_bf16 v[70:73], a[220:223], v[94:97], v[70:73]
s_waitcnt lgkmcnt(2)
global_load_lds_dwordx4 v2, s[38:39] offset:0 sc1
v_mfma_f32_16x16x32_bf16 v[58:61], a[224:227], v[98:101], v[58:61]
ds_read_b128 v[94:97], v8 offset:17408
v_mfma_f32_16x16x32_bf16 v[62:65], a[228:231], v[98:101], v[62:65]
v_mfma_f32_16x16x32_bf16 v[66:69], a[232:235], v[98:101], v[66:69]
global_load_lds_dwordx4 v2, s[38:39] offset:1024 sc1
v_mfma_f32_16x16x32_bf16 v[70:73], a[236:239], v[98:101], v[70:73]
s_waitcnt lgkmcnt(2)
v_mfma_f32_16x16x32_bf16 v[58:61], a[240:243], v[102:105], v[58:61]
s_waitcnt vmcnt(5)
s_barrier
ds_read_b128 v[98:101], v8 offset:18432
v_mfma_f32_16x16x32_bf16 v[62:65], a[244:247], v[102:105], v[62:65]
global_load_lds_dwordx4 v2, s[38:39] offset:2048 sc1
v_mfma_f32_16x16x32_bf16 v[66:69], a[248:251], v[102:105], v[66:69]
v_mfma_f32_16x16x32_bf16 v[70:73], a[252:255], v[102:105], v[70:73]
s_waitcnt lgkmcnt(2)
v_mfma_f32_16x16x32_bf16 v[58:61], v[26:29], v[90:93], v[58:61]
ds_read_b128 v[102:105], v8 offset:19456
global_load_lds_dwordx4 v2, s[38:39] offset:3072 sc1
v_mfma_f32_16x16x32_bf16 v[62:65], v[30:33], v[90:93], v[62:65]
v_add_u32 v2, 18688, v2
v_min_u32 v2, v2, v9
v_mfma_f32_16x16x32_bf16 v[66:69], v[34:37], v[90:93], v[66:69]
v_mfma_f32_16x16x32_bf16 v[70:73], v[38:41], v[90:93], v[70:73]
s_waitcnt lgkmcnt(2)
v_mfma_f32_16x16x32_bf16 v[58:61], v[42:45], v[94:97], v[58:61]
ds_read_b128 v[90:93], v8 offset:20480
v_mfma_f32_16x16x32_bf16 v[62:65], v[46:49], v[94:97], v[62:65]
v_mfma_f32_16x16x32_bf16 v[66:69], v[50:53], v[94:97], v[66:69]
v_mfma_f32_16x16x32_bf16 v[70:73], v[54:57], v[94:97], v[70:73]
s_waitcnt lgkmcnt(2)
v_mfma_f32_16x16x32_bf16 v[74:77], a[0:3], v[98:101], 0
ds_read_b128 v[94:97], v8 offset:21504
v_mfma_f32_16x16x32_bf16 v[78:81], a[4:7], v[98:101], 0
v_mfma_f32_16x16x32_bf16 v[82:85], a[8:11], v[98:101], 0
v_mfma_f32_16x16x32_bf16 v[86:89], a[12:15], v[98:101], 0
s_waitcnt lgkmcnt(2)
v_mul_f32 v106, v58, v66
v_mul_f32 v107, v59, v67
v_mfma_f32_16x16x32_bf16 v[74:77], a[16:19], v[102:105], v[74:77]
ds_read_b128 v[98:101], v8 offset:22528
v_mul_f32 v108, v60, v68
v_mfma_f32_16x16x32_bf16 v[78:81], a[20:23], v[102:105], v[78:81]
v_mul_f32 v109, v61, v69
v_mul_f32 v110, v62, v70
v_mfma_f32_16x16x32_bf16 v[82:85], a[24:27], v[102:105], v[82:85]
v_mul_f32 v111, v63, v71
v_mul_f32 v112, v64, v72
v_mfma_f32_16x16x32_bf16 v[86:89], a[28:31], v[102:105], v[86:89]
s_waitcnt lgkmcnt(2)
v_mfma_f32_16x16x32_bf16 v[74:77], a[32:35], v[90:93], v[74:77]
s_waitcnt vmcnt(6)
s_barrier
ds_read_b128 v[102:105], v8 offset:23552
v_mfma_f32_16x16x32_bf16 v[78:81], a[36:39], v[90:93], v[78:81]
v_mul_f32 v113, v65, v73
v_cvt_pk_bf16_f32 v114, v106, v107
v_mfma_f32_16x16x32_bf16 v[82:85], a[40:43], v[90:93], v[82:85]
v_cvt_pk_bf16_f32 v115, v108, v109
v_cvt_pk_bf16_f32 v116, v110, v111
v_mfma_f32_16x16x32_bf16 v[86:89], a[44:47], v[90:93], v[86:89]
s_waitcnt lgkmcnt(2)
v_cvt_pk_bf16_f32 v117, v112, v113
v_mfma_f32_16x16x32_bf16 v[74:77], a[48:51], v[94:97], v[74:77]
ds_read_b128 v[90:93], v8 offset:24576
v_mfma_f32_16x16x32_bf16 v[78:81], a[52:55], v[94:97], v[78:81]
global_store_dwordx4 v3, v[114:117], s[38:39] sc1
v_mfma_f32_16x16x32_bf16 v[82:85], a[56:59], v[94:97], v[82:85]
v_add_u32 v3, 147456, v3
v_mfma_f32_16x16x32_bf16 v[86:89], a[60:63], v[94:97], v[86:89]
s_waitcnt lgkmcnt(2)
v_mfma_f32_16x16x32_bf16 v[74:77], a[64:67], v[98:101], v[74:77]
ds_read_b128 v[94:97], v8 offset:25600
v_mfma_f32_16x16x32_bf16 v[78:81], a[68:71], v[98:101], v[78:81]
v_mfma_f32_16x16x32_bf16 v[82:85], a[72:75], v[98:101], v[82:85]
v_mfma_f32_16x16x32_bf16 v[86:89], a[76:79], v[98:101], v[86:89]
s_waitcnt lgkmcnt(2)
v_mfma_f32_16x16x32_bf16 v[74:77], a[80:83], v[102:105], v[74:77]
ds_read_b128 v[98:101], v8 offset:26624
v_mfma_f32_16x16x32_bf16 v[78:81], a[84:87], v[102:105], v[78:81]
v_mfma_f32_16x16x32_bf16 v[82:85], a[88:91], v[102:105], v[82:85]
v_mfma_f32_16x16x32_bf16 v[86:89], a[92:95], v[102:105], v[86:89]
s_waitcnt lgkmcnt(2)
v_mfma_f32_16x16x32_bf16 v[74:77], a[96:99], v[90:93], v[74:77]
ds_read_b128 v[102:105], v8 offset:27648
v_mfma_f32_16x16x32_bf16 v[78:81], a[100:103], v[90:93], v[78:81]
v_mfma_f32_16x16x32_bf16 v[82:85], a[104:107], v[90:93], v[82:85]
v_mfma_f32_16x16x32_bf16 v[86:89], a[108:111], v[90:93], v[86:89]
s_waitcnt lgkmcnt(2)
v_mfma_f32_16x16x32_bf16 v[74:77], a[112:115], v[94:97], v[74:77]
ds_read_b128 v[90:93], v8 offset:28672
v_mfma_f32_16x16x32_bf16 v[78:81], a[116:119], v[94:97], v[78:81]
v_mfma_f32_16x16x32_bf16 v[82:85], a[120:123], v[94:97], v[82:85]
v_mfma_f32_16x16x32_bf16 v[86:89], a[124:127], v[94:97], v[86:89]
s_waitcnt lgkmcnt(2)
v_mfma_f32_16x16x32_bf16 v[74:77], a[128:131], v[98:101], v[74:77]
ds_read_b128 v[94:97], v8 offset:29696
v_mfma_f32_16x16x32_bf16 v[78:81], a[132:135], v[98:101], v[78:81]
v_mfma_f32_16x16x32_bf16 v[82:85], a[136:139], v[98:101], v[82:85]
v_mfma_f32_16x16x32_bf16 v[86:89], a[140:143], v[98:101], v[86:89]
s_waitcnt lgkmcnt(2)
v_mfma_f32_16x16x32_bf16 v[74:77], a[144:147], v[102:105], v[74:77]
ds_read_b128 v[98:101], v8 offset:30720
v_mfma_f32_16x16x32_bf16 v[78:81], a[148:151], v[102:105], v[78:81]
v_mfma_f32_16x16x32_bf16 v[82:85], a[152:155], v[102:105], v[82:85]
v_mfma_f32_16x16x32_bf16 v[86:89], a[156:159], v[102:105], v[86:89]
s_waitcnt lgkmcnt(2)
v_mfma_f32_16x16x32_bf16 v[74:77], a[160:163], v[90:93], v[74:77]
ds_read_b128 v[102:105], v8 offset:31744
v_mfma_f32_16x16x32_bf16 v[78:81], a[164:167], v[90:93], v[78:81]
v_mfma_f32_16x16x32_bf16 v[82:85], a[168:171], v[90:93], v[82:85]
v_mfma_f32_16x16x32_bf16 v[86:89], a[172:175], v[90:93], v[86:89]
s_waitcnt lgkmcnt(2)
v_mfma_f32_16x16x32_bf16 v[74:77], a[176:179], v[94:97], v[74:77]
ds_read_b128 v[90:93], v8 offset:32768
v_mfma_f32_16x16x32_bf16 v[78:81], a[180:183], v[94:97], v[78:81]
v_mfma_f32_16x16x32_bf16 v[82:85], a[184:187], v[94:97], v[82:85]
s_add_u32 m0, s43, 18432
v_mfma_f32_16x16x32_bf16 v[86:89], a[188:191], v[94:97], v[86:89]
s_waitcnt lgkmcnt(2)
v_mfma_f32_16x16x32_bf16 v[74:77], a[192:195], v[98:101], v[74:77]
s_barrier
ds_read_b128 v[94:97], v8 offset:33792
v_mfma_f32_16x16x32_bf16 v[78:81], a[196:199], v[98:101], v[78:81]
global_load_lds_dwordx4 v2, s[38:39] offset:-2048 sc1
v_mfma_f32_16x16x32_bf16 v[82:85], a[200:203], v[98:101], v[82:85]
v_mfma_f32_16x16x32_bf16 v[86:89], a[204:207], v[98:101], v[86:89]
s_waitcnt lgkmcnt(2)
v_mfma_f32_16x16x32_bf16 v[74:77], a[208:211], v[102:105], v[74:77]
ds_read_b128 v[98:101], v8 offset:34816
global_load_lds_dwordx4 v2, s[38:39] offset:-1024 sc1
v_mfma_f32_16x16x32_bf16 v[78:81], a[212:215], v[102:105], v[78:81]
v_mfma_f32_16x16x32_bf16 v[82:85], a[216:219], v[102:105], v[82:85]
v_mfma_f32_16x16x32_bf16 v[86:89], a[220:223], v[102:105], v[86:89]
s_waitcnt lgkmcnt(2)
global_load_lds_dwordx4 v2, s[38:39] offset:0 sc1
v_mfma_f32_16x16x32_bf16 v[74:77], a[224:227], v[90:93], v[74:77]
ds_read_b128 v[102:105], v8 offset:35840
v_mfma_f32_16x16x32_bf16 v[78:81], a[228:231], v[90:93], v[78:81]
v_mfma_f32_16x16x32_bf16 v[82:85], a[232:235], v[90:93], v[82:85]
v_xor_b32 v8, 0x10000, v8
global_load_lds_dwordx4 v2, s[38:39] offset:1024 sc1
v_mfma_f32_16x16x32_bf16 v[86:89], a[236:239], v[90:93], v[86:89]
s_waitcnt lgkmcnt(2)
v_mfma_f32_16x16x32_bf16 v[74:77], a[240:243], v[94:97], v[74:77]
s_waitcnt vmcnt(5)
s_barrier
ds_read_b128 v[90:93], v8 offset:0
v_mfma_f32_16x16x32_bf16 v[78:81], a[244:247], v[94:97], v[78:81]
global_load_lds_dwordx4 v2, s[38:39] offset:2048 sc1
v_mfma_f32_16x16x32_bf16 v[82:85], a[248:251], v[94:97], v[82:85]
v_mfma_f32_16x16x32_bf16 v[86:89], a[252:255], v[94:97], v[86:89]
s_waitcnt lgkmcnt(2)
v_mfma_f32_16x16x32_bf16 v[74:77], v[26:29], v[98:101], v[74:77]
ds_read_b128 v[94:97], v8 offset:1024
global_load_lds_dwordx4 v2, s[38:39] offset:3072 sc1
v_mfma_f32_16x16x32_bf16 v[78:81], v[30:33], v[98:101], v[78:81]
v_add_u32 v2, 18688, v2
v_min_u32 v2, v2, v9
v_mfma_f32_16x16x32_bf16 v[82:85], v[34:37], v[98:101], v[82:85]
v_mfma_f32_16x16x32_bf16 v[86:89], v[38:41], v[98:101], v[86:89]
s_waitcnt lgkmcnt(2)
v_mfma_f32_16x16x32_bf16 v[74:77], v[42:45], v[102:105], v[74:77]
ds_read_b128 v[98:101], v8 offset:2048
v_mfma_f32_16x16x32_bf16 v[78:81], v[46:49], v[102:105], v[78:81]
v_mfma_f32_16x16x32_bf16 v[82:85], v[50:53], v[102:105], v[82:85]
v_mfma_f32_16x16x32_bf16 v[86:89], v[54:57], v[102:105], v[86:89]
s_sub_u32 s70, s70, 1
s_cbranch_scc0 .Lgu_top5
s_nop 4
v_mul_f32 v106, v74, v82
v_mul_f32 v107, v75, v83
v_mul_f32 v108, v76, v84
v_mul_f32 v109, v77, v85
v_mul_f32 v110, v78, v86
v_mul_f32 v111, v79, v87
v_mul_f32 v112, v80, v88
v_mul_f32 v113, v81, v89
v_cvt_pk_bf16_f32 v114, v106, v107
v_cvt_pk_bf16_f32 v115, v108, v109
v_cvt_pk_bf16_f32 v116, v110, v111
v_cvt_pk_bf16_f32 v117, v112, v113
global_store_dwordx4 v3, v[114:117], s[38:39] sc1
s_waitcnt vmcnt(1) lgkmcnt(0)
s_barrier
v_mov_b32 v186, 192
ds_read_b32 v186, v186 offset:36864
s_waitcnt vmcnt(0)
s_barrier
s_waitcnt lgkmcnt(0)
v_readfirstlane_b32 s60, v186
s_nop 3
s_max_i32 s60, s60, 2
s_min_i32 s60, s60, 66
s_add_u32 s60, s60, -2
s_branch .Lgu_fin
.Lgu_w2:
v_add_u32 v254, -1, v5
s_mov_b32 s70, 32
s_waitcnt vmcnt(0) lgkmcnt(0)
s_nop 7
.p2align 6
.Lgu_top6:
s_waitcnt lgkmcnt(2)
v_mfma_f32_16x16x32_bf16 v[58:61], a[0:3], v[90:93], 0
ds_read_b128 v[102:105], v8 offset:3072
v_mfma_f32_16x16x32_bf16 v[62:65], a[4:7], v[90:93], 0
v_mfma_f32_16x16x32_bf16 v[66:69], a[8:11], v[90:93], 0
v_mfma_f32_16x16x32_bf16 v[70:73], a[12:15], v[90:93], 0
s_waitcnt lgkmcnt(2)
v_mul_f32 v106, v74, v82
v_mul_f32 v107, v75, v83
v_mfma_f32_16x16x32_bf16 v[58:61], a[16:19], v[94:97], v[58:61]
ds_read_b128 v[90:93], v8 offset:4096
v_mul_f32 v108, v76, v84
v_mfma_f32_16x16x32_bf16 v[62:65], a[20:23], v[94:97], v[62:65]
v_mul_f32 v109, v77, v85
v_mul_f32 v110, v78, v86
v_mfma_f32_16x16x32_bf16 v[66:69], a[24:27], v[94:97], v[66:69]
v_mul_f32 v111, v79, v87
v_mul_f32 v112, v80, v88
v_mfma_f32_16x16x32_bf16 v[70:73], a[28:31], v[94:97], v[70:73]
s_waitcnt lgkmcnt(2)
v_mfma_f32_16x16x32_bf16 v[58:61], a[32:35], v[98:101], v[58:61]
s_waitcnt vmcnt(7)
s_barrier
ds_read_b128 v[94:97], v8 offset:5120
v_mfma_f32_16x16x32_bf16 v[62:65], a[36:39], v[98:101], v[62:65]
s_mov_b64 exec, 1
global_store_dword v6, v7, s[38:39] sc1
s_mov_b64 exec, -1
v_mfma_f32_16x16x32_bf16 v[66:69], a[40:43], v[98:101], v[66:69]
v_add_u32 v7, 1, v7
v_mul_f32 v113, v81, v89
v_mfma_f32_16x16x32_bf16 v[70:73], a[44:47], v[98:101], v[70:73]
s_waitcnt lgkmcnt(2)
v_cvt_pk_bf16_f32 v114, v106, v107
v_cvt_pk_bf16_f32 v115, v108, v109
v_mfma_f32_16x16x32_bf16 v[58:61], a[48:51], v[102:105], v[58:61]
ds_read_b128 v[98:101], v8 offset:6144
v_cvt_pk_bf16_f32 v116, v110, v111
v_mfma_f32_16x16x32_bf16 v[62:65], a[52:55], v[102:105], v[62:65]
v_cvt_pk_bf16_f32 v117, v112, v113
v_mfma_f32_16x16x32_bf16 v[66:69], a[56:59], v[102:105], v[66:69]
v_mfma_f32_16x16x32_bf16 v[70:73], a[60:63], v[102:105], v[70:73]
s_waitcnt lgkmcnt(2)
global_store_dwordx4 v3, v[114:117], s[38:39] sc1
v_mfma_f32_16x16x32_bf16 v[58:61], a[64:67], v[90:93], v[58:61]
ds_read_b128 v[102:105], v8 offset:7168
v_add_u32 v3, 147456, v3
v_mfma_f32_16x16x32_bf16 v[62:65], a[68:71], v[90:93], v[62:65]
v_mfma_f32_16x16x32_bf16 v[66:69], a[72:75], v[90:93], v[66:69]
v_mfma_f32_16x16x32_bf16 v[70:73], a[76:79], v[90:93], v[70:73]
s_waitcnt lgkmcnt(2)
v_mfma_f32_16x16x32_bf16 v[58:61], a[80:83], v[94:97], v[58:61]
ds_read_b128 v[90:93], v8 offset:8192
v_mfma_f32_16x16x32_bf16 v[62:65], a[84:87], v[94:97], v[62:65]
v_mfma_f32_16x16x32_bf16 v[66:69], a[88:91], v[94:97], v[66:69]
v_mfma_f32_16x16x32_bf16 v[70:73], a[92:95], v[94:97], v[70:73]
s_waitcnt lgkmcnt(2)
v_mfma_f32_16x16x32_bf16 v[58:61], a[96:99], v[98:101], v[58:61]
ds_read_b128 v[94:97], v8 offset:9216
v_mfma_f32_16x16x32_bf16 v[62:65], a[100:103], v[98:101], v[62:65]
v_mfma_f32_16x16x32_bf16 v[66:69], a[104:107], v[98:101], v[66:69]
v_mfma_f32_16x16x32_bf16 v[70:73], a[108:111], v[98:101], v[70:73]
s_waitcnt lgkmcnt(2)
v_mfma_f32_16x16x32_bf16 v[58:61], a[112:115], v[102:105], v[58:61]
ds_read_b128 v[98:101], v8 offset:10240
v_mfma_f32_16x16x32_bf16 v[62:65], a[116:119], v[102:105], v[62:65]
v_mfma_f32_16x16x32_bf16 v[66:69], a[120:123], v[102:105], v[66:69]
v_mfma_f32_16x16x32_bf16 v[70:73], a[124:127], v[102:105], v[70:73]
s_waitcnt lgkmcnt(2)
v_mfma_f32_16x16x32_bf16 v[58:61], a[128:131], v[90:93], v[58:61]
ds_read_b128 v[102:105], v8 offset:11264
v_mfma_f32_16x16x32_bf16 v[62:65], a[132:135], v[90:93], v[62:65]
v_mfma_f32_16x16x32_bf16 v[66:69], a[136:139], v[90:93], v[66:69]
v_mfma_f32_16x16x32_bf16 v[70:73], a[140:143], v[90:93], v[70:73]
s_waitcnt lgkmcnt(2)
v_mfma_f32_16x16x32_bf16 v[58:61], a[144:147], v[94:97], v[58:61]
ds_read_b128 v[90:93], v8 offset:12288
v_mfma_f32_16x16x32_bf16 v[62:65], a[148:151], v[94:97], v[62:65]
v_mfma_f32_16x16x32_bf16 v[66:69], a[152:155], v[94:97], v[66:69]
)"
R"(v_mfma_f32_16x16x32_bf16 v[70:73], a[156:159], v[94:97], v[70:73]
s_waitcnt lgkmcnt(2)
v_mfma_f32_16x16x32_bf16 v[58:61], a[160:163], v[98:101], v[58:61]
ds_read_b128 v[94:97], v8 offset:13312
v_mfma_f32_16x16x32_bf16 v[62:65], a[164:167], v[98:101], v[62:65]
v_mfma_f32_16x16x32_bf16 v[66:69], a[168:171], v[98:101], v[66:69]
v_mfma_f32_16x16x32_bf16 v[70:73], a[172:175], v[98:101], v[70:73]
s_waitcnt lgkmcnt(2)
v_mfma_f32_16x16x32_bf16 v[58:61], a[176:179], v[102:105], v[58:61]
ds_read_b128 v[98:101], v8 offset:14336
v_mfma_f32_16x16x32_bf16 v[62:65], a[180:183], v[102:105], v[62:65]
s_xor_b32 s43, s43, 0x10000
v_mfma_f32_16x16x32_bf16 v[66:69], a[184:187], v[102:105], v[66:69]
s_add_u32 m0, s43, 0
v_mfma_f32_16x16x32_bf16 v[70:73], a[188:191], v[102:105], v[70:73]
s_waitcnt lgkmcnt(2)
v_mfma_f32_16x16x32_bf16 v[58:61], a[192:195], v[90:93], v[58:61]
s_barrier
ds_read_b128 v[102:105], v8 offset:15360
v_mfma_f32_16x16x32_bf16 v[62:65], a[196:199], v[90:93], v[62:65]
v_mfma_f32_16x16x32_bf16 v[66:69], a[200:203], v[90:93], v[66:69]
global_load_lds_dwordx4 v2, s[38:39] offset:-2048 sc1
v_mfma_f32_16x16x32_bf16 v[70:73], a[204:207], v[90:93], v[70:73]
s_waitcnt lgkmcnt(2)
v_mfma_f32_16x16x32_bf16 v[58:61], a[208:211], v[94:97], v[58:61]
ds_read_b128 v[90:93], v8 offset:16384
v_mfma_f32_16x16x32_bf16 v[62:65], a[212:215], v[94:97], v[62:65]
global_load_lds_dwordx4 v2, s[38:39] offset:-1024 sc1
v_mfma_f32_16x16x32_bf16 v[66:69], a[216:219], v[94:97], v[66:69]
v_mfma_f32_16x16x32_bf16 v[70:73], a[220:223], v[94:97], v[70:73]
s_waitcnt lgkmcnt(2)
v_mfma_f32_16x16x32_bf16 v[58:61], a[224:227], v[98:101], v[58:61]
ds_read_b128 v[94:97], v8 offset:17408
global_load_lds_dwordx4 v2, s[38:39] offset:0 sc1
v_mfma_f32_16x16x32_bf16 v[62:65], a[228:231], v[98:101], v[62:65]
v_mfma_f32_16x16x32_bf16 v[66:69], a[232:235], v[98:101], v[66:69]
v_mfma_f32_16x16x32_bf16 v[70:73], a[236:239], v[98:101], v[70:73]
s_waitcnt lgkmcnt(2)
v_mfma_f32_16x16x32_bf16 v[58:61], a[240:243], v[102:105], v[58:61]
s_waitcnt vmcnt(5)
s_barrier
ds_read_b128 v[98:101], v8 offset:18432
v_mfma_f32_16x16x32_bf16 v[62:65], a[244:247], v[102:105], v[62:65]
global_load_lds_dwordx4 v2, s[38:39] offset:1024 sc1
v_mfma_f32_16x16x32_bf16 v[66:69], a[248:251], v[102:105], v[66:69]
v_mfma_f32_16x16x32_bf16 v[70:73], a[252:255], v[102:105], v[70:73]
s_waitcnt lgkmcnt(2)
v_mfma_f32_16x16x32_bf16 v[58:61], v[26:29], v[90:93], v[58:61]
ds_read_b128 v[102:105], v8 offset:19456
global_load_lds_dwordx4 v2, s[38:39] offset:2048 sc1
v_mfma_f32_16x16x32_bf16 v[62:65], v[30:33], v[90:93], v[62:65]
v_mfma_f32_16x16x32_bf16 v[66:69], v[34:37], v[90:93], v[66:69]
v_mfma_f32_16x16x32_bf16 v[70:73], v[38:41], v[90:93], v[70:73]
s_waitcnt lgkmcnt(2)
global_load_lds_dwordx4 v2, s[38:39] offset:3072 sc1
v_add_u32 v2, 18688, v2
v_mfma_f32_16x16x32_bf16 v[58:61], v[42:45], v[94:97], v[58:61]
ds_read_b128 v[90:93], v8 offset:20480
v_min_u32 v2, v2, v9
v_mfma_f32_16x16x32_bf16 v[62:65], v[46:49], v[94:97], v[62:65]
v_mfma_f32_16x16x32_bf16 v[66:69], v[50:53], v[94:97], v[66:69]
v_mfma_f32_16x16x32_bf16 v[70:73], v[54:57], v[94:97], v[70:73]
s_waitcnt lgkmcnt(2)
v_mfma_f32_16x16x32_bf16 v[74:77], a[0:3], v[98:101], 0
ds_read_b128 v[94:97], v8 offset:21504
v_mfma_f32_16x16x32_bf16 v[78:81], a[4:7], v[98:101], 0
v_mfma_f32_16x16x32_bf16 v[82:85], a[8:11], v[98:101], 0
v_mfma_f32_16x16x32_bf16 v[86:89], a[12:15], v[98:101], 0
s_waitcnt lgkmcnt(2)
v_mul_f32 v106, v58, v66
v_mul_f32 v107, v59, v67
v_mfma_f32_16x16x32_bf16 v[74:77], a[16:19], v[102:105], v[74:77]
ds_read_b128 v[98:101], v8 offset:22528
v_mul_f32 v108, v60, v68
v_mfma_f32_16x16x32_bf16 v[78:81], a[20:23], v[102:105], v[78:81]
v_mul_f32 v109, v61, v69
v_mul_f32 v110, v62, v70
v_mfma_f32_16x16x32_bf16 v[82:85], a[24:27], v[102:105], v[82:85]
v_mul_f32 v111, v63, v71
v_mul_f32 v112, v64, v72
v_mfma_f32_16x16x32_bf16 v[86:89], a[28:31], v[102:105], v[86:89]
s_waitcnt lgkmcnt(2)
v_mfma_f32_16x16x32_bf16 v[74:77], a[32:35], v[90:93], v[74:77]
s_waitcnt vmcnt(6)
s_barrier
ds_read_b128 v[102:105], v8 offset:23552
v_mfma_f32_16x16x32_bf16 v[78:81], a[36:39], v[90:93], v[78:81]
v_mul_f32 v113, v65, v73
v_cvt_pk_bf16_f32 v114, v106, v107
v_mfma_f32_16x16x32_bf16 v[82:85], a[40:43], v[90:93], v[82:85]
v_cvt_pk_bf16_f32 v115, v108, v109
v_cvt_pk_bf16_f32 v116, v110, v111
v_mfma_f32_16x16x32_bf16 v[86:89], a[44:47], v[90:93], v[86:89]
s_waitcnt lgkmcnt(2)
v_cvt_pk_bf16_f32 v117, v112, v113
v_mfma_f32_16x16x32_bf16 v[74:77], a[48:51], v[94:97], v[74:77]
ds_read_b128 v[90:93], v8 offset:24576
v_mfma_f32_16x16x32_bf16 v[78:81], a[52:55], v[94:97], v[78:81]
global_store_dwordx4 v3, v[114:117], s[38:39] sc1
v_mfma_f32_16x16x32_bf16 v[82:85], a[56:59], v[94:97], v[82:85]
v_add_u32 v3, 147456, v3
v_mfma_f32_16x16x32_bf16 v[86:89], a[60:63], v[94:97], v[86:89]
s_waitcnt lgkmcnt(2)
s_mov_b64 exec, 1
global_store_dword v6, v7, s[38:39] sc1
s_mov_b64 exec, -1
v_mfma_f32_16x16x32_bf16 v[74:77], a[64:67], v[98:101], v[74:77]
ds_read_b128 v[94:97], v8 offset:25600
v_add_u32 v7, 1, v7
v_mfma_f32_16x16x32_bf16 v[78:81], a[68:71], v[98:101], v[78:81]
v_mfma_f32_16x16x32_bf16 v[82:85], a[72:75], v[98:101], v[82:85]
v_mfma_f32_16x16x32_bf16 v[86:89], a[76:79], v[98:101], v[86:89]
s_waitcnt lgkmcnt(2)
v_mfma_f32_16x16x32_bf16 v[74:77], a[80:83], v[102:105], v[74:77]
ds_read_b128 v[98:101], v8 offset:26624
v_mfma_f32_16x16x32_bf16 v[78:81], a[84:87], v[102:105], v[78:81]
v_mfma_f32_16x16x32_bf16 v[82:85], a[88:91], v[102:105], v[82:85]
v_mfma_f32_16x16x32_bf16 v[86:89], a[92:95], v[102:105], v[86:89]
s_waitcnt lgkmcnt(2)
v_mfma_f32_16x16x32_bf16 v[74:77], a[96:99], v[90:93], v[74:77]
ds_read_b128 v[102:105], v8 offset:27648
v_mfma_f32_16x16x32_bf16 v[78:81], a[100:103], v[90:93], v[78:81]
v_mfma_f32_16x16x32_bf16 v[82:85], a[104:107], v[90:93], v[82:85]
v_mfma_f32_16x16x32_bf16 v[86:89], a[108:111], v[90:93], v[86:89]
s_waitcnt lgkmcnt(2)
v_mfma_f32_16x16x32_bf16 v[74:77], a[112:115], v[94:97], v[74:77]
ds_read_b128 v[90:93], v8 offset:28672
v_mfma_f32_16x16x32_bf16 v[78:81], a[116:119], v[94:97], v[78:81]
v_mfma_f32_16x16x32_bf16 v[82:85], a[120:123], v[94:97], v[82:85]
v_mfma_f32_16x16x32_bf16 v[86:89], a[124:127], v[94:97], v[86:89]
s_waitcnt lgkmcnt(2)
v_mfma_f32_16x16x32_bf16 v[74:77], a[128:131], v[98:101], v[74:77]
ds_read_b128 v[94:97], v8 offset:29696
v_mfma_f32_16x16x32_bf16 v[78:81], a[132:135], v[98:101], v[78:81]
v_mfma_f32_16x16x32_bf16 v[82:85], a[136:139], v[98:101], v[82:85]
v_mfma_f32_16x16x32_bf16 v[86:89], a[140:143], v[98:101], v[86:89]
s_waitcnt lgkmcnt(2)
v_mfma_f32_16x16x32_bf16 v[74:77], a[144:147], v[102:105], v[74:77]
ds_read_b128 v[98:101], v8 offset:30720
v_mfma_f32_16x16x32_bf16 v[78:81], a[148:151], v[102:105], v[78:81]
v_mfma_f32_16x16x32_bf16 v[82:85], a[152:155], v[102:105], v[82:85]
v_mfma_f32_16x16x32_bf16 v[86:89], a[156:159], v[102:105], v[86:89]
s_waitcnt lgkmcnt(2)
v_mfma_f32_16x16x32_bf16 v[74:77], a[160:163], v[90:93], v[74:77]
ds_read_b128 v[102:105], v8 offset:31744
v_mfma_f32_16x16x32_bf16 v[78:81], a[164:167], v[90:93], v[78:81]
v_mfma_f32_16x16x32_bf16 v[82:85], a[168:171], v[90:93], v[82:85]
v_mfma_f32_16x16x32_bf16 v[86:89], a[172:175], v[90:93], v[86:89]
s_waitcnt lgkmcnt(2)
v_mfma_f32_16x16x32_bf16 v[74:77], a[176:179], v[94:97], v[74:77]
ds_read_b128 v[90:93], v8 offset:32768
v_mfma_f32_16x16x32_bf16 v[78:81], a[180:183], v[94:97], v[78:81]
v_mfma_f32_16x16x32_bf16 v[82:85], a[184:187], v[94:97], v[82:85]
s_add_u32 m0, s43, 18432
v_mfma_f32_16x16x32_bf16 v[86:89], a[188:191], v[94:97], v[86:89]
s_waitcnt lgkmcnt(2)
v_mfma_f32_16x16x32_bf16 v[74:77], a[192:195], v[98:101], v[74:77]
s_barrier
ds_read_b128 v[94:97], v8 offset:33792
v_mfma_f32_16x16x32_bf16 v[78:81], a[196:199], v[98:101], v[78:81]
v_mfma_f32_16x16x32_bf16 v[82:85], a[200:203], v[98:101], v[82:85]
global_load_lds_dwordx4 v2, s[38:39] offset:-2048 sc1
v_mfma_f32_16x16x32_bf16 v[86:89], a[204:207], v[98:101], v[86:89]
s_waitcnt lgkmcnt(2)
v_mfma_f32_16x16x32_bf16 v[74:77], a[208:211], v[102:105], v[74:77]
ds_read_b128 v[98:101], v8 offset:34816
v_mfma_f32_16x16x32_bf16 v[78:81], a[212:215], v[102:105], v[78:81]
global_load_lds_dwordx4 v2, s[38:39] offset:-1024 sc1
v_mfma_f32_16x16x32_bf16 v[82:85], a[216:219], v[102:105], v[82:85]
v_mfma_f32_16x16x32_bf16 v[86:89], a[220:223], v[102:105], v[86:89]
s_waitcnt lgkmcnt(2)
v_mfma_f32_16x16x32_bf16 v[74:77], a[224:227], v[90:93], v[74:77]
ds_read_b128 v[102:105], v8 offset:35840
global_load_lds_dwordx4 v2, s[38:39] offset:0 sc1
v_mfma_f32_16x16x32_bf16 v[78:81], a[228:231], v[90:93], v[78:81]
v_mfma_f32_16x16x32_bf16 v[82:85], a[232:235], v[90:93], v[82:85]
v_xor_b32 v8, 0x10000, v8
v_mfma_f32_16x16x32_bf16 v[86:89], a[236:239], v[90:93], v[86:89]
s_waitcnt lgkmcnt(2)
v_mfma_f32_16x16x32_bf16 v[74:77], a[240:243], v[94:97], v[74:77]
s_waitcnt vmcnt(5)
s_barrier
ds_read_b128 v[90:93], v8 offset:0
v_mfma_f32_16x16x32_bf16 v[78:81], a[244:247], v[94:97], v[78:81]
global_load_lds_dwordx4 v2, s[38:39] offset:1024 sc1
v_mfma_f32_16x16x32_bf16 v[82:85], a[248:251], v[94:97], v[82:85]
v_mfma_f32_16x16x32_bf16 v[86:89], a[252:255], v[94:97], v[86:89]
s_waitcnt lgkmcnt(2)
v_mfma_f32_16x16x32_bf16 v[74:77], v[26:29], v[98:101], v[74:77]
ds_read_b128 v[94:97], v8 offset:1024
global_load_lds_dwordx4 v2, s[38:39] offset:2048 sc1
v_mfma_f32_16x16x32_bf16 v[78:81], v[30:33], v[98:101], v[78:81]
v_mfma_f32_16x16x32_bf16 v[82:85], v[34:37], v[98:101], v[82:85]
v_mfma_f32_16x16x32_bf16 v[86:89], v[38:41], v[98:101], v[86:89]
s_waitcnt lgkmcnt(2)
global_load_lds_dwordx4 v2, s[38:39] offset:3072 sc1
v_add_u32 v2, 18688, v2
v_mfma_f32_16x16x32_bf16 v[74:77], v[42:45], v[102:105], v[74:77]
ds_read_b128 v[98:101], v8 offset:2048
v_min_u32 v2, v2, v9
v_mfma_f32_16x16x32_bf16 v[78:81], v[46:49], v[102:105], v[78:81]
v_mfma_f32_16x16x32_bf16 v[82:85], v[50:53], v[102:105], v[82:85]
v_mfma_f32_16x16x32_bf16 v[86:89], v[54:57], v[102:105], v[86:89]
s_sub_u32 s70, s70, 1
s_cbranch_scc0 .Lgu_top6
s_nop 4
v_mul_f32 v106, v74, v82
v_mul_f32 v107, v75, v83
v_mul_f32 v108, v76, v84
v_mul_f32 v109, v77, v85
v_mul_f32 v110, v78, v86
v_mul_f32 v111, v79, v87
v_mul_f32 v112, v80, v88
v_mul_f32 v113, v81, v89
v_cvt_pk_bf16_f32 v114, v106, v107
v_cvt_pk_bf16_f32 v115, v108, v109
v_cvt_pk_bf16_f32 v116, v110, v111
v_cvt_pk_bf16_f32 v117, v112, v113
global_store_dwordx4 v3, v[114:117], s[38:39] sc1
s_waitcnt vmcnt(1) lgkmcnt(0)
s_barrier
s_mov_b64 exec, 1
global_store_dword v6, v7, s[38:39] sc1
s_mov_b64 exec, -1
v_add_u32 v7, 3, v7
v_mov_b32 v186, 192
ds_read_b32 v186, v186 offset:36864
s_waitcnt vmcnt(1)
s_barrier
s_mov_b64 exec, 1
global_store_dword v6, v7, s[38:39] sc1
s_mov_b64 exec, -1
s_waitcnt lgkmcnt(0)
v_readfirstlane_b32 s60, v186
s_nop 3
s_max_i32 s60, s60, 2
s_min_i32 s60, s60, 66
s_add_u32 s60, s60, -1
s_branch .Lgu_fin
.Lgu_w3:
v_add_u32 v254, -1, v5
s_mov_b32 s70, 32
s_waitcnt vmcnt(0) lgkmcnt(0)
s_nop 7
.p2align 6
.Lgu_top11:
s_waitcnt lgkmcnt(2)
v_mfma_f32_16x16x32_bf16 v[58:61], a[0:3], v[90:93], 0
ds_read_b128 v[102:105], v8 offset:3072
v_mfma_f32_16x16x32_bf16 v[62:65], a[4:7], v[90:93], 0
v_mfma_f32_16x16x32_bf16 v[66:69], a[8:11], v[90:93], 0
global_load_dword v254, v4, s[38:39] sc1
v_mfma_f32_16x16x32_bf16 v[70:73], a[12:15], v[90:93], 0
s_waitcnt lgkmcnt(2)
v_mul_f32 v106, v74, v82
v_mul_f32 v107, v75, v83
v_mfma_f32_16x16x32_bf16 v[58:61], a[16:19], v[94:97], v[58:61]
ds_read_b128 v[90:93], v8 offset:4096
v_mul_f32 v108, v76, v84
v_mfma_f32_16x16x32_bf16 v[62:65], a[20:23], v[94:97], v[62:65]
v_mul_f32 v109, v77, v85
v_mul_f32 v110, v78, v86
v_mfma_f32_16x16x32_bf16 v[66:69], a[24:27], v[94:97], v[66:69]
v_mul_f32 v111, v79, v87
v_mul_f32 v112, v80, v88
v_mfma_f32_16x16x32_bf16 v[70:73], a[28:31], v[94:97], v[70:73]
s_waitcnt lgkmcnt(2)
v_mfma_f32_16x16x32_bf16 v[58:61], a[32:35], v[98:101], v[58:61]
s_waitcnt vmcnt(7)
s_barrier
ds_read_b128 v[94:97], v8 offset:5120
v_mfma_f32_16x16x32_bf16 v[62:65], a[36:39], v[98:101], v[62:65]
v_mul_f32 v113, v81, v89
v_cvt_pk_bf16_f32 v114, v106, v107
v_mfma_f32_16x16x32_bf16 v[66:69], a[40:43], v[98:101], v[66:69]
v_cvt_pk_bf16_f32 v115, v108, v109
v_cvt_pk_bf16_f32 v116, v110, v111
v_mfma_f32_16x16x32_bf16 v[70:73], a[44:47], v[98:101], v[70:73]
s_waitcnt lgkmcnt(2)
v_cvt_pk_bf16_f32 v117, v112, v113
v_mfma_f32_16x16x32_bf16 v[58:61], a[48:51], v[102:105], v[58:61]
ds_read_b128 v[98:101], v8 offset:6144
v_mfma_f32_16x16x32_bf16 v[62:65], a[52:55], v[102:105], v[62:65]
v_mfma_f32_16x16x32_bf16 v[66:69], a[56:59], v[102:105], v[66:69]
global_store_dwordx4 v3, v[114:117], s[38:39] sc1
v_mfma_f32_16x16x32_bf16 v[70:73], a[60:63], v[102:105], v[70:73]
s_waitcnt lgkmcnt(2)
v_add_u32 v3, 147456, v3
v_mfma_f32_16x16x32_bf16 v[58:61], a[64:67], v[90:93], v[58:61]
ds_read_b128 v[102:105], v8 offset:7168
v_mfma_f32_16x16x32_bf16 v[62:65], a[68:71], v[90:93], v[62:65]
v_mfma_f32_16x16x32_bf16 v[66:69], a[72:75], v[90:93], v[66:69]
v_mfma_f32_16x16x32_bf16 v[70:73], a[76:79], v[90:93], v[70:73]
s_waitcnt lgkmcnt(2)
v_mfma_f32_16x16x32_bf16 v[58:61], a[80:83], v[94:97], v[58:61]
ds_read_b128 v[90:93], v8 offset:8192
v_mfma_f32_16x16x32_bf16 v[62:65], a[84:87], v[94:97], v[62:65]
v_mfma_f32_16x16x32_bf16 v[66:69], a[88:91], v[94:97], v[66:69]
v_mfma_f32_16x16x32_bf16 v[70:73], a[92:95], v[94:97], v[70:73]
s_waitcnt lgkmcnt(2)
v_mfma_f32_16x16x32_bf16 v[58:61], a[96:99], v[98:101], v[58:61]
ds_read_b128 v[94:97], v8 offset:9216
v_mfma_f32_16x16x32_bf16 v[62:65], a[100:103], v[98:101], v[62:65]
v_mfma_f32_16x16x32_bf16 v[66:69], a[104:107], v[98:101], v[66:69]
v_mfma_f32_16x16x32_bf16 v[70:73], a[108:111], v[98:101], v[70:73]
s_waitcnt lgkmcnt(2)
v_mfma_f32_16x16x32_bf16 v[58:61], a[112:115], v[102:105], v[58:61]
ds_read_b128 v[98:101], v8 offset:10240
v_mfma_f32_16x16x32_bf16 v[62:65], a[116:119], v[102:105], v[62:65]
v_mfma_f32_16x16x32_bf16 v[66:69], a[120:123], v[102:105], v[66:69]
v_mfma_f32_16x16x32_bf16 v[70:73], a[124:127], v[102:105], v[70:73]
s_waitcnt lgkmcnt(2)
v_mfma_f32_16x16x32_bf16 v[58:61], a[128:131], v[90:93], v[58:61]
ds_read_b128 v[102:105], v8 offset:11264
v_mfma_f32_16x16x32_bf16 v[62:65], a[132:135], v[90:93], v[62:65]
v_mfma_f32_16x16x32_bf16 v[66:69], a[136:139], v[90:93], v[66:69]
v_mfma_f32_16x16x32_bf16 v[70:73], a[140:143], v[90:93], v[70:73]
s_waitcnt lgkmcnt(2)
v_mfma_f32_16x16x32_bf16 v[58:61], a[144:147], v[94:97], v[58:61]
ds_read_b128 v[90:93], v8 offset:12288
v_mfma_f32_16x16x32_bf16 v[62:65], a[148:151], v[94:97], v[62:65]
v_mfma_f32_16x16x32_bf16 v[66:69], a[152:155], v[94:97], v[66:69]
v_mfma_f32_16x16x32_bf16 v[70:73], a[156:159], v[94:97], v[70:73]
s_waitcnt lgkmcnt(2)
v_mfma_f32_16x16x32_bf16 v[58:61], a[160:163], v[98:101], v[58:61]
ds_read_b128 v[94:97], v8 offset:13312
v_mfma_f32_16x16x32_bf16 v[62:65], a[164:167], v[98:101], v[62:65]
v_mfma_f32_16x16x32_bf16 v[66:69], a[168:171], v[98:101], v[66:69]
v_mfma_f32_16x16x32_bf16 v[70:73], a[172:175], v[98:101], v[70:73]
s_waitcnt vmcnt(1)
v_sub_u32 v254, v254, v5
v_cmp_lt_u32 vcc, 0x7f, v254
s_waitcnt lgkmcnt(2)
v_mfma_f32_16x16x32_bf16 v[58:61], a[176:179], v[102:105], v[58:61]
ds_read_b128 v[98:101], v8 offset:14336
v_mfma_f32_16x16x32_bf16 v[62:65], a[180:183], v[102:105], v[62:65]
s_xor_b32 s43, s43, 0x10000
v_mfma_f32_16x16x32_bf16 v[66:69], a[184:187], v[102:105], v[66:69]
s_cbranch_vccnz .Lgu_sp7
.Lgu_rt8:
s_add_u32 m0, s43, 0
v_mfma_f32_16x16x32_bf16 v[70:73], a[188:191], v[102:105], v[70:73]
s_waitcnt lgkmcnt(2)
v_mfma_f32_16x16x32_bf16 v[58:61], a[192:195], v[90:93], v[58:61]
s_barrier
ds_read_b128 v[102:105], v8 offset:15360
v_mfma_f32_16x16x32_bf16 v[62:65], a[196:199], v[90:93], v[62:65]
v_add_u32 v5, 1, v5
v_mfma_f32_16x16x32_bf16 v[66:69], a[200:203], v[90:93], v[66:69]
v_mfma_f32_16x16x32_bf16 v[70:73], a[204:207], v[90:93], v[70:73]
s_waitcnt lgkmcnt(2)
global_load_lds_dwordx4 v2, s[38:39] offset:-2048 sc1
v_mfma_f32_16x16x32_bf16 v[58:61], a[208:211], v[94:97], v[58:61]
ds_read_b128 v[90:93], v8 offset:16384
v_mfma_f32_16x16x32_bf16 v[62:65], a[212:215], v[94:97], v[62:65]
v_mfma_f32_16x16x32_bf16 v[66:69], a[216:219], v[94:97], v[66:69]
global_load_lds_dwordx4 v2, s[38:39] offset:-1024 sc1
v_mfma_f32_16x16x32_bf16 v[70:73], a[220:223], v[94:97], v[70:73]
s_waitcnt lgkmcnt(2)
v_mfma_f32_16x16x32_bf16 v[58:61], a[224:227], v[98:101], v[58:61]
ds_read_b128 v[94:97], v8 offset:17408
v_mfma_f32_16x16x32_bf16 v[62:65], a[228:231], v[98:101], v[62:65]
global_load_lds_dwordx4 v2, s[38:39] offset:0 sc1
v_mfma_f32_16x16x32_bf16 v[66:69], a[232:235], v[98:101], v[66:69]
v_mfma_f32_16x16x32_bf16 v[70:73], a[236:239], v[98:101], v[70:73]
s_waitcnt lgkmcnt(2)
v_mfma_f32_16x16x32_bf16 v[58:61], a[240:243], v[102:105], v[58:61]
s_barrier
ds_read_b128 v[98:101], v8 offset:18432
v_mfma_f32_16x16x32_bf16 v[62:65], a[244:247], v[102:105], v[62:65]
global_load_lds_dwordx4 v2, s[38:39] offset:1024 sc1
v_mfma_f32_16x16x32_bf16 v[66:69], a[248:251], v[102:105], v[66:69]
v_mfma_f32_16x16x32_bf16 v[70:73], a[252:255], v[102:105], v[70:73]
s_waitcnt lgkmcnt(2)
v_mfma_f32_16x16x32_bf16 v[58:61], v[26:29], v[90:93], v[58:61]
ds_read_b128 v[102:105], v8 offset:19456
global_load_lds_dwordx4 v2, s[38:39] offset:2048 sc1
v_mfma_f32_16x16x32_bf16 v[62:65], v[30:33], v[90:93], v[62:65]
v_mfma_f32_16x16x32_bf16 v[66:69], v[34:37], v[90:93], v[66:69]
v_mfma_f32_16x16x32_bf16 v[70:73], v[38:41], v[90:93], v[70:73]
s_waitcnt lgkmcnt(2)
global_load_lds_dwordx4 v2, s[38:39] offset:3072 sc1
v_add_u32 v2, 18688, v2
v_mfma_f32_16x16x32_bf16 v[58:61], v[42:45], v[94:97], v[58:61]
ds_read_b128 v[90:93], v8 offset:20480
v_min_u32 v2, v2, v9
v_mfma_f32_16x16x32_bf16 v[62:65], v[46:49], v[94:97], v[62:65]
v_mfma_f32_16x16x32_bf16 v[66:69], v[50:53], v[94:97], v[66:69]
v_mfma_f32_16x16x32_bf16 v[70:73], v[54:57], v[94:97], v[70:73]
s_waitcnt lgkmcnt(2)
v_mfma_f32_16x16x32_bf16 v[74:77], a[0:3], v[98:101], 0
ds_read_b128 v[94:97], v8 offset:21504
v_mfma_f32_16x16x32_bf16 v[78:81], a[4:7], v[98:101], 0
v_mfma_f32_16x16x32_bf16 v[82:85], a[8:11], v[98:101], 0
global_load_dword v254, v4, s[38:39] sc1
v_mfma_f32_16x16x32_bf16 v[86:89], a[12:15], v[98:101], 0
s_waitcnt lgkmcnt(2)
v_mul_f32 v106, v58, v66
v_mul_f32 v107, v59, v67
v_mfma_f32_16x16x32_bf16 v[74:77], a[16:19], v[102:105], v[74:77]
)"
R"(ds_read_b128 v[98:101], v8 offset:22528
v_mul_f32 v108, v60, v68
v_mfma_f32_16x16x32_bf16 v[78:81], a[20:23], v[102:105], v[78:81]
v_mul_f32 v109, v61, v69
v_mul_f32 v110, v62, v70
v_mfma_f32_16x16x32_bf16 v[82:85], a[24:27], v[102:105], v[82:85]
v_mul_f32 v111, v63, v71
v_mul_f32 v112, v64, v72
v_mfma_f32_16x16x32_bf16 v[86:89], a[28:31], v[102:105], v[86:89]
s_waitcnt lgkmcnt(2)
v_mfma_f32_16x16x32_bf16 v[74:77], a[32:35], v[90:93], v[74:77]
s_waitcnt vmcnt(7)
s_barrier
ds_read_b128 v[102:105], v8 offset:23552
v_mfma_f32_16x16x32_bf16 v[78:81], a[36:39], v[90:93], v[78:81]
v_mul_f32 v113, v65, v73
v_cvt_pk_bf16_f32 v114, v106, v107
v_mfma_f32_16x16x32_bf16 v[82:85], a[40:43], v[90:93], v[82:85]
v_cvt_pk_bf16_f32 v115, v108, v109
v_cvt_pk_bf16_f32 v116, v110, v111
v_mfma_f32_16x16x32_bf16 v[86:89], a[44:47], v[90:93], v[86:89]
s_waitcnt lgkmcnt(2)
v_cvt_pk_bf16_f32 v117, v112, v113
v_mfma_f32_16x16x32_bf16 v[74:77], a[48:51], v[94:97], v[74:77]
ds_read_b128 v[90:93], v8 offset:24576
v_mfma_f32_16x16x32_bf16 v[78:81], a[52:55], v[94:97], v[78:81]
v_mfma_f32_16x16x32_bf16 v[82:85], a[56:59], v[94:97], v[82:85]
global_store_dwordx4 v3, v[114:117], s[38:39] sc1
v_mfma_f32_16x16x32_bf16 v[86:89], a[60:63], v[94:97], v[86:89]
s_waitcnt lgkmcnt(2)
v_add_u32 v3, 147456, v3
v_mfma_f32_16x16x32_bf16 v[74:77], a[64:67], v[98:101], v[74:77]
ds_read_b128 v[94:97], v8 offset:25600
v_mfma_f32_16x16x32_bf16 v[78:81], a[68:71], v[98:101], v[78:81]
v_mfma_f32_16x16x32_bf16 v[82:85], a[72:75], v[98:101], v[82:85]
v_mfma_f32_16x16x32_bf16 v[86:89], a[76:79], v[98:101], v[86:89]
s_waitcnt lgkmcnt(2)
v_mfma_f32_16x16x32_bf16 v[74:77], a[80:83], v[102:105], v[74:77]
ds_read_b128 v[98:101], v8 offset:26624
v_mfma_f32_16x16x32_bf16 v[78:81], a[84:87], v[102:105], v[78:81]
v_mfma_f32_16x16x32_bf16 v[82:85], a[88:91], v[102:105], v[82:85]
v_mfma_f32_16x16x32_bf16 v[86:89], a[92:95], v[102:105], v[86:89]
s_waitcnt lgkmcnt(2)
v_mfma_f32_16x16x32_bf16 v[74:77], a[96:99], v[90:93], v[74:77]
ds_read_b128 v[102:105], v8 offset:27648
v_mfma_f32_16x16x32_bf16 v[78:81], a[100:103], v[90:93], v[78:81]
v_mfma_f32_16x16x32_bf16 v[82:85], a[104:107], v[90:93], v[82:85]
v_mfma_f32_16x16x32_bf16 v[86:89], a[108:111], v[90:93], v[86:89]
s_waitcnt lgkmcnt(2)
v_mfma_f32_16x16x32_bf16 v[74:77], a[112:115], v[94:97], v[74:77]
ds_read_b128 v[90:93], v8 offset:28672
v_mfma_f32_16x16x32_bf16 v[78:81], a[116:119], v[94:97], v[78:81]
v_mfma_f32_16x16x32_bf16 v[82:85], a[120:123], v[94:97], v[82:85]
v_mfma_f32_16x16x32_bf16 v[86:89], a[124:127], v[94:97], v[86:89]
s_waitcnt lgkmcnt(2)
v_mfma_f32_16x16x32_bf16 v[74:77], a[128:131], v[98:101], v[74:77]
ds_read_b128 v[94:97], v8 offset:29696
v_mfma_f32_16x16x32_bf16 v[78:81], a[132:135], v[98:101], v[78:81]
v_mfma_f32_16x16x32_bf16 v[82:85], a[136:139], v[98:101], v[82:85]
v_mfma_f32_16x16x32_bf16 v[86:89], a[140:143], v[98:101], v[86:89]
s_waitcnt lgkmcnt(2)
v_mfma_f32_16x16x32_bf16 v[74:77], a[144:147], v[102:105], v[74:77]
ds_read_b128 v[98:101], v8 offset:30720
v_mfma_f32_16x16x32_bf16 v[78:81], a[148:151], v[102:105], v[78:81]
v_mfma_f32_16x16x32_bf16 v[82:85], a[152:155], v[102:105], v[82:85]
v_mfma_f32_16x16x32_bf16 v[86:89], a[156:159], v[102:105], v[86:89]
s_waitcnt lgkmcnt(2)
v_mfma_f32_16x16x32_bf16 v[74:77], a[160:163], v[90:93], v[74:77]
ds_read_b128 v[102:105], v8 offset:31744
v_mfma_f32_16x16x32_bf16 v[78:81], a[164:167], v[90:93], v[78:81]
v_mfma_f32_16x16x32_bf16 v[82:85], a[168:171], v[90:93], v[82:85]
v_mfma_f32_16x16x32_bf16 v[86:89], a[172:175], v[90:93], v[86:89]
s_waitcnt vmcnt(1)
v_sub_u32 v254, v254, v5
v_cmp_lt_u32 vcc, 0x7f, v254
s_waitcnt lgkmcnt(2)
v_mfma_f32_16x16x32_bf16 v[74:77], a[176:179], v[94:97], v[74:77]
ds_read_b128 v[90:93], v8 offset:32768
v_mfma_f32_16x16x32_bf16 v[78:81], a[180:183], v[94:97], v[78:81]
v_mfma_f32_16x16x32_bf16 v[82:85], a[184:187], v[94:97], v[82:85]
s_cbranch_vccnz .Lgu_sp9
.Lgu_rt10:
s_add_u32 m0, s43, 18432
v_mfma_f32_16x16x32_bf16 v[86:89], a[188:191], v[94:97], v[86:89]
s_waitcnt lgkmcnt(2)
v_mfma_f32_16x16x32_bf16 v[74:77], a[192:195], v[98:101], v[74:77]
s_barrier
ds_read_b128 v[94:97], v8 offset:33792
v_mfma_f32_16x16x32_bf16 v[78:81], a[196:199], v[98:101], v[78:81]
v_add_u32 v5, 1, v5
v_mfma_f32_16x16x32_bf16 v[82:85], a[200:203], v[98:101], v[82:85]
v_mfma_f32_16x16x32_bf16 v[86:89], a[204:207], v[98:101], v[86:89]
s_waitcnt lgkmcnt(2)
global_load_lds_dwordx4 v2, s[38:39] offset:-2048 sc1
v_mfma_f32_16x16x32_bf16 v[74:77], a[208:211], v[102:105], v[74:77]
ds_read_b128 v[98:101], v8 offset:34816
v_mfma_f32_16x16x32_bf16 v[78:81], a[212:215], v[102:105], v[78:81]
v_mfma_f32_16x16x32_bf16 v[82:85], a[216:219], v[102:105], v[82:85]
global_load_lds_dwordx4 v2, s[38:39] offset:-1024 sc1
v_mfma_f32_16x16x32_bf16 v[86:89], a[220:223], v[102:105], v[86:89]
s_waitcnt lgkmcnt(2)
v_mfma_f32_16x16x32_bf16 v[74:77], a[224:227], v[90:93], v[74:77]
ds_read_b128 v[102:105], v8 offset:35840
v_mfma_f32_16x16x32_bf16 v[78:81], a[228:231], v[90:93], v[78:81]
global_load_lds_dwordx4 v2, s[38:39] offset:0 sc1
v_mfma_f32_16x16x32_bf16 v[82:85], a[232:235], v[90:93], v[82:85]
v_xor_b32 v8, 0x10000, v8
v_mfma_f32_16x16x32_bf16 v[86:89], a[236:239], v[90:93], v[86:89]
s_waitcnt lgkmcnt(2)
v_mfma_f32_16x16x32_bf16 v[74:77], a[240:243], v[94:97], v[74:77]
s_barrier
ds_read_b128 v[90:93], v8 offset:0
v_mfma_f32_16x16x32_bf16 v[78:81], a[244:247], v[94:97], v[78:81]
global_load_lds_dwordx4 v2, s[38:39] offset:1024 sc1
v_mfma_f32_16x16x32_bf16 v[82:85], a[248:251], v[94:97], v[82:85]
v_mfma_f32_16x16x32_bf16 v[86:89], a[252:255], v[94:97], v[86:89]
s_waitcnt lgkmcnt(2)
v_mfma_f32_16x16x32_bf16 v[74:77], v[26:29], v[98:101], v[74:77]
ds_read_b128 v[94:97], v8 offset:1024
global_load_lds_dwordx4 v2, s[38:39] offset:2048 sc1
v_mfma_f32_16x16x32_bf16 v[78:81], v[30:33], v[98:101], v[78:81]
v_mfma_f32_16x16x32_bf16 v[82:85], v[34:37], v[98:101], v[82:85]
v_mfma_f32_16x16x32_bf16 v[86:89], v[38:41], v[98:101], v[86:89]
s_waitcnt lgkmcnt(2)
global_load_lds_dwordx4 v2, s[38:39] offset:3072 sc1
v_add_u32 v2, 18688, v2
v_mfma_f32_16x16x32_bf16 v[74:77], v[42:45], v[102:105], v[74:77]
ds_read_b128 v[98:101], v8 offset:2048
v_min_u32 v2, v2, v9
v_mfma_f32_16x16x32_bf16 v[78:81], v[46:49], v[102:105], v[78:81]
v_mfma_f32_16x16x32_bf16 v[82:85], v[50:53], v[102:105], v[82:85]
v_mfma_f32_16x16x32_bf16 v[86:89], v[54:57], v[102:105], v[86:89]
s_sub_u32 s70, s70, 1
s_cbranch_scc0 .Lgu_top11
s_nop 4
v_mul_f32 v106, v74, v82
v_mul_f32 v107, v75, v83
v_mul_f32 v108, v76, v84
v_mul_f32 v109, v77, v85
v_mul_f32 v110, v78, v86
v_mul_f32 v111, v79, v87
v_mul_f32 v112, v80, v88
v_mul_f32 v113, v81, v89
v_cvt_pk_bf16_f32 v114, v106, v107
v_cvt_pk_bf16_f32 v115, v108, v109
v_cvt_pk_bf16_f32 v116, v110, v111
v_cvt_pk_bf16_f32 v117, v112, v113
global_store_dwordx4 v3, v[114:117], s[38:39] sc1
s_waitcnt vmcnt(1) lgkmcnt(0)
s_barrier
v_mov_b32 v186, 192
ds_read_b32 v186, v186 offset:36864
s_waitcnt vmcnt(0)
s_barrier
s_waitcnt lgkmcnt(0)
v_readfirstlane_b32 s60, v186
s_nop 3
s_max_i32 s60, s60, 2
s_min_i32 s60, s60, 66
s_add_u32 s60, s60, 0
s_branch .Lgu_fin
.Lgu_w4:
s_add_u32 s84, s52, 18688
s_addc_u32 s85, s53, 0
s_add_u32 s86, s50, 110592
s_addc_u32 s87, s51, 0
s_mov_b32 s80, 0
s_mov_b64 s[76:77], 0
s_mov_b32 s72, -14
v_add_u32 v185, -1, v17
v_add_u32 v125, -1, s37
v_add_u32 v182, -1, s37
v_add_u32 v183, -1, s37
v_add_u32 v184, -1, s37
v_add_u32 v254, -1, v5
s_mov_b32 s70, 32
s_waitcnt vmcnt(0) lgkmcnt(0)
s_nop 7
.p2align 6
.Lgu_top16:
s_waitcnt lgkmcnt(2)
v_mfma_f32_16x16x32_bf16 v[58:61], a[0:3], v[90:93], 0
s_sub_u32 s54, s54, s80
ds_read_b128 v[102:105], v8 offset:3072
v_add_f32_dpp v164, v168, v164 row_ror:8 row_mask:0xf bank_mask:0xf
v_mfma_f32_16x16x32_bf16 v[62:65], a[4:7], v[90:93], 0
s_subb_u32 s55, s55, 0
global_load_dword v185, v16, s[38:39] sc1
v_add_f32_dpp v165, v169, v165 row_ror:8 row_mask:0xf bank_mask:0xf
v_mfma_f32_16x16x32_bf16 v[66:69], a[8:11], v[90:93], 0
s_add_u32 s84, s52, 18688
v_fma_f32 v162, v162, v186, v170
v_fma_f32 v163, v163, v186, v171
v_mfma_f32_16x16x32_bf16 v[70:73], a[12:15], v[90:93], 0
s_waitcnt lgkmcnt(2)
v_mul_f32 v106, v74, v82
v_mul_f32 v107, v75, v83
v_mfma_f32_16x16x32_bf16 v[58:61], a[16:19], v[94:97], v[58:61]
s_addc_u32 s85, s53, 0
ds_read_b128 v[90:93], v8 offset:4096
v_mul_f32 v108, v76, v84
v_mfma_f32_16x16x32_bf16 v[62:65], a[20:23], v[94:97], v[62:65]
s_add_u32 s86, s50, 110592
v_mul_f32 v109, v77, v85
v_mul_f32 v110, v78, v86
v_mfma_f32_16x16x32_bf16 v[66:69], a[24:27], v[94:97], v[66:69]
s_addc_u32 s87, s51, 0
v_mul_f32 v111, v79, v87
v_mul_f32 v112, v80, v88
v_mfma_f32_16x16x32_bf16 v[70:73], a[28:31], v[94:97], v[70:73]
s_waitcnt lgkmcnt(2)
v_mfma_f32_16x16x32_bf16 v[58:61], a[32:35], v[98:101], v[58:61]
s_waitcnt vmcnt(8)
s_barrier
ds_read_b128 v[94:97], v8 offset:5120
v_mfma_f32_16x16x32_bf16 v[62:65], a[36:39], v[98:101], v[62:65]
s_add_u32 s72, s72, 1
v_mul_f32 v113, v81, v89
v_cvt_pk_bf16_f32 v114, v106, v107
v_mfma_f32_16x16x32_bf16 v[66:69], a[40:43], v[98:101], v[66:69]
s_cmp_le_i32 s60, s72
v_cvt_pk_bf16_f32 v115, v108, v109
v_cvt_pk_bf16_f32 v116, v110, v111
v_mfma_f32_16x16x32_bf16 v[70:73], a[44:47], v[98:101], v[70:73]
s_waitcnt lgkmcnt(2)
v_cvt_pk_bf16_f32 v117, v112, v113
v_fma_f32 v164, v164, v186, v172
v_mfma_f32_16x16x32_bf16 v[58:61], a[48:51], v[102:105], v[58:61]
s_cselect_b64 s[76:77], -1, 0
ds_read_b128 v[98:101], v8 offset:6144
v_fma_f32 v165, v165, v186, v173
v_mfma_f32_16x16x32_bf16 v[62:65], a[52:55], v[102:105], v[62:65]
v_cvt_pk_bf16_f32 v178, v162, v163
v_cvt_pk_bf16_f32 v179, v164, v165
v_mfma_f32_16x16x32_bf16 v[66:69], a[56:59], v[102:105], v[66:69]
ds_write_b64 v14, v[178:179]
v_mfma_f32_16x16x32_bf16 v[70:73], a[60:63], v[102:105], v[70:73]
s_waitcnt lgkmcnt(3)
global_store_dwordx4 v3, v[114:117], s[38:39] sc1
v_mfma_f32_16x16x32_bf16 v[58:61], a[64:67], v[90:93], v[58:61]
ds_read_b128 v[102:105], v8 offset:7168
v_add_u32 v3, 147456, v3
v_mfma_f32_16x16x32_bf16 v[62:65], a[68:71], v[90:93], v[62:65]
s_waitcnt vmcnt(8)
ds_read_b64 v[180:181], v15
s_mov_b64 exec, 1
global_store_dword v18, v184, s[38:39] sc1
s_mov_b64 exec, -1
v_mfma_f32_16x16x32_bf16 v[66:69], a[72:75], v[90:93], v[66:69]
v_mfma_f32_16x16x32_bf16 v[70:73], a[76:79], v[90:93], v[70:73]
s_waitcnt lgkmcnt(4)
v_mfma_f32_16x16x32_bf16 v[58:61], a[80:83], v[94:97], v[58:61]
ds_read_b128 v[90:93], v8 offset:8192
v_mfma_f32_16x16x32_bf16 v[62:65], a[84:87], v[94:97], v[62:65]
v_mfma_f32_16x16x32_bf16 v[66:69], a[88:91], v[94:97], v[66:69]
v_mfma_f32_16x16x32_bf16 v[70:73], a[92:95], v[94:97], v[70:73]
s_waitcnt lgkmcnt(4)
v_mfma_f32_16x16x32_bf16 v[58:61], a[96:99], v[98:101], v[58:61]
ds_read_b128 v[94:97], v8 offset:9216
v_mfma_f32_16x16x32_bf16 v[62:65], a[100:103], v[98:101], v[62:65]
s_waitcnt lgkmcnt(2)
global_store_dwordx2 v13, v[180:181], s[56:57] sc1
v_mfma_f32_16x16x32_bf16 v[66:69], a[104:107], v[98:101], v[66:69]
v_mov_b32 v183, v125
v_mfma_f32_16x16x32_bf16 v[70:73], a[108:111], v[98:101], v[70:73]
s_waitcnt vmcnt(8)
v_add_f32 v156, v156, v158
s_waitcnt vmcnt(7)
v_lshlrev_b32 v162, 16, v144
v_add_f32 v157, v157, v159
v_mfma_f32_16x16x32_bf16 v[58:61], a[112:115], v[102:105], v[58:61]
ds_read_b128 v[98:101], v8 offset:10240
v_and_b32 v163, 0xffff0000, v144
v_mul_f32 v156, 0x3ae38e39, v156
v_mfma_f32_16x16x32_bf16 v[62:65], a[116:119], v[102:105], v[62:65]
v_lshlrev_b32 v164, 16, v145
v_mul_f32 v157, 0x3ae38e39, v157
v_and_b32 v165, 0xffff0000, v145
v_mfma_f32_16x16x32_bf16 v[66:69], a[120:123], v[102:105], v[66:69]
v_fma_f32 v187, -v156, v156, v157
v_lshlrev_b32 v166, 16, v146
v_max_f32 v187, 0, v187
v_mfma_f32_16x16x32_bf16 v[70:73], a[124:127], v[102:105], v[70:73]
s_waitcnt lgkmcnt(2)
v_and_b32 v167, 0xffff0000, v146
v_cvt_pk_bf16_f32 v187, v187, v187
v_lshlrev_b32 v168, 16, v147
v_mfma_f32_16x16x32_bf16 v[58:61], a[128:131], v[90:93], v[58:61]
ds_read_b128 v[102:105], v8 offset:11264
v_lshlrev_b32 v187, 16, v187
v_and_b32 v169, 0xffff0000, v147
v_mfma_f32_16x16x32_bf16 v[62:65], a[132:135], v[90:93], v[62:65]
s_waitcnt vmcnt(6)
v_add_f32 v187, 0x3727c5ac, v187
v_lshlrev_b32 v170, 16, v148
v_cvt_pk_bf16_f32 v187, v187, v187
v_mfma_f32_16x16x32_bf16 v[66:69], a[136:139], v[90:93], v[66:69]
v_and_b32 v171, 0xffff0000, v148
v_lshlrev_b32 v187, 16, v187
v_lshlrev_b32 v172, 16, v149
v_mfma_f32_16x16x32_bf16 v[70:73], a[140:143], v[90:93], v[70:73]
s_waitcnt lgkmcnt(2)
v_sqrt_f32 v187, v187
v_and_b32 v173, 0xffff0000, v149
v_mfma_f32_16x16x32_bf16 v[58:61], a[144:147], v[94:97], v[58:61]
ds_read_b128 v[90:93], v8 offset:12288
v_cvt_pk_bf16_f32 v187, v187, v187
v_mfma_f32_16x16x32_bf16 v[62:65], a[148:151], v[94:97], v[62:65]
v_lshlrev_b32 v174, 16, v150
v_lshlrev_b32 v187, 16, v187
v_mfma_f32_16x16x32_bf16 v[66:69], a[152:155], v[94:97], v[66:69]
v_and_b32 v175, 0xffff0000, v150
v_mfma_f32_16x16x32_bf16 v[70:73], a[156:159], v[94:97], v[70:73]
s_waitcnt lgkmcnt(2)
v_rcp_f32 v186, v187
v_mfma_f32_16x16x32_bf16 v[58:61], a[160:163], v[98:101], v[58:61]
ds_read_b128 v[94:97], v8 offset:13312
v_lshlrev_b32 v176, 16, v151
v_mfma_f32_16x16x32_bf16 v[62:65], a[164:167], v[98:101], v[62:65]
v_mul_f32 v186, v186, v186
v_and_b32 v177, 0xffff0000, v151
v_mfma_f32_16x16x32_bf16 v[66:69], a[168:171], v[98:101], v[66:69]
v_add_f32 v162, v162, v170
v_add_f32 v163, v163, v171
v_mfma_f32_16x16x32_bf16 v[70:73], a[172:175], v[98:101], v[70:73]
s_waitcnt lgkmcnt(2)
v_add_f32 v164, v164, v172
v_add_f32 v165, v165, v173
v_mfma_f32_16x16x32_bf16 v[58:61], a[176:179], v[102:105], v[58:61]
ds_read_b128 v[98:101], v8 offset:14336
v_add_f32 v166, v166, v174
v_mfma_f32_16x16x32_bf16 v[62:65], a[180:183], v[102:105], v[62:65]
v_add_f32 v167, v167, v175
v_add_f32 v168, v168, v176
v_mfma_f32_16x16x32_bf16 v[66:69], a[184:187], v[102:105], v[66:69]
s_waitcnt vmcnt(5)
v_add_f32 v169, v169, v177
v_lshlrev_b32 v170, 16, v152
v_mfma_f32_16x16x32_bf16 v[70:73], a[188:191], v[102:105], v[70:73]
s_waitcnt lgkmcnt(2)
v_mfma_f32_16x16x32_bf16 v[58:61], a[192:195], v[90:93], v[58:61]
s_barrier
ds_read_b128 v[102:105], v8 offset:15360
v_mfma_f32_16x16x32_bf16 v[62:65], a[196:199], v[90:93], v[62:65]
s_waitcnt vmcnt(3)
v_sub_u32 v185, v185, v17
v_cmp_lt_u32 vcc, 0x7f, v185
v_mfma_f32_16x16x32_bf16 v[66:69], a[200:203], v[90:93], v[66:69]
v_and_b32 v171, 0xffff0000, v152
v_lshlrev_b32 v172, 16, v153
v_mfma_f32_16x16x32_bf16 v[70:73], a[204:207], v[90:93], v[70:73]
s_waitcnt lgkmcnt(2)
v_and_b32 v173, 0xffff0000, v153
v_lshlrev_b32 v174, 16, v154
v_mfma_f32_16x16x32_bf16 v[58:61], a[208:211], v[94:97], v[58:61]
ds_read_b128 v[90:93], v8 offset:16384
v_and_b32 v175, 0xffff0000, v154
v_mfma_f32_16x16x32_bf16 v[62:65], a[212:215], v[94:97], v[62:65]
s_and_b64 s[88:89], vcc, s[76:77]
s_cbranch_scc1 .Lgu_sp12
.Lgu_rt13:
v_mfma_f32_16x16x32_bf16 v[66:69], a[216:219], v[94:97], v[66:69]
s_cmp_eq_u64 vcc, 0
v_lshlrev_b32 v176, 16, v155
v_and_b32 v177, 0xffff0000, v155
v_mfma_f32_16x16x32_bf16 v[70:73], a[220:223], v[94:97], v[70:73]
s_waitcnt lgkmcnt(2)
v_add_f32 v162, v162, v170
v_add_f32 v163, v163, v171
v_mfma_f32_16x16x32_bf16 v[58:61], a[224:227], v[98:101], v[58:61]
s_cselect_b64 s[52:53], s[84:85], s[52:53]
ds_read_b128 v[94:97], v8 offset:17408
v_add_f32 v164, v164, v172
v_mfma_f32_16x16x32_bf16 v[62:65], a[228:231], v[98:101], v[62:65]
s_cselect_b64 s[50:51], s[86:87], s[50:51]
global_load_dwordx2 v[138:139], v12, s[52:53] offset:0 sc1
v_add_f32 v165, v165, v173
v_mfma_f32_16x16x32_bf16 v[66:69], a[232:235], v[98:101], v[66:69]
s_cselect_b64 s[56:57], s[54:55], s[48:49]
global_load_dwordx2 v[140:141], v12, s[52:53] offset:128 sc1
v_add_f32 v166, v166, v174
v_mfma_f32_16x16x32_bf16 v[70:73], a[236:239], v[98:101], v[70:73]
s_waitcnt lgkmcnt(2)
v_mfma_f32_16x16x32_bf16 v[58:61], a[240:243], v[102:105], v[58:61]
s_barrier
ds_read_b128 v[98:101], v8 offset:18432
v_mfma_f32_16x16x32_bf16 v[62:65], a[244:247], v[102:105], v[62:65]
s_cselect_b32 s82, 1, 0
global_load_dwordx4 v[126:129], v10, s[50:51] offset:0 sc1
v_add_f32 v167, v167, v175
v_mfma_f32_16x16x32_bf16 v[66:69], a[248:251], v[102:105], v[66:69]
s_add_u32 s60, s60, s82
global_load_dwordx4 v[130:133], v10, s[50:51] offset:1024 sc1
v_add_f32 v168, v168, v176
v_mfma_f32_16x16x32_bf16 v[70:73], a[252:255], v[102:105], v[70:73]
s_waitcnt lgkmcnt(2)
global_load_dwordx4 v[134:137], v10, s[50:51] offset:2048 sc1
v_add_f32 v169, v169, v177
v_mfma_f32_16x16x32_bf16 v[58:61], v[26:29], v[90:93], v[58:61]
s_add_u32 s83, s60, s37
)"
R"(ds_read_b128 v[102:105], v8 offset:19456
global_load_dwordx2 v[142:143], v11, s[52:53] sc1
v_mfma_f32_16x16x32_bf16 v[62:65], v[30:33], v[90:93], v[62:65]
s_cmp_lt_i32 s60, 32
v_add_u32 v17, s82, v17
v_add_u32 v125, -1, s83
v_mfma_f32_16x16x32_bf16 v[66:69], v[34:37], v[90:93], v[66:69]
s_cselect_b64 s[54:55], s[44:45], s[46:47]
v_lshlrev_b32 v170, 16, v160
v_and_b32 v171, 0xffff0000, v160
v_mfma_f32_16x16x32_bf16 v[70:73], v[38:41], v[90:93], v[70:73]
s_waitcnt lgkmcnt(2)
v_lshlrev_b32 v172, 16, v161
v_and_b32 v173, 0xffff0000, v161
v_mfma_f32_16x16x32_bf16 v[58:61], v[42:45], v[94:97], v[58:61]
s_mul_i32 s80, s60, 18432
ds_read_b128 v[90:93], v8 offset:20480
v_add_f32_dpp v162, v166, v162 row_ror:8 row_mask:0xf bank_mask:0xf
v_mfma_f32_16x16x32_bf16 v[62:65], v[46:49], v[94:97], v[62:65]
s_sub_u32 s54, s54, s80
v_add_f32_dpp v163, v167, v163 row_ror:8 row_mask:0xf bank_mask:0xf
v_add_f32_dpp v164, v168, v164 row_ror:8 row_mask:0xf bank_mask:0xf
v_mfma_f32_16x16x32_bf16 v[66:69], v[50:53], v[94:97], v[66:69]
s_subb_u32 s55, s55, 0
v_add_f32_dpp v165, v169, v165 row_ror:8 row_mask:0xf bank_mask:0xf
v_fma_f32 v162, v162, v186, v170
v_mfma_f32_16x16x32_bf16 v[70:73], v[54:57], v[94:97], v[70:73]
s_waitcnt lgkmcnt(2)
v_fma_f32 v163, v163, v186, v171
v_fma_f32 v164, v164, v186, v172
v_mfma_f32_16x16x32_bf16 v[74:77], a[0:3], v[98:101], 0
s_add_u32 s84, s52, 18688
ds_read_b128 v[94:97], v8 offset:21504
v_fma_f32 v165, v165, v186, v173
v_mfma_f32_16x16x32_bf16 v[78:81], a[4:7], v[98:101], 0
s_addc_u32 s85, s53, 0
global_load_dword v185, v16, s[38:39] sc1
v_cvt_pk_bf16_f32 v178, v162, v163
v_mfma_f32_16x16x32_bf16 v[82:85], a[8:11], v[98:101], 0
s_add_u32 s86, s50, 110592
v_cvt_pk_bf16_f32 v179, v164, v165
v_mfma_f32_16x16x32_bf16 v[86:89], a[12:15], v[98:101], 0
s_waitcnt lgkmcnt(2)
v_mul_f32 v106, v58, v66
v_mul_f32 v107, v59, v67
v_mfma_f32_16x16x32_bf16 v[74:77], a[16:19], v[102:105], v[74:77]
s_addc_u32 s87, s51, 0
ds_read_b128 v[98:101], v8 offset:22528
v_mul_f32 v108, v60, v68
v_mfma_f32_16x16x32_bf16 v[78:81], a[20:23], v[102:105], v[78:81]
s_add_u32 s72, s72, 1
v_mul_f32 v109, v61, v69
v_mul_f32 v110, v62, v70
v_mfma_f32_16x16x32_bf16 v[82:85], a[24:27], v[102:105], v[82:85]
s_cmp_le_i32 s60, s72
v_mul_f32 v111, v63, v71
v_mul_f32 v112, v64, v72
v_mfma_f32_16x16x32_bf16 v[86:89], a[28:31], v[102:105], v[86:89]
s_waitcnt lgkmcnt(2)
v_mfma_f32_16x16x32_bf16 v[74:77], a[32:35], v[90:93], v[74:77]
s_waitcnt vmcnt(9)
s_barrier
ds_read_b128 v[102:105], v8 offset:23552
v_mfma_f32_16x16x32_bf16 v[78:81], a[36:39], v[90:93], v[78:81]
s_cselect_b64 s[76:77], -1, 0
v_mul_f32 v113, v65, v73
v_cvt_pk_bf16_f32 v114, v106, v107
v_mfma_f32_16x16x32_bf16 v[82:85], a[40:43], v[90:93], v[82:85]
v_cvt_pk_bf16_f32 v115, v108, v109
v_cvt_pk_bf16_f32 v116, v110, v111
v_mfma_f32_16x16x32_bf16 v[86:89], a[44:47], v[90:93], v[86:89]
s_waitcnt vmcnt(7)
v_cvt_pk_bf16_f32 v117, v112, v113
s_mov_b64 exec, 1
global_store_dword v18, v183, s[38:39] sc1
s_mov_b64 exec, -1
s_waitcnt lgkmcnt(2)
v_mfma_f32_16x16x32_bf16 v[74:77], a[48:51], v[94:97], v[74:77]
ds_read_b128 v[90:93], v8 offset:24576
v_mfma_f32_16x16x32_bf16 v[78:81], a[52:55], v[94:97], v[78:81]
ds_write_b64 v14, v[178:179]
v_mfma_f32_16x16x32_bf16 v[82:85], a[56:59], v[94:97], v[82:85]
ds_read_b64 v[180:181], v15
v_mfma_f32_16x16x32_bf16 v[86:89], a[60:63], v[94:97], v[86:89]
s_waitcnt lgkmcnt(4)
global_store_dwordx4 v3, v[114:117], s[38:39] sc1
v_mfma_f32_16x16x32_bf16 v[74:77], a[64:67], v[98:101], v[74:77]
ds_read_b128 v[94:97], v8 offset:25600
v_add_u32 v3, 147456, v3
v_mfma_f32_16x16x32_bf16 v[78:81], a[68:71], v[98:101], v[78:81]
v_mfma_f32_16x16x32_bf16 v[82:85], a[72:75], v[98:101], v[82:85]
v_mfma_f32_16x16x32_bf16 v[86:89], a[76:79], v[98:101], v[86:89]
s_waitcnt lgkmcnt(4)
v_mfma_f32_16x16x32_bf16 v[74:77], a[80:83], v[102:105], v[74:77]
ds_read_b128 v[98:101], v8 offset:26624
v_mfma_f32_16x16x32_bf16 v[78:81], a[84:87], v[102:105], v[78:81]
s_waitcnt lgkmcnt(2)
global_store_dwordx2 v13, v[180:181], s[58:59] sc1
v_mfma_f32_16x16x32_bf16 v[82:85], a[88:91], v[102:105], v[82:85]
v_mov_b32 v184, v182
v_mfma_f32_16x16x32_bf16 v[86:89], a[92:95], v[102:105], v[86:89]
v_mfma_f32_16x16x32_bf16 v[74:77], a[96:99], v[90:93], v[74:77]
ds_read_b128 v[102:105], v8 offset:27648
v_mfma_f32_16x16x32_bf16 v[78:81], a[100:103], v[90:93], v[78:81]
v_mfma_f32_16x16x32_bf16 v[82:85], a[104:107], v[90:93], v[82:85]
v_mfma_f32_16x16x32_bf16 v[86:89], a[108:111], v[90:93], v[86:89]
s_waitcnt vmcnt(8)
v_add_f32 v138, v138, v140
s_waitcnt vmcnt(7)
v_lshlrev_b32 v162, 16, v126
v_add_f32 v139, v139, v141
s_waitcnt lgkmcnt(2)
v_mfma_f32_16x16x32_bf16 v[74:77], a[112:115], v[94:97], v[74:77]
ds_read_b128 v[90:93], v8 offset:28672
v_and_b32 v163, 0xffff0000, v126
v_mul_f32 v138, 0x3ae38e39, v138
v_mfma_f32_16x16x32_bf16 v[78:81], a[116:119], v[94:97], v[78:81]
v_lshlrev_b32 v164, 16, v127
v_mul_f32 v139, 0x3ae38e39, v139
v_and_b32 v165, 0xffff0000, v127
v_mfma_f32_16x16x32_bf16 v[82:85], a[120:123], v[94:97], v[82:85]
v_fma_f32 v187, -v138, v138, v139
v_lshlrev_b32 v166, 16, v128
v_max_f32 v187, 0, v187
v_mfma_f32_16x16x32_bf16 v[86:89], a[124:127], v[94:97], v[86:89]
s_waitcnt lgkmcnt(2)
v_and_b32 v167, 0xffff0000, v128
v_cvt_pk_bf16_f32 v187, v187, v187
v_lshlrev_b32 v168, 16, v129
v_mfma_f32_16x16x32_bf16 v[74:77], a[128:131], v[98:101], v[74:77]
ds_read_b128 v[94:97], v8 offset:29696
v_lshlrev_b32 v187, 16, v187
v_and_b32 v169, 0xffff0000, v129
v_mfma_f32_16x16x32_bf16 v[78:81], a[132:135], v[98:101], v[78:81]
s_waitcnt vmcnt(6)
v_add_f32 v187, 0x3727c5ac, v187
v_lshlrev_b32 v170, 16, v130
v_cvt_pk_bf16_f32 v187, v187, v187
v_mfma_f32_16x16x32_bf16 v[82:85], a[136:139], v[98:101], v[82:85]
v_and_b32 v171, 0xffff0000, v130
v_lshlrev_b32 v187, 16, v187
v_lshlrev_b32 v172, 16, v131
v_mfma_f32_16x16x32_bf16 v[86:89], a[140:143], v[98:101], v[86:89]
s_waitcnt lgkmcnt(2)
v_sqrt_f32 v187, v187
v_and_b32 v173, 0xffff0000, v131
v_mfma_f32_16x16x32_bf16 v[74:77], a[144:147], v[102:105], v[74:77]
ds_read_b128 v[98:101], v8 offset:30720
v_cvt_pk_bf16_f32 v187, v187, v187
v_mfma_f32_16x16x32_bf16 v[78:81], a[148:151], v[102:105], v[78:81]
v_lshlrev_b32 v174, 16, v132
v_lshlrev_b32 v187, 16, v187
v_mfma_f32_16x16x32_bf16 v[82:85], a[152:155], v[102:105], v[82:85]
v_and_b32 v175, 0xffff0000, v132
v_mfma_f32_16x16x32_bf16 v[86:89], a[156:159], v[102:105], v[86:89]
s_waitcnt lgkmcnt(2)
v_rcp_f32 v186, v187
v_mfma_f32_16x16x32_bf16 v[74:77], a[160:163], v[90:93], v[74:77]
ds_read_b128 v[102:105], v8 offset:31744
v_lshlrev_b32 v176, 16, v133
v_mfma_f32_16x16x32_bf16 v[78:81], a[164:167], v[90:93], v[78:81]
v_mul_f32 v186, v186, v186
v_and_b32 v177, 0xffff0000, v133
v_mfma_f32_16x16x32_bf16 v[82:85], a[168:171], v[90:93], v[82:85]
v_add_f32 v162, v162, v170
v_add_f32 v163, v163, v171
v_mfma_f32_16x16x32_bf16 v[86:89], a[172:175], v[90:93], v[86:89]
s_waitcnt lgkmcnt(2)
v_add_f32 v164, v164, v172
v_add_f32 v165, v165, v173
v_mfma_f32_16x16x32_bf16 v[74:77], a[176:179], v[94:97], v[74:77]
ds_read_b128 v[90:93], v8 offset:32768
v_add_f32 v166, v166, v174
v_mfma_f32_16x16x32_bf16 v[78:81], a[180:183], v[94:97], v[78:81]
v_add_f32 v167, v167, v175
v_add_f32 v168, v168, v176
v_mfma_f32_16x16x32_bf16 v[82:85], a[184:187], v[94:97], v[82:85]
s_waitcnt vmcnt(5)
v_add_f32 v169, v169, v177
v_lshlrev_b32 v170, 16, v134
v_mfma_f32_16x16x32_bf16 v[86:89], a[188:191], v[94:97], v[86:89]
s_waitcnt lgkmcnt(2)
v_mfma_f32_16x16x32_bf16 v[74:77], a[192:195], v[98:101], v[74:77]
s_barrier
ds_read_b128 v[94:97], v8 offset:33792
v_mfma_f32_16x16x32_bf16 v[78:81], a[196:199], v[98:101], v[78:81]
s_waitcnt vmcnt(3)
v_sub_u32 v185, v185, v17
v_cmp_lt_u32 vcc, 0x7f, v185
v_mfma_f32_16x16x32_bf16 v[82:85], a[200:203], v[98:101], v[82:85]
v_and_b32 v171, 0xffff0000, v134
v_lshlrev_b32 v172, 16, v135
v_mfma_f32_16x16x32_bf16 v[86:89], a[204:207], v[98:101], v[86:89]
s_waitcnt lgkmcnt(2)
v_and_b32 v173, 0xffff0000, v135
v_lshlrev_b32 v174, 16, v136
v_mfma_f32_16x16x32_bf16 v[74:77], a[208:211], v[102:105], v[74:77]
ds_read_b128 v[98:101], v8 offset:34816
v_and_b32 v175, 0xffff0000, v136
v_mfma_f32_16x16x32_bf16 v[78:81], a[212:215], v[102:105], v[78:81]
s_and_b64 s[88:89], vcc, s[76:77]
s_cbranch_scc1 .Lgu_sp14
.Lgu_rt15:
v_mfma_f32_16x16x32_bf16 v[82:85], a[216:219], v[102:105], v[82:85]
s_cmp_eq_u64 vcc, 0
v_lshlrev_b32 v176, 16, v137
v_and_b32 v177, 0xffff0000, v137
v_mfma_f32_16x16x32_bf16 v[86:89], a[220:223], v[102:105], v[86:89]
s_waitcnt lgkmcnt(2)
v_add_f32 v162, v162, v170
v_add_f32 v163, v163, v171
v_mfma_f32_16x16x32_bf16 v[74:77], a[224:227], v[90:93], v[74:77]
s_cselect_b64 s[52:53], s[84:85], s[52:53]
ds_read_b128 v[102:105], v8 offset:35840
v_add_f32 v164, v164, v172
v_mfma_f32_16x16x32_bf16 v[78:81], a[228:231], v[90:93], v[78:81]
s_cselect_b64 s[50:51], s[86:87], s[50:51]
global_load_dwordx2 v[156:157], v12, s[52:53] offset:0 sc1
v_add_f32 v165, v165, v173
v_mfma_f32_16x16x32_bf16 v[82:85], a[232:235], v[90:93], v[82:85]
s_cselect_b64 s[58:59], s[54:55], s[48:49]
v_xor_b32 v8, 0x10000, v8
global_load_dwordx2 v[158:159], v12, s[52:53] offset:128 sc1
v_mfma_f32_16x16x32_bf16 v[86:89], a[236:239], v[90:93], v[86:89]
s_waitcnt lgkmcnt(2)
v_mfma_f32_16x16x32_bf16 v[74:77], a[240:243], v[94:97], v[74:77]
s_barrier
ds_read_b128 v[90:93], v8 offset:0
v_mfma_f32_16x16x32_bf16 v[78:81], a[244:247], v[94:97], v[78:81]
s_cselect_b32 s82, 1, 0
global_load_dwordx4 v[144:147], v10, s[50:51] offset:0 sc1
v_add_f32 v166, v166, v174
v_mfma_f32_16x16x32_bf16 v[82:85], a[248:251], v[94:97], v[82:85]
s_add_u32 s60, s60, s82
global_load_dwordx4 v[148:151], v10, s[50:51] offset:1024 sc1
v_add_f32 v167, v167, v175
v_mfma_f32_16x16x32_bf16 v[86:89], a[252:255], v[94:97], v[86:89]
s_waitcnt lgkmcnt(2)
global_load_dwordx4 v[152:155], v10, s[50:51] offset:2048 sc1
v_add_f32 v168, v168, v176
v_mfma_f32_16x16x32_bf16 v[74:77], v[26:29], v[98:101], v[74:77]
s_add_u32 s83, s60, s37
ds_read_b128 v[94:97], v8 offset:1024
global_load_dwordx2 v[160:161], v11, s[52:53] sc1
v_mfma_f32_16x16x32_bf16 v[78:81], v[30:33], v[98:101], v[78:81]
s_cmp_lt_i32 s60, 32
v_add_u32 v17, s82, v17
v_add_u32 v182, -1, s83
v_mfma_f32_16x16x32_bf16 v[82:85], v[34:37], v[98:101], v[82:85]
s_cselect_b64 s[54:55], s[44:45], s[46:47]
v_add_f32 v169, v169, v177
v_lshlrev_b32 v170, 16, v142
v_mfma_f32_16x16x32_bf16 v[86:89], v[38:41], v[98:101], v[86:89]
s_waitcnt lgkmcnt(2)
v_and_b32 v171, 0xffff0000, v142
v_lshlrev_b32 v172, 16, v143
v_mfma_f32_16x16x32_bf16 v[74:77], v[42:45], v[102:105], v[74:77]
s_mul_i32 s80, s60, 18432
ds_read_b128 v[98:101], v8 offset:2048
v_and_b32 v173, 0xffff0000, v143
v_mfma_f32_16x16x32_bf16 v[78:81], v[46:49], v[102:105], v[78:81]
v_add_f32_dpp v162, v166, v162 row_ror:8 row_mask:0xf bank_mask:0xf
v_add_f32_dpp v163, v167, v163 row_ror:8 row_mask:0xf bank_mask:0xf
v_mfma_f32_16x16x32_bf16 v[82:85], v[50:53], v[102:105], v[82:85]
v_mfma_f32_16x16x32_bf16 v[86:89], v[54:57], v[102:105], v[86:89]
s_sub_u32 s70, s70, 1
s_cbranch_scc0 .Lgu_top16
s_nop 4
v_mul_f32 v106, v74, v82
v_mul_f32 v107, v75, v83
v_mul_f32 v108, v76, v84
v_mul_f32 v109, v77, v85
v_mul_f32 v110, v78, v86
v_mul_f32 v111, v79, v87
v_mul_f32 v112, v80, v88
v_mul_f32 v113, v81, v89
v_cvt_pk_bf16_f32 v114, v106, v107
v_cvt_pk_bf16_f32 v115, v108, v109
v_cvt_pk_bf16_f32 v116, v110, v111
v_cvt_pk_bf16_f32 v117, v112, v113
global_store_dwordx4 v3, v[114:117], s[38:39] sc1
v_mov_b32 v187, s60
ds_write_b32 v1, v187 offset:36864
v_subrev_u32 v17, s60, v17
s_waitcnt vmcnt(1) lgkmcnt(0)
s_barrier
v_mov_b32 v186, 192
ds_read_b32 v186, v186 offset:36864
s_waitcnt vmcnt(0)
s_barrier
s_waitcnt lgkmcnt(0)
v_readfirstlane_b32 s60, v186
s_nop 3
s_max_i32 s60, s60, 2
s_min_i32 s60, s60, 66
s_add_u32 s60, s60, 1
s_branch .Lgu_fin
.Lgu_fin:
s_cmp_gt_i32 s60, 63
s_cbranch_scc1 .Lgu_end
v_add_u32 v188, s60, v17
global_load_dword v185, v16, s[38:39] sc1
s_cmp_lt_i32 s60, 32
s_cselect_b64 s[54:55], s[44:45], s[46:47]
s_mul_i32 s80, s60, 18432
s_sub_u32 s54, s54, s80
s_subb_u32 s55, s55, 0
s_mov_b64 s[56:57], s[54:55]
s_mul_i32 s80, s60, 110592
s_mul_i32 s81, s60, 18688
s_mov_b64 s[50:51], s[38:39]
s_mov_b64 s[52:53], s[38:39]
s_add_u32 s50, s50, s80
s_addc_u32 s51, s51, 0
s_add_u32 s52, s52, s81
s_addc_u32 s53, s53, 0
s_waitcnt vmcnt(0)
v_sub_u32 v185, v185, v188
v_cmp_lt_u32 vcc, 0x7f, v185
s_cbranch_vccnz .Lgu_sp17
.Lgu_rt18:
global_load_dwordx2 v[138:139], v12, s[52:53] offset:0 sc1
global_load_dwordx2 v[140:141], v12, s[52:53] offset:128 sc1
global_load_dwordx4 v[126:129], v10, s[50:51] offset:0 sc1
global_load_dwordx4 v[130:133], v10, s[50:51] offset:1024 sc1
global_load_dwordx4 v[134:137], v10, s[50:51] offset:2048 sc1
global_load_dwordx2 v[142:143], v11, s[52:53] sc1
s_waitcnt vmcnt(4)
v_add_f32 v138, v138, v140
s_waitcnt vmcnt(3)
v_lshlrev_b32 v162, 16, v126
v_add_f32 v139, v139, v141
v_and_b32 v163, 0xffff0000, v126
v_mul_f32 v138, 0x3ae38e39, v138
v_lshlrev_b32 v164, 16, v127
v_mul_f32 v139, 0x3ae38e39, v139
v_and_b32 v165, 0xffff0000, v127
v_fma_f32 v187, -v138, v138, v139
v_lshlrev_b32 v166, 16, v128
v_max_f32 v187, 0, v187
v_and_b32 v167, 0xffff0000, v128
v_cvt_pk_bf16_f32 v187, v187, v187
v_lshlrev_b32 v168, 16, v129
v_lshlrev_b32 v187, 16, v187
v_and_b32 v169, 0xffff0000, v129
v_add_f32 v187, 0x3727c5ac, v187
s_waitcnt vmcnt(2)
v_lshlrev_b32 v170, 16, v130
v_cvt_pk_bf16_f32 v187, v187, v187
v_and_b32 v171, 0xffff0000, v130
v_lshlrev_b32 v187, 16, v187
v_lshlrev_b32 v172, 16, v131
v_sqrt_f32 v187, v187
v_and_b32 v173, 0xffff0000, v131
v_cvt_pk_bf16_f32 v187, v187, v187
v_lshlrev_b32 v174, 16, v132
v_lshlrev_b32 v187, 16, v187
v_and_b32 v175, 0xffff0000, v132
v_rcp_f32 v186, v187
v_lshlrev_b32 v176, 16, v133
v_mul_f32 v186, v186, v186
v_and_b32 v177, 0xffff0000, v133
v_add_f32 v162, v162, v170
v_add_f32 v163, v163, v171
v_add_f32 v164, v164, v172
v_add_f32 v165, v165, v173
v_add_f32 v166, v166, v174
v_add_f32 v167, v167, v175
v_add_f32 v168, v168, v176
v_add_f32 v169, v169, v177
s_waitcnt vmcnt(1)
v_lshlrev_b32 v170, 16, v134
v_and_b32 v171, 0xffff0000, v134
v_lshlrev_b32 v172, 16, v135
v_and_b32 v173, 0xffff0000, v135
v_lshlrev_b32 v174, 16, v136
v_and_b32 v175, 0xffff0000, v136
v_lshlrev_b32 v176, 16, v137
v_and_b32 v177, 0xffff0000, v137
v_add_f32 v162, v162, v170
v_add_f32 v163, v163, v171
v_add_f32 v164, v164, v172
v_add_f32 v165, v165, v173
v_add_f32 v166, v166, v174
v_add_f32 v167, v167, v175
v_add_f32 v168, v168, v176
v_add_f32 v169, v169, v177
s_waitcnt vmcnt(0)
v_lshlrev_b32 v170, 16, v142
v_and_b32 v171, 0xffff0000, v142
v_lshlrev_b32 v172, 16, v143
v_and_b32 v173, 0xffff0000, v143
v_add_f32_dpp v162, v166, v162 row_ror:8 row_mask:0xf bank_mask:0xf
v_add_f32_dpp v163, v167, v163 row_ror:8 row_mask:0xf bank_mask:0xf
v_add_f32_dpp v164, v168, v164 row_ror:8 row_mask:0xf bank_mask:0xf
v_add_f32_dpp v165, v169, v165 row_ror:8 row_mask:0xf bank_mask:0xf
v_fma_f32 v162, v162, v186, v170
v_fma_f32 v163, v163, v186, v171
v_fma_f32 v164, v164, v186, v172
v_fma_f32 v165, v165, v186, v173
)"
R"(v_cvt_pk_bf16_f32 v178, v162, v163
v_cvt_pk_bf16_f32 v179, v164, v165
ds_write_b64 v14, v[178:179]
ds_read_b64 v[180:181], v15
s_waitcnt lgkmcnt(0)
global_store_dwordx2 v13, v[180:181], s[56:57] sc1
s_add_u32 s60, s60, 4
s_branch .Lgu_fin
.Lgu_end:
s_endpgm
.Lgu_sp7:
s_sleep 1
global_load_dword v254, v4, s[38:39] sc1
s_add_u32 s71, s71, 1
s_waitcnt vmcnt(0)
v_sub_u32 v254, v254, v5
v_cmp_lt_u32 vcc, 0x7f, v254
s_cbranch_vccz .Lgu_rt8
s_cmp_lt_u32 s71, 3000000
s_cbranch_scc1 .Lgu_sp7
s_branch .Lgu_rt8
.Lgu_sp9:
s_sleep 1
global_load_dword v254, v4, s[38:39] sc1
s_add_u32 s71, s71, 1
s_waitcnt vmcnt(0)
v_sub_u32 v254, v254, v5
v_cmp_lt_u32 vcc, 0x7f, v254
s_cbranch_vccz .Lgu_rt10
s_cmp_lt_u32 s71, 3000000
s_cbranch_scc1 .Lgu_sp9
s_branch .Lgu_rt10
.Lgu_sp12:
s_sleep 1
global_load_dword v185, v16, s[38:39] sc1
s_add_u32 s71, s71, 1
s_waitcnt vmcnt(0)
v_sub_u32 v185, v185, v17
v_cmp_lt_u32 vcc, 0x7f, v185
s_and_b64 vcc, vcc, s[76:77]
s_cbranch_vccz .Lgu_rt13
s_cmp_lt_u32 s71, 3000000
s_cbranch_scc1 .Lgu_sp12
s_branch .Lgu_rt13
.Lgu_sp14:
s_sleep 1
global_load_dword v185, v16, s[38:39] sc1
s_add_u32 s71, s71, 1
s_waitcnt vmcnt(0)
v_sub_u32 v185, v185, v17
v_cmp_lt_u32 vcc, 0x7f, v185
s_and_b64 vcc, vcc, s[76:77]
s_cbranch_vccz .Lgu_rt15
s_cmp_lt_u32 s71, 3000000
s_cbranch_scc1 .Lgu_sp14
s_branch .Lgu_rt15
.Lgu_sp17:
s_sleep 1
global_load_dword v185, v16, s[38:39] sc1
s_add_u32 s71, s71, 1
s_waitcnt vmcnt(0)
v_sub_u32 v185, v185, v188
v_cmp_lt_u32 vcc, 0x7f, v185
s_cbranch_vccz .Lgu_rt18
s_cmp_lt_u32 s71, 3000000
s_cbranch_scc1 .Lgu_sp17
s_branch .Lgu_rt18
.Lkend:
)"
  :: "{v[0:31]}"(va), "{v[32:63]}"(vb), "{s[36:51]}"(sa), "{s[52:67]}"(sb) : "v255", "a255", "s99", "vcc", "memory");
}

extern "C" void kernel_launch(void* const* d_in, const int* in_sizes, int n_in,
                              void* d_out, int out_size, void* d_ws, size_t ws_size,
                              hipStream_t stream) {
  k_main<<<256, 256, 0, stream>>>((const char*)d_in[0], (const char*)d_in[1], (const char*)d_in[2], (const char*)d_in[4],
                                  (const char*)d_in[5], (const char*)d_in[6], (char*)d_ws, (char*)d_out);
}
```

```cpp
#include <hip/hip_runtime.h>
#include <hip/hip_bf16.h>
#include <stdint.h>
typedef __attribute__((ext_vector_type(32))) unsigned u32x32;
typedef __attribute__((ext_vector_type(16))) unsigned u32x16;
constexpr unsigned RP = 80u;
constexpr unsigned PAD = 12u;
constexpr unsigned XT = 18432u;
constexpr unsigned X1P = 18688u;
constexpr unsigned HT = 147456u;
constexpr unsigned PT = 110592u;
constexpr unsigned FL = 1024u;
constexpr unsigned FR = 102400u;
constexpr unsigned WS_FL = 4096u;
constexpr unsigned WS_ST = 413696u;
constexpr unsigned WS_O = 1048576u;
constexpr unsigned WS_X1 = 6946816u;
constexpr unsigned WS_X2 = 13000704u;
constexpr unsigned WS_XD = 15396864u;
constexpr unsigned WS_P = 17977344u;
constexpr unsigned WS_H = 53366784u;
constexpr unsigned WS_END = 100552704u;
constexpr unsigned FO = 0u;
constexpr unsigned FX1 = 8192u;
constexpr unsigned FX2 = 65536u;
constexpr unsigned FP = 10240u;
constexpr unsigned FH = 28672u;
__device__ __forceinline__ void touch(const void* p) {
  asm volatile("s_mov_b32 m0, 0x27f00\n\ts_nop 0\n\tglobal_load_lds_dword %0, off" :: "v"(p) : "memory");
}
__global__ __launch_bounds__(256, 1) void k_main(const char* x0, const char* wqkv, const char* wo, const char* wg, const char* wu, const char* wd,
                                                 char* ws, char* out) {
  __shared__ __attribute__((aligned(1024))) char smem[163840];
  asm volatile("" :: "s"((unsigned)(size_t)(__attribute__((address_space(3))) const char*)smem) : "memory");
  unsigned lane = threadIdx.x & 63; const unsigned wave = __builtin_amdgcn_readfirstlane(threadIdx.x >> 6);
  const unsigned xc = blockIdx.x & 7, jj = blockIdx.x >> 3;
  const unsigned role = jj < 4 ? 0 : jj == 4 ? 1 : jj < 14 ? 2 : 3;
  const unsigned qa_hg = xc, qa_rho = jj, wo_rho = xc >> 1, wo_cw = xc & 1, dn_i = xc * 9 + jj - 5, gu_i = xc * 18 + jj - 14;
  unsigned qv[9] = {};
  if (__builtin_expect(jj < 4, 1)) {
#pragma unroll
    for (unsigned j = 0; j < 9; ++j) {
      const unsigned c = j * 64 + lane, sg = (c * 5462u) >> 16;
      qv[j] = sg * (1152 - 192) + c * 16 + 4096 - (j & 7) * 1024;
    }
    const unsigned long long wb = (unsigned long long)(wqkv + ((xc * 144) & ~63u) + wave * (16 * 3456));
    asm volatile("s_add_u32 m0, %10, 4096\n\ts_add_u32 vcc_lo, %11, 0\n\ts_addc_u32 vcc_hi, %12, 0\n\tglobal_load_lds_dwordx4 %1, vcc offset:-4096\n\tglobal_load_lds_dwordx4 %2, vcc offset:-3072\n\tglobal_load_lds_dwordx4 %3, vcc offset:-2048\n\tglobal_load_lds_dwordx4 %4, vcc offset:-1024\n\tglobal_load_lds_dwordx4 %5, vcc offset:0\n\tglobal_load_lds_dwordx4 %6, vcc offset:1024\n\tglobal_load_lds_dwordx4 %7, vcc offset:2048\n\tglobal_load_lds_dwordx4 %8, vcc offset:3072\n\ts_add_u32 m0, %10, 12288\n\ts_nop 0\n\tglobal_load_lds_dwordx4 %9, vcc offset:-4096\n\t" : "+v"(lane) : "v"(qv[0]), "v"(qv[1]), "v"(qv[2]), "v"(qv[3]), "v"(qv[4]), "v"(qv[5]), "v"(qv[6]), "v"(qv[7]), "v"(qv[8]), "s"(47104 + wave * 9216),
                 "s"((unsigned)wb), "s"((unsigned)(wb >> 32)) : "vcc", "scc", "memory");
  } else {
    if (wave < 2) { const unsigned u = min((jj - 4) * 124 + threadIdx.x, 3455u), sg = u >> 1, k = sg / 3; touch(wqkv + k * 3456 + (sg - 3 * k) * 1152 + xc * 144 + (u & 1) * 140); }
    else if (wave == 2) { touch(ws + min(lane * 2097152u, WS_END - 4)); }
    else {
      unsigned long long pc; unsigned len;
      asm volatile("s_getpc_b64 %0\n.Lkbeg:\n\ts_mov_b32 %1, .Lkend-.Lkbeg" : "=s"(pc), "=s"(len));
      touch((const char*)pc + min(((jj - 4) * 64 + lane) * 128, len));
    }
    if (wave < 2) { const unsigned u = min((jj - 4) * 103 + threadIdx.x, 2879u), k = u / 5; touch(wo + k * 1152 + (xc & 1) * 576 + min((u - 5 * k) * 128, 572u)); }
    if (jj >= 14 && wave < 2) touch(x0 + ((blockIdx.x - 112) * 128 + threadIdx.x) * 128);
  }

  const unsigned r = lane & 15, g = lane >> 4, q = r >> 2, p = lane & 3;
  u32x32 va = {}, vb = {};
  u32x16 sa = {}, sb = {};
  unsigned sv[28] = {};
  sa[0] = __builtin_amdgcn_readfirstlane(role * 4 + wave); sa[2] = (unsigned)(unsigned long long)ws; sa[3] = (unsigned)((unsigned long long)ws >> 32);
  if (__builtin_expect(role == 0, 1)) {
    const unsigned hg = qa_hg, rho = qa_rho, row0 = rho * RP + PAD, pc0 = wave < 2 ? wave * 5 : 2 + wave * 4, hb = wave * 36 + (wave & 1) * 4;
    auto oa = [&](unsigned b) { return 43520 + (b >> 4) * 256 + r * 16 + (b & 15); };
    const unsigned m0 = 4 * g + (r & 1), m1 = m0 + 2, hm = (r & 1) ? 0xFFFF0000u : 0xFFFFu, ninf = 0xFF800000u;
    va[0] = qv[0];
    va[1] = qv[1];
    va[2] = qv[2];
    va[3] = qv[3];
    va[4] = qv[4];
    va[5] = qv[5];
    va[6] = qv[6];
    va[7] = qv[7];
    va[8] = qv[8];
    va[9] = (8 * g + q) * 576 + (hg & 3) * 16 + hb + p * 8;
    va[10] = (8 * g + q) * 576 + (hg & 3) * 16 + 32 + (p & 1) * 72 + (wave == 0 ? (p >> 1) * 192 : 384);
    va[11] = ((rho * 32 + 31) * 16 + ((pc0 + 0) & 1) * 8 + (lane >> 3)) * 1152 + ((pc0 + 0) >> 1) * 128 + (((lane ^ (lane >> 3)) & 7) << 4) + 4096;
    va[12] = ((rho * 32 + 31) * 16 + ((pc0 + 1) & 1) * 8 + (lane >> 3)) * 1152 + ((pc0 + 1) >> 1) * 128 + (((lane ^ (lane >> 3)) & 7) << 4) + 3072;
    va[13] = ((rho * 32 + 31) * 16 + ((pc0 + 2) & 1) * 8 + (lane >> 3)) * 1152 + ((pc0 + 2) >> 1) * 128 + (((lane ^ (lane >> 3)) & 7) << 4) + 2048;
    va[14] = ((rho * 32 + 31) * 16 + ((pc0 + 3) & 1) * 8 + (lane >> 3)) * 1152 + ((pc0 + 3) >> 1) * 128 + (((lane ^ (lane >> 3)) & 7) << 4) + 1024;
    va[15] = ((rho * 32 + 31) * 16 + ((pc0 + 4) & 1) * 8 + (lane >> 3)) * 1152 + ((pc0 + 4) >> 1) * 128 + (((lane ^ (lane >> 3)) & 7) << 4) + 0;
    va[16] = WS_O + (row0 - 3) * XT + hg * 2304 + wave * 1024 + (wave < 2 ? lane : r) * 16;
    va[17] = 43520 + wave * 1024 + (wave < 2 ? lane : r) * 16;
    va[18] = oa(hb + g * 8);
    va[19] = ((g & 1) == (wave >> 1)) ? oa(32 + (wave & 1) * 4 + (wave >> 1) * 72) : 46336 + lane * 4;
    va[20] = g == 0 ? 40960 + ((wave >> 1) * 16 + r) * 8 + (wave & 1) * 4 : 46080;
    va[21] = 41216 + ((wave >> 1) * 16 + r) * 8 + (wave & 1) * 4;
    va[22] = lane * 8;
    va[23] = lane * 16;
    va[24] = 43008 + r * 4;
    va[25] = 43008 + g * 16;
    va[26] = 41984 + (q * 16 + r) * 16 + p * 4;
    va[27] = 43136 + (((m0 > r) ? 0 : 1) * 16 + m0) * 4;
    va[28] = 43136 + (((m1 > r) ? 0 : 1) * 16 + m1) * 4;
    va[29] = 43136 + (((m0 > r) ? 1 : 0) * 16 + m0) * 4;
    va[30] = 43136 + (((m1 > r) ? 1 : 0) * 16 + m1) * 4;
    va[31] = 0u;
    vb[0] = (4 * g + 0 > r && !((r ^ 0) & 1)) ? 0u : ninf;
    vb[1] = (4 * g + 1 > r && !((r ^ 1) & 1)) ? 0u : ninf;
    vb[2] = (4 * g + 2 > r && !((r ^ 2) & 1)) ? 0u : ninf;
    vb[3] = (4 * g + 3 > r && !((r ^ 3) & 1)) ? 0u : ninf;
    vb[4] = (4 * g + 0 <= r && !((r ^ 0) & 1)) ? 0u : ninf;
    vb[5] = (4 * g + 1 <= r && !((r ^ 1) & 1)) ? 0u : ninf;
    vb[6] = (4 * g + 2 <= r && !((r ^ 2) & 1)) ? 0u : ninf;
    vb[7] = (4 * g + 3 <= r && !((r ^ 3) & 1)) ? 0u : ninf;
    vb[8] = m0 > r ? hm : 0u;
    vb[9] = m1 > r ? hm : 0u;
    vb[10] = m0 > r ? 0u : hm;
    vb[11] = m1 > r ? 0u : hm;
    vb[12] = WS_FL + rho * FR + FX2 + min(lane, 35u) * FL;
    vb[13] = WS_FL + rho * FR + FX2 + min(lane, 35u) * FL;
    vb[14] = 0u - 31;
    vb[15] = WS_FL + rho * FR + FO + hg * FL;
    vb[16] = 0u - 4;
    vb[17] = WS_FL + lane * FL;
    vb[18] = 0u;
    vb[19] = r * 128 + (((0 + g) ^ (r & 7)) << 4);
    vb[20] = r * 128 + (((4 + g) ^ (r & 7)) << 4);
    sv[0] = (unsigned)(unsigned long long)(wqkv + ((hg * 144) & ~63u) + wave * (16 * 3456) + 1 * 64 * 3456);
    sv[1] = (unsigned)((unsigned long long)(wqkv + ((hg * 144) & ~63u) + wave * (16 * 3456) + 1 * 64 * 3456) >> 32);
    sv[2] = (unsigned)(unsigned long long)(x0 - 2048);
    sv[3] = (unsigned)((unsigned long long)(x0 - 2048) >> 32);
    sv[4] = (unsigned)(unsigned long long)((ws + WS_X2) - x0 + (rho ? 32 : 31) * (long long)XT);
    sv[5] = (unsigned)((unsigned long long)((ws + WS_X2) - x0 + (rho ? 32 : 31) * (long long)XT) >> 32);
    sv[6] = (unsigned)(unsigned long long)(-(long long)XT);
    sv[7] = (unsigned)((unsigned long long)(-(long long)XT) >> 32);
    sv[8] = 47104 + wave * 9216;
    sv[9] = pc0 * 1024 + 2048;
    sv[10] = rho ? 0u : XT;
    sv[11] = rho ? 0u - FR : 0u;
    sv[12] = rho ? 0u : ninf;
    sv[13] = hg * 4 + rho;
  } else if (__builtin_expect(role == 1, 0)) {
    const unsigned rho = wo_rho, cw = wo_cw, row0 = rho * RP + PAD, pc0 = wave * 5;
    auto wv = [&](unsigned j) { const unsigned b = j * 1024 + lane * 16, rw = b / 576; return rw * 576 + b + 4096 - (j & 7) * 1024; };
    va[0] = wv(0);
    va[1] = wv(1);
    va[2] = wv(2);
    va[3] = wv(3);
    va[4] = wv(4);
    va[5] = wv(5);
    va[6] = wv(6);
    va[7] = wv(7);
    va[8] = wv(8);
    va[9] = (8 * g + q) * 576 + cw * 64 + wave * 128 + p * 16;
    va[10] = (8 * g + q) * 576 + (cw ? 0 : 512) + p * 16 + (wave >> 1) * 8;
    va[11] = WS_O + (row0 - 2) * XT + pc0 * 1024 + 2048 + lane * 16;
    va[12] = WS_O + (row0 + 63) * XT + pc0 * 1024 + 2048 + lane * 16;
    va[13] = WS_X1 + (row0 - 5) * X1P + (cw * 10 + wave * 2) * 1024 + lane * 16;
    va[14] = WS_X1 + (row0 - 5) * X1P + (8 + cw) * 1024 + lane * 16;
    va[15] = WS_X1 + (row0 - 7) * X1P + XT + cw * 128 + r * 8;
    va[16] = ((rho * 32 + 31) * 16 + 0 + (lane >> 3)) * 1152 + (cw * 5 + wave) * 128 + (((lane ^ (lane >> 3)) & 7) << 4) - 0;
    va[17] = ((rho * 32 + 31) * 16 + 8 + (lane >> 3)) * 1152 + (cw * 5 + wave) * 128 + (((lane ^ (lane >> 3)) & 7) << 4) - 1024;
    va[18] = ((rho * 32 + 31) * 16 + r) * 1152 + (8 + cw) * 64 + g * 16;
    va[19] = 47104 + wave * 2048 + r * 128 + (((0 + g) ^ (r & 7)) << 4);
    va[20] = 47104 + wave * 2048 + r * 128 + (((4 + g) ^ (r & 7)) << 4);
    va[21] = WS_FL + rho * FR + FO + (lane & 7) * FL;
    va[22] = 0u - 2;
    va[23] = WS_FL + rho * FR + FX1 + cw * FL;
    va[24] = 0u - 6;
    va[25] = lane * 16;
    va[26] = lane * 16;
    va[27] = lane * 8;
    sv[0] = (unsigned)(unsigned long long)(wo + wave * 16 * 1152 + cw * 576);
    sv[1] = (unsigned)((unsigned long long)(wo + wave * 16 * 1152 + cw * 576) >> 32);
    sv[2] = (unsigned)(unsigned long long)(x0);
    sv[3] = (unsigned)((unsigned long long)(x0) >> 32);
    sv[4] = (unsigned)(unsigned long long)(x0);
    sv[5] = (unsigned)((unsigned long long)(x0) >> 32);
    sv[6] = (unsigned)(unsigned long long)(ws + WS_X2);
    sv[7] = (unsigned)((unsigned long long)(ws + WS_X2) >> 32);
    sv[8] = wave * 9216;
    sv[9] = pc0 * 1024 + 2048;
  } else if (__builtin_expect(role == 2, 0)) {
    const unsigned tri = dn_i / 3, nu = dn_i % 3, kap = tri >> 2, rho = tri & 3, row0 = rho * RP + PAD;
    auto wv = [&](unsigned j) { const unsigned b = j * 1024 + lane * 16, rw = b / 384; return rw * (1152 - 384) + b + 4096 - j * 1024; };
    va[0] = wv(0);
    va[1] = wv(1);
    va[2] = wv(2);
    va[3] = wv(3);
    va[4] = wv(4);
    va[5] = wv(5);
    va[6] = (8 * g + q) * 384 + wave * 64 + p * 16 + (kap & 1) * 8;
    va[7] = (8 * g + q) * 384 + wave * 64 + p * 16 + 8 - (kap & 1) * 8;
    va[8] = (8 * g + q) * 384 + 256 + wave * 32 + (p ^ (kap & 1)) * 8;
    va[9] = WS_H + row0 * HT + (kap * 24 + wave * 6 + 2) * 1024 + lane * 16;
    va[10] = WS_H + (row0 + 63) * HT + (kap * 24 + wave * 6 + 2) * 1024 + lane * 16;
    va[11] = WS_P + (row0 - 3) * PT + (((nu * 3 + (wave >> 1)) * 4 + q) * 6 + kap) * 512 + (wave & 1) * 256 + g * 64 + p * 16;
    va[12] = WS_P + (row0 - 3) * PT + (((nu * 3 + 2) * 4 + q) * 6 + kap) * 512 + wave * 128 + (g >> 1) * 64 + p * 16;
    va[13] = WS_FL + rho * FR + FH + (kap * 6 + min(lane, 5u)) * FL;
    va[14] = 0u;
    va[15] = WS_FL + rho * FR + FP + (nu * 6 + kap) * FL;
    va[16] = 0u - 4;
    va[17] = lane * 16;
    sv[0] = (unsigned)(unsigned long long)(wd + (kap * 768 + wave * 16) * 1152 + nu * 384);
    sv[1] = (unsigned)((unsigned long long)(wd + (kap * 768 + wave * 16) * 1152 + nu * 384) >> 32);
    sv[2] = wave * 6144;
    sv[3] = wave * 6144 + 2048;
  } else {
    const unsigned ii = gu_i, gam = (ii % 72) >> 1, rho = ii / 72 * 2 + (ii & 1), row0 = rho * RP + PAD, pc0 = wave * 6;
    const unsigned bk = gam >> 2, qd = gam & 3, od = (lane >> 3) & 1, un = g * 8 + (lane & 7), jr = qd * 4 + p, jc = (un >> 2) * 8 + od * 4;
    va[0] = g * 9216 + ((r ^ (g << 2)) << 4) + 4096;
    va[1] = (8 * g + q) * 256 + (((4 * wave + p) ^ (q << 2)) << 4);
    va[2] = WS_X1 + row0 * X1P + pc0 * 1024 + 2048 + lane * 16;
    va[3] = WS_H + (row0 - 3) * HT + (gam * 4 + wave) * 1024 + lane * 16;
    va[4] = WS_FL + rho * FR + FX1 + (lane & 1) * FL;
    va[5] = 0u;
    va[6] = WS_FL + rho * FR + FH + gam * FL;
    va[7] = 0u - 4;
    va[8] = lane * 16;
    va[9] = WS_X1 + (row0 + 63) * X1P + pc0 * 1024 + 2048 + lane * 16;
    va[10] = WS_P + row0 * PT + gam * 3072 + od * 512 + un * 16;
    va[11] = WS_X1 + row0 * X1P + bk * 2048 + (un >> 2) * 256 + jr * 16 + od * 8;
    va[12] = WS_X1 + row0 * X1P + XT + jr * 8;
    va[13] = ((rho * 32 + 31) * 16 + qd * 4 + g) * 1152 + bk * 128 + r * 8;
    va[14] = 49152 + wave * 1024 + p * 144 + jc * 2;
    va[15] = 49152 + wave * 1024 + g * 144 + r * 8;
    va[16] = WS_FL + rho * FR + FP + (bk / 3 * 6 + min(lane, 5u)) * FL;
    va[17] = 0u;
    va[18] = WS_FL + rho * FR + FX2 + gam * FL;
    va[19] = (rho * 144 + (wave & 1) * 40 + (lane >> 1)) * 9216 + (lane & 1) * 128;
    va[20] = 0u;
    va[21] = 0u;
    va[22] = (g == q && p < 2) ? (0x3F80u << (16 * p)) : 0u;
    va[23] = (g == q && p >= 2) ? (0x3F80u << (16 * (p - 2))) : 0u;
    va[24] = 0u;
    va[25] = 0u;
    sv[0] = (unsigned)(unsigned long long)((wave < 2 ? wg : wu) + gam * 256 + (wave & 1) * (32 * 9216));
    sv[1] = (unsigned)((unsigned long long)((wave < 2 ? wg : wu) + gam * 256 + (wave & 1) * (32 * 9216)) >> 32);
    sv[2] = wave * 8192;
    sv[3] = pc0 * 1024 + 2048;
    sv[4] = (unsigned)(unsigned long long)(ws + WS_X2);
    sv[5] = (unsigned)((unsigned long long)(ws + WS_X2) >> 32);
    sv[6] = (unsigned)(unsigned long long)(out + 32 * XT);
    sv[7] = (unsigned)((unsigned long long)(out + 32 * XT) >> 32);
    sv[8] = (unsigned)(unsigned long long)(ws + WS_XD);
    sv[9] = (unsigned)((unsigned long long)(ws + WS_XD) >> 32);
    sv[10] = (unsigned)(unsigned long long)(ws - PT);
    sv[11] = (unsigned)((unsigned long long)(ws - PT) >> 32);
    sv[12] = (unsigned)(unsigned long long)(ws - X1P);
    sv[13] = (unsigned)((unsigned long long)(ws - X1P) >> 32);
    sv[14] = (unsigned)(unsigned long long)(ws + WS_X2);
    sv[15] = (unsigned)((unsigned long long)(ws + WS_X2) >> 32);
    sv[16] = (unsigned)(unsigned long long)(ws + WS_XD);
    sv[17] = (unsigned)((unsigned long long)(ws + WS_XD) >> 32);
    sv[18] = (unsigned)(unsigned long long)(ws + WS_XD);
    sv[19] = (unsigned)((unsigned long long)(ws + WS_XD) >> 32);
    sv[20] = 0u;
    sv[21] = rho & 1;
  }
#pragma unroll
  for (int i = 0; i < 28; ++i) { const unsigned u = __builtin_amdgcn_readfirstlane(sv[i]); if (i < 12) sa[i + 4] = u; else sb[i - 12] = u; }

  asm volatile(
R"(s_mov_b32 s71, 0
s_mov_b32 s74, 0
s_load_dword s37, s[38:39], 0x0
s_lshr_b32 s75, s36, 2
s_cmp_eq_u32 s75, 1
s_cbranch_scc1 .Lrole1
s_cmp_eq_u32 s75, 2
s_cbranch_scc1 .Lrole2
s_cmp_eq_u32 s75, 3
s_cbranch_scc1 .Lrole3
.Lrole0:
; requested in front
; requested in front
; requested in front
; requested in front
; requested in front
; requested in front
; requested in front
; requested in front
; requested in front
s_add_u32 m0, s48, 40960
s_nop 0
global_load_lds_dwordx4 v0, s[40:41] offset:-4096
global_load_lds_dwordx4 v1, s[40:41] offset:-3072
global_load_lds_dwordx4 v2, s[40:41] offset:-2048
global_load_lds_dwordx4 v3, s[40:41] offset:-1024
global_load_lds_dwordx4 v4, s[40:41] offset:0
global_load_lds_dwordx4 v5, s[40:41] offset:1024
global_load_lds_dwordx4 v6, s[40:41] offset:2048
global_load_lds_dwordx4 v7, s[40:41] offset:3072
s_add_u32 m0, s48, 49152
s_nop 0
global_load_lds_dwordx4 v8, s[40:41] offset:-4096
s_add_u32 s40, s40, 221184
s_addc_u32 s41, s41, 0
v_add_u32 v255, 0x10000, v9
v_add_u32 v162, 0x20000, v9
v_add_u32 v163, 0x10000, v10
v_add_u32 v164, 0x20000, v10
v_mov_b32 v106, 0x3F803F80
v_mov_b32 v107, 0x3F803F80
v_mov_b32 v108, 0x3F803F80
v_mov_b32 v109, 0x3F803F80
v_mov_b32 v141, 0
v_mov_b32 v145, 0
v_mov_b32 v149, 0
v_mov_b32 v153, 0
v_mov_b32 v165, 46080
s_waitcnt lgkmcnt(0)
s_mul_i32 s37, s37, 0x9E3779B1
s_add_u32 s37, s37, 0x7F4A7C15
v_add_u32 v46, s37, v46
v_add_u32 v48, s37, v48
v_add_u32 v50, s37, v50
ds_write_b32 v165, v141
s_mov_b64 s[76:77], 0
s_mov_b32 s73, 18
s_waitcnt vmcnt(9) lgkmcnt(0)
s_barrier
s_add_u32 m0, s48, 77824
ds_read_b64_tr_b16 a[0:1], v9 offset:47104
ds_read_b64_tr_b16 a[2:3], v9 offset:49408
global_load_lds_dwordx4 v0, s[40:41] offset:-4096
ds_read_b64_tr_b16 a[4:5], v9 offset:47296
global_load_lds_dwordx4 v1, s[40:41] offset:-3072
ds_read_b64_tr_b16 a[6:7], v9 offset:49600
global_load_lds_dwordx4 v2, s[40:41] offset:-2048
ds_read_b64_tr_b16 a[8:9], v9 offset:47488
global_load_lds_dwordx4 v3, s[40:41] offset:-1024
ds_read_b64_tr_b16 a[10:11], v9 offset:49792
ds_read_b64_tr_b16 a[216:217], v10 offset:47104
global_load_lds_dwordx4 v4, s[40:41] offset:0
ds_read_b64_tr_b16 a[218:219], v10 offset:49408
global_load_lds_dwordx4 v5, s[40:41] offset:1024
ds_read_b64_tr_b16 a[12:13], v255 offset:0
global_load_lds_dwordx4 v6, s[40:41] offset:2048
ds_read_b64_tr_b16 a[14:15], v255 offset:2304
global_load_lds_dwordx4 v7, s[40:41] offset:3072
ds_read_b64_tr_b16 a[16:17], v255 offset:192
ds_read_b64_tr_b16 a[18:19], v255 offset:2496
s_add_u32 m0, s48, 86016
ds_read_b64_tr_b16 a[20:21], v255 offset:384
global_load_lds_dwordx4 v8, s[40:41] offset:-4096
ds_read_b64_tr_b16 a[22:23], v255 offset:2688
s_add_u32 s40, s40, 221184
ds_read_b64_tr_b16 a[220:221], v163 offset:0
s_addc_u32 s41, s41, 0
ds_read_b64_tr_b16 a[222:223], v163 offset:2304
s_waitcnt vmcnt(9) lgkmcnt(0)
s_barrier
s_add_u32 m0, s48, 4096
ds_read_b64_tr_b16 a[24:25], v255 offset:18432
ds_read_b64_tr_b16 a[26:27], v255 offset:20736
global_load_lds_dwordx4 v0, s[40:41] offset:-4096
ds_read_b64_tr_b16 a[28:29], v255 offset:18624
ds_read_b64_tr_b16 a[30:31], v255 offset:20928
global_load_lds_dwordx4 v1, s[40:41] offset:-3072
ds_read_b64_tr_b16 a[32:33], v255 offset:18816
ds_read_b64_tr_b16 a[34:35], v255 offset:21120
global_load_lds_dwordx4 v2, s[40:41] offset:-2048
ds_read_b64_tr_b16 a[224:225], v163 offset:18432
ds_read_b64_tr_b16 a[226:227], v163 offset:20736
global_load_lds_dwordx4 v3, s[40:41] offset:-1024
ds_read_b64_tr_b16 a[36:37], v255 offset:36864
ds_read_b64_tr_b16 a[38:39], v255 offset:39168
global_load_lds_dwordx4 v4, s[40:41] offset:0
ds_read_b64_tr_b16 a[40:41], v255 offset:37056
ds_read_b64_tr_b16 a[42:43], v255 offset:39360
global_load_lds_dwordx4 v5, s[40:41] offset:1024
ds_read_b64_tr_b16 a[44:45], v255 offset:37248
ds_read_b64_tr_b16 a[46:47], v255 offset:39552
global_load_lds_dwordx4 v6, s[40:41] offset:2048
ds_read_b64_tr_b16 a[228:229], v163 offset:36864
ds_read_b64_tr_b16 a[230:231], v163 offset:39168
global_load_lds_dwordx4 v7, s[40:41] offset:3072
v_mfma_f32_16x16x32_bf16 v[86:89], a[0:3], v[106:109], 0
v_mfma_f32_16x16x32_bf16 v[90:93], a[4:7], v[106:109], 0
s_add_u32 m0, s48, 12288
v_mfma_f32_16x16x32_bf16 v[94:97], v[106:109], a[8:11], 0
v_mfma_f32_16x16x32_bf16 v[98:101], a[216:219], v[106:109], 0
global_load_lds_dwordx4 v8, s[40:41] offset:-4096
v_mfma_f32_16x16x32_bf16 v[102:105], v[106:109], a[216:219], 0
v_mfma_f32_16x16x32_bf16 v[86:89], a[12:15], v[106:109], v[86:89]
s_add_u32 s40, s40, 221184
v_mfma_f32_16x16x32_bf16 v[90:93], a[16:19], v[106:109], v[90:93]
v_mfma_f32_16x16x32_bf16 v[94:97], v[106:109], a[20:23], v[94:97]
s_addc_u32 s41, s41, 0
v_mfma_f32_16x16x32_bf16 v[98:101], a[220:223], v[106:109], v[98:101]
v_mfma_f32_16x16x32_bf16 v[102:105], v[106:109], a[220:223], v[102:105]
s_waitcnt vmcnt(9) lgkmcnt(0)
s_barrier
s_add_u32 m0, s48, 40960
ds_read_b64_tr_b16 a[48:49], v255 offset:55296
ds_read_b64_tr_b16 a[50:51], v255 offset:57600
global_load_lds_dwordx4 v0, s[40:41] offset:-4096
ds_read_b64_tr_b16 a[52:53], v255 offset:55488
ds_read_b64_tr_b16 a[54:55], v255 offset:57792
global_load_lds_dwordx4 v1, s[40:41] offset:-3072
ds_read_b64_tr_b16 a[56:57], v255 offset:55680
ds_read_b64_tr_b16 a[58:59], v255 offset:57984
global_load_lds_dwordx4 v2, s[40:41] offset:-2048
ds_read_b64_tr_b16 a[232:233], v163 offset:55296
ds_read_b64_tr_b16 a[234:235], v163 offset:57600
global_load_lds_dwordx4 v3, s[40:41] offset:-1024
ds_read_b64_tr_b16 a[60:61], v162 offset:8192
ds_read_b64_tr_b16 a[62:63], v162 offset:10496
global_load_lds_dwordx4 v4, s[40:41] offset:0
ds_read_b64_tr_b16 a[64:65], v162 offset:8384
ds_read_b64_tr_b16 a[66:67], v162 offset:10688
global_load_lds_dwordx4 v5, s[40:41] offset:1024
ds_read_b64_tr_b16 a[68:69], v162 offset:8576
ds_read_b64_tr_b16 a[70:71], v162 offset:10880
global_load_lds_dwordx4 v6, s[40:41] offset:2048
ds_read_b64_tr_b16 a[236:237], v164 offset:8192
ds_read_b64_tr_b16 a[238:239], v164 offset:10496
global_load_lds_dwordx4 v7, s[40:41] offset:3072
v_mfma_f32_16x16x32_bf16 v[86:89], a[24:27], v[106:109], v[86:89]
v_mfma_f32_16x16x32_bf16 v[90:93], a[28:31], v[106:109], v[90:93]
s_add_u32 m0, s48, 49152
v_mfma_f32_16x16x32_bf16 v[94:97], v[106:109], a[32:35], v[94:97]
v_mfma_f32_16x16x32_bf16 v[98:101], a[224:227], v[106:109], v[98:101]
global_load_lds_dwordx4 v8, s[40:41] offset:-4096
v_mfma_f32_16x16x32_bf16 v[102:105], v[106:109], a[224:227], v[102:105]
v_mfma_f32_16x16x32_bf16 v[86:89], a[36:39], v[106:109], v[86:89]
s_add_u32 s40, s40, 221184
v_mfma_f32_16x16x32_bf16 v[90:93], a[40:43], v[106:109], v[90:93]
v_mfma_f32_16x16x32_bf16 v[94:97], v[106:109], a[44:47], v[94:97]
s_addc_u32 s41, s41, 0
v_mfma_f32_16x16x32_bf16 v[98:101], a[228:231], v[106:109], v[98:101]
v_mfma_f32_16x16x32_bf16 v[102:105], v[106:109], a[228:231], v[102:105]
s_waitcnt vmcnt(9) lgkmcnt(0)
s_barrier
s_add_u32 m0, s48, 77824
ds_read_b64_tr_b16 a[72:73], v9 offset:47104
ds_read_b64_tr_b16 a[74:75], v9 offset:49408
global_load_lds_dwordx4 v0, s[40:41] offset:-4096
ds_read_b64_tr_b16 a[76:77], v9 offset:47296
ds_read_b64_tr_b16 a[78:79], v9 offset:49600
global_load_lds_dwordx4 v1, s[40:41] offset:-3072
ds_read_b64_tr_b16 a[80:81], v9 offset:47488
ds_read_b64_tr_b16 a[82:83], v9 offset:49792
global_load_lds_dwordx4 v2, s[40:41] offset:-2048
ds_read_b64_tr_b16 a[240:241], v10 offset:47104
ds_read_b64_tr_b16 a[242:243], v10 offset:49408
global_load_lds_dwordx4 v3, s[40:41] offset:-1024
ds_read_b64_tr_b16 a[84:85], v255 offset:0
ds_read_b64_tr_b16 a[86:87], v255 offset:2304
global_load_lds_dwordx4 v4, s[40:41] offset:0
ds_read_b64_tr_b16 a[88:89], v255 offset:192
ds_read_b64_tr_b16 a[90:91], v255 offset:2496
global_load_lds_dwordx4 v5, s[40:41] offset:1024
ds_read_b64_tr_b16 a[92:93], v255 offset:384
ds_read_b64_tr_b16 a[94:95], v255 offset:2688
global_load_lds_dwordx4 v6, s[40:41] offset:2048
ds_read_b64_tr_b16 a[244:245], v163 offset:0
ds_read_b64_tr_b16 a[246:247], v163 offset:2304
global_load_lds_dwordx4 v7, s[40:41] offset:3072
v_mfma_f32_16x16x32_bf16 v[86:89], a[48:51], v[106:109], v[86:89]
v_mfma_f32_16x16x32_bf16 v[90:93], a[52:55], v[106:109], v[90:93]
s_add_u32 m0, s48, 86016
v_mfma_f32_16x16x32_bf16 v[94:97], v[106:109], a[56:59], v[94:97]
v_mfma_f32_16x16x32_bf16 v[98:101], a[232:235], v[106:109], v[98:101]
global_load_lds_dwordx4 v8, s[40:41] offset:-4096
v_mfma_f32_16x16x32_bf16 v[102:105], v[106:109], a[232:235], v[102:105]
v_mfma_f32_16x16x32_bf16 v[86:89], a[60:63], v[106:109], v[86:89]
s_add_u32 s40, s40, 221184
v_mfma_f32_16x16x32_bf16 v[90:93], a[64:67], v[106:109], v[90:93]
v_mfma_f32_16x16x32_bf16 v[94:97], v[106:109], a[68:71], v[94:97]
s_addc_u32 s41, s41, 0
v_mfma_f32_16x16x32_bf16 v[98:101], a[236:239], v[106:109], v[98:101]
v_mfma_f32_16x16x32_bf16 v[102:105], v[106:109], a[236:239], v[102:105]
s_waitcnt vmcnt(9) lgkmcnt(0)
s_barrier
s_add_u32 m0, s48, 4096
ds_read_b64_tr_b16 a[96:97], v255 offset:18432
ds_read_b64_tr_b16 a[98:99], v255 offset:20736
global_load_lds_dwordx4 v0, s[40:41] offset:-4096
ds_read_b64_tr_b16 a[100:101], v255 offset:18624
ds_read_b64_tr_b16 a[102:103], v255 offset:20928
global_load_lds_dwordx4 v1, s[40:41] offset:-3072
ds_read_b64_tr_b16 a[104:105], v255 offset:18816
ds_read_b64_tr_b16 a[106:107], v255 offset:21120
global_load_lds_dwordx4 v2, s[40:41] offset:-2048
ds_read_b64_tr_b16 a[248:249], v163 offset:18432
ds_read_b64_tr_b16 a[250:251], v163 offset:20736
global_load_lds_dwordx4 v3, s[40:41] offset:-1024
ds_read_b64_tr_b16 a[108:109], v255 offset:36864
ds_read_b64_tr_b16 a[110:111], v255 offset:39168
global_load_lds_dwordx4 v4, s[40:41] offset:0
ds_read_b64_tr_b16 a[112:113], v255 offset:37056
ds_read_b64_tr_b16 a[114:115], v255 offset:39360
global_load_lds_dwordx4 v5, s[40:41] offset:1024
ds_read_b64_tr_b16 a[116:117], v255 offset:37248
ds_read_b64_tr_b16 a[118:119], v255 offset:39552
global_load_lds_dwordx4 v6, s[40:41] offset:2048
ds_read_b64_tr_b16 a[252:253], v163 offset:36864
ds_read_b64_tr_b16 a[254:255], v163 offset:39168
global_load_lds_dwordx4 v7, s[40:41] offset:3072
v_mfma_f32_16x16x32_bf16 v[86:89], a[72:75], v[106:109], v[86:89]
v_mfma_f32_16x16x32_bf16 v[90:93], a[76:79], v[106:109], v[90:93]
s_add_u32 m0, s48, 12288
v_mfma_f32_16x16x32_bf16 v[94:97], v[106:109], a[80:83], v[94:97]
v_mfma_f32_16x16x32_bf16 v[98:101], a[240:243], v[106:109], v[98:101]
global_load_lds_dwordx4 v8, s[40:41] offset:-4096
v_mfma_f32_16x16x32_bf16 v[102:105], v[106:109], a[240:243], v[102:105]
v_mfma_f32_16x16x32_bf16 v[86:89], a[84:87], v[106:109], v[86:89]
s_add_u32 s40, s40, 221184
v_mfma_f32_16x16x32_bf16 v[90:93], a[88:91], v[106:109], v[90:93]
v_mfma_f32_16x16x32_bf16 v[94:97], v[106:109], a[92:95], v[94:97]
s_addc_u32 s41, s41, 0
v_mfma_f32_16x16x32_bf16 v[98:101], a[244:247], v[106:109], v[98:101]
v_mfma_f32_16x16x32_bf16 v[102:105], v[106:109], a[244:247], v[102:105]
s_waitcnt vmcnt(9) lgkmcnt(0)
s_barrier
s_add_u32 m0, s48, 40960
ds_read_b64_tr_b16 a[120:121], v255 offset:55296
ds_read_b64_tr_b16 a[122:123], v255 offset:57600
global_load_lds_dwordx4 v0, s[40:41] offset:-4096
ds_read_b64_tr_b16 a[124:125], v255 offset:55488
ds_read_b64_tr_b16 a[126:127], v255 offset:57792
global_load_lds_dwordx4 v1, s[40:41] offset:-3072
ds_read_b64_tr_b16 a[128:129], v255 offset:55680
ds_read_b64_tr_b16 a[130:131], v255 offset:57984
global_load_lds_dwordx4 v2, s[40:41] offset:-2048
ds_read_b64_tr_b16 v[54:55], v163 offset:55296
ds_read_b64_tr_b16 v[56:57], v163 offset:57600
global_load_lds_dwordx4 v3, s[40:41] offset:-1024
ds_read_b64_tr_b16 a[132:133], v162 offset:8192
ds_read_b64_tr_b16 a[134:135], v162 offset:10496
global_load_lds_dwordx4 v4, s[40:41] offset:0
ds_read_b64_tr_b16 a[136:137], v162 offset:8384
ds_read_b64_tr_b16 a[138:139], v162 offset:10688
global_load_lds_dwordx4 v5, s[40:41] offset:1024
ds_read_b64_tr_b16 a[140:141], v162 offset:8576
ds_read_b64_tr_b16 a[142:143], v162 offset:10880
global_load_lds_dwordx4 v6, s[40:41] offset:2048
ds_read_b64_tr_b16 v[58:59], v164 offset:8192
ds_read_b64_tr_b16 v[60:61], v164 offset:10496
global_load_lds_dwordx4 v7, s[40:41] offset:3072
v_mfma_f32_16x16x32_bf16 v[86:89], a[96:99], v[106:109], v[86:89]
v_mfma_f32_16x16x32_bf16 v[90:93], a[100:103], v[106:109], v[90:93]
s_add_u32 m0, s48, 49152
v_mfma_f32_16x16x32_bf16 v[94:97], v[106:109], a[104:107], v[94:97]
v_mfma_f32_16x16x32_bf16 v[98:101], a[248:251], v[106:109], v[98:101]
global_load_lds_dwordx4 v8, s[40:41] offset:-4096
v_mfma_f32_16x16x32_bf16 v[102:105], v[106:109], a[248:251], v[102:105]
v_mfma_f32_16x16x32_bf16 v[86:89], a[108:111], v[106:109], v[86:89]
s_add_u32 s40, s40, 221184
v_mfma_f32_16x16x32_bf16 v[90:93], a[112:115], v[106:109], v[90:93]
v_mfma_f32_16x16x32_bf16 v[94:97], v[106:109], a[116:119], v[94:97]
s_addc_u32 s41, s41, 0
v_mfma_f32_16x16x32_bf16 v[98:101], a[252:255], v[106:109], v[98:101]
v_mfma_f32_16x16x32_bf16 v[102:105], v[106:109], a[252:255], v[102:105]
s_waitcnt vmcnt(9) lgkmcnt(0)
s_barrier
s_add_u32 m0, s48, 77824
ds_read_b64_tr_b16 a[144:145], v9 offset:47104
ds_read_b64_tr_b16 a[146:147], v9 offset:49408
global_load_lds_dwordx4 v0, s[40:41] offset:-4096
ds_read_b64_tr_b16 a[148:149], v9 offset:47296
ds_read_b64_tr_b16 a[150:151], v9 offset:49600
global_load_lds_dwordx4 v1, s[40:41] offset:-3072
ds_read_b64_tr_b16 a[152:153], v9 offset:47488
ds_read_b64_tr_b16 a[154:155], v9 offset:49792
global_load_lds_dwordx4 v2, s[40:41] offset:-2048
ds_read_b64_tr_b16 v[62:63], v10 offset:47104
ds_read_b64_tr_b16 v[64:65], v10 offset:49408
global_load_lds_dwordx4 v3, s[40:41] offset:-1024
ds_read_b64_tr_b16 a[156:157], v255 offset:0
ds_read_b64_tr_b16 a[158:159], v255 offset:2304
global_load_lds_dwordx4 v4, s[40:41] offset:0
ds_read_b64_tr_b16 a[160:161], v255 offset:192
ds_read_b64_tr_b16 a[162:163], v255 offset:2496
global_load_lds_dwordx4 v5, s[40:41] offset:1024
ds_read_b64_tr_b16 a[164:165], v255 offset:384
ds_read_b64_tr_b16 a[166:167], v255 offset:2688
global_load_lds_dwordx4 v6, s[40:41] offset:2048
ds_read_b64_tr_b16 v[66:67], v163 offset:0
ds_read_b64_tr_b16 v[68:69], v163 offset:2304
global_load_lds_dwordx4 v7, s[40:41] offset:3072
v_mfma_f32_16x16x32_bf16 v[86:89], a[120:123], v[106:109], v[86:89]
v_mfma_f32_16x16x32_bf16 v[90:93], a[124:127], v[106:109], v[90:93]
s_add_u32 m0, s48, 86016
v_mfma_f32_16x16x32_bf16 v[94:97], v[106:109], a[128:131], v[94:97]
v_mfma_f32_16x16x32_bf16 v[98:101], v[54:57], v[106:109], v[98:101]
global_load_lds_dwordx4 v8, s[40:41] offset:-4096
v_mfma_f32_16x16x32_bf16 v[102:105], v[106:109], v[54:57], v[102:105]
v_mfma_f32_16x16x32_bf16 v[86:89], a[132:135], v[106:109], v[86:89]
s_sub_u32 s40, s40, 1769472
v_mfma_f32_16x16x32_bf16 v[90:93], a[136:139], v[106:109], v[90:93]
v_mfma_f32_16x16x32_bf16 v[94:97], v[106:109], a[140:143], v[94:97]
s_subb_u32 s41, s41, 0
v_mfma_f32_16x16x32_bf16 v[98:101], v[58:61], v[106:109], v[98:101]
v_mfma_f32_16x16x32_bf16 v[102:105], v[106:109], v[58:61], v[102:105]
s_waitcnt vmcnt(9) lgkmcnt(0)
s_barrier
s_add_u32 m0, s49, 0
ds_read_b64_tr_b16 a[168:169], v255 offset:18432
ds_read_b64_tr_b16 a[170:171], v255 offset:20736
global_load_lds_dwordx4 v11, s[42:43] offset:-2048 sc1
ds_read_b64_tr_b16 a[172:173], v255 offset:18624
ds_read_b64_tr_b16 a[174:175], v255 offset:20928
global_load_lds_dwordx4 v12, s[42:43] offset:-1024 sc1
ds_read_b64_tr_b16 a[176:177], v255 offset:18816
ds_read_b64_tr_b16 a[178:179], v255 offset:21120
global_load_lds_dwordx4 v13, s[42:43] offset:0 sc1
ds_read_b64_tr_b16 v[70:71], v163 offset:18432
ds_read_b64_tr_b16 v[72:73], v163 offset:20736
global_load_lds_dwordx4 v14, s[42:43] offset:1024 sc1
ds_read_b64_tr_b16 a[180:181], v255 offset:36864
ds_read_b64_tr_b16 a[182:183], v255 offset:39168
s_bitcmp1_b32 s36, 1
s_cbranch_scc1 .Lqa_x0
global_load_lds_dwordx4 v15, s[42:43] offset:2048 sc1
.Lqa_x0:
ds_read_b64_tr_b16 a[184:185], v255 offset:37056
ds_read_b64_tr_b16 a[186:187], v255 offset:39360
s_sub_u32 s42, s42, 18432
s_subb_u32 s43, s43, 0
ds_read_b64_tr_b16 a[188:189], v255 offset:37248
s_add_u32 m0, s49, 18432
ds_read_b64_tr_b16 a[190:191], v255 offset:39552
ds_read_b64_tr_b16 v[74:75], v163 offset:36864
global_load_lds_dwordx4 v11, s[42:43] offset:-2048 sc1
ds_read_b64_tr_b16 v[76:77], v163 offset:39168
v_mfma_f32_16x16x32_bf16 v[86:89], a[144:147], v[106:109], v[86:89]
global_load_lds_dwordx4 v12, s[42:43] offset:-1024 sc1
v_mfma_f32_16x16x32_bf16 v[90:93], a[148:151], v[106:109], v[90:93]
v_mfma_f32_16x16x32_bf16 v[94:97], v[106:109], a[152:155], v[94:97]
global_load_lds_dwordx4 v13, s[42:43] offset:0 sc1
v_mfma_f32_16x16x32_bf16 v[98:101], v[62:65], v[106:109], v[98:101]
v_mfma_f32_16x16x32_bf16 v[102:105], v[106:109], v[62:65], v[102:105]
global_load_lds_dwordx4 v14, s[42:43] offset:1024 sc1
v_mfma_f32_16x16x32_bf16 v[86:89], a[156:159], v[106:109], v[86:89]
v_mfma_f32_16x16x32_bf16 v[90:93], a[160:163], v[106:109], v[90:93]
s_bitcmp1_b32 s36, 1
s_cbranch_scc1 .Lqa_x1
global_load_lds_dwordx4 v15, s[42:43] offset:2048 sc1
.Lqa_x1:
v_mfma_f32_16x16x32_bf16 v[94:97], v[106:109], a[164:167], v[94:97]
v_mfma_f32_16x16x32_bf16 v[98:101], v[66:69], v[106:109], v[98:101]
s_sub_u32 s42, s42, 18432
s_subb_u32 s43, s43, 0
v_mfma_f32_16x16x32_bf16 v[102:105], v[106:109], v[66:69], v[102:105]
s_waitcnt vmcnt(8) lgkmcnt(0)
s_barrier
ds_read_b64_tr_b16 a[192:193], v255 offset:55296
ds_read_b64_tr_b16 a[194:195], v255 offset:57600
ds_read_b64_tr_b16 a[196:197], v255 offset:55488
ds_read_b64_tr_b16 a[198:199], v255 offset:57792
ds_read_b64_tr_b16 a[200:201], v255 offset:55680
ds_read_b64_tr_b16 a[202:203], v255 offset:57984
ds_read_b64_tr_b16 v[78:79], v163 offset:55296
ds_read_b64_tr_b16 v[80:81], v163 offset:57600
ds_read_b64_tr_b16 a[204:205], v162 offset:8192
ds_read_b64_tr_b16 a[206:207], v162 offset:10496
ds_read_b64_tr_b16 a[208:209], v162 offset:8384
ds_read_b64_tr_b16 a[210:211], v162 offset:10688
ds_read_b64_tr_b16 a[212:213], v162 offset:8576
ds_read_b64_tr_b16 a[214:215], v162 offset:10880
)"
R"(ds_read_b64_tr_b16 v[82:83], v164 offset:8192
ds_read_b64_tr_b16 v[84:85], v164 offset:10496
v_mfma_f32_16x16x32_bf16 v[86:89], a[168:171], v[106:109], v[86:89]
v_mfma_f32_16x16x32_bf16 v[90:93], a[172:175], v[106:109], v[90:93]
v_mfma_f32_16x16x32_bf16 v[94:97], v[106:109], a[176:179], v[94:97]
v_mfma_f32_16x16x32_bf16 v[98:101], v[70:73], v[106:109], v[98:101]
v_mfma_f32_16x16x32_bf16 v[102:105], v[106:109], v[70:73], v[102:105]
v_mfma_f32_16x16x32_bf16 v[86:89], a[180:183], v[106:109], v[86:89]
v_mfma_f32_16x16x32_bf16 v[90:93], a[184:187], v[106:109], v[90:93]
v_mfma_f32_16x16x32_bf16 v[94:97], v[106:109], a[188:191], v[94:97]
v_mfma_f32_16x16x32_bf16 v[98:101], v[74:77], v[106:109], v[98:101]
v_mfma_f32_16x16x32_bf16 v[102:105], v[106:109], v[74:77], v[102:105]
s_waitcnt lgkmcnt(14)
v_mfma_f32_16x16x32_bf16 v[86:89], a[192:195], v[106:109], v[86:89]
s_waitcnt lgkmcnt(12)
v_mfma_f32_16x16x32_bf16 v[90:93], a[196:199], v[106:109], v[90:93]
s_waitcnt lgkmcnt(10)
v_mfma_f32_16x16x32_bf16 v[94:97], v[106:109], a[200:203], v[94:97]
s_waitcnt lgkmcnt(8)
v_mfma_f32_16x16x32_bf16 v[98:101], v[78:81], v[106:109], v[98:101]
v_mfma_f32_16x16x32_bf16 v[102:105], v[106:109], v[78:81], v[102:105]
s_waitcnt lgkmcnt(6)
v_mfma_f32_16x16x32_bf16 v[86:89], a[204:207], v[106:109], v[86:89]
s_waitcnt lgkmcnt(4)
v_mfma_f32_16x16x32_bf16 v[90:93], a[208:211], v[106:109], v[90:93]
s_waitcnt lgkmcnt(2)
v_mfma_f32_16x16x32_bf16 v[94:97], v[106:109], a[212:215], v[94:97]
s_waitcnt lgkmcnt(0)
v_mfma_f32_16x16x32_bf16 v[98:101], v[82:85], v[106:109], v[98:101]
v_mfma_f32_16x16x32_bf16 v[102:105], v[106:109], v[82:85], v[102:105]
s_waitcnt vmcnt(0) lgkmcnt(0)
s_barrier
s_and_b32 s75, s36, 3
s_cmp_eq_u32 s75, 1
s_cbranch_scc1 .Lqa_w2
s_cmp_eq_u32 s75, 2
s_cbranch_scc1 .Lqa_w3
s_cmp_eq_u32 s75, 3
s_cbranch_scc1 .Lqa_w4
.Lqa_w1:
s_nop 7
v_mov_b32 v234, v32
v_mov_b32 v235, v33
v_mov_b32 v236, v34
v_mov_b32 v237, v35
s_mov_b32 s70, 33
s_waitcnt vmcnt(0) lgkmcnt(0)
s_nop 7
s_branch .Lqa_in5
.p2align 6
.Lqa_top15:
s_waitcnt lgkmcnt(9)
v_mfma_f32_16x16x32_bf16 v[134:137], a[216:219], v[44:47], 0
s_waitcnt lgkmcnt(8)
ds_read_b128 v[162:165], v52 offset:4096
ds_read_b32 v8, v30
v_mfma_f32_16x16x32_bf16 v[134:137], a[220:223], v[102:105], v[134:137]
s_waitcnt lgkmcnt(9)
global_store_dwordx4 v16, v[230:233], s[38:39] sc1
v_mfma_f32_16x16x32_bf16 v[110:113], a[0:3], v[44:47], 0
s_waitcnt lgkmcnt(6)
ds_read_b128 v[166:169], v51 offset:6144
v_add_u32 v16, 18432, v16
v_mfma_f32_16x16x32_bf16 v[134:137], a[224:227], v[106:109], v[134:137]
s_waitcnt lgkmcnt(5)
ds_read_b128 v[170:173], v52 offset:6144
ds_read_b128 v[226:229], v25 offset:64
v_mfma_f32_16x16x32_bf16 v[134:137], a[228:231], v[0:3], v[134:137]
ds_read_b64 v[160:161], v22 offset:41472
ds_read_b32 v144, v20
v_mfma_f32_16x16x32_bf16 v[114:117], a[4:7], v[44:47], 0
s_waitcnt lgkmcnt(7)
ds_read_b128 v[174:177], v51 offset:8192
v_fma_f32 v238, v90, v23, v126
v_mfma_f32_16x16x32_bf16 v[134:137], a[232:235], v[4:7], v[134:137]
s_waitcnt lgkmcnt(7)
ds_read_b128 v[178:181], v52 offset:8192
v_fma_f32 v239, v91, v23, v127
v_mfma_f32_16x16x32_bf16 v[134:137], a[236:239], v[162:165], v[134:137]
v_fma_f32 v240, v92, v23, v128
v_fma_f32 v241, v93, v23, v129
v_mfma_f32_16x16x32_bf16 v[118:121], v[44:47], a[8:11], 0
s_waitcnt lgkmcnt(6)
ds_read_b128 v[182:185], v51 offset:10240
v_cvt_pk_bf16_f32 v150, v238, v239
v_mfma_f32_16x16x32_bf16 v[134:137], a[240:243], v[166:169], v[134:137]
s_waitcnt lgkmcnt(6)
ds_read_b128 v[186:189], v52 offset:10240
v_cvt_pk_bf16_f32 v151, v240, v241
v_mfma_f32_16x16x32_bf16 v[134:137], a[244:247], v[170:173], v[134:137]
s_nop 0
v_mfma_f32_16x16x32_bf16 v[214:217], v[150:153], v[138:141], v[32:35]
v_max_f32 v48, v218, v219
v_max_f32 v49, v220, v221
v_mfma_f32_16x16x32_bf16 v[110:113], a[12:15], v[102:105], v[110:113]
s_waitcnt lgkmcnt(6)
ds_read_b128 v[190:193], v51 offset:12288
v_fma_f32 v238, v94, v226, v130
s_waitcnt lgkmcnt(4)
v_mfma_f32_16x16x32_bf16 v[134:137], a[248:251], v[174:177], v[134:137]
s_xor_b32 s49, s49, 0x10000
ds_read_b128 v[194:197], v52 offset:12288
s_waitcnt lgkmcnt(4)
v_mfma_f32_16x16x32_bf16 v[134:137], a[252:255], v[178:181], v[134:137]
s_add_u32 m0, s49, 0
v_mul_f32 v26, 0x3eae1a93, v26
v_mul_f32 v9, v53, v26
v_mfma_f32_16x16x32_bf16 v[114:117], a[16:19], v[102:105], v[114:117]
s_waitcnt lgkmcnt(3)
ds_read_b128 v[198:201], v51 offset:14336
global_load_lds_dwordx4 v11, s[42:43] offset:-2048 sc1
v_mfma_f32_16x16x32_bf16 v[134:137], v[54:57], v[182:185], v[134:137]
s_waitcnt lgkmcnt(3)
ds_read_b128 v[202:205], v52 offset:14336
v_mul_f32 v10, v8, v26
v_mfma_f32_16x16x32_bf16 v[134:137], v[58:61], v[186:189], v[134:137]
v_max3_f32 v48, v214, v215, v48
v_max3_f32 v49, v216, v217, v49
v_mfma_f32_16x16x32_bf16 v[118:121], v[102:105], a[20:23], v[118:121]
s_waitcnt lgkmcnt(3)
ds_read_b128 v[206:209], v51 offset:16384
v_mul_f32 v48, v48, v9
v_mfma_f32_16x16x32_bf16 v[134:137], v[62:65], v[190:193], v[134:137]
s_waitcnt lgkmcnt(3)
ds_read_b128 v[210:213], v52 offset:16384
v_mul_f32 v49, v49, v10
v_mfma_f32_16x16x32_bf16 v[134:137], v[66:69], v[194:197], v[134:137]
v_max_f32 v50, v48, v49
v_mov_b32 v95, v50
v_mfma_f32_16x16x32_bf16 v[110:113], a[24:27], v[106:109], v[110:113]
s_waitcnt lgkmcnt(3)
v_permlane16_swap_b32 v50, v95
v_mfma_f32_16x16x32_bf16 v[134:137], v[70:73], v[198:201], v[134:137]
s_waitcnt lgkmcnt(2)
v_max_f32 v50, v50, v95
v_mov_b32 v95, v50
v_mfma_f32_16x16x32_bf16 v[134:137], v[74:77], v[202:205], v[134:137]
global_load_lds_dwordx4 v12, s[42:43] offset:-1024 sc1
v_fma_f32 v239, v94, v227, v131
v_mfma_f32_16x16x32_bf16 v[114:117], a[28:31], v[106:109], v[114:117]
s_waitcnt lgkmcnt(1)
v_permlane32_swap_b32 v50, v95
v_mfma_f32_16x16x32_bf16 v[134:137], v[78:81], v[206:209], v[134:137]
s_waitcnt lgkmcnt(0)
v_max_f32 v50, v50, v95
v_sub_f32 v48, v48, v50
v_mfma_f32_16x16x32_bf16 v[134:137], v[82:85], v[210:213], v[134:137]
v_sub_f32 v49, v49, v50
v_fma_f32 v240, v94, v228, v132
v_mfma_f32_16x16x32_bf16 v[118:121], v[106:109], a[32:35], v[118:121]
v_exp_f32 v96, v48
v_mfma_f32_16x16x32_bf16 v[110:113], a[36:39], v[0:3], v[110:113]
v_exp_f32 v97, v49
v_mfma_f32_16x16x32_bf16 v[114:117], a[40:43], v[0:3], v[114:117]
v_add_f32 v50, v96, v97
v_mov_b32 v95, v50
v_mfma_f32_16x16x32_bf16 v[118:121], v[0:3], a[44:47], v[118:121]
v_mul_f32 v96, v96, v53
v_fma_f32 v241, v94, v229, v133
v_mfma_f32_16x16x32_bf16 v[110:113], a[48:51], v[4:7], v[110:113]
global_load_lds_dwordx4 v13, s[42:43] offset:0 sc1
v_cvt_pk_bf16_f32 v156, v238, v239
v_mfma_f32_16x16x32_bf16 v[114:117], a[52:55], v[4:7], v[114:117]
v_permlane16_swap_b32 v50, v95
v_mfma_f32_16x16x32_bf16 v[118:121], v[4:7], a[56:59], v[118:121]
v_add_f32 v50, v50, v95
v_mov_b32 v95, v50
v_mfma_f32_16x16x32_bf16 v[110:113], a[60:63], v[162:165], v[110:113]
v_mul_f32 v97, v97, v8
v_cvt_pk_bf16_f32 v157, v240, v241
v_mfma_f32_16x16x32_bf16 v[114:117], a[64:67], v[162:165], v[114:117]
v_permlane32_swap_b32 v50, v95
v_mfma_f32_16x16x32_bf16 v[118:121], v[162:165], a[68:71], v[118:121]
v_mfma_f32_16x16x32_bf16 v[110:113], a[72:75], v[166:169], v[110:113]
s_barrier
v_mfma_f32_16x16x32_bf16 v[114:117], a[76:79], v[166:169], v[114:117]
ds_read_b32 v242, v24 offset:0
v_add_f32 v50, v50, v95
v_mfma_f32_16x16x32_bf16 v[118:121], v[166:169], a[80:83], v[118:121]
global_load_lds_dwordx4 v14, s[42:43] offset:1024 sc1
v_fma_f32 v238, v86, v23, v122
v_mfma_f32_16x16x32_bf16 v[110:113], a[84:87], v[170:173], v[110:113]
v_rcp_f32 v50, v50
v_mfma_f32_16x16x32_bf16 v[114:117], a[88:91], v[170:173], v[114:117]
v_mul_f32 v96, v96, v50
v_mul_f32 v97, v97, v50
v_mfma_f32_16x16x32_bf16 v[118:121], v[170:173], a[92:95], v[118:121]
v_cvt_pk_bf16_f32 v96, v96, v96
v_cvt_pk_bf16_f32 v97, v97, v97
v_mfma_f32_16x16x32_bf16 v[110:113], a[96:99], v[174:177], v[110:113]
v_and_b32 v224, v96, v40
v_and_b32 v225, v97, v41
v_mfma_f32_16x16x32_bf16 v[114:117], a[100:103], v[174:177], v[114:117]
v_and_b32 v222, v96, v42
v_and_b32 v223, v97, v43
v_mfma_f32_16x16x32_bf16 v[118:121], v[174:177], a[104:107], v[118:121]
s_nop 0
v_mfma_f32_16x16x32_bf16 v[218:221], v[154:157], v[222:225], 0
v_mfma_f32_16x16x32_bf16 v[214:217], v[158:161], v[222:225], 0
s_waitcnt lgkmcnt(0)
v_fma_f32 v230, v98, v242, v134
v_fma_f32 v231, v99, v242, v135
v_mfma_f32_16x16x32_bf16 v[110:113], a[108:111], v[178:181], v[110:113]
v_fma_f32 v232, v100, v242, v136
v_fma_f32 v233, v101, v242, v137
v_mfma_f32_16x16x32_bf16 v[114:117], a[112:115], v[178:181], v[114:117]
global_load_lds_dwordx4 v15, s[42:43] offset:2048 sc1
s_add_u32 s42, s42, s46
s_addc_u32 s43, s43, s47
v_mfma_f32_16x16x32_bf16 v[118:121], v[178:181], a[116:119], v[118:121]
v_cvt_pk_bf16_f32 v230, v230, v231
v_cvt_pk_bf16_f32 v231, v232, v233
v_mfma_f32_16x16x32_bf16 v[110:113], a[120:123], v[182:185], v[110:113]
v_cvt_pk_bf16_f32 v254, v218, v219
v_cvt_pk_bf16_f32 v255, v220, v221
v_mfma_f32_16x16x32_bf16 v[114:117], a[124:127], v[182:185], v[114:117]
ds_write_b64 v18, v[254:255]
v_mfma_f32_16x16x32_bf16 v[118:121], v[182:185], a[128:131], v[118:121]
v_cvt_pk_bf16_f32 v96, v214, v215
v_fma_f32 v239, v87, v23, v123
v_mfma_f32_16x16x32_bf16 v[110:113], a[132:135], v[186:189], v[110:113]
ds_write_b32 v19, v96
v_mfma_f32_16x16x32_bf16 v[114:117], a[136:139], v[186:189], v[114:117]
ds_write_b64 v22, v[230:231] offset:40960
v_mfma_f32_16x16x32_bf16 v[118:121], v[186:189], a[140:143], v[118:121]
v_fma_f32 v240, v88, v23, v124
v_fma_f32 v241, v89, v23, v125
v_mfma_f32_16x16x32_bf16 v[110:113], a[144:147], v[190:193], v[110:113]
v_cvt_pk_bf16_f32 v142, v238, v239
v_cvt_pk_bf16_f32 v143, v240, v241
v_mfma_f32_16x16x32_bf16 v[114:117], a[148:151], v[190:193], v[114:117]
v_mfma_f32_16x16x32_bf16 v[118:121], v[190:193], a[152:155], v[118:121]
v_mfma_f32_16x16x32_bf16 v[110:113], a[156:159], v[194:197], v[110:113]
v_mfma_f32_16x16x32_bf16 v[114:117], a[160:163], v[194:197], v[114:117]
v_mfma_f32_16x16x32_bf16 v[118:121], v[194:197], a[164:167], v[118:121]
v_mfma_f32_16x16x32_bf16 v[110:113], a[168:171], v[198:201], v[110:113]
v_mfma_f32_16x16x32_bf16 v[114:117], a[172:175], v[198:201], v[114:117]
v_mfma_f32_16x16x32_bf16 v[118:121], v[198:201], a[176:179], v[118:121]
v_mfma_f32_16x16x32_bf16 v[110:113], a[180:183], v[202:205], v[110:113]
v_mfma_f32_16x16x32_bf16 v[114:117], a[184:187], v[202:205], v[114:117]
s_waitcnt vmcnt(5) lgkmcnt(0)
s_barrier
v_mfma_f32_16x16x32_bf16 v[118:121], v[202:205], a[188:191], v[118:121]
ds_read_b128 v[44:47], v51 offset:18432
v_mfma_f32_16x16x32_bf16 v[110:113], a[192:195], v[206:209], v[110:113]
ds_read_b128 v[102:105], v52 offset:18432
ds_read_b128 v[230:233], v17 offset:0
v_mfma_f32_16x16x32_bf16 v[114:117], a[196:199], v[206:209], v[114:117]
ds_read_b32 v23, v24 offset:0
ds_read_b32 v148, v21
v_mfma_f32_16x16x32_bf16 v[218:221], v[150:153], v[142:145], v[36:39]
v_mfma_f32_16x16x32_bf16 v[118:121], v[206:209], a[200:203], v[118:121]
ds_read_b128 v[106:109], v51 offset:20480
ds_read_b32 v26, v24 offset:192
v_mfma_f32_16x16x32_bf16 v[110:113], a[204:207], v[210:213], v[110:113]
ds_read_b128 v[0:3], v52 offset:20480
ds_read_b32 v53, v27
v_mfma_f32_16x16x32_bf16 v[114:117], a[208:211], v[210:213], v[114:117]
ds_read_b32 v8, v28
ds_read_b128 v[226:229], v25 offset:0
v_mfma_f32_16x16x32_bf16 v[118:121], v[210:213], a[212:215], v[118:121]
s_waitcnt lgkmcnt(10)
ds_read_b128 v[4:7], v51 offset:22528
ds_read_b64 v[158:159], v22 offset:41472
v_mfma_f32_16x16x32_bf16 v[134:137], a[216:219], v[44:47], 0
s_waitcnt lgkmcnt(11)
ds_read_b128 v[162:165], v52 offset:22528
ds_read_b32 v140, v20
v_mfma_f32_16x16x32_bf16 v[134:137], a[220:223], v[102:105], v[134:137]
s_waitcnt lgkmcnt(12)
global_store_dwordx4 v16, v[230:233], s[38:39] sc1
v_mfma_f32_16x16x32_bf16 v[122:125], a[0:3], v[44:47], 0
s_waitcnt lgkmcnt(9)
ds_read_b128 v[166:169], v51 offset:24576
v_add_u32 v16, 18432, v16
v_mfma_f32_16x16x32_bf16 v[134:137], a[224:227], v[106:109], v[134:137]
s_waitcnt lgkmcnt(8)
ds_read_b128 v[170:173], v52 offset:24576
v_fma_f32 v238, v90, v23, v114
v_mfma_f32_16x16x32_bf16 v[134:137], a[228:231], v[0:3], v[134:137]
v_fma_f32 v239, v91, v23, v115
v_fma_f32 v240, v92, v23, v116
v_mfma_f32_16x16x32_bf16 v[126:129], a[4:7], v[44:47], 0
s_waitcnt lgkmcnt(5)
ds_read_b128 v[174:177], v51 offset:26624
v_fma_f32 v241, v93, v23, v117
v_mfma_f32_16x16x32_bf16 v[134:137], a[232:235], v[4:7], v[134:137]
s_waitcnt lgkmcnt(4)
ds_read_b128 v[178:181], v52 offset:26624
v_cvt_pk_bf16_f32 v146, v238, v239
v_mfma_f32_16x16x32_bf16 v[134:137], a[236:239], v[162:165], v[134:137]
v_cvt_pk_bf16_f32 v147, v240, v241
v_max_f32 v48, v218, v219
v_mfma_f32_16x16x32_bf16 v[130:133], v[44:47], a[8:11], 0
ds_read_b128 v[182:185], v51 offset:28672
v_mfma_f32_16x16x32_bf16 v[214:217], v[146:149], v[142:145], v[234:237]
s_waitcnt lgkmcnt(4)
v_max_f32 v49, v220, v221
v_fma_f32 v238, v94, v226, v118
v_mfma_f32_16x16x32_bf16 v[134:137], a[240:243], v[166:169], v[134:137]
s_waitcnt lgkmcnt(3)
ds_read_b128 v[186:189], v52 offset:28672
v_fma_f32 v239, v94, v227, v119
v_mfma_f32_16x16x32_bf16 v[134:137], a[244:247], v[170:173], v[134:137]
v_mul_f32 v26, 0x3eae1a93, v26
v_mul_f32 v9, v53, v26
v_mfma_f32_16x16x32_bf16 v[122:125], a[12:15], v[102:105], v[122:125]
s_waitcnt lgkmcnt(3)
ds_read_b128 v[190:193], v51 offset:30720
v_mul_f32 v10, v8, v26
v_mfma_f32_16x16x32_bf16 v[134:137], a[248:251], v[174:177], v[134:137]
s_add_u32 m0, s49, 18432
ds_read_b128 v[194:197], v52 offset:30720
s_waitcnt lgkmcnt(4)
v_mfma_f32_16x16x32_bf16 v[134:137], a[252:255], v[178:181], v[134:137]
global_load_lds_dwordx4 v11, s[42:43] offset:-2048 sc1
v_max3_f32 v48, v214, v215, v48
v_mfma_f32_16x16x32_bf16 v[126:129], a[16:19], v[102:105], v[126:129]
s_waitcnt lgkmcnt(3)
ds_read_b128 v[198:201], v51 offset:32768
v_max3_f32 v49, v216, v217, v49
v_mfma_f32_16x16x32_bf16 v[134:137], v[54:57], v[182:185], v[134:137]
s_waitcnt lgkmcnt(3)
ds_read_b128 v[202:205], v52 offset:32768
v_mul_f32 v48, v48, v9
v_mfma_f32_16x16x32_bf16 v[134:137], v[58:61], v[186:189], v[134:137]
v_mul_f32 v49, v49, v10
v_max_f32 v50, v48, v49
v_mfma_f32_16x16x32_bf16 v[130:133], v[102:105], a[20:23], v[130:133]
s_waitcnt lgkmcnt(3)
ds_read_b128 v[206:209], v51 offset:34816
v_mov_b32 v95, v50
v_mfma_f32_16x16x32_bf16 v[134:137], v[62:65], v[190:193], v[134:137]
s_waitcnt lgkmcnt(3)
ds_read_b128 v[210:213], v52 offset:34816
v_fma_f32 v240, v94, v228, v120
v_mfma_f32_16x16x32_bf16 v[134:137], v[66:69], v[194:197], v[134:137]
v_permlane16_swap_b32 v50, v95
v_mfma_f32_16x16x32_bf16 v[122:125], a[24:27], v[106:109], v[122:125]
s_waitcnt lgkmcnt(3)
v_max_f32 v50, v50, v95
v_mov_b32 v95, v50
v_mfma_f32_16x16x32_bf16 v[134:137], v[70:73], v[198:201], v[134:137]
s_waitcnt lgkmcnt(2)
global_load_lds_dwordx4 v12, s[42:43] offset:-1024 sc1
v_fma_f32 v241, v94, v229, v121
v_mfma_f32_16x16x32_bf16 v[134:137], v[74:77], v[202:205], v[134:137]
v_permlane32_swap_b32 v50, v95
v_mfma_f32_16x16x32_bf16 v[126:129], a[28:31], v[106:109], v[126:129]
s_waitcnt lgkmcnt(1)
v_max_f32 v50, v50, v95
v_sub_f32 v48, v48, v50
v_mfma_f32_16x16x32_bf16 v[134:137], v[78:81], v[206:209], v[134:137]
s_waitcnt lgkmcnt(0)
v_sub_f32 v49, v49, v50
v_cvt_pk_bf16_f32 v154, v238, v239
v_mfma_f32_16x16x32_bf16 v[134:137], v[82:85], v[210:213], v[134:137]
v_exp_f32 v96, v48
v_mfma_f32_16x16x32_bf16 v[130:133], v[106:109], a[32:35], v[130:133]
v_exp_f32 v97, v49
v_mfma_f32_16x16x32_bf16 v[122:125], a[36:39], v[0:3], v[122:125]
v_add_f32 v50, v96, v97
v_mov_b32 v95, v50
v_mfma_f32_16x16x32_bf16 v[126:129], a[40:43], v[0:3], v[126:129]
v_mul_f32 v96, v96, v53
v_cvt_pk_bf16_f32 v155, v240, v241
v_mfma_f32_16x16x32_bf16 v[130:133], v[0:3], a[44:47], v[130:133]
global_load_lds_dwordx4 v13, s[42:43] offset:0 sc1
v_fma_f32 v238, v86, v23, v110
v_mfma_f32_16x16x32_bf16 v[122:125], a[48:51], v[4:7], v[122:125]
v_permlane16_swap_b32 v50, v95
v_mfma_f32_16x16x32_bf16 v[126:129], a[52:55], v[4:7], v[126:129]
v_add_f32 v50, v50, v95
v_mov_b32 v95, v50
v_mfma_f32_16x16x32_bf16 v[130:133], v[4:7], a[56:59], v[130:133]
v_mul_f32 v97, v97, v8
v_fma_f32 v239, v87, v23, v111
v_mfma_f32_16x16x32_bf16 v[122:125], a[60:63], v[162:165], v[122:125]
v_permlane32_swap_b32 v50, v95
v_mfma_f32_16x16x32_bf16 v[126:129], a[64:67], v[162:165], v[126:129]
v_add_f32 v50, v50, v95
v_fma_f32 v240, v88, v23, v112
v_mfma_f32_16x16x32_bf16 v[130:133], v[162:165], a[68:71], v[130:133]
v_mfma_f32_16x16x32_bf16 v[122:125], a[72:75], v[166:169], v[122:125]
s_barrier
v_mfma_f32_16x16x32_bf16 v[126:129], a[76:79], v[166:169], v[126:129]
global_load_lds_dwordx4 v14, s[42:43] offset:1024 sc1
ds_read_b32 v242, v24 offset:64
v_mfma_f32_16x16x32_bf16 v[130:133], v[166:169], a[80:83], v[130:133]
v_rcp_f32 v50, v50
v_mfma_f32_16x16x32_bf16 v[122:125], a[84:87], v[170:173], v[122:125]
v_mul_f32 v96, v96, v50
v_mul_f32 v97, v97, v50
v_mfma_f32_16x16x32_bf16 v[126:129], a[88:91], v[170:173], v[126:129]
v_cvt_pk_bf16_f32 v96, v96, v96
v_cvt_pk_bf16_f32 v97, v97, v97
v_mfma_f32_16x16x32_bf16 v[130:133], v[170:173], a[92:95], v[130:133]
v_and_b32 v222, v96, v40
v_and_b32 v223, v97, v41
v_mfma_f32_16x16x32_bf16 v[122:125], a[96:99], v[174:177], v[122:125]
)"
R"(v_and_b32 v224, v96, v42
v_and_b32 v225, v97, v43
v_mfma_f32_16x16x32_bf16 v[126:129], a[100:103], v[174:177], v[126:129]
s_nop 0
v_mfma_f32_16x16x32_bf16 v[218:221], v[154:157], v[222:225], 0
v_mfma_f32_16x16x32_bf16 v[214:217], v[158:161], v[222:225], 0
s_waitcnt lgkmcnt(0)
v_fma_f32 v230, v98, v242, v134
v_fma_f32 v231, v99, v242, v135
v_mfma_f32_16x16x32_bf16 v[130:133], v[174:177], a[104:107], v[130:133]
v_fma_f32 v232, v100, v242, v136
v_fma_f32 v233, v101, v242, v137
v_mfma_f32_16x16x32_bf16 v[122:125], a[108:111], v[178:181], v[122:125]
global_load_lds_dwordx4 v15, s[42:43] offset:2048 sc1
s_sub_u32 s42, s42, 18432
s_subb_u32 s43, s43, 0
v_mfma_f32_16x16x32_bf16 v[126:129], a[112:115], v[178:181], v[126:129]
v_cvt_pk_bf16_f32 v230, v230, v231
v_cvt_pk_bf16_f32 v231, v232, v233
v_mfma_f32_16x16x32_bf16 v[130:133], v[178:181], a[116:119], v[130:133]
v_cvt_pk_bf16_f32 v254, v218, v219
v_cvt_pk_bf16_f32 v255, v220, v221
v_mfma_f32_16x16x32_bf16 v[122:125], a[120:123], v[182:185], v[122:125]
ds_write_b64 v18, v[254:255]
v_mfma_f32_16x16x32_bf16 v[126:129], a[124:127], v[182:185], v[126:129]
v_cvt_pk_bf16_f32 v96, v214, v215
v_fma_f32 v241, v89, v23, v113
v_mfma_f32_16x16x32_bf16 v[130:133], v[182:185], a[128:131], v[130:133]
ds_write_b32 v19, v96
v_mfma_f32_16x16x32_bf16 v[122:125], a[132:135], v[186:189], v[122:125]
ds_write_b64 v22, v[230:231] offset:40960
v_mfma_f32_16x16x32_bf16 v[126:129], a[136:139], v[186:189], v[126:129]
v_cvt_pk_bf16_f32 v138, v238, v239
v_cvt_pk_bf16_f32 v139, v240, v241
v_mfma_f32_16x16x32_bf16 v[130:133], v[186:189], a[140:143], v[130:133]
v_mfma_f32_16x16x32_bf16 v[122:125], a[144:147], v[190:193], v[122:125]
v_mfma_f32_16x16x32_bf16 v[126:129], a[148:151], v[190:193], v[126:129]
v_mfma_f32_16x16x32_bf16 v[130:133], v[190:193], a[152:155], v[130:133]
v_mfma_f32_16x16x32_bf16 v[122:125], a[156:159], v[194:197], v[122:125]
v_mfma_f32_16x16x32_bf16 v[126:129], a[160:163], v[194:197], v[126:129]
v_mfma_f32_16x16x32_bf16 v[130:133], v[194:197], a[164:167], v[130:133]
v_mfma_f32_16x16x32_bf16 v[122:125], a[168:171], v[198:201], v[122:125]
v_mfma_f32_16x16x32_bf16 v[126:129], a[172:175], v[198:201], v[126:129]
v_xor_b32 v51, 0x10000, v51
v_xor_b32 v52, 0x10000, v52
v_mfma_f32_16x16x32_bf16 v[130:133], v[198:201], a[176:179], v[130:133]
v_mfma_f32_16x16x32_bf16 v[122:125], a[180:183], v[202:205], v[122:125]
v_mfma_f32_16x16x32_bf16 v[126:129], a[184:187], v[202:205], v[126:129]
s_waitcnt vmcnt(5) lgkmcnt(0)
s_barrier
.Lqa_in5:
v_mfma_f32_16x16x32_bf16 v[130:133], v[202:205], a[188:191], v[130:133]
s_cmp_eq_u32 s70, s73
s_cbranch_scc1 .Lqa_hk6
.Lqa_hr7:
ds_read_b128 v[44:47], v51 offset:0
v_mfma_f32_16x16x32_bf16 v[122:125], a[192:195], v[206:209], v[122:125]
ds_read_b128 v[102:105], v52 offset:0
ds_read_b128 v[230:233], v17 offset:0
v_mfma_f32_16x16x32_bf16 v[126:129], a[196:199], v[206:209], v[126:129]
ds_read_b32 v23, v24 offset:64
ds_read_b32 v152, v21
v_mfma_f32_16x16x32_bf16 v[218:221], v[146:149], v[138:141], v[36:39]
v_mfma_f32_16x16x32_bf16 v[130:133], v[206:209], a[200:203], v[130:133]
ds_read_b128 v[106:109], v51 offset:2048
ds_read_b32 v26, v24 offset:128
v_mfma_f32_16x16x32_bf16 v[122:125], a[204:207], v[210:213], v[122:125]
ds_read_b128 v[0:3], v52 offset:2048
ds_read_b32 v53, v29
v_mfma_f32_16x16x32_bf16 v[126:129], a[208:211], v[210:213], v[126:129]
v_mfma_f32_16x16x32_bf16 v[130:133], v[210:213], a[212:215], v[130:133]
s_sub_u32 s70, s70, 1
ds_read_b128 v[4:7], v51 offset:4096
s_cbranch_scc0 .Lqa_top15
s_waitcnt lgkmcnt(0)
ds_read_b32 v8, v30
global_store_dwordx4 v16, v[230:233], s[38:39] sc1
v_add_u32 v16, 18432, v16
ds_read_b128 v[226:229], v25 offset:64
ds_read_b64 v[160:161], v22 offset:41472
ds_read_b32 v144, v20
v_fma_f32 v238, v90, v23, v126
v_fma_f32 v239, v91, v23, v127
v_fma_f32 v240, v92, v23, v128
v_fma_f32 v241, v93, v23, v129
v_cvt_pk_bf16_f32 v150, v238, v239
v_cvt_pk_bf16_f32 v151, v240, v241
s_nop 1
v_mfma_f32_16x16x32_bf16 v[214:217], v[150:153], v[138:141], v[32:35]
s_waitcnt lgkmcnt(2)
v_max_f32 v48, v218, v219
v_max_f32 v49, v220, v221
v_fma_f32 v238, v94, v226, v130
v_mul_f32 v26, 0x3eae1a93, v26
v_mul_f32 v9, v53, v26
v_mul_f32 v10, v8, v26
s_nop 0
v_max3_f32 v48, v214, v215, v48
v_max3_f32 v49, v216, v217, v49
v_mul_f32 v48, v48, v9
v_mul_f32 v49, v49, v10
v_max_f32 v50, v48, v49
v_mov_b32 v95, v50
s_nop 1
v_permlane16_swap_b32 v50, v95
v_max_f32 v50, v50, v95
v_mov_b32 v95, v50
v_fma_f32 v239, v94, v227, v131
s_nop 0
v_permlane32_swap_b32 v50, v95
v_max_f32 v50, v50, v95
v_sub_f32 v48, v48, v50
v_sub_f32 v49, v49, v50
v_fma_f32 v240, v94, v228, v132
v_exp_f32 v96, v48
v_exp_f32 v97, v49
s_nop 0
v_add_f32 v50, v96, v97
v_mov_b32 v95, v50
v_mul_f32 v96, v96, v53
v_fma_f32 v241, v94, v229, v133
v_cvt_pk_bf16_f32 v156, v238, v239
v_permlane16_swap_b32 v50, v95
v_add_f32 v50, v50, v95
v_mov_b32 v95, v50
v_mul_f32 v97, v97, v8
v_cvt_pk_bf16_f32 v157, v240, v241
v_permlane32_swap_b32 v50, v95
s_waitcnt lgkmcnt(0)
s_barrier
v_add_f32 v50, v50, v95
v_fma_f32 v238, v86, v23, v122
v_rcp_f32 v50, v50
s_nop 0
v_mul_f32 v96, v96, v50
v_mul_f32 v97, v97, v50
v_cvt_pk_bf16_f32 v96, v96, v96
v_cvt_pk_bf16_f32 v97, v97, v97
v_and_b32 v224, v96, v40
v_and_b32 v225, v97, v41
v_and_b32 v222, v96, v42
v_and_b32 v223, v97, v43
s_nop 1
v_mfma_f32_16x16x32_bf16 v[218:221], v[154:157], v[222:225], 0
v_mfma_f32_16x16x32_bf16 v[214:217], v[158:161], v[222:225], 0
s_nop 6
v_cvt_pk_bf16_f32 v254, v218, v219
v_cvt_pk_bf16_f32 v255, v220, v221
ds_write_b64 v18, v[254:255]
v_cvt_pk_bf16_f32 v96, v214, v215
v_fma_f32 v239, v87, v23, v123
ds_write_b32 v19, v96
v_fma_f32 v240, v88, v23, v124
v_fma_f32 v241, v89, v23, v125
v_cvt_pk_bf16_f32 v142, v238, v239
v_cvt_pk_bf16_f32 v143, v240, v241
s_waitcnt vmcnt(0)
s_waitcnt lgkmcnt(0)
s_barrier
ds_read_b128 v[230:233], v17 offset:0
s_waitcnt lgkmcnt(0)
global_store_dwordx4 v16, v[230:233], s[38:39] sc1
s_waitcnt vmcnt(0)
s_barrier
s_endpgm
.Lqa_w2:
s_nop 7
v_mov_b32 v234, v32
v_mov_b32 v235, v33
v_mov_b32 v236, v34
v_mov_b32 v237, v35
s_mov_b32 s70, 33
s_waitcnt vmcnt(0) lgkmcnt(0)
s_nop 7
s_branch .Lqa_in16
.p2align 6
.Lqa_top26:
s_waitcnt lgkmcnt(9)
v_mfma_f32_16x16x32_bf16 v[134:137], v[44:47], a[216:219], 0
s_waitcnt lgkmcnt(8)
ds_read_b128 v[162:165], v52 offset:4096
ds_read_b32 v95, v30
v_mfma_f32_16x16x32_bf16 v[134:137], v[96:99], a[220:223], v[134:137]
ds_read_b128 v[226:229], v25 offset:64
ds_read_b64 v[160:161], v22 offset:41472
v_mfma_f32_16x16x32_bf16 v[110:113], a[0:3], v[44:47], 0
s_waitcnt lgkmcnt(8)
ds_read_b128 v[166:169], v51 offset:6144
ds_read_b32 v144, v20
v_mfma_f32_16x16x32_bf16 v[134:137], v[104:107], a[224:227], v[134:137]
s_waitcnt lgkmcnt(8)
ds_read_b128 v[170:173], v52 offset:6144
v_fma_f32 v238, v90, v23, v126
v_mfma_f32_16x16x32_bf16 v[134:137], v[0:3], a[228:231], v[134:137]
global_store_dwordx4 v16, v[230:233], s[38:39] sc1
v_mfma_f32_16x16x32_bf16 v[114:117], a[4:7], v[44:47], 0
s_waitcnt lgkmcnt(7)
ds_read_b128 v[174:177], v51 offset:8192
v_add_u32 v16, 18432, v16
v_mfma_f32_16x16x32_bf16 v[134:137], v[4:7], a[232:235], v[134:137]
s_waitcnt lgkmcnt(7)
ds_read_b128 v[178:181], v52 offset:8192
v_fma_f32 v239, v91, v23, v127
v_mfma_f32_16x16x32_bf16 v[134:137], v[162:165], a[236:239], v[134:137]
v_fma_f32 v240, v92, v23, v128
v_fma_f32 v241, v93, v23, v129
v_mfma_f32_16x16x32_bf16 v[118:121], v[44:47], a[8:11], 0
s_waitcnt lgkmcnt(4)
ds_read_b128 v[182:185], v51 offset:10240
v_cvt_pk_bf16_f32 v150, v238, v239
v_mfma_f32_16x16x32_bf16 v[134:137], v[166:169], a[240:243], v[134:137]
s_waitcnt lgkmcnt(3)
ds_read_b128 v[186:189], v52 offset:10240
v_cvt_pk_bf16_f32 v151, v240, v241
v_mfma_f32_16x16x32_bf16 v[134:137], v[170:173], a[244:247], v[134:137]
s_nop 0
v_mfma_f32_16x16x32_bf16 v[214:217], v[150:153], v[138:141], v[32:35]
v_max_f32 v109, v218, v219
v_max_f32 v254, v220, v221
v_mfma_f32_16x16x32_bf16 v[110:113], a[12:15], v[96:99], v[110:113]
s_waitcnt lgkmcnt(3)
ds_read_b128 v[190:193], v51 offset:12288
v_fma_f32 v238, v94, v226, v130
v_mfma_f32_16x16x32_bf16 v[134:137], v[174:177], a[248:251], v[134:137]
s_xor_b32 s49, s49, 0x10000
ds_read_b128 v[194:197], v52 offset:12288
s_waitcnt lgkmcnt(4)
v_mfma_f32_16x16x32_bf16 v[134:137], v[178:181], a[252:255], v[134:137]
s_add_u32 m0, s49, 0
v_mul_f32 v26, 0x3eae1a93, v26
v_mul_f32 v103, v53, v26
v_mfma_f32_16x16x32_bf16 v[114:117], a[16:19], v[96:99], v[114:117]
s_waitcnt lgkmcnt(3)
ds_read_b128 v[198:201], v51 offset:14336
v_mul_f32 v108, v95, v26
v_mfma_f32_16x16x32_bf16 v[134:137], v[182:185], v[54:57], v[134:137]
s_waitcnt lgkmcnt(3)
ds_read_b128 v[202:205], v52 offset:14336
v_max3_f32 v109, v214, v215, v109
v_mfma_f32_16x16x32_bf16 v[134:137], v[186:189], v[58:61], v[134:137]
global_load_lds_dwordx4 v11, s[42:43] offset:-2048 sc1
v_max3_f32 v254, v216, v217, v254
v_mfma_f32_16x16x32_bf16 v[118:121], v[96:99], a[20:23], v[118:121]
s_waitcnt lgkmcnt(3)
ds_read_b128 v[206:209], v51 offset:16384
v_mul_f32 v109, v109, v103
v_mfma_f32_16x16x32_bf16 v[134:137], v[190:193], v[62:65], v[134:137]
s_waitcnt lgkmcnt(3)
ds_read_b128 v[210:213], v52 offset:16384
v_mul_f32 v254, v254, v108
v_mfma_f32_16x16x32_bf16 v[134:137], v[194:197], v[66:69], v[134:137]
v_max_f32 v255, v109, v254
v_mov_b32 v8, v255
v_mfma_f32_16x16x32_bf16 v[110:113], a[24:27], v[104:107], v[110:113]
s_waitcnt lgkmcnt(3)
v_permlane16_swap_b32 v255, v8
v_mfma_f32_16x16x32_bf16 v[134:137], v[198:201], v[70:73], v[134:137]
s_waitcnt lgkmcnt(2)
v_max_f32 v255, v255, v8
v_mov_b32 v8, v255
v_mfma_f32_16x16x32_bf16 v[134:137], v[202:205], v[74:77], v[134:137]
s_nop 0
v_permlane32_swap_b32 v255, v8
v_mfma_f32_16x16x32_bf16 v[114:117], a[28:31], v[104:107], v[114:117]
s_waitcnt lgkmcnt(1)
v_max_f32 v255, v255, v8
v_sub_f32 v109, v109, v255
v_mfma_f32_16x16x32_bf16 v[134:137], v[206:209], v[78:81], v[134:137]
s_waitcnt lgkmcnt(0)
global_load_lds_dwordx4 v12, s[42:43] offset:-1024 sc1
v_sub_f32 v254, v254, v255
v_mfma_f32_16x16x32_bf16 v[134:137], v[210:213], v[82:85], v[134:137]
v_exp_f32 v9, v109
v_mfma_f32_16x16x32_bf16 v[118:121], v[104:107], a[32:35], v[118:121]
v_exp_f32 v10, v254
v_mfma_f32_16x16x32_bf16 v[110:113], a[36:39], v[0:3], v[110:113]
v_add_f32 v255, v9, v10
v_mov_b32 v8, v255
v_mfma_f32_16x16x32_bf16 v[114:117], a[40:43], v[0:3], v[114:117]
v_mul_f32 v9, v9, v53
v_fma_f32 v239, v94, v227, v131
v_mfma_f32_16x16x32_bf16 v[118:121], v[0:3], a[44:47], v[118:121]
v_permlane16_swap_b32 v255, v8
v_mfma_f32_16x16x32_bf16 v[110:113], a[48:51], v[4:7], v[110:113]
v_add_f32 v255, v255, v8
v_mov_b32 v8, v255
v_mfma_f32_16x16x32_bf16 v[114:117], a[52:55], v[4:7], v[114:117]
v_mul_f32 v10, v10, v95
v_fma_f32 v240, v94, v228, v132
v_mfma_f32_16x16x32_bf16 v[118:121], v[4:7], a[56:59], v[118:121]
global_load_lds_dwordx4 v13, s[42:43] offset:0 sc1
v_fma_f32 v241, v94, v229, v133
v_mfma_f32_16x16x32_bf16 v[110:113], a[60:63], v[162:165], v[110:113]
v_permlane32_swap_b32 v255, v8
v_mfma_f32_16x16x32_bf16 v[114:117], a[64:67], v[162:165], v[114:117]
v_add_f32 v255, v255, v8
v_cvt_pk_bf16_f32 v156, v238, v239
v_mfma_f32_16x16x32_bf16 v[118:121], v[162:165], a[68:71], v[118:121]
v_mfma_f32_16x16x32_bf16 v[110:113], a[72:75], v[166:169], v[110:113]
s_barrier
v_mfma_f32_16x16x32_bf16 v[114:117], a[76:79], v[166:169], v[114:117]
ds_read_b128 v[242:245], v25 offset:0
v_cvt_pk_bf16_f32 v157, v240, v241
v_mfma_f32_16x16x32_bf16 v[118:121], v[166:169], a[80:83], v[118:121]
v_rcp_f32 v255, v255
v_mfma_f32_16x16x32_bf16 v[110:113], a[84:87], v[170:173], v[110:113]
v_mul_f32 v9, v9, v255
v_mul_f32 v10, v10, v255
v_mfma_f32_16x16x32_bf16 v[114:117], a[88:91], v[170:173], v[114:117]
global_load_lds_dwordx4 v14, s[42:43] offset:1024 sc1
v_cvt_pk_bf16_f32 v9, v9, v9
v_mfma_f32_16x16x32_bf16 v[118:121], v[170:173], a[92:95], v[118:121]
v_cvt_pk_bf16_f32 v10, v10, v10
v_and_b32 v224, v9, v40
v_mfma_f32_16x16x32_bf16 v[110:113], a[96:99], v[174:177], v[110:113]
v_and_b32 v225, v10, v41
v_and_b32 v222, v9, v42
v_mfma_f32_16x16x32_bf16 v[114:117], a[100:103], v[174:177], v[114:117]
s_waitcnt lgkmcnt(0)
v_and_b32 v223, v10, v43
v_fma_f32 v230, v102, v242, v134
v_mfma_f32_16x16x32_bf16 v[118:121], v[174:177], a[104:107], v[118:121]
v_mfma_f32_16x16x32_bf16 v[218:221], v[154:157], v[222:225], 0
v_mfma_f32_16x16x32_bf16 v[214:217], v[158:161], v[222:225], 0
v_fma_f32 v231, v102, v243, v135
v_fma_f32 v232, v102, v244, v136
v_mfma_f32_16x16x32_bf16 v[110:113], a[108:111], v[178:181], v[110:113]
v_fma_f32 v233, v102, v245, v137
v_cvt_pk_bf16_f32 v230, v230, v231
v_mfma_f32_16x16x32_bf16 v[114:117], a[112:115], v[178:181], v[114:117]
v_cvt_pk_bf16_f32 v231, v232, v233
v_fma_f32 v238, v86, v23, v122
v_mfma_f32_16x16x32_bf16 v[118:121], v[178:181], a[116:119], v[118:121]
ds_write_b64 v22, v[230:231] offset:41472
v_mfma_f32_16x16x32_bf16 v[110:113], a[120:123], v[182:185], v[110:113]
global_load_lds_dwordx4 v15, s[42:43] offset:2048 sc1
s_add_u32 s42, s42, s46
s_addc_u32 s43, s43, s47
v_mfma_f32_16x16x32_bf16 v[114:117], a[124:127], v[182:185], v[114:117]
v_cvt_pk_bf16_f32 v100, v218, v219
v_cvt_pk_bf16_f32 v101, v220, v221
v_mfma_f32_16x16x32_bf16 v[118:121], v[182:185], a[128:131], v[118:121]
ds_write_b64 v18, v[100:101]
v_mfma_f32_16x16x32_bf16 v[110:113], a[132:135], v[186:189], v[110:113]
v_cvt_pk_bf16_f32 v9, v216, v217
v_fma_f32 v239, v87, v23, v123
v_mfma_f32_16x16x32_bf16 v[114:117], a[136:139], v[186:189], v[114:117]
ds_write_b32 v19, v9
v_mfma_f32_16x16x32_bf16 v[118:121], v[186:189], a[140:143], v[118:121]
v_fma_f32 v240, v88, v23, v124
v_fma_f32 v241, v89, v23, v125
v_mfma_f32_16x16x32_bf16 v[110:113], a[144:147], v[190:193], v[110:113]
v_cvt_pk_bf16_f32 v142, v238, v239
v_cvt_pk_bf16_f32 v143, v240, v241
v_mfma_f32_16x16x32_bf16 v[114:117], a[148:151], v[190:193], v[114:117]
v_mfma_f32_16x16x32_bf16 v[118:121], v[190:193], a[152:155], v[118:121]
v_mfma_f32_16x16x32_bf16 v[110:113], a[156:159], v[194:197], v[110:113]
v_mfma_f32_16x16x32_bf16 v[114:117], a[160:163], v[194:197], v[114:117]
v_mfma_f32_16x16x32_bf16 v[118:121], v[194:197], a[164:167], v[118:121]
v_mfma_f32_16x16x32_bf16 v[110:113], a[168:171], v[198:201], v[110:113]
v_mfma_f32_16x16x32_bf16 v[114:117], a[172:175], v[198:201], v[114:117]
v_mfma_f32_16x16x32_bf16 v[118:121], v[198:201], a[176:179], v[118:121]
v_mfma_f32_16x16x32_bf16 v[110:113], a[180:183], v[202:205], v[110:113]
v_mfma_f32_16x16x32_bf16 v[114:117], a[184:187], v[202:205], v[114:117]
s_waitcnt vmcnt(5) lgkmcnt(0)
s_barrier
v_mfma_f32_16x16x32_bf16 v[118:121], v[202:205], a[188:191], v[118:121]
ds_read_b128 v[44:47], v51 offset:18432
v_mfma_f32_16x16x32_bf16 v[110:113], a[192:195], v[206:209], v[110:113]
ds_read_b128 v[96:99], v52 offset:18432
ds_read_b128 v[230:233], v17 offset:0
v_mfma_f32_16x16x32_bf16 v[114:117], a[196:199], v[206:209], v[114:117]
ds_read_b32 v23, v24 offset:0
ds_read_b32 v148, v21
v_mfma_f32_16x16x32_bf16 v[218:221], v[150:153], v[142:145], v[36:39]
v_mfma_f32_16x16x32_bf16 v[118:121], v[206:209], a[200:203], v[118:121]
ds_read_b128 v[104:107], v51 offset:20480
ds_read_b32 v26, v24 offset:192
v_mfma_f32_16x16x32_bf16 v[110:113], a[204:207], v[210:213], v[110:113]
ds_read_b128 v[0:3], v52 offset:20480
ds_read_b32 v53, v27
v_mfma_f32_16x16x32_bf16 v[114:117], a[208:211], v[210:213], v[114:117]
ds_read_b32 v95, v28
ds_read_b128 v[226:229], v25 offset:0
v_mfma_f32_16x16x32_bf16 v[118:121], v[210:213], a[212:215], v[118:121]
s_waitcnt lgkmcnt(10)
ds_read_b128 v[4:7], v51 offset:22528
ds_read_b64 v[158:159], v22 offset:41472
v_mfma_f32_16x16x32_bf16 v[134:137], v[44:47], a[216:219], 0
s_waitcnt lgkmcnt(11)
ds_read_b128 v[162:165], v52 offset:22528
ds_read_b32 v140, v20
v_mfma_f32_16x16x32_bf16 v[134:137], v[96:99], a[220:223], v[134:137]
v_max_f32 v109, v218, v219
v_max_f32 v254, v220, v221
v_mfma_f32_16x16x32_bf16 v[122:125], a[0:3], v[44:47], 0
s_waitcnt lgkmcnt(11)
ds_read_b128 v[166:169], v51 offset:24576
v_fma_f32 v238, v90, v23, v114
s_waitcnt lgkmcnt(10)
)"
R"(v_mfma_f32_16x16x32_bf16 v[134:137], v[104:107], a[224:227], v[134:137]
s_waitcnt lgkmcnt(8)
ds_read_b128 v[170:173], v52 offset:24576
v_fma_f32 v239, v91, v23, v115
v_mfma_f32_16x16x32_bf16 v[134:137], v[0:3], a[228:231], v[134:137]
global_store_dwordx4 v16, v[230:233], s[38:39] sc1
v_mfma_f32_16x16x32_bf16 v[126:129], a[4:7], v[44:47], 0
s_waitcnt lgkmcnt(5)
ds_read_b128 v[174:177], v51 offset:26624
v_add_u32 v16, 18432, v16
v_mfma_f32_16x16x32_bf16 v[134:137], v[4:7], a[232:235], v[134:137]
s_waitcnt lgkmcnt(4)
ds_read_b128 v[178:181], v52 offset:26624
v_fma_f32 v240, v92, v23, v116
v_mfma_f32_16x16x32_bf16 v[134:137], v[162:165], a[236:239], v[134:137]
v_fma_f32 v241, v93, v23, v117
v_cvt_pk_bf16_f32 v146, v238, v239
v_mfma_f32_16x16x32_bf16 v[130:133], v[44:47], a[8:11], 0
s_waitcnt lgkmcnt(3)
ds_read_b128 v[182:185], v51 offset:28672
v_cvt_pk_bf16_f32 v147, v240, v241
v_mfma_f32_16x16x32_bf16 v[134:137], v[166:169], a[240:243], v[134:137]
ds_read_b128 v[186:189], v52 offset:28672
v_mfma_f32_16x16x32_bf16 v[214:217], v[146:149], v[142:145], v[234:237]
s_waitcnt lgkmcnt(4)
v_mul_f32 v26, 0x3eae1a93, v26
v_mul_f32 v103, v53, v26
v_mfma_f32_16x16x32_bf16 v[134:137], v[170:173], a[244:247], v[134:137]
v_mul_f32 v108, v95, v26
v_fma_f32 v238, v94, v226, v118
v_mfma_f32_16x16x32_bf16 v[122:125], a[12:15], v[96:99], v[122:125]
s_waitcnt lgkmcnt(3)
ds_read_b128 v[190:193], v51 offset:30720
v_fma_f32 v239, v94, v227, v119
v_mfma_f32_16x16x32_bf16 v[134:137], v[174:177], a[248:251], v[134:137]
s_add_u32 m0, s49, 18432
ds_read_b128 v[194:197], v52 offset:30720
s_waitcnt lgkmcnt(4)
v_mfma_f32_16x16x32_bf16 v[134:137], v[178:181], a[252:255], v[134:137]
v_max3_f32 v109, v214, v215, v109
v_max3_f32 v254, v216, v217, v254
v_mfma_f32_16x16x32_bf16 v[126:129], a[16:19], v[96:99], v[126:129]
s_waitcnt lgkmcnt(3)
ds_read_b128 v[198:201], v51 offset:32768
v_mul_f32 v109, v109, v103
v_mfma_f32_16x16x32_bf16 v[134:137], v[182:185], v[54:57], v[134:137]
s_waitcnt lgkmcnt(3)
ds_read_b128 v[202:205], v52 offset:32768
global_load_lds_dwordx4 v11, s[42:43] offset:-2048 sc1
v_mfma_f32_16x16x32_bf16 v[134:137], v[186:189], v[58:61], v[134:137]
v_mul_f32 v254, v254, v108
v_max_f32 v255, v109, v254
v_mfma_f32_16x16x32_bf16 v[130:133], v[96:99], a[20:23], v[130:133]
s_waitcnt lgkmcnt(3)
ds_read_b128 v[206:209], v51 offset:34816
v_mov_b32 v8, v255
v_mfma_f32_16x16x32_bf16 v[134:137], v[190:193], v[62:65], v[134:137]
s_waitcnt lgkmcnt(3)
ds_read_b128 v[210:213], v52 offset:34816
v_fma_f32 v240, v94, v228, v120
v_mfma_f32_16x16x32_bf16 v[134:137], v[194:197], v[66:69], v[134:137]
v_permlane16_swap_b32 v255, v8
v_mfma_f32_16x16x32_bf16 v[122:125], a[24:27], v[104:107], v[122:125]
s_waitcnt lgkmcnt(3)
v_max_f32 v255, v255, v8
v_mov_b32 v8, v255
v_mfma_f32_16x16x32_bf16 v[134:137], v[198:201], v[70:73], v[134:137]
s_waitcnt lgkmcnt(2)
v_permlane32_swap_b32 v255, v8
v_mfma_f32_16x16x32_bf16 v[134:137], v[202:205], v[74:77], v[134:137]
v_max_f32 v255, v255, v8
v_sub_f32 v109, v109, v255
v_mfma_f32_16x16x32_bf16 v[126:129], a[28:31], v[104:107], v[126:129]
s_waitcnt lgkmcnt(1)
global_load_lds_dwordx4 v12, s[42:43] offset:-1024 sc1
v_sub_f32 v254, v254, v255
v_mfma_f32_16x16x32_bf16 v[134:137], v[206:209], v[78:81], v[134:137]
s_waitcnt lgkmcnt(0)
v_exp_f32 v9, v109
v_mfma_f32_16x16x32_bf16 v[134:137], v[210:213], v[82:85], v[134:137]
v_exp_f32 v10, v254
v_mfma_f32_16x16x32_bf16 v[130:133], v[104:107], a[32:35], v[130:133]
v_add_f32 v255, v9, v10
v_mov_b32 v8, v255
v_mfma_f32_16x16x32_bf16 v[122:125], a[36:39], v[0:3], v[122:125]
v_mul_f32 v9, v9, v53
v_fma_f32 v241, v94, v229, v121
v_mfma_f32_16x16x32_bf16 v[126:129], a[40:43], v[0:3], v[126:129]
v_permlane16_swap_b32 v255, v8
v_mfma_f32_16x16x32_bf16 v[130:133], v[0:3], a[44:47], v[130:133]
v_add_f32 v255, v255, v8
v_mov_b32 v8, v255
v_mfma_f32_16x16x32_bf16 v[122:125], a[48:51], v[4:7], v[122:125]
v_mul_f32 v10, v10, v95
v_cvt_pk_bf16_f32 v154, v238, v239
v_mfma_f32_16x16x32_bf16 v[126:129], a[52:55], v[4:7], v[126:129]
global_load_lds_dwordx4 v13, s[42:43] offset:0 sc1
v_cvt_pk_bf16_f32 v155, v240, v241
v_mfma_f32_16x16x32_bf16 v[130:133], v[4:7], a[56:59], v[130:133]
v_permlane32_swap_b32 v255, v8
v_mfma_f32_16x16x32_bf16 v[122:125], a[60:63], v[162:165], v[122:125]
v_add_f32 v255, v255, v8
v_fma_f32 v238, v86, v23, v110
v_mfma_f32_16x16x32_bf16 v[126:129], a[64:67], v[162:165], v[126:129]
v_rcp_f32 v255, v255
v_mfma_f32_16x16x32_bf16 v[130:133], v[162:165], a[68:71], v[130:133]
v_mfma_f32_16x16x32_bf16 v[122:125], a[72:75], v[166:169], v[122:125]
s_barrier
v_mfma_f32_16x16x32_bf16 v[126:129], a[76:79], v[166:169], v[126:129]
ds_read_b128 v[242:245], v25 offset:64
v_mul_f32 v9, v9, v255
v_mfma_f32_16x16x32_bf16 v[130:133], v[166:169], a[80:83], v[130:133]
v_mul_f32 v10, v10, v255
v_cvt_pk_bf16_f32 v9, v9, v9
v_mfma_f32_16x16x32_bf16 v[122:125], a[84:87], v[170:173], v[122:125]
global_load_lds_dwordx4 v14, s[42:43] offset:1024 sc1
v_cvt_pk_bf16_f32 v10, v10, v10
v_mfma_f32_16x16x32_bf16 v[126:129], a[88:91], v[170:173], v[126:129]
v_and_b32 v222, v9, v40
v_and_b32 v223, v10, v41
v_mfma_f32_16x16x32_bf16 v[130:133], v[170:173], a[92:95], v[130:133]
v_and_b32 v224, v9, v42
v_and_b32 v225, v10, v43
v_mfma_f32_16x16x32_bf16 v[122:125], a[96:99], v[174:177], v[122:125]
s_nop 0
v_mfma_f32_16x16x32_bf16 v[218:221], v[154:157], v[222:225], 0
v_mfma_f32_16x16x32_bf16 v[214:217], v[158:161], v[222:225], 0
v_fma_f32 v239, v87, v23, v111
v_fma_f32 v240, v88, v23, v112
v_mfma_f32_16x16x32_bf16 v[126:129], a[100:103], v[174:177], v[126:129]
s_waitcnt lgkmcnt(0)
v_fma_f32 v230, v102, v242, v134
v_fma_f32 v231, v102, v243, v135
v_mfma_f32_16x16x32_bf16 v[130:133], v[174:177], a[104:107], v[130:133]
v_fma_f32 v232, v102, v244, v136
v_fma_f32 v233, v102, v245, v137
v_mfma_f32_16x16x32_bf16 v[122:125], a[108:111], v[178:181], v[122:125]
v_cvt_pk_bf16_f32 v230, v230, v231
v_cvt_pk_bf16_f32 v231, v232, v233
v_mfma_f32_16x16x32_bf16 v[126:129], a[112:115], v[178:181], v[126:129]
v_cvt_pk_bf16_f32 v100, v218, v219
v_cvt_pk_bf16_f32 v101, v220, v221
v_mfma_f32_16x16x32_bf16 v[130:133], v[178:181], a[116:119], v[130:133]
global_load_lds_dwordx4 v15, s[42:43] offset:2048 sc1
s_sub_u32 s42, s42, 18432
s_subb_u32 s43, s43, 0
v_mfma_f32_16x16x32_bf16 v[122:125], a[120:123], v[182:185], v[122:125]
ds_write_b64 v18, v[100:101]
v_mfma_f32_16x16x32_bf16 v[126:129], a[124:127], v[182:185], v[126:129]
v_cvt_pk_bf16_f32 v9, v216, v217
v_fma_f32 v241, v89, v23, v113
v_mfma_f32_16x16x32_bf16 v[130:133], v[182:185], a[128:131], v[130:133]
ds_write_b32 v19, v9
v_mfma_f32_16x16x32_bf16 v[122:125], a[132:135], v[186:189], v[122:125]
ds_write_b64 v22, v[230:231] offset:41472
v_mfma_f32_16x16x32_bf16 v[126:129], a[136:139], v[186:189], v[126:129]
v_cvt_pk_bf16_f32 v138, v238, v239
v_cvt_pk_bf16_f32 v139, v240, v241
v_mfma_f32_16x16x32_bf16 v[130:133], v[186:189], a[140:143], v[130:133]
v_mfma_f32_16x16x32_bf16 v[122:125], a[144:147], v[190:193], v[122:125]
v_mfma_f32_16x16x32_bf16 v[126:129], a[148:151], v[190:193], v[126:129]
v_mfma_f32_16x16x32_bf16 v[130:133], v[190:193], a[152:155], v[130:133]
v_mfma_f32_16x16x32_bf16 v[122:125], a[156:159], v[194:197], v[122:125]
v_mfma_f32_16x16x32_bf16 v[126:129], a[160:163], v[194:197], v[126:129]
v_mfma_f32_16x16x32_bf16 v[130:133], v[194:197], a[164:167], v[130:133]
v_mfma_f32_16x16x32_bf16 v[122:125], a[168:171], v[198:201], v[122:125]
v_mfma_f32_16x16x32_bf16 v[126:129], a[172:175], v[198:201], v[126:129]
v_xor_b32 v51, 0x10000, v51
v_xor_b32 v52, 0x10000, v52
v_mfma_f32_16x16x32_bf16 v[130:133], v[198:201], a[176:179], v[130:133]
v_mfma_f32_16x16x32_bf16 v[122:125], a[180:183], v[202:205], v[122:125]
v_mfma_f32_16x16x32_bf16 v[126:129], a[184:187], v[202:205], v[126:129]
s_waitcnt vmcnt(5) lgkmcnt(0)
s_barrier
.Lqa_in16:
v_mfma_f32_16x16x32_bf16 v[130:133], v[202:205], a[188:191], v[130:133]
s_cmp_eq_u32 s70, s73
s_cbranch_scc1 .Lqa_hk17
.Lqa_hr18:
ds_read_b128 v[44:47], v51 offset:0
v_mfma_f32_16x16x32_bf16 v[122:125], a[192:195], v[206:209], v[122:125]
ds_read_b128 v[96:99], v52 offset:0
ds_read_b128 v[230:233], v17 offset:0
v_mfma_f32_16x16x32_bf16 v[126:129], a[196:199], v[206:209], v[126:129]
ds_read_b32 v23, v24 offset:64
ds_read_b32 v152, v21
v_mfma_f32_16x16x32_bf16 v[218:221], v[146:149], v[138:141], v[36:39]
v_mfma_f32_16x16x32_bf16 v[130:133], v[206:209], a[200:203], v[130:133]
ds_read_b128 v[104:107], v51 offset:2048
ds_read_b32 v26, v24 offset:128
v_mfma_f32_16x16x32_bf16 v[122:125], a[204:207], v[210:213], v[122:125]
ds_read_b128 v[0:3], v52 offset:2048
ds_read_b32 v53, v29
v_mfma_f32_16x16x32_bf16 v[126:129], a[208:211], v[210:213], v[126:129]
v_mfma_f32_16x16x32_bf16 v[130:133], v[210:213], a[212:215], v[130:133]
s_sub_u32 s70, s70, 1
ds_read_b128 v[4:7], v51 offset:4096
s_cbranch_scc0 .Lqa_top26
s_waitcnt lgkmcnt(0)
ds_read_b32 v95, v30
ds_read_b128 v[226:229], v25 offset:64
ds_read_b64 v[160:161], v22 offset:41472
ds_read_b32 v144, v20
v_fma_f32 v238, v90, v23, v126
global_store_dwordx4 v16, v[230:233], s[38:39] sc1
v_add_u32 v16, 18432, v16
v_fma_f32 v239, v91, v23, v127
v_fma_f32 v240, v92, v23, v128
v_fma_f32 v241, v93, v23, v129
v_cvt_pk_bf16_f32 v150, v238, v239
v_cvt_pk_bf16_f32 v151, v240, v241
s_nop 1
v_mfma_f32_16x16x32_bf16 v[214:217], v[150:153], v[138:141], v[32:35]
s_waitcnt lgkmcnt(2)
v_max_f32 v109, v218, v219
v_max_f32 v254, v220, v221
v_fma_f32 v238, v94, v226, v130
v_mul_f32 v26, 0x3eae1a93, v26
v_mul_f32 v103, v53, v26
v_mul_f32 v108, v95, v26
s_nop 0
v_max3_f32 v109, v214, v215, v109
v_max3_f32 v254, v216, v217, v254
v_mul_f32 v109, v109, v103
v_mul_f32 v254, v254, v108
v_max_f32 v255, v109, v254
v_mov_b32 v8, v255
s_nop 1
v_permlane16_swap_b32 v255, v8
v_max_f32 v255, v255, v8
v_mov_b32 v8, v255
s_nop 1
v_permlane32_swap_b32 v255, v8
v_max_f32 v255, v255, v8
v_sub_f32 v109, v109, v255
v_sub_f32 v254, v254, v255
v_exp_f32 v9, v109
v_exp_f32 v10, v254
s_nop 0
v_add_f32 v255, v9, v10
v_mov_b32 v8, v255
v_mul_f32 v9, v9, v53
v_fma_f32 v239, v94, v227, v131
v_permlane16_swap_b32 v255, v8
v_add_f32 v255, v255, v8
v_mov_b32 v8, v255
v_mul_f32 v10, v10, v95
v_fma_f32 v240, v94, v228, v132
v_fma_f32 v241, v94, v229, v133
v_permlane32_swap_b32 v255, v8
v_add_f32 v255, v255, v8
v_cvt_pk_bf16_f32 v156, v238, v239
s_waitcnt lgkmcnt(0)
s_barrier
v_cvt_pk_bf16_f32 v157, v240, v241
v_rcp_f32 v255, v255
s_nop 0
v_mul_f32 v9, v9, v255
v_mul_f32 v10, v10, v255
v_cvt_pk_bf16_f32 v9, v9, v9
v_cvt_pk_bf16_f32 v10, v10, v10
v_and_b32 v224, v9, v40
v_and_b32 v225, v10, v41
v_and_b32 v222, v9, v42
v_and_b32 v223, v10, v43
s_nop 1
v_mfma_f32_16x16x32_bf16 v[218:221], v[154:157], v[222:225], 0
v_mfma_f32_16x16x32_bf16 v[214:217], v[158:161], v[222:225], 0
v_fma_f32 v238, v86, v23, v122
s_nop 5
v_cvt_pk_bf16_f32 v100, v218, v219
v_cvt_pk_bf16_f32 v101, v220, v221
ds_write_b64 v18, v[100:101]
v_cvt_pk_bf16_f32 v9, v216, v217
v_fma_f32 v239, v87, v23, v123
ds_write_b32 v19, v9
v_fma_f32 v240, v88, v23, v124
v_fma_f32 v241, v89, v23, v125
v_cvt_pk_bf16_f32 v142, v238, v239
v_cvt_pk_bf16_f32 v143, v240, v241
s_waitcnt vmcnt(0)
s_waitcnt lgkmcnt(0)
s_barrier
ds_read_b128 v[230:233], v17 offset:0
s_waitcnt lgkmcnt(0)
global_store_dwordx4 v16, v[230:233], s[38:39] sc1
s_waitcnt vmcnt(0)
s_barrier
s_endpgm
.Lqa_w3:
s_nop 7
v_mov_b32 v202, v32
v_mov_b32 v203, v33
v_mov_b32 v204, v34
v_mov_b32 v205, v35
s_mov_b32 s70, 33
s_waitcnt vmcnt(0) lgkmcnt(0)
s_nop 7
s_branch .Lqa_in27
.p2align 6
.Lqa_top41:
s_waitcnt lgkmcnt(9)
v_mfma_f32_16x16x32_bf16 v[134:137], v[0:3], v[0:3], 0
s_waitcnt lgkmcnt(8)
ds_read_b128 v[54:57], v52 offset:4096
ds_read_b32 v8, v30
v_mfma_f32_16x16x32_bf16 v[134:137], v[4:7], v[4:7], v[134:137]
ds_read_b128 v[194:197], v25 offset:64
ds_read_b64 v[160:161], v22 offset:41472
v_mfma_f32_16x16x32_bf16 v[110:113], a[0:3], v[0:3], 0
s_waitcnt lgkmcnt(8)
ds_read_b128 v[58:61], v51 offset:6144
ds_read_b32 v144, v20
v_mfma_f32_16x16x32_bf16 v[134:137], v[96:99], v[96:99], v[134:137]
s_waitcnt lgkmcnt(8)
ds_read_b128 v[62:65], v52 offset:6144
v_add_u32 v46, 1, v46
v_mfma_f32_16x16x32_bf16 v[134:137], v[100:103], v[100:103], v[134:137]
s_mov_b64 exec, 65535
global_store_dwordx4 v16, v[198:201], s[38:39] sc1
s_mov_b64 exec, -1
v_mfma_f32_16x16x32_bf16 v[114:117], a[4:7], v[0:3], 0
s_waitcnt lgkmcnt(7)
ds_read_b128 v[66:69], v51 offset:8192
v_add_u32 v16, 18432, v16
v_mfma_f32_16x16x32_bf16 v[134:137], v[104:107], v[104:107], v[134:137]
s_waitcnt lgkmcnt(7)
ds_read_b128 v[70:73], v52 offset:8192
v_fma_f32 v206, v90, v26, v126
v_mfma_f32_16x16x32_bf16 v[134:137], v[54:57], v[54:57], v[134:137]
v_fma_f32 v207, v91, v26, v127
v_fma_f32 v208, v92, v26, v128
v_mfma_f32_16x16x32_bf16 v[118:121], v[0:3], a[8:11], 0
s_waitcnt lgkmcnt(4)
ds_read_b128 v[74:77], v51 offset:10240
v_fma_f32 v209, v93, v26, v129
v_mfma_f32_16x16x32_bf16 v[134:137], v[58:61], v[58:61], v[134:137]
s_waitcnt lgkmcnt(3)
ds_read_b128 v[78:81], v52 offset:10240
v_cvt_pk_bf16_f32 v150, v206, v207
v_mfma_f32_16x16x32_bf16 v[134:137], v[62:65], v[62:65], v[134:137]
v_cvt_pk_bf16_f32 v151, v208, v209
v_max_f32 v47, v186, v187
v_mfma_f32_16x16x32_bf16 v[110:113], a[12:15], v[4:7], v[110:113]
ds_read_b128 v[82:85], v51 offset:12288
v_mfma_f32_16x16x32_bf16 v[182:185], v[150:153], v[138:141], v[32:35]
s_waitcnt lgkmcnt(4)
v_max_f32 v48, v188, v189
v_fma_f32 v206, v94, v194, v130
v_mfma_f32_16x16x32_bf16 v[134:137], v[66:69], v[66:69], v[134:137]
s_xor_b32 s49, s49, 0x10000
ds_read_b128 v[162:165], v52 offset:12288
s_waitcnt lgkmcnt(4)
v_mfma_f32_16x16x32_bf16 v[134:137], v[70:73], v[70:73], v[134:137]
s_add_u32 m0, s49, 0
v_mul_f32 v95, 0x3eae1a93, v95
v_mul_f32 v9, v255, v95
v_mfma_f32_16x16x32_bf16 v[114:117], a[16:19], v[4:7], v[114:117]
s_waitcnt lgkmcnt(3)
ds_read_b128 v[166:169], v51 offset:14336
v_mul_f32 v10, v8, v95
v_mfma_f32_16x16x32_bf16 v[134:137], v[74:77], v[74:77], v[134:137]
s_waitcnt lgkmcnt(3)
ds_read_b128 v[170:173], v52 offset:14336
v_max3_f32 v47, v182, v183, v47
v_mfma_f32_16x16x32_bf16 v[134:137], v[78:81], v[78:81], v[134:137]
v_max3_f32 v48, v184, v185, v48
v_mul_f32 v47, v47, v9
v_mfma_f32_16x16x32_bf16 v[118:121], v[4:7], a[20:23], v[118:121]
s_waitcnt lgkmcnt(3)
ds_read_b128 v[174:177], v51 offset:16384
v_mul_f32 v48, v48, v10
v_mfma_f32_16x16x32_bf16 v[134:137], v[82:85], v[82:85], v[134:137]
s_waitcnt lgkmcnt(3)
ds_read_b128 v[178:181], v52 offset:16384
global_load_lds_dwordx4 v11, s[42:43] offset:-2048 sc1
v_mfma_f32_16x16x32_bf16 v[134:137], v[162:165], v[162:165], v[134:137]
v_max_f32 v49, v47, v48
v_mov_b32 v50, v49
v_mfma_f32_16x16x32_bf16 v[110:113], a[24:27], v[96:99], v[110:113]
s_waitcnt lgkmcnt(3)
v_permlane16_swap_b32 v49, v50
v_mfma_f32_16x16x32_bf16 v[134:137], v[166:169], v[166:169], v[134:137]
s_waitcnt lgkmcnt(2)
v_max_f32 v49, v49, v50
v_mov_b32 v50, v49
v_mfma_f32_16x16x32_bf16 v[134:137], v[170:173], v[170:173], v[134:137]
s_nop 0
v_permlane32_swap_b32 v49, v50
v_mfma_f32_16x16x32_bf16 v[114:117], a[28:31], v[96:99], v[114:117]
s_waitcnt lgkmcnt(1)
v_max_f32 v49, v49, v50
v_sub_f32 v47, v47, v49
v_mfma_f32_16x16x32_bf16 v[134:137], v[174:177], v[174:177], v[134:137]
s_waitcnt lgkmcnt(0)
v_sub_f32 v48, v48, v49
v_fma_f32 v207, v94, v195, v131
v_mfma_f32_16x16x32_bf16 v[134:137], v[178:181], v[178:181], v[134:137]
v_exp_f32 v210, v47
v_mfma_f32_16x16x32_bf16 v[118:121], v[96:99], a[32:35], v[118:121]
)"
R"(global_load_lds_dwordx4 v12, s[42:43] offset:-1024 sc1
v_fma_f32 v208, v94, v196, v132
v_mfma_f32_16x16x32_bf16 v[110:113], a[36:39], v[100:103], v[110:113]
v_exp_f32 v211, v48
v_mfma_f32_16x16x32_bf16 v[114:117], a[40:43], v[100:103], v[114:117]
v_add_f32 v49, v210, v211
v_mov_b32 v50, v49
v_mfma_f32_16x16x32_bf16 v[118:121], v[100:103], a[44:47], v[118:121]
ds_write_b128 v23, v[134:137] offset:41984
v_mfma_f32_16x16x32_bf16 v[110:113], a[48:51], v[104:107], v[110:113]
v_mul_f32 v210, v210, v255
v_fma_f32 v209, v94, v197, v133
v_mfma_f32_16x16x32_bf16 v[114:117], a[52:55], v[104:107], v[114:117]
v_permlane16_swap_b32 v49, v50
v_mfma_f32_16x16x32_bf16 v[118:121], v[104:107], a[56:59], v[118:121]
v_add_f32 v49, v49, v50
v_mov_b32 v50, v49
v_mfma_f32_16x16x32_bf16 v[110:113], a[60:63], v[54:57], v[110:113]
v_mul_f32 v211, v211, v8
v_cvt_pk_bf16_f32 v156, v206, v207
v_mfma_f32_16x16x32_bf16 v[114:117], a[64:67], v[54:57], v[114:117]
global_load_lds_dwordx4 v13, s[42:43] offset:0 sc1
v_cvt_pk_bf16_f32 v157, v208, v209
v_mfma_f32_16x16x32_bf16 v[118:121], v[54:57], a[68:71], v[118:121]
v_mfma_f32_16x16x32_bf16 v[110:113], a[72:75], v[58:61], v[110:113]
s_waitcnt lgkmcnt(0)
s_barrier
v_mfma_f32_16x16x32_bf16 v[114:117], a[76:79], v[58:61], v[114:117]
v_permlane32_swap_b32 v49, v50
v_mfma_f32_16x16x32_bf16 v[118:121], v[58:61], a[80:83], v[118:121]
v_add_f32 v49, v49, v50
v_fma_f32 v206, v86, v26, v122
v_mfma_f32_16x16x32_bf16 v[110:113], a[84:87], v[62:65], v[110:113]
v_rcp_f32 v49, v49
v_mfma_f32_16x16x32_bf16 v[114:117], a[88:91], v[62:65], v[114:117]
v_mul_f32 v210, v210, v49
v_mul_f32 v211, v211, v49
v_mfma_f32_16x16x32_bf16 v[118:121], v[62:65], a[92:95], v[118:121]
v_cvt_pk_bf16_f32 v210, v210, v210
v_cvt_pk_bf16_f32 v211, v211, v211
v_mfma_f32_16x16x32_bf16 v[110:113], a[96:99], v[66:69], v[110:113]
global_load_lds_dwordx4 v14, s[42:43] offset:1024 sc1
s_add_u32 s42, s42, s46
s_addc_u32 s43, s43, s47
v_mfma_f32_16x16x32_bf16 v[114:117], a[100:103], v[66:69], v[114:117]
v_and_b32 v192, v210, v40
v_and_b32 v193, v211, v41
v_mfma_f32_16x16x32_bf16 v[118:121], v[66:69], a[104:107], v[118:121]
v_and_b32 v190, v210, v42
v_and_b32 v191, v211, v43
v_mfma_f32_16x16x32_bf16 v[110:113], a[108:111], v[70:73], v[110:113]
s_nop 0
v_mfma_f32_16x16x32_bf16 v[186:189], v[154:157], v[190:193], 0
v_mfma_f32_16x16x32_bf16 v[182:185], v[158:161], v[190:193], 0
v_fma_f32 v207, v87, v26, v123
v_fma_f32 v208, v88, v26, v124
v_mfma_f32_16x16x32_bf16 v[114:117], a[112:115], v[70:73], v[114:117]
v_fma_f32 v209, v89, v26, v125
v_cvt_pk_bf16_f32 v142, v206, v207
v_mfma_f32_16x16x32_bf16 v[118:121], v[70:73], a[116:119], v[118:121]
v_cvt_pk_bf16_f32 v143, v208, v209
v_mfma_f32_16x16x32_bf16 v[110:113], a[120:123], v[74:77], v[110:113]
v_mfma_f32_16x16x32_bf16 v[114:117], a[124:127], v[74:77], v[114:117]
v_cvt_pk_bf16_f32 v108, v186, v187
v_cvt_pk_bf16_f32 v109, v188, v189
v_mfma_f32_16x16x32_bf16 v[118:121], v[74:77], a[128:131], v[118:121]
ds_write_b64 v18, v[108:109]
v_mfma_f32_16x16x32_bf16 v[110:113], a[132:135], v[78:81], v[110:113]
v_cvt_pk_bf16_f32 v210, v182, v183
v_mfma_f32_16x16x32_bf16 v[114:117], a[136:139], v[78:81], v[114:117]
ds_write_b32 v19, v210
v_mfma_f32_16x16x32_bf16 v[118:121], v[78:81], a[140:143], v[118:121]
v_mfma_f32_16x16x32_bf16 v[110:113], a[144:147], v[82:85], v[110:113]
v_mfma_f32_16x16x32_bf16 v[114:117], a[148:151], v[82:85], v[114:117]
v_mfma_f32_16x16x32_bf16 v[118:121], v[82:85], a[152:155], v[118:121]
v_mfma_f32_16x16x32_bf16 v[110:113], a[156:159], v[162:165], v[110:113]
v_mfma_f32_16x16x32_bf16 v[114:117], a[160:163], v[162:165], v[114:117]
v_mfma_f32_16x16x32_bf16 v[118:121], v[162:165], a[164:167], v[118:121]
s_waitcnt vmcnt(5)
v_sub_u32 v53, v53, v46
v_cmp_lt_u32 vcc, 0x7f, v53
v_mfma_f32_16x16x32_bf16 v[110:113], a[168:171], v[166:169], v[110:113]
global_load_dword v254, v44, s[38:39] sc1
v_mfma_f32_16x16x32_bf16 v[114:117], a[172:175], v[166:169], v[114:117]
v_mfma_f32_16x16x32_bf16 v[118:121], v[166:169], a[176:179], v[118:121]
s_and_b64 vcc, vcc, s[76:77]
s_cbranch_vccnz .Lqa_sp30
.Lqa_rt31:
v_mfma_f32_16x16x32_bf16 v[110:113], a[180:183], v[170:173], v[110:113]
v_mfma_f32_16x16x32_bf16 v[114:117], a[184:187], v[170:173], v[114:117]
s_waitcnt vmcnt(5) lgkmcnt(0)
s_barrier
v_mfma_f32_16x16x32_bf16 v[118:121], v[170:173], a[188:191], v[118:121]
ds_read_b128 v[0:3], v51 offset:18432
v_mfma_f32_16x16x32_bf16 v[110:113], a[192:195], v[174:177], v[110:113]
ds_read_b128 v[4:7], v52 offset:18432
ds_read_b128 v[198:201], v17 offset:0
v_mfma_f32_16x16x32_bf16 v[114:117], a[196:199], v[174:177], v[114:117]
ds_read_b32 v26, v24 offset:0
ds_read_b32 v148, v21
v_mfma_f32_16x16x32_bf16 v[186:189], v[150:153], v[142:145], v[36:39]
v_mfma_f32_16x16x32_bf16 v[118:121], v[174:177], a[200:203], v[118:121]
ds_read_b128 v[96:99], v51 offset:20480
ds_read_b32 v95, v24 offset:192
v_mfma_f32_16x16x32_bf16 v[110:113], a[204:207], v[178:181], v[110:113]
ds_read_b128 v[100:103], v52 offset:20480
ds_read_b32 v255, v27
v_mfma_f32_16x16x32_bf16 v[114:117], a[208:211], v[178:181], v[114:117]
ds_read_b32 v8, v28
ds_read_b128 v[194:197], v25 offset:0
v_mfma_f32_16x16x32_bf16 v[118:121], v[178:181], a[212:215], v[118:121]
s_waitcnt lgkmcnt(10)
ds_read_b128 v[104:107], v51 offset:22528
ds_read_b64 v[158:159], v22 offset:41472
v_mfma_f32_16x16x32_bf16 v[134:137], v[0:3], v[0:3], 0
s_waitcnt lgkmcnt(11)
ds_read_b128 v[54:57], v52 offset:22528
ds_read_b32 v140, v20
v_mfma_f32_16x16x32_bf16 v[134:137], v[4:7], v[4:7], v[134:137]
v_add_u32 v46, 1, v46
v_max_f32 v47, v186, v187
v_mfma_f32_16x16x32_bf16 v[122:125], a[0:3], v[0:3], 0
s_waitcnt lgkmcnt(11)
ds_read_b128 v[58:61], v51 offset:24576
v_fma_f32 v206, v90, v26, v114
s_waitcnt lgkmcnt(10)
v_mfma_f32_16x16x32_bf16 v[134:137], v[96:99], v[96:99], v[134:137]
s_waitcnt lgkmcnt(8)
ds_read_b128 v[62:65], v52 offset:24576
v_fma_f32 v207, v91, v26, v115
v_mfma_f32_16x16x32_bf16 v[134:137], v[100:103], v[100:103], v[134:137]
s_mov_b64 exec, 65535
global_store_dwordx4 v16, v[198:201], s[38:39] sc1
s_mov_b64 exec, -1
v_mfma_f32_16x16x32_bf16 v[126:129], a[4:7], v[0:3], 0
s_waitcnt lgkmcnt(5)
ds_read_b128 v[66:69], v51 offset:26624
v_add_u32 v16, 18432, v16
v_mfma_f32_16x16x32_bf16 v[134:137], v[104:107], v[104:107], v[134:137]
s_waitcnt lgkmcnt(4)
ds_read_b128 v[70:73], v52 offset:26624
v_fma_f32 v208, v92, v26, v116
v_mfma_f32_16x16x32_bf16 v[134:137], v[54:57], v[54:57], v[134:137]
v_fma_f32 v209, v93, v26, v117
v_cvt_pk_bf16_f32 v146, v206, v207
v_mfma_f32_16x16x32_bf16 v[130:133], v[0:3], a[8:11], 0
s_waitcnt lgkmcnt(3)
ds_read_b128 v[74:77], v51 offset:28672
v_cvt_pk_bf16_f32 v147, v208, v209
v_mfma_f32_16x16x32_bf16 v[134:137], v[58:61], v[58:61], v[134:137]
ds_read_b128 v[78:81], v52 offset:28672
v_mfma_f32_16x16x32_bf16 v[182:185], v[146:149], v[142:145], v[202:205]
s_waitcnt lgkmcnt(4)
v_max_f32 v48, v188, v189
v_fma_f32 v206, v94, v194, v118
v_mfma_f32_16x16x32_bf16 v[134:137], v[62:65], v[62:65], v[134:137]
v_fma_f32 v207, v94, v195, v119
v_fma_f32 v208, v94, v196, v120
v_mfma_f32_16x16x32_bf16 v[122:125], a[12:15], v[4:7], v[122:125]
s_waitcnt lgkmcnt(3)
ds_read_b128 v[82:85], v51 offset:30720
v_mul_f32 v95, 0x3eae1a93, v95
v_mfma_f32_16x16x32_bf16 v[134:137], v[66:69], v[66:69], v[134:137]
s_add_u32 m0, s49, 18432
ds_read_b128 v[162:165], v52 offset:30720
s_waitcnt lgkmcnt(4)
v_mfma_f32_16x16x32_bf16 v[134:137], v[70:73], v[70:73], v[134:137]
v_mul_f32 v9, v255, v95
v_mul_f32 v10, v8, v95
v_mfma_f32_16x16x32_bf16 v[126:129], a[16:19], v[4:7], v[126:129]
s_waitcnt lgkmcnt(3)
ds_read_b128 v[166:169], v51 offset:32768
v_max3_f32 v47, v182, v183, v47
v_mfma_f32_16x16x32_bf16 v[134:137], v[74:77], v[74:77], v[134:137]
s_waitcnt lgkmcnt(3)
ds_read_b128 v[170:173], v52 offset:32768
v_max3_f32 v48, v184, v185, v48
v_mfma_f32_16x16x32_bf16 v[134:137], v[78:81], v[78:81], v[134:137]
v_mul_f32 v47, v47, v9
v_mul_f32 v48, v48, v10
v_mfma_f32_16x16x32_bf16 v[130:133], v[4:7], a[20:23], v[130:133]
s_waitcnt lgkmcnt(3)
ds_read_b128 v[174:177], v51 offset:34816
global_load_lds_dwordx4 v11, s[42:43] offset:-2048 sc1
v_mfma_f32_16x16x32_bf16 v[134:137], v[82:85], v[82:85], v[134:137]
s_waitcnt lgkmcnt(3)
ds_read_b128 v[178:181], v52 offset:34816
v_max_f32 v49, v47, v48
v_mfma_f32_16x16x32_bf16 v[134:137], v[162:165], v[162:165], v[134:137]
v_mov_b32 v50, v49
v_fma_f32 v209, v94, v197, v121
v_mfma_f32_16x16x32_bf16 v[122:125], a[24:27], v[96:99], v[122:125]
s_waitcnt lgkmcnt(3)
v_permlane16_swap_b32 v49, v50
v_mfma_f32_16x16x32_bf16 v[134:137], v[166:169], v[166:169], v[134:137]
s_waitcnt lgkmcnt(2)
v_max_f32 v49, v49, v50
v_mov_b32 v50, v49
v_mfma_f32_16x16x32_bf16 v[134:137], v[170:173], v[170:173], v[134:137]
s_nop 0
v_permlane32_swap_b32 v49, v50
v_mfma_f32_16x16x32_bf16 v[126:129], a[28:31], v[96:99], v[126:129]
s_waitcnt lgkmcnt(1)
v_max_f32 v49, v49, v50
v_sub_f32 v47, v47, v49
v_mfma_f32_16x16x32_bf16 v[134:137], v[174:177], v[174:177], v[134:137]
s_waitcnt lgkmcnt(0)
v_sub_f32 v48, v48, v49
v_cvt_pk_bf16_f32 v154, v206, v207
v_mfma_f32_16x16x32_bf16 v[134:137], v[178:181], v[178:181], v[134:137]
global_load_lds_dwordx4 v12, s[42:43] offset:-1024 sc1
v_cvt_pk_bf16_f32 v155, v208, v209
v_mfma_f32_16x16x32_bf16 v[130:133], v[96:99], a[32:35], v[130:133]
v_exp_f32 v210, v47
v_mfma_f32_16x16x32_bf16 v[122:125], a[36:39], v[100:103], v[122:125]
v_exp_f32 v211, v48
v_mfma_f32_16x16x32_bf16 v[126:129], a[40:43], v[100:103], v[126:129]
v_add_f32 v49, v210, v211
v_mov_b32 v50, v49
v_mfma_f32_16x16x32_bf16 v[130:133], v[100:103], a[44:47], v[130:133]
ds_write_b128 v23, v[134:137] offset:41984
v_mfma_f32_16x16x32_bf16 v[122:125], a[48:51], v[104:107], v[122:125]
v_mul_f32 v210, v210, v255
v_fma_f32 v206, v86, v26, v110
v_mfma_f32_16x16x32_bf16 v[126:129], a[52:55], v[104:107], v[126:129]
v_permlane16_swap_b32 v49, v50
v_mfma_f32_16x16x32_bf16 v[130:133], v[104:107], a[56:59], v[130:133]
v_add_f32 v49, v49, v50
v_mov_b32 v50, v49
v_mfma_f32_16x16x32_bf16 v[122:125], a[60:63], v[54:57], v[122:125]
global_load_lds_dwordx4 v13, s[42:43] offset:0 sc1
v_mul_f32 v211, v211, v8
v_mfma_f32_16x16x32_bf16 v[126:129], a[64:67], v[54:57], v[126:129]
v_permlane32_swap_b32 v49, v50
v_mfma_f32_16x16x32_bf16 v[130:133], v[54:57], a[68:71], v[130:133]
v_mfma_f32_16x16x32_bf16 v[122:125], a[72:75], v[58:61], v[122:125]
s_waitcnt lgkmcnt(0)
s_barrier
v_mfma_f32_16x16x32_bf16 v[126:129], a[76:79], v[58:61], v[126:129]
v_add_f32 v49, v49, v50
v_fma_f32 v207, v87, v26, v111
v_mfma_f32_16x16x32_bf16 v[130:133], v[58:61], a[80:83], v[130:133]
v_rcp_f32 v49, v49
v_mfma_f32_16x16x32_bf16 v[122:125], a[84:87], v[62:65], v[122:125]
v_mul_f32 v210, v210, v49
v_mul_f32 v211, v211, v49
v_mfma_f32_16x16x32_bf16 v[126:129], a[88:91], v[62:65], v[126:129]
v_cvt_pk_bf16_f32 v210, v210, v210
v_cvt_pk_bf16_f32 v211, v211, v211
v_mfma_f32_16x16x32_bf16 v[130:133], v[62:65], a[92:95], v[130:133]
global_load_lds_dwordx4 v14, s[42:43] offset:1024 sc1
s_sub_u32 s42, s42, 18432
s_subb_u32 s43, s43, 0
v_mfma_f32_16x16x32_bf16 v[122:125], a[96:99], v[66:69], v[122:125]
v_and_b32 v190, v210, v40
v_and_b32 v191, v211, v41
v_mfma_f32_16x16x32_bf16 v[126:129], a[100:103], v[66:69], v[126:129]
v_and_b32 v192, v210, v42
v_and_b32 v193, v211, v43
v_mfma_f32_16x16x32_bf16 v[130:133], v[66:69], a[104:107], v[130:133]
s_nop 0
v_mfma_f32_16x16x32_bf16 v[186:189], v[154:157], v[190:193], 0
v_mfma_f32_16x16x32_bf16 v[182:185], v[158:161], v[190:193], 0
v_fma_f32 v208, v88, v26, v112
v_fma_f32 v209, v89, v26, v113
v_mfma_f32_16x16x32_bf16 v[122:125], a[108:111], v[70:73], v[122:125]
v_cvt_pk_bf16_f32 v138, v206, v207
v_cvt_pk_bf16_f32 v139, v208, v209
v_mfma_f32_16x16x32_bf16 v[126:129], a[112:115], v[70:73], v[126:129]
v_mfma_f32_16x16x32_bf16 v[130:133], v[70:73], a[116:119], v[130:133]
v_mfma_f32_16x16x32_bf16 v[122:125], a[120:123], v[74:77], v[122:125]
v_cvt_pk_bf16_f32 v108, v186, v187
v_cvt_pk_bf16_f32 v109, v188, v189
v_mfma_f32_16x16x32_bf16 v[126:129], a[124:127], v[74:77], v[126:129]
ds_write_b64 v18, v[108:109]
v_mfma_f32_16x16x32_bf16 v[130:133], v[74:77], a[128:131], v[130:133]
v_cvt_pk_bf16_f32 v210, v182, v183
v_mfma_f32_16x16x32_bf16 v[122:125], a[132:135], v[78:81], v[122:125]
ds_write_b32 v19, v210
v_mfma_f32_16x16x32_bf16 v[126:129], a[136:139], v[78:81], v[126:129]
v_mfma_f32_16x16x32_bf16 v[130:133], v[78:81], a[140:143], v[130:133]
v_mfma_f32_16x16x32_bf16 v[122:125], a[144:147], v[82:85], v[122:125]
v_mfma_f32_16x16x32_bf16 v[126:129], a[148:151], v[82:85], v[126:129]
v_mfma_f32_16x16x32_bf16 v[130:133], v[82:85], a[152:155], v[130:133]
v_mfma_f32_16x16x32_bf16 v[122:125], a[156:159], v[162:165], v[122:125]
v_mfma_f32_16x16x32_bf16 v[126:129], a[160:163], v[162:165], v[126:129]
v_mfma_f32_16x16x32_bf16 v[130:133], v[162:165], a[164:167], v[130:133]
s_waitcnt vmcnt(5)
v_sub_u32 v254, v254, v46
v_cmp_lt_u32 vcc, 0x7f, v254
v_mfma_f32_16x16x32_bf16 v[122:125], a[168:171], v[166:169], v[122:125]
global_load_dword v53, v45, s[38:39] sc1
v_mfma_f32_16x16x32_bf16 v[126:129], a[172:175], v[166:169], v[126:129]
v_xor_b32 v51, 0x10000, v51
v_xor_b32 v52, 0x10000, v52
v_mfma_f32_16x16x32_bf16 v[130:133], v[166:169], a[176:179], v[130:133]
s_and_b64 vcc, vcc, s[76:77]
s_cbranch_vccnz .Lqa_sp32
.Lqa_rt33:
v_mfma_f32_16x16x32_bf16 v[122:125], a[180:183], v[170:173], v[122:125]
v_mfma_f32_16x16x32_bf16 v[126:129], a[184:187], v[170:173], v[126:129]
s_waitcnt vmcnt(5) lgkmcnt(0)
s_barrier
.Lqa_in27:
v_mfma_f32_16x16x32_bf16 v[130:133], v[170:173], a[188:191], v[130:133]
s_cmp_eq_u32 s70, s73
s_cbranch_scc1 .Lqa_hk28
.Lqa_hr29:
ds_read_b128 v[0:3], v51 offset:0
v_mfma_f32_16x16x32_bf16 v[122:125], a[192:195], v[174:177], v[122:125]
ds_read_b128 v[4:7], v52 offset:0
ds_read_b128 v[198:201], v17 offset:0
v_mfma_f32_16x16x32_bf16 v[126:129], a[196:199], v[174:177], v[126:129]
ds_read_b32 v26, v24 offset:64
ds_read_b32 v152, v21
v_mfma_f32_16x16x32_bf16 v[186:189], v[146:149], v[138:141], v[36:39]
v_mfma_f32_16x16x32_bf16 v[130:133], v[174:177], a[200:203], v[130:133]
ds_read_b128 v[96:99], v51 offset:2048
ds_read_b32 v95, v24 offset:128
v_mfma_f32_16x16x32_bf16 v[122:125], a[204:207], v[178:181], v[122:125]
ds_read_b128 v[100:103], v52 offset:2048
ds_read_b32 v255, v29
v_mfma_f32_16x16x32_bf16 v[126:129], a[208:211], v[178:181], v[126:129]
v_mfma_f32_16x16x32_bf16 v[130:133], v[178:181], a[212:215], v[130:133]
s_sub_u32 s70, s70, 1
ds_read_b128 v[104:107], v51 offset:4096
s_cbranch_scc0 .Lqa_top41
s_waitcnt lgkmcnt(0)
ds_read_b32 v8, v30
ds_read_b128 v[194:197], v25 offset:64
ds_read_b64 v[160:161], v22 offset:41472
ds_read_b32 v144, v20
s_mov_b64 exec, 65535
global_store_dwordx4 v16, v[198:201], s[38:39] sc1
s_mov_b64 exec, -1
v_add_u32 v16, 18432, v16
v_fma_f32 v206, v90, v26, v126
v_fma_f32 v207, v91, v26, v127
v_fma_f32 v208, v92, v26, v128
v_fma_f32 v209, v93, v26, v129
v_cvt_pk_bf16_f32 v150, v206, v207
v_cvt_pk_bf16_f32 v151, v208, v209
v_max_f32 v47, v186, v187
s_nop 0
v_mfma_f32_16x16x32_bf16 v[182:185], v[150:153], v[138:141], v[32:35]
s_waitcnt lgkmcnt(2)
v_max_f32 v48, v188, v189
v_fma_f32 v206, v94, v194, v130
v_mul_f32 v95, 0x3eae1a93, v95
v_mul_f32 v9, v255, v95
v_mul_f32 v10, v8, v95
s_nop 1
v_max3_f32 v47, v182, v183, v47
v_max3_f32 v48, v184, v185, v48
v_mul_f32 v47, v47, v9
v_mul_f32 v48, v48, v10
v_max_f32 v49, v47, v48
v_mov_b32 v50, v49
s_nop 1
v_permlane16_swap_b32 v49, v50
v_max_f32 v49, v49, v50
v_mov_b32 v50, v49
s_nop 1
v_permlane32_swap_b32 v49, v50
v_max_f32 v49, v49, v50
v_sub_f32 v47, v47, v49
v_sub_f32 v48, v48, v49
v_fma_f32 v207, v94, v195, v131
v_exp_f32 v210, v47
v_fma_f32 v208, v94, v196, v132
v_exp_f32 v211, v48
s_nop 0
v_add_f32 v49, v210, v211
v_mov_b32 v50, v49
v_mul_f32 v210, v210, v255
v_fma_f32 v209, v94, v197, v133
v_permlane16_swap_b32 v49, v50
v_add_f32 v49, v49, v50
v_mov_b32 v50, v49
v_mul_f32 v211, v211, v8
v_cvt_pk_bf16_f32 v156, v206, v207
v_cvt_pk_bf16_f32 v157, v208, v209
s_waitcnt lgkmcnt(0)
s_barrier
v_permlane32_swap_b32 v49, v50
v_add_f32 v49, v49, v50
v_fma_f32 v206, v86, v26, v122
v_rcp_f32 v49, v49
s_nop 0
v_mul_f32 v210, v210, v49
v_mul_f32 v211, v211, v49
v_cvt_pk_bf16_f32 v210, v210, v210
v_cvt_pk_bf16_f32 v211, v211, v211
v_and_b32 v192, v210, v40
v_and_b32 v193, v211, v41
v_and_b32 v190, v210, v42
v_and_b32 v191, v211, v43
s_nop 1
v_mfma_f32_16x16x32_bf16 v[186:189], v[154:157], v[190:193], 0
v_mfma_f32_16x16x32_bf16 v[182:185], v[158:161], v[190:193], 0
)"
R"(v_fma_f32 v207, v87, v26, v123
v_fma_f32 v208, v88, v26, v124
v_fma_f32 v209, v89, v26, v125
v_cvt_pk_bf16_f32 v142, v206, v207
v_cvt_pk_bf16_f32 v143, v208, v209
s_nop 1
v_cvt_pk_bf16_f32 v108, v186, v187
v_cvt_pk_bf16_f32 v109, v188, v189
ds_write_b64 v18, v[108:109]
v_cvt_pk_bf16_f32 v210, v182, v183
ds_write_b32 v19, v210
s_waitcnt vmcnt(0)
s_waitcnt lgkmcnt(0)
s_barrier
ds_read_b128 v[198:201], v17 offset:0
s_waitcnt lgkmcnt(0)
global_store_dwordx4 v16, v[198:201], s[38:39] sc1
s_waitcnt vmcnt(0)
s_barrier
s_endpgm
.Lqa_w4:
s_nop 7
v_mov_b32 v206, v32
v_mov_b32 v207, v33
v_mov_b32 v208, v34
v_mov_b32 v209, v35
s_mov_b32 s70, 33
s_waitcnt vmcnt(0) lgkmcnt(0)
s_nop 7
s_branch .Lqa_in42
.p2align 6
.Lqa_top52:
s_waitcnt lgkmcnt(6)
v_mfma_f32_16x16x32_bf16 v[134:137], v[106:109], v[0:3], 0
s_waitcnt lgkmcnt(5)
ds_read_b128 v[58:61], v52 offset:4096
ds_read_b32 v95, v24 offset:128
v_mfma_f32_16x16x32_bf16 v[134:137], v[106:109], v[4:7], v[134:137]
ds_read_b32 v254, v29
ds_read_b32 v255, v30
v_mfma_f32_16x16x32_bf16 v[110:113], a[0:3], v[0:3], 0
s_waitcnt lgkmcnt(7)
ds_read_b128 v[62:65], v51 offset:6144
ds_read_b128 v[198:201], v25 offset:64
v_mfma_f32_16x16x32_bf16 v[134:137], v[106:109], v[96:99], v[134:137]
s_waitcnt lgkmcnt(8)
ds_read_b128 v[66:69], v52 offset:6144
ds_read_b64 v[160:161], v22 offset:41472
v_mfma_f32_16x16x32_bf16 v[134:137], v[106:109], v[100:103], v[134:137]
ds_read_b32 v144, v20
v_fma_f32 v210, v90, v23, v126
v_mfma_f32_16x16x32_bf16 v[114:117], a[4:7], v[0:3], 0
s_waitcnt lgkmcnt(9)
ds_read_b128 v[70:73], v51 offset:8192
v_fma_f32 v211, v91, v23, v127
v_mfma_f32_16x16x32_bf16 v[134:137], v[106:109], v[54:57], v[134:137]
s_waitcnt lgkmcnt(9)
ds_read_b128 v[74:77], v52 offset:8192
v_fma_f32 v212, v92, v23, v128
v_mfma_f32_16x16x32_bf16 v[134:137], v[106:109], v[58:61], v[134:137]
v_fma_f32 v213, v93, v23, v129
v_cvt_pk_bf16_f32 v150, v210, v211
v_mfma_f32_16x16x32_bf16 v[118:121], v[0:3], a[8:11], 0
s_waitcnt lgkmcnt(6)
ds_read_b128 v[78:81], v51 offset:10240
v_cvt_pk_bf16_f32 v151, v212, v213
v_mfma_f32_16x16x32_bf16 v[134:137], v[106:109], v[62:65], v[134:137]
ds_read_b128 v[82:85], v52 offset:10240
v_mfma_f32_16x16x32_bf16 v[186:189], v[150:153], v[138:141], v[32:35]
s_waitcnt lgkmcnt(6)
v_max_f32 v10, v190, v191
v_max_f32 v44, v192, v193
v_mfma_f32_16x16x32_bf16 v[134:137], v[106:109], v[66:69], v[134:137]
v_fma_f32 v210, v94, v198, v130
v_fma_f32 v211, v94, v199, v131
v_mfma_f32_16x16x32_bf16 v[110:113], a[12:15], v[4:7], v[110:113]
s_waitcnt lgkmcnt(3)
ds_read_b128 v[162:165], v51 offset:12288
v_mul_f32 v95, 0x3eae1a93, v95
v_mfma_f32_16x16x32_bf16 v[134:137], v[106:109], v[70:73], v[134:137]
s_xor_b32 s49, s49, 0x10000
ds_read_b128 v[166:169], v52 offset:12288
s_waitcnt lgkmcnt(4)
v_mfma_f32_16x16x32_bf16 v[134:137], v[106:109], v[74:77], v[134:137]
s_add_u32 m0, s49, 0
v_mul_f32 v8, v254, v95
v_mul_f32 v9, v255, v95
v_mfma_f32_16x16x32_bf16 v[114:117], a[16:19], v[4:7], v[114:117]
s_waitcnt lgkmcnt(3)
ds_read_b128 v[170:173], v51 offset:14336
v_max3_f32 v10, v186, v187, v10
v_mfma_f32_16x16x32_bf16 v[134:137], v[106:109], v[78:81], v[134:137]
s_waitcnt lgkmcnt(3)
ds_read_b128 v[174:177], v52 offset:14336
v_max3_f32 v44, v188, v189, v44
v_mfma_f32_16x16x32_bf16 v[134:137], v[106:109], v[82:85], v[134:137]
v_mul_f32 v10, v10, v8
v_mul_f32 v44, v44, v9
v_mfma_f32_16x16x32_bf16 v[118:121], v[4:7], a[20:23], v[118:121]
s_waitcnt lgkmcnt(3)
ds_read_b128 v[178:181], v51 offset:16384
v_max_f32 v45, v10, v44
v_mfma_f32_16x16x32_bf16 v[134:137], v[106:109], v[162:165], v[134:137]
s_waitcnt lgkmcnt(3)
ds_read_b128 v[182:185], v52 offset:16384
v_mov_b32 v46, v45
v_mfma_f32_16x16x32_bf16 v[134:137], v[106:109], v[166:169], v[134:137]
s_nop 0
v_permlane16_swap_b32 v45, v46
v_mfma_f32_16x16x32_bf16 v[110:113], a[24:27], v[96:99], v[110:113]
s_waitcnt lgkmcnt(3)
global_load_lds_dwordx4 v11, s[42:43] offset:-2048 sc1
v_max_f32 v45, v45, v46
v_mfma_f32_16x16x32_bf16 v[134:137], v[106:109], v[170:173], v[134:137]
s_waitcnt lgkmcnt(2)
v_mov_b32 v46, v45
v_fma_f32 v212, v94, v200, v132
v_mfma_f32_16x16x32_bf16 v[134:137], v[106:109], v[174:177], v[134:137]
v_permlane32_swap_b32 v45, v46
v_mfma_f32_16x16x32_bf16 v[114:117], a[28:31], v[96:99], v[114:117]
s_waitcnt lgkmcnt(1)
v_max_f32 v45, v45, v46
v_sub_f32 v10, v10, v45
v_mfma_f32_16x16x32_bf16 v[134:137], v[106:109], v[178:181], v[134:137]
s_waitcnt lgkmcnt(0)
v_sub_f32 v44, v44, v45
v_fma_f32 v213, v94, v201, v133
v_mfma_f32_16x16x32_bf16 v[134:137], v[106:109], v[182:185], v[134:137]
v_exp_f32 v214, v10
v_mfma_f32_16x16x32_bf16 v[118:121], v[96:99], a[32:35], v[118:121]
v_exp_f32 v215, v44
v_mfma_f32_16x16x32_bf16 v[110:113], a[36:39], v[100:103], v[110:113]
v_add_f32 v45, v214, v215
v_mov_b32 v46, v45
v_mfma_f32_16x16x32_bf16 v[114:117], a[40:43], v[100:103], v[114:117]
s_nop 0
v_mul_f32 v202, 0x3ae38e39, v134
v_cvt_pk_bf16_f32 v216, -v202, -v202
v_mfma_f32_16x16x32_bf16 v[118:121], v[100:103], a[44:47], v[118:121]
v_lshlrev_b32 v216, 16, v216
global_load_lds_dwordx4 v12, s[42:43] offset:-1024 sc1
v_mfma_f32_16x16x32_bf16 v[110:113], a[48:51], v[54:57], v[110:113]
ds_write_b32 v24, v216 offset:0
v_mfma_f32_16x16x32_bf16 v[114:117], a[52:55], v[54:57], v[114:117]
v_mul_f32 v214, v214, v254
v_cvt_pk_bf16_f32 v156, v210, v211
v_mfma_f32_16x16x32_bf16 v[118:121], v[54:57], a[56:59], v[118:121]
v_permlane16_swap_b32 v45, v46
v_mfma_f32_16x16x32_bf16 v[110:113], a[60:63], v[58:61], v[110:113]
v_add_f32 v45, v45, v46
v_mov_b32 v46, v45
v_mfma_f32_16x16x32_bf16 v[114:117], a[64:67], v[58:61], v[114:117]
v_mul_f32 v215, v215, v255
v_cvt_pk_bf16_f32 v157, v212, v213
v_mfma_f32_16x16x32_bf16 v[118:121], v[58:61], a[68:71], v[118:121]
v_mfma_f32_16x16x32_bf16 v[110:113], a[72:75], v[62:65], v[110:113]
s_waitcnt lgkmcnt(0)
s_barrier
v_mfma_f32_16x16x32_bf16 v[114:117], a[76:79], v[62:65], v[114:117]
global_load_lds_dwordx4 v13, s[42:43] offset:0 sc1
ds_read_b32 v216, v26
v_mfma_f32_16x16x32_bf16 v[118:121], v[62:65], a[80:83], v[118:121]
v_permlane32_swap_b32 v45, v46
v_mfma_f32_16x16x32_bf16 v[110:113], a[84:87], v[66:69], v[110:113]
v_add_f32 v45, v45, v46
v_fma_f32 v210, v86, v23, v122
v_mfma_f32_16x16x32_bf16 v[114:117], a[88:91], v[66:69], v[114:117]
v_rcp_f32 v45, v45
v_mfma_f32_16x16x32_bf16 v[118:121], v[66:69], a[92:95], v[118:121]
v_mul_f32 v214, v214, v45
v_mul_f32 v215, v215, v45
v_mfma_f32_16x16x32_bf16 v[110:113], a[96:99], v[70:73], v[110:113]
v_cvt_pk_bf16_f32 v214, v214, v214
v_cvt_pk_bf16_f32 v215, v215, v215
v_mfma_f32_16x16x32_bf16 v[114:117], a[100:103], v[70:73], v[114:117]
v_and_b32 v196, v214, v40
v_and_b32 v197, v215, v41
v_mfma_f32_16x16x32_bf16 v[118:121], v[70:73], a[104:107], v[118:121]
v_and_b32 v194, v214, v42
v_and_b32 v195, v215, v43
v_mfma_f32_16x16x32_bf16 v[110:113], a[108:111], v[74:77], v[110:113]
global_load_lds_dwordx4 v14, s[42:43] offset:1024 sc1
s_add_u32 s42, s42, s46
s_addc_u32 s43, s43, s47
v_mfma_f32_16x16x32_bf16 v[190:193], v[154:157], v[194:197], 0
v_mfma_f32_16x16x32_bf16 v[186:189], v[158:161], v[194:197], 0
s_waitcnt lgkmcnt(0)
v_mul_f32 v216, 0x3ae38e39, v216
v_fma_f32 v203, -v202, v202, v216
v_mfma_f32_16x16x32_bf16 v[114:117], a[112:115], v[74:77], v[114:117]
v_max_f32 v203, 0, v203
v_cvt_pk_bf16_f32 v203, v203, v203
v_mfma_f32_16x16x32_bf16 v[118:121], v[74:77], a[116:119], v[118:121]
v_lshlrev_b32 v203, 16, v203
v_add_f32 v203, 0x3727c5ac, v203
v_mfma_f32_16x16x32_bf16 v[110:113], a[120:123], v[78:81], v[110:113]
v_cvt_pk_bf16_f32 v203, v203, v203
v_lshlrev_b32 v203, 16, v203
v_mfma_f32_16x16x32_bf16 v[114:117], a[124:127], v[78:81], v[114:117]
v_cvt_pk_bf16_f32 v104, v190, v191
v_cvt_pk_bf16_f32 v105, v192, v193
v_mfma_f32_16x16x32_bf16 v[118:121], v[78:81], a[128:131], v[118:121]
ds_write_b64 v18, v[104:105]
v_mfma_f32_16x16x32_bf16 v[110:113], a[132:135], v[82:85], v[110:113]
v_cvt_pk_bf16_f32 v214, v188, v189
v_fma_f32 v211, v87, v23, v123
v_mfma_f32_16x16x32_bf16 v[114:117], a[136:139], v[82:85], v[114:117]
ds_write_b32 v19, v214
v_mfma_f32_16x16x32_bf16 v[118:121], v[82:85], a[140:143], v[118:121]
v_sqrt_f32 v203, v203
v_mfma_f32_16x16x32_bf16 v[110:113], a[144:147], v[162:165], v[110:113]
v_cvt_pk_bf16_f32 v203, v203, v203
v_lshlrev_b32 v203, 16, v203
v_mfma_f32_16x16x32_bf16 v[114:117], a[148:151], v[162:165], v[114:117]
v_rcp_f32 v216, v203
v_mfma_f32_16x16x32_bf16 v[118:121], v[162:165], a[152:155], v[118:121]
ds_write_b32 v24, v216 offset:128
v_mfma_f32_16x16x32_bf16 v[110:113], a[156:159], v[166:169], v[110:113]
v_fma_f32 v212, v88, v23, v124
v_fma_f32 v213, v89, v23, v125
v_mfma_f32_16x16x32_bf16 v[114:117], a[160:163], v[166:169], v[114:117]
v_cvt_pk_bf16_f32 v142, v210, v211
v_cvt_pk_bf16_f32 v143, v212, v213
v_mfma_f32_16x16x32_bf16 v[118:121], v[166:169], a[164:167], v[118:121]
v_mfma_f32_16x16x32_bf16 v[110:113], a[168:171], v[170:173], v[110:113]
v_mfma_f32_16x16x32_bf16 v[114:117], a[172:175], v[170:173], v[114:117]
v_mfma_f32_16x16x32_bf16 v[118:121], v[170:173], a[176:179], v[118:121]
v_mfma_f32_16x16x32_bf16 v[110:113], a[180:183], v[174:177], v[110:113]
v_mfma_f32_16x16x32_bf16 v[114:117], a[184:187], v[174:177], v[114:117]
s_waitcnt vmcnt(5) lgkmcnt(0)
s_barrier
v_mfma_f32_16x16x32_bf16 v[118:121], v[174:177], a[188:191], v[118:121]
ds_read_b128 v[0:3], v51 offset:18432
v_mfma_f32_16x16x32_bf16 v[110:113], a[192:195], v[178:181], v[110:113]
ds_read_b128 v[4:7], v52 offset:18432
ds_read_b32 v23, v24 offset:0
v_mfma_f32_16x16x32_bf16 v[114:117], a[196:199], v[178:181], v[114:117]
s_mov_b64 exec, 1
global_store_dword v47, v48, s[38:39] sc1
s_mov_b64 exec, -1
v_mfma_f32_16x16x32_bf16 v[190:193], v[150:153], v[142:145], v[36:39]
v_mfma_f32_16x16x32_bf16 v[118:121], v[178:181], a[200:203], v[118:121]
ds_read_b128 v[96:99], v51 offset:20480
v_add_u32 v48, 1, v48
v_mfma_f32_16x16x32_bf16 v[110:113], a[204:207], v[182:185], v[110:113]
ds_read_b128 v[100:103], v52 offset:20480
ds_read_b32 v148, v21
v_mfma_f32_16x16x32_bf16 v[114:117], a[208:211], v[182:185], v[114:117]
ds_read_b32 v95, v24 offset:192
ds_read_b32 v254, v27
v_mfma_f32_16x16x32_bf16 v[118:121], v[182:185], a[212:215], v[118:121]
s_waitcnt lgkmcnt(7)
ds_read_b128 v[54:57], v51 offset:22528
ds_read_b32 v255, v28
v_mfma_f32_16x16x32_bf16 v[134:137], v[106:109], v[0:3], 0
s_waitcnt lgkmcnt(8)
ds_read_b128 v[58:61], v52 offset:22528
ds_read_b128 v[198:201], v25 offset:0
v_mfma_f32_16x16x32_bf16 v[134:137], v[106:109], v[4:7], v[134:137]
ds_read_b64 v[158:159], v22 offset:41472
ds_read_b32 v140, v20
v_mfma_f32_16x16x32_bf16 v[122:125], a[0:3], v[0:3], 0
s_waitcnt lgkmcnt(11)
ds_read_b128 v[62:65], v51 offset:24576
v_fma_f32 v210, v90, v23, v114
s_waitcnt lgkmcnt(11)
v_mfma_f32_16x16x32_bf16 v[134:137], v[106:109], v[96:99], v[134:137]
s_waitcnt lgkmcnt(10)
ds_read_b128 v[66:69], v52 offset:24576
v_fma_f32 v211, v91, v23, v115
v_mfma_f32_16x16x32_bf16 v[134:137], v[106:109], v[100:103], v[134:137]
v_fma_f32 v212, v92, v23, v116
v_fma_f32 v213, v93, v23, v117
v_mfma_f32_16x16x32_bf16 v[126:129], a[4:7], v[0:3], 0
s_waitcnt lgkmcnt(7)
ds_read_b128 v[70:73], v51 offset:26624
v_cvt_pk_bf16_f32 v146, v210, v211
v_mfma_f32_16x16x32_bf16 v[134:137], v[106:109], v[54:57], v[134:137]
s_waitcnt lgkmcnt(6)
ds_read_b128 v[74:77], v52 offset:26624
v_cvt_pk_bf16_f32 v147, v212, v213
v_mfma_f32_16x16x32_bf16 v[134:137], v[106:109], v[58:61], v[134:137]
s_nop 0
v_mfma_f32_16x16x32_bf16 v[186:189], v[146:149], v[142:145], v[206:209]
v_max_f32 v10, v190, v191
v_max_f32 v44, v192, v193
v_mfma_f32_16x16x32_bf16 v[130:133], v[0:3], a[8:11], 0
s_waitcnt lgkmcnt(6)
ds_read_b128 v[78:81], v51 offset:28672
v_fma_f32 v210, v94, v198, v118
s_waitcnt lgkmcnt(4)
v_mfma_f32_16x16x32_bf16 v[134:137], v[106:109], v[62:65], v[134:137]
s_waitcnt lgkmcnt(3)
ds_read_b128 v[82:85], v52 offset:28672
v_mul_f32 v95, 0x3eae1a93, v95
v_mfma_f32_16x16x32_bf16 v[134:137], v[106:109], v[66:69], v[134:137]
v_mul_f32 v8, v254, v95
v_mul_f32 v9, v255, v95
v_mfma_f32_16x16x32_bf16 v[122:125], a[12:15], v[4:7], v[122:125]
s_waitcnt lgkmcnt(3)
ds_read_b128 v[162:165], v51 offset:30720
v_max3_f32 v10, v186, v187, v10
v_mfma_f32_16x16x32_bf16 v[134:137], v[106:109], v[70:73], v[134:137]
s_add_u32 m0, s49, 18432
ds_read_b128 v[166:169], v52 offset:30720
s_waitcnt lgkmcnt(4)
v_mfma_f32_16x16x32_bf16 v[134:137], v[106:109], v[74:77], v[134:137]
v_max3_f32 v44, v188, v189, v44
v_mul_f32 v10, v10, v8
v_mfma_f32_16x16x32_bf16 v[126:129], a[16:19], v[4:7], v[126:129]
s_waitcnt lgkmcnt(3)
ds_read_b128 v[170:173], v51 offset:32768
v_mul_f32 v44, v44, v9
v_mfma_f32_16x16x32_bf16 v[134:137], v[106:109], v[78:81], v[134:137]
s_waitcnt lgkmcnt(3)
ds_read_b128 v[174:177], v52 offset:32768
v_max_f32 v45, v10, v44
v_mfma_f32_16x16x32_bf16 v[134:137], v[106:109], v[82:85], v[134:137]
v_mov_b32 v46, v45
v_fma_f32 v211, v94, v199, v119
v_mfma_f32_16x16x32_bf16 v[130:133], v[4:7], a[20:23], v[130:133]
s_waitcnt lgkmcnt(3)
ds_read_b128 v[178:181], v51 offset:34816
v_fma_f32 v212, v94, v200, v120
v_mfma_f32_16x16x32_bf16 v[134:137], v[106:109], v[162:165], v[134:137]
s_waitcnt lgkmcnt(3)
ds_read_b128 v[182:185], v52 offset:34816
v_fma_f32 v213, v94, v201, v121
v_mfma_f32_16x16x32_bf16 v[134:137], v[106:109], v[166:169], v[134:137]
global_load_lds_dwordx4 v11, s[42:43] offset:-2048 sc1
v_cvt_pk_bf16_f32 v154, v210, v211
v_mfma_f32_16x16x32_bf16 v[122:125], a[24:27], v[96:99], v[122:125]
s_waitcnt lgkmcnt(3)
v_permlane16_swap_b32 v45, v46
v_mfma_f32_16x16x32_bf16 v[134:137], v[106:109], v[170:173], v[134:137]
s_waitcnt lgkmcnt(2)
v_max_f32 v45, v45, v46
v_mov_b32 v46, v45
v_mfma_f32_16x16x32_bf16 v[134:137], v[106:109], v[174:177], v[134:137]
s_nop 0
v_permlane32_swap_b32 v45, v46
v_mfma_f32_16x16x32_bf16 v[126:129], a[28:31], v[96:99], v[126:129]
s_waitcnt lgkmcnt(1)
v_max_f32 v45, v45, v46
v_sub_f32 v10, v10, v45
v_mfma_f32_16x16x32_bf16 v[134:137], v[106:109], v[178:181], v[134:137]
s_waitcnt lgkmcnt(0)
v_sub_f32 v44, v44, v45
v_cvt_pk_bf16_f32 v155, v212, v213
v_mfma_f32_16x16x32_bf16 v[134:137], v[106:109], v[182:185], v[134:137]
v_exp_f32 v214, v10
v_mfma_f32_16x16x32_bf16 v[130:133], v[96:99], a[32:35], v[130:133]
v_exp_f32 v215, v44
v_mfma_f32_16x16x32_bf16 v[122:125], a[36:39], v[100:103], v[122:125]
global_load_lds_dwordx4 v12, s[42:43] offset:-1024 sc1
v_add_f32 v45, v214, v215
v_mfma_f32_16x16x32_bf16 v[126:129], a[40:43], v[100:103], v[126:129]
s_nop 0
v_mul_f32 v202, 0x3ae38e39, v134
v_cvt_pk_bf16_f32 v216, -v202, -v202
v_mfma_f32_16x16x32_bf16 v[130:133], v[100:103], a[44:47], v[130:133]
v_lshlrev_b32 v216, 16, v216
v_mov_b32 v46, v45
v_mfma_f32_16x16x32_bf16 v[122:125], a[48:51], v[54:57], v[122:125]
ds_write_b32 v24, v216 offset:64
v_mfma_f32_16x16x32_bf16 v[126:129], a[52:55], v[54:57], v[126:129]
v_mul_f32 v214, v214, v254
v_fma_f32 v210, v86, v23, v110
v_mfma_f32_16x16x32_bf16 v[130:133], v[54:57], a[56:59], v[130:133]
v_permlane16_swap_b32 v45, v46
v_mfma_f32_16x16x32_bf16 v[122:125], a[60:63], v[58:61], v[122:125]
v_add_f32 v45, v45, v46
v_mov_b32 v46, v45
v_mfma_f32_16x16x32_bf16 v[126:129], a[64:67], v[58:61], v[126:129]
v_mul_f32 v215, v215, v255
v_fma_f32 v211, v87, v23, v111
v_mfma_f32_16x16x32_bf16 v[130:133], v[58:61], a[68:71], v[130:133]
v_mfma_f32_16x16x32_bf16 v[122:125], a[72:75], v[62:65], v[122:125]
s_waitcnt lgkmcnt(0)
s_barrier
v_mfma_f32_16x16x32_bf16 v[126:129], a[76:79], v[62:65], v[126:129]
global_load_lds_dwordx4 v13, s[42:43] offset:0 sc1
ds_read_b32 v216, v26
v_mfma_f32_16x16x32_bf16 v[130:133], v[62:65], a[80:83], v[130:133]
v_permlane32_swap_b32 v45, v46
v_mfma_f32_16x16x32_bf16 v[122:125], a[84:87], v[66:69], v[122:125]
v_add_f32 v45, v45, v46
v_fma_f32 v212, v88, v23, v112
v_mfma_f32_16x16x32_bf16 v[126:129], a[88:91], v[66:69], v[126:129]
v_rcp_f32 v45, v45
v_mfma_f32_16x16x32_bf16 v[130:133], v[66:69], a[92:95], v[130:133]
v_mul_f32 v214, v214, v45
v_mul_f32 v215, v215, v45
v_mfma_f32_16x16x32_bf16 v[122:125], a[96:99], v[70:73], v[122:125]
v_cvt_pk_bf16_f32 v214, v214, v214
v_cvt_pk_bf16_f32 v215, v215, v215
v_mfma_f32_16x16x32_bf16 v[126:129], a[100:103], v[70:73], v[126:129]
v_and_b32 v194, v214, v40
v_and_b32 v195, v215, v41
v_mfma_f32_16x16x32_bf16 v[130:133], v[70:73], a[104:107], v[130:133]
)"
R"(v_and_b32 v196, v214, v42
v_and_b32 v197, v215, v43
v_mfma_f32_16x16x32_bf16 v[122:125], a[108:111], v[74:77], v[122:125]
global_load_lds_dwordx4 v14, s[42:43] offset:1024 sc1
s_sub_u32 s42, s42, 18432
s_subb_u32 s43, s43, 0
v_mfma_f32_16x16x32_bf16 v[190:193], v[154:157], v[194:197], 0
v_mfma_f32_16x16x32_bf16 v[186:189], v[158:161], v[194:197], 0
s_waitcnt lgkmcnt(0)
v_mul_f32 v216, 0x3ae38e39, v216
v_fma_f32 v203, -v202, v202, v216
v_mfma_f32_16x16x32_bf16 v[126:129], a[112:115], v[74:77], v[126:129]
v_max_f32 v203, 0, v203
v_cvt_pk_bf16_f32 v203, v203, v203
v_mfma_f32_16x16x32_bf16 v[130:133], v[74:77], a[116:119], v[130:133]
v_lshlrev_b32 v203, 16, v203
v_add_f32 v203, 0x3727c5ac, v203
v_mfma_f32_16x16x32_bf16 v[122:125], a[120:123], v[78:81], v[122:125]
v_cvt_pk_bf16_f32 v203, v203, v203
v_lshlrev_b32 v203, 16, v203
v_mfma_f32_16x16x32_bf16 v[126:129], a[124:127], v[78:81], v[126:129]
v_cvt_pk_bf16_f32 v104, v190, v191
v_cvt_pk_bf16_f32 v105, v192, v193
v_mfma_f32_16x16x32_bf16 v[130:133], v[78:81], a[128:131], v[130:133]
ds_write_b64 v18, v[104:105]
v_mfma_f32_16x16x32_bf16 v[122:125], a[132:135], v[82:85], v[122:125]
v_cvt_pk_bf16_f32 v214, v188, v189
v_fma_f32 v213, v89, v23, v113
v_mfma_f32_16x16x32_bf16 v[126:129], a[136:139], v[82:85], v[126:129]
ds_write_b32 v19, v214
v_mfma_f32_16x16x32_bf16 v[130:133], v[82:85], a[140:143], v[130:133]
v_sqrt_f32 v203, v203
v_mfma_f32_16x16x32_bf16 v[122:125], a[144:147], v[162:165], v[122:125]
v_cvt_pk_bf16_f32 v203, v203, v203
v_lshlrev_b32 v203, 16, v203
v_mfma_f32_16x16x32_bf16 v[126:129], a[148:151], v[162:165], v[126:129]
v_rcp_f32 v216, v203
v_mfma_f32_16x16x32_bf16 v[130:133], v[162:165], a[152:155], v[130:133]
ds_write_b32 v24, v216 offset:192
v_mfma_f32_16x16x32_bf16 v[122:125], a[156:159], v[166:169], v[122:125]
v_cvt_pk_bf16_f32 v138, v210, v211
v_cvt_pk_bf16_f32 v139, v212, v213
v_mfma_f32_16x16x32_bf16 v[126:129], a[160:163], v[166:169], v[126:129]
v_mfma_f32_16x16x32_bf16 v[130:133], v[166:169], a[164:167], v[130:133]
v_mfma_f32_16x16x32_bf16 v[122:125], a[168:171], v[170:173], v[122:125]
v_mfma_f32_16x16x32_bf16 v[126:129], a[172:175], v[170:173], v[126:129]
v_xor_b32 v51, 0x10000, v51
v_xor_b32 v52, 0x10000, v52
v_mfma_f32_16x16x32_bf16 v[130:133], v[170:173], a[176:179], v[130:133]
v_mfma_f32_16x16x32_bf16 v[122:125], a[180:183], v[174:177], v[122:125]
v_mfma_f32_16x16x32_bf16 v[126:129], a[184:187], v[174:177], v[126:129]
s_waitcnt vmcnt(5) lgkmcnt(0)
s_barrier
.Lqa_in42:
v_mfma_f32_16x16x32_bf16 v[130:133], v[174:177], a[188:191], v[130:133]
s_cmp_eq_u32 s70, s73
s_cbranch_scc1 .Lqa_hk43
.Lqa_hr44:
ds_read_b128 v[0:3], v51 offset:0
v_mfma_f32_16x16x32_bf16 v[122:125], a[192:195], v[178:181], v[122:125]
ds_read_b128 v[4:7], v52 offset:0
ds_read_b32 v23, v24 offset:64
v_mfma_f32_16x16x32_bf16 v[126:129], a[196:199], v[178:181], v[126:129]
s_mov_b64 exec, 1
global_store_dword v47, v48, s[38:39] sc1
s_mov_b64 exec, -1
v_mfma_f32_16x16x32_bf16 v[190:193], v[146:149], v[138:141], v[36:39]
v_mfma_f32_16x16x32_bf16 v[130:133], v[178:181], a[200:203], v[130:133]
ds_read_b128 v[96:99], v51 offset:2048
v_add_u32 v48, 1, v48
v_mfma_f32_16x16x32_bf16 v[122:125], a[204:207], v[182:185], v[122:125]
ds_read_b128 v[100:103], v52 offset:2048
ds_read_b32 v152, v21
v_mfma_f32_16x16x32_bf16 v[126:129], a[208:211], v[182:185], v[126:129]
v_mfma_f32_16x16x32_bf16 v[130:133], v[182:185], a[212:215], v[130:133]
s_sub_u32 s70, s70, 1
ds_read_b128 v[54:57], v51 offset:4096
s_cbranch_scc0 .Lqa_top52
s_waitcnt lgkmcnt(0)
ds_read_b32 v95, v24 offset:128
ds_read_b32 v254, v29
ds_read_b32 v255, v30
ds_read_b128 v[198:201], v25 offset:64
ds_read_b64 v[160:161], v22 offset:41472
ds_read_b32 v144, v20
v_fma_f32 v210, v90, v23, v126
v_fma_f32 v211, v91, v23, v127
v_fma_f32 v212, v92, v23, v128
v_fma_f32 v213, v93, v23, v129
v_cvt_pk_bf16_f32 v150, v210, v211
v_cvt_pk_bf16_f32 v151, v212, v213
s_nop 1
v_mfma_f32_16x16x32_bf16 v[186:189], v[150:153], v[138:141], v[32:35]
s_waitcnt lgkmcnt(2)
v_max_f32 v10, v190, v191
v_max_f32 v44, v192, v193
v_fma_f32 v210, v94, v198, v130
v_fma_f32 v211, v94, v199, v131
v_mul_f32 v95, 0x3eae1a93, v95
v_mul_f32 v8, v254, v95
v_mul_f32 v9, v255, v95
v_max3_f32 v10, v186, v187, v10
v_max3_f32 v44, v188, v189, v44
v_mul_f32 v10, v10, v8
v_mul_f32 v44, v44, v9
v_max_f32 v45, v10, v44
v_mov_b32 v46, v45
s_nop 1
v_permlane16_swap_b32 v45, v46
v_max_f32 v45, v45, v46
v_mov_b32 v46, v45
v_fma_f32 v212, v94, v200, v132
s_nop 0
v_permlane32_swap_b32 v45, v46
v_max_f32 v45, v45, v46
v_sub_f32 v10, v10, v45
v_sub_f32 v44, v44, v45
v_fma_f32 v213, v94, v201, v133
v_exp_f32 v214, v10
v_exp_f32 v215, v44
s_nop 0
v_add_f32 v45, v214, v215
v_mov_b32 v46, v45
v_mul_f32 v214, v214, v254
v_cvt_pk_bf16_f32 v156, v210, v211
v_permlane16_swap_b32 v45, v46
v_add_f32 v45, v45, v46
v_mov_b32 v46, v45
v_mul_f32 v215, v215, v255
v_cvt_pk_bf16_f32 v157, v212, v213
s_waitcnt lgkmcnt(0)
s_barrier
v_permlane32_swap_b32 v45, v46
v_add_f32 v45, v45, v46
v_fma_f32 v210, v86, v23, v122
v_rcp_f32 v45, v45
s_nop 0
v_mul_f32 v214, v214, v45
v_mul_f32 v215, v215, v45
v_cvt_pk_bf16_f32 v214, v214, v214
v_cvt_pk_bf16_f32 v215, v215, v215
v_and_b32 v196, v214, v40
v_and_b32 v197, v215, v41
v_and_b32 v194, v214, v42
v_and_b32 v195, v215, v43
s_nop 1
v_mfma_f32_16x16x32_bf16 v[190:193], v[154:157], v[194:197], 0
v_mfma_f32_16x16x32_bf16 v[186:189], v[158:161], v[194:197], 0
s_nop 6
v_cvt_pk_bf16_f32 v104, v190, v191
v_cvt_pk_bf16_f32 v105, v192, v193
ds_write_b64 v18, v[104:105]
v_cvt_pk_bf16_f32 v214, v188, v189
v_fma_f32 v211, v87, v23, v123
ds_write_b32 v19, v214
v_fma_f32 v212, v88, v23, v124
v_fma_f32 v213, v89, v23, v125
v_cvt_pk_bf16_f32 v142, v210, v211
v_cvt_pk_bf16_f32 v143, v212, v213
s_waitcnt vmcnt(0)
s_waitcnt lgkmcnt(0)
s_barrier
s_mov_b64 exec, 1
global_store_dword v47, v48, s[38:39] sc1
s_mov_b64 exec, -1
v_add_u32 v48, 8, v48
s_waitcnt vmcnt(1)
s_barrier
s_mov_b64 exec, 1
global_store_dword v47, v48, s[38:39] sc1
s_mov_b64 exec, -1
s_cmp_lg_u32 s53, 0
s_cbranch_scc1 .Lqa_n53
global_load_dword v53, v49, s[38:39] sc1
s_waitcnt vmcnt(0)
v_sub_u32 v53, v53, v50
v_cmp_lt_u32 vcc, 0x7f, v53
s_cbranch_vccnz .Lqa_sp54
.Lqa_rt55:
v_add_u32 v49, 102400, v49
global_load_dword v53, v49, s[38:39] sc1
s_waitcnt vmcnt(0)
v_sub_u32 v53, v53, v50
v_cmp_lt_u32 vcc, 0x7f, v53
s_cbranch_vccnz .Lqa_sp56
.Lqa_rt57:
v_add_u32 v49, 102400, v49
global_load_dword v53, v49, s[38:39] sc1
s_waitcnt vmcnt(0)
v_sub_u32 v53, v53, v50
v_cmp_lt_u32 vcc, 0x7f, v53
s_cbranch_vccnz .Lqa_sp58
.Lqa_rt59:
v_add_u32 v49, 102400, v49
global_load_dword v53, v49, s[38:39] sc1
s_waitcnt vmcnt(0)
v_sub_u32 v53, v53, v50
v_cmp_lt_u32 vcc, 0x7f, v53
s_cbranch_vccnz .Lqa_sp60
.Lqa_rt61:
v_add_u32 v49, 102400, v49
s_load_dword s75, s[38:39], 0x0
s_waitcnt vmcnt(0) lgkmcnt(0)
s_add_u32 s75, s75, 1
v_mov_b32 v53, s75
v_mov_b32 v49, 0
global_store_dword v49, v53, s[38:39] sc1
.Lqa_n53:
s_endpgm
.Lqa_hk6:
s_cmp_lg_u32 s70, 18
s_cbranch_scc1 .Lqa_e8
s_add_u32 s42, s42, s50
s_addc_u32 s43, s43, 0
s_mov_b64 s[76:77], -1
s_mov_b64 s[46:47], s[44:45]
s_mov_b32 s73, 17
s_nop 1
s_branch .Lqa_hr7
.Lqa_e8:
s_cmp_lg_u32 s70, 17
s_cbranch_scc1 .Lqa_e9
s_mov_b32 s46, -18432
s_mov_b32 s47, -1
v_or_b32 v234, s52, v234
v_or_b32 v235, s52, v235
v_or_b32 v236, s52, v236
v_or_b32 v237, s52, v237
s_mov_b32 s73, 16
s_nop 1
s_branch .Lqa_hr7
.Lqa_e9:
s_cmp_lg_u32 s70, 16
s_cbranch_scc1 .Lqa_e10
v_mov_b32 v234, v32
v_mov_b32 v235, v33
v_mov_b32 v236, v34
v_mov_b32 v237, v35
s_mov_b32 s73, 15
s_nop 1
s_branch .Lqa_hr7
.Lqa_e10:
s_cmp_lg_u32 s70, 15
s_cbranch_scc1 .Lqa_e11
v_subrev_u32 v16, 18432, v16
s_mov_b32 s73, 3
s_nop 1
s_branch .Lqa_hr7
.Lqa_e11:
s_cmp_lg_u32 s70, 3
s_cbranch_scc1 .Lqa_e12
s_mov_b32 s73, 2
s_nop 1
s_branch .Lqa_hr7
.Lqa_e12:
s_cmp_lg_u32 s70, 2
s_cbranch_scc1 .Lqa_e13
s_add_u32 s46, s46, s50
s_addc_u32 s47, s47, 0
s_mov_b32 s73, 0
s_nop 1
s_branch .Lqa_hr7
.Lqa_e13:
s_cmp_lg_u32 s70, 0
s_cbranch_scc1 .Lqa_e14
v_or_b32 v32, s52, v32
v_or_b32 v33, s52, v33
v_or_b32 v34, s52, v34
v_or_b32 v35, s52, v35
s_mov_b32 s73, 999
s_nop 1
s_branch .Lqa_hr7
.Lqa_e14:
s_branch .Lqa_hr7
.Lqa_hk17:
s_cmp_lg_u32 s70, 18
s_cbranch_scc1 .Lqa_e19
s_add_u32 s42, s42, s50
s_addc_u32 s43, s43, 0
s_mov_b64 s[76:77], -1
s_mov_b64 s[46:47], s[44:45]
s_mov_b32 s73, 17
s_nop 1
s_branch .Lqa_hr18
.Lqa_e19:
s_cmp_lg_u32 s70, 17
s_cbranch_scc1 .Lqa_e20
s_mov_b32 s46, -18432
s_mov_b32 s47, -1
v_or_b32 v234, s52, v234
v_or_b32 v235, s52, v235
v_or_b32 v236, s52, v236
v_or_b32 v237, s52, v237
s_mov_b32 s73, 16
s_nop 1
s_branch .Lqa_hr18
.Lqa_e20:
s_cmp_lg_u32 s70, 16
s_cbranch_scc1 .Lqa_e21
v_mov_b32 v234, v32
v_mov_b32 v235, v33
v_mov_b32 v236, v34
v_mov_b32 v237, v35
s_mov_b32 s73, 15
s_nop 1
s_branch .Lqa_hr18
.Lqa_e21:
s_cmp_lg_u32 s70, 15
s_cbranch_scc1 .Lqa_e22
v_subrev_u32 v16, 18432, v16
s_mov_b32 s73, 3
s_nop 1
s_branch .Lqa_hr18
.Lqa_e22:
s_cmp_lg_u32 s70, 3
s_cbranch_scc1 .Lqa_e23
s_mov_b32 s73, 2
s_nop 1
s_branch .Lqa_hr18
.Lqa_e23:
s_cmp_lg_u32 s70, 2
s_cbranch_scc1 .Lqa_e24
s_add_u32 s46, s46, s50
s_addc_u32 s47, s47, 0
s_mov_b32 s73, 0
s_nop 1
s_branch .Lqa_hr18
.Lqa_e24:
s_cmp_lg_u32 s70, 0
s_cbranch_scc1 .Lqa_e25
v_or_b32 v32, s52, v32
v_or_b32 v33, s52, v33
v_or_b32 v34, s52, v34
v_or_b32 v35, s52, v35
s_mov_b32 s73, 999
s_nop 1
s_branch .Lqa_hr18
.Lqa_e25:
s_branch .Lqa_hr18
.Lqa_sp30:
s_sleep 1
global_load_dword v53, v44, s[38:39] sc1
s_add_u32 s71, s71, 1
s_waitcnt vmcnt(0)
v_sub_u32 v53, v53, v46
v_cmp_lt_u32 vcc, 0x7f, v53
s_and_b64 vcc, vcc, s[76:77]
s_cbranch_vccz .Lqa_rt31
s_cmp_lt_u32 s71, 3000000
s_cbranch_scc1 .Lqa_sp30
s_branch .Lqa_rt31
.Lqa_sp32:
s_sleep 1
global_load_dword v254, v44, s[38:39] sc1
s_add_u32 s71, s71, 1
s_waitcnt vmcnt(0)
v_sub_u32 v254, v254, v46
v_cmp_lt_u32 vcc, 0x7f, v254
s_and_b64 vcc, vcc, s[76:77]
s_cbranch_vccz .Lqa_rt33
s_cmp_lt_u32 s71, 3000000
s_cbranch_scc1 .Lqa_sp32
s_branch .Lqa_rt33
.Lqa_hk28:
s_cmp_lg_u32 s70, 18
s_cbranch_scc1 .Lqa_e34
s_add_u32 s42, s42, s50
s_addc_u32 s43, s43, 0
s_mov_b64 s[76:77], -1
s_mov_b64 s[46:47], s[44:45]
s_mov_b32 s73, 17
s_nop 1
s_branch .Lqa_hr29
.Lqa_e34:
s_cmp_lg_u32 s70, 17
s_cbranch_scc1 .Lqa_e35
s_mov_b32 s46, -18432
s_mov_b32 s47, -1
v_or_b32 v202, s52, v202
v_or_b32 v203, s52, v203
v_or_b32 v204, s52, v204
v_or_b32 v205, s52, v205
s_mov_b32 s73, 16
s_nop 1
s_branch .Lqa_hr29
.Lqa_e35:
s_cmp_lg_u32 s70, 16
s_cbranch_scc1 .Lqa_e36
v_mov_b32 v202, v32
v_mov_b32 v203, v33
v_mov_b32 v204, v34
v_mov_b32 v205, v35
s_mov_b32 s73, 15
s_nop 1
s_branch .Lqa_hr29
.Lqa_e36:
s_cmp_lg_u32 s70, 15
s_cbranch_scc1 .Lqa_e37
v_subrev_u32 v16, 18432, v16
s_mov_b32 s73, 3
)"
R"(s_nop 1
s_branch .Lqa_hr29
.Lqa_e37:
s_cmp_lg_u32 s70, 3
s_cbranch_scc1 .Lqa_e38
v_add_u32 v45, s51, v45
s_mov_b32 s73, 2
s_nop 1
s_branch .Lqa_hr29
.Lqa_e38:
s_cmp_lg_u32 s70, 2
s_cbranch_scc1 .Lqa_e39
v_add_u32 v44, s51, v44
v_add_u32 v46, -32, v46
s_add_u32 s46, s46, s50
s_addc_u32 s47, s47, 0
s_mov_b32 s73, 0
s_nop 1
s_branch .Lqa_hr29
.Lqa_e39:
s_cmp_lg_u32 s70, 0
s_cbranch_scc1 .Lqa_e40
v_or_b32 v32, s52, v32
v_or_b32 v33, s52, v33
v_or_b32 v34, s52, v34
v_or_b32 v35, s52, v35
s_mov_b32 s73, 999
s_nop 1
s_branch .Lqa_hr29
.Lqa_e40:
s_branch .Lqa_hr29
.Lqa_hk43:
s_cmp_lg_u32 s70, 18
s_cbranch_scc1 .Lqa_e45
s_add_u32 s42, s42, s50
s_addc_u32 s43, s43, 0
s_mov_b64 s[76:77], -1
s_mov_b64 s[46:47], s[44:45]
s_mov_b32 s73, 17
s_nop 1
s_branch .Lqa_hr44
.Lqa_e45:
s_cmp_lg_u32 s70, 17
s_cbranch_scc1 .Lqa_e46
s_mov_b32 s46, -18432
s_mov_b32 s47, -1
v_or_b32 v206, s52, v206
v_or_b32 v207, s52, v207
v_or_b32 v208, s52, v208
v_or_b32 v209, s52, v209
s_mov_b32 s73, 16
s_nop 1
s_branch .Lqa_hr44
.Lqa_e46:
s_cmp_lg_u32 s70, 16
s_cbranch_scc1 .Lqa_e47
v_mov_b32 v206, v32
v_mov_b32 v207, v33
v_mov_b32 v208, v34
v_mov_b32 v209, v35
s_mov_b32 s73, 15
s_nop 1
s_branch .Lqa_hr44
.Lqa_e47:
s_cmp_lg_u32 s70, 15
s_cbranch_scc1 .Lqa_e48
v_add_u32 v48, -1, v48
s_mov_b32 s73, 3
s_nop 1
s_branch .Lqa_hr44
.Lqa_e48:
s_cmp_lg_u32 s70, 3
s_cbranch_scc1 .Lqa_e49
s_mov_b32 s73, 2
s_nop 1
s_branch .Lqa_hr44
.Lqa_e49:
s_cmp_lg_u32 s70, 2
s_cbranch_scc1 .Lqa_e50
s_add_u32 s46, s46, s50
s_addc_u32 s47, s47, 0
s_mov_b32 s73, 0
s_nop 1
s_branch .Lqa_hr44
.Lqa_e50:
s_cmp_lg_u32 s70, 0
s_cbranch_scc1 .Lqa_e51
v_or_b32 v32, s52, v32
v_or_b32 v33, s52, v33
v_or_b32 v34, s52, v34
v_or_b32 v35, s52, v35
s_mov_b32 s73, 999
s_nop 1
s_branch .Lqa_hr44
.Lqa_e51:
s_branch .Lqa_hr44
.Lqa_sp54:
s_sleep 1
global_load_dword v53, v49, s[38:39] sc1
s_add_u32 s71, s71, 1
s_waitcnt vmcnt(0)
v_sub_u32 v53, v53, v50
v_cmp_lt_u32 vcc, 0x7f, v53
s_cbranch_vccz .Lqa_rt55
s_cmp_lt_u32 s71, 3000000
s_cbranch_scc1 .Lqa_sp54
s_branch .Lqa_rt55
.Lqa_sp56:
s_sleep 1
global_load_dword v53, v49, s[38:39] sc1
s_add_u32 s71, s71, 1
s_waitcnt vmcnt(0)
v_sub_u32 v53, v53, v50
v_cmp_lt_u32 vcc, 0x7f, v53
s_cbranch_vccz .Lqa_rt57
s_cmp_lt_u32 s71, 3000000
s_cbranch_scc1 .Lqa_sp56
s_branch .Lqa_rt57
.Lqa_sp58:
s_sleep 1
global_load_dword v53, v49, s[38:39] sc1
s_add_u32 s71, s71, 1
s_waitcnt vmcnt(0)
v_sub_u32 v53, v53, v50
v_cmp_lt_u32 vcc, 0x7f, v53
s_cbranch_vccz .Lqa_rt59
s_cmp_lt_u32 s71, 3000000
s_cbranch_scc1 .Lqa_sp58
s_branch .Lqa_rt59
.Lqa_sp60:
s_sleep 1
global_load_dword v53, v49, s[38:39] sc1
s_add_u32 s71, s71, 1
s_waitcnt vmcnt(0)
v_sub_u32 v53, v53, v50
v_cmp_lt_u32 vcc, 0x7f, v53
s_cbranch_vccz .Lqa_rt61
s_cmp_lt_u32 s71, 3000000
s_cbranch_scc1 .Lqa_sp60
s_branch .Lqa_rt61
.Lrole1:
s_and_b32 s75, s36, 3
s_cmp_eq_u32 s75, 1
s_cbranch_scc1 .Lwo_w2
s_cmp_eq_u32 s75, 2
s_cbranch_scc1 .Lwo_w3
s_cmp_eq_u32 s75, 3
s_cbranch_scc1 .Lwo_w4
.Lwo_w1:
s_add_u32 m0, s48, 4096
s_nop 0
global_load_lds_dwordx4 v0, s[40:41] offset:-4096
global_load_lds_dwordx4 v1, s[40:41] offset:-3072
global_load_lds_dwordx4 v2, s[40:41] offset:-2048
global_load_lds_dwordx4 v3, s[40:41] offset:-1024
global_load_lds_dwordx4 v4, s[40:41] offset:0
global_load_lds_dwordx4 v5, s[40:41] offset:1024
global_load_lds_dwordx4 v6, s[40:41] offset:2048
global_load_lds_dwordx4 v7, s[40:41] offset:3072
s_add_u32 m0, s48, 12288
s_nop 0
global_load_lds_dwordx4 v8, s[40:41] offset:-4096
s_add_u32 s40, s40, 73728
s_addc_u32 s41, s41, 0
s_add_u32 m0, s48, 40960
s_nop 0
global_load_lds_dwordx4 v0, s[40:41] offset:-4096
global_load_lds_dwordx4 v1, s[40:41] offset:-3072
global_load_lds_dwordx4 v2, s[40:41] offset:-2048
global_load_lds_dwordx4 v3, s[40:41] offset:-1024
global_load_lds_dwordx4 v4, s[40:41] offset:0
global_load_lds_dwordx4 v5, s[40:41] offset:1024
global_load_lds_dwordx4 v6, s[40:41] offset:2048
global_load_lds_dwordx4 v7, s[40:41] offset:3072
s_add_u32 m0, s48, 49152
s_nop 0
global_load_lds_dwordx4 v8, s[40:41] offset:-4096
s_add_u32 s40, s40, 73728
s_addc_u32 s41, s41, 0
s_add_u32 m0, s48, 77824
s_nop 0
global_load_lds_dwordx4 v0, s[40:41] offset:-4096
global_load_lds_dwordx4 v1, s[40:41] offset:-3072
global_load_lds_dwordx4 v2, s[40:41] offset:-2048
global_load_lds_dwordx4 v3, s[40:41] offset:-1024
global_load_lds_dwordx4 v4, s[40:41] offset:0
global_load_lds_dwordx4 v5, s[40:41] offset:1024
global_load_lds_dwordx4 v6, s[40:41] offset:2048
global_load_lds_dwordx4 v7, s[40:41] offset:3072
s_add_u32 m0, s48, 86016
s_nop 0
global_load_lds_dwordx4 v8, s[40:41] offset:-4096
s_add_u32 s40, s40, 73728
s_addc_u32 s41, s41, 0
v_add_u32 v204, 0x10000, v9
v_add_u32 v205, 0x20000, v9
v_add_u32 v206, 0x10000, v10
v_add_u32 v207, 0x20000, v10
s_waitcnt lgkmcnt(0)
s_mul_i32 s37, s37, 0x9E3779B1
s_add_u32 s37, s37, 0x7F4A7C15
v_add_u32 v22, s37, v22
v_add_u32 v24, s37, v24
s_waitcnt vmcnt(18)
s_barrier
s_add_u32 m0, s48, 114688
ds_read_b64_tr_b16 a[0:1], v9 offset:0
ds_read_b64_tr_b16 a[2:3], v9 offset:2304
global_load_lds_dwordx4 v0, s[40:41] offset:-4096
ds_read_b64_tr_b16 a[4:5], v9 offset:8
ds_read_b64_tr_b16 a[6:7], v9 offset:2312
global_load_lds_dwordx4 v1, s[40:41] offset:-3072
ds_read_b64_tr_b16 a[8:9], v9 offset:64
global_load_lds_dwordx4 v2, s[40:41] offset:-2048
ds_read_b64_tr_b16 a[10:11], v9 offset:2368
ds_read_b64_tr_b16 a[12:13], v9 offset:72
global_load_lds_dwordx4 v3, s[40:41] offset:-1024
ds_read_b64_tr_b16 a[14:15], v9 offset:2376
global_load_lds_dwordx4 v4, s[40:41] offset:0
ds_read_b64_tr_b16 v[60:61], v10 offset:0
ds_read_b64_tr_b16 v[62:63], v10 offset:2304
global_load_lds_dwordx4 v5, s[40:41] offset:1024
ds_read_b64_tr_b16 a[16:17], v9 offset:18432
global_load_lds_dwordx4 v6, s[40:41] offset:2048
ds_read_b64_tr_b16 a[18:19], v9 offset:20736
ds_read_b64_tr_b16 a[20:21], v9 offset:18440
global_load_lds_dwordx4 v7, s[40:41] offset:3072
ds_read_b64_tr_b16 a[22:23], v9 offset:20744
s_add_u32 m0, s48, 122880
ds_read_b64_tr_b16 a[24:25], v9 offset:18496
ds_read_b64_tr_b16 a[26:27], v9 offset:20800
global_load_lds_dwordx4 v8, s[40:41] offset:-4096
ds_read_b64_tr_b16 a[28:29], v9 offset:18504
s_add_u32 s40, s40, 73728
ds_read_b64_tr_b16 a[30:31], v9 offset:20808
ds_read_b64_tr_b16 v[64:65], v10 offset:18432
s_addc_u32 s41, s41, 0
ds_read_b64_tr_b16 v[66:67], v10 offset:20736
s_waitcnt vmcnt(18) lgkmcnt(0)
s_barrier
s_add_u32 m0, s48, 4096
ds_read_b64_tr_b16 a[32:33], v9 offset:36864
ds_read_b64_tr_b16 a[34:35], v9 offset:39168
global_load_lds_dwordx4 v0, s[40:41] offset:-4096
ds_read_b64_tr_b16 a[36:37], v9 offset:36872
ds_read_b64_tr_b16 a[38:39], v9 offset:39176
global_load_lds_dwordx4 v1, s[40:41] offset:-3072
ds_read_b64_tr_b16 a[40:41], v9 offset:36928
global_load_lds_dwordx4 v2, s[40:41] offset:-2048
ds_read_b64_tr_b16 a[42:43], v9 offset:39232
ds_read_b64_tr_b16 a[44:45], v9 offset:36936
global_load_lds_dwordx4 v3, s[40:41] offset:-1024
ds_read_b64_tr_b16 a[46:47], v9 offset:39240
global_load_lds_dwordx4 v4, s[40:41] offset:0
ds_read_b64_tr_b16 v[68:69], v10 offset:36864
ds_read_b64_tr_b16 v[70:71], v10 offset:39168
global_load_lds_dwordx4 v5, s[40:41] offset:1024
ds_read_b64_tr_b16 a[48:49], v9 offset:55296
global_load_lds_dwordx4 v6, s[40:41] offset:2048
ds_read_b64_tr_b16 a[50:51], v9 offset:57600
ds_read_b64_tr_b16 a[52:53], v9 offset:55304
global_load_lds_dwordx4 v7, s[40:41] offset:3072
ds_read_b64_tr_b16 a[54:55], v9 offset:57608
s_add_u32 m0, s48, 12288
ds_read_b64_tr_b16 a[56:57], v9 offset:55360
ds_read_b64_tr_b16 a[58:59], v9 offset:57664
global_load_lds_dwordx4 v8, s[40:41] offset:-4096
ds_read_b64_tr_b16 a[60:61], v9 offset:55368
s_add_u32 s40, s40, 73728
ds_read_b64_tr_b16 a[62:63], v9 offset:57672
ds_read_b64_tr_b16 v[72:73], v10 offset:55296
s_addc_u32 s41, s41, 0
ds_read_b64_tr_b16 v[74:75], v10 offset:57600
s_waitcnt vmcnt(18) lgkmcnt(0)
s_barrier
s_add_u32 m0, s48, 40960
ds_read_b64_tr_b16 a[64:65], v204 offset:8192
ds_read_b64_tr_b16 a[66:67], v204 offset:10496
global_load_lds_dwordx4 v0, s[40:41] offset:-4096
ds_read_b64_tr_b16 a[68:69], v204 offset:8200
ds_read_b64_tr_b16 a[70:71], v204 offset:10504
global_load_lds_dwordx4 v1, s[40:41] offset:-3072
ds_read_b64_tr_b16 a[72:73], v204 offset:8256
global_load_lds_dwordx4 v2, s[40:41] offset:-2048
ds_read_b64_tr_b16 a[74:75], v204 offset:10560
ds_read_b64_tr_b16 a[76:77], v204 offset:8264
global_load_lds_dwordx4 v3, s[40:41] offset:-1024
ds_read_b64_tr_b16 a[78:79], v204 offset:10568
global_load_lds_dwordx4 v4, s[40:41] offset:0
ds_read_b64_tr_b16 v[76:77], v206 offset:8192
ds_read_b64_tr_b16 v[78:79], v206 offset:10496
global_load_lds_dwordx4 v5, s[40:41] offset:1024
ds_read_b64_tr_b16 a[80:81], v204 offset:26624
global_load_lds_dwordx4 v6, s[40:41] offset:2048
ds_read_b64_tr_b16 a[82:83], v204 offset:28928
ds_read_b64_tr_b16 a[84:85], v204 offset:26632
global_load_lds_dwordx4 v7, s[40:41] offset:3072
ds_read_b64_tr_b16 a[86:87], v204 offset:28936
s_add_u32 m0, s48, 49152
ds_read_b64_tr_b16 a[88:89], v204 offset:26688
ds_read_b64_tr_b16 a[90:91], v204 offset:28992
global_load_lds_dwordx4 v8, s[40:41] offset:-4096
ds_read_b64_tr_b16 a[92:93], v204 offset:26696
s_add_u32 s40, s40, 73728
ds_read_b64_tr_b16 a[94:95], v204 offset:29000
ds_read_b64_tr_b16 v[80:81], v206 offset:26624
s_addc_u32 s41, s41, 0
ds_read_b64_tr_b16 v[82:83], v206 offset:28928
s_waitcnt vmcnt(18) lgkmcnt(0)
s_barrier
s_add_u32 m0, s48, 77824
ds_read_b64_tr_b16 a[96:97], v204 offset:45056
ds_read_b64_tr_b16 a[98:99], v204 offset:47360
global_load_lds_dwordx4 v0, s[40:41] offset:-4096
ds_read_b64_tr_b16 a[100:101], v204 offset:45064
ds_read_b64_tr_b16 a[102:103], v204 offset:47368
global_load_lds_dwordx4 v1, s[40:41] offset:-3072
ds_read_b64_tr_b16 a[104:105], v204 offset:45120
global_load_lds_dwordx4 v2, s[40:41] offset:-2048
ds_read_b64_tr_b16 a[106:107], v204 offset:47424
ds_read_b64_tr_b16 a[108:109], v204 offset:45128
global_load_lds_dwordx4 v3, s[40:41] offset:-1024
ds_read_b64_tr_b16 a[110:111], v204 offset:47432
global_load_lds_dwordx4 v4, s[40:41] offset:0
ds_read_b64_tr_b16 v[84:85], v206 offset:45056
ds_read_b64_tr_b16 v[86:87], v206 offset:47360
global_load_lds_dwordx4 v5, s[40:41] offset:1024
ds_read_b64_tr_b16 a[112:113], v204 offset:63488
global_load_lds_dwordx4 v6, s[40:41] offset:2048
ds_read_b64_tr_b16 a[114:115], v205 offset:256
ds_read_b64_tr_b16 a[116:117], v204 offset:63496
global_load_lds_dwordx4 v7, s[40:41] offset:3072
ds_read_b64_tr_b16 a[118:119], v205 offset:264
s_add_u32 m0, s48, 86016
ds_read_b64_tr_b16 a[120:121], v204 offset:63552
ds_read_b64_tr_b16 a[122:123], v205 offset:320
global_load_lds_dwordx4 v8, s[40:41] offset:-4096
ds_read_b64_tr_b16 a[124:125], v204 offset:63560
s_add_u32 s40, s40, 73728
ds_read_b64_tr_b16 a[126:127], v205 offset:328
ds_read_b64_tr_b16 v[88:89], v206 offset:63488
s_addc_u32 s41, s41, 0
ds_read_b64_tr_b16 v[90:91], v207 offset:256
s_waitcnt vmcnt(18) lgkmcnt(0)
s_barrier
s_add_u32 m0, s48, 114688
ds_read_b64_tr_b16 a[128:129], v9 offset:0
ds_read_b64_tr_b16 a[130:131], v9 offset:2304
global_load_lds_dwordx4 v0, s[40:41] offset:-4096
ds_read_b64_tr_b16 a[132:133], v9 offset:8
global_load_lds_dwordx4 v1, s[40:41] offset:-3072
ds_read_b64_tr_b16 a[134:135], v9 offset:2312
ds_read_b64_tr_b16 a[136:137], v9 offset:64
global_load_lds_dwordx4 v2, s[40:41] offset:-2048
ds_read_b64_tr_b16 a[138:139], v9 offset:2368
global_load_lds_dwordx4 v3, s[40:41] offset:-1024
ds_read_b64_tr_b16 a[140:141], v9 offset:72
global_load_lds_dwordx4 v4, s[40:41] offset:0
ds_read_b64_tr_b16 a[142:143], v9 offset:2376
ds_read_b64_tr_b16 v[92:93], v10 offset:0
global_load_lds_dwordx4 v5, s[40:41] offset:1024
ds_read_b64_tr_b16 v[94:95], v10 offset:2304
global_load_lds_dwordx4 v6, s[40:41] offset:2048
ds_read_b64_tr_b16 a[144:145], v9 offset:18432
ds_read_b64_tr_b16 a[146:147], v9 offset:20736
global_load_lds_dwordx4 v7, s[40:41] offset:3072
ds_read_b64_tr_b16 a[148:149], v9 offset:18440
s_add_u32 m0, s48, 122880
ds_read_b64_tr_b16 a[150:151], v9 offset:20744
global_load_lds_dwordx4 v8, s[40:41] offset:-4096
ds_read_b64_tr_b16 a[152:153], v9 offset:18496
ds_read_b64_tr_b16 a[154:155], v9 offset:20800
s_add_u32 s40, s40, 73728
ds_read_b64_tr_b16 a[156:157], v9 offset:18504
s_addc_u32 s41, s41, 0
ds_read_b64_tr_b16 a[158:159], v9 offset:20808
s_waitcnt vmcnt(18) lgkmcnt(0)
s_barrier
s_add_u32 m0, s48, 4096
ds_read_b64_tr_b16 a[160:161], v9 offset:36864
ds_read_b64_tr_b16 a[162:163], v9 offset:39168
global_load_lds_dwordx4 v0, s[40:41] offset:-4096
ds_read_b64_tr_b16 a[164:165], v9 offset:36872
global_load_lds_dwordx4 v1, s[40:41] offset:-3072
ds_read_b64_tr_b16 a[166:167], v9 offset:39176
global_load_lds_dwordx4 v2, s[40:41] offset:-2048
ds_read_b64_tr_b16 a[168:169], v9 offset:36928
global_load_lds_dwordx4 v3, s[40:41] offset:-1024
ds_read_b64_tr_b16 a[170:171], v9 offset:39232
ds_read_b64_tr_b16 a[172:173], v9 offset:36936
global_load_lds_dwordx4 v4, s[40:41] offset:0
ds_read_b64_tr_b16 a[174:175], v9 offset:39240
global_load_lds_dwordx4 v5, s[40:41] offset:1024
ds_read_b64_tr_b16 a[176:177], v9 offset:55296
global_load_lds_dwordx4 v6, s[40:41] offset:2048
ds_read_b64_tr_b16 a[178:179], v9 offset:57600
global_load_lds_dwordx4 v7, s[40:41] offset:3072
ds_read_b64_tr_b16 a[180:181], v9 offset:55304
ds_read_b64_tr_b16 a[182:183], v9 offset:57608
s_add_u32 m0, s48, 12288
)"
R"(ds_read_b64_tr_b16 a[184:185], v9 offset:55360
global_load_lds_dwordx4 v8, s[40:41] offset:-4096
ds_read_b64_tr_b16 a[186:187], v9 offset:57664
s_sub_u32 s40, s40, 589824
ds_read_b64_tr_b16 a[188:189], v9 offset:55368
s_subb_u32 s41, s41, 0
ds_read_b64_tr_b16 a[190:191], v9 offset:57672
s_waitcnt vmcnt(18) lgkmcnt(0)
s_barrier
ds_read_b64_tr_b16 a[192:193], v204 offset:8192
ds_read_b64_tr_b16 a[194:195], v204 offset:10496
ds_read_b64_tr_b16 a[196:197], v204 offset:8200
ds_read_b64_tr_b16 a[198:199], v204 offset:10504
ds_read_b64_tr_b16 a[200:201], v204 offset:8256
ds_read_b64_tr_b16 a[202:203], v204 offset:10560
ds_read_b64_tr_b16 a[204:205], v204 offset:8264
ds_read_b64_tr_b16 a[206:207], v204 offset:10568
ds_read_b64_tr_b16 a[208:209], v204 offset:26624
ds_read_b64_tr_b16 a[210:211], v204 offset:28928
ds_read_b64_tr_b16 a[212:213], v204 offset:26632
ds_read_b64_tr_b16 a[214:215], v204 offset:28936
ds_read_b64_tr_b16 a[216:217], v204 offset:26688
ds_read_b64_tr_b16 a[218:219], v204 offset:28992
ds_read_b64_tr_b16 a[220:221], v204 offset:26696
ds_read_b64_tr_b16 a[222:223], v204 offset:29000
s_waitcnt vmcnt(9) lgkmcnt(0)
s_barrier
ds_read_b64_tr_b16 a[224:225], v204 offset:45056
ds_read_b64_tr_b16 a[226:227], v204 offset:47360
ds_read_b64_tr_b16 a[228:229], v204 offset:45064
ds_read_b64_tr_b16 a[230:231], v204 offset:47368
ds_read_b64_tr_b16 a[232:233], v204 offset:45120
ds_read_b64_tr_b16 a[234:235], v204 offset:47424
ds_read_b64_tr_b16 a[236:237], v204 offset:45128
ds_read_b64_tr_b16 a[238:239], v204 offset:47432
ds_read_b64_tr_b16 a[240:241], v204 offset:63488
ds_read_b64_tr_b16 a[242:243], v205 offset:256
ds_read_b64_tr_b16 a[244:245], v204 offset:63496
ds_read_b64_tr_b16 a[246:247], v205 offset:264
ds_read_b64_tr_b16 a[248:249], v204 offset:63552
ds_read_b64_tr_b16 a[250:251], v205 offset:320
ds_read_b64_tr_b16 a[252:253], v204 offset:63560
ds_read_b64_tr_b16 a[254:255], v205 offset:328
s_waitcnt vmcnt(0) lgkmcnt(0)
s_barrier
ds_read_b64_tr_b16 v[28:29], v9 offset:0
ds_read_b64_tr_b16 v[30:31], v9 offset:2304
ds_read_b64_tr_b16 v[32:33], v9 offset:8
ds_read_b64_tr_b16 v[34:35], v9 offset:2312
ds_read_b64_tr_b16 v[36:37], v9 offset:64
ds_read_b64_tr_b16 v[38:39], v9 offset:2368
ds_read_b64_tr_b16 v[40:41], v9 offset:72
ds_read_b64_tr_b16 v[42:43], v9 offset:2376
ds_read_b64_tr_b16 v[44:45], v9 offset:18432
ds_read_b64_tr_b16 v[46:47], v9 offset:20736
ds_read_b64_tr_b16 v[48:49], v9 offset:18440
ds_read_b64_tr_b16 v[50:51], v9 offset:20744
ds_read_b64_tr_b16 v[52:53], v9 offset:18496
ds_read_b64_tr_b16 v[54:55], v9 offset:20800
ds_read_b64_tr_b16 v[56:57], v9 offset:18504
ds_read_b64_tr_b16 v[58:59], v9 offset:20808
s_waitcnt lgkmcnt(0)
s_barrier
s_mov_b64 s[76:77], 0
s_add_u32 m0, s49, 18432
s_mov_b32 s70, 33
s_waitcnt vmcnt(0) lgkmcnt(0)
s_nop 7
.p2align 6
.Lwo_top5:
s_waitcnt lgkmcnt(2)
v_mfma_f32_16x16x32_bf16 v[96:99], a[0:3], v[132:135], v[96:99]
ds_read_b128 v[144:147], v25 offset:3072
v_mfma_f32_16x16x32_bf16 v[100:103], a[4:7], v[132:135], v[100:103]
v_mfma_f32_16x16x32_bf16 v[104:107], a[8:11], v[132:135], v[104:107]
s_waitcnt vmcnt(7)
s_barrier
v_mfma_f32_16x16x32_bf16 v[108:111], a[12:15], v[132:135], v[108:111]
v_cvt_pk_bf16_f32 v164, v112, v113
v_cvt_pk_bf16_f32 v165, v114, v115
s_mov_b64 exec, 1
global_store_dword v23, v24, s[38:39] sc1
s_mov_b64 exec, -1
v_mfma_f32_16x16x32_bf16 v[128:131], v[60:63], v[132:135], 0
s_waitcnt lgkmcnt(2)
v_cvt_pk_bf16_f32 v166, v116, v117
v_cvt_pk_bf16_f32 v167, v118, v119
v_add_u32 v24, 1, v24
v_mfma_f32_16x16x32_bf16 v[96:99], a[16:19], v[136:139], v[96:99]
ds_read_b128 v[132:135], v25 offset:4096
v_add_f32 v254, v112, v113
v_mfma_f32_16x16x32_bf16 v[100:103], a[20:23], v[136:139], v[100:103]
global_store_dwordx4 v13, v[164:167], s[38:39] offset:0 sc1
v_mfma_f32_16x16x32_bf16 v[104:107], a[24:27], v[136:139], v[104:107]
v_cvt_pk_bf16_f32 v168, v120, v121
v_cvt_pk_bf16_f32 v169, v122, v123
v_mfma_f32_16x16x32_bf16 v[108:111], a[28:31], v[136:139], v[108:111]
v_cvt_pk_bf16_f32 v170, v124, v125
v_cvt_pk_bf16_f32 v171, v126, v127
v_mfma_f32_16x16x32_bf16 v[128:131], v[64:67], v[136:139], v[128:131]
s_waitcnt lgkmcnt(2)
global_store_dwordx4 v13, v[168:171], s[38:39] offset:1024 sc1
v_mfma_f32_16x16x32_bf16 v[96:99], a[32:35], v[140:143], v[96:99]
s_cmp_eq_u32 s70, 32
ds_read_b128 v[136:139], v25 offset:5120
v_add_u32 v13, 18688, v13
v_mfma_f32_16x16x32_bf16 v[100:103], a[36:39], v[140:143], v[100:103]
s_cselect_b64 s[42:43], s[44:45], s[42:43]
v_mul_f32 v255, v112, v112
v_fmac_f32 v255, v113, v113
v_mfma_f32_16x16x32_bf16 v[104:107], a[40:43], v[140:143], v[104:107]
s_cmp_eq_u32 s70, 16
v_add_f32 v254, v254, v114
v_fmac_f32 v255, v114, v114
v_mfma_f32_16x16x32_bf16 v[108:111], a[44:47], v[140:143], v[108:111]
s_cselect_b64 s[42:43], s[46:47], s[42:43]
v_add_f32 v254, v254, v115
v_fmac_f32 v255, v115, v115
v_mfma_f32_16x16x32_bf16 v[128:131], v[68:71], v[140:143], v[128:131]
s_waitcnt lgkmcnt(2)
v_add_f32 v254, v254, v116
v_fmac_f32 v255, v116, v116
v_mfma_f32_16x16x32_bf16 v[96:99], a[48:51], v[144:147], v[96:99]
s_xor_b32 s49, s49, 0x10000
ds_read_b128 v[140:143], v25 offset:6144
v_add_f32 v254, v254, v117
v_mfma_f32_16x16x32_bf16 v[100:103], a[52:55], v[144:147], v[100:103]
s_mov_b32 m0, 47104
v_fmac_f32 v255, v117, v117
v_add_f32 v254, v254, v118
v_mfma_f32_16x16x32_bf16 v[104:107], a[56:59], v[144:147], v[104:107]
v_fmac_f32 v255, v118, v118
v_add_f32 v254, v254, v119
v_mfma_f32_16x16x32_bf16 v[108:111], a[60:63], v[144:147], v[108:111]
global_load_lds_dwordx4 v16, s[42:43] offset:0 sc1
v_fmac_f32 v255, v119, v119
v_mfma_f32_16x16x32_bf16 v[128:131], v[72:75], v[144:147], v[128:131]
s_waitcnt lgkmcnt(2)
v_add_f32 v254, v254, v120
v_fmac_f32 v255, v120, v120
v_mfma_f32_16x16x32_bf16 v[96:99], a[64:67], v[132:135], v[96:99]
ds_read_b128 v[144:147], v25 offset:7168
v_add_f32 v254, v254, v121
v_mfma_f32_16x16x32_bf16 v[100:103], a[68:71], v[132:135], v[100:103]
v_fmac_f32 v255, v121, v121
v_add_f32 v254, v254, v122
v_mfma_f32_16x16x32_bf16 v[104:107], a[72:75], v[132:135], v[104:107]
v_fmac_f32 v255, v122, v122
v_add_f32 v254, v254, v123
v_mfma_f32_16x16x32_bf16 v[108:111], a[76:79], v[132:135], v[108:111]
v_fmac_f32 v255, v123, v123
v_add_f32 v254, v254, v124
v_mfma_f32_16x16x32_bf16 v[128:131], v[76:79], v[132:135], v[128:131]
s_waitcnt vmcnt(9)
ds_read_b128 v[148:151], v19 offset:8192
ds_read_b128 v[152:155], v20 offset:8192
s_waitcnt lgkmcnt(4)
v_mfma_f32_16x16x32_bf16 v[96:99], a[80:83], v[136:139], v[96:99]
ds_read_b128 v[132:135], v25 offset:8192
global_load_lds_dwordx4 v17, s[42:43] offset:1024 sc1
v_mfma_f32_16x16x32_bf16 v[100:103], a[84:87], v[136:139], v[100:103]
v_fmac_f32 v255, v124, v124
v_add_f32 v254, v254, v125
v_mfma_f32_16x16x32_bf16 v[104:107], a[88:91], v[136:139], v[104:107]
v_fmac_f32 v255, v125, v125
v_add_f32 v254, v254, v126
v_mfma_f32_16x16x32_bf16 v[108:111], a[92:95], v[136:139], v[108:111]
v_fmac_f32 v255, v126, v126
v_add_f32 v254, v254, v127
v_mfma_f32_16x16x32_bf16 v[128:131], v[80:83], v[136:139], v[128:131]
s_waitcnt lgkmcnt(4)
v_fmac_f32 v255, v127, v127
v_mfma_f32_16x16x32_bf16 v[96:99], a[96:99], v[140:143], v[96:99]
s_add_u32 m0, s49, 0
ds_read_b128 v[136:139], v25 offset:9216
v_mfma_f32_16x16x32_bf16 v[100:103], a[100:103], v[140:143], v[100:103]
ds_write_b64 v27, v[254:255] offset:45056
v_mfma_f32_16x16x32_bf16 v[104:107], a[104:107], v[140:143], v[104:107]
s_waitcnt lgkmcnt(4)
v_lshlrev_b32 v112, 16, v148
v_and_b32 v113, 0xffff0000, v148
v_mfma_f32_16x16x32_bf16 v[108:111], a[108:111], v[140:143], v[108:111]
s_sub_u32 s42, s42, 18432
v_lshlrev_b32 v114, 16, v149
v_and_b32 v115, 0xffff0000, v149
v_mfma_f32_16x16x32_bf16 v[128:131], v[84:87], v[140:143], v[128:131]
v_lshlrev_b32 v116, 16, v150
v_and_b32 v117, 0xffff0000, v150
v_mfma_f32_16x16x32_bf16 v[96:99], a[112:115], v[144:147], v[96:99]
s_subb_u32 s43, s43, 0
ds_read_b128 v[140:143], v25 offset:10240
v_lshlrev_b32 v118, 16, v151
v_mfma_f32_16x16x32_bf16 v[100:103], a[116:119], v[144:147], v[100:103]
s_waitcnt lgkmcnt(4)
v_and_b32 v119, 0xffff0000, v151
v_lshlrev_b32 v120, 16, v152
v_mfma_f32_16x16x32_bf16 v[104:107], a[120:123], v[144:147], v[104:107]
v_and_b32 v121, 0xffff0000, v152
v_lshlrev_b32 v122, 16, v153
v_mfma_f32_16x16x32_bf16 v[108:111], a[124:127], v[144:147], v[108:111]
v_and_b32 v123, 0xffff0000, v153
v_lshlrev_b32 v124, 16, v154
v_mfma_f32_16x16x32_bf16 v[128:131], v[88:91], v[144:147], v[128:131]
s_waitcnt lgkmcnt(3)
v_and_b32 v125, 0xffff0000, v154
v_lshlrev_b32 v126, 16, v155
v_mfma_f32_16x16x32_bf16 v[96:99], a[128:131], v[132:135], v[96:99]
ds_read_b128 v[144:147], v25 offset:11264
v_and_b32 v127, 0xffff0000, v155
v_mfma_f32_16x16x32_bf16 v[100:103], a[132:135], v[132:135], v[100:103]
v_mfma_f32_16x16x32_bf16 v[104:107], a[136:139], v[132:135], v[104:107]
v_mfma_f32_16x16x32_bf16 v[108:111], a[140:143], v[132:135], v[108:111]
v_mfma_f32_16x16x32_bf16 v[128:131], v[92:95], v[132:135], v[128:131]
s_waitcnt lgkmcnt(3)
v_mfma_f32_16x16x32_bf16 v[96:99], a[144:147], v[136:139], v[96:99]
ds_read_b128 v[132:135], v25 offset:12288
v_mfma_f32_16x16x32_bf16 v[100:103], a[148:151], v[136:139], v[100:103]
v_mfma_f32_16x16x32_bf16 v[104:107], a[152:155], v[136:139], v[104:107]
v_mfma_f32_16x16x32_bf16 v[108:111], a[156:159], v[136:139], v[108:111]
s_waitcnt lgkmcnt(2)
v_mfma_f32_16x16x32_bf16 v[96:99], a[160:163], v[140:143], v[96:99]
ds_read_b128 v[136:139], v25 offset:13312
v_mfma_f32_16x16x32_bf16 v[100:103], a[164:167], v[140:143], v[100:103]
v_mfma_f32_16x16x32_bf16 v[104:107], a[168:171], v[140:143], v[104:107]
ds_write_b128 v26, v[128:131] offset:36864
v_mfma_f32_16x16x32_bf16 v[108:111], a[172:175], v[140:143], v[108:111]
s_waitcnt lgkmcnt(3)
v_mfma_f32_16x16x32_bf16 v[96:99], a[176:179], v[144:147], v[96:99]
ds_read_b128 v[140:143], v25 offset:14336
v_mfma_f32_16x16x32_bf16 v[100:103], a[180:183], v[144:147], v[100:103]
v_mfma_f32_16x16x32_bf16 v[104:107], a[184:187], v[144:147], v[104:107]
s_barrier
v_mfma_f32_16x16x32_bf16 v[108:111], a[188:191], v[144:147], v[108:111]
s_waitcnt lgkmcnt(3)
global_load_lds_dwordx4 v11, s[38:39] offset:-2048 sc1
v_mfma_f32_16x16x32_bf16 v[96:99], a[192:195], v[132:135], v[96:99]
ds_read_b128 v[144:147], v25 offset:15360
v_mfma_f32_16x16x32_bf16 v[100:103], a[196:199], v[132:135], v[100:103]
v_mfma_f32_16x16x32_bf16 v[104:107], a[200:203], v[132:135], v[104:107]
global_load_lds_dwordx4 v11, s[38:39] offset:-1024 sc1
v_mfma_f32_16x16x32_bf16 v[108:111], a[204:207], v[132:135], v[108:111]
s_waitcnt lgkmcnt(3)
v_mfma_f32_16x16x32_bf16 v[96:99], a[208:211], v[136:139], v[96:99]
ds_read_b128 v[132:135], v25 offset:16384
v_mfma_f32_16x16x32_bf16 v[100:103], a[212:215], v[136:139], v[100:103]
global_load_lds_dwordx4 v11, s[38:39] offset:0 sc1
v_mfma_f32_16x16x32_bf16 v[104:107], a[216:219], v[136:139], v[104:107]
v_mfma_f32_16x16x32_bf16 v[108:111], a[220:223], v[136:139], v[108:111]
s_waitcnt lgkmcnt(2)
v_mfma_f32_16x16x32_bf16 v[96:99], a[224:227], v[140:143], v[96:99]
ds_read_b128 v[136:139], v25 offset:17408
global_load_lds_dwordx4 v11, s[38:39] offset:1024 sc1
v_mfma_f32_16x16x32_bf16 v[100:103], a[228:231], v[140:143], v[100:103]
v_mfma_f32_16x16x32_bf16 v[104:107], a[232:235], v[140:143], v[104:107]
v_mfma_f32_16x16x32_bf16 v[108:111], a[236:239], v[140:143], v[108:111]
s_waitcnt lgkmcnt(2)
global_load_lds_dwordx4 v11, s[38:39] offset:2048 sc1
v_add_u32 v11, 18432, v11
v_mfma_f32_16x16x32_bf16 v[96:99], a[240:243], v[144:147], v[96:99]
v_min_u32 v11, v11, v12
v_mfma_f32_16x16x32_bf16 v[100:103], a[244:247], v[144:147], v[100:103]
v_mfma_f32_16x16x32_bf16 v[104:107], a[248:251], v[144:147], v[104:107]
s_waitcnt vmcnt(10)
s_barrier
v_mfma_f32_16x16x32_bf16 v[108:111], a[252:255], v[144:147], v[108:111]
s_waitcnt lgkmcnt(1)
ds_read_b128 v[140:143], v25 offset:18432
v_mfma_f32_16x16x32_bf16 v[96:99], v[28:31], v[132:135], v[96:99]
ds_read_b128 v[144:147], v25 offset:19456
v_mfma_f32_16x16x32_bf16 v[100:103], v[32:35], v[132:135], v[100:103]
v_mfma_f32_16x16x32_bf16 v[104:107], v[36:39], v[132:135], v[104:107]
v_mfma_f32_16x16x32_bf16 v[108:111], v[40:43], v[132:135], v[108:111]
s_waitcnt lgkmcnt(2)
v_mfma_f32_16x16x32_bf16 v[96:99], v[44:47], v[136:139], v[96:99]
ds_read_b128 v[132:135], v25 offset:20480
v_mfma_f32_16x16x32_bf16 v[100:103], v[48:51], v[136:139], v[100:103]
v_mfma_f32_16x16x32_bf16 v[104:107], v[52:55], v[136:139], v[104:107]
v_mfma_f32_16x16x32_bf16 v[108:111], v[56:59], v[136:139], v[108:111]
s_waitcnt lgkmcnt(2)
v_mfma_f32_16x16x32_bf16 v[112:115], a[0:3], v[140:143], v[112:115]
ds_read_b128 v[136:139], v25 offset:21504
v_mfma_f32_16x16x32_bf16 v[116:119], a[4:7], v[140:143], v[116:119]
v_mfma_f32_16x16x32_bf16 v[120:123], a[8:11], v[140:143], v[120:123]
s_waitcnt vmcnt(7)
s_barrier
v_mfma_f32_16x16x32_bf16 v[124:127], a[12:15], v[140:143], v[124:127]
s_mov_b64 exec, 1
global_store_dword v23, v24, s[38:39] sc1
s_mov_b64 exec, -1
v_add_u32 v24, 1, v24
v_mfma_f32_16x16x32_bf16 v[128:131], v[60:63], v[140:143], 0
s_waitcnt lgkmcnt(2)
v_cvt_pk_bf16_f32 v164, v96, v97
v_cvt_pk_bf16_f32 v165, v98, v99
v_mfma_f32_16x16x32_bf16 v[112:115], a[16:19], v[144:147], v[112:115]
ds_read_b128 v[140:143], v25 offset:22528
v_cvt_pk_bf16_f32 v166, v100, v101
v_mfma_f32_16x16x32_bf16 v[116:119], a[20:23], v[144:147], v[116:119]
v_cvt_pk_bf16_f32 v167, v102, v103
v_add_f32 v254, v96, v97
v_mfma_f32_16x16x32_bf16 v[120:123], a[24:27], v[144:147], v[120:123]
global_store_dwordx4 v13, v[164:167], s[38:39] offset:0 sc1
v_mfma_f32_16x16x32_bf16 v[124:127], a[28:31], v[144:147], v[124:127]
v_cvt_pk_bf16_f32 v168, v104, v105
v_cvt_pk_bf16_f32 v169, v106, v107
v_mfma_f32_16x16x32_bf16 v[128:131], v[64:67], v[144:147], v[128:131]
s_waitcnt lgkmcnt(2)
v_cvt_pk_bf16_f32 v170, v108, v109
v_cvt_pk_bf16_f32 v171, v110, v111
v_mfma_f32_16x16x32_bf16 v[112:115], a[32:35], v[132:135], v[112:115]
s_mov_b32 m0, 55296
ds_read_b128 v[144:147], v25 offset:23552
v_mul_f32 v255, v96, v96
v_mfma_f32_16x16x32_bf16 v[116:119], a[36:39], v[132:135], v[116:119]
global_store_dwordx4 v13, v[168:171], s[38:39] offset:1024 sc1
v_mfma_f32_16x16x32_bf16 v[120:123], a[40:43], v[132:135], v[120:123]
v_add_u32 v13, 18688, v13
v_fmac_f32 v255, v97, v97
v_mfma_f32_16x16x32_bf16 v[124:127], a[44:47], v[132:135], v[124:127]
v_add_f32 v254, v254, v98
v_fmac_f32 v255, v98, v98
v_mfma_f32_16x16x32_bf16 v[128:131], v[68:71], v[132:135], v[128:131]
s_waitcnt lgkmcnt(2)
v_add_f32 v254, v254, v99
v_fmac_f32 v255, v99, v99
v_mfma_f32_16x16x32_bf16 v[112:115], a[48:51], v[136:139], v[112:115]
ds_read_b128 v[132:135], v25 offset:24576
v_add_f32 v254, v254, v100
v_mfma_f32_16x16x32_bf16 v[116:119], a[52:55], v[136:139], v[116:119]
v_fmac_f32 v255, v100, v100
v_add_f32 v254, v254, v101
v_mfma_f32_16x16x32_bf16 v[120:123], a[56:59], v[136:139], v[120:123]
v_fmac_f32 v255, v101, v101
v_add_f32 v254, v254, v102
v_mfma_f32_16x16x32_bf16 v[124:127], a[60:63], v[136:139], v[124:127]
global_load_lds_dwordx4 v16, s[42:43] offset:0 sc1
v_fmac_f32 v255, v102, v102
v_mfma_f32_16x16x32_bf16 v[128:131], v[72:75], v[136:139], v[128:131]
s_waitcnt lgkmcnt(2)
v_add_f32 v254, v254, v103
v_fmac_f32 v255, v103, v103
v_mfma_f32_16x16x32_bf16 v[112:115], a[64:67], v[140:143], v[112:115]
ds_read_b128 v[136:139], v25 offset:25600
v_add_f32 v254, v254, v104
v_mfma_f32_16x16x32_bf16 v[116:119], a[68:71], v[140:143], v[116:119]
v_fmac_f32 v255, v104, v104
v_add_f32 v254, v254, v105
v_mfma_f32_16x16x32_bf16 v[120:123], a[72:75], v[140:143], v[120:123]
v_fmac_f32 v255, v105, v105
v_add_f32 v254, v254, v106
v_mfma_f32_16x16x32_bf16 v[124:127], a[76:79], v[140:143], v[124:127]
v_fmac_f32 v255, v106, v106
v_add_f32 v254, v254, v107
v_mfma_f32_16x16x32_bf16 v[128:131], v[76:79], v[140:143], v[128:131]
s_waitcnt vmcnt(9)
ds_read_b128 v[148:151], v19 offset:0
ds_read_b128 v[152:155], v20 offset:0
s_waitcnt lgkmcnt(4)
v_mfma_f32_16x16x32_bf16 v[112:115], a[80:83], v[144:147], v[112:115]
ds_read_b128 v[140:143], v25 offset:26624
global_load_lds_dwordx4 v17, s[42:43] offset:1024 sc1
v_mfma_f32_16x16x32_bf16 v[116:119], a[84:87], v[144:147], v[116:119]
v_fmac_f32 v255, v107, v107
v_add_f32 v254, v254, v108
v_mfma_f32_16x16x32_bf16 v[120:123], a[88:91], v[144:147], v[120:123]
v_fmac_f32 v255, v108, v108
v_add_f32 v254, v254, v109
v_mfma_f32_16x16x32_bf16 v[124:127], a[92:95], v[144:147], v[124:127]
v_fmac_f32 v255, v109, v109
v_add_f32 v254, v254, v110
v_mfma_f32_16x16x32_bf16 v[128:131], v[80:83], v[144:147], v[128:131]
s_waitcnt lgkmcnt(4)
v_fmac_f32 v255, v110, v110
v_add_f32 v254, v254, v111
v_mfma_f32_16x16x32_bf16 v[112:115], a[96:99], v[132:135], v[112:115]
s_add_u32 m0, s49, 18432
ds_read_b128 v[144:147], v25 offset:27648
v_fmac_f32 v255, v111, v111
v_mfma_f32_16x16x32_bf16 v[116:119], a[100:103], v[132:135], v[116:119]
ds_write_b64 v27, v[254:255] offset:43008
v_mfma_f32_16x16x32_bf16 v[120:123], a[104:107], v[132:135], v[120:123]
s_waitcnt lgkmcnt(4)
v_lshlrev_b32 v96, 16, v148
v_and_b32 v97, 0xffff0000, v148
v_mfma_f32_16x16x32_bf16 v[124:127], a[108:111], v[132:135], v[124:127]
s_sub_u32 s42, s42, 18432
v_lshlrev_b32 v98, 16, v149
v_and_b32 v99, 0xffff0000, v149
v_mfma_f32_16x16x32_bf16 v[128:131], v[84:87], v[132:135], v[128:131]
v_lshlrev_b32 v100, 16, v150
v_and_b32 v101, 0xffff0000, v150
v_mfma_f32_16x16x32_bf16 v[112:115], a[112:115], v[136:139], v[112:115]
s_subb_u32 s43, s43, 0
ds_read_b128 v[132:135], v25 offset:28672
v_lshlrev_b32 v102, 16, v151
v_mfma_f32_16x16x32_bf16 v[116:119], a[116:119], v[136:139], v[116:119]
s_waitcnt lgkmcnt(4)
v_and_b32 v103, 0xffff0000, v151
v_lshlrev_b32 v104, 16, v152
v_mfma_f32_16x16x32_bf16 v[120:123], a[120:123], v[136:139], v[120:123]
)"
R"(v_and_b32 v105, 0xffff0000, v152
v_lshlrev_b32 v106, 16, v153
v_mfma_f32_16x16x32_bf16 v[124:127], a[124:127], v[136:139], v[124:127]
v_and_b32 v107, 0xffff0000, v153
v_lshlrev_b32 v108, 16, v154
v_mfma_f32_16x16x32_bf16 v[128:131], v[88:91], v[136:139], v[128:131]
s_waitcnt lgkmcnt(3)
v_and_b32 v109, 0xffff0000, v154
v_lshlrev_b32 v110, 16, v155
v_mfma_f32_16x16x32_bf16 v[112:115], a[128:131], v[140:143], v[112:115]
ds_read_b128 v[136:139], v25 offset:29696
v_and_b32 v111, 0xffff0000, v155
v_mfma_f32_16x16x32_bf16 v[116:119], a[132:135], v[140:143], v[116:119]
v_mfma_f32_16x16x32_bf16 v[120:123], a[136:139], v[140:143], v[120:123]
v_mfma_f32_16x16x32_bf16 v[124:127], a[140:143], v[140:143], v[124:127]
v_mfma_f32_16x16x32_bf16 v[128:131], v[92:95], v[140:143], v[128:131]
s_waitcnt lgkmcnt(3)
v_mfma_f32_16x16x32_bf16 v[112:115], a[144:147], v[144:147], v[112:115]
ds_read_b128 v[140:143], v25 offset:30720
v_mfma_f32_16x16x32_bf16 v[116:119], a[148:151], v[144:147], v[116:119]
v_mfma_f32_16x16x32_bf16 v[120:123], a[152:155], v[144:147], v[120:123]
v_mfma_f32_16x16x32_bf16 v[124:127], a[156:159], v[144:147], v[124:127]
s_waitcnt lgkmcnt(2)
v_mfma_f32_16x16x32_bf16 v[112:115], a[160:163], v[132:135], v[112:115]
ds_read_b128 v[144:147], v25 offset:31744
v_mfma_f32_16x16x32_bf16 v[116:119], a[164:167], v[132:135], v[116:119]
v_mfma_f32_16x16x32_bf16 v[120:123], a[168:171], v[132:135], v[120:123]
ds_write_b128 v26, v[128:131] offset:39936
v_mfma_f32_16x16x32_bf16 v[124:127], a[172:175], v[132:135], v[124:127]
s_waitcnt lgkmcnt(3)
v_mfma_f32_16x16x32_bf16 v[112:115], a[176:179], v[136:139], v[112:115]
ds_read_b128 v[132:135], v25 offset:32768
v_mfma_f32_16x16x32_bf16 v[116:119], a[180:183], v[136:139], v[116:119]
v_mfma_f32_16x16x32_bf16 v[120:123], a[184:187], v[136:139], v[120:123]
s_barrier
v_mfma_f32_16x16x32_bf16 v[124:127], a[188:191], v[136:139], v[124:127]
s_waitcnt lgkmcnt(3)
global_load_lds_dwordx4 v11, s[38:39] offset:-2048 sc1
v_mfma_f32_16x16x32_bf16 v[112:115], a[192:195], v[140:143], v[112:115]
s_mov_b64 s[76:77], -1
ds_read_b128 v[136:139], v25 offset:33792
v_mfma_f32_16x16x32_bf16 v[116:119], a[196:199], v[140:143], v[116:119]
v_mfma_f32_16x16x32_bf16 v[120:123], a[200:203], v[140:143], v[120:123]
global_load_lds_dwordx4 v11, s[38:39] offset:-1024 sc1
v_mfma_f32_16x16x32_bf16 v[124:127], a[204:207], v[140:143], v[124:127]
s_waitcnt lgkmcnt(3)
v_mfma_f32_16x16x32_bf16 v[112:115], a[208:211], v[144:147], v[112:115]
ds_read_b128 v[140:143], v25 offset:34816
v_mfma_f32_16x16x32_bf16 v[116:119], a[212:215], v[144:147], v[116:119]
global_load_lds_dwordx4 v11, s[38:39] offset:0 sc1
v_mfma_f32_16x16x32_bf16 v[120:123], a[216:219], v[144:147], v[120:123]
v_mfma_f32_16x16x32_bf16 v[124:127], a[220:223], v[144:147], v[124:127]
s_waitcnt lgkmcnt(2)
v_mfma_f32_16x16x32_bf16 v[112:115], a[224:227], v[132:135], v[112:115]
ds_read_b128 v[144:147], v25 offset:35840
global_load_lds_dwordx4 v11, s[38:39] offset:1024 sc1
v_mfma_f32_16x16x32_bf16 v[116:119], a[228:231], v[132:135], v[116:119]
v_mfma_f32_16x16x32_bf16 v[120:123], a[232:235], v[132:135], v[120:123]
v_mfma_f32_16x16x32_bf16 v[124:127], a[236:239], v[132:135], v[124:127]
s_waitcnt lgkmcnt(2)
v_xor_b32 v25, 0x10000, v25
global_load_lds_dwordx4 v11, s[38:39] offset:2048 sc1
v_mfma_f32_16x16x32_bf16 v[112:115], a[240:243], v[136:139], v[112:115]
v_add_u32 v11, 18432, v11
v_min_u32 v11, v11, v12
v_mfma_f32_16x16x32_bf16 v[116:119], a[244:247], v[136:139], v[116:119]
v_mfma_f32_16x16x32_bf16 v[120:123], a[248:251], v[136:139], v[120:123]
s_waitcnt vmcnt(10)
s_barrier
v_mfma_f32_16x16x32_bf16 v[124:127], a[252:255], v[136:139], v[124:127]
s_waitcnt lgkmcnt(1)
ds_read_b128 v[132:135], v25 offset:0
v_mfma_f32_16x16x32_bf16 v[112:115], v[28:31], v[140:143], v[112:115]
ds_read_b128 v[136:139], v25 offset:1024
v_mfma_f32_16x16x32_bf16 v[116:119], v[32:35], v[140:143], v[116:119]
v_mfma_f32_16x16x32_bf16 v[120:123], v[36:39], v[140:143], v[120:123]
v_mfma_f32_16x16x32_bf16 v[124:127], v[40:43], v[140:143], v[124:127]
s_waitcnt lgkmcnt(2)
v_mfma_f32_16x16x32_bf16 v[112:115], v[44:47], v[144:147], v[112:115]
ds_read_b128 v[140:143], v25 offset:2048
v_mfma_f32_16x16x32_bf16 v[116:119], v[48:51], v[144:147], v[116:119]
v_mfma_f32_16x16x32_bf16 v[120:123], v[52:55], v[144:147], v[120:123]
v_mfma_f32_16x16x32_bf16 v[124:127], v[56:59], v[144:147], v[124:127]
s_sub_u32 s70, s70, 1
s_cbranch_scc0 .Lwo_top5
s_waitcnt vmcnt(7)
s_barrier
s_mov_b64 exec, 1
global_store_dword v23, v24, s[38:39] sc1
s_mov_b64 exec, -1
v_add_u32 v24, 1, v24
v_cvt_pk_bf16_f32 v164, v112, v113
v_cvt_pk_bf16_f32 v165, v114, v115
v_cvt_pk_bf16_f32 v166, v116, v117
v_cvt_pk_bf16_f32 v167, v118, v119
global_store_dwordx4 v13, v[164:167], s[38:39] offset:0 sc1
v_cvt_pk_bf16_f32 v168, v120, v121
v_cvt_pk_bf16_f32 v169, v122, v123
v_cvt_pk_bf16_f32 v170, v124, v125
v_cvt_pk_bf16_f32 v171, v126, v127
global_store_dwordx4 v13, v[168:171], s[38:39] offset:1024 sc1
v_add_f32 v254, v112, v113
v_mul_f32 v255, v112, v112
v_fmac_f32 v255, v113, v113
v_add_f32 v254, v254, v114
v_fmac_f32 v255, v114, v114
v_add_f32 v254, v254, v115
v_fmac_f32 v255, v115, v115
v_add_f32 v254, v254, v116
v_fmac_f32 v255, v116, v116
v_add_f32 v254, v254, v117
v_fmac_f32 v255, v117, v117
v_add_f32 v254, v254, v118
v_fmac_f32 v255, v118, v118
v_add_f32 v254, v254, v119
v_fmac_f32 v255, v119, v119
v_add_f32 v254, v254, v120
v_fmac_f32 v255, v120, v120
v_add_f32 v254, v254, v121
v_fmac_f32 v255, v121, v121
v_add_f32 v254, v254, v122
v_fmac_f32 v255, v122, v122
v_add_f32 v254, v254, v123
v_fmac_f32 v255, v123, v123
v_add_f32 v254, v254, v124
v_fmac_f32 v255, v124, v124
v_add_f32 v254, v254, v125
v_fmac_f32 v255, v125, v125
v_add_f32 v254, v254, v126
v_fmac_f32 v255, v126, v126
v_add_f32 v254, v254, v127
v_fmac_f32 v255, v127, v127
ds_write_b64 v27, v[254:255] offset:45056
s_waitcnt vmcnt(0) lgkmcnt(0)
s_barrier
s_mov_b64 exec, 1
global_store_dword v23, v24, s[38:39] sc1
s_mov_b64 exec, -1
v_add_u32 v24, 3, v24
s_waitcnt vmcnt(0)
s_barrier
s_mov_b64 exec, 1
global_store_dword v23, v24, s[38:39] sc1
s_mov_b64 exec, -1
s_endpgm
.Lwo_w2:
s_add_u32 m0, s48, 4096
s_nop 0
global_load_lds_dwordx4 v0, s[40:41] offset:-4096
global_load_lds_dwordx4 v1, s[40:41] offset:-3072
global_load_lds_dwordx4 v2, s[40:41] offset:-2048
global_load_lds_dwordx4 v3, s[40:41] offset:-1024
global_load_lds_dwordx4 v4, s[40:41] offset:0
global_load_lds_dwordx4 v5, s[40:41] offset:1024
global_load_lds_dwordx4 v6, s[40:41] offset:2048
global_load_lds_dwordx4 v7, s[40:41] offset:3072
s_add_u32 m0, s48, 12288
s_nop 0
global_load_lds_dwordx4 v8, s[40:41] offset:-4096
s_add_u32 s40, s40, 73728
s_addc_u32 s41, s41, 0
s_add_u32 m0, s48, 40960
s_nop 0
global_load_lds_dwordx4 v0, s[40:41] offset:-4096
global_load_lds_dwordx4 v1, s[40:41] offset:-3072
global_load_lds_dwordx4 v2, s[40:41] offset:-2048
global_load_lds_dwordx4 v3, s[40:41] offset:-1024
global_load_lds_dwordx4 v4, s[40:41] offset:0
global_load_lds_dwordx4 v5, s[40:41] offset:1024
global_load_lds_dwordx4 v6, s[40:41] offset:2048
global_load_lds_dwordx4 v7, s[40:41] offset:3072
s_add_u32 m0, s48, 49152
s_nop 0
global_load_lds_dwordx4 v8, s[40:41] offset:-4096
s_add_u32 s40, s40, 73728
s_addc_u32 s41, s41, 0
s_add_u32 m0, s48, 77824
s_nop 0
global_load_lds_dwordx4 v0, s[40:41] offset:-4096
global_load_lds_dwordx4 v1, s[40:41] offset:-3072
global_load_lds_dwordx4 v2, s[40:41] offset:-2048
global_load_lds_dwordx4 v3, s[40:41] offset:-1024
global_load_lds_dwordx4 v4, s[40:41] offset:0
global_load_lds_dwordx4 v5, s[40:41] offset:1024
global_load_lds_dwordx4 v6, s[40:41] offset:2048
global_load_lds_dwordx4 v7, s[40:41] offset:3072
s_add_u32 m0, s48, 86016
s_nop 0
global_load_lds_dwordx4 v8, s[40:41] offset:-4096
s_add_u32 s40, s40, 73728
s_addc_u32 s41, s41, 0
v_add_u32 v204, 0x10000, v9
v_add_u32 v205, 0x20000, v9
v_add_u32 v206, 0x10000, v10
v_add_u32 v207, 0x20000, v10
s_waitcnt lgkmcnt(0)
s_mul_i32 s37, s37, 0x9E3779B1
s_add_u32 s37, s37, 0x7F4A7C15
v_add_u32 v22, s37, v22
v_add_u32 v24, s37, v24
s_waitcnt vmcnt(18)
s_barrier
s_add_u32 m0, s48, 114688
ds_read_b64_tr_b16 a[0:1], v9 offset:0
ds_read_b64_tr_b16 a[2:3], v9 offset:2304
global_load_lds_dwordx4 v0, s[40:41] offset:-4096
ds_read_b64_tr_b16 a[4:5], v9 offset:8
global_load_lds_dwordx4 v1, s[40:41] offset:-3072
ds_read_b64_tr_b16 a[6:7], v9 offset:2312
global_load_lds_dwordx4 v2, s[40:41] offset:-2048
ds_read_b64_tr_b16 a[8:9], v9 offset:64
global_load_lds_dwordx4 v3, s[40:41] offset:-1024
ds_read_b64_tr_b16 a[10:11], v9 offset:2368
ds_read_b64_tr_b16 a[12:13], v9 offset:72
global_load_lds_dwordx4 v4, s[40:41] offset:0
ds_read_b64_tr_b16 a[14:15], v9 offset:2376
global_load_lds_dwordx4 v5, s[40:41] offset:1024
ds_read_b64_tr_b16 a[16:17], v9 offset:18432
global_load_lds_dwordx4 v6, s[40:41] offset:2048
ds_read_b64_tr_b16 a[18:19], v9 offset:20736
global_load_lds_dwordx4 v7, s[40:41] offset:3072
ds_read_b64_tr_b16 a[20:21], v9 offset:18440
ds_read_b64_tr_b16 a[22:23], v9 offset:20744
s_add_u32 m0, s48, 122880
ds_read_b64_tr_b16 a[24:25], v9 offset:18496
global_load_lds_dwordx4 v8, s[40:41] offset:-4096
ds_read_b64_tr_b16 a[26:27], v9 offset:20800
s_add_u32 s40, s40, 73728
ds_read_b64_tr_b16 a[28:29], v9 offset:18504
s_addc_u32 s41, s41, 0
ds_read_b64_tr_b16 a[30:31], v9 offset:20808
s_waitcnt vmcnt(18) lgkmcnt(0)
s_barrier
s_add_u32 m0, s48, 4096
ds_read_b64_tr_b16 a[32:33], v9 offset:36864
ds_read_b64_tr_b16 a[34:35], v9 offset:39168
global_load_lds_dwordx4 v0, s[40:41] offset:-4096
ds_read_b64_tr_b16 a[36:37], v9 offset:36872
global_load_lds_dwordx4 v1, s[40:41] offset:-3072
ds_read_b64_tr_b16 a[38:39], v9 offset:39176
global_load_lds_dwordx4 v2, s[40:41] offset:-2048
ds_read_b64_tr_b16 a[40:41], v9 offset:36928
global_load_lds_dwordx4 v3, s[40:41] offset:-1024
ds_read_b64_tr_b16 a[42:43], v9 offset:39232
ds_read_b64_tr_b16 a[44:45], v9 offset:36936
global_load_lds_dwordx4 v4, s[40:41] offset:0
ds_read_b64_tr_b16 a[46:47], v9 offset:39240
global_load_lds_dwordx4 v5, s[40:41] offset:1024
ds_read_b64_tr_b16 a[48:49], v9 offset:55296
global_load_lds_dwordx4 v6, s[40:41] offset:2048
ds_read_b64_tr_b16 a[50:51], v9 offset:57600
global_load_lds_dwordx4 v7, s[40:41] offset:3072
ds_read_b64_tr_b16 a[52:53], v9 offset:55304
ds_read_b64_tr_b16 a[54:55], v9 offset:57608
s_add_u32 m0, s48, 12288
ds_read_b64_tr_b16 a[56:57], v9 offset:55360
global_load_lds_dwordx4 v8, s[40:41] offset:-4096
ds_read_b64_tr_b16 a[58:59], v9 offset:57664
s_add_u32 s40, s40, 73728
ds_read_b64_tr_b16 a[60:61], v9 offset:55368
s_addc_u32 s41, s41, 0
ds_read_b64_tr_b16 a[62:63], v9 offset:57672
s_waitcnt vmcnt(18) lgkmcnt(0)
s_barrier
s_add_u32 m0, s48, 40960
ds_read_b64_tr_b16 a[64:65], v204 offset:8192
ds_read_b64_tr_b16 a[66:67], v204 offset:10496
global_load_lds_dwordx4 v0, s[40:41] offset:-4096
ds_read_b64_tr_b16 a[68:69], v204 offset:8200
global_load_lds_dwordx4 v1, s[40:41] offset:-3072
ds_read_b64_tr_b16 a[70:71], v204 offset:10504
global_load_lds_dwordx4 v2, s[40:41] offset:-2048
ds_read_b64_tr_b16 a[72:73], v204 offset:8256
global_load_lds_dwordx4 v3, s[40:41] offset:-1024
ds_read_b64_tr_b16 a[74:75], v204 offset:10560
ds_read_b64_tr_b16 a[76:77], v204 offset:8264
global_load_lds_dwordx4 v4, s[40:41] offset:0
ds_read_b64_tr_b16 a[78:79], v204 offset:10568
global_load_lds_dwordx4 v5, s[40:41] offset:1024
ds_read_b64_tr_b16 a[80:81], v204 offset:26624
global_load_lds_dwordx4 v6, s[40:41] offset:2048
ds_read_b64_tr_b16 a[82:83], v204 offset:28928
global_load_lds_dwordx4 v7, s[40:41] offset:3072
ds_read_b64_tr_b16 a[84:85], v204 offset:26632
ds_read_b64_tr_b16 a[86:87], v204 offset:28936
s_add_u32 m0, s48, 49152
ds_read_b64_tr_b16 a[88:89], v204 offset:26688
global_load_lds_dwordx4 v8, s[40:41] offset:-4096
ds_read_b64_tr_b16 a[90:91], v204 offset:28992
s_add_u32 s40, s40, 73728
ds_read_b64_tr_b16 a[92:93], v204 offset:26696
s_addc_u32 s41, s41, 0
ds_read_b64_tr_b16 a[94:95], v204 offset:29000
s_waitcnt vmcnt(18) lgkmcnt(0)
s_barrier
s_add_u32 m0, s48, 77824
ds_read_b64_tr_b16 a[96:97], v204 offset:45056
ds_read_b64_tr_b16 a[98:99], v204 offset:47360
global_load_lds_dwordx4 v0, s[40:41] offset:-4096
ds_read_b64_tr_b16 a[100:101], v204 offset:45064
global_load_lds_dwordx4 v1, s[40:41] offset:-3072
ds_read_b64_tr_b16 a[102:103], v204 offset:47368
global_load_lds_dwordx4 v2, s[40:41] offset:-2048
ds_read_b64_tr_b16 a[104:105], v204 offset:45120
global_load_lds_dwordx4 v3, s[40:41] offset:-1024
ds_read_b64_tr_b16 a[106:107], v204 offset:47424
ds_read_b64_tr_b16 a[108:109], v204 offset:45128
global_load_lds_dwordx4 v4, s[40:41] offset:0
ds_read_b64_tr_b16 a[110:111], v204 offset:47432
global_load_lds_dwordx4 v5, s[40:41] offset:1024
ds_read_b64_tr_b16 a[112:113], v204 offset:63488
global_load_lds_dwordx4 v6, s[40:41] offset:2048
ds_read_b64_tr_b16 a[114:115], v205 offset:256
global_load_lds_dwordx4 v7, s[40:41] offset:3072
ds_read_b64_tr_b16 a[116:117], v204 offset:63496
ds_read_b64_tr_b16 a[118:119], v205 offset:264
s_add_u32 m0, s48, 86016
ds_read_b64_tr_b16 a[120:121], v204 offset:63552
global_load_lds_dwordx4 v8, s[40:41] offset:-4096
ds_read_b64_tr_b16 a[122:123], v205 offset:320
s_add_u32 s40, s40, 73728
ds_read_b64_tr_b16 a[124:125], v204 offset:63560
s_addc_u32 s41, s41, 0
ds_read_b64_tr_b16 a[126:127], v205 offset:328
s_waitcnt vmcnt(18) lgkmcnt(0)
s_barrier
s_add_u32 m0, s48, 114688
ds_read_b64_tr_b16 a[128:129], v9 offset:0
ds_read_b64_tr_b16 a[130:131], v9 offset:2304
global_load_lds_dwordx4 v0, s[40:41] offset:-4096
ds_read_b64_tr_b16 a[132:133], v9 offset:8
global_load_lds_dwordx4 v1, s[40:41] offset:-3072
ds_read_b64_tr_b16 a[134:135], v9 offset:2312
ds_read_b64_tr_b16 a[136:137], v9 offset:64
global_load_lds_dwordx4 v2, s[40:41] offset:-2048
ds_read_b64_tr_b16 a[138:139], v9 offset:2368
global_load_lds_dwordx4 v3, s[40:41] offset:-1024
ds_read_b64_tr_b16 a[140:141], v9 offset:72
global_load_lds_dwordx4 v4, s[40:41] offset:0
ds_read_b64_tr_b16 a[142:143], v9 offset:2376
ds_read_b64_tr_b16 a[144:145], v9 offset:18432
global_load_lds_dwordx4 v5, s[40:41] offset:1024
ds_read_b64_tr_b16 a[146:147], v9 offset:20736
global_load_lds_dwordx4 v6, s[40:41] offset:2048
ds_read_b64_tr_b16 a[148:149], v9 offset:18440
ds_read_b64_tr_b16 a[150:151], v9 offset:20744
global_load_lds_dwordx4 v7, s[40:41] offset:3072
ds_read_b64_tr_b16 a[152:153], v9 offset:18496
s_add_u32 m0, s48, 122880
ds_read_b64_tr_b16 a[154:155], v9 offset:20800
global_load_lds_dwordx4 v8, s[40:41] offset:-4096
ds_read_b64_tr_b16 a[156:157], v9 offset:18504
ds_read_b64_tr_b16 a[158:159], v9 offset:20808
s_add_u32 s40, s40, 73728
ds_read_b64_tr_b16 v[60:61], v10 offset:18432
s_addc_u32 s41, s41, 0
ds_read_b64_tr_b16 v[62:63], v10 offset:20736
s_waitcnt vmcnt(18) lgkmcnt(0)
s_barrier
s_add_u32 m0, s48, 4096
ds_read_b64_tr_b16 a[160:161], v9 offset:36864
ds_read_b64_tr_b16 a[162:163], v9 offset:39168
global_load_lds_dwordx4 v0, s[40:41] offset:-4096
ds_read_b64_tr_b16 a[164:165], v9 offset:36872
ds_read_b64_tr_b16 a[166:167], v9 offset:39176
global_load_lds_dwordx4 v1, s[40:41] offset:-3072
ds_read_b64_tr_b16 a[168:169], v9 offset:36928
global_load_lds_dwordx4 v2, s[40:41] offset:-2048
ds_read_b64_tr_b16 a[170:171], v9 offset:39232
ds_read_b64_tr_b16 a[172:173], v9 offset:36936
global_load_lds_dwordx4 v3, s[40:41] offset:-1024
ds_read_b64_tr_b16 a[174:175], v9 offset:39240
global_load_lds_dwordx4 v4, s[40:41] offset:0
ds_read_b64_tr_b16 v[64:65], v10 offset:36864
ds_read_b64_tr_b16 v[66:67], v10 offset:39168
global_load_lds_dwordx4 v5, s[40:41] offset:1024
ds_read_b64_tr_b16 a[176:177], v9 offset:55296
global_load_lds_dwordx4 v6, s[40:41] offset:2048
ds_read_b64_tr_b16 a[178:179], v9 offset:57600
ds_read_b64_tr_b16 a[180:181], v9 offset:55304
global_load_lds_dwordx4 v7, s[40:41] offset:3072
ds_read_b64_tr_b16 a[182:183], v9 offset:57608
s_add_u32 m0, s48, 12288
ds_read_b64_tr_b16 a[184:185], v9 offset:55360
ds_read_b64_tr_b16 a[186:187], v9 offset:57664
global_load_lds_dwordx4 v8, s[40:41] offset:-4096
ds_read_b64_tr_b16 a[188:189], v9 offset:55368
s_sub_u32 s40, s40, 589824
ds_read_b64_tr_b16 a[190:191], v9 offset:57672
ds_read_b64_tr_b16 v[68:69], v10 offset:55296
s_subb_u32 s41, s41, 0
ds_read_b64_tr_b16 v[70:71], v10 offset:57600
s_waitcnt vmcnt(18) lgkmcnt(0)
s_barrier
ds_read_b64_tr_b16 a[192:193], v204 offset:8192
ds_read_b64_tr_b16 a[194:195], v204 offset:10496
ds_read_b64_tr_b16 a[196:197], v204 offset:8200
ds_read_b64_tr_b16 a[198:199], v204 offset:10504
ds_read_b64_tr_b16 a[200:201], v204 offset:8256
ds_read_b64_tr_b16 a[202:203], v204 offset:10560
)"
R"(ds_read_b64_tr_b16 a[204:205], v204 offset:8264
ds_read_b64_tr_b16 a[206:207], v204 offset:10568
ds_read_b64_tr_b16 v[72:73], v206 offset:8192
ds_read_b64_tr_b16 v[74:75], v206 offset:10496
ds_read_b64_tr_b16 a[208:209], v204 offset:26624
ds_read_b64_tr_b16 a[210:211], v204 offset:28928
ds_read_b64_tr_b16 a[212:213], v204 offset:26632
ds_read_b64_tr_b16 a[214:215], v204 offset:28936
ds_read_b64_tr_b16 a[216:217], v204 offset:26688
ds_read_b64_tr_b16 a[218:219], v204 offset:28992
ds_read_b64_tr_b16 a[220:221], v204 offset:26696
ds_read_b64_tr_b16 a[222:223], v204 offset:29000
ds_read_b64_tr_b16 v[76:77], v206 offset:26624
ds_read_b64_tr_b16 v[78:79], v206 offset:28928
s_waitcnt vmcnt(9) lgkmcnt(0)
s_barrier
ds_read_b64_tr_b16 a[224:225], v204 offset:45056
ds_read_b64_tr_b16 a[226:227], v204 offset:47360
ds_read_b64_tr_b16 a[228:229], v204 offset:45064
ds_read_b64_tr_b16 a[230:231], v204 offset:47368
ds_read_b64_tr_b16 a[232:233], v204 offset:45120
ds_read_b64_tr_b16 a[234:235], v204 offset:47424
ds_read_b64_tr_b16 a[236:237], v204 offset:45128
ds_read_b64_tr_b16 a[238:239], v204 offset:47432
ds_read_b64_tr_b16 v[80:81], v206 offset:45056
ds_read_b64_tr_b16 v[82:83], v206 offset:47360
ds_read_b64_tr_b16 a[240:241], v204 offset:63488
ds_read_b64_tr_b16 a[242:243], v205 offset:256
ds_read_b64_tr_b16 a[244:245], v204 offset:63496
ds_read_b64_tr_b16 a[246:247], v205 offset:264
ds_read_b64_tr_b16 a[248:249], v204 offset:63552
ds_read_b64_tr_b16 a[250:251], v205 offset:320
ds_read_b64_tr_b16 a[252:253], v204 offset:63560
ds_read_b64_tr_b16 a[254:255], v205 offset:328
ds_read_b64_tr_b16 v[84:85], v206 offset:63488
ds_read_b64_tr_b16 v[86:87], v207 offset:256
s_waitcnt vmcnt(0) lgkmcnt(0)
s_barrier
ds_read_b64_tr_b16 v[28:29], v9 offset:0
ds_read_b64_tr_b16 v[30:31], v9 offset:2304
ds_read_b64_tr_b16 v[32:33], v9 offset:8
ds_read_b64_tr_b16 v[34:35], v9 offset:2312
ds_read_b64_tr_b16 v[36:37], v9 offset:64
ds_read_b64_tr_b16 v[38:39], v9 offset:2368
ds_read_b64_tr_b16 v[40:41], v9 offset:72
ds_read_b64_tr_b16 v[42:43], v9 offset:2376
ds_read_b64_tr_b16 v[88:89], v10 offset:0
ds_read_b64_tr_b16 v[90:91], v10 offset:2304
ds_read_b64_tr_b16 v[44:45], v9 offset:18432
ds_read_b64_tr_b16 v[46:47], v9 offset:20736
ds_read_b64_tr_b16 v[48:49], v9 offset:18440
ds_read_b64_tr_b16 v[50:51], v9 offset:20744
ds_read_b64_tr_b16 v[52:53], v9 offset:18496
ds_read_b64_tr_b16 v[54:55], v9 offset:20800
ds_read_b64_tr_b16 v[56:57], v9 offset:18504
ds_read_b64_tr_b16 v[58:59], v9 offset:20808
ds_read_b64_tr_b16 v[92:93], v10 offset:18432
ds_read_b64_tr_b16 v[94:95], v10 offset:20736
s_waitcnt lgkmcnt(0)
s_barrier
s_mov_b64 s[76:77], 0
s_add_u32 m0, s49, 18432
s_mov_b32 s70, 33
s_waitcnt vmcnt(0) lgkmcnt(0)
s_nop 7
.p2align 6
.Lwo_top12:
s_waitcnt lgkmcnt(2)
v_mfma_f32_16x16x32_bf16 v[96:99], a[144:147], v[132:135], v[96:99]
ds_read_b128 v[144:147], v25 offset:12288
v_mfma_f32_16x16x32_bf16 v[100:103], a[148:151], v[132:135], v[100:103]
v_mfma_f32_16x16x32_bf16 v[104:107], a[152:155], v[132:135], v[104:107]
s_waitcnt vmcnt(8)
s_barrier
v_mfma_f32_16x16x32_bf16 v[108:111], a[156:159], v[132:135], v[108:111]
v_cvt_pk_bf16_f32 v164, v112, v113
v_cvt_pk_bf16_f32 v165, v114, v115
v_mfma_f32_16x16x32_bf16 v[128:131], v[60:63], v[132:135], 0
s_waitcnt lgkmcnt(2)
v_cvt_pk_bf16_f32 v166, v116, v117
v_cvt_pk_bf16_f32 v167, v118, v119
v_mfma_f32_16x16x32_bf16 v[96:99], a[160:163], v[136:139], v[96:99]
ds_read_b128 v[132:135], v25 offset:13312
v_add_f32 v254, v112, v113
v_mfma_f32_16x16x32_bf16 v[100:103], a[164:167], v[136:139], v[100:103]
global_store_dwordx4 v13, v[164:167], s[38:39] offset:0 sc1
v_mfma_f32_16x16x32_bf16 v[104:107], a[168:171], v[136:139], v[104:107]
v_cvt_pk_bf16_f32 v168, v120, v121
v_cvt_pk_bf16_f32 v169, v122, v123
v_mfma_f32_16x16x32_bf16 v[108:111], a[172:175], v[136:139], v[108:111]
v_cvt_pk_bf16_f32 v170, v124, v125
v_cvt_pk_bf16_f32 v171, v126, v127
v_mfma_f32_16x16x32_bf16 v[128:131], v[64:67], v[136:139], v[128:131]
s_waitcnt lgkmcnt(2)
v_mul_f32 v255, v112, v112
v_fmac_f32 v255, v113, v113
v_mfma_f32_16x16x32_bf16 v[96:99], a[176:179], v[140:143], v[96:99]
s_cmp_eq_u32 s70, 32
ds_read_b128 v[136:139], v25 offset:14336
v_add_f32 v254, v254, v114
v_mfma_f32_16x16x32_bf16 v[100:103], a[180:183], v[140:143], v[100:103]
s_cselect_b64 s[42:43], s[44:45], s[42:43]
global_store_dwordx4 v13, v[168:171], s[38:39] offset:1024 sc1
v_mfma_f32_16x16x32_bf16 v[104:107], a[184:187], v[140:143], v[104:107]
s_cmp_eq_u32 s70, 16
v_add_u32 v13, 18688, v13
v_fmac_f32 v255, v114, v114
v_mfma_f32_16x16x32_bf16 v[108:111], a[188:191], v[140:143], v[108:111]
s_cselect_b64 s[42:43], s[46:47], s[42:43]
v_add_f32 v254, v254, v115
v_fmac_f32 v255, v115, v115
v_mfma_f32_16x16x32_bf16 v[128:131], v[68:71], v[140:143], v[128:131]
s_waitcnt lgkmcnt(2)
v_add_f32 v254, v254, v116
v_fmac_f32 v255, v116, v116
v_mfma_f32_16x16x32_bf16 v[96:99], a[192:195], v[144:147], v[96:99]
s_xor_b32 s49, s49, 0x10000
ds_read_b128 v[140:143], v25 offset:15360
v_add_f32 v254, v254, v117
v_mfma_f32_16x16x32_bf16 v[100:103], a[196:199], v[144:147], v[100:103]
s_mov_b32 m0, 49152
v_fmac_f32 v255, v117, v117
v_add_f32 v254, v254, v118
v_mfma_f32_16x16x32_bf16 v[104:107], a[200:203], v[144:147], v[104:107]
v_fmac_f32 v255, v118, v118
v_add_f32 v254, v254, v119
v_mfma_f32_16x16x32_bf16 v[108:111], a[204:207], v[144:147], v[108:111]
v_fmac_f32 v255, v119, v119
v_add_f32 v254, v254, v120
v_mfma_f32_16x16x32_bf16 v[128:131], v[72:75], v[144:147], v[128:131]
s_waitcnt lgkmcnt(2)
v_fmac_f32 v255, v120, v120
v_add_f32 v254, v254, v121
v_mfma_f32_16x16x32_bf16 v[96:99], a[208:211], v[132:135], v[96:99]
ds_read_b128 v[144:147], v25 offset:16384
global_load_lds_dwordx4 v16, s[42:43] offset:0 sc1
v_mfma_f32_16x16x32_bf16 v[100:103], a[212:215], v[132:135], v[100:103]
v_fmac_f32 v255, v121, v121
v_add_f32 v254, v254, v122
v_mfma_f32_16x16x32_bf16 v[104:107], a[216:219], v[132:135], v[104:107]
v_fmac_f32 v255, v122, v122
v_add_f32 v254, v254, v123
v_mfma_f32_16x16x32_bf16 v[108:111], a[220:223], v[132:135], v[108:111]
v_fmac_f32 v255, v123, v123
v_add_f32 v254, v254, v124
v_mfma_f32_16x16x32_bf16 v[128:131], v[76:79], v[132:135], v[128:131]
s_waitcnt vmcnt(9)
ds_read_b128 v[148:151], v19 offset:8192
ds_read_b128 v[152:155], v20 offset:8192
s_waitcnt lgkmcnt(4)
v_mfma_f32_16x16x32_bf16 v[96:99], a[224:227], v[136:139], v[96:99]
ds_read_b128 v[132:135], v25 offset:17408
v_fmac_f32 v255, v124, v124
v_mfma_f32_16x16x32_bf16 v[100:103], a[228:231], v[136:139], v[100:103]
v_add_f32 v254, v254, v125
v_fmac_f32 v255, v125, v125
v_mfma_f32_16x16x32_bf16 v[104:107], a[232:235], v[136:139], v[104:107]
global_load_lds_dwordx4 v17, s[42:43] offset:1024 sc1
v_add_f32 v254, v254, v126
v_mfma_f32_16x16x32_bf16 v[108:111], a[236:239], v[136:139], v[108:111]
v_fmac_f32 v255, v126, v126
v_add_f32 v254, v254, v127
v_mfma_f32_16x16x32_bf16 v[128:131], v[80:83], v[136:139], v[128:131]
s_waitcnt lgkmcnt(4)
v_fmac_f32 v255, v127, v127
v_mfma_f32_16x16x32_bf16 v[96:99], a[240:243], v[140:143], v[96:99]
ds_read_b128 v[136:139], v25 offset:0
v_mfma_f32_16x16x32_bf16 v[100:103], a[244:247], v[140:143], v[100:103]
ds_write_b64 v27, v[254:255] offset:45568
v_mfma_f32_16x16x32_bf16 v[104:107], a[248:251], v[140:143], v[104:107]
s_add_u32 m0, s49, 0
s_waitcnt lgkmcnt(4)
v_lshlrev_b32 v112, 16, v148
v_and_b32 v113, 0xffff0000, v148
v_mfma_f32_16x16x32_bf16 v[108:111], a[252:255], v[140:143], v[108:111]
v_lshlrev_b32 v114, 16, v149
v_and_b32 v115, 0xffff0000, v149
v_mfma_f32_16x16x32_bf16 v[128:131], v[84:87], v[140:143], v[128:131]
v_lshlrev_b32 v116, 16, v150
v_and_b32 v117, 0xffff0000, v150
v_mfma_f32_16x16x32_bf16 v[96:99], v[28:31], v[144:147], v[96:99]
s_sub_u32 s42, s42, 18432
ds_read_b128 v[140:143], v25 offset:1024
v_lshlrev_b32 v118, 16, v151
v_mfma_f32_16x16x32_bf16 v[100:103], v[32:35], v[144:147], v[100:103]
s_subb_u32 s43, s43, 0
v_and_b32 v119, 0xffff0000, v151
s_waitcnt lgkmcnt(4)
v_lshlrev_b32 v120, 16, v152
v_mfma_f32_16x16x32_bf16 v[104:107], v[36:39], v[144:147], v[104:107]
v_and_b32 v121, 0xffff0000, v152
v_lshlrev_b32 v122, 16, v153
v_mfma_f32_16x16x32_bf16 v[108:111], v[40:43], v[144:147], v[108:111]
v_and_b32 v123, 0xffff0000, v153
v_lshlrev_b32 v124, 16, v154
v_mfma_f32_16x16x32_bf16 v[128:131], v[88:91], v[144:147], v[128:131]
s_waitcnt lgkmcnt(3)
v_and_b32 v125, 0xffff0000, v154
v_lshlrev_b32 v126, 16, v155
v_mfma_f32_16x16x32_bf16 v[96:99], v[44:47], v[132:135], v[96:99]
ds_read_b128 v[144:147], v25 offset:2048
v_and_b32 v127, 0xffff0000, v155
v_mfma_f32_16x16x32_bf16 v[100:103], v[48:51], v[132:135], v[100:103]
v_mfma_f32_16x16x32_bf16 v[104:107], v[52:55], v[132:135], v[104:107]
v_mfma_f32_16x16x32_bf16 v[108:111], v[56:59], v[132:135], v[108:111]
v_mfma_f32_16x16x32_bf16 v[128:131], v[92:95], v[132:135], v[128:131]
s_waitcnt lgkmcnt(3)
v_mfma_f32_16x16x32_bf16 v[96:99], a[0:3], v[136:139], v[96:99]
ds_read_b128 v[132:135], v25 offset:3072
v_mfma_f32_16x16x32_bf16 v[100:103], a[4:7], v[136:139], v[100:103]
v_mfma_f32_16x16x32_bf16 v[104:107], a[8:11], v[136:139], v[104:107]
v_mfma_f32_16x16x32_bf16 v[108:111], a[12:15], v[136:139], v[108:111]
s_waitcnt lgkmcnt(2)
v_mfma_f32_16x16x32_bf16 v[96:99], a[16:19], v[140:143], v[96:99]
ds_read_b128 v[136:139], v25 offset:4096
v_mfma_f32_16x16x32_bf16 v[100:103], a[20:23], v[140:143], v[100:103]
v_sub_u32 v201, v201, v22
v_cmp_lt_u32 vcc, 0x7f, v201
v_mfma_f32_16x16x32_bf16 v[104:107], a[24:27], v[140:143], v[104:107]
v_mfma_f32_16x16x32_bf16 v[108:111], a[28:31], v[140:143], v[108:111]
s_waitcnt lgkmcnt(2)
v_mfma_f32_16x16x32_bf16 v[96:99], a[32:35], v[144:147], v[96:99]
s_and_b64 vcc, vcc, s[76:77]
s_cbranch_vccnz .Lwo_fr8
.Lwo_rt7:
ds_read_b128 v[140:143], v25 offset:5120
v_mfma_f32_16x16x32_bf16 v[100:103], a[36:39], v[144:147], v[100:103]
v_mfma_f32_16x16x32_bf16 v[104:107], a[40:43], v[144:147], v[104:107]
s_barrier
v_mfma_f32_16x16x32_bf16 v[108:111], a[44:47], v[144:147], v[108:111]
s_waitcnt lgkmcnt(2)
ds_write_b128 v26, v[128:131] offset:37888
v_mfma_f32_16x16x32_bf16 v[96:99], a[48:51], v[132:135], v[96:99]
ds_read_b128 v[144:147], v25 offset:6144
v_add_u32 v22, 1, v22
v_mfma_f32_16x16x32_bf16 v[100:103], a[52:55], v[132:135], v[100:103]
global_load_lds_dwordx4 v11, s[38:39] offset:-2048 sc1
v_mfma_f32_16x16x32_bf16 v[104:107], a[56:59], v[132:135], v[104:107]
v_mfma_f32_16x16x32_bf16 v[108:111], a[60:63], v[132:135], v[108:111]
s_waitcnt lgkmcnt(3)
v_mfma_f32_16x16x32_bf16 v[96:99], a[64:67], v[136:139], v[96:99]
ds_read_b128 v[132:135], v25 offset:7168
global_load_lds_dwordx4 v11, s[38:39] offset:-1024 sc1
v_mfma_f32_16x16x32_bf16 v[100:103], a[68:71], v[136:139], v[100:103]
v_mfma_f32_16x16x32_bf16 v[104:107], a[72:75], v[136:139], v[104:107]
v_mfma_f32_16x16x32_bf16 v[108:111], a[76:79], v[136:139], v[108:111]
s_waitcnt lgkmcnt(3)
global_load_lds_dwordx4 v11, s[38:39] offset:0 sc1
v_mfma_f32_16x16x32_bf16 v[96:99], a[80:83], v[140:143], v[96:99]
ds_read_b128 v[136:139], v25 offset:8192
v_mfma_f32_16x16x32_bf16 v[100:103], a[84:87], v[140:143], v[100:103]
v_mfma_f32_16x16x32_bf16 v[104:107], a[88:91], v[140:143], v[104:107]
global_load_lds_dwordx4 v11, s[38:39] offset:1024 sc1
v_mfma_f32_16x16x32_bf16 v[108:111], a[92:95], v[140:143], v[108:111]
s_waitcnt lgkmcnt(2)
global_load_dword v201, v21, s[38:39] sc1
v_mfma_f32_16x16x32_bf16 v[96:99], a[96:99], v[144:147], v[96:99]
v_mfma_f32_16x16x32_bf16 v[100:103], a[100:103], v[144:147], v[100:103]
v_mfma_f32_16x16x32_bf16 v[104:107], a[104:107], v[144:147], v[104:107]
s_waitcnt vmcnt(9)
s_barrier
v_mfma_f32_16x16x32_bf16 v[108:111], a[108:111], v[144:147], v[108:111]
s_waitcnt lgkmcnt(1)
ds_read_b128 v[140:143], v25 offset:27648
global_load_lds_dwordx4 v11, s[38:39] offset:2048 sc1
v_mfma_f32_16x16x32_bf16 v[96:99], a[112:115], v[132:135], v[96:99]
ds_read_b128 v[144:147], v25 offset:28672
v_add_u32 v11, 18432, v11
v_mfma_f32_16x16x32_bf16 v[100:103], a[116:119], v[132:135], v[100:103]
v_min_u32 v11, v11, v12
v_mfma_f32_16x16x32_bf16 v[104:107], a[120:123], v[132:135], v[104:107]
v_mfma_f32_16x16x32_bf16 v[108:111], a[124:127], v[132:135], v[108:111]
s_waitcnt lgkmcnt(2)
v_mfma_f32_16x16x32_bf16 v[96:99], a[128:131], v[136:139], v[96:99]
ds_read_b128 v[132:135], v25 offset:29696
v_mfma_f32_16x16x32_bf16 v[100:103], a[132:135], v[136:139], v[100:103]
v_mfma_f32_16x16x32_bf16 v[104:107], a[136:139], v[136:139], v[104:107]
v_mfma_f32_16x16x32_bf16 v[108:111], a[140:143], v[136:139], v[108:111]
s_waitcnt lgkmcnt(2)
v_mfma_f32_16x16x32_bf16 v[112:115], a[144:147], v[140:143], v[112:115]
ds_read_b128 v[136:139], v25 offset:30720
v_mfma_f32_16x16x32_bf16 v[116:119], a[148:151], v[140:143], v[116:119]
v_mfma_f32_16x16x32_bf16 v[120:123], a[152:155], v[140:143], v[120:123]
s_waitcnt vmcnt(8)
s_barrier
v_mfma_f32_16x16x32_bf16 v[124:127], a[156:159], v[140:143], v[124:127]
v_cvt_pk_bf16_f32 v164, v96, v97
v_cvt_pk_bf16_f32 v165, v98, v99
v_mfma_f32_16x16x32_bf16 v[128:131], v[60:63], v[140:143], 0
s_waitcnt lgkmcnt(2)
v_cvt_pk_bf16_f32 v166, v100, v101
v_cvt_pk_bf16_f32 v167, v102, v103
v_mfma_f32_16x16x32_bf16 v[112:115], a[160:163], v[144:147], v[112:115]
ds_read_b128 v[140:143], v25 offset:31744
v_add_f32 v254, v96, v97
v_mfma_f32_16x16x32_bf16 v[116:119], a[164:167], v[144:147], v[116:119]
global_store_dwordx4 v13, v[164:167], s[38:39] offset:0 sc1
v_mfma_f32_16x16x32_bf16 v[120:123], a[168:171], v[144:147], v[120:123]
v_cvt_pk_bf16_f32 v168, v104, v105
v_cvt_pk_bf16_f32 v169, v106, v107
v_mfma_f32_16x16x32_bf16 v[124:127], a[172:175], v[144:147], v[124:127]
v_cvt_pk_bf16_f32 v170, v108, v109
v_cvt_pk_bf16_f32 v171, v110, v111
v_mfma_f32_16x16x32_bf16 v[128:131], v[64:67], v[144:147], v[128:131]
s_waitcnt lgkmcnt(2)
v_mul_f32 v255, v96, v96
v_fmac_f32 v255, v97, v97
v_mfma_f32_16x16x32_bf16 v[112:115], a[176:179], v[132:135], v[112:115]
s_mov_b32 m0, 57344
ds_read_b128 v[144:147], v25 offset:32768
v_add_f32 v254, v254, v98
v_mfma_f32_16x16x32_bf16 v[116:119], a[180:183], v[132:135], v[116:119]
global_store_dwordx4 v13, v[168:171], s[38:39] offset:1024 sc1
v_mfma_f32_16x16x32_bf16 v[120:123], a[184:187], v[132:135], v[120:123]
v_add_u32 v13, 18688, v13
v_fmac_f32 v255, v98, v98
v_mfma_f32_16x16x32_bf16 v[124:127], a[188:191], v[132:135], v[124:127]
v_add_f32 v254, v254, v99
v_fmac_f32 v255, v99, v99
v_mfma_f32_16x16x32_bf16 v[128:131], v[68:71], v[132:135], v[128:131]
s_waitcnt lgkmcnt(2)
v_add_f32 v254, v254, v100
v_fmac_f32 v255, v100, v100
v_mfma_f32_16x16x32_bf16 v[112:115], a[192:195], v[136:139], v[112:115]
ds_read_b128 v[132:135], v25 offset:33792
v_add_f32 v254, v254, v101
v_mfma_f32_16x16x32_bf16 v[116:119], a[196:199], v[136:139], v[116:119]
v_fmac_f32 v255, v101, v101
v_add_f32 v254, v254, v102
v_mfma_f32_16x16x32_bf16 v[120:123], a[200:203], v[136:139], v[120:123]
v_fmac_f32 v255, v102, v102
v_add_f32 v254, v254, v103
v_mfma_f32_16x16x32_bf16 v[124:127], a[204:207], v[136:139], v[124:127]
v_fmac_f32 v255, v103, v103
v_add_f32 v254, v254, v104
v_mfma_f32_16x16x32_bf16 v[128:131], v[72:75], v[136:139], v[128:131]
s_waitcnt lgkmcnt(2)
v_fmac_f32 v255, v104, v104
v_add_f32 v254, v254, v105
v_mfma_f32_16x16x32_bf16 v[112:115], a[208:211], v[140:143], v[112:115]
ds_read_b128 v[136:139], v25 offset:34816
global_load_lds_dwordx4 v16, s[42:43] offset:0 sc1
v_mfma_f32_16x16x32_bf16 v[116:119], a[212:215], v[140:143], v[116:119]
v_fmac_f32 v255, v105, v105
v_add_f32 v254, v254, v106
v_mfma_f32_16x16x32_bf16 v[120:123], a[216:219], v[140:143], v[120:123]
v_fmac_f32 v255, v106, v106
v_add_f32 v254, v254, v107
v_mfma_f32_16x16x32_bf16 v[124:127], a[220:223], v[140:143], v[124:127]
v_fmac_f32 v255, v107, v107
v_add_f32 v254, v254, v108
v_mfma_f32_16x16x32_bf16 v[128:131], v[76:79], v[140:143], v[128:131]
s_waitcnt vmcnt(9)
ds_read_b128 v[148:151], v19 offset:0
ds_read_b128 v[152:155], v20 offset:0
s_waitcnt lgkmcnt(4)
v_mfma_f32_16x16x32_bf16 v[112:115], a[224:227], v[144:147], v[112:115]
ds_read_b128 v[140:143], v25 offset:35840
v_fmac_f32 v255, v108, v108
v_mfma_f32_16x16x32_bf16 v[116:119], a[228:231], v[144:147], v[116:119]
v_add_f32 v254, v254, v109
v_fmac_f32 v255, v109, v109
v_mfma_f32_16x16x32_bf16 v[120:123], a[232:235], v[144:147], v[120:123]
global_load_lds_dwordx4 v17, s[42:43] offset:1024 sc1
v_add_f32 v254, v254, v110
v_mfma_f32_16x16x32_bf16 v[124:127], a[236:239], v[144:147], v[124:127]
v_fmac_f32 v255, v110, v110
v_add_f32 v254, v254, v111
v_mfma_f32_16x16x32_bf16 v[128:131], v[80:83], v[144:147], v[128:131]
s_waitcnt lgkmcnt(4)
v_fmac_f32 v255, v111, v111
v_mfma_f32_16x16x32_bf16 v[112:115], a[240:243], v[132:135], v[112:115]
ds_read_b128 v[144:147], v25 offset:18432
v_mfma_f32_16x16x32_bf16 v[116:119], a[244:247], v[132:135], v[116:119]
ds_write_b64 v27, v[254:255] offset:43520
v_mfma_f32_16x16x32_bf16 v[120:123], a[248:251], v[132:135], v[120:123]
s_add_u32 m0, s49, 18432
s_waitcnt lgkmcnt(4)
v_lshlrev_b32 v96, 16, v148
v_and_b32 v97, 0xffff0000, v148
v_mfma_f32_16x16x32_bf16 v[124:127], a[252:255], v[132:135], v[124:127]
v_lshlrev_b32 v98, 16, v149
v_and_b32 v99, 0xffff0000, v149
v_mfma_f32_16x16x32_bf16 v[128:131], v[84:87], v[132:135], v[128:131]
v_lshlrev_b32 v100, 16, v150
v_and_b32 v101, 0xffff0000, v150
v_mfma_f32_16x16x32_bf16 v[112:115], v[28:31], v[136:139], v[112:115]
s_sub_u32 s42, s42, 18432
ds_read_b128 v[132:135], v25 offset:19456
v_lshlrev_b32 v102, 16, v151
v_mfma_f32_16x16x32_bf16 v[116:119], v[32:35], v[136:139], v[116:119]
s_subb_u32 s43, s43, 0
v_and_b32 v103, 0xffff0000, v151
s_waitcnt lgkmcnt(4)
v_lshlrev_b32 v104, 16, v152
v_mfma_f32_16x16x32_bf16 v[120:123], v[36:39], v[136:139], v[120:123]
v_and_b32 v105, 0xffff0000, v152
v_lshlrev_b32 v106, 16, v153
v_mfma_f32_16x16x32_bf16 v[124:127], v[40:43], v[136:139], v[124:127]
v_and_b32 v107, 0xffff0000, v153
)"
R"(v_lshlrev_b32 v108, 16, v154
v_mfma_f32_16x16x32_bf16 v[128:131], v[88:91], v[136:139], v[128:131]
s_waitcnt lgkmcnt(3)
v_and_b32 v109, 0xffff0000, v154
v_lshlrev_b32 v110, 16, v155
v_mfma_f32_16x16x32_bf16 v[112:115], v[44:47], v[140:143], v[112:115]
ds_read_b128 v[136:139], v25 offset:20480
v_and_b32 v111, 0xffff0000, v155
v_mfma_f32_16x16x32_bf16 v[116:119], v[48:51], v[140:143], v[116:119]
v_mfma_f32_16x16x32_bf16 v[120:123], v[52:55], v[140:143], v[120:123]
v_mfma_f32_16x16x32_bf16 v[124:127], v[56:59], v[140:143], v[124:127]
v_mfma_f32_16x16x32_bf16 v[128:131], v[92:95], v[140:143], v[128:131]
s_waitcnt lgkmcnt(3)
v_mfma_f32_16x16x32_bf16 v[112:115], a[0:3], v[144:147], v[112:115]
ds_read_b128 v[140:143], v25 offset:21504
v_mfma_f32_16x16x32_bf16 v[116:119], a[4:7], v[144:147], v[116:119]
v_mfma_f32_16x16x32_bf16 v[120:123], a[8:11], v[144:147], v[120:123]
v_mfma_f32_16x16x32_bf16 v[124:127], a[12:15], v[144:147], v[124:127]
s_waitcnt lgkmcnt(2)
v_mfma_f32_16x16x32_bf16 v[112:115], a[16:19], v[132:135], v[112:115]
ds_read_b128 v[144:147], v25 offset:22528
v_mfma_f32_16x16x32_bf16 v[116:119], a[20:23], v[132:135], v[116:119]
v_sub_u32 v200, v200, v22
v_cmp_lt_u32 vcc, 0x7f, v200
v_mfma_f32_16x16x32_bf16 v[120:123], a[24:27], v[132:135], v[120:123]
v_mfma_f32_16x16x32_bf16 v[124:127], a[28:31], v[132:135], v[124:127]
s_waitcnt lgkmcnt(2)
v_mfma_f32_16x16x32_bf16 v[112:115], a[32:35], v[136:139], v[112:115]
s_and_b64 vcc, vcc, s[76:77]
s_cbranch_vccnz .Lwo_fr11
.Lwo_rt10:
ds_read_b128 v[132:135], v25 offset:23552
v_mfma_f32_16x16x32_bf16 v[116:119], a[36:39], v[136:139], v[116:119]
v_mfma_f32_16x16x32_bf16 v[120:123], a[40:43], v[136:139], v[120:123]
s_barrier
v_mfma_f32_16x16x32_bf16 v[124:127], a[44:47], v[136:139], v[124:127]
s_waitcnt lgkmcnt(2)
ds_write_b128 v26, v[128:131] offset:40960
v_mfma_f32_16x16x32_bf16 v[112:115], a[48:51], v[140:143], v[112:115]
s_mov_b64 s[76:77], -1
ds_read_b128 v[136:139], v25 offset:24576
v_add_u32 v22, 1, v22
v_mfma_f32_16x16x32_bf16 v[116:119], a[52:55], v[140:143], v[116:119]
global_load_lds_dwordx4 v11, s[38:39] offset:-2048 sc1
v_mfma_f32_16x16x32_bf16 v[120:123], a[56:59], v[140:143], v[120:123]
v_mfma_f32_16x16x32_bf16 v[124:127], a[60:63], v[140:143], v[124:127]
s_waitcnt lgkmcnt(3)
v_mfma_f32_16x16x32_bf16 v[112:115], a[64:67], v[144:147], v[112:115]
ds_read_b128 v[140:143], v25 offset:25600
global_load_lds_dwordx4 v11, s[38:39] offset:-1024 sc1
v_mfma_f32_16x16x32_bf16 v[116:119], a[68:71], v[144:147], v[116:119]
v_mfma_f32_16x16x32_bf16 v[120:123], a[72:75], v[144:147], v[120:123]
v_mfma_f32_16x16x32_bf16 v[124:127], a[76:79], v[144:147], v[124:127]
s_waitcnt lgkmcnt(3)
global_load_lds_dwordx4 v11, s[38:39] offset:0 sc1
v_mfma_f32_16x16x32_bf16 v[112:115], a[80:83], v[132:135], v[112:115]
ds_read_b128 v[144:147], v25 offset:26624
v_mfma_f32_16x16x32_bf16 v[116:119], a[84:87], v[132:135], v[116:119]
v_mfma_f32_16x16x32_bf16 v[120:123], a[88:91], v[132:135], v[120:123]
global_load_lds_dwordx4 v11, s[38:39] offset:1024 sc1
v_mfma_f32_16x16x32_bf16 v[124:127], a[92:95], v[132:135], v[124:127]
s_waitcnt lgkmcnt(2)
v_xor_b32 v25, 0x10000, v25
global_load_dword v200, v21, s[38:39] sc1
v_mfma_f32_16x16x32_bf16 v[112:115], a[96:99], v[136:139], v[112:115]
v_mfma_f32_16x16x32_bf16 v[116:119], a[100:103], v[136:139], v[116:119]
v_mfma_f32_16x16x32_bf16 v[120:123], a[104:107], v[136:139], v[120:123]
s_waitcnt vmcnt(9)
s_barrier
v_mfma_f32_16x16x32_bf16 v[124:127], a[108:111], v[136:139], v[124:127]
s_waitcnt lgkmcnt(1)
ds_read_b128 v[132:135], v25 offset:9216
global_load_lds_dwordx4 v11, s[38:39] offset:2048 sc1
v_mfma_f32_16x16x32_bf16 v[112:115], a[112:115], v[140:143], v[112:115]
ds_read_b128 v[136:139], v25 offset:10240
v_add_u32 v11, 18432, v11
v_mfma_f32_16x16x32_bf16 v[116:119], a[116:119], v[140:143], v[116:119]
v_min_u32 v11, v11, v12
v_mfma_f32_16x16x32_bf16 v[120:123], a[120:123], v[140:143], v[120:123]
v_mfma_f32_16x16x32_bf16 v[124:127], a[124:127], v[140:143], v[124:127]
s_waitcnt lgkmcnt(2)
v_mfma_f32_16x16x32_bf16 v[112:115], a[128:131], v[144:147], v[112:115]
ds_read_b128 v[140:143], v25 offset:11264
v_mfma_f32_16x16x32_bf16 v[116:119], a[132:135], v[144:147], v[116:119]
v_mfma_f32_16x16x32_bf16 v[120:123], a[136:139], v[144:147], v[120:123]
v_mfma_f32_16x16x32_bf16 v[124:127], a[140:143], v[144:147], v[124:127]
s_sub_u32 s70, s70, 1
s_cbranch_scc0 .Lwo_top12
s_waitcnt vmcnt(8)
s_barrier
v_cvt_pk_bf16_f32 v164, v112, v113
v_cvt_pk_bf16_f32 v165, v114, v115
v_cvt_pk_bf16_f32 v166, v116, v117
v_cvt_pk_bf16_f32 v167, v118, v119
global_store_dwordx4 v13, v[164:167], s[38:39] offset:0 sc1
v_cvt_pk_bf16_f32 v168, v120, v121
v_cvt_pk_bf16_f32 v169, v122, v123
v_cvt_pk_bf16_f32 v170, v124, v125
v_cvt_pk_bf16_f32 v171, v126, v127
global_store_dwordx4 v13, v[168:171], s[38:39] offset:1024 sc1
v_add_f32 v254, v112, v113
v_mul_f32 v255, v112, v112
v_fmac_f32 v255, v113, v113
v_add_f32 v254, v254, v114
v_fmac_f32 v255, v114, v114
v_add_f32 v254, v254, v115
v_fmac_f32 v255, v115, v115
v_add_f32 v254, v254, v116
v_fmac_f32 v255, v116, v116
v_add_f32 v254, v254, v117
v_fmac_f32 v255, v117, v117
v_add_f32 v254, v254, v118
v_fmac_f32 v255, v118, v118
v_add_f32 v254, v254, v119
v_fmac_f32 v255, v119, v119
v_add_f32 v254, v254, v120
v_fmac_f32 v255, v120, v120
v_add_f32 v254, v254, v121
v_fmac_f32 v255, v121, v121
v_add_f32 v254, v254, v122
v_fmac_f32 v255, v122, v122
v_add_f32 v254, v254, v123
v_fmac_f32 v255, v123, v123
v_add_f32 v254, v254, v124
v_fmac_f32 v255, v124, v124
v_add_f32 v254, v254, v125
v_fmac_f32 v255, v125, v125
v_add_f32 v254, v254, v126
v_fmac_f32 v255, v126, v126
v_add_f32 v254, v254, v127
v_fmac_f32 v255, v127, v127
ds_write_b64 v27, v[254:255] offset:45568
s_waitcnt vmcnt(0) lgkmcnt(0)
s_barrier
s_barrier
s_endpgm
.Lwo_w3:
s_add_u32 m0, s48, 4096
s_nop 0
global_load_lds_dwordx4 v0, s[40:41] offset:-4096
global_load_lds_dwordx4 v1, s[40:41] offset:-3072
global_load_lds_dwordx4 v2, s[40:41] offset:-2048
global_load_lds_dwordx4 v3, s[40:41] offset:-1024
global_load_lds_dwordx4 v4, s[40:41] offset:0
global_load_lds_dwordx4 v5, s[40:41] offset:1024
global_load_lds_dwordx4 v6, s[40:41] offset:2048
global_load_lds_dwordx4 v7, s[40:41] offset:3072
s_add_u32 m0, s48, 12288
s_nop 0
global_load_lds_dwordx4 v8, s[40:41] offset:-4096
s_add_u32 s40, s40, 73728
s_addc_u32 s41, s41, 0
s_add_u32 m0, s48, 40960
s_nop 0
global_load_lds_dwordx4 v0, s[40:41] offset:-4096
global_load_lds_dwordx4 v1, s[40:41] offset:-3072
global_load_lds_dwordx4 v2, s[40:41] offset:-2048
global_load_lds_dwordx4 v3, s[40:41] offset:-1024
global_load_lds_dwordx4 v4, s[40:41] offset:0
global_load_lds_dwordx4 v5, s[40:41] offset:1024
global_load_lds_dwordx4 v6, s[40:41] offset:2048
global_load_lds_dwordx4 v7, s[40:41] offset:3072
s_add_u32 m0, s48, 49152
s_nop 0
global_load_lds_dwordx4 v8, s[40:41] offset:-4096
s_add_u32 s40, s40, 73728
s_addc_u32 s41, s41, 0
s_add_u32 m0, s48, 77824
s_nop 0
global_load_lds_dwordx4 v0, s[40:41] offset:-4096
global_load_lds_dwordx4 v1, s[40:41] offset:-3072
global_load_lds_dwordx4 v2, s[40:41] offset:-2048
global_load_lds_dwordx4 v3, s[40:41] offset:-1024
global_load_lds_dwordx4 v4, s[40:41] offset:0
global_load_lds_dwordx4 v5, s[40:41] offset:1024
global_load_lds_dwordx4 v6, s[40:41] offset:2048
global_load_lds_dwordx4 v7, s[40:41] offset:3072
s_add_u32 m0, s48, 86016
s_nop 0
global_load_lds_dwordx4 v8, s[40:41] offset:-4096
s_add_u32 s40, s40, 73728
s_addc_u32 s41, s41, 0
v_add_u32 v204, 0x10000, v9
v_add_u32 v205, 0x20000, v9
v_add_u32 v206, 0x10000, v10
v_add_u32 v207, 0x20000, v10
s_waitcnt lgkmcnt(0)
s_mul_i32 s37, s37, 0x9E3779B1
s_add_u32 s37, s37, 0x7F4A7C15
v_add_u32 v22, s37, v22
v_add_u32 v24, s37, v24
s_waitcnt vmcnt(18)
s_barrier
s_add_u32 m0, s48, 114688
ds_read_b64_tr_b16 a[0:1], v9 offset:0
ds_read_b64_tr_b16 a[2:3], v9 offset:2304
global_load_lds_dwordx4 v0, s[40:41] offset:-4096
ds_read_b64_tr_b16 a[4:5], v9 offset:8
ds_read_b64_tr_b16 a[6:7], v9 offset:2312
global_load_lds_dwordx4 v1, s[40:41] offset:-3072
ds_read_b64_tr_b16 a[8:9], v9 offset:64
global_load_lds_dwordx4 v2, s[40:41] offset:-2048
ds_read_b64_tr_b16 a[10:11], v9 offset:2368
ds_read_b64_tr_b16 a[12:13], v9 offset:72
global_load_lds_dwordx4 v3, s[40:41] offset:-1024
ds_read_b64_tr_b16 a[14:15], v9 offset:2376
global_load_lds_dwordx4 v4, s[40:41] offset:0
ds_read_b64_tr_b16 v[60:61], v10 offset:0
ds_read_b64_tr_b16 v[62:63], v10 offset:2304
global_load_lds_dwordx4 v5, s[40:41] offset:1024
ds_read_b64_tr_b16 a[16:17], v9 offset:18432
global_load_lds_dwordx4 v6, s[40:41] offset:2048
ds_read_b64_tr_b16 a[18:19], v9 offset:20736
ds_read_b64_tr_b16 a[20:21], v9 offset:18440
global_load_lds_dwordx4 v7, s[40:41] offset:3072
ds_read_b64_tr_b16 a[22:23], v9 offset:20744
s_add_u32 m0, s48, 122880
ds_read_b64_tr_b16 a[24:25], v9 offset:18496
ds_read_b64_tr_b16 a[26:27], v9 offset:20800
global_load_lds_dwordx4 v8, s[40:41] offset:-4096
ds_read_b64_tr_b16 a[28:29], v9 offset:18504
s_add_u32 s40, s40, 73728
ds_read_b64_tr_b16 a[30:31], v9 offset:20808
ds_read_b64_tr_b16 v[64:65], v10 offset:18432
s_addc_u32 s41, s41, 0
ds_read_b64_tr_b16 v[66:67], v10 offset:20736
s_waitcnt vmcnt(18) lgkmcnt(0)
s_barrier
s_add_u32 m0, s48, 4096
ds_read_b64_tr_b16 a[32:33], v9 offset:36864
ds_read_b64_tr_b16 a[34:35], v9 offset:39168
global_load_lds_dwordx4 v0, s[40:41] offset:-4096
ds_read_b64_tr_b16 a[36:37], v9 offset:36872
ds_read_b64_tr_b16 a[38:39], v9 offset:39176
global_load_lds_dwordx4 v1, s[40:41] offset:-3072
ds_read_b64_tr_b16 a[40:41], v9 offset:36928
global_load_lds_dwordx4 v2, s[40:41] offset:-2048
ds_read_b64_tr_b16 a[42:43], v9 offset:39232
ds_read_b64_tr_b16 a[44:45], v9 offset:36936
global_load_lds_dwordx4 v3, s[40:41] offset:-1024
ds_read_b64_tr_b16 a[46:47], v9 offset:39240
global_load_lds_dwordx4 v4, s[40:41] offset:0
ds_read_b64_tr_b16 v[68:69], v10 offset:36864
ds_read_b64_tr_b16 v[70:71], v10 offset:39168
global_load_lds_dwordx4 v5, s[40:41] offset:1024
ds_read_b64_tr_b16 a[48:49], v9 offset:55296
global_load_lds_dwordx4 v6, s[40:41] offset:2048
ds_read_b64_tr_b16 a[50:51], v9 offset:57600
ds_read_b64_tr_b16 a[52:53], v9 offset:55304
global_load_lds_dwordx4 v7, s[40:41] offset:3072
ds_read_b64_tr_b16 a[54:55], v9 offset:57608
s_add_u32 m0, s48, 12288
ds_read_b64_tr_b16 a[56:57], v9 offset:55360
ds_read_b64_tr_b16 a[58:59], v9 offset:57664
global_load_lds_dwordx4 v8, s[40:41] offset:-4096
ds_read_b64_tr_b16 a[60:61], v9 offset:55368
s_add_u32 s40, s40, 73728
ds_read_b64_tr_b16 a[62:63], v9 offset:57672
ds_read_b64_tr_b16 v[72:73], v10 offset:55296
s_addc_u32 s41, s41, 0
ds_read_b64_tr_b16 v[74:75], v10 offset:57600
s_waitcnt vmcnt(18) lgkmcnt(0)
s_barrier
s_add_u32 m0, s48, 40960
ds_read_b64_tr_b16 a[64:65], v204 offset:8192
ds_read_b64_tr_b16 a[66:67], v204 offset:10496
global_load_lds_dwordx4 v0, s[40:41] offset:-4096
ds_read_b64_tr_b16 a[68:69], v204 offset:8200
ds_read_b64_tr_b16 a[70:71], v204 offset:10504
global_load_lds_dwordx4 v1, s[40:41] offset:-3072
ds_read_b64_tr_b16 a[72:73], v204 offset:8256
global_load_lds_dwordx4 v2, s[40:41] offset:-2048
ds_read_b64_tr_b16 a[74:75], v204 offset:10560
ds_read_b64_tr_b16 a[76:77], v204 offset:8264
global_load_lds_dwordx4 v3, s[40:41] offset:-1024
ds_read_b64_tr_b16 a[78:79], v204 offset:10568
global_load_lds_dwordx4 v4, s[40:41] offset:0
ds_read_b64_tr_b16 v[76:77], v206 offset:8192
ds_read_b64_tr_b16 v[78:79], v206 offset:10496
global_load_lds_dwordx4 v5, s[40:41] offset:1024
ds_read_b64_tr_b16 a[80:81], v204 offset:26624
global_load_lds_dwordx4 v6, s[40:41] offset:2048
ds_read_b64_tr_b16 a[82:83], v204 offset:28928
ds_read_b64_tr_b16 a[84:85], v204 offset:26632
global_load_lds_dwordx4 v7, s[40:41] offset:3072
ds_read_b64_tr_b16 a[86:87], v204 offset:28936
s_add_u32 m0, s48, 49152
ds_read_b64_tr_b16 a[88:89], v204 offset:26688
ds_read_b64_tr_b16 a[90:91], v204 offset:28992
global_load_lds_dwordx4 v8, s[40:41] offset:-4096
ds_read_b64_tr_b16 a[92:93], v204 offset:26696
s_add_u32 s40, s40, 73728
ds_read_b64_tr_b16 a[94:95], v204 offset:29000
ds_read_b64_tr_b16 v[80:81], v206 offset:26624
s_addc_u32 s41, s41, 0
ds_read_b64_tr_b16 v[82:83], v206 offset:28928
s_waitcnt vmcnt(18) lgkmcnt(0)
s_barrier
s_add_u32 m0, s48, 77824
ds_read_b64_tr_b16 a[96:97], v204 offset:45056
ds_read_b64_tr_b16 a[98:99], v204 offset:47360
global_load_lds_dwordx4 v0, s[40:41] offset:-4096
ds_read_b64_tr_b16 a[100:101], v204 offset:45064
ds_read_b64_tr_b16 a[102:103], v204 offset:47368
global_load_lds_dwordx4 v1, s[40:41] offset:-3072
ds_read_b64_tr_b16 a[104:105], v204 offset:45120
global_load_lds_dwordx4 v2, s[40:41] offset:-2048
ds_read_b64_tr_b16 a[106:107], v204 offset:47424
ds_read_b64_tr_b16 a[108:109], v204 offset:45128
global_load_lds_dwordx4 v3, s[40:41] offset:-1024
ds_read_b64_tr_b16 a[110:111], v204 offset:47432
global_load_lds_dwordx4 v4, s[40:41] offset:0
ds_read_b64_tr_b16 v[84:85], v206 offset:45056
ds_read_b64_tr_b16 v[86:87], v206 offset:47360
global_load_lds_dwordx4 v5, s[40:41] offset:1024
ds_read_b64_tr_b16 a[112:113], v204 offset:63488
global_load_lds_dwordx4 v6, s[40:41] offset:2048
ds_read_b64_tr_b16 a[114:115], v205 offset:256
ds_read_b64_tr_b16 a[116:117], v204 offset:63496
global_load_lds_dwordx4 v7, s[40:41] offset:3072
ds_read_b64_tr_b16 a[118:119], v205 offset:264
s_add_u32 m0, s48, 86016
ds_read_b64_tr_b16 a[120:121], v204 offset:63552
ds_read_b64_tr_b16 a[122:123], v205 offset:320
global_load_lds_dwordx4 v8, s[40:41] offset:-4096
ds_read_b64_tr_b16 a[124:125], v204 offset:63560
s_add_u32 s40, s40, 73728
ds_read_b64_tr_b16 a[126:127], v205 offset:328
ds_read_b64_tr_b16 v[88:89], v206 offset:63488
s_addc_u32 s41, s41, 0
ds_read_b64_tr_b16 v[90:91], v207 offset:256
s_waitcnt vmcnt(18) lgkmcnt(0)
s_barrier
s_add_u32 m0, s48, 114688
ds_read_b64_tr_b16 a[128:129], v9 offset:0
ds_read_b64_tr_b16 a[130:131], v9 offset:2304
global_load_lds_dwordx4 v0, s[40:41] offset:-4096
ds_read_b64_tr_b16 a[132:133], v9 offset:8
global_load_lds_dwordx4 v1, s[40:41] offset:-3072
ds_read_b64_tr_b16 a[134:135], v9 offset:2312
ds_read_b64_tr_b16 a[136:137], v9 offset:64
global_load_lds_dwordx4 v2, s[40:41] offset:-2048
ds_read_b64_tr_b16 a[138:139], v9 offset:2368
global_load_lds_dwordx4 v3, s[40:41] offset:-1024
ds_read_b64_tr_b16 a[140:141], v9 offset:72
global_load_lds_dwordx4 v4, s[40:41] offset:0
ds_read_b64_tr_b16 a[142:143], v9 offset:2376
ds_read_b64_tr_b16 v[92:93], v10 offset:0
global_load_lds_dwordx4 v5, s[40:41] offset:1024
ds_read_b64_tr_b16 v[94:95], v10 offset:2304
global_load_lds_dwordx4 v6, s[40:41] offset:2048
ds_read_b64_tr_b16 a[144:145], v9 offset:18432
ds_read_b64_tr_b16 a[146:147], v9 offset:20736
global_load_lds_dwordx4 v7, s[40:41] offset:3072
ds_read_b64_tr_b16 a[148:149], v9 offset:18440
s_add_u32 m0, s48, 122880
ds_read_b64_tr_b16 a[150:151], v9 offset:20744
global_load_lds_dwordx4 v8, s[40:41] offset:-4096
ds_read_b64_tr_b16 a[152:153], v9 offset:18496
ds_read_b64_tr_b16 a[154:155], v9 offset:20800
s_add_u32 s40, s40, 73728
ds_read_b64_tr_b16 a[156:157], v9 offset:18504
s_addc_u32 s41, s41, 0
ds_read_b64_tr_b16 a[158:159], v9 offset:20808
s_waitcnt vmcnt(18) lgkmcnt(0)
s_barrier
s_add_u32 m0, s48, 4096
ds_read_b64_tr_b16 a[160:161], v9 offset:36864
ds_read_b64_tr_b16 a[162:163], v9 offset:39168
global_load_lds_dwordx4 v0, s[40:41] offset:-4096
ds_read_b64_tr_b16 a[164:165], v9 offset:36872
global_load_lds_dwordx4 v1, s[40:41] offset:-3072
ds_read_b64_tr_b16 a[166:167], v9 offset:39176
global_load_lds_dwordx4 v2, s[40:41] offset:-2048
ds_read_b64_tr_b16 a[168:169], v9 offset:36928
global_load_lds_dwordx4 v3, s[40:41] offset:-1024
ds_read_b64_tr_b16 a[170:171], v9 offset:39232
ds_read_b64_tr_b16 a[172:173], v9 offset:36936
global_load_lds_dwordx4 v4, s[40:41] offset:0
ds_read_b64_tr_b16 a[174:175], v9 offset:39240
global_load_lds_dwordx4 v5, s[40:41] offset:1024
ds_read_b64_tr_b16 a[176:177], v9 offset:55296
global_load_lds_dwordx4 v6, s[40:41] offset:2048
ds_read_b64_tr_b16 a[178:179], v9 offset:57600
global_load_lds_dwordx4 v7, s[40:41] offset:3072
ds_read_b64_tr_b16 a[180:181], v9 offset:55304
ds_read_b64_tr_b16 a[182:183], v9 offset:57608
s_add_u32 m0, s48, 12288
ds_read_b64_tr_b16 a[184:185], v9 offset:55360
global_load_lds_dwordx4 v8, s[40:41] offset:-4096
ds_read_b64_tr_b16 a[186:187], v9 offset:57664
s_sub_u32 s40, s40, 589824
ds_read_b64_tr_b16 a[188:189], v9 offset:55368
s_subb_u32 s41, s41, 0
ds_read_b64_tr_b16 a[190:191], v9 offset:57672
s_waitcnt vmcnt(18) lgkmcnt(0)
s_barrier
ds_read_b64_tr_b16 a[192:193], v204 offset:8192
ds_read_b64_tr_b16 a[194:195], v204 offset:10496
ds_read_b64_tr_b16 a[196:197], v204 offset:8200
)"
R"(ds_read_b64_tr_b16 a[198:199], v204 offset:10504
ds_read_b64_tr_b16 a[200:201], v204 offset:8256
ds_read_b64_tr_b16 a[202:203], v204 offset:10560
ds_read_b64_tr_b16 a[204:205], v204 offset:8264
ds_read_b64_tr_b16 a[206:207], v204 offset:10568
ds_read_b64_tr_b16 a[208:209], v204 offset:26624
ds_read_b64_tr_b16 a[210:211], v204 offset:28928
ds_read_b64_tr_b16 a[212:213], v204 offset:26632
ds_read_b64_tr_b16 a[214:215], v204 offset:28936
ds_read_b64_tr_b16 a[216:217], v204 offset:26688
ds_read_b64_tr_b16 a[218:219], v204 offset:28992
ds_read_b64_tr_b16 a[220:221], v204 offset:26696
ds_read_b64_tr_b16 a[222:223], v204 offset:29000
s_waitcnt vmcnt(9) lgkmcnt(0)
s_barrier
ds_read_b64_tr_b16 a[224:225], v204 offset:45056
ds_read_b64_tr_b16 a[226:227], v204 offset:47360
ds_read_b64_tr_b16 a[228:229], v204 offset:45064
ds_read_b64_tr_b16 a[230:231], v204 offset:47368
ds_read_b64_tr_b16 a[232:233], v204 offset:45120
ds_read_b64_tr_b16 a[234:235], v204 offset:47424
ds_read_b64_tr_b16 a[236:237], v204 offset:45128
ds_read_b64_tr_b16 a[238:239], v204 offset:47432
ds_read_b64_tr_b16 a[240:241], v204 offset:63488
ds_read_b64_tr_b16 a[242:243], v205 offset:256
ds_read_b64_tr_b16 a[244:245], v204 offset:63496
ds_read_b64_tr_b16 a[246:247], v205 offset:264
ds_read_b64_tr_b16 a[248:249], v204 offset:63552
ds_read_b64_tr_b16 a[250:251], v205 offset:320
ds_read_b64_tr_b16 a[252:253], v204 offset:63560
ds_read_b64_tr_b16 a[254:255], v205 offset:328
s_waitcnt vmcnt(0) lgkmcnt(0)
s_barrier
ds_read_b64_tr_b16 v[28:29], v9 offset:0
ds_read_b64_tr_b16 v[30:31], v9 offset:2304
ds_read_b64_tr_b16 v[32:33], v9 offset:8
ds_read_b64_tr_b16 v[34:35], v9 offset:2312
ds_read_b64_tr_b16 v[36:37], v9 offset:64
ds_read_b64_tr_b16 v[38:39], v9 offset:2368
ds_read_b64_tr_b16 v[40:41], v9 offset:72
ds_read_b64_tr_b16 v[42:43], v9 offset:2376
ds_read_b64_tr_b16 v[44:45], v9 offset:18432
ds_read_b64_tr_b16 v[46:47], v9 offset:20736
ds_read_b64_tr_b16 v[48:49], v9 offset:18440
ds_read_b64_tr_b16 v[50:51], v9 offset:20744
ds_read_b64_tr_b16 v[52:53], v9 offset:18496
ds_read_b64_tr_b16 v[54:55], v9 offset:20800
ds_read_b64_tr_b16 v[56:57], v9 offset:18504
ds_read_b64_tr_b16 v[58:59], v9 offset:20808
s_waitcnt lgkmcnt(0)
s_barrier
s_mov_b64 s[76:77], 0
s_add_u32 m0, s49, 18432
s_mov_b32 s70, 33
s_waitcnt vmcnt(0) lgkmcnt(0)
s_nop 7
.p2align 6
.Lwo_top13:
s_waitcnt lgkmcnt(2)
v_mfma_f32_16x16x32_bf16 v[96:99], a[0:3], v[132:135], v[96:99]
ds_read_b128 v[144:147], v25 offset:3072
v_mfma_f32_16x16x32_bf16 v[100:103], a[4:7], v[132:135], v[100:103]
v_mfma_f32_16x16x32_bf16 v[104:107], a[8:11], v[132:135], v[104:107]
s_waitcnt vmcnt(7)
s_barrier
v_mfma_f32_16x16x32_bf16 v[108:111], a[12:15], v[132:135], v[108:111]
v_cvt_pk_bf16_f32 v164, v112, v113
v_cvt_pk_bf16_f32 v165, v114, v115
v_mfma_f32_16x16x32_bf16 v[128:131], v[60:63], v[132:135], 0
s_waitcnt lgkmcnt(2)
v_cvt_pk_bf16_f32 v166, v116, v117
v_cvt_pk_bf16_f32 v167, v118, v119
s_mov_b64 exec, 65535
global_store_dwordx2 v15, v[176:177], s[38:39] sc1
s_mov_b64 exec, -1
v_mfma_f32_16x16x32_bf16 v[96:99], a[16:19], v[136:139], v[96:99]
ds_read_b128 v[132:135], v25 offset:4096
v_add_f32 v254, v112, v113
v_add_u32 v15, 18688, v15
v_mfma_f32_16x16x32_bf16 v[100:103], a[20:23], v[136:139], v[100:103]
v_mul_f32 v255, v112, v112
v_fmac_f32 v255, v113, v113
v_mfma_f32_16x16x32_bf16 v[104:107], a[24:27], v[136:139], v[104:107]
global_store_dwordx4 v13, v[164:167], s[38:39] offset:0 sc1
v_mfma_f32_16x16x32_bf16 v[108:111], a[28:31], v[136:139], v[108:111]
v_cvt_pk_bf16_f32 v168, v120, v121
v_cvt_pk_bf16_f32 v169, v122, v123
v_mfma_f32_16x16x32_bf16 v[128:131], v[64:67], v[136:139], v[128:131]
s_waitcnt lgkmcnt(2)
v_cvt_pk_bf16_f32 v170, v124, v125
v_cvt_pk_bf16_f32 v171, v126, v127
v_mfma_f32_16x16x32_bf16 v[96:99], a[32:35], v[140:143], v[96:99]
s_cmp_eq_u32 s70, 32
ds_read_b128 v[136:139], v25 offset:5120
v_add_f32 v254, v254, v114
v_mfma_f32_16x16x32_bf16 v[100:103], a[36:39], v[140:143], v[100:103]
s_cselect_b64 s[42:43], s[44:45], s[42:43]
global_store_dwordx4 v13, v[168:171], s[38:39] offset:1024 sc1
v_mfma_f32_16x16x32_bf16 v[104:107], a[40:43], v[140:143], v[104:107]
s_cmp_eq_u32 s70, 16
v_add_u32 v13, 18688, v13
v_fmac_f32 v255, v114, v114
v_mfma_f32_16x16x32_bf16 v[108:111], a[44:47], v[140:143], v[108:111]
s_cselect_b64 s[42:43], s[46:47], s[42:43]
v_add_f32 v254, v254, v115
v_fmac_f32 v255, v115, v115
v_mfma_f32_16x16x32_bf16 v[128:131], v[68:71], v[140:143], v[128:131]
s_waitcnt lgkmcnt(2)
v_add_f32 v254, v254, v116
v_fmac_f32 v255, v116, v116
v_mfma_f32_16x16x32_bf16 v[96:99], a[48:51], v[144:147], v[96:99]
s_xor_b32 s49, s49, 0x10000
ds_read_b128 v[140:143], v25 offset:6144
v_add_f32 v254, v254, v117
v_mfma_f32_16x16x32_bf16 v[100:103], a[52:55], v[144:147], v[100:103]
s_mov_b32 m0, 51200
v_fmac_f32 v255, v117, v117
v_add_f32 v254, v254, v118
v_mfma_f32_16x16x32_bf16 v[104:107], a[56:59], v[144:147], v[104:107]
v_fmac_f32 v255, v118, v118
v_add_f32 v254, v254, v119
v_mfma_f32_16x16x32_bf16 v[108:111], a[60:63], v[144:147], v[108:111]
v_fmac_f32 v255, v119, v119
v_add_f32 v254, v254, v120
v_mfma_f32_16x16x32_bf16 v[128:131], v[72:75], v[144:147], v[128:131]
s_waitcnt lgkmcnt(2)
v_fmac_f32 v255, v120, v120
v_add_f32 v254, v254, v121
v_mfma_f32_16x16x32_bf16 v[96:99], a[64:67], v[132:135], v[96:99]
ds_read_b128 v[144:147], v25 offset:7168
v_fmac_f32 v255, v121, v121
v_mfma_f32_16x16x32_bf16 v[100:103], a[68:71], v[132:135], v[100:103]
v_add_f32 v254, v254, v122
v_fmac_f32 v255, v122, v122
v_mfma_f32_16x16x32_bf16 v[104:107], a[72:75], v[132:135], v[104:107]
global_load_lds_dwordx4 v16, s[42:43] offset:0 sc1
v_add_f32 v254, v254, v123
v_mfma_f32_16x16x32_bf16 v[108:111], a[76:79], v[132:135], v[108:111]
v_fmac_f32 v255, v123, v123
v_add_f32 v254, v254, v124
v_mfma_f32_16x16x32_bf16 v[128:131], v[76:79], v[132:135], v[128:131]
s_waitcnt vmcnt(9)
ds_read_b128 v[148:151], v19 offset:8192
ds_read_b128 v[152:155], v20 offset:8192
s_waitcnt lgkmcnt(4)
v_mfma_f32_16x16x32_bf16 v[96:99], a[80:83], v[136:139], v[96:99]
ds_read_b128 v[132:135], v25 offset:8192
v_fmac_f32 v255, v124, v124
v_mfma_f32_16x16x32_bf16 v[100:103], a[84:87], v[136:139], v[100:103]
v_add_f32 v254, v254, v125
v_fmac_f32 v255, v125, v125
v_mfma_f32_16x16x32_bf16 v[104:107], a[88:91], v[136:139], v[104:107]
v_add_f32 v254, v254, v126
v_fmac_f32 v255, v126, v126
v_mfma_f32_16x16x32_bf16 v[108:111], a[92:95], v[136:139], v[108:111]
v_add_f32 v254, v254, v127
v_fmac_f32 v255, v127, v127
v_mfma_f32_16x16x32_bf16 v[128:131], v[80:83], v[136:139], v[128:131]
s_waitcnt lgkmcnt(4)
global_load_lds_dwordx4 v17, s[42:43] offset:1024 sc1
v_mfma_f32_16x16x32_bf16 v[96:99], a[96:99], v[140:143], v[96:99]
ds_read_b128 v[136:139], v25 offset:9216
v_mfma_f32_16x16x32_bf16 v[100:103], a[100:103], v[140:143], v[100:103]
ds_write_b64 v27, v[254:255] offset:46080
v_mfma_f32_16x16x32_bf16 v[104:107], a[104:107], v[140:143], v[104:107]
s_waitcnt lgkmcnt(4)
v_lshlrev_b32 v112, 16, v148
v_and_b32 v113, 0xffff0000, v148
v_mfma_f32_16x16x32_bf16 v[108:111], a[108:111], v[140:143], v[108:111]
v_lshlrev_b32 v114, 16, v149
v_and_b32 v115, 0xffff0000, v149
v_mfma_f32_16x16x32_bf16 v[128:131], v[84:87], v[140:143], v[128:131]
v_lshlrev_b32 v116, 16, v150
v_and_b32 v117, 0xffff0000, v150
v_mfma_f32_16x16x32_bf16 v[96:99], a[112:115], v[144:147], v[96:99]
s_add_u32 m0, s49, 0
ds_read_b128 v[140:143], v25 offset:10240
v_lshlrev_b32 v118, 16, v151
v_mfma_f32_16x16x32_bf16 v[100:103], a[116:119], v[144:147], v[100:103]
s_waitcnt lgkmcnt(4)
v_and_b32 v119, 0xffff0000, v151
v_lshlrev_b32 v120, 16, v152
v_mfma_f32_16x16x32_bf16 v[104:107], a[120:123], v[144:147], v[104:107]
s_sub_u32 s42, s42, 18432
v_and_b32 v121, 0xffff0000, v152
v_lshlrev_b32 v122, 16, v153
v_mfma_f32_16x16x32_bf16 v[108:111], a[124:127], v[144:147], v[108:111]
s_subb_u32 s43, s43, 0
v_and_b32 v123, 0xffff0000, v153
v_lshlrev_b32 v124, 16, v154
v_mfma_f32_16x16x32_bf16 v[128:131], v[88:91], v[144:147], v[128:131]
s_waitcnt lgkmcnt(3)
v_and_b32 v125, 0xffff0000, v154
v_lshlrev_b32 v126, 16, v155
v_mfma_f32_16x16x32_bf16 v[96:99], a[128:131], v[132:135], v[96:99]
ds_read_b128 v[144:147], v25 offset:11264
v_and_b32 v127, 0xffff0000, v155
v_mfma_f32_16x16x32_bf16 v[100:103], a[132:135], v[132:135], v[100:103]
v_mfma_f32_16x16x32_bf16 v[104:107], a[136:139], v[132:135], v[104:107]
v_mfma_f32_16x16x32_bf16 v[108:111], a[140:143], v[132:135], v[108:111]
v_mfma_f32_16x16x32_bf16 v[128:131], v[92:95], v[132:135], v[128:131]
s_waitcnt lgkmcnt(3)
ds_read_b64 v[176:177], v27 offset:43008
ds_read_b64 v[180:181], v27 offset:43520
v_mfma_f32_16x16x32_bf16 v[96:99], a[144:147], v[136:139], v[96:99]
ds_read_b128 v[132:135], v25 offset:12288
ds_read_b64 v[184:185], v27 offset:44032
v_mfma_f32_16x16x32_bf16 v[100:103], a[148:151], v[136:139], v[100:103]
ds_read_b64 v[188:189], v27 offset:44544
v_mfma_f32_16x16x32_bf16 v[104:107], a[152:155], v[136:139], v[104:107]
v_mfma_f32_16x16x32_bf16 v[108:111], a[156:159], v[136:139], v[108:111]
s_waitcnt lgkmcnt(6)
v_mfma_f32_16x16x32_bf16 v[96:99], a[160:163], v[140:143], v[96:99]
ds_read_b128 v[136:139], v25 offset:13312
v_mfma_f32_16x16x32_bf16 v[100:103], a[164:167], v[140:143], v[100:103]
v_mfma_f32_16x16x32_bf16 v[104:107], a[168:171], v[140:143], v[104:107]
v_mfma_f32_16x16x32_bf16 v[108:111], a[172:175], v[140:143], v[108:111]
s_waitcnt lgkmcnt(4)
v_add_f32 v176, v176, v180
v_add_f32 v177, v177, v181
v_mfma_f32_16x16x32_bf16 v[96:99], a[176:179], v[144:147], v[96:99]
s_waitcnt lgkmcnt(1)
ds_read_b128 v[140:143], v25 offset:14336
v_add_f32 v184, v184, v188
v_mfma_f32_16x16x32_bf16 v[100:103], a[180:183], v[144:147], v[100:103]
v_mfma_f32_16x16x32_bf16 v[104:107], a[184:187], v[144:147], v[104:107]
s_barrier
v_mfma_f32_16x16x32_bf16 v[108:111], a[188:191], v[144:147], v[108:111]
global_load_lds_dwordx4 v11, s[38:39] offset:-2048 sc1
v_add_f32 v185, v185, v189
v_mfma_f32_16x16x32_bf16 v[96:99], a[192:195], v[132:135], v[96:99]
ds_read_b128 v[144:147], v25 offset:15360
v_add_f32 v176, v176, v184
v_mfma_f32_16x16x32_bf16 v[100:103], a[196:199], v[132:135], v[100:103]
v_add_f32 v177, v177, v185
v_mov_b32 v202, v176
v_mfma_f32_16x16x32_bf16 v[104:107], a[200:203], v[132:135], v[104:107]
ds_write_b128 v26, v[128:131] offset:38912
v_mfma_f32_16x16x32_bf16 v[108:111], a[204:207], v[132:135], v[108:111]
s_waitcnt lgkmcnt(3)
global_load_lds_dwordx4 v11, s[38:39] offset:-1024 sc1
v_mov_b32 v203, v177
v_mfma_f32_16x16x32_bf16 v[96:99], a[208:211], v[136:139], v[96:99]
ds_read_b128 v[132:135], v25 offset:16384
v_mfma_f32_16x16x32_bf16 v[100:103], a[212:215], v[136:139], v[100:103]
v_permlane16_swap_b32 v176, v202
v_mfma_f32_16x16x32_bf16 v[104:107], a[216:219], v[136:139], v[104:107]
global_load_lds_dwordx4 v11, s[38:39] offset:0 sc1
v_mfma_f32_16x16x32_bf16 v[108:111], a[220:223], v[136:139], v[108:111]
s_waitcnt lgkmcnt(3)
v_permlane16_swap_b32 v177, v203
v_mfma_f32_16x16x32_bf16 v[96:99], a[224:227], v[140:143], v[96:99]
ds_read_b128 v[136:139], v25 offset:17408
v_add_f32 v176, v176, v202
v_mfma_f32_16x16x32_bf16 v[100:103], a[228:231], v[140:143], v[100:103]
global_load_lds_dwordx4 v11, s[38:39] offset:1024 sc1
v_add_f32 v177, v177, v203
v_mfma_f32_16x16x32_bf16 v[104:107], a[232:235], v[140:143], v[104:107]
v_mov_b32 v202, v176
v_mov_b32 v203, v177
v_mfma_f32_16x16x32_bf16 v[108:111], a[236:239], v[140:143], v[108:111]
s_waitcnt lgkmcnt(3)
v_permlane32_swap_b32 v176, v202
v_mfma_f32_16x16x32_bf16 v[96:99], a[240:243], v[144:147], v[96:99]
global_load_lds_dwordx4 v11, s[38:39] offset:2048 sc1
v_add_u32 v11, 18432, v11
v_mfma_f32_16x16x32_bf16 v[100:103], a[244:247], v[144:147], v[100:103]
v_mfma_f32_16x16x32_bf16 v[104:107], a[248:251], v[144:147], v[104:107]
s_waitcnt vmcnt(10) lgkmcnt(2)
s_barrier
v_mfma_f32_16x16x32_bf16 v[108:111], a[252:255], v[144:147], v[108:111]
s_waitcnt lgkmcnt(1)
ds_read_b128 v[140:143], v25 offset:18432
v_min_u32 v11, v11, v12
v_mfma_f32_16x16x32_bf16 v[96:99], v[28:31], v[132:135], v[96:99]
ds_read_b128 v[144:147], v25 offset:19456
v_mfma_f32_16x16x32_bf16 v[100:103], v[32:35], v[132:135], v[100:103]
v_permlane32_swap_b32 v177, v203
v_mfma_f32_16x16x32_bf16 v[104:107], v[36:39], v[132:135], v[104:107]
v_add_f32 v176, v176, v202
v_add_f32 v177, v177, v203
v_mfma_f32_16x16x32_bf16 v[108:111], v[40:43], v[132:135], v[108:111]
s_waitcnt lgkmcnt(2)
v_mfma_f32_16x16x32_bf16 v[96:99], v[44:47], v[136:139], v[96:99]
ds_read_b128 v[132:135], v25 offset:20480
v_mfma_f32_16x16x32_bf16 v[100:103], v[48:51], v[136:139], v[100:103]
v_mfma_f32_16x16x32_bf16 v[104:107], v[52:55], v[136:139], v[104:107]
v_mfma_f32_16x16x32_bf16 v[108:111], v[56:59], v[136:139], v[108:111]
s_waitcnt lgkmcnt(2)
v_mfma_f32_16x16x32_bf16 v[112:115], a[0:3], v[140:143], v[112:115]
ds_read_b128 v[136:139], v25 offset:21504
v_mfma_f32_16x16x32_bf16 v[116:119], a[4:7], v[140:143], v[116:119]
v_mfma_f32_16x16x32_bf16 v[120:123], a[8:11], v[140:143], v[120:123]
s_waitcnt vmcnt(7)
s_barrier
v_mfma_f32_16x16x32_bf16 v[124:127], a[12:15], v[140:143], v[124:127]
v_cvt_pk_bf16_f32 v164, v96, v97
v_cvt_pk_bf16_f32 v165, v98, v99
v_mfma_f32_16x16x32_bf16 v[128:131], v[60:63], v[140:143], 0
s_waitcnt lgkmcnt(2)
v_cvt_pk_bf16_f32 v166, v100, v101
v_cvt_pk_bf16_f32 v167, v102, v103
s_mov_b64 exec, 65535
global_store_dwordx2 v15, v[176:177], s[38:39] sc1
s_mov_b64 exec, -1
v_mfma_f32_16x16x32_bf16 v[112:115], a[16:19], v[144:147], v[112:115]
ds_read_b128 v[140:143], v25 offset:22528
v_add_u32 v15, 18688, v15
v_mfma_f32_16x16x32_bf16 v[116:119], a[20:23], v[144:147], v[116:119]
v_add_f32 v254, v96, v97
v_mul_f32 v255, v96, v96
v_mfma_f32_16x16x32_bf16 v[120:123], a[24:27], v[144:147], v[120:123]
global_store_dwordx4 v13, v[164:167], s[38:39] offset:0 sc1
v_mfma_f32_16x16x32_bf16 v[124:127], a[28:31], v[144:147], v[124:127]
v_cvt_pk_bf16_f32 v168, v104, v105
v_cvt_pk_bf16_f32 v169, v106, v107
v_mfma_f32_16x16x32_bf16 v[128:131], v[64:67], v[144:147], v[128:131]
s_waitcnt lgkmcnt(2)
v_cvt_pk_bf16_f32 v170, v108, v109
v_cvt_pk_bf16_f32 v171, v110, v111
v_mfma_f32_16x16x32_bf16 v[112:115], a[32:35], v[132:135], v[112:115]
s_mov_b32 m0, 59392
ds_read_b128 v[144:147], v25 offset:23552
v_fmac_f32 v255, v97, v97
v_mfma_f32_16x16x32_bf16 v[116:119], a[36:39], v[132:135], v[116:119]
global_store_dwordx4 v13, v[168:171], s[38:39] offset:1024 sc1
v_mfma_f32_16x16x32_bf16 v[120:123], a[40:43], v[132:135], v[120:123]
v_add_u32 v13, 18688, v13
v_add_f32 v254, v254, v98
v_mfma_f32_16x16x32_bf16 v[124:127], a[44:47], v[132:135], v[124:127]
v_fmac_f32 v255, v98, v98
v_add_f32 v254, v254, v99
v_mfma_f32_16x16x32_bf16 v[128:131], v[68:71], v[132:135], v[128:131]
s_waitcnt lgkmcnt(2)
v_fmac_f32 v255, v99, v99
v_add_f32 v254, v254, v100
v_mfma_f32_16x16x32_bf16 v[112:115], a[48:51], v[136:139], v[112:115]
ds_read_b128 v[132:135], v25 offset:24576
v_fmac_f32 v255, v100, v100
v_mfma_f32_16x16x32_bf16 v[116:119], a[52:55], v[136:139], v[116:119]
v_add_f32 v254, v254, v101
v_fmac_f32 v255, v101, v101
v_mfma_f32_16x16x32_bf16 v[120:123], a[56:59], v[136:139], v[120:123]
v_add_f32 v254, v254, v102
v_fmac_f32 v255, v102, v102
v_mfma_f32_16x16x32_bf16 v[124:127], a[60:63], v[136:139], v[124:127]
v_add_f32 v254, v254, v103
v_fmac_f32 v255, v103, v103
v_mfma_f32_16x16x32_bf16 v[128:131], v[72:75], v[136:139], v[128:131]
s_waitcnt lgkmcnt(2)
v_add_f32 v254, v254, v104
v_fmac_f32 v255, v104, v104
v_mfma_f32_16x16x32_bf16 v[112:115], a[64:67], v[140:143], v[112:115]
ds_read_b128 v[136:139], v25 offset:25600
v_add_f32 v254, v254, v105
v_mfma_f32_16x16x32_bf16 v[116:119], a[68:71], v[140:143], v[116:119]
v_fmac_f32 v255, v105, v105
v_add_f32 v254, v254, v106
v_mfma_f32_16x16x32_bf16 v[120:123], a[72:75], v[140:143], v[120:123]
global_load_lds_dwordx4 v16, s[42:43] offset:0 sc1
v_fmac_f32 v255, v106, v106
v_mfma_f32_16x16x32_bf16 v[124:127], a[76:79], v[140:143], v[124:127]
v_add_f32 v254, v254, v107
v_fmac_f32 v255, v107, v107
v_mfma_f32_16x16x32_bf16 v[128:131], v[76:79], v[140:143], v[128:131]
s_waitcnt vmcnt(9)
ds_read_b128 v[148:151], v19 offset:0
ds_read_b128 v[152:155], v20 offset:0
s_waitcnt lgkmcnt(4)
v_mfma_f32_16x16x32_bf16 v[112:115], a[80:83], v[144:147], v[112:115]
ds_read_b128 v[140:143], v25 offset:26624
v_add_f32 v254, v254, v108
v_mfma_f32_16x16x32_bf16 v[116:119], a[84:87], v[144:147], v[116:119]
v_fmac_f32 v255, v108, v108
v_add_f32 v254, v254, v109
v_mfma_f32_16x16x32_bf16 v[120:123], a[88:91], v[144:147], v[120:123]
v_fmac_f32 v255, v109, v109
v_add_f32 v254, v254, v110
v_mfma_f32_16x16x32_bf16 v[124:127], a[92:95], v[144:147], v[124:127]
v_fmac_f32 v255, v110, v110
v_add_f32 v254, v254, v111
v_mfma_f32_16x16x32_bf16 v[128:131], v[80:83], v[144:147], v[128:131]
s_waitcnt lgkmcnt(4)
global_load_lds_dwordx4 v17, s[42:43] offset:1024 sc1
v_fmac_f32 v255, v111, v111
v_mfma_f32_16x16x32_bf16 v[112:115], a[96:99], v[132:135], v[112:115]
ds_read_b128 v[144:147], v25 offset:27648
v_mfma_f32_16x16x32_bf16 v[116:119], a[100:103], v[132:135], v[116:119]
ds_write_b64 v27, v[254:255] offset:44032
v_mfma_f32_16x16x32_bf16 v[120:123], a[104:107], v[132:135], v[120:123]
s_waitcnt lgkmcnt(4)
v_lshlrev_b32 v96, 16, v148
v_and_b32 v97, 0xffff0000, v148
v_mfma_f32_16x16x32_bf16 v[124:127], a[108:111], v[132:135], v[124:127]
v_lshlrev_b32 v98, 16, v149
v_and_b32 v99, 0xffff0000, v149
v_mfma_f32_16x16x32_bf16 v[128:131], v[84:87], v[132:135], v[128:131]
v_lshlrev_b32 v100, 16, v150
v_and_b32 v101, 0xffff0000, v150
v_mfma_f32_16x16x32_bf16 v[112:115], a[112:115], v[136:139], v[112:115]
)"
R"(s_add_u32 m0, s49, 18432
ds_read_b128 v[132:135], v25 offset:28672
v_lshlrev_b32 v102, 16, v151
v_mfma_f32_16x16x32_bf16 v[116:119], a[116:119], v[136:139], v[116:119]
s_waitcnt lgkmcnt(4)
v_and_b32 v103, 0xffff0000, v151
v_lshlrev_b32 v104, 16, v152
v_mfma_f32_16x16x32_bf16 v[120:123], a[120:123], v[136:139], v[120:123]
s_sub_u32 s42, s42, 18432
v_and_b32 v105, 0xffff0000, v152
v_lshlrev_b32 v106, 16, v153
v_mfma_f32_16x16x32_bf16 v[124:127], a[124:127], v[136:139], v[124:127]
s_subb_u32 s43, s43, 0
v_and_b32 v107, 0xffff0000, v153
v_lshlrev_b32 v108, 16, v154
v_mfma_f32_16x16x32_bf16 v[128:131], v[88:91], v[136:139], v[128:131]
s_waitcnt lgkmcnt(3)
v_and_b32 v109, 0xffff0000, v154
v_lshlrev_b32 v110, 16, v155
v_mfma_f32_16x16x32_bf16 v[112:115], a[128:131], v[140:143], v[112:115]
ds_read_b128 v[136:139], v25 offset:29696
v_and_b32 v111, 0xffff0000, v155
v_mfma_f32_16x16x32_bf16 v[116:119], a[132:135], v[140:143], v[116:119]
v_mfma_f32_16x16x32_bf16 v[120:123], a[136:139], v[140:143], v[120:123]
v_mfma_f32_16x16x32_bf16 v[124:127], a[140:143], v[140:143], v[124:127]
v_mfma_f32_16x16x32_bf16 v[128:131], v[92:95], v[140:143], v[128:131]
s_waitcnt lgkmcnt(3)
ds_read_b64 v[176:177], v27 offset:45056
ds_read_b64 v[180:181], v27 offset:45568
v_mfma_f32_16x16x32_bf16 v[112:115], a[144:147], v[144:147], v[112:115]
ds_read_b128 v[140:143], v25 offset:30720
ds_read_b64 v[184:185], v27 offset:46080
v_mfma_f32_16x16x32_bf16 v[116:119], a[148:151], v[144:147], v[116:119]
ds_read_b64 v[188:189], v27 offset:46592
v_mfma_f32_16x16x32_bf16 v[120:123], a[152:155], v[144:147], v[120:123]
v_mfma_f32_16x16x32_bf16 v[124:127], a[156:159], v[144:147], v[124:127]
s_waitcnt lgkmcnt(6)
v_mfma_f32_16x16x32_bf16 v[112:115], a[160:163], v[132:135], v[112:115]
ds_read_b128 v[144:147], v25 offset:31744
v_mfma_f32_16x16x32_bf16 v[116:119], a[164:167], v[132:135], v[116:119]
v_mfma_f32_16x16x32_bf16 v[120:123], a[168:171], v[132:135], v[120:123]
v_mfma_f32_16x16x32_bf16 v[124:127], a[172:175], v[132:135], v[124:127]
s_waitcnt lgkmcnt(4)
v_add_f32 v176, v176, v180
v_add_f32 v177, v177, v181
v_mfma_f32_16x16x32_bf16 v[112:115], a[176:179], v[136:139], v[112:115]
s_waitcnt lgkmcnt(1)
ds_read_b128 v[132:135], v25 offset:32768
v_add_f32 v184, v184, v188
v_mfma_f32_16x16x32_bf16 v[116:119], a[180:183], v[136:139], v[116:119]
v_mfma_f32_16x16x32_bf16 v[120:123], a[184:187], v[136:139], v[120:123]
s_barrier
v_mfma_f32_16x16x32_bf16 v[124:127], a[188:191], v[136:139], v[124:127]
global_load_lds_dwordx4 v11, s[38:39] offset:-2048 sc1
v_add_f32 v185, v185, v189
v_mfma_f32_16x16x32_bf16 v[112:115], a[192:195], v[140:143], v[112:115]
s_mov_b64 s[76:77], -1
ds_read_b128 v[136:139], v25 offset:33792
v_add_f32 v176, v176, v184
v_mfma_f32_16x16x32_bf16 v[116:119], a[196:199], v[140:143], v[116:119]
v_add_f32 v177, v177, v185
v_mov_b32 v202, v176
v_mfma_f32_16x16x32_bf16 v[120:123], a[200:203], v[140:143], v[120:123]
ds_write_b128 v26, v[128:131] offset:41984
v_mfma_f32_16x16x32_bf16 v[124:127], a[204:207], v[140:143], v[124:127]
s_waitcnt lgkmcnt(3)
global_load_lds_dwordx4 v11, s[38:39] offset:-1024 sc1
v_mov_b32 v203, v177
v_mfma_f32_16x16x32_bf16 v[112:115], a[208:211], v[144:147], v[112:115]
ds_read_b128 v[140:143], v25 offset:34816
v_mfma_f32_16x16x32_bf16 v[116:119], a[212:215], v[144:147], v[116:119]
v_permlane16_swap_b32 v176, v202
v_mfma_f32_16x16x32_bf16 v[120:123], a[216:219], v[144:147], v[120:123]
global_load_lds_dwordx4 v11, s[38:39] offset:0 sc1
v_mfma_f32_16x16x32_bf16 v[124:127], a[220:223], v[144:147], v[124:127]
s_waitcnt lgkmcnt(3)
v_permlane16_swap_b32 v177, v203
v_mfma_f32_16x16x32_bf16 v[112:115], a[224:227], v[132:135], v[112:115]
ds_read_b128 v[144:147], v25 offset:35840
v_add_f32 v176, v176, v202
v_mfma_f32_16x16x32_bf16 v[116:119], a[228:231], v[132:135], v[116:119]
global_load_lds_dwordx4 v11, s[38:39] offset:1024 sc1
v_add_f32 v177, v177, v203
v_mfma_f32_16x16x32_bf16 v[120:123], a[232:235], v[132:135], v[120:123]
v_mov_b32 v202, v176
v_mov_b32 v203, v177
v_mfma_f32_16x16x32_bf16 v[124:127], a[236:239], v[132:135], v[124:127]
s_waitcnt lgkmcnt(3)
v_xor_b32 v25, 0x10000, v25
v_mfma_f32_16x16x32_bf16 v[112:115], a[240:243], v[136:139], v[112:115]
global_load_lds_dwordx4 v11, s[38:39] offset:2048 sc1
v_add_u32 v11, 18432, v11
v_mfma_f32_16x16x32_bf16 v[116:119], a[244:247], v[136:139], v[116:119]
v_mfma_f32_16x16x32_bf16 v[120:123], a[248:251], v[136:139], v[120:123]
s_waitcnt vmcnt(10) lgkmcnt(2)
s_barrier
v_mfma_f32_16x16x32_bf16 v[124:127], a[252:255], v[136:139], v[124:127]
s_waitcnt lgkmcnt(1)
ds_read_b128 v[132:135], v25 offset:0
v_min_u32 v11, v11, v12
v_mfma_f32_16x16x32_bf16 v[112:115], v[28:31], v[140:143], v[112:115]
ds_read_b128 v[136:139], v25 offset:1024
v_mfma_f32_16x16x32_bf16 v[116:119], v[32:35], v[140:143], v[116:119]
v_permlane32_swap_b32 v176, v202
v_mfma_f32_16x16x32_bf16 v[120:123], v[36:39], v[140:143], v[120:123]
v_permlane32_swap_b32 v177, v203
v_mfma_f32_16x16x32_bf16 v[124:127], v[40:43], v[140:143], v[124:127]
s_waitcnt lgkmcnt(2)
v_add_f32 v176, v176, v202
v_add_f32 v177, v177, v203
v_mfma_f32_16x16x32_bf16 v[112:115], v[44:47], v[144:147], v[112:115]
ds_read_b128 v[140:143], v25 offset:2048
v_mfma_f32_16x16x32_bf16 v[116:119], v[48:51], v[144:147], v[116:119]
v_mfma_f32_16x16x32_bf16 v[120:123], v[52:55], v[144:147], v[120:123]
v_mfma_f32_16x16x32_bf16 v[124:127], v[56:59], v[144:147], v[124:127]
s_sub_u32 s70, s70, 1
s_cbranch_scc0 .Lwo_top13
s_waitcnt vmcnt(7)
s_barrier
s_mov_b64 exec, 65535
global_store_dwordx2 v15, v[176:177], s[38:39] sc1
s_mov_b64 exec, -1
v_add_u32 v15, 18688, v15
v_cvt_pk_bf16_f32 v164, v112, v113
v_cvt_pk_bf16_f32 v165, v114, v115
v_cvt_pk_bf16_f32 v166, v116, v117
v_cvt_pk_bf16_f32 v167, v118, v119
global_store_dwordx4 v13, v[164:167], s[38:39] offset:0 sc1
v_cvt_pk_bf16_f32 v168, v120, v121
v_cvt_pk_bf16_f32 v169, v122, v123
v_cvt_pk_bf16_f32 v170, v124, v125
v_cvt_pk_bf16_f32 v171, v126, v127
global_store_dwordx4 v13, v[168:171], s[38:39] offset:1024 sc1
v_add_f32 v254, v112, v113
v_mul_f32 v255, v112, v112
v_fmac_f32 v255, v113, v113
v_add_f32 v254, v254, v114
v_fmac_f32 v255, v114, v114
v_add_f32 v254, v254, v115
v_fmac_f32 v255, v115, v115
v_add_f32 v254, v254, v116
v_fmac_f32 v255, v116, v116
v_add_f32 v254, v254, v117
v_fmac_f32 v255, v117, v117
v_add_f32 v254, v254, v118
v_fmac_f32 v255, v118, v118
v_add_f32 v254, v254, v119
v_fmac_f32 v255, v119, v119
v_add_f32 v254, v254, v120
v_fmac_f32 v255, v120, v120
v_add_f32 v254, v254, v121
v_fmac_f32 v255, v121, v121
v_add_f32 v254, v254, v122
v_fmac_f32 v255, v122, v122
v_add_f32 v254, v254, v123
v_fmac_f32 v255, v123, v123
v_add_f32 v254, v254, v124
v_fmac_f32 v255, v124, v124
v_add_f32 v254, v254, v125
v_fmac_f32 v255, v125, v125
v_add_f32 v254, v254, v126
v_fmac_f32 v255, v126, v126
v_add_f32 v254, v254, v127
v_fmac_f32 v255, v127, v127
ds_write_b64 v27, v[254:255] offset:46080
s_waitcnt vmcnt(0) lgkmcnt(0)
s_barrier
ds_read_b64 v[176:177], v27 offset:43008
ds_read_b64 v[180:181], v27 offset:43520
ds_read_b64 v[184:185], v27 offset:44032
ds_read_b64 v[188:189], v27 offset:44544
s_waitcnt lgkmcnt(2)
v_add_f32 v176, v176, v180
v_add_f32 v177, v177, v181
s_waitcnt lgkmcnt(0)
v_add_f32 v184, v184, v188
v_add_f32 v185, v185, v189
v_add_f32 v176, v176, v184
v_add_f32 v177, v177, v185
v_mov_b32 v202, v176
v_mov_b32 v203, v177
s_nop 0
v_permlane16_swap_b32 v176, v202
v_permlane16_swap_b32 v177, v203
v_add_f32 v176, v176, v202
v_add_f32 v177, v177, v203
v_mov_b32 v202, v176
v_mov_b32 v203, v177
s_nop 0
v_permlane32_swap_b32 v176, v202
v_permlane32_swap_b32 v177, v203
v_add_f32 v176, v176, v202
v_add_f32 v177, v177, v203
s_mov_b64 exec, 65535
global_store_dwordx2 v15, v[176:177], s[38:39] sc1
s_mov_b64 exec, -1
v_add_u32 v15, 18688, v15
ds_read_b64 v[176:177], v27 offset:45056
ds_read_b64 v[180:181], v27 offset:45568
ds_read_b64 v[184:185], v27 offset:46080
ds_read_b64 v[188:189], v27 offset:46592
s_waitcnt lgkmcnt(2)
v_add_f32 v176, v176, v180
v_add_f32 v177, v177, v181
s_waitcnt lgkmcnt(0)
v_add_f32 v184, v184, v188
v_add_f32 v185, v185, v189
v_add_f32 v176, v176, v184
v_add_f32 v177, v177, v185
v_mov_b32 v202, v176
v_mov_b32 v203, v177
s_nop 0
v_permlane16_swap_b32 v176, v202
v_permlane16_swap_b32 v177, v203
v_add_f32 v176, v176, v202
v_add_f32 v177, v177, v203
v_mov_b32 v202, v176
v_mov_b32 v203, v177
s_nop 0
v_permlane32_swap_b32 v176, v202
v_permlane32_swap_b32 v177, v203
v_add_f32 v176, v176, v202
v_add_f32 v177, v177, v203
s_mov_b64 exec, 65535
global_store_dwordx2 v15, v[176:177], s[38:39] sc1
s_mov_b64 exec, -1
v_add_u32 v15, 18688, v15
s_waitcnt vmcnt(0)
s_barrier
s_endpgm
.Lwo_w4:
s_add_u32 m0, s48, 4096
s_nop 0
global_load_lds_dwordx4 v0, s[40:41] offset:-4096
global_load_lds_dwordx4 v1, s[40:41] offset:-3072
global_load_lds_dwordx4 v2, s[40:41] offset:-2048
global_load_lds_dwordx4 v3, s[40:41] offset:-1024
global_load_lds_dwordx4 v4, s[40:41] offset:0
global_load_lds_dwordx4 v5, s[40:41] offset:1024
global_load_lds_dwordx4 v6, s[40:41] offset:2048
global_load_lds_dwordx4 v7, s[40:41] offset:3072
s_add_u32 m0, s48, 12288
s_nop 0
global_load_lds_dwordx4 v8, s[40:41] offset:-4096
s_add_u32 s40, s40, 73728
s_addc_u32 s41, s41, 0
s_add_u32 m0, s48, 40960
s_nop 0
global_load_lds_dwordx4 v0, s[40:41] offset:-4096
global_load_lds_dwordx4 v1, s[40:41] offset:-3072
global_load_lds_dwordx4 v2, s[40:41] offset:-2048
global_load_lds_dwordx4 v3, s[40:41] offset:-1024
global_load_lds_dwordx4 v4, s[40:41] offset:0
global_load_lds_dwordx4 v5, s[40:41] offset:1024
global_load_lds_dwordx4 v6, s[40:41] offset:2048
global_load_lds_dwordx4 v7, s[40:41] offset:3072
s_add_u32 m0, s48, 49152
s_nop 0
global_load_lds_dwordx4 v8, s[40:41] offset:-4096
s_add_u32 s40, s40, 73728
s_addc_u32 s41, s41, 0
s_add_u32 m0, s48, 77824
s_nop 0
global_load_lds_dwordx4 v0, s[40:41] offset:-4096
global_load_lds_dwordx4 v1, s[40:41] offset:-3072
global_load_lds_dwordx4 v2, s[40:41] offset:-2048
global_load_lds_dwordx4 v3, s[40:41] offset:-1024
global_load_lds_dwordx4 v4, s[40:41] offset:0
global_load_lds_dwordx4 v5, s[40:41] offset:1024
global_load_lds_dwordx4 v6, s[40:41] offset:2048
global_load_lds_dwordx4 v7, s[40:41] offset:3072
s_add_u32 m0, s48, 86016
s_nop 0
global_load_lds_dwordx4 v8, s[40:41] offset:-4096
s_add_u32 s40, s40, 73728
s_addc_u32 s41, s41, 0
v_add_u32 v204, 0x10000, v9
v_add_u32 v205, 0x20000, v9
v_add_u32 v206, 0x10000, v10
v_add_u32 v207, 0x20000, v10
s_waitcnt lgkmcnt(0)
s_mul_i32 s37, s37, 0x9E3779B1
s_add_u32 s37, s37, 0x7F4A7C15
v_add_u32 v22, s37, v22
v_add_u32 v24, s37, v24
s_waitcnt vmcnt(18)
s_barrier
s_add_u32 m0, s48, 114688
ds_read_b64_tr_b16 a[0:1], v9 offset:0
ds_read_b64_tr_b16 a[2:3], v9 offset:2304
global_load_lds_dwordx4 v0, s[40:41] offset:-4096
ds_read_b64_tr_b16 a[4:5], v9 offset:8
global_load_lds_dwordx4 v1, s[40:41] offset:-3072
ds_read_b64_tr_b16 a[6:7], v9 offset:2312
global_load_lds_dwordx4 v2, s[40:41] offset:-2048
ds_read_b64_tr_b16 a[8:9], v9 offset:64
global_load_lds_dwordx4 v3, s[40:41] offset:-1024
ds_read_b64_tr_b16 a[10:11], v9 offset:2368
ds_read_b64_tr_b16 a[12:13], v9 offset:72
global_load_lds_dwordx4 v4, s[40:41] offset:0
ds_read_b64_tr_b16 a[14:15], v9 offset:2376
global_load_lds_dwordx4 v5, s[40:41] offset:1024
ds_read_b64_tr_b16 a[16:17], v9 offset:18432
global_load_lds_dwordx4 v6, s[40:41] offset:2048
ds_read_b64_tr_b16 a[18:19], v9 offset:20736
global_load_lds_dwordx4 v7, s[40:41] offset:3072
ds_read_b64_tr_b16 a[20:21], v9 offset:18440
ds_read_b64_tr_b16 a[22:23], v9 offset:20744
s_add_u32 m0, s48, 122880
ds_read_b64_tr_b16 a[24:25], v9 offset:18496
global_load_lds_dwordx4 v8, s[40:41] offset:-4096
ds_read_b64_tr_b16 a[26:27], v9 offset:20800
s_add_u32 s40, s40, 73728
ds_read_b64_tr_b16 a[28:29], v9 offset:18504
s_addc_u32 s41, s41, 0
ds_read_b64_tr_b16 a[30:31], v9 offset:20808
s_waitcnt vmcnt(18) lgkmcnt(0)
s_barrier
s_add_u32 m0, s48, 4096
ds_read_b64_tr_b16 a[32:33], v9 offset:36864
ds_read_b64_tr_b16 a[34:35], v9 offset:39168
global_load_lds_dwordx4 v0, s[40:41] offset:-4096
ds_read_b64_tr_b16 a[36:37], v9 offset:36872
global_load_lds_dwordx4 v1, s[40:41] offset:-3072
ds_read_b64_tr_b16 a[38:39], v9 offset:39176
global_load_lds_dwordx4 v2, s[40:41] offset:-2048
ds_read_b64_tr_b16 a[40:41], v9 offset:36928
global_load_lds_dwordx4 v3, s[40:41] offset:-1024
ds_read_b64_tr_b16 a[42:43], v9 offset:39232
ds_read_b64_tr_b16 a[44:45], v9 offset:36936
global_load_lds_dwordx4 v4, s[40:41] offset:0
ds_read_b64_tr_b16 a[46:47], v9 offset:39240
global_load_lds_dwordx4 v5, s[40:41] offset:1024
ds_read_b64_tr_b16 a[48:49], v9 offset:55296
global_load_lds_dwordx4 v6, s[40:41] offset:2048
ds_read_b64_tr_b16 a[50:51], v9 offset:57600
global_load_lds_dwordx4 v7, s[40:41] offset:3072
ds_read_b64_tr_b16 a[52:53], v9 offset:55304
ds_read_b64_tr_b16 a[54:55], v9 offset:57608
s_add_u32 m0, s48, 12288
ds_read_b64_tr_b16 a[56:57], v9 offset:55360
global_load_lds_dwordx4 v8, s[40:41] offset:-4096
ds_read_b64_tr_b16 a[58:59], v9 offset:57664
s_add_u32 s40, s40, 73728
ds_read_b64_tr_b16 a[60:61], v9 offset:55368
s_addc_u32 s41, s41, 0
ds_read_b64_tr_b16 a[62:63], v9 offset:57672
s_waitcnt vmcnt(18) lgkmcnt(0)
s_barrier
s_add_u32 m0, s48, 40960
ds_read_b64_tr_b16 a[64:65], v204 offset:8192
ds_read_b64_tr_b16 a[66:67], v204 offset:10496
global_load_lds_dwordx4 v0, s[40:41] offset:-4096
ds_read_b64_tr_b16 a[68:69], v204 offset:8200
global_load_lds_dwordx4 v1, s[40:41] offset:-3072
ds_read_b64_tr_b16 a[70:71], v204 offset:10504
global_load_lds_dwordx4 v2, s[40:41] offset:-2048
ds_read_b64_tr_b16 a[72:73], v204 offset:8256
global_load_lds_dwordx4 v3, s[40:41] offset:-1024
ds_read_b64_tr_b16 a[74:75], v204 offset:10560
ds_read_b64_tr_b16 a[76:77], v204 offset:8264
global_load_lds_dwordx4 v4, s[40:41] offset:0
ds_read_b64_tr_b16 a[78:79], v204 offset:10568
global_load_lds_dwordx4 v5, s[40:41] offset:1024
ds_read_b64_tr_b16 a[80:81], v204 offset:26624
global_load_lds_dwordx4 v6, s[40:41] offset:2048
ds_read_b64_tr_b16 a[82:83], v204 offset:28928
global_load_lds_dwordx4 v7, s[40:41] offset:3072
ds_read_b64_tr_b16 a[84:85], v204 offset:26632
ds_read_b64_tr_b16 a[86:87], v204 offset:28936
s_add_u32 m0, s48, 49152
ds_read_b64_tr_b16 a[88:89], v204 offset:26688
global_load_lds_dwordx4 v8, s[40:41] offset:-4096
ds_read_b64_tr_b16 a[90:91], v204 offset:28992
s_add_u32 s40, s40, 73728
ds_read_b64_tr_b16 a[92:93], v204 offset:26696
s_addc_u32 s41, s41, 0
ds_read_b64_tr_b16 a[94:95], v204 offset:29000
s_waitcnt vmcnt(18) lgkmcnt(0)
s_barrier
s_add_u32 m0, s48, 77824
ds_read_b64_tr_b16 a[96:97], v204 offset:45056
ds_read_b64_tr_b16 a[98:99], v204 offset:47360
global_load_lds_dwordx4 v0, s[40:41] offset:-4096
ds_read_b64_tr_b16 a[100:101], v204 offset:45064
global_load_lds_dwordx4 v1, s[40:41] offset:-3072
ds_read_b64_tr_b16 a[102:103], v204 offset:47368
global_load_lds_dwordx4 v2, s[40:41] offset:-2048
ds_read_b64_tr_b16 a[104:105], v204 offset:45120
global_load_lds_dwordx4 v3, s[40:41] offset:-1024
ds_read_b64_tr_b16 a[106:107], v204 offset:47424
ds_read_b64_tr_b16 a[108:109], v204 offset:45128
global_load_lds_dwordx4 v4, s[40:41] offset:0
ds_read_b64_tr_b16 a[110:111], v204 offset:47432
global_load_lds_dwordx4 v5, s[40:41] offset:1024
ds_read_b64_tr_b16 a[112:113], v204 offset:63488
global_load_lds_dwordx4 v6, s[40:41] offset:2048
ds_read_b64_tr_b16 a[114:115], v205 offset:256
global_load_lds_dwordx4 v7, s[40:41] offset:3072
ds_read_b64_tr_b16 a[116:117], v204 offset:63496
)"
R"(ds_read_b64_tr_b16 a[118:119], v205 offset:264
s_add_u32 m0, s48, 86016
ds_read_b64_tr_b16 a[120:121], v204 offset:63552
global_load_lds_dwordx4 v8, s[40:41] offset:-4096
ds_read_b64_tr_b16 a[122:123], v205 offset:320
s_add_u32 s40, s40, 73728
ds_read_b64_tr_b16 a[124:125], v204 offset:63560
s_addc_u32 s41, s41, 0
ds_read_b64_tr_b16 a[126:127], v205 offset:328
s_waitcnt vmcnt(18) lgkmcnt(0)
s_barrier
s_add_u32 m0, s48, 114688
ds_read_b64_tr_b16 a[128:129], v9 offset:0
ds_read_b64_tr_b16 a[130:131], v9 offset:2304
global_load_lds_dwordx4 v0, s[40:41] offset:-4096
ds_read_b64_tr_b16 a[132:133], v9 offset:8
global_load_lds_dwordx4 v1, s[40:41] offset:-3072
ds_read_b64_tr_b16 a[134:135], v9 offset:2312
ds_read_b64_tr_b16 a[136:137], v9 offset:64
global_load_lds_dwordx4 v2, s[40:41] offset:-2048
ds_read_b64_tr_b16 a[138:139], v9 offset:2368
global_load_lds_dwordx4 v3, s[40:41] offset:-1024
ds_read_b64_tr_b16 a[140:141], v9 offset:72
global_load_lds_dwordx4 v4, s[40:41] offset:0
ds_read_b64_tr_b16 a[142:143], v9 offset:2376
ds_read_b64_tr_b16 a[144:145], v9 offset:18432
global_load_lds_dwordx4 v5, s[40:41] offset:1024
ds_read_b64_tr_b16 a[146:147], v9 offset:20736
global_load_lds_dwordx4 v6, s[40:41] offset:2048
ds_read_b64_tr_b16 a[148:149], v9 offset:18440
ds_read_b64_tr_b16 a[150:151], v9 offset:20744
global_load_lds_dwordx4 v7, s[40:41] offset:3072
ds_read_b64_tr_b16 a[152:153], v9 offset:18496
s_add_u32 m0, s48, 122880
ds_read_b64_tr_b16 a[154:155], v9 offset:20800
global_load_lds_dwordx4 v8, s[40:41] offset:-4096
ds_read_b64_tr_b16 a[156:157], v9 offset:18504
ds_read_b64_tr_b16 a[158:159], v9 offset:20808
s_add_u32 s40, s40, 73728
ds_read_b64_tr_b16 v[60:61], v10 offset:18432
s_addc_u32 s41, s41, 0
ds_read_b64_tr_b16 v[62:63], v10 offset:20736
s_waitcnt vmcnt(18) lgkmcnt(0)
s_barrier
s_add_u32 m0, s48, 4096
ds_read_b64_tr_b16 a[160:161], v9 offset:36864
ds_read_b64_tr_b16 a[162:163], v9 offset:39168
global_load_lds_dwordx4 v0, s[40:41] offset:-4096
ds_read_b64_tr_b16 a[164:165], v9 offset:36872
ds_read_b64_tr_b16 a[166:167], v9 offset:39176
global_load_lds_dwordx4 v1, s[40:41] offset:-3072
ds_read_b64_tr_b16 a[168:169], v9 offset:36928
global_load_lds_dwordx4 v2, s[40:41] offset:-2048
ds_read_b64_tr_b16 a[170:171], v9 offset:39232
ds_read_b64_tr_b16 a[172:173], v9 offset:36936
global_load_lds_dwordx4 v3, s[40:41] offset:-1024
ds_read_b64_tr_b16 a[174:175], v9 offset:39240
global_load_lds_dwordx4 v4, s[40:41] offset:0
ds_read_b64_tr_b16 v[64:65], v10 offset:36864
ds_read_b64_tr_b16 v[66:67], v10 offset:39168
global_load_lds_dwordx4 v5, s[40:41] offset:1024
ds_read_b64_tr_b16 a[176:177], v9 offset:55296
global_load_lds_dwordx4 v6, s[40:41] offset:2048
ds_read_b64_tr_b16 a[178:179], v9 offset:57600
ds_read_b64_tr_b16 a[180:181], v9 offset:55304
global_load_lds_dwordx4 v7, s[40:41] offset:3072
ds_read_b64_tr_b16 a[182:183], v9 offset:57608
s_add_u32 m0, s48, 12288
ds_read_b64_tr_b16 a[184:185], v9 offset:55360
ds_read_b64_tr_b16 a[186:187], v9 offset:57664
global_load_lds_dwordx4 v8, s[40:41] offset:-4096
ds_read_b64_tr_b16 a[188:189], v9 offset:55368
s_sub_u32 s40, s40, 589824
ds_read_b64_tr_b16 a[190:191], v9 offset:57672
ds_read_b64_tr_b16 v[68:69], v10 offset:55296
s_subb_u32 s41, s41, 0
ds_read_b64_tr_b16 v[70:71], v10 offset:57600
s_waitcnt vmcnt(18) lgkmcnt(0)
s_barrier
ds_read_b64_tr_b16 a[192:193], v204 offset:8192
ds_read_b64_tr_b16 a[194:195], v204 offset:10496
ds_read_b64_tr_b16 a[196:197], v204 offset:8200
ds_read_b64_tr_b16 a[198:199], v204 offset:10504
ds_read_b64_tr_b16 a[200:201], v204 offset:8256
ds_read_b64_tr_b16 a[202:203], v204 offset:10560
ds_read_b64_tr_b16 a[204:205], v204 offset:8264
ds_read_b64_tr_b16 a[206:207], v204 offset:10568
ds_read_b64_tr_b16 v[72:73], v206 offset:8192
ds_read_b64_tr_b16 v[74:75], v206 offset:10496
ds_read_b64_tr_b16 a[208:209], v204 offset:26624
ds_read_b64_tr_b16 a[210:211], v204 offset:28928
ds_read_b64_tr_b16 a[212:213], v204 offset:26632
ds_read_b64_tr_b16 a[214:215], v204 offset:28936
ds_read_b64_tr_b16 a[216:217], v204 offset:26688
ds_read_b64_tr_b16 a[218:219], v204 offset:28992
ds_read_b64_tr_b16 a[220:221], v204 offset:26696
ds_read_b64_tr_b16 a[222:223], v204 offset:29000
ds_read_b64_tr_b16 v[76:77], v206 offset:26624
ds_read_b64_tr_b16 v[78:79], v206 offset:28928
s_waitcnt vmcnt(9) lgkmcnt(0)
s_barrier
ds_read_b64_tr_b16 a[224:225], v204 offset:45056
ds_read_b64_tr_b16 a[226:227], v204 offset:47360
ds_read_b64_tr_b16 a[228:229], v204 offset:45064
ds_read_b64_tr_b16 a[230:231], v204 offset:47368
ds_read_b64_tr_b16 a[232:233], v204 offset:45120
ds_read_b64_tr_b16 a[234:235], v204 offset:47424
ds_read_b64_tr_b16 a[236:237], v204 offset:45128
ds_read_b64_tr_b16 a[238:239], v204 offset:47432
ds_read_b64_tr_b16 v[80:81], v206 offset:45056
ds_read_b64_tr_b16 v[82:83], v206 offset:47360
ds_read_b64_tr_b16 a[240:241], v204 offset:63488
ds_read_b64_tr_b16 a[242:243], v205 offset:256
ds_read_b64_tr_b16 a[244:245], v204 offset:63496
ds_read_b64_tr_b16 a[246:247], v205 offset:264
ds_read_b64_tr_b16 a[248:249], v204 offset:63552
ds_read_b64_tr_b16 a[250:251], v205 offset:320
ds_read_b64_tr_b16 a[252:253], v204 offset:63560
ds_read_b64_tr_b16 a[254:255], v205 offset:328
ds_read_b64_tr_b16 v[84:85], v206 offset:63488
ds_read_b64_tr_b16 v[86:87], v207 offset:256
s_waitcnt vmcnt(0) lgkmcnt(0)
s_barrier
ds_read_b64_tr_b16 v[28:29], v9 offset:0
ds_read_b64_tr_b16 v[30:31], v9 offset:2304
ds_read_b64_tr_b16 v[32:33], v9 offset:8
ds_read_b64_tr_b16 v[34:35], v9 offset:2312
ds_read_b64_tr_b16 v[36:37], v9 offset:64
ds_read_b64_tr_b16 v[38:39], v9 offset:2368
ds_read_b64_tr_b16 v[40:41], v9 offset:72
ds_read_b64_tr_b16 v[42:43], v9 offset:2376
ds_read_b64_tr_b16 v[88:89], v10 offset:0
ds_read_b64_tr_b16 v[90:91], v10 offset:2304
ds_read_b64_tr_b16 v[44:45], v9 offset:18432
ds_read_b64_tr_b16 v[46:47], v9 offset:20736
ds_read_b64_tr_b16 v[48:49], v9 offset:18440
ds_read_b64_tr_b16 v[50:51], v9 offset:20744
ds_read_b64_tr_b16 v[52:53], v9 offset:18496
ds_read_b64_tr_b16 v[54:55], v9 offset:20800
ds_read_b64_tr_b16 v[56:57], v9 offset:18504
ds_read_b64_tr_b16 v[58:59], v9 offset:20808
ds_read_b64_tr_b16 v[92:93], v10 offset:18432
ds_read_b64_tr_b16 v[94:95], v10 offset:20736
s_waitcnt lgkmcnt(0)
s_barrier
s_mov_b64 s[76:77], 0
s_add_u32 m0, s49, 18432
s_mov_b32 s70, 33
s_waitcnt vmcnt(0) lgkmcnt(0)
s_nop 7
.p2align 6
.Lwo_top14:
s_waitcnt lgkmcnt(5)
v_mfma_f32_16x16x32_bf16 v[96:99], a[144:147], v[132:135], v[96:99]
s_waitcnt lgkmcnt(4)
ds_read_b128 v[144:147], v25 offset:12288
v_add_f32 v176, v176, v184
v_mfma_f32_16x16x32_bf16 v[100:103], a[148:151], v[132:135], v[100:103]
v_mfma_f32_16x16x32_bf16 v[104:107], a[152:155], v[132:135], v[104:107]
s_waitcnt vmcnt(6)
s_barrier
v_mfma_f32_16x16x32_bf16 v[108:111], a[156:159], v[132:135], v[108:111]
v_add_f32 v177, v177, v185
v_add_f32 v178, v178, v186
v_mfma_f32_16x16x32_bf16 v[128:131], v[60:63], v[132:135], 0
s_waitcnt lgkmcnt(3)
v_add_f32 v179, v179, v187
v_add_f32 v176, v176, v188
v_mfma_f32_16x16x32_bf16 v[96:99], a[160:163], v[136:139], v[96:99]
ds_read_b128 v[132:135], v25 offset:13312
v_add_f32 v177, v177, v189
v_mfma_f32_16x16x32_bf16 v[100:103], a[164:167], v[136:139], v[100:103]
v_add_f32 v178, v178, v190
v_add_f32 v179, v179, v191
v_mfma_f32_16x16x32_bf16 v[104:107], a[168:171], v[136:139], v[104:107]
s_waitcnt lgkmcnt(3)
v_add_f32 v180, v180, v192
v_add_f32 v181, v181, v193
v_mfma_f32_16x16x32_bf16 v[108:111], a[172:175], v[136:139], v[108:111]
v_add_f32 v182, v182, v194
v_add_f32 v183, v183, v195
v_mfma_f32_16x16x32_bf16 v[128:131], v[64:67], v[136:139], v[128:131]
s_waitcnt lgkmcnt(2)
v_cvt_pk_bf16_f32 v172, v176, v177
v_cvt_pk_bf16_f32 v173, v178, v179
v_mfma_f32_16x16x32_bf16 v[96:99], a[176:179], v[140:143], v[96:99]
s_cmp_eq_u32 s70, 32
ds_read_b128 v[136:139], v25 offset:14336
v_cvt_pk_bf16_f32 v174, v180, v181
v_mfma_f32_16x16x32_bf16 v[100:103], a[180:183], v[140:143], v[100:103]
s_cselect_b64 s[42:43], s[44:45], s[42:43]
v_cvt_pk_bf16_f32 v175, v182, v183
v_cvt_pk_bf16_f32 v164, v112, v113
v_mfma_f32_16x16x32_bf16 v[104:107], a[184:187], v[140:143], v[104:107]
s_cmp_eq_u32 s70, 16
global_store_dwordx4 v14, v[172:175], s[38:39] sc1
v_mfma_f32_16x16x32_bf16 v[108:111], a[188:191], v[140:143], v[108:111]
s_cselect_b64 s[42:43], s[46:47], s[42:43]
v_add_u32 v14, 18688, v14
v_cvt_pk_bf16_f32 v165, v114, v115
v_mfma_f32_16x16x32_bf16 v[128:131], v[68:71], v[140:143], v[128:131]
s_waitcnt lgkmcnt(2)
v_cvt_pk_bf16_f32 v166, v116, v117
v_cvt_pk_bf16_f32 v167, v118, v119
v_mfma_f32_16x16x32_bf16 v[96:99], a[192:195], v[144:147], v[96:99]
s_xor_b32 s49, s49, 0x10000
ds_read_b128 v[140:143], v25 offset:15360
v_add_f32 v254, v112, v113
v_mfma_f32_16x16x32_bf16 v[100:103], a[196:199], v[144:147], v[100:103]
s_mov_b32 m0, 53248
global_store_dwordx4 v13, v[164:167], s[38:39] offset:0 sc1
v_mfma_f32_16x16x32_bf16 v[104:107], a[200:203], v[144:147], v[104:107]
v_cvt_pk_bf16_f32 v168, v120, v121
v_cvt_pk_bf16_f32 v169, v122, v123
v_mfma_f32_16x16x32_bf16 v[108:111], a[204:207], v[144:147], v[108:111]
v_cvt_pk_bf16_f32 v170, v124, v125
v_cvt_pk_bf16_f32 v171, v126, v127
v_mfma_f32_16x16x32_bf16 v[128:131], v[72:75], v[144:147], v[128:131]
s_waitcnt lgkmcnt(2)
global_store_dwordx4 v13, v[168:171], s[38:39] offset:1024 sc1
v_mfma_f32_16x16x32_bf16 v[96:99], a[208:211], v[132:135], v[96:99]
ds_read_b128 v[144:147], v25 offset:16384
v_add_u32 v13, 18688, v13
v_mfma_f32_16x16x32_bf16 v[100:103], a[212:215], v[132:135], v[100:103]
v_mul_f32 v255, v112, v112
v_fmac_f32 v255, v113, v113
v_mfma_f32_16x16x32_bf16 v[104:107], a[216:219], v[132:135], v[104:107]
v_add_f32 v254, v254, v114
v_fmac_f32 v255, v114, v114
v_mfma_f32_16x16x32_bf16 v[108:111], a[220:223], v[132:135], v[108:111]
v_add_f32 v254, v254, v115
v_fmac_f32 v255, v115, v115
v_mfma_f32_16x16x32_bf16 v[128:131], v[76:79], v[132:135], v[128:131]
s_waitcnt vmcnt(7)
global_load_lds_dwordx4 v16, s[42:43] offset:0 sc1
ds_read_b128 v[148:151], v19 offset:8192
s_waitcnt lgkmcnt(3)
v_mfma_f32_16x16x32_bf16 v[96:99], a[224:227], v[136:139], v[96:99]
ds_read_b128 v[132:135], v25 offset:17408
ds_read_b128 v[152:155], v20 offset:8192
v_mfma_f32_16x16x32_bf16 v[100:103], a[228:231], v[136:139], v[100:103]
v_add_f32 v254, v254, v116
v_fmac_f32 v255, v116, v116
v_mfma_f32_16x16x32_bf16 v[104:107], a[232:235], v[136:139], v[104:107]
v_add_f32 v254, v254, v117
v_fmac_f32 v255, v117, v117
v_mfma_f32_16x16x32_bf16 v[108:111], a[236:239], v[136:139], v[108:111]
v_add_f32 v254, v254, v118
v_fmac_f32 v255, v118, v118
v_mfma_f32_16x16x32_bf16 v[128:131], v[80:83], v[136:139], v[128:131]
s_waitcnt lgkmcnt(4)
v_add_f32 v254, v254, v119
v_fmac_f32 v255, v119, v119
v_mfma_f32_16x16x32_bf16 v[96:99], a[240:243], v[140:143], v[96:99]
ds_read_b128 v[136:139], v25 offset:0
v_add_f32 v254, v254, v120
v_mfma_f32_16x16x32_bf16 v[100:103], a[244:247], v[140:143], v[100:103]
global_load_lds_dwordx4 v17, s[42:43] offset:1024 sc1
v_fmac_f32 v255, v120, v120
v_mfma_f32_16x16x32_bf16 v[104:107], a[248:251], v[140:143], v[104:107]
v_add_f32 v254, v254, v121
v_fmac_f32 v255, v121, v121
v_mfma_f32_16x16x32_bf16 v[108:111], a[252:255], v[140:143], v[108:111]
v_add_f32 v254, v254, v122
v_fmac_f32 v255, v122, v122
v_mfma_f32_16x16x32_bf16 v[128:131], v[84:87], v[140:143], v[128:131]
s_waitcnt lgkmcnt(4)
v_add_f32 v254, v254, v123
v_fmac_f32 v255, v123, v123
v_mfma_f32_16x16x32_bf16 v[96:99], v[28:31], v[144:147], v[96:99]
ds_read_b128 v[140:143], v25 offset:1024
v_add_f32 v254, v254, v124
v_mfma_f32_16x16x32_bf16 v[100:103], v[32:35], v[144:147], v[100:103]
s_add_u32 m0, s49, 0
v_fmac_f32 v255, v124, v124
v_add_f32 v254, v254, v125
v_mfma_f32_16x16x32_bf16 v[104:107], v[36:39], v[144:147], v[104:107]
v_fmac_f32 v255, v125, v125
v_add_f32 v254, v254, v126
v_mfma_f32_16x16x32_bf16 v[108:111], v[40:43], v[144:147], v[108:111]
v_fmac_f32 v255, v126, v126
v_add_f32 v254, v254, v127
v_mfma_f32_16x16x32_bf16 v[128:131], v[88:91], v[144:147], v[128:131]
s_waitcnt lgkmcnt(3)
v_fmac_f32 v255, v127, v127
v_add_f32 v254, v254, v176
v_mfma_f32_16x16x32_bf16 v[96:99], v[44:47], v[132:135], v[96:99]
ds_read_b128 v[144:147], v25 offset:2048
v_fmac_f32 v255, v176, v176
v_mfma_f32_16x16x32_bf16 v[100:103], v[48:51], v[132:135], v[100:103]
v_add_f32 v254, v254, v177
v_fmac_f32 v255, v177, v177
v_mfma_f32_16x16x32_bf16 v[104:107], v[52:55], v[132:135], v[104:107]
v_add_f32 v254, v254, v178
v_fmac_f32 v255, v178, v178
v_mfma_f32_16x16x32_bf16 v[108:111], v[56:59], v[132:135], v[108:111]
v_add_f32 v254, v254, v179
v_fmac_f32 v255, v179, v179
v_mfma_f32_16x16x32_bf16 v[128:131], v[92:95], v[132:135], v[128:131]
s_waitcnt lgkmcnt(2)
v_add_f32 v254, v254, v180
v_fmac_f32 v255, v180, v180
v_mfma_f32_16x16x32_bf16 v[96:99], a[0:3], v[136:139], v[96:99]
ds_read_b128 v[132:135], v25 offset:3072
v_add_f32 v254, v254, v181
v_mfma_f32_16x16x32_bf16 v[100:103], a[4:7], v[136:139], v[100:103]
v_fmac_f32 v255, v181, v181
v_add_f32 v254, v254, v182
v_mfma_f32_16x16x32_bf16 v[104:107], a[8:11], v[136:139], v[104:107]
v_fmac_f32 v255, v182, v182
v_add_f32 v254, v254, v183
v_mfma_f32_16x16x32_bf16 v[108:111], a[12:15], v[136:139], v[108:111]
s_waitcnt lgkmcnt(2)
v_fmac_f32 v255, v183, v183
v_mfma_f32_16x16x32_bf16 v[96:99], a[16:19], v[140:143], v[96:99]
ds_read_b128 v[136:139], v25 offset:4096
v_mfma_f32_16x16x32_bf16 v[100:103], a[20:23], v[140:143], v[100:103]
ds_write_b64 v27, v[254:255] offset:46592
v_mfma_f32_16x16x32_bf16 v[104:107], a[24:27], v[140:143], v[104:107]
v_lshlrev_b32 v176, 16, v156
v_and_b32 v177, 0xffff0000, v156
v_mfma_f32_16x16x32_bf16 v[108:111], a[28:31], v[140:143], v[108:111]
s_waitcnt lgkmcnt(3)
v_lshlrev_b32 v178, 16, v157
v_and_b32 v179, 0xffff0000, v157
v_mfma_f32_16x16x32_bf16 v[96:99], a[32:35], v[144:147], v[96:99]
ds_read_b128 v[140:143], v25 offset:5120
v_lshlrev_b32 v180, 16, v158
v_mfma_f32_16x16x32_bf16 v[100:103], a[36:39], v[144:147], v[100:103]
v_mfma_f32_16x16x32_bf16 v[104:107], a[40:43], v[144:147], v[104:107]
s_barrier
v_mfma_f32_16x16x32_bf16 v[108:111], a[44:47], v[144:147], v[108:111]
s_waitcnt lgkmcnt(3)
v_and_b32 v181, 0xffff0000, v158
v_lshlrev_b32 v182, 16, v159
v_mfma_f32_16x16x32_bf16 v[96:99], a[48:51], v[132:135], v[96:99]
ds_read_b128 v[144:147], v25 offset:6144
global_load_lds_dwordx4 v11, s[38:39] offset:-2048 sc1
v_mfma_f32_16x16x32_bf16 v[100:103], a[52:55], v[132:135], v[100:103]
v_and_b32 v183, 0xffff0000, v159
v_add_f32 v180, v180, v128
v_mfma_f32_16x16x32_bf16 v[104:107], a[56:59], v[132:135], v[104:107]
v_add_f32 v181, v181, v129
v_add_f32 v182, v182, v130
v_mfma_f32_16x16x32_bf16 v[108:111], a[60:63], v[132:135], v[108:111]
s_waitcnt lgkmcnt(3)
global_load_lds_dwordx4 v11, s[38:39] offset:-1024 sc1
v_add_f32 v183, v183, v131
v_mfma_f32_16x16x32_bf16 v[96:99], a[64:67], v[136:139], v[96:99]
ds_read_b128 v[132:135], v25 offset:7168
global_load_dwordx4 v[156:159], v18, s[42:43] sc1
v_mfma_f32_16x16x32_bf16 v[100:103], a[68:71], v[136:139], v[100:103]
s_sub_u32 s42, s42, 18432
v_lshlrev_b32 v112, 16, v148
v_and_b32 v113, 0xffff0000, v148
v_mfma_f32_16x16x32_bf16 v[104:107], a[72:75], v[136:139], v[104:107]
s_subb_u32 s43, s43, 0
global_load_lds_dwordx4 v11, s[38:39] offset:0 sc1
v_add_u32 v11, 18432, v11
v_mfma_f32_16x16x32_bf16 v[108:111], a[76:79], v[136:139], v[108:111]
s_waitcnt lgkmcnt(2)
v_min_u32 v11, v11, v12
v_lshlrev_b32 v114, 16, v149
v_mfma_f32_16x16x32_bf16 v[96:99], a[80:83], v[140:143], v[96:99]
ds_read_b128 v[136:139], v25 offset:8192
v_and_b32 v115, 0xffff0000, v149
v_mfma_f32_16x16x32_bf16 v[100:103], a[84:87], v[140:143], v[100:103]
v_lshlrev_b32 v116, 16, v150
v_and_b32 v117, 0xffff0000, v150
v_mfma_f32_16x16x32_bf16 v[104:107], a[88:91], v[140:143], v[104:107]
v_lshlrev_b32 v118, 16, v151
v_and_b32 v119, 0xffff0000, v151
v_mfma_f32_16x16x32_bf16 v[108:111], a[92:95], v[140:143], v[108:111]
s_waitcnt lgkmcnt(2)
v_lshlrev_b32 v120, 16, v152
v_and_b32 v121, 0xffff0000, v152
v_mfma_f32_16x16x32_bf16 v[96:99], a[96:99], v[144:147], v[96:99]
v_lshlrev_b32 v122, 16, v153
v_and_b32 v123, 0xffff0000, v153
v_mfma_f32_16x16x32_bf16 v[100:103], a[100:103], v[144:147], v[100:103]
v_mfma_f32_16x16x32_bf16 v[104:107], a[104:107], v[144:147], v[104:107]
s_waitcnt vmcnt(9)
s_barrier
v_mfma_f32_16x16x32_bf16 v[108:111], a[108:111], v[144:147], v[108:111]
s_waitcnt lgkmcnt(1)
ds_read_b128 v[140:143], v25 offset:27648
ds_read_b128 v[184:187], v26 offset:36864
v_mfma_f32_16x16x32_bf16 v[96:99], a[112:115], v[132:135], v[96:99]
ds_read_b128 v[144:147], v25 offset:28672
ds_read_b128 v[188:191], v26 offset:37888
v_mfma_f32_16x16x32_bf16 v[100:103], a[116:119], v[132:135], v[100:103]
ds_read_b128 v[192:195], v26 offset:38912
v_lshlrev_b32 v124, 16, v154
v_mfma_f32_16x16x32_bf16 v[104:107], a[120:123], v[132:135], v[104:107]
v_and_b32 v125, 0xffff0000, v154
v_lshlrev_b32 v126, 16, v155
v_mfma_f32_16x16x32_bf16 v[108:111], a[124:127], v[132:135], v[108:111]
s_waitcnt lgkmcnt(5)
v_and_b32 v127, 0xffff0000, v155
v_mfma_f32_16x16x32_bf16 v[96:99], a[128:131], v[136:139], v[96:99]
)"
R"(ds_read_b128 v[132:135], v25 offset:29696
v_mfma_f32_16x16x32_bf16 v[100:103], a[132:135], v[136:139], v[100:103]
v_mfma_f32_16x16x32_bf16 v[104:107], a[136:139], v[136:139], v[104:107]
s_waitcnt lgkmcnt(4)
v_add_f32 v176, v176, v184
v_add_f32 v177, v177, v185
v_mfma_f32_16x16x32_bf16 v[108:111], a[140:143], v[136:139], v[108:111]
v_add_f32 v178, v178, v186
v_add_f32 v179, v179, v187
v_mfma_f32_16x16x32_bf16 v[112:115], a[144:147], v[140:143], v[112:115]
s_waitcnt lgkmcnt(2)
ds_read_b128 v[136:139], v25 offset:30720
v_add_f32 v176, v176, v188
v_mfma_f32_16x16x32_bf16 v[116:119], a[148:151], v[140:143], v[116:119]
v_mfma_f32_16x16x32_bf16 v[120:123], a[152:155], v[140:143], v[120:123]
s_waitcnt vmcnt(6)
s_barrier
v_mfma_f32_16x16x32_bf16 v[124:127], a[156:159], v[140:143], v[124:127]
v_add_f32 v177, v177, v189
v_add_f32 v178, v178, v190
v_mfma_f32_16x16x32_bf16 v[128:131], v[60:63], v[140:143], 0
s_waitcnt lgkmcnt(2)
v_add_f32 v179, v179, v191
v_add_f32 v180, v180, v192
v_mfma_f32_16x16x32_bf16 v[112:115], a[160:163], v[144:147], v[112:115]
ds_read_b128 v[140:143], v25 offset:31744
v_add_f32 v181, v181, v193
v_mfma_f32_16x16x32_bf16 v[116:119], a[164:167], v[144:147], v[116:119]
v_add_f32 v182, v182, v194
v_add_f32 v183, v183, v195
v_mfma_f32_16x16x32_bf16 v[120:123], a[168:171], v[144:147], v[120:123]
v_cvt_pk_bf16_f32 v172, v176, v177
v_cvt_pk_bf16_f32 v173, v178, v179
v_mfma_f32_16x16x32_bf16 v[124:127], a[172:175], v[144:147], v[124:127]
v_cvt_pk_bf16_f32 v174, v180, v181
v_cvt_pk_bf16_f32 v175, v182, v183
v_mfma_f32_16x16x32_bf16 v[128:131], v[64:67], v[144:147], v[128:131]
s_waitcnt lgkmcnt(2)
global_store_dwordx4 v14, v[172:175], s[38:39] sc1
v_mfma_f32_16x16x32_bf16 v[112:115], a[176:179], v[132:135], v[112:115]
s_mov_b32 m0, 61440
ds_read_b128 v[144:147], v25 offset:32768
v_add_u32 v14, 18688, v14
v_mfma_f32_16x16x32_bf16 v[116:119], a[180:183], v[132:135], v[116:119]
v_cvt_pk_bf16_f32 v164, v96, v97
v_cvt_pk_bf16_f32 v165, v98, v99
v_mfma_f32_16x16x32_bf16 v[120:123], a[184:187], v[132:135], v[120:123]
v_cvt_pk_bf16_f32 v166, v100, v101
v_cvt_pk_bf16_f32 v167, v102, v103
v_mfma_f32_16x16x32_bf16 v[124:127], a[188:191], v[132:135], v[124:127]
global_store_dwordx4 v13, v[164:167], s[38:39] offset:0 sc1
v_mfma_f32_16x16x32_bf16 v[128:131], v[68:71], v[132:135], v[128:131]
s_waitcnt lgkmcnt(2)
v_cvt_pk_bf16_f32 v168, v104, v105
v_cvt_pk_bf16_f32 v169, v106, v107
v_mfma_f32_16x16x32_bf16 v[112:115], a[192:195], v[136:139], v[112:115]
ds_read_b128 v[132:135], v25 offset:33792
v_cvt_pk_bf16_f32 v170, v108, v109
v_mfma_f32_16x16x32_bf16 v[116:119], a[196:199], v[136:139], v[116:119]
v_cvt_pk_bf16_f32 v171, v110, v111
v_add_f32 v254, v96, v97
v_mfma_f32_16x16x32_bf16 v[120:123], a[200:203], v[136:139], v[120:123]
global_store_dwordx4 v13, v[168:171], s[38:39] offset:1024 sc1
v_mfma_f32_16x16x32_bf16 v[124:127], a[204:207], v[136:139], v[124:127]
v_add_u32 v13, 18688, v13
v_mul_f32 v255, v96, v96
v_mfma_f32_16x16x32_bf16 v[128:131], v[72:75], v[136:139], v[128:131]
s_waitcnt lgkmcnt(2)
v_fmac_f32 v255, v97, v97
v_add_f32 v254, v254, v98
v_mfma_f32_16x16x32_bf16 v[112:115], a[208:211], v[140:143], v[112:115]
ds_read_b128 v[136:139], v25 offset:34816
v_fmac_f32 v255, v98, v98
v_mfma_f32_16x16x32_bf16 v[116:119], a[212:215], v[140:143], v[116:119]
v_add_f32 v254, v254, v99
v_fmac_f32 v255, v99, v99
v_mfma_f32_16x16x32_bf16 v[120:123], a[216:219], v[140:143], v[120:123]
v_add_f32 v254, v254, v100
v_fmac_f32 v255, v100, v100
v_mfma_f32_16x16x32_bf16 v[124:127], a[220:223], v[140:143], v[124:127]
v_add_f32 v254, v254, v101
v_fmac_f32 v255, v101, v101
v_mfma_f32_16x16x32_bf16 v[128:131], v[76:79], v[140:143], v[128:131]
s_waitcnt vmcnt(7)
global_load_lds_dwordx4 v16, s[42:43] offset:0 sc1
ds_read_b128 v[148:151], v19 offset:0
s_waitcnt lgkmcnt(3)
v_mfma_f32_16x16x32_bf16 v[112:115], a[224:227], v[144:147], v[112:115]
ds_read_b128 v[140:143], v25 offset:35840
ds_read_b128 v[152:155], v20 offset:0
v_mfma_f32_16x16x32_bf16 v[116:119], a[228:231], v[144:147], v[116:119]
v_add_f32 v254, v254, v102
v_fmac_f32 v255, v102, v102
v_mfma_f32_16x16x32_bf16 v[120:123], a[232:235], v[144:147], v[120:123]
v_add_f32 v254, v254, v103
v_fmac_f32 v255, v103, v103
v_mfma_f32_16x16x32_bf16 v[124:127], a[236:239], v[144:147], v[124:127]
v_add_f32 v254, v254, v104
v_fmac_f32 v255, v104, v104
v_mfma_f32_16x16x32_bf16 v[128:131], v[80:83], v[144:147], v[128:131]
s_waitcnt lgkmcnt(4)
v_add_f32 v254, v254, v105
v_fmac_f32 v255, v105, v105
v_mfma_f32_16x16x32_bf16 v[112:115], a[240:243], v[132:135], v[112:115]
ds_read_b128 v[144:147], v25 offset:18432
v_add_f32 v254, v254, v106
v_mfma_f32_16x16x32_bf16 v[116:119], a[244:247], v[132:135], v[116:119]
global_load_lds_dwordx4 v17, s[42:43] offset:1024 sc1
v_fmac_f32 v255, v106, v106
v_mfma_f32_16x16x32_bf16 v[120:123], a[248:251], v[132:135], v[120:123]
v_add_f32 v254, v254, v107
v_fmac_f32 v255, v107, v107
v_mfma_f32_16x16x32_bf16 v[124:127], a[252:255], v[132:135], v[124:127]
v_add_f32 v254, v254, v108
v_fmac_f32 v255, v108, v108
v_mfma_f32_16x16x32_bf16 v[128:131], v[84:87], v[132:135], v[128:131]
s_waitcnt lgkmcnt(4)
v_add_f32 v254, v254, v109
v_fmac_f32 v255, v109, v109
v_mfma_f32_16x16x32_bf16 v[112:115], v[28:31], v[136:139], v[112:115]
ds_read_b128 v[132:135], v25 offset:19456
v_add_f32 v254, v254, v110
v_mfma_f32_16x16x32_bf16 v[116:119], v[32:35], v[136:139], v[116:119]
s_add_u32 m0, s49, 18432
v_fmac_f32 v255, v110, v110
v_add_f32 v254, v254, v111
v_mfma_f32_16x16x32_bf16 v[120:123], v[36:39], v[136:139], v[120:123]
v_fmac_f32 v255, v111, v111
v_add_f32 v254, v254, v176
v_mfma_f32_16x16x32_bf16 v[124:127], v[40:43], v[136:139], v[124:127]
v_fmac_f32 v255, v176, v176
v_add_f32 v254, v254, v177
v_mfma_f32_16x16x32_bf16 v[128:131], v[88:91], v[136:139], v[128:131]
s_waitcnt lgkmcnt(3)
v_fmac_f32 v255, v177, v177
v_add_f32 v254, v254, v178
v_mfma_f32_16x16x32_bf16 v[112:115], v[44:47], v[140:143], v[112:115]
ds_read_b128 v[136:139], v25 offset:20480
v_fmac_f32 v255, v178, v178
v_mfma_f32_16x16x32_bf16 v[116:119], v[48:51], v[140:143], v[116:119]
v_add_f32 v254, v254, v179
v_fmac_f32 v255, v179, v179
v_mfma_f32_16x16x32_bf16 v[120:123], v[52:55], v[140:143], v[120:123]
v_add_f32 v254, v254, v180
v_fmac_f32 v255, v180, v180
v_mfma_f32_16x16x32_bf16 v[124:127], v[56:59], v[140:143], v[124:127]
v_add_f32 v254, v254, v181
v_fmac_f32 v255, v181, v181
v_mfma_f32_16x16x32_bf16 v[128:131], v[92:95], v[140:143], v[128:131]
s_waitcnt lgkmcnt(2)
v_add_f32 v254, v254, v182
v_fmac_f32 v255, v182, v182
v_mfma_f32_16x16x32_bf16 v[112:115], a[0:3], v[144:147], v[112:115]
ds_read_b128 v[140:143], v25 offset:21504
v_add_f32 v254, v254, v183
v_mfma_f32_16x16x32_bf16 v[116:119], a[4:7], v[144:147], v[116:119]
v_fmac_f32 v255, v183, v183
v_mfma_f32_16x16x32_bf16 v[120:123], a[8:11], v[144:147], v[120:123]
ds_write_b64 v27, v[254:255] offset:44544
v_mfma_f32_16x16x32_bf16 v[124:127], a[12:15], v[144:147], v[124:127]
s_waitcnt lgkmcnt(3)
v_lshlrev_b32 v176, 16, v160
v_and_b32 v177, 0xffff0000, v160
v_mfma_f32_16x16x32_bf16 v[112:115], a[16:19], v[132:135], v[112:115]
ds_read_b128 v[144:147], v25 offset:22528
v_lshlrev_b32 v178, 16, v161
v_mfma_f32_16x16x32_bf16 v[116:119], a[20:23], v[132:135], v[116:119]
v_and_b32 v179, 0xffff0000, v161
v_lshlrev_b32 v180, 16, v162
v_mfma_f32_16x16x32_bf16 v[120:123], a[24:27], v[132:135], v[120:123]
v_and_b32 v181, 0xffff0000, v162
v_lshlrev_b32 v182, 16, v163
v_mfma_f32_16x16x32_bf16 v[124:127], a[28:31], v[132:135], v[124:127]
s_waitcnt lgkmcnt(3)
v_and_b32 v183, 0xffff0000, v163
v_add_f32 v180, v180, v128
v_mfma_f32_16x16x32_bf16 v[112:115], a[32:35], v[136:139], v[112:115]
ds_read_b128 v[132:135], v25 offset:23552
v_add_f32 v181, v181, v129
v_mfma_f32_16x16x32_bf16 v[116:119], a[36:39], v[136:139], v[116:119]
v_mfma_f32_16x16x32_bf16 v[120:123], a[40:43], v[136:139], v[120:123]
s_barrier
v_mfma_f32_16x16x32_bf16 v[124:127], a[44:47], v[136:139], v[124:127]
s_waitcnt lgkmcnt(3)
v_add_f32 v182, v182, v130
v_add_f32 v183, v183, v131
v_mfma_f32_16x16x32_bf16 v[112:115], a[48:51], v[140:143], v[112:115]
s_mov_b64 s[76:77], -1
ds_read_b128 v[136:139], v25 offset:24576
global_load_lds_dwordx4 v11, s[38:39] offset:-2048 sc1
v_mfma_f32_16x16x32_bf16 v[116:119], a[52:55], v[140:143], v[116:119]
global_load_dwordx4 v[160:163], v18, s[42:43] sc1
v_lshlrev_b32 v96, 16, v148
v_mfma_f32_16x16x32_bf16 v[120:123], a[56:59], v[140:143], v[120:123]
s_sub_u32 s42, s42, 18432
v_and_b32 v97, 0xffff0000, v148
v_lshlrev_b32 v98, 16, v149
v_mfma_f32_16x16x32_bf16 v[124:127], a[60:63], v[140:143], v[124:127]
s_waitcnt lgkmcnt(2)
global_load_lds_dwordx4 v11, s[38:39] offset:-1024 sc1
v_and_b32 v99, 0xffff0000, v149
v_mfma_f32_16x16x32_bf16 v[112:115], a[64:67], v[144:147], v[112:115]
s_subb_u32 s43, s43, 0
ds_read_b128 v[140:143], v25 offset:25600
v_lshlrev_b32 v100, 16, v150
v_mfma_f32_16x16x32_bf16 v[116:119], a[68:71], v[144:147], v[116:119]
v_and_b32 v101, 0xffff0000, v150
v_lshlrev_b32 v102, 16, v151
v_mfma_f32_16x16x32_bf16 v[120:123], a[72:75], v[144:147], v[120:123]
global_load_lds_dwordx4 v11, s[38:39] offset:0 sc1
v_add_u32 v11, 18432, v11
v_mfma_f32_16x16x32_bf16 v[124:127], a[76:79], v[144:147], v[124:127]
s_waitcnt lgkmcnt(2)
v_min_u32 v11, v11, v12
v_and_b32 v103, 0xffff0000, v151
v_mfma_f32_16x16x32_bf16 v[112:115], a[80:83], v[132:135], v[112:115]
ds_read_b128 v[144:147], v25 offset:26624
v_lshlrev_b32 v104, 16, v152
v_mfma_f32_16x16x32_bf16 v[116:119], a[84:87], v[132:135], v[116:119]
v_and_b32 v105, 0xffff0000, v152
v_lshlrev_b32 v106, 16, v153
v_mfma_f32_16x16x32_bf16 v[120:123], a[88:91], v[132:135], v[120:123]
v_and_b32 v107, 0xffff0000, v153
v_lshlrev_b32 v108, 16, v154
v_mfma_f32_16x16x32_bf16 v[124:127], a[92:95], v[132:135], v[124:127]
s_waitcnt lgkmcnt(2)
v_xor_b32 v25, 0x10000, v25
v_and_b32 v109, 0xffff0000, v154
v_mfma_f32_16x16x32_bf16 v[112:115], a[96:99], v[136:139], v[112:115]
v_lshlrev_b32 v110, 16, v155
v_and_b32 v111, 0xffff0000, v155
v_mfma_f32_16x16x32_bf16 v[116:119], a[100:103], v[136:139], v[116:119]
v_mfma_f32_16x16x32_bf16 v[120:123], a[104:107], v[136:139], v[120:123]
s_waitcnt vmcnt(9)
s_barrier
v_mfma_f32_16x16x32_bf16 v[124:127], a[108:111], v[136:139], v[124:127]
s_waitcnt lgkmcnt(1)
ds_read_b128 v[132:135], v25 offset:9216
ds_read_b128 v[184:187], v26 offset:39936
v_mfma_f32_16x16x32_bf16 v[112:115], a[112:115], v[140:143], v[112:115]
ds_read_b128 v[136:139], v25 offset:10240
ds_read_b128 v[188:191], v26 offset:40960
v_mfma_f32_16x16x32_bf16 v[116:119], a[116:119], v[140:143], v[116:119]
ds_read_b128 v[192:195], v26 offset:41984
v_mfma_f32_16x16x32_bf16 v[120:123], a[120:123], v[140:143], v[120:123]
v_mfma_f32_16x16x32_bf16 v[124:127], a[124:127], v[140:143], v[124:127]
s_waitcnt lgkmcnt(5)
v_mfma_f32_16x16x32_bf16 v[112:115], a[128:131], v[144:147], v[112:115]
ds_read_b128 v[140:143], v25 offset:11264
v_mfma_f32_16x16x32_bf16 v[116:119], a[132:135], v[144:147], v[116:119]
v_mfma_f32_16x16x32_bf16 v[120:123], a[136:139], v[144:147], v[120:123]
v_mfma_f32_16x16x32_bf16 v[124:127], a[140:143], v[144:147], v[124:127]
s_sub_u32 s70, s70, 1
s_cbranch_scc0 .Lwo_top14
s_waitcnt vmcnt(6)
s_barrier
s_waitcnt lgkmcnt(4)
v_add_f32 v176, v176, v184
v_add_f32 v177, v177, v185
v_add_f32 v178, v178, v186
v_add_f32 v179, v179, v187
s_waitcnt lgkmcnt(2)
v_add_f32 v176, v176, v188
v_add_f32 v177, v177, v189
v_add_f32 v178, v178, v190
v_add_f32 v179, v179, v191
s_waitcnt lgkmcnt(1)
v_add_f32 v180, v180, v192
v_add_f32 v181, v181, v193
v_add_f32 v182, v182, v194
v_add_f32 v183, v183, v195
v_cvt_pk_bf16_f32 v172, v176, v177
v_cvt_pk_bf16_f32 v173, v178, v179
v_cvt_pk_bf16_f32 v174, v180, v181
v_cvt_pk_bf16_f32 v175, v182, v183
v_cvt_pk_bf16_f32 v164, v112, v113
global_store_dwordx4 v14, v[172:175], s[38:39] sc1
v_cvt_pk_bf16_f32 v165, v114, v115
v_cvt_pk_bf16_f32 v166, v116, v117
v_cvt_pk_bf16_f32 v167, v118, v119
global_store_dwordx4 v13, v[164:167], s[38:39] offset:0 sc1
v_cvt_pk_bf16_f32 v168, v120, v121
v_cvt_pk_bf16_f32 v169, v122, v123
v_cvt_pk_bf16_f32 v170, v124, v125
v_cvt_pk_bf16_f32 v171, v126, v127
global_store_dwordx4 v13, v[168:171], s[38:39] offset:1024 sc1
v_add_f32 v254, v112, v113
v_mul_f32 v255, v112, v112
v_fmac_f32 v255, v113, v113
v_add_f32 v254, v254, v114
v_fmac_f32 v255, v114, v114
v_add_f32 v254, v254, v115
v_fmac_f32 v255, v115, v115
v_add_f32 v254, v254, v116
v_fmac_f32 v255, v116, v116
v_add_f32 v254, v254, v117
v_fmac_f32 v255, v117, v117
v_add_f32 v254, v254, v118
v_fmac_f32 v255, v118, v118
v_add_f32 v254, v254, v119
v_fmac_f32 v255, v119, v119
v_add_f32 v254, v254, v120
v_fmac_f32 v255, v120, v120
v_add_f32 v254, v254, v121
v_fmac_f32 v255, v121, v121
v_add_f32 v254, v254, v122
v_fmac_f32 v255, v122, v122
v_add_f32 v254, v254, v123
v_fmac_f32 v255, v123, v123
v_add_f32 v254, v254, v124
v_fmac_f32 v255, v124, v124
v_add_f32 v254, v254, v125
v_fmac_f32 v255, v125, v125
v_add_f32 v254, v254, v126
v_fmac_f32 v255, v126, v126
v_add_f32 v254, v254, v127
v_fmac_f32 v255, v127, v127
v_add_f32 v254, v254, v176
v_fmac_f32 v255, v176, v176
v_add_f32 v254, v254, v177
v_fmac_f32 v255, v177, v177
v_add_f32 v254, v254, v178
v_fmac_f32 v255, v178, v178
v_add_f32 v254, v254, v179
v_fmac_f32 v255, v179, v179
v_add_f32 v254, v254, v180
v_fmac_f32 v255, v180, v180
v_add_f32 v254, v254, v181
v_fmac_f32 v255, v181, v181
v_add_f32 v254, v254, v182
v_fmac_f32 v255, v182, v182
v_add_f32 v254, v254, v183
v_fmac_f32 v255, v183, v183
ds_write_b64 v27, v[254:255] offset:46592
s_waitcnt vmcnt(0) lgkmcnt(0)
s_barrier
s_barrier
s_endpgm
.Lwo_sp6:
s_sleep 1
global_load_dword v201, v21, s[38:39] sc1
s_add_u32 s71, s71, 1
s_waitcnt vmcnt(0)
v_sub_u32 v201, v201, v22
v_cmp_lt_u32 vcc, 0x7f, v201
s_and_b64 vcc, vcc, s[76:77]
s_cbranch_vccz .Lwo_rt7
s_cmp_lt_u32 s71, 3000000
s_cbranch_scc1 .Lwo_sp6
s_branch .Lwo_rt7
.Lwo_fr8:
s_waitcnt vmcnt(5)
v_mov_b32 v201, v200
v_sub_u32 v201, v201, v22
v_cmp_lt_u32 vcc, 0x7f, v201
s_and_b64 vcc, vcc, s[76:77]
s_cbranch_vccz .Lwo_rt7
s_branch .Lwo_sp6
.Lwo_sp9:
s_sleep 1
global_load_dword v200, v21, s[38:39] sc1
s_add_u32 s71, s71, 1
s_waitcnt vmcnt(0)
v_sub_u32 v200, v200, v22
v_cmp_lt_u32 vcc, 0x7f, v200
s_and_b64 vcc, vcc, s[76:77]
s_cbranch_vccz .Lwo_rt10
s_cmp_lt_u32 s71, 3000000
s_cbranch_scc1 .Lwo_sp9
s_branch .Lwo_rt10
.Lwo_fr11:
s_waitcnt vmcnt(5)
v_mov_b32 v200, v201
v_sub_u32 v200, v200, v22
v_cmp_lt_u32 vcc, 0x7f, v200
s_and_b64 vcc, vcc, s[76:77]
s_cbranch_vccz .Lwo_rt10
s_branch .Lwo_sp9
.Lrole2:
s_sleep 127
s_sleep 73
s_add_u32 m0, s42, 4096
s_nop 0
global_load_lds_dwordx4 v0, s[40:41] offset:-4096 nt
global_load_lds_dwordx4 v1, s[40:41] offset:-3072 nt
global_load_lds_dwordx4 v2, s[40:41] offset:-2048 nt
global_load_lds_dwordx4 v3, s[40:41] offset:-1024 nt
global_load_lds_dwordx4 v4, s[40:41] offset:0 nt
global_load_lds_dwordx4 v5, s[40:41] offset:1024 nt
s_add_u32 s40, s40, 73728
s_addc_u32 s41, s41, 0
s_add_u32 m0, s42, 28672
s_nop 0
global_load_lds_dwordx4 v0, s[40:41] offset:-4096 nt
global_load_lds_dwordx4 v1, s[40:41] offset:-3072 nt
global_load_lds_dwordx4 v2, s[40:41] offset:-2048 nt
global_load_lds_dwordx4 v3, s[40:41] offset:-1024 nt
global_load_lds_dwordx4 v4, s[40:41] offset:0 nt
global_load_lds_dwordx4 v5, s[40:41] offset:1024 nt
s_add_u32 s40, s40, 73728
)"
R"(s_addc_u32 s41, s41, 0
s_add_u32 m0, s42, 53248
s_nop 0
global_load_lds_dwordx4 v0, s[40:41] offset:-4096 nt
global_load_lds_dwordx4 v1, s[40:41] offset:-3072 nt
global_load_lds_dwordx4 v2, s[40:41] offset:-2048 nt
global_load_lds_dwordx4 v3, s[40:41] offset:-1024 nt
global_load_lds_dwordx4 v4, s[40:41] offset:0 nt
global_load_lds_dwordx4 v5, s[40:41] offset:1024 nt
s_add_u32 s40, s40, 73728
s_addc_u32 s41, s41, 0
s_add_u32 m0, s42, 77824
s_nop 0
global_load_lds_dwordx4 v0, s[40:41] offset:-4096 nt
global_load_lds_dwordx4 v1, s[40:41] offset:-3072 nt
global_load_lds_dwordx4 v2, s[40:41] offset:-2048 nt
global_load_lds_dwordx4 v3, s[40:41] offset:-1024 nt
global_load_lds_dwordx4 v4, s[40:41] offset:0 nt
global_load_lds_dwordx4 v5, s[40:41] offset:1024 nt
s_add_u32 s40, s40, 73728
s_addc_u32 s41, s41, 0
s_add_u32 m0, s42, 102400
s_nop 0
global_load_lds_dwordx4 v0, s[40:41] offset:-4096 nt
global_load_lds_dwordx4 v1, s[40:41] offset:-3072 nt
global_load_lds_dwordx4 v2, s[40:41] offset:-2048 nt
global_load_lds_dwordx4 v3, s[40:41] offset:-1024 nt
global_load_lds_dwordx4 v4, s[40:41] offset:0 nt
global_load_lds_dwordx4 v5, s[40:41] offset:1024 nt
s_add_u32 s40, s40, 73728
s_addc_u32 s41, s41, 0
v_add_u32 v255, 0x10000, v6
v_add_u32 v130, 0x20000, v6
v_add_u32 v131, 0x10000, v7
v_add_u32 v132, 0x20000, v7
v_add_u32 v133, 0x10000, v8
v_add_u32 v134, 0x20000, v8
s_waitcnt lgkmcnt(0)
s_mul_i32 s37, s37, 0x9E3779B1
s_add_u32 s37, s37, 0x7F4A7C15
v_add_u32 v14, s37, v14
v_add_u32 v16, s37, v16
s_waitcnt vmcnt(24)
s_barrier
s_add_u32 m0, s42, 126976
ds_read_b64_tr_b16 a[0:1], v6 offset:0
ds_read_b64_tr_b16 a[2:3], v6 offset:1536
global_load_lds_dwordx4 v0, s[40:41] offset:-4096 nt
ds_read_b64_tr_b16 a[4:5], v7 offset:0
global_load_lds_dwordx4 v1, s[40:41] offset:-3072 nt
ds_read_b64_tr_b16 a[6:7], v7 offset:1536
global_load_lds_dwordx4 v2, s[40:41] offset:-2048 nt
ds_read_b64_tr_b16 a[8:9], v8 offset:0
ds_read_b64_tr_b16 a[10:11], v8 offset:1536
global_load_lds_dwordx4 v3, s[40:41] offset:-1024 nt
ds_read_b64_tr_b16 a[12:13], v6 offset:12288
global_load_lds_dwordx4 v4, s[40:41] offset:0 nt
ds_read_b64_tr_b16 a[14:15], v6 offset:13824
global_load_lds_dwordx4 v5, s[40:41] offset:1024 nt
ds_read_b64_tr_b16 a[16:17], v7 offset:12288
ds_read_b64_tr_b16 a[18:19], v7 offset:13824
s_add_u32 s40, s40, 73728
ds_read_b64_tr_b16 a[20:21], v8 offset:12288
s_addc_u32 s41, s41, 0
ds_read_b64_tr_b16 a[22:23], v8 offset:13824
s_waitcnt vmcnt(24) lgkmcnt(0)
s_barrier
s_add_u32 m0, s42, 4096
ds_read_b64_tr_b16 a[24:25], v6 offset:24576
ds_read_b64_tr_b16 a[26:27], v6 offset:26112
global_load_lds_dwordx4 v0, s[40:41] offset:-4096 nt
ds_read_b64_tr_b16 a[28:29], v7 offset:24576
global_load_lds_dwordx4 v1, s[40:41] offset:-3072 nt
ds_read_b64_tr_b16 a[30:31], v7 offset:26112
global_load_lds_dwordx4 v2, s[40:41] offset:-2048 nt
ds_read_b64_tr_b16 a[32:33], v8 offset:24576
ds_read_b64_tr_b16 a[34:35], v8 offset:26112
global_load_lds_dwordx4 v3, s[40:41] offset:-1024 nt
ds_read_b64_tr_b16 a[36:37], v6 offset:36864
global_load_lds_dwordx4 v4, s[40:41] offset:0 nt
ds_read_b64_tr_b16 a[38:39], v6 offset:38400
global_load_lds_dwordx4 v5, s[40:41] offset:1024 nt
ds_read_b64_tr_b16 a[40:41], v7 offset:36864
ds_read_b64_tr_b16 a[42:43], v7 offset:38400
s_add_u32 s40, s40, 73728
ds_read_b64_tr_b16 a[44:45], v8 offset:36864
s_addc_u32 s41, s41, 0
ds_read_b64_tr_b16 a[46:47], v8 offset:38400
s_waitcnt vmcnt(24) lgkmcnt(0)
s_barrier
s_add_u32 m0, s42, 28672
ds_read_b64_tr_b16 a[48:49], v6 offset:49152
ds_read_b64_tr_b16 a[50:51], v6 offset:50688
global_load_lds_dwordx4 v0, s[40:41] offset:-4096 nt
ds_read_b64_tr_b16 a[52:53], v7 offset:49152
global_load_lds_dwordx4 v1, s[40:41] offset:-3072 nt
ds_read_b64_tr_b16 a[54:55], v7 offset:50688
global_load_lds_dwordx4 v2, s[40:41] offset:-2048 nt
ds_read_b64_tr_b16 a[56:57], v8 offset:49152
ds_read_b64_tr_b16 a[58:59], v8 offset:50688
global_load_lds_dwordx4 v3, s[40:41] offset:-1024 nt
ds_read_b64_tr_b16 a[60:61], v6 offset:61440
global_load_lds_dwordx4 v4, s[40:41] offset:0 nt
ds_read_b64_tr_b16 a[62:63], v6 offset:62976
global_load_lds_dwordx4 v5, s[40:41] offset:1024 nt
ds_read_b64_tr_b16 a[64:65], v7 offset:61440
ds_read_b64_tr_b16 a[66:67], v7 offset:62976
s_add_u32 s40, s40, 73728
ds_read_b64_tr_b16 a[68:69], v8 offset:61440
s_addc_u32 s41, s41, 0
ds_read_b64_tr_b16 a[70:71], v8 offset:62976
s_waitcnt vmcnt(24) lgkmcnt(0)
s_barrier
s_add_u32 m0, s42, 53248
ds_read_b64_tr_b16 a[72:73], v255 offset:8192
ds_read_b64_tr_b16 a[74:75], v255 offset:9728
global_load_lds_dwordx4 v0, s[40:41] offset:-4096 nt
ds_read_b64_tr_b16 a[76:77], v131 offset:8192
global_load_lds_dwordx4 v1, s[40:41] offset:-3072 nt
ds_read_b64_tr_b16 a[78:79], v131 offset:9728
global_load_lds_dwordx4 v2, s[40:41] offset:-2048 nt
ds_read_b64_tr_b16 a[80:81], v133 offset:8192
ds_read_b64_tr_b16 a[82:83], v133 offset:9728
global_load_lds_dwordx4 v3, s[40:41] offset:-1024 nt
ds_read_b64_tr_b16 a[84:85], v255 offset:20480
global_load_lds_dwordx4 v4, s[40:41] offset:0 nt
ds_read_b64_tr_b16 a[86:87], v255 offset:22016
global_load_lds_dwordx4 v5, s[40:41] offset:1024 nt
ds_read_b64_tr_b16 a[88:89], v131 offset:20480
ds_read_b64_tr_b16 a[90:91], v131 offset:22016
s_add_u32 s40, s40, 73728
ds_read_b64_tr_b16 a[92:93], v133 offset:20480
s_addc_u32 s41, s41, 0
ds_read_b64_tr_b16 a[94:95], v133 offset:22016
s_waitcnt vmcnt(24) lgkmcnt(0)
s_barrier
s_add_u32 m0, s42, 77824
ds_read_b64_tr_b16 a[96:97], v255 offset:32768
ds_read_b64_tr_b16 a[98:99], v255 offset:34304
global_load_lds_dwordx4 v0, s[40:41] offset:-4096 nt
ds_read_b64_tr_b16 a[100:101], v131 offset:32768
global_load_lds_dwordx4 v1, s[40:41] offset:-3072 nt
ds_read_b64_tr_b16 a[102:103], v131 offset:34304
global_load_lds_dwordx4 v2, s[40:41] offset:-2048 nt
ds_read_b64_tr_b16 a[104:105], v133 offset:32768
ds_read_b64_tr_b16 a[106:107], v133 offset:34304
global_load_lds_dwordx4 v3, s[40:41] offset:-1024 nt
ds_read_b64_tr_b16 a[108:109], v255 offset:45056
global_load_lds_dwordx4 v4, s[40:41] offset:0 nt
ds_read_b64_tr_b16 a[110:111], v255 offset:46592
global_load_lds_dwordx4 v5, s[40:41] offset:1024 nt
ds_read_b64_tr_b16 a[112:113], v131 offset:45056
ds_read_b64_tr_b16 a[114:115], v131 offset:46592
s_add_u32 s40, s40, 73728
ds_read_b64_tr_b16 a[116:117], v133 offset:45056
s_addc_u32 s41, s41, 0
ds_read_b64_tr_b16 a[118:119], v133 offset:46592
s_waitcnt vmcnt(24) lgkmcnt(0)
s_barrier
s_add_u32 m0, s42, 102400
ds_read_b64_tr_b16 a[120:121], v255 offset:57344
ds_read_b64_tr_b16 a[122:123], v255 offset:58880
global_load_lds_dwordx4 v0, s[40:41] offset:-4096 nt
ds_read_b64_tr_b16 a[124:125], v131 offset:57344
global_load_lds_dwordx4 v1, s[40:41] offset:-3072 nt
ds_read_b64_tr_b16 a[126:127], v131 offset:58880
global_load_lds_dwordx4 v2, s[40:41] offset:-2048 nt
ds_read_b64_tr_b16 a[128:129], v133 offset:57344
ds_read_b64_tr_b16 a[130:131], v133 offset:58880
global_load_lds_dwordx4 v3, s[40:41] offset:-1024 nt
ds_read_b64_tr_b16 a[132:133], v130 offset:4096
global_load_lds_dwordx4 v4, s[40:41] offset:0 nt
ds_read_b64_tr_b16 a[134:135], v130 offset:5632
global_load_lds_dwordx4 v5, s[40:41] offset:1024 nt
ds_read_b64_tr_b16 a[136:137], v132 offset:4096
ds_read_b64_tr_b16 a[138:139], v132 offset:5632
s_add_u32 s40, s40, 73728
ds_read_b64_tr_b16 a[140:141], v134 offset:4096
s_addc_u32 s41, s41, 0
ds_read_b64_tr_b16 a[142:143], v134 offset:5632
s_waitcnt vmcnt(24) lgkmcnt(0)
s_barrier
s_add_u32 m0, s42, 126976
ds_read_b64_tr_b16 a[144:145], v6 offset:0
ds_read_b64_tr_b16 a[146:147], v6 offset:1536
global_load_lds_dwordx4 v0, s[40:41] offset:-4096 nt
ds_read_b64_tr_b16 a[148:149], v7 offset:0
global_load_lds_dwordx4 v1, s[40:41] offset:-3072 nt
ds_read_b64_tr_b16 a[150:151], v7 offset:1536
global_load_lds_dwordx4 v2, s[40:41] offset:-2048 nt
ds_read_b64_tr_b16 a[152:153], v8 offset:0
ds_read_b64_tr_b16 a[154:155], v8 offset:1536
global_load_lds_dwordx4 v3, s[40:41] offset:-1024 nt
ds_read_b64_tr_b16 a[156:157], v6 offset:12288
global_load_lds_dwordx4 v4, s[40:41] offset:0 nt
ds_read_b64_tr_b16 a[158:159], v6 offset:13824
global_load_lds_dwordx4 v5, s[40:41] offset:1024 nt
ds_read_b64_tr_b16 a[160:161], v7 offset:12288
ds_read_b64_tr_b16 a[162:163], v7 offset:13824
s_sub_u32 s40, s40, 811008
ds_read_b64_tr_b16 a[164:165], v8 offset:12288
s_subb_u32 s41, s41, 0
ds_read_b64_tr_b16 a[166:167], v8 offset:13824
s_waitcnt vmcnt(24) lgkmcnt(0)
s_barrier
ds_read_b64_tr_b16 a[168:169], v6 offset:24576
ds_read_b64_tr_b16 a[170:171], v6 offset:26112
ds_read_b64_tr_b16 a[172:173], v7 offset:24576
ds_read_b64_tr_b16 a[174:175], v7 offset:26112
ds_read_b64_tr_b16 a[176:177], v8 offset:24576
ds_read_b64_tr_b16 a[178:179], v8 offset:26112
ds_read_b64_tr_b16 a[180:181], v6 offset:36864
ds_read_b64_tr_b16 a[182:183], v6 offset:38400
ds_read_b64_tr_b16 a[184:185], v7 offset:36864
ds_read_b64_tr_b16 a[186:187], v7 offset:38400
ds_read_b64_tr_b16 a[188:189], v8 offset:36864
ds_read_b64_tr_b16 a[190:191], v8 offset:38400
s_waitcnt vmcnt(18) lgkmcnt(0)
s_barrier
ds_read_b64_tr_b16 a[192:193], v6 offset:49152
ds_read_b64_tr_b16 a[194:195], v6 offset:50688
ds_read_b64_tr_b16 a[196:197], v7 offset:49152
ds_read_b64_tr_b16 a[198:199], v7 offset:50688
ds_read_b64_tr_b16 a[200:201], v8 offset:49152
ds_read_b64_tr_b16 a[202:203], v8 offset:50688
ds_read_b64_tr_b16 a[204:205], v6 offset:61440
ds_read_b64_tr_b16 a[206:207], v6 offset:62976
ds_read_b64_tr_b16 a[208:209], v7 offset:61440
ds_read_b64_tr_b16 a[210:211], v7 offset:62976
ds_read_b64_tr_b16 a[212:213], v8 offset:61440
ds_read_b64_tr_b16 a[214:215], v8 offset:62976
s_waitcnt vmcnt(12) lgkmcnt(0)
s_barrier
ds_read_b64_tr_b16 a[216:217], v255 offset:8192
ds_read_b64_tr_b16 a[218:219], v255 offset:9728
ds_read_b64_tr_b16 a[220:221], v131 offset:8192
ds_read_b64_tr_b16 a[222:223], v131 offset:9728
ds_read_b64_tr_b16 a[224:225], v133 offset:8192
ds_read_b64_tr_b16 a[226:227], v133 offset:9728
ds_read_b64_tr_b16 a[228:229], v255 offset:20480
ds_read_b64_tr_b16 a[230:231], v255 offset:22016
ds_read_b64_tr_b16 a[232:233], v131 offset:20480
ds_read_b64_tr_b16 a[234:235], v131 offset:22016
ds_read_b64_tr_b16 a[236:237], v133 offset:20480
ds_read_b64_tr_b16 a[238:239], v133 offset:22016
s_waitcnt vmcnt(6) lgkmcnt(0)
s_barrier
ds_read_b64_tr_b16 a[240:241], v255 offset:32768
ds_read_b64_tr_b16 a[242:243], v255 offset:34304
ds_read_b64_tr_b16 a[244:245], v131 offset:32768
ds_read_b64_tr_b16 a[246:247], v131 offset:34304
ds_read_b64_tr_b16 a[248:249], v133 offset:32768
ds_read_b64_tr_b16 a[250:251], v133 offset:34304
ds_read_b64_tr_b16 a[252:253], v255 offset:45056
ds_read_b64_tr_b16 a[254:255], v255 offset:46592
ds_read_b64_tr_b16 v[18:19], v131 offset:45056
ds_read_b64_tr_b16 v[20:21], v131 offset:46592
ds_read_b64_tr_b16 v[22:23], v133 offset:45056
ds_read_b64_tr_b16 v[24:25], v133 offset:46592
s_waitcnt vmcnt(0) lgkmcnt(0)
s_barrier
ds_read_b64_tr_b16 v[26:27], v255 offset:57344
ds_read_b64_tr_b16 v[28:29], v255 offset:58880
ds_read_b64_tr_b16 v[30:31], v131 offset:57344
ds_read_b64_tr_b16 v[32:33], v131 offset:58880
ds_read_b64_tr_b16 v[34:35], v133 offset:57344
ds_read_b64_tr_b16 v[36:37], v133 offset:58880
ds_read_b64_tr_b16 v[38:39], v130 offset:4096
ds_read_b64_tr_b16 v[40:41], v130 offset:5632
ds_read_b64_tr_b16 v[42:43], v132 offset:4096
ds_read_b64_tr_b16 v[44:45], v132 offset:5632
ds_read_b64_tr_b16 v[46:47], v134 offset:4096
ds_read_b64_tr_b16 v[48:49], v134 offset:5632
s_waitcnt lgkmcnt(0)
s_barrier
s_and_b32 s75, s36, 3
s_cmp_eq_u32 s75, 1
s_cbranch_scc1 .Ldn_w2
s_cmp_eq_u32 s75, 2
s_cbranch_scc1 .Ldn_w3
s_cmp_eq_u32 s75, 3
s_cbranch_scc1 .Ldn_w4
.Ldn_w1:
v_add_u32 v254, -1, v14
s_mov_b32 s76, 0xffff
s_mov_b32 s77, 0xffff
s_mov_b32 s70, 32
s_waitcnt vmcnt(0) lgkmcnt(0)
s_nop 7
.p2align 6
.Ldn_top5:
s_waitcnt lgkmcnt(5)
v_mfma_f32_16x16x32_bf16 v[50:53], a[0:3], v[74:77], 0
s_waitcnt lgkmcnt(4)
v_mfma_f32_16x16x32_bf16 v[50:53], a[12:15], v[78:81], v[50:53]
s_waitcnt lgkmcnt(3)
v_mfma_f32_16x16x32_bf16 v[50:53], a[24:27], v[82:85], v[50:53]
s_waitcnt lgkmcnt(2)
v_mfma_f32_16x16x32_bf16 v[50:53], a[36:39], v[86:89], v[50:53]
s_waitcnt lgkmcnt(1)
ds_read_b128 v[98:101], v17 offset:6144
v_cvt_pk_bf16_f32 v126, v70, v71
v_mfma_f32_16x16x32_bf16 v[50:53], a[48:51], v[90:93], v[50:53]
s_waitcnt lgkmcnt(1)
v_mfma_f32_16x16x32_bf16 v[50:53], a[60:63], v[94:97], v[50:53]
s_waitcnt vmcnt(6)
s_barrier
v_mfma_f32_16x16x32_bf16 v[54:57], a[4:7], v[74:77], 0
ds_read_b128 v[102:105], v17 offset:7168
v_cvt_pk_bf16_f32 v128, v70, v71
v_mfma_f32_16x16x32_bf16 v[54:57], a[16:19], v[78:81], v[54:57]
global_store_dwordx4 v11, v[122:125], s[38:39] sc1
v_mfma_f32_16x16x32_bf16 v[54:57], a[28:31], v[82:85], v[54:57]
ds_read_b128 v[106:109], v17 offset:8192
v_add_u32 v11, 110592, v11
v_mfma_f32_16x16x32_bf16 v[54:57], a[40:43], v[86:89], v[54:57]
v_cvt_pk_bf16_f32 v127, v72, v73
v_cvt_pk_bf16_f32 v129, v72, v73
v_mfma_f32_16x16x32_bf16 v[54:57], a[52:55], v[90:93], v[54:57]
ds_read_b128 v[110:113], v17 offset:9216
v_mfma_f32_16x16x32_bf16 v[54:57], a[64:67], v[94:97], v[54:57]
v_permlane16_swap_b32 v126, v128
v_mfma_f32_16x16x32_bf16 v[58:61], a[8:11], v[74:77], 0
ds_read_b128 v[114:117], v17 offset:10240
v_mfma_f32_16x16x32_bf16 v[58:61], a[20:23], v[78:81], v[58:61]
v_permlane16_swap_b32 v127, v129
v_mfma_f32_16x16x32_bf16 v[58:61], a[32:35], v[82:85], v[58:61]
ds_read_b128 v[118:121], v17 offset:11264
v_mfma_f32_16x16x32_bf16 v[58:61], a[44:47], v[86:89], v[58:61]
s_mov_b64 exec, s[76:77]
global_store_dwordx4 v12, v[126:129], s[38:39] sc1
s_mov_b64 exec, -1
v_mfma_f32_16x16x32_bf16 v[58:61], a[56:59], v[90:93], v[58:61]
v_add_u32 v12, 110592, v12
v_mfma_f32_16x16x32_bf16 v[58:61], a[68:71], v[94:97], v[58:61]
s_waitcnt lgkmcnt(5)
v_mfma_f32_16x16x32_bf16 v[50:53], a[72:75], v[98:101], v[50:53]
s_waitcnt lgkmcnt(4)
v_mfma_f32_16x16x32_bf16 v[50:53], a[84:87], v[102:105], v[50:53]
s_waitcnt lgkmcnt(3)
v_mfma_f32_16x16x32_bf16 v[50:53], a[96:99], v[106:109], v[50:53]
s_waitcnt lgkmcnt(2)
v_mfma_f32_16x16x32_bf16 v[50:53], a[108:111], v[110:113], v[50:53]
s_waitcnt lgkmcnt(1)
v_mfma_f32_16x16x32_bf16 v[50:53], a[120:123], v[114:117], v[50:53]
s_waitcnt lgkmcnt(0)
ds_read_b128 v[74:77], v17 offset:12288
v_mfma_f32_16x16x32_bf16 v[50:53], a[132:135], v[118:121], v[50:53]
v_mfma_f32_16x16x32_bf16 v[54:57], a[76:79], v[98:101], v[54:57]
ds_read_b128 v[78:81], v17 offset:13312
v_mfma_f32_16x16x32_bf16 v[54:57], a[88:91], v[102:105], v[54:57]
v_mfma_f32_16x16x32_bf16 v[54:57], a[100:103], v[106:109], v[54:57]
ds_read_b128 v[82:85], v17 offset:14336
v_mfma_f32_16x16x32_bf16 v[54:57], a[112:115], v[110:113], v[54:57]
v_mfma_f32_16x16x32_bf16 v[54:57], a[124:127], v[114:117], v[54:57]
ds_read_b128 v[86:89], v17 offset:15360
v_mfma_f32_16x16x32_bf16 v[54:57], a[136:139], v[118:121], v[54:57]
v_mfma_f32_16x16x32_bf16 v[58:61], a[80:83], v[98:101], v[58:61]
ds_read_b128 v[90:93], v17 offset:16384
v_mfma_f32_16x16x32_bf16 v[58:61], a[92:95], v[102:105], v[58:61]
v_mfma_f32_16x16x32_bf16 v[58:61], a[104:107], v[106:109], v[58:61]
ds_read_b128 v[94:97], v17 offset:17408
v_mfma_f32_16x16x32_bf16 v[58:61], a[116:119], v[110:113], v[58:61]
v_mfma_f32_16x16x32_bf16 v[58:61], a[128:131], v[114:117], v[58:61]
v_mfma_f32_16x16x32_bf16 v[58:61], a[140:143], v[118:121], v[58:61]
s_waitcnt lgkmcnt(5)
v_mfma_f32_16x16x32_bf16 v[50:53], a[144:147], v[74:77], v[50:53]
s_waitcnt lgkmcnt(4)
v_mfma_f32_16x16x32_bf16 v[50:53], a[156:159], v[78:81], v[50:53]
s_waitcnt lgkmcnt(3)
v_mfma_f32_16x16x32_bf16 v[50:53], a[168:171], v[82:85], v[50:53]
s_waitcnt lgkmcnt(2)
v_mfma_f32_16x16x32_bf16 v[50:53], a[180:183], v[86:89], v[50:53]
s_waitcnt lgkmcnt(1)
v_mfma_f32_16x16x32_bf16 v[50:53], a[192:195], v[90:93], v[50:53]
s_xor_b32 s43, s43, 0x10000
ds_read_b128 v[98:101], v17 offset:18432
s_waitcnt lgkmcnt(1)
v_mfma_f32_16x16x32_bf16 v[50:53], a[204:207], v[94:97], v[50:53]
s_add_u32 m0, s43, 0
v_mfma_f32_16x16x32_bf16 v[54:57], a[148:151], v[74:77], v[54:57]
ds_read_b128 v[102:105], v17 offset:19456
ds_read_b128 v[106:109], v17 offset:20480
v_mfma_f32_16x16x32_bf16 v[54:57], a[160:163], v[78:81], v[54:57]
v_mfma_f32_16x16x32_bf16 v[54:57], a[172:175], v[82:85], v[54:57]
s_barrier
v_mfma_f32_16x16x32_bf16 v[54:57], a[184:187], v[86:89], v[54:57]
global_load_lds_dwordx4 v9, s[38:39] offset:-2048 sc1
v_mfma_f32_16x16x32_bf16 v[54:57], a[196:199], v[90:93], v[54:57]
ds_read_b128 v[110:113], v17 offset:21504
v_mfma_f32_16x16x32_bf16 v[54:57], a[208:211], v[94:97], v[54:57]
v_mfma_f32_16x16x32_bf16 v[58:61], a[152:155], v[74:77], v[58:61]
ds_read_b128 v[114:117], v17 offset:22528
global_load_lds_dwordx4 v9, s[38:39] offset:-1024 sc1
v_mfma_f32_16x16x32_bf16 v[58:61], a[164:167], v[78:81], v[58:61]
v_mfma_f32_16x16x32_bf16 v[58:61], a[176:179], v[82:85], v[58:61]
ds_read_b128 v[118:121], v17 offset:23552
v_mfma_f32_16x16x32_bf16 v[58:61], a[188:191], v[86:89], v[58:61]
global_load_lds_dwordx4 v9, s[38:39] offset:0 sc1
v_mfma_f32_16x16x32_bf16 v[58:61], a[200:203], v[90:93], v[58:61]
)"
R"(v_mfma_f32_16x16x32_bf16 v[58:61], a[212:215], v[94:97], v[58:61]
s_waitcnt lgkmcnt(5)
v_mfma_f32_16x16x32_bf16 v[50:53], a[216:219], v[98:101], v[50:53]
s_waitcnt lgkmcnt(4)
global_load_lds_dwordx4 v9, s[38:39] offset:1024 sc1
v_mfma_f32_16x16x32_bf16 v[50:53], a[228:231], v[102:105], v[50:53]
s_waitcnt lgkmcnt(3)
v_mfma_f32_16x16x32_bf16 v[50:53], a[240:243], v[106:109], v[50:53]
s_waitcnt lgkmcnt(2)
v_mfma_f32_16x16x32_bf16 v[50:53], a[252:255], v[110:113], v[50:53]
s_waitcnt lgkmcnt(1)
global_load_lds_dwordx4 v9, s[38:39] offset:2048 sc1
v_mfma_f32_16x16x32_bf16 v[50:53], v[26:29], v[114:117], v[50:53]
s_waitcnt lgkmcnt(0)
v_mfma_f32_16x16x32_bf16 v[50:53], v[38:41], v[118:121], v[50:53]
v_mfma_f32_16x16x32_bf16 v[54:57], a[220:223], v[98:101], v[54:57]
global_load_lds_dwordx4 v9, s[38:39] offset:3072 sc1
v_add_u32 v9, 147456, v9
v_mfma_f32_16x16x32_bf16 v[54:57], a[232:235], v[102:105], v[54:57]
v_mfma_f32_16x16x32_bf16 v[54:57], a[244:247], v[106:109], v[54:57]
s_waitcnt vmcnt(8)
s_barrier
v_mfma_f32_16x16x32_bf16 v[54:57], v[18:21], v[110:113], v[54:57]
ds_read_b128 v[74:77], v17 offset:24576
v_min_u32 v9, v9, v10
v_mfma_f32_16x16x32_bf16 v[54:57], v[30:33], v[114:117], v[54:57]
ds_read_b128 v[78:81], v17 offset:25600
v_cvt_pk_bf16_f32 v122, v50, v51
v_mfma_f32_16x16x32_bf16 v[54:57], v[42:45], v[118:121], v[54:57]
ds_read_b128 v[82:85], v17 offset:26624
v_cvt_pk_bf16_f32 v123, v52, v53
v_mfma_f32_16x16x32_bf16 v[58:61], a[224:227], v[98:101], v[58:61]
ds_read_b128 v[86:89], v17 offset:27648
v_mfma_f32_16x16x32_bf16 v[58:61], a[236:239], v[102:105], v[58:61]
ds_read_b128 v[90:93], v17 offset:28672
v_mfma_f32_16x16x32_bf16 v[58:61], a[248:251], v[106:109], v[58:61]
ds_read_b128 v[94:97], v17 offset:29696
v_mfma_f32_16x16x32_bf16 v[58:61], v[22:25], v[110:113], v[58:61]
v_cvt_pk_bf16_f32 v124, v54, v55
v_cvt_pk_bf16_f32 v125, v56, v57
v_mfma_f32_16x16x32_bf16 v[58:61], v[34:37], v[114:117], v[58:61]
v_mfma_f32_16x16x32_bf16 v[58:61], v[46:49], v[118:121], v[58:61]
s_waitcnt lgkmcnt(5)
v_mfma_f32_16x16x32_bf16 v[62:65], a[0:3], v[74:77], 0
s_waitcnt lgkmcnt(4)
v_mfma_f32_16x16x32_bf16 v[62:65], a[12:15], v[78:81], v[62:65]
s_waitcnt lgkmcnt(3)
v_mfma_f32_16x16x32_bf16 v[62:65], a[24:27], v[82:85], v[62:65]
s_waitcnt lgkmcnt(2)
v_mfma_f32_16x16x32_bf16 v[62:65], a[36:39], v[86:89], v[62:65]
s_waitcnt lgkmcnt(1)
ds_read_b128 v[98:101], v17 offset:30720
v_cvt_pk_bf16_f32 v126, v58, v59
v_mfma_f32_16x16x32_bf16 v[62:65], a[48:51], v[90:93], v[62:65]
s_waitcnt lgkmcnt(1)
v_mfma_f32_16x16x32_bf16 v[62:65], a[60:63], v[94:97], v[62:65]
s_waitcnt vmcnt(6)
s_barrier
v_mfma_f32_16x16x32_bf16 v[66:69], a[4:7], v[74:77], 0
ds_read_b128 v[102:105], v17 offset:31744
v_cvt_pk_bf16_f32 v128, v58, v59
v_mfma_f32_16x16x32_bf16 v[66:69], a[16:19], v[78:81], v[66:69]
global_store_dwordx4 v11, v[122:125], s[38:39] sc1
v_mfma_f32_16x16x32_bf16 v[66:69], a[28:31], v[82:85], v[66:69]
ds_read_b128 v[106:109], v17 offset:32768
v_add_u32 v11, 110592, v11
v_mfma_f32_16x16x32_bf16 v[66:69], a[40:43], v[86:89], v[66:69]
v_cvt_pk_bf16_f32 v127, v60, v61
v_cvt_pk_bf16_f32 v129, v60, v61
v_mfma_f32_16x16x32_bf16 v[66:69], a[52:55], v[90:93], v[66:69]
ds_read_b128 v[110:113], v17 offset:33792
v_mfma_f32_16x16x32_bf16 v[66:69], a[64:67], v[94:97], v[66:69]
v_permlane16_swap_b32 v126, v128
v_mfma_f32_16x16x32_bf16 v[70:73], a[8:11], v[74:77], 0
ds_read_b128 v[114:117], v17 offset:34816
v_mfma_f32_16x16x32_bf16 v[70:73], a[20:23], v[78:81], v[70:73]
v_permlane16_swap_b32 v127, v129
v_mfma_f32_16x16x32_bf16 v[70:73], a[32:35], v[82:85], v[70:73]
ds_read_b128 v[118:121], v17 offset:35840
v_mfma_f32_16x16x32_bf16 v[70:73], a[44:47], v[86:89], v[70:73]
s_mov_b64 exec, s[76:77]
global_store_dwordx4 v12, v[126:129], s[38:39] sc1
s_mov_b64 exec, -1
v_mfma_f32_16x16x32_bf16 v[70:73], a[56:59], v[90:93], v[70:73]
v_add_u32 v12, 110592, v12
v_mfma_f32_16x16x32_bf16 v[70:73], a[68:71], v[94:97], v[70:73]
s_waitcnt lgkmcnt(5)
v_mfma_f32_16x16x32_bf16 v[62:65], a[72:75], v[98:101], v[62:65]
s_waitcnt lgkmcnt(4)
v_mfma_f32_16x16x32_bf16 v[62:65], a[84:87], v[102:105], v[62:65]
s_waitcnt lgkmcnt(3)
v_mfma_f32_16x16x32_bf16 v[62:65], a[96:99], v[106:109], v[62:65]
s_waitcnt lgkmcnt(2)
v_mfma_f32_16x16x32_bf16 v[62:65], a[108:111], v[110:113], v[62:65]
s_waitcnt lgkmcnt(1)
v_mfma_f32_16x16x32_bf16 v[62:65], a[120:123], v[114:117], v[62:65]
s_waitcnt lgkmcnt(0)
ds_read_b128 v[74:77], v17 offset:36864
v_mfma_f32_16x16x32_bf16 v[62:65], a[132:135], v[118:121], v[62:65]
v_mfma_f32_16x16x32_bf16 v[66:69], a[76:79], v[98:101], v[66:69]
ds_read_b128 v[78:81], v17 offset:37888
v_mfma_f32_16x16x32_bf16 v[66:69], a[88:91], v[102:105], v[66:69]
v_mfma_f32_16x16x32_bf16 v[66:69], a[100:103], v[106:109], v[66:69]
ds_read_b128 v[82:85], v17 offset:38912
v_mfma_f32_16x16x32_bf16 v[66:69], a[112:115], v[110:113], v[66:69]
v_mfma_f32_16x16x32_bf16 v[66:69], a[124:127], v[114:117], v[66:69]
ds_read_b128 v[86:89], v17 offset:39936
v_mfma_f32_16x16x32_bf16 v[66:69], a[136:139], v[118:121], v[66:69]
v_mfma_f32_16x16x32_bf16 v[70:73], a[80:83], v[98:101], v[70:73]
ds_read_b128 v[90:93], v17 offset:40960
v_mfma_f32_16x16x32_bf16 v[70:73], a[92:95], v[102:105], v[70:73]
v_mfma_f32_16x16x32_bf16 v[70:73], a[104:107], v[106:109], v[70:73]
ds_read_b128 v[94:97], v17 offset:41984
v_mfma_f32_16x16x32_bf16 v[70:73], a[116:119], v[110:113], v[70:73]
v_mfma_f32_16x16x32_bf16 v[70:73], a[128:131], v[114:117], v[70:73]
v_mfma_f32_16x16x32_bf16 v[70:73], a[140:143], v[118:121], v[70:73]
s_waitcnt lgkmcnt(5)
v_mfma_f32_16x16x32_bf16 v[62:65], a[144:147], v[74:77], v[62:65]
s_waitcnt lgkmcnt(4)
v_mfma_f32_16x16x32_bf16 v[62:65], a[156:159], v[78:81], v[62:65]
s_waitcnt lgkmcnt(3)
v_mfma_f32_16x16x32_bf16 v[62:65], a[168:171], v[82:85], v[62:65]
s_waitcnt lgkmcnt(2)
v_mfma_f32_16x16x32_bf16 v[62:65], a[180:183], v[86:89], v[62:65]
s_waitcnt lgkmcnt(1)
v_mfma_f32_16x16x32_bf16 v[62:65], a[192:195], v[90:93], v[62:65]
s_waitcnt lgkmcnt(0)
ds_read_b128 v[98:101], v17 offset:43008
v_mfma_f32_16x16x32_bf16 v[62:65], a[204:207], v[94:97], v[62:65]
s_add_u32 m0, s43, 24576
v_mfma_f32_16x16x32_bf16 v[66:69], a[148:151], v[74:77], v[66:69]
ds_read_b128 v[102:105], v17 offset:44032
ds_read_b128 v[106:109], v17 offset:45056
v_mfma_f32_16x16x32_bf16 v[66:69], a[160:163], v[78:81], v[66:69]
v_mfma_f32_16x16x32_bf16 v[66:69], a[172:175], v[82:85], v[66:69]
s_barrier
v_mfma_f32_16x16x32_bf16 v[66:69], a[184:187], v[86:89], v[66:69]
global_load_lds_dwordx4 v9, s[38:39] offset:-2048 sc1
v_mfma_f32_16x16x32_bf16 v[66:69], a[196:199], v[90:93], v[66:69]
ds_read_b128 v[110:113], v17 offset:46080
v_mfma_f32_16x16x32_bf16 v[66:69], a[208:211], v[94:97], v[66:69]
v_mfma_f32_16x16x32_bf16 v[70:73], a[152:155], v[74:77], v[70:73]
ds_read_b128 v[114:117], v17 offset:47104
global_load_lds_dwordx4 v9, s[38:39] offset:-1024 sc1
v_mfma_f32_16x16x32_bf16 v[70:73], a[164:167], v[78:81], v[70:73]
v_mfma_f32_16x16x32_bf16 v[70:73], a[176:179], v[82:85], v[70:73]
ds_read_b128 v[118:121], v17 offset:48128
v_mfma_f32_16x16x32_bf16 v[70:73], a[188:191], v[86:89], v[70:73]
global_load_lds_dwordx4 v9, s[38:39] offset:0 sc1
v_mfma_f32_16x16x32_bf16 v[70:73], a[200:203], v[90:93], v[70:73]
v_mfma_f32_16x16x32_bf16 v[70:73], a[212:215], v[94:97], v[70:73]
s_waitcnt lgkmcnt(5)
v_mfma_f32_16x16x32_bf16 v[62:65], a[216:219], v[98:101], v[62:65]
s_waitcnt lgkmcnt(4)
global_load_lds_dwordx4 v9, s[38:39] offset:1024 sc1
v_mfma_f32_16x16x32_bf16 v[62:65], a[228:231], v[102:105], v[62:65]
s_waitcnt lgkmcnt(3)
v_mfma_f32_16x16x32_bf16 v[62:65], a[240:243], v[106:109], v[62:65]
s_waitcnt lgkmcnt(2)
v_mfma_f32_16x16x32_bf16 v[62:65], a[252:255], v[110:113], v[62:65]
s_waitcnt lgkmcnt(1)
global_load_lds_dwordx4 v9, s[38:39] offset:2048 sc1
v_mfma_f32_16x16x32_bf16 v[62:65], v[26:29], v[114:117], v[62:65]
s_waitcnt lgkmcnt(0)
v_mfma_f32_16x16x32_bf16 v[62:65], v[38:41], v[118:121], v[62:65]
v_mfma_f32_16x16x32_bf16 v[66:69], a[220:223], v[98:101], v[66:69]
v_xor_b32 v17, 0x10000, v17
global_load_lds_dwordx4 v9, s[38:39] offset:3072 sc1
v_mfma_f32_16x16x32_bf16 v[66:69], a[232:235], v[102:105], v[66:69]
v_mfma_f32_16x16x32_bf16 v[66:69], a[244:247], v[106:109], v[66:69]
s_waitcnt vmcnt(8)
s_barrier
v_mfma_f32_16x16x32_bf16 v[66:69], v[18:21], v[110:113], v[66:69]
ds_read_b128 v[74:77], v17 offset:0
v_add_u32 v9, 147456, v9
v_mfma_f32_16x16x32_bf16 v[66:69], v[30:33], v[114:117], v[66:69]
ds_read_b128 v[78:81], v17 offset:1024
v_min_u32 v9, v9, v10
v_mfma_f32_16x16x32_bf16 v[66:69], v[42:45], v[118:121], v[66:69]
ds_read_b128 v[82:85], v17 offset:2048
v_cvt_pk_bf16_f32 v122, v62, v63
v_mfma_f32_16x16x32_bf16 v[70:73], a[224:227], v[98:101], v[70:73]
ds_read_b128 v[86:89], v17 offset:3072
v_cvt_pk_bf16_f32 v123, v64, v65
v_mfma_f32_16x16x32_bf16 v[70:73], a[236:239], v[102:105], v[70:73]
ds_read_b128 v[90:93], v17 offset:4096
v_mfma_f32_16x16x32_bf16 v[70:73], a[248:251], v[106:109], v[70:73]
ds_read_b128 v[94:97], v17 offset:5120
v_mfma_f32_16x16x32_bf16 v[70:73], v[22:25], v[110:113], v[70:73]
v_cvt_pk_bf16_f32 v124, v66, v67
v_cvt_pk_bf16_f32 v125, v68, v69
v_mfma_f32_16x16x32_bf16 v[70:73], v[34:37], v[114:117], v[70:73]
v_mfma_f32_16x16x32_bf16 v[70:73], v[46:49], v[118:121], v[70:73]
s_sub_u32 s70, s70, 1
s_cbranch_scc0 .Ldn_top5
v_cvt_pk_bf16_f32 v122, v62, v63
v_cvt_pk_bf16_f32 v123, v64, v65
v_cvt_pk_bf16_f32 v124, v66, v67
v_cvt_pk_bf16_f32 v125, v68, v69
global_store_dwordx4 v11, v[122:125], s[38:39] sc1
v_add_u32 v11, 110592, v11
v_cvt_pk_bf16_f32 v126, v70, v71
v_cvt_pk_bf16_f32 v128, v70, v71
v_cvt_pk_bf16_f32 v127, v72, v73
v_cvt_pk_bf16_f32 v129, v72, v73
v_permlane16_swap_b32 v126, v128
s_nop 0
v_permlane16_swap_b32 v127, v129
s_mov_b64 exec, s[76:77]
global_store_dwordx4 v12, v[126:129], s[38:39] sc1
s_mov_b64 exec, -1
v_add_u32 v12, 110592, v12
s_waitcnt vmcnt(2)
s_barrier
s_waitcnt vmcnt(0)
s_barrier
s_endpgm
.Ldn_w2:
v_add_u32 v254, -1, v14
s_mov_b32 s76, 0xffff
s_mov_b32 s77, 0xffff
s_mov_b32 s70, 32
s_waitcnt vmcnt(0) lgkmcnt(0)
s_nop 7
.p2align 6
.Ldn_top6:
s_waitcnt lgkmcnt(5)
v_mfma_f32_16x16x32_bf16 v[50:53], a[0:3], v[74:77], 0
s_waitcnt lgkmcnt(4)
v_mfma_f32_16x16x32_bf16 v[50:53], a[12:15], v[78:81], v[50:53]
s_waitcnt lgkmcnt(3)
v_mfma_f32_16x16x32_bf16 v[50:53], a[24:27], v[82:85], v[50:53]
s_waitcnt lgkmcnt(2)
v_mfma_f32_16x16x32_bf16 v[50:53], a[36:39], v[86:89], v[50:53]
s_waitcnt lgkmcnt(1)
ds_read_b128 v[98:101], v17 offset:6144
v_cvt_pk_bf16_f32 v126, v70, v71
v_mfma_f32_16x16x32_bf16 v[50:53], a[48:51], v[90:93], v[50:53]
s_waitcnt lgkmcnt(1)
v_mfma_f32_16x16x32_bf16 v[50:53], a[60:63], v[94:97], v[50:53]
s_waitcnt vmcnt(6)
s_barrier
v_mfma_f32_16x16x32_bf16 v[54:57], a[4:7], v[74:77], 0
ds_read_b128 v[102:105], v17 offset:7168
v_cvt_pk_bf16_f32 v128, v70, v71
v_mfma_f32_16x16x32_bf16 v[54:57], a[16:19], v[78:81], v[54:57]
v_cvt_pk_bf16_f32 v127, v72, v73
v_cvt_pk_bf16_f32 v129, v72, v73
v_mfma_f32_16x16x32_bf16 v[54:57], a[28:31], v[82:85], v[54:57]
ds_read_b128 v[106:109], v17 offset:8192
v_mfma_f32_16x16x32_bf16 v[54:57], a[40:43], v[86:89], v[54:57]
global_store_dwordx4 v11, v[122:125], s[38:39] sc1
v_mfma_f32_16x16x32_bf16 v[54:57], a[52:55], v[90:93], v[54:57]
ds_read_b128 v[110:113], v17 offset:9216
v_add_u32 v11, 110592, v11
v_mfma_f32_16x16x32_bf16 v[54:57], a[64:67], v[94:97], v[54:57]
v_permlane16_swap_b32 v126, v128
v_mfma_f32_16x16x32_bf16 v[58:61], a[8:11], v[74:77], 0
ds_read_b128 v[114:117], v17 offset:10240
v_mfma_f32_16x16x32_bf16 v[58:61], a[20:23], v[78:81], v[58:61]
v_permlane16_swap_b32 v127, v129
v_mfma_f32_16x16x32_bf16 v[58:61], a[32:35], v[82:85], v[58:61]
ds_read_b128 v[118:121], v17 offset:11264
v_mfma_f32_16x16x32_bf16 v[58:61], a[44:47], v[86:89], v[58:61]
s_mov_b64 exec, s[76:77]
global_store_dwordx4 v12, v[126:129], s[38:39] sc1
s_mov_b64 exec, -1
v_mfma_f32_16x16x32_bf16 v[58:61], a[56:59], v[90:93], v[58:61]
v_add_u32 v12, 110592, v12
v_mfma_f32_16x16x32_bf16 v[58:61], a[68:71], v[94:97], v[58:61]
s_waitcnt lgkmcnt(5)
v_mfma_f32_16x16x32_bf16 v[50:53], a[72:75], v[98:101], v[50:53]
s_waitcnt lgkmcnt(4)
v_mfma_f32_16x16x32_bf16 v[50:53], a[84:87], v[102:105], v[50:53]
s_waitcnt lgkmcnt(3)
v_mfma_f32_16x16x32_bf16 v[50:53], a[96:99], v[106:109], v[50:53]
s_waitcnt lgkmcnt(2)
v_mfma_f32_16x16x32_bf16 v[50:53], a[108:111], v[110:113], v[50:53]
s_waitcnt lgkmcnt(1)
v_mfma_f32_16x16x32_bf16 v[50:53], a[120:123], v[114:117], v[50:53]
s_waitcnt lgkmcnt(0)
ds_read_b128 v[74:77], v17 offset:12288
v_mfma_f32_16x16x32_bf16 v[50:53], a[132:135], v[118:121], v[50:53]
v_mfma_f32_16x16x32_bf16 v[54:57], a[76:79], v[98:101], v[54:57]
ds_read_b128 v[78:81], v17 offset:13312
v_mfma_f32_16x16x32_bf16 v[54:57], a[88:91], v[102:105], v[54:57]
v_mfma_f32_16x16x32_bf16 v[54:57], a[100:103], v[106:109], v[54:57]
ds_read_b128 v[82:85], v17 offset:14336
v_mfma_f32_16x16x32_bf16 v[54:57], a[112:115], v[110:113], v[54:57]
v_mfma_f32_16x16x32_bf16 v[54:57], a[124:127], v[114:117], v[54:57]
ds_read_b128 v[86:89], v17 offset:15360
v_mfma_f32_16x16x32_bf16 v[54:57], a[136:139], v[118:121], v[54:57]
v_mfma_f32_16x16x32_bf16 v[58:61], a[80:83], v[98:101], v[58:61]
ds_read_b128 v[90:93], v17 offset:16384
v_mfma_f32_16x16x32_bf16 v[58:61], a[92:95], v[102:105], v[58:61]
v_mfma_f32_16x16x32_bf16 v[58:61], a[104:107], v[106:109], v[58:61]
ds_read_b128 v[94:97], v17 offset:17408
v_mfma_f32_16x16x32_bf16 v[58:61], a[116:119], v[110:113], v[58:61]
v_mfma_f32_16x16x32_bf16 v[58:61], a[128:131], v[114:117], v[58:61]
v_mfma_f32_16x16x32_bf16 v[58:61], a[140:143], v[118:121], v[58:61]
s_waitcnt lgkmcnt(5)
v_mfma_f32_16x16x32_bf16 v[50:53], a[144:147], v[74:77], v[50:53]
s_waitcnt lgkmcnt(4)
v_mfma_f32_16x16x32_bf16 v[50:53], a[156:159], v[78:81], v[50:53]
s_waitcnt lgkmcnt(3)
v_mfma_f32_16x16x32_bf16 v[50:53], a[168:171], v[82:85], v[50:53]
s_waitcnt lgkmcnt(2)
v_mfma_f32_16x16x32_bf16 v[50:53], a[180:183], v[86:89], v[50:53]
s_waitcnt lgkmcnt(1)
v_mfma_f32_16x16x32_bf16 v[50:53], a[192:195], v[90:93], v[50:53]
s_xor_b32 s43, s43, 0x10000
ds_read_b128 v[98:101], v17 offset:18432
s_waitcnt lgkmcnt(1)
v_mfma_f32_16x16x32_bf16 v[50:53], a[204:207], v[94:97], v[50:53]
s_add_u32 m0, s43, 0
v_mfma_f32_16x16x32_bf16 v[54:57], a[148:151], v[74:77], v[54:57]
ds_read_b128 v[102:105], v17 offset:19456
ds_read_b128 v[106:109], v17 offset:20480
v_mfma_f32_16x16x32_bf16 v[54:57], a[160:163], v[78:81], v[54:57]
v_mfma_f32_16x16x32_bf16 v[54:57], a[172:175], v[82:85], v[54:57]
s_barrier
v_mfma_f32_16x16x32_bf16 v[54:57], a[184:187], v[86:89], v[54:57]
v_mfma_f32_16x16x32_bf16 v[54:57], a[196:199], v[90:93], v[54:57]
ds_read_b128 v[110:113], v17 offset:21504
global_load_lds_dwordx4 v9, s[38:39] offset:-2048 sc1
v_mfma_f32_16x16x32_bf16 v[54:57], a[208:211], v[94:97], v[54:57]
v_mfma_f32_16x16x32_bf16 v[58:61], a[152:155], v[74:77], v[58:61]
ds_read_b128 v[114:117], v17 offset:22528
v_mfma_f32_16x16x32_bf16 v[58:61], a[164:167], v[78:81], v[58:61]
global_load_lds_dwordx4 v9, s[38:39] offset:-1024 sc1
v_mfma_f32_16x16x32_bf16 v[58:61], a[176:179], v[82:85], v[58:61]
ds_read_b128 v[118:121], v17 offset:23552
v_mfma_f32_16x16x32_bf16 v[58:61], a[188:191], v[86:89], v[58:61]
v_mfma_f32_16x16x32_bf16 v[58:61], a[200:203], v[90:93], v[58:61]
global_load_lds_dwordx4 v9, s[38:39] offset:0 sc1
v_mfma_f32_16x16x32_bf16 v[58:61], a[212:215], v[94:97], v[58:61]
s_waitcnt lgkmcnt(5)
v_mfma_f32_16x16x32_bf16 v[50:53], a[216:219], v[98:101], v[50:53]
s_waitcnt lgkmcnt(4)
v_mfma_f32_16x16x32_bf16 v[50:53], a[228:231], v[102:105], v[50:53]
s_waitcnt lgkmcnt(3)
global_load_lds_dwordx4 v9, s[38:39] offset:1024 sc1
v_mfma_f32_16x16x32_bf16 v[50:53], a[240:243], v[106:109], v[50:53]
s_waitcnt lgkmcnt(2)
v_mfma_f32_16x16x32_bf16 v[50:53], a[252:255], v[110:113], v[50:53]
s_waitcnt lgkmcnt(1)
v_mfma_f32_16x16x32_bf16 v[50:53], v[26:29], v[114:117], v[50:53]
s_waitcnt lgkmcnt(0)
global_load_lds_dwordx4 v9, s[38:39] offset:2048 sc1
v_mfma_f32_16x16x32_bf16 v[50:53], v[38:41], v[118:121], v[50:53]
v_mfma_f32_16x16x32_bf16 v[54:57], a[220:223], v[98:101], v[54:57]
v_mfma_f32_16x16x32_bf16 v[54:57], a[232:235], v[102:105], v[54:57]
v_mfma_f32_16x16x32_bf16 v[54:57], a[244:247], v[106:109], v[54:57]
s_waitcnt vmcnt(7)
s_barrier
v_mfma_f32_16x16x32_bf16 v[54:57], v[18:21], v[110:113], v[54:57]
ds_read_b128 v[74:77], v17 offset:24576
global_load_lds_dwordx4 v9, s[38:39] offset:3072 sc1
v_mfma_f32_16x16x32_bf16 v[54:57], v[30:33], v[114:117], v[54:57]
ds_read_b128 v[78:81], v17 offset:25600
v_add_u32 v9, 147456, v9
v_mfma_f32_16x16x32_bf16 v[54:57], v[42:45], v[118:121], v[54:57]
ds_read_b128 v[82:85], v17 offset:26624
v_min_u32 v9, v9, v10
v_mfma_f32_16x16x32_bf16 v[58:61], a[224:227], v[98:101], v[58:61]
ds_read_b128 v[86:89], v17 offset:27648
v_cvt_pk_bf16_f32 v122, v50, v51
v_mfma_f32_16x16x32_bf16 v[58:61], a[236:239], v[102:105], v[58:61]
ds_read_b128 v[90:93], v17 offset:28672
v_cvt_pk_bf16_f32 v123, v52, v53
v_mfma_f32_16x16x32_bf16 v[58:61], a[248:251], v[106:109], v[58:61]
ds_read_b128 v[94:97], v17 offset:29696
v_mfma_f32_16x16x32_bf16 v[58:61], v[22:25], v[110:113], v[58:61]
v_cvt_pk_bf16_f32 v124, v54, v55
v_cvt_pk_bf16_f32 v125, v56, v57
v_mfma_f32_16x16x32_bf16 v[58:61], v[34:37], v[114:117], v[58:61]
v_mfma_f32_16x16x32_bf16 v[58:61], v[46:49], v[118:121], v[58:61]
s_waitcnt lgkmcnt(5)
v_mfma_f32_16x16x32_bf16 v[62:65], a[0:3], v[74:77], 0
s_waitcnt lgkmcnt(4)
v_mfma_f32_16x16x32_bf16 v[62:65], a[12:15], v[78:81], v[62:65]
s_waitcnt lgkmcnt(3)
v_mfma_f32_16x16x32_bf16 v[62:65], a[24:27], v[82:85], v[62:65]
s_waitcnt lgkmcnt(2)
v_mfma_f32_16x16x32_bf16 v[62:65], a[36:39], v[86:89], v[62:65]
s_waitcnt lgkmcnt(1)
ds_read_b128 v[98:101], v17 offset:30720
v_cvt_pk_bf16_f32 v126, v58, v59
v_mfma_f32_16x16x32_bf16 v[62:65], a[48:51], v[90:93], v[62:65]
s_waitcnt lgkmcnt(1)
v_mfma_f32_16x16x32_bf16 v[62:65], a[60:63], v[94:97], v[62:65]
s_waitcnt vmcnt(6)
s_barrier
v_mfma_f32_16x16x32_bf16 v[66:69], a[4:7], v[74:77], 0
ds_read_b128 v[102:105], v17 offset:31744
v_cvt_pk_bf16_f32 v128, v58, v59
v_mfma_f32_16x16x32_bf16 v[66:69], a[16:19], v[78:81], v[66:69]
v_cvt_pk_bf16_f32 v127, v60, v61
v_cvt_pk_bf16_f32 v129, v60, v61
v_mfma_f32_16x16x32_bf16 v[66:69], a[28:31], v[82:85], v[66:69]
)"
R"(ds_read_b128 v[106:109], v17 offset:32768
v_mfma_f32_16x16x32_bf16 v[66:69], a[40:43], v[86:89], v[66:69]
global_store_dwordx4 v11, v[122:125], s[38:39] sc1
v_mfma_f32_16x16x32_bf16 v[66:69], a[52:55], v[90:93], v[66:69]
ds_read_b128 v[110:113], v17 offset:33792
v_add_u32 v11, 110592, v11
v_mfma_f32_16x16x32_bf16 v[66:69], a[64:67], v[94:97], v[66:69]
v_permlane16_swap_b32 v126, v128
v_mfma_f32_16x16x32_bf16 v[70:73], a[8:11], v[74:77], 0
ds_read_b128 v[114:117], v17 offset:34816
v_mfma_f32_16x16x32_bf16 v[70:73], a[20:23], v[78:81], v[70:73]
v_permlane16_swap_b32 v127, v129
v_mfma_f32_16x16x32_bf16 v[70:73], a[32:35], v[82:85], v[70:73]
ds_read_b128 v[118:121], v17 offset:35840
v_mfma_f32_16x16x32_bf16 v[70:73], a[44:47], v[86:89], v[70:73]
s_mov_b64 exec, s[76:77]
global_store_dwordx4 v12, v[126:129], s[38:39] sc1
s_mov_b64 exec, -1
v_mfma_f32_16x16x32_bf16 v[70:73], a[56:59], v[90:93], v[70:73]
v_add_u32 v12, 110592, v12
v_mfma_f32_16x16x32_bf16 v[70:73], a[68:71], v[94:97], v[70:73]
s_waitcnt lgkmcnt(5)
v_mfma_f32_16x16x32_bf16 v[62:65], a[72:75], v[98:101], v[62:65]
s_waitcnt lgkmcnt(4)
v_mfma_f32_16x16x32_bf16 v[62:65], a[84:87], v[102:105], v[62:65]
s_waitcnt lgkmcnt(3)
v_mfma_f32_16x16x32_bf16 v[62:65], a[96:99], v[106:109], v[62:65]
s_waitcnt lgkmcnt(2)
v_mfma_f32_16x16x32_bf16 v[62:65], a[108:111], v[110:113], v[62:65]
s_waitcnt lgkmcnt(1)
v_mfma_f32_16x16x32_bf16 v[62:65], a[120:123], v[114:117], v[62:65]
s_waitcnt lgkmcnt(0)
ds_read_b128 v[74:77], v17 offset:36864
v_mfma_f32_16x16x32_bf16 v[62:65], a[132:135], v[118:121], v[62:65]
v_mfma_f32_16x16x32_bf16 v[66:69], a[76:79], v[98:101], v[66:69]
ds_read_b128 v[78:81], v17 offset:37888
v_mfma_f32_16x16x32_bf16 v[66:69], a[88:91], v[102:105], v[66:69]
v_mfma_f32_16x16x32_bf16 v[66:69], a[100:103], v[106:109], v[66:69]
ds_read_b128 v[82:85], v17 offset:38912
v_mfma_f32_16x16x32_bf16 v[66:69], a[112:115], v[110:113], v[66:69]
v_mfma_f32_16x16x32_bf16 v[66:69], a[124:127], v[114:117], v[66:69]
ds_read_b128 v[86:89], v17 offset:39936
v_mfma_f32_16x16x32_bf16 v[66:69], a[136:139], v[118:121], v[66:69]
v_mfma_f32_16x16x32_bf16 v[70:73], a[80:83], v[98:101], v[70:73]
ds_read_b128 v[90:93], v17 offset:40960
v_mfma_f32_16x16x32_bf16 v[70:73], a[92:95], v[102:105], v[70:73]
v_mfma_f32_16x16x32_bf16 v[70:73], a[104:107], v[106:109], v[70:73]
ds_read_b128 v[94:97], v17 offset:41984
v_mfma_f32_16x16x32_bf16 v[70:73], a[116:119], v[110:113], v[70:73]
v_mfma_f32_16x16x32_bf16 v[70:73], a[128:131], v[114:117], v[70:73]
v_mfma_f32_16x16x32_bf16 v[70:73], a[140:143], v[118:121], v[70:73]
s_waitcnt lgkmcnt(5)
v_mfma_f32_16x16x32_bf16 v[62:65], a[144:147], v[74:77], v[62:65]
s_waitcnt lgkmcnt(4)
v_mfma_f32_16x16x32_bf16 v[62:65], a[156:159], v[78:81], v[62:65]
s_waitcnt lgkmcnt(3)
v_mfma_f32_16x16x32_bf16 v[62:65], a[168:171], v[82:85], v[62:65]
s_waitcnt lgkmcnt(2)
v_mfma_f32_16x16x32_bf16 v[62:65], a[180:183], v[86:89], v[62:65]
s_waitcnt lgkmcnt(1)
v_mfma_f32_16x16x32_bf16 v[62:65], a[192:195], v[90:93], v[62:65]
s_waitcnt lgkmcnt(0)
ds_read_b128 v[98:101], v17 offset:43008
v_mfma_f32_16x16x32_bf16 v[62:65], a[204:207], v[94:97], v[62:65]
s_add_u32 m0, s43, 24576
v_mfma_f32_16x16x32_bf16 v[66:69], a[148:151], v[74:77], v[66:69]
ds_read_b128 v[102:105], v17 offset:44032
ds_read_b128 v[106:109], v17 offset:45056
v_mfma_f32_16x16x32_bf16 v[66:69], a[160:163], v[78:81], v[66:69]
v_mfma_f32_16x16x32_bf16 v[66:69], a[172:175], v[82:85], v[66:69]
s_barrier
v_mfma_f32_16x16x32_bf16 v[66:69], a[184:187], v[86:89], v[66:69]
v_mfma_f32_16x16x32_bf16 v[66:69], a[196:199], v[90:93], v[66:69]
ds_read_b128 v[110:113], v17 offset:46080
global_load_lds_dwordx4 v9, s[38:39] offset:-2048 sc1
v_mfma_f32_16x16x32_bf16 v[66:69], a[208:211], v[94:97], v[66:69]
v_mfma_f32_16x16x32_bf16 v[70:73], a[152:155], v[74:77], v[70:73]
ds_read_b128 v[114:117], v17 offset:47104
v_mfma_f32_16x16x32_bf16 v[70:73], a[164:167], v[78:81], v[70:73]
global_load_lds_dwordx4 v9, s[38:39] offset:-1024 sc1
v_mfma_f32_16x16x32_bf16 v[70:73], a[176:179], v[82:85], v[70:73]
ds_read_b128 v[118:121], v17 offset:48128
v_mfma_f32_16x16x32_bf16 v[70:73], a[188:191], v[86:89], v[70:73]
v_mfma_f32_16x16x32_bf16 v[70:73], a[200:203], v[90:93], v[70:73]
global_load_lds_dwordx4 v9, s[38:39] offset:0 sc1
v_mfma_f32_16x16x32_bf16 v[70:73], a[212:215], v[94:97], v[70:73]
s_waitcnt lgkmcnt(5)
v_mfma_f32_16x16x32_bf16 v[62:65], a[216:219], v[98:101], v[62:65]
s_waitcnt lgkmcnt(4)
v_mfma_f32_16x16x32_bf16 v[62:65], a[228:231], v[102:105], v[62:65]
s_waitcnt lgkmcnt(3)
global_load_lds_dwordx4 v9, s[38:39] offset:1024 sc1
v_mfma_f32_16x16x32_bf16 v[62:65], a[240:243], v[106:109], v[62:65]
s_waitcnt lgkmcnt(2)
v_mfma_f32_16x16x32_bf16 v[62:65], a[252:255], v[110:113], v[62:65]
s_waitcnt lgkmcnt(1)
v_mfma_f32_16x16x32_bf16 v[62:65], v[26:29], v[114:117], v[62:65]
s_waitcnt lgkmcnt(0)
global_load_lds_dwordx4 v9, s[38:39] offset:2048 sc1
v_mfma_f32_16x16x32_bf16 v[62:65], v[38:41], v[118:121], v[62:65]
v_mfma_f32_16x16x32_bf16 v[66:69], a[220:223], v[98:101], v[66:69]
v_xor_b32 v17, 0x10000, v17
v_mfma_f32_16x16x32_bf16 v[66:69], a[232:235], v[102:105], v[66:69]
v_mfma_f32_16x16x32_bf16 v[66:69], a[244:247], v[106:109], v[66:69]
s_waitcnt vmcnt(7)
s_barrier
v_mfma_f32_16x16x32_bf16 v[66:69], v[18:21], v[110:113], v[66:69]
ds_read_b128 v[74:77], v17 offset:0
global_load_lds_dwordx4 v9, s[38:39] offset:3072 sc1
v_mfma_f32_16x16x32_bf16 v[66:69], v[30:33], v[114:117], v[66:69]
ds_read_b128 v[78:81], v17 offset:1024
v_add_u32 v9, 147456, v9
v_mfma_f32_16x16x32_bf16 v[66:69], v[42:45], v[118:121], v[66:69]
ds_read_b128 v[82:85], v17 offset:2048
v_min_u32 v9, v9, v10
v_mfma_f32_16x16x32_bf16 v[70:73], a[224:227], v[98:101], v[70:73]
ds_read_b128 v[86:89], v17 offset:3072
v_cvt_pk_bf16_f32 v122, v62, v63
v_mfma_f32_16x16x32_bf16 v[70:73], a[236:239], v[102:105], v[70:73]
ds_read_b128 v[90:93], v17 offset:4096
v_cvt_pk_bf16_f32 v123, v64, v65
v_mfma_f32_16x16x32_bf16 v[70:73], a[248:251], v[106:109], v[70:73]
ds_read_b128 v[94:97], v17 offset:5120
v_mfma_f32_16x16x32_bf16 v[70:73], v[22:25], v[110:113], v[70:73]
v_cvt_pk_bf16_f32 v124, v66, v67
v_cvt_pk_bf16_f32 v125, v68, v69
v_mfma_f32_16x16x32_bf16 v[70:73], v[34:37], v[114:117], v[70:73]
v_mfma_f32_16x16x32_bf16 v[70:73], v[46:49], v[118:121], v[70:73]
s_sub_u32 s70, s70, 1
s_cbranch_scc0 .Ldn_top6
v_cvt_pk_bf16_f32 v122, v62, v63
v_cvt_pk_bf16_f32 v123, v64, v65
v_cvt_pk_bf16_f32 v124, v66, v67
v_cvt_pk_bf16_f32 v125, v68, v69
global_store_dwordx4 v11, v[122:125], s[38:39] sc1
v_add_u32 v11, 110592, v11
v_cvt_pk_bf16_f32 v126, v70, v71
v_cvt_pk_bf16_f32 v128, v70, v71
v_cvt_pk_bf16_f32 v127, v72, v73
v_cvt_pk_bf16_f32 v129, v72, v73
v_permlane16_swap_b32 v126, v128
s_nop 0
v_permlane16_swap_b32 v127, v129
s_mov_b64 exec, s[76:77]
global_store_dwordx4 v12, v[126:129], s[38:39] sc1
s_mov_b64 exec, -1
v_add_u32 v12, 110592, v12
s_waitcnt vmcnt(2)
s_barrier
s_waitcnt vmcnt(0)
s_barrier
s_endpgm
.Ldn_w3:
v_add_u32 v254, -1, v14
s_mov_b32 s76, 0xffff
s_mov_b32 s77, 0xffff
s_mov_b32 s70, 32
s_waitcnt vmcnt(0) lgkmcnt(0)
s_nop 7
.p2align 6
.Ldn_top7:
s_waitcnt lgkmcnt(5)
v_mfma_f32_16x16x32_bf16 v[50:53], a[0:3], v[74:77], 0
s_waitcnt lgkmcnt(4)
v_mfma_f32_16x16x32_bf16 v[50:53], a[12:15], v[78:81], v[50:53]
s_waitcnt lgkmcnt(3)
v_mfma_f32_16x16x32_bf16 v[50:53], a[24:27], v[82:85], v[50:53]
s_waitcnt lgkmcnt(2)
v_mfma_f32_16x16x32_bf16 v[50:53], a[36:39], v[86:89], v[50:53]
s_waitcnt lgkmcnt(1)
ds_read_b128 v[98:101], v17 offset:6144
v_cvt_pk_bf16_f32 v126, v70, v71
v_mfma_f32_16x16x32_bf16 v[50:53], a[48:51], v[90:93], v[50:53]
s_waitcnt lgkmcnt(1)
v_mfma_f32_16x16x32_bf16 v[50:53], a[60:63], v[94:97], v[50:53]
s_waitcnt vmcnt(7)
s_barrier
v_mfma_f32_16x16x32_bf16 v[54:57], a[4:7], v[74:77], 0
ds_read_b128 v[102:105], v17 offset:7168
v_cvt_pk_bf16_f32 v128, v70, v71
v_mfma_f32_16x16x32_bf16 v[54:57], a[16:19], v[78:81], v[54:57]
s_mov_b64 exec, 1
global_store_dword v15, v16, s[38:39] sc1
s_mov_b64 exec, -1
v_mfma_f32_16x16x32_bf16 v[54:57], a[28:31], v[82:85], v[54:57]
ds_read_b128 v[106:109], v17 offset:8192
v_add_u32 v16, 1, v16
v_mfma_f32_16x16x32_bf16 v[54:57], a[40:43], v[86:89], v[54:57]
global_store_dwordx4 v11, v[122:125], s[38:39] sc1
v_mfma_f32_16x16x32_bf16 v[54:57], a[52:55], v[90:93], v[54:57]
ds_read_b128 v[110:113], v17 offset:9216
v_add_u32 v11, 110592, v11
v_mfma_f32_16x16x32_bf16 v[54:57], a[64:67], v[94:97], v[54:57]
v_cvt_pk_bf16_f32 v127, v72, v73
v_cvt_pk_bf16_f32 v129, v72, v73
v_mfma_f32_16x16x32_bf16 v[58:61], a[8:11], v[74:77], 0
ds_read_b128 v[114:117], v17 offset:10240
v_mfma_f32_16x16x32_bf16 v[58:61], a[20:23], v[78:81], v[58:61]
v_permlane16_swap_b32 v126, v128
v_mfma_f32_16x16x32_bf16 v[58:61], a[32:35], v[82:85], v[58:61]
ds_read_b128 v[118:121], v17 offset:11264
v_mfma_f32_16x16x32_bf16 v[58:61], a[44:47], v[86:89], v[58:61]
v_permlane16_swap_b32 v127, v129
v_mfma_f32_16x16x32_bf16 v[58:61], a[56:59], v[90:93], v[58:61]
s_mov_b64 exec, s[76:77]
global_store_dwordx4 v12, v[126:129], s[38:39] sc1
s_mov_b64 exec, -1
v_mfma_f32_16x16x32_bf16 v[58:61], a[68:71], v[94:97], v[58:61]
s_waitcnt lgkmcnt(5)
v_add_u32 v12, 110592, v12
v_mfma_f32_16x16x32_bf16 v[50:53], a[72:75], v[98:101], v[50:53]
s_waitcnt lgkmcnt(4)
v_mfma_f32_16x16x32_bf16 v[50:53], a[84:87], v[102:105], v[50:53]
s_waitcnt lgkmcnt(3)
v_mfma_f32_16x16x32_bf16 v[50:53], a[96:99], v[106:109], v[50:53]
s_waitcnt lgkmcnt(2)
v_mfma_f32_16x16x32_bf16 v[50:53], a[108:111], v[110:113], v[50:53]
s_waitcnt lgkmcnt(1)
v_mfma_f32_16x16x32_bf16 v[50:53], a[120:123], v[114:117], v[50:53]
s_waitcnt lgkmcnt(0)
ds_read_b128 v[74:77], v17 offset:12288
v_mfma_f32_16x16x32_bf16 v[50:53], a[132:135], v[118:121], v[50:53]
v_mfma_f32_16x16x32_bf16 v[54:57], a[76:79], v[98:101], v[54:57]
ds_read_b128 v[78:81], v17 offset:13312
v_mfma_f32_16x16x32_bf16 v[54:57], a[88:91], v[102:105], v[54:57]
v_mfma_f32_16x16x32_bf16 v[54:57], a[100:103], v[106:109], v[54:57]
ds_read_b128 v[82:85], v17 offset:14336
v_mfma_f32_16x16x32_bf16 v[54:57], a[112:115], v[110:113], v[54:57]
v_mfma_f32_16x16x32_bf16 v[54:57], a[124:127], v[114:117], v[54:57]
ds_read_b128 v[86:89], v17 offset:15360
v_mfma_f32_16x16x32_bf16 v[54:57], a[136:139], v[118:121], v[54:57]
v_mfma_f32_16x16x32_bf16 v[58:61], a[80:83], v[98:101], v[58:61]
ds_read_b128 v[90:93], v17 offset:16384
v_mfma_f32_16x16x32_bf16 v[58:61], a[92:95], v[102:105], v[58:61]
v_mfma_f32_16x16x32_bf16 v[58:61], a[104:107], v[106:109], v[58:61]
ds_read_b128 v[94:97], v17 offset:17408
v_mfma_f32_16x16x32_bf16 v[58:61], a[116:119], v[110:113], v[58:61]
v_mfma_f32_16x16x32_bf16 v[58:61], a[128:131], v[114:117], v[58:61]
v_mfma_f32_16x16x32_bf16 v[58:61], a[140:143], v[118:121], v[58:61]
s_waitcnt lgkmcnt(5)
v_mfma_f32_16x16x32_bf16 v[50:53], a[144:147], v[74:77], v[50:53]
s_waitcnt lgkmcnt(4)
v_mfma_f32_16x16x32_bf16 v[50:53], a[156:159], v[78:81], v[50:53]
s_waitcnt lgkmcnt(3)
v_mfma_f32_16x16x32_bf16 v[50:53], a[168:171], v[82:85], v[50:53]
s_waitcnt lgkmcnt(2)
v_mfma_f32_16x16x32_bf16 v[50:53], a[180:183], v[86:89], v[50:53]
s_waitcnt lgkmcnt(1)
v_mfma_f32_16x16x32_bf16 v[50:53], a[192:195], v[90:93], v[50:53]
s_xor_b32 s43, s43, 0x10000
ds_read_b128 v[98:101], v17 offset:18432
s_waitcnt lgkmcnt(1)
v_mfma_f32_16x16x32_bf16 v[50:53], a[204:207], v[94:97], v[50:53]
s_add_u32 m0, s43, 0
v_mfma_f32_16x16x32_bf16 v[54:57], a[148:151], v[74:77], v[54:57]
ds_read_b128 v[102:105], v17 offset:19456
ds_read_b128 v[106:109], v17 offset:20480
v_mfma_f32_16x16x32_bf16 v[54:57], a[160:163], v[78:81], v[54:57]
v_mfma_f32_16x16x32_bf16 v[54:57], a[172:175], v[82:85], v[54:57]
s_barrier
v_mfma_f32_16x16x32_bf16 v[54:57], a[184:187], v[86:89], v[54:57]
v_mfma_f32_16x16x32_bf16 v[54:57], a[196:199], v[90:93], v[54:57]
ds_read_b128 v[110:113], v17 offset:21504
v_mfma_f32_16x16x32_bf16 v[54:57], a[208:211], v[94:97], v[54:57]
global_load_lds_dwordx4 v9, s[38:39] offset:-2048 sc1
v_mfma_f32_16x16x32_bf16 v[58:61], a[152:155], v[74:77], v[58:61]
ds_read_b128 v[114:117], v17 offset:22528
v_mfma_f32_16x16x32_bf16 v[58:61], a[164:167], v[78:81], v[58:61]
v_mfma_f32_16x16x32_bf16 v[58:61], a[176:179], v[82:85], v[58:61]
ds_read_b128 v[118:121], v17 offset:23552
global_load_lds_dwordx4 v9, s[38:39] offset:-1024 sc1
v_mfma_f32_16x16x32_bf16 v[58:61], a[188:191], v[86:89], v[58:61]
v_mfma_f32_16x16x32_bf16 v[58:61], a[200:203], v[90:93], v[58:61]
v_mfma_f32_16x16x32_bf16 v[58:61], a[212:215], v[94:97], v[58:61]
s_waitcnt lgkmcnt(5)
global_load_lds_dwordx4 v9, s[38:39] offset:0 sc1
v_mfma_f32_16x16x32_bf16 v[50:53], a[216:219], v[98:101], v[50:53]
s_waitcnt lgkmcnt(4)
v_mfma_f32_16x16x32_bf16 v[50:53], a[228:231], v[102:105], v[50:53]
s_waitcnt lgkmcnt(3)
v_mfma_f32_16x16x32_bf16 v[50:53], a[240:243], v[106:109], v[50:53]
s_waitcnt lgkmcnt(2)
global_load_lds_dwordx4 v9, s[38:39] offset:1024 sc1
v_mfma_f32_16x16x32_bf16 v[50:53], a[252:255], v[110:113], v[50:53]
s_waitcnt lgkmcnt(1)
v_mfma_f32_16x16x32_bf16 v[50:53], v[26:29], v[114:117], v[50:53]
s_waitcnt lgkmcnt(0)
v_mfma_f32_16x16x32_bf16 v[50:53], v[38:41], v[118:121], v[50:53]
global_load_lds_dwordx4 v9, s[38:39] offset:2048 sc1
v_mfma_f32_16x16x32_bf16 v[54:57], a[220:223], v[98:101], v[54:57]
v_mfma_f32_16x16x32_bf16 v[54:57], a[232:235], v[102:105], v[54:57]
v_mfma_f32_16x16x32_bf16 v[54:57], a[244:247], v[106:109], v[54:57]
s_waitcnt vmcnt(8)
s_barrier
v_mfma_f32_16x16x32_bf16 v[54:57], v[18:21], v[110:113], v[54:57]
ds_read_b128 v[74:77], v17 offset:24576
global_load_lds_dwordx4 v9, s[38:39] offset:3072 sc1
v_mfma_f32_16x16x32_bf16 v[54:57], v[30:33], v[114:117], v[54:57]
ds_read_b128 v[78:81], v17 offset:25600
v_add_u32 v9, 147456, v9
v_mfma_f32_16x16x32_bf16 v[54:57], v[42:45], v[118:121], v[54:57]
ds_read_b128 v[82:85], v17 offset:26624
v_min_u32 v9, v9, v10
v_mfma_f32_16x16x32_bf16 v[58:61], a[224:227], v[98:101], v[58:61]
ds_read_b128 v[86:89], v17 offset:27648
v_cvt_pk_bf16_f32 v122, v50, v51
v_mfma_f32_16x16x32_bf16 v[58:61], a[236:239], v[102:105], v[58:61]
ds_read_b128 v[90:93], v17 offset:28672
v_cvt_pk_bf16_f32 v123, v52, v53
v_mfma_f32_16x16x32_bf16 v[58:61], a[248:251], v[106:109], v[58:61]
ds_read_b128 v[94:97], v17 offset:29696
v_mfma_f32_16x16x32_bf16 v[58:61], v[22:25], v[110:113], v[58:61]
v_cvt_pk_bf16_f32 v124, v54, v55
v_cvt_pk_bf16_f32 v125, v56, v57
v_mfma_f32_16x16x32_bf16 v[58:61], v[34:37], v[114:117], v[58:61]
v_mfma_f32_16x16x32_bf16 v[58:61], v[46:49], v[118:121], v[58:61]
s_waitcnt lgkmcnt(5)
v_mfma_f32_16x16x32_bf16 v[62:65], a[0:3], v[74:77], 0
s_waitcnt lgkmcnt(4)
v_mfma_f32_16x16x32_bf16 v[62:65], a[12:15], v[78:81], v[62:65]
s_waitcnt lgkmcnt(3)
v_mfma_f32_16x16x32_bf16 v[62:65], a[24:27], v[82:85], v[62:65]
s_waitcnt lgkmcnt(2)
v_mfma_f32_16x16x32_bf16 v[62:65], a[36:39], v[86:89], v[62:65]
s_waitcnt lgkmcnt(1)
ds_read_b128 v[98:101], v17 offset:30720
v_cvt_pk_bf16_f32 v126, v58, v59
v_mfma_f32_16x16x32_bf16 v[62:65], a[48:51], v[90:93], v[62:65]
s_waitcnt lgkmcnt(1)
v_mfma_f32_16x16x32_bf16 v[62:65], a[60:63], v[94:97], v[62:65]
s_waitcnt vmcnt(6)
s_barrier
v_mfma_f32_16x16x32_bf16 v[66:69], a[4:7], v[74:77], 0
ds_read_b128 v[102:105], v17 offset:31744
v_cvt_pk_bf16_f32 v128, v58, v59
v_mfma_f32_16x16x32_bf16 v[66:69], a[16:19], v[78:81], v[66:69]
v_cvt_pk_bf16_f32 v127, v60, v61
v_cvt_pk_bf16_f32 v129, v60, v61
v_mfma_f32_16x16x32_bf16 v[66:69], a[28:31], v[82:85], v[66:69]
ds_read_b128 v[106:109], v17 offset:32768
v_mfma_f32_16x16x32_bf16 v[66:69], a[40:43], v[86:89], v[66:69]
global_store_dwordx4 v11, v[122:125], s[38:39] sc1
v_mfma_f32_16x16x32_bf16 v[66:69], a[52:55], v[90:93], v[66:69]
ds_read_b128 v[110:113], v17 offset:33792
v_add_u32 v11, 110592, v11
v_mfma_f32_16x16x32_bf16 v[66:69], a[64:67], v[94:97], v[66:69]
v_permlane16_swap_b32 v126, v128
v_mfma_f32_16x16x32_bf16 v[70:73], a[8:11], v[74:77], 0
ds_read_b128 v[114:117], v17 offset:34816
v_mfma_f32_16x16x32_bf16 v[70:73], a[20:23], v[78:81], v[70:73]
v_permlane16_swap_b32 v127, v129
v_mfma_f32_16x16x32_bf16 v[70:73], a[32:35], v[82:85], v[70:73]
ds_read_b128 v[118:121], v17 offset:35840
v_mfma_f32_16x16x32_bf16 v[70:73], a[44:47], v[86:89], v[70:73]
s_mov_b64 exec, s[76:77]
global_store_dwordx4 v12, v[126:129], s[38:39] sc1
s_mov_b64 exec, -1
v_mfma_f32_16x16x32_bf16 v[70:73], a[56:59], v[90:93], v[70:73]
v_add_u32 v12, 110592, v12
v_mfma_f32_16x16x32_bf16 v[70:73], a[68:71], v[94:97], v[70:73]
s_waitcnt lgkmcnt(5)
s_mov_b64 exec, 1
global_store_dword v15, v16, s[38:39] sc1
s_mov_b64 exec, -1
v_mfma_f32_16x16x32_bf16 v[62:65], a[72:75], v[98:101], v[62:65]
s_waitcnt lgkmcnt(4)
v_add_u32 v16, 1, v16
v_mfma_f32_16x16x32_bf16 v[62:65], a[84:87], v[102:105], v[62:65]
s_waitcnt lgkmcnt(3)
v_mfma_f32_16x16x32_bf16 v[62:65], a[96:99], v[106:109], v[62:65]
s_waitcnt lgkmcnt(2)
v_mfma_f32_16x16x32_bf16 v[62:65], a[108:111], v[110:113], v[62:65]
s_waitcnt lgkmcnt(1)
v_mfma_f32_16x16x32_bf16 v[62:65], a[120:123], v[114:117], v[62:65]
s_waitcnt lgkmcnt(0)
ds_read_b128 v[74:77], v17 offset:36864
v_mfma_f32_16x16x32_bf16 v[62:65], a[132:135], v[118:121], v[62:65]
v_mfma_f32_16x16x32_bf16 v[66:69], a[76:79], v[98:101], v[66:69]
ds_read_b128 v[78:81], v17 offset:37888
v_mfma_f32_16x16x32_bf16 v[66:69], a[88:91], v[102:105], v[66:69]
v_mfma_f32_16x16x32_bf16 v[66:69], a[100:103], v[106:109], v[66:69]
ds_read_b128 v[82:85], v17 offset:38912
v_mfma_f32_16x16x32_bf16 v[66:69], a[112:115], v[110:113], v[66:69]
v_mfma_f32_16x16x32_bf16 v[66:69], a[124:127], v[114:117], v[66:69]
ds_read_b128 v[86:89], v17 offset:39936
v_mfma_f32_16x16x32_bf16 v[66:69], a[136:139], v[118:121], v[66:69]
v_mfma_f32_16x16x32_bf16 v[70:73], a[80:83], v[98:101], v[70:73]
ds_read_b128 v[90:93], v17 offset:40960
v_mfma_f32_16x16x32_bf16 v[70:73], a[92:95], v[102:105], v[70:73]
v_mfma_f32_16x16x32_bf16 v[70:73], a[104:107], v[106:109], v[70:73]
ds_read_b128 v[94:97], v17 offset:41984
v_mfma_f32_16x16x32_bf16 v[70:73], a[116:119], v[110:113], v[70:73]
v_mfma_f32_16x16x32_bf16 v[70:73], a[128:131], v[114:117], v[70:73]
v_mfma_f32_16x16x32_bf16 v[70:73], a[140:143], v[118:121], v[70:73]
s_waitcnt lgkmcnt(5)
v_mfma_f32_16x16x32_bf16 v[62:65], a[144:147], v[74:77], v[62:65]
s_waitcnt lgkmcnt(4)
v_mfma_f32_16x16x32_bf16 v[62:65], a[156:159], v[78:81], v[62:65]
s_waitcnt lgkmcnt(3)
v_mfma_f32_16x16x32_bf16 v[62:65], a[168:171], v[82:85], v[62:65]
)"
R"(s_waitcnt lgkmcnt(2)
v_mfma_f32_16x16x32_bf16 v[62:65], a[180:183], v[86:89], v[62:65]
s_waitcnt lgkmcnt(1)
v_mfma_f32_16x16x32_bf16 v[62:65], a[192:195], v[90:93], v[62:65]
s_waitcnt lgkmcnt(0)
ds_read_b128 v[98:101], v17 offset:43008
v_mfma_f32_16x16x32_bf16 v[62:65], a[204:207], v[94:97], v[62:65]
s_add_u32 m0, s43, 24576
v_mfma_f32_16x16x32_bf16 v[66:69], a[148:151], v[74:77], v[66:69]
ds_read_b128 v[102:105], v17 offset:44032
ds_read_b128 v[106:109], v17 offset:45056
v_mfma_f32_16x16x32_bf16 v[66:69], a[160:163], v[78:81], v[66:69]
v_mfma_f32_16x16x32_bf16 v[66:69], a[172:175], v[82:85], v[66:69]
s_barrier
v_mfma_f32_16x16x32_bf16 v[66:69], a[184:187], v[86:89], v[66:69]
v_mfma_f32_16x16x32_bf16 v[66:69], a[196:199], v[90:93], v[66:69]
ds_read_b128 v[110:113], v17 offset:46080
v_mfma_f32_16x16x32_bf16 v[66:69], a[208:211], v[94:97], v[66:69]
global_load_lds_dwordx4 v9, s[38:39] offset:-2048 sc1
v_mfma_f32_16x16x32_bf16 v[70:73], a[152:155], v[74:77], v[70:73]
ds_read_b128 v[114:117], v17 offset:47104
v_mfma_f32_16x16x32_bf16 v[70:73], a[164:167], v[78:81], v[70:73]
v_mfma_f32_16x16x32_bf16 v[70:73], a[176:179], v[82:85], v[70:73]
ds_read_b128 v[118:121], v17 offset:48128
global_load_lds_dwordx4 v9, s[38:39] offset:-1024 sc1
v_mfma_f32_16x16x32_bf16 v[70:73], a[188:191], v[86:89], v[70:73]
v_mfma_f32_16x16x32_bf16 v[70:73], a[200:203], v[90:93], v[70:73]
v_mfma_f32_16x16x32_bf16 v[70:73], a[212:215], v[94:97], v[70:73]
s_waitcnt lgkmcnt(5)
global_load_lds_dwordx4 v9, s[38:39] offset:0 sc1
v_mfma_f32_16x16x32_bf16 v[62:65], a[216:219], v[98:101], v[62:65]
s_waitcnt lgkmcnt(4)
v_mfma_f32_16x16x32_bf16 v[62:65], a[228:231], v[102:105], v[62:65]
s_waitcnt lgkmcnt(3)
v_mfma_f32_16x16x32_bf16 v[62:65], a[240:243], v[106:109], v[62:65]
s_waitcnt lgkmcnt(2)
global_load_lds_dwordx4 v9, s[38:39] offset:1024 sc1
v_mfma_f32_16x16x32_bf16 v[62:65], a[252:255], v[110:113], v[62:65]
s_waitcnt lgkmcnt(1)
v_mfma_f32_16x16x32_bf16 v[62:65], v[26:29], v[114:117], v[62:65]
s_waitcnt lgkmcnt(0)
v_mfma_f32_16x16x32_bf16 v[62:65], v[38:41], v[118:121], v[62:65]
global_load_lds_dwordx4 v9, s[38:39] offset:2048 sc1
v_mfma_f32_16x16x32_bf16 v[66:69], a[220:223], v[98:101], v[66:69]
v_xor_b32 v17, 0x10000, v17
v_mfma_f32_16x16x32_bf16 v[66:69], a[232:235], v[102:105], v[66:69]
v_mfma_f32_16x16x32_bf16 v[66:69], a[244:247], v[106:109], v[66:69]
s_waitcnt vmcnt(8)
s_barrier
v_mfma_f32_16x16x32_bf16 v[66:69], v[18:21], v[110:113], v[66:69]
ds_read_b128 v[74:77], v17 offset:0
global_load_lds_dwordx4 v9, s[38:39] offset:3072 sc1
v_mfma_f32_16x16x32_bf16 v[66:69], v[30:33], v[114:117], v[66:69]
ds_read_b128 v[78:81], v17 offset:1024
v_add_u32 v9, 147456, v9
v_mfma_f32_16x16x32_bf16 v[66:69], v[42:45], v[118:121], v[66:69]
ds_read_b128 v[82:85], v17 offset:2048
v_min_u32 v9, v9, v10
v_mfma_f32_16x16x32_bf16 v[70:73], a[224:227], v[98:101], v[70:73]
ds_read_b128 v[86:89], v17 offset:3072
v_cvt_pk_bf16_f32 v122, v62, v63
v_mfma_f32_16x16x32_bf16 v[70:73], a[236:239], v[102:105], v[70:73]
ds_read_b128 v[90:93], v17 offset:4096
v_cvt_pk_bf16_f32 v123, v64, v65
v_mfma_f32_16x16x32_bf16 v[70:73], a[248:251], v[106:109], v[70:73]
ds_read_b128 v[94:97], v17 offset:5120
v_mfma_f32_16x16x32_bf16 v[70:73], v[22:25], v[110:113], v[70:73]
v_cvt_pk_bf16_f32 v124, v66, v67
v_cvt_pk_bf16_f32 v125, v68, v69
v_mfma_f32_16x16x32_bf16 v[70:73], v[34:37], v[114:117], v[70:73]
v_mfma_f32_16x16x32_bf16 v[70:73], v[46:49], v[118:121], v[70:73]
s_sub_u32 s70, s70, 1
s_cbranch_scc0 .Ldn_top7
v_cvt_pk_bf16_f32 v122, v62, v63
v_cvt_pk_bf16_f32 v123, v64, v65
v_cvt_pk_bf16_f32 v124, v66, v67
v_cvt_pk_bf16_f32 v125, v68, v69
global_store_dwordx4 v11, v[122:125], s[38:39] sc1
v_add_u32 v11, 110592, v11
v_cvt_pk_bf16_f32 v126, v70, v71
v_cvt_pk_bf16_f32 v128, v70, v71
v_cvt_pk_bf16_f32 v127, v72, v73
v_cvt_pk_bf16_f32 v129, v72, v73
v_permlane16_swap_b32 v126, v128
s_nop 0
v_permlane16_swap_b32 v127, v129
s_mov_b64 exec, s[76:77]
global_store_dwordx4 v12, v[126:129], s[38:39] sc1
s_mov_b64 exec, -1
v_add_u32 v12, 110592, v12
s_waitcnt vmcnt(2)
s_barrier
s_mov_b64 exec, 1
global_store_dword v15, v16, s[38:39] sc1
s_mov_b64 exec, -1
v_add_u32 v16, 1, v16
s_waitcnt vmcnt(1)
s_barrier
s_mov_b64 exec, 1
global_store_dword v15, v16, s[38:39] sc1
s_mov_b64 exec, -1
s_endpgm
.Ldn_w4:
v_add_u32 v254, -1, v14
s_mov_b32 s76, 0xffff
s_mov_b32 s77, 0xffff
s_mov_b32 s70, 32
s_waitcnt vmcnt(0) lgkmcnt(0)
s_nop 7
.p2align 6
.Ldn_top12:
s_waitcnt lgkmcnt(5)
v_mfma_f32_16x16x32_bf16 v[50:53], a[0:3], v[74:77], 0
s_waitcnt lgkmcnt(4)
v_mfma_f32_16x16x32_bf16 v[50:53], a[12:15], v[78:81], v[50:53]
s_waitcnt lgkmcnt(3)
global_load_dword v254, v13, s[38:39] sc1
v_mfma_f32_16x16x32_bf16 v[50:53], a[24:27], v[82:85], v[50:53]
s_waitcnt lgkmcnt(2)
v_mfma_f32_16x16x32_bf16 v[50:53], a[36:39], v[86:89], v[50:53]
s_waitcnt lgkmcnt(1)
ds_read_b128 v[98:101], v17 offset:6144
v_cvt_pk_bf16_f32 v126, v70, v71
v_mfma_f32_16x16x32_bf16 v[50:53], a[48:51], v[90:93], v[50:53]
s_waitcnt lgkmcnt(1)
v_mfma_f32_16x16x32_bf16 v[50:53], a[60:63], v[94:97], v[50:53]
s_waitcnt vmcnt(7)
s_barrier
v_mfma_f32_16x16x32_bf16 v[54:57], a[4:7], v[74:77], 0
ds_read_b128 v[102:105], v17 offset:7168
v_cvt_pk_bf16_f32 v128, v70, v71
v_mfma_f32_16x16x32_bf16 v[54:57], a[16:19], v[78:81], v[54:57]
v_cvt_pk_bf16_f32 v127, v72, v73
v_cvt_pk_bf16_f32 v129, v72, v73
v_mfma_f32_16x16x32_bf16 v[54:57], a[28:31], v[82:85], v[54:57]
ds_read_b128 v[106:109], v17 offset:8192
v_mfma_f32_16x16x32_bf16 v[54:57], a[40:43], v[86:89], v[54:57]
v_permlane16_swap_b32 v126, v128
v_mfma_f32_16x16x32_bf16 v[54:57], a[52:55], v[90:93], v[54:57]
ds_read_b128 v[110:113], v17 offset:9216
v_mfma_f32_16x16x32_bf16 v[54:57], a[64:67], v[94:97], v[54:57]
global_store_dwordx4 v11, v[122:125], s[38:39] sc1
v_mfma_f32_16x16x32_bf16 v[58:61], a[8:11], v[74:77], 0
ds_read_b128 v[114:117], v17 offset:10240
v_add_u32 v11, 110592, v11
v_mfma_f32_16x16x32_bf16 v[58:61], a[20:23], v[78:81], v[58:61]
v_permlane16_swap_b32 v127, v129
v_mfma_f32_16x16x32_bf16 v[58:61], a[32:35], v[82:85], v[58:61]
ds_read_b128 v[118:121], v17 offset:11264
v_mfma_f32_16x16x32_bf16 v[58:61], a[44:47], v[86:89], v[58:61]
s_mov_b64 exec, s[76:77]
global_store_dwordx4 v12, v[126:129], s[38:39] sc1
s_mov_b64 exec, -1
v_mfma_f32_16x16x32_bf16 v[58:61], a[56:59], v[90:93], v[58:61]
v_add_u32 v12, 110592, v12
v_mfma_f32_16x16x32_bf16 v[58:61], a[68:71], v[94:97], v[58:61]
s_waitcnt lgkmcnt(5)
v_mfma_f32_16x16x32_bf16 v[50:53], a[72:75], v[98:101], v[50:53]
s_waitcnt lgkmcnt(4)
v_mfma_f32_16x16x32_bf16 v[50:53], a[84:87], v[102:105], v[50:53]
s_waitcnt lgkmcnt(3)
v_mfma_f32_16x16x32_bf16 v[50:53], a[96:99], v[106:109], v[50:53]
s_waitcnt lgkmcnt(2)
v_mfma_f32_16x16x32_bf16 v[50:53], a[108:111], v[110:113], v[50:53]
s_waitcnt lgkmcnt(1)
v_mfma_f32_16x16x32_bf16 v[50:53], a[120:123], v[114:117], v[50:53]
s_waitcnt lgkmcnt(0)
ds_read_b128 v[74:77], v17 offset:12288
v_mfma_f32_16x16x32_bf16 v[50:53], a[132:135], v[118:121], v[50:53]
v_mfma_f32_16x16x32_bf16 v[54:57], a[76:79], v[98:101], v[54:57]
ds_read_b128 v[78:81], v17 offset:13312
v_mfma_f32_16x16x32_bf16 v[54:57], a[88:91], v[102:105], v[54:57]
v_mfma_f32_16x16x32_bf16 v[54:57], a[100:103], v[106:109], v[54:57]
ds_read_b128 v[82:85], v17 offset:14336
v_mfma_f32_16x16x32_bf16 v[54:57], a[112:115], v[110:113], v[54:57]
v_mfma_f32_16x16x32_bf16 v[54:57], a[124:127], v[114:117], v[54:57]
ds_read_b128 v[86:89], v17 offset:15360
v_mfma_f32_16x16x32_bf16 v[54:57], a[136:139], v[118:121], v[54:57]
v_mfma_f32_16x16x32_bf16 v[58:61], a[80:83], v[98:101], v[58:61]
ds_read_b128 v[90:93], v17 offset:16384
v_mfma_f32_16x16x32_bf16 v[58:61], a[92:95], v[102:105], v[58:61]
v_mfma_f32_16x16x32_bf16 v[58:61], a[104:107], v[106:109], v[58:61]
ds_read_b128 v[94:97], v17 offset:17408
v_mfma_f32_16x16x32_bf16 v[58:61], a[116:119], v[110:113], v[58:61]
v_mfma_f32_16x16x32_bf16 v[58:61], a[128:131], v[114:117], v[58:61]
v_mfma_f32_16x16x32_bf16 v[58:61], a[140:143], v[118:121], v[58:61]
s_waitcnt lgkmcnt(5)
v_mfma_f32_16x16x32_bf16 v[50:53], a[144:147], v[74:77], v[50:53]
s_waitcnt lgkmcnt(4)
v_mfma_f32_16x16x32_bf16 v[50:53], a[156:159], v[78:81], v[50:53]
s_waitcnt lgkmcnt(3)
v_mfma_f32_16x16x32_bf16 v[50:53], a[168:171], v[82:85], v[50:53]
s_waitcnt lgkmcnt(2)
v_mfma_f32_16x16x32_bf16 v[50:53], a[180:183], v[86:89], v[50:53]
s_waitcnt vmcnt(2)
v_sub_u32 v254, v254, v14
v_cmp_lt_u32 vcc, 0x7f, v254
s_waitcnt lgkmcnt(1)
v_mfma_f32_16x16x32_bf16 v[50:53], a[192:195], v[90:93], v[50:53]
s_xor_b32 s43, s43, 0x10000
ds_read_b128 v[98:101], v17 offset:18432
s_waitcnt lgkmcnt(1)
v_mfma_f32_16x16x32_bf16 v[50:53], a[204:207], v[94:97], v[50:53]
s_add_u32 m0, s43, 0
v_mfma_f32_16x16x32_bf16 v[54:57], a[148:151], v[74:77], v[54:57]
s_cbranch_vccnz .Ldn_sp8
.Ldn_rt9:
ds_read_b128 v[102:105], v17 offset:19456
ds_read_b128 v[106:109], v17 offset:20480
v_mfma_f32_16x16x32_bf16 v[54:57], a[160:163], v[78:81], v[54:57]
v_mfma_f32_16x16x32_bf16 v[54:57], a[172:175], v[82:85], v[54:57]
s_barrier
v_mfma_f32_16x16x32_bf16 v[54:57], a[184:187], v[86:89], v[54:57]
v_add_u32 v14, 1, v14
v_mfma_f32_16x16x32_bf16 v[54:57], a[196:199], v[90:93], v[54:57]
ds_read_b128 v[110:113], v17 offset:21504
v_mfma_f32_16x16x32_bf16 v[54:57], a[208:211], v[94:97], v[54:57]
v_mfma_f32_16x16x32_bf16 v[58:61], a[152:155], v[74:77], v[58:61]
ds_read_b128 v[114:117], v17 offset:22528
global_load_lds_dwordx4 v9, s[38:39] offset:-2048 sc1
v_mfma_f32_16x16x32_bf16 v[58:61], a[164:167], v[78:81], v[58:61]
v_mfma_f32_16x16x32_bf16 v[58:61], a[176:179], v[82:85], v[58:61]
ds_read_b128 v[118:121], v17 offset:23552
v_mfma_f32_16x16x32_bf16 v[58:61], a[188:191], v[86:89], v[58:61]
global_load_lds_dwordx4 v9, s[38:39] offset:-1024 sc1
v_mfma_f32_16x16x32_bf16 v[58:61], a[200:203], v[90:93], v[58:61]
v_mfma_f32_16x16x32_bf16 v[58:61], a[212:215], v[94:97], v[58:61]
s_waitcnt lgkmcnt(5)
v_mfma_f32_16x16x32_bf16 v[50:53], a[216:219], v[98:101], v[50:53]
s_waitcnt lgkmcnt(4)
global_load_lds_dwordx4 v9, s[38:39] offset:0 sc1
v_mfma_f32_16x16x32_bf16 v[50:53], a[228:231], v[102:105], v[50:53]
s_waitcnt lgkmcnt(3)
v_mfma_f32_16x16x32_bf16 v[50:53], a[240:243], v[106:109], v[50:53]
s_waitcnt lgkmcnt(2)
v_mfma_f32_16x16x32_bf16 v[50:53], a[252:255], v[110:113], v[50:53]
s_waitcnt lgkmcnt(1)
global_load_lds_dwordx4 v9, s[38:39] offset:1024 sc1
v_mfma_f32_16x16x32_bf16 v[50:53], v[26:29], v[114:117], v[50:53]
s_waitcnt lgkmcnt(0)
v_mfma_f32_16x16x32_bf16 v[50:53], v[38:41], v[118:121], v[50:53]
v_mfma_f32_16x16x32_bf16 v[54:57], a[220:223], v[98:101], v[54:57]
global_load_lds_dwordx4 v9, s[38:39] offset:2048 sc1
v_mfma_f32_16x16x32_bf16 v[54:57], a[232:235], v[102:105], v[54:57]
v_mfma_f32_16x16x32_bf16 v[54:57], a[244:247], v[106:109], v[54:57]
s_barrier
v_mfma_f32_16x16x32_bf16 v[54:57], v[18:21], v[110:113], v[54:57]
ds_read_b128 v[74:77], v17 offset:24576
global_load_lds_dwordx4 v9, s[38:39] offset:3072 sc1
v_mfma_f32_16x16x32_bf16 v[54:57], v[30:33], v[114:117], v[54:57]
ds_read_b128 v[78:81], v17 offset:25600
v_add_u32 v9, 147456, v9
v_mfma_f32_16x16x32_bf16 v[54:57], v[42:45], v[118:121], v[54:57]
ds_read_b128 v[82:85], v17 offset:26624
v_min_u32 v9, v9, v10
v_mfma_f32_16x16x32_bf16 v[58:61], a[224:227], v[98:101], v[58:61]
ds_read_b128 v[86:89], v17 offset:27648
v_cvt_pk_bf16_f32 v122, v50, v51
v_mfma_f32_16x16x32_bf16 v[58:61], a[236:239], v[102:105], v[58:61]
ds_read_b128 v[90:93], v17 offset:28672
v_cvt_pk_bf16_f32 v123, v52, v53
v_mfma_f32_16x16x32_bf16 v[58:61], a[248:251], v[106:109], v[58:61]
ds_read_b128 v[94:97], v17 offset:29696
v_mfma_f32_16x16x32_bf16 v[58:61], v[22:25], v[110:113], v[58:61]
v_cvt_pk_bf16_f32 v124, v54, v55
v_cvt_pk_bf16_f32 v125, v56, v57
v_mfma_f32_16x16x32_bf16 v[58:61], v[34:37], v[114:117], v[58:61]
v_mfma_f32_16x16x32_bf16 v[58:61], v[46:49], v[118:121], v[58:61]
s_waitcnt lgkmcnt(5)
v_mfma_f32_16x16x32_bf16 v[62:65], a[0:3], v[74:77], 0
s_waitcnt lgkmcnt(4)
v_mfma_f32_16x16x32_bf16 v[62:65], a[12:15], v[78:81], v[62:65]
s_waitcnt lgkmcnt(3)
global_load_dword v254, v13, s[38:39] sc1
v_mfma_f32_16x16x32_bf16 v[62:65], a[24:27], v[82:85], v[62:65]
s_waitcnt lgkmcnt(2)
v_mfma_f32_16x16x32_bf16 v[62:65], a[36:39], v[86:89], v[62:65]
s_waitcnt lgkmcnt(1)
ds_read_b128 v[98:101], v17 offset:30720
v_cvt_pk_bf16_f32 v126, v58, v59
v_mfma_f32_16x16x32_bf16 v[62:65], a[48:51], v[90:93], v[62:65]
s_waitcnt lgkmcnt(1)
v_mfma_f32_16x16x32_bf16 v[62:65], a[60:63], v[94:97], v[62:65]
s_waitcnt vmcnt(7)
s_barrier
v_mfma_f32_16x16x32_bf16 v[66:69], a[4:7], v[74:77], 0
ds_read_b128 v[102:105], v17 offset:31744
v_cvt_pk_bf16_f32 v128, v58, v59
v_mfma_f32_16x16x32_bf16 v[66:69], a[16:19], v[78:81], v[66:69]
v_cvt_pk_bf16_f32 v127, v60, v61
v_cvt_pk_bf16_f32 v129, v60, v61
v_mfma_f32_16x16x32_bf16 v[66:69], a[28:31], v[82:85], v[66:69]
ds_read_b128 v[106:109], v17 offset:32768
v_mfma_f32_16x16x32_bf16 v[66:69], a[40:43], v[86:89], v[66:69]
v_permlane16_swap_b32 v126, v128
v_mfma_f32_16x16x32_bf16 v[66:69], a[52:55], v[90:93], v[66:69]
ds_read_b128 v[110:113], v17 offset:33792
v_mfma_f32_16x16x32_bf16 v[66:69], a[64:67], v[94:97], v[66:69]
global_store_dwordx4 v11, v[122:125], s[38:39] sc1
v_mfma_f32_16x16x32_bf16 v[70:73], a[8:11], v[74:77], 0
ds_read_b128 v[114:117], v17 offset:34816
v_add_u32 v11, 110592, v11
v_mfma_f32_16x16x32_bf16 v[70:73], a[20:23], v[78:81], v[70:73]
v_permlane16_swap_b32 v127, v129
v_mfma_f32_16x16x32_bf16 v[70:73], a[32:35], v[82:85], v[70:73]
ds_read_b128 v[118:121], v17 offset:35840
v_mfma_f32_16x16x32_bf16 v[70:73], a[44:47], v[86:89], v[70:73]
s_mov_b64 exec, s[76:77]
global_store_dwordx4 v12, v[126:129], s[38:39] sc1
s_mov_b64 exec, -1
v_mfma_f32_16x16x32_bf16 v[70:73], a[56:59], v[90:93], v[70:73]
v_add_u32 v12, 110592, v12
v_mfma_f32_16x16x32_bf16 v[70:73], a[68:71], v[94:97], v[70:73]
s_waitcnt lgkmcnt(5)
v_mfma_f32_16x16x32_bf16 v[62:65], a[72:75], v[98:101], v[62:65]
s_waitcnt lgkmcnt(4)
v_mfma_f32_16x16x32_bf16 v[62:65], a[84:87], v[102:105], v[62:65]
s_waitcnt lgkmcnt(3)
v_mfma_f32_16x16x32_bf16 v[62:65], a[96:99], v[106:109], v[62:65]
s_waitcnt lgkmcnt(2)
v_mfma_f32_16x16x32_bf16 v[62:65], a[108:111], v[110:113], v[62:65]
s_waitcnt lgkmcnt(1)
v_mfma_f32_16x16x32_bf16 v[62:65], a[120:123], v[114:117], v[62:65]
s_waitcnt lgkmcnt(0)
ds_read_b128 v[74:77], v17 offset:36864
v_mfma_f32_16x16x32_bf16 v[62:65], a[132:135], v[118:121], v[62:65]
v_mfma_f32_16x16x32_bf16 v[66:69], a[76:79], v[98:101], v[66:69]
ds_read_b128 v[78:81], v17 offset:37888
v_mfma_f32_16x16x32_bf16 v[66:69], a[88:91], v[102:105], v[66:69]
v_mfma_f32_16x16x32_bf16 v[66:69], a[100:103], v[106:109], v[66:69]
ds_read_b128 v[82:85], v17 offset:38912
v_mfma_f32_16x16x32_bf16 v[66:69], a[112:115], v[110:113], v[66:69]
v_mfma_f32_16x16x32_bf16 v[66:69], a[124:127], v[114:117], v[66:69]
ds_read_b128 v[86:89], v17 offset:39936
v_mfma_f32_16x16x32_bf16 v[66:69], a[136:139], v[118:121], v[66:69]
v_mfma_f32_16x16x32_bf16 v[70:73], a[80:83], v[98:101], v[70:73]
ds_read_b128 v[90:93], v17 offset:40960
v_mfma_f32_16x16x32_bf16 v[70:73], a[92:95], v[102:105], v[70:73]
v_mfma_f32_16x16x32_bf16 v[70:73], a[104:107], v[106:109], v[70:73]
ds_read_b128 v[94:97], v17 offset:41984
v_mfma_f32_16x16x32_bf16 v[70:73], a[116:119], v[110:113], v[70:73]
v_mfma_f32_16x16x32_bf16 v[70:73], a[128:131], v[114:117], v[70:73]
v_mfma_f32_16x16x32_bf16 v[70:73], a[140:143], v[118:121], v[70:73]
s_waitcnt lgkmcnt(5)
v_mfma_f32_16x16x32_bf16 v[62:65], a[144:147], v[74:77], v[62:65]
s_waitcnt lgkmcnt(4)
v_mfma_f32_16x16x32_bf16 v[62:65], a[156:159], v[78:81], v[62:65]
s_waitcnt lgkmcnt(3)
v_mfma_f32_16x16x32_bf16 v[62:65], a[168:171], v[82:85], v[62:65]
s_waitcnt lgkmcnt(2)
v_mfma_f32_16x16x32_bf16 v[62:65], a[180:183], v[86:89], v[62:65]
s_waitcnt vmcnt(2)
v_sub_u32 v254, v254, v14
v_cmp_lt_u32 vcc, 0x7f, v254
s_waitcnt lgkmcnt(1)
v_mfma_f32_16x16x32_bf16 v[62:65], a[192:195], v[90:93], v[62:65]
s_waitcnt lgkmcnt(0)
ds_read_b128 v[98:101], v17 offset:43008
v_mfma_f32_16x16x32_bf16 v[62:65], a[204:207], v[94:97], v[62:65]
s_add_u32 m0, s43, 24576
v_mfma_f32_16x16x32_bf16 v[66:69], a[148:151], v[74:77], v[66:69]
s_cbranch_vccnz .Ldn_sp10
.Ldn_rt11:
ds_read_b128 v[102:105], v17 offset:44032
ds_read_b128 v[106:109], v17 offset:45056
v_mfma_f32_16x16x32_bf16 v[66:69], a[160:163], v[78:81], v[66:69]
v_mfma_f32_16x16x32_bf16 v[66:69], a[172:175], v[82:85], v[66:69]
s_barrier
v_mfma_f32_16x16x32_bf16 v[66:69], a[184:187], v[86:89], v[66:69]
v_add_u32 v14, 1, v14
v_mfma_f32_16x16x32_bf16 v[66:69], a[196:199], v[90:93], v[66:69]
ds_read_b128 v[110:113], v17 offset:46080
v_mfma_f32_16x16x32_bf16 v[66:69], a[208:211], v[94:97], v[66:69]
v_mfma_f32_16x16x32_bf16 v[70:73], a[152:155], v[74:77], v[70:73]
ds_read_b128 v[114:117], v17 offset:47104
global_load_lds_dwordx4 v9, s[38:39] offset:-2048 sc1
v_mfma_f32_16x16x32_bf16 v[70:73], a[164:167], v[78:81], v[70:73]
v_mfma_f32_16x16x32_bf16 v[70:73], a[176:179], v[82:85], v[70:73]
ds_read_b128 v[118:121], v17 offset:48128
v_mfma_f32_16x16x32_bf16 v[70:73], a[188:191], v[86:89], v[70:73]
global_load_lds_dwordx4 v9, s[38:39] offset:-1024 sc1
v_mfma_f32_16x16x32_bf16 v[70:73], a[200:203], v[90:93], v[70:73]
v_mfma_f32_16x16x32_bf16 v[70:73], a[212:215], v[94:97], v[70:73]
s_waitcnt lgkmcnt(5)
v_mfma_f32_16x16x32_bf16 v[62:65], a[216:219], v[98:101], v[62:65]
s_waitcnt lgkmcnt(4)
global_load_lds_dwordx4 v9, s[38:39] offset:0 sc1
v_mfma_f32_16x16x32_bf16 v[62:65], a[228:231], v[102:105], v[62:65]
s_waitcnt lgkmcnt(3)
v_mfma_f32_16x16x32_bf16 v[62:65], a[240:243], v[106:109], v[62:65]
s_waitcnt lgkmcnt(2)
v_mfma_f32_16x16x32_bf16 v[62:65], a[252:255], v[110:113], v[62:65]
s_waitcnt lgkmcnt(1)
global_load_lds_dwordx4 v9, s[38:39] offset:1024 sc1
v_mfma_f32_16x16x32_bf16 v[62:65], v[26:29], v[114:117], v[62:65]
s_waitcnt lgkmcnt(0)
v_mfma_f32_16x16x32_bf16 v[62:65], v[38:41], v[118:121], v[62:65]
v_mfma_f32_16x16x32_bf16 v[66:69], a[220:223], v[98:101], v[66:69]
v_xor_b32 v17, 0x10000, v17
global_load_lds_dwordx4 v9, s[38:39] offset:2048 sc1
)"
R"(v_mfma_f32_16x16x32_bf16 v[66:69], a[232:235], v[102:105], v[66:69]
v_mfma_f32_16x16x32_bf16 v[66:69], a[244:247], v[106:109], v[66:69]
s_barrier
v_mfma_f32_16x16x32_bf16 v[66:69], v[18:21], v[110:113], v[66:69]
ds_read_b128 v[74:77], v17 offset:0
global_load_lds_dwordx4 v9, s[38:39] offset:3072 sc1
v_mfma_f32_16x16x32_bf16 v[66:69], v[30:33], v[114:117], v[66:69]
ds_read_b128 v[78:81], v17 offset:1024
v_add_u32 v9, 147456, v9
v_mfma_f32_16x16x32_bf16 v[66:69], v[42:45], v[118:121], v[66:69]
ds_read_b128 v[82:85], v17 offset:2048
v_min_u32 v9, v9, v10
v_mfma_f32_16x16x32_bf16 v[70:73], a[224:227], v[98:101], v[70:73]
ds_read_b128 v[86:89], v17 offset:3072
v_cvt_pk_bf16_f32 v122, v62, v63
v_mfma_f32_16x16x32_bf16 v[70:73], a[236:239], v[102:105], v[70:73]
ds_read_b128 v[90:93], v17 offset:4096
v_cvt_pk_bf16_f32 v123, v64, v65
v_mfma_f32_16x16x32_bf16 v[70:73], a[248:251], v[106:109], v[70:73]
ds_read_b128 v[94:97], v17 offset:5120
v_mfma_f32_16x16x32_bf16 v[70:73], v[22:25], v[110:113], v[70:73]
v_cvt_pk_bf16_f32 v124, v66, v67
v_cvt_pk_bf16_f32 v125, v68, v69
v_mfma_f32_16x16x32_bf16 v[70:73], v[34:37], v[114:117], v[70:73]
v_mfma_f32_16x16x32_bf16 v[70:73], v[46:49], v[118:121], v[70:73]
s_sub_u32 s70, s70, 1
s_cbranch_scc0 .Ldn_top12
v_cvt_pk_bf16_f32 v122, v62, v63
v_cvt_pk_bf16_f32 v123, v64, v65
v_cvt_pk_bf16_f32 v124, v66, v67
v_cvt_pk_bf16_f32 v125, v68, v69
global_store_dwordx4 v11, v[122:125], s[38:39] sc1
v_add_u32 v11, 110592, v11
v_cvt_pk_bf16_f32 v126, v70, v71
v_cvt_pk_bf16_f32 v128, v70, v71
v_cvt_pk_bf16_f32 v127, v72, v73
v_cvt_pk_bf16_f32 v129, v72, v73
v_permlane16_swap_b32 v126, v128
s_nop 0
v_permlane16_swap_b32 v127, v129
s_mov_b64 exec, s[76:77]
global_store_dwordx4 v12, v[126:129], s[38:39] sc1
s_mov_b64 exec, -1
v_add_u32 v12, 110592, v12
s_waitcnt vmcnt(2)
s_barrier
s_waitcnt vmcnt(0)
s_barrier
s_endpgm
.Ldn_sp8:
s_sleep 1
global_load_dword v254, v13, s[38:39] sc1
s_add_u32 s71, s71, 1
s_waitcnt vmcnt(0)
v_sub_u32 v254, v254, v14
v_cmp_lt_u32 vcc, 0x7f, v254
s_cbranch_vccz .Ldn_rt9
s_cmp_lt_u32 s71, 3000000
s_cbranch_scc1 .Ldn_sp8
s_branch .Ldn_rt9
.Ldn_sp10:
s_sleep 1
global_load_dword v254, v13, s[38:39] sc1
s_add_u32 s71, s71, 1
s_waitcnt vmcnt(0)
v_sub_u32 v254, v254, v14
v_cmp_lt_u32 vcc, 0x7f, v254
s_cbranch_vccz .Ldn_rt11
s_cmp_lt_u32 s71, 3000000
s_cbranch_scc1 .Ldn_sp10
s_branch .Ldn_rt11
.Lrole3:
v_add_u32 v255, 35840, v0
v_add_u32 v150, 71680, v0
v_add_u32 v151, 107520, v0
v_add_u32 v152, 143360, v0
v_add_u32 v153, 179200, v0
v_add_u32 v154, 215040, v0
v_add_u32 v155, 250880, v0
s_sleep 20
s_add_u32 m0, s42, 4096
s_nop 0
global_load_lds_dwordx4 v0, s[40:41] offset:-4096 nt
global_load_lds_dwordx4 v255, s[40:41] offset:-3072 nt
global_load_lds_dwordx4 v150, s[40:41] offset:-2048 nt
global_load_lds_dwordx4 v151, s[40:41] offset:-1024 nt
global_load_lds_dwordx4 v152, s[40:41] offset:0 nt
global_load_lds_dwordx4 v153, s[40:41] offset:1024 nt
global_load_lds_dwordx4 v154, s[40:41] offset:2048 nt
global_load_lds_dwordx4 v155, s[40:41] offset:3072 nt
s_add_u32 s40, s40, 589824
s_addc_u32 s41, s41, 0
s_add_u32 m0, s42, 36864
s_nop 0
global_load_lds_dwordx4 v0, s[40:41] offset:-4096 nt
global_load_lds_dwordx4 v255, s[40:41] offset:-3072 nt
global_load_lds_dwordx4 v150, s[40:41] offset:-2048 nt
global_load_lds_dwordx4 v151, s[40:41] offset:-1024 nt
global_load_lds_dwordx4 v152, s[40:41] offset:0 nt
global_load_lds_dwordx4 v153, s[40:41] offset:1024 nt
global_load_lds_dwordx4 v154, s[40:41] offset:2048 nt
global_load_lds_dwordx4 v155, s[40:41] offset:3072 nt
s_add_u32 s40, s40, 589824
s_addc_u32 s41, s41, 0
s_add_u32 m0, s42, 69632
s_nop 0
global_load_lds_dwordx4 v0, s[40:41] offset:-4096 nt
global_load_lds_dwordx4 v255, s[40:41] offset:-3072 nt
global_load_lds_dwordx4 v150, s[40:41] offset:-2048 nt
global_load_lds_dwordx4 v151, s[40:41] offset:-1024 nt
global_load_lds_dwordx4 v152, s[40:41] offset:0 nt
global_load_lds_dwordx4 v153, s[40:41] offset:1024 nt
global_load_lds_dwordx4 v154, s[40:41] offset:2048 nt
global_load_lds_dwordx4 v155, s[40:41] offset:3072 nt
s_add_u32 s40, s40, 589824
s_addc_u32 s41, s41, 0
v_add_u32 v156, 0x10000, v1
v_mov_b32 v174, 0x3F803F80
v_mov_b32 v175, 0x3F803F80
v_mov_b32 v176, 0x3F803F80
v_mov_b32 v177, 0x3F803F80
s_waitcnt lgkmcnt(0)
s_mul_i32 s37, s37, 0x9E3779B1
s_add_u32 s37, s37, 0x7F4A7C15
v_add_u32 v5, s37, v5
v_add_u32 v7, s37, v7
v_add_u32 v17, s37, v17
s_waitcnt vmcnt(16)
s_barrier
s_add_u32 m0, s42, 102400
ds_read_b64_tr_b16 a[0:1], v1 offset:0
ds_read_b64_tr_b16 a[2:3], v1 offset:1024
global_load_lds_dwordx4 v0, s[40:41] offset:-4096 nt
ds_read_b64_tr_b16 a[4:5], v1 offset:8
global_load_lds_dwordx4 v255, s[40:41] offset:-3072 nt
ds_read_b64_tr_b16 a[6:7], v1 offset:1032
ds_read_b64_tr_b16 a[8:9], v1 offset:16384
global_load_lds_dwordx4 v150, s[40:41] offset:-2048 nt
ds_read_b64_tr_b16 a[10:11], v1 offset:17408
global_load_lds_dwordx4 v151, s[40:41] offset:-1024 nt
ds_read_b64_tr_b16 a[12:13], v1 offset:16392
ds_read_b64_tr_b16 a[14:15], v1 offset:17416
global_load_lds_dwordx4 v152, s[40:41] offset:0 nt
ds_read_b64_tr_b16 a[16:17], v1 offset:8192
global_load_lds_dwordx4 v153, s[40:41] offset:1024 nt
ds_read_b64_tr_b16 a[18:19], v1 offset:9216
ds_read_b64_tr_b16 a[20:21], v1 offset:8200
global_load_lds_dwordx4 v154, s[40:41] offset:2048 nt
ds_read_b64_tr_b16 a[22:23], v1 offset:9224
global_load_lds_dwordx4 v155, s[40:41] offset:3072 nt
ds_read_b64_tr_b16 a[24:25], v1 offset:24576
ds_read_b64_tr_b16 a[26:27], v1 offset:25600
s_add_u32 s40, s40, 589824
ds_read_b64_tr_b16 a[28:29], v1 offset:24584
s_addc_u32 s41, s41, 0
ds_read_b64_tr_b16 a[30:31], v1 offset:25608
s_waitcnt vmcnt(16) lgkmcnt(0)
s_barrier
s_add_u32 m0, s42, 4096
ds_read_b64_tr_b16 a[32:33], v1 offset:32768
ds_read_b64_tr_b16 a[34:35], v1 offset:33792
ds_read_b64_tr_b16 a[36:37], v1 offset:32776
global_load_lds_dwordx4 v0, s[40:41] offset:-4096 nt
ds_read_b64_tr_b16 a[38:39], v1 offset:33800
ds_read_b64_tr_b16 a[40:41], v1 offset:49152
global_load_lds_dwordx4 v255, s[40:41] offset:-3072 nt
ds_read_b64_tr_b16 a[42:43], v1 offset:50176
ds_read_b64_tr_b16 a[44:45], v1 offset:49160
global_load_lds_dwordx4 v150, s[40:41] offset:-2048 nt
ds_read_b64_tr_b16 a[46:47], v1 offset:50184
ds_read_b64_tr_b16 a[48:49], v1 offset:40960
global_load_lds_dwordx4 v151, s[40:41] offset:-1024 nt
ds_read_b64_tr_b16 a[50:51], v1 offset:41984
ds_read_b64_tr_b16 a[52:53], v1 offset:40968
global_load_lds_dwordx4 v152, s[40:41] offset:0 nt
ds_read_b64_tr_b16 a[54:55], v1 offset:41992
ds_read_b64_tr_b16 a[56:57], v1 offset:57344
ds_read_b64_tr_b16 a[58:59], v1 offset:58368
global_load_lds_dwordx4 v153, s[40:41] offset:1024 nt
ds_read_b64_tr_b16 a[60:61], v1 offset:57352
ds_read_b64_tr_b16 a[62:63], v1 offset:58376
global_load_lds_dwordx4 v154, s[40:41] offset:2048 nt
v_mfma_f32_16x16x32_bf16 v[158:161], v[174:177], a[0:3], 0
v_mfma_f32_16x16x32_bf16 v[162:165], v[174:177], a[4:7], 0
global_load_lds_dwordx4 v155, s[40:41] offset:3072 nt
v_mfma_f32_16x16x32_bf16 v[166:169], v[174:177], a[8:11], 0
v_mfma_f32_16x16x32_bf16 v[170:173], v[174:177], a[12:15], 0
s_add_u32 s40, s40, 589824
v_mfma_f32_16x16x32_bf16 v[158:161], v[174:177], a[16:19], v[158:161]
v_mfma_f32_16x16x32_bf16 v[162:165], v[174:177], a[20:23], v[162:165]
s_addc_u32 s41, s41, 0
v_mfma_f32_16x16x32_bf16 v[166:169], v[174:177], a[24:27], v[166:169]
v_mfma_f32_16x16x32_bf16 v[170:173], v[174:177], a[28:31], v[170:173]
s_waitcnt vmcnt(16) lgkmcnt(0)
s_barrier
s_add_u32 m0, s42, 36864
ds_read_b64_tr_b16 a[64:65], v156 offset:0
ds_read_b64_tr_b16 a[66:67], v156 offset:1024
ds_read_b64_tr_b16 a[68:69], v156 offset:8
global_load_lds_dwordx4 v0, s[40:41] offset:-4096 nt
ds_read_b64_tr_b16 a[70:71], v156 offset:1032
ds_read_b64_tr_b16 a[72:73], v156 offset:16384
global_load_lds_dwordx4 v255, s[40:41] offset:-3072 nt
ds_read_b64_tr_b16 a[74:75], v156 offset:17408
ds_read_b64_tr_b16 a[76:77], v156 offset:16392
global_load_lds_dwordx4 v150, s[40:41] offset:-2048 nt
ds_read_b64_tr_b16 a[78:79], v156 offset:17416
ds_read_b64_tr_b16 a[80:81], v156 offset:8192
global_load_lds_dwordx4 v151, s[40:41] offset:-1024 nt
ds_read_b64_tr_b16 a[82:83], v156 offset:9216
ds_read_b64_tr_b16 a[84:85], v156 offset:8200
global_load_lds_dwordx4 v152, s[40:41] offset:0 nt
ds_read_b64_tr_b16 a[86:87], v156 offset:9224
ds_read_b64_tr_b16 a[88:89], v156 offset:24576
ds_read_b64_tr_b16 a[90:91], v156 offset:25600
global_load_lds_dwordx4 v153, s[40:41] offset:1024 nt
ds_read_b64_tr_b16 a[92:93], v156 offset:24584
ds_read_b64_tr_b16 a[94:95], v156 offset:25608
global_load_lds_dwordx4 v154, s[40:41] offset:2048 nt
v_mfma_f32_16x16x32_bf16 v[158:161], v[174:177], a[32:35], v[158:161]
v_mfma_f32_16x16x32_bf16 v[162:165], v[174:177], a[36:39], v[162:165]
global_load_lds_dwordx4 v155, s[40:41] offset:3072 nt
v_mfma_f32_16x16x32_bf16 v[166:169], v[174:177], a[40:43], v[166:169]
v_mfma_f32_16x16x32_bf16 v[170:173], v[174:177], a[44:47], v[170:173]
s_add_u32 s40, s40, 589824
v_mfma_f32_16x16x32_bf16 v[158:161], v[174:177], a[48:51], v[158:161]
v_mfma_f32_16x16x32_bf16 v[162:165], v[174:177], a[52:55], v[162:165]
s_addc_u32 s41, s41, 0
v_mfma_f32_16x16x32_bf16 v[166:169], v[174:177], a[56:59], v[166:169]
v_mfma_f32_16x16x32_bf16 v[170:173], v[174:177], a[60:63], v[170:173]
s_waitcnt vmcnt(16) lgkmcnt(0)
s_barrier
s_add_u32 m0, s42, 69632
ds_read_b64_tr_b16 a[96:97], v156 offset:32768
ds_read_b64_tr_b16 a[98:99], v156 offset:33792
ds_read_b64_tr_b16 a[100:101], v156 offset:32776
global_load_lds_dwordx4 v0, s[40:41] offset:-4096 nt
ds_read_b64_tr_b16 a[102:103], v156 offset:33800
ds_read_b64_tr_b16 a[104:105], v156 offset:49152
global_load_lds_dwordx4 v255, s[40:41] offset:-3072 nt
ds_read_b64_tr_b16 a[106:107], v156 offset:50176
ds_read_b64_tr_b16 a[108:109], v156 offset:49160
global_load_lds_dwordx4 v150, s[40:41] offset:-2048 nt
ds_read_b64_tr_b16 a[110:111], v156 offset:50184
ds_read_b64_tr_b16 a[112:113], v156 offset:40960
global_load_lds_dwordx4 v151, s[40:41] offset:-1024 nt
ds_read_b64_tr_b16 a[114:115], v156 offset:41984
ds_read_b64_tr_b16 a[116:117], v156 offset:40968
global_load_lds_dwordx4 v152, s[40:41] offset:0 nt
ds_read_b64_tr_b16 a[118:119], v156 offset:41992
ds_read_b64_tr_b16 a[120:121], v156 offset:57344
ds_read_b64_tr_b16 a[122:123], v156 offset:58368
global_load_lds_dwordx4 v153, s[40:41] offset:1024 nt
ds_read_b64_tr_b16 a[124:125], v156 offset:57352
ds_read_b64_tr_b16 a[126:127], v156 offset:58376
global_load_lds_dwordx4 v154, s[40:41] offset:2048 nt
v_mfma_f32_16x16x32_bf16 v[158:161], v[174:177], a[64:67], v[158:161]
v_mfma_f32_16x16x32_bf16 v[162:165], v[174:177], a[68:71], v[162:165]
global_load_lds_dwordx4 v155, s[40:41] offset:3072 nt
v_mfma_f32_16x16x32_bf16 v[166:169], v[174:177], a[72:75], v[166:169]
v_mfma_f32_16x16x32_bf16 v[170:173], v[174:177], a[76:79], v[170:173]
s_add_u32 s40, s40, 589824
v_mfma_f32_16x16x32_bf16 v[158:161], v[174:177], a[80:83], v[158:161]
v_mfma_f32_16x16x32_bf16 v[162:165], v[174:177], a[84:87], v[162:165]
s_addc_u32 s41, s41, 0
v_mfma_f32_16x16x32_bf16 v[166:169], v[174:177], a[88:91], v[166:169]
v_mfma_f32_16x16x32_bf16 v[170:173], v[174:177], a[92:95], v[170:173]
s_waitcnt vmcnt(16) lgkmcnt(0)
s_barrier
s_add_u32 m0, s42, 102400
ds_read_b64_tr_b16 a[128:129], v1 offset:0
ds_read_b64_tr_b16 a[130:131], v1 offset:1024
ds_read_b64_tr_b16 a[132:133], v1 offset:8
global_load_lds_dwordx4 v0, s[40:41] offset:-4096 nt
ds_read_b64_tr_b16 a[134:135], v1 offset:1032
ds_read_b64_tr_b16 a[136:137], v1 offset:16384
global_load_lds_dwordx4 v255, s[40:41] offset:-3072 nt
ds_read_b64_tr_b16 a[138:139], v1 offset:17408
ds_read_b64_tr_b16 a[140:141], v1 offset:16392
global_load_lds_dwordx4 v150, s[40:41] offset:-2048 nt
ds_read_b64_tr_b16 a[142:143], v1 offset:17416
ds_read_b64_tr_b16 a[144:145], v1 offset:8192
global_load_lds_dwordx4 v151, s[40:41] offset:-1024 nt
ds_read_b64_tr_b16 a[146:147], v1 offset:9216
ds_read_b64_tr_b16 a[148:149], v1 offset:8200
global_load_lds_dwordx4 v152, s[40:41] offset:0 nt
ds_read_b64_tr_b16 a[150:151], v1 offset:9224
ds_read_b64_tr_b16 a[152:153], v1 offset:24576
ds_read_b64_tr_b16 a[154:155], v1 offset:25600
global_load_lds_dwordx4 v153, s[40:41] offset:1024 nt
ds_read_b64_tr_b16 a[156:157], v1 offset:24584
ds_read_b64_tr_b16 a[158:159], v1 offset:25608
global_load_lds_dwordx4 v154, s[40:41] offset:2048 nt
v_mfma_f32_16x16x32_bf16 v[158:161], v[174:177], a[96:99], v[158:161]
v_mfma_f32_16x16x32_bf16 v[162:165], v[174:177], a[100:103], v[162:165]
global_load_lds_dwordx4 v155, s[40:41] offset:3072 nt
v_mfma_f32_16x16x32_bf16 v[166:169], v[174:177], a[104:107], v[166:169]
v_mfma_f32_16x16x32_bf16 v[170:173], v[174:177], a[108:111], v[170:173]
s_add_u32 s40, s40, 589824
v_mfma_f32_16x16x32_bf16 v[158:161], v[174:177], a[112:115], v[158:161]
v_mfma_f32_16x16x32_bf16 v[162:165], v[174:177], a[116:119], v[162:165]
s_addc_u32 s41, s41, 0
v_mfma_f32_16x16x32_bf16 v[166:169], v[174:177], a[120:123], v[166:169]
v_mfma_f32_16x16x32_bf16 v[170:173], v[174:177], a[124:127], v[170:173]
s_waitcnt vmcnt(16) lgkmcnt(0)
s_barrier
s_add_u32 m0, s42, 4096
ds_read_b64_tr_b16 a[160:161], v1 offset:32768
ds_read_b64_tr_b16 a[162:163], v1 offset:33792
ds_read_b64_tr_b16 a[164:165], v1 offset:32776
global_load_lds_dwordx4 v0, s[40:41] offset:-4096 nt
ds_read_b64_tr_b16 a[166:167], v1 offset:33800
ds_read_b64_tr_b16 a[168:169], v1 offset:49152
global_load_lds_dwordx4 v255, s[40:41] offset:-3072 nt
ds_read_b64_tr_b16 a[170:171], v1 offset:50176
ds_read_b64_tr_b16 a[172:173], v1 offset:49160
global_load_lds_dwordx4 v150, s[40:41] offset:-2048 nt
ds_read_b64_tr_b16 a[174:175], v1 offset:50184
ds_read_b64_tr_b16 a[176:177], v1 offset:40960
global_load_lds_dwordx4 v151, s[40:41] offset:-1024 nt
ds_read_b64_tr_b16 a[178:179], v1 offset:41984
ds_read_b64_tr_b16 a[180:181], v1 offset:40968
global_load_lds_dwordx4 v152, s[40:41] offset:0 nt
ds_read_b64_tr_b16 a[182:183], v1 offset:41992
ds_read_b64_tr_b16 a[184:185], v1 offset:57344
ds_read_b64_tr_b16 a[186:187], v1 offset:58368
global_load_lds_dwordx4 v153, s[40:41] offset:1024 nt
ds_read_b64_tr_b16 a[188:189], v1 offset:57352
ds_read_b64_tr_b16 a[190:191], v1 offset:58376
global_load_lds_dwordx4 v154, s[40:41] offset:2048 nt
v_mfma_f32_16x16x32_bf16 v[158:161], v[174:177], a[128:131], v[158:161]
v_mfma_f32_16x16x32_bf16 v[162:165], v[174:177], a[132:135], v[162:165]
global_load_lds_dwordx4 v155, s[40:41] offset:3072 nt
v_mfma_f32_16x16x32_bf16 v[166:169], v[174:177], a[136:139], v[166:169]
v_mfma_f32_16x16x32_bf16 v[170:173], v[174:177], a[140:143], v[170:173]
s_sub_u32 s40, s40, 4718592
v_mfma_f32_16x16x32_bf16 v[158:161], v[174:177], a[144:147], v[158:161]
v_mfma_f32_16x16x32_bf16 v[162:165], v[174:177], a[148:151], v[162:165]
s_subb_u32 s41, s41, 0
v_mfma_f32_16x16x32_bf16 v[166:169], v[174:177], a[152:155], v[166:169]
v_mfma_f32_16x16x32_bf16 v[170:173], v[174:177], a[156:159], v[170:173]
s_waitcnt vmcnt(16) lgkmcnt(0)
s_barrier
ds_read_b64_tr_b16 a[192:193], v156 offset:0
ds_read_b64_tr_b16 a[194:195], v156 offset:1024
ds_read_b64_tr_b16 a[196:197], v156 offset:8
ds_read_b64_tr_b16 a[198:199], v156 offset:1032
ds_read_b64_tr_b16 a[200:201], v156 offset:16384
ds_read_b64_tr_b16 a[202:203], v156 offset:17408
ds_read_b64_tr_b16 a[204:205], v156 offset:16392
ds_read_b64_tr_b16 a[206:207], v156 offset:17416
ds_read_b64_tr_b16 a[208:209], v156 offset:8192
ds_read_b64_tr_b16 a[210:211], v156 offset:9216
ds_read_b64_tr_b16 a[212:213], v156 offset:8200
ds_read_b64_tr_b16 a[214:215], v156 offset:9224
ds_read_b64_tr_b16 a[216:217], v156 offset:24576
ds_read_b64_tr_b16 a[218:219], v156 offset:25600
ds_read_b64_tr_b16 a[220:221], v156 offset:24584
ds_read_b64_tr_b16 a[222:223], v156 offset:25608
v_mfma_f32_16x16x32_bf16 v[158:161], v[174:177], a[160:163], v[158:161]
v_mfma_f32_16x16x32_bf16 v[162:165], v[174:177], a[164:167], v[162:165]
v_mfma_f32_16x16x32_bf16 v[166:169], v[174:177], a[168:171], v[166:169]
v_mfma_f32_16x16x32_bf16 v[170:173], v[174:177], a[172:175], v[170:173]
v_mfma_f32_16x16x32_bf16 v[158:161], v[174:177], a[176:179], v[158:161]
v_mfma_f32_16x16x32_bf16 v[162:165], v[174:177], a[180:183], v[162:165]
v_mfma_f32_16x16x32_bf16 v[166:169], v[174:177], a[184:187], v[166:169]
v_mfma_f32_16x16x32_bf16 v[170:173], v[174:177], a[188:191], v[170:173]
s_waitcnt vmcnt(8) lgkmcnt(0)
s_barrier
ds_read_b64_tr_b16 a[224:225], v156 offset:32768
ds_read_b64_tr_b16 a[226:227], v156 offset:33792
ds_read_b64_tr_b16 a[228:229], v156 offset:32776
ds_read_b64_tr_b16 a[230:231], v156 offset:33800
ds_read_b64_tr_b16 a[232:233], v156 offset:49152
ds_read_b64_tr_b16 a[234:235], v156 offset:50176
ds_read_b64_tr_b16 a[236:237], v156 offset:49160
ds_read_b64_tr_b16 a[238:239], v156 offset:50184
ds_read_b64_tr_b16 a[240:241], v156 offset:40960
ds_read_b64_tr_b16 a[242:243], v156 offset:41984
ds_read_b64_tr_b16 a[244:245], v156 offset:40968
ds_read_b64_tr_b16 a[246:247], v156 offset:41992
ds_read_b64_tr_b16 a[248:249], v156 offset:57344
ds_read_b64_tr_b16 a[250:251], v156 offset:58368
ds_read_b64_tr_b16 a[252:253], v156 offset:57352
ds_read_b64_tr_b16 a[254:255], v156 offset:58376
v_mfma_f32_16x16x32_bf16 v[158:161], v[174:177], a[192:195], v[158:161]
v_mfma_f32_16x16x32_bf16 v[162:165], v[174:177], a[196:199], v[162:165]
v_mfma_f32_16x16x32_bf16 v[166:169], v[174:177], a[200:203], v[166:169]
v_mfma_f32_16x16x32_bf16 v[170:173], v[174:177], a[204:207], v[170:173]
v_mfma_f32_16x16x32_bf16 v[158:161], v[174:177], a[208:211], v[158:161]
v_mfma_f32_16x16x32_bf16 v[162:165], v[174:177], a[212:215], v[162:165]
v_mfma_f32_16x16x32_bf16 v[166:169], v[174:177], a[216:219], v[166:169]
v_mfma_f32_16x16x32_bf16 v[170:173], v[174:177], a[220:223], v[170:173]
s_waitcnt vmcnt(0) lgkmcnt(0)
s_barrier
ds_read_b64_tr_b16 v[26:27], v1 offset:0
ds_read_b64_tr_b16 v[28:29], v1 offset:1024
ds_read_b64_tr_b16 v[30:31], v1 offset:8
ds_read_b64_tr_b16 v[32:33], v1 offset:1032
ds_read_b64_tr_b16 v[34:35], v1 offset:16384
)"
R"(ds_read_b64_tr_b16 v[36:37], v1 offset:17408
ds_read_b64_tr_b16 v[38:39], v1 offset:16392
ds_read_b64_tr_b16 v[40:41], v1 offset:17416
ds_read_b64_tr_b16 v[42:43], v1 offset:8192
ds_read_b64_tr_b16 v[44:45], v1 offset:9216
ds_read_b64_tr_b16 v[46:47], v1 offset:8200
ds_read_b64_tr_b16 v[48:49], v1 offset:9224
ds_read_b64_tr_b16 v[50:51], v1 offset:24576
ds_read_b64_tr_b16 v[52:53], v1 offset:25600
ds_read_b64_tr_b16 v[54:55], v1 offset:24584
ds_read_b64_tr_b16 v[56:57], v1 offset:25608
v_mfma_f32_16x16x32_bf16 v[158:161], v[174:177], a[224:227], v[158:161]
v_mfma_f32_16x16x32_bf16 v[162:165], v[174:177], a[228:231], v[162:165]
v_mfma_f32_16x16x32_bf16 v[166:169], v[174:177], a[232:235], v[166:169]
v_mfma_f32_16x16x32_bf16 v[170:173], v[174:177], a[236:239], v[170:173]
v_mfma_f32_16x16x32_bf16 v[158:161], v[174:177], a[240:243], v[158:161]
v_mfma_f32_16x16x32_bf16 v[162:165], v[174:177], a[244:247], v[162:165]
v_mfma_f32_16x16x32_bf16 v[166:169], v[174:177], a[248:251], v[166:169]
v_mfma_f32_16x16x32_bf16 v[170:173], v[174:177], a[252:255], v[170:173]
s_waitcnt lgkmcnt(14)
v_mfma_f32_16x16x32_bf16 v[158:161], v[174:177], v[26:29], v[158:161]
s_waitcnt lgkmcnt(12)
v_mfma_f32_16x16x32_bf16 v[162:165], v[174:177], v[30:33], v[162:165]
s_waitcnt lgkmcnt(10)
v_mfma_f32_16x16x32_bf16 v[166:169], v[174:177], v[34:37], v[166:169]
s_waitcnt lgkmcnt(8)
v_mfma_f32_16x16x32_bf16 v[170:173], v[174:177], v[38:41], v[170:173]
s_waitcnt lgkmcnt(6)
v_mfma_f32_16x16x32_bf16 v[158:161], v[174:177], v[42:45], v[158:161]
s_waitcnt lgkmcnt(4)
v_mfma_f32_16x16x32_bf16 v[162:165], v[174:177], v[46:49], v[162:165]
s_waitcnt lgkmcnt(2)
v_mfma_f32_16x16x32_bf16 v[166:169], v[174:177], v[50:53], v[166:169]
s_waitcnt lgkmcnt(0)
v_mfma_f32_16x16x32_bf16 v[170:173], v[174:177], v[54:57], v[170:173]
s_nop 1
v_mul_f32 v158, 0xbae38e39, v158
v_mul_f32 v159, 0xbae38e39, v159
v_mul_f32 v160, 0xbae38e39, v160
v_mul_f32 v161, 0xbae38e39, v161
v_mul_f32 v162, 0xbae38e39, v162
v_mul_f32 v163, 0xbae38e39, v163
v_mul_f32 v164, 0xbae38e39, v164
v_mul_f32 v165, 0xbae38e39, v165
v_mul_f32 v166, 0xbae38e39, v166
v_mul_f32 v167, 0xbae38e39, v167
v_mul_f32 v168, 0xbae38e39, v168
v_mul_f32 v169, 0xbae38e39, v169
v_mul_f32 v170, 0xbae38e39, v170
v_mul_f32 v171, 0xbae38e39, v171
v_mul_f32 v172, 0xbae38e39, v172
v_mul_f32 v173, 0xbae38e39, v173
v_mfma_f32_16x16x32_bf16 v[178:181], v[22:25], a[0:3], v[158:161]
v_mfma_f32_16x16x32_bf16 v[182:185], v[20:23], a[0:3], v[158:161]
v_mfma_f32_16x16x32_bf16 v[186:189], v[22:25], a[4:7], v[162:165]
v_mfma_f32_16x16x32_bf16 v[190:193], v[20:23], a[4:7], v[162:165]
v_mfma_f32_16x16x32_bf16 v[194:197], v[22:25], a[8:11], v[166:169]
s_nop 3
v_cvt_pk_bf16_f32 v210, v178, v179
v_cvt_pk_bf16_f32 v211, v180, v181
v_cvt_pk_bf16_f32 v212, v182, v183
v_cvt_pk_bf16_f32 v213, v184, v185
v_mfma_f32_16x16x32_bf16 v[198:201], v[20:23], a[8:11], v[166:169]
v_accvgpr_write_b32 a0, v210
v_accvgpr_write_b32 a1, v211
v_accvgpr_write_b32 a2, v212
v_accvgpr_write_b32 a3, v213
v_mfma_f32_16x16x32_bf16 v[202:205], v[22:25], a[12:15], v[170:173]
v_cvt_pk_bf16_f32 v214, v186, v187
v_cvt_pk_bf16_f32 v215, v188, v189
v_cvt_pk_bf16_f32 v216, v190, v191
v_cvt_pk_bf16_f32 v217, v192, v193
v_mfma_f32_16x16x32_bf16 v[206:209], v[20:23], a[12:15], v[170:173]
v_accvgpr_write_b32 a4, v214
v_accvgpr_write_b32 a5, v215
v_accvgpr_write_b32 a6, v216
v_accvgpr_write_b32 a7, v217
v_mfma_f32_16x16x32_bf16 v[178:181], v[22:25], a[16:19], v[158:161]
v_cvt_pk_bf16_f32 v210, v194, v195
v_cvt_pk_bf16_f32 v211, v196, v197
v_cvt_pk_bf16_f32 v212, v198, v199
v_cvt_pk_bf16_f32 v213, v200, v201
v_mfma_f32_16x16x32_bf16 v[182:185], v[20:23], a[16:19], v[158:161]
v_accvgpr_write_b32 a8, v210
v_accvgpr_write_b32 a9, v211
v_accvgpr_write_b32 a10, v212
v_accvgpr_write_b32 a11, v213
v_mfma_f32_16x16x32_bf16 v[186:189], v[22:25], a[20:23], v[162:165]
v_cvt_pk_bf16_f32 v214, v202, v203
v_cvt_pk_bf16_f32 v215, v204, v205
v_cvt_pk_bf16_f32 v216, v206, v207
v_cvt_pk_bf16_f32 v217, v208, v209
v_mfma_f32_16x16x32_bf16 v[190:193], v[20:23], a[20:23], v[162:165]
v_accvgpr_write_b32 a12, v214
v_accvgpr_write_b32 a13, v215
v_accvgpr_write_b32 a14, v216
v_accvgpr_write_b32 a15, v217
v_mfma_f32_16x16x32_bf16 v[194:197], v[22:25], a[24:27], v[166:169]
v_cvt_pk_bf16_f32 v210, v178, v179
v_cvt_pk_bf16_f32 v211, v180, v181
v_cvt_pk_bf16_f32 v212, v182, v183
v_cvt_pk_bf16_f32 v213, v184, v185
v_mfma_f32_16x16x32_bf16 v[198:201], v[20:23], a[24:27], v[166:169]
v_accvgpr_write_b32 a16, v210
v_accvgpr_write_b32 a17, v211
v_accvgpr_write_b32 a18, v212
v_accvgpr_write_b32 a19, v213
v_mfma_f32_16x16x32_bf16 v[202:205], v[22:25], a[28:31], v[170:173]
v_cvt_pk_bf16_f32 v214, v186, v187
v_cvt_pk_bf16_f32 v215, v188, v189
v_cvt_pk_bf16_f32 v216, v190, v191
v_cvt_pk_bf16_f32 v217, v192, v193
v_mfma_f32_16x16x32_bf16 v[206:209], v[20:23], a[28:31], v[170:173]
v_accvgpr_write_b32 a20, v214
v_accvgpr_write_b32 a21, v215
v_accvgpr_write_b32 a22, v216
v_accvgpr_write_b32 a23, v217
v_mfma_f32_16x16x32_bf16 v[178:181], v[22:25], a[32:35], v[158:161]
v_cvt_pk_bf16_f32 v210, v194, v195
v_cvt_pk_bf16_f32 v211, v196, v197
v_cvt_pk_bf16_f32 v212, v198, v199
v_cvt_pk_bf16_f32 v213, v200, v201
v_mfma_f32_16x16x32_bf16 v[182:185], v[20:23], a[32:35], v[158:161]
v_accvgpr_write_b32 a24, v210
v_accvgpr_write_b32 a25, v211
v_accvgpr_write_b32 a26, v212
v_accvgpr_write_b32 a27, v213
v_mfma_f32_16x16x32_bf16 v[186:189], v[22:25], a[36:39], v[162:165]
v_cvt_pk_bf16_f32 v214, v202, v203
v_cvt_pk_bf16_f32 v215, v204, v205
v_cvt_pk_bf16_f32 v216, v206, v207
v_cvt_pk_bf16_f32 v217, v208, v209
v_mfma_f32_16x16x32_bf16 v[190:193], v[20:23], a[36:39], v[162:165]
v_accvgpr_write_b32 a28, v214
v_accvgpr_write_b32 a29, v215
v_accvgpr_write_b32 a30, v216
v_accvgpr_write_b32 a31, v217
v_mfma_f32_16x16x32_bf16 v[194:197], v[22:25], a[40:43], v[166:169]
v_cvt_pk_bf16_f32 v210, v178, v179
v_cvt_pk_bf16_f32 v211, v180, v181
v_cvt_pk_bf16_f32 v212, v182, v183
v_cvt_pk_bf16_f32 v213, v184, v185
v_mfma_f32_16x16x32_bf16 v[198:201], v[20:23], a[40:43], v[166:169]
v_accvgpr_write_b32 a32, v210
v_accvgpr_write_b32 a33, v211
v_accvgpr_write_b32 a34, v212
v_accvgpr_write_b32 a35, v213
v_mfma_f32_16x16x32_bf16 v[202:205], v[22:25], a[44:47], v[170:173]
v_cvt_pk_bf16_f32 v214, v186, v187
v_cvt_pk_bf16_f32 v215, v188, v189
v_cvt_pk_bf16_f32 v216, v190, v191
v_cvt_pk_bf16_f32 v217, v192, v193
v_mfma_f32_16x16x32_bf16 v[206:209], v[20:23], a[44:47], v[170:173]
v_accvgpr_write_b32 a36, v214
v_accvgpr_write_b32 a37, v215
v_accvgpr_write_b32 a38, v216
v_accvgpr_write_b32 a39, v217
v_mfma_f32_16x16x32_bf16 v[178:181], v[22:25], a[48:51], v[158:161]
v_cvt_pk_bf16_f32 v210, v194, v195
v_cvt_pk_bf16_f32 v211, v196, v197
v_cvt_pk_bf16_f32 v212, v198, v199
v_cvt_pk_bf16_f32 v213, v200, v201
v_mfma_f32_16x16x32_bf16 v[182:185], v[20:23], a[48:51], v[158:161]
v_accvgpr_write_b32 a40, v210
v_accvgpr_write_b32 a41, v211
v_accvgpr_write_b32 a42, v212
v_accvgpr_write_b32 a43, v213
v_mfma_f32_16x16x32_bf16 v[186:189], v[22:25], a[52:55], v[162:165]
v_cvt_pk_bf16_f32 v214, v202, v203
v_cvt_pk_bf16_f32 v215, v204, v205
v_cvt_pk_bf16_f32 v216, v206, v207
v_cvt_pk_bf16_f32 v217, v208, v209
v_mfma_f32_16x16x32_bf16 v[190:193], v[20:23], a[52:55], v[162:165]
v_accvgpr_write_b32 a44, v214
v_accvgpr_write_b32 a45, v215
v_accvgpr_write_b32 a46, v216
v_accvgpr_write_b32 a47, v217
v_mfma_f32_16x16x32_bf16 v[194:197], v[22:25], a[56:59], v[166:169]
v_cvt_pk_bf16_f32 v210, v178, v179
v_cvt_pk_bf16_f32 v211, v180, v181
v_cvt_pk_bf16_f32 v212, v182, v183
v_cvt_pk_bf16_f32 v213, v184, v185
v_mfma_f32_16x16x32_bf16 v[198:201], v[20:23], a[56:59], v[166:169]
v_accvgpr_write_b32 a48, v210
v_accvgpr_write_b32 a49, v211
v_accvgpr_write_b32 a50, v212
v_accvgpr_write_b32 a51, v213
v_mfma_f32_16x16x32_bf16 v[202:205], v[22:25], a[60:63], v[170:173]
v_cvt_pk_bf16_f32 v214, v186, v187
v_cvt_pk_bf16_f32 v215, v188, v189
v_cvt_pk_bf16_f32 v216, v190, v191
v_cvt_pk_bf16_f32 v217, v192, v193
v_mfma_f32_16x16x32_bf16 v[206:209], v[20:23], a[60:63], v[170:173]
v_accvgpr_write_b32 a52, v214
v_accvgpr_write_b32 a53, v215
v_accvgpr_write_b32 a54, v216
v_accvgpr_write_b32 a55, v217
v_mfma_f32_16x16x32_bf16 v[178:181], v[22:25], a[64:67], v[158:161]
v_cvt_pk_bf16_f32 v210, v194, v195
v_cvt_pk_bf16_f32 v211, v196, v197
v_cvt_pk_bf16_f32 v212, v198, v199
v_cvt_pk_bf16_f32 v213, v200, v201
v_mfma_f32_16x16x32_bf16 v[182:185], v[20:23], a[64:67], v[158:161]
v_accvgpr_write_b32 a56, v210
v_accvgpr_write_b32 a57, v211
v_accvgpr_write_b32 a58, v212
v_accvgpr_write_b32 a59, v213
v_mfma_f32_16x16x32_bf16 v[186:189], v[22:25], a[68:71], v[162:165]
v_cvt_pk_bf16_f32 v214, v202, v203
v_cvt_pk_bf16_f32 v215, v204, v205
v_cvt_pk_bf16_f32 v216, v206, v207
v_cvt_pk_bf16_f32 v217, v208, v209
v_mfma_f32_16x16x32_bf16 v[190:193], v[20:23], a[68:71], v[162:165]
v_accvgpr_write_b32 a60, v214
v_accvgpr_write_b32 a61, v215
v_accvgpr_write_b32 a62, v216
v_accvgpr_write_b32 a63, v217
v_mfma_f32_16x16x32_bf16 v[194:197], v[22:25], a[72:75], v[166:169]
v_cvt_pk_bf16_f32 v210, v178, v179
v_cvt_pk_bf16_f32 v211, v180, v181
v_cvt_pk_bf16_f32 v212, v182, v183
v_cvt_pk_bf16_f32 v213, v184, v185
v_mfma_f32_16x16x32_bf16 v[198:201], v[20:23], a[72:75], v[166:169]
v_accvgpr_write_b32 a64, v210
v_accvgpr_write_b32 a65, v211
v_accvgpr_write_b32 a66, v212
v_accvgpr_write_b32 a67, v213
v_mfma_f32_16x16x32_bf16 v[202:205], v[22:25], a[76:79], v[170:173]
v_cvt_pk_bf16_f32 v214, v186, v187
v_cvt_pk_bf16_f32 v215, v188, v189
v_cvt_pk_bf16_f32 v216, v190, v191
v_cvt_pk_bf16_f32 v217, v192, v193
v_mfma_f32_16x16x32_bf16 v[206:209], v[20:23], a[76:79], v[170:173]
v_accvgpr_write_b32 a68, v214
v_accvgpr_write_b32 a69, v215
v_accvgpr_write_b32 a70, v216
v_accvgpr_write_b32 a71, v217
v_mfma_f32_16x16x32_bf16 v[178:181], v[22:25], a[80:83], v[158:161]
v_cvt_pk_bf16_f32 v210, v194, v195
v_cvt_pk_bf16_f32 v211, v196, v197
v_cvt_pk_bf16_f32 v212, v198, v199
v_cvt_pk_bf16_f32 v213, v200, v201
v_mfma_f32_16x16x32_bf16 v[182:185], v[20:23], a[80:83], v[158:161]
v_accvgpr_write_b32 a72, v210
v_accvgpr_write_b32 a73, v211
v_accvgpr_write_b32 a74, v212
v_accvgpr_write_b32 a75, v213
v_mfma_f32_16x16x32_bf16 v[186:189], v[22:25], a[84:87], v[162:165]
v_cvt_pk_bf16_f32 v214, v202, v203
v_cvt_pk_bf16_f32 v215, v204, v205
v_cvt_pk_bf16_f32 v216, v206, v207
v_cvt_pk_bf16_f32 v217, v208, v209
v_mfma_f32_16x16x32_bf16 v[190:193], v[20:23], a[84:87], v[162:165]
v_accvgpr_write_b32 a76, v214
v_accvgpr_write_b32 a77, v215
v_accvgpr_write_b32 a78, v216
v_accvgpr_write_b32 a79, v217
v_mfma_f32_16x16x32_bf16 v[194:197], v[22:25], a[88:91], v[166:169]
v_cvt_pk_bf16_f32 v210, v178, v179
v_cvt_pk_bf16_f32 v211, v180, v181
v_cvt_pk_bf16_f32 v212, v182, v183
v_cvt_pk_bf16_f32 v213, v184, v185
v_mfma_f32_16x16x32_bf16 v[198:201], v[20:23], a[88:91], v[166:169]
v_accvgpr_write_b32 a80, v210
v_accvgpr_write_b32 a81, v211
v_accvgpr_write_b32 a82, v212
v_accvgpr_write_b32 a83, v213
v_mfma_f32_16x16x32_bf16 v[202:205], v[22:25], a[92:95], v[170:173]
v_cvt_pk_bf16_f32 v214, v186, v187
v_cvt_pk_bf16_f32 v215, v188, v189
v_cvt_pk_bf16_f32 v216, v190, v191
v_cvt_pk_bf16_f32 v217, v192, v193
v_mfma_f32_16x16x32_bf16 v[206:209], v[20:23], a[92:95], v[170:173]
v_accvgpr_write_b32 a84, v214
v_accvgpr_write_b32 a85, v215
v_accvgpr_write_b32 a86, v216
v_accvgpr_write_b32 a87, v217
v_mfma_f32_16x16x32_bf16 v[178:181], v[22:25], a[96:99], v[158:161]
v_cvt_pk_bf16_f32 v210, v194, v195
v_cvt_pk_bf16_f32 v211, v196, v197
v_cvt_pk_bf16_f32 v212, v198, v199
v_cvt_pk_bf16_f32 v213, v200, v201
v_mfma_f32_16x16x32_bf16 v[182:185], v[20:23], a[96:99], v[158:161]
v_accvgpr_write_b32 a88, v210
v_accvgpr_write_b32 a89, v211
v_accvgpr_write_b32 a90, v212
v_accvgpr_write_b32 a91, v213
v_mfma_f32_16x16x32_bf16 v[186:189], v[22:25], a[100:103], v[162:165]
v_cvt_pk_bf16_f32 v214, v202, v203
v_cvt_pk_bf16_f32 v215, v204, v205
v_cvt_pk_bf16_f32 v216, v206, v207
v_cvt_pk_bf16_f32 v217, v208, v209
v_mfma_f32_16x16x32_bf16 v[190:193], v[20:23], a[100:103], v[162:165]
v_accvgpr_write_b32 a92, v214
v_accvgpr_write_b32 a93, v215
v_accvgpr_write_b32 a94, v216
v_accvgpr_write_b32 a95, v217
v_mfma_f32_16x16x32_bf16 v[194:197], v[22:25], a[104:107], v[166:169]
v_cvt_pk_bf16_f32 v210, v178, v179
v_cvt_pk_bf16_f32 v211, v180, v181
v_cvt_pk_bf16_f32 v212, v182, v183
v_cvt_pk_bf16_f32 v213, v184, v185
v_mfma_f32_16x16x32_bf16 v[198:201], v[20:23], a[104:107], v[166:169]
v_accvgpr_write_b32 a96, v210
v_accvgpr_write_b32 a97, v211
v_accvgpr_write_b32 a98, v212
v_accvgpr_write_b32 a99, v213
v_mfma_f32_16x16x32_bf16 v[202:205], v[22:25], a[108:111], v[170:173]
v_cvt_pk_bf16_f32 v214, v186, v187
v_cvt_pk_bf16_f32 v215, v188, v189
v_cvt_pk_bf16_f32 v216, v190, v191
v_cvt_pk_bf16_f32 v217, v192, v193
v_mfma_f32_16x16x32_bf16 v[206:209], v[20:23], a[108:111], v[170:173]
v_accvgpr_write_b32 a100, v214
v_accvgpr_write_b32 a101, v215
v_accvgpr_write_b32 a102, v216
v_accvgpr_write_b32 a103, v217
v_mfma_f32_16x16x32_bf16 v[178:181], v[22:25], a[112:115], v[158:161]
v_cvt_pk_bf16_f32 v210, v194, v195
v_cvt_pk_bf16_f32 v211, v196, v197
v_cvt_pk_bf16_f32 v212, v198, v199
v_cvt_pk_bf16_f32 v213, v200, v201
v_mfma_f32_16x16x32_bf16 v[182:185], v[20:23], a[112:115], v[158:161]
v_accvgpr_write_b32 a104, v210
v_accvgpr_write_b32 a105, v211
v_accvgpr_write_b32 a106, v212
v_accvgpr_write_b32 a107, v213
v_mfma_f32_16x16x32_bf16 v[186:189], v[22:25], a[116:119], v[162:165]
v_cvt_pk_bf16_f32 v214, v202, v203
v_cvt_pk_bf16_f32 v215, v204, v205
v_cvt_pk_bf16_f32 v216, v206, v207
v_cvt_pk_bf16_f32 v217, v208, v209
v_mfma_f32_16x16x32_bf16 v[190:193], v[20:23], a[116:119], v[162:165]
v_accvgpr_write_b32 a108, v214
v_accvgpr_write_b32 a109, v215
v_accvgpr_write_b32 a110, v216
v_accvgpr_write_b32 a111, v217
v_mfma_f32_16x16x32_bf16 v[194:197], v[22:25], a[120:123], v[166:169]
v_cvt_pk_bf16_f32 v210, v178, v179
v_cvt_pk_bf16_f32 v211, v180, v181
v_cvt_pk_bf16_f32 v212, v182, v183
v_cvt_pk_bf16_f32 v213, v184, v185
v_mfma_f32_16x16x32_bf16 v[198:201], v[20:23], a[120:123], v[166:169]
v_accvgpr_write_b32 a112, v210
v_accvgpr_write_b32 a113, v211
v_accvgpr_write_b32 a114, v212
v_accvgpr_write_b32 a115, v213
v_mfma_f32_16x16x32_bf16 v[202:205], v[22:25], a[124:127], v[170:173]
v_cvt_pk_bf16_f32 v214, v186, v187
v_cvt_pk_bf16_f32 v215, v188, v189
v_cvt_pk_bf16_f32 v216, v190, v191
v_cvt_pk_bf16_f32 v217, v192, v193
v_mfma_f32_16x16x32_bf16 v[206:209], v[20:23], a[124:127], v[170:173]
v_accvgpr_write_b32 a116, v214
v_accvgpr_write_b32 a117, v215
v_accvgpr_write_b32 a118, v216
v_accvgpr_write_b32 a119, v217
v_mfma_f32_16x16x32_bf16 v[178:181], v[22:25], a[128:131], v[158:161]
v_cvt_pk_bf16_f32 v210, v194, v195
v_cvt_pk_bf16_f32 v211, v196, v197
v_cvt_pk_bf16_f32 v212, v198, v199
v_cvt_pk_bf16_f32 v213, v200, v201
v_mfma_f32_16x16x32_bf16 v[182:185], v[20:23], a[128:131], v[158:161]
v_accvgpr_write_b32 a120, v210
v_accvgpr_write_b32 a121, v211
v_accvgpr_write_b32 a122, v212
v_accvgpr_write_b32 a123, v213
v_mfma_f32_16x16x32_bf16 v[186:189], v[22:25], a[132:135], v[162:165]
v_cvt_pk_bf16_f32 v214, v202, v203
v_cvt_pk_bf16_f32 v215, v204, v205
v_cvt_pk_bf16_f32 v216, v206, v207
v_cvt_pk_bf16_f32 v217, v208, v209
v_mfma_f32_16x16x32_bf16 v[190:193], v[20:23], a[132:135], v[162:165]
v_accvgpr_write_b32 a124, v214
v_accvgpr_write_b32 a125, v215
v_accvgpr_write_b32 a126, v216
v_accvgpr_write_b32 a127, v217
v_mfma_f32_16x16x32_bf16 v[194:197], v[22:25], a[136:139], v[166:169]
v_cvt_pk_bf16_f32 v210, v178, v179
v_cvt_pk_bf16_f32 v211, v180, v181
v_cvt_pk_bf16_f32 v212, v182, v183
v_cvt_pk_bf16_f32 v213, v184, v185
v_mfma_f32_16x16x32_bf16 v[198:201], v[20:23], a[136:139], v[166:169]
v_accvgpr_write_b32 a128, v210
v_accvgpr_write_b32 a129, v211
v_accvgpr_write_b32 a130, v212
v_accvgpr_write_b32 a131, v213
v_mfma_f32_16x16x32_bf16 v[202:205], v[22:25], a[140:143], v[170:173]
v_cvt_pk_bf16_f32 v214, v186, v187
v_cvt_pk_bf16_f32 v215, v188, v189
v_cvt_pk_bf16_f32 v216, v190, v191
v_cvt_pk_bf16_f32 v217, v192, v193
v_mfma_f32_16x16x32_bf16 v[206:209], v[20:23], a[140:143], v[170:173]
v_accvgpr_write_b32 a132, v214
v_accvgpr_write_b32 a133, v215
v_accvgpr_write_b32 a134, v216
v_accvgpr_write_b32 a135, v217
v_mfma_f32_16x16x32_bf16 v[178:181], v[22:25], a[144:147], v[158:161]
v_cvt_pk_bf16_f32 v210, v194, v195
v_cvt_pk_bf16_f32 v211, v196, v197
)"
R"(v_cvt_pk_bf16_f32 v212, v198, v199
v_cvt_pk_bf16_f32 v213, v200, v201
v_mfma_f32_16x16x32_bf16 v[182:185], v[20:23], a[144:147], v[158:161]
v_accvgpr_write_b32 a136, v210
v_accvgpr_write_b32 a137, v211
v_accvgpr_write_b32 a138, v212
v_accvgpr_write_b32 a139, v213
v_mfma_f32_16x16x32_bf16 v[186:189], v[22:25], a[148:151], v[162:165]
v_cvt_pk_bf16_f32 v214, v202, v203
v_cvt_pk_bf16_f32 v215, v204, v205
v_cvt_pk_bf16_f32 v216, v206, v207
v_cvt_pk_bf16_f32 v217, v208, v209
v_mfma_f32_16x16x32_bf16 v[190:193], v[20:23], a[148:151], v[162:165]
v_accvgpr_write_b32 a140, v214
v_accvgpr_write_b32 a141, v215
v_accvgpr_write_b32 a142, v216
v_accvgpr_write_b32 a143, v217
v_mfma_f32_16x16x32_bf16 v[194:197], v[22:25], a[152:155], v[166:169]
v_cvt_pk_bf16_f32 v210, v178, v179
v_cvt_pk_bf16_f32 v211, v180, v181
v_cvt_pk_bf16_f32 v212, v182, v183
v_cvt_pk_bf16_f32 v213, v184, v185
v_mfma_f32_16x16x32_bf16 v[198:201], v[20:23], a[152:155], v[166:169]
v_accvgpr_write_b32 a144, v210
v_accvgpr_write_b32 a145, v211
v_accvgpr_write_b32 a146, v212
v_accvgpr_write_b32 a147, v213
v_mfma_f32_16x16x32_bf16 v[202:205], v[22:25], a[156:159], v[170:173]
v_cvt_pk_bf16_f32 v214, v186, v187
v_cvt_pk_bf16_f32 v215, v188, v189
v_cvt_pk_bf16_f32 v216, v190, v191
v_cvt_pk_bf16_f32 v217, v192, v193
v_mfma_f32_16x16x32_bf16 v[206:209], v[20:23], a[156:159], v[170:173]
v_accvgpr_write_b32 a148, v214
v_accvgpr_write_b32 a149, v215
v_accvgpr_write_b32 a150, v216
v_accvgpr_write_b32 a151, v217
v_mfma_f32_16x16x32_bf16 v[178:181], v[22:25], a[160:163], v[158:161]
v_cvt_pk_bf16_f32 v210, v194, v195
v_cvt_pk_bf16_f32 v211, v196, v197
v_cvt_pk_bf16_f32 v212, v198, v199
v_cvt_pk_bf16_f32 v213, v200, v201
v_mfma_f32_16x16x32_bf16 v[182:185], v[20:23], a[160:163], v[158:161]
v_accvgpr_write_b32 a152, v210
v_accvgpr_write_b32 a153, v211
v_accvgpr_write_b32 a154, v212
v_accvgpr_write_b32 a155, v213
v_mfma_f32_16x16x32_bf16 v[186:189], v[22:25], a[164:167], v[162:165]
v_cvt_pk_bf16_f32 v214, v202, v203
v_cvt_pk_bf16_f32 v215, v204, v205
v_cvt_pk_bf16_f32 v216, v206, v207
v_cvt_pk_bf16_f32 v217, v208, v209
v_mfma_f32_16x16x32_bf16 v[190:193], v[20:23], a[164:167], v[162:165]
v_accvgpr_write_b32 a156, v214
v_accvgpr_write_b32 a157, v215
v_accvgpr_write_b32 a158, v216
v_accvgpr_write_b32 a159, v217
v_mfma_f32_16x16x32_bf16 v[194:197], v[22:25], a[168:171], v[166:169]
v_cvt_pk_bf16_f32 v210, v178, v179
v_cvt_pk_bf16_f32 v211, v180, v181
v_cvt_pk_bf16_f32 v212, v182, v183
v_cvt_pk_bf16_f32 v213, v184, v185
v_mfma_f32_16x16x32_bf16 v[198:201], v[20:23], a[168:171], v[166:169]
v_accvgpr_write_b32 a160, v210
v_accvgpr_write_b32 a161, v211
v_accvgpr_write_b32 a162, v212
v_accvgpr_write_b32 a163, v213
v_mfma_f32_16x16x32_bf16 v[202:205], v[22:25], a[172:175], v[170:173]
v_cvt_pk_bf16_f32 v214, v186, v187
v_cvt_pk_bf16_f32 v215, v188, v189
v_cvt_pk_bf16_f32 v216, v190, v191
v_cvt_pk_bf16_f32 v217, v192, v193
v_mfma_f32_16x16x32_bf16 v[206:209], v[20:23], a[172:175], v[170:173]
v_accvgpr_write_b32 a164, v214
v_accvgpr_write_b32 a165, v215
v_accvgpr_write_b32 a166, v216
v_accvgpr_write_b32 a167, v217
v_mfma_f32_16x16x32_bf16 v[178:181], v[22:25], a[176:179], v[158:161]
v_cvt_pk_bf16_f32 v210, v194, v195
v_cvt_pk_bf16_f32 v211, v196, v197
v_cvt_pk_bf16_f32 v212, v198, v199
v_cvt_pk_bf16_f32 v213, v200, v201
v_mfma_f32_16x16x32_bf16 v[182:185], v[20:23], a[176:179], v[158:161]
v_accvgpr_write_b32 a168, v210
v_accvgpr_write_b32 a169, v211
v_accvgpr_write_b32 a170, v212
v_accvgpr_write_b32 a171, v213
v_mfma_f32_16x16x32_bf16 v[186:189], v[22:25], a[180:183], v[162:165]
v_cvt_pk_bf16_f32 v214, v202, v203
v_cvt_pk_bf16_f32 v215, v204, v205
v_cvt_pk_bf16_f32 v216, v206, v207
v_cvt_pk_bf16_f32 v217, v208, v209
v_mfma_f32_16x16x32_bf16 v[190:193], v[20:23], a[180:183], v[162:165]
v_accvgpr_write_b32 a172, v214
v_accvgpr_write_b32 a173, v215
v_accvgpr_write_b32 a174, v216
v_accvgpr_write_b32 a175, v217
v_mfma_f32_16x16x32_bf16 v[194:197], v[22:25], a[184:187], v[166:169]
v_cvt_pk_bf16_f32 v210, v178, v179
v_cvt_pk_bf16_f32 v211, v180, v181
v_cvt_pk_bf16_f32 v212, v182, v183
v_cvt_pk_bf16_f32 v213, v184, v185
v_mfma_f32_16x16x32_bf16 v[198:201], v[20:23], a[184:187], v[166:169]
v_accvgpr_write_b32 a176, v210
v_accvgpr_write_b32 a177, v211
v_accvgpr_write_b32 a178, v212
v_accvgpr_write_b32 a179, v213
v_mfma_f32_16x16x32_bf16 v[202:205], v[22:25], a[188:191], v[170:173]
v_cvt_pk_bf16_f32 v214, v186, v187
v_cvt_pk_bf16_f32 v215, v188, v189
v_cvt_pk_bf16_f32 v216, v190, v191
v_cvt_pk_bf16_f32 v217, v192, v193
v_mfma_f32_16x16x32_bf16 v[206:209], v[20:23], a[188:191], v[170:173]
v_accvgpr_write_b32 a180, v214
v_accvgpr_write_b32 a181, v215
v_accvgpr_write_b32 a182, v216
v_accvgpr_write_b32 a183, v217
v_mfma_f32_16x16x32_bf16 v[178:181], v[22:25], a[192:195], v[158:161]
v_cvt_pk_bf16_f32 v210, v194, v195
v_cvt_pk_bf16_f32 v211, v196, v197
v_cvt_pk_bf16_f32 v212, v198, v199
v_cvt_pk_bf16_f32 v213, v200, v201
v_mfma_f32_16x16x32_bf16 v[182:185], v[20:23], a[192:195], v[158:161]
v_accvgpr_write_b32 a184, v210
v_accvgpr_write_b32 a185, v211
v_accvgpr_write_b32 a186, v212
v_accvgpr_write_b32 a187, v213
v_mfma_f32_16x16x32_bf16 v[186:189], v[22:25], a[196:199], v[162:165]
v_cvt_pk_bf16_f32 v214, v202, v203
v_cvt_pk_bf16_f32 v215, v204, v205
v_cvt_pk_bf16_f32 v216, v206, v207
v_cvt_pk_bf16_f32 v217, v208, v209
v_mfma_f32_16x16x32_bf16 v[190:193], v[20:23], a[196:199], v[162:165]
v_accvgpr_write_b32 a188, v214
v_accvgpr_write_b32 a189, v215
v_accvgpr_write_b32 a190, v216
v_accvgpr_write_b32 a191, v217
v_mfma_f32_16x16x32_bf16 v[194:197], v[22:25], a[200:203], v[166:169]
v_cvt_pk_bf16_f32 v210, v178, v179
v_cvt_pk_bf16_f32 v211, v180, v181
v_cvt_pk_bf16_f32 v212, v182, v183
v_cvt_pk_bf16_f32 v213, v184, v185
v_mfma_f32_16x16x32_bf16 v[198:201], v[20:23], a[200:203], v[166:169]
v_accvgpr_write_b32 a192, v210
v_accvgpr_write_b32 a193, v211
v_accvgpr_write_b32 a194, v212
v_accvgpr_write_b32 a195, v213
v_mfma_f32_16x16x32_bf16 v[202:205], v[22:25], a[204:207], v[170:173]
v_cvt_pk_bf16_f32 v214, v186, v187
v_cvt_pk_bf16_f32 v215, v188, v189
v_cvt_pk_bf16_f32 v216, v190, v191
v_cvt_pk_bf16_f32 v217, v192, v193
v_mfma_f32_16x16x32_bf16 v[206:209], v[20:23], a[204:207], v[170:173]
v_accvgpr_write_b32 a196, v214
v_accvgpr_write_b32 a197, v215
v_accvgpr_write_b32 a198, v216
v_accvgpr_write_b32 a199, v217
v_mfma_f32_16x16x32_bf16 v[178:181], v[22:25], a[208:211], v[158:161]
v_cvt_pk_bf16_f32 v210, v194, v195
v_cvt_pk_bf16_f32 v211, v196, v197
v_cvt_pk_bf16_f32 v212, v198, v199
v_cvt_pk_bf16_f32 v213, v200, v201
v_mfma_f32_16x16x32_bf16 v[182:185], v[20:23], a[208:211], v[158:161]
v_accvgpr_write_b32 a200, v210
v_accvgpr_write_b32 a201, v211
v_accvgpr_write_b32 a202, v212
v_accvgpr_write_b32 a203, v213
v_mfma_f32_16x16x32_bf16 v[186:189], v[22:25], a[212:215], v[162:165]
v_cvt_pk_bf16_f32 v214, v202, v203
v_cvt_pk_bf16_f32 v215, v204, v205
v_cvt_pk_bf16_f32 v216, v206, v207
v_cvt_pk_bf16_f32 v217, v208, v209
v_mfma_f32_16x16x32_bf16 v[190:193], v[20:23], a[212:215], v[162:165]
v_accvgpr_write_b32 a204, v214
v_accvgpr_write_b32 a205, v215
v_accvgpr_write_b32 a206, v216
v_accvgpr_write_b32 a207, v217
v_mfma_f32_16x16x32_bf16 v[194:197], v[22:25], a[216:219], v[166:169]
v_cvt_pk_bf16_f32 v210, v178, v179
v_cvt_pk_bf16_f32 v211, v180, v181
v_cvt_pk_bf16_f32 v212, v182, v183
v_cvt_pk_bf16_f32 v213, v184, v185
v_mfma_f32_16x16x32_bf16 v[198:201], v[20:23], a[216:219], v[166:169]
v_accvgpr_write_b32 a208, v210
v_accvgpr_write_b32 a209, v211
v_accvgpr_write_b32 a210, v212
v_accvgpr_write_b32 a211, v213
v_mfma_f32_16x16x32_bf16 v[202:205], v[22:25], a[220:223], v[170:173]
v_cvt_pk_bf16_f32 v214, v186, v187
v_cvt_pk_bf16_f32 v215, v188, v189
v_cvt_pk_bf16_f32 v216, v190, v191
v_cvt_pk_bf16_f32 v217, v192, v193
v_mfma_f32_16x16x32_bf16 v[206:209], v[20:23], a[220:223], v[170:173]
v_accvgpr_write_b32 a212, v214
v_accvgpr_write_b32 a213, v215
v_accvgpr_write_b32 a214, v216
v_accvgpr_write_b32 a215, v217
v_mfma_f32_16x16x32_bf16 v[178:181], v[22:25], a[224:227], v[158:161]
v_cvt_pk_bf16_f32 v210, v194, v195
v_cvt_pk_bf16_f32 v211, v196, v197
v_cvt_pk_bf16_f32 v212, v198, v199
v_cvt_pk_bf16_f32 v213, v200, v201
v_mfma_f32_16x16x32_bf16 v[182:185], v[20:23], a[224:227], v[158:161]
v_accvgpr_write_b32 a216, v210
v_accvgpr_write_b32 a217, v211
v_accvgpr_write_b32 a218, v212
v_accvgpr_write_b32 a219, v213
v_mfma_f32_16x16x32_bf16 v[186:189], v[22:25], a[228:231], v[162:165]
v_cvt_pk_bf16_f32 v214, v202, v203
v_cvt_pk_bf16_f32 v215, v204, v205
v_cvt_pk_bf16_f32 v216, v206, v207
v_cvt_pk_bf16_f32 v217, v208, v209
v_mfma_f32_16x16x32_bf16 v[190:193], v[20:23], a[228:231], v[162:165]
v_accvgpr_write_b32 a220, v214
v_accvgpr_write_b32 a221, v215
v_accvgpr_write_b32 a222, v216
v_accvgpr_write_b32 a223, v217
v_mfma_f32_16x16x32_bf16 v[194:197], v[22:25], a[232:235], v[166:169]
v_cvt_pk_bf16_f32 v210, v178, v179
v_cvt_pk_bf16_f32 v211, v180, v181
v_cvt_pk_bf16_f32 v212, v182, v183
v_cvt_pk_bf16_f32 v213, v184, v185
v_mfma_f32_16x16x32_bf16 v[198:201], v[20:23], a[232:235], v[166:169]
v_accvgpr_write_b32 a224, v210
v_accvgpr_write_b32 a225, v211
v_accvgpr_write_b32 a226, v212
v_accvgpr_write_b32 a227, v213
v_mfma_f32_16x16x32_bf16 v[202:205], v[22:25], a[236:239], v[170:173]
v_cvt_pk_bf16_f32 v214, v186, v187
v_cvt_pk_bf16_f32 v215, v188, v189
v_cvt_pk_bf16_f32 v216, v190, v191
v_cvt_pk_bf16_f32 v217, v192, v193
v_mfma_f32_16x16x32_bf16 v[206:209], v[20:23], a[236:239], v[170:173]
v_accvgpr_write_b32 a228, v214
v_accvgpr_write_b32 a229, v215
v_accvgpr_write_b32 a230, v216
v_accvgpr_write_b32 a231, v217
v_mfma_f32_16x16x32_bf16 v[178:181], v[22:25], a[240:243], v[158:161]
v_cvt_pk_bf16_f32 v210, v194, v195
v_cvt_pk_bf16_f32 v211, v196, v197
v_cvt_pk_bf16_f32 v212, v198, v199
v_cvt_pk_bf16_f32 v213, v200, v201
v_mfma_f32_16x16x32_bf16 v[182:185], v[20:23], a[240:243], v[158:161]
v_accvgpr_write_b32 a232, v210
v_accvgpr_write_b32 a233, v211
v_accvgpr_write_b32 a234, v212
v_accvgpr_write_b32 a235, v213
v_mfma_f32_16x16x32_bf16 v[186:189], v[22:25], a[244:247], v[162:165]
v_cvt_pk_bf16_f32 v214, v202, v203
v_cvt_pk_bf16_f32 v215, v204, v205
v_cvt_pk_bf16_f32 v216, v206, v207
v_cvt_pk_bf16_f32 v217, v208, v209
v_mfma_f32_16x16x32_bf16 v[190:193], v[20:23], a[244:247], v[162:165]
v_accvgpr_write_b32 a236, v214
v_accvgpr_write_b32 a237, v215
v_accvgpr_write_b32 a238, v216
v_accvgpr_write_b32 a239, v217
v_mfma_f32_16x16x32_bf16 v[194:197], v[22:25], a[248:251], v[166:169]
v_cvt_pk_bf16_f32 v210, v178, v179
v_cvt_pk_bf16_f32 v211, v180, v181
v_cvt_pk_bf16_f32 v212, v182, v183
v_cvt_pk_bf16_f32 v213, v184, v185
v_mfma_f32_16x16x32_bf16 v[198:201], v[20:23], a[248:251], v[166:169]
v_accvgpr_write_b32 a240, v210
v_accvgpr_write_b32 a241, v211
v_accvgpr_write_b32 a242, v212
v_accvgpr_write_b32 a243, v213
v_mfma_f32_16x16x32_bf16 v[202:205], v[22:25], a[252:255], v[170:173]
v_cvt_pk_bf16_f32 v214, v186, v187
v_cvt_pk_bf16_f32 v215, v188, v189
v_cvt_pk_bf16_f32 v216, v190, v191
v_cvt_pk_bf16_f32 v217, v192, v193
v_mfma_f32_16x16x32_bf16 v[206:209], v[20:23], a[252:255], v[170:173]
v_accvgpr_write_b32 a244, v214
v_accvgpr_write_b32 a245, v215
v_accvgpr_write_b32 a246, v216
v_accvgpr_write_b32 a247, v217
v_mfma_f32_16x16x32_bf16 v[178:181], v[22:25], v[26:29], v[158:161]
v_cvt_pk_bf16_f32 v210, v194, v195
v_cvt_pk_bf16_f32 v211, v196, v197
v_cvt_pk_bf16_f32 v212, v198, v199
v_cvt_pk_bf16_f32 v213, v200, v201
v_mfma_f32_16x16x32_bf16 v[182:185], v[20:23], v[26:29], v[158:161]
v_accvgpr_write_b32 a248, v210
v_accvgpr_write_b32 a249, v211
v_accvgpr_write_b32 a250, v212
v_accvgpr_write_b32 a251, v213
v_mfma_f32_16x16x32_bf16 v[186:189], v[22:25], v[30:33], v[162:165]
v_cvt_pk_bf16_f32 v214, v202, v203
v_cvt_pk_bf16_f32 v215, v204, v205
v_cvt_pk_bf16_f32 v216, v206, v207
v_cvt_pk_bf16_f32 v217, v208, v209
v_mfma_f32_16x16x32_bf16 v[190:193], v[20:23], v[30:33], v[162:165]
v_accvgpr_write_b32 a252, v214
v_accvgpr_write_b32 a253, v215
v_accvgpr_write_b32 a254, v216
v_accvgpr_write_b32 a255, v217
v_mfma_f32_16x16x32_bf16 v[194:197], v[22:25], v[34:37], v[166:169]
v_cvt_pk_bf16_f32 v26, v178, v179
v_cvt_pk_bf16_f32 v27, v180, v181
v_cvt_pk_bf16_f32 v28, v182, v183
v_cvt_pk_bf16_f32 v29, v184, v185
v_mfma_f32_16x16x32_bf16 v[198:201], v[20:23], v[34:37], v[166:169]
v_mfma_f32_16x16x32_bf16 v[202:205], v[22:25], v[38:41], v[170:173]
v_cvt_pk_bf16_f32 v30, v186, v187
v_cvt_pk_bf16_f32 v31, v188, v189
v_cvt_pk_bf16_f32 v32, v190, v191
v_cvt_pk_bf16_f32 v33, v192, v193
v_mfma_f32_16x16x32_bf16 v[206:209], v[20:23], v[38:41], v[170:173]
v_mfma_f32_16x16x32_bf16 v[178:181], v[22:25], v[42:45], v[158:161]
v_cvt_pk_bf16_f32 v34, v194, v195
v_cvt_pk_bf16_f32 v35, v196, v197
v_cvt_pk_bf16_f32 v36, v198, v199
v_cvt_pk_bf16_f32 v37, v200, v201
v_mfma_f32_16x16x32_bf16 v[182:185], v[20:23], v[42:45], v[158:161]
v_mfma_f32_16x16x32_bf16 v[186:189], v[22:25], v[46:49], v[162:165]
v_cvt_pk_bf16_f32 v38, v202, v203
v_cvt_pk_bf16_f32 v39, v204, v205
v_cvt_pk_bf16_f32 v40, v206, v207
v_cvt_pk_bf16_f32 v41, v208, v209
v_mfma_f32_16x16x32_bf16 v[190:193], v[20:23], v[46:49], v[162:165]
v_mfma_f32_16x16x32_bf16 v[194:197], v[22:25], v[50:53], v[166:169]
v_cvt_pk_bf16_f32 v42, v178, v179
v_cvt_pk_bf16_f32 v43, v180, v181
v_cvt_pk_bf16_f32 v44, v182, v183
v_cvt_pk_bf16_f32 v45, v184, v185
v_mfma_f32_16x16x32_bf16 v[198:201], v[20:23], v[50:53], v[166:169]
v_mfma_f32_16x16x32_bf16 v[202:205], v[22:25], v[54:57], v[170:173]
v_cvt_pk_bf16_f32 v46, v186, v187
v_cvt_pk_bf16_f32 v47, v188, v189
v_cvt_pk_bf16_f32 v48, v190, v191
v_cvt_pk_bf16_f32 v49, v192, v193
v_mfma_f32_16x16x32_bf16 v[206:209], v[20:23], v[54:57], v[170:173]
v_cvt_pk_bf16_f32 v50, v194, v195
v_cvt_pk_bf16_f32 v51, v196, v197
v_cvt_pk_bf16_f32 v52, v198, v199
v_cvt_pk_bf16_f32 v53, v200, v201
v_cvt_pk_bf16_f32 v54, v202, v203
v_cvt_pk_bf16_f32 v55, v204, v205
s_nop 1
v_cvt_pk_bf16_f32 v56, v206, v207
v_cvt_pk_bf16_f32 v57, v208, v209
s_barrier
s_and_b32 s75, s36, 3
s_cmp_eq_u32 s75, 1
s_cbranch_scc1 .Lgu_w2
s_cmp_eq_u32 s75, 2
s_cbranch_scc1 .Lgu_w3
s_cmp_eq_u32 s75, 3
s_cbranch_scc1 .Lgu_w4
.Lgu_w1:
v_add_u32 v254, -1, v5
s_mov_b32 s70, 32
s_waitcnt vmcnt(0) lgkmcnt(0)
s_nop 7
.p2align 6
.Lgu_top5:
s_waitcnt lgkmcnt(5)
v_mfma_f32_16x16x32_bf16 v[58:61], a[0:3], v[90:93], 0
s_waitcnt lgkmcnt(4)
v_cvt_pk_bf16_f32 v146, v138, v139
v_cvt_pk_bf16_f32 v147, v140, v141
v_mfma_f32_16x16x32_bf16 v[58:61], a[16:19], v[94:97], v[58:61]
s_waitcnt lgkmcnt(3)
v_mfma_f32_16x16x32_bf16 v[58:61], a[32:35], v[98:101], v[58:61]
s_waitcnt lgkmcnt(2)
v_mfma_f32_16x16x32_bf16 v[58:61], a[48:51], v[102:105], v[58:61]
s_waitcnt lgkmcnt(1)
v_mul_f32 v142, v78, v86
v_mul_f32 v143, v79, v87
v_mfma_f32_16x16x32_bf16 v[58:61], a[64:67], v[106:109], v[58:61]
s_waitcnt lgkmcnt(0)
v_mul_f32 v144, v80, v88
v_mul_f32 v145, v81, v89
v_mfma_f32_16x16x32_bf16 v[58:61], a[80:83], v[110:113], v[58:61]
ds_read_b128 v[114:117], v8 offset:6144
v_cvt_pk_bf16_f32 v148, v142, v143
v_mfma_f32_16x16x32_bf16 v[66:69], a[8:11], v[90:93], 0
ds_read_b128 v[118:121], v8 offset:7168
v_cvt_pk_bf16_f32 v149, v144, v145
v_mfma_f32_16x16x32_bf16 v[66:69], a[24:27], v[94:97], v[66:69]
v_mfma_f32_16x16x32_bf16 v[66:69], a[40:43], v[98:101], v[66:69]
s_waitcnt vmcnt(6)
s_barrier
v_mfma_f32_16x16x32_bf16 v[66:69], a[56:59], v[102:105], v[66:69]
v_mfma_f32_16x16x32_bf16 v[66:69], a[72:75], v[106:109], v[66:69]
global_store_dwordx4 v3, v[146:149], s[38:39] sc1
v_mfma_f32_16x16x32_bf16 v[66:69], a[88:91], v[110:113], v[66:69]
ds_read_b128 v[122:125], v8 offset:8192
v_add_u32 v3, 147456, v3
v_mfma_f32_16x16x32_bf16 v[62:65], a[4:7], v[90:93], 0
v_mfma_f32_16x16x32_bf16 v[62:65], a[20:23], v[94:97], v[62:65]
v_mfma_f32_16x16x32_bf16 v[62:65], a[36:39], v[98:101], v[62:65]
ds_read_b128 v[126:129], v8 offset:9216
v_mfma_f32_16x16x32_bf16 v[62:65], a[52:55], v[102:105], v[62:65]
v_mfma_f32_16x16x32_bf16 v[62:65], a[68:71], v[106:109], v[62:65]
v_mfma_f32_16x16x32_bf16 v[62:65], a[84:87], v[110:113], v[62:65]
ds_read_b128 v[130:133], v8 offset:10240
v_mfma_f32_16x16x32_bf16 v[70:73], a[12:15], v[90:93], 0
v_mfma_f32_16x16x32_bf16 v[70:73], a[28:31], v[94:97], v[70:73]
)"
R"(v_mfma_f32_16x16x32_bf16 v[70:73], a[44:47], v[98:101], v[70:73]
ds_read_b128 v[134:137], v8 offset:11264
v_mfma_f32_16x16x32_bf16 v[70:73], a[60:63], v[102:105], v[70:73]
v_mfma_f32_16x16x32_bf16 v[70:73], a[76:79], v[106:109], v[70:73]
v_mfma_f32_16x16x32_bf16 v[70:73], a[92:95], v[110:113], v[70:73]
s_waitcnt lgkmcnt(5)
v_mfma_f32_16x16x32_bf16 v[58:61], a[96:99], v[114:117], v[58:61]
s_waitcnt lgkmcnt(4)
v_mfma_f32_16x16x32_bf16 v[58:61], a[112:115], v[118:121], v[58:61]
s_waitcnt lgkmcnt(3)
v_mfma_f32_16x16x32_bf16 v[58:61], a[128:131], v[122:125], v[58:61]
s_waitcnt lgkmcnt(2)
v_mfma_f32_16x16x32_bf16 v[58:61], a[144:147], v[126:129], v[58:61]
s_waitcnt lgkmcnt(1)
v_mfma_f32_16x16x32_bf16 v[58:61], a[160:163], v[130:133], v[58:61]
s_waitcnt lgkmcnt(0)
v_mfma_f32_16x16x32_bf16 v[58:61], a[176:179], v[134:137], v[58:61]
ds_read_b128 v[90:93], v8 offset:12288
v_mfma_f32_16x16x32_bf16 v[66:69], a[104:107], v[114:117], v[66:69]
v_mfma_f32_16x16x32_bf16 v[66:69], a[120:123], v[118:121], v[66:69]
v_mfma_f32_16x16x32_bf16 v[66:69], a[136:139], v[122:125], v[66:69]
ds_read_b128 v[94:97], v8 offset:13312
v_mfma_f32_16x16x32_bf16 v[66:69], a[152:155], v[126:129], v[66:69]
v_mfma_f32_16x16x32_bf16 v[66:69], a[168:171], v[130:133], v[66:69]
v_mfma_f32_16x16x32_bf16 v[66:69], a[184:187], v[134:137], v[66:69]
ds_read_b128 v[98:101], v8 offset:14336
v_mfma_f32_16x16x32_bf16 v[62:65], a[100:103], v[114:117], v[62:65]
v_mfma_f32_16x16x32_bf16 v[62:65], a[116:119], v[118:121], v[62:65]
v_mfma_f32_16x16x32_bf16 v[62:65], a[132:135], v[122:125], v[62:65]
ds_read_b128 v[102:105], v8 offset:15360
v_mfma_f32_16x16x32_bf16 v[62:65], a[148:151], v[126:129], v[62:65]
v_mfma_f32_16x16x32_bf16 v[62:65], a[164:167], v[130:133], v[62:65]
v_mfma_f32_16x16x32_bf16 v[62:65], a[180:183], v[134:137], v[62:65]
ds_read_b128 v[106:109], v8 offset:16384
v_mfma_f32_16x16x32_bf16 v[70:73], a[108:111], v[114:117], v[70:73]
v_mfma_f32_16x16x32_bf16 v[70:73], a[124:127], v[118:121], v[70:73]
v_mfma_f32_16x16x32_bf16 v[70:73], a[140:143], v[122:125], v[70:73]
ds_read_b128 v[110:113], v8 offset:17408
v_mfma_f32_16x16x32_bf16 v[70:73], a[156:159], v[126:129], v[70:73]
s_xor_b32 s43, s43, 0x10000
v_mfma_f32_16x16x32_bf16 v[70:73], a[172:175], v[130:133], v[70:73]
s_add_u32 m0, s43, 0
v_mfma_f32_16x16x32_bf16 v[70:73], a[188:191], v[134:137], v[70:73]
s_waitcnt lgkmcnt(5)
v_mfma_f32_16x16x32_bf16 v[58:61], a[192:195], v[90:93], v[58:61]
s_barrier
s_waitcnt lgkmcnt(4)
v_mfma_f32_16x16x32_bf16 v[58:61], a[208:211], v[94:97], v[58:61]
s_waitcnt lgkmcnt(3)
global_load_lds_dwordx4 v2, s[38:39] offset:-2048 sc1
v_mfma_f32_16x16x32_bf16 v[58:61], a[224:227], v[98:101], v[58:61]
s_waitcnt lgkmcnt(2)
v_mfma_f32_16x16x32_bf16 v[58:61], a[240:243], v[102:105], v[58:61]
s_waitcnt lgkmcnt(1)
v_mfma_f32_16x16x32_bf16 v[58:61], v[26:29], v[106:109], v[58:61]
s_waitcnt lgkmcnt(0)
global_load_lds_dwordx4 v2, s[38:39] offset:-1024 sc1
v_mfma_f32_16x16x32_bf16 v[58:61], v[42:45], v[110:113], v[58:61]
v_mfma_f32_16x16x32_bf16 v[66:69], a[200:203], v[90:93], v[66:69]
v_mfma_f32_16x16x32_bf16 v[66:69], a[216:219], v[94:97], v[66:69]
global_load_lds_dwordx4 v2, s[38:39] offset:0 sc1
v_mfma_f32_16x16x32_bf16 v[66:69], a[232:235], v[98:101], v[66:69]
v_mfma_f32_16x16x32_bf16 v[66:69], a[248:251], v[102:105], v[66:69]
v_mfma_f32_16x16x32_bf16 v[66:69], v[34:37], v[106:109], v[66:69]
global_load_lds_dwordx4 v2, s[38:39] offset:1024 sc1
v_mfma_f32_16x16x32_bf16 v[66:69], v[50:53], v[110:113], v[66:69]
v_mfma_f32_16x16x32_bf16 v[62:65], a[196:199], v[90:93], v[62:65]
s_waitcnt vmcnt(5)
s_barrier
v_mfma_f32_16x16x32_bf16 v[62:65], a[212:215], v[94:97], v[62:65]
global_load_lds_dwordx4 v2, s[38:39] offset:2048 sc1
v_mfma_f32_16x16x32_bf16 v[62:65], a[228:231], v[98:101], v[62:65]
v_mfma_f32_16x16x32_bf16 v[62:65], a[244:247], v[102:105], v[62:65]
ds_read_b128 v[114:117], v8 offset:18432
v_mfma_f32_16x16x32_bf16 v[62:65], v[30:33], v[106:109], v[62:65]
ds_read_b128 v[118:121], v8 offset:19456
global_load_lds_dwordx4 v2, s[38:39] offset:3072 sc1
v_mfma_f32_16x16x32_bf16 v[62:65], v[46:49], v[110:113], v[62:65]
ds_read_b128 v[122:125], v8 offset:20480
v_add_u32 v2, 18688, v2
v_mfma_f32_16x16x32_bf16 v[70:73], a[204:207], v[90:93], v[70:73]
ds_read_b128 v[126:129], v8 offset:21504
v_min_u32 v2, v2, v9
v_mfma_f32_16x16x32_bf16 v[70:73], a[220:223], v[94:97], v[70:73]
ds_read_b128 v[130:133], v8 offset:22528
v_mul_f32 v138, v58, v66
v_mfma_f32_16x16x32_bf16 v[70:73], a[236:239], v[98:101], v[70:73]
ds_read_b128 v[134:137], v8 offset:23552
v_mul_f32 v139, v59, v67
v_mfma_f32_16x16x32_bf16 v[70:73], a[252:255], v[102:105], v[70:73]
v_mul_f32 v140, v60, v68
v_mul_f32 v141, v61, v69
v_mfma_f32_16x16x32_bf16 v[70:73], v[38:41], v[106:109], v[70:73]
v_cvt_pk_bf16_f32 v146, v138, v139
v_cvt_pk_bf16_f32 v147, v140, v141
v_mfma_f32_16x16x32_bf16 v[70:73], v[54:57], v[110:113], v[70:73]
s_waitcnt lgkmcnt(5)
v_mfma_f32_16x16x32_bf16 v[74:77], a[0:3], v[114:117], 0
s_waitcnt lgkmcnt(4)
v_mfma_f32_16x16x32_bf16 v[74:77], a[16:19], v[118:121], v[74:77]
s_waitcnt lgkmcnt(3)
v_mfma_f32_16x16x32_bf16 v[74:77], a[32:35], v[122:125], v[74:77]
s_waitcnt lgkmcnt(2)
v_mfma_f32_16x16x32_bf16 v[74:77], a[48:51], v[126:129], v[74:77]
s_waitcnt lgkmcnt(1)
v_mul_f32 v142, v62, v70
v_mul_f32 v143, v63, v71
v_mfma_f32_16x16x32_bf16 v[74:77], a[64:67], v[130:133], v[74:77]
s_waitcnt lgkmcnt(0)
v_mul_f32 v144, v64, v72
v_mul_f32 v145, v65, v73
v_mfma_f32_16x16x32_bf16 v[74:77], a[80:83], v[134:137], v[74:77]
ds_read_b128 v[90:93], v8 offset:24576
v_cvt_pk_bf16_f32 v148, v142, v143
v_mfma_f32_16x16x32_bf16 v[82:85], a[8:11], v[114:117], 0
ds_read_b128 v[94:97], v8 offset:25600
v_cvt_pk_bf16_f32 v149, v144, v145
v_mfma_f32_16x16x32_bf16 v[82:85], a[24:27], v[118:121], v[82:85]
v_mfma_f32_16x16x32_bf16 v[82:85], a[40:43], v[122:125], v[82:85]
s_waitcnt vmcnt(6)
s_barrier
v_mfma_f32_16x16x32_bf16 v[82:85], a[56:59], v[126:129], v[82:85]
v_mfma_f32_16x16x32_bf16 v[82:85], a[72:75], v[130:133], v[82:85]
global_store_dwordx4 v3, v[146:149], s[38:39] sc1
v_mfma_f32_16x16x32_bf16 v[82:85], a[88:91], v[134:137], v[82:85]
ds_read_b128 v[98:101], v8 offset:26624
v_add_u32 v3, 147456, v3
v_mfma_f32_16x16x32_bf16 v[78:81], a[4:7], v[114:117], 0
v_mfma_f32_16x16x32_bf16 v[78:81], a[20:23], v[118:121], v[78:81]
v_mfma_f32_16x16x32_bf16 v[78:81], a[36:39], v[122:125], v[78:81]
ds_read_b128 v[102:105], v8 offset:27648
v_mfma_f32_16x16x32_bf16 v[78:81], a[52:55], v[126:129], v[78:81]
v_mfma_f32_16x16x32_bf16 v[78:81], a[68:71], v[130:133], v[78:81]
v_mfma_f32_16x16x32_bf16 v[78:81], a[84:87], v[134:137], v[78:81]
ds_read_b128 v[106:109], v8 offset:28672
v_mfma_f32_16x16x32_bf16 v[86:89], a[12:15], v[114:117], 0
v_mfma_f32_16x16x32_bf16 v[86:89], a[28:31], v[118:121], v[86:89]
v_mfma_f32_16x16x32_bf16 v[86:89], a[44:47], v[122:125], v[86:89]
ds_read_b128 v[110:113], v8 offset:29696
v_mfma_f32_16x16x32_bf16 v[86:89], a[60:63], v[126:129], v[86:89]
v_mfma_f32_16x16x32_bf16 v[86:89], a[76:79], v[130:133], v[86:89]
v_mfma_f32_16x16x32_bf16 v[86:89], a[92:95], v[134:137], v[86:89]
s_waitcnt lgkmcnt(5)
v_mfma_f32_16x16x32_bf16 v[74:77], a[96:99], v[90:93], v[74:77]
s_waitcnt lgkmcnt(4)
v_mfma_f32_16x16x32_bf16 v[74:77], a[112:115], v[94:97], v[74:77]
s_waitcnt lgkmcnt(3)
v_mfma_f32_16x16x32_bf16 v[74:77], a[128:131], v[98:101], v[74:77]
s_waitcnt lgkmcnt(2)
v_mfma_f32_16x16x32_bf16 v[74:77], a[144:147], v[102:105], v[74:77]
s_waitcnt lgkmcnt(1)
v_mfma_f32_16x16x32_bf16 v[74:77], a[160:163], v[106:109], v[74:77]
s_waitcnt lgkmcnt(0)
v_mfma_f32_16x16x32_bf16 v[74:77], a[176:179], v[110:113], v[74:77]
ds_read_b128 v[114:117], v8 offset:30720
v_mfma_f32_16x16x32_bf16 v[82:85], a[104:107], v[90:93], v[82:85]
v_mfma_f32_16x16x32_bf16 v[82:85], a[120:123], v[94:97], v[82:85]
v_mfma_f32_16x16x32_bf16 v[82:85], a[136:139], v[98:101], v[82:85]
ds_read_b128 v[118:121], v8 offset:31744
v_mfma_f32_16x16x32_bf16 v[82:85], a[152:155], v[102:105], v[82:85]
v_mfma_f32_16x16x32_bf16 v[82:85], a[168:171], v[106:109], v[82:85]
v_mfma_f32_16x16x32_bf16 v[82:85], a[184:187], v[110:113], v[82:85]
ds_read_b128 v[122:125], v8 offset:32768
v_mfma_f32_16x16x32_bf16 v[78:81], a[100:103], v[90:93], v[78:81]
v_mfma_f32_16x16x32_bf16 v[78:81], a[116:119], v[94:97], v[78:81]
v_mfma_f32_16x16x32_bf16 v[78:81], a[132:135], v[98:101], v[78:81]
ds_read_b128 v[126:129], v8 offset:33792
v_mfma_f32_16x16x32_bf16 v[78:81], a[148:151], v[102:105], v[78:81]
v_mfma_f32_16x16x32_bf16 v[78:81], a[164:167], v[106:109], v[78:81]
v_mfma_f32_16x16x32_bf16 v[78:81], a[180:183], v[110:113], v[78:81]
ds_read_b128 v[130:133], v8 offset:34816
v_mfma_f32_16x16x32_bf16 v[86:89], a[108:111], v[90:93], v[86:89]
v_mfma_f32_16x16x32_bf16 v[86:89], a[124:127], v[94:97], v[86:89]
v_mfma_f32_16x16x32_bf16 v[86:89], a[140:143], v[98:101], v[86:89]
ds_read_b128 v[134:137], v8 offset:35840
v_mfma_f32_16x16x32_bf16 v[86:89], a[156:159], v[102:105], v[86:89]
v_mfma_f32_16x16x32_bf16 v[86:89], a[172:175], v[106:109], v[86:89]
s_add_u32 m0, s43, 18432
v_mfma_f32_16x16x32_bf16 v[86:89], a[188:191], v[110:113], v[86:89]
s_waitcnt lgkmcnt(5)
v_mfma_f32_16x16x32_bf16 v[74:77], a[192:195], v[114:117], v[74:77]
s_barrier
s_waitcnt lgkmcnt(4)
v_mfma_f32_16x16x32_bf16 v[74:77], a[208:211], v[118:121], v[74:77]
s_waitcnt lgkmcnt(3)
global_load_lds_dwordx4 v2, s[38:39] offset:-2048 sc1
v_mfma_f32_16x16x32_bf16 v[74:77], a[224:227], v[122:125], v[74:77]
s_waitcnt lgkmcnt(2)
v_mfma_f32_16x16x32_bf16 v[74:77], a[240:243], v[126:129], v[74:77]
s_waitcnt lgkmcnt(1)
v_mfma_f32_16x16x32_bf16 v[74:77], v[26:29], v[130:133], v[74:77]
s_waitcnt lgkmcnt(0)
global_load_lds_dwordx4 v2, s[38:39] offset:-1024 sc1
v_mfma_f32_16x16x32_bf16 v[74:77], v[42:45], v[134:137], v[74:77]
v_mfma_f32_16x16x32_bf16 v[82:85], a[200:203], v[114:117], v[82:85]
v_mfma_f32_16x16x32_bf16 v[82:85], a[216:219], v[118:121], v[82:85]
global_load_lds_dwordx4 v2, s[38:39] offset:0 sc1
v_mfma_f32_16x16x32_bf16 v[82:85], a[232:235], v[122:125], v[82:85]
v_mfma_f32_16x16x32_bf16 v[82:85], a[248:251], v[126:129], v[82:85]
v_mfma_f32_16x16x32_bf16 v[82:85], v[34:37], v[130:133], v[82:85]
v_xor_b32 v8, 0x10000, v8
global_load_lds_dwordx4 v2, s[38:39] offset:1024 sc1
v_mfma_f32_16x16x32_bf16 v[82:85], v[50:53], v[134:137], v[82:85]
v_mfma_f32_16x16x32_bf16 v[78:81], a[196:199], v[114:117], v[78:81]
s_waitcnt vmcnt(5)
s_barrier
v_mfma_f32_16x16x32_bf16 v[78:81], a[212:215], v[118:121], v[78:81]
global_load_lds_dwordx4 v2, s[38:39] offset:2048 sc1
v_mfma_f32_16x16x32_bf16 v[78:81], a[228:231], v[122:125], v[78:81]
v_mfma_f32_16x16x32_bf16 v[78:81], a[244:247], v[126:129], v[78:81]
ds_read_b128 v[90:93], v8 offset:0
v_mfma_f32_16x16x32_bf16 v[78:81], v[30:33], v[130:133], v[78:81]
ds_read_b128 v[94:97], v8 offset:1024
global_load_lds_dwordx4 v2, s[38:39] offset:3072 sc1
v_mfma_f32_16x16x32_bf16 v[78:81], v[46:49], v[134:137], v[78:81]
ds_read_b128 v[98:101], v8 offset:2048
v_add_u32 v2, 18688, v2
v_mfma_f32_16x16x32_bf16 v[86:89], a[204:207], v[114:117], v[86:89]
ds_read_b128 v[102:105], v8 offset:3072
v_min_u32 v2, v2, v9
v_mfma_f32_16x16x32_bf16 v[86:89], a[220:223], v[118:121], v[86:89]
ds_read_b128 v[106:109], v8 offset:4096
v_mul_f32 v138, v74, v82
v_mfma_f32_16x16x32_bf16 v[86:89], a[236:239], v[122:125], v[86:89]
ds_read_b128 v[110:113], v8 offset:5120
v_mul_f32 v139, v75, v83
v_mfma_f32_16x16x32_bf16 v[86:89], a[252:255], v[126:129], v[86:89]
v_mul_f32 v140, v76, v84
v_mul_f32 v141, v77, v85
v_mfma_f32_16x16x32_bf16 v[86:89], v[38:41], v[130:133], v[86:89]
v_mfma_f32_16x16x32_bf16 v[86:89], v[54:57], v[134:137], v[86:89]
s_sub_u32 s70, s70, 1
s_cbranch_scc0 .Lgu_top5
v_mul_f32 v138, v74, v82
v_mul_f32 v139, v75, v83
v_mul_f32 v140, v76, v84
v_mul_f32 v141, v77, v85
v_cvt_pk_bf16_f32 v146, v138, v139
v_cvt_pk_bf16_f32 v147, v140, v141
v_mul_f32 v142, v78, v86
v_mul_f32 v143, v79, v87
v_mul_f32 v144, v80, v88
v_mul_f32 v145, v81, v89
v_cvt_pk_bf16_f32 v148, v142, v143
v_cvt_pk_bf16_f32 v149, v144, v145
global_store_dwordx4 v3, v[146:149], s[38:39] sc1
s_waitcnt vmcnt(1) lgkmcnt(0)
s_barrier
v_mov_b32 v218, 192
ds_read_b32 v218, v218 offset:36864
s_waitcnt vmcnt(0)
s_barrier
s_waitcnt lgkmcnt(0)
v_readfirstlane_b32 s60, v218
s_nop 3
s_max_i32 s60, s60, 2
s_min_i32 s60, s60, 66
s_add_u32 s60, s60, -2
s_branch .Lgu_fin
.Lgu_w2:
v_add_u32 v254, -1, v5
s_mov_b32 s70, 32
s_waitcnt vmcnt(0) lgkmcnt(0)
s_nop 7
.p2align 6
.Lgu_top6:
s_waitcnt lgkmcnt(5)
v_mfma_f32_16x16x32_bf16 v[58:61], a[0:3], v[90:93], 0
s_waitcnt lgkmcnt(4)
v_mul_f32 v141, v77, v85
v_cvt_pk_bf16_f32 v146, v138, v139
v_mfma_f32_16x16x32_bf16 v[58:61], a[16:19], v[94:97], v[58:61]
s_waitcnt lgkmcnt(3)
v_cvt_pk_bf16_f32 v147, v140, v141
v_mfma_f32_16x16x32_bf16 v[58:61], a[32:35], v[98:101], v[58:61]
s_waitcnt lgkmcnt(2)
v_mfma_f32_16x16x32_bf16 v[58:61], a[48:51], v[102:105], v[58:61]
s_waitcnt lgkmcnt(1)
v_mul_f32 v142, v78, v86
v_mul_f32 v143, v79, v87
v_mfma_f32_16x16x32_bf16 v[58:61], a[64:67], v[106:109], v[58:61]
s_waitcnt lgkmcnt(0)
v_mul_f32 v144, v80, v88
v_mul_f32 v145, v81, v89
v_mfma_f32_16x16x32_bf16 v[58:61], a[80:83], v[110:113], v[58:61]
ds_read_b128 v[114:117], v8 offset:6144
v_cvt_pk_bf16_f32 v148, v142, v143
v_mfma_f32_16x16x32_bf16 v[66:69], a[8:11], v[90:93], 0
ds_read_b128 v[118:121], v8 offset:7168
v_cvt_pk_bf16_f32 v149, v144, v145
v_mfma_f32_16x16x32_bf16 v[66:69], a[24:27], v[94:97], v[66:69]
v_mfma_f32_16x16x32_bf16 v[66:69], a[40:43], v[98:101], v[66:69]
s_waitcnt vmcnt(6)
s_barrier
v_mfma_f32_16x16x32_bf16 v[66:69], a[56:59], v[102:105], v[66:69]
s_mov_b64 exec, 1
global_store_dword v6, v7, s[38:39] sc1
s_mov_b64 exec, -1
v_mfma_f32_16x16x32_bf16 v[66:69], a[72:75], v[106:109], v[66:69]
v_add_u32 v7, 1, v7
v_mfma_f32_16x16x32_bf16 v[66:69], a[88:91], v[110:113], v[66:69]
ds_read_b128 v[122:125], v8 offset:8192
v_mfma_f32_16x16x32_bf16 v[62:65], a[4:7], v[90:93], 0
global_store_dwordx4 v3, v[146:149], s[38:39] sc1
v_mfma_f32_16x16x32_bf16 v[62:65], a[20:23], v[94:97], v[62:65]
v_add_u32 v3, 147456, v3
v_mfma_f32_16x16x32_bf16 v[62:65], a[36:39], v[98:101], v[62:65]
ds_read_b128 v[126:129], v8 offset:9216
v_mfma_f32_16x16x32_bf16 v[62:65], a[52:55], v[102:105], v[62:65]
v_mfma_f32_16x16x32_bf16 v[62:65], a[68:71], v[106:109], v[62:65]
v_mfma_f32_16x16x32_bf16 v[62:65], a[84:87], v[110:113], v[62:65]
ds_read_b128 v[130:133], v8 offset:10240
v_mfma_f32_16x16x32_bf16 v[70:73], a[12:15], v[90:93], 0
v_mfma_f32_16x16x32_bf16 v[70:73], a[28:31], v[94:97], v[70:73]
v_mfma_f32_16x16x32_bf16 v[70:73], a[44:47], v[98:101], v[70:73]
ds_read_b128 v[134:137], v8 offset:11264
v_mfma_f32_16x16x32_bf16 v[70:73], a[60:63], v[102:105], v[70:73]
v_mfma_f32_16x16x32_bf16 v[70:73], a[76:79], v[106:109], v[70:73]
v_mfma_f32_16x16x32_bf16 v[70:73], a[92:95], v[110:113], v[70:73]
s_waitcnt lgkmcnt(5)
v_mfma_f32_16x16x32_bf16 v[58:61], a[96:99], v[114:117], v[58:61]
s_waitcnt lgkmcnt(4)
v_mfma_f32_16x16x32_bf16 v[58:61], a[112:115], v[118:121], v[58:61]
s_waitcnt lgkmcnt(3)
v_mfma_f32_16x16x32_bf16 v[58:61], a[128:131], v[122:125], v[58:61]
s_waitcnt lgkmcnt(2)
v_mfma_f32_16x16x32_bf16 v[58:61], a[144:147], v[126:129], v[58:61]
s_waitcnt lgkmcnt(1)
v_mfma_f32_16x16x32_bf16 v[58:61], a[160:163], v[130:133], v[58:61]
s_waitcnt lgkmcnt(0)
v_mfma_f32_16x16x32_bf16 v[58:61], a[176:179], v[134:137], v[58:61]
ds_read_b128 v[90:93], v8 offset:12288
v_mfma_f32_16x16x32_bf16 v[66:69], a[104:107], v[114:117], v[66:69]
v_mfma_f32_16x16x32_bf16 v[66:69], a[120:123], v[118:121], v[66:69]
v_mfma_f32_16x16x32_bf16 v[66:69], a[136:139], v[122:125], v[66:69]
ds_read_b128 v[94:97], v8 offset:13312
v_mfma_f32_16x16x32_bf16 v[66:69], a[152:155], v[126:129], v[66:69]
v_mfma_f32_16x16x32_bf16 v[66:69], a[168:171], v[130:133], v[66:69]
v_mfma_f32_16x16x32_bf16 v[66:69], a[184:187], v[134:137], v[66:69]
ds_read_b128 v[98:101], v8 offset:14336
v_mfma_f32_16x16x32_bf16 v[62:65], a[100:103], v[114:117], v[62:65]
v_mfma_f32_16x16x32_bf16 v[62:65], a[116:119], v[118:121], v[62:65]
v_mfma_f32_16x16x32_bf16 v[62:65], a[132:135], v[122:125], v[62:65]
ds_read_b128 v[102:105], v8 offset:15360
v_mfma_f32_16x16x32_bf16 v[62:65], a[148:151], v[126:129], v[62:65]
v_mfma_f32_16x16x32_bf16 v[62:65], a[164:167], v[130:133], v[62:65]
v_mfma_f32_16x16x32_bf16 v[62:65], a[180:183], v[134:137], v[62:65]
ds_read_b128 v[106:109], v8 offset:16384
v_mfma_f32_16x16x32_bf16 v[70:73], a[108:111], v[114:117], v[70:73]
v_mfma_f32_16x16x32_bf16 v[70:73], a[124:127], v[118:121], v[70:73]
v_mfma_f32_16x16x32_bf16 v[70:73], a[140:143], v[122:125], v[70:73]
ds_read_b128 v[110:113], v8 offset:17408
v_mfma_f32_16x16x32_bf16 v[70:73], a[156:159], v[126:129], v[70:73]
s_xor_b32 s43, s43, 0x10000
v_mfma_f32_16x16x32_bf16 v[70:73], a[172:175], v[130:133], v[70:73]
s_add_u32 m0, s43, 0
v_mfma_f32_16x16x32_bf16 v[70:73], a[188:191], v[134:137], v[70:73]
s_waitcnt lgkmcnt(5)
v_mfma_f32_16x16x32_bf16 v[58:61], a[192:195], v[90:93], v[58:61]
s_barrier
s_waitcnt lgkmcnt(4)
v_mfma_f32_16x16x32_bf16 v[58:61], a[208:211], v[94:97], v[58:61]
s_waitcnt lgkmcnt(3)
v_mfma_f32_16x16x32_bf16 v[58:61], a[224:227], v[98:101], v[58:61]
s_waitcnt lgkmcnt(2)
global_load_lds_dwordx4 v2, s[38:39] offset:-2048 sc1
v_mfma_f32_16x16x32_bf16 v[58:61], a[240:243], v[102:105], v[58:61]
s_waitcnt lgkmcnt(1)
v_mfma_f32_16x16x32_bf16 v[58:61], v[26:29], v[106:109], v[58:61]
s_waitcnt lgkmcnt(0)
v_mfma_f32_16x16x32_bf16 v[58:61], v[42:45], v[110:113], v[58:61]
global_load_lds_dwordx4 v2, s[38:39] offset:-1024 sc1
v_mfma_f32_16x16x32_bf16 v[66:69], a[200:203], v[90:93], v[66:69]
v_mfma_f32_16x16x32_bf16 v[66:69], a[216:219], v[94:97], v[66:69]
v_mfma_f32_16x16x32_bf16 v[66:69], a[232:235], v[98:101], v[66:69]
global_load_lds_dwordx4 v2, s[38:39] offset:0 sc1
v_mfma_f32_16x16x32_bf16 v[66:69], a[248:251], v[102:105], v[66:69]
v_mfma_f32_16x16x32_bf16 v[66:69], v[34:37], v[106:109], v[66:69]
v_mfma_f32_16x16x32_bf16 v[66:69], v[50:53], v[110:113], v[66:69]
v_mfma_f32_16x16x32_bf16 v[62:65], a[196:199], v[90:93], v[62:65]
s_waitcnt vmcnt(5)
s_barrier
v_mfma_f32_16x16x32_bf16 v[62:65], a[212:215], v[94:97], v[62:65]
global_load_lds_dwordx4 v2, s[38:39] offset:1024 sc1
v_mfma_f32_16x16x32_bf16 v[62:65], a[228:231], v[98:101], v[62:65]
v_mfma_f32_16x16x32_bf16 v[62:65], a[244:247], v[102:105], v[62:65]
ds_read_b128 v[114:117], v8 offset:18432
v_mfma_f32_16x16x32_bf16 v[62:65], v[30:33], v[106:109], v[62:65]
ds_read_b128 v[118:121], v8 offset:19456
global_load_lds_dwordx4 v2, s[38:39] offset:2048 sc1
v_mfma_f32_16x16x32_bf16 v[62:65], v[46:49], v[110:113], v[62:65]
ds_read_b128 v[122:125], v8 offset:20480
v_mul_f32 v138, v58, v66
v_mfma_f32_16x16x32_bf16 v[70:73], a[204:207], v[90:93], v[70:73]
ds_read_b128 v[126:129], v8 offset:21504
v_mul_f32 v139, v59, v67
v_mfma_f32_16x16x32_bf16 v[70:73], a[220:223], v[94:97], v[70:73]
ds_read_b128 v[130:133], v8 offset:22528
global_load_lds_dwordx4 v2, s[38:39] offset:3072 sc1
v_mfma_f32_16x16x32_bf16 v[70:73], a[236:239], v[98:101], v[70:73]
)"
R"(ds_read_b128 v[134:137], v8 offset:23552
v_add_u32 v2, 18688, v2
v_mfma_f32_16x16x32_bf16 v[70:73], a[252:255], v[102:105], v[70:73]
v_min_u32 v2, v2, v9
v_mul_f32 v140, v60, v68
v_mfma_f32_16x16x32_bf16 v[70:73], v[38:41], v[106:109], v[70:73]
v_mul_f32 v141, v61, v69
v_cvt_pk_bf16_f32 v146, v138, v139
v_mfma_f32_16x16x32_bf16 v[70:73], v[54:57], v[110:113], v[70:73]
s_waitcnt lgkmcnt(5)
v_cvt_pk_bf16_f32 v147, v140, v141
v_mfma_f32_16x16x32_bf16 v[74:77], a[0:3], v[114:117], 0
s_waitcnt lgkmcnt(4)
v_mfma_f32_16x16x32_bf16 v[74:77], a[16:19], v[118:121], v[74:77]
s_waitcnt lgkmcnt(3)
v_mfma_f32_16x16x32_bf16 v[74:77], a[32:35], v[122:125], v[74:77]
s_waitcnt lgkmcnt(2)
v_mfma_f32_16x16x32_bf16 v[74:77], a[48:51], v[126:129], v[74:77]
s_waitcnt lgkmcnt(1)
v_mul_f32 v142, v62, v70
v_mul_f32 v143, v63, v71
v_mfma_f32_16x16x32_bf16 v[74:77], a[64:67], v[130:133], v[74:77]
s_waitcnt lgkmcnt(0)
v_mul_f32 v144, v64, v72
v_mul_f32 v145, v65, v73
v_mfma_f32_16x16x32_bf16 v[74:77], a[80:83], v[134:137], v[74:77]
ds_read_b128 v[90:93], v8 offset:24576
v_cvt_pk_bf16_f32 v148, v142, v143
v_mfma_f32_16x16x32_bf16 v[82:85], a[8:11], v[114:117], 0
ds_read_b128 v[94:97], v8 offset:25600
v_cvt_pk_bf16_f32 v149, v144, v145
v_mfma_f32_16x16x32_bf16 v[82:85], a[24:27], v[118:121], v[82:85]
v_mfma_f32_16x16x32_bf16 v[82:85], a[40:43], v[122:125], v[82:85]
s_waitcnt vmcnt(6)
s_barrier
v_mfma_f32_16x16x32_bf16 v[82:85], a[56:59], v[126:129], v[82:85]
s_mov_b64 exec, 1
global_store_dword v6, v7, s[38:39] sc1
s_mov_b64 exec, -1
v_mfma_f32_16x16x32_bf16 v[82:85], a[72:75], v[130:133], v[82:85]
v_add_u32 v7, 1, v7
v_mfma_f32_16x16x32_bf16 v[82:85], a[88:91], v[134:137], v[82:85]
ds_read_b128 v[98:101], v8 offset:26624
v_mfma_f32_16x16x32_bf16 v[78:81], a[4:7], v[114:117], 0
global_store_dwordx4 v3, v[146:149], s[38:39] sc1
v_mfma_f32_16x16x32_bf16 v[78:81], a[20:23], v[118:121], v[78:81]
v_add_u32 v3, 147456, v3
v_mfma_f32_16x16x32_bf16 v[78:81], a[36:39], v[122:125], v[78:81]
ds_read_b128 v[102:105], v8 offset:27648
v_mfma_f32_16x16x32_bf16 v[78:81], a[52:55], v[126:129], v[78:81]
v_mfma_f32_16x16x32_bf16 v[78:81], a[68:71], v[130:133], v[78:81]
v_mfma_f32_16x16x32_bf16 v[78:81], a[84:87], v[134:137], v[78:81]
ds_read_b128 v[106:109], v8 offset:28672
v_mfma_f32_16x16x32_bf16 v[86:89], a[12:15], v[114:117], 0
v_mfma_f32_16x16x32_bf16 v[86:89], a[28:31], v[118:121], v[86:89]
v_mfma_f32_16x16x32_bf16 v[86:89], a[44:47], v[122:125], v[86:89]
ds_read_b128 v[110:113], v8 offset:29696
v_mfma_f32_16x16x32_bf16 v[86:89], a[60:63], v[126:129], v[86:89]
v_mfma_f32_16x16x32_bf16 v[86:89], a[76:79], v[130:133], v[86:89]
v_mfma_f32_16x16x32_bf16 v[86:89], a[92:95], v[134:137], v[86:89]
s_waitcnt lgkmcnt(5)
v_mfma_f32_16x16x32_bf16 v[74:77], a[96:99], v[90:93], v[74:77]
s_waitcnt lgkmcnt(4)
v_mfma_f32_16x16x32_bf16 v[74:77], a[112:115], v[94:97], v[74:77]
s_waitcnt lgkmcnt(3)
v_mfma_f32_16x16x32_bf16 v[74:77], a[128:131], v[98:101], v[74:77]
s_waitcnt lgkmcnt(2)
v_mfma_f32_16x16x32_bf16 v[74:77], a[144:147], v[102:105], v[74:77]
s_waitcnt lgkmcnt(1)
v_mfma_f32_16x16x32_bf16 v[74:77], a[160:163], v[106:109], v[74:77]
s_waitcnt lgkmcnt(0)
v_mfma_f32_16x16x32_bf16 v[74:77], a[176:179], v[110:113], v[74:77]
ds_read_b128 v[114:117], v8 offset:30720
v_mfma_f32_16x16x32_bf16 v[82:85], a[104:107], v[90:93], v[82:85]
v_mfma_f32_16x16x32_bf16 v[82:85], a[120:123], v[94:97], v[82:85]
v_mfma_f32_16x16x32_bf16 v[82:85], a[136:139], v[98:101], v[82:85]
ds_read_b128 v[118:121], v8 offset:31744
v_mfma_f32_16x16x32_bf16 v[82:85], a[152:155], v[102:105], v[82:85]
v_mfma_f32_16x16x32_bf16 v[82:85], a[168:171], v[106:109], v[82:85]
v_mfma_f32_16x16x32_bf16 v[82:85], a[184:187], v[110:113], v[82:85]
ds_read_b128 v[122:125], v8 offset:32768
v_mfma_f32_16x16x32_bf16 v[78:81], a[100:103], v[90:93], v[78:81]
v_mfma_f32_16x16x32_bf16 v[78:81], a[116:119], v[94:97], v[78:81]
v_mfma_f32_16x16x32_bf16 v[78:81], a[132:135], v[98:101], v[78:81]
ds_read_b128 v[126:129], v8 offset:33792
v_mfma_f32_16x16x32_bf16 v[78:81], a[148:151], v[102:105], v[78:81]
v_mfma_f32_16x16x32_bf16 v[78:81], a[164:167], v[106:109], v[78:81]
v_mfma_f32_16x16x32_bf16 v[78:81], a[180:183], v[110:113], v[78:81]
ds_read_b128 v[130:133], v8 offset:34816
v_mfma_f32_16x16x32_bf16 v[86:89], a[108:111], v[90:93], v[86:89]
v_mfma_f32_16x16x32_bf16 v[86:89], a[124:127], v[94:97], v[86:89]
v_mfma_f32_16x16x32_bf16 v[86:89], a[140:143], v[98:101], v[86:89]
ds_read_b128 v[134:137], v8 offset:35840
v_mfma_f32_16x16x32_bf16 v[86:89], a[156:159], v[102:105], v[86:89]
v_mfma_f32_16x16x32_bf16 v[86:89], a[172:175], v[106:109], v[86:89]
s_add_u32 m0, s43, 18432
v_mfma_f32_16x16x32_bf16 v[86:89], a[188:191], v[110:113], v[86:89]
s_waitcnt lgkmcnt(5)
v_mfma_f32_16x16x32_bf16 v[74:77], a[192:195], v[114:117], v[74:77]
s_barrier
s_waitcnt lgkmcnt(4)
v_mfma_f32_16x16x32_bf16 v[74:77], a[208:211], v[118:121], v[74:77]
s_waitcnt lgkmcnt(3)
v_mfma_f32_16x16x32_bf16 v[74:77], a[224:227], v[122:125], v[74:77]
s_waitcnt lgkmcnt(2)
global_load_lds_dwordx4 v2, s[38:39] offset:-2048 sc1
v_mfma_f32_16x16x32_bf16 v[74:77], a[240:243], v[126:129], v[74:77]
s_waitcnt lgkmcnt(1)
v_mfma_f32_16x16x32_bf16 v[74:77], v[26:29], v[130:133], v[74:77]
s_waitcnt lgkmcnt(0)
v_mfma_f32_16x16x32_bf16 v[74:77], v[42:45], v[134:137], v[74:77]
global_load_lds_dwordx4 v2, s[38:39] offset:-1024 sc1
v_mfma_f32_16x16x32_bf16 v[82:85], a[200:203], v[114:117], v[82:85]
v_mfma_f32_16x16x32_bf16 v[82:85], a[216:219], v[118:121], v[82:85]
v_mfma_f32_16x16x32_bf16 v[82:85], a[232:235], v[122:125], v[82:85]
global_load_lds_dwordx4 v2, s[38:39] offset:0 sc1
v_mfma_f32_16x16x32_bf16 v[82:85], a[248:251], v[126:129], v[82:85]
v_mfma_f32_16x16x32_bf16 v[82:85], v[34:37], v[130:133], v[82:85]
v_xor_b32 v8, 0x10000, v8
v_mfma_f32_16x16x32_bf16 v[82:85], v[50:53], v[134:137], v[82:85]
v_mfma_f32_16x16x32_bf16 v[78:81], a[196:199], v[114:117], v[78:81]
s_waitcnt vmcnt(5)
s_barrier
v_mfma_f32_16x16x32_bf16 v[78:81], a[212:215], v[118:121], v[78:81]
global_load_lds_dwordx4 v2, s[38:39] offset:1024 sc1
v_mfma_f32_16x16x32_bf16 v[78:81], a[228:231], v[122:125], v[78:81]
v_mfma_f32_16x16x32_bf16 v[78:81], a[244:247], v[126:129], v[78:81]
ds_read_b128 v[90:93], v8 offset:0
v_mfma_f32_16x16x32_bf16 v[78:81], v[30:33], v[130:133], v[78:81]
ds_read_b128 v[94:97], v8 offset:1024
global_load_lds_dwordx4 v2, s[38:39] offset:2048 sc1
v_mfma_f32_16x16x32_bf16 v[78:81], v[46:49], v[134:137], v[78:81]
ds_read_b128 v[98:101], v8 offset:2048
v_mul_f32 v138, v74, v82
v_mfma_f32_16x16x32_bf16 v[86:89], a[204:207], v[114:117], v[86:89]
ds_read_b128 v[102:105], v8 offset:3072
v_mul_f32 v139, v75, v83
v_mfma_f32_16x16x32_bf16 v[86:89], a[220:223], v[118:121], v[86:89]
ds_read_b128 v[106:109], v8 offset:4096
global_load_lds_dwordx4 v2, s[38:39] offset:3072 sc1
v_mfma_f32_16x16x32_bf16 v[86:89], a[236:239], v[122:125], v[86:89]
ds_read_b128 v[110:113], v8 offset:5120
v_add_u32 v2, 18688, v2
v_mfma_f32_16x16x32_bf16 v[86:89], a[252:255], v[126:129], v[86:89]
v_min_u32 v2, v2, v9
v_mul_f32 v140, v76, v84
v_mfma_f32_16x16x32_bf16 v[86:89], v[38:41], v[130:133], v[86:89]
v_mfma_f32_16x16x32_bf16 v[86:89], v[54:57], v[134:137], v[86:89]
s_sub_u32 s70, s70, 1
s_cbranch_scc0 .Lgu_top6
v_mul_f32 v138, v74, v82
v_mul_f32 v139, v75, v83
v_mul_f32 v140, v76, v84
v_mul_f32 v141, v77, v85
v_cvt_pk_bf16_f32 v146, v138, v139
v_cvt_pk_bf16_f32 v147, v140, v141
v_mul_f32 v142, v78, v86
v_mul_f32 v143, v79, v87
v_mul_f32 v144, v80, v88
v_mul_f32 v145, v81, v89
v_cvt_pk_bf16_f32 v148, v142, v143
v_cvt_pk_bf16_f32 v149, v144, v145
global_store_dwordx4 v3, v[146:149], s[38:39] sc1
s_waitcnt vmcnt(1) lgkmcnt(0)
s_barrier
s_mov_b64 exec, 1
global_store_dword v6, v7, s[38:39] sc1
s_mov_b64 exec, -1
v_add_u32 v7, 3, v7
v_mov_b32 v218, 192
ds_read_b32 v218, v218 offset:36864
s_waitcnt vmcnt(1)
s_barrier
s_mov_b64 exec, 1
global_store_dword v6, v7, s[38:39] sc1
s_mov_b64 exec, -1
s_waitcnt lgkmcnt(0)
v_readfirstlane_b32 s60, v218
s_nop 3
s_max_i32 s60, s60, 2
s_min_i32 s60, s60, 66
s_add_u32 s60, s60, -1
s_branch .Lgu_fin
.Lgu_w3:
v_add_u32 v254, -1, v5
s_mov_b32 s70, 32
s_waitcnt vmcnt(0) lgkmcnt(0)
s_nop 7
.p2align 6
.Lgu_top11:
s_waitcnt lgkmcnt(5)
v_mfma_f32_16x16x32_bf16 v[58:61], a[0:3], v[90:93], 0
s_waitcnt lgkmcnt(4)
v_mul_f32 v141, v77, v85
v_cvt_pk_bf16_f32 v146, v138, v139
v_mfma_f32_16x16x32_bf16 v[58:61], a[16:19], v[94:97], v[58:61]
s_waitcnt lgkmcnt(3)
v_cvt_pk_bf16_f32 v147, v140, v141
v_mfma_f32_16x16x32_bf16 v[58:61], a[32:35], v[98:101], v[58:61]
s_waitcnt lgkmcnt(2)
global_load_dword v254, v4, s[38:39] sc1
v_mfma_f32_16x16x32_bf16 v[58:61], a[48:51], v[102:105], v[58:61]
s_waitcnt lgkmcnt(1)
v_mul_f32 v142, v78, v86
v_mul_f32 v143, v79, v87
v_mfma_f32_16x16x32_bf16 v[58:61], a[64:67], v[106:109], v[58:61]
s_waitcnt lgkmcnt(0)
v_mul_f32 v144, v80, v88
v_mul_f32 v145, v81, v89
v_mfma_f32_16x16x32_bf16 v[58:61], a[80:83], v[110:113], v[58:61]
ds_read_b128 v[114:117], v8 offset:6144
v_cvt_pk_bf16_f32 v148, v142, v143
v_mfma_f32_16x16x32_bf16 v[66:69], a[8:11], v[90:93], 0
ds_read_b128 v[118:121], v8 offset:7168
v_cvt_pk_bf16_f32 v149, v144, v145
v_mfma_f32_16x16x32_bf16 v[66:69], a[24:27], v[94:97], v[66:69]
v_mfma_f32_16x16x32_bf16 v[66:69], a[40:43], v[98:101], v[66:69]
s_waitcnt vmcnt(7)
s_barrier
v_mfma_f32_16x16x32_bf16 v[66:69], a[56:59], v[102:105], v[66:69]
v_mfma_f32_16x16x32_bf16 v[66:69], a[72:75], v[106:109], v[66:69]
v_mfma_f32_16x16x32_bf16 v[66:69], a[88:91], v[110:113], v[66:69]
ds_read_b128 v[122:125], v8 offset:8192
v_mfma_f32_16x16x32_bf16 v[62:65], a[4:7], v[90:93], 0
global_store_dwordx4 v3, v[146:149], s[38:39] sc1
v_mfma_f32_16x16x32_bf16 v[62:65], a[20:23], v[94:97], v[62:65]
v_add_u32 v3, 147456, v3
v_mfma_f32_16x16x32_bf16 v[62:65], a[36:39], v[98:101], v[62:65]
ds_read_b128 v[126:129], v8 offset:9216
v_mfma_f32_16x16x32_bf16 v[62:65], a[52:55], v[102:105], v[62:65]
v_mfma_f32_16x16x32_bf16 v[62:65], a[68:71], v[106:109], v[62:65]
v_mfma_f32_16x16x32_bf16 v[62:65], a[84:87], v[110:113], v[62:65]
ds_read_b128 v[130:133], v8 offset:10240
v_mfma_f32_16x16x32_bf16 v[70:73], a[12:15], v[90:93], 0
v_mfma_f32_16x16x32_bf16 v[70:73], a[28:31], v[94:97], v[70:73]
v_mfma_f32_16x16x32_bf16 v[70:73], a[44:47], v[98:101], v[70:73]
ds_read_b128 v[134:137], v8 offset:11264
v_mfma_f32_16x16x32_bf16 v[70:73], a[60:63], v[102:105], v[70:73]
v_mfma_f32_16x16x32_bf16 v[70:73], a[76:79], v[106:109], v[70:73]
v_mfma_f32_16x16x32_bf16 v[70:73], a[92:95], v[110:113], v[70:73]
s_waitcnt lgkmcnt(5)
v_mfma_f32_16x16x32_bf16 v[58:61], a[96:99], v[114:117], v[58:61]
s_waitcnt lgkmcnt(4)
v_mfma_f32_16x16x32_bf16 v[58:61], a[112:115], v[118:121], v[58:61]
s_waitcnt lgkmcnt(3)
v_mfma_f32_16x16x32_bf16 v[58:61], a[128:131], v[122:125], v[58:61]
s_waitcnt lgkmcnt(2)
v_mfma_f32_16x16x32_bf16 v[58:61], a[144:147], v[126:129], v[58:61]
s_waitcnt lgkmcnt(1)
v_mfma_f32_16x16x32_bf16 v[58:61], a[160:163], v[130:133], v[58:61]
s_waitcnt lgkmcnt(0)
v_mfma_f32_16x16x32_bf16 v[58:61], a[176:179], v[134:137], v[58:61]
ds_read_b128 v[90:93], v8 offset:12288
v_mfma_f32_16x16x32_bf16 v[66:69], a[104:107], v[114:117], v[66:69]
v_mfma_f32_16x16x32_bf16 v[66:69], a[120:123], v[118:121], v[66:69]
v_mfma_f32_16x16x32_bf16 v[66:69], a[136:139], v[122:125], v[66:69]
ds_read_b128 v[94:97], v8 offset:13312
v_mfma_f32_16x16x32_bf16 v[66:69], a[152:155], v[126:129], v[66:69]
v_mfma_f32_16x16x32_bf16 v[66:69], a[168:171], v[130:133], v[66:69]
v_mfma_f32_16x16x32_bf16 v[66:69], a[184:187], v[134:137], v[66:69]
ds_read_b128 v[98:101], v8 offset:14336
v_mfma_f32_16x16x32_bf16 v[62:65], a[100:103], v[114:117], v[62:65]
v_mfma_f32_16x16x32_bf16 v[62:65], a[116:119], v[118:121], v[62:65]
v_mfma_f32_16x16x32_bf16 v[62:65], a[132:135], v[122:125], v[62:65]
ds_read_b128 v[102:105], v8 offset:15360
v_mfma_f32_16x16x32_bf16 v[62:65], a[148:151], v[126:129], v[62:65]
v_mfma_f32_16x16x32_bf16 v[62:65], a[164:167], v[130:133], v[62:65]
v_mfma_f32_16x16x32_bf16 v[62:65], a[180:183], v[134:137], v[62:65]
ds_read_b128 v[106:109], v8 offset:16384
v_mfma_f32_16x16x32_bf16 v[70:73], a[108:111], v[114:117], v[70:73]
v_mfma_f32_16x16x32_bf16 v[70:73], a[124:127], v[118:121], v[70:73]
s_waitcnt vmcnt(1)
v_sub_u32 v254, v254, v5
v_cmp_lt_u32 vcc, 0x7f, v254
v_mfma_f32_16x16x32_bf16 v[70:73], a[140:143], v[122:125], v[70:73]
ds_read_b128 v[110:113], v8 offset:17408
v_mfma_f32_16x16x32_bf16 v[70:73], a[156:159], v[126:129], v[70:73]
s_xor_b32 s43, s43, 0x10000
v_mfma_f32_16x16x32_bf16 v[70:73], a[172:175], v[130:133], v[70:73]
s_cbranch_vccnz .Lgu_sp7
.Lgu_rt8:
s_add_u32 m0, s43, 0
v_mfma_f32_16x16x32_bf16 v[70:73], a[188:191], v[134:137], v[70:73]
s_waitcnt lgkmcnt(5)
v_mfma_f32_16x16x32_bf16 v[58:61], a[192:195], v[90:93], v[58:61]
s_barrier
s_waitcnt lgkmcnt(4)
v_mfma_f32_16x16x32_bf16 v[58:61], a[208:211], v[94:97], v[58:61]
s_waitcnt lgkmcnt(3)
v_add_u32 v5, 1, v5
v_mfma_f32_16x16x32_bf16 v[58:61], a[224:227], v[98:101], v[58:61]
s_waitcnt lgkmcnt(2)
v_mfma_f32_16x16x32_bf16 v[58:61], a[240:243], v[102:105], v[58:61]
s_waitcnt lgkmcnt(1)
global_load_lds_dwordx4 v2, s[38:39] offset:-2048 sc1
v_mfma_f32_16x16x32_bf16 v[58:61], v[26:29], v[106:109], v[58:61]
s_waitcnt lgkmcnt(0)
v_mfma_f32_16x16x32_bf16 v[58:61], v[42:45], v[110:113], v[58:61]
v_mfma_f32_16x16x32_bf16 v[66:69], a[200:203], v[90:93], v[66:69]
global_load_lds_dwordx4 v2, s[38:39] offset:-1024 sc1
v_mfma_f32_16x16x32_bf16 v[66:69], a[216:219], v[94:97], v[66:69]
v_mfma_f32_16x16x32_bf16 v[66:69], a[232:235], v[98:101], v[66:69]
v_mfma_f32_16x16x32_bf16 v[66:69], a[248:251], v[102:105], v[66:69]
global_load_lds_dwordx4 v2, s[38:39] offset:0 sc1
v_mfma_f32_16x16x32_bf16 v[66:69], v[34:37], v[106:109], v[66:69]
v_mfma_f32_16x16x32_bf16 v[66:69], v[50:53], v[110:113], v[66:69]
v_mfma_f32_16x16x32_bf16 v[62:65], a[196:199], v[90:93], v[62:65]
s_barrier
v_mfma_f32_16x16x32_bf16 v[62:65], a[212:215], v[94:97], v[62:65]
global_load_lds_dwordx4 v2, s[38:39] offset:1024 sc1
v_mfma_f32_16x16x32_bf16 v[62:65], a[228:231], v[98:101], v[62:65]
v_mfma_f32_16x16x32_bf16 v[62:65], a[244:247], v[102:105], v[62:65]
ds_read_b128 v[114:117], v8 offset:18432
v_mfma_f32_16x16x32_bf16 v[62:65], v[30:33], v[106:109], v[62:65]
ds_read_b128 v[118:121], v8 offset:19456
global_load_lds_dwordx4 v2, s[38:39] offset:2048 sc1
v_mfma_f32_16x16x32_bf16 v[62:65], v[46:49], v[110:113], v[62:65]
ds_read_b128 v[122:125], v8 offset:20480
v_mul_f32 v138, v58, v66
v_mfma_f32_16x16x32_bf16 v[70:73], a[204:207], v[90:93], v[70:73]
ds_read_b128 v[126:129], v8 offset:21504
v_mul_f32 v139, v59, v67
v_mfma_f32_16x16x32_bf16 v[70:73], a[220:223], v[94:97], v[70:73]
ds_read_b128 v[130:133], v8 offset:22528
global_load_lds_dwordx4 v2, s[38:39] offset:3072 sc1
v_mfma_f32_16x16x32_bf16 v[70:73], a[236:239], v[98:101], v[70:73]
ds_read_b128 v[134:137], v8 offset:23552
v_add_u32 v2, 18688, v2
v_mfma_f32_16x16x32_bf16 v[70:73], a[252:255], v[102:105], v[70:73]
v_min_u32 v2, v2, v9
v_mul_f32 v140, v60, v68
v_mfma_f32_16x16x32_bf16 v[70:73], v[38:41], v[106:109], v[70:73]
v_mul_f32 v141, v61, v69
v_cvt_pk_bf16_f32 v146, v138, v139
v_mfma_f32_16x16x32_bf16 v[70:73], v[54:57], v[110:113], v[70:73]
s_waitcnt lgkmcnt(5)
v_cvt_pk_bf16_f32 v147, v140, v141
v_mfma_f32_16x16x32_bf16 v[74:77], a[0:3], v[114:117], 0
s_waitcnt lgkmcnt(4)
v_mfma_f32_16x16x32_bf16 v[74:77], a[16:19], v[118:121], v[74:77]
s_waitcnt lgkmcnt(3)
v_mfma_f32_16x16x32_bf16 v[74:77], a[32:35], v[122:125], v[74:77]
s_waitcnt lgkmcnt(2)
global_load_dword v254, v4, s[38:39] sc1
v_mfma_f32_16x16x32_bf16 v[74:77], a[48:51], v[126:129], v[74:77]
s_waitcnt lgkmcnt(1)
v_mul_f32 v142, v62, v70
v_mul_f32 v143, v63, v71
v_mfma_f32_16x16x32_bf16 v[74:77], a[64:67], v[130:133], v[74:77]
s_waitcnt lgkmcnt(0)
v_mul_f32 v144, v64, v72
v_mul_f32 v145, v65, v73
v_mfma_f32_16x16x32_bf16 v[74:77], a[80:83], v[134:137], v[74:77]
ds_read_b128 v[90:93], v8 offset:24576
v_cvt_pk_bf16_f32 v148, v142, v143
v_mfma_f32_16x16x32_bf16 v[82:85], a[8:11], v[114:117], 0
ds_read_b128 v[94:97], v8 offset:25600
v_cvt_pk_bf16_f32 v149, v144, v145
v_mfma_f32_16x16x32_bf16 v[82:85], a[24:27], v[118:121], v[82:85]
v_mfma_f32_16x16x32_bf16 v[82:85], a[40:43], v[122:125], v[82:85]
s_waitcnt vmcnt(7)
s_barrier
v_mfma_f32_16x16x32_bf16 v[82:85], a[56:59], v[126:129], v[82:85]
v_mfma_f32_16x16x32_bf16 v[82:85], a[72:75], v[130:133], v[82:85]
v_mfma_f32_16x16x32_bf16 v[82:85], a[88:91], v[134:137], v[82:85]
ds_read_b128 v[98:101], v8 offset:26624
v_mfma_f32_16x16x32_bf16 v[78:81], a[4:7], v[114:117], 0
global_store_dwordx4 v3, v[146:149], s[38:39] sc1
v_mfma_f32_16x16x32_bf16 v[78:81], a[20:23], v[118:121], v[78:81]
v_add_u32 v3, 147456, v3
v_mfma_f32_16x16x32_bf16 v[78:81], a[36:39], v[122:125], v[78:81]
ds_read_b128 v[102:105], v8 offset:27648
v_mfma_f32_16x16x32_bf16 v[78:81], a[52:55], v[126:129], v[78:81]
v_mfma_f32_16x16x32_bf16 v[78:81], a[68:71], v[130:133], v[78:81]
v_mfma_f32_16x16x32_bf16 v[78:81], a[84:87], v[134:137], v[78:81]
ds_read_b128 v[106:109], v8 offset:28672
v_mfma_f32_16x16x32_bf16 v[86:89], a[12:15], v[114:117], 0
v_mfma_f32_16x16x32_bf16 v[86:89], a[28:31], v[118:121], v[86:89]
v_mfma_f32_16x16x32_bf16 v[86:89], a[44:47], v[122:125], v[86:89]
ds_read_b128 v[110:113], v8 offset:29696
v_mfma_f32_16x16x32_bf16 v[86:89], a[60:63], v[126:129], v[86:89]
v_mfma_f32_16x16x32_bf16 v[86:89], a[76:79], v[130:133], v[86:89]
v_mfma_f32_16x16x32_bf16 v[86:89], a[92:95], v[134:137], v[86:89]
s_waitcnt lgkmcnt(5)
v_mfma_f32_16x16x32_bf16 v[74:77], a[96:99], v[90:93], v[74:77]
s_waitcnt lgkmcnt(4)
v_mfma_f32_16x16x32_bf16 v[74:77], a[112:115], v[94:97], v[74:77]
s_waitcnt lgkmcnt(3)
v_mfma_f32_16x16x32_bf16 v[74:77], a[128:131], v[98:101], v[74:77]
s_waitcnt lgkmcnt(2)
v_mfma_f32_16x16x32_bf16 v[74:77], a[144:147], v[102:105], v[74:77]
s_waitcnt lgkmcnt(1)
v_mfma_f32_16x16x32_bf16 v[74:77], a[160:163], v[106:109], v[74:77]
s_waitcnt lgkmcnt(0)
v_mfma_f32_16x16x32_bf16 v[74:77], a[176:179], v[110:113], v[74:77]
ds_read_b128 v[114:117], v8 offset:30720
v_mfma_f32_16x16x32_bf16 v[82:85], a[104:107], v[90:93], v[82:85]
v_mfma_f32_16x16x32_bf16 v[82:85], a[120:123], v[94:97], v[82:85]
v_mfma_f32_16x16x32_bf16 v[82:85], a[136:139], v[98:101], v[82:85]
ds_read_b128 v[118:121], v8 offset:31744
)"
R"(v_mfma_f32_16x16x32_bf16 v[82:85], a[152:155], v[102:105], v[82:85]
v_mfma_f32_16x16x32_bf16 v[82:85], a[168:171], v[106:109], v[82:85]
v_mfma_f32_16x16x32_bf16 v[82:85], a[184:187], v[110:113], v[82:85]
ds_read_b128 v[122:125], v8 offset:32768
v_mfma_f32_16x16x32_bf16 v[78:81], a[100:103], v[90:93], v[78:81]
v_mfma_f32_16x16x32_bf16 v[78:81], a[116:119], v[94:97], v[78:81]
v_mfma_f32_16x16x32_bf16 v[78:81], a[132:135], v[98:101], v[78:81]
ds_read_b128 v[126:129], v8 offset:33792
v_mfma_f32_16x16x32_bf16 v[78:81], a[148:151], v[102:105], v[78:81]
v_mfma_f32_16x16x32_bf16 v[78:81], a[164:167], v[106:109], v[78:81]
v_mfma_f32_16x16x32_bf16 v[78:81], a[180:183], v[110:113], v[78:81]
ds_read_b128 v[130:133], v8 offset:34816
v_mfma_f32_16x16x32_bf16 v[86:89], a[108:111], v[90:93], v[86:89]
v_mfma_f32_16x16x32_bf16 v[86:89], a[124:127], v[94:97], v[86:89]
s_waitcnt vmcnt(1)
v_sub_u32 v254, v254, v5
v_cmp_lt_u32 vcc, 0x7f, v254
v_mfma_f32_16x16x32_bf16 v[86:89], a[140:143], v[98:101], v[86:89]
ds_read_b128 v[134:137], v8 offset:35840
v_mfma_f32_16x16x32_bf16 v[86:89], a[156:159], v[102:105], v[86:89]
v_mfma_f32_16x16x32_bf16 v[86:89], a[172:175], v[106:109], v[86:89]
s_cbranch_vccnz .Lgu_sp9
.Lgu_rt10:
s_add_u32 m0, s43, 18432
v_mfma_f32_16x16x32_bf16 v[86:89], a[188:191], v[110:113], v[86:89]
s_waitcnt lgkmcnt(5)
v_mfma_f32_16x16x32_bf16 v[74:77], a[192:195], v[114:117], v[74:77]
s_barrier
s_waitcnt lgkmcnt(4)
v_mfma_f32_16x16x32_bf16 v[74:77], a[208:211], v[118:121], v[74:77]
s_waitcnt lgkmcnt(3)
v_add_u32 v5, 1, v5
v_mfma_f32_16x16x32_bf16 v[74:77], a[224:227], v[122:125], v[74:77]
s_waitcnt lgkmcnt(2)
v_mfma_f32_16x16x32_bf16 v[74:77], a[240:243], v[126:129], v[74:77]
s_waitcnt lgkmcnt(1)
global_load_lds_dwordx4 v2, s[38:39] offset:-2048 sc1
v_mfma_f32_16x16x32_bf16 v[74:77], v[26:29], v[130:133], v[74:77]
s_waitcnt lgkmcnt(0)
v_mfma_f32_16x16x32_bf16 v[74:77], v[42:45], v[134:137], v[74:77]
v_mfma_f32_16x16x32_bf16 v[82:85], a[200:203], v[114:117], v[82:85]
global_load_lds_dwordx4 v2, s[38:39] offset:-1024 sc1
v_mfma_f32_16x16x32_bf16 v[82:85], a[216:219], v[118:121], v[82:85]
v_mfma_f32_16x16x32_bf16 v[82:85], a[232:235], v[122:125], v[82:85]
v_mfma_f32_16x16x32_bf16 v[82:85], a[248:251], v[126:129], v[82:85]
global_load_lds_dwordx4 v2, s[38:39] offset:0 sc1
v_mfma_f32_16x16x32_bf16 v[82:85], v[34:37], v[130:133], v[82:85]
v_xor_b32 v8, 0x10000, v8
v_mfma_f32_16x16x32_bf16 v[82:85], v[50:53], v[134:137], v[82:85]
v_mfma_f32_16x16x32_bf16 v[78:81], a[196:199], v[114:117], v[78:81]
s_barrier
v_mfma_f32_16x16x32_bf16 v[78:81], a[212:215], v[118:121], v[78:81]
global_load_lds_dwordx4 v2, s[38:39] offset:1024 sc1
v_mfma_f32_16x16x32_bf16 v[78:81], a[228:231], v[122:125], v[78:81]
v_mfma_f32_16x16x32_bf16 v[78:81], a[244:247], v[126:129], v[78:81]
ds_read_b128 v[90:93], v8 offset:0
v_mfma_f32_16x16x32_bf16 v[78:81], v[30:33], v[130:133], v[78:81]
ds_read_b128 v[94:97], v8 offset:1024
global_load_lds_dwordx4 v2, s[38:39] offset:2048 sc1
v_mfma_f32_16x16x32_bf16 v[78:81], v[46:49], v[134:137], v[78:81]
ds_read_b128 v[98:101], v8 offset:2048
v_mul_f32 v138, v74, v82
v_mfma_f32_16x16x32_bf16 v[86:89], a[204:207], v[114:117], v[86:89]
ds_read_b128 v[102:105], v8 offset:3072
v_mul_f32 v139, v75, v83
v_mfma_f32_16x16x32_bf16 v[86:89], a[220:223], v[118:121], v[86:89]
ds_read_b128 v[106:109], v8 offset:4096
global_load_lds_dwordx4 v2, s[38:39] offset:3072 sc1
v_mfma_f32_16x16x32_bf16 v[86:89], a[236:239], v[122:125], v[86:89]
ds_read_b128 v[110:113], v8 offset:5120
v_add_u32 v2, 18688, v2
v_mfma_f32_16x16x32_bf16 v[86:89], a[252:255], v[126:129], v[86:89]
v_min_u32 v2, v2, v9
v_mul_f32 v140, v76, v84
v_mfma_f32_16x16x32_bf16 v[86:89], v[38:41], v[130:133], v[86:89]
v_mfma_f32_16x16x32_bf16 v[86:89], v[54:57], v[134:137], v[86:89]
s_sub_u32 s70, s70, 1
s_cbranch_scc0 .Lgu_top11
v_mul_f32 v138, v74, v82
v_mul_f32 v139, v75, v83
v_mul_f32 v140, v76, v84
v_mul_f32 v141, v77, v85
v_cvt_pk_bf16_f32 v146, v138, v139
v_cvt_pk_bf16_f32 v147, v140, v141
v_mul_f32 v142, v78, v86
v_mul_f32 v143, v79, v87
v_mul_f32 v144, v80, v88
v_mul_f32 v145, v81, v89
v_cvt_pk_bf16_f32 v148, v142, v143
v_cvt_pk_bf16_f32 v149, v144, v145
global_store_dwordx4 v3, v[146:149], s[38:39] sc1
s_waitcnt vmcnt(1) lgkmcnt(0)
s_barrier
v_mov_b32 v218, 192
ds_read_b32 v218, v218 offset:36864
s_waitcnt vmcnt(0)
s_barrier
s_waitcnt lgkmcnt(0)
v_readfirstlane_b32 s60, v218
s_nop 3
s_max_i32 s60, s60, 2
s_min_i32 s60, s60, 66
s_add_u32 s60, s60, 0
s_branch .Lgu_fin
.Lgu_w4:
s_add_u32 s84, s52, 18688
s_addc_u32 s85, s53, 0
s_add_u32 s86, s50, 110592
s_addc_u32 s87, s51, 0
s_mov_b32 s80, 0
s_mov_b64 s[76:77], 0
s_mov_b32 s72, -14
v_add_u32 v217, -1, v17
v_add_u32 v157, -1, s37
v_add_u32 v214, -1, s37
v_add_u32 v215, -1, s37
v_add_u32 v216, -1, s37
v_add_u32 v254, -1, v5
s_mov_b32 s70, 32
s_waitcnt vmcnt(0) lgkmcnt(0)
s_nop 7
.p2align 6
.Lgu_top16:
s_waitcnt lgkmcnt(5)
v_mfma_f32_16x16x32_bf16 v[58:61], a[0:3], v[90:93], 0
s_waitcnt lgkmcnt(4)
v_and_b32 v205, 0xffff0000, v175
v_add_f32_dpp v194, v198, v194 row_ror:8 row_mask:0xf bank_mask:0xf
v_mfma_f32_16x16x32_bf16 v[58:61], a[16:19], v[94:97], v[58:61]
s_waitcnt lgkmcnt(3)
global_load_dword v217, v16, s[38:39] sc1
v_add_f32_dpp v195, v199, v195 row_ror:8 row_mask:0xf bank_mask:0xf
v_mfma_f32_16x16x32_bf16 v[58:61], a[32:35], v[98:101], v[58:61]
s_waitcnt lgkmcnt(2)
v_add_f32_dpp v196, v200, v196 row_ror:8 row_mask:0xf bank_mask:0xf
v_add_f32_dpp v197, v201, v197 row_ror:8 row_mask:0xf bank_mask:0xf
v_mfma_f32_16x16x32_bf16 v[58:61], a[48:51], v[102:105], v[58:61]
s_waitcnt lgkmcnt(1)
v_mul_f32 v142, v78, v86
v_mul_f32 v143, v79, v87
v_mfma_f32_16x16x32_bf16 v[58:61], a[64:67], v[106:109], v[58:61]
s_waitcnt lgkmcnt(0)
v_mul_f32 v144, v80, v88
v_mul_f32 v145, v81, v89
v_mfma_f32_16x16x32_bf16 v[58:61], a[80:83], v[110:113], v[58:61]
s_sub_u32 s54, s54, s80
ds_read_b128 v[114:117], v8 offset:6144
v_cvt_pk_bf16_f32 v148, v142, v143
v_mfma_f32_16x16x32_bf16 v[66:69], a[8:11], v[90:93], 0
s_subb_u32 s55, s55, 0
ds_read_b128 v[118:121], v8 offset:7168
v_cvt_pk_bf16_f32 v149, v144, v145
v_mfma_f32_16x16x32_bf16 v[66:69], a[24:27], v[94:97], v[66:69]
v_mfma_f32_16x16x32_bf16 v[66:69], a[40:43], v[98:101], v[66:69]
s_waitcnt vmcnt(8)
s_barrier
v_mfma_f32_16x16x32_bf16 v[66:69], a[56:59], v[102:105], v[66:69]
s_add_u32 s84, s52, 18688
v_fma_f32 v194, v194, v218, v202
v_fma_f32 v195, v195, v218, v203
v_mfma_f32_16x16x32_bf16 v[66:69], a[72:75], v[106:109], v[66:69]
s_addc_u32 s85, s53, 0
v_fma_f32 v196, v196, v218, v204
v_fma_f32 v197, v197, v218, v205
v_mfma_f32_16x16x32_bf16 v[66:69], a[88:91], v[110:113], v[66:69]
s_add_u32 s86, s50, 110592
ds_read_b128 v[122:125], v8 offset:8192
v_cvt_pk_bf16_f32 v210, v194, v195
v_mfma_f32_16x16x32_bf16 v[62:65], a[4:7], v[90:93], 0
s_addc_u32 s87, s51, 0
v_cvt_pk_bf16_f32 v211, v196, v197
s_waitcnt vmcnt(7)
s_mov_b64 exec, 1
global_store_dword v18, v216, s[38:39] sc1
s_mov_b64 exec, -1
v_mfma_f32_16x16x32_bf16 v[62:65], a[20:23], v[94:97], v[62:65]
s_add_u32 s72, s72, 1
global_store_dwordx4 v3, v[146:149], s[38:39] sc1
v_mfma_f32_16x16x32_bf16 v[62:65], a[36:39], v[98:101], v[62:65]
s_cmp_le_i32 s60, s72
ds_read_b128 v[126:129], v8 offset:9216
v_add_u32 v3, 147456, v3
v_mfma_f32_16x16x32_bf16 v[62:65], a[52:55], v[102:105], v[62:65]
s_cselect_b64 s[76:77], -1, 0
ds_write_b64 v14, v[210:211]
v_mfma_f32_16x16x32_bf16 v[62:65], a[68:71], v[106:109], v[62:65]
ds_read_b64 v[212:213], v15
v_mfma_f32_16x16x32_bf16 v[62:65], a[84:87], v[110:113], v[62:65]
ds_read_b128 v[130:133], v8 offset:10240
v_mfma_f32_16x16x32_bf16 v[70:73], a[12:15], v[90:93], 0
v_mfma_f32_16x16x32_bf16 v[70:73], a[28:31], v[94:97], v[70:73]
v_mfma_f32_16x16x32_bf16 v[70:73], a[44:47], v[98:101], v[70:73]
ds_read_b128 v[134:137], v8 offset:11264
v_mfma_f32_16x16x32_bf16 v[70:73], a[60:63], v[102:105], v[70:73]
v_mfma_f32_16x16x32_bf16 v[70:73], a[76:79], v[106:109], v[70:73]
v_mfma_f32_16x16x32_bf16 v[70:73], a[92:95], v[110:113], v[70:73]
s_waitcnt lgkmcnt(2)
global_store_dwordx2 v13, v[212:213], s[56:57] sc1
v_mov_b32 v215, v157
v_mfma_f32_16x16x32_bf16 v[58:61], a[96:99], v[114:117], v[58:61]
v_mfma_f32_16x16x32_bf16 v[58:61], a[112:115], v[118:121], v[58:61]
v_mfma_f32_16x16x32_bf16 v[58:61], a[128:131], v[122:125], v[58:61]
v_mfma_f32_16x16x32_bf16 v[58:61], a[144:147], v[126:129], v[58:61]
s_waitcnt vmcnt(8)
v_add_f32 v188, v188, v190
s_waitcnt vmcnt(7)
v_lshlrev_b32 v194, 16, v176
v_add_f32 v189, v189, v191
s_waitcnt lgkmcnt(1)
v_mfma_f32_16x16x32_bf16 v[58:61], a[160:163], v[130:133], v[58:61]
s_waitcnt lgkmcnt(0)
v_and_b32 v195, 0xffff0000, v176
v_mul_f32 v188, 0x3ae38e39, v188
v_lshlrev_b32 v196, 16, v177
v_mfma_f32_16x16x32_bf16 v[58:61], a[176:179], v[134:137], v[58:61]
ds_read_b128 v[90:93], v8 offset:12288
v_mul_f32 v189, 0x3ae38e39, v189
v_and_b32 v197, 0xffff0000, v177
v_mfma_f32_16x16x32_bf16 v[66:69], a[104:107], v[114:117], v[66:69]
v_fma_f32 v219, -v188, v188, v189
v_lshlrev_b32 v198, 16, v178
v_max_f32 v219, 0, v219
v_mfma_f32_16x16x32_bf16 v[66:69], a[120:123], v[118:121], v[66:69]
v_and_b32 v199, 0xffff0000, v178
v_add_f32 v219, 0x3727c5ac, v219
v_mfma_f32_16x16x32_bf16 v[66:69], a[136:139], v[122:125], v[66:69]
ds_read_b128 v[94:97], v8 offset:13312
v_lshlrev_b32 v200, 16, v179
v_mfma_f32_16x16x32_bf16 v[66:69], a[152:155], v[126:129], v[66:69]
v_rcp_f32 v218, v219
v_mfma_f32_16x16x32_bf16 v[66:69], a[168:171], v[130:133], v[66:69]
s_waitcnt vmcnt(6)
v_and_b32 v201, 0xffff0000, v179
v_lshlrev_b32 v202, 16, v180
v_mfma_f32_16x16x32_bf16 v[66:69], a[184:187], v[134:137], v[66:69]
ds_read_b128 v[98:101], v8 offset:14336
v_and_b32 v203, 0xffff0000, v180
v_mfma_f32_16x16x32_bf16 v[62:65], a[100:103], v[114:117], v[62:65]
v_lshlrev_b32 v204, 16, v181
v_and_b32 v205, 0xffff0000, v181
v_mfma_f32_16x16x32_bf16 v[62:65], a[116:119], v[118:121], v[62:65]
v_lshlrev_b32 v206, 16, v182
v_and_b32 v207, 0xffff0000, v182
v_mfma_f32_16x16x32_bf16 v[62:65], a[132:135], v[122:125], v[62:65]
ds_read_b128 v[102:105], v8 offset:15360
v_lshlrev_b32 v208, 16, v183
v_mfma_f32_16x16x32_bf16 v[62:65], a[148:151], v[126:129], v[62:65]
v_and_b32 v209, 0xffff0000, v183
v_add_f32 v194, v194, v202
v_mfma_f32_16x16x32_bf16 v[62:65], a[164:167], v[130:133], v[62:65]
v_add_f32 v195, v195, v203
v_add_f32 v196, v196, v204
v_mfma_f32_16x16x32_bf16 v[62:65], a[180:183], v[134:137], v[62:65]
ds_read_b128 v[106:109], v8 offset:16384
v_add_f32 v197, v197, v205
v_mfma_f32_16x16x32_bf16 v[70:73], a[108:111], v[114:117], v[70:73]
v_add_f32 v198, v198, v206
v_add_f32 v199, v199, v207
v_mfma_f32_16x16x32_bf16 v[70:73], a[124:127], v[118:121], v[70:73]
v_add_f32 v200, v200, v208
v_add_f32 v201, v201, v209
v_mfma_f32_16x16x32_bf16 v[70:73], a[140:143], v[122:125], v[70:73]
s_waitcnt vmcnt(5)
ds_read_b128 v[110:113], v8 offset:17408
v_lshlrev_b32 v202, 16, v184
v_mfma_f32_16x16x32_bf16 v[70:73], a[156:159], v[126:129], v[70:73]
v_and_b32 v203, 0xffff0000, v184
v_lshlrev_b32 v204, 16, v185
v_mfma_f32_16x16x32_bf16 v[70:73], a[172:175], v[130:133], v[70:73]
v_and_b32 v205, 0xffff0000, v185
v_lshlrev_b32 v206, 16, v186
v_mfma_f32_16x16x32_bf16 v[70:73], a[188:191], v[134:137], v[70:73]
s_waitcnt lgkmcnt(5)
v_mfma_f32_16x16x32_bf16 v[58:61], a[192:195], v[90:93], v[58:61]
s_barrier
s_waitcnt lgkmcnt(4)
v_mfma_f32_16x16x32_bf16 v[58:61], a[208:211], v[94:97], v[58:61]
s_waitcnt vmcnt(3)
v_sub_u32 v217, v217, v17
v_cmp_lt_u32 vcc, 0x7f, v217
s_waitcnt lgkmcnt(3)
v_mfma_f32_16x16x32_bf16 v[58:61], a[224:227], v[98:101], v[58:61]
s_waitcnt lgkmcnt(2)
v_and_b32 v207, 0xffff0000, v186
v_lshlrev_b32 v208, 16, v187
v_mfma_f32_16x16x32_bf16 v[58:61], a[240:243], v[102:105], v[58:61]
s_waitcnt lgkmcnt(1)
v_and_b32 v209, 0xffff0000, v187
v_add_f32 v194, v194, v202
v_mfma_f32_16x16x32_bf16 v[58:61], v[26:29], v[106:109], v[58:61]
s_waitcnt lgkmcnt(0)
v_add_f32 v195, v195, v203
v_add_f32 v196, v196, v204
v_mfma_f32_16x16x32_bf16 v[58:61], v[42:45], v[110:113], v[58:61]
s_and_b64 s[88:89], vcc, s[76:77]
s_cbranch_scc1 .Lgu_sp12
.Lgu_rt13:
v_mfma_f32_16x16x32_bf16 v[66:69], a[200:203], v[90:93], v[66:69]
s_cmp_eq_u64 vcc, 0
v_add_f32 v197, v197, v205
v_add_f32 v198, v198, v206
v_mfma_f32_16x16x32_bf16 v[66:69], a[216:219], v[94:97], v[66:69]
s_cselect_b64 s[52:53], s[84:85], s[52:53]
v_add_f32 v199, v199, v207
v_add_f32 v200, v200, v208
v_mfma_f32_16x16x32_bf16 v[66:69], a[232:235], v[98:101], v[66:69]
s_cselect_b64 s[50:51], s[86:87], s[50:51]
global_load_dwordx2 v[170:171], v12, s[52:53] offset:0 sc1
v_add_f32 v201, v201, v209
v_mfma_f32_16x16x32_bf16 v[66:69], a[248:251], v[102:105], v[66:69]
s_cselect_b64 s[56:57], s[54:55], s[48:49]
global_load_dwordx2 v[172:173], v12, s[52:53] offset:128 sc1
v_lshlrev_b32 v202, 16, v192
v_mfma_f32_16x16x32_bf16 v[66:69], v[34:37], v[106:109], v[66:69]
s_cselect_b32 s82, 1, 0
global_load_dwordx4 v[158:161], v10, s[50:51] offset:0 sc1
v_and_b32 v203, 0xffff0000, v192
v_mfma_f32_16x16x32_bf16 v[66:69], v[50:53], v[110:113], v[66:69]
v_mfma_f32_16x16x32_bf16 v[62:65], a[196:199], v[90:93], v[62:65]
s_barrier
v_mfma_f32_16x16x32_bf16 v[62:65], a[212:215], v[94:97], v[62:65]
s_add_u32 s60, s60, s82
global_load_dwordx4 v[162:165], v10, s[50:51] offset:1024 sc1
v_lshlrev_b32 v204, 16, v193
v_mfma_f32_16x16x32_bf16 v[62:65], a[228:231], v[98:101], v[62:65]
s_add_u32 s83, s60, s37
global_load_dwordx4 v[166:169], v10, s[50:51] offset:2048 sc1
v_add_u32 v17, s82, v17
v_add_u32 v157, -1, s83
v_mfma_f32_16x16x32_bf16 v[62:65], a[244:247], v[102:105], v[62:65]
s_cmp_lt_i32 s60, 32
ds_read_b128 v[114:117], v8 offset:18432
global_load_dwordx2 v[174:175], v11, s[52:53] sc1
v_mfma_f32_16x16x32_bf16 v[62:65], v[30:33], v[106:109], v[62:65]
s_cselect_b64 s[54:55], s[44:45], s[46:47]
ds_read_b128 v[118:121], v8 offset:19456
v_mul_f32 v138, v58, v66
v_mfma_f32_16x16x32_bf16 v[62:65], v[46:49], v[110:113], v[62:65]
s_mul_i32 s80, s60, 18432
ds_read_b128 v[122:125], v8 offset:20480
v_mul_f32 v139, v59, v67
v_mfma_f32_16x16x32_bf16 v[70:73], a[204:207], v[90:93], v[70:73]
s_sub_u32 s54, s54, s80
ds_read_b128 v[126:129], v8 offset:21504
v_mul_f32 v140, v60, v68
v_mfma_f32_16x16x32_bf16 v[70:73], a[220:223], v[94:97], v[70:73]
s_subb_u32 s55, s55, 0
ds_read_b128 v[130:133], v8 offset:22528
v_mul_f32 v141, v61, v69
v_mfma_f32_16x16x32_bf16 v[70:73], a[236:239], v[98:101], v[70:73]
s_add_u32 s84, s52, 18688
ds_read_b128 v[134:137], v8 offset:23552
v_cvt_pk_bf16_f32 v146, v138, v139
v_mfma_f32_16x16x32_bf16 v[70:73], a[252:255], v[102:105], v[70:73]
s_addc_u32 s85, s53, 0
v_cvt_pk_bf16_f32 v147, v140, v141
v_and_b32 v205, 0xffff0000, v193
v_mfma_f32_16x16x32_bf16 v[70:73], v[38:41], v[106:109], v[70:73]
s_add_u32 s86, s50, 110592
v_add_f32_dpp v194, v198, v194 row_ror:8 row_mask:0xf bank_mask:0xf
v_add_f32_dpp v195, v199, v195 row_ror:8 row_mask:0xf bank_mask:0xf
v_mfma_f32_16x16x32_bf16 v[70:73], v[54:57], v[110:113], v[70:73]
s_waitcnt lgkmcnt(5)
v_add_f32_dpp v196, v200, v196 row_ror:8 row_mask:0xf bank_mask:0xf
v_add_f32_dpp v197, v201, v197 row_ror:8 row_mask:0xf bank_mask:0xf
v_mfma_f32_16x16x32_bf16 v[74:77], a[0:3], v[114:117], 0
s_waitcnt lgkmcnt(4)
v_fma_f32 v194, v194, v218, v202
v_fma_f32 v195, v195, v218, v203
v_mfma_f32_16x16x32_bf16 v[74:77], a[16:19], v[118:121], v[74:77]
s_waitcnt lgkmcnt(3)
global_load_dword v217, v16, s[38:39] sc1
v_fma_f32 v196, v196, v218, v204
v_mfma_f32_16x16x32_bf16 v[74:77], a[32:35], v[122:125], v[74:77]
s_waitcnt lgkmcnt(2)
v_fma_f32 v197, v197, v218, v205
v_cvt_pk_bf16_f32 v210, v194, v195
v_mfma_f32_16x16x32_bf16 v[74:77], a[48:51], v[126:129], v[74:77]
s_waitcnt lgkmcnt(1)
v_mul_f32 v142, v62, v70
v_mul_f32 v143, v63, v71
v_mfma_f32_16x16x32_bf16 v[74:77], a[64:67], v[130:133], v[74:77]
s_waitcnt lgkmcnt(0)
v_mul_f32 v144, v64, v72
v_mul_f32 v145, v65, v73
v_mfma_f32_16x16x32_bf16 v[74:77], a[80:83], v[134:137], v[74:77]
s_addc_u32 s87, s51, 0
ds_read_b128 v[90:93], v8 offset:24576
v_cvt_pk_bf16_f32 v148, v142, v143
v_mfma_f32_16x16x32_bf16 v[82:85], a[8:11], v[114:117], 0
s_add_u32 s72, s72, 1
ds_read_b128 v[94:97], v8 offset:25600
v_cvt_pk_bf16_f32 v149, v144, v145
v_mfma_f32_16x16x32_bf16 v[82:85], a[24:27], v[118:121], v[82:85]
v_mfma_f32_16x16x32_bf16 v[82:85], a[40:43], v[122:125], v[82:85]
s_waitcnt vmcnt(8)
s_barrier
v_mfma_f32_16x16x32_bf16 v[82:85], a[56:59], v[126:129], v[82:85]
s_cmp_le_i32 s60, s72
s_waitcnt vmcnt(7)
)"
R"(s_mov_b64 exec, 1
global_store_dword v18, v215, s[38:39] sc1
s_mov_b64 exec, -1
v_mfma_f32_16x16x32_bf16 v[82:85], a[72:75], v[130:133], v[82:85]
s_cselect_b64 s[76:77], -1, 0
v_cvt_pk_bf16_f32 v211, v196, v197
v_mfma_f32_16x16x32_bf16 v[82:85], a[88:91], v[134:137], v[82:85]
ds_read_b128 v[98:101], v8 offset:26624
v_mfma_f32_16x16x32_bf16 v[78:81], a[4:7], v[114:117], 0
ds_write_b64 v14, v[210:211]
v_mfma_f32_16x16x32_bf16 v[78:81], a[20:23], v[118:121], v[78:81]
global_store_dwordx4 v3, v[146:149], s[38:39] sc1
v_mfma_f32_16x16x32_bf16 v[78:81], a[36:39], v[122:125], v[78:81]
ds_read_b128 v[102:105], v8 offset:27648
v_add_u32 v3, 147456, v3
v_mfma_f32_16x16x32_bf16 v[78:81], a[52:55], v[126:129], v[78:81]
ds_read_b64 v[212:213], v15
v_mfma_f32_16x16x32_bf16 v[78:81], a[68:71], v[130:133], v[78:81]
v_mfma_f32_16x16x32_bf16 v[78:81], a[84:87], v[134:137], v[78:81]
ds_read_b128 v[106:109], v8 offset:28672
v_mfma_f32_16x16x32_bf16 v[86:89], a[12:15], v[114:117], 0
v_mfma_f32_16x16x32_bf16 v[86:89], a[28:31], v[118:121], v[86:89]
v_mfma_f32_16x16x32_bf16 v[86:89], a[44:47], v[122:125], v[86:89]
ds_read_b128 v[110:113], v8 offset:29696
v_mfma_f32_16x16x32_bf16 v[86:89], a[60:63], v[126:129], v[86:89]
v_mfma_f32_16x16x32_bf16 v[86:89], a[76:79], v[130:133], v[86:89]
s_waitcnt lgkmcnt(2)
global_store_dwordx2 v13, v[212:213], s[58:59] sc1
v_mov_b32 v216, v214
v_mfma_f32_16x16x32_bf16 v[86:89], a[92:95], v[134:137], v[86:89]
v_mfma_f32_16x16x32_bf16 v[74:77], a[96:99], v[90:93], v[74:77]
v_mfma_f32_16x16x32_bf16 v[74:77], a[112:115], v[94:97], v[74:77]
v_mfma_f32_16x16x32_bf16 v[74:77], a[128:131], v[98:101], v[74:77]
v_mfma_f32_16x16x32_bf16 v[74:77], a[144:147], v[102:105], v[74:77]
s_waitcnt vmcnt(8)
v_add_f32 v170, v170, v172
s_waitcnt vmcnt(7)
v_lshlrev_b32 v194, 16, v158
v_add_f32 v171, v171, v173
s_waitcnt lgkmcnt(1)
v_mfma_f32_16x16x32_bf16 v[74:77], a[160:163], v[106:109], v[74:77]
s_waitcnt lgkmcnt(0)
v_and_b32 v195, 0xffff0000, v158
v_mul_f32 v170, 0x3ae38e39, v170
v_lshlrev_b32 v196, 16, v159
v_mfma_f32_16x16x32_bf16 v[74:77], a[176:179], v[110:113], v[74:77]
ds_read_b128 v[114:117], v8 offset:30720
v_mul_f32 v171, 0x3ae38e39, v171
v_and_b32 v197, 0xffff0000, v159
v_mfma_f32_16x16x32_bf16 v[82:85], a[104:107], v[90:93], v[82:85]
v_fma_f32 v219, -v170, v170, v171
v_lshlrev_b32 v198, 16, v160
v_max_f32 v219, 0, v219
v_mfma_f32_16x16x32_bf16 v[82:85], a[120:123], v[94:97], v[82:85]
v_and_b32 v199, 0xffff0000, v160
v_add_f32 v219, 0x3727c5ac, v219
v_mfma_f32_16x16x32_bf16 v[82:85], a[136:139], v[98:101], v[82:85]
ds_read_b128 v[118:121], v8 offset:31744
v_lshlrev_b32 v200, 16, v161
v_mfma_f32_16x16x32_bf16 v[82:85], a[152:155], v[102:105], v[82:85]
v_rcp_f32 v218, v219
v_mfma_f32_16x16x32_bf16 v[82:85], a[168:171], v[106:109], v[82:85]
s_waitcnt vmcnt(6)
v_and_b32 v201, 0xffff0000, v161
v_lshlrev_b32 v202, 16, v162
v_mfma_f32_16x16x32_bf16 v[82:85], a[184:187], v[110:113], v[82:85]
ds_read_b128 v[122:125], v8 offset:32768
v_and_b32 v203, 0xffff0000, v162
v_mfma_f32_16x16x32_bf16 v[78:81], a[100:103], v[90:93], v[78:81]
v_lshlrev_b32 v204, 16, v163
v_and_b32 v205, 0xffff0000, v163
v_mfma_f32_16x16x32_bf16 v[78:81], a[116:119], v[94:97], v[78:81]
v_lshlrev_b32 v206, 16, v164
v_and_b32 v207, 0xffff0000, v164
v_mfma_f32_16x16x32_bf16 v[78:81], a[132:135], v[98:101], v[78:81]
ds_read_b128 v[126:129], v8 offset:33792
v_lshlrev_b32 v208, 16, v165
v_mfma_f32_16x16x32_bf16 v[78:81], a[148:151], v[102:105], v[78:81]
v_and_b32 v209, 0xffff0000, v165
v_add_f32 v194, v194, v202
v_mfma_f32_16x16x32_bf16 v[78:81], a[164:167], v[106:109], v[78:81]
v_add_f32 v195, v195, v203
v_add_f32 v196, v196, v204
v_mfma_f32_16x16x32_bf16 v[78:81], a[180:183], v[110:113], v[78:81]
ds_read_b128 v[130:133], v8 offset:34816
v_add_f32 v197, v197, v205
v_mfma_f32_16x16x32_bf16 v[86:89], a[108:111], v[90:93], v[86:89]
v_add_f32 v198, v198, v206
v_add_f32 v199, v199, v207
v_mfma_f32_16x16x32_bf16 v[86:89], a[124:127], v[94:97], v[86:89]
v_add_f32 v200, v200, v208
v_add_f32 v201, v201, v209
v_mfma_f32_16x16x32_bf16 v[86:89], a[140:143], v[98:101], v[86:89]
s_waitcnt vmcnt(5)
ds_read_b128 v[134:137], v8 offset:35840
v_lshlrev_b32 v202, 16, v166
v_mfma_f32_16x16x32_bf16 v[86:89], a[156:159], v[102:105], v[86:89]
v_and_b32 v203, 0xffff0000, v166
v_lshlrev_b32 v204, 16, v167
v_mfma_f32_16x16x32_bf16 v[86:89], a[172:175], v[106:109], v[86:89]
v_and_b32 v205, 0xffff0000, v167
v_lshlrev_b32 v206, 16, v168
v_mfma_f32_16x16x32_bf16 v[86:89], a[188:191], v[110:113], v[86:89]
s_waitcnt lgkmcnt(5)
v_mfma_f32_16x16x32_bf16 v[74:77], a[192:195], v[114:117], v[74:77]
s_barrier
s_waitcnt lgkmcnt(4)
v_mfma_f32_16x16x32_bf16 v[74:77], a[208:211], v[118:121], v[74:77]
s_waitcnt vmcnt(3)
v_sub_u32 v217, v217, v17
v_cmp_lt_u32 vcc, 0x7f, v217
s_waitcnt lgkmcnt(3)
v_mfma_f32_16x16x32_bf16 v[74:77], a[224:227], v[122:125], v[74:77]
s_waitcnt lgkmcnt(2)
v_and_b32 v207, 0xffff0000, v168
v_lshlrev_b32 v208, 16, v169
v_mfma_f32_16x16x32_bf16 v[74:77], a[240:243], v[126:129], v[74:77]
s_waitcnt lgkmcnt(1)
v_and_b32 v209, 0xffff0000, v169
v_add_f32 v194, v194, v202
v_mfma_f32_16x16x32_bf16 v[74:77], v[26:29], v[130:133], v[74:77]
s_waitcnt lgkmcnt(0)
v_add_f32 v195, v195, v203
v_add_f32 v196, v196, v204
v_mfma_f32_16x16x32_bf16 v[74:77], v[42:45], v[134:137], v[74:77]
s_and_b64 s[88:89], vcc, s[76:77]
s_cbranch_scc1 .Lgu_sp14
.Lgu_rt15:
v_mfma_f32_16x16x32_bf16 v[82:85], a[200:203], v[114:117], v[82:85]
s_cmp_eq_u64 vcc, 0
v_add_f32 v197, v197, v205
v_add_f32 v198, v198, v206
v_mfma_f32_16x16x32_bf16 v[82:85], a[216:219], v[118:121], v[82:85]
s_cselect_b64 s[52:53], s[84:85], s[52:53]
v_add_f32 v199, v199, v207
v_add_f32 v200, v200, v208
v_mfma_f32_16x16x32_bf16 v[82:85], a[232:235], v[122:125], v[82:85]
s_cselect_b64 s[50:51], s[86:87], s[50:51]
global_load_dwordx2 v[188:189], v12, s[52:53] offset:0 sc1
v_add_f32 v201, v201, v209
v_mfma_f32_16x16x32_bf16 v[82:85], a[248:251], v[126:129], v[82:85]
s_cselect_b64 s[58:59], s[54:55], s[48:49]
global_load_dwordx2 v[190:191], v12, s[52:53] offset:128 sc1
v_lshlrev_b32 v202, 16, v174
v_mfma_f32_16x16x32_bf16 v[82:85], v[34:37], v[130:133], v[82:85]
s_cselect_b32 s82, 1, 0
v_xor_b32 v8, 0x10000, v8
global_load_dwordx4 v[176:179], v10, s[50:51] offset:0 sc1
v_mfma_f32_16x16x32_bf16 v[82:85], v[50:53], v[134:137], v[82:85]
v_mfma_f32_16x16x32_bf16 v[78:81], a[196:199], v[114:117], v[78:81]
s_barrier
v_mfma_f32_16x16x32_bf16 v[78:81], a[212:215], v[118:121], v[78:81]
s_add_u32 s60, s60, s82
global_load_dwordx4 v[180:183], v10, s[50:51] offset:1024 sc1
v_and_b32 v203, 0xffff0000, v174
v_mfma_f32_16x16x32_bf16 v[78:81], a[228:231], v[122:125], v[78:81]
s_add_u32 s83, s60, s37
global_load_dwordx4 v[184:187], v10, s[50:51] offset:2048 sc1
v_add_u32 v17, s82, v17
v_add_u32 v214, -1, s83
v_mfma_f32_16x16x32_bf16 v[78:81], a[244:247], v[126:129], v[78:81]
s_cmp_lt_i32 s60, 32
ds_read_b128 v[90:93], v8 offset:0
global_load_dwordx2 v[192:193], v11, s[52:53] sc1
v_mfma_f32_16x16x32_bf16 v[78:81], v[30:33], v[130:133], v[78:81]
s_cselect_b64 s[54:55], s[44:45], s[46:47]
ds_read_b128 v[94:97], v8 offset:1024
v_mul_f32 v138, v74, v82
v_mfma_f32_16x16x32_bf16 v[78:81], v[46:49], v[134:137], v[78:81]
s_mul_i32 s80, s60, 18432
ds_read_b128 v[98:101], v8 offset:2048
v_mul_f32 v139, v75, v83
v_mfma_f32_16x16x32_bf16 v[86:89], a[204:207], v[114:117], v[86:89]
ds_read_b128 v[102:105], v8 offset:3072
v_mul_f32 v140, v76, v84
v_mfma_f32_16x16x32_bf16 v[86:89], a[220:223], v[118:121], v[86:89]
ds_read_b128 v[106:109], v8 offset:4096
v_mul_f32 v141, v77, v85
v_mfma_f32_16x16x32_bf16 v[86:89], a[236:239], v[122:125], v[86:89]
ds_read_b128 v[110:113], v8 offset:5120
v_cvt_pk_bf16_f32 v146, v138, v139
v_mfma_f32_16x16x32_bf16 v[86:89], a[252:255], v[126:129], v[86:89]
v_cvt_pk_bf16_f32 v147, v140, v141
v_lshlrev_b32 v204, 16, v175
v_mfma_f32_16x16x32_bf16 v[86:89], v[38:41], v[130:133], v[86:89]
v_mfma_f32_16x16x32_bf16 v[86:89], v[54:57], v[134:137], v[86:89]
s_sub_u32 s70, s70, 1
s_cbranch_scc0 .Lgu_top16
v_mul_f32 v138, v74, v82
v_mul_f32 v139, v75, v83
v_mul_f32 v140, v76, v84
v_mul_f32 v141, v77, v85
v_cvt_pk_bf16_f32 v146, v138, v139
v_cvt_pk_bf16_f32 v147, v140, v141
v_mul_f32 v142, v78, v86
v_mul_f32 v143, v79, v87
v_mul_f32 v144, v80, v88
v_mul_f32 v145, v81, v89
v_cvt_pk_bf16_f32 v148, v142, v143
v_cvt_pk_bf16_f32 v149, v144, v145
global_store_dwordx4 v3, v[146:149], s[38:39] sc1
v_mov_b32 v219, s60
ds_write_b32 v1, v219 offset:36864
v_subrev_u32 v17, s60, v17
s_waitcnt vmcnt(1) lgkmcnt(0)
s_barrier
v_mov_b32 v218, 192
ds_read_b32 v218, v218 offset:36864
s_waitcnt vmcnt(0)
s_barrier
s_waitcnt lgkmcnt(0)
v_readfirstlane_b32 s60, v218
s_nop 3
s_max_i32 s60, s60, 2
s_min_i32 s60, s60, 66
s_add_u32 s60, s60, 1
s_branch .Lgu_fin
.Lgu_fin:
s_cmp_gt_i32 s60, 63
s_cbranch_scc1 .Lgu_end
v_add_u32 v220, s60, v17
global_load_dword v217, v16, s[38:39] sc1
s_cmp_lt_i32 s60, 32
s_cselect_b64 s[54:55], s[44:45], s[46:47]
s_mul_i32 s80, s60, 18432
s_sub_u32 s54, s54, s80
s_subb_u32 s55, s55, 0
s_mov_b64 s[56:57], s[54:55]
s_mul_i32 s80, s60, 110592
s_mul_i32 s81, s60, 18688
s_mov_b64 s[50:51], s[38:39]
s_mov_b64 s[52:53], s[38:39]
s_add_u32 s50, s50, s80
s_addc_u32 s51, s51, 0
s_add_u32 s52, s52, s81
s_addc_u32 s53, s53, 0
s_waitcnt vmcnt(0)
v_sub_u32 v217, v217, v220
v_cmp_lt_u32 vcc, 0x7f, v217
s_cbranch_vccnz .Lgu_sp17
.Lgu_rt18:
global_load_dwordx2 v[170:171], v12, s[52:53] offset:0 sc1
global_load_dwordx2 v[172:173], v12, s[52:53] offset:128 sc1
global_load_dwordx4 v[158:161], v10, s[50:51] offset:0 sc1
global_load_dwordx4 v[162:165], v10, s[50:51] offset:1024 sc1
global_load_dwordx4 v[166:169], v10, s[50:51] offset:2048 sc1
global_load_dwordx2 v[174:175], v11, s[52:53] sc1
s_waitcnt vmcnt(4)
v_add_f32 v170, v170, v172
s_waitcnt vmcnt(3)
v_lshlrev_b32 v194, 16, v158
v_add_f32 v171, v171, v173
v_and_b32 v195, 0xffff0000, v158
v_mul_f32 v170, 0x3ae38e39, v170
v_lshlrev_b32 v196, 16, v159
v_mul_f32 v171, 0x3ae38e39, v171
v_and_b32 v197, 0xffff0000, v159
v_fma_f32 v219, -v170, v170, v171
v_lshlrev_b32 v198, 16, v160
v_max_f32 v219, 0, v219
v_and_b32 v199, 0xffff0000, v160
v_add_f32 v219, 0x3727c5ac, v219
v_lshlrev_b32 v200, 16, v161
v_rcp_f32 v218, v219
v_and_b32 v201, 0xffff0000, v161
s_waitcnt vmcnt(2)
v_lshlrev_b32 v202, 16, v162
v_and_b32 v203, 0xffff0000, v162
v_lshlrev_b32 v204, 16, v163
v_and_b32 v205, 0xffff0000, v163
v_lshlrev_b32 v206, 16, v164
v_and_b32 v207, 0xffff0000, v164
v_lshlrev_b32 v208, 16, v165
v_and_b32 v209, 0xffff0000, v165
v_add_f32 v194, v194, v202
v_add_f32 v195, v195, v203
v_add_f32 v196, v196, v204
v_add_f32 v197, v197, v205
v_add_f32 v198, v198, v206
v_add_f32 v199, v199, v207
v_add_f32 v200, v200, v208
v_add_f32 v201, v201, v209
s_waitcnt vmcnt(1)
v_lshlrev_b32 v202, 16, v166
v_and_b32 v203, 0xffff0000, v166
v_lshlrev_b32 v204, 16, v167
v_and_b32 v205, 0xffff0000, v167
v_lshlrev_b32 v206, 16, v168
v_and_b32 v207, 0xffff0000, v168
v_lshlrev_b32 v208, 16, v169
v_and_b32 v209, 0xffff0000, v169
v_add_f32 v194, v194, v202
v_add_f32 v195, v195, v203
v_add_f32 v196, v196, v204
v_add_f32 v197, v197, v205
v_add_f32 v198, v198, v206
v_add_f32 v199, v199, v207
v_add_f32 v200, v200, v208
v_add_f32 v201, v201, v209
s_waitcnt vmcnt(0)
v_lshlrev_b32 v202, 16, v174
v_and_b32 v203, 0xffff0000, v174
v_lshlrev_b32 v204, 16, v175
v_and_b32 v205, 0xffff0000, v175
v_add_f32_dpp v194, v198, v194 row_ror:8 row_mask:0xf bank_mask:0xf
v_add_f32_dpp v195, v199, v195 row_ror:8 row_mask:0xf bank_mask:0xf
v_add_f32_dpp v196, v200, v196 row_ror:8 row_mask:0xf bank_mask:0xf
v_add_f32_dpp v197, v201, v197 row_ror:8 row_mask:0xf bank_mask:0xf
v_fma_f32 v194, v194, v218, v202
v_fma_f32 v195, v195, v218, v203
v_fma_f32 v196, v196, v218, v204
v_fma_f32 v197, v197, v218, v205
v_cvt_pk_bf16_f32 v210, v194, v195
v_cvt_pk_bf16_f32 v211, v196, v197
ds_write_b64 v14, v[210:211]
ds_read_b64 v[212:213], v15
s_waitcnt lgkmcnt(0)
global_store_dwordx2 v13, v[212:213], s[56:57] sc1
s_add_u32 s60, s60, 4
s_branch .Lgu_fin
.Lgu_end:
s_endpgm
.Lgu_sp7:
s_sleep 1
global_load_dword v254, v4, s[38:39] sc1
s_add_u32 s71, s71, 1
s_waitcnt vmcnt(0)
v_sub_u32 v254, v254, v5
v_cmp_lt_u32 vcc, 0x7f, v254
s_cbranch_vccz .Lgu_rt8
s_cmp_lt_u32 s71, 3000000
s_cbranch_scc1 .Lgu_sp7
s_branch .Lgu_rt8
.Lgu_sp9:
s_sleep 1
global_load_dword v254, v4, s[38:39] sc1
s_add_u32 s71, s71, 1
s_waitcnt vmcnt(0)
v_sub_u32 v254, v254, v5
v_cmp_lt_u32 vcc, 0x7f, v254
s_cbranch_vccz .Lgu_rt10
s_cmp_lt_u32 s71, 3000000
s_cbranch_scc1 .Lgu_sp9
s_branch .Lgu_rt10
.Lgu_sp12:
s_sleep 1
global_load_dword v217, v16, s[38:39] sc1
s_add_u32 s71, s71, 1
s_waitcnt vmcnt(0)
v_sub_u32 v217, v217, v17
v_cmp_lt_u32 vcc, 0x7f, v217
s_and_b64 vcc, vcc, s[76:77]
s_cbranch_vccz .Lgu_rt13
s_cmp_lt_u32 s71, 3000000
s_cbranch_scc1 .Lgu_sp12
s_branch .Lgu_rt13
.Lgu_sp14:
s_sleep 1
global_load_dword v217, v16, s[38:39] sc1
s_add_u32 s71, s71, 1
s_waitcnt vmcnt(0)
v_sub_u32 v217, v217, v17
v_cmp_lt_u32 vcc, 0x7f, v217
s_and_b64 vcc, vcc, s[76:77]
s_cbranch_vccz .Lgu_rt15
s_cmp_lt_u32 s71, 3000000
s_cbranch_scc1 .Lgu_sp14
s_branch .Lgu_rt15
.Lgu_sp17:
s_sleep 1
global_load_dword v217, v16, s[38:39] sc1
s_add_u32 s71, s71, 1
s_waitcnt vmcnt(0)
v_sub_u32 v217, v217, v220
v_cmp_lt_u32 vcc, 0x7f, v217
s_cbranch_vccz .Lgu_rt18
s_cmp_lt_u32 s71, 3000000
s_cbranch_scc1 .Lgu_sp17
s_branch .Lgu_rt18
.Lkend:
)"
  :: "{v[0:31]}"(va), "{v[32:63]}"(vb), "{s[36:51]}"(sa), "{s[52:67]}"(sb) : "v255", "a255", "s99", "vcc", "memory");
}

extern "C" void kernel_launch(void* const* d_in, const int* in_sizes, int n_in,
                              void* d_out, int out_size, void* d_ws, size_t ws_size,
                              hipStream_t stream) {
  k_main<<<256, 256, 0, stream>>>((const char*)d_in[0], (const char*)d_in[1], (const char*)d_in[2], (const char*)d_in[4],
                                  (const char*)d_in[5], (const char*)d_in[6], (char*)d_ws, (char*)d_out);
}
```

```cpp
#include <hip/hip_runtime.h>
#include <hip/hip_bf16.h>
#include <stdint.h>
typedef __attribute__((ext_vector_type(32))) unsigned u32x32;
typedef __attribute__((ext_vector_type(16))) unsigned u32x16;
constexpr unsigned RP = 80u;
constexpr unsigned PAD = 12u;
constexpr unsigned XT = 18432u;
constexpr unsigned X1P = 18688u;
constexpr unsigned HT = 147456u;
constexpr unsigned PT = 110592u;
constexpr unsigned FL = 1024u;
constexpr unsigned FR = 102400u;
constexpr unsigned WS_FL = 4096u;
constexpr unsigned WS_ST = 413696u;
constexpr unsigned WS_O = 1048576u;
constexpr unsigned WS_X1 = 6946816u;
constexpr unsigned WS_X2 = 13000704u;
constexpr unsigned WS_XD = 15396864u;
constexpr unsigned WS_P = 17977344u;
constexpr unsigned WS_H = 53366784u;
constexpr unsigned WS_END = 100552704u;
constexpr unsigned FO = 0u;
constexpr unsigned FX1 = 8192u;
constexpr unsigned FX2 = 65536u;
constexpr unsigned FP = 10240u;
constexpr unsigned FH = 28672u;
__device__ __forceinline__ void touch(const void* p) {
  asm volatile("s_mov_b32 m0, 0x27f00\n\ts_nop 0\n\tglobal_load_lds_dword %0, off" :: "v"(p) : "memory");
}
__global__ __launch_bounds__(256, 1) void k_main(const char* x0, const char* wqkv, const char* wo, const char* wg, const char* wu, const char* wd,
                                                 char* ws, char* out) {
  __shared__ __attribute__((aligned(1024))) char smem[163840];
  asm volatile("" :: "s"((unsigned)(size_t)(__attribute__((address_space(3))) const char*)smem) : "memory");
  unsigned lane = threadIdx.x & 63; const unsigned wave = __builtin_amdgcn_readfirstlane(threadIdx.x >> 6);
  const unsigned xc = blockIdx.x & 7, jj = blockIdx.x >> 3;
  const unsigned role = jj < 4 ? 0 : (jj == 4 || jj == 31) ? ((xc & 1) == 0 ? 1 : 3) : jj < 14 ? 2 : 3;
  const unsigned qa_hg = xc, qa_rho = jj, wo_rho = xc >> 1, wo_cw = jj & 1, dn_i = xc * 9 + jj - 5, gu_i = jj == 4 ? (xc ^ 1) * 18 + 17 : xc * 18 + jj - 14;
  unsigned qv[9] = {}, qx[5] = {};
  if (__builtin_expect(jj < 4, 1)) {
#pragma unroll
    for (unsigned j = 0; j < 9; ++j) {
      const unsigned c = j * 64 + lane, sg = (c * 5462u) >> 16;
      qv[j] = sg * (1152 - 192) + c * 16 + 4096 - (j & 7) * 1024;
    }
    const unsigned pc0 = wave < 2 ? wave * 5 : 2 + wave * 4;
#pragma unroll
    for (unsigned j = 0; j < 5; ++j)
      qx[j] = ((jj * 32 + 31) * 16 + ((pc0 + j) & 1) * 8 + (lane >> 3)) * 1152 + ((pc0 + j) >> 1) * 128 + (((lane ^ (lane >> 3)) & 7) << 4) + 4096 - j * 1024;
    const unsigned long long xb = (unsigned long long)(x0 - 2048);
    const unsigned long long wb = (unsigned long long)(wqkv + ((xc * 144) & ~63u) + wave * (16 * 3456));
    asm volatile("s_add_u32 m0, %18, 0\n\ts_sub_u32 vcc_lo, %19, 0\n\ts_subb_u32 vcc_hi, %20, 0\n\tglobal_load_lds_dwordx4 %13, vcc offset:-2048 sc1\n\tglobal_load_lds_dwordx4 %14, vcc offset:-1024 sc1\n\tglobal_load_lds_dwordx4 %15, vcc offset:0 sc1\n\tglobal_load_lds_dwordx4 %16, vcc offset:1024 sc1\n\ts_cmp_gt_u32 %21, 1\n\ts_cbranch_scc1 .Lqx0\n\tglobal_load_lds_dwordx4 %17, vcc offset:2048 sc1\n.Lqx0:\n\ts_add_u32 m0, %18, 18432\n\ts_sub_u32 vcc_lo, %19, 18432\n\ts_subb_u32 vcc_hi, %20, 0\n\tglobal_load_lds_dwordx4 %13, vcc offset:-2048 sc1\n\tglobal_load_lds_dwordx4 %14, vcc offset:-1024 sc1\n\tglobal_load_lds_dwordx4 %15, vcc offset:0 sc1\n\tglobal_load_lds_dwordx4 %16, vcc offset:1024 sc1\n\ts_cmp_gt_u32 %21, 1\n\ts_cbranch_scc1 .Lqx1\n\tglobal_load_lds_dwordx4 %17, vcc offset:2048 sc1\n.Lqx1:\n\t" : "+v"(lane) : "v"(qv[0]), "v"(qv[1]), "v"(qv[2]), "v"(qv[3]), "v"(qv[4]), "v"(qv[5]), "v"(qv[6]), "v"(qv[7]), "v"(qv[8]), "s"(47104 + wave * 9216),
                 "s"((unsigned)wb), "s"((unsigned)(wb >> 32)), "v"(qx[0]), "v"(qx[1]), "v"(qx[2]), "v"(qx[3]), "v"(qx[4]), "s"(pc0 * 1024 + 2048),
                 "s"((unsigned)xb), "s"((unsigned)(xb >> 32)), "s"(wave) : "vcc", "scc", "memory");
  } else {
    if (wave < 2) { const unsigned u = min((jj - 4) * 124 + threadIdx.x, 3455u), sg = u >> 1, k = sg / 3; touch(wqkv + k * 3456 + (sg - 3 * k) * 1152 + xc * 144 + (u & 1) * 140); }
    else if (wave == 2) {
      const unsigned o = min((lane & 7) * 1048576u, 5308412u);
      touch(wg + o); touch(wu + o); touch(wd + o); touch(out + min(o, 2359292u));
      touch(ws + min(lane * 2097152u, WS_END - 4));
    } else {
      unsigned long long pc; unsigned len;
      asm volatile("s_getpc_b64 %0\n.Lkbeg:\n\ts_mov_b32 %1, .Lkend-.Lkbeg" : "=s"(pc), "=s"(len));
      touch((const char*)pc + min(((jj - 4) * 64 + lane) * 128, len));
    }
    if (wave < 3 && (xc & 1) == 0) touch(wo + min((jj - 4) * 186 + threadIdx.x, 5183u) * 128);
    if (jj >= 14 && wave < 2) touch(x0 + ((blockIdx.x - 112) * 128 + threadIdx.x) * 128);
  }

  const unsigned r = lane & 15, g = lane >> 4, q = r >> 2, p = lane & 3;
  u32x32 va = {}, vb = {};
  u32x16 sa = {}, sb = {};
  unsigned sv[28] = {};
  sa[0] = __builtin_amdgcn_readfirstlane(role * 4 + wave); sa[2] = (unsigned)(unsigned long long)ws; sa[3] = (unsigned)((unsigned long long)ws >> 32);
  if (__builtin_expect(role == 0, 1)) {
    const unsigned hg = qa_hg, rho = qa_rho, row0 = rho * RP + PAD, pc0 = wave < 2 ? wave * 5 : 2 + wave * 4, hb = wave * 36 + (wave & 1) * 4;
    auto oa = [&](unsigned b) { return 43520 + (b >> 4) * 256 + r * 16 + (b & 15); };
    const unsigned m0 = 4 * g + (r & 1), m1 = m0 + 2, hm = (r & 1) ? 0xFFFF0000u : 0xFFFFu, ninf = 0xFF800000u;
    va[0] = qv[0];
    va[1] = qv[1];
    va[2] = qv[2];
    va[3] = qv[3];
    va[4] = qv[4];
    va[5] = qv[5];
    va[6] = qv[6];
    va[7] = qv[7];
    va[8] = qv[8];
    va[9] = (8 * g + q) * 576 + (hg & 3) * 16 + hb + p * 8;
    va[10] = (8 * g + q) * 576 + (hg & 3) * 16 + 32 + (p & 1) * 72 + (wave == 0 ? (p >> 1) * 192 : 384);
    va[11] = qx[0];
    va[12] = qx[1];
    va[13] = qx[2];
    va[14] = qx[3];
    va[15] = qx[4];
    va[16] = WS_O + (row0 - 1) * XT + hg * 2304 + wave * 1024 + (wave < 2 ? lane : r) * 16;
    va[17] = 43520 + wave * 1024 + (wave < 2 ? lane : r) * 16;
    va[18] = oa(hb + g * 8);
    va[19] = ((g & 1) == (wave >> 1)) ? oa(32 + (wave & 1) * 4 + (wave >> 1) * 72) : 46336 + lane * 4;
    va[20] = g == 0 ? 40960 + ((wave >> 1) * 16 + r) * 8 + (wave & 1) * 4 : 46080;
    va[21] = 41216 + ((wave >> 1) * 16 + r) * 8 + (wave & 1) * 4;
    va[22] = lane * 8;
    va[23] = lane * 16;
    va[24] = 43008 + r * 4;
    va[25] = 43008 + g * 16;
    va[26] = 41984 + (q * 16 + r) * 16 + p * 4;
    va[27] = 43136 + (((m0 > r) ? 0 : 1) * 16 + m0) * 4;
    va[28] = 43136 + (((m1 > r) ? 0 : 1) * 16 + m1) * 4;
    va[29] = 43136 + (((m0 > r) ? 1 : 0) * 16 + m0) * 4;
    va[30] = 43136 + (((m1 > r) ? 1 : 0) * 16 + m1) * 4;
    va[31] = 0u;
    vb[0] = (4 * g + 0 > r && !((r ^ 0) & 1)) ? 0u : ninf;
    vb[1] = (4 * g + 1 > r && !((r ^ 1) & 1)) ? 0u : ninf;
    vb[2] = (4 * g + 2 > r && !((r ^ 2) & 1)) ? 0u : ninf;
    vb[3] = (4 * g + 3 > r && !((r ^ 3) & 1)) ? 0u : ninf;
    vb[4] = (4 * g + 0 <= r && !((r ^ 0) & 1)) ? 0u : ninf;
    vb[5] = (4 * g + 1 <= r && !((r ^ 1) & 1)) ? 0u : ninf;
    vb[6] = (4 * g + 2 <= r && !((r ^ 2) & 1)) ? 0u : ninf;
    vb[7] = (4 * g + 3 <= r && !((r ^ 3) & 1)) ? 0u : ninf;
    vb[8] = m0 > r ? hm : 0u;
    vb[9] = m1 > r ? hm : 0u;
    vb[10] = m0 > r ? 0u : hm;
    vb[11] = m1 > r ? 0u : hm;
    vb[12] = WS_FL + rho * FR + FX2 + min(lane, 35u) * FL;
    vb[13] = WS_FL + rho * FR + FX2 + min(lane, 35u) * FL;
    vb[14] = 0u - 29;
    vb[15] = WS_FL + rho * FR + FO + hg * FL;
    vb[16] = 0u - 2;
    vb[17] = WS_FL + lane * FL;
    vb[18] = 0u;
    vb[19] = r * 128 + (((0 + g) ^ (r & 7)) << 4);
    vb[20] = r * 128 + (((4 + g) ^ (r & 7)) << 4);
    sv[0] = (unsigned)(unsigned long long)(wqkv + ((hg * 144) & ~63u) + wave * (16 * 3456) + 0 * 64 * 3456);
    sv[1] = (unsigned)((unsigned long long)(wqkv + ((hg * 144) & ~63u) + wave * (16 * 3456) + 0 * 64 * 3456) >> 32);
    sv[2] = (unsigned)(unsigned long long)(x0 - 2048 - 2 * XT);
    sv[3] = (unsigned)((unsigned long long)(x0 - 2048 - 2 * XT) >> 32);
    sv[4] = (unsigned)(unsigned long long)((ws + WS_X2) - x0 + (rho ? 32 : 31) * (long long)XT);
    sv[5] = (unsigned)((unsigned long long)((ws + WS_X2) - x0 + (rho ? 32 : 31) * (long long)XT) >> 32);
    sv[6] = (unsigned)(unsigned long long)(-(long long)XT);
    sv[7] = (unsigned)((unsigned long long)(-(long long)XT) >> 32);
    sv[8] = 47104 + wave * 9216;
    sv[9] = pc0 * 1024 + 2048 + 0x10000;
    sv[10] = rho ? 0u : XT;
    sv[11] = rho ? 0u - FR : 0u;
    sv[12] = rho ? 0u : ninf;
    sv[13] = hg * 4 + rho;
  } else if (__builtin_expect(role == 1, 0)) {
    const unsigned rho = wo_rho, cw = wo_cw, row0 = rho * RP + PAD, pc0 = wave * 5;
    auto wv = [&](unsigned j) { const unsigned b = j * 1024 + lane * 16, rw = b / 576; return rw * 576 + b + 4096 - (j & 7) * 1024; };
    va[0] = wv(0);
    va[1] = wv(1);
    va[2] = wv(2);
    va[3] = wv(3);
    va[4] = wv(4);
    va[5] = wv(5);
    va[6] = wv(6);
    va[7] = wv(7);
    va[8] = wv(8);
    va[9] = (8 * g + q) * 576 + cw * 64 + wave * 128 + p * 16;
    va[10] = (8 * g + q) * 576 + (cw ? 0 : 512) + p * 16 + (wave >> 1) * 8;
    va[11] = WS_O + (row0 - 2) * XT + pc0 * 1024 + 2048 + lane * 16;
    va[12] = WS_O + (row0 + 63) * XT + pc0 * 1024 + 2048 + lane * 16;
    va[13] = WS_X1 + (row0 - 5) * X1P + (cw * 10 + wave * 2) * 1024 + lane * 16;
    va[14] = WS_X1 + (row0 - 5) * X1P + (8 + cw) * 1024 + lane * 16;
    va[15] = WS_X1 + (row0 - 7) * X1P + XT + cw * 128 + r * 8;
    va[16] = ((rho * 32 + 31) * 16 + 0 + (lane >> 3)) * 1152 + (cw * 5 + wave) * 128 + (((lane ^ (lane >> 3)) & 7) << 4) - 0;
    va[17] = ((rho * 32 + 31) * 16 + 8 + (lane >> 3)) * 1152 + (cw * 5 + wave) * 128 + (((lane ^ (lane >> 3)) & 7) << 4) - 1024;
    va[18] = ((rho * 32 + 31) * 16 + r) * 1152 + (8 + cw) * 64 + g * 16;
    va[19] = 47104 + wave * 2048 + r * 128 + (((0 + g) ^ (r & 7)) << 4);
    va[20] = 47104 + wave * 2048 + r * 128 + (((4 + g) ^ (r & 7)) << 4);
    va[21] = WS_FL + rho * FR + FO + (lane & 7) * FL;
    va[22] = 0u - 2;
    va[23] = WS_FL + rho * FR + FX1 + cw * FL;
    va[24] = 0u - 6;
    va[25] = lane * 16;
    va[26] = lane * 16;
    va[27] = lane * 8;
    sv[0] = (unsigned)(unsigned long long)(wo + wave * 16 * 1152 + cw * 576);
    sv[1] = (unsigned)((unsigned long long)(wo + wave * 16 * 1152 + cw * 576) >> 32);
    sv[2] = (unsigned)(unsigned long long)(x0);
    sv[3] = (unsigned)((unsigned long long)(x0) >> 32);
    sv[4] = (unsigned)(unsigned long long)(x0);
    sv[5] = (unsigned)((unsigned long long)(x0) >> 32);
    sv[6] = (unsigned)(unsigned long long)(ws + WS_X2);
    sv[7] = (unsigned)((unsigned long long)(ws + WS_X2) >> 32);
    sv[8] = wave * 9216;
    sv[9] = pc0 * 1024 + 2048;
  } else if (__builtin_expect(role == 2, 0)) {
    const unsigned tri = dn_i / 3, nu = dn_i % 3, kap = tri >> 2, rho = tri & 3, row0 = rho * RP + PAD, pg = dn_i % 36;
    auto wv = [&](unsigned j) { const unsigned b = j * 1024 + lane * 16, rw = b / 384; return rw * (1152 - 384) + b + 4096 - j * 1024; };
    va[0] = wv(0);
    va[1] = wv(1);
    va[2] = wv(2);
    va[3] = wv(3);
    va[4] = wv(4);
    va[5] = wv(5);
    va[6] = (8 * g + q) * 384 + wave * 64 + p * 16 + (kap & 1) * 8;
    va[7] = (8 * g + q) * 384 + wave * 64 + p * 16 + 8 - (kap & 1) * 8;
    va[8] = (8 * g + q) * 384 + 256 + wave * 32 + (p ^ (kap & 1)) * 8;
    va[9] = WS_H + row0 * HT + (kap * 24 + wave * 6 + 2) * 1024 + lane * 16;
    va[10] = WS_H + (row0 + 63) * HT + (kap * 24 + wave * 6 + 2) * 1024 + lane * 16;
    va[11] = WS_P + (row0 - 3) * PT + (((nu * 3 + (wave >> 1)) * 4 + q) * 6 + kap) * 512 + (wave & 1) * 256 + g * 64 + p * 16;
    va[12] = WS_P + (row0 - 3) * PT + (((nu * 3 + 2) * 4 + q) * 6 + kap) * 512 + wave * 128 + (g >> 1) * 64 + p * 16;
    va[13] = WS_FL + rho * FR + FH + (kap * 6 + min(lane, 5u)) * FL;
    va[14] = 0u;
    va[15] = WS_FL + rho * FR + FP + (nu * 6 + kap) * FL;
    va[16] = 0u - 4;
    va[17] = lane * 16;
    va[18] = (320 + (wave & 1) * 128 + (lane >> 1)) * 9216 + (lane & 1) * 768;
    sv[0] = (unsigned)(unsigned long long)((wave < 2 ? wg : wu) + pg / 6 * 1536 + (pg % 6) * 128);
    sv[1] = (unsigned)((unsigned long long)((wave < 2 ? wg : wu) + pg / 6 * 1536 + (pg % 6) * 128) >> 32);
    sv[2] = (unsigned)(unsigned long long)(wd + (kap * 768 + wave * 16) * 1152 + nu * 384);
    sv[3] = (unsigned)((unsigned long long)(wd + (kap * 768 + wave * 16) * 1152 + nu * 384) >> 32);
    sv[4] = wave * 6144;
    sv[5] = wave * 6144 + 2048;
  } else {
    const unsigned ii = gu_i, gam = (ii % 72) >> 1, rho = ii / 72 * 2 + (ii & 1), row0 = rho * RP + PAD, pc0 = wave * 6;
    const unsigned bk = gam >> 2, qd = gam & 3, od = (lane >> 3) & 1, un = g * 8 + (lane & 7), jr = qd * 4 + p, jc = (un >> 2) * 8 + od * 4;
    va[0] = g * 9216 + ((r ^ (g << 2)) << 4) + 4096 + (((r ^ (g << 2)) >> 3) & 1) * 640;
    va[1] = (8 * g + q) * 256 + (((4 * wave + p) ^ (q << 2)) << 4);
    va[2] = WS_X1 + row0 * X1P + pc0 * 1024 + 2048 + lane * 16;
    va[3] = WS_H + (row0 - 3) * HT + (gam / 6 * 24 + (gam % 6) * 2 + (wave >> 1) * 12 + (wave & 1)) * 1024 + lane * 16;
    va[4] = WS_FL + rho * FR + FX1 + (lane & 1) * FL;
    va[5] = 0u;
    va[6] = WS_FL + rho * FR + FH + gam * FL;
    va[7] = 0u - 4;
    va[8] = lane * 16;
    va[9] = WS_X1 + (row0 + 63) * X1P + pc0 * 1024 + 2048 + lane * 16;
    va[10] = WS_P + row0 * PT + gam * 3072 + od * 512 + un * 16;
    va[11] = WS_X1 + row0 * X1P + bk * 2048 + (un >> 2) * 256 + jr * 16 + od * 8;
    va[12] = WS_X1 + row0 * X1P + XT + jr * 8;
    va[13] = ((rho * 32 + 31) * 16 + qd * 4 + g) * 1152 + bk * 128 + r * 8;
    va[14] = 49152 + wave * 1024 + p * 144 + jc * 2;
    va[15] = 49152 + wave * 1024 + g * 144 + r * 8;
    va[16] = WS_FL + rho * FR + FP + (bk / 3 * 6 + min(lane, 5u)) * FL;
    va[17] = 0u;
    va[18] = WS_FL + rho * FR + FX2 + gam * FL;
    va[19] = (rho * 144 + (wave & 1) * 40 + (lane >> 1)) * 9216 + (lane & 1) * 128;
    va[20] = 0u;
    va[21] = 0u;
    va[22] = (g == q && p < 2) ? (0x3F80u << (16 * p)) : 0u;
    va[23] = (g == q && p >= 2) ? (0x3F80u << (16 * (p - 2))) : 0u;
    va[24] = 0u;
    va[25] = 0u;
    sv[0] = (unsigned)(unsigned long long)((wave < 2 ? wg : wu) + gam / 6 * 1536 + (gam % 6) * 128 + (wave & 1) * (32 * 9216) + 0 * 64 * 9216);
    sv[1] = (unsigned)((unsigned long long)((wave < 2 ? wg : wu) + gam / 6 * 1536 + (gam % 6) * 128 + (wave & 1) * (32 * 9216) + 0 * 64 * 9216) >> 32);
    sv[2] = wave * 8192;
    sv[3] = pc0 * 1024 + 2048;
    sv[4] = (unsigned)(unsigned long long)(ws + WS_X2);
    sv[5] = (unsigned)((unsigned long long)(ws + WS_X2) >> 32);
    sv[6] = (unsigned)(unsigned long long)(out + 32 * XT);
    sv[7] = (unsigned)((unsigned long long)(out + 32 * XT) >> 32);
    sv[8] = (unsigned)(unsigned long long)(ws + WS_XD);
    sv[9] = (unsigned)((unsigned long long)(ws + WS_XD) >> 32);
    sv[10] = (unsigned)(unsigned long long)(ws - PT);
    sv[11] = (unsigned)((unsigned long long)(ws - PT) >> 32);
    sv[12] = (unsigned)(unsigned long long)(ws - X1P);
    sv[13] = (unsigned)((unsigned long long)(ws - X1P) >> 32);
    sv[14] = (unsigned)(unsigned long long)(ws + WS_X2);
    sv[15] = (unsigned)((unsigned long long)(ws + WS_X2) >> 32);
    sv[16] = (unsigned)(unsigned long long)(ws + WS_XD);
    sv[17] = (unsigned)((unsigned long long)(ws + WS_XD) >> 32);
    sv[18] = (unsigned)(unsigned long long)(ws + WS_XD);
    sv[19] = (unsigned)((unsigned long long)(ws + WS_XD) >> 32);
    sv[20] = 0u;
    sv[21] = rho & 1;
  }
#pragma unroll
  for (int i = 0; i < 28; ++i) { const unsigned u = __builtin_amdgcn_readfirstlane(sv[i]); if (i < 12) sa[i + 4] = u; else sb[i - 12] = u; }

  asm volatile(
R"(s_mov_b32 s71, 0
s_mov_b32 s74, 0
s_load_dword s37, s[38:39], 0x0
s_lshr_b32 s75, s36, 2
s_cmp_eq_u32 s75, 1
s_cbranch_scc1 .Lrole1
s_cmp_eq_u32 s75, 2
s_cbranch_scc1 .Lrole2
s_cmp_eq_u32 s75, 3
s_cbranch_scc1 .Lrole3
.Lrole0:
s_and_b32 s75, s36, 3
s_cmp_eq_u32 s75, 1
s_cbranch_scc1 .Lqa_w2
s_cmp_eq_u32 s75, 2
s_cbranch_scc1 .Lqa_w3
s_cmp_eq_u32 s75, 3
s_cbranch_scc1 .Lqa_w4
.Lqa_w1:
s_add_u32 m0, s48, 4096
s_nop 0
global_load_lds_dwordx4 v0, s[40:41] offset:-4096
global_load_lds_dwordx4 v1, s[40:41] offset:-3072
global_load_lds_dwordx4 v2, s[40:41] offset:-2048
global_load_lds_dwordx4 v3, s[40:41] offset:-1024
global_load_lds_dwordx4 v4, s[40:41] offset:0
global_load_lds_dwordx4 v5, s[40:41] offset:1024
global_load_lds_dwordx4 v6, s[40:41] offset:2048
global_load_lds_dwordx4 v7, s[40:41] offset:3072
s_add_u32 m0, s48, 12288
s_nop 0
global_load_lds_dwordx4 v8, s[40:41] offset:-4096
s_add_u32 s40, s40, 221184
s_addc_u32 s41, s41, 0
s_add_u32 m0, s48, 40960
s_nop 0
global_load_lds_dwordx4 v0, s[40:41] offset:-4096
global_load_lds_dwordx4 v1, s[40:41] offset:-3072
global_load_lds_dwordx4 v2, s[40:41] offset:-2048
global_load_lds_dwordx4 v3, s[40:41] offset:-1024
global_load_lds_dwordx4 v4, s[40:41] offset:0
global_load_lds_dwordx4 v5, s[40:41] offset:1024
global_load_lds_dwordx4 v6, s[40:41] offset:2048
global_load_lds_dwordx4 v7, s[40:41] offset:3072
s_add_u32 m0, s48, 49152
s_nop 0
global_load_lds_dwordx4 v8, s[40:41] offset:-4096
s_add_u32 s40, s40, 221184
s_addc_u32 s41, s41, 0
v_add_u32 v255, 0x10000, v9
v_add_u32 v162, 0x20000, v9
v_add_u32 v163, 0x10000, v10
v_add_u32 v164, 0x20000, v10
v_mov_b32 v106, 0x3F803F80
v_mov_b32 v107, 0x3F803F80
v_mov_b32 v108, 0x3F803F80
v_mov_b32 v109, 0x3F803F80
v_mov_b32 v141, 0
v_mov_b32 v145, 0
v_mov_b32 v149, 0
v_mov_b32 v153, 0
v_mov_b32 v165, 46080
s_waitcnt lgkmcnt(0)
s_mul_i32 s37, s37, 0x9E3779B1
s_add_u32 s37, s37, 0x7F4A7C15
v_add_u32 v46, s37, v46
v_add_u32 v48, s37, v48
v_add_u32 v50, s37, v50
ds_write_b32 v165, v141
s_mov_b64 s[76:77], 0
s_mov_b32 s73, 18
s_waitcnt vmcnt(9) lgkmcnt(0)
s_barrier
s_add_u32 m0, s48, 77824
ds_read_b64_tr_b16 a[0:1], v9 offset:47104
ds_read_b64_tr_b16 a[2:3], v9 offset:49408
global_load_lds_dwordx4 v0, s[40:41] offset:-4096
ds_read_b64_tr_b16 a[4:5], v9 offset:47296
ds_read_b64_tr_b16 a[6:7], v9 offset:49600
global_load_lds_dwordx4 v1, s[40:41] offset:-3072
ds_read_b64_tr_b16 a[8:9], v9 offset:47488
global_load_lds_dwordx4 v2, s[40:41] offset:-2048
ds_read_b64_tr_b16 a[10:11], v9 offset:49792
ds_read_b64_tr_b16 a[216:217], v10 offset:47104
global_load_lds_dwordx4 v3, s[40:41] offset:-1024
ds_read_b64_tr_b16 a[218:219], v10 offset:49408
global_load_lds_dwordx4 v4, s[40:41] offset:0
ds_read_b128 v[166:169], v51 offset:0
ds_read_b128 v[174:177], v51 offset:18432
global_load_lds_dwordx4 v5, s[40:41] offset:1024
ds_read_b64_tr_b16 a[12:13], v255 offset:0
global_load_lds_dwordx4 v6, s[40:41] offset:2048
ds_read_b64_tr_b16 a[14:15], v255 offset:2304
ds_read_b64_tr_b16 a[16:17], v255 offset:192
global_load_lds_dwordx4 v7, s[40:41] offset:3072
ds_read_b64_tr_b16 a[18:19], v255 offset:2496
s_add_u32 m0, s48, 86016
ds_read_b64_tr_b16 a[20:21], v255 offset:384
ds_read_b64_tr_b16 a[22:23], v255 offset:2688
global_load_lds_dwordx4 v8, s[40:41] offset:-4096
ds_read_b64_tr_b16 a[220:221], v163 offset:0
s_add_u32 s40, s40, 221184
ds_read_b64_tr_b16 a[222:223], v163 offset:2304
ds_read_b128 v[170:173], v52 offset:0
s_addc_u32 s41, s41, 0
ds_read_b128 v[178:181], v52 offset:18432
s_waitcnt vmcnt(9) lgkmcnt(0)
s_barrier
s_add_u32 m0, s48, 4096
ds_read_b64_tr_b16 a[24:25], v255 offset:18432
v_mfma_f32_16x16x32_bf16 v[86:89], a[0:3], v[106:109], 0
ds_read_b64_tr_b16 a[26:27], v255 offset:20736
v_mfma_f32_16x16x32_bf16 v[90:93], a[4:7], v[106:109], 0
global_load_lds_dwordx4 v0, s[40:41] offset:-4096
ds_read_b64_tr_b16 a[28:29], v255 offset:18624
v_mfma_f32_16x16x32_bf16 v[94:97], v[106:109], a[8:11], 0
ds_read_b64_tr_b16 a[30:31], v255 offset:20928
global_load_lds_dwordx4 v1, s[40:41] offset:-3072
v_mfma_f32_16x16x32_bf16 v[98:101], a[216:219], v[106:109], 0
ds_read_b64_tr_b16 a[32:33], v255 offset:18816
v_mfma_f32_16x16x32_bf16 v[110:113], a[0:3], v[166:169], 0
ds_read_b64_tr_b16 a[34:35], v255 offset:21120
global_load_lds_dwordx4 v2, s[40:41] offset:-2048
v_mfma_f32_16x16x32_bf16 v[114:117], a[4:7], v[166:169], 0
ds_read_b64_tr_b16 a[224:225], v163 offset:18432
v_mfma_f32_16x16x32_bf16 v[118:121], v[166:169], a[8:11], 0
global_load_lds_dwordx4 v3, s[40:41] offset:-1024
ds_read_b64_tr_b16 a[226:227], v163 offset:20736
v_mfma_f32_16x16x32_bf16 v[134:137], a[216:219], v[166:169], 0
ds_read_b128 v[182:185], v51 offset:2048
global_load_lds_dwordx4 v4, s[40:41] offset:0
v_mfma_f32_16x16x32_bf16 v[122:125], a[0:3], v[174:177], 0
ds_read_b128 v[190:193], v51 offset:20480
v_mfma_f32_16x16x32_bf16 v[126:129], a[4:7], v[174:177], 0
ds_read_b64_tr_b16 a[36:37], v255 offset:36864
global_load_lds_dwordx4 v5, s[40:41] offset:1024
v_mfma_f32_16x16x32_bf16 v[130:133], v[174:177], a[8:11], 0
ds_read_b64_tr_b16 a[38:39], v255 offset:39168
v_mfma_f32_16x16x32_bf16 v[158:161], a[216:219], v[174:177], 0
global_load_lds_dwordx4 v6, s[40:41] offset:2048
ds_read_b64_tr_b16 a[40:41], v255 offset:37056
v_mfma_f32_16x16x32_bf16 v[86:89], a[12:15], v[106:109], v[86:89]
ds_read_b64_tr_b16 a[42:43], v255 offset:39360
v_mfma_f32_16x16x32_bf16 v[90:93], a[16:19], v[106:109], v[90:93]
global_load_lds_dwordx4 v7, s[40:41] offset:3072
ds_read_b64_tr_b16 a[44:45], v255 offset:37248
v_mfma_f32_16x16x32_bf16 v[94:97], v[106:109], a[20:23], v[94:97]
s_add_u32 m0, s48, 12288
ds_read_b64_tr_b16 a[46:47], v255 offset:39552
v_mfma_f32_16x16x32_bf16 v[98:101], a[220:223], v[106:109], v[98:101]
ds_read_b64_tr_b16 a[228:229], v163 offset:36864
v_mfma_f32_16x16x32_bf16 v[110:113], a[12:15], v[170:173], v[110:113]
global_load_lds_dwordx4 v8, s[40:41] offset:-4096
ds_read_b64_tr_b16 a[230:231], v163 offset:39168
v_mfma_f32_16x16x32_bf16 v[114:117], a[16:19], v[170:173], v[114:117]
ds_read_b128 v[186:189], v52 offset:2048
v_mfma_f32_16x16x32_bf16 v[118:121], v[170:173], a[20:23], v[118:121]
s_add_u32 s40, s40, 221184
ds_read_b128 v[194:197], v52 offset:20480
v_mfma_f32_16x16x32_bf16 v[134:137], a[220:223], v[170:173], v[134:137]
v_mfma_f32_16x16x32_bf16 v[122:125], a[12:15], v[178:181], v[122:125]
s_addc_u32 s41, s41, 0
v_mfma_f32_16x16x32_bf16 v[126:129], a[16:19], v[178:181], v[126:129]
v_mfma_f32_16x16x32_bf16 v[130:133], v[178:181], a[20:23], v[130:133]
v_mfma_f32_16x16x32_bf16 v[158:161], a[220:223], v[178:181], v[158:161]
s_waitcnt vmcnt(9) lgkmcnt(0)
s_barrier
s_add_u32 m0, s48, 40960
ds_read_b64_tr_b16 a[48:49], v255 offset:55296
v_mfma_f32_16x16x32_bf16 v[86:89], a[24:27], v[106:109], v[86:89]
ds_read_b64_tr_b16 a[50:51], v255 offset:57600
v_mfma_f32_16x16x32_bf16 v[90:93], a[28:31], v[106:109], v[90:93]
global_load_lds_dwordx4 v0, s[40:41] offset:-4096
ds_read_b64_tr_b16 a[52:53], v255 offset:55488
v_mfma_f32_16x16x32_bf16 v[94:97], v[106:109], a[32:35], v[94:97]
ds_read_b64_tr_b16 a[54:55], v255 offset:57792
global_load_lds_dwordx4 v1, s[40:41] offset:-3072
v_mfma_f32_16x16x32_bf16 v[98:101], a[224:227], v[106:109], v[98:101]
ds_read_b64_tr_b16 a[56:57], v255 offset:55680
v_mfma_f32_16x16x32_bf16 v[110:113], a[24:27], v[182:185], v[110:113]
ds_read_b64_tr_b16 a[58:59], v255 offset:57984
global_load_lds_dwordx4 v2, s[40:41] offset:-2048
v_mfma_f32_16x16x32_bf16 v[114:117], a[28:31], v[182:185], v[114:117]
ds_read_b64_tr_b16 a[232:233], v163 offset:55296
v_mfma_f32_16x16x32_bf16 v[118:121], v[182:185], a[32:35], v[118:121]
global_load_lds_dwordx4 v3, s[40:41] offset:-1024
ds_read_b64_tr_b16 a[234:235], v163 offset:57600
v_mfma_f32_16x16x32_bf16 v[134:137], a[224:227], v[182:185], v[134:137]
ds_read_b128 v[166:169], v51 offset:4096
global_load_lds_dwordx4 v4, s[40:41] offset:0
v_mfma_f32_16x16x32_bf16 v[122:125], a[24:27], v[190:193], v[122:125]
ds_read_b128 v[174:177], v51 offset:22528
v_mfma_f32_16x16x32_bf16 v[126:129], a[28:31], v[190:193], v[126:129]
ds_read_b64_tr_b16 a[60:61], v162 offset:8192
global_load_lds_dwordx4 v5, s[40:41] offset:1024
v_mfma_f32_16x16x32_bf16 v[130:133], v[190:193], a[32:35], v[130:133]
ds_read_b64_tr_b16 a[62:63], v162 offset:10496
v_mfma_f32_16x16x32_bf16 v[158:161], a[224:227], v[190:193], v[158:161]
global_load_lds_dwordx4 v6, s[40:41] offset:2048
ds_read_b64_tr_b16 a[64:65], v162 offset:8384
v_mfma_f32_16x16x32_bf16 v[86:89], a[36:39], v[106:109], v[86:89]
ds_read_b64_tr_b16 a[66:67], v162 offset:10688
v_mfma_f32_16x16x32_bf16 v[90:93], a[40:43], v[106:109], v[90:93]
global_load_lds_dwordx4 v7, s[40:41] offset:3072
ds_read_b64_tr_b16 a[68:69], v162 offset:8576
v_mfma_f32_16x16x32_bf16 v[94:97], v[106:109], a[44:47], v[94:97]
s_add_u32 m0, s48, 49152
ds_read_b64_tr_b16 a[70:71], v162 offset:10880
v_mfma_f32_16x16x32_bf16 v[98:101], a[228:231], v[106:109], v[98:101]
ds_read_b64_tr_b16 a[236:237], v164 offset:8192
v_mfma_f32_16x16x32_bf16 v[110:113], a[36:39], v[186:189], v[110:113]
global_load_lds_dwordx4 v8, s[40:41] offset:-4096
ds_read_b64_tr_b16 a[238:239], v164 offset:10496
v_mfma_f32_16x16x32_bf16 v[114:117], a[40:43], v[186:189], v[114:117]
ds_read_b128 v[170:173], v52 offset:4096
v_mfma_f32_16x16x32_bf16 v[118:121], v[186:189], a[44:47], v[118:121]
s_add_u32 s40, s40, 221184
ds_read_b128 v[178:181], v52 offset:22528
v_mfma_f32_16x16x32_bf16 v[134:137], a[228:231], v[186:189], v[134:137]
v_mfma_f32_16x16x32_bf16 v[122:125], a[36:39], v[194:197], v[122:125]
s_addc_u32 s41, s41, 0
v_mfma_f32_16x16x32_bf16 v[126:129], a[40:43], v[194:197], v[126:129]
v_mfma_f32_16x16x32_bf16 v[130:133], v[194:197], a[44:47], v[130:133]
v_mfma_f32_16x16x32_bf16 v[158:161], a[228:231], v[194:197], v[158:161]
s_waitcnt vmcnt(9) lgkmcnt(0)
s_barrier
s_add_u32 m0, s48, 77824
ds_read_b64_tr_b16 a[72:73], v9 offset:47104
v_mfma_f32_16x16x32_bf16 v[86:89], a[48:51], v[106:109], v[86:89]
ds_read_b64_tr_b16 a[74:75], v9 offset:49408
v_mfma_f32_16x16x32_bf16 v[90:93], a[52:55], v[106:109], v[90:93]
global_load_lds_dwordx4 v0, s[40:41] offset:-4096
ds_read_b64_tr_b16 a[76:77], v9 offset:47296
v_mfma_f32_16x16x32_bf16 v[94:97], v[106:109], a[56:59], v[94:97]
ds_read_b64_tr_b16 a[78:79], v9 offset:49600
global_load_lds_dwordx4 v1, s[40:41] offset:-3072
v_mfma_f32_16x16x32_bf16 v[98:101], a[232:235], v[106:109], v[98:101]
ds_read_b64_tr_b16 a[80:81], v9 offset:47488
v_mfma_f32_16x16x32_bf16 v[110:113], a[48:51], v[166:169], v[110:113]
ds_read_b64_tr_b16 a[82:83], v9 offset:49792
global_load_lds_dwordx4 v2, s[40:41] offset:-2048
v_mfma_f32_16x16x32_bf16 v[114:117], a[52:55], v[166:169], v[114:117]
ds_read_b64_tr_b16 a[240:241], v10 offset:47104
v_mfma_f32_16x16x32_bf16 v[118:121], v[166:169], a[56:59], v[118:121]
global_load_lds_dwordx4 v3, s[40:41] offset:-1024
ds_read_b64_tr_b16 a[242:243], v10 offset:49408
v_mfma_f32_16x16x32_bf16 v[134:137], a[232:235], v[166:169], v[134:137]
ds_read_b128 v[182:185], v51 offset:6144
global_load_lds_dwordx4 v4, s[40:41] offset:0
v_mfma_f32_16x16x32_bf16 v[122:125], a[48:51], v[174:177], v[122:125]
ds_read_b128 v[190:193], v51 offset:24576
v_mfma_f32_16x16x32_bf16 v[126:129], a[52:55], v[174:177], v[126:129]
ds_read_b64_tr_b16 a[84:85], v255 offset:0
global_load_lds_dwordx4 v5, s[40:41] offset:1024
v_mfma_f32_16x16x32_bf16 v[130:133], v[174:177], a[56:59], v[130:133]
ds_read_b64_tr_b16 a[86:87], v255 offset:2304
v_mfma_f32_16x16x32_bf16 v[158:161], a[232:235], v[174:177], v[158:161]
global_load_lds_dwordx4 v6, s[40:41] offset:2048
ds_read_b64_tr_b16 a[88:89], v255 offset:192
v_mfma_f32_16x16x32_bf16 v[86:89], a[60:63], v[106:109], v[86:89]
ds_read_b64_tr_b16 a[90:91], v255 offset:2496
v_mfma_f32_16x16x32_bf16 v[90:93], a[64:67], v[106:109], v[90:93]
global_load_lds_dwordx4 v7, s[40:41] offset:3072
ds_read_b64_tr_b16 a[92:93], v255 offset:384
v_mfma_f32_16x16x32_bf16 v[94:97], v[106:109], a[68:71], v[94:97]
s_add_u32 m0, s48, 86016
ds_read_b64_tr_b16 a[94:95], v255 offset:2688
v_mfma_f32_16x16x32_bf16 v[98:101], a[236:239], v[106:109], v[98:101]
ds_read_b64_tr_b16 a[244:245], v163 offset:0
v_mfma_f32_16x16x32_bf16 v[110:113], a[60:63], v[170:173], v[110:113]
global_load_lds_dwordx4 v8, s[40:41] offset:-4096
ds_read_b64_tr_b16 a[246:247], v163 offset:2304
v_mfma_f32_16x16x32_bf16 v[114:117], a[64:67], v[170:173], v[114:117]
ds_read_b128 v[186:189], v52 offset:6144
v_mfma_f32_16x16x32_bf16 v[118:121], v[170:173], a[68:71], v[118:121]
s_add_u32 s40, s40, 221184
ds_read_b128 v[194:197], v52 offset:24576
v_mfma_f32_16x16x32_bf16 v[134:137], a[236:239], v[170:173], v[134:137]
v_mfma_f32_16x16x32_bf16 v[122:125], a[60:63], v[178:181], v[122:125]
s_addc_u32 s41, s41, 0
v_mfma_f32_16x16x32_bf16 v[126:129], a[64:67], v[178:181], v[126:129]
v_mfma_f32_16x16x32_bf16 v[130:133], v[178:181], a[68:71], v[130:133]
v_mfma_f32_16x16x32_bf16 v[158:161], a[236:239], v[178:181], v[158:161]
s_waitcnt vmcnt(9) lgkmcnt(0)
s_barrier
s_add_u32 m0, s48, 4096
ds_read_b64_tr_b16 a[96:97], v255 offset:18432
v_mfma_f32_16x16x32_bf16 v[86:89], a[72:75], v[106:109], v[86:89]
ds_read_b64_tr_b16 a[98:99], v255 offset:20736
v_mfma_f32_16x16x32_bf16 v[90:93], a[76:79], v[106:109], v[90:93]
global_load_lds_dwordx4 v0, s[40:41] offset:-4096
ds_read_b64_tr_b16 a[100:101], v255 offset:18624
v_mfma_f32_16x16x32_bf16 v[94:97], v[106:109], a[80:83], v[94:97]
ds_read_b64_tr_b16 a[102:103], v255 offset:20928
global_load_lds_dwordx4 v1, s[40:41] offset:-3072
v_mfma_f32_16x16x32_bf16 v[98:101], a[240:243], v[106:109], v[98:101]
ds_read_b64_tr_b16 a[104:105], v255 offset:18816
v_mfma_f32_16x16x32_bf16 v[110:113], a[72:75], v[182:185], v[110:113]
ds_read_b64_tr_b16 a[106:107], v255 offset:21120
global_load_lds_dwordx4 v2, s[40:41] offset:-2048
v_mfma_f32_16x16x32_bf16 v[114:117], a[76:79], v[182:185], v[114:117]
ds_read_b64_tr_b16 a[248:249], v163 offset:18432
v_mfma_f32_16x16x32_bf16 v[118:121], v[182:185], a[80:83], v[118:121]
global_load_lds_dwordx4 v3, s[40:41] offset:-1024
ds_read_b64_tr_b16 a[250:251], v163 offset:20736
v_mfma_f32_16x16x32_bf16 v[134:137], a[240:243], v[182:185], v[134:137]
ds_read_b128 v[166:169], v51 offset:8192
global_load_lds_dwordx4 v4, s[40:41] offset:0
v_mfma_f32_16x16x32_bf16 v[122:125], a[72:75], v[190:193], v[122:125]
ds_read_b128 v[174:177], v51 offset:26624
v_mfma_f32_16x16x32_bf16 v[126:129], a[76:79], v[190:193], v[126:129]
ds_read_b64_tr_b16 a[108:109], v255 offset:36864
global_load_lds_dwordx4 v5, s[40:41] offset:1024
v_mfma_f32_16x16x32_bf16 v[130:133], v[190:193], a[80:83], v[130:133]
ds_read_b64_tr_b16 a[110:111], v255 offset:39168
v_mfma_f32_16x16x32_bf16 v[158:161], a[240:243], v[190:193], v[158:161]
global_load_lds_dwordx4 v6, s[40:41] offset:2048
ds_read_b64_tr_b16 a[112:113], v255 offset:37056
v_mfma_f32_16x16x32_bf16 v[86:89], a[84:87], v[106:109], v[86:89]
ds_read_b64_tr_b16 a[114:115], v255 offset:39360
v_mfma_f32_16x16x32_bf16 v[90:93], a[88:91], v[106:109], v[90:93]
global_load_lds_dwordx4 v7, s[40:41] offset:3072
ds_read_b64_tr_b16 a[116:117], v255 offset:37248
v_mfma_f32_16x16x32_bf16 v[94:97], v[106:109], a[92:95], v[94:97]
s_add_u32 m0, s48, 12288
ds_read_b64_tr_b16 a[118:119], v255 offset:39552
v_mfma_f32_16x16x32_bf16 v[98:101], a[244:247], v[106:109], v[98:101]
ds_read_b64_tr_b16 a[252:253], v163 offset:36864
v_mfma_f32_16x16x32_bf16 v[110:113], a[84:87], v[186:189], v[110:113]
global_load_lds_dwordx4 v8, s[40:41] offset:-4096
ds_read_b64_tr_b16 a[254:255], v163 offset:39168
v_mfma_f32_16x16x32_bf16 v[114:117], a[88:91], v[186:189], v[114:117]
ds_read_b128 v[170:173], v52 offset:8192
v_mfma_f32_16x16x32_bf16 v[118:121], v[186:189], a[92:95], v[118:121]
s_add_u32 s40, s40, 221184
ds_read_b128 v[178:181], v52 offset:26624
v_mfma_f32_16x16x32_bf16 v[134:137], a[244:247], v[186:189], v[134:137]
v_mfma_f32_16x16x32_bf16 v[122:125], a[84:87], v[194:197], v[122:125]
s_addc_u32 s41, s41, 0
v_mfma_f32_16x16x32_bf16 v[126:129], a[88:91], v[194:197], v[126:129]
v_mfma_f32_16x16x32_bf16 v[130:133], v[194:197], a[92:95], v[130:133]
v_mfma_f32_16x16x32_bf16 v[158:161], a[244:247], v[194:197], v[158:161]
s_waitcnt vmcnt(9) lgkmcnt(0)
s_barrier
s_add_u32 m0, s48, 40960
ds_read_b64_tr_b16 a[120:121], v255 offset:55296
v_mfma_f32_16x16x32_bf16 v[86:89], a[96:99], v[106:109], v[86:89]
ds_read_b64_tr_b16 a[122:123], v255 offset:57600
v_mfma_f32_16x16x32_bf16 v[90:93], a[100:103], v[106:109], v[90:93]
global_load_lds_dwordx4 v0, s[40:41] offset:-4096
ds_read_b64_tr_b16 a[124:125], v255 offset:55488
v_mfma_f32_16x16x32_bf16 v[94:97], v[106:109], a[104:107], v[94:97]
ds_read_b64_tr_b16 a[126:127], v255 offset:57792
global_load_lds_dwordx4 v1, s[40:41] offset:-3072
v_mfma_f32_16x16x32_bf16 v[98:101], a[248:251], v[106:109], v[98:101]
ds_read_b64_tr_b16 a[128:129], v255 offset:55680
v_mfma_f32_16x16x32_bf16 v[110:113], a[96:99], v[166:169], v[110:113]
ds_read_b64_tr_b16 a[130:131], v255 offset:57984
global_load_lds_dwordx4 v2, s[40:41] offset:-2048
v_mfma_f32_16x16x32_bf16 v[114:117], a[100:103], v[166:169], v[114:117]
ds_read_b64_tr_b16 v[54:55], v163 offset:55296
v_mfma_f32_16x16x32_bf16 v[118:121], v[166:169], a[104:107], v[118:121]
global_load_lds_dwordx4 v3, s[40:41] offset:-1024
ds_read_b64_tr_b16 v[56:57], v163 offset:57600
v_mfma_f32_16x16x32_bf16 v[134:137], a[248:251], v[166:169], v[134:137]
ds_read_b128 v[182:185], v51 offset:10240
global_load_lds_dwordx4 v4, s[40:41] offset:0
v_mfma_f32_16x16x32_bf16 v[122:125], a[96:99], v[174:177], v[122:125]
ds_read_b128 v[190:193], v51 offset:28672
v_mfma_f32_16x16x32_bf16 v[126:129], a[100:103], v[174:177], v[126:129]
ds_read_b64_tr_b16 a[132:133], v162 offset:8192
global_load_lds_dwordx4 v5, s[40:41] offset:1024
v_mfma_f32_16x16x32_bf16 v[130:133], v[174:177], a[104:107], v[130:133]
ds_read_b64_tr_b16 a[134:135], v162 offset:10496
v_mfma_f32_16x16x32_bf16 v[158:161], a[248:251], v[174:177], v[158:161]
global_load_lds_dwordx4 v6, s[40:41] offset:2048
ds_read_b64_tr_b16 a[136:137], v162 offset:8384
v_mfma_f32_16x16x32_bf16 v[86:89], a[108:111], v[106:109], v[86:89]
ds_read_b64_tr_b16 a[138:139], v162 offset:10688
v_mfma_f32_16x16x32_bf16 v[90:93], a[112:115], v[106:109], v[90:93]
global_load_lds_dwordx4 v7, s[40:41] offset:3072
ds_read_b64_tr_b16 a[140:141], v162 offset:8576
v_mfma_f32_16x16x32_bf16 v[94:97], v[106:109], a[116:119], v[94:97]
s_add_u32 m0, s48, 49152
ds_read_b64_tr_b16 a[142:143], v162 offset:10880
v_mfma_f32_16x16x32_bf16 v[98:101], a[252:255], v[106:109], v[98:101]
ds_read_b64_tr_b16 v[58:59], v164 offset:8192
v_mfma_f32_16x16x32_bf16 v[110:113], a[108:111], v[170:173], v[110:113]
global_load_lds_dwordx4 v8, s[40:41] offset:-4096
ds_read_b64_tr_b16 v[60:61], v164 offset:10496
v_mfma_f32_16x16x32_bf16 v[114:117], a[112:115], v[170:173], v[114:117]
ds_read_b128 v[186:189], v52 offset:10240
v_mfma_f32_16x16x32_bf16 v[118:121], v[170:173], a[116:119], v[118:121]
s_add_u32 s40, s40, 221184
ds_read_b128 v[194:197], v52 offset:28672
v_mfma_f32_16x16x32_bf16 v[134:137], a[252:255], v[170:173], v[134:137]
v_mfma_f32_16x16x32_bf16 v[122:125], a[108:111], v[178:181], v[122:125]
s_addc_u32 s41, s41, 0
v_mfma_f32_16x16x32_bf16 v[126:129], a[112:115], v[178:181], v[126:129]
v_mfma_f32_16x16x32_bf16 v[130:133], v[178:181], a[116:119], v[130:133]
)"
R"(v_mfma_f32_16x16x32_bf16 v[158:161], a[252:255], v[178:181], v[158:161]
s_waitcnt vmcnt(9) lgkmcnt(0)
s_barrier
s_add_u32 m0, s48, 77824
ds_read_b64_tr_b16 a[144:145], v9 offset:47104
v_mfma_f32_16x16x32_bf16 v[86:89], a[120:123], v[106:109], v[86:89]
ds_read_b64_tr_b16 a[146:147], v9 offset:49408
v_mfma_f32_16x16x32_bf16 v[90:93], a[124:127], v[106:109], v[90:93]
global_load_lds_dwordx4 v0, s[40:41] offset:-4096
ds_read_b64_tr_b16 a[148:149], v9 offset:47296
v_mfma_f32_16x16x32_bf16 v[94:97], v[106:109], a[128:131], v[94:97]
ds_read_b64_tr_b16 a[150:151], v9 offset:49600
global_load_lds_dwordx4 v1, s[40:41] offset:-3072
v_mfma_f32_16x16x32_bf16 v[98:101], v[54:57], v[106:109], v[98:101]
ds_read_b64_tr_b16 a[152:153], v9 offset:47488
v_mfma_f32_16x16x32_bf16 v[110:113], a[120:123], v[182:185], v[110:113]
ds_read_b64_tr_b16 a[154:155], v9 offset:49792
global_load_lds_dwordx4 v2, s[40:41] offset:-2048
v_mfma_f32_16x16x32_bf16 v[114:117], a[124:127], v[182:185], v[114:117]
ds_read_b64_tr_b16 v[62:63], v10 offset:47104
v_mfma_f32_16x16x32_bf16 v[118:121], v[182:185], a[128:131], v[118:121]
global_load_lds_dwordx4 v3, s[40:41] offset:-1024
ds_read_b64_tr_b16 v[64:65], v10 offset:49408
v_mfma_f32_16x16x32_bf16 v[134:137], v[54:57], v[182:185], v[134:137]
ds_read_b128 v[166:169], v51 offset:12288
global_load_lds_dwordx4 v4, s[40:41] offset:0
v_mfma_f32_16x16x32_bf16 v[122:125], a[120:123], v[190:193], v[122:125]
ds_read_b128 v[174:177], v51 offset:30720
v_mfma_f32_16x16x32_bf16 v[126:129], a[124:127], v[190:193], v[126:129]
ds_read_b64_tr_b16 a[156:157], v255 offset:0
global_load_lds_dwordx4 v5, s[40:41] offset:1024
v_mfma_f32_16x16x32_bf16 v[130:133], v[190:193], a[128:131], v[130:133]
ds_read_b64_tr_b16 a[158:159], v255 offset:2304
v_mfma_f32_16x16x32_bf16 v[158:161], v[54:57], v[190:193], v[158:161]
global_load_lds_dwordx4 v6, s[40:41] offset:2048
ds_read_b64_tr_b16 a[160:161], v255 offset:192
v_mfma_f32_16x16x32_bf16 v[86:89], a[132:135], v[106:109], v[86:89]
ds_read_b64_tr_b16 a[162:163], v255 offset:2496
v_mfma_f32_16x16x32_bf16 v[90:93], a[136:139], v[106:109], v[90:93]
global_load_lds_dwordx4 v7, s[40:41] offset:3072
ds_read_b64_tr_b16 a[164:165], v255 offset:384
v_mfma_f32_16x16x32_bf16 v[94:97], v[106:109], a[140:143], v[94:97]
s_add_u32 m0, s48, 86016
ds_read_b64_tr_b16 a[166:167], v255 offset:2688
v_mfma_f32_16x16x32_bf16 v[98:101], v[58:61], v[106:109], v[98:101]
ds_read_b64_tr_b16 v[66:67], v163 offset:0
v_mfma_f32_16x16x32_bf16 v[110:113], a[132:135], v[186:189], v[110:113]
global_load_lds_dwordx4 v8, s[40:41] offset:-4096
ds_read_b64_tr_b16 v[68:69], v163 offset:2304
v_mfma_f32_16x16x32_bf16 v[114:117], a[136:139], v[186:189], v[114:117]
ds_read_b128 v[170:173], v52 offset:12288
v_mfma_f32_16x16x32_bf16 v[118:121], v[186:189], a[140:143], v[118:121]
s_sub_u32 s40, s40, 1769472
ds_read_b128 v[178:181], v52 offset:30720
v_mfma_f32_16x16x32_bf16 v[134:137], v[58:61], v[186:189], v[134:137]
v_mfma_f32_16x16x32_bf16 v[122:125], a[132:135], v[194:197], v[122:125]
s_subb_u32 s41, s41, 0
v_mfma_f32_16x16x32_bf16 v[126:129], a[136:139], v[194:197], v[126:129]
v_mfma_f32_16x16x32_bf16 v[130:133], v[194:197], a[140:143], v[130:133]
v_mfma_f32_16x16x32_bf16 v[158:161], v[58:61], v[194:197], v[158:161]
s_waitcnt vmcnt(9) lgkmcnt(0)
s_barrier
s_add_u32 m0, s49, 0
ds_read_b64_tr_b16 a[168:169], v255 offset:18432
v_mfma_f32_16x16x32_bf16 v[86:89], a[144:147], v[106:109], v[86:89]
ds_read_b64_tr_b16 a[170:171], v255 offset:20736
v_mfma_f32_16x16x32_bf16 v[90:93], a[148:151], v[106:109], v[90:93]
ds_read_b64_tr_b16 a[172:173], v255 offset:18624
v_mfma_f32_16x16x32_bf16 v[94:97], v[106:109], a[152:155], v[94:97]
ds_read_b64_tr_b16 a[174:175], v255 offset:20928
global_load_lds_dwordx4 v11, s[42:43] offset:-2048 sc1
v_mfma_f32_16x16x32_bf16 v[98:101], v[62:65], v[106:109], v[98:101]
ds_read_b64_tr_b16 a[176:177], v255 offset:18816
v_mfma_f32_16x16x32_bf16 v[110:113], a[144:147], v[166:169], v[110:113]
ds_read_b64_tr_b16 a[178:179], v255 offset:21120
v_mfma_f32_16x16x32_bf16 v[114:117], a[148:151], v[166:169], v[114:117]
ds_read_b64_tr_b16 v[70:71], v163 offset:18432
global_load_lds_dwordx4 v12, s[42:43] offset:-1024 sc1
v_mfma_f32_16x16x32_bf16 v[118:121], v[166:169], a[152:155], v[118:121]
ds_read_b64_tr_b16 v[72:73], v163 offset:20736
v_mfma_f32_16x16x32_bf16 v[134:137], v[62:65], v[166:169], v[134:137]
ds_read_b128 v[182:185], v51 offset:14336
v_mfma_f32_16x16x32_bf16 v[122:125], a[144:147], v[174:177], v[122:125]
ds_read_b128 v[190:193], v51 offset:32768
global_load_lds_dwordx4 v13, s[42:43] offset:0 sc1
v_mfma_f32_16x16x32_bf16 v[126:129], a[148:151], v[174:177], v[126:129]
ds_read_b64_tr_b16 a[180:181], v255 offset:36864
v_mfma_f32_16x16x32_bf16 v[130:133], v[174:177], a[152:155], v[130:133]
ds_read_b64_tr_b16 a[182:183], v255 offset:39168
v_mfma_f32_16x16x32_bf16 v[158:161], v[62:65], v[174:177], v[158:161]
ds_read_b64_tr_b16 a[184:185], v255 offset:37056
v_mfma_f32_16x16x32_bf16 v[86:89], a[156:159], v[106:109], v[86:89]
global_load_lds_dwordx4 v14, s[42:43] offset:1024 sc1
ds_read_b64_tr_b16 a[186:187], v255 offset:39360
v_mfma_f32_16x16x32_bf16 v[90:93], a[160:163], v[106:109], v[90:93]
ds_read_b64_tr_b16 a[188:189], v255 offset:37248
v_mfma_f32_16x16x32_bf16 v[94:97], v[106:109], a[164:167], v[94:97]
ds_read_b64_tr_b16 a[190:191], v255 offset:39552
v_mfma_f32_16x16x32_bf16 v[98:101], v[66:69], v[106:109], v[98:101]
global_load_lds_dwordx4 v15, s[42:43] offset:2048 sc1
ds_read_b64_tr_b16 v[74:75], v163 offset:36864
v_mfma_f32_16x16x32_bf16 v[110:113], a[156:159], v[170:173], v[110:113]
ds_read_b64_tr_b16 v[76:77], v163 offset:39168
v_mfma_f32_16x16x32_bf16 v[114:117], a[160:163], v[170:173], v[114:117]
ds_read_b128 v[186:189], v52 offset:14336
v_mfma_f32_16x16x32_bf16 v[118:121], v[170:173], a[164:167], v[118:121]
s_sub_u32 s42, s42, 18432
s_subb_u32 s43, s43, 0
ds_read_b128 v[194:197], v52 offset:32768
v_mfma_f32_16x16x32_bf16 v[134:137], v[66:69], v[170:173], v[134:137]
v_mfma_f32_16x16x32_bf16 v[122:125], a[156:159], v[178:181], v[122:125]
v_mfma_f32_16x16x32_bf16 v[126:129], a[160:163], v[178:181], v[126:129]
v_mfma_f32_16x16x32_bf16 v[130:133], v[178:181], a[164:167], v[130:133]
v_mfma_f32_16x16x32_bf16 v[158:161], v[66:69], v[178:181], v[158:161]
s_waitcnt vmcnt(5) lgkmcnt(0)
s_barrier
s_add_u32 m0, s49, 18432
ds_read_b64_tr_b16 a[192:193], v255 offset:55296
v_mfma_f32_16x16x32_bf16 v[86:89], a[168:171], v[106:109], v[86:89]
ds_read_b64_tr_b16 a[194:195], v255 offset:57600
v_mfma_f32_16x16x32_bf16 v[90:93], a[172:175], v[106:109], v[90:93]
ds_read_b64_tr_b16 a[196:197], v255 offset:55488
v_mfma_f32_16x16x32_bf16 v[94:97], v[106:109], a[176:179], v[94:97]
ds_read_b64_tr_b16 a[198:199], v255 offset:57792
global_load_lds_dwordx4 v11, s[42:43] offset:-2048 sc1
v_mfma_f32_16x16x32_bf16 v[98:101], v[70:73], v[106:109], v[98:101]
ds_read_b64_tr_b16 a[200:201], v255 offset:55680
v_mfma_f32_16x16x32_bf16 v[110:113], a[168:171], v[182:185], v[110:113]
ds_read_b64_tr_b16 a[202:203], v255 offset:57984
v_mfma_f32_16x16x32_bf16 v[114:117], a[172:175], v[182:185], v[114:117]
ds_read_b64_tr_b16 v[78:79], v163 offset:55296
global_load_lds_dwordx4 v12, s[42:43] offset:-1024 sc1
v_mfma_f32_16x16x32_bf16 v[118:121], v[182:185], a[176:179], v[118:121]
ds_read_b64_tr_b16 v[80:81], v163 offset:57600
v_mfma_f32_16x16x32_bf16 v[134:137], v[70:73], v[182:185], v[134:137]
ds_read_b128 v[166:169], v51 offset:16384
v_mfma_f32_16x16x32_bf16 v[122:125], a[168:171], v[190:193], v[122:125]
ds_read_b128 v[174:177], v51 offset:34816
global_load_lds_dwordx4 v13, s[42:43] offset:0 sc1
v_mfma_f32_16x16x32_bf16 v[126:129], a[172:175], v[190:193], v[126:129]
ds_read_b64_tr_b16 a[204:205], v162 offset:8192
v_mfma_f32_16x16x32_bf16 v[130:133], v[190:193], a[176:179], v[130:133]
ds_read_b64_tr_b16 a[206:207], v162 offset:10496
v_mfma_f32_16x16x32_bf16 v[158:161], v[70:73], v[190:193], v[158:161]
ds_read_b64_tr_b16 a[208:209], v162 offset:8384
global_load_lds_dwordx4 v14, s[42:43] offset:1024 sc1
v_mfma_f32_16x16x32_bf16 v[86:89], a[180:183], v[106:109], v[86:89]
ds_read_b64_tr_b16 a[210:211], v162 offset:10688
v_mfma_f32_16x16x32_bf16 v[90:93], a[184:187], v[106:109], v[90:93]
ds_read_b64_tr_b16 a[212:213], v162 offset:8576
v_mfma_f32_16x16x32_bf16 v[94:97], v[106:109], a[188:191], v[94:97]
ds_read_b64_tr_b16 a[214:215], v162 offset:10880
global_load_lds_dwordx4 v15, s[42:43] offset:2048 sc1
v_mfma_f32_16x16x32_bf16 v[98:101], v[74:77], v[106:109], v[98:101]
ds_read_b64_tr_b16 v[82:83], v164 offset:8192
v_mfma_f32_16x16x32_bf16 v[110:113], a[180:183], v[186:189], v[110:113]
ds_read_b64_tr_b16 v[84:85], v164 offset:10496
v_mfma_f32_16x16x32_bf16 v[114:117], a[184:187], v[186:189], v[114:117]
ds_read_b128 v[170:173], v52 offset:16384
s_sub_u32 s42, s42, 18432
s_subb_u32 s43, s43, 0
v_mfma_f32_16x16x32_bf16 v[118:121], v[186:189], a[188:191], v[118:121]
ds_read_b128 v[178:181], v52 offset:34816
v_mfma_f32_16x16x32_bf16 v[134:137], v[74:77], v[186:189], v[134:137]
v_mfma_f32_16x16x32_bf16 v[122:125], a[180:183], v[194:197], v[122:125]
v_mfma_f32_16x16x32_bf16 v[126:129], a[184:187], v[194:197], v[126:129]
v_mfma_f32_16x16x32_bf16 v[158:161], v[74:77], v[194:197], v[158:161]
s_waitcnt lgkmcnt(15)
v_mfma_f32_16x16x32_bf16 v[86:89], a[192:195], v[106:109], v[86:89]
v_mfma_f32_16x16x32_bf16 v[90:93], a[196:199], v[106:109], v[90:93]
s_waitcnt lgkmcnt(14)
v_mfma_f32_16x16x32_bf16 v[94:97], v[106:109], a[200:203], v[94:97]
s_waitcnt lgkmcnt(12)
v_mfma_f32_16x16x32_bf16 v[98:101], v[78:81], v[106:109], v[98:101]
s_waitcnt lgkmcnt(11)
v_mfma_f32_16x16x32_bf16 v[110:113], a[192:195], v[166:169], v[110:113]
v_mfma_f32_16x16x32_bf16 v[114:117], a[196:199], v[166:169], v[114:117]
v_mfma_f32_16x16x32_bf16 v[118:121], v[166:169], a[200:203], v[118:121]
v_mfma_f32_16x16x32_bf16 v[134:137], v[78:81], v[166:169], v[134:137]
s_waitcnt lgkmcnt(10)
v_mfma_f32_16x16x32_bf16 v[158:161], v[78:81], v[174:177], v[158:161]
s_waitcnt lgkmcnt(8)
v_mfma_f32_16x16x32_bf16 v[86:89], a[204:207], v[106:109], v[86:89]
s_waitcnt lgkmcnt(6)
v_mfma_f32_16x16x32_bf16 v[90:93], a[208:211], v[106:109], v[90:93]
s_waitcnt lgkmcnt(4)
v_mfma_f32_16x16x32_bf16 v[94:97], v[106:109], a[212:215], v[94:97]
s_waitcnt lgkmcnt(2)
v_mfma_f32_16x16x32_bf16 v[98:101], v[82:85], v[106:109], v[98:101]
s_waitcnt lgkmcnt(1)
v_mfma_f32_16x16x32_bf16 v[110:113], a[204:207], v[170:173], v[110:113]
v_mfma_f32_16x16x32_bf16 v[114:117], a[208:211], v[170:173], v[114:117]
v_mfma_f32_16x16x32_bf16 v[118:121], v[170:173], a[212:215], v[118:121]
v_mfma_f32_16x16x32_bf16 v[134:137], v[82:85], v[170:173], v[134:137]
s_waitcnt lgkmcnt(0)
v_mfma_f32_16x16x32_bf16 v[158:161], v[82:85], v[178:181], v[158:161]
s_barrier
ds_read_b32 v242, v24 offset:0
s_waitcnt lgkmcnt(0)
s_nop 2
v_fma_f32 v230, v98, v242, v134
v_fma_f32 v231, v99, v242, v135
v_fma_f32 v232, v100, v242, v136
v_fma_f32 v233, v101, v242, v137
v_cvt_pk_bf16_f32 v230, v230, v231
v_cvt_pk_bf16_f32 v231, v232, v233
ds_write_b64 v22, v[230:231] offset:40960
ds_read_b128 v[198:201], v51 offset:32768
ds_read_b128 v[202:205], v52 offset:32768
ds_read_b128 v[206:209], v51 offset:34816
ds_read_b128 v[210:213], v52 offset:34816
v_mov_b32 v234, v32
v_mov_b32 v235, v33
v_mov_b32 v236, v34
v_mov_b32 v237, v35
v_mov_b32 v134, v158
v_mov_b32 v135, v159
v_mov_b32 v136, v160
v_mov_b32 v137, v161
s_waitcnt lgkmcnt(4)
s_barrier
ds_read_b32 v23, v24 offset:0
ds_read_b32 v148, v21
ds_read_b32 v26, v24 offset:192
ds_read_b32 v53, v27
ds_read_b32 v8, v28
ds_read_b128 v[226:229], v25 offset:0
ds_read_b64 v[158:159], v22 offset:41472
ds_read_b32 v140, v20
v_xor_b32 v51, 0x10000, v51
v_xor_b32 v52, 0x10000, v52
s_waitcnt lgkmcnt(7)
v_fma_f32 v238, v90, v23, v114
v_fma_f32 v239, v91, v23, v115
v_fma_f32 v240, v92, v23, v116
v_fma_f32 v241, v93, v23, v117
v_cvt_pk_bf16_f32 v146, v238, v239
v_cvt_pk_bf16_f32 v147, v240, v241
s_waitcnt lgkmcnt(0)
s_barrier
ds_read_b32 v242, v24 offset:64
v_fma_f32 v238, v94, v226, v118
v_fma_f32 v239, v94, v227, v119
v_fma_f32 v240, v94, v228, v120
v_fma_f32 v241, v94, v229, v121
v_cvt_pk_bf16_f32 v154, v238, v239
v_cvt_pk_bf16_f32 v155, v240, v241
v_fma_f32 v238, v86, v23, v110
v_fma_f32 v239, v87, v23, v111
v_fma_f32 v240, v88, v23, v112
v_fma_f32 v241, v89, v23, v113
v_cvt_pk_bf16_f32 v138, v238, v239
v_cvt_pk_bf16_f32 v139, v240, v241
s_waitcnt lgkmcnt(0)
v_fma_f32 v230, v98, v242, v134
v_fma_f32 v231, v99, v242, v135
v_fma_f32 v232, v100, v242, v136
v_fma_f32 v233, v101, v242, v137
v_cvt_pk_bf16_f32 v230, v230, v231
v_cvt_pk_bf16_f32 v231, v232, v233
ds_write_b64 v22, v[230:231] offset:40960
s_waitcnt vmcnt(5) lgkmcnt(0)
s_barrier
s_mov_b32 s70, 32
s_waitcnt lgkmcnt(0)
s_nop 1
s_branch .Lqa_in5
.p2align 6
.Lqa_top15:
s_waitcnt lgkmcnt(9)
v_mfma_f32_16x16x32_bf16 v[134:137], a[216:219], v[44:47], 0
s_waitcnt lgkmcnt(8)
ds_read_b128 v[162:165], v52 offset:4096
ds_read_b32 v8, v30
v_mfma_f32_16x16x32_bf16 v[134:137], a[220:223], v[102:105], v[134:137]
s_waitcnt lgkmcnt(9)
global_store_dwordx4 v16, v[230:233], s[38:39] sc1
v_mfma_f32_16x16x32_bf16 v[110:113], a[0:3], v[44:47], 0
s_waitcnt lgkmcnt(6)
ds_read_b128 v[166:169], v51 offset:6144
v_add_u32 v16, 18432, v16
v_mfma_f32_16x16x32_bf16 v[134:137], a[224:227], v[106:109], v[134:137]
s_waitcnt lgkmcnt(5)
ds_read_b128 v[170:173], v52 offset:6144
ds_read_b128 v[226:229], v25 offset:64
v_mfma_f32_16x16x32_bf16 v[134:137], a[228:231], v[0:3], v[134:137]
ds_read_b64 v[160:161], v22 offset:41472
ds_read_b32 v144, v20
v_mfma_f32_16x16x32_bf16 v[114:117], a[4:7], v[44:47], 0
s_waitcnt lgkmcnt(7)
ds_read_b128 v[174:177], v51 offset:8192
v_fma_f32 v238, v90, v23, v126
v_mfma_f32_16x16x32_bf16 v[134:137], a[232:235], v[4:7], v[134:137]
s_waitcnt lgkmcnt(7)
ds_read_b128 v[178:181], v52 offset:8192
v_fma_f32 v239, v91, v23, v127
v_mfma_f32_16x16x32_bf16 v[134:137], a[236:239], v[162:165], v[134:137]
v_fma_f32 v240, v92, v23, v128
v_fma_f32 v241, v93, v23, v129
v_mfma_f32_16x16x32_bf16 v[118:121], v[44:47], a[8:11], 0
s_waitcnt lgkmcnt(6)
ds_read_b128 v[182:185], v51 offset:10240
v_cvt_pk_bf16_f32 v150, v238, v239
v_mfma_f32_16x16x32_bf16 v[134:137], a[240:243], v[166:169], v[134:137]
s_waitcnt lgkmcnt(6)
ds_read_b128 v[186:189], v52 offset:10240
v_cvt_pk_bf16_f32 v151, v240, v241
v_mfma_f32_16x16x32_bf16 v[134:137], a[244:247], v[170:173], v[134:137]
s_nop 0
v_mfma_f32_16x16x32_bf16 v[214:217], v[150:153], v[138:141], v[32:35]
v_max_f32 v48, v218, v219
v_max_f32 v49, v220, v221
v_mfma_f32_16x16x32_bf16 v[110:113], a[12:15], v[102:105], v[110:113]
s_waitcnt lgkmcnt(6)
ds_read_b128 v[190:193], v51 offset:12288
v_fma_f32 v238, v94, v226, v130
s_waitcnt lgkmcnt(4)
v_mfma_f32_16x16x32_bf16 v[134:137], a[248:251], v[174:177], v[134:137]
s_xor_b32 s49, s49, 0x10000
ds_read_b128 v[194:197], v52 offset:12288
s_waitcnt lgkmcnt(4)
v_mfma_f32_16x16x32_bf16 v[134:137], a[252:255], v[178:181], v[134:137]
s_add_u32 m0, s49, 0
v_mul_f32 v26, 0x3eae1a93, v26
v_mul_f32 v9, v53, v26
v_mfma_f32_16x16x32_bf16 v[114:117], a[16:19], v[102:105], v[114:117]
s_waitcnt lgkmcnt(3)
ds_read_b128 v[198:201], v51 offset:14336
global_load_lds_dwordx4 v11, s[42:43] offset:-2048 sc1
v_mfma_f32_16x16x32_bf16 v[134:137], v[54:57], v[182:185], v[134:137]
s_waitcnt lgkmcnt(3)
ds_read_b128 v[202:205], v52 offset:14336
v_mul_f32 v10, v8, v26
v_mfma_f32_16x16x32_bf16 v[134:137], v[58:61], v[186:189], v[134:137]
v_max3_f32 v48, v214, v215, v48
v_max3_f32 v49, v216, v217, v49
v_mfma_f32_16x16x32_bf16 v[118:121], v[102:105], a[20:23], v[118:121]
s_waitcnt lgkmcnt(3)
ds_read_b128 v[206:209], v51 offset:16384
v_mul_f32 v48, v48, v9
v_mfma_f32_16x16x32_bf16 v[134:137], v[62:65], v[190:193], v[134:137]
s_waitcnt lgkmcnt(3)
ds_read_b128 v[210:213], v52 offset:16384
v_mul_f32 v49, v49, v10
v_mfma_f32_16x16x32_bf16 v[134:137], v[66:69], v[194:197], v[134:137]
v_max_f32 v50, v48, v49
v_mov_b32 v95, v50
v_mfma_f32_16x16x32_bf16 v[110:113], a[24:27], v[106:109], v[110:113]
s_waitcnt lgkmcnt(3)
v_permlane16_swap_b32 v50, v95
v_mfma_f32_16x16x32_bf16 v[134:137], v[70:73], v[198:201], v[134:137]
s_waitcnt lgkmcnt(2)
v_max_f32 v50, v50, v95
v_mov_b32 v95, v50
v_mfma_f32_16x16x32_bf16 v[134:137], v[74:77], v[202:205], v[134:137]
global_load_lds_dwordx4 v12, s[42:43] offset:-1024 sc1
v_fma_f32 v239, v94, v227, v131
v_mfma_f32_16x16x32_bf16 v[114:117], a[28:31], v[106:109], v[114:117]
s_waitcnt lgkmcnt(1)
v_permlane32_swap_b32 v50, v95
v_mfma_f32_16x16x32_bf16 v[134:137], v[78:81], v[206:209], v[134:137]
s_waitcnt lgkmcnt(0)
v_max_f32 v50, v50, v95
v_sub_f32 v48, v48, v50
v_mfma_f32_16x16x32_bf16 v[134:137], v[82:85], v[210:213], v[134:137]
v_sub_f32 v49, v49, v50
v_fma_f32 v240, v94, v228, v132
v_mfma_f32_16x16x32_bf16 v[118:121], v[106:109], a[32:35], v[118:121]
v_exp_f32 v96, v48
v_mfma_f32_16x16x32_bf16 v[110:113], a[36:39], v[0:3], v[110:113]
v_exp_f32 v97, v49
v_mfma_f32_16x16x32_bf16 v[114:117], a[40:43], v[0:3], v[114:117]
v_add_f32 v50, v96, v97
v_mov_b32 v95, v50
v_mfma_f32_16x16x32_bf16 v[118:121], v[0:3], a[44:47], v[118:121]
v_mul_f32 v96, v96, v53
v_fma_f32 v241, v94, v229, v133
v_mfma_f32_16x16x32_bf16 v[110:113], a[48:51], v[4:7], v[110:113]
global_load_lds_dwordx4 v13, s[42:43] offset:0 sc1
v_cvt_pk_bf16_f32 v156, v238, v239
v_mfma_f32_16x16x32_bf16 v[114:117], a[52:55], v[4:7], v[114:117]
v_permlane16_swap_b32 v50, v95
v_mfma_f32_16x16x32_bf16 v[118:121], v[4:7], a[56:59], v[118:121]
v_add_f32 v50, v50, v95
v_mov_b32 v95, v50
v_mfma_f32_16x16x32_bf16 v[110:113], a[60:63], v[162:165], v[110:113]
v_mul_f32 v97, v97, v8
v_cvt_pk_bf16_f32 v157, v240, v241
v_mfma_f32_16x16x32_bf16 v[114:117], a[64:67], v[162:165], v[114:117]
v_permlane32_swap_b32 v50, v95
v_mfma_f32_16x16x32_bf16 v[118:121], v[162:165], a[68:71], v[118:121]
v_mfma_f32_16x16x32_bf16 v[110:113], a[72:75], v[166:169], v[110:113]
s_barrier
v_mfma_f32_16x16x32_bf16 v[114:117], a[76:79], v[166:169], v[114:117]
ds_read_b32 v242, v24 offset:0
v_add_f32 v50, v50, v95
v_mfma_f32_16x16x32_bf16 v[118:121], v[166:169], a[80:83], v[118:121]
global_load_lds_dwordx4 v14, s[42:43] offset:1024 sc1
v_fma_f32 v238, v86, v23, v122
v_mfma_f32_16x16x32_bf16 v[110:113], a[84:87], v[170:173], v[110:113]
)"
R"(v_rcp_f32 v50, v50
v_mfma_f32_16x16x32_bf16 v[114:117], a[88:91], v[170:173], v[114:117]
v_mul_f32 v96, v96, v50
v_mul_f32 v97, v97, v50
v_mfma_f32_16x16x32_bf16 v[118:121], v[170:173], a[92:95], v[118:121]
v_cvt_pk_bf16_f32 v96, v96, v96
v_cvt_pk_bf16_f32 v97, v97, v97
v_mfma_f32_16x16x32_bf16 v[110:113], a[96:99], v[174:177], v[110:113]
v_and_b32 v224, v96, v40
v_and_b32 v225, v97, v41
v_mfma_f32_16x16x32_bf16 v[114:117], a[100:103], v[174:177], v[114:117]
v_and_b32 v222, v96, v42
v_and_b32 v223, v97, v43
v_mfma_f32_16x16x32_bf16 v[118:121], v[174:177], a[104:107], v[118:121]
s_nop 0
v_mfma_f32_16x16x32_bf16 v[218:221], v[154:157], v[222:225], 0
v_mfma_f32_16x16x32_bf16 v[214:217], v[158:161], v[222:225], 0
s_waitcnt lgkmcnt(0)
v_fma_f32 v230, v98, v242, v134
v_fma_f32 v231, v99, v242, v135
v_mfma_f32_16x16x32_bf16 v[110:113], a[108:111], v[178:181], v[110:113]
v_fma_f32 v232, v100, v242, v136
v_fma_f32 v233, v101, v242, v137
v_mfma_f32_16x16x32_bf16 v[114:117], a[112:115], v[178:181], v[114:117]
global_load_lds_dwordx4 v15, s[42:43] offset:2048 sc1
s_add_u32 s42, s42, s46
s_addc_u32 s43, s43, s47
v_mfma_f32_16x16x32_bf16 v[118:121], v[178:181], a[116:119], v[118:121]
v_cvt_pk_bf16_f32 v230, v230, v231
v_cvt_pk_bf16_f32 v231, v232, v233
v_mfma_f32_16x16x32_bf16 v[110:113], a[120:123], v[182:185], v[110:113]
v_cvt_pk_bf16_f32 v254, v218, v219
v_cvt_pk_bf16_f32 v255, v220, v221
v_mfma_f32_16x16x32_bf16 v[114:117], a[124:127], v[182:185], v[114:117]
ds_write_b64 v18, v[254:255]
v_mfma_f32_16x16x32_bf16 v[118:121], v[182:185], a[128:131], v[118:121]
v_cvt_pk_bf16_f32 v96, v214, v215
v_fma_f32 v239, v87, v23, v123
v_mfma_f32_16x16x32_bf16 v[110:113], a[132:135], v[186:189], v[110:113]
ds_write_b32 v19, v96
v_mfma_f32_16x16x32_bf16 v[114:117], a[136:139], v[186:189], v[114:117]
ds_write_b64 v22, v[230:231] offset:40960
v_mfma_f32_16x16x32_bf16 v[118:121], v[186:189], a[140:143], v[118:121]
v_fma_f32 v240, v88, v23, v124
v_fma_f32 v241, v89, v23, v125
v_mfma_f32_16x16x32_bf16 v[110:113], a[144:147], v[190:193], v[110:113]
v_cvt_pk_bf16_f32 v142, v238, v239
v_cvt_pk_bf16_f32 v143, v240, v241
v_mfma_f32_16x16x32_bf16 v[114:117], a[148:151], v[190:193], v[114:117]
v_mfma_f32_16x16x32_bf16 v[118:121], v[190:193], a[152:155], v[118:121]
v_mfma_f32_16x16x32_bf16 v[110:113], a[156:159], v[194:197], v[110:113]
v_mfma_f32_16x16x32_bf16 v[114:117], a[160:163], v[194:197], v[114:117]
v_mfma_f32_16x16x32_bf16 v[118:121], v[194:197], a[164:167], v[118:121]
v_mfma_f32_16x16x32_bf16 v[110:113], a[168:171], v[198:201], v[110:113]
v_mfma_f32_16x16x32_bf16 v[114:117], a[172:175], v[198:201], v[114:117]
v_mfma_f32_16x16x32_bf16 v[118:121], v[198:201], a[176:179], v[118:121]
v_mfma_f32_16x16x32_bf16 v[110:113], a[180:183], v[202:205], v[110:113]
v_mfma_f32_16x16x32_bf16 v[114:117], a[184:187], v[202:205], v[114:117]
s_waitcnt vmcnt(5) lgkmcnt(0)
s_barrier
v_mfma_f32_16x16x32_bf16 v[118:121], v[202:205], a[188:191], v[118:121]
ds_read_b128 v[44:47], v51 offset:18432
v_mfma_f32_16x16x32_bf16 v[110:113], a[192:195], v[206:209], v[110:113]
ds_read_b128 v[102:105], v52 offset:18432
ds_read_b128 v[230:233], v17 offset:0
v_mfma_f32_16x16x32_bf16 v[114:117], a[196:199], v[206:209], v[114:117]
ds_read_b32 v23, v24 offset:0
ds_read_b32 v148, v21
v_mfma_f32_16x16x32_bf16 v[218:221], v[150:153], v[142:145], v[36:39]
v_mfma_f32_16x16x32_bf16 v[118:121], v[206:209], a[200:203], v[118:121]
ds_read_b128 v[106:109], v51 offset:20480
ds_read_b32 v26, v24 offset:192
v_mfma_f32_16x16x32_bf16 v[110:113], a[204:207], v[210:213], v[110:113]
ds_read_b128 v[0:3], v52 offset:20480
ds_read_b32 v53, v27
v_mfma_f32_16x16x32_bf16 v[114:117], a[208:211], v[210:213], v[114:117]
ds_read_b32 v8, v28
ds_read_b128 v[226:229], v25 offset:0
v_mfma_f32_16x16x32_bf16 v[118:121], v[210:213], a[212:215], v[118:121]
s_waitcnt lgkmcnt(10)
ds_read_b128 v[4:7], v51 offset:22528
ds_read_b64 v[158:159], v22 offset:41472
v_mfma_f32_16x16x32_bf16 v[134:137], a[216:219], v[44:47], 0
s_waitcnt lgkmcnt(11)
ds_read_b128 v[162:165], v52 offset:22528
ds_read_b32 v140, v20
v_mfma_f32_16x16x32_bf16 v[134:137], a[220:223], v[102:105], v[134:137]
s_waitcnt lgkmcnt(12)
global_store_dwordx4 v16, v[230:233], s[38:39] sc1
v_mfma_f32_16x16x32_bf16 v[122:125], a[0:3], v[44:47], 0
s_waitcnt lgkmcnt(9)
ds_read_b128 v[166:169], v51 offset:24576
v_add_u32 v16, 18432, v16
v_mfma_f32_16x16x32_bf16 v[134:137], a[224:227], v[106:109], v[134:137]
s_waitcnt lgkmcnt(8)
ds_read_b128 v[170:173], v52 offset:24576
v_fma_f32 v238, v90, v23, v114
v_mfma_f32_16x16x32_bf16 v[134:137], a[228:231], v[0:3], v[134:137]
v_fma_f32 v239, v91, v23, v115
v_fma_f32 v240, v92, v23, v116
v_mfma_f32_16x16x32_bf16 v[126:129], a[4:7], v[44:47], 0
s_waitcnt lgkmcnt(5)
ds_read_b128 v[174:177], v51 offset:26624
v_fma_f32 v241, v93, v23, v117
v_mfma_f32_16x16x32_bf16 v[134:137], a[232:235], v[4:7], v[134:137]
s_waitcnt lgkmcnt(4)
ds_read_b128 v[178:181], v52 offset:26624
v_cvt_pk_bf16_f32 v146, v238, v239
v_mfma_f32_16x16x32_bf16 v[134:137], a[236:239], v[162:165], v[134:137]
v_cvt_pk_bf16_f32 v147, v240, v241
v_max_f32 v48, v218, v219
v_mfma_f32_16x16x32_bf16 v[130:133], v[44:47], a[8:11], 0
ds_read_b128 v[182:185], v51 offset:28672
v_mfma_f32_16x16x32_bf16 v[214:217], v[146:149], v[142:145], v[234:237]
s_waitcnt lgkmcnt(4)
v_max_f32 v49, v220, v221
v_fma_f32 v238, v94, v226, v118
v_mfma_f32_16x16x32_bf16 v[134:137], a[240:243], v[166:169], v[134:137]
s_waitcnt lgkmcnt(3)
ds_read_b128 v[186:189], v52 offset:28672
v_fma_f32 v239, v94, v227, v119
v_mfma_f32_16x16x32_bf16 v[134:137], a[244:247], v[170:173], v[134:137]
v_mul_f32 v26, 0x3eae1a93, v26
v_mul_f32 v9, v53, v26
v_mfma_f32_16x16x32_bf16 v[122:125], a[12:15], v[102:105], v[122:125]
s_waitcnt lgkmcnt(3)
ds_read_b128 v[190:193], v51 offset:30720
v_mul_f32 v10, v8, v26
v_mfma_f32_16x16x32_bf16 v[134:137], a[248:251], v[174:177], v[134:137]
s_add_u32 m0, s49, 18432
ds_read_b128 v[194:197], v52 offset:30720
s_waitcnt lgkmcnt(4)
v_mfma_f32_16x16x32_bf16 v[134:137], a[252:255], v[178:181], v[134:137]
global_load_lds_dwordx4 v11, s[42:43] offset:-2048 sc1
v_max3_f32 v48, v214, v215, v48
v_mfma_f32_16x16x32_bf16 v[126:129], a[16:19], v[102:105], v[126:129]
s_waitcnt lgkmcnt(3)
ds_read_b128 v[198:201], v51 offset:32768
v_max3_f32 v49, v216, v217, v49
v_mfma_f32_16x16x32_bf16 v[134:137], v[54:57], v[182:185], v[134:137]
s_waitcnt lgkmcnt(3)
ds_read_b128 v[202:205], v52 offset:32768
v_mul_f32 v48, v48, v9
v_mfma_f32_16x16x32_bf16 v[134:137], v[58:61], v[186:189], v[134:137]
v_mul_f32 v49, v49, v10
v_max_f32 v50, v48, v49
v_mfma_f32_16x16x32_bf16 v[130:133], v[102:105], a[20:23], v[130:133]
s_waitcnt lgkmcnt(3)
ds_read_b128 v[206:209], v51 offset:34816
v_mov_b32 v95, v50
v_mfma_f32_16x16x32_bf16 v[134:137], v[62:65], v[190:193], v[134:137]
s_waitcnt lgkmcnt(3)
ds_read_b128 v[210:213], v52 offset:34816
v_fma_f32 v240, v94, v228, v120
v_mfma_f32_16x16x32_bf16 v[134:137], v[66:69], v[194:197], v[134:137]
v_permlane16_swap_b32 v50, v95
v_mfma_f32_16x16x32_bf16 v[122:125], a[24:27], v[106:109], v[122:125]
s_waitcnt lgkmcnt(3)
v_max_f32 v50, v50, v95
v_mov_b32 v95, v50
v_mfma_f32_16x16x32_bf16 v[134:137], v[70:73], v[198:201], v[134:137]
s_waitcnt lgkmcnt(2)
global_load_lds_dwordx4 v12, s[42:43] offset:-1024 sc1
v_fma_f32 v241, v94, v229, v121
v_mfma_f32_16x16x32_bf16 v[134:137], v[74:77], v[202:205], v[134:137]
v_permlane32_swap_b32 v50, v95
v_mfma_f32_16x16x32_bf16 v[126:129], a[28:31], v[106:109], v[126:129]
s_waitcnt lgkmcnt(1)
v_max_f32 v50, v50, v95
v_sub_f32 v48, v48, v50
v_mfma_f32_16x16x32_bf16 v[134:137], v[78:81], v[206:209], v[134:137]
s_waitcnt lgkmcnt(0)
v_sub_f32 v49, v49, v50
v_cvt_pk_bf16_f32 v154, v238, v239
v_mfma_f32_16x16x32_bf16 v[134:137], v[82:85], v[210:213], v[134:137]
v_exp_f32 v96, v48
v_mfma_f32_16x16x32_bf16 v[130:133], v[106:109], a[32:35], v[130:133]
v_exp_f32 v97, v49
v_mfma_f32_16x16x32_bf16 v[122:125], a[36:39], v[0:3], v[122:125]
v_add_f32 v50, v96, v97
v_mov_b32 v95, v50
v_mfma_f32_16x16x32_bf16 v[126:129], a[40:43], v[0:3], v[126:129]
v_mul_f32 v96, v96, v53
v_cvt_pk_bf16_f32 v155, v240, v241
v_mfma_f32_16x16x32_bf16 v[130:133], v[0:3], a[44:47], v[130:133]
global_load_lds_dwordx4 v13, s[42:43] offset:0 sc1
v_fma_f32 v238, v86, v23, v110
v_mfma_f32_16x16x32_bf16 v[122:125], a[48:51], v[4:7], v[122:125]
v_permlane16_swap_b32 v50, v95
v_mfma_f32_16x16x32_bf16 v[126:129], a[52:55], v[4:7], v[126:129]
v_add_f32 v50, v50, v95
v_mov_b32 v95, v50
v_mfma_f32_16x16x32_bf16 v[130:133], v[4:7], a[56:59], v[130:133]
v_mul_f32 v97, v97, v8
v_fma_f32 v239, v87, v23, v111
v_mfma_f32_16x16x32_bf16 v[122:125], a[60:63], v[162:165], v[122:125]
v_permlane32_swap_b32 v50, v95
v_mfma_f32_16x16x32_bf16 v[126:129], a[64:67], v[162:165], v[126:129]
v_add_f32 v50, v50, v95
v_fma_f32 v240, v88, v23, v112
v_mfma_f32_16x16x32_bf16 v[130:133], v[162:165], a[68:71], v[130:133]
v_mfma_f32_16x16x32_bf16 v[122:125], a[72:75], v[166:169], v[122:125]
s_barrier
v_mfma_f32_16x16x32_bf16 v[126:129], a[76:79], v[166:169], v[126:129]
global_load_lds_dwordx4 v14, s[42:43] offset:1024 sc1
ds_read_b32 v242, v24 offset:64
v_mfma_f32_16x16x32_bf16 v[130:133], v[166:169], a[80:83], v[130:133]
v_rcp_f32 v50, v50
v_mfma_f32_16x16x32_bf16 v[122:125], a[84:87], v[170:173], v[122:125]
v_mul_f32 v96, v96, v50
v_mul_f32 v97, v97, v50
v_mfma_f32_16x16x32_bf16 v[126:129], a[88:91], v[170:173], v[126:129]
v_cvt_pk_bf16_f32 v96, v96, v96
v_cvt_pk_bf16_f32 v97, v97, v97
v_mfma_f32_16x16x32_bf16 v[130:133], v[170:173], a[92:95], v[130:133]
v_and_b32 v222, v96, v40
v_and_b32 v223, v97, v41
v_mfma_f32_16x16x32_bf16 v[122:125], a[96:99], v[174:177], v[122:125]
v_and_b32 v224, v96, v42
v_and_b32 v225, v97, v43
v_mfma_f32_16x16x32_bf16 v[126:129], a[100:103], v[174:177], v[126:129]
s_nop 0
v_mfma_f32_16x16x32_bf16 v[218:221], v[154:157], v[222:225], 0
v_mfma_f32_16x16x32_bf16 v[214:217], v[158:161], v[222:225], 0
s_waitcnt lgkmcnt(0)
v_fma_f32 v230, v98, v242, v134
v_fma_f32 v231, v99, v242, v135
v_mfma_f32_16x16x32_bf16 v[130:133], v[174:177], a[104:107], v[130:133]
v_fma_f32 v232, v100, v242, v136
v_fma_f32 v233, v101, v242, v137
v_mfma_f32_16x16x32_bf16 v[122:125], a[108:111], v[178:181], v[122:125]
global_load_lds_dwordx4 v15, s[42:43] offset:2048 sc1
s_sub_u32 s42, s42, 18432
s_subb_u32 s43, s43, 0
v_mfma_f32_16x16x32_bf16 v[126:129], a[112:115], v[178:181], v[126:129]
v_cvt_pk_bf16_f32 v230, v230, v231
v_cvt_pk_bf16_f32 v231, v232, v233
v_mfma_f32_16x16x32_bf16 v[130:133], v[178:181], a[116:119], v[130:133]
v_cvt_pk_bf16_f32 v254, v218, v219
v_cvt_pk_bf16_f32 v255, v220, v221
v_mfma_f32_16x16x32_bf16 v[122:125], a[120:123], v[182:185], v[122:125]
ds_write_b64 v18, v[254:255]
v_mfma_f32_16x16x32_bf16 v[126:129], a[124:127], v[182:185], v[126:129]
v_cvt_pk_bf16_f32 v96, v214, v215
v_fma_f32 v241, v89, v23, v113
v_mfma_f32_16x16x32_bf16 v[130:133], v[182:185], a[128:131], v[130:133]
ds_write_b32 v19, v96
v_mfma_f32_16x16x32_bf16 v[122:125], a[132:135], v[186:189], v[122:125]
ds_write_b64 v22, v[230:231] offset:40960
v_mfma_f32_16x16x32_bf16 v[126:129], a[136:139], v[186:189], v[126:129]
v_cvt_pk_bf16_f32 v138, v238, v239
v_cvt_pk_bf16_f32 v139, v240, v241
v_mfma_f32_16x16x32_bf16 v[130:133], v[186:189], a[140:143], v[130:133]
v_mfma_f32_16x16x32_bf16 v[122:125], a[144:147], v[190:193], v[122:125]
v_mfma_f32_16x16x32_bf16 v[126:129], a[148:151], v[190:193], v[126:129]
v_mfma_f32_16x16x32_bf16 v[130:133], v[190:193], a[152:155], v[130:133]
v_mfma_f32_16x16x32_bf16 v[122:125], a[156:159], v[194:197], v[122:125]
v_mfma_f32_16x16x32_bf16 v[126:129], a[160:163], v[194:197], v[126:129]
v_mfma_f32_16x16x32_bf16 v[130:133], v[194:197], a[164:167], v[130:133]
v_mfma_f32_16x16x32_bf16 v[122:125], a[168:171], v[198:201], v[122:125]
v_mfma_f32_16x16x32_bf16 v[126:129], a[172:175], v[198:201], v[126:129]
v_xor_b32 v51, 0x10000, v51
v_xor_b32 v52, 0x10000, v52
v_mfma_f32_16x16x32_bf16 v[130:133], v[198:201], a[176:179], v[130:133]
v_mfma_f32_16x16x32_bf16 v[122:125], a[180:183], v[202:205], v[122:125]
v_mfma_f32_16x16x32_bf16 v[126:129], a[184:187], v[202:205], v[126:129]
s_waitcnt vmcnt(5) lgkmcnt(0)
s_barrier
.Lqa_in5:
v_mfma_f32_16x16x32_bf16 v[130:133], v[202:205], a[188:191], v[130:133]
s_cmp_eq_u32 s70, s73
s_cbranch_scc1 .Lqa_hk6
.Lqa_hr7:
ds_read_b128 v[44:47], v51 offset:0
v_mfma_f32_16x16x32_bf16 v[122:125], a[192:195], v[206:209], v[122:125]
ds_read_b128 v[102:105], v52 offset:0
ds_read_b128 v[230:233], v17 offset:0
v_mfma_f32_16x16x32_bf16 v[126:129], a[196:199], v[206:209], v[126:129]
ds_read_b32 v23, v24 offset:64
ds_read_b32 v152, v21
v_mfma_f32_16x16x32_bf16 v[218:221], v[146:149], v[138:141], v[36:39]
v_mfma_f32_16x16x32_bf16 v[130:133], v[206:209], a[200:203], v[130:133]
ds_read_b128 v[106:109], v51 offset:2048
ds_read_b32 v26, v24 offset:128
v_mfma_f32_16x16x32_bf16 v[122:125], a[204:207], v[210:213], v[122:125]
ds_read_b128 v[0:3], v52 offset:2048
ds_read_b32 v53, v29
v_mfma_f32_16x16x32_bf16 v[126:129], a[208:211], v[210:213], v[126:129]
v_mfma_f32_16x16x32_bf16 v[130:133], v[210:213], a[212:215], v[130:133]
s_sub_u32 s70, s70, 1
ds_read_b128 v[4:7], v51 offset:4096
s_cbranch_scc0 .Lqa_top15
s_waitcnt lgkmcnt(0)
ds_read_b32 v8, v30
global_store_dwordx4 v16, v[230:233], s[38:39] sc1
v_add_u32 v16, 18432, v16
ds_read_b128 v[226:229], v25 offset:64
ds_read_b64 v[160:161], v22 offset:41472
ds_read_b32 v144, v20
v_fma_f32 v238, v90, v23, v126
v_fma_f32 v239, v91, v23, v127
v_fma_f32 v240, v92, v23, v128
v_fma_f32 v241, v93, v23, v129
v_cvt_pk_bf16_f32 v150, v238, v239
v_cvt_pk_bf16_f32 v151, v240, v241
s_nop 1
v_mfma_f32_16x16x32_bf16 v[214:217], v[150:153], v[138:141], v[32:35]
s_waitcnt lgkmcnt(2)
v_max_f32 v48, v218, v219
v_max_f32 v49, v220, v221
v_fma_f32 v238, v94, v226, v130
v_mul_f32 v26, 0x3eae1a93, v26
v_mul_f32 v9, v53, v26
v_mul_f32 v10, v8, v26
s_nop 0
v_max3_f32 v48, v214, v215, v48
v_max3_f32 v49, v216, v217, v49
v_mul_f32 v48, v48, v9
v_mul_f32 v49, v49, v10
v_max_f32 v50, v48, v49
v_mov_b32 v95, v50
s_nop 1
v_permlane16_swap_b32 v50, v95
v_max_f32 v50, v50, v95
v_mov_b32 v95, v50
v_fma_f32 v239, v94, v227, v131
s_nop 0
v_permlane32_swap_b32 v50, v95
v_max_f32 v50, v50, v95
v_sub_f32 v48, v48, v50
v_sub_f32 v49, v49, v50
v_fma_f32 v240, v94, v228, v132
v_exp_f32 v96, v48
v_exp_f32 v97, v49
s_nop 0
v_add_f32 v50, v96, v97
v_mov_b32 v95, v50
v_mul_f32 v96, v96, v53
v_fma_f32 v241, v94, v229, v133
v_cvt_pk_bf16_f32 v156, v238, v239
v_permlane16_swap_b32 v50, v95
v_add_f32 v50, v50, v95
v_mov_b32 v95, v50
v_mul_f32 v97, v97, v8
v_cvt_pk_bf16_f32 v157, v240, v241
v_permlane32_swap_b32 v50, v95
s_waitcnt lgkmcnt(0)
s_barrier
v_add_f32 v50, v50, v95
v_fma_f32 v238, v86, v23, v122
v_rcp_f32 v50, v50
s_nop 0
v_mul_f32 v96, v96, v50
v_mul_f32 v97, v97, v50
v_cvt_pk_bf16_f32 v96, v96, v96
v_cvt_pk_bf16_f32 v97, v97, v97
v_and_b32 v224, v96, v40
v_and_b32 v225, v97, v41
v_and_b32 v222, v96, v42
v_and_b32 v223, v97, v43
s_nop 1
v_mfma_f32_16x16x32_bf16 v[218:221], v[154:157], v[222:225], 0
v_mfma_f32_16x16x32_bf16 v[214:217], v[158:161], v[222:225], 0
s_nop 6
v_cvt_pk_bf16_f32 v254, v218, v219
v_cvt_pk_bf16_f32 v255, v220, v221
ds_write_b64 v18, v[254:255]
v_cvt_pk_bf16_f32 v96, v214, v215
v_fma_f32 v239, v87, v23, v123
ds_write_b32 v19, v96
v_fma_f32 v240, v88, v23, v124
v_fma_f32 v241, v89, v23, v125
v_cvt_pk_bf16_f32 v142, v238, v239
v_cvt_pk_bf16_f32 v143, v240, v241
s_waitcnt vmcnt(0)
s_waitcnt lgkmcnt(0)
s_barrier
ds_read_b128 v[230:233], v17 offset:0
s_waitcnt lgkmcnt(0)
global_store_dwordx4 v16, v[230:233], s[38:39] sc1
s_waitcnt vmcnt(0)
s_barrier
s_endpgm
.Lqa_w2:
s_add_u32 m0, s48, 4096
s_nop 0
global_load_lds_dwordx4 v0, s[40:41] offset:-4096
global_load_lds_dwordx4 v1, s[40:41] offset:-3072
global_load_lds_dwordx4 v2, s[40:41] offset:-2048
global_load_lds_dwordx4 v3, s[40:41] offset:-1024
global_load_lds_dwordx4 v4, s[40:41] offset:0
global_load_lds_dwordx4 v5, s[40:41] offset:1024
global_load_lds_dwordx4 v6, s[40:41] offset:2048
global_load_lds_dwordx4 v7, s[40:41] offset:3072
s_add_u32 m0, s48, 12288
s_nop 0
global_load_lds_dwordx4 v8, s[40:41] offset:-4096
s_add_u32 s40, s40, 221184
s_addc_u32 s41, s41, 0
s_add_u32 m0, s48, 40960
)"
R"(s_nop 0
global_load_lds_dwordx4 v0, s[40:41] offset:-4096
global_load_lds_dwordx4 v1, s[40:41] offset:-3072
global_load_lds_dwordx4 v2, s[40:41] offset:-2048
global_load_lds_dwordx4 v3, s[40:41] offset:-1024
global_load_lds_dwordx4 v4, s[40:41] offset:0
global_load_lds_dwordx4 v5, s[40:41] offset:1024
global_load_lds_dwordx4 v6, s[40:41] offset:2048
global_load_lds_dwordx4 v7, s[40:41] offset:3072
s_add_u32 m0, s48, 49152
s_nop 0
global_load_lds_dwordx4 v8, s[40:41] offset:-4096
s_add_u32 s40, s40, 221184
s_addc_u32 s41, s41, 0
v_add_u32 v255, 0x10000, v9
v_add_u32 v162, 0x20000, v9
v_add_u32 v163, 0x10000, v10
v_add_u32 v164, 0x20000, v10
v_mov_b32 v106, 0x3F803F80
v_mov_b32 v107, 0x3F803F80
v_mov_b32 v108, 0x3F803F80
v_mov_b32 v109, 0x3F803F80
v_mov_b32 v141, 0
v_mov_b32 v145, 0
v_mov_b32 v149, 0
v_mov_b32 v153, 0
v_mov_b32 v165, 46080
s_waitcnt lgkmcnt(0)
s_mul_i32 s37, s37, 0x9E3779B1
s_add_u32 s37, s37, 0x7F4A7C15
v_add_u32 v46, s37, v46
v_add_u32 v48, s37, v48
v_add_u32 v50, s37, v50
ds_write_b32 v165, v141
s_mov_b64 s[76:77], 0
s_mov_b32 s73, 18
s_waitcnt vmcnt(9) lgkmcnt(0)
s_barrier
s_add_u32 m0, s48, 77824
ds_read_b64_tr_b16 a[0:1], v9 offset:47104
ds_read_b64_tr_b16 a[2:3], v9 offset:49408
global_load_lds_dwordx4 v0, s[40:41] offset:-4096
ds_read_b64_tr_b16 a[4:5], v9 offset:47296
ds_read_b64_tr_b16 a[6:7], v9 offset:49600
global_load_lds_dwordx4 v1, s[40:41] offset:-3072
ds_read_b64_tr_b16 a[8:9], v9 offset:47488
global_load_lds_dwordx4 v2, s[40:41] offset:-2048
ds_read_b64_tr_b16 a[10:11], v9 offset:49792
ds_read_b64_tr_b16 a[216:217], v10 offset:47104
global_load_lds_dwordx4 v3, s[40:41] offset:-1024
ds_read_b64_tr_b16 a[218:219], v10 offset:49408
global_load_lds_dwordx4 v4, s[40:41] offset:0
ds_read_b128 v[166:169], v51 offset:0
ds_read_b128 v[174:177], v51 offset:18432
global_load_lds_dwordx4 v5, s[40:41] offset:1024
ds_read_b64_tr_b16 a[12:13], v255 offset:0
global_load_lds_dwordx4 v6, s[40:41] offset:2048
ds_read_b64_tr_b16 a[14:15], v255 offset:2304
ds_read_b64_tr_b16 a[16:17], v255 offset:192
global_load_lds_dwordx4 v7, s[40:41] offset:3072
ds_read_b64_tr_b16 a[18:19], v255 offset:2496
s_add_u32 m0, s48, 86016
ds_read_b64_tr_b16 a[20:21], v255 offset:384
ds_read_b64_tr_b16 a[22:23], v255 offset:2688
global_load_lds_dwordx4 v8, s[40:41] offset:-4096
ds_read_b64_tr_b16 a[220:221], v163 offset:0
s_add_u32 s40, s40, 221184
ds_read_b64_tr_b16 a[222:223], v163 offset:2304
ds_read_b128 v[170:173], v52 offset:0
s_addc_u32 s41, s41, 0
ds_read_b128 v[178:181], v52 offset:18432
s_waitcnt vmcnt(9) lgkmcnt(0)
s_barrier
s_add_u32 m0, s48, 4096
ds_read_b64_tr_b16 a[24:25], v255 offset:18432
v_mfma_f32_16x16x32_bf16 v[86:89], a[0:3], v[106:109], 0
ds_read_b64_tr_b16 a[26:27], v255 offset:20736
v_mfma_f32_16x16x32_bf16 v[90:93], a[4:7], v[106:109], 0
global_load_lds_dwordx4 v0, s[40:41] offset:-4096
ds_read_b64_tr_b16 a[28:29], v255 offset:18624
v_mfma_f32_16x16x32_bf16 v[94:97], v[106:109], a[8:11], 0
ds_read_b64_tr_b16 a[30:31], v255 offset:20928
global_load_lds_dwordx4 v1, s[40:41] offset:-3072
v_mfma_f32_16x16x32_bf16 v[102:105], v[106:109], a[216:219], 0
ds_read_b64_tr_b16 a[32:33], v255 offset:18816
v_mfma_f32_16x16x32_bf16 v[110:113], a[0:3], v[166:169], 0
ds_read_b64_tr_b16 a[34:35], v255 offset:21120
global_load_lds_dwordx4 v2, s[40:41] offset:-2048
v_mfma_f32_16x16x32_bf16 v[114:117], a[4:7], v[166:169], 0
ds_read_b64_tr_b16 a[224:225], v163 offset:18432
v_mfma_f32_16x16x32_bf16 v[118:121], v[166:169], a[8:11], 0
global_load_lds_dwordx4 v3, s[40:41] offset:-1024
ds_read_b64_tr_b16 a[226:227], v163 offset:20736
v_mfma_f32_16x16x32_bf16 v[134:137], v[166:169], a[216:219], 0
ds_read_b128 v[182:185], v51 offset:2048
global_load_lds_dwordx4 v4, s[40:41] offset:0
v_mfma_f32_16x16x32_bf16 v[122:125], a[0:3], v[174:177], 0
ds_read_b128 v[190:193], v51 offset:20480
v_mfma_f32_16x16x32_bf16 v[126:129], a[4:7], v[174:177], 0
ds_read_b64_tr_b16 a[36:37], v255 offset:36864
global_load_lds_dwordx4 v5, s[40:41] offset:1024
v_mfma_f32_16x16x32_bf16 v[130:133], v[174:177], a[8:11], 0
ds_read_b64_tr_b16 a[38:39], v255 offset:39168
v_mfma_f32_16x16x32_bf16 v[158:161], v[174:177], a[216:219], 0
global_load_lds_dwordx4 v6, s[40:41] offset:2048
ds_read_b64_tr_b16 a[40:41], v255 offset:37056
v_mfma_f32_16x16x32_bf16 v[86:89], a[12:15], v[106:109], v[86:89]
ds_read_b64_tr_b16 a[42:43], v255 offset:39360
v_mfma_f32_16x16x32_bf16 v[90:93], a[16:19], v[106:109], v[90:93]
global_load_lds_dwordx4 v7, s[40:41] offset:3072
ds_read_b64_tr_b16 a[44:45], v255 offset:37248
v_mfma_f32_16x16x32_bf16 v[94:97], v[106:109], a[20:23], v[94:97]
s_add_u32 m0, s48, 12288
ds_read_b64_tr_b16 a[46:47], v255 offset:39552
v_mfma_f32_16x16x32_bf16 v[102:105], v[106:109], a[220:223], v[102:105]
ds_read_b64_tr_b16 a[228:229], v163 offset:36864
v_mfma_f32_16x16x32_bf16 v[110:113], a[12:15], v[170:173], v[110:113]
global_load_lds_dwordx4 v8, s[40:41] offset:-4096
ds_read_b64_tr_b16 a[230:231], v163 offset:39168
v_mfma_f32_16x16x32_bf16 v[114:117], a[16:19], v[170:173], v[114:117]
ds_read_b128 v[186:189], v52 offset:2048
v_mfma_f32_16x16x32_bf16 v[118:121], v[170:173], a[20:23], v[118:121]
s_add_u32 s40, s40, 221184
ds_read_b128 v[194:197], v52 offset:20480
v_mfma_f32_16x16x32_bf16 v[134:137], v[170:173], a[220:223], v[134:137]
v_mfma_f32_16x16x32_bf16 v[122:125], a[12:15], v[178:181], v[122:125]
s_addc_u32 s41, s41, 0
v_mfma_f32_16x16x32_bf16 v[126:129], a[16:19], v[178:181], v[126:129]
v_mfma_f32_16x16x32_bf16 v[130:133], v[178:181], a[20:23], v[130:133]
v_mfma_f32_16x16x32_bf16 v[158:161], v[178:181], a[220:223], v[158:161]
s_waitcnt vmcnt(9) lgkmcnt(0)
s_barrier
s_add_u32 m0, s48, 40960
ds_read_b64_tr_b16 a[48:49], v255 offset:55296
v_mfma_f32_16x16x32_bf16 v[86:89], a[24:27], v[106:109], v[86:89]
ds_read_b64_tr_b16 a[50:51], v255 offset:57600
v_mfma_f32_16x16x32_bf16 v[90:93], a[28:31], v[106:109], v[90:93]
global_load_lds_dwordx4 v0, s[40:41] offset:-4096
ds_read_b64_tr_b16 a[52:53], v255 offset:55488
v_mfma_f32_16x16x32_bf16 v[94:97], v[106:109], a[32:35], v[94:97]
ds_read_b64_tr_b16 a[54:55], v255 offset:57792
global_load_lds_dwordx4 v1, s[40:41] offset:-3072
v_mfma_f32_16x16x32_bf16 v[102:105], v[106:109], a[224:227], v[102:105]
ds_read_b64_tr_b16 a[56:57], v255 offset:55680
v_mfma_f32_16x16x32_bf16 v[110:113], a[24:27], v[182:185], v[110:113]
ds_read_b64_tr_b16 a[58:59], v255 offset:57984
global_load_lds_dwordx4 v2, s[40:41] offset:-2048
v_mfma_f32_16x16x32_bf16 v[114:117], a[28:31], v[182:185], v[114:117]
ds_read_b64_tr_b16 a[232:233], v163 offset:55296
v_mfma_f32_16x16x32_bf16 v[118:121], v[182:185], a[32:35], v[118:121]
global_load_lds_dwordx4 v3, s[40:41] offset:-1024
ds_read_b64_tr_b16 a[234:235], v163 offset:57600
v_mfma_f32_16x16x32_bf16 v[134:137], v[182:185], a[224:227], v[134:137]
ds_read_b128 v[166:169], v51 offset:4096
global_load_lds_dwordx4 v4, s[40:41] offset:0
v_mfma_f32_16x16x32_bf16 v[122:125], a[24:27], v[190:193], v[122:125]
ds_read_b128 v[174:177], v51 offset:22528
v_mfma_f32_16x16x32_bf16 v[126:129], a[28:31], v[190:193], v[126:129]
ds_read_b64_tr_b16 a[60:61], v162 offset:8192
global_load_lds_dwordx4 v5, s[40:41] offset:1024
v_mfma_f32_16x16x32_bf16 v[130:133], v[190:193], a[32:35], v[130:133]
ds_read_b64_tr_b16 a[62:63], v162 offset:10496
v_mfma_f32_16x16x32_bf16 v[158:161], v[190:193], a[224:227], v[158:161]
global_load_lds_dwordx4 v6, s[40:41] offset:2048
ds_read_b64_tr_b16 a[64:65], v162 offset:8384
v_mfma_f32_16x16x32_bf16 v[86:89], a[36:39], v[106:109], v[86:89]
ds_read_b64_tr_b16 a[66:67], v162 offset:10688
v_mfma_f32_16x16x32_bf16 v[90:93], a[40:43], v[106:109], v[90:93]
global_load_lds_dwordx4 v7, s[40:41] offset:3072
ds_read_b64_tr_b16 a[68:69], v162 offset:8576
v_mfma_f32_16x16x32_bf16 v[94:97], v[106:109], a[44:47], v[94:97]
s_add_u32 m0, s48, 49152
ds_read_b64_tr_b16 a[70:71], v162 offset:10880
v_mfma_f32_16x16x32_bf16 v[102:105], v[106:109], a[228:231], v[102:105]
ds_read_b64_tr_b16 a[236:237], v164 offset:8192
v_mfma_f32_16x16x32_bf16 v[110:113], a[36:39], v[186:189], v[110:113]
global_load_lds_dwordx4 v8, s[40:41] offset:-4096
ds_read_b64_tr_b16 a[238:239], v164 offset:10496
v_mfma_f32_16x16x32_bf16 v[114:117], a[40:43], v[186:189], v[114:117]
ds_read_b128 v[170:173], v52 offset:4096
v_mfma_f32_16x16x32_bf16 v[118:121], v[186:189], a[44:47], v[118:121]
s_add_u32 s40, s40, 221184
ds_read_b128 v[178:181], v52 offset:22528
v_mfma_f32_16x16x32_bf16 v[134:137], v[186:189], a[228:231], v[134:137]
v_mfma_f32_16x16x32_bf16 v[122:125], a[36:39], v[194:197], v[122:125]
s_addc_u32 s41, s41, 0
v_mfma_f32_16x16x32_bf16 v[126:129], a[40:43], v[194:197], v[126:129]
v_mfma_f32_16x16x32_bf16 v[130:133], v[194:197], a[44:47], v[130:133]
v_mfma_f32_16x16x32_bf16 v[158:161], v[194:197], a[228:231], v[158:161]
s_waitcnt vmcnt(9) lgkmcnt(0)
s_barrier
s_add_u32 m0, s48, 77824
ds_read_b64_tr_b16 a[72:73], v9 offset:47104
v_mfma_f32_16x16x32_bf16 v[86:89], a[48:51], v[106:109], v[86:89]
ds_read_b64_tr_b16 a[74:75], v9 offset:49408
v_mfma_f32_16x16x32_bf16 v[90:93], a[52:55], v[106:109], v[90:93]
global_load_lds_dwordx4 v0, s[40:41] offset:-4096
ds_read_b64_tr_b16 a[76:77], v9 offset:47296
v_mfma_f32_16x16x32_bf16 v[94:97], v[106:109], a[56:59], v[94:97]
ds_read_b64_tr_b16 a[78:79], v9 offset:49600
global_load_lds_dwordx4 v1, s[40:41] offset:-3072
v_mfma_f32_16x16x32_bf16 v[102:105], v[106:109], a[232:235], v[102:105]
ds_read_b64_tr_b16 a[80:81], v9 offset:47488
v_mfma_f32_16x16x32_bf16 v[110:113], a[48:51], v[166:169], v[110:113]
ds_read_b64_tr_b16 a[82:83], v9 offset:49792
global_load_lds_dwordx4 v2, s[40:41] offset:-2048
v_mfma_f32_16x16x32_bf16 v[114:117], a[52:55], v[166:169], v[114:117]
ds_read_b64_tr_b16 a[240:241], v10 offset:47104
v_mfma_f32_16x16x32_bf16 v[118:121], v[166:169], a[56:59], v[118:121]
global_load_lds_dwordx4 v3, s[40:41] offset:-1024
ds_read_b64_tr_b16 a[242:243], v10 offset:49408
v_mfma_f32_16x16x32_bf16 v[134:137], v[166:169], a[232:235], v[134:137]
ds_read_b128 v[182:185], v51 offset:6144
global_load_lds_dwordx4 v4, s[40:41] offset:0
v_mfma_f32_16x16x32_bf16 v[122:125], a[48:51], v[174:177], v[122:125]
ds_read_b128 v[190:193], v51 offset:24576
v_mfma_f32_16x16x32_bf16 v[126:129], a[52:55], v[174:177], v[126:129]
ds_read_b64_tr_b16 a[84:85], v255 offset:0
global_load_lds_dwordx4 v5, s[40:41] offset:1024
v_mfma_f32_16x16x32_bf16 v[130:133], v[174:177], a[56:59], v[130:133]
ds_read_b64_tr_b16 a[86:87], v255 offset:2304
v_mfma_f32_16x16x32_bf16 v[158:161], v[174:177], a[232:235], v[158:161]
global_load_lds_dwordx4 v6, s[40:41] offset:2048
ds_read_b64_tr_b16 a[88:89], v255 offset:192
v_mfma_f32_16x16x32_bf16 v[86:89], a[60:63], v[106:109], v[86:89]
ds_read_b64_tr_b16 a[90:91], v255 offset:2496
v_mfma_f32_16x16x32_bf16 v[90:93], a[64:67], v[106:109], v[90:93]
global_load_lds_dwordx4 v7, s[40:41] offset:3072
ds_read_b64_tr_b16 a[92:93], v255 offset:384
v_mfma_f32_16x16x32_bf16 v[94:97], v[106:109], a[68:71], v[94:97]
s_add_u32 m0, s48, 86016
ds_read_b64_tr_b16 a[94:95], v255 offset:2688
v_mfma_f32_16x16x32_bf16 v[102:105], v[106:109], a[236:239], v[102:105]
ds_read_b64_tr_b16 a[244:245], v163 offset:0
v_mfma_f32_16x16x32_bf16 v[110:113], a[60:63], v[170:173], v[110:113]
global_load_lds_dwordx4 v8, s[40:41] offset:-4096
ds_read_b64_tr_b16 a[246:247], v163 offset:2304
v_mfma_f32_16x16x32_bf16 v[114:117], a[64:67], v[170:173], v[114:117]
ds_read_b128 v[186:189], v52 offset:6144
v_mfma_f32_16x16x32_bf16 v[118:121], v[170:173], a[68:71], v[118:121]
s_add_u32 s40, s40, 221184
ds_read_b128 v[194:197], v52 offset:24576
v_mfma_f32_16x16x32_bf16 v[134:137], v[170:173], a[236:239], v[134:137]
v_mfma_f32_16x16x32_bf16 v[122:125], a[60:63], v[178:181], v[122:125]
s_addc_u32 s41, s41, 0
v_mfma_f32_16x16x32_bf16 v[126:129], a[64:67], v[178:181], v[126:129]
v_mfma_f32_16x16x32_bf16 v[130:133], v[178:181], a[68:71], v[130:133]
v_mfma_f32_16x16x32_bf16 v[158:161], v[178:181], a[236:239], v[158:161]
s_waitcnt vmcnt(9) lgkmcnt(0)
s_barrier
s_add_u32 m0, s48, 4096
ds_read_b64_tr_b16 a[96:97], v255 offset:18432
v_mfma_f32_16x16x32_bf16 v[86:89], a[72:75], v[106:109], v[86:89]
ds_read_b64_tr_b16 a[98:99], v255 offset:20736
v_mfma_f32_16x16x32_bf16 v[90:93], a[76:79], v[106:109], v[90:93]
global_load_lds_dwordx4 v0, s[40:41] offset:-4096
ds_read_b64_tr_b16 a[100:101], v255 offset:18624
v_mfma_f32_16x16x32_bf16 v[94:97], v[106:109], a[80:83], v[94:97]
ds_read_b64_tr_b16 a[102:103], v255 offset:20928
global_load_lds_dwordx4 v1, s[40:41] offset:-3072
v_mfma_f32_16x16x32_bf16 v[102:105], v[106:109], a[240:243], v[102:105]
ds_read_b64_tr_b16 a[104:105], v255 offset:18816
v_mfma_f32_16x16x32_bf16 v[110:113], a[72:75], v[182:185], v[110:113]
ds_read_b64_tr_b16 a[106:107], v255 offset:21120
global_load_lds_dwordx4 v2, s[40:41] offset:-2048
v_mfma_f32_16x16x32_bf16 v[114:117], a[76:79], v[182:185], v[114:117]
ds_read_b64_tr_b16 a[248:249], v163 offset:18432
v_mfma_f32_16x16x32_bf16 v[118:121], v[182:185], a[80:83], v[118:121]
global_load_lds_dwordx4 v3, s[40:41] offset:-1024
ds_read_b64_tr_b16 a[250:251], v163 offset:20736
v_mfma_f32_16x16x32_bf16 v[134:137], v[182:185], a[240:243], v[134:137]
ds_read_b128 v[166:169], v51 offset:8192
global_load_lds_dwordx4 v4, s[40:41] offset:0
v_mfma_f32_16x16x32_bf16 v[122:125], a[72:75], v[190:193], v[122:125]
ds_read_b128 v[174:177], v51 offset:26624
v_mfma_f32_16x16x32_bf16 v[126:129], a[76:79], v[190:193], v[126:129]
ds_read_b64_tr_b16 a[108:109], v255 offset:36864
global_load_lds_dwordx4 v5, s[40:41] offset:1024
v_mfma_f32_16x16x32_bf16 v[130:133], v[190:193], a[80:83], v[130:133]
ds_read_b64_tr_b16 a[110:111], v255 offset:39168
v_mfma_f32_16x16x32_bf16 v[158:161], v[190:193], a[240:243], v[158:161]
global_load_lds_dwordx4 v6, s[40:41] offset:2048
ds_read_b64_tr_b16 a[112:113], v255 offset:37056
v_mfma_f32_16x16x32_bf16 v[86:89], a[84:87], v[106:109], v[86:89]
ds_read_b64_tr_b16 a[114:115], v255 offset:39360
v_mfma_f32_16x16x32_bf16 v[90:93], a[88:91], v[106:109], v[90:93]
global_load_lds_dwordx4 v7, s[40:41] offset:3072
ds_read_b64_tr_b16 a[116:117], v255 offset:37248
v_mfma_f32_16x16x32_bf16 v[94:97], v[106:109], a[92:95], v[94:97]
s_add_u32 m0, s48, 12288
ds_read_b64_tr_b16 a[118:119], v255 offset:39552
v_mfma_f32_16x16x32_bf16 v[102:105], v[106:109], a[244:247], v[102:105]
ds_read_b64_tr_b16 a[252:253], v163 offset:36864
v_mfma_f32_16x16x32_bf16 v[110:113], a[84:87], v[186:189], v[110:113]
global_load_lds_dwordx4 v8, s[40:41] offset:-4096
ds_read_b64_tr_b16 a[254:255], v163 offset:39168
v_mfma_f32_16x16x32_bf16 v[114:117], a[88:91], v[186:189], v[114:117]
ds_read_b128 v[170:173], v52 offset:8192
v_mfma_f32_16x16x32_bf16 v[118:121], v[186:189], a[92:95], v[118:121]
s_add_u32 s40, s40, 221184
ds_read_b128 v[178:181], v52 offset:26624
v_mfma_f32_16x16x32_bf16 v[134:137], v[186:189], a[244:247], v[134:137]
v_mfma_f32_16x16x32_bf16 v[122:125], a[84:87], v[194:197], v[122:125]
s_addc_u32 s41, s41, 0
v_mfma_f32_16x16x32_bf16 v[126:129], a[88:91], v[194:197], v[126:129]
v_mfma_f32_16x16x32_bf16 v[130:133], v[194:197], a[92:95], v[130:133]
v_mfma_f32_16x16x32_bf16 v[158:161], v[194:197], a[244:247], v[158:161]
s_waitcnt vmcnt(9) lgkmcnt(0)
s_barrier
s_add_u32 m0, s48, 40960
ds_read_b64_tr_b16 a[120:121], v255 offset:55296
v_mfma_f32_16x16x32_bf16 v[86:89], a[96:99], v[106:109], v[86:89]
ds_read_b64_tr_b16 a[122:123], v255 offset:57600
v_mfma_f32_16x16x32_bf16 v[90:93], a[100:103], v[106:109], v[90:93]
global_load_lds_dwordx4 v0, s[40:41] offset:-4096
ds_read_b64_tr_b16 a[124:125], v255 offset:55488
v_mfma_f32_16x16x32_bf16 v[94:97], v[106:109], a[104:107], v[94:97]
ds_read_b64_tr_b16 a[126:127], v255 offset:57792
global_load_lds_dwordx4 v1, s[40:41] offset:-3072
v_mfma_f32_16x16x32_bf16 v[102:105], v[106:109], a[248:251], v[102:105]
ds_read_b64_tr_b16 a[128:129], v255 offset:55680
v_mfma_f32_16x16x32_bf16 v[110:113], a[96:99], v[166:169], v[110:113]
ds_read_b64_tr_b16 a[130:131], v255 offset:57984
global_load_lds_dwordx4 v2, s[40:41] offset:-2048
v_mfma_f32_16x16x32_bf16 v[114:117], a[100:103], v[166:169], v[114:117]
ds_read_b64_tr_b16 v[54:55], v163 offset:55296
v_mfma_f32_16x16x32_bf16 v[118:121], v[166:169], a[104:107], v[118:121]
global_load_lds_dwordx4 v3, s[40:41] offset:-1024
ds_read_b64_tr_b16 v[56:57], v163 offset:57600
v_mfma_f32_16x16x32_bf16 v[134:137], v[166:169], a[248:251], v[134:137]
ds_read_b128 v[182:185], v51 offset:10240
global_load_lds_dwordx4 v4, s[40:41] offset:0
v_mfma_f32_16x16x32_bf16 v[122:125], a[96:99], v[174:177], v[122:125]
ds_read_b128 v[190:193], v51 offset:28672
v_mfma_f32_16x16x32_bf16 v[126:129], a[100:103], v[174:177], v[126:129]
ds_read_b64_tr_b16 a[132:133], v162 offset:8192
global_load_lds_dwordx4 v5, s[40:41] offset:1024
v_mfma_f32_16x16x32_bf16 v[130:133], v[174:177], a[104:107], v[130:133]
ds_read_b64_tr_b16 a[134:135], v162 offset:10496
v_mfma_f32_16x16x32_bf16 v[158:161], v[174:177], a[248:251], v[158:161]
global_load_lds_dwordx4 v6, s[40:41] offset:2048
ds_read_b64_tr_b16 a[136:137], v162 offset:8384
v_mfma_f32_16x16x32_bf16 v[86:89], a[108:111], v[106:109], v[86:89]
ds_read_b64_tr_b16 a[138:139], v162 offset:10688
v_mfma_f32_16x16x32_bf16 v[90:93], a[112:115], v[106:109], v[90:93]
global_load_lds_dwordx4 v7, s[40:41] offset:3072
ds_read_b64_tr_b16 a[140:141], v162 offset:8576
v_mfma_f32_16x16x32_bf16 v[94:97], v[106:109], a[116:119], v[94:97]
s_add_u32 m0, s48, 49152
ds_read_b64_tr_b16 a[142:143], v162 offset:10880
v_mfma_f32_16x16x32_bf16 v[102:105], v[106:109], a[252:255], v[102:105]
ds_read_b64_tr_b16 v[58:59], v164 offset:8192
v_mfma_f32_16x16x32_bf16 v[110:113], a[108:111], v[170:173], v[110:113]
global_load_lds_dwordx4 v8, s[40:41] offset:-4096
ds_read_b64_tr_b16 v[60:61], v164 offset:10496
v_mfma_f32_16x16x32_bf16 v[114:117], a[112:115], v[170:173], v[114:117]
ds_read_b128 v[186:189], v52 offset:10240
v_mfma_f32_16x16x32_bf16 v[118:121], v[170:173], a[116:119], v[118:121]
s_add_u32 s40, s40, 221184
ds_read_b128 v[194:197], v52 offset:28672
v_mfma_f32_16x16x32_bf16 v[134:137], v[170:173], a[252:255], v[134:137]
v_mfma_f32_16x16x32_bf16 v[122:125], a[108:111], v[178:181], v[122:125]
s_addc_u32 s41, s41, 0
v_mfma_f32_16x16x32_bf16 v[126:129], a[112:115], v[178:181], v[126:129]
v_mfma_f32_16x16x32_bf16 v[130:133], v[178:181], a[116:119], v[130:133]
v_mfma_f32_16x16x32_bf16 v[158:161], v[178:181], a[252:255], v[158:161]
s_waitcnt vmcnt(9) lgkmcnt(0)
s_barrier
s_add_u32 m0, s48, 77824
ds_read_b64_tr_b16 a[144:145], v9 offset:47104
v_mfma_f32_16x16x32_bf16 v[86:89], a[120:123], v[106:109], v[86:89]
ds_read_b64_tr_b16 a[146:147], v9 offset:49408
v_mfma_f32_16x16x32_bf16 v[90:93], a[124:127], v[106:109], v[90:93]
global_load_lds_dwordx4 v0, s[40:41] offset:-4096
ds_read_b64_tr_b16 a[148:149], v9 offset:47296
v_mfma_f32_16x16x32_bf16 v[94:97], v[106:109], a[128:131], v[94:97]
ds_read_b64_tr_b16 a[150:151], v9 offset:49600
global_load_lds_dwordx4 v1, s[40:41] offset:-3072
v_mfma_f32_16x16x32_bf16 v[102:105], v[106:109], v[54:57], v[102:105]
ds_read_b64_tr_b16 a[152:153], v9 offset:47488
v_mfma_f32_16x16x32_bf16 v[110:113], a[120:123], v[182:185], v[110:113]
ds_read_b64_tr_b16 a[154:155], v9 offset:49792
global_load_lds_dwordx4 v2, s[40:41] offset:-2048
v_mfma_f32_16x16x32_bf16 v[114:117], a[124:127], v[182:185], v[114:117]
ds_read_b64_tr_b16 v[62:63], v10 offset:47104
v_mfma_f32_16x16x32_bf16 v[118:121], v[182:185], a[128:131], v[118:121]
global_load_lds_dwordx4 v3, s[40:41] offset:-1024
ds_read_b64_tr_b16 v[64:65], v10 offset:49408
v_mfma_f32_16x16x32_bf16 v[134:137], v[182:185], v[54:57], v[134:137]
ds_read_b128 v[166:169], v51 offset:12288
global_load_lds_dwordx4 v4, s[40:41] offset:0
v_mfma_f32_16x16x32_bf16 v[122:125], a[120:123], v[190:193], v[122:125]
ds_read_b128 v[174:177], v51 offset:30720
v_mfma_f32_16x16x32_bf16 v[126:129], a[124:127], v[190:193], v[126:129]
ds_read_b64_tr_b16 a[156:157], v255 offset:0
global_load_lds_dwordx4 v5, s[40:41] offset:1024
v_mfma_f32_16x16x32_bf16 v[130:133], v[190:193], a[128:131], v[130:133]
ds_read_b64_tr_b16 a[158:159], v255 offset:2304
v_mfma_f32_16x16x32_bf16 v[158:161], v[190:193], v[54:57], v[158:161]
global_load_lds_dwordx4 v6, s[40:41] offset:2048
)"
R"(ds_read_b64_tr_b16 a[160:161], v255 offset:192
v_mfma_f32_16x16x32_bf16 v[86:89], a[132:135], v[106:109], v[86:89]
ds_read_b64_tr_b16 a[162:163], v255 offset:2496
v_mfma_f32_16x16x32_bf16 v[90:93], a[136:139], v[106:109], v[90:93]
global_load_lds_dwordx4 v7, s[40:41] offset:3072
ds_read_b64_tr_b16 a[164:165], v255 offset:384
v_mfma_f32_16x16x32_bf16 v[94:97], v[106:109], a[140:143], v[94:97]
s_add_u32 m0, s48, 86016
ds_read_b64_tr_b16 a[166:167], v255 offset:2688
v_mfma_f32_16x16x32_bf16 v[102:105], v[106:109], v[58:61], v[102:105]
ds_read_b64_tr_b16 v[66:67], v163 offset:0
v_mfma_f32_16x16x32_bf16 v[110:113], a[132:135], v[186:189], v[110:113]
global_load_lds_dwordx4 v8, s[40:41] offset:-4096
ds_read_b64_tr_b16 v[68:69], v163 offset:2304
v_mfma_f32_16x16x32_bf16 v[114:117], a[136:139], v[186:189], v[114:117]
ds_read_b128 v[170:173], v52 offset:12288
v_mfma_f32_16x16x32_bf16 v[118:121], v[186:189], a[140:143], v[118:121]
s_sub_u32 s40, s40, 1769472
ds_read_b128 v[178:181], v52 offset:30720
v_mfma_f32_16x16x32_bf16 v[134:137], v[186:189], v[58:61], v[134:137]
v_mfma_f32_16x16x32_bf16 v[122:125], a[132:135], v[194:197], v[122:125]
s_subb_u32 s41, s41, 0
v_mfma_f32_16x16x32_bf16 v[126:129], a[136:139], v[194:197], v[126:129]
v_mfma_f32_16x16x32_bf16 v[130:133], v[194:197], a[140:143], v[130:133]
v_mfma_f32_16x16x32_bf16 v[158:161], v[194:197], v[58:61], v[158:161]
s_waitcnt vmcnt(9) lgkmcnt(0)
s_barrier
s_add_u32 m0, s49, 0
ds_read_b64_tr_b16 a[168:169], v255 offset:18432
v_mfma_f32_16x16x32_bf16 v[86:89], a[144:147], v[106:109], v[86:89]
ds_read_b64_tr_b16 a[170:171], v255 offset:20736
v_mfma_f32_16x16x32_bf16 v[90:93], a[148:151], v[106:109], v[90:93]
ds_read_b64_tr_b16 a[172:173], v255 offset:18624
v_mfma_f32_16x16x32_bf16 v[94:97], v[106:109], a[152:155], v[94:97]
ds_read_b64_tr_b16 a[174:175], v255 offset:20928
global_load_lds_dwordx4 v11, s[42:43] offset:-2048 sc1
v_mfma_f32_16x16x32_bf16 v[102:105], v[106:109], v[62:65], v[102:105]
ds_read_b64_tr_b16 a[176:177], v255 offset:18816
v_mfma_f32_16x16x32_bf16 v[110:113], a[144:147], v[166:169], v[110:113]
ds_read_b64_tr_b16 a[178:179], v255 offset:21120
v_mfma_f32_16x16x32_bf16 v[114:117], a[148:151], v[166:169], v[114:117]
ds_read_b64_tr_b16 v[70:71], v163 offset:18432
global_load_lds_dwordx4 v12, s[42:43] offset:-1024 sc1
v_mfma_f32_16x16x32_bf16 v[118:121], v[166:169], a[152:155], v[118:121]
ds_read_b64_tr_b16 v[72:73], v163 offset:20736
v_mfma_f32_16x16x32_bf16 v[134:137], v[166:169], v[62:65], v[134:137]
ds_read_b128 v[182:185], v51 offset:14336
v_mfma_f32_16x16x32_bf16 v[122:125], a[144:147], v[174:177], v[122:125]
ds_read_b128 v[190:193], v51 offset:32768
global_load_lds_dwordx4 v13, s[42:43] offset:0 sc1
v_mfma_f32_16x16x32_bf16 v[126:129], a[148:151], v[174:177], v[126:129]
ds_read_b64_tr_b16 a[180:181], v255 offset:36864
v_mfma_f32_16x16x32_bf16 v[130:133], v[174:177], a[152:155], v[130:133]
ds_read_b64_tr_b16 a[182:183], v255 offset:39168
v_mfma_f32_16x16x32_bf16 v[158:161], v[174:177], v[62:65], v[158:161]
ds_read_b64_tr_b16 a[184:185], v255 offset:37056
v_mfma_f32_16x16x32_bf16 v[86:89], a[156:159], v[106:109], v[86:89]
global_load_lds_dwordx4 v14, s[42:43] offset:1024 sc1
ds_read_b64_tr_b16 a[186:187], v255 offset:39360
v_mfma_f32_16x16x32_bf16 v[90:93], a[160:163], v[106:109], v[90:93]
ds_read_b64_tr_b16 a[188:189], v255 offset:37248
v_mfma_f32_16x16x32_bf16 v[94:97], v[106:109], a[164:167], v[94:97]
ds_read_b64_tr_b16 a[190:191], v255 offset:39552
v_mfma_f32_16x16x32_bf16 v[102:105], v[106:109], v[66:69], v[102:105]
global_load_lds_dwordx4 v15, s[42:43] offset:2048 sc1
ds_read_b64_tr_b16 v[74:75], v163 offset:36864
v_mfma_f32_16x16x32_bf16 v[110:113], a[156:159], v[170:173], v[110:113]
ds_read_b64_tr_b16 v[76:77], v163 offset:39168
v_mfma_f32_16x16x32_bf16 v[114:117], a[160:163], v[170:173], v[114:117]
ds_read_b128 v[186:189], v52 offset:14336
v_mfma_f32_16x16x32_bf16 v[118:121], v[170:173], a[164:167], v[118:121]
s_sub_u32 s42, s42, 18432
s_subb_u32 s43, s43, 0
ds_read_b128 v[194:197], v52 offset:32768
v_mfma_f32_16x16x32_bf16 v[134:137], v[170:173], v[66:69], v[134:137]
v_mfma_f32_16x16x32_bf16 v[122:125], a[156:159], v[178:181], v[122:125]
v_mfma_f32_16x16x32_bf16 v[126:129], a[160:163], v[178:181], v[126:129]
v_mfma_f32_16x16x32_bf16 v[130:133], v[178:181], a[164:167], v[130:133]
v_mfma_f32_16x16x32_bf16 v[158:161], v[178:181], v[66:69], v[158:161]
s_waitcnt vmcnt(5) lgkmcnt(0)
s_barrier
s_add_u32 m0, s49, 18432
ds_read_b64_tr_b16 a[192:193], v255 offset:55296
v_mfma_f32_16x16x32_bf16 v[86:89], a[168:171], v[106:109], v[86:89]
ds_read_b64_tr_b16 a[194:195], v255 offset:57600
v_mfma_f32_16x16x32_bf16 v[90:93], a[172:175], v[106:109], v[90:93]
ds_read_b64_tr_b16 a[196:197], v255 offset:55488
v_mfma_f32_16x16x32_bf16 v[94:97], v[106:109], a[176:179], v[94:97]
ds_read_b64_tr_b16 a[198:199], v255 offset:57792
global_load_lds_dwordx4 v11, s[42:43] offset:-2048 sc1
v_mfma_f32_16x16x32_bf16 v[102:105], v[106:109], v[70:73], v[102:105]
ds_read_b64_tr_b16 a[200:201], v255 offset:55680
v_mfma_f32_16x16x32_bf16 v[110:113], a[168:171], v[182:185], v[110:113]
ds_read_b64_tr_b16 a[202:203], v255 offset:57984
v_mfma_f32_16x16x32_bf16 v[114:117], a[172:175], v[182:185], v[114:117]
ds_read_b64_tr_b16 v[78:79], v163 offset:55296
global_load_lds_dwordx4 v12, s[42:43] offset:-1024 sc1
v_mfma_f32_16x16x32_bf16 v[118:121], v[182:185], a[176:179], v[118:121]
ds_read_b64_tr_b16 v[80:81], v163 offset:57600
v_mfma_f32_16x16x32_bf16 v[134:137], v[182:185], v[70:73], v[134:137]
ds_read_b128 v[166:169], v51 offset:16384
v_mfma_f32_16x16x32_bf16 v[122:125], a[168:171], v[190:193], v[122:125]
ds_read_b128 v[174:177], v51 offset:34816
global_load_lds_dwordx4 v13, s[42:43] offset:0 sc1
v_mfma_f32_16x16x32_bf16 v[126:129], a[172:175], v[190:193], v[126:129]
ds_read_b64_tr_b16 a[204:205], v162 offset:8192
v_mfma_f32_16x16x32_bf16 v[130:133], v[190:193], a[176:179], v[130:133]
ds_read_b64_tr_b16 a[206:207], v162 offset:10496
v_mfma_f32_16x16x32_bf16 v[158:161], v[190:193], v[70:73], v[158:161]
ds_read_b64_tr_b16 a[208:209], v162 offset:8384
global_load_lds_dwordx4 v14, s[42:43] offset:1024 sc1
v_mfma_f32_16x16x32_bf16 v[86:89], a[180:183], v[106:109], v[86:89]
ds_read_b64_tr_b16 a[210:211], v162 offset:10688
v_mfma_f32_16x16x32_bf16 v[90:93], a[184:187], v[106:109], v[90:93]
ds_read_b64_tr_b16 a[212:213], v162 offset:8576
v_mfma_f32_16x16x32_bf16 v[94:97], v[106:109], a[188:191], v[94:97]
ds_read_b64_tr_b16 a[214:215], v162 offset:10880
global_load_lds_dwordx4 v15, s[42:43] offset:2048 sc1
v_mfma_f32_16x16x32_bf16 v[102:105], v[106:109], v[74:77], v[102:105]
ds_read_b64_tr_b16 v[82:83], v164 offset:8192
v_mfma_f32_16x16x32_bf16 v[110:113], a[180:183], v[186:189], v[110:113]
ds_read_b64_tr_b16 v[84:85], v164 offset:10496
v_mfma_f32_16x16x32_bf16 v[114:117], a[184:187], v[186:189], v[114:117]
ds_read_b128 v[170:173], v52 offset:16384
s_sub_u32 s42, s42, 18432
s_subb_u32 s43, s43, 0
v_mfma_f32_16x16x32_bf16 v[118:121], v[186:189], a[188:191], v[118:121]
ds_read_b128 v[178:181], v52 offset:34816
v_mfma_f32_16x16x32_bf16 v[134:137], v[186:189], v[74:77], v[134:137]
v_mfma_f32_16x16x32_bf16 v[122:125], a[180:183], v[194:197], v[122:125]
v_mfma_f32_16x16x32_bf16 v[126:129], a[184:187], v[194:197], v[126:129]
v_mfma_f32_16x16x32_bf16 v[158:161], v[194:197], v[74:77], v[158:161]
s_waitcnt lgkmcnt(15)
v_mfma_f32_16x16x32_bf16 v[86:89], a[192:195], v[106:109], v[86:89]
v_mfma_f32_16x16x32_bf16 v[90:93], a[196:199], v[106:109], v[90:93]
s_waitcnt lgkmcnt(14)
v_mfma_f32_16x16x32_bf16 v[94:97], v[106:109], a[200:203], v[94:97]
s_waitcnt lgkmcnt(12)
v_mfma_f32_16x16x32_bf16 v[102:105], v[106:109], v[78:81], v[102:105]
s_waitcnt lgkmcnt(11)
v_mfma_f32_16x16x32_bf16 v[110:113], a[192:195], v[166:169], v[110:113]
v_mfma_f32_16x16x32_bf16 v[114:117], a[196:199], v[166:169], v[114:117]
v_mfma_f32_16x16x32_bf16 v[118:121], v[166:169], a[200:203], v[118:121]
v_mfma_f32_16x16x32_bf16 v[134:137], v[166:169], v[78:81], v[134:137]
s_waitcnt lgkmcnt(10)
v_mfma_f32_16x16x32_bf16 v[158:161], v[174:177], v[78:81], v[158:161]
s_waitcnt lgkmcnt(8)
v_mfma_f32_16x16x32_bf16 v[86:89], a[204:207], v[106:109], v[86:89]
s_waitcnt lgkmcnt(6)
v_mfma_f32_16x16x32_bf16 v[90:93], a[208:211], v[106:109], v[90:93]
s_waitcnt lgkmcnt(4)
v_mfma_f32_16x16x32_bf16 v[94:97], v[106:109], a[212:215], v[94:97]
s_waitcnt lgkmcnt(2)
v_mfma_f32_16x16x32_bf16 v[102:105], v[106:109], v[82:85], v[102:105]
s_waitcnt lgkmcnt(1)
v_mfma_f32_16x16x32_bf16 v[110:113], a[204:207], v[170:173], v[110:113]
v_mfma_f32_16x16x32_bf16 v[114:117], a[208:211], v[170:173], v[114:117]
v_mfma_f32_16x16x32_bf16 v[118:121], v[170:173], a[212:215], v[118:121]
v_mfma_f32_16x16x32_bf16 v[134:137], v[170:173], v[82:85], v[134:137]
s_waitcnt lgkmcnt(0)
v_mfma_f32_16x16x32_bf16 v[158:161], v[178:181], v[82:85], v[158:161]
s_barrier
ds_read_b128 v[242:245], v25 offset:0
s_waitcnt lgkmcnt(0)
s_nop 2
v_fma_f32 v230, v102, v242, v134
v_fma_f32 v231, v102, v243, v135
v_fma_f32 v232, v102, v244, v136
v_fma_f32 v233, v102, v245, v137
v_cvt_pk_bf16_f32 v230, v230, v231
v_cvt_pk_bf16_f32 v231, v232, v233
ds_write_b64 v22, v[230:231] offset:41472
ds_read_b128 v[198:201], v51 offset:32768
ds_read_b128 v[202:205], v52 offset:32768
ds_read_b128 v[206:209], v51 offset:34816
ds_read_b128 v[210:213], v52 offset:34816
v_mov_b32 v234, v32
v_mov_b32 v235, v33
v_mov_b32 v236, v34
v_mov_b32 v237, v35
v_mov_b32 v134, v158
v_mov_b32 v135, v159
v_mov_b32 v136, v160
v_mov_b32 v137, v161
s_waitcnt lgkmcnt(4)
s_barrier
ds_read_b32 v23, v24 offset:0
ds_read_b32 v148, v21
ds_read_b32 v26, v24 offset:192
ds_read_b32 v53, v27
ds_read_b32 v95, v28
ds_read_b128 v[226:229], v25 offset:0
ds_read_b64 v[158:159], v22 offset:41472
ds_read_b32 v140, v20
v_xor_b32 v51, 0x10000, v51
v_xor_b32 v52, 0x10000, v52
s_waitcnt lgkmcnt(7)
v_fma_f32 v238, v90, v23, v114
v_fma_f32 v239, v91, v23, v115
v_fma_f32 v240, v92, v23, v116
v_fma_f32 v241, v93, v23, v117
v_cvt_pk_bf16_f32 v146, v238, v239
v_cvt_pk_bf16_f32 v147, v240, v241
s_waitcnt lgkmcnt(0)
s_barrier
ds_read_b128 v[242:245], v25 offset:64
v_fma_f32 v238, v94, v226, v118
v_fma_f32 v239, v94, v227, v119
v_fma_f32 v240, v94, v228, v120
v_fma_f32 v241, v94, v229, v121
v_cvt_pk_bf16_f32 v154, v238, v239
v_cvt_pk_bf16_f32 v155, v240, v241
v_fma_f32 v238, v86, v23, v110
v_fma_f32 v239, v87, v23, v111
v_fma_f32 v240, v88, v23, v112
v_fma_f32 v241, v89, v23, v113
v_cvt_pk_bf16_f32 v138, v238, v239
v_cvt_pk_bf16_f32 v139, v240, v241
s_waitcnt lgkmcnt(0)
v_fma_f32 v230, v102, v242, v134
v_fma_f32 v231, v102, v243, v135
v_fma_f32 v232, v102, v244, v136
v_fma_f32 v233, v102, v245, v137
v_cvt_pk_bf16_f32 v230, v230, v231
v_cvt_pk_bf16_f32 v231, v232, v233
ds_write_b64 v22, v[230:231] offset:41472
s_waitcnt vmcnt(5) lgkmcnt(0)
s_barrier
s_mov_b32 s70, 32
s_waitcnt lgkmcnt(0)
s_nop 1
s_branch .Lqa_in16
.p2align 6
.Lqa_top26:
s_waitcnt lgkmcnt(9)
v_mfma_f32_16x16x32_bf16 v[134:137], v[44:47], a[216:219], 0
s_waitcnt lgkmcnt(8)
ds_read_b128 v[162:165], v52 offset:4096
ds_read_b32 v95, v30
v_mfma_f32_16x16x32_bf16 v[134:137], v[96:99], a[220:223], v[134:137]
ds_read_b128 v[226:229], v25 offset:64
ds_read_b64 v[160:161], v22 offset:41472
v_mfma_f32_16x16x32_bf16 v[110:113], a[0:3], v[44:47], 0
s_waitcnt lgkmcnt(8)
ds_read_b128 v[166:169], v51 offset:6144
ds_read_b32 v144, v20
v_mfma_f32_16x16x32_bf16 v[134:137], v[104:107], a[224:227], v[134:137]
s_waitcnt lgkmcnt(8)
ds_read_b128 v[170:173], v52 offset:6144
v_fma_f32 v238, v90, v23, v126
v_mfma_f32_16x16x32_bf16 v[134:137], v[0:3], a[228:231], v[134:137]
global_store_dwordx4 v16, v[230:233], s[38:39] sc1
v_mfma_f32_16x16x32_bf16 v[114:117], a[4:7], v[44:47], 0
s_waitcnt lgkmcnt(7)
ds_read_b128 v[174:177], v51 offset:8192
v_add_u32 v16, 18432, v16
v_mfma_f32_16x16x32_bf16 v[134:137], v[4:7], a[232:235], v[134:137]
s_waitcnt lgkmcnt(7)
ds_read_b128 v[178:181], v52 offset:8192
v_fma_f32 v239, v91, v23, v127
v_mfma_f32_16x16x32_bf16 v[134:137], v[162:165], a[236:239], v[134:137]
v_fma_f32 v240, v92, v23, v128
v_fma_f32 v241, v93, v23, v129
v_mfma_f32_16x16x32_bf16 v[118:121], v[44:47], a[8:11], 0
s_waitcnt lgkmcnt(4)
ds_read_b128 v[182:185], v51 offset:10240
v_cvt_pk_bf16_f32 v150, v238, v239
v_mfma_f32_16x16x32_bf16 v[134:137], v[166:169], a[240:243], v[134:137]
s_waitcnt lgkmcnt(3)
ds_read_b128 v[186:189], v52 offset:10240
v_cvt_pk_bf16_f32 v151, v240, v241
v_mfma_f32_16x16x32_bf16 v[134:137], v[170:173], a[244:247], v[134:137]
s_nop 0
v_mfma_f32_16x16x32_bf16 v[214:217], v[150:153], v[138:141], v[32:35]
v_max_f32 v109, v218, v219
v_max_f32 v254, v220, v221
v_mfma_f32_16x16x32_bf16 v[110:113], a[12:15], v[96:99], v[110:113]
s_waitcnt lgkmcnt(3)
ds_read_b128 v[190:193], v51 offset:12288
v_fma_f32 v238, v94, v226, v130
v_mfma_f32_16x16x32_bf16 v[134:137], v[174:177], a[248:251], v[134:137]
s_xor_b32 s49, s49, 0x10000
ds_read_b128 v[194:197], v52 offset:12288
s_waitcnt lgkmcnt(4)
v_mfma_f32_16x16x32_bf16 v[134:137], v[178:181], a[252:255], v[134:137]
s_add_u32 m0, s49, 0
v_mul_f32 v26, 0x3eae1a93, v26
v_mul_f32 v103, v53, v26
v_mfma_f32_16x16x32_bf16 v[114:117], a[16:19], v[96:99], v[114:117]
s_waitcnt lgkmcnt(3)
ds_read_b128 v[198:201], v51 offset:14336
v_mul_f32 v108, v95, v26
v_mfma_f32_16x16x32_bf16 v[134:137], v[182:185], v[54:57], v[134:137]
s_waitcnt lgkmcnt(3)
ds_read_b128 v[202:205], v52 offset:14336
v_max3_f32 v109, v214, v215, v109
v_mfma_f32_16x16x32_bf16 v[134:137], v[186:189], v[58:61], v[134:137]
global_load_lds_dwordx4 v11, s[42:43] offset:-2048 sc1
v_max3_f32 v254, v216, v217, v254
v_mfma_f32_16x16x32_bf16 v[118:121], v[96:99], a[20:23], v[118:121]
s_waitcnt lgkmcnt(3)
ds_read_b128 v[206:209], v51 offset:16384
v_mul_f32 v109, v109, v103
v_mfma_f32_16x16x32_bf16 v[134:137], v[190:193], v[62:65], v[134:137]
s_waitcnt lgkmcnt(3)
ds_read_b128 v[210:213], v52 offset:16384
v_mul_f32 v254, v254, v108
v_mfma_f32_16x16x32_bf16 v[134:137], v[194:197], v[66:69], v[134:137]
v_max_f32 v255, v109, v254
v_mov_b32 v8, v255
v_mfma_f32_16x16x32_bf16 v[110:113], a[24:27], v[104:107], v[110:113]
s_waitcnt lgkmcnt(3)
v_permlane16_swap_b32 v255, v8
v_mfma_f32_16x16x32_bf16 v[134:137], v[198:201], v[70:73], v[134:137]
s_waitcnt lgkmcnt(2)
v_max_f32 v255, v255, v8
v_mov_b32 v8, v255
v_mfma_f32_16x16x32_bf16 v[134:137], v[202:205], v[74:77], v[134:137]
s_nop 0
v_permlane32_swap_b32 v255, v8
v_mfma_f32_16x16x32_bf16 v[114:117], a[28:31], v[104:107], v[114:117]
s_waitcnt lgkmcnt(1)
v_max_f32 v255, v255, v8
v_sub_f32 v109, v109, v255
v_mfma_f32_16x16x32_bf16 v[134:137], v[206:209], v[78:81], v[134:137]
s_waitcnt lgkmcnt(0)
global_load_lds_dwordx4 v12, s[42:43] offset:-1024 sc1
v_sub_f32 v254, v254, v255
v_mfma_f32_16x16x32_bf16 v[134:137], v[210:213], v[82:85], v[134:137]
v_exp_f32 v9, v109
v_mfma_f32_16x16x32_bf16 v[118:121], v[104:107], a[32:35], v[118:121]
v_exp_f32 v10, v254
v_mfma_f32_16x16x32_bf16 v[110:113], a[36:39], v[0:3], v[110:113]
v_add_f32 v255, v9, v10
v_mov_b32 v8, v255
v_mfma_f32_16x16x32_bf16 v[114:117], a[40:43], v[0:3], v[114:117]
v_mul_f32 v9, v9, v53
v_fma_f32 v239, v94, v227, v131
v_mfma_f32_16x16x32_bf16 v[118:121], v[0:3], a[44:47], v[118:121]
v_permlane16_swap_b32 v255, v8
v_mfma_f32_16x16x32_bf16 v[110:113], a[48:51], v[4:7], v[110:113]
v_add_f32 v255, v255, v8
v_mov_b32 v8, v255
v_mfma_f32_16x16x32_bf16 v[114:117], a[52:55], v[4:7], v[114:117]
v_mul_f32 v10, v10, v95
v_fma_f32 v240, v94, v228, v132
v_mfma_f32_16x16x32_bf16 v[118:121], v[4:7], a[56:59], v[118:121]
global_load_lds_dwordx4 v13, s[42:43] offset:0 sc1
v_fma_f32 v241, v94, v229, v133
v_mfma_f32_16x16x32_bf16 v[110:113], a[60:63], v[162:165], v[110:113]
v_permlane32_swap_b32 v255, v8
v_mfma_f32_16x16x32_bf16 v[114:117], a[64:67], v[162:165], v[114:117]
v_add_f32 v255, v255, v8
v_cvt_pk_bf16_f32 v156, v238, v239
v_mfma_f32_16x16x32_bf16 v[118:121], v[162:165], a[68:71], v[118:121]
v_mfma_f32_16x16x32_bf16 v[110:113], a[72:75], v[166:169], v[110:113]
s_barrier
v_mfma_f32_16x16x32_bf16 v[114:117], a[76:79], v[166:169], v[114:117]
ds_read_b128 v[242:245], v25 offset:0
v_cvt_pk_bf16_f32 v157, v240, v241
v_mfma_f32_16x16x32_bf16 v[118:121], v[166:169], a[80:83], v[118:121]
v_rcp_f32 v255, v255
v_mfma_f32_16x16x32_bf16 v[110:113], a[84:87], v[170:173], v[110:113]
v_mul_f32 v9, v9, v255
v_mul_f32 v10, v10, v255
v_mfma_f32_16x16x32_bf16 v[114:117], a[88:91], v[170:173], v[114:117]
global_load_lds_dwordx4 v14, s[42:43] offset:1024 sc1
v_cvt_pk_bf16_f32 v9, v9, v9
v_mfma_f32_16x16x32_bf16 v[118:121], v[170:173], a[92:95], v[118:121]
v_cvt_pk_bf16_f32 v10, v10, v10
v_and_b32 v224, v9, v40
v_mfma_f32_16x16x32_bf16 v[110:113], a[96:99], v[174:177], v[110:113]
v_and_b32 v225, v10, v41
v_and_b32 v222, v9, v42
v_mfma_f32_16x16x32_bf16 v[114:117], a[100:103], v[174:177], v[114:117]
s_waitcnt lgkmcnt(0)
v_and_b32 v223, v10, v43
v_fma_f32 v230, v102, v242, v134
v_mfma_f32_16x16x32_bf16 v[118:121], v[174:177], a[104:107], v[118:121]
v_mfma_f32_16x16x32_bf16 v[218:221], v[154:157], v[222:225], 0
v_mfma_f32_16x16x32_bf16 v[214:217], v[158:161], v[222:225], 0
v_fma_f32 v231, v102, v243, v135
v_fma_f32 v232, v102, v244, v136
v_mfma_f32_16x16x32_bf16 v[110:113], a[108:111], v[178:181], v[110:113]
v_fma_f32 v233, v102, v245, v137
v_cvt_pk_bf16_f32 v230, v230, v231
v_mfma_f32_16x16x32_bf16 v[114:117], a[112:115], v[178:181], v[114:117]
v_cvt_pk_bf16_f32 v231, v232, v233
v_fma_f32 v238, v86, v23, v122
v_mfma_f32_16x16x32_bf16 v[118:121], v[178:181], a[116:119], v[118:121]
ds_write_b64 v22, v[230:231] offset:41472
v_mfma_f32_16x16x32_bf16 v[110:113], a[120:123], v[182:185], v[110:113]
global_load_lds_dwordx4 v15, s[42:43] offset:2048 sc1
s_add_u32 s42, s42, s46
s_addc_u32 s43, s43, s47
v_mfma_f32_16x16x32_bf16 v[114:117], a[124:127], v[182:185], v[114:117]
v_cvt_pk_bf16_f32 v100, v218, v219
v_cvt_pk_bf16_f32 v101, v220, v221
v_mfma_f32_16x16x32_bf16 v[118:121], v[182:185], a[128:131], v[118:121]
ds_write_b64 v18, v[100:101]
)"
R"(v_mfma_f32_16x16x32_bf16 v[110:113], a[132:135], v[186:189], v[110:113]
v_cvt_pk_bf16_f32 v9, v216, v217
v_fma_f32 v239, v87, v23, v123
v_mfma_f32_16x16x32_bf16 v[114:117], a[136:139], v[186:189], v[114:117]
ds_write_b32 v19, v9
v_mfma_f32_16x16x32_bf16 v[118:121], v[186:189], a[140:143], v[118:121]
v_fma_f32 v240, v88, v23, v124
v_fma_f32 v241, v89, v23, v125
v_mfma_f32_16x16x32_bf16 v[110:113], a[144:147], v[190:193], v[110:113]
v_cvt_pk_bf16_f32 v142, v238, v239
v_cvt_pk_bf16_f32 v143, v240, v241
v_mfma_f32_16x16x32_bf16 v[114:117], a[148:151], v[190:193], v[114:117]
v_mfma_f32_16x16x32_bf16 v[118:121], v[190:193], a[152:155], v[118:121]
v_mfma_f32_16x16x32_bf16 v[110:113], a[156:159], v[194:197], v[110:113]
v_mfma_f32_16x16x32_bf16 v[114:117], a[160:163], v[194:197], v[114:117]
v_mfma_f32_16x16x32_bf16 v[118:121], v[194:197], a[164:167], v[118:121]
v_mfma_f32_16x16x32_bf16 v[110:113], a[168:171], v[198:201], v[110:113]
v_mfma_f32_16x16x32_bf16 v[114:117], a[172:175], v[198:201], v[114:117]
v_mfma_f32_16x16x32_bf16 v[118:121], v[198:201], a[176:179], v[118:121]
v_mfma_f32_16x16x32_bf16 v[110:113], a[180:183], v[202:205], v[110:113]
v_mfma_f32_16x16x32_bf16 v[114:117], a[184:187], v[202:205], v[114:117]
s_waitcnt vmcnt(5) lgkmcnt(0)
s_barrier
v_mfma_f32_16x16x32_bf16 v[118:121], v[202:205], a[188:191], v[118:121]
ds_read_b128 v[44:47], v51 offset:18432
v_mfma_f32_16x16x32_bf16 v[110:113], a[192:195], v[206:209], v[110:113]
ds_read_b128 v[96:99], v52 offset:18432
ds_read_b128 v[230:233], v17 offset:0
v_mfma_f32_16x16x32_bf16 v[114:117], a[196:199], v[206:209], v[114:117]
ds_read_b32 v23, v24 offset:0
ds_read_b32 v148, v21
v_mfma_f32_16x16x32_bf16 v[218:221], v[150:153], v[142:145], v[36:39]
v_mfma_f32_16x16x32_bf16 v[118:121], v[206:209], a[200:203], v[118:121]
ds_read_b128 v[104:107], v51 offset:20480
ds_read_b32 v26, v24 offset:192
v_mfma_f32_16x16x32_bf16 v[110:113], a[204:207], v[210:213], v[110:113]
ds_read_b128 v[0:3], v52 offset:20480
ds_read_b32 v53, v27
v_mfma_f32_16x16x32_bf16 v[114:117], a[208:211], v[210:213], v[114:117]
ds_read_b32 v95, v28
ds_read_b128 v[226:229], v25 offset:0
v_mfma_f32_16x16x32_bf16 v[118:121], v[210:213], a[212:215], v[118:121]
s_waitcnt lgkmcnt(10)
ds_read_b128 v[4:7], v51 offset:22528
ds_read_b64 v[158:159], v22 offset:41472
v_mfma_f32_16x16x32_bf16 v[134:137], v[44:47], a[216:219], 0
s_waitcnt lgkmcnt(11)
ds_read_b128 v[162:165], v52 offset:22528
ds_read_b32 v140, v20
v_mfma_f32_16x16x32_bf16 v[134:137], v[96:99], a[220:223], v[134:137]
v_max_f32 v109, v218, v219
v_max_f32 v254, v220, v221
v_mfma_f32_16x16x32_bf16 v[122:125], a[0:3], v[44:47], 0
s_waitcnt lgkmcnt(11)
ds_read_b128 v[166:169], v51 offset:24576
v_fma_f32 v238, v90, v23, v114
s_waitcnt lgkmcnt(10)
v_mfma_f32_16x16x32_bf16 v[134:137], v[104:107], a[224:227], v[134:137]
s_waitcnt lgkmcnt(8)
ds_read_b128 v[170:173], v52 offset:24576
v_fma_f32 v239, v91, v23, v115
v_mfma_f32_16x16x32_bf16 v[134:137], v[0:3], a[228:231], v[134:137]
global_store_dwordx4 v16, v[230:233], s[38:39] sc1
v_mfma_f32_16x16x32_bf16 v[126:129], a[4:7], v[44:47], 0
s_waitcnt lgkmcnt(5)
ds_read_b128 v[174:177], v51 offset:26624
v_add_u32 v16, 18432, v16
v_mfma_f32_16x16x32_bf16 v[134:137], v[4:7], a[232:235], v[134:137]
s_waitcnt lgkmcnt(4)
ds_read_b128 v[178:181], v52 offset:26624
v_fma_f32 v240, v92, v23, v116
v_mfma_f32_16x16x32_bf16 v[134:137], v[162:165], a[236:239], v[134:137]
v_fma_f32 v241, v93, v23, v117
v_cvt_pk_bf16_f32 v146, v238, v239
v_mfma_f32_16x16x32_bf16 v[130:133], v[44:47], a[8:11], 0
s_waitcnt lgkmcnt(3)
ds_read_b128 v[182:185], v51 offset:28672
v_cvt_pk_bf16_f32 v147, v240, v241
v_mfma_f32_16x16x32_bf16 v[134:137], v[166:169], a[240:243], v[134:137]
ds_read_b128 v[186:189], v52 offset:28672
v_mfma_f32_16x16x32_bf16 v[214:217], v[146:149], v[142:145], v[234:237]
s_waitcnt lgkmcnt(4)
v_mul_f32 v26, 0x3eae1a93, v26
v_mul_f32 v103, v53, v26
v_mfma_f32_16x16x32_bf16 v[134:137], v[170:173], a[244:247], v[134:137]
v_mul_f32 v108, v95, v26
v_fma_f32 v238, v94, v226, v118
v_mfma_f32_16x16x32_bf16 v[122:125], a[12:15], v[96:99], v[122:125]
s_waitcnt lgkmcnt(3)
ds_read_b128 v[190:193], v51 offset:30720
v_fma_f32 v239, v94, v227, v119
v_mfma_f32_16x16x32_bf16 v[134:137], v[174:177], a[248:251], v[134:137]
s_add_u32 m0, s49, 18432
ds_read_b128 v[194:197], v52 offset:30720
s_waitcnt lgkmcnt(4)
v_mfma_f32_16x16x32_bf16 v[134:137], v[178:181], a[252:255], v[134:137]
v_max3_f32 v109, v214, v215, v109
v_max3_f32 v254, v216, v217, v254
v_mfma_f32_16x16x32_bf16 v[126:129], a[16:19], v[96:99], v[126:129]
s_waitcnt lgkmcnt(3)
ds_read_b128 v[198:201], v51 offset:32768
v_mul_f32 v109, v109, v103
v_mfma_f32_16x16x32_bf16 v[134:137], v[182:185], v[54:57], v[134:137]
s_waitcnt lgkmcnt(3)
ds_read_b128 v[202:205], v52 offset:32768
global_load_lds_dwordx4 v11, s[42:43] offset:-2048 sc1
v_mfma_f32_16x16x32_bf16 v[134:137], v[186:189], v[58:61], v[134:137]
v_mul_f32 v254, v254, v108
v_max_f32 v255, v109, v254
v_mfma_f32_16x16x32_bf16 v[130:133], v[96:99], a[20:23], v[130:133]
s_waitcnt lgkmcnt(3)
ds_read_b128 v[206:209], v51 offset:34816
v_mov_b32 v8, v255
v_mfma_f32_16x16x32_bf16 v[134:137], v[190:193], v[62:65], v[134:137]
s_waitcnt lgkmcnt(3)
ds_read_b128 v[210:213], v52 offset:34816
v_fma_f32 v240, v94, v228, v120
v_mfma_f32_16x16x32_bf16 v[134:137], v[194:197], v[66:69], v[134:137]
v_permlane16_swap_b32 v255, v8
v_mfma_f32_16x16x32_bf16 v[122:125], a[24:27], v[104:107], v[122:125]
s_waitcnt lgkmcnt(3)
v_max_f32 v255, v255, v8
v_mov_b32 v8, v255
v_mfma_f32_16x16x32_bf16 v[134:137], v[198:201], v[70:73], v[134:137]
s_waitcnt lgkmcnt(2)
v_permlane32_swap_b32 v255, v8
v_mfma_f32_16x16x32_bf16 v[134:137], v[202:205], v[74:77], v[134:137]
v_max_f32 v255, v255, v8
v_sub_f32 v109, v109, v255
v_mfma_f32_16x16x32_bf16 v[126:129], a[28:31], v[104:107], v[126:129]
s_waitcnt lgkmcnt(1)
global_load_lds_dwordx4 v12, s[42:43] offset:-1024 sc1
v_sub_f32 v254, v254, v255
v_mfma_f32_16x16x32_bf16 v[134:137], v[206:209], v[78:81], v[134:137]
s_waitcnt lgkmcnt(0)
v_exp_f32 v9, v109
v_mfma_f32_16x16x32_bf16 v[134:137], v[210:213], v[82:85], v[134:137]
v_exp_f32 v10, v254
v_mfma_f32_16x16x32_bf16 v[130:133], v[104:107], a[32:35], v[130:133]
v_add_f32 v255, v9, v10
v_mov_b32 v8, v255
v_mfma_f32_16x16x32_bf16 v[122:125], a[36:39], v[0:3], v[122:125]
v_mul_f32 v9, v9, v53
v_fma_f32 v241, v94, v229, v121
v_mfma_f32_16x16x32_bf16 v[126:129], a[40:43], v[0:3], v[126:129]
v_permlane16_swap_b32 v255, v8
v_mfma_f32_16x16x32_bf16 v[130:133], v[0:3], a[44:47], v[130:133]
v_add_f32 v255, v255, v8
v_mov_b32 v8, v255
v_mfma_f32_16x16x32_bf16 v[122:125], a[48:51], v[4:7], v[122:125]
v_mul_f32 v10, v10, v95
v_cvt_pk_bf16_f32 v154, v238, v239
v_mfma_f32_16x16x32_bf16 v[126:129], a[52:55], v[4:7], v[126:129]
global_load_lds_dwordx4 v13, s[42:43] offset:0 sc1
v_cvt_pk_bf16_f32 v155, v240, v241
v_mfma_f32_16x16x32_bf16 v[130:133], v[4:7], a[56:59], v[130:133]
v_permlane32_swap_b32 v255, v8
v_mfma_f32_16x16x32_bf16 v[122:125], a[60:63], v[162:165], v[122:125]
v_add_f32 v255, v255, v8
v_fma_f32 v238, v86, v23, v110
v_mfma_f32_16x16x32_bf16 v[126:129], a[64:67], v[162:165], v[126:129]
v_rcp_f32 v255, v255
v_mfma_f32_16x16x32_bf16 v[130:133], v[162:165], a[68:71], v[130:133]
v_mfma_f32_16x16x32_bf16 v[122:125], a[72:75], v[166:169], v[122:125]
s_barrier
v_mfma_f32_16x16x32_bf16 v[126:129], a[76:79], v[166:169], v[126:129]
ds_read_b128 v[242:245], v25 offset:64
v_mul_f32 v9, v9, v255
v_mfma_f32_16x16x32_bf16 v[130:133], v[166:169], a[80:83], v[130:133]
v_mul_f32 v10, v10, v255
v_cvt_pk_bf16_f32 v9, v9, v9
v_mfma_f32_16x16x32_bf16 v[122:125], a[84:87], v[170:173], v[122:125]
global_load_lds_dwordx4 v14, s[42:43] offset:1024 sc1
v_cvt_pk_bf16_f32 v10, v10, v10
v_mfma_f32_16x16x32_bf16 v[126:129], a[88:91], v[170:173], v[126:129]
v_and_b32 v222, v9, v40
v_and_b32 v223, v10, v41
v_mfma_f32_16x16x32_bf16 v[130:133], v[170:173], a[92:95], v[130:133]
v_and_b32 v224, v9, v42
v_and_b32 v225, v10, v43
v_mfma_f32_16x16x32_bf16 v[122:125], a[96:99], v[174:177], v[122:125]
s_nop 0
v_mfma_f32_16x16x32_bf16 v[218:221], v[154:157], v[222:225], 0
v_mfma_f32_16x16x32_bf16 v[214:217], v[158:161], v[222:225], 0
v_fma_f32 v239, v87, v23, v111
v_fma_f32 v240, v88, v23, v112
v_mfma_f32_16x16x32_bf16 v[126:129], a[100:103], v[174:177], v[126:129]
s_waitcnt lgkmcnt(0)
v_fma_f32 v230, v102, v242, v134
v_fma_f32 v231, v102, v243, v135
v_mfma_f32_16x16x32_bf16 v[130:133], v[174:177], a[104:107], v[130:133]
v_fma_f32 v232, v102, v244, v136
v_fma_f32 v233, v102, v245, v137
v_mfma_f32_16x16x32_bf16 v[122:125], a[108:111], v[178:181], v[122:125]
v_cvt_pk_bf16_f32 v230, v230, v231
v_cvt_pk_bf16_f32 v231, v232, v233
v_mfma_f32_16x16x32_bf16 v[126:129], a[112:115], v[178:181], v[126:129]
v_cvt_pk_bf16_f32 v100, v218, v219
v_cvt_pk_bf16_f32 v101, v220, v221
v_mfma_f32_16x16x32_bf16 v[130:133], v[178:181], a[116:119], v[130:133]
global_load_lds_dwordx4 v15, s[42:43] offset:2048 sc1
s_sub_u32 s42, s42, 18432
s_subb_u32 s43, s43, 0
v_mfma_f32_16x16x32_bf16 v[122:125], a[120:123], v[182:185], v[122:125]
ds_write_b64 v18, v[100:101]
v_mfma_f32_16x16x32_bf16 v[126:129], a[124:127], v[182:185], v[126:129]
v_cvt_pk_bf16_f32 v9, v216, v217
v_fma_f32 v241, v89, v23, v113
v_mfma_f32_16x16x32_bf16 v[130:133], v[182:185], a[128:131], v[130:133]
ds_write_b32 v19, v9
v_mfma_f32_16x16x32_bf16 v[122:125], a[132:135], v[186:189], v[122:125]
ds_write_b64 v22, v[230:231] offset:41472
v_mfma_f32_16x16x32_bf16 v[126:129], a[136:139], v[186:189], v[126:129]
v_cvt_pk_bf16_f32 v138, v238, v239
v_cvt_pk_bf16_f32 v139, v240, v241
v_mfma_f32_16x16x32_bf16 v[130:133], v[186:189], a[140:143], v[130:133]
v_mfma_f32_16x16x32_bf16 v[122:125], a[144:147], v[190:193], v[122:125]
v_mfma_f32_16x16x32_bf16 v[126:129], a[148:151], v[190:193], v[126:129]
v_mfma_f32_16x16x32_bf16 v[130:133], v[190:193], a[152:155], v[130:133]
v_mfma_f32_16x16x32_bf16 v[122:125], a[156:159], v[194:197], v[122:125]
v_mfma_f32_16x16x32_bf16 v[126:129], a[160:163], v[194:197], v[126:129]
v_mfma_f32_16x16x32_bf16 v[130:133], v[194:197], a[164:167], v[130:133]
v_mfma_f32_16x16x32_bf16 v[122:125], a[168:171], v[198:201], v[122:125]
v_mfma_f32_16x16x32_bf16 v[126:129], a[172:175], v[198:201], v[126:129]
v_xor_b32 v51, 0x10000, v51
v_xor_b32 v52, 0x10000, v52
v_mfma_f32_16x16x32_bf16 v[130:133], v[198:201], a[176:179], v[130:133]
v_mfma_f32_16x16x32_bf16 v[122:125], a[180:183], v[202:205], v[122:125]
v_mfma_f32_16x16x32_bf16 v[126:129], a[184:187], v[202:205], v[126:129]
s_waitcnt vmcnt(5) lgkmcnt(0)
s_barrier
.Lqa_in16:
v_mfma_f32_16x16x32_bf16 v[130:133], v[202:205], a[188:191], v[130:133]
s_cmp_eq_u32 s70, s73
s_cbranch_scc1 .Lqa_hk17
.Lqa_hr18:
ds_read_b128 v[44:47], v51 offset:0
v_mfma_f32_16x16x32_bf16 v[122:125], a[192:195], v[206:209], v[122:125]
ds_read_b128 v[96:99], v52 offset:0
ds_read_b128 v[230:233], v17 offset:0
v_mfma_f32_16x16x32_bf16 v[126:129], a[196:199], v[206:209], v[126:129]
ds_read_b32 v23, v24 offset:64
ds_read_b32 v152, v21
v_mfma_f32_16x16x32_bf16 v[218:221], v[146:149], v[138:141], v[36:39]
v_mfma_f32_16x16x32_bf16 v[130:133], v[206:209], a[200:203], v[130:133]
ds_read_b128 v[104:107], v51 offset:2048
ds_read_b32 v26, v24 offset:128
v_mfma_f32_16x16x32_bf16 v[122:125], a[204:207], v[210:213], v[122:125]
ds_read_b128 v[0:3], v52 offset:2048
ds_read_b32 v53, v29
v_mfma_f32_16x16x32_bf16 v[126:129], a[208:211], v[210:213], v[126:129]
v_mfma_f32_16x16x32_bf16 v[130:133], v[210:213], a[212:215], v[130:133]
s_sub_u32 s70, s70, 1
ds_read_b128 v[4:7], v51 offset:4096
s_cbranch_scc0 .Lqa_top26
s_waitcnt lgkmcnt(0)
ds_read_b32 v95, v30
ds_read_b128 v[226:229], v25 offset:64
ds_read_b64 v[160:161], v22 offset:41472
ds_read_b32 v144, v20
v_fma_f32 v238, v90, v23, v126
global_store_dwordx4 v16, v[230:233], s[38:39] sc1
v_add_u32 v16, 18432, v16
v_fma_f32 v239, v91, v23, v127
v_fma_f32 v240, v92, v23, v128
v_fma_f32 v241, v93, v23, v129
v_cvt_pk_bf16_f32 v150, v238, v239
v_cvt_pk_bf16_f32 v151, v240, v241
s_nop 1
v_mfma_f32_16x16x32_bf16 v[214:217], v[150:153], v[138:141], v[32:35]
s_waitcnt lgkmcnt(2)
v_max_f32 v109, v218, v219
v_max_f32 v254, v220, v221
v_fma_f32 v238, v94, v226, v130
v_mul_f32 v26, 0x3eae1a93, v26
v_mul_f32 v103, v53, v26
v_mul_f32 v108, v95, v26
s_nop 0
v_max3_f32 v109, v214, v215, v109
v_max3_f32 v254, v216, v217, v254
v_mul_f32 v109, v109, v103
v_mul_f32 v254, v254, v108
v_max_f32 v255, v109, v254
v_mov_b32 v8, v255
s_nop 1
v_permlane16_swap_b32 v255, v8
v_max_f32 v255, v255, v8
v_mov_b32 v8, v255
s_nop 1
v_permlane32_swap_b32 v255, v8
v_max_f32 v255, v255, v8
v_sub_f32 v109, v109, v255
v_sub_f32 v254, v254, v255
v_exp_f32 v9, v109
v_exp_f32 v10, v254
s_nop 0
v_add_f32 v255, v9, v10
v_mov_b32 v8, v255
v_mul_f32 v9, v9, v53
v_fma_f32 v239, v94, v227, v131
v_permlane16_swap_b32 v255, v8
v_add_f32 v255, v255, v8
v_mov_b32 v8, v255
v_mul_f32 v10, v10, v95
v_fma_f32 v240, v94, v228, v132
v_fma_f32 v241, v94, v229, v133
v_permlane32_swap_b32 v255, v8
v_add_f32 v255, v255, v8
v_cvt_pk_bf16_f32 v156, v238, v239
s_waitcnt lgkmcnt(0)
s_barrier
v_cvt_pk_bf16_f32 v157, v240, v241
v_rcp_f32 v255, v255
s_nop 0
v_mul_f32 v9, v9, v255
v_mul_f32 v10, v10, v255
v_cvt_pk_bf16_f32 v9, v9, v9
v_cvt_pk_bf16_f32 v10, v10, v10
v_and_b32 v224, v9, v40
v_and_b32 v225, v10, v41
v_and_b32 v222, v9, v42
v_and_b32 v223, v10, v43
s_nop 1
v_mfma_f32_16x16x32_bf16 v[218:221], v[154:157], v[222:225], 0
v_mfma_f32_16x16x32_bf16 v[214:217], v[158:161], v[222:225], 0
v_fma_f32 v238, v86, v23, v122
s_nop 5
v_cvt_pk_bf16_f32 v100, v218, v219
v_cvt_pk_bf16_f32 v101, v220, v221
ds_write_b64 v18, v[100:101]
v_cvt_pk_bf16_f32 v9, v216, v217
v_fma_f32 v239, v87, v23, v123
ds_write_b32 v19, v9
v_fma_f32 v240, v88, v23, v124
v_fma_f32 v241, v89, v23, v125
v_cvt_pk_bf16_f32 v142, v238, v239
v_cvt_pk_bf16_f32 v143, v240, v241
s_waitcnt vmcnt(0)
s_waitcnt lgkmcnt(0)
s_barrier
ds_read_b128 v[230:233], v17 offset:0
s_waitcnt lgkmcnt(0)
global_store_dwordx4 v16, v[230:233], s[38:39] sc1
s_waitcnt vmcnt(0)
s_barrier
s_endpgm
.Lqa_w3:
s_add_u32 m0, s48, 4096
s_nop 0
global_load_lds_dwordx4 v0, s[40:41] offset:-4096
global_load_lds_dwordx4 v1, s[40:41] offset:-3072
global_load_lds_dwordx4 v2, s[40:41] offset:-2048
global_load_lds_dwordx4 v3, s[40:41] offset:-1024
global_load_lds_dwordx4 v4, s[40:41] offset:0
global_load_lds_dwordx4 v5, s[40:41] offset:1024
global_load_lds_dwordx4 v6, s[40:41] offset:2048
global_load_lds_dwordx4 v7, s[40:41] offset:3072
s_add_u32 m0, s48, 12288
s_nop 0
global_load_lds_dwordx4 v8, s[40:41] offset:-4096
s_add_u32 s40, s40, 221184
s_addc_u32 s41, s41, 0
s_add_u32 m0, s48, 40960
s_nop 0
global_load_lds_dwordx4 v0, s[40:41] offset:-4096
global_load_lds_dwordx4 v1, s[40:41] offset:-3072
global_load_lds_dwordx4 v2, s[40:41] offset:-2048
global_load_lds_dwordx4 v3, s[40:41] offset:-1024
global_load_lds_dwordx4 v4, s[40:41] offset:0
global_load_lds_dwordx4 v5, s[40:41] offset:1024
global_load_lds_dwordx4 v6, s[40:41] offset:2048
global_load_lds_dwordx4 v7, s[40:41] offset:3072
s_add_u32 m0, s48, 49152
s_nop 0
global_load_lds_dwordx4 v8, s[40:41] offset:-4096
s_add_u32 s40, s40, 221184
s_addc_u32 s41, s41, 0
v_add_u32 v255, 0x10000, v9
v_add_u32 v162, 0x20000, v9
v_add_u32 v163, 0x10000, v10
v_add_u32 v164, 0x20000, v10
v_mov_b32 v106, 0x3F803F80
v_mov_b32 v107, 0x3F803F80
v_mov_b32 v108, 0x3F803F80
v_mov_b32 v109, 0x3F803F80
v_mov_b32 v141, 0
v_mov_b32 v145, 0
v_mov_b32 v149, 0
v_mov_b32 v153, 0
v_mov_b32 v165, 46080
s_waitcnt lgkmcnt(0)
s_mul_i32 s37, s37, 0x9E3779B1
s_add_u32 s37, s37, 0x7F4A7C15
v_add_u32 v46, s37, v46
v_add_u32 v48, s37, v48
v_add_u32 v50, s37, v50
ds_write_b32 v165, v141
s_mov_b64 s[76:77], 0
s_mov_b32 s73, 18
s_waitcnt vmcnt(9) lgkmcnt(0)
)"
R"(s_barrier
s_add_u32 m0, s48, 77824
ds_read_b64_tr_b16 a[0:1], v9 offset:47104
ds_read_b64_tr_b16 a[2:3], v9 offset:49408
global_load_lds_dwordx4 v0, s[40:41] offset:-4096
ds_read_b64_tr_b16 a[4:5], v9 offset:47296
global_load_lds_dwordx4 v1, s[40:41] offset:-3072
ds_read_b64_tr_b16 a[6:7], v9 offset:49600
global_load_lds_dwordx4 v2, s[40:41] offset:-2048
ds_read_b64_tr_b16 a[8:9], v9 offset:47488
global_load_lds_dwordx4 v3, s[40:41] offset:-1024
ds_read_b64_tr_b16 a[10:11], v9 offset:49792
ds_read_b128 v[166:169], v51 offset:0
global_load_lds_dwordx4 v4, s[40:41] offset:0
ds_read_b128 v[174:177], v51 offset:18432
global_load_lds_dwordx4 v5, s[40:41] offset:1024
ds_read_b64_tr_b16 a[12:13], v255 offset:0
global_load_lds_dwordx4 v6, s[40:41] offset:2048
ds_read_b64_tr_b16 a[14:15], v255 offset:2304
global_load_lds_dwordx4 v7, s[40:41] offset:3072
ds_read_b64_tr_b16 a[16:17], v255 offset:192
ds_read_b64_tr_b16 a[18:19], v255 offset:2496
s_add_u32 m0, s48, 86016
ds_read_b64_tr_b16 a[20:21], v255 offset:384
global_load_lds_dwordx4 v8, s[40:41] offset:-4096
ds_read_b64_tr_b16 a[22:23], v255 offset:2688
s_add_u32 s40, s40, 221184
ds_read_b128 v[170:173], v52 offset:0
s_addc_u32 s41, s41, 0
ds_read_b128 v[178:181], v52 offset:18432
s_waitcnt vmcnt(9) lgkmcnt(0)
s_barrier
s_add_u32 m0, s48, 4096
ds_read_b64_tr_b16 a[24:25], v255 offset:18432
v_mfma_f32_16x16x32_bf16 v[86:89], a[0:3], v[106:109], 0
v_mfma_f32_16x16x32_bf16 v[90:93], a[4:7], v[106:109], 0
global_load_lds_dwordx4 v0, s[40:41] offset:-4096
ds_read_b64_tr_b16 a[26:27], v255 offset:20736
v_mfma_f32_16x16x32_bf16 v[94:97], v[106:109], a[8:11], 0
ds_read_b64_tr_b16 a[28:29], v255 offset:18624
global_load_lds_dwordx4 v1, s[40:41] offset:-3072
v_mfma_f32_16x16x32_bf16 v[110:113], a[0:3], v[166:169], 0
ds_read_b64_tr_b16 a[30:31], v255 offset:20928
v_mfma_f32_16x16x32_bf16 v[114:117], a[4:7], v[166:169], 0
global_load_lds_dwordx4 v2, s[40:41] offset:-2048
ds_read_b64_tr_b16 a[32:33], v255 offset:18816
v_mfma_f32_16x16x32_bf16 v[118:121], v[166:169], a[8:11], 0
ds_read_b64_tr_b16 a[34:35], v255 offset:21120
global_load_lds_dwordx4 v3, s[40:41] offset:-1024
v_mfma_f32_16x16x32_bf16 v[134:137], v[166:169], v[166:169], 0
ds_read_b128 v[182:185], v51 offset:2048
v_mfma_f32_16x16x32_bf16 v[122:125], a[0:3], v[174:177], 0
global_load_lds_dwordx4 v4, s[40:41] offset:0
ds_read_b128 v[190:193], v51 offset:20480
v_mfma_f32_16x16x32_bf16 v[126:129], a[4:7], v[174:177], 0
ds_read_b64_tr_b16 a[36:37], v255 offset:36864
global_load_lds_dwordx4 v5, s[40:41] offset:1024
v_mfma_f32_16x16x32_bf16 v[130:133], v[174:177], a[8:11], 0
v_mfma_f32_16x16x32_bf16 v[158:161], v[174:177], v[174:177], 0
ds_read_b64_tr_b16 a[38:39], v255 offset:39168
global_load_lds_dwordx4 v6, s[40:41] offset:2048
v_mfma_f32_16x16x32_bf16 v[86:89], a[12:15], v[106:109], v[86:89]
ds_read_b64_tr_b16 a[40:41], v255 offset:37056
v_mfma_f32_16x16x32_bf16 v[90:93], a[16:19], v[106:109], v[90:93]
global_load_lds_dwordx4 v7, s[40:41] offset:3072
ds_read_b64_tr_b16 a[42:43], v255 offset:39360
v_mfma_f32_16x16x32_bf16 v[94:97], v[106:109], a[20:23], v[94:97]
s_add_u32 m0, s48, 12288
ds_read_b64_tr_b16 a[44:45], v255 offset:37248
v_mfma_f32_16x16x32_bf16 v[110:113], a[12:15], v[170:173], v[110:113]
ds_read_b64_tr_b16 a[46:47], v255 offset:39552
v_mfma_f32_16x16x32_bf16 v[114:117], a[16:19], v[170:173], v[114:117]
global_load_lds_dwordx4 v8, s[40:41] offset:-4096
ds_read_b128 v[186:189], v52 offset:2048
v_mfma_f32_16x16x32_bf16 v[118:121], v[170:173], a[20:23], v[118:121]
s_add_u32 s40, s40, 221184
ds_read_b128 v[194:197], v52 offset:20480
v_mfma_f32_16x16x32_bf16 v[134:137], v[170:173], v[170:173], v[134:137]
v_mfma_f32_16x16x32_bf16 v[122:125], a[12:15], v[178:181], v[122:125]
v_mfma_f32_16x16x32_bf16 v[126:129], a[16:19], v[178:181], v[126:129]
s_addc_u32 s41, s41, 0
v_mfma_f32_16x16x32_bf16 v[130:133], v[178:181], a[20:23], v[130:133]
v_mfma_f32_16x16x32_bf16 v[158:161], v[178:181], v[178:181], v[158:161]
s_waitcnt vmcnt(9) lgkmcnt(0)
s_barrier
s_add_u32 m0, s48, 40960
ds_read_b64_tr_b16 a[48:49], v255 offset:55296
v_mfma_f32_16x16x32_bf16 v[86:89], a[24:27], v[106:109], v[86:89]
v_mfma_f32_16x16x32_bf16 v[90:93], a[28:31], v[106:109], v[90:93]
global_load_lds_dwordx4 v0, s[40:41] offset:-4096
ds_read_b64_tr_b16 a[50:51], v255 offset:57600
v_mfma_f32_16x16x32_bf16 v[94:97], v[106:109], a[32:35], v[94:97]
ds_read_b64_tr_b16 a[52:53], v255 offset:55488
global_load_lds_dwordx4 v1, s[40:41] offset:-3072
v_mfma_f32_16x16x32_bf16 v[110:113], a[24:27], v[182:185], v[110:113]
ds_read_b64_tr_b16 a[54:55], v255 offset:57792
v_mfma_f32_16x16x32_bf16 v[114:117], a[28:31], v[182:185], v[114:117]
global_load_lds_dwordx4 v2, s[40:41] offset:-2048
ds_read_b64_tr_b16 a[56:57], v255 offset:55680
v_mfma_f32_16x16x32_bf16 v[118:121], v[182:185], a[32:35], v[118:121]
ds_read_b64_tr_b16 a[58:59], v255 offset:57984
global_load_lds_dwordx4 v3, s[40:41] offset:-1024
v_mfma_f32_16x16x32_bf16 v[134:137], v[182:185], v[182:185], v[134:137]
ds_read_b128 v[166:169], v51 offset:4096
v_mfma_f32_16x16x32_bf16 v[122:125], a[24:27], v[190:193], v[122:125]
global_load_lds_dwordx4 v4, s[40:41] offset:0
ds_read_b128 v[174:177], v51 offset:22528
v_mfma_f32_16x16x32_bf16 v[126:129], a[28:31], v[190:193], v[126:129]
ds_read_b64_tr_b16 a[60:61], v162 offset:8192
global_load_lds_dwordx4 v5, s[40:41] offset:1024
v_mfma_f32_16x16x32_bf16 v[130:133], v[190:193], a[32:35], v[130:133]
v_mfma_f32_16x16x32_bf16 v[158:161], v[190:193], v[190:193], v[158:161]
ds_read_b64_tr_b16 a[62:63], v162 offset:10496
global_load_lds_dwordx4 v6, s[40:41] offset:2048
v_mfma_f32_16x16x32_bf16 v[86:89], a[36:39], v[106:109], v[86:89]
ds_read_b64_tr_b16 a[64:65], v162 offset:8384
v_mfma_f32_16x16x32_bf16 v[90:93], a[40:43], v[106:109], v[90:93]
global_load_lds_dwordx4 v7, s[40:41] offset:3072
ds_read_b64_tr_b16 a[66:67], v162 offset:10688
v_mfma_f32_16x16x32_bf16 v[94:97], v[106:109], a[44:47], v[94:97]
s_add_u32 m0, s48, 49152
ds_read_b64_tr_b16 a[68:69], v162 offset:8576
v_mfma_f32_16x16x32_bf16 v[110:113], a[36:39], v[186:189], v[110:113]
ds_read_b64_tr_b16 a[70:71], v162 offset:10880
v_mfma_f32_16x16x32_bf16 v[114:117], a[40:43], v[186:189], v[114:117]
global_load_lds_dwordx4 v8, s[40:41] offset:-4096
ds_read_b128 v[170:173], v52 offset:4096
v_mfma_f32_16x16x32_bf16 v[118:121], v[186:189], a[44:47], v[118:121]
s_add_u32 s40, s40, 221184
ds_read_b128 v[178:181], v52 offset:22528
v_mfma_f32_16x16x32_bf16 v[134:137], v[186:189], v[186:189], v[134:137]
v_mfma_f32_16x16x32_bf16 v[122:125], a[36:39], v[194:197], v[122:125]
v_mfma_f32_16x16x32_bf16 v[126:129], a[40:43], v[194:197], v[126:129]
s_addc_u32 s41, s41, 0
v_mfma_f32_16x16x32_bf16 v[130:133], v[194:197], a[44:47], v[130:133]
v_mfma_f32_16x16x32_bf16 v[158:161], v[194:197], v[194:197], v[158:161]
s_waitcnt vmcnt(9) lgkmcnt(0)
s_barrier
s_add_u32 m0, s48, 77824
ds_read_b64_tr_b16 a[72:73], v9 offset:47104
v_mfma_f32_16x16x32_bf16 v[86:89], a[48:51], v[106:109], v[86:89]
v_mfma_f32_16x16x32_bf16 v[90:93], a[52:55], v[106:109], v[90:93]
global_load_lds_dwordx4 v0, s[40:41] offset:-4096
ds_read_b64_tr_b16 a[74:75], v9 offset:49408
v_mfma_f32_16x16x32_bf16 v[94:97], v[106:109], a[56:59], v[94:97]
ds_read_b64_tr_b16 a[76:77], v9 offset:47296
global_load_lds_dwordx4 v1, s[40:41] offset:-3072
v_mfma_f32_16x16x32_bf16 v[110:113], a[48:51], v[166:169], v[110:113]
ds_read_b64_tr_b16 a[78:79], v9 offset:49600
v_mfma_f32_16x16x32_bf16 v[114:117], a[52:55], v[166:169], v[114:117]
global_load_lds_dwordx4 v2, s[40:41] offset:-2048
ds_read_b64_tr_b16 a[80:81], v9 offset:47488
v_mfma_f32_16x16x32_bf16 v[118:121], v[166:169], a[56:59], v[118:121]
ds_read_b64_tr_b16 a[82:83], v9 offset:49792
global_load_lds_dwordx4 v3, s[40:41] offset:-1024
v_mfma_f32_16x16x32_bf16 v[134:137], v[166:169], v[166:169], v[134:137]
ds_read_b128 v[182:185], v51 offset:6144
v_mfma_f32_16x16x32_bf16 v[122:125], a[48:51], v[174:177], v[122:125]
global_load_lds_dwordx4 v4, s[40:41] offset:0
ds_read_b128 v[190:193], v51 offset:24576
v_mfma_f32_16x16x32_bf16 v[126:129], a[52:55], v[174:177], v[126:129]
ds_read_b64_tr_b16 a[84:85], v255 offset:0
global_load_lds_dwordx4 v5, s[40:41] offset:1024
v_mfma_f32_16x16x32_bf16 v[130:133], v[174:177], a[56:59], v[130:133]
v_mfma_f32_16x16x32_bf16 v[158:161], v[174:177], v[174:177], v[158:161]
ds_read_b64_tr_b16 a[86:87], v255 offset:2304
global_load_lds_dwordx4 v6, s[40:41] offset:2048
v_mfma_f32_16x16x32_bf16 v[86:89], a[60:63], v[106:109], v[86:89]
ds_read_b64_tr_b16 a[88:89], v255 offset:192
v_mfma_f32_16x16x32_bf16 v[90:93], a[64:67], v[106:109], v[90:93]
global_load_lds_dwordx4 v7, s[40:41] offset:3072
ds_read_b64_tr_b16 a[90:91], v255 offset:2496
v_mfma_f32_16x16x32_bf16 v[94:97], v[106:109], a[68:71], v[94:97]
s_add_u32 m0, s48, 86016
ds_read_b64_tr_b16 a[92:93], v255 offset:384
v_mfma_f32_16x16x32_bf16 v[110:113], a[60:63], v[170:173], v[110:113]
ds_read_b64_tr_b16 a[94:95], v255 offset:2688
v_mfma_f32_16x16x32_bf16 v[114:117], a[64:67], v[170:173], v[114:117]
global_load_lds_dwordx4 v8, s[40:41] offset:-4096
ds_read_b128 v[186:189], v52 offset:6144
v_mfma_f32_16x16x32_bf16 v[118:121], v[170:173], a[68:71], v[118:121]
s_add_u32 s40, s40, 221184
ds_read_b128 v[194:197], v52 offset:24576
v_mfma_f32_16x16x32_bf16 v[134:137], v[170:173], v[170:173], v[134:137]
v_mfma_f32_16x16x32_bf16 v[122:125], a[60:63], v[178:181], v[122:125]
v_mfma_f32_16x16x32_bf16 v[126:129], a[64:67], v[178:181], v[126:129]
s_addc_u32 s41, s41, 0
v_mfma_f32_16x16x32_bf16 v[130:133], v[178:181], a[68:71], v[130:133]
v_mfma_f32_16x16x32_bf16 v[158:161], v[178:181], v[178:181], v[158:161]
s_waitcnt vmcnt(9) lgkmcnt(0)
s_barrier
s_add_u32 m0, s48, 4096
ds_read_b64_tr_b16 a[96:97], v255 offset:18432
v_mfma_f32_16x16x32_bf16 v[86:89], a[72:75], v[106:109], v[86:89]
v_mfma_f32_16x16x32_bf16 v[90:93], a[76:79], v[106:109], v[90:93]
global_load_lds_dwordx4 v0, s[40:41] offset:-4096
ds_read_b64_tr_b16 a[98:99], v255 offset:20736
v_mfma_f32_16x16x32_bf16 v[94:97], v[106:109], a[80:83], v[94:97]
ds_read_b64_tr_b16 a[100:101], v255 offset:18624
global_load_lds_dwordx4 v1, s[40:41] offset:-3072
v_mfma_f32_16x16x32_bf16 v[110:113], a[72:75], v[182:185], v[110:113]
ds_read_b64_tr_b16 a[102:103], v255 offset:20928
v_mfma_f32_16x16x32_bf16 v[114:117], a[76:79], v[182:185], v[114:117]
global_load_lds_dwordx4 v2, s[40:41] offset:-2048
ds_read_b64_tr_b16 a[104:105], v255 offset:18816
v_mfma_f32_16x16x32_bf16 v[118:121], v[182:185], a[80:83], v[118:121]
ds_read_b64_tr_b16 a[106:107], v255 offset:21120
global_load_lds_dwordx4 v3, s[40:41] offset:-1024
v_mfma_f32_16x16x32_bf16 v[134:137], v[182:185], v[182:185], v[134:137]
ds_read_b128 v[166:169], v51 offset:8192
v_mfma_f32_16x16x32_bf16 v[122:125], a[72:75], v[190:193], v[122:125]
global_load_lds_dwordx4 v4, s[40:41] offset:0
ds_read_b128 v[174:177], v51 offset:26624
v_mfma_f32_16x16x32_bf16 v[126:129], a[76:79], v[190:193], v[126:129]
ds_read_b64_tr_b16 a[108:109], v255 offset:36864
global_load_lds_dwordx4 v5, s[40:41] offset:1024
v_mfma_f32_16x16x32_bf16 v[130:133], v[190:193], a[80:83], v[130:133]
v_mfma_f32_16x16x32_bf16 v[158:161], v[190:193], v[190:193], v[158:161]
ds_read_b64_tr_b16 a[110:111], v255 offset:39168
global_load_lds_dwordx4 v6, s[40:41] offset:2048
v_mfma_f32_16x16x32_bf16 v[86:89], a[84:87], v[106:109], v[86:89]
ds_read_b64_tr_b16 a[112:113], v255 offset:37056
v_mfma_f32_16x16x32_bf16 v[90:93], a[88:91], v[106:109], v[90:93]
global_load_lds_dwordx4 v7, s[40:41] offset:3072
ds_read_b64_tr_b16 a[114:115], v255 offset:39360
v_mfma_f32_16x16x32_bf16 v[94:97], v[106:109], a[92:95], v[94:97]
s_add_u32 m0, s48, 12288
ds_read_b64_tr_b16 a[116:117], v255 offset:37248
v_mfma_f32_16x16x32_bf16 v[110:113], a[84:87], v[186:189], v[110:113]
ds_read_b64_tr_b16 a[118:119], v255 offset:39552
v_mfma_f32_16x16x32_bf16 v[114:117], a[88:91], v[186:189], v[114:117]
global_load_lds_dwordx4 v8, s[40:41] offset:-4096
ds_read_b128 v[170:173], v52 offset:8192
v_mfma_f32_16x16x32_bf16 v[118:121], v[186:189], a[92:95], v[118:121]
s_add_u32 s40, s40, 221184
ds_read_b128 v[178:181], v52 offset:26624
v_mfma_f32_16x16x32_bf16 v[134:137], v[186:189], v[186:189], v[134:137]
v_mfma_f32_16x16x32_bf16 v[122:125], a[84:87], v[194:197], v[122:125]
v_mfma_f32_16x16x32_bf16 v[126:129], a[88:91], v[194:197], v[126:129]
s_addc_u32 s41, s41, 0
v_mfma_f32_16x16x32_bf16 v[130:133], v[194:197], a[92:95], v[130:133]
v_mfma_f32_16x16x32_bf16 v[158:161], v[194:197], v[194:197], v[158:161]
s_waitcnt vmcnt(9) lgkmcnt(0)
s_barrier
s_add_u32 m0, s48, 40960
ds_read_b64_tr_b16 a[120:121], v255 offset:55296
v_mfma_f32_16x16x32_bf16 v[86:89], a[96:99], v[106:109], v[86:89]
v_mfma_f32_16x16x32_bf16 v[90:93], a[100:103], v[106:109], v[90:93]
global_load_lds_dwordx4 v0, s[40:41] offset:-4096
ds_read_b64_tr_b16 a[122:123], v255 offset:57600
v_mfma_f32_16x16x32_bf16 v[94:97], v[106:109], a[104:107], v[94:97]
ds_read_b64_tr_b16 a[124:125], v255 offset:55488
global_load_lds_dwordx4 v1, s[40:41] offset:-3072
v_mfma_f32_16x16x32_bf16 v[110:113], a[96:99], v[166:169], v[110:113]
ds_read_b64_tr_b16 a[126:127], v255 offset:57792
v_mfma_f32_16x16x32_bf16 v[114:117], a[100:103], v[166:169], v[114:117]
global_load_lds_dwordx4 v2, s[40:41] offset:-2048
ds_read_b64_tr_b16 a[128:129], v255 offset:55680
v_mfma_f32_16x16x32_bf16 v[118:121], v[166:169], a[104:107], v[118:121]
ds_read_b64_tr_b16 a[130:131], v255 offset:57984
global_load_lds_dwordx4 v3, s[40:41] offset:-1024
v_mfma_f32_16x16x32_bf16 v[134:137], v[166:169], v[166:169], v[134:137]
ds_read_b128 v[182:185], v51 offset:10240
v_mfma_f32_16x16x32_bf16 v[122:125], a[96:99], v[174:177], v[122:125]
global_load_lds_dwordx4 v4, s[40:41] offset:0
ds_read_b128 v[190:193], v51 offset:28672
v_mfma_f32_16x16x32_bf16 v[126:129], a[100:103], v[174:177], v[126:129]
ds_read_b64_tr_b16 a[132:133], v162 offset:8192
global_load_lds_dwordx4 v5, s[40:41] offset:1024
v_mfma_f32_16x16x32_bf16 v[130:133], v[174:177], a[104:107], v[130:133]
v_mfma_f32_16x16x32_bf16 v[158:161], v[174:177], v[174:177], v[158:161]
ds_read_b64_tr_b16 a[134:135], v162 offset:10496
global_load_lds_dwordx4 v6, s[40:41] offset:2048
v_mfma_f32_16x16x32_bf16 v[86:89], a[108:111], v[106:109], v[86:89]
ds_read_b64_tr_b16 a[136:137], v162 offset:8384
v_mfma_f32_16x16x32_bf16 v[90:93], a[112:115], v[106:109], v[90:93]
global_load_lds_dwordx4 v7, s[40:41] offset:3072
ds_read_b64_tr_b16 a[138:139], v162 offset:10688
v_mfma_f32_16x16x32_bf16 v[94:97], v[106:109], a[116:119], v[94:97]
s_add_u32 m0, s48, 49152
ds_read_b64_tr_b16 a[140:141], v162 offset:8576
v_mfma_f32_16x16x32_bf16 v[110:113], a[108:111], v[170:173], v[110:113]
ds_read_b64_tr_b16 a[142:143], v162 offset:10880
v_mfma_f32_16x16x32_bf16 v[114:117], a[112:115], v[170:173], v[114:117]
global_load_lds_dwordx4 v8, s[40:41] offset:-4096
ds_read_b128 v[186:189], v52 offset:10240
v_mfma_f32_16x16x32_bf16 v[118:121], v[170:173], a[116:119], v[118:121]
s_add_u32 s40, s40, 221184
ds_read_b128 v[194:197], v52 offset:28672
v_mfma_f32_16x16x32_bf16 v[134:137], v[170:173], v[170:173], v[134:137]
v_mfma_f32_16x16x32_bf16 v[122:125], a[108:111], v[178:181], v[122:125]
v_mfma_f32_16x16x32_bf16 v[126:129], a[112:115], v[178:181], v[126:129]
s_addc_u32 s41, s41, 0
v_mfma_f32_16x16x32_bf16 v[130:133], v[178:181], a[116:119], v[130:133]
v_mfma_f32_16x16x32_bf16 v[158:161], v[178:181], v[178:181], v[158:161]
s_waitcnt vmcnt(9) lgkmcnt(0)
s_barrier
s_add_u32 m0, s48, 77824
ds_read_b64_tr_b16 a[144:145], v9 offset:47104
v_mfma_f32_16x16x32_bf16 v[86:89], a[120:123], v[106:109], v[86:89]
v_mfma_f32_16x16x32_bf16 v[90:93], a[124:127], v[106:109], v[90:93]
global_load_lds_dwordx4 v0, s[40:41] offset:-4096
ds_read_b64_tr_b16 a[146:147], v9 offset:49408
v_mfma_f32_16x16x32_bf16 v[94:97], v[106:109], a[128:131], v[94:97]
ds_read_b64_tr_b16 a[148:149], v9 offset:47296
global_load_lds_dwordx4 v1, s[40:41] offset:-3072
v_mfma_f32_16x16x32_bf16 v[110:113], a[120:123], v[182:185], v[110:113]
ds_read_b64_tr_b16 a[150:151], v9 offset:49600
v_mfma_f32_16x16x32_bf16 v[114:117], a[124:127], v[182:185], v[114:117]
global_load_lds_dwordx4 v2, s[40:41] offset:-2048
ds_read_b64_tr_b16 a[152:153], v9 offset:47488
v_mfma_f32_16x16x32_bf16 v[118:121], v[182:185], a[128:131], v[118:121]
ds_read_b64_tr_b16 a[154:155], v9 offset:49792
global_load_lds_dwordx4 v3, s[40:41] offset:-1024
v_mfma_f32_16x16x32_bf16 v[134:137], v[182:185], v[182:185], v[134:137]
ds_read_b128 v[166:169], v51 offset:12288
v_mfma_f32_16x16x32_bf16 v[122:125], a[120:123], v[190:193], v[122:125]
global_load_lds_dwordx4 v4, s[40:41] offset:0
ds_read_b128 v[174:177], v51 offset:30720
v_mfma_f32_16x16x32_bf16 v[126:129], a[124:127], v[190:193], v[126:129]
ds_read_b64_tr_b16 a[156:157], v255 offset:0
global_load_lds_dwordx4 v5, s[40:41] offset:1024
v_mfma_f32_16x16x32_bf16 v[130:133], v[190:193], a[128:131], v[130:133]
v_mfma_f32_16x16x32_bf16 v[158:161], v[190:193], v[190:193], v[158:161]
ds_read_b64_tr_b16 a[158:159], v255 offset:2304
global_load_lds_dwordx4 v6, s[40:41] offset:2048
v_mfma_f32_16x16x32_bf16 v[86:89], a[132:135], v[106:109], v[86:89]
ds_read_b64_tr_b16 a[160:161], v255 offset:192
v_mfma_f32_16x16x32_bf16 v[90:93], a[136:139], v[106:109], v[90:93]
global_load_lds_dwordx4 v7, s[40:41] offset:3072
ds_read_b64_tr_b16 a[162:163], v255 offset:2496
v_mfma_f32_16x16x32_bf16 v[94:97], v[106:109], a[140:143], v[94:97]
s_add_u32 m0, s48, 86016
ds_read_b64_tr_b16 a[164:165], v255 offset:384
v_mfma_f32_16x16x32_bf16 v[110:113], a[132:135], v[186:189], v[110:113]
ds_read_b64_tr_b16 a[166:167], v255 offset:2688
v_mfma_f32_16x16x32_bf16 v[114:117], a[136:139], v[186:189], v[114:117]
global_load_lds_dwordx4 v8, s[40:41] offset:-4096
ds_read_b128 v[170:173], v52 offset:12288
v_mfma_f32_16x16x32_bf16 v[118:121], v[186:189], a[140:143], v[118:121]
s_sub_u32 s40, s40, 1769472
ds_read_b128 v[178:181], v52 offset:30720
v_mfma_f32_16x16x32_bf16 v[134:137], v[186:189], v[186:189], v[134:137]
v_mfma_f32_16x16x32_bf16 v[122:125], a[132:135], v[194:197], v[122:125]
v_mfma_f32_16x16x32_bf16 v[126:129], a[136:139], v[194:197], v[126:129]
s_subb_u32 s41, s41, 0
v_mfma_f32_16x16x32_bf16 v[130:133], v[194:197], a[140:143], v[130:133]
v_mfma_f32_16x16x32_bf16 v[158:161], v[194:197], v[194:197], v[158:161]
s_waitcnt vmcnt(9) lgkmcnt(0)
s_barrier
s_add_u32 m0, s49, 0
ds_read_b64_tr_b16 a[168:169], v255 offset:18432
v_mfma_f32_16x16x32_bf16 v[86:89], a[144:147], v[106:109], v[86:89]
v_mfma_f32_16x16x32_bf16 v[90:93], a[148:151], v[106:109], v[90:93]
ds_read_b64_tr_b16 a[170:171], v255 offset:20736
v_mfma_f32_16x16x32_bf16 v[94:97], v[106:109], a[152:155], v[94:97]
ds_read_b64_tr_b16 a[172:173], v255 offset:18624
v_mfma_f32_16x16x32_bf16 v[110:113], a[144:147], v[166:169], v[110:113]
global_load_lds_dwordx4 v11, s[42:43] offset:-2048 sc1
ds_read_b64_tr_b16 a[174:175], v255 offset:20928
v_mfma_f32_16x16x32_bf16 v[114:117], a[148:151], v[166:169], v[114:117]
ds_read_b64_tr_b16 a[176:177], v255 offset:18816
v_mfma_f32_16x16x32_bf16 v[118:121], v[166:169], a[152:155], v[118:121]
ds_read_b64_tr_b16 a[178:179], v255 offset:21120
v_mfma_f32_16x16x32_bf16 v[134:137], v[166:169], v[166:169], v[134:137]
global_load_lds_dwordx4 v12, s[42:43] offset:-1024 sc1
ds_read_b128 v[182:185], v51 offset:14336
v_mfma_f32_16x16x32_bf16 v[122:125], a[144:147], v[174:177], v[122:125]
ds_read_b128 v[190:193], v51 offset:32768
v_mfma_f32_16x16x32_bf16 v[126:129], a[148:151], v[174:177], v[126:129]
ds_read_b64_tr_b16 a[180:181], v255 offset:36864
v_mfma_f32_16x16x32_bf16 v[130:133], v[174:177], a[152:155], v[130:133]
global_load_lds_dwordx4 v13, s[42:43] offset:0 sc1
v_mfma_f32_16x16x32_bf16 v[158:161], v[174:177], v[174:177], v[158:161]
ds_read_b64_tr_b16 a[182:183], v255 offset:39168
v_mfma_f32_16x16x32_bf16 v[86:89], a[156:159], v[106:109], v[86:89]
ds_read_b64_tr_b16 a[184:185], v255 offset:37056
v_mfma_f32_16x16x32_bf16 v[90:93], a[160:163], v[106:109], v[90:93]
ds_read_b64_tr_b16 a[186:187], v255 offset:39360
v_mfma_f32_16x16x32_bf16 v[94:97], v[106:109], a[164:167], v[94:97]
global_load_lds_dwordx4 v14, s[42:43] offset:1024 sc1
ds_read_b64_tr_b16 a[188:189], v255 offset:37248
v_mfma_f32_16x16x32_bf16 v[110:113], a[156:159], v[170:173], v[110:113]
ds_read_b64_tr_b16 a[190:191], v255 offset:39552
v_mfma_f32_16x16x32_bf16 v[114:117], a[160:163], v[170:173], v[114:117]
ds_read_b128 v[186:189], v52 offset:14336
v_mfma_f32_16x16x32_bf16 v[118:121], v[170:173], a[164:167], v[118:121]
s_sub_u32 s42, s42, 18432
s_subb_u32 s43, s43, 0
ds_read_b128 v[194:197], v52 offset:32768
v_mfma_f32_16x16x32_bf16 v[134:137], v[170:173], v[170:173], v[134:137]
v_mfma_f32_16x16x32_bf16 v[122:125], a[156:159], v[178:181], v[122:125]
v_mfma_f32_16x16x32_bf16 v[126:129], a[160:163], v[178:181], v[126:129]
v_mfma_f32_16x16x32_bf16 v[130:133], v[178:181], a[164:167], v[130:133]
v_mfma_f32_16x16x32_bf16 v[158:161], v[178:181], v[178:181], v[158:161]
s_waitcnt vmcnt(4) lgkmcnt(0)
s_barrier
s_add_u32 m0, s49, 18432
ds_read_b64_tr_b16 a[192:193], v255 offset:55296
v_mfma_f32_16x16x32_bf16 v[86:89], a[168:171], v[106:109], v[86:89]
)"
R"(v_mfma_f32_16x16x32_bf16 v[90:93], a[172:175], v[106:109], v[90:93]
ds_read_b64_tr_b16 a[194:195], v255 offset:57600
v_mfma_f32_16x16x32_bf16 v[94:97], v[106:109], a[176:179], v[94:97]
ds_read_b64_tr_b16 a[196:197], v255 offset:55488
v_mfma_f32_16x16x32_bf16 v[110:113], a[168:171], v[182:185], v[110:113]
global_load_lds_dwordx4 v11, s[42:43] offset:-2048 sc1
ds_read_b64_tr_b16 a[198:199], v255 offset:57792
v_mfma_f32_16x16x32_bf16 v[114:117], a[172:175], v[182:185], v[114:117]
ds_read_b64_tr_b16 a[200:201], v255 offset:55680
v_mfma_f32_16x16x32_bf16 v[118:121], v[182:185], a[176:179], v[118:121]
ds_read_b64_tr_b16 a[202:203], v255 offset:57984
v_mfma_f32_16x16x32_bf16 v[134:137], v[182:185], v[182:185], v[134:137]
global_load_lds_dwordx4 v12, s[42:43] offset:-1024 sc1
ds_read_b128 v[166:169], v51 offset:16384
v_mfma_f32_16x16x32_bf16 v[122:125], a[168:171], v[190:193], v[122:125]
ds_read_b128 v[174:177], v51 offset:34816
v_mfma_f32_16x16x32_bf16 v[126:129], a[172:175], v[190:193], v[126:129]
ds_read_b64_tr_b16 a[204:205], v162 offset:8192
v_mfma_f32_16x16x32_bf16 v[130:133], v[190:193], a[176:179], v[130:133]
global_load_lds_dwordx4 v13, s[42:43] offset:0 sc1
ds_read_b64_tr_b16 a[206:207], v162 offset:10496
v_mfma_f32_16x16x32_bf16 v[158:161], v[190:193], v[190:193], v[158:161]
ds_read_b64_tr_b16 a[208:209], v162 offset:8384
v_mfma_f32_16x16x32_bf16 v[86:89], a[180:183], v[106:109], v[86:89]
ds_read_b64_tr_b16 a[210:211], v162 offset:10688
v_mfma_f32_16x16x32_bf16 v[90:93], a[184:187], v[106:109], v[90:93]
global_load_lds_dwordx4 v14, s[42:43] offset:1024 sc1
ds_read_b64_tr_b16 a[212:213], v162 offset:8576
v_mfma_f32_16x16x32_bf16 v[94:97], v[106:109], a[188:191], v[94:97]
ds_read_b64_tr_b16 a[214:215], v162 offset:10880
v_mfma_f32_16x16x32_bf16 v[110:113], a[180:183], v[186:189], v[110:113]
ds_read_b128 v[170:173], v52 offset:16384
v_mfma_f32_16x16x32_bf16 v[114:117], a[184:187], v[186:189], v[114:117]
s_sub_u32 s42, s42, 18432
s_subb_u32 s43, s43, 0
ds_read_b128 v[178:181], v52 offset:34816
v_mfma_f32_16x16x32_bf16 v[118:121], v[186:189], a[188:191], v[118:121]
v_mfma_f32_16x16x32_bf16 v[134:137], v[186:189], v[186:189], v[134:137]
v_mfma_f32_16x16x32_bf16 v[122:125], a[180:183], v[194:197], v[122:125]
v_mfma_f32_16x16x32_bf16 v[126:129], a[184:187], v[194:197], v[126:129]
v_mfma_f32_16x16x32_bf16 v[158:161], v[194:197], v[194:197], v[158:161]
s_waitcnt lgkmcnt(14)
v_mfma_f32_16x16x32_bf16 v[86:89], a[192:195], v[106:109], v[86:89]
s_waitcnt lgkmcnt(12)
v_mfma_f32_16x16x32_bf16 v[90:93], a[196:199], v[106:109], v[90:93]
s_waitcnt lgkmcnt(10)
v_mfma_f32_16x16x32_bf16 v[94:97], v[106:109], a[200:203], v[94:97]
s_waitcnt lgkmcnt(9)
v_mfma_f32_16x16x32_bf16 v[110:113], a[192:195], v[166:169], v[110:113]
v_mfma_f32_16x16x32_bf16 v[114:117], a[196:199], v[166:169], v[114:117]
v_mfma_f32_16x16x32_bf16 v[118:121], v[166:169], a[200:203], v[118:121]
v_mfma_f32_16x16x32_bf16 v[134:137], v[166:169], v[166:169], v[134:137]
s_waitcnt lgkmcnt(8)
v_mfma_f32_16x16x32_bf16 v[158:161], v[174:177], v[174:177], v[158:161]
s_waitcnt lgkmcnt(6)
v_mfma_f32_16x16x32_bf16 v[86:89], a[204:207], v[106:109], v[86:89]
s_waitcnt lgkmcnt(4)
v_mfma_f32_16x16x32_bf16 v[90:93], a[208:211], v[106:109], v[90:93]
s_waitcnt lgkmcnt(2)
v_mfma_f32_16x16x32_bf16 v[94:97], v[106:109], a[212:215], v[94:97]
s_waitcnt lgkmcnt(1)
v_mfma_f32_16x16x32_bf16 v[110:113], a[204:207], v[170:173], v[110:113]
v_mfma_f32_16x16x32_bf16 v[114:117], a[208:211], v[170:173], v[114:117]
v_mfma_f32_16x16x32_bf16 v[118:121], v[170:173], a[212:215], v[118:121]
v_mfma_f32_16x16x32_bf16 v[134:137], v[170:173], v[170:173], v[134:137]
s_waitcnt lgkmcnt(0)
v_mfma_f32_16x16x32_bf16 v[158:161], v[178:181], v[178:181], v[158:161]
s_nop 5
ds_write_b128 v23, v[134:137] offset:41984
s_waitcnt lgkmcnt(0)
s_barrier
ds_read_b128 v[166:169], v51 offset:32768
ds_read_b128 v[170:173], v52 offset:32768
ds_read_b128 v[174:177], v51 offset:34816
ds_read_b128 v[178:181], v52 offset:34816
v_mov_b32 v202, v32
v_mov_b32 v203, v33
v_mov_b32 v204, v34
v_mov_b32 v205, v35
v_mov_b32 v134, v158
v_mov_b32 v135, v159
v_mov_b32 v136, v160
v_mov_b32 v137, v161
s_barrier
ds_read_b32 v26, v24 offset:0
ds_read_b32 v148, v21
ds_read_b32 v95, v24 offset:192
ds_read_b32 v255, v27
ds_read_b32 v8, v28
ds_read_b128 v[194:197], v25 offset:0
ds_read_b64 v[158:159], v22 offset:41472
ds_read_b32 v140, v20
v_xor_b32 v51, 0x10000, v51
v_xor_b32 v52, 0x10000, v52
ds_write_b128 v23, v[134:137] offset:41984
s_waitcnt lgkmcnt(8)
v_fma_f32 v206, v90, v26, v114
v_fma_f32 v207, v91, v26, v115
v_fma_f32 v208, v92, v26, v116
v_fma_f32 v209, v93, v26, v117
v_cvt_pk_bf16_f32 v146, v206, v207
v_cvt_pk_bf16_f32 v147, v208, v209
s_waitcnt lgkmcnt(0)
s_barrier
v_fma_f32 v206, v94, v194, v118
v_fma_f32 v207, v94, v195, v119
v_fma_f32 v208, v94, v196, v120
v_fma_f32 v209, v94, v197, v121
v_cvt_pk_bf16_f32 v154, v206, v207
v_cvt_pk_bf16_f32 v155, v208, v209
v_fma_f32 v206, v86, v26, v110
v_fma_f32 v207, v87, v26, v111
v_fma_f32 v208, v88, v26, v112
v_fma_f32 v209, v89, v26, v113
v_cvt_pk_bf16_f32 v138, v206, v207
v_cvt_pk_bf16_f32 v139, v208, v209
s_waitcnt vmcnt(4) lgkmcnt(0)
s_barrier
s_mov_b32 s70, 32
s_waitcnt lgkmcnt(0)
s_nop 1
s_branch .Lqa_in27
.p2align 6
.Lqa_top41:
s_waitcnt lgkmcnt(9)
v_mfma_f32_16x16x32_bf16 v[134:137], v[0:3], v[0:3], 0
s_waitcnt lgkmcnt(8)
ds_read_b128 v[54:57], v52 offset:4096
ds_read_b32 v8, v30
v_mfma_f32_16x16x32_bf16 v[134:137], v[4:7], v[4:7], v[134:137]
ds_read_b128 v[194:197], v25 offset:64
ds_read_b64 v[160:161], v22 offset:41472
v_mfma_f32_16x16x32_bf16 v[110:113], a[0:3], v[0:3], 0
s_waitcnt lgkmcnt(8)
ds_read_b128 v[58:61], v51 offset:6144
ds_read_b32 v144, v20
v_mfma_f32_16x16x32_bf16 v[134:137], v[96:99], v[96:99], v[134:137]
s_waitcnt lgkmcnt(8)
ds_read_b128 v[62:65], v52 offset:6144
v_add_u32 v46, 1, v46
v_mfma_f32_16x16x32_bf16 v[134:137], v[100:103], v[100:103], v[134:137]
s_mov_b64 exec, 65535
global_store_dwordx4 v16, v[198:201], s[38:39] sc1
s_mov_b64 exec, -1
v_mfma_f32_16x16x32_bf16 v[114:117], a[4:7], v[0:3], 0
s_waitcnt lgkmcnt(7)
ds_read_b128 v[66:69], v51 offset:8192
v_add_u32 v16, 18432, v16
v_mfma_f32_16x16x32_bf16 v[134:137], v[104:107], v[104:107], v[134:137]
s_waitcnt lgkmcnt(7)
ds_read_b128 v[70:73], v52 offset:8192
v_fma_f32 v206, v90, v26, v126
v_mfma_f32_16x16x32_bf16 v[134:137], v[54:57], v[54:57], v[134:137]
v_fma_f32 v207, v91, v26, v127
v_fma_f32 v208, v92, v26, v128
v_mfma_f32_16x16x32_bf16 v[118:121], v[0:3], a[8:11], 0
s_waitcnt lgkmcnt(4)
ds_read_b128 v[74:77], v51 offset:10240
v_fma_f32 v209, v93, v26, v129
v_mfma_f32_16x16x32_bf16 v[134:137], v[58:61], v[58:61], v[134:137]
s_waitcnt lgkmcnt(3)
ds_read_b128 v[78:81], v52 offset:10240
v_cvt_pk_bf16_f32 v150, v206, v207
v_mfma_f32_16x16x32_bf16 v[134:137], v[62:65], v[62:65], v[134:137]
v_cvt_pk_bf16_f32 v151, v208, v209
v_max_f32 v47, v186, v187
v_mfma_f32_16x16x32_bf16 v[110:113], a[12:15], v[4:7], v[110:113]
ds_read_b128 v[82:85], v51 offset:12288
v_mfma_f32_16x16x32_bf16 v[182:185], v[150:153], v[138:141], v[32:35]
s_waitcnt lgkmcnt(4)
v_max_f32 v48, v188, v189
v_fma_f32 v206, v94, v194, v130
v_mfma_f32_16x16x32_bf16 v[134:137], v[66:69], v[66:69], v[134:137]
s_xor_b32 s49, s49, 0x10000
ds_read_b128 v[162:165], v52 offset:12288
s_waitcnt lgkmcnt(4)
v_mfma_f32_16x16x32_bf16 v[134:137], v[70:73], v[70:73], v[134:137]
s_add_u32 m0, s49, 0
v_mul_f32 v95, 0x3eae1a93, v95
v_mul_f32 v9, v255, v95
v_mfma_f32_16x16x32_bf16 v[114:117], a[16:19], v[4:7], v[114:117]
s_waitcnt lgkmcnt(3)
ds_read_b128 v[166:169], v51 offset:14336
v_mul_f32 v10, v8, v95
v_mfma_f32_16x16x32_bf16 v[134:137], v[74:77], v[74:77], v[134:137]
s_waitcnt lgkmcnt(3)
ds_read_b128 v[170:173], v52 offset:14336
v_max3_f32 v47, v182, v183, v47
v_mfma_f32_16x16x32_bf16 v[134:137], v[78:81], v[78:81], v[134:137]
v_max3_f32 v48, v184, v185, v48
v_mul_f32 v47, v47, v9
v_mfma_f32_16x16x32_bf16 v[118:121], v[4:7], a[20:23], v[118:121]
s_waitcnt lgkmcnt(3)
ds_read_b128 v[174:177], v51 offset:16384
v_mul_f32 v48, v48, v10
v_mfma_f32_16x16x32_bf16 v[134:137], v[82:85], v[82:85], v[134:137]
s_waitcnt lgkmcnt(3)
ds_read_b128 v[178:181], v52 offset:16384
global_load_lds_dwordx4 v11, s[42:43] offset:-2048 sc1
v_mfma_f32_16x16x32_bf16 v[134:137], v[162:165], v[162:165], v[134:137]
v_max_f32 v49, v47, v48
v_mov_b32 v50, v49
v_mfma_f32_16x16x32_bf16 v[110:113], a[24:27], v[96:99], v[110:113]
s_waitcnt lgkmcnt(3)
v_permlane16_swap_b32 v49, v50
v_mfma_f32_16x16x32_bf16 v[134:137], v[166:169], v[166:169], v[134:137]
s_waitcnt lgkmcnt(2)
v_max_f32 v49, v49, v50
v_mov_b32 v50, v49
v_mfma_f32_16x16x32_bf16 v[134:137], v[170:173], v[170:173], v[134:137]
s_nop 0
v_permlane32_swap_b32 v49, v50
v_mfma_f32_16x16x32_bf16 v[114:117], a[28:31], v[96:99], v[114:117]
s_waitcnt lgkmcnt(1)
v_max_f32 v49, v49, v50
v_sub_f32 v47, v47, v49
v_mfma_f32_16x16x32_bf16 v[134:137], v[174:177], v[174:177], v[134:137]
s_waitcnt lgkmcnt(0)
v_sub_f32 v48, v48, v49
v_fma_f32 v207, v94, v195, v131
v_mfma_f32_16x16x32_bf16 v[134:137], v[178:181], v[178:181], v[134:137]
v_exp_f32 v210, v47
v_mfma_f32_16x16x32_bf16 v[118:121], v[96:99], a[32:35], v[118:121]
global_load_lds_dwordx4 v12, s[42:43] offset:-1024 sc1
v_fma_f32 v208, v94, v196, v132
v_mfma_f32_16x16x32_bf16 v[110:113], a[36:39], v[100:103], v[110:113]
v_exp_f32 v211, v48
v_mfma_f32_16x16x32_bf16 v[114:117], a[40:43], v[100:103], v[114:117]
v_add_f32 v49, v210, v211
v_mov_b32 v50, v49
v_mfma_f32_16x16x32_bf16 v[118:121], v[100:103], a[44:47], v[118:121]
ds_write_b128 v23, v[134:137] offset:41984
v_mfma_f32_16x16x32_bf16 v[110:113], a[48:51], v[104:107], v[110:113]
v_mul_f32 v210, v210, v255
v_fma_f32 v209, v94, v197, v133
v_mfma_f32_16x16x32_bf16 v[114:117], a[52:55], v[104:107], v[114:117]
v_permlane16_swap_b32 v49, v50
v_mfma_f32_16x16x32_bf16 v[118:121], v[104:107], a[56:59], v[118:121]
v_add_f32 v49, v49, v50
v_mov_b32 v50, v49
v_mfma_f32_16x16x32_bf16 v[110:113], a[60:63], v[54:57], v[110:113]
v_mul_f32 v211, v211, v8
v_cvt_pk_bf16_f32 v156, v206, v207
v_mfma_f32_16x16x32_bf16 v[114:117], a[64:67], v[54:57], v[114:117]
global_load_lds_dwordx4 v13, s[42:43] offset:0 sc1
v_cvt_pk_bf16_f32 v157, v208, v209
v_mfma_f32_16x16x32_bf16 v[118:121], v[54:57], a[68:71], v[118:121]
v_mfma_f32_16x16x32_bf16 v[110:113], a[72:75], v[58:61], v[110:113]
s_waitcnt lgkmcnt(0)
s_barrier
v_mfma_f32_16x16x32_bf16 v[114:117], a[76:79], v[58:61], v[114:117]
v_permlane32_swap_b32 v49, v50
v_mfma_f32_16x16x32_bf16 v[118:121], v[58:61], a[80:83], v[118:121]
v_add_f32 v49, v49, v50
v_fma_f32 v206, v86, v26, v122
v_mfma_f32_16x16x32_bf16 v[110:113], a[84:87], v[62:65], v[110:113]
v_rcp_f32 v49, v49
v_mfma_f32_16x16x32_bf16 v[114:117], a[88:91], v[62:65], v[114:117]
v_mul_f32 v210, v210, v49
v_mul_f32 v211, v211, v49
v_mfma_f32_16x16x32_bf16 v[118:121], v[62:65], a[92:95], v[118:121]
v_cvt_pk_bf16_f32 v210, v210, v210
v_cvt_pk_bf16_f32 v211, v211, v211
v_mfma_f32_16x16x32_bf16 v[110:113], a[96:99], v[66:69], v[110:113]
global_load_lds_dwordx4 v14, s[42:43] offset:1024 sc1
s_add_u32 s42, s42, s46
s_addc_u32 s43, s43, s47
v_mfma_f32_16x16x32_bf16 v[114:117], a[100:103], v[66:69], v[114:117]
v_and_b32 v192, v210, v40
v_and_b32 v193, v211, v41
v_mfma_f32_16x16x32_bf16 v[118:121], v[66:69], a[104:107], v[118:121]
v_and_b32 v190, v210, v42
v_and_b32 v191, v211, v43
v_mfma_f32_16x16x32_bf16 v[110:113], a[108:111], v[70:73], v[110:113]
s_nop 0
v_mfma_f32_16x16x32_bf16 v[186:189], v[154:157], v[190:193], 0
v_mfma_f32_16x16x32_bf16 v[182:185], v[158:161], v[190:193], 0
v_fma_f32 v207, v87, v26, v123
v_fma_f32 v208, v88, v26, v124
v_mfma_f32_16x16x32_bf16 v[114:117], a[112:115], v[70:73], v[114:117]
v_fma_f32 v209, v89, v26, v125
v_cvt_pk_bf16_f32 v142, v206, v207
v_mfma_f32_16x16x32_bf16 v[118:121], v[70:73], a[116:119], v[118:121]
v_cvt_pk_bf16_f32 v143, v208, v209
v_mfma_f32_16x16x32_bf16 v[110:113], a[120:123], v[74:77], v[110:113]
v_mfma_f32_16x16x32_bf16 v[114:117], a[124:127], v[74:77], v[114:117]
v_cvt_pk_bf16_f32 v108, v186, v187
v_cvt_pk_bf16_f32 v109, v188, v189
v_mfma_f32_16x16x32_bf16 v[118:121], v[74:77], a[128:131], v[118:121]
ds_write_b64 v18, v[108:109]
v_mfma_f32_16x16x32_bf16 v[110:113], a[132:135], v[78:81], v[110:113]
v_cvt_pk_bf16_f32 v210, v182, v183
v_mfma_f32_16x16x32_bf16 v[114:117], a[136:139], v[78:81], v[114:117]
ds_write_b32 v19, v210
v_mfma_f32_16x16x32_bf16 v[118:121], v[78:81], a[140:143], v[118:121]
v_mfma_f32_16x16x32_bf16 v[110:113], a[144:147], v[82:85], v[110:113]
v_mfma_f32_16x16x32_bf16 v[114:117], a[148:151], v[82:85], v[114:117]
v_mfma_f32_16x16x32_bf16 v[118:121], v[82:85], a[152:155], v[118:121]
v_mfma_f32_16x16x32_bf16 v[110:113], a[156:159], v[162:165], v[110:113]
v_mfma_f32_16x16x32_bf16 v[114:117], a[160:163], v[162:165], v[114:117]
v_mfma_f32_16x16x32_bf16 v[118:121], v[162:165], a[164:167], v[118:121]
s_waitcnt vmcnt(5)
v_sub_u32 v53, v53, v46
v_cmp_lt_u32 vcc, 0x7f, v53
v_mfma_f32_16x16x32_bf16 v[110:113], a[168:171], v[166:169], v[110:113]
global_load_dword v254, v44, s[38:39] sc1
v_mfma_f32_16x16x32_bf16 v[114:117], a[172:175], v[166:169], v[114:117]
v_mfma_f32_16x16x32_bf16 v[118:121], v[166:169], a[176:179], v[118:121]
s_and_b64 vcc, vcc, s[76:77]
s_cbranch_vccnz .Lqa_sp30
.Lqa_rt31:
v_mfma_f32_16x16x32_bf16 v[110:113], a[180:183], v[170:173], v[110:113]
v_mfma_f32_16x16x32_bf16 v[114:117], a[184:187], v[170:173], v[114:117]
s_waitcnt vmcnt(5) lgkmcnt(0)
s_barrier
v_mfma_f32_16x16x32_bf16 v[118:121], v[170:173], a[188:191], v[118:121]
ds_read_b128 v[0:3], v51 offset:18432
v_mfma_f32_16x16x32_bf16 v[110:113], a[192:195], v[174:177], v[110:113]
ds_read_b128 v[4:7], v52 offset:18432
ds_read_b128 v[198:201], v17 offset:0
v_mfma_f32_16x16x32_bf16 v[114:117], a[196:199], v[174:177], v[114:117]
ds_read_b32 v26, v24 offset:0
ds_read_b32 v148, v21
v_mfma_f32_16x16x32_bf16 v[186:189], v[150:153], v[142:145], v[36:39]
v_mfma_f32_16x16x32_bf16 v[118:121], v[174:177], a[200:203], v[118:121]
ds_read_b128 v[96:99], v51 offset:20480
ds_read_b32 v95, v24 offset:192
v_mfma_f32_16x16x32_bf16 v[110:113], a[204:207], v[178:181], v[110:113]
ds_read_b128 v[100:103], v52 offset:20480
ds_read_b32 v255, v27
v_mfma_f32_16x16x32_bf16 v[114:117], a[208:211], v[178:181], v[114:117]
ds_read_b32 v8, v28
ds_read_b128 v[194:197], v25 offset:0
v_mfma_f32_16x16x32_bf16 v[118:121], v[178:181], a[212:215], v[118:121]
s_waitcnt lgkmcnt(10)
ds_read_b128 v[104:107], v51 offset:22528
ds_read_b64 v[158:159], v22 offset:41472
v_mfma_f32_16x16x32_bf16 v[134:137], v[0:3], v[0:3], 0
s_waitcnt lgkmcnt(11)
ds_read_b128 v[54:57], v52 offset:22528
ds_read_b32 v140, v20
v_mfma_f32_16x16x32_bf16 v[134:137], v[4:7], v[4:7], v[134:137]
v_add_u32 v46, 1, v46
v_max_f32 v47, v186, v187
v_mfma_f32_16x16x32_bf16 v[122:125], a[0:3], v[0:3], 0
s_waitcnt lgkmcnt(11)
ds_read_b128 v[58:61], v51 offset:24576
v_fma_f32 v206, v90, v26, v114
s_waitcnt lgkmcnt(10)
v_mfma_f32_16x16x32_bf16 v[134:137], v[96:99], v[96:99], v[134:137]
s_waitcnt lgkmcnt(8)
ds_read_b128 v[62:65], v52 offset:24576
v_fma_f32 v207, v91, v26, v115
v_mfma_f32_16x16x32_bf16 v[134:137], v[100:103], v[100:103], v[134:137]
s_mov_b64 exec, 65535
global_store_dwordx4 v16, v[198:201], s[38:39] sc1
s_mov_b64 exec, -1
v_mfma_f32_16x16x32_bf16 v[126:129], a[4:7], v[0:3], 0
s_waitcnt lgkmcnt(5)
ds_read_b128 v[66:69], v51 offset:26624
v_add_u32 v16, 18432, v16
v_mfma_f32_16x16x32_bf16 v[134:137], v[104:107], v[104:107], v[134:137]
s_waitcnt lgkmcnt(4)
ds_read_b128 v[70:73], v52 offset:26624
v_fma_f32 v208, v92, v26, v116
v_mfma_f32_16x16x32_bf16 v[134:137], v[54:57], v[54:57], v[134:137]
v_fma_f32 v209, v93, v26, v117
v_cvt_pk_bf16_f32 v146, v206, v207
v_mfma_f32_16x16x32_bf16 v[130:133], v[0:3], a[8:11], 0
s_waitcnt lgkmcnt(3)
ds_read_b128 v[74:77], v51 offset:28672
v_cvt_pk_bf16_f32 v147, v208, v209
v_mfma_f32_16x16x32_bf16 v[134:137], v[58:61], v[58:61], v[134:137]
ds_read_b128 v[78:81], v52 offset:28672
v_mfma_f32_16x16x32_bf16 v[182:185], v[146:149], v[142:145], v[202:205]
s_waitcnt lgkmcnt(4)
v_max_f32 v48, v188, v189
v_fma_f32 v206, v94, v194, v118
v_mfma_f32_16x16x32_bf16 v[134:137], v[62:65], v[62:65], v[134:137]
v_fma_f32 v207, v94, v195, v119
v_fma_f32 v208, v94, v196, v120
v_mfma_f32_16x16x32_bf16 v[122:125], a[12:15], v[4:7], v[122:125]
s_waitcnt lgkmcnt(3)
ds_read_b128 v[82:85], v51 offset:30720
v_mul_f32 v95, 0x3eae1a93, v95
v_mfma_f32_16x16x32_bf16 v[134:137], v[66:69], v[66:69], v[134:137]
s_add_u32 m0, s49, 18432
ds_read_b128 v[162:165], v52 offset:30720
s_waitcnt lgkmcnt(4)
v_mfma_f32_16x16x32_bf16 v[134:137], v[70:73], v[70:73], v[134:137]
v_mul_f32 v9, v255, v95
v_mul_f32 v10, v8, v95
v_mfma_f32_16x16x32_bf16 v[126:129], a[16:19], v[4:7], v[126:129]
s_waitcnt lgkmcnt(3)
ds_read_b128 v[166:169], v51 offset:32768
v_max3_f32 v47, v182, v183, v47
v_mfma_f32_16x16x32_bf16 v[134:137], v[74:77], v[74:77], v[134:137]
s_waitcnt lgkmcnt(3)
ds_read_b128 v[170:173], v52 offset:32768
v_max3_f32 v48, v184, v185, v48
)"
R"(v_mfma_f32_16x16x32_bf16 v[134:137], v[78:81], v[78:81], v[134:137]
v_mul_f32 v47, v47, v9
v_mul_f32 v48, v48, v10
v_mfma_f32_16x16x32_bf16 v[130:133], v[4:7], a[20:23], v[130:133]
s_waitcnt lgkmcnt(3)
ds_read_b128 v[174:177], v51 offset:34816
global_load_lds_dwordx4 v11, s[42:43] offset:-2048 sc1
v_mfma_f32_16x16x32_bf16 v[134:137], v[82:85], v[82:85], v[134:137]
s_waitcnt lgkmcnt(3)
ds_read_b128 v[178:181], v52 offset:34816
v_max_f32 v49, v47, v48
v_mfma_f32_16x16x32_bf16 v[134:137], v[162:165], v[162:165], v[134:137]
v_mov_b32 v50, v49
v_fma_f32 v209, v94, v197, v121
v_mfma_f32_16x16x32_bf16 v[122:125], a[24:27], v[96:99], v[122:125]
s_waitcnt lgkmcnt(3)
v_permlane16_swap_b32 v49, v50
v_mfma_f32_16x16x32_bf16 v[134:137], v[166:169], v[166:169], v[134:137]
s_waitcnt lgkmcnt(2)
v_max_f32 v49, v49, v50
v_mov_b32 v50, v49
v_mfma_f32_16x16x32_bf16 v[134:137], v[170:173], v[170:173], v[134:137]
s_nop 0
v_permlane32_swap_b32 v49, v50
v_mfma_f32_16x16x32_bf16 v[126:129], a[28:31], v[96:99], v[126:129]
s_waitcnt lgkmcnt(1)
v_max_f32 v49, v49, v50
v_sub_f32 v47, v47, v49
v_mfma_f32_16x16x32_bf16 v[134:137], v[174:177], v[174:177], v[134:137]
s_waitcnt lgkmcnt(0)
v_sub_f32 v48, v48, v49
v_cvt_pk_bf16_f32 v154, v206, v207
v_mfma_f32_16x16x32_bf16 v[134:137], v[178:181], v[178:181], v[134:137]
global_load_lds_dwordx4 v12, s[42:43] offset:-1024 sc1
v_cvt_pk_bf16_f32 v155, v208, v209
v_mfma_f32_16x16x32_bf16 v[130:133], v[96:99], a[32:35], v[130:133]
v_exp_f32 v210, v47
v_mfma_f32_16x16x32_bf16 v[122:125], a[36:39], v[100:103], v[122:125]
v_exp_f32 v211, v48
v_mfma_f32_16x16x32_bf16 v[126:129], a[40:43], v[100:103], v[126:129]
v_add_f32 v49, v210, v211
v_mov_b32 v50, v49
v_mfma_f32_16x16x32_bf16 v[130:133], v[100:103], a[44:47], v[130:133]
ds_write_b128 v23, v[134:137] offset:41984
v_mfma_f32_16x16x32_bf16 v[122:125], a[48:51], v[104:107], v[122:125]
v_mul_f32 v210, v210, v255
v_fma_f32 v206, v86, v26, v110
v_mfma_f32_16x16x32_bf16 v[126:129], a[52:55], v[104:107], v[126:129]
v_permlane16_swap_b32 v49, v50
v_mfma_f32_16x16x32_bf16 v[130:133], v[104:107], a[56:59], v[130:133]
v_add_f32 v49, v49, v50
v_mov_b32 v50, v49
v_mfma_f32_16x16x32_bf16 v[122:125], a[60:63], v[54:57], v[122:125]
global_load_lds_dwordx4 v13, s[42:43] offset:0 sc1
v_mul_f32 v211, v211, v8
v_mfma_f32_16x16x32_bf16 v[126:129], a[64:67], v[54:57], v[126:129]
v_permlane32_swap_b32 v49, v50
v_mfma_f32_16x16x32_bf16 v[130:133], v[54:57], a[68:71], v[130:133]
v_mfma_f32_16x16x32_bf16 v[122:125], a[72:75], v[58:61], v[122:125]
s_waitcnt lgkmcnt(0)
s_barrier
v_mfma_f32_16x16x32_bf16 v[126:129], a[76:79], v[58:61], v[126:129]
v_add_f32 v49, v49, v50
v_fma_f32 v207, v87, v26, v111
v_mfma_f32_16x16x32_bf16 v[130:133], v[58:61], a[80:83], v[130:133]
v_rcp_f32 v49, v49
v_mfma_f32_16x16x32_bf16 v[122:125], a[84:87], v[62:65], v[122:125]
v_mul_f32 v210, v210, v49
v_mul_f32 v211, v211, v49
v_mfma_f32_16x16x32_bf16 v[126:129], a[88:91], v[62:65], v[126:129]
v_cvt_pk_bf16_f32 v210, v210, v210
v_cvt_pk_bf16_f32 v211, v211, v211
v_mfma_f32_16x16x32_bf16 v[130:133], v[62:65], a[92:95], v[130:133]
global_load_lds_dwordx4 v14, s[42:43] offset:1024 sc1
s_sub_u32 s42, s42, 18432
s_subb_u32 s43, s43, 0
v_mfma_f32_16x16x32_bf16 v[122:125], a[96:99], v[66:69], v[122:125]
v_and_b32 v190, v210, v40
v_and_b32 v191, v211, v41
v_mfma_f32_16x16x32_bf16 v[126:129], a[100:103], v[66:69], v[126:129]
v_and_b32 v192, v210, v42
v_and_b32 v193, v211, v43
v_mfma_f32_16x16x32_bf16 v[130:133], v[66:69], a[104:107], v[130:133]
s_nop 0
v_mfma_f32_16x16x32_bf16 v[186:189], v[154:157], v[190:193], 0
v_mfma_f32_16x16x32_bf16 v[182:185], v[158:161], v[190:193], 0
v_fma_f32 v208, v88, v26, v112
v_fma_f32 v209, v89, v26, v113
v_mfma_f32_16x16x32_bf16 v[122:125], a[108:111], v[70:73], v[122:125]
v_cvt_pk_bf16_f32 v138, v206, v207
v_cvt_pk_bf16_f32 v139, v208, v209
v_mfma_f32_16x16x32_bf16 v[126:129], a[112:115], v[70:73], v[126:129]
v_mfma_f32_16x16x32_bf16 v[130:133], v[70:73], a[116:119], v[130:133]
v_mfma_f32_16x16x32_bf16 v[122:125], a[120:123], v[74:77], v[122:125]
v_cvt_pk_bf16_f32 v108, v186, v187
v_cvt_pk_bf16_f32 v109, v188, v189
v_mfma_f32_16x16x32_bf16 v[126:129], a[124:127], v[74:77], v[126:129]
ds_write_b64 v18, v[108:109]
v_mfma_f32_16x16x32_bf16 v[130:133], v[74:77], a[128:131], v[130:133]
v_cvt_pk_bf16_f32 v210, v182, v183
v_mfma_f32_16x16x32_bf16 v[122:125], a[132:135], v[78:81], v[122:125]
ds_write_b32 v19, v210
v_mfma_f32_16x16x32_bf16 v[126:129], a[136:139], v[78:81], v[126:129]
v_mfma_f32_16x16x32_bf16 v[130:133], v[78:81], a[140:143], v[130:133]
v_mfma_f32_16x16x32_bf16 v[122:125], a[144:147], v[82:85], v[122:125]
v_mfma_f32_16x16x32_bf16 v[126:129], a[148:151], v[82:85], v[126:129]
v_mfma_f32_16x16x32_bf16 v[130:133], v[82:85], a[152:155], v[130:133]
v_mfma_f32_16x16x32_bf16 v[122:125], a[156:159], v[162:165], v[122:125]
v_mfma_f32_16x16x32_bf16 v[126:129], a[160:163], v[162:165], v[126:129]
v_mfma_f32_16x16x32_bf16 v[130:133], v[162:165], a[164:167], v[130:133]
s_waitcnt vmcnt(5)
v_sub_u32 v254, v254, v46
v_cmp_lt_u32 vcc, 0x7f, v254
v_mfma_f32_16x16x32_bf16 v[122:125], a[168:171], v[166:169], v[122:125]
global_load_dword v53, v45, s[38:39] sc1
v_mfma_f32_16x16x32_bf16 v[126:129], a[172:175], v[166:169], v[126:129]
v_xor_b32 v51, 0x10000, v51
v_xor_b32 v52, 0x10000, v52
v_mfma_f32_16x16x32_bf16 v[130:133], v[166:169], a[176:179], v[130:133]
s_and_b64 vcc, vcc, s[76:77]
s_cbranch_vccnz .Lqa_sp32
.Lqa_rt33:
v_mfma_f32_16x16x32_bf16 v[122:125], a[180:183], v[170:173], v[122:125]
v_mfma_f32_16x16x32_bf16 v[126:129], a[184:187], v[170:173], v[126:129]
s_waitcnt vmcnt(5) lgkmcnt(0)
s_barrier
.Lqa_in27:
v_mfma_f32_16x16x32_bf16 v[130:133], v[170:173], a[188:191], v[130:133]
s_cmp_eq_u32 s70, s73
s_cbranch_scc1 .Lqa_hk28
.Lqa_hr29:
ds_read_b128 v[0:3], v51 offset:0
v_mfma_f32_16x16x32_bf16 v[122:125], a[192:195], v[174:177], v[122:125]
ds_read_b128 v[4:7], v52 offset:0
ds_read_b128 v[198:201], v17 offset:0
v_mfma_f32_16x16x32_bf16 v[126:129], a[196:199], v[174:177], v[126:129]
ds_read_b32 v26, v24 offset:64
ds_read_b32 v152, v21
v_mfma_f32_16x16x32_bf16 v[186:189], v[146:149], v[138:141], v[36:39]
v_mfma_f32_16x16x32_bf16 v[130:133], v[174:177], a[200:203], v[130:133]
ds_read_b128 v[96:99], v51 offset:2048
ds_read_b32 v95, v24 offset:128
v_mfma_f32_16x16x32_bf16 v[122:125], a[204:207], v[178:181], v[122:125]
ds_read_b128 v[100:103], v52 offset:2048
ds_read_b32 v255, v29
v_mfma_f32_16x16x32_bf16 v[126:129], a[208:211], v[178:181], v[126:129]
v_mfma_f32_16x16x32_bf16 v[130:133], v[178:181], a[212:215], v[130:133]
s_sub_u32 s70, s70, 1
ds_read_b128 v[104:107], v51 offset:4096
s_cbranch_scc0 .Lqa_top41
s_waitcnt lgkmcnt(0)
ds_read_b32 v8, v30
ds_read_b128 v[194:197], v25 offset:64
ds_read_b64 v[160:161], v22 offset:41472
ds_read_b32 v144, v20
s_mov_b64 exec, 65535
global_store_dwordx4 v16, v[198:201], s[38:39] sc1
s_mov_b64 exec, -1
v_add_u32 v16, 18432, v16
v_fma_f32 v206, v90, v26, v126
v_fma_f32 v207, v91, v26, v127
v_fma_f32 v208, v92, v26, v128
v_fma_f32 v209, v93, v26, v129
v_cvt_pk_bf16_f32 v150, v206, v207
v_cvt_pk_bf16_f32 v151, v208, v209
v_max_f32 v47, v186, v187
s_nop 0
v_mfma_f32_16x16x32_bf16 v[182:185], v[150:153], v[138:141], v[32:35]
s_waitcnt lgkmcnt(2)
v_max_f32 v48, v188, v189
v_fma_f32 v206, v94, v194, v130
v_mul_f32 v95, 0x3eae1a93, v95
v_mul_f32 v9, v255, v95
v_mul_f32 v10, v8, v95
s_nop 1
v_max3_f32 v47, v182, v183, v47
v_max3_f32 v48, v184, v185, v48
v_mul_f32 v47, v47, v9
v_mul_f32 v48, v48, v10
v_max_f32 v49, v47, v48
v_mov_b32 v50, v49
s_nop 1
v_permlane16_swap_b32 v49, v50
v_max_f32 v49, v49, v50
v_mov_b32 v50, v49
s_nop 1
v_permlane32_swap_b32 v49, v50
v_max_f32 v49, v49, v50
v_sub_f32 v47, v47, v49
v_sub_f32 v48, v48, v49
v_fma_f32 v207, v94, v195, v131
v_exp_f32 v210, v47
v_fma_f32 v208, v94, v196, v132
v_exp_f32 v211, v48
s_nop 0
v_add_f32 v49, v210, v211
v_mov_b32 v50, v49
v_mul_f32 v210, v210, v255
v_fma_f32 v209, v94, v197, v133
v_permlane16_swap_b32 v49, v50
v_add_f32 v49, v49, v50
v_mov_b32 v50, v49
v_mul_f32 v211, v211, v8
v_cvt_pk_bf16_f32 v156, v206, v207
v_cvt_pk_bf16_f32 v157, v208, v209
s_waitcnt lgkmcnt(0)
s_barrier
v_permlane32_swap_b32 v49, v50
v_add_f32 v49, v49, v50
v_fma_f32 v206, v86, v26, v122
v_rcp_f32 v49, v49
s_nop 0
v_mul_f32 v210, v210, v49
v_mul_f32 v211, v211, v49
v_cvt_pk_bf16_f32 v210, v210, v210
v_cvt_pk_bf16_f32 v211, v211, v211
v_and_b32 v192, v210, v40
v_and_b32 v193, v211, v41
v_and_b32 v190, v210, v42
v_and_b32 v191, v211, v43
s_nop 1
v_mfma_f32_16x16x32_bf16 v[186:189], v[154:157], v[190:193], 0
v_mfma_f32_16x16x32_bf16 v[182:185], v[158:161], v[190:193], 0
v_fma_f32 v207, v87, v26, v123
v_fma_f32 v208, v88, v26, v124
v_fma_f32 v209, v89, v26, v125
v_cvt_pk_bf16_f32 v142, v206, v207
v_cvt_pk_bf16_f32 v143, v208, v209
s_nop 1
v_cvt_pk_bf16_f32 v108, v186, v187
v_cvt_pk_bf16_f32 v109, v188, v189
ds_write_b64 v18, v[108:109]
v_cvt_pk_bf16_f32 v210, v182, v183
ds_write_b32 v19, v210
s_waitcnt vmcnt(0)
s_waitcnt lgkmcnt(0)
s_barrier
ds_read_b128 v[198:201], v17 offset:0
s_waitcnt lgkmcnt(0)
global_store_dwordx4 v16, v[198:201], s[38:39] sc1
s_waitcnt vmcnt(0)
s_barrier
s_endpgm
.Lqa_w4:
s_add_u32 m0, s48, 4096
s_nop 0
global_load_lds_dwordx4 v0, s[40:41] offset:-4096
global_load_lds_dwordx4 v1, s[40:41] offset:-3072
global_load_lds_dwordx4 v2, s[40:41] offset:-2048
global_load_lds_dwordx4 v3, s[40:41] offset:-1024
global_load_lds_dwordx4 v4, s[40:41] offset:0
global_load_lds_dwordx4 v5, s[40:41] offset:1024
global_load_lds_dwordx4 v6, s[40:41] offset:2048
global_load_lds_dwordx4 v7, s[40:41] offset:3072
s_add_u32 m0, s48, 12288
s_nop 0
global_load_lds_dwordx4 v8, s[40:41] offset:-4096
s_add_u32 s40, s40, 221184
s_addc_u32 s41, s41, 0
s_add_u32 m0, s48, 40960
s_nop 0
global_load_lds_dwordx4 v0, s[40:41] offset:-4096
global_load_lds_dwordx4 v1, s[40:41] offset:-3072
global_load_lds_dwordx4 v2, s[40:41] offset:-2048
global_load_lds_dwordx4 v3, s[40:41] offset:-1024
global_load_lds_dwordx4 v4, s[40:41] offset:0
global_load_lds_dwordx4 v5, s[40:41] offset:1024
global_load_lds_dwordx4 v6, s[40:41] offset:2048
global_load_lds_dwordx4 v7, s[40:41] offset:3072
s_add_u32 m0, s48, 49152
s_nop 0
global_load_lds_dwordx4 v8, s[40:41] offset:-4096
s_add_u32 s40, s40, 221184
s_addc_u32 s41, s41, 0
v_add_u32 v255, 0x10000, v9
v_add_u32 v162, 0x20000, v9
v_add_u32 v163, 0x10000, v10
v_add_u32 v164, 0x20000, v10
v_mov_b32 v106, 0x3F803F80
v_mov_b32 v107, 0x3F803F80
v_mov_b32 v108, 0x3F803F80
v_mov_b32 v109, 0x3F803F80
v_mov_b32 v141, 0
v_mov_b32 v145, 0
v_mov_b32 v149, 0
v_mov_b32 v153, 0
v_mov_b32 v165, 46080
s_waitcnt lgkmcnt(0)
s_mul_i32 s37, s37, 0x9E3779B1
s_add_u32 s37, s37, 0x7F4A7C15
v_add_u32 v46, s37, v46
v_add_u32 v48, s37, v48
v_add_u32 v50, s37, v50
ds_write_b32 v165, v141
s_mov_b64 s[76:77], 0
s_mov_b32 s73, 18
s_waitcnt vmcnt(9) lgkmcnt(0)
s_barrier
s_add_u32 m0, s48, 77824
ds_read_b64_tr_b16 a[0:1], v9 offset:47104
ds_read_b64_tr_b16 a[2:3], v9 offset:49408
global_load_lds_dwordx4 v0, s[40:41] offset:-4096
ds_read_b64_tr_b16 a[4:5], v9 offset:47296
global_load_lds_dwordx4 v1, s[40:41] offset:-3072
ds_read_b64_tr_b16 a[6:7], v9 offset:49600
global_load_lds_dwordx4 v2, s[40:41] offset:-2048
ds_read_b64_tr_b16 a[8:9], v9 offset:47488
global_load_lds_dwordx4 v3, s[40:41] offset:-1024
ds_read_b64_tr_b16 a[10:11], v9 offset:49792
ds_read_b128 v[166:169], v51 offset:0
global_load_lds_dwordx4 v4, s[40:41] offset:0
ds_read_b128 v[174:177], v51 offset:18432
global_load_lds_dwordx4 v5, s[40:41] offset:1024
ds_read_b64_tr_b16 a[12:13], v255 offset:0
global_load_lds_dwordx4 v6, s[40:41] offset:2048
ds_read_b64_tr_b16 a[14:15], v255 offset:2304
global_load_lds_dwordx4 v7, s[40:41] offset:3072
ds_read_b64_tr_b16 a[16:17], v255 offset:192
ds_read_b64_tr_b16 a[18:19], v255 offset:2496
s_add_u32 m0, s48, 86016
ds_read_b64_tr_b16 a[20:21], v255 offset:384
global_load_lds_dwordx4 v8, s[40:41] offset:-4096
ds_read_b64_tr_b16 a[22:23], v255 offset:2688
s_add_u32 s40, s40, 221184
ds_read_b128 v[170:173], v52 offset:0
s_addc_u32 s41, s41, 0
ds_read_b128 v[178:181], v52 offset:18432
s_waitcnt vmcnt(9) lgkmcnt(0)
s_barrier
s_add_u32 m0, s48, 4096
ds_read_b64_tr_b16 a[24:25], v255 offset:18432
v_mfma_f32_16x16x32_bf16 v[86:89], a[0:3], v[106:109], 0
v_mfma_f32_16x16x32_bf16 v[90:93], a[4:7], v[106:109], 0
global_load_lds_dwordx4 v0, s[40:41] offset:-4096
ds_read_b64_tr_b16 a[26:27], v255 offset:20736
v_mfma_f32_16x16x32_bf16 v[94:97], v[106:109], a[8:11], 0
ds_read_b64_tr_b16 a[28:29], v255 offset:18624
global_load_lds_dwordx4 v1, s[40:41] offset:-3072
v_mfma_f32_16x16x32_bf16 v[110:113], a[0:3], v[166:169], 0
ds_read_b64_tr_b16 a[30:31], v255 offset:20928
v_mfma_f32_16x16x32_bf16 v[114:117], a[4:7], v[166:169], 0
global_load_lds_dwordx4 v2, s[40:41] offset:-2048
ds_read_b64_tr_b16 a[32:33], v255 offset:18816
v_mfma_f32_16x16x32_bf16 v[118:121], v[166:169], a[8:11], 0
ds_read_b64_tr_b16 a[34:35], v255 offset:21120
global_load_lds_dwordx4 v3, s[40:41] offset:-1024
v_mfma_f32_16x16x32_bf16 v[134:137], v[106:109], v[166:169], 0
ds_read_b128 v[182:185], v51 offset:2048
v_mfma_f32_16x16x32_bf16 v[122:125], a[0:3], v[174:177], 0
global_load_lds_dwordx4 v4, s[40:41] offset:0
ds_read_b128 v[190:193], v51 offset:20480
v_mfma_f32_16x16x32_bf16 v[126:129], a[4:7], v[174:177], 0
ds_read_b64_tr_b16 a[36:37], v255 offset:36864
global_load_lds_dwordx4 v5, s[40:41] offset:1024
v_mfma_f32_16x16x32_bf16 v[130:133], v[174:177], a[8:11], 0
v_mfma_f32_16x16x32_bf16 v[158:161], v[106:109], v[174:177], 0
ds_read_b64_tr_b16 a[38:39], v255 offset:39168
global_load_lds_dwordx4 v6, s[40:41] offset:2048
v_mfma_f32_16x16x32_bf16 v[86:89], a[12:15], v[106:109], v[86:89]
ds_read_b64_tr_b16 a[40:41], v255 offset:37056
v_mfma_f32_16x16x32_bf16 v[90:93], a[16:19], v[106:109], v[90:93]
global_load_lds_dwordx4 v7, s[40:41] offset:3072
ds_read_b64_tr_b16 a[42:43], v255 offset:39360
v_mfma_f32_16x16x32_bf16 v[94:97], v[106:109], a[20:23], v[94:97]
s_add_u32 m0, s48, 12288
ds_read_b64_tr_b16 a[44:45], v255 offset:37248
v_mfma_f32_16x16x32_bf16 v[110:113], a[12:15], v[170:173], v[110:113]
ds_read_b64_tr_b16 a[46:47], v255 offset:39552
v_mfma_f32_16x16x32_bf16 v[114:117], a[16:19], v[170:173], v[114:117]
global_load_lds_dwordx4 v8, s[40:41] offset:-4096
ds_read_b128 v[186:189], v52 offset:2048
v_mfma_f32_16x16x32_bf16 v[118:121], v[170:173], a[20:23], v[118:121]
s_add_u32 s40, s40, 221184
ds_read_b128 v[194:197], v52 offset:20480
v_mfma_f32_16x16x32_bf16 v[134:137], v[106:109], v[170:173], v[134:137]
v_mfma_f32_16x16x32_bf16 v[122:125], a[12:15], v[178:181], v[122:125]
v_mfma_f32_16x16x32_bf16 v[126:129], a[16:19], v[178:181], v[126:129]
s_addc_u32 s41, s41, 0
v_mfma_f32_16x16x32_bf16 v[130:133], v[178:181], a[20:23], v[130:133]
v_mfma_f32_16x16x32_bf16 v[158:161], v[106:109], v[178:181], v[158:161]
s_waitcnt vmcnt(9) lgkmcnt(0)
s_barrier
s_add_u32 m0, s48, 40960
ds_read_b64_tr_b16 a[48:49], v255 offset:55296
v_mfma_f32_16x16x32_bf16 v[86:89], a[24:27], v[106:109], v[86:89]
v_mfma_f32_16x16x32_bf16 v[90:93], a[28:31], v[106:109], v[90:93]
global_load_lds_dwordx4 v0, s[40:41] offset:-4096
ds_read_b64_tr_b16 a[50:51], v255 offset:57600
v_mfma_f32_16x16x32_bf16 v[94:97], v[106:109], a[32:35], v[94:97]
ds_read_b64_tr_b16 a[52:53], v255 offset:55488
global_load_lds_dwordx4 v1, s[40:41] offset:-3072
v_mfma_f32_16x16x32_bf16 v[110:113], a[24:27], v[182:185], v[110:113]
ds_read_b64_tr_b16 a[54:55], v255 offset:57792
v_mfma_f32_16x16x32_bf16 v[114:117], a[28:31], v[182:185], v[114:117]
global_load_lds_dwordx4 v2, s[40:41] offset:-2048
ds_read_b64_tr_b16 a[56:57], v255 offset:55680
v_mfma_f32_16x16x32_bf16 v[118:121], v[182:185], a[32:35], v[118:121]
ds_read_b64_tr_b16 a[58:59], v255 offset:57984
global_load_lds_dwordx4 v3, s[40:41] offset:-1024
v_mfma_f32_16x16x32_bf16 v[134:137], v[106:109], v[182:185], v[134:137]
)"
R"(ds_read_b128 v[166:169], v51 offset:4096
v_mfma_f32_16x16x32_bf16 v[122:125], a[24:27], v[190:193], v[122:125]
global_load_lds_dwordx4 v4, s[40:41] offset:0
ds_read_b128 v[174:177], v51 offset:22528
v_mfma_f32_16x16x32_bf16 v[126:129], a[28:31], v[190:193], v[126:129]
ds_read_b64_tr_b16 a[60:61], v162 offset:8192
global_load_lds_dwordx4 v5, s[40:41] offset:1024
v_mfma_f32_16x16x32_bf16 v[130:133], v[190:193], a[32:35], v[130:133]
v_mfma_f32_16x16x32_bf16 v[158:161], v[106:109], v[190:193], v[158:161]
ds_read_b64_tr_b16 a[62:63], v162 offset:10496
global_load_lds_dwordx4 v6, s[40:41] offset:2048
v_mfma_f32_16x16x32_bf16 v[86:89], a[36:39], v[106:109], v[86:89]
ds_read_b64_tr_b16 a[64:65], v162 offset:8384
v_mfma_f32_16x16x32_bf16 v[90:93], a[40:43], v[106:109], v[90:93]
global_load_lds_dwordx4 v7, s[40:41] offset:3072
ds_read_b64_tr_b16 a[66:67], v162 offset:10688
v_mfma_f32_16x16x32_bf16 v[94:97], v[106:109], a[44:47], v[94:97]
s_add_u32 m0, s48, 49152
ds_read_b64_tr_b16 a[68:69], v162 offset:8576
v_mfma_f32_16x16x32_bf16 v[110:113], a[36:39], v[186:189], v[110:113]
ds_read_b64_tr_b16 a[70:71], v162 offset:10880
v_mfma_f32_16x16x32_bf16 v[114:117], a[40:43], v[186:189], v[114:117]
global_load_lds_dwordx4 v8, s[40:41] offset:-4096
ds_read_b128 v[170:173], v52 offset:4096
v_mfma_f32_16x16x32_bf16 v[118:121], v[186:189], a[44:47], v[118:121]
s_add_u32 s40, s40, 221184
ds_read_b128 v[178:181], v52 offset:22528
v_mfma_f32_16x16x32_bf16 v[134:137], v[106:109], v[186:189], v[134:137]
v_mfma_f32_16x16x32_bf16 v[122:125], a[36:39], v[194:197], v[122:125]
v_mfma_f32_16x16x32_bf16 v[126:129], a[40:43], v[194:197], v[126:129]
s_addc_u32 s41, s41, 0
v_mfma_f32_16x16x32_bf16 v[130:133], v[194:197], a[44:47], v[130:133]
v_mfma_f32_16x16x32_bf16 v[158:161], v[106:109], v[194:197], v[158:161]
s_waitcnt vmcnt(9) lgkmcnt(0)
s_barrier
s_add_u32 m0, s48, 77824
ds_read_b64_tr_b16 a[72:73], v9 offset:47104
v_mfma_f32_16x16x32_bf16 v[86:89], a[48:51], v[106:109], v[86:89]
v_mfma_f32_16x16x32_bf16 v[90:93], a[52:55], v[106:109], v[90:93]
global_load_lds_dwordx4 v0, s[40:41] offset:-4096
ds_read_b64_tr_b16 a[74:75], v9 offset:49408
v_mfma_f32_16x16x32_bf16 v[94:97], v[106:109], a[56:59], v[94:97]
ds_read_b64_tr_b16 a[76:77], v9 offset:47296
global_load_lds_dwordx4 v1, s[40:41] offset:-3072
v_mfma_f32_16x16x32_bf16 v[110:113], a[48:51], v[166:169], v[110:113]
ds_read_b64_tr_b16 a[78:79], v9 offset:49600
v_mfma_f32_16x16x32_bf16 v[114:117], a[52:55], v[166:169], v[114:117]
global_load_lds_dwordx4 v2, s[40:41] offset:-2048
ds_read_b64_tr_b16 a[80:81], v9 offset:47488
v_mfma_f32_16x16x32_bf16 v[118:121], v[166:169], a[56:59], v[118:121]
ds_read_b64_tr_b16 a[82:83], v9 offset:49792
global_load_lds_dwordx4 v3, s[40:41] offset:-1024
v_mfma_f32_16x16x32_bf16 v[134:137], v[106:109], v[166:169], v[134:137]
ds_read_b128 v[182:185], v51 offset:6144
v_mfma_f32_16x16x32_bf16 v[122:125], a[48:51], v[174:177], v[122:125]
global_load_lds_dwordx4 v4, s[40:41] offset:0
ds_read_b128 v[190:193], v51 offset:24576
v_mfma_f32_16x16x32_bf16 v[126:129], a[52:55], v[174:177], v[126:129]
ds_read_b64_tr_b16 a[84:85], v255 offset:0
global_load_lds_dwordx4 v5, s[40:41] offset:1024
v_mfma_f32_16x16x32_bf16 v[130:133], v[174:177], a[56:59], v[130:133]
v_mfma_f32_16x16x32_bf16 v[158:161], v[106:109], v[174:177], v[158:161]
ds_read_b64_tr_b16 a[86:87], v255 offset:2304
global_load_lds_dwordx4 v6, s[40:41] offset:2048
v_mfma_f32_16x16x32_bf16 v[86:89], a[60:63], v[106:109], v[86:89]
ds_read_b64_tr_b16 a[88:89], v255 offset:192
v_mfma_f32_16x16x32_bf16 v[90:93], a[64:67], v[106:109], v[90:93]
global_load_lds_dwordx4 v7, s[40:41] offset:3072
ds_read_b64_tr_b16 a[90:91], v255 offset:2496
v_mfma_f32_16x16x32_bf16 v[94:97], v[106:109], a[68:71], v[94:97]
s_add_u32 m0, s48, 86016
ds_read_b64_tr_b16 a[92:93], v255 offset:384
v_mfma_f32_16x16x32_bf16 v[110:113], a[60:63], v[170:173], v[110:113]
ds_read_b64_tr_b16 a[94:95], v255 offset:2688
v_mfma_f32_16x16x32_bf16 v[114:117], a[64:67], v[170:173], v[114:117]
global_load_lds_dwordx4 v8, s[40:41] offset:-4096
ds_read_b128 v[186:189], v52 offset:6144
v_mfma_f32_16x16x32_bf16 v[118:121], v[170:173], a[68:71], v[118:121]
s_add_u32 s40, s40, 221184
ds_read_b128 v[194:197], v52 offset:24576
v_mfma_f32_16x16x32_bf16 v[134:137], v[106:109], v[170:173], v[134:137]
v_mfma_f32_16x16x32_bf16 v[122:125], a[60:63], v[178:181], v[122:125]
v_mfma_f32_16x16x32_bf16 v[126:129], a[64:67], v[178:181], v[126:129]
s_addc_u32 s41, s41, 0
v_mfma_f32_16x16x32_bf16 v[130:133], v[178:181], a[68:71], v[130:133]
v_mfma_f32_16x16x32_bf16 v[158:161], v[106:109], v[178:181], v[158:161]
s_waitcnt vmcnt(9) lgkmcnt(0)
s_barrier
s_add_u32 m0, s48, 4096
ds_read_b64_tr_b16 a[96:97], v255 offset:18432
v_mfma_f32_16x16x32_bf16 v[86:89], a[72:75], v[106:109], v[86:89]
v_mfma_f32_16x16x32_bf16 v[90:93], a[76:79], v[106:109], v[90:93]
global_load_lds_dwordx4 v0, s[40:41] offset:-4096
ds_read_b64_tr_b16 a[98:99], v255 offset:20736
v_mfma_f32_16x16x32_bf16 v[94:97], v[106:109], a[80:83], v[94:97]
ds_read_b64_tr_b16 a[100:101], v255 offset:18624
global_load_lds_dwordx4 v1, s[40:41] offset:-3072
v_mfma_f32_16x16x32_bf16 v[110:113], a[72:75], v[182:185], v[110:113]
ds_read_b64_tr_b16 a[102:103], v255 offset:20928
v_mfma_f32_16x16x32_bf16 v[114:117], a[76:79], v[182:185], v[114:117]
global_load_lds_dwordx4 v2, s[40:41] offset:-2048
ds_read_b64_tr_b16 a[104:105], v255 offset:18816
v_mfma_f32_16x16x32_bf16 v[118:121], v[182:185], a[80:83], v[118:121]
ds_read_b64_tr_b16 a[106:107], v255 offset:21120
global_load_lds_dwordx4 v3, s[40:41] offset:-1024
v_mfma_f32_16x16x32_bf16 v[134:137], v[106:109], v[182:185], v[134:137]
ds_read_b128 v[166:169], v51 offset:8192
v_mfma_f32_16x16x32_bf16 v[122:125], a[72:75], v[190:193], v[122:125]
global_load_lds_dwordx4 v4, s[40:41] offset:0
ds_read_b128 v[174:177], v51 offset:26624
v_mfma_f32_16x16x32_bf16 v[126:129], a[76:79], v[190:193], v[126:129]
ds_read_b64_tr_b16 a[108:109], v255 offset:36864
global_load_lds_dwordx4 v5, s[40:41] offset:1024
v_mfma_f32_16x16x32_bf16 v[130:133], v[190:193], a[80:83], v[130:133]
v_mfma_f32_16x16x32_bf16 v[158:161], v[106:109], v[190:193], v[158:161]
ds_read_b64_tr_b16 a[110:111], v255 offset:39168
global_load_lds_dwordx4 v6, s[40:41] offset:2048
v_mfma_f32_16x16x32_bf16 v[86:89], a[84:87], v[106:109], v[86:89]
ds_read_b64_tr_b16 a[112:113], v255 offset:37056
v_mfma_f32_16x16x32_bf16 v[90:93], a[88:91], v[106:109], v[90:93]
global_load_lds_dwordx4 v7, s[40:41] offset:3072
ds_read_b64_tr_b16 a[114:115], v255 offset:39360
v_mfma_f32_16x16x32_bf16 v[94:97], v[106:109], a[92:95], v[94:97]
s_add_u32 m0, s48, 12288
ds_read_b64_tr_b16 a[116:117], v255 offset:37248
v_mfma_f32_16x16x32_bf16 v[110:113], a[84:87], v[186:189], v[110:113]
ds_read_b64_tr_b16 a[118:119], v255 offset:39552
v_mfma_f32_16x16x32_bf16 v[114:117], a[88:91], v[186:189], v[114:117]
global_load_lds_dwordx4 v8, s[40:41] offset:-4096
ds_read_b128 v[170:173], v52 offset:8192
v_mfma_f32_16x16x32_bf16 v[118:121], v[186:189], a[92:95], v[118:121]
s_add_u32 s40, s40, 221184
ds_read_b128 v[178:181], v52 offset:26624
v_mfma_f32_16x16x32_bf16 v[134:137], v[106:109], v[186:189], v[134:137]
v_mfma_f32_16x16x32_bf16 v[122:125], a[84:87], v[194:197], v[122:125]
v_mfma_f32_16x16x32_bf16 v[126:129], a[88:91], v[194:197], v[126:129]
s_addc_u32 s41, s41, 0
v_mfma_f32_16x16x32_bf16 v[130:133], v[194:197], a[92:95], v[130:133]
v_mfma_f32_16x16x32_bf16 v[158:161], v[106:109], v[194:197], v[158:161]
s_waitcnt vmcnt(9) lgkmcnt(0)
s_barrier
s_add_u32 m0, s48, 40960
ds_read_b64_tr_b16 a[120:121], v255 offset:55296
v_mfma_f32_16x16x32_bf16 v[86:89], a[96:99], v[106:109], v[86:89]
v_mfma_f32_16x16x32_bf16 v[90:93], a[100:103], v[106:109], v[90:93]
global_load_lds_dwordx4 v0, s[40:41] offset:-4096
ds_read_b64_tr_b16 a[122:123], v255 offset:57600
v_mfma_f32_16x16x32_bf16 v[94:97], v[106:109], a[104:107], v[94:97]
ds_read_b64_tr_b16 a[124:125], v255 offset:55488
global_load_lds_dwordx4 v1, s[40:41] offset:-3072
v_mfma_f32_16x16x32_bf16 v[110:113], a[96:99], v[166:169], v[110:113]
ds_read_b64_tr_b16 a[126:127], v255 offset:57792
v_mfma_f32_16x16x32_bf16 v[114:117], a[100:103], v[166:169], v[114:117]
global_load_lds_dwordx4 v2, s[40:41] offset:-2048
ds_read_b64_tr_b16 a[128:129], v255 offset:55680
v_mfma_f32_16x16x32_bf16 v[118:121], v[166:169], a[104:107], v[118:121]
ds_read_b64_tr_b16 a[130:131], v255 offset:57984
global_load_lds_dwordx4 v3, s[40:41] offset:-1024
v_mfma_f32_16x16x32_bf16 v[134:137], v[106:109], v[166:169], v[134:137]
ds_read_b128 v[182:185], v51 offset:10240
v_mfma_f32_16x16x32_bf16 v[122:125], a[96:99], v[174:177], v[122:125]
global_load_lds_dwordx4 v4, s[40:41] offset:0
ds_read_b128 v[190:193], v51 offset:28672
v_mfma_f32_16x16x32_bf16 v[126:129], a[100:103], v[174:177], v[126:129]
ds_read_b64_tr_b16 a[132:133], v162 offset:8192
global_load_lds_dwordx4 v5, s[40:41] offset:1024
v_mfma_f32_16x16x32_bf16 v[130:133], v[174:177], a[104:107], v[130:133]
v_mfma_f32_16x16x32_bf16 v[158:161], v[106:109], v[174:177], v[158:161]
ds_read_b64_tr_b16 a[134:135], v162 offset:10496
global_load_lds_dwordx4 v6, s[40:41] offset:2048
v_mfma_f32_16x16x32_bf16 v[86:89], a[108:111], v[106:109], v[86:89]
ds_read_b64_tr_b16 a[136:137], v162 offset:8384
v_mfma_f32_16x16x32_bf16 v[90:93], a[112:115], v[106:109], v[90:93]
global_load_lds_dwordx4 v7, s[40:41] offset:3072
ds_read_b64_tr_b16 a[138:139], v162 offset:10688
v_mfma_f32_16x16x32_bf16 v[94:97], v[106:109], a[116:119], v[94:97]
s_add_u32 m0, s48, 49152
ds_read_b64_tr_b16 a[140:141], v162 offset:8576
v_mfma_f32_16x16x32_bf16 v[110:113], a[108:111], v[170:173], v[110:113]
ds_read_b64_tr_b16 a[142:143], v162 offset:10880
v_mfma_f32_16x16x32_bf16 v[114:117], a[112:115], v[170:173], v[114:117]
global_load_lds_dwordx4 v8, s[40:41] offset:-4096
ds_read_b128 v[186:189], v52 offset:10240
v_mfma_f32_16x16x32_bf16 v[118:121], v[170:173], a[116:119], v[118:121]
s_add_u32 s40, s40, 221184
ds_read_b128 v[194:197], v52 offset:28672
v_mfma_f32_16x16x32_bf16 v[134:137], v[106:109], v[170:173], v[134:137]
v_mfma_f32_16x16x32_bf16 v[122:125], a[108:111], v[178:181], v[122:125]
v_mfma_f32_16x16x32_bf16 v[126:129], a[112:115], v[178:181], v[126:129]
s_addc_u32 s41, s41, 0
v_mfma_f32_16x16x32_bf16 v[130:133], v[178:181], a[116:119], v[130:133]
v_mfma_f32_16x16x32_bf16 v[158:161], v[106:109], v[178:181], v[158:161]
s_waitcnt vmcnt(9) lgkmcnt(0)
s_barrier
s_add_u32 m0, s48, 77824
ds_read_b64_tr_b16 a[144:145], v9 offset:47104
v_mfma_f32_16x16x32_bf16 v[86:89], a[120:123], v[106:109], v[86:89]
v_mfma_f32_16x16x32_bf16 v[90:93], a[124:127], v[106:109], v[90:93]
global_load_lds_dwordx4 v0, s[40:41] offset:-4096
ds_read_b64_tr_b16 a[146:147], v9 offset:49408
v_mfma_f32_16x16x32_bf16 v[94:97], v[106:109], a[128:131], v[94:97]
ds_read_b64_tr_b16 a[148:149], v9 offset:47296
global_load_lds_dwordx4 v1, s[40:41] offset:-3072
v_mfma_f32_16x16x32_bf16 v[110:113], a[120:123], v[182:185], v[110:113]
ds_read_b64_tr_b16 a[150:151], v9 offset:49600
v_mfma_f32_16x16x32_bf16 v[114:117], a[124:127], v[182:185], v[114:117]
global_load_lds_dwordx4 v2, s[40:41] offset:-2048
ds_read_b64_tr_b16 a[152:153], v9 offset:47488
v_mfma_f32_16x16x32_bf16 v[118:121], v[182:185], a[128:131], v[118:121]
ds_read_b64_tr_b16 a[154:155], v9 offset:49792
global_load_lds_dwordx4 v3, s[40:41] offset:-1024
v_mfma_f32_16x16x32_bf16 v[134:137], v[106:109], v[182:185], v[134:137]
ds_read_b128 v[166:169], v51 offset:12288
v_mfma_f32_16x16x32_bf16 v[122:125], a[120:123], v[190:193], v[122:125]
global_load_lds_dwordx4 v4, s[40:41] offset:0
ds_read_b128 v[174:177], v51 offset:30720
v_mfma_f32_16x16x32_bf16 v[126:129], a[124:127], v[190:193], v[126:129]
ds_read_b64_tr_b16 a[156:157], v255 offset:0
global_load_lds_dwordx4 v5, s[40:41] offset:1024
v_mfma_f32_16x16x32_bf16 v[130:133], v[190:193], a[128:131], v[130:133]
v_mfma_f32_16x16x32_bf16 v[158:161], v[106:109], v[190:193], v[158:161]
ds_read_b64_tr_b16 a[158:159], v255 offset:2304
global_load_lds_dwordx4 v6, s[40:41] offset:2048
v_mfma_f32_16x16x32_bf16 v[86:89], a[132:135], v[106:109], v[86:89]
ds_read_b64_tr_b16 a[160:161], v255 offset:192
v_mfma_f32_16x16x32_bf16 v[90:93], a[136:139], v[106:109], v[90:93]
global_load_lds_dwordx4 v7, s[40:41] offset:3072
ds_read_b64_tr_b16 a[162:163], v255 offset:2496
v_mfma_f32_16x16x32_bf16 v[94:97], v[106:109], a[140:143], v[94:97]
s_add_u32 m0, s48, 86016
ds_read_b64_tr_b16 a[164:165], v255 offset:384
v_mfma_f32_16x16x32_bf16 v[110:113], a[132:135], v[186:189], v[110:113]
ds_read_b64_tr_b16 a[166:167], v255 offset:2688
v_mfma_f32_16x16x32_bf16 v[114:117], a[136:139], v[186:189], v[114:117]
global_load_lds_dwordx4 v8, s[40:41] offset:-4096
ds_read_b128 v[170:173], v52 offset:12288
v_mfma_f32_16x16x32_bf16 v[118:121], v[186:189], a[140:143], v[118:121]
s_sub_u32 s40, s40, 1769472
ds_read_b128 v[178:181], v52 offset:30720
v_mfma_f32_16x16x32_bf16 v[134:137], v[106:109], v[186:189], v[134:137]
v_mfma_f32_16x16x32_bf16 v[122:125], a[132:135], v[194:197], v[122:125]
v_mfma_f32_16x16x32_bf16 v[126:129], a[136:139], v[194:197], v[126:129]
s_subb_u32 s41, s41, 0
v_mfma_f32_16x16x32_bf16 v[130:133], v[194:197], a[140:143], v[130:133]
v_mfma_f32_16x16x32_bf16 v[158:161], v[106:109], v[194:197], v[158:161]
s_waitcnt vmcnt(9) lgkmcnt(0)
s_barrier
s_add_u32 m0, s49, 0
ds_read_b64_tr_b16 a[168:169], v255 offset:18432
v_mfma_f32_16x16x32_bf16 v[86:89], a[144:147], v[106:109], v[86:89]
v_mfma_f32_16x16x32_bf16 v[90:93], a[148:151], v[106:109], v[90:93]
ds_read_b64_tr_b16 a[170:171], v255 offset:20736
v_mfma_f32_16x16x32_bf16 v[94:97], v[106:109], a[152:155], v[94:97]
ds_read_b64_tr_b16 a[172:173], v255 offset:18624
v_mfma_f32_16x16x32_bf16 v[110:113], a[144:147], v[166:169], v[110:113]
global_load_lds_dwordx4 v11, s[42:43] offset:-2048 sc1
ds_read_b64_tr_b16 a[174:175], v255 offset:20928
v_mfma_f32_16x16x32_bf16 v[114:117], a[148:151], v[166:169], v[114:117]
ds_read_b64_tr_b16 a[176:177], v255 offset:18816
v_mfma_f32_16x16x32_bf16 v[118:121], v[166:169], a[152:155], v[118:121]
ds_read_b64_tr_b16 a[178:179], v255 offset:21120
v_mfma_f32_16x16x32_bf16 v[134:137], v[106:109], v[166:169], v[134:137]
global_load_lds_dwordx4 v12, s[42:43] offset:-1024 sc1
ds_read_b128 v[182:185], v51 offset:14336
v_mfma_f32_16x16x32_bf16 v[122:125], a[144:147], v[174:177], v[122:125]
ds_read_b128 v[190:193], v51 offset:32768
v_mfma_f32_16x16x32_bf16 v[126:129], a[148:151], v[174:177], v[126:129]
ds_read_b64_tr_b16 a[180:181], v255 offset:36864
v_mfma_f32_16x16x32_bf16 v[130:133], v[174:177], a[152:155], v[130:133]
global_load_lds_dwordx4 v13, s[42:43] offset:0 sc1
v_mfma_f32_16x16x32_bf16 v[158:161], v[106:109], v[174:177], v[158:161]
ds_read_b64_tr_b16 a[182:183], v255 offset:39168
v_mfma_f32_16x16x32_bf16 v[86:89], a[156:159], v[106:109], v[86:89]
ds_read_b64_tr_b16 a[184:185], v255 offset:37056
v_mfma_f32_16x16x32_bf16 v[90:93], a[160:163], v[106:109], v[90:93]
ds_read_b64_tr_b16 a[186:187], v255 offset:39360
v_mfma_f32_16x16x32_bf16 v[94:97], v[106:109], a[164:167], v[94:97]
global_load_lds_dwordx4 v14, s[42:43] offset:1024 sc1
ds_read_b64_tr_b16 a[188:189], v255 offset:37248
v_mfma_f32_16x16x32_bf16 v[110:113], a[156:159], v[170:173], v[110:113]
ds_read_b64_tr_b16 a[190:191], v255 offset:39552
v_mfma_f32_16x16x32_bf16 v[114:117], a[160:163], v[170:173], v[114:117]
ds_read_b128 v[186:189], v52 offset:14336
v_mfma_f32_16x16x32_bf16 v[118:121], v[170:173], a[164:167], v[118:121]
s_sub_u32 s42, s42, 18432
s_subb_u32 s43, s43, 0
ds_read_b128 v[194:197], v52 offset:32768
v_mfma_f32_16x16x32_bf16 v[134:137], v[106:109], v[170:173], v[134:137]
v_mfma_f32_16x16x32_bf16 v[122:125], a[156:159], v[178:181], v[122:125]
v_mfma_f32_16x16x32_bf16 v[126:129], a[160:163], v[178:181], v[126:129]
v_mfma_f32_16x16x32_bf16 v[130:133], v[178:181], a[164:167], v[130:133]
v_mfma_f32_16x16x32_bf16 v[158:161], v[106:109], v[178:181], v[158:161]
s_waitcnt vmcnt(4) lgkmcnt(0)
s_barrier
s_add_u32 m0, s49, 18432
ds_read_b64_tr_b16 a[192:193], v255 offset:55296
v_mfma_f32_16x16x32_bf16 v[86:89], a[168:171], v[106:109], v[86:89]
v_mfma_f32_16x16x32_bf16 v[90:93], a[172:175], v[106:109], v[90:93]
ds_read_b64_tr_b16 a[194:195], v255 offset:57600
v_mfma_f32_16x16x32_bf16 v[94:97], v[106:109], a[176:179], v[94:97]
ds_read_b64_tr_b16 a[196:197], v255 offset:55488
v_mfma_f32_16x16x32_bf16 v[110:113], a[168:171], v[182:185], v[110:113]
global_load_lds_dwordx4 v11, s[42:43] offset:-2048 sc1
ds_read_b64_tr_b16 a[198:199], v255 offset:57792
v_mfma_f32_16x16x32_bf16 v[114:117], a[172:175], v[182:185], v[114:117]
ds_read_b64_tr_b16 a[200:201], v255 offset:55680
v_mfma_f32_16x16x32_bf16 v[118:121], v[182:185], a[176:179], v[118:121]
ds_read_b64_tr_b16 a[202:203], v255 offset:57984
v_mfma_f32_16x16x32_bf16 v[134:137], v[106:109], v[182:185], v[134:137]
global_load_lds_dwordx4 v12, s[42:43] offset:-1024 sc1
ds_read_b128 v[166:169], v51 offset:16384
v_mfma_f32_16x16x32_bf16 v[122:125], a[168:171], v[190:193], v[122:125]
ds_read_b128 v[174:177], v51 offset:34816
v_mfma_f32_16x16x32_bf16 v[126:129], a[172:175], v[190:193], v[126:129]
ds_read_b64_tr_b16 a[204:205], v162 offset:8192
v_mfma_f32_16x16x32_bf16 v[130:133], v[190:193], a[176:179], v[130:133]
global_load_lds_dwordx4 v13, s[42:43] offset:0 sc1
ds_read_b64_tr_b16 a[206:207], v162 offset:10496
v_mfma_f32_16x16x32_bf16 v[158:161], v[106:109], v[190:193], v[158:161]
ds_read_b64_tr_b16 a[208:209], v162 offset:8384
v_mfma_f32_16x16x32_bf16 v[86:89], a[180:183], v[106:109], v[86:89]
ds_read_b64_tr_b16 a[210:211], v162 offset:10688
v_mfma_f32_16x16x32_bf16 v[90:93], a[184:187], v[106:109], v[90:93]
global_load_lds_dwordx4 v14, s[42:43] offset:1024 sc1
ds_read_b64_tr_b16 a[212:213], v162 offset:8576
v_mfma_f32_16x16x32_bf16 v[94:97], v[106:109], a[188:191], v[94:97]
ds_read_b64_tr_b16 a[214:215], v162 offset:10880
v_mfma_f32_16x16x32_bf16 v[110:113], a[180:183], v[186:189], v[110:113]
ds_read_b128 v[170:173], v52 offset:16384
v_mfma_f32_16x16x32_bf16 v[114:117], a[184:187], v[186:189], v[114:117]
s_sub_u32 s42, s42, 18432
s_subb_u32 s43, s43, 0
ds_read_b128 v[178:181], v52 offset:34816
v_mfma_f32_16x16x32_bf16 v[118:121], v[186:189], a[188:191], v[118:121]
v_mfma_f32_16x16x32_bf16 v[134:137], v[106:109], v[186:189], v[134:137]
v_mfma_f32_16x16x32_bf16 v[122:125], a[180:183], v[194:197], v[122:125]
v_mfma_f32_16x16x32_bf16 v[126:129], a[184:187], v[194:197], v[126:129]
v_mfma_f32_16x16x32_bf16 v[158:161], v[106:109], v[194:197], v[158:161]
s_waitcnt lgkmcnt(14)
v_mfma_f32_16x16x32_bf16 v[86:89], a[192:195], v[106:109], v[86:89]
s_waitcnt lgkmcnt(12)
v_mfma_f32_16x16x32_bf16 v[90:93], a[196:199], v[106:109], v[90:93]
s_waitcnt lgkmcnt(10)
v_mfma_f32_16x16x32_bf16 v[94:97], v[106:109], a[200:203], v[94:97]
s_waitcnt lgkmcnt(9)
v_mfma_f32_16x16x32_bf16 v[110:113], a[192:195], v[166:169], v[110:113]
v_mfma_f32_16x16x32_bf16 v[114:117], a[196:199], v[166:169], v[114:117]
v_mfma_f32_16x16x32_bf16 v[118:121], v[166:169], a[200:203], v[118:121]
v_mfma_f32_16x16x32_bf16 v[134:137], v[106:109], v[166:169], v[134:137]
s_waitcnt lgkmcnt(8)
v_mfma_f32_16x16x32_bf16 v[158:161], v[106:109], v[174:177], v[158:161]
s_waitcnt lgkmcnt(6)
v_mfma_f32_16x16x32_bf16 v[86:89], a[204:207], v[106:109], v[86:89]
s_waitcnt lgkmcnt(4)
v_mfma_f32_16x16x32_bf16 v[90:93], a[208:211], v[106:109], v[90:93]
s_waitcnt lgkmcnt(2)
v_mfma_f32_16x16x32_bf16 v[94:97], v[106:109], a[212:215], v[94:97]
s_waitcnt lgkmcnt(1)
v_mfma_f32_16x16x32_bf16 v[110:113], a[204:207], v[170:173], v[110:113]
v_mfma_f32_16x16x32_bf16 v[114:117], a[208:211], v[170:173], v[114:117]
v_mfma_f32_16x16x32_bf16 v[118:121], v[170:173], a[212:215], v[118:121]
v_mfma_f32_16x16x32_bf16 v[134:137], v[106:109], v[170:173], v[134:137]
s_waitcnt lgkmcnt(0)
v_mfma_f32_16x16x32_bf16 v[158:161], v[106:109], v[178:181], v[158:161]
s_nop 5
v_mul_f32 v202, 0x3ae38e39, v134
v_cvt_pk_bf16_f32 v216, -v202, -v202
v_lshlrev_b32 v216, 16, v216
ds_write_b32 v24, v216 offset:0
s_waitcnt lgkmcnt(0)
s_barrier
ds_read_b32 v216, v26
s_waitcnt lgkmcnt(0)
v_mul_f32 v216, 0x3ae38e39, v216
v_fma_f32 v203, -v202, v202, v216
v_max_f32 v203, 0, v203
v_cvt_pk_bf16_f32 v203, v203, v203
v_lshlrev_b32 v203, 16, v203
v_add_f32 v203, 0x3727c5ac, v203
v_cvt_pk_bf16_f32 v203, v203, v203
v_lshlrev_b32 v203, 16, v203
v_sqrt_f32 v203, v203
s_nop 0
v_cvt_pk_bf16_f32 v203, v203, v203
v_lshlrev_b32 v203, 16, v203
v_rcp_f32 v216, v203
s_nop 0
ds_write_b32 v24, v216 offset:128
ds_read_b128 v[170:173], v51 offset:32768
ds_read_b128 v[174:177], v52 offset:32768
ds_read_b128 v[178:181], v51 offset:34816
ds_read_b128 v[182:185], v52 offset:34816
v_mov_b32 v206, v32
v_mov_b32 v207, v33
v_mov_b32 v208, v34
v_mov_b32 v209, v35
v_mov_b32 v134, v158
v_mov_b32 v135, v159
v_mov_b32 v136, v160
v_mov_b32 v137, v161
)"
R"(s_waitcnt lgkmcnt(4)
s_barrier
ds_read_b32 v23, v24 offset:0
ds_read_b32 v148, v21
ds_read_b32 v95, v24 offset:192
ds_read_b32 v254, v27
ds_read_b32 v255, v28
ds_read_b128 v[198:201], v25 offset:0
ds_read_b64 v[158:159], v22 offset:41472
ds_read_b32 v140, v20
v_xor_b32 v51, 0x10000, v51
v_xor_b32 v52, 0x10000, v52
v_mul_f32 v202, 0x3ae38e39, v134
v_cvt_pk_bf16_f32 v216, -v202, -v202
v_lshlrev_b32 v216, 16, v216
ds_write_b32 v24, v216 offset:64
s_waitcnt lgkmcnt(8)
v_fma_f32 v210, v90, v23, v114
v_fma_f32 v211, v91, v23, v115
v_fma_f32 v212, v92, v23, v116
v_fma_f32 v213, v93, v23, v117
v_cvt_pk_bf16_f32 v146, v210, v211
v_cvt_pk_bf16_f32 v147, v212, v213
s_waitcnt lgkmcnt(0)
s_barrier
ds_read_b32 v216, v26
v_fma_f32 v210, v94, v198, v118
v_fma_f32 v211, v94, v199, v119
v_fma_f32 v212, v94, v200, v120
v_fma_f32 v213, v94, v201, v121
v_cvt_pk_bf16_f32 v154, v210, v211
v_cvt_pk_bf16_f32 v155, v212, v213
v_fma_f32 v210, v86, v23, v110
v_fma_f32 v211, v87, v23, v111
v_fma_f32 v212, v88, v23, v112
v_fma_f32 v213, v89, v23, v113
v_cvt_pk_bf16_f32 v138, v210, v211
v_cvt_pk_bf16_f32 v139, v212, v213
s_waitcnt lgkmcnt(0)
v_mul_f32 v216, 0x3ae38e39, v216
v_fma_f32 v203, -v202, v202, v216
v_max_f32 v203, 0, v203
v_cvt_pk_bf16_f32 v203, v203, v203
v_lshlrev_b32 v203, 16, v203
v_add_f32 v203, 0x3727c5ac, v203
v_cvt_pk_bf16_f32 v203, v203, v203
v_lshlrev_b32 v203, 16, v203
v_sqrt_f32 v203, v203
s_nop 0
v_cvt_pk_bf16_f32 v203, v203, v203
v_lshlrev_b32 v203, 16, v203
v_rcp_f32 v216, v203
s_nop 0
ds_write_b32 v24, v216 offset:192
s_waitcnt vmcnt(4) lgkmcnt(0)
s_barrier
s_mov_b32 s70, 32
s_waitcnt lgkmcnt(0)
s_nop 1
s_branch .Lqa_in42
.p2align 6
.Lqa_top52:
s_waitcnt lgkmcnt(6)
v_mfma_f32_16x16x32_bf16 v[134:137], v[106:109], v[0:3], 0
s_waitcnt lgkmcnt(5)
ds_read_b128 v[58:61], v52 offset:4096
ds_read_b32 v95, v24 offset:128
v_mfma_f32_16x16x32_bf16 v[134:137], v[106:109], v[4:7], v[134:137]
ds_read_b32 v254, v29
ds_read_b32 v255, v30
v_mfma_f32_16x16x32_bf16 v[110:113], a[0:3], v[0:3], 0
s_waitcnt lgkmcnt(7)
ds_read_b128 v[62:65], v51 offset:6144
ds_read_b128 v[198:201], v25 offset:64
v_mfma_f32_16x16x32_bf16 v[134:137], v[106:109], v[96:99], v[134:137]
s_waitcnt lgkmcnt(8)
ds_read_b128 v[66:69], v52 offset:6144
ds_read_b64 v[160:161], v22 offset:41472
v_mfma_f32_16x16x32_bf16 v[134:137], v[106:109], v[100:103], v[134:137]
ds_read_b32 v144, v20
v_fma_f32 v210, v90, v23, v126
v_mfma_f32_16x16x32_bf16 v[114:117], a[4:7], v[0:3], 0
s_waitcnt lgkmcnt(9)
ds_read_b128 v[70:73], v51 offset:8192
v_fma_f32 v211, v91, v23, v127
v_mfma_f32_16x16x32_bf16 v[134:137], v[106:109], v[54:57], v[134:137]
s_waitcnt lgkmcnt(9)
ds_read_b128 v[74:77], v52 offset:8192
v_fma_f32 v212, v92, v23, v128
v_mfma_f32_16x16x32_bf16 v[134:137], v[106:109], v[58:61], v[134:137]
v_fma_f32 v213, v93, v23, v129
v_cvt_pk_bf16_f32 v150, v210, v211
v_mfma_f32_16x16x32_bf16 v[118:121], v[0:3], a[8:11], 0
s_waitcnt lgkmcnt(6)
ds_read_b128 v[78:81], v51 offset:10240
v_cvt_pk_bf16_f32 v151, v212, v213
v_mfma_f32_16x16x32_bf16 v[134:137], v[106:109], v[62:65], v[134:137]
ds_read_b128 v[82:85], v52 offset:10240
v_mfma_f32_16x16x32_bf16 v[186:189], v[150:153], v[138:141], v[32:35]
s_waitcnt lgkmcnt(6)
v_max_f32 v10, v190, v191
v_max_f32 v44, v192, v193
v_mfma_f32_16x16x32_bf16 v[134:137], v[106:109], v[66:69], v[134:137]
v_fma_f32 v210, v94, v198, v130
v_fma_f32 v211, v94, v199, v131
v_mfma_f32_16x16x32_bf16 v[110:113], a[12:15], v[4:7], v[110:113]
s_waitcnt lgkmcnt(3)
ds_read_b128 v[162:165], v51 offset:12288
v_mul_f32 v95, 0x3eae1a93, v95
v_mfma_f32_16x16x32_bf16 v[134:137], v[106:109], v[70:73], v[134:137]
s_xor_b32 s49, s49, 0x10000
ds_read_b128 v[166:169], v52 offset:12288
s_waitcnt lgkmcnt(4)
v_mfma_f32_16x16x32_bf16 v[134:137], v[106:109], v[74:77], v[134:137]
s_add_u32 m0, s49, 0
v_mul_f32 v8, v254, v95
v_mul_f32 v9, v255, v95
v_mfma_f32_16x16x32_bf16 v[114:117], a[16:19], v[4:7], v[114:117]
s_waitcnt lgkmcnt(3)
ds_read_b128 v[170:173], v51 offset:14336
v_max3_f32 v10, v186, v187, v10
v_mfma_f32_16x16x32_bf16 v[134:137], v[106:109], v[78:81], v[134:137]
s_waitcnt lgkmcnt(3)
ds_read_b128 v[174:177], v52 offset:14336
v_max3_f32 v44, v188, v189, v44
v_mfma_f32_16x16x32_bf16 v[134:137], v[106:109], v[82:85], v[134:137]
v_mul_f32 v10, v10, v8
v_mul_f32 v44, v44, v9
v_mfma_f32_16x16x32_bf16 v[118:121], v[4:7], a[20:23], v[118:121]
s_waitcnt lgkmcnt(3)
ds_read_b128 v[178:181], v51 offset:16384
v_max_f32 v45, v10, v44
v_mfma_f32_16x16x32_bf16 v[134:137], v[106:109], v[162:165], v[134:137]
s_waitcnt lgkmcnt(3)
ds_read_b128 v[182:185], v52 offset:16384
v_mov_b32 v46, v45
v_mfma_f32_16x16x32_bf16 v[134:137], v[106:109], v[166:169], v[134:137]
s_nop 0
v_permlane16_swap_b32 v45, v46
v_mfma_f32_16x16x32_bf16 v[110:113], a[24:27], v[96:99], v[110:113]
s_waitcnt lgkmcnt(3)
global_load_lds_dwordx4 v11, s[42:43] offset:-2048 sc1
v_max_f32 v45, v45, v46
v_mfma_f32_16x16x32_bf16 v[134:137], v[106:109], v[170:173], v[134:137]
s_waitcnt lgkmcnt(2)
v_mov_b32 v46, v45
v_fma_f32 v212, v94, v200, v132
v_mfma_f32_16x16x32_bf16 v[134:137], v[106:109], v[174:177], v[134:137]
v_permlane32_swap_b32 v45, v46
v_mfma_f32_16x16x32_bf16 v[114:117], a[28:31], v[96:99], v[114:117]
s_waitcnt lgkmcnt(1)
v_max_f32 v45, v45, v46
v_sub_f32 v10, v10, v45
v_mfma_f32_16x16x32_bf16 v[134:137], v[106:109], v[178:181], v[134:137]
s_waitcnt lgkmcnt(0)
v_sub_f32 v44, v44, v45
v_fma_f32 v213, v94, v201, v133
v_mfma_f32_16x16x32_bf16 v[134:137], v[106:109], v[182:185], v[134:137]
v_exp_f32 v214, v10
v_mfma_f32_16x16x32_bf16 v[118:121], v[96:99], a[32:35], v[118:121]
v_exp_f32 v215, v44
v_mfma_f32_16x16x32_bf16 v[110:113], a[36:39], v[100:103], v[110:113]
v_add_f32 v45, v214, v215
v_mov_b32 v46, v45
v_mfma_f32_16x16x32_bf16 v[114:117], a[40:43], v[100:103], v[114:117]
s_nop 0
v_mul_f32 v202, 0x3ae38e39, v134
v_cvt_pk_bf16_f32 v216, -v202, -v202
v_mfma_f32_16x16x32_bf16 v[118:121], v[100:103], a[44:47], v[118:121]
v_lshlrev_b32 v216, 16, v216
global_load_lds_dwordx4 v12, s[42:43] offset:-1024 sc1
v_mfma_f32_16x16x32_bf16 v[110:113], a[48:51], v[54:57], v[110:113]
ds_write_b32 v24, v216 offset:0
v_mfma_f32_16x16x32_bf16 v[114:117], a[52:55], v[54:57], v[114:117]
v_mul_f32 v214, v214, v254
v_cvt_pk_bf16_f32 v156, v210, v211
v_mfma_f32_16x16x32_bf16 v[118:121], v[54:57], a[56:59], v[118:121]
v_permlane16_swap_b32 v45, v46
v_mfma_f32_16x16x32_bf16 v[110:113], a[60:63], v[58:61], v[110:113]
v_add_f32 v45, v45, v46
v_mov_b32 v46, v45
v_mfma_f32_16x16x32_bf16 v[114:117], a[64:67], v[58:61], v[114:117]
v_mul_f32 v215, v215, v255
v_cvt_pk_bf16_f32 v157, v212, v213
v_mfma_f32_16x16x32_bf16 v[118:121], v[58:61], a[68:71], v[118:121]
v_mfma_f32_16x16x32_bf16 v[110:113], a[72:75], v[62:65], v[110:113]
s_waitcnt lgkmcnt(0)
s_barrier
v_mfma_f32_16x16x32_bf16 v[114:117], a[76:79], v[62:65], v[114:117]
global_load_lds_dwordx4 v13, s[42:43] offset:0 sc1
ds_read_b32 v216, v26
v_mfma_f32_16x16x32_bf16 v[118:121], v[62:65], a[80:83], v[118:121]
v_permlane32_swap_b32 v45, v46
v_mfma_f32_16x16x32_bf16 v[110:113], a[84:87], v[66:69], v[110:113]
v_add_f32 v45, v45, v46
v_fma_f32 v210, v86, v23, v122
v_mfma_f32_16x16x32_bf16 v[114:117], a[88:91], v[66:69], v[114:117]
v_rcp_f32 v45, v45
v_mfma_f32_16x16x32_bf16 v[118:121], v[66:69], a[92:95], v[118:121]
v_mul_f32 v214, v214, v45
v_mul_f32 v215, v215, v45
v_mfma_f32_16x16x32_bf16 v[110:113], a[96:99], v[70:73], v[110:113]
v_cvt_pk_bf16_f32 v214, v214, v214
v_cvt_pk_bf16_f32 v215, v215, v215
v_mfma_f32_16x16x32_bf16 v[114:117], a[100:103], v[70:73], v[114:117]
v_and_b32 v196, v214, v40
v_and_b32 v197, v215, v41
v_mfma_f32_16x16x32_bf16 v[118:121], v[70:73], a[104:107], v[118:121]
v_and_b32 v194, v214, v42
v_and_b32 v195, v215, v43
v_mfma_f32_16x16x32_bf16 v[110:113], a[108:111], v[74:77], v[110:113]
global_load_lds_dwordx4 v14, s[42:43] offset:1024 sc1
s_add_u32 s42, s42, s46
s_addc_u32 s43, s43, s47
v_mfma_f32_16x16x32_bf16 v[190:193], v[154:157], v[194:197], 0
v_mfma_f32_16x16x32_bf16 v[186:189], v[158:161], v[194:197], 0
s_waitcnt lgkmcnt(0)
v_mul_f32 v216, 0x3ae38e39, v216
v_fma_f32 v203, -v202, v202, v216
v_mfma_f32_16x16x32_bf16 v[114:117], a[112:115], v[74:77], v[114:117]
v_max_f32 v203, 0, v203
v_cvt_pk_bf16_f32 v203, v203, v203
v_mfma_f32_16x16x32_bf16 v[118:121], v[74:77], a[116:119], v[118:121]
v_lshlrev_b32 v203, 16, v203
v_add_f32 v203, 0x3727c5ac, v203
v_mfma_f32_16x16x32_bf16 v[110:113], a[120:123], v[78:81], v[110:113]
v_cvt_pk_bf16_f32 v203, v203, v203
v_lshlrev_b32 v203, 16, v203
v_mfma_f32_16x16x32_bf16 v[114:117], a[124:127], v[78:81], v[114:117]
v_cvt_pk_bf16_f32 v104, v190, v191
v_cvt_pk_bf16_f32 v105, v192, v193
v_mfma_f32_16x16x32_bf16 v[118:121], v[78:81], a[128:131], v[118:121]
ds_write_b64 v18, v[104:105]
v_mfma_f32_16x16x32_bf16 v[110:113], a[132:135], v[82:85], v[110:113]
v_cvt_pk_bf16_f32 v214, v188, v189
v_fma_f32 v211, v87, v23, v123
v_mfma_f32_16x16x32_bf16 v[114:117], a[136:139], v[82:85], v[114:117]
ds_write_b32 v19, v214
v_mfma_f32_16x16x32_bf16 v[118:121], v[82:85], a[140:143], v[118:121]
v_sqrt_f32 v203, v203
v_mfma_f32_16x16x32_bf16 v[110:113], a[144:147], v[162:165], v[110:113]
v_cvt_pk_bf16_f32 v203, v203, v203
v_lshlrev_b32 v203, 16, v203
v_mfma_f32_16x16x32_bf16 v[114:117], a[148:151], v[162:165], v[114:117]
v_rcp_f32 v216, v203
v_mfma_f32_16x16x32_bf16 v[118:121], v[162:165], a[152:155], v[118:121]
ds_write_b32 v24, v216 offset:128
v_mfma_f32_16x16x32_bf16 v[110:113], a[156:159], v[166:169], v[110:113]
v_fma_f32 v212, v88, v23, v124
v_fma_f32 v213, v89, v23, v125
v_mfma_f32_16x16x32_bf16 v[114:117], a[160:163], v[166:169], v[114:117]
v_cvt_pk_bf16_f32 v142, v210, v211
v_cvt_pk_bf16_f32 v143, v212, v213
v_mfma_f32_16x16x32_bf16 v[118:121], v[166:169], a[164:167], v[118:121]
v_mfma_f32_16x16x32_bf16 v[110:113], a[168:171], v[170:173], v[110:113]
v_mfma_f32_16x16x32_bf16 v[114:117], a[172:175], v[170:173], v[114:117]
v_mfma_f32_16x16x32_bf16 v[118:121], v[170:173], a[176:179], v[118:121]
v_mfma_f32_16x16x32_bf16 v[110:113], a[180:183], v[174:177], v[110:113]
v_mfma_f32_16x16x32_bf16 v[114:117], a[184:187], v[174:177], v[114:117]
s_waitcnt vmcnt(5) lgkmcnt(0)
s_barrier
v_mfma_f32_16x16x32_bf16 v[118:121], v[174:177], a[188:191], v[118:121]
ds_read_b128 v[0:3], v51 offset:18432
v_mfma_f32_16x16x32_bf16 v[110:113], a[192:195], v[178:181], v[110:113]
ds_read_b128 v[4:7], v52 offset:18432
ds_read_b32 v23, v24 offset:0
v_mfma_f32_16x16x32_bf16 v[114:117], a[196:199], v[178:181], v[114:117]
s_mov_b64 exec, 1
global_store_dword v47, v48, s[38:39] sc1
s_mov_b64 exec, -1
v_mfma_f32_16x16x32_bf16 v[190:193], v[150:153], v[142:145], v[36:39]
v_mfma_f32_16x16x32_bf16 v[118:121], v[178:181], a[200:203], v[118:121]
ds_read_b128 v[96:99], v51 offset:20480
v_add_u32 v48, 1, v48
v_mfma_f32_16x16x32_bf16 v[110:113], a[204:207], v[182:185], v[110:113]
ds_read_b128 v[100:103], v52 offset:20480
ds_read_b32 v148, v21
v_mfma_f32_16x16x32_bf16 v[114:117], a[208:211], v[182:185], v[114:117]
ds_read_b32 v95, v24 offset:192
ds_read_b32 v254, v27
v_mfma_f32_16x16x32_bf16 v[118:121], v[182:185], a[212:215], v[118:121]
s_waitcnt lgkmcnt(7)
ds_read_b128 v[54:57], v51 offset:22528
ds_read_b32 v255, v28
v_mfma_f32_16x16x32_bf16 v[134:137], v[106:109], v[0:3], 0
s_waitcnt lgkmcnt(8)
ds_read_b128 v[58:61], v52 offset:22528
ds_read_b128 v[198:201], v25 offset:0
v_mfma_f32_16x16x32_bf16 v[134:137], v[106:109], v[4:7], v[134:137]
ds_read_b64 v[158:159], v22 offset:41472
ds_read_b32 v140, v20
v_mfma_f32_16x16x32_bf16 v[122:125], a[0:3], v[0:3], 0
s_waitcnt lgkmcnt(11)
ds_read_b128 v[62:65], v51 offset:24576
v_fma_f32 v210, v90, v23, v114
s_waitcnt lgkmcnt(11)
v_mfma_f32_16x16x32_bf16 v[134:137], v[106:109], v[96:99], v[134:137]
s_waitcnt lgkmcnt(10)
ds_read_b128 v[66:69], v52 offset:24576
v_fma_f32 v211, v91, v23, v115
v_mfma_f32_16x16x32_bf16 v[134:137], v[106:109], v[100:103], v[134:137]
v_fma_f32 v212, v92, v23, v116
v_fma_f32 v213, v93, v23, v117
v_mfma_f32_16x16x32_bf16 v[126:129], a[4:7], v[0:3], 0
s_waitcnt lgkmcnt(7)
ds_read_b128 v[70:73], v51 offset:26624
v_cvt_pk_bf16_f32 v146, v210, v211
v_mfma_f32_16x16x32_bf16 v[134:137], v[106:109], v[54:57], v[134:137]
s_waitcnt lgkmcnt(6)
ds_read_b128 v[74:77], v52 offset:26624
v_cvt_pk_bf16_f32 v147, v212, v213
v_mfma_f32_16x16x32_bf16 v[134:137], v[106:109], v[58:61], v[134:137]
s_nop 0
v_mfma_f32_16x16x32_bf16 v[186:189], v[146:149], v[142:145], v[206:209]
v_max_f32 v10, v190, v191
v_max_f32 v44, v192, v193
v_mfma_f32_16x16x32_bf16 v[130:133], v[0:3], a[8:11], 0
s_waitcnt lgkmcnt(6)
ds_read_b128 v[78:81], v51 offset:28672
v_fma_f32 v210, v94, v198, v118
s_waitcnt lgkmcnt(4)
v_mfma_f32_16x16x32_bf16 v[134:137], v[106:109], v[62:65], v[134:137]
s_waitcnt lgkmcnt(3)
ds_read_b128 v[82:85], v52 offset:28672
v_mul_f32 v95, 0x3eae1a93, v95
v_mfma_f32_16x16x32_bf16 v[134:137], v[106:109], v[66:69], v[134:137]
v_mul_f32 v8, v254, v95
v_mul_f32 v9, v255, v95
v_mfma_f32_16x16x32_bf16 v[122:125], a[12:15], v[4:7], v[122:125]
s_waitcnt lgkmcnt(3)
ds_read_b128 v[162:165], v51 offset:30720
v_max3_f32 v10, v186, v187, v10
v_mfma_f32_16x16x32_bf16 v[134:137], v[106:109], v[70:73], v[134:137]
s_add_u32 m0, s49, 18432
ds_read_b128 v[166:169], v52 offset:30720
s_waitcnt lgkmcnt(4)
v_mfma_f32_16x16x32_bf16 v[134:137], v[106:109], v[74:77], v[134:137]
v_max3_f32 v44, v188, v189, v44
v_mul_f32 v10, v10, v8
v_mfma_f32_16x16x32_bf16 v[126:129], a[16:19], v[4:7], v[126:129]
s_waitcnt lgkmcnt(3)
ds_read_b128 v[170:173], v51 offset:32768
v_mul_f32 v44, v44, v9
v_mfma_f32_16x16x32_bf16 v[134:137], v[106:109], v[78:81], v[134:137]
s_waitcnt lgkmcnt(3)
ds_read_b128 v[174:177], v52 offset:32768
v_max_f32 v45, v10, v44
v_mfma_f32_16x16x32_bf16 v[134:137], v[106:109], v[82:85], v[134:137]
v_mov_b32 v46, v45
v_fma_f32 v211, v94, v199, v119
v_mfma_f32_16x16x32_bf16 v[130:133], v[4:7], a[20:23], v[130:133]
s_waitcnt lgkmcnt(3)
ds_read_b128 v[178:181], v51 offset:34816
v_fma_f32 v212, v94, v200, v120
v_mfma_f32_16x16x32_bf16 v[134:137], v[106:109], v[162:165], v[134:137]
s_waitcnt lgkmcnt(3)
ds_read_b128 v[182:185], v52 offset:34816
v_fma_f32 v213, v94, v201, v121
v_mfma_f32_16x16x32_bf16 v[134:137], v[106:109], v[166:169], v[134:137]
global_load_lds_dwordx4 v11, s[42:43] offset:-2048 sc1
v_cvt_pk_bf16_f32 v154, v210, v211
v_mfma_f32_16x16x32_bf16 v[122:125], a[24:27], v[96:99], v[122:125]
s_waitcnt lgkmcnt(3)
v_permlane16_swap_b32 v45, v46
v_mfma_f32_16x16x32_bf16 v[134:137], v[106:109], v[170:173], v[134:137]
s_waitcnt lgkmcnt(2)
v_max_f32 v45, v45, v46
v_mov_b32 v46, v45
v_mfma_f32_16x16x32_bf16 v[134:137], v[106:109], v[174:177], v[134:137]
s_nop 0
v_permlane32_swap_b32 v45, v46
v_mfma_f32_16x16x32_bf16 v[126:129], a[28:31], v[96:99], v[126:129]
s_waitcnt lgkmcnt(1)
v_max_f32 v45, v45, v46
v_sub_f32 v10, v10, v45
v_mfma_f32_16x16x32_bf16 v[134:137], v[106:109], v[178:181], v[134:137]
s_waitcnt lgkmcnt(0)
v_sub_f32 v44, v44, v45
v_cvt_pk_bf16_f32 v155, v212, v213
v_mfma_f32_16x16x32_bf16 v[134:137], v[106:109], v[182:185], v[134:137]
v_exp_f32 v214, v10
v_mfma_f32_16x16x32_bf16 v[130:133], v[96:99], a[32:35], v[130:133]
v_exp_f32 v215, v44
v_mfma_f32_16x16x32_bf16 v[122:125], a[36:39], v[100:103], v[122:125]
global_load_lds_dwordx4 v12, s[42:43] offset:-1024 sc1
v_add_f32 v45, v214, v215
v_mfma_f32_16x16x32_bf16 v[126:129], a[40:43], v[100:103], v[126:129]
s_nop 0
v_mul_f32 v202, 0x3ae38e39, v134
v_cvt_pk_bf16_f32 v216, -v202, -v202
v_mfma_f32_16x16x32_bf16 v[130:133], v[100:103], a[44:47], v[130:133]
v_lshlrev_b32 v216, 16, v216
v_mov_b32 v46, v45
v_mfma_f32_16x16x32_bf16 v[122:125], a[48:51], v[54:57], v[122:125]
ds_write_b32 v24, v216 offset:64
v_mfma_f32_16x16x32_bf16 v[126:129], a[52:55], v[54:57], v[126:129]
v_mul_f32 v214, v214, v254
v_fma_f32 v210, v86, v23, v110
v_mfma_f32_16x16x32_bf16 v[130:133], v[54:57], a[56:59], v[130:133]
v_permlane16_swap_b32 v45, v46
)"
R"(v_mfma_f32_16x16x32_bf16 v[122:125], a[60:63], v[58:61], v[122:125]
v_add_f32 v45, v45, v46
v_mov_b32 v46, v45
v_mfma_f32_16x16x32_bf16 v[126:129], a[64:67], v[58:61], v[126:129]
v_mul_f32 v215, v215, v255
v_fma_f32 v211, v87, v23, v111
v_mfma_f32_16x16x32_bf16 v[130:133], v[58:61], a[68:71], v[130:133]
v_mfma_f32_16x16x32_bf16 v[122:125], a[72:75], v[62:65], v[122:125]
s_waitcnt lgkmcnt(0)
s_barrier
v_mfma_f32_16x16x32_bf16 v[126:129], a[76:79], v[62:65], v[126:129]
global_load_lds_dwordx4 v13, s[42:43] offset:0 sc1
ds_read_b32 v216, v26
v_mfma_f32_16x16x32_bf16 v[130:133], v[62:65], a[80:83], v[130:133]
v_permlane32_swap_b32 v45, v46
v_mfma_f32_16x16x32_bf16 v[122:125], a[84:87], v[66:69], v[122:125]
v_add_f32 v45, v45, v46
v_fma_f32 v212, v88, v23, v112
v_mfma_f32_16x16x32_bf16 v[126:129], a[88:91], v[66:69], v[126:129]
v_rcp_f32 v45, v45
v_mfma_f32_16x16x32_bf16 v[130:133], v[66:69], a[92:95], v[130:133]
v_mul_f32 v214, v214, v45
v_mul_f32 v215, v215, v45
v_mfma_f32_16x16x32_bf16 v[122:125], a[96:99], v[70:73], v[122:125]
v_cvt_pk_bf16_f32 v214, v214, v214
v_cvt_pk_bf16_f32 v215, v215, v215
v_mfma_f32_16x16x32_bf16 v[126:129], a[100:103], v[70:73], v[126:129]
v_and_b32 v194, v214, v40
v_and_b32 v195, v215, v41
v_mfma_f32_16x16x32_bf16 v[130:133], v[70:73], a[104:107], v[130:133]
v_and_b32 v196, v214, v42
v_and_b32 v197, v215, v43
v_mfma_f32_16x16x32_bf16 v[122:125], a[108:111], v[74:77], v[122:125]
global_load_lds_dwordx4 v14, s[42:43] offset:1024 sc1
s_sub_u32 s42, s42, 18432
s_subb_u32 s43, s43, 0
v_mfma_f32_16x16x32_bf16 v[190:193], v[154:157], v[194:197], 0
v_mfma_f32_16x16x32_bf16 v[186:189], v[158:161], v[194:197], 0
s_waitcnt lgkmcnt(0)
v_mul_f32 v216, 0x3ae38e39, v216
v_fma_f32 v203, -v202, v202, v216
v_mfma_f32_16x16x32_bf16 v[126:129], a[112:115], v[74:77], v[126:129]
v_max_f32 v203, 0, v203
v_cvt_pk_bf16_f32 v203, v203, v203
v_mfma_f32_16x16x32_bf16 v[130:133], v[74:77], a[116:119], v[130:133]
v_lshlrev_b32 v203, 16, v203
v_add_f32 v203, 0x3727c5ac, v203
v_mfma_f32_16x16x32_bf16 v[122:125], a[120:123], v[78:81], v[122:125]
v_cvt_pk_bf16_f32 v203, v203, v203
v_lshlrev_b32 v203, 16, v203
v_mfma_f32_16x16x32_bf16 v[126:129], a[124:127], v[78:81], v[126:129]
v_cvt_pk_bf16_f32 v104, v190, v191
v_cvt_pk_bf16_f32 v105, v192, v193
v_mfma_f32_16x16x32_bf16 v[130:133], v[78:81], a[128:131], v[130:133]
ds_write_b64 v18, v[104:105]
v_mfma_f32_16x16x32_bf16 v[122:125], a[132:135], v[82:85], v[122:125]
v_cvt_pk_bf16_f32 v214, v188, v189
v_fma_f32 v213, v89, v23, v113
v_mfma_f32_16x16x32_bf16 v[126:129], a[136:139], v[82:85], v[126:129]
ds_write_b32 v19, v214
v_mfma_f32_16x16x32_bf16 v[130:133], v[82:85], a[140:143], v[130:133]
v_sqrt_f32 v203, v203
v_mfma_f32_16x16x32_bf16 v[122:125], a[144:147], v[162:165], v[122:125]
v_cvt_pk_bf16_f32 v203, v203, v203
v_lshlrev_b32 v203, 16, v203
v_mfma_f32_16x16x32_bf16 v[126:129], a[148:151], v[162:165], v[126:129]
v_rcp_f32 v216, v203
v_mfma_f32_16x16x32_bf16 v[130:133], v[162:165], a[152:155], v[130:133]
ds_write_b32 v24, v216 offset:192
v_mfma_f32_16x16x32_bf16 v[122:125], a[156:159], v[166:169], v[122:125]
v_cvt_pk_bf16_f32 v138, v210, v211
v_cvt_pk_bf16_f32 v139, v212, v213
v_mfma_f32_16x16x32_bf16 v[126:129], a[160:163], v[166:169], v[126:129]
v_mfma_f32_16x16x32_bf16 v[130:133], v[166:169], a[164:167], v[130:133]
v_mfma_f32_16x16x32_bf16 v[122:125], a[168:171], v[170:173], v[122:125]
v_mfma_f32_16x16x32_bf16 v[126:129], a[172:175], v[170:173], v[126:129]
v_xor_b32 v51, 0x10000, v51
v_xor_b32 v52, 0x10000, v52
v_mfma_f32_16x16x32_bf16 v[130:133], v[170:173], a[176:179], v[130:133]
v_mfma_f32_16x16x32_bf16 v[122:125], a[180:183], v[174:177], v[122:125]
v_mfma_f32_16x16x32_bf16 v[126:129], a[184:187], v[174:177], v[126:129]
s_waitcnt vmcnt(5) lgkmcnt(0)
s_barrier
.Lqa_in42:
v_mfma_f32_16x16x32_bf16 v[130:133], v[174:177], a[188:191], v[130:133]
s_cmp_eq_u32 s70, s73
s_cbranch_scc1 .Lqa_hk43
.Lqa_hr44:
ds_read_b128 v[0:3], v51 offset:0
v_mfma_f32_16x16x32_bf16 v[122:125], a[192:195], v[178:181], v[122:125]
ds_read_b128 v[4:7], v52 offset:0
ds_read_b32 v23, v24 offset:64
v_mfma_f32_16x16x32_bf16 v[126:129], a[196:199], v[178:181], v[126:129]
s_mov_b64 exec, 1
global_store_dword v47, v48, s[38:39] sc1
s_mov_b64 exec, -1
v_mfma_f32_16x16x32_bf16 v[190:193], v[146:149], v[138:141], v[36:39]
v_mfma_f32_16x16x32_bf16 v[130:133], v[178:181], a[200:203], v[130:133]
ds_read_b128 v[96:99], v51 offset:2048
v_add_u32 v48, 1, v48
v_mfma_f32_16x16x32_bf16 v[122:125], a[204:207], v[182:185], v[122:125]
ds_read_b128 v[100:103], v52 offset:2048
ds_read_b32 v152, v21
v_mfma_f32_16x16x32_bf16 v[126:129], a[208:211], v[182:185], v[126:129]
v_mfma_f32_16x16x32_bf16 v[130:133], v[182:185], a[212:215], v[130:133]
s_sub_u32 s70, s70, 1
ds_read_b128 v[54:57], v51 offset:4096
s_cbranch_scc0 .Lqa_top52
s_waitcnt lgkmcnt(0)
ds_read_b32 v95, v24 offset:128
ds_read_b32 v254, v29
ds_read_b32 v255, v30
ds_read_b128 v[198:201], v25 offset:64
ds_read_b64 v[160:161], v22 offset:41472
ds_read_b32 v144, v20
v_fma_f32 v210, v90, v23, v126
v_fma_f32 v211, v91, v23, v127
v_fma_f32 v212, v92, v23, v128
v_fma_f32 v213, v93, v23, v129
v_cvt_pk_bf16_f32 v150, v210, v211
v_cvt_pk_bf16_f32 v151, v212, v213
s_nop 1
v_mfma_f32_16x16x32_bf16 v[186:189], v[150:153], v[138:141], v[32:35]
s_waitcnt lgkmcnt(2)
v_max_f32 v10, v190, v191
v_max_f32 v44, v192, v193
v_fma_f32 v210, v94, v198, v130
v_fma_f32 v211, v94, v199, v131
v_mul_f32 v95, 0x3eae1a93, v95
v_mul_f32 v8, v254, v95
v_mul_f32 v9, v255, v95
v_max3_f32 v10, v186, v187, v10
v_max3_f32 v44, v188, v189, v44
v_mul_f32 v10, v10, v8
v_mul_f32 v44, v44, v9
v_max_f32 v45, v10, v44
v_mov_b32 v46, v45
s_nop 1
v_permlane16_swap_b32 v45, v46
v_max_f32 v45, v45, v46
v_mov_b32 v46, v45
v_fma_f32 v212, v94, v200, v132
s_nop 0
v_permlane32_swap_b32 v45, v46
v_max_f32 v45, v45, v46
v_sub_f32 v10, v10, v45
v_sub_f32 v44, v44, v45
v_fma_f32 v213, v94, v201, v133
v_exp_f32 v214, v10
v_exp_f32 v215, v44
s_nop 0
v_add_f32 v45, v214, v215
v_mov_b32 v46, v45
v_mul_f32 v214, v214, v254
v_cvt_pk_bf16_f32 v156, v210, v211
v_permlane16_swap_b32 v45, v46
v_add_f32 v45, v45, v46
v_mov_b32 v46, v45
v_mul_f32 v215, v215, v255
v_cvt_pk_bf16_f32 v157, v212, v213
s_waitcnt lgkmcnt(0)
s_barrier
v_permlane32_swap_b32 v45, v46
v_add_f32 v45, v45, v46
v_fma_f32 v210, v86, v23, v122
v_rcp_f32 v45, v45
s_nop 0
v_mul_f32 v214, v214, v45
v_mul_f32 v215, v215, v45
v_cvt_pk_bf16_f32 v214, v214, v214
v_cvt_pk_bf16_f32 v215, v215, v215
v_and_b32 v196, v214, v40
v_and_b32 v197, v215, v41
v_and_b32 v194, v214, v42
v_and_b32 v195, v215, v43
s_nop 1
v_mfma_f32_16x16x32_bf16 v[190:193], v[154:157], v[194:197], 0
v_mfma_f32_16x16x32_bf16 v[186:189], v[158:161], v[194:197], 0
s_nop 6
v_cvt_pk_bf16_f32 v104, v190, v191
v_cvt_pk_bf16_f32 v105, v192, v193
ds_write_b64 v18, v[104:105]
v_cvt_pk_bf16_f32 v214, v188, v189
v_fma_f32 v211, v87, v23, v123
ds_write_b32 v19, v214
v_fma_f32 v212, v88, v23, v124
v_fma_f32 v213, v89, v23, v125
v_cvt_pk_bf16_f32 v142, v210, v211
v_cvt_pk_bf16_f32 v143, v212, v213
s_waitcnt vmcnt(0)
s_waitcnt lgkmcnt(0)
s_barrier
s_mov_b64 exec, 1
global_store_dword v47, v48, s[38:39] sc1
s_mov_b64 exec, -1
v_add_u32 v48, 8, v48
s_waitcnt vmcnt(1)
s_barrier
s_mov_b64 exec, 1
global_store_dword v47, v48, s[38:39] sc1
s_mov_b64 exec, -1
s_cmp_lg_u32 s53, 0
s_cbranch_scc1 .Lqa_n53
global_load_dword v53, v49, s[38:39] sc1
s_waitcnt vmcnt(0)
v_sub_u32 v53, v53, v50
v_cmp_lt_u32 vcc, 0x7f, v53
s_cbranch_vccnz .Lqa_sp54
.Lqa_rt55:
v_add_u32 v49, 102400, v49
global_load_dword v53, v49, s[38:39] sc1
s_waitcnt vmcnt(0)
v_sub_u32 v53, v53, v50
v_cmp_lt_u32 vcc, 0x7f, v53
s_cbranch_vccnz .Lqa_sp56
.Lqa_rt57:
v_add_u32 v49, 102400, v49
global_load_dword v53, v49, s[38:39] sc1
s_waitcnt vmcnt(0)
v_sub_u32 v53, v53, v50
v_cmp_lt_u32 vcc, 0x7f, v53
s_cbranch_vccnz .Lqa_sp58
.Lqa_rt59:
v_add_u32 v49, 102400, v49
global_load_dword v53, v49, s[38:39] sc1
s_waitcnt vmcnt(0)
v_sub_u32 v53, v53, v50
v_cmp_lt_u32 vcc, 0x7f, v53
s_cbranch_vccnz .Lqa_sp60
.Lqa_rt61:
v_add_u32 v49, 102400, v49
s_load_dword s75, s[38:39], 0x0
s_waitcnt vmcnt(0) lgkmcnt(0)
s_add_u32 s75, s75, 1
v_mov_b32 v53, s75
v_mov_b32 v49, 0
global_store_dword v49, v53, s[38:39] sc1
.Lqa_n53:
s_endpgm
.Lqa_hk6:
s_cmp_lg_u32 s70, 18
s_cbranch_scc1 .Lqa_e8
s_add_u32 s42, s42, s50
s_addc_u32 s43, s43, 0
s_mov_b64 s[76:77], -1
s_mov_b64 s[46:47], s[44:45]
s_mov_b32 s73, 17
s_nop 1
s_branch .Lqa_hr7
.Lqa_e8:
s_cmp_lg_u32 s70, 17
s_cbranch_scc1 .Lqa_e9
s_mov_b32 s46, -18432
s_mov_b32 s47, -1
v_or_b32 v234, s52, v234
v_or_b32 v235, s52, v235
v_or_b32 v236, s52, v236
v_or_b32 v237, s52, v237
s_mov_b32 s73, 16
s_nop 1
s_branch .Lqa_hr7
.Lqa_e9:
s_cmp_lg_u32 s70, 16
s_cbranch_scc1 .Lqa_e10
v_mov_b32 v234, v32
v_mov_b32 v235, v33
v_mov_b32 v236, v34
v_mov_b32 v237, v35
s_mov_b32 s73, 15
s_nop 1
s_branch .Lqa_hr7
.Lqa_e10:
s_cmp_lg_u32 s70, 15
s_cbranch_scc1 .Lqa_e11
v_subrev_u32 v16, 18432, v16
s_mov_b32 s73, 3
s_nop 1
s_branch .Lqa_hr7
.Lqa_e11:
s_cmp_lg_u32 s70, 3
s_cbranch_scc1 .Lqa_e12
s_mov_b32 s73, 2
s_nop 1
s_branch .Lqa_hr7
.Lqa_e12:
s_cmp_lg_u32 s70, 2
s_cbranch_scc1 .Lqa_e13
s_add_u32 s46, s46, s50
s_addc_u32 s47, s47, 0
s_mov_b32 s73, 0
s_nop 1
s_branch .Lqa_hr7
.Lqa_e13:
s_cmp_lg_u32 s70, 0
s_cbranch_scc1 .Lqa_e14
v_or_b32 v32, s52, v32
v_or_b32 v33, s52, v33
v_or_b32 v34, s52, v34
v_or_b32 v35, s52, v35
s_mov_b32 s73, 999
s_nop 1
s_branch .Lqa_hr7
.Lqa_e14:
s_branch .Lqa_hr7
.Lqa_hk17:
s_cmp_lg_u32 s70, 18
s_cbranch_scc1 .Lqa_e19
s_add_u32 s42, s42, s50
s_addc_u32 s43, s43, 0
s_mov_b64 s[76:77], -1
s_mov_b64 s[46:47], s[44:45]
s_mov_b32 s73, 17
s_nop 1
s_branch .Lqa_hr18
.Lqa_e19:
s_cmp_lg_u32 s70, 17
s_cbranch_scc1 .Lqa_e20
s_mov_b32 s46, -18432
s_mov_b32 s47, -1
v_or_b32 v234, s52, v234
v_or_b32 v235, s52, v235
v_or_b32 v236, s52, v236
v_or_b32 v237, s52, v237
s_mov_b32 s73, 16
s_nop 1
s_branch .Lqa_hr18
.Lqa_e20:
s_cmp_lg_u32 s70, 16
s_cbranch_scc1 .Lqa_e21
v_mov_b32 v234, v32
v_mov_b32 v235, v33
v_mov_b32 v236, v34
v_mov_b32 v237, v35
s_mov_b32 s73, 15
s_nop 1
s_branch .Lqa_hr18
.Lqa_e21:
s_cmp_lg_u32 s70, 15
s_cbranch_scc1 .Lqa_e22
v_subrev_u32 v16, 18432, v16
s_mov_b32 s73, 3
s_nop 1
s_branch .Lqa_hr18
.Lqa_e22:
s_cmp_lg_u32 s70, 3
s_cbranch_scc1 .Lqa_e23
s_mov_b32 s73, 2
s_nop 1
s_branch .Lqa_hr18
.Lqa_e23:
s_cmp_lg_u32 s70, 2
s_cbranch_scc1 .Lqa_e24
s_add_u32 s46, s46, s50
s_addc_u32 s47, s47, 0
s_mov_b32 s73, 0
s_nop 1
s_branch .Lqa_hr18
.Lqa_e24:
s_cmp_lg_u32 s70, 0
s_cbranch_scc1 .Lqa_e25
v_or_b32 v32, s52, v32
v_or_b32 v33, s52, v33
v_or_b32 v34, s52, v34
v_or_b32 v35, s52, v35
s_mov_b32 s73, 999
s_nop 1
s_branch .Lqa_hr18
.Lqa_e25:
s_branch .Lqa_hr18
.Lqa_sp30:
s_sleep 1
global_load_dword v53, v44, s[38:39] sc1
s_add_u32 s71, s71, 1
s_waitcnt vmcnt(0)
v_sub_u32 v53, v53, v46
v_cmp_lt_u32 vcc, 0x7f, v53
s_and_b64 vcc, vcc, s[76:77]
s_cbranch_vccz .Lqa_rt31
s_cmp_lt_u32 s71, 3000000
s_cbranch_scc1 .Lqa_sp30
s_branch .Lqa_rt31
.Lqa_sp32:
s_sleep 1
global_load_dword v254, v44, s[38:39] sc1
s_add_u32 s71, s71, 1
s_waitcnt vmcnt(0)
v_sub_u32 v254, v254, v46
v_cmp_lt_u32 vcc, 0x7f, v254
s_and_b64 vcc, vcc, s[76:77]
s_cbranch_vccz .Lqa_rt33
s_cmp_lt_u32 s71, 3000000
s_cbranch_scc1 .Lqa_sp32
s_branch .Lqa_rt33
.Lqa_hk28:
s_cmp_lg_u32 s70, 18
s_cbranch_scc1 .Lqa_e34
s_add_u32 s42, s42, s50
s_addc_u32 s43, s43, 0
s_mov_b64 s[76:77], -1
s_mov_b64 s[46:47], s[44:45]
)"
R"(s_mov_b32 s73, 17
s_nop 1
s_branch .Lqa_hr29
.Lqa_e34:
s_cmp_lg_u32 s70, 17
s_cbranch_scc1 .Lqa_e35
s_mov_b32 s46, -18432
s_mov_b32 s47, -1
v_or_b32 v202, s52, v202
v_or_b32 v203, s52, v203
v_or_b32 v204, s52, v204
v_or_b32 v205, s52, v205
s_mov_b32 s73, 16
s_nop 1
s_branch .Lqa_hr29
.Lqa_e35:
s_cmp_lg_u32 s70, 16
s_cbranch_scc1 .Lqa_e36
v_mov_b32 v202, v32
v_mov_b32 v203, v33
v_mov_b32 v204, v34
v_mov_b32 v205, v35
s_mov_b32 s73, 15
s_nop 1
s_branch .Lqa_hr29
.Lqa_e36:
s_cmp_lg_u32 s70, 15
s_cbranch_scc1 .Lqa_e37
v_subrev_u32 v16, 18432, v16
s_mov_b32 s73, 3
s_nop 1
s_branch .Lqa_hr29
.Lqa_e37:
s_cmp_lg_u32 s70, 3
s_cbranch_scc1 .Lqa_e38
v_add_u32 v45, s51, v45
s_mov_b32 s73, 2
s_nop 1
s_branch .Lqa_hr29
.Lqa_e38:
s_cmp_lg_u32 s70, 2
s_cbranch_scc1 .Lqa_e39
v_add_u32 v44, s51, v44
v_add_u32 v46, -32, v46
s_add_u32 s46, s46, s50
s_addc_u32 s47, s47, 0
s_mov_b32 s73, 0
s_nop 1
s_branch .Lqa_hr29
.Lqa_e39:
s_cmp_lg_u32 s70, 0
s_cbranch_scc1 .Lqa_e40
v_or_b32 v32, s52, v32
v_or_b32 v33, s52, v33
v_or_b32 v34, s52, v34
v_or_b32 v35, s52, v35
s_mov_b32 s73, 999
s_nop 1
s_branch .Lqa_hr29
.Lqa_e40:
s_branch .Lqa_hr29
.Lqa_hk43:
s_cmp_lg_u32 s70, 18
s_cbranch_scc1 .Lqa_e45
s_add_u32 s42, s42, s50
s_addc_u32 s43, s43, 0
s_mov_b64 s[76:77], -1
s_mov_b64 s[46:47], s[44:45]
s_mov_b32 s73, 17
s_nop 1
s_branch .Lqa_hr44
.Lqa_e45:
s_cmp_lg_u32 s70, 17
s_cbranch_scc1 .Lqa_e46
s_mov_b32 s46, -18432
s_mov_b32 s47, -1
v_or_b32 v206, s52, v206
v_or_b32 v207, s52, v207
v_or_b32 v208, s52, v208
v_or_b32 v209, s52, v209
s_mov_b32 s73, 16
s_nop 1
s_branch .Lqa_hr44
.Lqa_e46:
s_cmp_lg_u32 s70, 16
s_cbranch_scc1 .Lqa_e47
v_mov_b32 v206, v32
v_mov_b32 v207, v33
v_mov_b32 v208, v34
v_mov_b32 v209, v35
s_mov_b32 s73, 15
s_nop 1
s_branch .Lqa_hr44
.Lqa_e47:
s_cmp_lg_u32 s70, 15
s_cbranch_scc1 .Lqa_e48
v_add_u32 v48, -1, v48
s_mov_b32 s73, 3
s_nop 1
s_branch .Lqa_hr44
.Lqa_e48:
s_cmp_lg_u32 s70, 3
s_cbranch_scc1 .Lqa_e49
s_mov_b32 s73, 2
s_nop 1
s_branch .Lqa_hr44
.Lqa_e49:
s_cmp_lg_u32 s70, 2
s_cbranch_scc1 .Lqa_e50
s_add_u32 s46, s46, s50
s_addc_u32 s47, s47, 0
s_mov_b32 s73, 0
s_nop 1
s_branch .Lqa_hr44
.Lqa_e50:
s_cmp_lg_u32 s70, 0
s_cbranch_scc1 .Lqa_e51
v_or_b32 v32, s52, v32
v_or_b32 v33, s52, v33
v_or_b32 v34, s52, v34
v_or_b32 v35, s52, v35
s_mov_b32 s73, 999
s_nop 1
s_branch .Lqa_hr44
.Lqa_e51:
s_branch .Lqa_hr44
.Lqa_sp54:
s_sleep 1
global_load_dword v53, v49, s[38:39] sc1
s_add_u32 s71, s71, 1
s_waitcnt vmcnt(0)
v_sub_u32 v53, v53, v50
v_cmp_lt_u32 vcc, 0x7f, v53
s_cbranch_vccz .Lqa_rt55
s_cmp_lt_u32 s71, 3000000
s_cbranch_scc1 .Lqa_sp54
s_branch .Lqa_rt55
.Lqa_sp56:
s_sleep 1
global_load_dword v53, v49, s[38:39] sc1
s_add_u32 s71, s71, 1
s_waitcnt vmcnt(0)
v_sub_u32 v53, v53, v50
v_cmp_lt_u32 vcc, 0x7f, v53
s_cbranch_vccz .Lqa_rt57
s_cmp_lt_u32 s71, 3000000
s_cbranch_scc1 .Lqa_sp56
s_branch .Lqa_rt57
.Lqa_sp58:
s_sleep 1
global_load_dword v53, v49, s[38:39] sc1
s_add_u32 s71, s71, 1
s_waitcnt vmcnt(0)
v_sub_u32 v53, v53, v50
v_cmp_lt_u32 vcc, 0x7f, v53
s_cbranch_vccz .Lqa_rt59
s_cmp_lt_u32 s71, 3000000
s_cbranch_scc1 .Lqa_sp58
s_branch .Lqa_rt59
.Lqa_sp60:
s_sleep 1
global_load_dword v53, v49, s[38:39] sc1
s_add_u32 s71, s71, 1
s_waitcnt vmcnt(0)
v_sub_u32 v53, v53, v50
v_cmp_lt_u32 vcc, 0x7f, v53
s_cbranch_vccz .Lqa_rt61
s_cmp_lt_u32 s71, 3000000
s_cbranch_scc1 .Lqa_sp60
s_branch .Lqa_rt61
.Lrole1:
s_and_b32 s75, s36, 3
s_cmp_eq_u32 s75, 1
s_cbranch_scc1 .Lwo_w2
s_cmp_eq_u32 s75, 2
s_cbranch_scc1 .Lwo_w3
s_cmp_eq_u32 s75, 3
s_cbranch_scc1 .Lwo_w4
.Lwo_w1:
s_add_u32 m0, s48, 4096
s_nop 0
global_load_lds_dwordx4 v0, s[40:41] offset:-4096
global_load_lds_dwordx4 v1, s[40:41] offset:-3072
global_load_lds_dwordx4 v2, s[40:41] offset:-2048
global_load_lds_dwordx4 v3, s[40:41] offset:-1024
global_load_lds_dwordx4 v4, s[40:41] offset:0
global_load_lds_dwordx4 v5, s[40:41] offset:1024
global_load_lds_dwordx4 v6, s[40:41] offset:2048
global_load_lds_dwordx4 v7, s[40:41] offset:3072
s_add_u32 m0, s48, 12288
s_nop 0
global_load_lds_dwordx4 v8, s[40:41] offset:-4096
s_add_u32 s40, s40, 73728
s_addc_u32 s41, s41, 0
s_add_u32 m0, s48, 40960
s_nop 0
global_load_lds_dwordx4 v0, s[40:41] offset:-4096
global_load_lds_dwordx4 v1, s[40:41] offset:-3072
global_load_lds_dwordx4 v2, s[40:41] offset:-2048
global_load_lds_dwordx4 v3, s[40:41] offset:-1024
global_load_lds_dwordx4 v4, s[40:41] offset:0
global_load_lds_dwordx4 v5, s[40:41] offset:1024
global_load_lds_dwordx4 v6, s[40:41] offset:2048
global_load_lds_dwordx4 v7, s[40:41] offset:3072
s_add_u32 m0, s48, 49152
s_nop 0
global_load_lds_dwordx4 v8, s[40:41] offset:-4096
s_add_u32 s40, s40, 73728
s_addc_u32 s41, s41, 0
s_add_u32 m0, s48, 77824
s_nop 0
global_load_lds_dwordx4 v0, s[40:41] offset:-4096
global_load_lds_dwordx4 v1, s[40:41] offset:-3072
global_load_lds_dwordx4 v2, s[40:41] offset:-2048
global_load_lds_dwordx4 v3, s[40:41] offset:-1024
global_load_lds_dwordx4 v4, s[40:41] offset:0
global_load_lds_dwordx4 v5, s[40:41] offset:1024
global_load_lds_dwordx4 v6, s[40:41] offset:2048
global_load_lds_dwordx4 v7, s[40:41] offset:3072
s_add_u32 m0, s48, 86016
s_nop 0
global_load_lds_dwordx4 v8, s[40:41] offset:-4096
s_add_u32 s40, s40, 73728
s_addc_u32 s41, s41, 0
v_add_u32 v204, 0x10000, v9
v_add_u32 v205, 0x20000, v9
v_add_u32 v206, 0x10000, v10
v_add_u32 v207, 0x20000, v10
s_waitcnt lgkmcnt(0)
s_mul_i32 s37, s37, 0x9E3779B1
s_add_u32 s37, s37, 0x7F4A7C15
v_add_u32 v22, s37, v22
v_add_u32 v24, s37, v24
s_waitcnt vmcnt(18)
s_barrier
s_add_u32 m0, s48, 114688
ds_read_b64_tr_b16 a[0:1], v9 offset:0
ds_read_b64_tr_b16 a[2:3], v9 offset:2304
global_load_lds_dwordx4 v0, s[40:41] offset:-4096
ds_read_b64_tr_b16 a[4:5], v9 offset:8
ds_read_b64_tr_b16 a[6:7], v9 offset:2312
global_load_lds_dwordx4 v1, s[40:41] offset:-3072
ds_read_b64_tr_b16 a[8:9], v9 offset:64
global_load_lds_dwordx4 v2, s[40:41] offset:-2048
ds_read_b64_tr_b16 a[10:11], v9 offset:2368
ds_read_b64_tr_b16 a[12:13], v9 offset:72
global_load_lds_dwordx4 v3, s[40:41] offset:-1024
ds_read_b64_tr_b16 a[14:15], v9 offset:2376
global_load_lds_dwordx4 v4, s[40:41] offset:0
ds_read_b64_tr_b16 v[60:61], v10 offset:0
ds_read_b64_tr_b16 v[62:63], v10 offset:2304
global_load_lds_dwordx4 v5, s[40:41] offset:1024
ds_read_b64_tr_b16 a[16:17], v9 offset:18432
global_load_lds_dwordx4 v6, s[40:41] offset:2048
ds_read_b64_tr_b16 a[18:19], v9 offset:20736
ds_read_b64_tr_b16 a[20:21], v9 offset:18440
global_load_lds_dwordx4 v7, s[40:41] offset:3072
ds_read_b64_tr_b16 a[22:23], v9 offset:20744
s_add_u32 m0, s48, 122880
ds_read_b64_tr_b16 a[24:25], v9 offset:18496
ds_read_b64_tr_b16 a[26:27], v9 offset:20800
global_load_lds_dwordx4 v8, s[40:41] offset:-4096
ds_read_b64_tr_b16 a[28:29], v9 offset:18504
s_add_u32 s40, s40, 73728
ds_read_b64_tr_b16 a[30:31], v9 offset:20808
ds_read_b64_tr_b16 v[64:65], v10 offset:18432
s_addc_u32 s41, s41, 0
ds_read_b64_tr_b16 v[66:67], v10 offset:20736
s_waitcnt vmcnt(18) lgkmcnt(0)
s_barrier
s_add_u32 m0, s48, 4096
ds_read_b64_tr_b16 a[32:33], v9 offset:36864
ds_read_b64_tr_b16 a[34:35], v9 offset:39168
global_load_lds_dwordx4 v0, s[40:41] offset:-4096
ds_read_b64_tr_b16 a[36:37], v9 offset:36872
ds_read_b64_tr_b16 a[38:39], v9 offset:39176
global_load_lds_dwordx4 v1, s[40:41] offset:-3072
ds_read_b64_tr_b16 a[40:41], v9 offset:36928
global_load_lds_dwordx4 v2, s[40:41] offset:-2048
ds_read_b64_tr_b16 a[42:43], v9 offset:39232
ds_read_b64_tr_b16 a[44:45], v9 offset:36936
global_load_lds_dwordx4 v3, s[40:41] offset:-1024
ds_read_b64_tr_b16 a[46:47], v9 offset:39240
global_load_lds_dwordx4 v4, s[40:41] offset:0
ds_read_b64_tr_b16 v[68:69], v10 offset:36864
ds_read_b64_tr_b16 v[70:71], v10 offset:39168
global_load_lds_dwordx4 v5, s[40:41] offset:1024
ds_read_b64_tr_b16 a[48:49], v9 offset:55296
global_load_lds_dwordx4 v6, s[40:41] offset:2048
ds_read_b64_tr_b16 a[50:51], v9 offset:57600
ds_read_b64_tr_b16 a[52:53], v9 offset:55304
global_load_lds_dwordx4 v7, s[40:41] offset:3072
ds_read_b64_tr_b16 a[54:55], v9 offset:57608
s_add_u32 m0, s48, 12288
ds_read_b64_tr_b16 a[56:57], v9 offset:55360
ds_read_b64_tr_b16 a[58:59], v9 offset:57664
global_load_lds_dwordx4 v8, s[40:41] offset:-4096
ds_read_b64_tr_b16 a[60:61], v9 offset:55368
s_add_u32 s40, s40, 73728
ds_read_b64_tr_b16 a[62:63], v9 offset:57672
ds_read_b64_tr_b16 v[72:73], v10 offset:55296
s_addc_u32 s41, s41, 0
ds_read_b64_tr_b16 v[74:75], v10 offset:57600
s_waitcnt vmcnt(18) lgkmcnt(0)
s_barrier
s_add_u32 m0, s48, 40960
ds_read_b64_tr_b16 a[64:65], v204 offset:8192
ds_read_b64_tr_b16 a[66:67], v204 offset:10496
global_load_lds_dwordx4 v0, s[40:41] offset:-4096
ds_read_b64_tr_b16 a[68:69], v204 offset:8200
ds_read_b64_tr_b16 a[70:71], v204 offset:10504
global_load_lds_dwordx4 v1, s[40:41] offset:-3072
ds_read_b64_tr_b16 a[72:73], v204 offset:8256
global_load_lds_dwordx4 v2, s[40:41] offset:-2048
ds_read_b64_tr_b16 a[74:75], v204 offset:10560
ds_read_b64_tr_b16 a[76:77], v204 offset:8264
global_load_lds_dwordx4 v3, s[40:41] offset:-1024
ds_read_b64_tr_b16 a[78:79], v204 offset:10568
global_load_lds_dwordx4 v4, s[40:41] offset:0
ds_read_b64_tr_b16 v[76:77], v206 offset:8192
ds_read_b64_tr_b16 v[78:79], v206 offset:10496
global_load_lds_dwordx4 v5, s[40:41] offset:1024
ds_read_b64_tr_b16 a[80:81], v204 offset:26624
global_load_lds_dwordx4 v6, s[40:41] offset:2048
ds_read_b64_tr_b16 a[82:83], v204 offset:28928
ds_read_b64_tr_b16 a[84:85], v204 offset:26632
global_load_lds_dwordx4 v7, s[40:41] offset:3072
ds_read_b64_tr_b16 a[86:87], v204 offset:28936
s_add_u32 m0, s48, 49152
ds_read_b64_tr_b16 a[88:89], v204 offset:26688
ds_read_b64_tr_b16 a[90:91], v204 offset:28992
global_load_lds_dwordx4 v8, s[40:41] offset:-4096
ds_read_b64_tr_b16 a[92:93], v204 offset:26696
s_add_u32 s40, s40, 73728
ds_read_b64_tr_b16 a[94:95], v204 offset:29000
ds_read_b64_tr_b16 v[80:81], v206 offset:26624
s_addc_u32 s41, s41, 0
ds_read_b64_tr_b16 v[82:83], v206 offset:28928
s_waitcnt vmcnt(18) lgkmcnt(0)
s_barrier
s_add_u32 m0, s48, 77824
ds_read_b64_tr_b16 a[96:97], v204 offset:45056
ds_read_b64_tr_b16 a[98:99], v204 offset:47360
global_load_lds_dwordx4 v0, s[40:41] offset:-4096
ds_read_b64_tr_b16 a[100:101], v204 offset:45064
ds_read_b64_tr_b16 a[102:103], v204 offset:47368
global_load_lds_dwordx4 v1, s[40:41] offset:-3072
ds_read_b64_tr_b16 a[104:105], v204 offset:45120
global_load_lds_dwordx4 v2, s[40:41] offset:-2048
ds_read_b64_tr_b16 a[106:107], v204 offset:47424
ds_read_b64_tr_b16 a[108:109], v204 offset:45128
global_load_lds_dwordx4 v3, s[40:41] offset:-1024
ds_read_b64_tr_b16 a[110:111], v204 offset:47432
global_load_lds_dwordx4 v4, s[40:41] offset:0
ds_read_b64_tr_b16 v[84:85], v206 offset:45056
ds_read_b64_tr_b16 v[86:87], v206 offset:47360
global_load_lds_dwordx4 v5, s[40:41] offset:1024
ds_read_b64_tr_b16 a[112:113], v204 offset:63488
global_load_lds_dwordx4 v6, s[40:41] offset:2048
ds_read_b64_tr_b16 a[114:115], v205 offset:256
ds_read_b64_tr_b16 a[116:117], v204 offset:63496
global_load_lds_dwordx4 v7, s[40:41] offset:3072
ds_read_b64_tr_b16 a[118:119], v205 offset:264
s_add_u32 m0, s48, 86016
ds_read_b64_tr_b16 a[120:121], v204 offset:63552
ds_read_b64_tr_b16 a[122:123], v205 offset:320
global_load_lds_dwordx4 v8, s[40:41] offset:-4096
ds_read_b64_tr_b16 a[124:125], v204 offset:63560
s_add_u32 s40, s40, 73728
ds_read_b64_tr_b16 a[126:127], v205 offset:328
ds_read_b64_tr_b16 v[88:89], v206 offset:63488
s_addc_u32 s41, s41, 0
ds_read_b64_tr_b16 v[90:91], v207 offset:256
s_waitcnt vmcnt(18) lgkmcnt(0)
s_barrier
s_add_u32 m0, s48, 114688
ds_read_b64_tr_b16 a[128:129], v9 offset:0
ds_read_b64_tr_b16 a[130:131], v9 offset:2304
global_load_lds_dwordx4 v0, s[40:41] offset:-4096
ds_read_b64_tr_b16 a[132:133], v9 offset:8
global_load_lds_dwordx4 v1, s[40:41] offset:-3072
ds_read_b64_tr_b16 a[134:135], v9 offset:2312
ds_read_b64_tr_b16 a[136:137], v9 offset:64
global_load_lds_dwordx4 v2, s[40:41] offset:-2048
ds_read_b64_tr_b16 a[138:139], v9 offset:2368
global_load_lds_dwordx4 v3, s[40:41] offset:-1024
ds_read_b64_tr_b16 a[140:141], v9 offset:72
global_load_lds_dwordx4 v4, s[40:41] offset:0
ds_read_b64_tr_b16 a[142:143], v9 offset:2376
ds_read_b64_tr_b16 v[92:93], v10 offset:0
global_load_lds_dwordx4 v5, s[40:41] offset:1024
ds_read_b64_tr_b16 v[94:95], v10 offset:2304
global_load_lds_dwordx4 v6, s[40:41] offset:2048
ds_read_b64_tr_b16 a[144:145], v9 offset:18432
ds_read_b64_tr_b16 a[146:147], v9 offset:20736
global_load_lds_dwordx4 v7, s[40:41] offset:3072
ds_read_b64_tr_b16 a[148:149], v9 offset:18440
s_add_u32 m0, s48, 122880
ds_read_b64_tr_b16 a[150:151], v9 offset:20744
global_load_lds_dwordx4 v8, s[40:41] offset:-4096
)"
R"(ds_read_b64_tr_b16 a[152:153], v9 offset:18496
ds_read_b64_tr_b16 a[154:155], v9 offset:20800
s_add_u32 s40, s40, 73728
ds_read_b64_tr_b16 a[156:157], v9 offset:18504
s_addc_u32 s41, s41, 0
ds_read_b64_tr_b16 a[158:159], v9 offset:20808
s_waitcnt vmcnt(18) lgkmcnt(0)
s_barrier
s_add_u32 m0, s48, 4096
ds_read_b64_tr_b16 a[160:161], v9 offset:36864
ds_read_b64_tr_b16 a[162:163], v9 offset:39168
global_load_lds_dwordx4 v0, s[40:41] offset:-4096
ds_read_b64_tr_b16 a[164:165], v9 offset:36872
global_load_lds_dwordx4 v1, s[40:41] offset:-3072
ds_read_b64_tr_b16 a[166:167], v9 offset:39176
global_load_lds_dwordx4 v2, s[40:41] offset:-2048
ds_read_b64_tr_b16 a[168:169], v9 offset:36928
global_load_lds_dwordx4 v3, s[40:41] offset:-1024
ds_read_b64_tr_b16 a[170:171], v9 offset:39232
ds_read_b64_tr_b16 a[172:173], v9 offset:36936
global_load_lds_dwordx4 v4, s[40:41] offset:0
ds_read_b64_tr_b16 a[174:175], v9 offset:39240
global_load_lds_dwordx4 v5, s[40:41] offset:1024
ds_read_b64_tr_b16 a[176:177], v9 offset:55296
global_load_lds_dwordx4 v6, s[40:41] offset:2048
ds_read_b64_tr_b16 a[178:179], v9 offset:57600
global_load_lds_dwordx4 v7, s[40:41] offset:3072
ds_read_b64_tr_b16 a[180:181], v9 offset:55304
ds_read_b64_tr_b16 a[182:183], v9 offset:57608
s_add_u32 m0, s48, 12288
ds_read_b64_tr_b16 a[184:185], v9 offset:55360
global_load_lds_dwordx4 v8, s[40:41] offset:-4096
ds_read_b64_tr_b16 a[186:187], v9 offset:57664
s_sub_u32 s40, s40, 589824
ds_read_b64_tr_b16 a[188:189], v9 offset:55368
s_subb_u32 s41, s41, 0
ds_read_b64_tr_b16 a[190:191], v9 offset:57672
s_waitcnt vmcnt(18) lgkmcnt(0)
s_barrier
ds_read_b64_tr_b16 a[192:193], v204 offset:8192
ds_read_b64_tr_b16 a[194:195], v204 offset:10496
ds_read_b64_tr_b16 a[196:197], v204 offset:8200
ds_read_b64_tr_b16 a[198:199], v204 offset:10504
ds_read_b64_tr_b16 a[200:201], v204 offset:8256
ds_read_b64_tr_b16 a[202:203], v204 offset:10560
ds_read_b64_tr_b16 a[204:205], v204 offset:8264
ds_read_b64_tr_b16 a[206:207], v204 offset:10568
ds_read_b64_tr_b16 a[208:209], v204 offset:26624
ds_read_b64_tr_b16 a[210:211], v204 offset:28928
ds_read_b64_tr_b16 a[212:213], v204 offset:26632
ds_read_b64_tr_b16 a[214:215], v204 offset:28936
ds_read_b64_tr_b16 a[216:217], v204 offset:26688
ds_read_b64_tr_b16 a[218:219], v204 offset:28992
ds_read_b64_tr_b16 a[220:221], v204 offset:26696
ds_read_b64_tr_b16 a[222:223], v204 offset:29000
s_waitcnt vmcnt(9) lgkmcnt(0)
s_barrier
ds_read_b64_tr_b16 a[224:225], v204 offset:45056
ds_read_b64_tr_b16 a[226:227], v204 offset:47360
ds_read_b64_tr_b16 a[228:229], v204 offset:45064
ds_read_b64_tr_b16 a[230:231], v204 offset:47368
ds_read_b64_tr_b16 a[232:233], v204 offset:45120
ds_read_b64_tr_b16 a[234:235], v204 offset:47424
ds_read_b64_tr_b16 a[236:237], v204 offset:45128
ds_read_b64_tr_b16 a[238:239], v204 offset:47432
ds_read_b64_tr_b16 a[240:241], v204 offset:63488
ds_read_b64_tr_b16 a[242:243], v205 offset:256
ds_read_b64_tr_b16 a[244:245], v204 offset:63496
ds_read_b64_tr_b16 a[246:247], v205 offset:264
ds_read_b64_tr_b16 a[248:249], v204 offset:63552
ds_read_b64_tr_b16 a[250:251], v205 offset:320
ds_read_b64_tr_b16 a[252:253], v204 offset:63560
ds_read_b64_tr_b16 a[254:255], v205 offset:328
s_waitcnt vmcnt(0) lgkmcnt(0)
s_barrier
ds_read_b64_tr_b16 v[28:29], v9 offset:0
ds_read_b64_tr_b16 v[30:31], v9 offset:2304
ds_read_b64_tr_b16 v[32:33], v9 offset:8
ds_read_b64_tr_b16 v[34:35], v9 offset:2312
ds_read_b64_tr_b16 v[36:37], v9 offset:64
ds_read_b64_tr_b16 v[38:39], v9 offset:2368
ds_read_b64_tr_b16 v[40:41], v9 offset:72
ds_read_b64_tr_b16 v[42:43], v9 offset:2376
ds_read_b64_tr_b16 v[44:45], v9 offset:18432
ds_read_b64_tr_b16 v[46:47], v9 offset:20736
ds_read_b64_tr_b16 v[48:49], v9 offset:18440
ds_read_b64_tr_b16 v[50:51], v9 offset:20744
ds_read_b64_tr_b16 v[52:53], v9 offset:18496
ds_read_b64_tr_b16 v[54:55], v9 offset:20800
ds_read_b64_tr_b16 v[56:57], v9 offset:18504
ds_read_b64_tr_b16 v[58:59], v9 offset:20808
s_waitcnt lgkmcnt(0)
s_barrier
s_mov_b64 s[76:77], 0
s_add_u32 m0, s49, 18432
s_mov_b32 s70, 33
s_waitcnt vmcnt(0) lgkmcnt(0)
s_nop 7
.p2align 6
.Lwo_top5:
s_waitcnt lgkmcnt(2)
v_mfma_f32_16x16x32_bf16 v[96:99], a[0:3], v[132:135], v[96:99]
ds_read_b128 v[144:147], v25 offset:3072
v_mfma_f32_16x16x32_bf16 v[100:103], a[4:7], v[132:135], v[100:103]
v_mfma_f32_16x16x32_bf16 v[104:107], a[8:11], v[132:135], v[104:107]
s_waitcnt vmcnt(7)
s_barrier
v_mfma_f32_16x16x32_bf16 v[108:111], a[12:15], v[132:135], v[108:111]
v_cvt_pk_bf16_f32 v164, v112, v113
v_cvt_pk_bf16_f32 v165, v114, v115
s_mov_b64 exec, 1
global_store_dword v23, v24, s[38:39] sc1
s_mov_b64 exec, -1
v_mfma_f32_16x16x32_bf16 v[128:131], v[60:63], v[132:135], 0
s_waitcnt lgkmcnt(2)
v_cvt_pk_bf16_f32 v166, v116, v117
v_cvt_pk_bf16_f32 v167, v118, v119
v_add_u32 v24, 1, v24
v_mfma_f32_16x16x32_bf16 v[96:99], a[16:19], v[136:139], v[96:99]
ds_read_b128 v[132:135], v25 offset:4096
v_add_f32 v254, v112, v113
v_mfma_f32_16x16x32_bf16 v[100:103], a[20:23], v[136:139], v[100:103]
global_store_dwordx4 v13, v[164:167], s[38:39] offset:0 sc1
v_mfma_f32_16x16x32_bf16 v[104:107], a[24:27], v[136:139], v[104:107]
v_cvt_pk_bf16_f32 v168, v120, v121
v_cvt_pk_bf16_f32 v169, v122, v123
v_mfma_f32_16x16x32_bf16 v[108:111], a[28:31], v[136:139], v[108:111]
v_cvt_pk_bf16_f32 v170, v124, v125
v_cvt_pk_bf16_f32 v171, v126, v127
v_mfma_f32_16x16x32_bf16 v[128:131], v[64:67], v[136:139], v[128:131]
s_waitcnt lgkmcnt(2)
global_store_dwordx4 v13, v[168:171], s[38:39] offset:1024 sc1
v_mfma_f32_16x16x32_bf16 v[96:99], a[32:35], v[140:143], v[96:99]
s_cmp_eq_u32 s70, 32
ds_read_b128 v[136:139], v25 offset:5120
v_add_u32 v13, 18688, v13
v_mfma_f32_16x16x32_bf16 v[100:103], a[36:39], v[140:143], v[100:103]
s_cselect_b64 s[42:43], s[44:45], s[42:43]
v_mul_f32 v255, v112, v112
v_fmac_f32 v255, v113, v113
v_mfma_f32_16x16x32_bf16 v[104:107], a[40:43], v[140:143], v[104:107]
s_cmp_eq_u32 s70, 16
v_add_f32 v254, v254, v114
v_fmac_f32 v255, v114, v114
v_mfma_f32_16x16x32_bf16 v[108:111], a[44:47], v[140:143], v[108:111]
s_cselect_b64 s[42:43], s[46:47], s[42:43]
v_add_f32 v254, v254, v115
v_fmac_f32 v255, v115, v115
v_mfma_f32_16x16x32_bf16 v[128:131], v[68:71], v[140:143], v[128:131]
s_waitcnt lgkmcnt(2)
v_add_f32 v254, v254, v116
v_fmac_f32 v255, v116, v116
v_mfma_f32_16x16x32_bf16 v[96:99], a[48:51], v[144:147], v[96:99]
s_xor_b32 s49, s49, 0x10000
ds_read_b128 v[140:143], v25 offset:6144
v_add_f32 v254, v254, v117
v_mfma_f32_16x16x32_bf16 v[100:103], a[52:55], v[144:147], v[100:103]
s_mov_b32 m0, 47104
v_fmac_f32 v255, v117, v117
v_add_f32 v254, v254, v118
v_mfma_f32_16x16x32_bf16 v[104:107], a[56:59], v[144:147], v[104:107]
v_fmac_f32 v255, v118, v118
v_add_f32 v254, v254, v119
v_mfma_f32_16x16x32_bf16 v[108:111], a[60:63], v[144:147], v[108:111]
global_load_lds_dwordx4 v16, s[42:43] offset:0 sc1
v_fmac_f32 v255, v119, v119
v_mfma_f32_16x16x32_bf16 v[128:131], v[72:75], v[144:147], v[128:131]
s_waitcnt lgkmcnt(2)
v_add_f32 v254, v254, v120
v_fmac_f32 v255, v120, v120
v_mfma_f32_16x16x32_bf16 v[96:99], a[64:67], v[132:135], v[96:99]
ds_read_b128 v[144:147], v25 offset:7168
v_add_f32 v254, v254, v121
v_mfma_f32_16x16x32_bf16 v[100:103], a[68:71], v[132:135], v[100:103]
v_fmac_f32 v255, v121, v121
v_add_f32 v254, v254, v122
v_mfma_f32_16x16x32_bf16 v[104:107], a[72:75], v[132:135], v[104:107]
v_fmac_f32 v255, v122, v122
v_add_f32 v254, v254, v123
v_mfma_f32_16x16x32_bf16 v[108:111], a[76:79], v[132:135], v[108:111]
v_fmac_f32 v255, v123, v123
v_add_f32 v254, v254, v124
v_mfma_f32_16x16x32_bf16 v[128:131], v[76:79], v[132:135], v[128:131]
s_waitcnt vmcnt(9)
ds_read_b128 v[148:151], v19 offset:8192
ds_read_b128 v[152:155], v20 offset:8192
s_waitcnt lgkmcnt(4)
v_mfma_f32_16x16x32_bf16 v[96:99], a[80:83], v[136:139], v[96:99]
ds_read_b128 v[132:135], v25 offset:8192
global_load_lds_dwordx4 v17, s[42:43] offset:1024 sc1
v_mfma_f32_16x16x32_bf16 v[100:103], a[84:87], v[136:139], v[100:103]
v_fmac_f32 v255, v124, v124
v_add_f32 v254, v254, v125
v_mfma_f32_16x16x32_bf16 v[104:107], a[88:91], v[136:139], v[104:107]
v_fmac_f32 v255, v125, v125
v_add_f32 v254, v254, v126
v_mfma_f32_16x16x32_bf16 v[108:111], a[92:95], v[136:139], v[108:111]
v_fmac_f32 v255, v126, v126
v_add_f32 v254, v254, v127
v_mfma_f32_16x16x32_bf16 v[128:131], v[80:83], v[136:139], v[128:131]
s_waitcnt lgkmcnt(4)
v_fmac_f32 v255, v127, v127
v_mfma_f32_16x16x32_bf16 v[96:99], a[96:99], v[140:143], v[96:99]
s_add_u32 m0, s49, 0
ds_read_b128 v[136:139], v25 offset:9216
v_mfma_f32_16x16x32_bf16 v[100:103], a[100:103], v[140:143], v[100:103]
ds_write_b64 v27, v[254:255] offset:45056
v_mfma_f32_16x16x32_bf16 v[104:107], a[104:107], v[140:143], v[104:107]
s_waitcnt lgkmcnt(4)
v_lshlrev_b32 v112, 16, v148
v_and_b32 v113, 0xffff0000, v148
v_mfma_f32_16x16x32_bf16 v[108:111], a[108:111], v[140:143], v[108:111]
s_sub_u32 s42, s42, 18432
v_lshlrev_b32 v114, 16, v149
v_and_b32 v115, 0xffff0000, v149
v_mfma_f32_16x16x32_bf16 v[128:131], v[84:87], v[140:143], v[128:131]
v_lshlrev_b32 v116, 16, v150
v_and_b32 v117, 0xffff0000, v150
v_mfma_f32_16x16x32_bf16 v[96:99], a[112:115], v[144:147], v[96:99]
s_subb_u32 s43, s43, 0
ds_read_b128 v[140:143], v25 offset:10240
v_lshlrev_b32 v118, 16, v151
v_mfma_f32_16x16x32_bf16 v[100:103], a[116:119], v[144:147], v[100:103]
s_waitcnt lgkmcnt(4)
v_and_b32 v119, 0xffff0000, v151
v_lshlrev_b32 v120, 16, v152
v_mfma_f32_16x16x32_bf16 v[104:107], a[120:123], v[144:147], v[104:107]
v_and_b32 v121, 0xffff0000, v152
v_lshlrev_b32 v122, 16, v153
v_mfma_f32_16x16x32_bf16 v[108:111], a[124:127], v[144:147], v[108:111]
v_and_b32 v123, 0xffff0000, v153
v_lshlrev_b32 v124, 16, v154
v_mfma_f32_16x16x32_bf16 v[128:131], v[88:91], v[144:147], v[128:131]
s_waitcnt lgkmcnt(3)
v_and_b32 v125, 0xffff0000, v154
v_lshlrev_b32 v126, 16, v155
v_mfma_f32_16x16x32_bf16 v[96:99], a[128:131], v[132:135], v[96:99]
ds_read_b128 v[144:147], v25 offset:11264
v_and_b32 v127, 0xffff0000, v155
v_mfma_f32_16x16x32_bf16 v[100:103], a[132:135], v[132:135], v[100:103]
v_mfma_f32_16x16x32_bf16 v[104:107], a[136:139], v[132:135], v[104:107]
v_mfma_f32_16x16x32_bf16 v[108:111], a[140:143], v[132:135], v[108:111]
v_mfma_f32_16x16x32_bf16 v[128:131], v[92:95], v[132:135], v[128:131]
s_waitcnt lgkmcnt(3)
v_mfma_f32_16x16x32_bf16 v[96:99], a[144:147], v[136:139], v[96:99]
ds_read_b128 v[132:135], v25 offset:12288
v_mfma_f32_16x16x32_bf16 v[100:103], a[148:151], v[136:139], v[100:103]
v_mfma_f32_16x16x32_bf16 v[104:107], a[152:155], v[136:139], v[104:107]
v_mfma_f32_16x16x32_bf16 v[108:111], a[156:159], v[136:139], v[108:111]
s_waitcnt lgkmcnt(2)
v_mfma_f32_16x16x32_bf16 v[96:99], a[160:163], v[140:143], v[96:99]
ds_read_b128 v[136:139], v25 offset:13312
v_mfma_f32_16x16x32_bf16 v[100:103], a[164:167], v[140:143], v[100:103]
v_mfma_f32_16x16x32_bf16 v[104:107], a[168:171], v[140:143], v[104:107]
ds_write_b128 v26, v[128:131] offset:36864
v_mfma_f32_16x16x32_bf16 v[108:111], a[172:175], v[140:143], v[108:111]
s_waitcnt lgkmcnt(3)
v_mfma_f32_16x16x32_bf16 v[96:99], a[176:179], v[144:147], v[96:99]
ds_read_b128 v[140:143], v25 offset:14336
v_mfma_f32_16x16x32_bf16 v[100:103], a[180:183], v[144:147], v[100:103]
v_mfma_f32_16x16x32_bf16 v[104:107], a[184:187], v[144:147], v[104:107]
s_barrier
v_mfma_f32_16x16x32_bf16 v[108:111], a[188:191], v[144:147], v[108:111]
s_waitcnt lgkmcnt(3)
global_load_lds_dwordx4 v11, s[38:39] offset:-2048 sc1
v_mfma_f32_16x16x32_bf16 v[96:99], a[192:195], v[132:135], v[96:99]
ds_read_b128 v[144:147], v25 offset:15360
v_mfma_f32_16x16x32_bf16 v[100:103], a[196:199], v[132:135], v[100:103]
v_mfma_f32_16x16x32_bf16 v[104:107], a[200:203], v[132:135], v[104:107]
global_load_lds_dwordx4 v11, s[38:39] offset:-1024 sc1
v_mfma_f32_16x16x32_bf16 v[108:111], a[204:207], v[132:135], v[108:111]
s_waitcnt lgkmcnt(3)
v_mfma_f32_16x16x32_bf16 v[96:99], a[208:211], v[136:139], v[96:99]
ds_read_b128 v[132:135], v25 offset:16384
v_mfma_f32_16x16x32_bf16 v[100:103], a[212:215], v[136:139], v[100:103]
global_load_lds_dwordx4 v11, s[38:39] offset:0 sc1
v_mfma_f32_16x16x32_bf16 v[104:107], a[216:219], v[136:139], v[104:107]
v_mfma_f32_16x16x32_bf16 v[108:111], a[220:223], v[136:139], v[108:111]
s_waitcnt lgkmcnt(2)
v_mfma_f32_16x16x32_bf16 v[96:99], a[224:227], v[140:143], v[96:99]
ds_read_b128 v[136:139], v25 offset:17408
global_load_lds_dwordx4 v11, s[38:39] offset:1024 sc1
v_mfma_f32_16x16x32_bf16 v[100:103], a[228:231], v[140:143], v[100:103]
v_mfma_f32_16x16x32_bf16 v[104:107], a[232:235], v[140:143], v[104:107]
v_mfma_f32_16x16x32_bf16 v[108:111], a[236:239], v[140:143], v[108:111]
s_waitcnt lgkmcnt(2)
global_load_lds_dwordx4 v11, s[38:39] offset:2048 sc1
v_add_u32 v11, 18432, v11
v_mfma_f32_16x16x32_bf16 v[96:99], a[240:243], v[144:147], v[96:99]
v_min_u32 v11, v11, v12
v_mfma_f32_16x16x32_bf16 v[100:103], a[244:247], v[144:147], v[100:103]
v_mfma_f32_16x16x32_bf16 v[104:107], a[248:251], v[144:147], v[104:107]
s_waitcnt vmcnt(10)
s_barrier
v_mfma_f32_16x16x32_bf16 v[108:111], a[252:255], v[144:147], v[108:111]
s_waitcnt lgkmcnt(1)
ds_read_b128 v[140:143], v25 offset:18432
v_mfma_f32_16x16x32_bf16 v[96:99], v[28:31], v[132:135], v[96:99]
ds_read_b128 v[144:147], v25 offset:19456
v_mfma_f32_16x16x32_bf16 v[100:103], v[32:35], v[132:135], v[100:103]
v_mfma_f32_16x16x32_bf16 v[104:107], v[36:39], v[132:135], v[104:107]
v_mfma_f32_16x16x32_bf16 v[108:111], v[40:43], v[132:135], v[108:111]
s_waitcnt lgkmcnt(2)
v_mfma_f32_16x16x32_bf16 v[96:99], v[44:47], v[136:139], v[96:99]
ds_read_b128 v[132:135], v25 offset:20480
v_mfma_f32_16x16x32_bf16 v[100:103], v[48:51], v[136:139], v[100:103]
v_mfma_f32_16x16x32_bf16 v[104:107], v[52:55], v[136:139], v[104:107]
v_mfma_f32_16x16x32_bf16 v[108:111], v[56:59], v[136:139], v[108:111]
s_waitcnt lgkmcnt(2)
v_mfma_f32_16x16x32_bf16 v[112:115], a[0:3], v[140:143], v[112:115]
ds_read_b128 v[136:139], v25 offset:21504
v_mfma_f32_16x16x32_bf16 v[116:119], a[4:7], v[140:143], v[116:119]
v_mfma_f32_16x16x32_bf16 v[120:123], a[8:11], v[140:143], v[120:123]
s_waitcnt vmcnt(7)
s_barrier
v_mfma_f32_16x16x32_bf16 v[124:127], a[12:15], v[140:143], v[124:127]
s_mov_b64 exec, 1
global_store_dword v23, v24, s[38:39] sc1
s_mov_b64 exec, -1
v_add_u32 v24, 1, v24
v_mfma_f32_16x16x32_bf16 v[128:131], v[60:63], v[140:143], 0
s_waitcnt lgkmcnt(2)
v_cvt_pk_bf16_f32 v164, v96, v97
v_cvt_pk_bf16_f32 v165, v98, v99
v_mfma_f32_16x16x32_bf16 v[112:115], a[16:19], v[144:147], v[112:115]
ds_read_b128 v[140:143], v25 offset:22528
v_cvt_pk_bf16_f32 v166, v100, v101
v_mfma_f32_16x16x32_bf16 v[116:119], a[20:23], v[144:147], v[116:119]
v_cvt_pk_bf16_f32 v167, v102, v103
v_add_f32 v254, v96, v97
v_mfma_f32_16x16x32_bf16 v[120:123], a[24:27], v[144:147], v[120:123]
global_store_dwordx4 v13, v[164:167], s[38:39] offset:0 sc1
v_mfma_f32_16x16x32_bf16 v[124:127], a[28:31], v[144:147], v[124:127]
v_cvt_pk_bf16_f32 v168, v104, v105
v_cvt_pk_bf16_f32 v169, v106, v107
v_mfma_f32_16x16x32_bf16 v[128:131], v[64:67], v[144:147], v[128:131]
s_waitcnt lgkmcnt(2)
v_cvt_pk_bf16_f32 v170, v108, v109
v_cvt_pk_bf16_f32 v171, v110, v111
v_mfma_f32_16x16x32_bf16 v[112:115], a[32:35], v[132:135], v[112:115]
s_mov_b32 m0, 55296
ds_read_b128 v[144:147], v25 offset:23552
v_mul_f32 v255, v96, v96
v_mfma_f32_16x16x32_bf16 v[116:119], a[36:39], v[132:135], v[116:119]
global_store_dwordx4 v13, v[168:171], s[38:39] offset:1024 sc1
v_mfma_f32_16x16x32_bf16 v[120:123], a[40:43], v[132:135], v[120:123]
v_add_u32 v13, 18688, v13
v_fmac_f32 v255, v97, v97
v_mfma_f32_16x16x32_bf16 v[124:127], a[44:47], v[132:135], v[124:127]
v_add_f32 v254, v254, v98
v_fmac_f32 v255, v98, v98
v_mfma_f32_16x16x32_bf16 v[128:131], v[68:71], v[132:135], v[128:131]
s_waitcnt lgkmcnt(2)
v_add_f32 v254, v254, v99
v_fmac_f32 v255, v99, v99
v_mfma_f32_16x16x32_bf16 v[112:115], a[48:51], v[136:139], v[112:115]
ds_read_b128 v[132:135], v25 offset:24576
v_add_f32 v254, v254, v100
v_mfma_f32_16x16x32_bf16 v[116:119], a[52:55], v[136:139], v[116:119]
v_fmac_f32 v255, v100, v100
v_add_f32 v254, v254, v101
v_mfma_f32_16x16x32_bf16 v[120:123], a[56:59], v[136:139], v[120:123]
v_fmac_f32 v255, v101, v101
v_add_f32 v254, v254, v102
v_mfma_f32_16x16x32_bf16 v[124:127], a[60:63], v[136:139], v[124:127]
global_load_lds_dwordx4 v16, s[42:43] offset:0 sc1
v_fmac_f32 v255, v102, v102
v_mfma_f32_16x16x32_bf16 v[128:131], v[72:75], v[136:139], v[128:131]
s_waitcnt lgkmcnt(2)
v_add_f32 v254, v254, v103
v_fmac_f32 v255, v103, v103
v_mfma_f32_16x16x32_bf16 v[112:115], a[64:67], v[140:143], v[112:115]
ds_read_b128 v[136:139], v25 offset:25600
v_add_f32 v254, v254, v104
v_mfma_f32_16x16x32_bf16 v[116:119], a[68:71], v[140:143], v[116:119]
v_fmac_f32 v255, v104, v104
v_add_f32 v254, v254, v105
v_mfma_f32_16x16x32_bf16 v[120:123], a[72:75], v[140:143], v[120:123]
v_fmac_f32 v255, v105, v105
v_add_f32 v254, v254, v106
v_mfma_f32_16x16x32_bf16 v[124:127], a[76:79], v[140:143], v[124:127]
v_fmac_f32 v255, v106, v106
v_add_f32 v254, v254, v107
v_mfma_f32_16x16x32_bf16 v[128:131], v[76:79], v[140:143], v[128:131]
s_waitcnt vmcnt(9)
ds_read_b128 v[148:151], v19 offset:0
ds_read_b128 v[152:155], v20 offset:0
s_waitcnt lgkmcnt(4)
v_mfma_f32_16x16x32_bf16 v[112:115], a[80:83], v[144:147], v[112:115]
ds_read_b128 v[140:143], v25 offset:26624
global_load_lds_dwordx4 v17, s[42:43] offset:1024 sc1
v_mfma_f32_16x16x32_bf16 v[116:119], a[84:87], v[144:147], v[116:119]
v_fmac_f32 v255, v107, v107
v_add_f32 v254, v254, v108
v_mfma_f32_16x16x32_bf16 v[120:123], a[88:91], v[144:147], v[120:123]
v_fmac_f32 v255, v108, v108
v_add_f32 v254, v254, v109
v_mfma_f32_16x16x32_bf16 v[124:127], a[92:95], v[144:147], v[124:127]
v_fmac_f32 v255, v109, v109
v_add_f32 v254, v254, v110
)"
R"(v_mfma_f32_16x16x32_bf16 v[128:131], v[80:83], v[144:147], v[128:131]
s_waitcnt lgkmcnt(4)
v_fmac_f32 v255, v110, v110
v_add_f32 v254, v254, v111
v_mfma_f32_16x16x32_bf16 v[112:115], a[96:99], v[132:135], v[112:115]
s_add_u32 m0, s49, 18432
ds_read_b128 v[144:147], v25 offset:27648
v_fmac_f32 v255, v111, v111
v_mfma_f32_16x16x32_bf16 v[116:119], a[100:103], v[132:135], v[116:119]
ds_write_b64 v27, v[254:255] offset:43008
v_mfma_f32_16x16x32_bf16 v[120:123], a[104:107], v[132:135], v[120:123]
s_waitcnt lgkmcnt(4)
v_lshlrev_b32 v96, 16, v148
v_and_b32 v97, 0xffff0000, v148
v_mfma_f32_16x16x32_bf16 v[124:127], a[108:111], v[132:135], v[124:127]
s_sub_u32 s42, s42, 18432
v_lshlrev_b32 v98, 16, v149
v_and_b32 v99, 0xffff0000, v149
v_mfma_f32_16x16x32_bf16 v[128:131], v[84:87], v[132:135], v[128:131]
v_lshlrev_b32 v100, 16, v150
v_and_b32 v101, 0xffff0000, v150
v_mfma_f32_16x16x32_bf16 v[112:115], a[112:115], v[136:139], v[112:115]
s_subb_u32 s43, s43, 0
ds_read_b128 v[132:135], v25 offset:28672
v_lshlrev_b32 v102, 16, v151
v_mfma_f32_16x16x32_bf16 v[116:119], a[116:119], v[136:139], v[116:119]
s_waitcnt lgkmcnt(4)
v_and_b32 v103, 0xffff0000, v151
v_lshlrev_b32 v104, 16, v152
v_mfma_f32_16x16x32_bf16 v[120:123], a[120:123], v[136:139], v[120:123]
v_and_b32 v105, 0xffff0000, v152
v_lshlrev_b32 v106, 16, v153
v_mfma_f32_16x16x32_bf16 v[124:127], a[124:127], v[136:139], v[124:127]
v_and_b32 v107, 0xffff0000, v153
v_lshlrev_b32 v108, 16, v154
v_mfma_f32_16x16x32_bf16 v[128:131], v[88:91], v[136:139], v[128:131]
s_waitcnt lgkmcnt(3)
v_and_b32 v109, 0xffff0000, v154
v_lshlrev_b32 v110, 16, v155
v_mfma_f32_16x16x32_bf16 v[112:115], a[128:131], v[140:143], v[112:115]
ds_read_b128 v[136:139], v25 offset:29696
v_and_b32 v111, 0xffff0000, v155
v_mfma_f32_16x16x32_bf16 v[116:119], a[132:135], v[140:143], v[116:119]
v_mfma_f32_16x16x32_bf16 v[120:123], a[136:139], v[140:143], v[120:123]
v_mfma_f32_16x16x32_bf16 v[124:127], a[140:143], v[140:143], v[124:127]
v_mfma_f32_16x16x32_bf16 v[128:131], v[92:95], v[140:143], v[128:131]
s_waitcnt lgkmcnt(3)
v_mfma_f32_16x16x32_bf16 v[112:115], a[144:147], v[144:147], v[112:115]
ds_read_b128 v[140:143], v25 offset:30720
v_mfma_f32_16x16x32_bf16 v[116:119], a[148:151], v[144:147], v[116:119]
v_mfma_f32_16x16x32_bf16 v[120:123], a[152:155], v[144:147], v[120:123]
v_mfma_f32_16x16x32_bf16 v[124:127], a[156:159], v[144:147], v[124:127]
s_waitcnt lgkmcnt(2)
v_mfma_f32_16x16x32_bf16 v[112:115], a[160:163], v[132:135], v[112:115]
ds_read_b128 v[144:147], v25 offset:31744
v_mfma_f32_16x16x32_bf16 v[116:119], a[164:167], v[132:135], v[116:119]
v_mfma_f32_16x16x32_bf16 v[120:123], a[168:171], v[132:135], v[120:123]
ds_write_b128 v26, v[128:131] offset:39936
v_mfma_f32_16x16x32_bf16 v[124:127], a[172:175], v[132:135], v[124:127]
s_waitcnt lgkmcnt(3)
v_mfma_f32_16x16x32_bf16 v[112:115], a[176:179], v[136:139], v[112:115]
ds_read_b128 v[132:135], v25 offset:32768
v_mfma_f32_16x16x32_bf16 v[116:119], a[180:183], v[136:139], v[116:119]
v_mfma_f32_16x16x32_bf16 v[120:123], a[184:187], v[136:139], v[120:123]
s_barrier
v_mfma_f32_16x16x32_bf16 v[124:127], a[188:191], v[136:139], v[124:127]
s_waitcnt lgkmcnt(3)
global_load_lds_dwordx4 v11, s[38:39] offset:-2048 sc1
v_mfma_f32_16x16x32_bf16 v[112:115], a[192:195], v[140:143], v[112:115]
s_mov_b64 s[76:77], -1
ds_read_b128 v[136:139], v25 offset:33792
v_mfma_f32_16x16x32_bf16 v[116:119], a[196:199], v[140:143], v[116:119]
v_mfma_f32_16x16x32_bf16 v[120:123], a[200:203], v[140:143], v[120:123]
global_load_lds_dwordx4 v11, s[38:39] offset:-1024 sc1
v_mfma_f32_16x16x32_bf16 v[124:127], a[204:207], v[140:143], v[124:127]
s_waitcnt lgkmcnt(3)
v_mfma_f32_16x16x32_bf16 v[112:115], a[208:211], v[144:147], v[112:115]
ds_read_b128 v[140:143], v25 offset:34816
v_mfma_f32_16x16x32_bf16 v[116:119], a[212:215], v[144:147], v[116:119]
global_load_lds_dwordx4 v11, s[38:39] offset:0 sc1
v_mfma_f32_16x16x32_bf16 v[120:123], a[216:219], v[144:147], v[120:123]
v_mfma_f32_16x16x32_bf16 v[124:127], a[220:223], v[144:147], v[124:127]
s_waitcnt lgkmcnt(2)
v_mfma_f32_16x16x32_bf16 v[112:115], a[224:227], v[132:135], v[112:115]
ds_read_b128 v[144:147], v25 offset:35840
global_load_lds_dwordx4 v11, s[38:39] offset:1024 sc1
v_mfma_f32_16x16x32_bf16 v[116:119], a[228:231], v[132:135], v[116:119]
v_mfma_f32_16x16x32_bf16 v[120:123], a[232:235], v[132:135], v[120:123]
v_mfma_f32_16x16x32_bf16 v[124:127], a[236:239], v[132:135], v[124:127]
s_waitcnt lgkmcnt(2)
v_xor_b32 v25, 0x10000, v25
global_load_lds_dwordx4 v11, s[38:39] offset:2048 sc1
v_mfma_f32_16x16x32_bf16 v[112:115], a[240:243], v[136:139], v[112:115]
v_add_u32 v11, 18432, v11
v_min_u32 v11, v11, v12
v_mfma_f32_16x16x32_bf16 v[116:119], a[244:247], v[136:139], v[116:119]
v_mfma_f32_16x16x32_bf16 v[120:123], a[248:251], v[136:139], v[120:123]
s_waitcnt vmcnt(10)
s_barrier
v_mfma_f32_16x16x32_bf16 v[124:127], a[252:255], v[136:139], v[124:127]
s_waitcnt lgkmcnt(1)
ds_read_b128 v[132:135], v25 offset:0
v_mfma_f32_16x16x32_bf16 v[112:115], v[28:31], v[140:143], v[112:115]
ds_read_b128 v[136:139], v25 offset:1024
v_mfma_f32_16x16x32_bf16 v[116:119], v[32:35], v[140:143], v[116:119]
v_mfma_f32_16x16x32_bf16 v[120:123], v[36:39], v[140:143], v[120:123]
v_mfma_f32_16x16x32_bf16 v[124:127], v[40:43], v[140:143], v[124:127]
s_waitcnt lgkmcnt(2)
v_mfma_f32_16x16x32_bf16 v[112:115], v[44:47], v[144:147], v[112:115]
ds_read_b128 v[140:143], v25 offset:2048
v_mfma_f32_16x16x32_bf16 v[116:119], v[48:51], v[144:147], v[116:119]
v_mfma_f32_16x16x32_bf16 v[120:123], v[52:55], v[144:147], v[120:123]
v_mfma_f32_16x16x32_bf16 v[124:127], v[56:59], v[144:147], v[124:127]
s_sub_u32 s70, s70, 1
s_cbranch_scc0 .Lwo_top5
s_waitcnt vmcnt(7)
s_barrier
s_mov_b64 exec, 1
global_store_dword v23, v24, s[38:39] sc1
s_mov_b64 exec, -1
v_add_u32 v24, 1, v24
v_cvt_pk_bf16_f32 v164, v112, v113
v_cvt_pk_bf16_f32 v165, v114, v115
v_cvt_pk_bf16_f32 v166, v116, v117
v_cvt_pk_bf16_f32 v167, v118, v119
global_store_dwordx4 v13, v[164:167], s[38:39] offset:0 sc1
v_cvt_pk_bf16_f32 v168, v120, v121
v_cvt_pk_bf16_f32 v169, v122, v123
v_cvt_pk_bf16_f32 v170, v124, v125
v_cvt_pk_bf16_f32 v171, v126, v127
global_store_dwordx4 v13, v[168:171], s[38:39] offset:1024 sc1
v_add_f32 v254, v112, v113
v_mul_f32 v255, v112, v112
v_fmac_f32 v255, v113, v113
v_add_f32 v254, v254, v114
v_fmac_f32 v255, v114, v114
v_add_f32 v254, v254, v115
v_fmac_f32 v255, v115, v115
v_add_f32 v254, v254, v116
v_fmac_f32 v255, v116, v116
v_add_f32 v254, v254, v117
v_fmac_f32 v255, v117, v117
v_add_f32 v254, v254, v118
v_fmac_f32 v255, v118, v118
v_add_f32 v254, v254, v119
v_fmac_f32 v255, v119, v119
v_add_f32 v254, v254, v120
v_fmac_f32 v255, v120, v120
v_add_f32 v254, v254, v121
v_fmac_f32 v255, v121, v121
v_add_f32 v254, v254, v122
v_fmac_f32 v255, v122, v122
v_add_f32 v254, v254, v123
v_fmac_f32 v255, v123, v123
v_add_f32 v254, v254, v124
v_fmac_f32 v255, v124, v124
v_add_f32 v254, v254, v125
v_fmac_f32 v255, v125, v125
v_add_f32 v254, v254, v126
v_fmac_f32 v255, v126, v126
v_add_f32 v254, v254, v127
v_fmac_f32 v255, v127, v127
ds_write_b64 v27, v[254:255] offset:45056
s_waitcnt vmcnt(0) lgkmcnt(0)
s_barrier
s_mov_b64 exec, 1
global_store_dword v23, v24, s[38:39] sc1
s_mov_b64 exec, -1
v_add_u32 v24, 3, v24
s_waitcnt vmcnt(0)
s_barrier
s_mov_b64 exec, 1
global_store_dword v23, v24, s[38:39] sc1
s_mov_b64 exec, -1
s_endpgm
.Lwo_w2:
s_add_u32 m0, s48, 4096
s_nop 0
global_load_lds_dwordx4 v0, s[40:41] offset:-4096
global_load_lds_dwordx4 v1, s[40:41] offset:-3072
global_load_lds_dwordx4 v2, s[40:41] offset:-2048
global_load_lds_dwordx4 v3, s[40:41] offset:-1024
global_load_lds_dwordx4 v4, s[40:41] offset:0
global_load_lds_dwordx4 v5, s[40:41] offset:1024
global_load_lds_dwordx4 v6, s[40:41] offset:2048
global_load_lds_dwordx4 v7, s[40:41] offset:3072
s_add_u32 m0, s48, 12288
s_nop 0
global_load_lds_dwordx4 v8, s[40:41] offset:-4096
s_add_u32 s40, s40, 73728
s_addc_u32 s41, s41, 0
s_add_u32 m0, s48, 40960
s_nop 0
global_load_lds_dwordx4 v0, s[40:41] offset:-4096
global_load_lds_dwordx4 v1, s[40:41] offset:-3072
global_load_lds_dwordx4 v2, s[40:41] offset:-2048
global_load_lds_dwordx4 v3, s[40:41] offset:-1024
global_load_lds_dwordx4 v4, s[40:41] offset:0
global_load_lds_dwordx4 v5, s[40:41] offset:1024
global_load_lds_dwordx4 v6, s[40:41] offset:2048
global_load_lds_dwordx4 v7, s[40:41] offset:3072
s_add_u32 m0, s48, 49152
s_nop 0
global_load_lds_dwordx4 v8, s[40:41] offset:-4096
s_add_u32 s40, s40, 73728
s_addc_u32 s41, s41, 0
s_add_u32 m0, s48, 77824
s_nop 0
global_load_lds_dwordx4 v0, s[40:41] offset:-4096
global_load_lds_dwordx4 v1, s[40:41] offset:-3072
global_load_lds_dwordx4 v2, s[40:41] offset:-2048
global_load_lds_dwordx4 v3, s[40:41] offset:-1024
global_load_lds_dwordx4 v4, s[40:41] offset:0
global_load_lds_dwordx4 v5, s[40:41] offset:1024
global_load_lds_dwordx4 v6, s[40:41] offset:2048
global_load_lds_dwordx4 v7, s[40:41] offset:3072
s_add_u32 m0, s48, 86016
s_nop 0
global_load_lds_dwordx4 v8, s[40:41] offset:-4096
s_add_u32 s40, s40, 73728
s_addc_u32 s41, s41, 0
v_add_u32 v204, 0x10000, v9
v_add_u32 v205, 0x20000, v9
v_add_u32 v206, 0x10000, v10
v_add_u32 v207, 0x20000, v10
s_waitcnt lgkmcnt(0)
s_mul_i32 s37, s37, 0x9E3779B1
s_add_u32 s37, s37, 0x7F4A7C15
v_add_u32 v22, s37, v22
v_add_u32 v24, s37, v24
s_waitcnt vmcnt(18)
s_barrier
s_add_u32 m0, s48, 114688
ds_read_b64_tr_b16 a[0:1], v9 offset:0
ds_read_b64_tr_b16 a[2:3], v9 offset:2304
global_load_lds_dwordx4 v0, s[40:41] offset:-4096
ds_read_b64_tr_b16 a[4:5], v9 offset:8
global_load_lds_dwordx4 v1, s[40:41] offset:-3072
ds_read_b64_tr_b16 a[6:7], v9 offset:2312
global_load_lds_dwordx4 v2, s[40:41] offset:-2048
ds_read_b64_tr_b16 a[8:9], v9 offset:64
global_load_lds_dwordx4 v3, s[40:41] offset:-1024
ds_read_b64_tr_b16 a[10:11], v9 offset:2368
ds_read_b64_tr_b16 a[12:13], v9 offset:72
global_load_lds_dwordx4 v4, s[40:41] offset:0
ds_read_b64_tr_b16 a[14:15], v9 offset:2376
global_load_lds_dwordx4 v5, s[40:41] offset:1024
ds_read_b64_tr_b16 a[16:17], v9 offset:18432
global_load_lds_dwordx4 v6, s[40:41] offset:2048
ds_read_b64_tr_b16 a[18:19], v9 offset:20736
global_load_lds_dwordx4 v7, s[40:41] offset:3072
ds_read_b64_tr_b16 a[20:21], v9 offset:18440
ds_read_b64_tr_b16 a[22:23], v9 offset:20744
s_add_u32 m0, s48, 122880
ds_read_b64_tr_b16 a[24:25], v9 offset:18496
global_load_lds_dwordx4 v8, s[40:41] offset:-4096
ds_read_b64_tr_b16 a[26:27], v9 offset:20800
s_add_u32 s40, s40, 73728
ds_read_b64_tr_b16 a[28:29], v9 offset:18504
s_addc_u32 s41, s41, 0
ds_read_b64_tr_b16 a[30:31], v9 offset:20808
s_waitcnt vmcnt(18) lgkmcnt(0)
s_barrier
s_add_u32 m0, s48, 4096
ds_read_b64_tr_b16 a[32:33], v9 offset:36864
ds_read_b64_tr_b16 a[34:35], v9 offset:39168
global_load_lds_dwordx4 v0, s[40:41] offset:-4096
ds_read_b64_tr_b16 a[36:37], v9 offset:36872
global_load_lds_dwordx4 v1, s[40:41] offset:-3072
ds_read_b64_tr_b16 a[38:39], v9 offset:39176
global_load_lds_dwordx4 v2, s[40:41] offset:-2048
ds_read_b64_tr_b16 a[40:41], v9 offset:36928
global_load_lds_dwordx4 v3, s[40:41] offset:-1024
ds_read_b64_tr_b16 a[42:43], v9 offset:39232
ds_read_b64_tr_b16 a[44:45], v9 offset:36936
global_load_lds_dwordx4 v4, s[40:41] offset:0
ds_read_b64_tr_b16 a[46:47], v9 offset:39240
global_load_lds_dwordx4 v5, s[40:41] offset:1024
ds_read_b64_tr_b16 a[48:49], v9 offset:55296
global_load_lds_dwordx4 v6, s[40:41] offset:2048
ds_read_b64_tr_b16 a[50:51], v9 offset:57600
global_load_lds_dwordx4 v7, s[40:41] offset:3072
ds_read_b64_tr_b16 a[52:53], v9 offset:55304
ds_read_b64_tr_b16 a[54:55], v9 offset:57608
s_add_u32 m0, s48, 12288
ds_read_b64_tr_b16 a[56:57], v9 offset:55360
global_load_lds_dwordx4 v8, s[40:41] offset:-4096
ds_read_b64_tr_b16 a[58:59], v9 offset:57664
s_add_u32 s40, s40, 73728
ds_read_b64_tr_b16 a[60:61], v9 offset:55368
s_addc_u32 s41, s41, 0
ds_read_b64_tr_b16 a[62:63], v9 offset:57672
s_waitcnt vmcnt(18) lgkmcnt(0)
s_barrier
s_add_u32 m0, s48, 40960
ds_read_b64_tr_b16 a[64:65], v204 offset:8192
ds_read_b64_tr_b16 a[66:67], v204 offset:10496
global_load_lds_dwordx4 v0, s[40:41] offset:-4096
ds_read_b64_tr_b16 a[68:69], v204 offset:8200
global_load_lds_dwordx4 v1, s[40:41] offset:-3072
ds_read_b64_tr_b16 a[70:71], v204 offset:10504
global_load_lds_dwordx4 v2, s[40:41] offset:-2048
ds_read_b64_tr_b16 a[72:73], v204 offset:8256
global_load_lds_dwordx4 v3, s[40:41] offset:-1024
ds_read_b64_tr_b16 a[74:75], v204 offset:10560
ds_read_b64_tr_b16 a[76:77], v204 offset:8264
global_load_lds_dwordx4 v4, s[40:41] offset:0
ds_read_b64_tr_b16 a[78:79], v204 offset:10568
global_load_lds_dwordx4 v5, s[40:41] offset:1024
ds_read_b64_tr_b16 a[80:81], v204 offset:26624
global_load_lds_dwordx4 v6, s[40:41] offset:2048
ds_read_b64_tr_b16 a[82:83], v204 offset:28928
global_load_lds_dwordx4 v7, s[40:41] offset:3072
ds_read_b64_tr_b16 a[84:85], v204 offset:26632
ds_read_b64_tr_b16 a[86:87], v204 offset:28936
s_add_u32 m0, s48, 49152
ds_read_b64_tr_b16 a[88:89], v204 offset:26688
global_load_lds_dwordx4 v8, s[40:41] offset:-4096
ds_read_b64_tr_b16 a[90:91], v204 offset:28992
s_add_u32 s40, s40, 73728
ds_read_b64_tr_b16 a[92:93], v204 offset:26696
s_addc_u32 s41, s41, 0
ds_read_b64_tr_b16 a[94:95], v204 offset:29000
s_waitcnt vmcnt(18) lgkmcnt(0)
s_barrier
s_add_u32 m0, s48, 77824
ds_read_b64_tr_b16 a[96:97], v204 offset:45056
ds_read_b64_tr_b16 a[98:99], v204 offset:47360
global_load_lds_dwordx4 v0, s[40:41] offset:-4096
ds_read_b64_tr_b16 a[100:101], v204 offset:45064
global_load_lds_dwordx4 v1, s[40:41] offset:-3072
ds_read_b64_tr_b16 a[102:103], v204 offset:47368
global_load_lds_dwordx4 v2, s[40:41] offset:-2048
ds_read_b64_tr_b16 a[104:105], v204 offset:45120
global_load_lds_dwordx4 v3, s[40:41] offset:-1024
ds_read_b64_tr_b16 a[106:107], v204 offset:47424
ds_read_b64_tr_b16 a[108:109], v204 offset:45128
global_load_lds_dwordx4 v4, s[40:41] offset:0
ds_read_b64_tr_b16 a[110:111], v204 offset:47432
global_load_lds_dwordx4 v5, s[40:41] offset:1024
ds_read_b64_tr_b16 a[112:113], v204 offset:63488
global_load_lds_dwordx4 v6, s[40:41] offset:2048
ds_read_b64_tr_b16 a[114:115], v205 offset:256
global_load_lds_dwordx4 v7, s[40:41] offset:3072
ds_read_b64_tr_b16 a[116:117], v204 offset:63496
ds_read_b64_tr_b16 a[118:119], v205 offset:264
s_add_u32 m0, s48, 86016
ds_read_b64_tr_b16 a[120:121], v204 offset:63552
global_load_lds_dwordx4 v8, s[40:41] offset:-4096
ds_read_b64_tr_b16 a[122:123], v205 offset:320
s_add_u32 s40, s40, 73728
ds_read_b64_tr_b16 a[124:125], v204 offset:63560
s_addc_u32 s41, s41, 0
ds_read_b64_tr_b16 a[126:127], v205 offset:328
s_waitcnt vmcnt(18) lgkmcnt(0)
s_barrier
s_add_u32 m0, s48, 114688
ds_read_b64_tr_b16 a[128:129], v9 offset:0
ds_read_b64_tr_b16 a[130:131], v9 offset:2304
global_load_lds_dwordx4 v0, s[40:41] offset:-4096
ds_read_b64_tr_b16 a[132:133], v9 offset:8
global_load_lds_dwordx4 v1, s[40:41] offset:-3072
ds_read_b64_tr_b16 a[134:135], v9 offset:2312
ds_read_b64_tr_b16 a[136:137], v9 offset:64
global_load_lds_dwordx4 v2, s[40:41] offset:-2048
ds_read_b64_tr_b16 a[138:139], v9 offset:2368
global_load_lds_dwordx4 v3, s[40:41] offset:-1024
ds_read_b64_tr_b16 a[140:141], v9 offset:72
global_load_lds_dwordx4 v4, s[40:41] offset:0
ds_read_b64_tr_b16 a[142:143], v9 offset:2376
ds_read_b64_tr_b16 a[144:145], v9 offset:18432
global_load_lds_dwordx4 v5, s[40:41] offset:1024
ds_read_b64_tr_b16 a[146:147], v9 offset:20736
global_load_lds_dwordx4 v6, s[40:41] offset:2048
ds_read_b64_tr_b16 a[148:149], v9 offset:18440
ds_read_b64_tr_b16 a[150:151], v9 offset:20744
global_load_lds_dwordx4 v7, s[40:41] offset:3072
ds_read_b64_tr_b16 a[152:153], v9 offset:18496
s_add_u32 m0, s48, 122880
ds_read_b64_tr_b16 a[154:155], v9 offset:20800
global_load_lds_dwordx4 v8, s[40:41] offset:-4096
ds_read_b64_tr_b16 a[156:157], v9 offset:18504
ds_read_b64_tr_b16 a[158:159], v9 offset:20808
s_add_u32 s40, s40, 73728
ds_read_b64_tr_b16 v[60:61], v10 offset:18432
s_addc_u32 s41, s41, 0
ds_read_b64_tr_b16 v[62:63], v10 offset:20736
s_waitcnt vmcnt(18) lgkmcnt(0)
s_barrier
s_add_u32 m0, s48, 4096
ds_read_b64_tr_b16 a[160:161], v9 offset:36864
ds_read_b64_tr_b16 a[162:163], v9 offset:39168
global_load_lds_dwordx4 v0, s[40:41] offset:-4096
ds_read_b64_tr_b16 a[164:165], v9 offset:36872
ds_read_b64_tr_b16 a[166:167], v9 offset:39176
global_load_lds_dwordx4 v1, s[40:41] offset:-3072
ds_read_b64_tr_b16 a[168:169], v9 offset:36928
global_load_lds_dwordx4 v2, s[40:41] offset:-2048
ds_read_b64_tr_b16 a[170:171], v9 offset:39232
ds_read_b64_tr_b16 a[172:173], v9 offset:36936
)"
R"(global_load_lds_dwordx4 v3, s[40:41] offset:-1024
ds_read_b64_tr_b16 a[174:175], v9 offset:39240
global_load_lds_dwordx4 v4, s[40:41] offset:0
ds_read_b64_tr_b16 v[64:65], v10 offset:36864
ds_read_b64_tr_b16 v[66:67], v10 offset:39168
global_load_lds_dwordx4 v5, s[40:41] offset:1024
ds_read_b64_tr_b16 a[176:177], v9 offset:55296
global_load_lds_dwordx4 v6, s[40:41] offset:2048
ds_read_b64_tr_b16 a[178:179], v9 offset:57600
ds_read_b64_tr_b16 a[180:181], v9 offset:55304
global_load_lds_dwordx4 v7, s[40:41] offset:3072
ds_read_b64_tr_b16 a[182:183], v9 offset:57608
s_add_u32 m0, s48, 12288
ds_read_b64_tr_b16 a[184:185], v9 offset:55360
ds_read_b64_tr_b16 a[186:187], v9 offset:57664
global_load_lds_dwordx4 v8, s[40:41] offset:-4096
ds_read_b64_tr_b16 a[188:189], v9 offset:55368
s_sub_u32 s40, s40, 589824
ds_read_b64_tr_b16 a[190:191], v9 offset:57672
ds_read_b64_tr_b16 v[68:69], v10 offset:55296
s_subb_u32 s41, s41, 0
ds_read_b64_tr_b16 v[70:71], v10 offset:57600
s_waitcnt vmcnt(18) lgkmcnt(0)
s_barrier
ds_read_b64_tr_b16 a[192:193], v204 offset:8192
ds_read_b64_tr_b16 a[194:195], v204 offset:10496
ds_read_b64_tr_b16 a[196:197], v204 offset:8200
ds_read_b64_tr_b16 a[198:199], v204 offset:10504
ds_read_b64_tr_b16 a[200:201], v204 offset:8256
ds_read_b64_tr_b16 a[202:203], v204 offset:10560
ds_read_b64_tr_b16 a[204:205], v204 offset:8264
ds_read_b64_tr_b16 a[206:207], v204 offset:10568
ds_read_b64_tr_b16 v[72:73], v206 offset:8192
ds_read_b64_tr_b16 v[74:75], v206 offset:10496
ds_read_b64_tr_b16 a[208:209], v204 offset:26624
ds_read_b64_tr_b16 a[210:211], v204 offset:28928
ds_read_b64_tr_b16 a[212:213], v204 offset:26632
ds_read_b64_tr_b16 a[214:215], v204 offset:28936
ds_read_b64_tr_b16 a[216:217], v204 offset:26688
ds_read_b64_tr_b16 a[218:219], v204 offset:28992
ds_read_b64_tr_b16 a[220:221], v204 offset:26696
ds_read_b64_tr_b16 a[222:223], v204 offset:29000
ds_read_b64_tr_b16 v[76:77], v206 offset:26624
ds_read_b64_tr_b16 v[78:79], v206 offset:28928
s_waitcnt vmcnt(9) lgkmcnt(0)
s_barrier
ds_read_b64_tr_b16 a[224:225], v204 offset:45056
ds_read_b64_tr_b16 a[226:227], v204 offset:47360
ds_read_b64_tr_b16 a[228:229], v204 offset:45064
ds_read_b64_tr_b16 a[230:231], v204 offset:47368
ds_read_b64_tr_b16 a[232:233], v204 offset:45120
ds_read_b64_tr_b16 a[234:235], v204 offset:47424
ds_read_b64_tr_b16 a[236:237], v204 offset:45128
ds_read_b64_tr_b16 a[238:239], v204 offset:47432
ds_read_b64_tr_b16 v[80:81], v206 offset:45056
ds_read_b64_tr_b16 v[82:83], v206 offset:47360
ds_read_b64_tr_b16 a[240:241], v204 offset:63488
ds_read_b64_tr_b16 a[242:243], v205 offset:256
ds_read_b64_tr_b16 a[244:245], v204 offset:63496
ds_read_b64_tr_b16 a[246:247], v205 offset:264
ds_read_b64_tr_b16 a[248:249], v204 offset:63552
ds_read_b64_tr_b16 a[250:251], v205 offset:320
ds_read_b64_tr_b16 a[252:253], v204 offset:63560
ds_read_b64_tr_b16 a[254:255], v205 offset:328
ds_read_b64_tr_b16 v[84:85], v206 offset:63488
ds_read_b64_tr_b16 v[86:87], v207 offset:256
s_waitcnt vmcnt(0) lgkmcnt(0)
s_barrier
ds_read_b64_tr_b16 v[28:29], v9 offset:0
ds_read_b64_tr_b16 v[30:31], v9 offset:2304
ds_read_b64_tr_b16 v[32:33], v9 offset:8
ds_read_b64_tr_b16 v[34:35], v9 offset:2312
ds_read_b64_tr_b16 v[36:37], v9 offset:64
ds_read_b64_tr_b16 v[38:39], v9 offset:2368
ds_read_b64_tr_b16 v[40:41], v9 offset:72
ds_read_b64_tr_b16 v[42:43], v9 offset:2376
ds_read_b64_tr_b16 v[88:89], v10 offset:0
ds_read_b64_tr_b16 v[90:91], v10 offset:2304
ds_read_b64_tr_b16 v[44:45], v9 offset:18432
ds_read_b64_tr_b16 v[46:47], v9 offset:20736
ds_read_b64_tr_b16 v[48:49], v9 offset:18440
ds_read_b64_tr_b16 v[50:51], v9 offset:20744
ds_read_b64_tr_b16 v[52:53], v9 offset:18496
ds_read_b64_tr_b16 v[54:55], v9 offset:20800
ds_read_b64_tr_b16 v[56:57], v9 offset:18504
ds_read_b64_tr_b16 v[58:59], v9 offset:20808
ds_read_b64_tr_b16 v[92:93], v10 offset:18432
ds_read_b64_tr_b16 v[94:95], v10 offset:20736
s_waitcnt lgkmcnt(0)
s_barrier
s_mov_b64 s[76:77], 0
s_add_u32 m0, s49, 18432
s_mov_b32 s70, 33
s_waitcnt vmcnt(0) lgkmcnt(0)
s_nop 7
.p2align 6
.Lwo_top12:
s_waitcnt lgkmcnt(2)
v_mfma_f32_16x16x32_bf16 v[96:99], a[144:147], v[132:135], v[96:99]
ds_read_b128 v[144:147], v25 offset:12288
v_mfma_f32_16x16x32_bf16 v[100:103], a[148:151], v[132:135], v[100:103]
v_mfma_f32_16x16x32_bf16 v[104:107], a[152:155], v[132:135], v[104:107]
s_waitcnt vmcnt(8)
s_barrier
v_mfma_f32_16x16x32_bf16 v[108:111], a[156:159], v[132:135], v[108:111]
v_cvt_pk_bf16_f32 v164, v112, v113
v_cvt_pk_bf16_f32 v165, v114, v115
v_mfma_f32_16x16x32_bf16 v[128:131], v[60:63], v[132:135], 0
s_waitcnt lgkmcnt(2)
v_cvt_pk_bf16_f32 v166, v116, v117
v_cvt_pk_bf16_f32 v167, v118, v119
v_mfma_f32_16x16x32_bf16 v[96:99], a[160:163], v[136:139], v[96:99]
ds_read_b128 v[132:135], v25 offset:13312
v_add_f32 v254, v112, v113
v_mfma_f32_16x16x32_bf16 v[100:103], a[164:167], v[136:139], v[100:103]
global_store_dwordx4 v13, v[164:167], s[38:39] offset:0 sc1
v_mfma_f32_16x16x32_bf16 v[104:107], a[168:171], v[136:139], v[104:107]
v_cvt_pk_bf16_f32 v168, v120, v121
v_cvt_pk_bf16_f32 v169, v122, v123
v_mfma_f32_16x16x32_bf16 v[108:111], a[172:175], v[136:139], v[108:111]
v_cvt_pk_bf16_f32 v170, v124, v125
v_cvt_pk_bf16_f32 v171, v126, v127
v_mfma_f32_16x16x32_bf16 v[128:131], v[64:67], v[136:139], v[128:131]
s_waitcnt lgkmcnt(2)
v_mul_f32 v255, v112, v112
v_fmac_f32 v255, v113, v113
v_mfma_f32_16x16x32_bf16 v[96:99], a[176:179], v[140:143], v[96:99]
s_cmp_eq_u32 s70, 32
ds_read_b128 v[136:139], v25 offset:14336
v_add_f32 v254, v254, v114
v_mfma_f32_16x16x32_bf16 v[100:103], a[180:183], v[140:143], v[100:103]
s_cselect_b64 s[42:43], s[44:45], s[42:43]
global_store_dwordx4 v13, v[168:171], s[38:39] offset:1024 sc1
v_mfma_f32_16x16x32_bf16 v[104:107], a[184:187], v[140:143], v[104:107]
s_cmp_eq_u32 s70, 16
v_add_u32 v13, 18688, v13
v_fmac_f32 v255, v114, v114
v_mfma_f32_16x16x32_bf16 v[108:111], a[188:191], v[140:143], v[108:111]
s_cselect_b64 s[42:43], s[46:47], s[42:43]
v_add_f32 v254, v254, v115
v_fmac_f32 v255, v115, v115
v_mfma_f32_16x16x32_bf16 v[128:131], v[68:71], v[140:143], v[128:131]
s_waitcnt lgkmcnt(2)
v_add_f32 v254, v254, v116
v_fmac_f32 v255, v116, v116
v_mfma_f32_16x16x32_bf16 v[96:99], a[192:195], v[144:147], v[96:99]
s_xor_b32 s49, s49, 0x10000
ds_read_b128 v[140:143], v25 offset:15360
v_add_f32 v254, v254, v117
v_mfma_f32_16x16x32_bf16 v[100:103], a[196:199], v[144:147], v[100:103]
s_mov_b32 m0, 49152
v_fmac_f32 v255, v117, v117
v_add_f32 v254, v254, v118
v_mfma_f32_16x16x32_bf16 v[104:107], a[200:203], v[144:147], v[104:107]
v_fmac_f32 v255, v118, v118
v_add_f32 v254, v254, v119
v_mfma_f32_16x16x32_bf16 v[108:111], a[204:207], v[144:147], v[108:111]
v_fmac_f32 v255, v119, v119
v_add_f32 v254, v254, v120
v_mfma_f32_16x16x32_bf16 v[128:131], v[72:75], v[144:147], v[128:131]
s_waitcnt lgkmcnt(2)
v_fmac_f32 v255, v120, v120
v_add_f32 v254, v254, v121
v_mfma_f32_16x16x32_bf16 v[96:99], a[208:211], v[132:135], v[96:99]
ds_read_b128 v[144:147], v25 offset:16384
global_load_lds_dwordx4 v16, s[42:43] offset:0 sc1
v_mfma_f32_16x16x32_bf16 v[100:103], a[212:215], v[132:135], v[100:103]
v_fmac_f32 v255, v121, v121
v_add_f32 v254, v254, v122
v_mfma_f32_16x16x32_bf16 v[104:107], a[216:219], v[132:135], v[104:107]
v_fmac_f32 v255, v122, v122
v_add_f32 v254, v254, v123
v_mfma_f32_16x16x32_bf16 v[108:111], a[220:223], v[132:135], v[108:111]
v_fmac_f32 v255, v123, v123
v_add_f32 v254, v254, v124
v_mfma_f32_16x16x32_bf16 v[128:131], v[76:79], v[132:135], v[128:131]
s_waitcnt vmcnt(9)
ds_read_b128 v[148:151], v19 offset:8192
ds_read_b128 v[152:155], v20 offset:8192
s_waitcnt lgkmcnt(4)
v_mfma_f32_16x16x32_bf16 v[96:99], a[224:227], v[136:139], v[96:99]
ds_read_b128 v[132:135], v25 offset:17408
v_fmac_f32 v255, v124, v124
v_mfma_f32_16x16x32_bf16 v[100:103], a[228:231], v[136:139], v[100:103]
v_add_f32 v254, v254, v125
v_fmac_f32 v255, v125, v125
v_mfma_f32_16x16x32_bf16 v[104:107], a[232:235], v[136:139], v[104:107]
global_load_lds_dwordx4 v17, s[42:43] offset:1024 sc1
v_add_f32 v254, v254, v126
v_mfma_f32_16x16x32_bf16 v[108:111], a[236:239], v[136:139], v[108:111]
v_fmac_f32 v255, v126, v126
v_add_f32 v254, v254, v127
v_mfma_f32_16x16x32_bf16 v[128:131], v[80:83], v[136:139], v[128:131]
s_waitcnt lgkmcnt(4)
v_fmac_f32 v255, v127, v127
v_mfma_f32_16x16x32_bf16 v[96:99], a[240:243], v[140:143], v[96:99]
ds_read_b128 v[136:139], v25 offset:0
v_mfma_f32_16x16x32_bf16 v[100:103], a[244:247], v[140:143], v[100:103]
ds_write_b64 v27, v[254:255] offset:45568
v_mfma_f32_16x16x32_bf16 v[104:107], a[248:251], v[140:143], v[104:107]
s_add_u32 m0, s49, 0
s_waitcnt lgkmcnt(4)
v_lshlrev_b32 v112, 16, v148
v_and_b32 v113, 0xffff0000, v148
v_mfma_f32_16x16x32_bf16 v[108:111], a[252:255], v[140:143], v[108:111]
v_lshlrev_b32 v114, 16, v149
v_and_b32 v115, 0xffff0000, v149
v_mfma_f32_16x16x32_bf16 v[128:131], v[84:87], v[140:143], v[128:131]
v_lshlrev_b32 v116, 16, v150
v_and_b32 v117, 0xffff0000, v150
v_mfma_f32_16x16x32_bf16 v[96:99], v[28:31], v[144:147], v[96:99]
s_sub_u32 s42, s42, 18432
ds_read_b128 v[140:143], v25 offset:1024
v_lshlrev_b32 v118, 16, v151
v_mfma_f32_16x16x32_bf16 v[100:103], v[32:35], v[144:147], v[100:103]
s_subb_u32 s43, s43, 0
v_and_b32 v119, 0xffff0000, v151
s_waitcnt lgkmcnt(4)
v_lshlrev_b32 v120, 16, v152
v_mfma_f32_16x16x32_bf16 v[104:107], v[36:39], v[144:147], v[104:107]
v_and_b32 v121, 0xffff0000, v152
v_lshlrev_b32 v122, 16, v153
v_mfma_f32_16x16x32_bf16 v[108:111], v[40:43], v[144:147], v[108:111]
v_and_b32 v123, 0xffff0000, v153
v_lshlrev_b32 v124, 16, v154
v_mfma_f32_16x16x32_bf16 v[128:131], v[88:91], v[144:147], v[128:131]
s_waitcnt lgkmcnt(3)
v_and_b32 v125, 0xffff0000, v154
v_lshlrev_b32 v126, 16, v155
v_mfma_f32_16x16x32_bf16 v[96:99], v[44:47], v[132:135], v[96:99]
ds_read_b128 v[144:147], v25 offset:2048
v_and_b32 v127, 0xffff0000, v155
v_mfma_f32_16x16x32_bf16 v[100:103], v[48:51], v[132:135], v[100:103]
v_mfma_f32_16x16x32_bf16 v[104:107], v[52:55], v[132:135], v[104:107]
v_mfma_f32_16x16x32_bf16 v[108:111], v[56:59], v[132:135], v[108:111]
v_mfma_f32_16x16x32_bf16 v[128:131], v[92:95], v[132:135], v[128:131]
s_waitcnt lgkmcnt(3)
v_mfma_f32_16x16x32_bf16 v[96:99], a[0:3], v[136:139], v[96:99]
ds_read_b128 v[132:135], v25 offset:3072
v_mfma_f32_16x16x32_bf16 v[100:103], a[4:7], v[136:139], v[100:103]
v_mfma_f32_16x16x32_bf16 v[104:107], a[8:11], v[136:139], v[104:107]
v_mfma_f32_16x16x32_bf16 v[108:111], a[12:15], v[136:139], v[108:111]
s_waitcnt lgkmcnt(2)
v_mfma_f32_16x16x32_bf16 v[96:99], a[16:19], v[140:143], v[96:99]
ds_read_b128 v[136:139], v25 offset:4096
v_mfma_f32_16x16x32_bf16 v[100:103], a[20:23], v[140:143], v[100:103]
v_sub_u32 v201, v201, v22
v_cmp_lt_u32 vcc, 0x7f, v201
v_mfma_f32_16x16x32_bf16 v[104:107], a[24:27], v[140:143], v[104:107]
v_mfma_f32_16x16x32_bf16 v[108:111], a[28:31], v[140:143], v[108:111]
s_waitcnt lgkmcnt(2)
v_mfma_f32_16x16x32_bf16 v[96:99], a[32:35], v[144:147], v[96:99]
s_and_b64 vcc, vcc, s[76:77]
s_cbranch_vccnz .Lwo_fr8
.Lwo_rt7:
ds_read_b128 v[140:143], v25 offset:5120
v_mfma_f32_16x16x32_bf16 v[100:103], a[36:39], v[144:147], v[100:103]
v_mfma_f32_16x16x32_bf16 v[104:107], a[40:43], v[144:147], v[104:107]
s_barrier
v_mfma_f32_16x16x32_bf16 v[108:111], a[44:47], v[144:147], v[108:111]
s_waitcnt lgkmcnt(2)
ds_write_b128 v26, v[128:131] offset:37888
v_mfma_f32_16x16x32_bf16 v[96:99], a[48:51], v[132:135], v[96:99]
ds_read_b128 v[144:147], v25 offset:6144
v_add_u32 v22, 1, v22
v_mfma_f32_16x16x32_bf16 v[100:103], a[52:55], v[132:135], v[100:103]
global_load_lds_dwordx4 v11, s[38:39] offset:-2048 sc1
v_mfma_f32_16x16x32_bf16 v[104:107], a[56:59], v[132:135], v[104:107]
v_mfma_f32_16x16x32_bf16 v[108:111], a[60:63], v[132:135], v[108:111]
s_waitcnt lgkmcnt(3)
v_mfma_f32_16x16x32_bf16 v[96:99], a[64:67], v[136:139], v[96:99]
ds_read_b128 v[132:135], v25 offset:7168
global_load_lds_dwordx4 v11, s[38:39] offset:-1024 sc1
v_mfma_f32_16x16x32_bf16 v[100:103], a[68:71], v[136:139], v[100:103]
v_mfma_f32_16x16x32_bf16 v[104:107], a[72:75], v[136:139], v[104:107]
v_mfma_f32_16x16x32_bf16 v[108:111], a[76:79], v[136:139], v[108:111]
s_waitcnt lgkmcnt(3)
global_load_lds_dwordx4 v11, s[38:39] offset:0 sc1
v_mfma_f32_16x16x32_bf16 v[96:99], a[80:83], v[140:143], v[96:99]
ds_read_b128 v[136:139], v25 offset:8192
v_mfma_f32_16x16x32_bf16 v[100:103], a[84:87], v[140:143], v[100:103]
v_mfma_f32_16x16x32_bf16 v[104:107], a[88:91], v[140:143], v[104:107]
global_load_lds_dwordx4 v11, s[38:39] offset:1024 sc1
v_mfma_f32_16x16x32_bf16 v[108:111], a[92:95], v[140:143], v[108:111]
s_waitcnt lgkmcnt(2)
global_load_dword v201, v21, s[38:39] sc1
v_mfma_f32_16x16x32_bf16 v[96:99], a[96:99], v[144:147], v[96:99]
v_mfma_f32_16x16x32_bf16 v[100:103], a[100:103], v[144:147], v[100:103]
v_mfma_f32_16x16x32_bf16 v[104:107], a[104:107], v[144:147], v[104:107]
s_waitcnt vmcnt(9)
s_barrier
v_mfma_f32_16x16x32_bf16 v[108:111], a[108:111], v[144:147], v[108:111]
s_waitcnt lgkmcnt(1)
ds_read_b128 v[140:143], v25 offset:27648
global_load_lds_dwordx4 v11, s[38:39] offset:2048 sc1
v_mfma_f32_16x16x32_bf16 v[96:99], a[112:115], v[132:135], v[96:99]
ds_read_b128 v[144:147], v25 offset:28672
v_add_u32 v11, 18432, v11
v_mfma_f32_16x16x32_bf16 v[100:103], a[116:119], v[132:135], v[100:103]
v_min_u32 v11, v11, v12
v_mfma_f32_16x16x32_bf16 v[104:107], a[120:123], v[132:135], v[104:107]
v_mfma_f32_16x16x32_bf16 v[108:111], a[124:127], v[132:135], v[108:111]
s_waitcnt lgkmcnt(2)
v_mfma_f32_16x16x32_bf16 v[96:99], a[128:131], v[136:139], v[96:99]
ds_read_b128 v[132:135], v25 offset:29696
v_mfma_f32_16x16x32_bf16 v[100:103], a[132:135], v[136:139], v[100:103]
v_mfma_f32_16x16x32_bf16 v[104:107], a[136:139], v[136:139], v[104:107]
v_mfma_f32_16x16x32_bf16 v[108:111], a[140:143], v[136:139], v[108:111]
s_waitcnt lgkmcnt(2)
v_mfma_f32_16x16x32_bf16 v[112:115], a[144:147], v[140:143], v[112:115]
ds_read_b128 v[136:139], v25 offset:30720
v_mfma_f32_16x16x32_bf16 v[116:119], a[148:151], v[140:143], v[116:119]
v_mfma_f32_16x16x32_bf16 v[120:123], a[152:155], v[140:143], v[120:123]
s_waitcnt vmcnt(8)
s_barrier
v_mfma_f32_16x16x32_bf16 v[124:127], a[156:159], v[140:143], v[124:127]
v_cvt_pk_bf16_f32 v164, v96, v97
v_cvt_pk_bf16_f32 v165, v98, v99
v_mfma_f32_16x16x32_bf16 v[128:131], v[60:63], v[140:143], 0
s_waitcnt lgkmcnt(2)
v_cvt_pk_bf16_f32 v166, v100, v101
v_cvt_pk_bf16_f32 v167, v102, v103
v_mfma_f32_16x16x32_bf16 v[112:115], a[160:163], v[144:147], v[112:115]
ds_read_b128 v[140:143], v25 offset:31744
v_add_f32 v254, v96, v97
v_mfma_f32_16x16x32_bf16 v[116:119], a[164:167], v[144:147], v[116:119]
global_store_dwordx4 v13, v[164:167], s[38:39] offset:0 sc1
v_mfma_f32_16x16x32_bf16 v[120:123], a[168:171], v[144:147], v[120:123]
v_cvt_pk_bf16_f32 v168, v104, v105
v_cvt_pk_bf16_f32 v169, v106, v107
v_mfma_f32_16x16x32_bf16 v[124:127], a[172:175], v[144:147], v[124:127]
v_cvt_pk_bf16_f32 v170, v108, v109
v_cvt_pk_bf16_f32 v171, v110, v111
v_mfma_f32_16x16x32_bf16 v[128:131], v[64:67], v[144:147], v[128:131]
s_waitcnt lgkmcnt(2)
v_mul_f32 v255, v96, v96
v_fmac_f32 v255, v97, v97
v_mfma_f32_16x16x32_bf16 v[112:115], a[176:179], v[132:135], v[112:115]
s_mov_b32 m0, 57344
ds_read_b128 v[144:147], v25 offset:32768
v_add_f32 v254, v254, v98
v_mfma_f32_16x16x32_bf16 v[116:119], a[180:183], v[132:135], v[116:119]
global_store_dwordx4 v13, v[168:171], s[38:39] offset:1024 sc1
v_mfma_f32_16x16x32_bf16 v[120:123], a[184:187], v[132:135], v[120:123]
v_add_u32 v13, 18688, v13
v_fmac_f32 v255, v98, v98
v_mfma_f32_16x16x32_bf16 v[124:127], a[188:191], v[132:135], v[124:127]
v_add_f32 v254, v254, v99
v_fmac_f32 v255, v99, v99
v_mfma_f32_16x16x32_bf16 v[128:131], v[68:71], v[132:135], v[128:131]
s_waitcnt lgkmcnt(2)
v_add_f32 v254, v254, v100
v_fmac_f32 v255, v100, v100
v_mfma_f32_16x16x32_bf16 v[112:115], a[192:195], v[136:139], v[112:115]
ds_read_b128 v[132:135], v25 offset:33792
v_add_f32 v254, v254, v101
v_mfma_f32_16x16x32_bf16 v[116:119], a[196:199], v[136:139], v[116:119]
v_fmac_f32 v255, v101, v101
v_add_f32 v254, v254, v102
v_mfma_f32_16x16x32_bf16 v[120:123], a[200:203], v[136:139], v[120:123]
v_fmac_f32 v255, v102, v102
v_add_f32 v254, v254, v103
v_mfma_f32_16x16x32_bf16 v[124:127], a[204:207], v[136:139], v[124:127]
v_fmac_f32 v255, v103, v103
v_add_f32 v254, v254, v104
v_mfma_f32_16x16x32_bf16 v[128:131], v[72:75], v[136:139], v[128:131]
s_waitcnt lgkmcnt(2)
v_fmac_f32 v255, v104, v104
v_add_f32 v254, v254, v105
v_mfma_f32_16x16x32_bf16 v[112:115], a[208:211], v[140:143], v[112:115]
ds_read_b128 v[136:139], v25 offset:34816
global_load_lds_dwordx4 v16, s[42:43] offset:0 sc1
v_mfma_f32_16x16x32_bf16 v[116:119], a[212:215], v[140:143], v[116:119]
v_fmac_f32 v255, v105, v105
v_add_f32 v254, v254, v106
v_mfma_f32_16x16x32_bf16 v[120:123], a[216:219], v[140:143], v[120:123]
v_fmac_f32 v255, v106, v106
v_add_f32 v254, v254, v107
v_mfma_f32_16x16x32_bf16 v[124:127], a[220:223], v[140:143], v[124:127]
v_fmac_f32 v255, v107, v107
v_add_f32 v254, v254, v108
v_mfma_f32_16x16x32_bf16 v[128:131], v[76:79], v[140:143], v[128:131]
s_waitcnt vmcnt(9)
ds_read_b128 v[148:151], v19 offset:0
ds_read_b128 v[152:155], v20 offset:0
s_waitcnt lgkmcnt(4)
v_mfma_f32_16x16x32_bf16 v[112:115], a[224:227], v[144:147], v[112:115]
ds_read_b128 v[140:143], v25 offset:35840
v_fmac_f32 v255, v108, v108
v_mfma_f32_16x16x32_bf16 v[116:119], a[228:231], v[144:147], v[116:119]
v_add_f32 v254, v254, v109
v_fmac_f32 v255, v109, v109
v_mfma_f32_16x16x32_bf16 v[120:123], a[232:235], v[144:147], v[120:123]
global_load_lds_dwordx4 v17, s[42:43] offset:1024 sc1
v_add_f32 v254, v254, v110
v_mfma_f32_16x16x32_bf16 v[124:127], a[236:239], v[144:147], v[124:127]
v_fmac_f32 v255, v110, v110
v_add_f32 v254, v254, v111
v_mfma_f32_16x16x32_bf16 v[128:131], v[80:83], v[144:147], v[128:131]
s_waitcnt lgkmcnt(4)
)"
R"(v_fmac_f32 v255, v111, v111
v_mfma_f32_16x16x32_bf16 v[112:115], a[240:243], v[132:135], v[112:115]
ds_read_b128 v[144:147], v25 offset:18432
v_mfma_f32_16x16x32_bf16 v[116:119], a[244:247], v[132:135], v[116:119]
ds_write_b64 v27, v[254:255] offset:43520
v_mfma_f32_16x16x32_bf16 v[120:123], a[248:251], v[132:135], v[120:123]
s_add_u32 m0, s49, 18432
s_waitcnt lgkmcnt(4)
v_lshlrev_b32 v96, 16, v148
v_and_b32 v97, 0xffff0000, v148
v_mfma_f32_16x16x32_bf16 v[124:127], a[252:255], v[132:135], v[124:127]
v_lshlrev_b32 v98, 16, v149
v_and_b32 v99, 0xffff0000, v149
v_mfma_f32_16x16x32_bf16 v[128:131], v[84:87], v[132:135], v[128:131]
v_lshlrev_b32 v100, 16, v150
v_and_b32 v101, 0xffff0000, v150
v_mfma_f32_16x16x32_bf16 v[112:115], v[28:31], v[136:139], v[112:115]
s_sub_u32 s42, s42, 18432
ds_read_b128 v[132:135], v25 offset:19456
v_lshlrev_b32 v102, 16, v151
v_mfma_f32_16x16x32_bf16 v[116:119], v[32:35], v[136:139], v[116:119]
s_subb_u32 s43, s43, 0
v_and_b32 v103, 0xffff0000, v151
s_waitcnt lgkmcnt(4)
v_lshlrev_b32 v104, 16, v152
v_mfma_f32_16x16x32_bf16 v[120:123], v[36:39], v[136:139], v[120:123]
v_and_b32 v105, 0xffff0000, v152
v_lshlrev_b32 v106, 16, v153
v_mfma_f32_16x16x32_bf16 v[124:127], v[40:43], v[136:139], v[124:127]
v_and_b32 v107, 0xffff0000, v153
v_lshlrev_b32 v108, 16, v154
v_mfma_f32_16x16x32_bf16 v[128:131], v[88:91], v[136:139], v[128:131]
s_waitcnt lgkmcnt(3)
v_and_b32 v109, 0xffff0000, v154
v_lshlrev_b32 v110, 16, v155
v_mfma_f32_16x16x32_bf16 v[112:115], v[44:47], v[140:143], v[112:115]
ds_read_b128 v[136:139], v25 offset:20480
v_and_b32 v111, 0xffff0000, v155
v_mfma_f32_16x16x32_bf16 v[116:119], v[48:51], v[140:143], v[116:119]
v_mfma_f32_16x16x32_bf16 v[120:123], v[52:55], v[140:143], v[120:123]
v_mfma_f32_16x16x32_bf16 v[124:127], v[56:59], v[140:143], v[124:127]
v_mfma_f32_16x16x32_bf16 v[128:131], v[92:95], v[140:143], v[128:131]
s_waitcnt lgkmcnt(3)
v_mfma_f32_16x16x32_bf16 v[112:115], a[0:3], v[144:147], v[112:115]
ds_read_b128 v[140:143], v25 offset:21504
v_mfma_f32_16x16x32_bf16 v[116:119], a[4:7], v[144:147], v[116:119]
v_mfma_f32_16x16x32_bf16 v[120:123], a[8:11], v[144:147], v[120:123]
v_mfma_f32_16x16x32_bf16 v[124:127], a[12:15], v[144:147], v[124:127]
s_waitcnt lgkmcnt(2)
v_mfma_f32_16x16x32_bf16 v[112:115], a[16:19], v[132:135], v[112:115]
ds_read_b128 v[144:147], v25 offset:22528
v_mfma_f32_16x16x32_bf16 v[116:119], a[20:23], v[132:135], v[116:119]
v_sub_u32 v200, v200, v22
v_cmp_lt_u32 vcc, 0x7f, v200
v_mfma_f32_16x16x32_bf16 v[120:123], a[24:27], v[132:135], v[120:123]
v_mfma_f32_16x16x32_bf16 v[124:127], a[28:31], v[132:135], v[124:127]
s_waitcnt lgkmcnt(2)
v_mfma_f32_16x16x32_bf16 v[112:115], a[32:35], v[136:139], v[112:115]
s_and_b64 vcc, vcc, s[76:77]
s_cbranch_vccnz .Lwo_fr11
.Lwo_rt10:
ds_read_b128 v[132:135], v25 offset:23552
v_mfma_f32_16x16x32_bf16 v[116:119], a[36:39], v[136:139], v[116:119]
v_mfma_f32_16x16x32_bf16 v[120:123], a[40:43], v[136:139], v[120:123]
s_barrier
v_mfma_f32_16x16x32_bf16 v[124:127], a[44:47], v[136:139], v[124:127]
s_waitcnt lgkmcnt(2)
ds_write_b128 v26, v[128:131] offset:40960
v_mfma_f32_16x16x32_bf16 v[112:115], a[48:51], v[140:143], v[112:115]
s_mov_b64 s[76:77], -1
ds_read_b128 v[136:139], v25 offset:24576
v_add_u32 v22, 1, v22
v_mfma_f32_16x16x32_bf16 v[116:119], a[52:55], v[140:143], v[116:119]
global_load_lds_dwordx4 v11, s[38:39] offset:-2048 sc1
v_mfma_f32_16x16x32_bf16 v[120:123], a[56:59], v[140:143], v[120:123]
v_mfma_f32_16x16x32_bf16 v[124:127], a[60:63], v[140:143], v[124:127]
s_waitcnt lgkmcnt(3)
v_mfma_f32_16x16x32_bf16 v[112:115], a[64:67], v[144:147], v[112:115]
ds_read_b128 v[140:143], v25 offset:25600
global_load_lds_dwordx4 v11, s[38:39] offset:-1024 sc1
v_mfma_f32_16x16x32_bf16 v[116:119], a[68:71], v[144:147], v[116:119]
v_mfma_f32_16x16x32_bf16 v[120:123], a[72:75], v[144:147], v[120:123]
v_mfma_f32_16x16x32_bf16 v[124:127], a[76:79], v[144:147], v[124:127]
s_waitcnt lgkmcnt(3)
global_load_lds_dwordx4 v11, s[38:39] offset:0 sc1
v_mfma_f32_16x16x32_bf16 v[112:115], a[80:83], v[132:135], v[112:115]
ds_read_b128 v[144:147], v25 offset:26624
v_mfma_f32_16x16x32_bf16 v[116:119], a[84:87], v[132:135], v[116:119]
v_mfma_f32_16x16x32_bf16 v[120:123], a[88:91], v[132:135], v[120:123]
global_load_lds_dwordx4 v11, s[38:39] offset:1024 sc1
v_mfma_f32_16x16x32_bf16 v[124:127], a[92:95], v[132:135], v[124:127]
s_waitcnt lgkmcnt(2)
v_xor_b32 v25, 0x10000, v25
global_load_dword v200, v21, s[38:39] sc1
v_mfma_f32_16x16x32_bf16 v[112:115], a[96:99], v[136:139], v[112:115]
v_mfma_f32_16x16x32_bf16 v[116:119], a[100:103], v[136:139], v[116:119]
v_mfma_f32_16x16x32_bf16 v[120:123], a[104:107], v[136:139], v[120:123]
s_waitcnt vmcnt(9)
s_barrier
v_mfma_f32_16x16x32_bf16 v[124:127], a[108:111], v[136:139], v[124:127]
s_waitcnt lgkmcnt(1)
ds_read_b128 v[132:135], v25 offset:9216
global_load_lds_dwordx4 v11, s[38:39] offset:2048 sc1
v_mfma_f32_16x16x32_bf16 v[112:115], a[112:115], v[140:143], v[112:115]
ds_read_b128 v[136:139], v25 offset:10240
v_add_u32 v11, 18432, v11
v_mfma_f32_16x16x32_bf16 v[116:119], a[116:119], v[140:143], v[116:119]
v_min_u32 v11, v11, v12
v_mfma_f32_16x16x32_bf16 v[120:123], a[120:123], v[140:143], v[120:123]
v_mfma_f32_16x16x32_bf16 v[124:127], a[124:127], v[140:143], v[124:127]
s_waitcnt lgkmcnt(2)
v_mfma_f32_16x16x32_bf16 v[112:115], a[128:131], v[144:147], v[112:115]
ds_read_b128 v[140:143], v25 offset:11264
v_mfma_f32_16x16x32_bf16 v[116:119], a[132:135], v[144:147], v[116:119]
v_mfma_f32_16x16x32_bf16 v[120:123], a[136:139], v[144:147], v[120:123]
v_mfma_f32_16x16x32_bf16 v[124:127], a[140:143], v[144:147], v[124:127]
s_sub_u32 s70, s70, 1
s_cbranch_scc0 .Lwo_top12
s_waitcnt vmcnt(8)
s_barrier
v_cvt_pk_bf16_f32 v164, v112, v113
v_cvt_pk_bf16_f32 v165, v114, v115
v_cvt_pk_bf16_f32 v166, v116, v117
v_cvt_pk_bf16_f32 v167, v118, v119
global_store_dwordx4 v13, v[164:167], s[38:39] offset:0 sc1
v_cvt_pk_bf16_f32 v168, v120, v121
v_cvt_pk_bf16_f32 v169, v122, v123
v_cvt_pk_bf16_f32 v170, v124, v125
v_cvt_pk_bf16_f32 v171, v126, v127
global_store_dwordx4 v13, v[168:171], s[38:39] offset:1024 sc1
v_add_f32 v254, v112, v113
v_mul_f32 v255, v112, v112
v_fmac_f32 v255, v113, v113
v_add_f32 v254, v254, v114
v_fmac_f32 v255, v114, v114
v_add_f32 v254, v254, v115
v_fmac_f32 v255, v115, v115
v_add_f32 v254, v254, v116
v_fmac_f32 v255, v116, v116
v_add_f32 v254, v254, v117
v_fmac_f32 v255, v117, v117
v_add_f32 v254, v254, v118
v_fmac_f32 v255, v118, v118
v_add_f32 v254, v254, v119
v_fmac_f32 v255, v119, v119
v_add_f32 v254, v254, v120
v_fmac_f32 v255, v120, v120
v_add_f32 v254, v254, v121
v_fmac_f32 v255, v121, v121
v_add_f32 v254, v254, v122
v_fmac_f32 v255, v122, v122
v_add_f32 v254, v254, v123
v_fmac_f32 v255, v123, v123
v_add_f32 v254, v254, v124
v_fmac_f32 v255, v124, v124
v_add_f32 v254, v254, v125
v_fmac_f32 v255, v125, v125
v_add_f32 v254, v254, v126
v_fmac_f32 v255, v126, v126
v_add_f32 v254, v254, v127
v_fmac_f32 v255, v127, v127
ds_write_b64 v27, v[254:255] offset:45568
s_waitcnt vmcnt(0) lgkmcnt(0)
s_barrier
s_barrier
s_endpgm
.Lwo_w3:
s_add_u32 m0, s48, 4096
s_nop 0
global_load_lds_dwordx4 v0, s[40:41] offset:-4096
global_load_lds_dwordx4 v1, s[40:41] offset:-3072
global_load_lds_dwordx4 v2, s[40:41] offset:-2048
global_load_lds_dwordx4 v3, s[40:41] offset:-1024
global_load_lds_dwordx4 v4, s[40:41] offset:0
global_load_lds_dwordx4 v5, s[40:41] offset:1024
global_load_lds_dwordx4 v6, s[40:41] offset:2048
global_load_lds_dwordx4 v7, s[40:41] offset:3072
s_add_u32 m0, s48, 12288
s_nop 0
global_load_lds_dwordx4 v8, s[40:41] offset:-4096
s_add_u32 s40, s40, 73728
s_addc_u32 s41, s41, 0
s_add_u32 m0, s48, 40960
s_nop 0
global_load_lds_dwordx4 v0, s[40:41] offset:-4096
global_load_lds_dwordx4 v1, s[40:41] offset:-3072
global_load_lds_dwordx4 v2, s[40:41] offset:-2048
global_load_lds_dwordx4 v3, s[40:41] offset:-1024
global_load_lds_dwordx4 v4, s[40:41] offset:0
global_load_lds_dwordx4 v5, s[40:41] offset:1024
global_load_lds_dwordx4 v6, s[40:41] offset:2048
global_load_lds_dwordx4 v7, s[40:41] offset:3072
s_add_u32 m0, s48, 49152
s_nop 0
global_load_lds_dwordx4 v8, s[40:41] offset:-4096
s_add_u32 s40, s40, 73728
s_addc_u32 s41, s41, 0
s_add_u32 m0, s48, 77824
s_nop 0
global_load_lds_dwordx4 v0, s[40:41] offset:-4096
global_load_lds_dwordx4 v1, s[40:41] offset:-3072
global_load_lds_dwordx4 v2, s[40:41] offset:-2048
global_load_lds_dwordx4 v3, s[40:41] offset:-1024
global_load_lds_dwordx4 v4, s[40:41] offset:0
global_load_lds_dwordx4 v5, s[40:41] offset:1024
global_load_lds_dwordx4 v6, s[40:41] offset:2048
global_load_lds_dwordx4 v7, s[40:41] offset:3072
s_add_u32 m0, s48, 86016
s_nop 0
global_load_lds_dwordx4 v8, s[40:41] offset:-4096
s_add_u32 s40, s40, 73728
s_addc_u32 s41, s41, 0
v_add_u32 v204, 0x10000, v9
v_add_u32 v205, 0x20000, v9
v_add_u32 v206, 0x10000, v10
v_add_u32 v207, 0x20000, v10
s_waitcnt lgkmcnt(0)
s_mul_i32 s37, s37, 0x9E3779B1
s_add_u32 s37, s37, 0x7F4A7C15
v_add_u32 v22, s37, v22
v_add_u32 v24, s37, v24
s_waitcnt vmcnt(18)
s_barrier
s_add_u32 m0, s48, 114688
ds_read_b64_tr_b16 a[0:1], v9 offset:0
ds_read_b64_tr_b16 a[2:3], v9 offset:2304
global_load_lds_dwordx4 v0, s[40:41] offset:-4096
ds_read_b64_tr_b16 a[4:5], v9 offset:8
ds_read_b64_tr_b16 a[6:7], v9 offset:2312
global_load_lds_dwordx4 v1, s[40:41] offset:-3072
ds_read_b64_tr_b16 a[8:9], v9 offset:64
global_load_lds_dwordx4 v2, s[40:41] offset:-2048
ds_read_b64_tr_b16 a[10:11], v9 offset:2368
ds_read_b64_tr_b16 a[12:13], v9 offset:72
global_load_lds_dwordx4 v3, s[40:41] offset:-1024
ds_read_b64_tr_b16 a[14:15], v9 offset:2376
global_load_lds_dwordx4 v4, s[40:41] offset:0
ds_read_b64_tr_b16 v[60:61], v10 offset:0
ds_read_b64_tr_b16 v[62:63], v10 offset:2304
global_load_lds_dwordx4 v5, s[40:41] offset:1024
ds_read_b64_tr_b16 a[16:17], v9 offset:18432
global_load_lds_dwordx4 v6, s[40:41] offset:2048
ds_read_b64_tr_b16 a[18:19], v9 offset:20736
ds_read_b64_tr_b16 a[20:21], v9 offset:18440
global_load_lds_dwordx4 v7, s[40:41] offset:3072
ds_read_b64_tr_b16 a[22:23], v9 offset:20744
s_add_u32 m0, s48, 122880
ds_read_b64_tr_b16 a[24:25], v9 offset:18496
ds_read_b64_tr_b16 a[26:27], v9 offset:20800
global_load_lds_dwordx4 v8, s[40:41] offset:-4096
ds_read_b64_tr_b16 a[28:29], v9 offset:18504
s_add_u32 s40, s40, 73728
ds_read_b64_tr_b16 a[30:31], v9 offset:20808
ds_read_b64_tr_b16 v[64:65], v10 offset:18432
s_addc_u32 s41, s41, 0
ds_read_b64_tr_b16 v[66:67], v10 offset:20736
s_waitcnt vmcnt(18) lgkmcnt(0)
s_barrier
s_add_u32 m0, s48, 4096
ds_read_b64_tr_b16 a[32:33], v9 offset:36864
ds_read_b64_tr_b16 a[34:35], v9 offset:39168
global_load_lds_dwordx4 v0, s[40:41] offset:-4096
ds_read_b64_tr_b16 a[36:37], v9 offset:36872
ds_read_b64_tr_b16 a[38:39], v9 offset:39176
global_load_lds_dwordx4 v1, s[40:41] offset:-3072
ds_read_b64_tr_b16 a[40:41], v9 offset:36928
global_load_lds_dwordx4 v2, s[40:41] offset:-2048
ds_read_b64_tr_b16 a[42:43], v9 offset:39232
ds_read_b64_tr_b16 a[44:45], v9 offset:36936
global_load_lds_dwordx4 v3, s[40:41] offset:-1024
ds_read_b64_tr_b16 a[46:47], v9 offset:39240
global_load_lds_dwordx4 v4, s[40:41] offset:0
ds_read_b64_tr_b16 v[68:69], v10 offset:36864
ds_read_b64_tr_b16 v[70:71], v10 offset:39168
global_load_lds_dwordx4 v5, s[40:41] offset:1024
ds_read_b64_tr_b16 a[48:49], v9 offset:55296
global_load_lds_dwordx4 v6, s[40:41] offset:2048
ds_read_b64_tr_b16 a[50:51], v9 offset:57600
ds_read_b64_tr_b16 a[52:53], v9 offset:55304
global_load_lds_dwordx4 v7, s[40:41] offset:3072
ds_read_b64_tr_b16 a[54:55], v9 offset:57608
s_add_u32 m0, s48, 12288
ds_read_b64_tr_b16 a[56:57], v9 offset:55360
ds_read_b64_tr_b16 a[58:59], v9 offset:57664
global_load_lds_dwordx4 v8, s[40:41] offset:-4096
ds_read_b64_tr_b16 a[60:61], v9 offset:55368
s_add_u32 s40, s40, 73728
ds_read_b64_tr_b16 a[62:63], v9 offset:57672
ds_read_b64_tr_b16 v[72:73], v10 offset:55296
s_addc_u32 s41, s41, 0
ds_read_b64_tr_b16 v[74:75], v10 offset:57600
s_waitcnt vmcnt(18) lgkmcnt(0)
s_barrier
s_add_u32 m0, s48, 40960
ds_read_b64_tr_b16 a[64:65], v204 offset:8192
ds_read_b64_tr_b16 a[66:67], v204 offset:10496
global_load_lds_dwordx4 v0, s[40:41] offset:-4096
ds_read_b64_tr_b16 a[68:69], v204 offset:8200
ds_read_b64_tr_b16 a[70:71], v204 offset:10504
global_load_lds_dwordx4 v1, s[40:41] offset:-3072
ds_read_b64_tr_b16 a[72:73], v204 offset:8256
global_load_lds_dwordx4 v2, s[40:41] offset:-2048
ds_read_b64_tr_b16 a[74:75], v204 offset:10560
ds_read_b64_tr_b16 a[76:77], v204 offset:8264
global_load_lds_dwordx4 v3, s[40:41] offset:-1024
ds_read_b64_tr_b16 a[78:79], v204 offset:10568
global_load_lds_dwordx4 v4, s[40:41] offset:0
ds_read_b64_tr_b16 v[76:77], v206 offset:8192
ds_read_b64_tr_b16 v[78:79], v206 offset:10496
global_load_lds_dwordx4 v5, s[40:41] offset:1024
ds_read_b64_tr_b16 a[80:81], v204 offset:26624
global_load_lds_dwordx4 v6, s[40:41] offset:2048
ds_read_b64_tr_b16 a[82:83], v204 offset:28928
ds_read_b64_tr_b16 a[84:85], v204 offset:26632
global_load_lds_dwordx4 v7, s[40:41] offset:3072
ds_read_b64_tr_b16 a[86:87], v204 offset:28936
s_add_u32 m0, s48, 49152
ds_read_b64_tr_b16 a[88:89], v204 offset:26688
ds_read_b64_tr_b16 a[90:91], v204 offset:28992
global_load_lds_dwordx4 v8, s[40:41] offset:-4096
ds_read_b64_tr_b16 a[92:93], v204 offset:26696
s_add_u32 s40, s40, 73728
ds_read_b64_tr_b16 a[94:95], v204 offset:29000
ds_read_b64_tr_b16 v[80:81], v206 offset:26624
s_addc_u32 s41, s41, 0
ds_read_b64_tr_b16 v[82:83], v206 offset:28928
s_waitcnt vmcnt(18) lgkmcnt(0)
s_barrier
s_add_u32 m0, s48, 77824
ds_read_b64_tr_b16 a[96:97], v204 offset:45056
ds_read_b64_tr_b16 a[98:99], v204 offset:47360
global_load_lds_dwordx4 v0, s[40:41] offset:-4096
ds_read_b64_tr_b16 a[100:101], v204 offset:45064
ds_read_b64_tr_b16 a[102:103], v204 offset:47368
global_load_lds_dwordx4 v1, s[40:41] offset:-3072
ds_read_b64_tr_b16 a[104:105], v204 offset:45120
global_load_lds_dwordx4 v2, s[40:41] offset:-2048
ds_read_b64_tr_b16 a[106:107], v204 offset:47424
ds_read_b64_tr_b16 a[108:109], v204 offset:45128
global_load_lds_dwordx4 v3, s[40:41] offset:-1024
ds_read_b64_tr_b16 a[110:111], v204 offset:47432
global_load_lds_dwordx4 v4, s[40:41] offset:0
ds_read_b64_tr_b16 v[84:85], v206 offset:45056
ds_read_b64_tr_b16 v[86:87], v206 offset:47360
global_load_lds_dwordx4 v5, s[40:41] offset:1024
ds_read_b64_tr_b16 a[112:113], v204 offset:63488
global_load_lds_dwordx4 v6, s[40:41] offset:2048
ds_read_b64_tr_b16 a[114:115], v205 offset:256
ds_read_b64_tr_b16 a[116:117], v204 offset:63496
global_load_lds_dwordx4 v7, s[40:41] offset:3072
ds_read_b64_tr_b16 a[118:119], v205 offset:264
s_add_u32 m0, s48, 86016
ds_read_b64_tr_b16 a[120:121], v204 offset:63552
ds_read_b64_tr_b16 a[122:123], v205 offset:320
global_load_lds_dwordx4 v8, s[40:41] offset:-4096
ds_read_b64_tr_b16 a[124:125], v204 offset:63560
s_add_u32 s40, s40, 73728
ds_read_b64_tr_b16 a[126:127], v205 offset:328
ds_read_b64_tr_b16 v[88:89], v206 offset:63488
s_addc_u32 s41, s41, 0
ds_read_b64_tr_b16 v[90:91], v207 offset:256
s_waitcnt vmcnt(18) lgkmcnt(0)
s_barrier
s_add_u32 m0, s48, 114688
ds_read_b64_tr_b16 a[128:129], v9 offset:0
ds_read_b64_tr_b16 a[130:131], v9 offset:2304
global_load_lds_dwordx4 v0, s[40:41] offset:-4096
ds_read_b64_tr_b16 a[132:133], v9 offset:8
global_load_lds_dwordx4 v1, s[40:41] offset:-3072
ds_read_b64_tr_b16 a[134:135], v9 offset:2312
ds_read_b64_tr_b16 a[136:137], v9 offset:64
global_load_lds_dwordx4 v2, s[40:41] offset:-2048
ds_read_b64_tr_b16 a[138:139], v9 offset:2368
global_load_lds_dwordx4 v3, s[40:41] offset:-1024
ds_read_b64_tr_b16 a[140:141], v9 offset:72
global_load_lds_dwordx4 v4, s[40:41] offset:0
ds_read_b64_tr_b16 a[142:143], v9 offset:2376
ds_read_b64_tr_b16 v[92:93], v10 offset:0
global_load_lds_dwordx4 v5, s[40:41] offset:1024
ds_read_b64_tr_b16 v[94:95], v10 offset:2304
global_load_lds_dwordx4 v6, s[40:41] offset:2048
ds_read_b64_tr_b16 a[144:145], v9 offset:18432
ds_read_b64_tr_b16 a[146:147], v9 offset:20736
global_load_lds_dwordx4 v7, s[40:41] offset:3072
ds_read_b64_tr_b16 a[148:149], v9 offset:18440
s_add_u32 m0, s48, 122880
ds_read_b64_tr_b16 a[150:151], v9 offset:20744
global_load_lds_dwordx4 v8, s[40:41] offset:-4096
ds_read_b64_tr_b16 a[152:153], v9 offset:18496
ds_read_b64_tr_b16 a[154:155], v9 offset:20800
s_add_u32 s40, s40, 73728
ds_read_b64_tr_b16 a[156:157], v9 offset:18504
s_addc_u32 s41, s41, 0
ds_read_b64_tr_b16 a[158:159], v9 offset:20808
s_waitcnt vmcnt(18) lgkmcnt(0)
s_barrier
s_add_u32 m0, s48, 4096
ds_read_b64_tr_b16 a[160:161], v9 offset:36864
ds_read_b64_tr_b16 a[162:163], v9 offset:39168
global_load_lds_dwordx4 v0, s[40:41] offset:-4096
)"
R"(ds_read_b64_tr_b16 a[164:165], v9 offset:36872
global_load_lds_dwordx4 v1, s[40:41] offset:-3072
ds_read_b64_tr_b16 a[166:167], v9 offset:39176
global_load_lds_dwordx4 v2, s[40:41] offset:-2048
ds_read_b64_tr_b16 a[168:169], v9 offset:36928
global_load_lds_dwordx4 v3, s[40:41] offset:-1024
ds_read_b64_tr_b16 a[170:171], v9 offset:39232
ds_read_b64_tr_b16 a[172:173], v9 offset:36936
global_load_lds_dwordx4 v4, s[40:41] offset:0
ds_read_b64_tr_b16 a[174:175], v9 offset:39240
global_load_lds_dwordx4 v5, s[40:41] offset:1024
ds_read_b64_tr_b16 a[176:177], v9 offset:55296
global_load_lds_dwordx4 v6, s[40:41] offset:2048
ds_read_b64_tr_b16 a[178:179], v9 offset:57600
global_load_lds_dwordx4 v7, s[40:41] offset:3072
ds_read_b64_tr_b16 a[180:181], v9 offset:55304
ds_read_b64_tr_b16 a[182:183], v9 offset:57608
s_add_u32 m0, s48, 12288
ds_read_b64_tr_b16 a[184:185], v9 offset:55360
global_load_lds_dwordx4 v8, s[40:41] offset:-4096
ds_read_b64_tr_b16 a[186:187], v9 offset:57664
s_sub_u32 s40, s40, 589824
ds_read_b64_tr_b16 a[188:189], v9 offset:55368
s_subb_u32 s41, s41, 0
ds_read_b64_tr_b16 a[190:191], v9 offset:57672
s_waitcnt vmcnt(18) lgkmcnt(0)
s_barrier
ds_read_b64_tr_b16 a[192:193], v204 offset:8192
ds_read_b64_tr_b16 a[194:195], v204 offset:10496
ds_read_b64_tr_b16 a[196:197], v204 offset:8200
ds_read_b64_tr_b16 a[198:199], v204 offset:10504
ds_read_b64_tr_b16 a[200:201], v204 offset:8256
ds_read_b64_tr_b16 a[202:203], v204 offset:10560
ds_read_b64_tr_b16 a[204:205], v204 offset:8264
ds_read_b64_tr_b16 a[206:207], v204 offset:10568
ds_read_b64_tr_b16 a[208:209], v204 offset:26624
ds_read_b64_tr_b16 a[210:211], v204 offset:28928
ds_read_b64_tr_b16 a[212:213], v204 offset:26632
ds_read_b64_tr_b16 a[214:215], v204 offset:28936
ds_read_b64_tr_b16 a[216:217], v204 offset:26688
ds_read_b64_tr_b16 a[218:219], v204 offset:28992
ds_read_b64_tr_b16 a[220:221], v204 offset:26696
ds_read_b64_tr_b16 a[222:223], v204 offset:29000
s_waitcnt vmcnt(9) lgkmcnt(0)
s_barrier
ds_read_b64_tr_b16 a[224:225], v204 offset:45056
ds_read_b64_tr_b16 a[226:227], v204 offset:47360
ds_read_b64_tr_b16 a[228:229], v204 offset:45064
ds_read_b64_tr_b16 a[230:231], v204 offset:47368
ds_read_b64_tr_b16 a[232:233], v204 offset:45120
ds_read_b64_tr_b16 a[234:235], v204 offset:47424
ds_read_b64_tr_b16 a[236:237], v204 offset:45128
ds_read_b64_tr_b16 a[238:239], v204 offset:47432
ds_read_b64_tr_b16 a[240:241], v204 offset:63488
ds_read_b64_tr_b16 a[242:243], v205 offset:256
ds_read_b64_tr_b16 a[244:245], v204 offset:63496
ds_read_b64_tr_b16 a[246:247], v205 offset:264
ds_read_b64_tr_b16 a[248:249], v204 offset:63552
ds_read_b64_tr_b16 a[250:251], v205 offset:320
ds_read_b64_tr_b16 a[252:253], v204 offset:63560
ds_read_b64_tr_b16 a[254:255], v205 offset:328
s_waitcnt vmcnt(0) lgkmcnt(0)
s_barrier
ds_read_b64_tr_b16 v[28:29], v9 offset:0
ds_read_b64_tr_b16 v[30:31], v9 offset:2304
ds_read_b64_tr_b16 v[32:33], v9 offset:8
ds_read_b64_tr_b16 v[34:35], v9 offset:2312
ds_read_b64_tr_b16 v[36:37], v9 offset:64
ds_read_b64_tr_b16 v[38:39], v9 offset:2368
ds_read_b64_tr_b16 v[40:41], v9 offset:72
ds_read_b64_tr_b16 v[42:43], v9 offset:2376
ds_read_b64_tr_b16 v[44:45], v9 offset:18432
ds_read_b64_tr_b16 v[46:47], v9 offset:20736
ds_read_b64_tr_b16 v[48:49], v9 offset:18440
ds_read_b64_tr_b16 v[50:51], v9 offset:20744
ds_read_b64_tr_b16 v[52:53], v9 offset:18496
ds_read_b64_tr_b16 v[54:55], v9 offset:20800
ds_read_b64_tr_b16 v[56:57], v9 offset:18504
ds_read_b64_tr_b16 v[58:59], v9 offset:20808
s_waitcnt lgkmcnt(0)
s_barrier
s_mov_b64 s[76:77], 0
s_add_u32 m0, s49, 18432
s_mov_b32 s70, 33
s_waitcnt vmcnt(0) lgkmcnt(0)
s_nop 7
.p2align 6
.Lwo_top13:
s_waitcnt lgkmcnt(2)
v_mfma_f32_16x16x32_bf16 v[96:99], a[0:3], v[132:135], v[96:99]
ds_read_b128 v[144:147], v25 offset:3072
v_mfma_f32_16x16x32_bf16 v[100:103], a[4:7], v[132:135], v[100:103]
v_mfma_f32_16x16x32_bf16 v[104:107], a[8:11], v[132:135], v[104:107]
s_waitcnt vmcnt(7)
s_barrier
v_mfma_f32_16x16x32_bf16 v[108:111], a[12:15], v[132:135], v[108:111]
v_cvt_pk_bf16_f32 v164, v112, v113
v_cvt_pk_bf16_f32 v165, v114, v115
v_mfma_f32_16x16x32_bf16 v[128:131], v[60:63], v[132:135], 0
s_waitcnt lgkmcnt(2)
v_cvt_pk_bf16_f32 v166, v116, v117
v_cvt_pk_bf16_f32 v167, v118, v119
s_mov_b64 exec, 65535
global_store_dwordx2 v15, v[176:177], s[38:39] sc1
s_mov_b64 exec, -1
v_mfma_f32_16x16x32_bf16 v[96:99], a[16:19], v[136:139], v[96:99]
ds_read_b128 v[132:135], v25 offset:4096
v_add_f32 v254, v112, v113
v_add_u32 v15, 18688, v15
v_mfma_f32_16x16x32_bf16 v[100:103], a[20:23], v[136:139], v[100:103]
v_mul_f32 v255, v112, v112
v_fmac_f32 v255, v113, v113
v_mfma_f32_16x16x32_bf16 v[104:107], a[24:27], v[136:139], v[104:107]
global_store_dwordx4 v13, v[164:167], s[38:39] offset:0 sc1
v_mfma_f32_16x16x32_bf16 v[108:111], a[28:31], v[136:139], v[108:111]
v_cvt_pk_bf16_f32 v168, v120, v121
v_cvt_pk_bf16_f32 v169, v122, v123
v_mfma_f32_16x16x32_bf16 v[128:131], v[64:67], v[136:139], v[128:131]
s_waitcnt lgkmcnt(2)
v_cvt_pk_bf16_f32 v170, v124, v125
v_cvt_pk_bf16_f32 v171, v126, v127
v_mfma_f32_16x16x32_bf16 v[96:99], a[32:35], v[140:143], v[96:99]
s_cmp_eq_u32 s70, 32
ds_read_b128 v[136:139], v25 offset:5120
v_add_f32 v254, v254, v114
v_mfma_f32_16x16x32_bf16 v[100:103], a[36:39], v[140:143], v[100:103]
s_cselect_b64 s[42:43], s[44:45], s[42:43]
global_store_dwordx4 v13, v[168:171], s[38:39] offset:1024 sc1
v_mfma_f32_16x16x32_bf16 v[104:107], a[40:43], v[140:143], v[104:107]
s_cmp_eq_u32 s70, 16
v_add_u32 v13, 18688, v13
v_fmac_f32 v255, v114, v114
v_mfma_f32_16x16x32_bf16 v[108:111], a[44:47], v[140:143], v[108:111]
s_cselect_b64 s[42:43], s[46:47], s[42:43]
v_add_f32 v254, v254, v115
v_fmac_f32 v255, v115, v115
v_mfma_f32_16x16x32_bf16 v[128:131], v[68:71], v[140:143], v[128:131]
s_waitcnt lgkmcnt(2)
v_add_f32 v254, v254, v116
v_fmac_f32 v255, v116, v116
v_mfma_f32_16x16x32_bf16 v[96:99], a[48:51], v[144:147], v[96:99]
s_xor_b32 s49, s49, 0x10000
ds_read_b128 v[140:143], v25 offset:6144
v_add_f32 v254, v254, v117
v_mfma_f32_16x16x32_bf16 v[100:103], a[52:55], v[144:147], v[100:103]
s_mov_b32 m0, 51200
v_fmac_f32 v255, v117, v117
v_add_f32 v254, v254, v118
v_mfma_f32_16x16x32_bf16 v[104:107], a[56:59], v[144:147], v[104:107]
v_fmac_f32 v255, v118, v118
v_add_f32 v254, v254, v119
v_mfma_f32_16x16x32_bf16 v[108:111], a[60:63], v[144:147], v[108:111]
v_fmac_f32 v255, v119, v119
v_add_f32 v254, v254, v120
v_mfma_f32_16x16x32_bf16 v[128:131], v[72:75], v[144:147], v[128:131]
s_waitcnt lgkmcnt(2)
v_fmac_f32 v255, v120, v120
v_add_f32 v254, v254, v121
v_mfma_f32_16x16x32_bf16 v[96:99], a[64:67], v[132:135], v[96:99]
ds_read_b128 v[144:147], v25 offset:7168
v_fmac_f32 v255, v121, v121
v_mfma_f32_16x16x32_bf16 v[100:103], a[68:71], v[132:135], v[100:103]
v_add_f32 v254, v254, v122
v_fmac_f32 v255, v122, v122
v_mfma_f32_16x16x32_bf16 v[104:107], a[72:75], v[132:135], v[104:107]
global_load_lds_dwordx4 v16, s[42:43] offset:0 sc1
v_add_f32 v254, v254, v123
v_mfma_f32_16x16x32_bf16 v[108:111], a[76:79], v[132:135], v[108:111]
v_fmac_f32 v255, v123, v123
v_add_f32 v254, v254, v124
v_mfma_f32_16x16x32_bf16 v[128:131], v[76:79], v[132:135], v[128:131]
s_waitcnt vmcnt(9)
ds_read_b128 v[148:151], v19 offset:8192
ds_read_b128 v[152:155], v20 offset:8192
s_waitcnt lgkmcnt(4)
v_mfma_f32_16x16x32_bf16 v[96:99], a[80:83], v[136:139], v[96:99]
ds_read_b128 v[132:135], v25 offset:8192
v_fmac_f32 v255, v124, v124
v_mfma_f32_16x16x32_bf16 v[100:103], a[84:87], v[136:139], v[100:103]
v_add_f32 v254, v254, v125
v_fmac_f32 v255, v125, v125
v_mfma_f32_16x16x32_bf16 v[104:107], a[88:91], v[136:139], v[104:107]
v_add_f32 v254, v254, v126
v_fmac_f32 v255, v126, v126
v_mfma_f32_16x16x32_bf16 v[108:111], a[92:95], v[136:139], v[108:111]
v_add_f32 v254, v254, v127
v_fmac_f32 v255, v127, v127
v_mfma_f32_16x16x32_bf16 v[128:131], v[80:83], v[136:139], v[128:131]
s_waitcnt lgkmcnt(4)
global_load_lds_dwordx4 v17, s[42:43] offset:1024 sc1
v_mfma_f32_16x16x32_bf16 v[96:99], a[96:99], v[140:143], v[96:99]
ds_read_b128 v[136:139], v25 offset:9216
v_mfma_f32_16x16x32_bf16 v[100:103], a[100:103], v[140:143], v[100:103]
ds_write_b64 v27, v[254:255] offset:46080
v_mfma_f32_16x16x32_bf16 v[104:107], a[104:107], v[140:143], v[104:107]
s_waitcnt lgkmcnt(4)
v_lshlrev_b32 v112, 16, v148
v_and_b32 v113, 0xffff0000, v148
v_mfma_f32_16x16x32_bf16 v[108:111], a[108:111], v[140:143], v[108:111]
v_lshlrev_b32 v114, 16, v149
v_and_b32 v115, 0xffff0000, v149
v_mfma_f32_16x16x32_bf16 v[128:131], v[84:87], v[140:143], v[128:131]
v_lshlrev_b32 v116, 16, v150
v_and_b32 v117, 0xffff0000, v150
v_mfma_f32_16x16x32_bf16 v[96:99], a[112:115], v[144:147], v[96:99]
s_add_u32 m0, s49, 0
ds_read_b128 v[140:143], v25 offset:10240
v_lshlrev_b32 v118, 16, v151
v_mfma_f32_16x16x32_bf16 v[100:103], a[116:119], v[144:147], v[100:103]
s_waitcnt lgkmcnt(4)
v_and_b32 v119, 0xffff0000, v151
v_lshlrev_b32 v120, 16, v152
v_mfma_f32_16x16x32_bf16 v[104:107], a[120:123], v[144:147], v[104:107]
s_sub_u32 s42, s42, 18432
v_and_b32 v121, 0xffff0000, v152
v_lshlrev_b32 v122, 16, v153
v_mfma_f32_16x16x32_bf16 v[108:111], a[124:127], v[144:147], v[108:111]
s_subb_u32 s43, s43, 0
v_and_b32 v123, 0xffff0000, v153
v_lshlrev_b32 v124, 16, v154
v_mfma_f32_16x16x32_bf16 v[128:131], v[88:91], v[144:147], v[128:131]
s_waitcnt lgkmcnt(3)
v_and_b32 v125, 0xffff0000, v154
v_lshlrev_b32 v126, 16, v155
v_mfma_f32_16x16x32_bf16 v[96:99], a[128:131], v[132:135], v[96:99]
ds_read_b128 v[144:147], v25 offset:11264
v_and_b32 v127, 0xffff0000, v155
v_mfma_f32_16x16x32_bf16 v[100:103], a[132:135], v[132:135], v[100:103]
v_mfma_f32_16x16x32_bf16 v[104:107], a[136:139], v[132:135], v[104:107]
v_mfma_f32_16x16x32_bf16 v[108:111], a[140:143], v[132:135], v[108:111]
v_mfma_f32_16x16x32_bf16 v[128:131], v[92:95], v[132:135], v[128:131]
s_waitcnt lgkmcnt(3)
ds_read_b64 v[176:177], v27 offset:43008
ds_read_b64 v[180:181], v27 offset:43520
v_mfma_f32_16x16x32_bf16 v[96:99], a[144:147], v[136:139], v[96:99]
ds_read_b128 v[132:135], v25 offset:12288
ds_read_b64 v[184:185], v27 offset:44032
v_mfma_f32_16x16x32_bf16 v[100:103], a[148:151], v[136:139], v[100:103]
ds_read_b64 v[188:189], v27 offset:44544
v_mfma_f32_16x16x32_bf16 v[104:107], a[152:155], v[136:139], v[104:107]
v_mfma_f32_16x16x32_bf16 v[108:111], a[156:159], v[136:139], v[108:111]
s_waitcnt lgkmcnt(6)
v_mfma_f32_16x16x32_bf16 v[96:99], a[160:163], v[140:143], v[96:99]
ds_read_b128 v[136:139], v25 offset:13312
v_mfma_f32_16x16x32_bf16 v[100:103], a[164:167], v[140:143], v[100:103]
v_mfma_f32_16x16x32_bf16 v[104:107], a[168:171], v[140:143], v[104:107]
v_mfma_f32_16x16x32_bf16 v[108:111], a[172:175], v[140:143], v[108:111]
s_waitcnt lgkmcnt(4)
v_add_f32 v176, v176, v180
v_add_f32 v177, v177, v181
v_mfma_f32_16x16x32_bf16 v[96:99], a[176:179], v[144:147], v[96:99]
s_waitcnt lgkmcnt(1)
ds_read_b128 v[140:143], v25 offset:14336
v_add_f32 v184, v184, v188
v_mfma_f32_16x16x32_bf16 v[100:103], a[180:183], v[144:147], v[100:103]
v_mfma_f32_16x16x32_bf16 v[104:107], a[184:187], v[144:147], v[104:107]
s_barrier
v_mfma_f32_16x16x32_bf16 v[108:111], a[188:191], v[144:147], v[108:111]
global_load_lds_dwordx4 v11, s[38:39] offset:-2048 sc1
v_add_f32 v185, v185, v189
v_mfma_f32_16x16x32_bf16 v[96:99], a[192:195], v[132:135], v[96:99]
ds_read_b128 v[144:147], v25 offset:15360
v_add_f32 v176, v176, v184
v_mfma_f32_16x16x32_bf16 v[100:103], a[196:199], v[132:135], v[100:103]
v_add_f32 v177, v177, v185
v_mov_b32 v202, v176
v_mfma_f32_16x16x32_bf16 v[104:107], a[200:203], v[132:135], v[104:107]
ds_write_b128 v26, v[128:131] offset:38912
v_mfma_f32_16x16x32_bf16 v[108:111], a[204:207], v[132:135], v[108:111]
s_waitcnt lgkmcnt(3)
global_load_lds_dwordx4 v11, s[38:39] offset:-1024 sc1
v_mov_b32 v203, v177
v_mfma_f32_16x16x32_bf16 v[96:99], a[208:211], v[136:139], v[96:99]
ds_read_b128 v[132:135], v25 offset:16384
v_mfma_f32_16x16x32_bf16 v[100:103], a[212:215], v[136:139], v[100:103]
v_permlane16_swap_b32 v176, v202
v_mfma_f32_16x16x32_bf16 v[104:107], a[216:219], v[136:139], v[104:107]
global_load_lds_dwordx4 v11, s[38:39] offset:0 sc1
v_mfma_f32_16x16x32_bf16 v[108:111], a[220:223], v[136:139], v[108:111]
s_waitcnt lgkmcnt(3)
v_permlane16_swap_b32 v177, v203
v_mfma_f32_16x16x32_bf16 v[96:99], a[224:227], v[140:143], v[96:99]
ds_read_b128 v[136:139], v25 offset:17408
v_add_f32 v176, v176, v202
v_mfma_f32_16x16x32_bf16 v[100:103], a[228:231], v[140:143], v[100:103]
global_load_lds_dwordx4 v11, s[38:39] offset:1024 sc1
v_add_f32 v177, v177, v203
v_mfma_f32_16x16x32_bf16 v[104:107], a[232:235], v[140:143], v[104:107]
v_mov_b32 v202, v176
v_mov_b32 v203, v177
v_mfma_f32_16x16x32_bf16 v[108:111], a[236:239], v[140:143], v[108:111]
s_waitcnt lgkmcnt(3)
v_permlane32_swap_b32 v176, v202
v_mfma_f32_16x16x32_bf16 v[96:99], a[240:243], v[144:147], v[96:99]
global_load_lds_dwordx4 v11, s[38:39] offset:2048 sc1
v_add_u32 v11, 18432, v11
v_mfma_f32_16x16x32_bf16 v[100:103], a[244:247], v[144:147], v[100:103]
v_mfma_f32_16x16x32_bf16 v[104:107], a[248:251], v[144:147], v[104:107]
s_waitcnt vmcnt(10) lgkmcnt(2)
s_barrier
v_mfma_f32_16x16x32_bf16 v[108:111], a[252:255], v[144:147], v[108:111]
s_waitcnt lgkmcnt(1)
ds_read_b128 v[140:143], v25 offset:18432
v_min_u32 v11, v11, v12
v_mfma_f32_16x16x32_bf16 v[96:99], v[28:31], v[132:135], v[96:99]
ds_read_b128 v[144:147], v25 offset:19456
v_mfma_f32_16x16x32_bf16 v[100:103], v[32:35], v[132:135], v[100:103]
v_permlane32_swap_b32 v177, v203
v_mfma_f32_16x16x32_bf16 v[104:107], v[36:39], v[132:135], v[104:107]
v_add_f32 v176, v176, v202
v_add_f32 v177, v177, v203
v_mfma_f32_16x16x32_bf16 v[108:111], v[40:43], v[132:135], v[108:111]
s_waitcnt lgkmcnt(2)
v_mfma_f32_16x16x32_bf16 v[96:99], v[44:47], v[136:139], v[96:99]
ds_read_b128 v[132:135], v25 offset:20480
v_mfma_f32_16x16x32_bf16 v[100:103], v[48:51], v[136:139], v[100:103]
v_mfma_f32_16x16x32_bf16 v[104:107], v[52:55], v[136:139], v[104:107]
v_mfma_f32_16x16x32_bf16 v[108:111], v[56:59], v[136:139], v[108:111]
s_waitcnt lgkmcnt(2)
v_mfma_f32_16x16x32_bf16 v[112:115], a[0:3], v[140:143], v[112:115]
ds_read_b128 v[136:139], v25 offset:21504
v_mfma_f32_16x16x32_bf16 v[116:119], a[4:7], v[140:143], v[116:119]
v_mfma_f32_16x16x32_bf16 v[120:123], a[8:11], v[140:143], v[120:123]
s_waitcnt vmcnt(7)
s_barrier
v_mfma_f32_16x16x32_bf16 v[124:127], a[12:15], v[140:143], v[124:127]
v_cvt_pk_bf16_f32 v164, v96, v97
v_cvt_pk_bf16_f32 v165, v98, v99
v_mfma_f32_16x16x32_bf16 v[128:131], v[60:63], v[140:143], 0
s_waitcnt lgkmcnt(2)
v_cvt_pk_bf16_f32 v166, v100, v101
v_cvt_pk_bf16_f32 v167, v102, v103
s_mov_b64 exec, 65535
global_store_dwordx2 v15, v[176:177], s[38:39] sc1
s_mov_b64 exec, -1
v_mfma_f32_16x16x32_bf16 v[112:115], a[16:19], v[144:147], v[112:115]
ds_read_b128 v[140:143], v25 offset:22528
v_add_u32 v15, 18688, v15
v_mfma_f32_16x16x32_bf16 v[116:119], a[20:23], v[144:147], v[116:119]
v_add_f32 v254, v96, v97
v_mul_f32 v255, v96, v96
v_mfma_f32_16x16x32_bf16 v[120:123], a[24:27], v[144:147], v[120:123]
global_store_dwordx4 v13, v[164:167], s[38:39] offset:0 sc1
v_mfma_f32_16x16x32_bf16 v[124:127], a[28:31], v[144:147], v[124:127]
v_cvt_pk_bf16_f32 v168, v104, v105
v_cvt_pk_bf16_f32 v169, v106, v107
v_mfma_f32_16x16x32_bf16 v[128:131], v[64:67], v[144:147], v[128:131]
s_waitcnt lgkmcnt(2)
v_cvt_pk_bf16_f32 v170, v108, v109
v_cvt_pk_bf16_f32 v171, v110, v111
v_mfma_f32_16x16x32_bf16 v[112:115], a[32:35], v[132:135], v[112:115]
s_mov_b32 m0, 59392
ds_read_b128 v[144:147], v25 offset:23552
v_fmac_f32 v255, v97, v97
v_mfma_f32_16x16x32_bf16 v[116:119], a[36:39], v[132:135], v[116:119]
global_store_dwordx4 v13, v[168:171], s[38:39] offset:1024 sc1
v_mfma_f32_16x16x32_bf16 v[120:123], a[40:43], v[132:135], v[120:123]
v_add_u32 v13, 18688, v13
v_add_f32 v254, v254, v98
v_mfma_f32_16x16x32_bf16 v[124:127], a[44:47], v[132:135], v[124:127]
v_fmac_f32 v255, v98, v98
v_add_f32 v254, v254, v99
v_mfma_f32_16x16x32_bf16 v[128:131], v[68:71], v[132:135], v[128:131]
s_waitcnt lgkmcnt(2)
v_fmac_f32 v255, v99, v99
v_add_f32 v254, v254, v100
v_mfma_f32_16x16x32_bf16 v[112:115], a[48:51], v[136:139], v[112:115]
ds_read_b128 v[132:135], v25 offset:24576
v_fmac_f32 v255, v100, v100
v_mfma_f32_16x16x32_bf16 v[116:119], a[52:55], v[136:139], v[116:119]
v_add_f32 v254, v254, v101
v_fmac_f32 v255, v101, v101
v_mfma_f32_16x16x32_bf16 v[120:123], a[56:59], v[136:139], v[120:123]
v_add_f32 v254, v254, v102
v_fmac_f32 v255, v102, v102
v_mfma_f32_16x16x32_bf16 v[124:127], a[60:63], v[136:139], v[124:127]
v_add_f32 v254, v254, v103
v_fmac_f32 v255, v103, v103
v_mfma_f32_16x16x32_bf16 v[128:131], v[72:75], v[136:139], v[128:131]
s_waitcnt lgkmcnt(2)
v_add_f32 v254, v254, v104
v_fmac_f32 v255, v104, v104
v_mfma_f32_16x16x32_bf16 v[112:115], a[64:67], v[140:143], v[112:115]
ds_read_b128 v[136:139], v25 offset:25600
v_add_f32 v254, v254, v105
v_mfma_f32_16x16x32_bf16 v[116:119], a[68:71], v[140:143], v[116:119]
v_fmac_f32 v255, v105, v105
v_add_f32 v254, v254, v106
v_mfma_f32_16x16x32_bf16 v[120:123], a[72:75], v[140:143], v[120:123]
global_load_lds_dwordx4 v16, s[42:43] offset:0 sc1
v_fmac_f32 v255, v106, v106
v_mfma_f32_16x16x32_bf16 v[124:127], a[76:79], v[140:143], v[124:127]
v_add_f32 v254, v254, v107
v_fmac_f32 v255, v107, v107
v_mfma_f32_16x16x32_bf16 v[128:131], v[76:79], v[140:143], v[128:131]
s_waitcnt vmcnt(9)
ds_read_b128 v[148:151], v19 offset:0
ds_read_b128 v[152:155], v20 offset:0
s_waitcnt lgkmcnt(4)
v_mfma_f32_16x16x32_bf16 v[112:115], a[80:83], v[144:147], v[112:115]
)"
R"(ds_read_b128 v[140:143], v25 offset:26624
v_add_f32 v254, v254, v108
v_mfma_f32_16x16x32_bf16 v[116:119], a[84:87], v[144:147], v[116:119]
v_fmac_f32 v255, v108, v108
v_add_f32 v254, v254, v109
v_mfma_f32_16x16x32_bf16 v[120:123], a[88:91], v[144:147], v[120:123]
v_fmac_f32 v255, v109, v109
v_add_f32 v254, v254, v110
v_mfma_f32_16x16x32_bf16 v[124:127], a[92:95], v[144:147], v[124:127]
v_fmac_f32 v255, v110, v110
v_add_f32 v254, v254, v111
v_mfma_f32_16x16x32_bf16 v[128:131], v[80:83], v[144:147], v[128:131]
s_waitcnt lgkmcnt(4)
global_load_lds_dwordx4 v17, s[42:43] offset:1024 sc1
v_fmac_f32 v255, v111, v111
v_mfma_f32_16x16x32_bf16 v[112:115], a[96:99], v[132:135], v[112:115]
ds_read_b128 v[144:147], v25 offset:27648
v_mfma_f32_16x16x32_bf16 v[116:119], a[100:103], v[132:135], v[116:119]
ds_write_b64 v27, v[254:255] offset:44032
v_mfma_f32_16x16x32_bf16 v[120:123], a[104:107], v[132:135], v[120:123]
s_waitcnt lgkmcnt(4)
v_lshlrev_b32 v96, 16, v148
v_and_b32 v97, 0xffff0000, v148
v_mfma_f32_16x16x32_bf16 v[124:127], a[108:111], v[132:135], v[124:127]
v_lshlrev_b32 v98, 16, v149
v_and_b32 v99, 0xffff0000, v149
v_mfma_f32_16x16x32_bf16 v[128:131], v[84:87], v[132:135], v[128:131]
v_lshlrev_b32 v100, 16, v150
v_and_b32 v101, 0xffff0000, v150
v_mfma_f32_16x16x32_bf16 v[112:115], a[112:115], v[136:139], v[112:115]
s_add_u32 m0, s49, 18432
ds_read_b128 v[132:135], v25 offset:28672
v_lshlrev_b32 v102, 16, v151
v_mfma_f32_16x16x32_bf16 v[116:119], a[116:119], v[136:139], v[116:119]
s_waitcnt lgkmcnt(4)
v_and_b32 v103, 0xffff0000, v151
v_lshlrev_b32 v104, 16, v152
v_mfma_f32_16x16x32_bf16 v[120:123], a[120:123], v[136:139], v[120:123]
s_sub_u32 s42, s42, 18432
v_and_b32 v105, 0xffff0000, v152
v_lshlrev_b32 v106, 16, v153
v_mfma_f32_16x16x32_bf16 v[124:127], a[124:127], v[136:139], v[124:127]
s_subb_u32 s43, s43, 0
v_and_b32 v107, 0xffff0000, v153
v_lshlrev_b32 v108, 16, v154
v_mfma_f32_16x16x32_bf16 v[128:131], v[88:91], v[136:139], v[128:131]
s_waitcnt lgkmcnt(3)
v_and_b32 v109, 0xffff0000, v154
v_lshlrev_b32 v110, 16, v155
v_mfma_f32_16x16x32_bf16 v[112:115], a[128:131], v[140:143], v[112:115]
ds_read_b128 v[136:139], v25 offset:29696
v_and_b32 v111, 0xffff0000, v155
v_mfma_f32_16x16x32_bf16 v[116:119], a[132:135], v[140:143], v[116:119]
v_mfma_f32_16x16x32_bf16 v[120:123], a[136:139], v[140:143], v[120:123]
v_mfma_f32_16x16x32_bf16 v[124:127], a[140:143], v[140:143], v[124:127]
v_mfma_f32_16x16x32_bf16 v[128:131], v[92:95], v[140:143], v[128:131]
s_waitcnt lgkmcnt(3)
ds_read_b64 v[176:177], v27 offset:45056
ds_read_b64 v[180:181], v27 offset:45568
v_mfma_f32_16x16x32_bf16 v[112:115], a[144:147], v[144:147], v[112:115]
ds_read_b128 v[140:143], v25 offset:30720
ds_read_b64 v[184:185], v27 offset:46080
v_mfma_f32_16x16x32_bf16 v[116:119], a[148:151], v[144:147], v[116:119]
ds_read_b64 v[188:189], v27 offset:46592
v_mfma_f32_16x16x32_bf16 v[120:123], a[152:155], v[144:147], v[120:123]
v_mfma_f32_16x16x32_bf16 v[124:127], a[156:159], v[144:147], v[124:127]
s_waitcnt lgkmcnt(6)
v_mfma_f32_16x16x32_bf16 v[112:115], a[160:163], v[132:135], v[112:115]
ds_read_b128 v[144:147], v25 offset:31744
v_mfma_f32_16x16x32_bf16 v[116:119], a[164:167], v[132:135], v[116:119]
v_mfma_f32_16x16x32_bf16 v[120:123], a[168:171], v[132:135], v[120:123]
v_mfma_f32_16x16x32_bf16 v[124:127], a[172:175], v[132:135], v[124:127]
s_waitcnt lgkmcnt(4)
v_add_f32 v176, v176, v180
v_add_f32 v177, v177, v181
v_mfma_f32_16x16x32_bf16 v[112:115], a[176:179], v[136:139], v[112:115]
s_waitcnt lgkmcnt(1)
ds_read_b128 v[132:135], v25 offset:32768
v_add_f32 v184, v184, v188
v_mfma_f32_16x16x32_bf16 v[116:119], a[180:183], v[136:139], v[116:119]
v_mfma_f32_16x16x32_bf16 v[120:123], a[184:187], v[136:139], v[120:123]
s_barrier
v_mfma_f32_16x16x32_bf16 v[124:127], a[188:191], v[136:139], v[124:127]
global_load_lds_dwordx4 v11, s[38:39] offset:-2048 sc1
v_add_f32 v185, v185, v189
v_mfma_f32_16x16x32_bf16 v[112:115], a[192:195], v[140:143], v[112:115]
s_mov_b64 s[76:77], -1
ds_read_b128 v[136:139], v25 offset:33792
v_add_f32 v176, v176, v184
v_mfma_f32_16x16x32_bf16 v[116:119], a[196:199], v[140:143], v[116:119]
v_add_f32 v177, v177, v185
v_mov_b32 v202, v176
v_mfma_f32_16x16x32_bf16 v[120:123], a[200:203], v[140:143], v[120:123]
ds_write_b128 v26, v[128:131] offset:41984
v_mfma_f32_16x16x32_bf16 v[124:127], a[204:207], v[140:143], v[124:127]
s_waitcnt lgkmcnt(3)
global_load_lds_dwordx4 v11, s[38:39] offset:-1024 sc1
v_mov_b32 v203, v177
v_mfma_f32_16x16x32_bf16 v[112:115], a[208:211], v[144:147], v[112:115]
ds_read_b128 v[140:143], v25 offset:34816
v_mfma_f32_16x16x32_bf16 v[116:119], a[212:215], v[144:147], v[116:119]
v_permlane16_swap_b32 v176, v202
v_mfma_f32_16x16x32_bf16 v[120:123], a[216:219], v[144:147], v[120:123]
global_load_lds_dwordx4 v11, s[38:39] offset:0 sc1
v_mfma_f32_16x16x32_bf16 v[124:127], a[220:223], v[144:147], v[124:127]
s_waitcnt lgkmcnt(3)
v_permlane16_swap_b32 v177, v203
v_mfma_f32_16x16x32_bf16 v[112:115], a[224:227], v[132:135], v[112:115]
ds_read_b128 v[144:147], v25 offset:35840
v_add_f32 v176, v176, v202
v_mfma_f32_16x16x32_bf16 v[116:119], a[228:231], v[132:135], v[116:119]
global_load_lds_dwordx4 v11, s[38:39] offset:1024 sc1
v_add_f32 v177, v177, v203
v_mfma_f32_16x16x32_bf16 v[120:123], a[232:235], v[132:135], v[120:123]
v_mov_b32 v202, v176
v_mov_b32 v203, v177
v_mfma_f32_16x16x32_bf16 v[124:127], a[236:239], v[132:135], v[124:127]
s_waitcnt lgkmcnt(3)
v_xor_b32 v25, 0x10000, v25
v_mfma_f32_16x16x32_bf16 v[112:115], a[240:243], v[136:139], v[112:115]
global_load_lds_dwordx4 v11, s[38:39] offset:2048 sc1
v_add_u32 v11, 18432, v11
v_mfma_f32_16x16x32_bf16 v[116:119], a[244:247], v[136:139], v[116:119]
v_mfma_f32_16x16x32_bf16 v[120:123], a[248:251], v[136:139], v[120:123]
s_waitcnt vmcnt(10) lgkmcnt(2)
s_barrier
v_mfma_f32_16x16x32_bf16 v[124:127], a[252:255], v[136:139], v[124:127]
s_waitcnt lgkmcnt(1)
ds_read_b128 v[132:135], v25 offset:0
v_min_u32 v11, v11, v12
v_mfma_f32_16x16x32_bf16 v[112:115], v[28:31], v[140:143], v[112:115]
ds_read_b128 v[136:139], v25 offset:1024
v_mfma_f32_16x16x32_bf16 v[116:119], v[32:35], v[140:143], v[116:119]
v_permlane32_swap_b32 v176, v202
v_mfma_f32_16x16x32_bf16 v[120:123], v[36:39], v[140:143], v[120:123]
v_permlane32_swap_b32 v177, v203
v_mfma_f32_16x16x32_bf16 v[124:127], v[40:43], v[140:143], v[124:127]
s_waitcnt lgkmcnt(2)
v_add_f32 v176, v176, v202
v_add_f32 v177, v177, v203
v_mfma_f32_16x16x32_bf16 v[112:115], v[44:47], v[144:147], v[112:115]
ds_read_b128 v[140:143], v25 offset:2048
v_mfma_f32_16x16x32_bf16 v[116:119], v[48:51], v[144:147], v[116:119]
v_mfma_f32_16x16x32_bf16 v[120:123], v[52:55], v[144:147], v[120:123]
v_mfma_f32_16x16x32_bf16 v[124:127], v[56:59], v[144:147], v[124:127]
s_sub_u32 s70, s70, 1
s_cbranch_scc0 .Lwo_top13
s_waitcnt vmcnt(7)
s_barrier
s_mov_b64 exec, 65535
global_store_dwordx2 v15, v[176:177], s[38:39] sc1
s_mov_b64 exec, -1
v_add_u32 v15, 18688, v15
v_cvt_pk_bf16_f32 v164, v112, v113
v_cvt_pk_bf16_f32 v165, v114, v115
v_cvt_pk_bf16_f32 v166, v116, v117
v_cvt_pk_bf16_f32 v167, v118, v119
global_store_dwordx4 v13, v[164:167], s[38:39] offset:0 sc1
v_cvt_pk_bf16_f32 v168, v120, v121
v_cvt_pk_bf16_f32 v169, v122, v123
v_cvt_pk_bf16_f32 v170, v124, v125
v_cvt_pk_bf16_f32 v171, v126, v127
global_store_dwordx4 v13, v[168:171], s[38:39] offset:1024 sc1
v_add_f32 v254, v112, v113
v_mul_f32 v255, v112, v112
v_fmac_f32 v255, v113, v113
v_add_f32 v254, v254, v114
v_fmac_f32 v255, v114, v114
v_add_f32 v254, v254, v115
v_fmac_f32 v255, v115, v115
v_add_f32 v254, v254, v116
v_fmac_f32 v255, v116, v116
v_add_f32 v254, v254, v117
v_fmac_f32 v255, v117, v117
v_add_f32 v254, v254, v118
v_fmac_f32 v255, v118, v118
v_add_f32 v254, v254, v119
v_fmac_f32 v255, v119, v119
v_add_f32 v254, v254, v120
v_fmac_f32 v255, v120, v120
v_add_f32 v254, v254, v121
v_fmac_f32 v255, v121, v121
v_add_f32 v254, v254, v122
v_fmac_f32 v255, v122, v122
v_add_f32 v254, v254, v123
v_fmac_f32 v255, v123, v123
v_add_f32 v254, v254, v124
v_fmac_f32 v255, v124, v124
v_add_f32 v254, v254, v125
v_fmac_f32 v255, v125, v125
v_add_f32 v254, v254, v126
v_fmac_f32 v255, v126, v126
v_add_f32 v254, v254, v127
v_fmac_f32 v255, v127, v127
ds_write_b64 v27, v[254:255] offset:46080
s_waitcnt vmcnt(0) lgkmcnt(0)
s_barrier
ds_read_b64 v[176:177], v27 offset:43008
ds_read_b64 v[180:181], v27 offset:43520
ds_read_b64 v[184:185], v27 offset:44032
ds_read_b64 v[188:189], v27 offset:44544
s_waitcnt lgkmcnt(2)
v_add_f32 v176, v176, v180
v_add_f32 v177, v177, v181
s_waitcnt lgkmcnt(0)
v_add_f32 v184, v184, v188
v_add_f32 v185, v185, v189
v_add_f32 v176, v176, v184
v_add_f32 v177, v177, v185
v_mov_b32 v202, v176
v_mov_b32 v203, v177
s_nop 0
v_permlane16_swap_b32 v176, v202
v_permlane16_swap_b32 v177, v203
v_add_f32 v176, v176, v202
v_add_f32 v177, v177, v203
v_mov_b32 v202, v176
v_mov_b32 v203, v177
s_nop 0
v_permlane32_swap_b32 v176, v202
v_permlane32_swap_b32 v177, v203
v_add_f32 v176, v176, v202
v_add_f32 v177, v177, v203
s_mov_b64 exec, 65535
global_store_dwordx2 v15, v[176:177], s[38:39] sc1
s_mov_b64 exec, -1
v_add_u32 v15, 18688, v15
ds_read_b64 v[176:177], v27 offset:45056
ds_read_b64 v[180:181], v27 offset:45568
ds_read_b64 v[184:185], v27 offset:46080
ds_read_b64 v[188:189], v27 offset:46592
s_waitcnt lgkmcnt(2)
v_add_f32 v176, v176, v180
v_add_f32 v177, v177, v181
s_waitcnt lgkmcnt(0)
v_add_f32 v184, v184, v188
v_add_f32 v185, v185, v189
v_add_f32 v176, v176, v184
v_add_f32 v177, v177, v185
v_mov_b32 v202, v176
v_mov_b32 v203, v177
s_nop 0
v_permlane16_swap_b32 v176, v202
v_permlane16_swap_b32 v177, v203
v_add_f32 v176, v176, v202
v_add_f32 v177, v177, v203
v_mov_b32 v202, v176
v_mov_b32 v203, v177
s_nop 0
v_permlane32_swap_b32 v176, v202
v_permlane32_swap_b32 v177, v203
v_add_f32 v176, v176, v202
v_add_f32 v177, v177, v203
s_mov_b64 exec, 65535
global_store_dwordx2 v15, v[176:177], s[38:39] sc1
s_mov_b64 exec, -1
v_add_u32 v15, 18688, v15
s_waitcnt vmcnt(0)
s_barrier
s_endpgm
.Lwo_w4:
s_add_u32 m0, s48, 4096
s_nop 0
global_load_lds_dwordx4 v0, s[40:41] offset:-4096
global_load_lds_dwordx4 v1, s[40:41] offset:-3072
global_load_lds_dwordx4 v2, s[40:41] offset:-2048
global_load_lds_dwordx4 v3, s[40:41] offset:-1024
global_load_lds_dwordx4 v4, s[40:41] offset:0
global_load_lds_dwordx4 v5, s[40:41] offset:1024
global_load_lds_dwordx4 v6, s[40:41] offset:2048
global_load_lds_dwordx4 v7, s[40:41] offset:3072
s_add_u32 m0, s48, 12288
s_nop 0
global_load_lds_dwordx4 v8, s[40:41] offset:-4096
s_add_u32 s40, s40, 73728
s_addc_u32 s41, s41, 0
s_add_u32 m0, s48, 40960
s_nop 0
global_load_lds_dwordx4 v0, s[40:41] offset:-4096
global_load_lds_dwordx4 v1, s[40:41] offset:-3072
global_load_lds_dwordx4 v2, s[40:41] offset:-2048
global_load_lds_dwordx4 v3, s[40:41] offset:-1024
global_load_lds_dwordx4 v4, s[40:41] offset:0
global_load_lds_dwordx4 v5, s[40:41] offset:1024
global_load_lds_dwordx4 v6, s[40:41] offset:2048
global_load_lds_dwordx4 v7, s[40:41] offset:3072
s_add_u32 m0, s48, 49152
s_nop 0
global_load_lds_dwordx4 v8, s[40:41] offset:-4096
s_add_u32 s40, s40, 73728
s_addc_u32 s41, s41, 0
s_add_u32 m0, s48, 77824
s_nop 0
global_load_lds_dwordx4 v0, s[40:41] offset:-4096
global_load_lds_dwordx4 v1, s[40:41] offset:-3072
global_load_lds_dwordx4 v2, s[40:41] offset:-2048
global_load_lds_dwordx4 v3, s[40:41] offset:-1024
global_load_lds_dwordx4 v4, s[40:41] offset:0
global_load_lds_dwordx4 v5, s[40:41] offset:1024
global_load_lds_dwordx4 v6, s[40:41] offset:2048
global_load_lds_dwordx4 v7, s[40:41] offset:3072
s_add_u32 m0, s48, 86016
s_nop 0
global_load_lds_dwordx4 v8, s[40:41] offset:-4096
s_add_u32 s40, s40, 73728
s_addc_u32 s41, s41, 0
v_add_u32 v204, 0x10000, v9
v_add_u32 v205, 0x20000, v9
v_add_u32 v206, 0x10000, v10
v_add_u32 v207, 0x20000, v10
s_waitcnt lgkmcnt(0)
s_mul_i32 s37, s37, 0x9E3779B1
s_add_u32 s37, s37, 0x7F4A7C15
v_add_u32 v22, s37, v22
v_add_u32 v24, s37, v24
s_waitcnt vmcnt(18)
s_barrier
s_add_u32 m0, s48, 114688
ds_read_b64_tr_b16 a[0:1], v9 offset:0
ds_read_b64_tr_b16 a[2:3], v9 offset:2304
global_load_lds_dwordx4 v0, s[40:41] offset:-4096
ds_read_b64_tr_b16 a[4:5], v9 offset:8
global_load_lds_dwordx4 v1, s[40:41] offset:-3072
ds_read_b64_tr_b16 a[6:7], v9 offset:2312
global_load_lds_dwordx4 v2, s[40:41] offset:-2048
ds_read_b64_tr_b16 a[8:9], v9 offset:64
global_load_lds_dwordx4 v3, s[40:41] offset:-1024
ds_read_b64_tr_b16 a[10:11], v9 offset:2368
ds_read_b64_tr_b16 a[12:13], v9 offset:72
global_load_lds_dwordx4 v4, s[40:41] offset:0
ds_read_b64_tr_b16 a[14:15], v9 offset:2376
global_load_lds_dwordx4 v5, s[40:41] offset:1024
ds_read_b64_tr_b16 a[16:17], v9 offset:18432
global_load_lds_dwordx4 v6, s[40:41] offset:2048
ds_read_b64_tr_b16 a[18:19], v9 offset:20736
global_load_lds_dwordx4 v7, s[40:41] offset:3072
ds_read_b64_tr_b16 a[20:21], v9 offset:18440
ds_read_b64_tr_b16 a[22:23], v9 offset:20744
s_add_u32 m0, s48, 122880
ds_read_b64_tr_b16 a[24:25], v9 offset:18496
global_load_lds_dwordx4 v8, s[40:41] offset:-4096
ds_read_b64_tr_b16 a[26:27], v9 offset:20800
s_add_u32 s40, s40, 73728
ds_read_b64_tr_b16 a[28:29], v9 offset:18504
s_addc_u32 s41, s41, 0
ds_read_b64_tr_b16 a[30:31], v9 offset:20808
s_waitcnt vmcnt(18) lgkmcnt(0)
s_barrier
s_add_u32 m0, s48, 4096
ds_read_b64_tr_b16 a[32:33], v9 offset:36864
ds_read_b64_tr_b16 a[34:35], v9 offset:39168
global_load_lds_dwordx4 v0, s[40:41] offset:-4096
ds_read_b64_tr_b16 a[36:37], v9 offset:36872
global_load_lds_dwordx4 v1, s[40:41] offset:-3072
ds_read_b64_tr_b16 a[38:39], v9 offset:39176
global_load_lds_dwordx4 v2, s[40:41] offset:-2048
ds_read_b64_tr_b16 a[40:41], v9 offset:36928
global_load_lds_dwordx4 v3, s[40:41] offset:-1024
ds_read_b64_tr_b16 a[42:43], v9 offset:39232
ds_read_b64_tr_b16 a[44:45], v9 offset:36936
global_load_lds_dwordx4 v4, s[40:41] offset:0
ds_read_b64_tr_b16 a[46:47], v9 offset:39240
global_load_lds_dwordx4 v5, s[40:41] offset:1024
ds_read_b64_tr_b16 a[48:49], v9 offset:55296
global_load_lds_dwordx4 v6, s[40:41] offset:2048
ds_read_b64_tr_b16 a[50:51], v9 offset:57600
global_load_lds_dwordx4 v7, s[40:41] offset:3072
ds_read_b64_tr_b16 a[52:53], v9 offset:55304
ds_read_b64_tr_b16 a[54:55], v9 offset:57608
s_add_u32 m0, s48, 12288
ds_read_b64_tr_b16 a[56:57], v9 offset:55360
global_load_lds_dwordx4 v8, s[40:41] offset:-4096
ds_read_b64_tr_b16 a[58:59], v9 offset:57664
s_add_u32 s40, s40, 73728
ds_read_b64_tr_b16 a[60:61], v9 offset:55368
s_addc_u32 s41, s41, 0
ds_read_b64_tr_b16 a[62:63], v9 offset:57672
s_waitcnt vmcnt(18) lgkmcnt(0)
s_barrier
s_add_u32 m0, s48, 40960
ds_read_b64_tr_b16 a[64:65], v204 offset:8192
ds_read_b64_tr_b16 a[66:67], v204 offset:10496
global_load_lds_dwordx4 v0, s[40:41] offset:-4096
ds_read_b64_tr_b16 a[68:69], v204 offset:8200
global_load_lds_dwordx4 v1, s[40:41] offset:-3072
ds_read_b64_tr_b16 a[70:71], v204 offset:10504
global_load_lds_dwordx4 v2, s[40:41] offset:-2048
ds_read_b64_tr_b16 a[72:73], v204 offset:8256
global_load_lds_dwordx4 v3, s[40:41] offset:-1024
ds_read_b64_tr_b16 a[74:75], v204 offset:10560
ds_read_b64_tr_b16 a[76:77], v204 offset:8264
global_load_lds_dwordx4 v4, s[40:41] offset:0
ds_read_b64_tr_b16 a[78:79], v204 offset:10568
global_load_lds_dwordx4 v5, s[40:41] offset:1024
ds_read_b64_tr_b16 a[80:81], v204 offset:26624
global_load_lds_dwordx4 v6, s[40:41] offset:2048
ds_read_b64_tr_b16 a[82:83], v204 offset:28928
global_load_lds_dwordx4 v7, s[40:41] offset:3072
ds_read_b64_tr_b16 a[84:85], v204 offset:26632
ds_read_b64_tr_b16 a[86:87], v204 offset:28936
)"
R"(s_add_u32 m0, s48, 49152
ds_read_b64_tr_b16 a[88:89], v204 offset:26688
global_load_lds_dwordx4 v8, s[40:41] offset:-4096
ds_read_b64_tr_b16 a[90:91], v204 offset:28992
s_add_u32 s40, s40, 73728
ds_read_b64_tr_b16 a[92:93], v204 offset:26696
s_addc_u32 s41, s41, 0
ds_read_b64_tr_b16 a[94:95], v204 offset:29000
s_waitcnt vmcnt(18) lgkmcnt(0)
s_barrier
s_add_u32 m0, s48, 77824
ds_read_b64_tr_b16 a[96:97], v204 offset:45056
ds_read_b64_tr_b16 a[98:99], v204 offset:47360
global_load_lds_dwordx4 v0, s[40:41] offset:-4096
ds_read_b64_tr_b16 a[100:101], v204 offset:45064
global_load_lds_dwordx4 v1, s[40:41] offset:-3072
ds_read_b64_tr_b16 a[102:103], v204 offset:47368
global_load_lds_dwordx4 v2, s[40:41] offset:-2048
ds_read_b64_tr_b16 a[104:105], v204 offset:45120
global_load_lds_dwordx4 v3, s[40:41] offset:-1024
ds_read_b64_tr_b16 a[106:107], v204 offset:47424
ds_read_b64_tr_b16 a[108:109], v204 offset:45128
global_load_lds_dwordx4 v4, s[40:41] offset:0
ds_read_b64_tr_b16 a[110:111], v204 offset:47432
global_load_lds_dwordx4 v5, s[40:41] offset:1024
ds_read_b64_tr_b16 a[112:113], v204 offset:63488
global_load_lds_dwordx4 v6, s[40:41] offset:2048
ds_read_b64_tr_b16 a[114:115], v205 offset:256
global_load_lds_dwordx4 v7, s[40:41] offset:3072
ds_read_b64_tr_b16 a[116:117], v204 offset:63496
ds_read_b64_tr_b16 a[118:119], v205 offset:264
s_add_u32 m0, s48, 86016
ds_read_b64_tr_b16 a[120:121], v204 offset:63552
global_load_lds_dwordx4 v8, s[40:41] offset:-4096
ds_read_b64_tr_b16 a[122:123], v205 offset:320
s_add_u32 s40, s40, 73728
ds_read_b64_tr_b16 a[124:125], v204 offset:63560
s_addc_u32 s41, s41, 0
ds_read_b64_tr_b16 a[126:127], v205 offset:328
s_waitcnt vmcnt(18) lgkmcnt(0)
s_barrier
s_add_u32 m0, s48, 114688
ds_read_b64_tr_b16 a[128:129], v9 offset:0
ds_read_b64_tr_b16 a[130:131], v9 offset:2304
global_load_lds_dwordx4 v0, s[40:41] offset:-4096
ds_read_b64_tr_b16 a[132:133], v9 offset:8
global_load_lds_dwordx4 v1, s[40:41] offset:-3072
ds_read_b64_tr_b16 a[134:135], v9 offset:2312
ds_read_b64_tr_b16 a[136:137], v9 offset:64
global_load_lds_dwordx4 v2, s[40:41] offset:-2048
ds_read_b64_tr_b16 a[138:139], v9 offset:2368
global_load_lds_dwordx4 v3, s[40:41] offset:-1024
ds_read_b64_tr_b16 a[140:141], v9 offset:72
global_load_lds_dwordx4 v4, s[40:41] offset:0
ds_read_b64_tr_b16 a[142:143], v9 offset:2376
ds_read_b64_tr_b16 a[144:145], v9 offset:18432
global_load_lds_dwordx4 v5, s[40:41] offset:1024
ds_read_b64_tr_b16 a[146:147], v9 offset:20736
global_load_lds_dwordx4 v6, s[40:41] offset:2048
ds_read_b64_tr_b16 a[148:149], v9 offset:18440
ds_read_b64_tr_b16 a[150:151], v9 offset:20744
global_load_lds_dwordx4 v7, s[40:41] offset:3072
ds_read_b64_tr_b16 a[152:153], v9 offset:18496
s_add_u32 m0, s48, 122880
ds_read_b64_tr_b16 a[154:155], v9 offset:20800
global_load_lds_dwordx4 v8, s[40:41] offset:-4096
ds_read_b64_tr_b16 a[156:157], v9 offset:18504
ds_read_b64_tr_b16 a[158:159], v9 offset:20808
s_add_u32 s40, s40, 73728
ds_read_b64_tr_b16 v[60:61], v10 offset:18432
s_addc_u32 s41, s41, 0
ds_read_b64_tr_b16 v[62:63], v10 offset:20736
s_waitcnt vmcnt(18) lgkmcnt(0)
s_barrier
s_add_u32 m0, s48, 4096
ds_read_b64_tr_b16 a[160:161], v9 offset:36864
ds_read_b64_tr_b16 a[162:163], v9 offset:39168
global_load_lds_dwordx4 v0, s[40:41] offset:-4096
ds_read_b64_tr_b16 a[164:165], v9 offset:36872
ds_read_b64_tr_b16 a[166:167], v9 offset:39176
global_load_lds_dwordx4 v1, s[40:41] offset:-3072
ds_read_b64_tr_b16 a[168:169], v9 offset:36928
global_load_lds_dwordx4 v2, s[40:41] offset:-2048
ds_read_b64_tr_b16 a[170:171], v9 offset:39232
ds_read_b64_tr_b16 a[172:173], v9 offset:36936
global_load_lds_dwordx4 v3, s[40:41] offset:-1024
ds_read_b64_tr_b16 a[174:175], v9 offset:39240
global_load_lds_dwordx4 v4, s[40:41] offset:0
ds_read_b64_tr_b16 v[64:65], v10 offset:36864
ds_read_b64_tr_b16 v[66:67], v10 offset:39168
global_load_lds_dwordx4 v5, s[40:41] offset:1024
ds_read_b64_tr_b16 a[176:177], v9 offset:55296
global_load_lds_dwordx4 v6, s[40:41] offset:2048
ds_read_b64_tr_b16 a[178:179], v9 offset:57600
ds_read_b64_tr_b16 a[180:181], v9 offset:55304
global_load_lds_dwordx4 v7, s[40:41] offset:3072
ds_read_b64_tr_b16 a[182:183], v9 offset:57608
s_add_u32 m0, s48, 12288
ds_read_b64_tr_b16 a[184:185], v9 offset:55360
ds_read_b64_tr_b16 a[186:187], v9 offset:57664
global_load_lds_dwordx4 v8, s[40:41] offset:-4096
ds_read_b64_tr_b16 a[188:189], v9 offset:55368
s_sub_u32 s40, s40, 589824
ds_read_b64_tr_b16 a[190:191], v9 offset:57672
ds_read_b64_tr_b16 v[68:69], v10 offset:55296
s_subb_u32 s41, s41, 0
ds_read_b64_tr_b16 v[70:71], v10 offset:57600
s_waitcnt vmcnt(18) lgkmcnt(0)
s_barrier
ds_read_b64_tr_b16 a[192:193], v204 offset:8192
ds_read_b64_tr_b16 a[194:195], v204 offset:10496
ds_read_b64_tr_b16 a[196:197], v204 offset:8200
ds_read_b64_tr_b16 a[198:199], v204 offset:10504
ds_read_b64_tr_b16 a[200:201], v204 offset:8256
ds_read_b64_tr_b16 a[202:203], v204 offset:10560
ds_read_b64_tr_b16 a[204:205], v204 offset:8264
ds_read_b64_tr_b16 a[206:207], v204 offset:10568
ds_read_b64_tr_b16 v[72:73], v206 offset:8192
ds_read_b64_tr_b16 v[74:75], v206 offset:10496
ds_read_b64_tr_b16 a[208:209], v204 offset:26624
ds_read_b64_tr_b16 a[210:211], v204 offset:28928
ds_read_b64_tr_b16 a[212:213], v204 offset:26632
ds_read_b64_tr_b16 a[214:215], v204 offset:28936
ds_read_b64_tr_b16 a[216:217], v204 offset:26688
ds_read_b64_tr_b16 a[218:219], v204 offset:28992
ds_read_b64_tr_b16 a[220:221], v204 offset:26696
ds_read_b64_tr_b16 a[222:223], v204 offset:29000
ds_read_b64_tr_b16 v[76:77], v206 offset:26624
ds_read_b64_tr_b16 v[78:79], v206 offset:28928
s_waitcnt vmcnt(9) lgkmcnt(0)
s_barrier
ds_read_b64_tr_b16 a[224:225], v204 offset:45056
ds_read_b64_tr_b16 a[226:227], v204 offset:47360
ds_read_b64_tr_b16 a[228:229], v204 offset:45064
ds_read_b64_tr_b16 a[230:231], v204 offset:47368
ds_read_b64_tr_b16 a[232:233], v204 offset:45120
ds_read_b64_tr_b16 a[234:235], v204 offset:47424
ds_read_b64_tr_b16 a[236:237], v204 offset:45128
ds_read_b64_tr_b16 a[238:239], v204 offset:47432
ds_read_b64_tr_b16 v[80:81], v206 offset:45056
ds_read_b64_tr_b16 v[82:83], v206 offset:47360
ds_read_b64_tr_b16 a[240:241], v204 offset:63488
ds_read_b64_tr_b16 a[242:243], v205 offset:256
ds_read_b64_tr_b16 a[244:245], v204 offset:63496
ds_read_b64_tr_b16 a[246:247], v205 offset:264
ds_read_b64_tr_b16 a[248:249], v204 offset:63552
ds_read_b64_tr_b16 a[250:251], v205 offset:320
ds_read_b64_tr_b16 a[252:253], v204 offset:63560
ds_read_b64_tr_b16 a[254:255], v205 offset:328
ds_read_b64_tr_b16 v[84:85], v206 offset:63488
ds_read_b64_tr_b16 v[86:87], v207 offset:256
s_waitcnt vmcnt(0) lgkmcnt(0)
s_barrier
ds_read_b64_tr_b16 v[28:29], v9 offset:0
ds_read_b64_tr_b16 v[30:31], v9 offset:2304
ds_read_b64_tr_b16 v[32:33], v9 offset:8
ds_read_b64_tr_b16 v[34:35], v9 offset:2312
ds_read_b64_tr_b16 v[36:37], v9 offset:64
ds_read_b64_tr_b16 v[38:39], v9 offset:2368
ds_read_b64_tr_b16 v[40:41], v9 offset:72
ds_read_b64_tr_b16 v[42:43], v9 offset:2376
ds_read_b64_tr_b16 v[88:89], v10 offset:0
ds_read_b64_tr_b16 v[90:91], v10 offset:2304
ds_read_b64_tr_b16 v[44:45], v9 offset:18432
ds_read_b64_tr_b16 v[46:47], v9 offset:20736
ds_read_b64_tr_b16 v[48:49], v9 offset:18440
ds_read_b64_tr_b16 v[50:51], v9 offset:20744
ds_read_b64_tr_b16 v[52:53], v9 offset:18496
ds_read_b64_tr_b16 v[54:55], v9 offset:20800
ds_read_b64_tr_b16 v[56:57], v9 offset:18504
ds_read_b64_tr_b16 v[58:59], v9 offset:20808
ds_read_b64_tr_b16 v[92:93], v10 offset:18432
ds_read_b64_tr_b16 v[94:95], v10 offset:20736
s_waitcnt lgkmcnt(0)
s_barrier
s_mov_b64 s[76:77], 0
s_add_u32 m0, s49, 18432
s_mov_b32 s70, 33
s_waitcnt vmcnt(0) lgkmcnt(0)
s_nop 7
.p2align 6
.Lwo_top14:
s_waitcnt lgkmcnt(5)
v_mfma_f32_16x16x32_bf16 v[96:99], a[144:147], v[132:135], v[96:99]
s_waitcnt lgkmcnt(4)
ds_read_b128 v[144:147], v25 offset:12288
v_add_f32 v176, v176, v184
v_mfma_f32_16x16x32_bf16 v[100:103], a[148:151], v[132:135], v[100:103]
v_mfma_f32_16x16x32_bf16 v[104:107], a[152:155], v[132:135], v[104:107]
s_waitcnt vmcnt(6)
s_barrier
v_mfma_f32_16x16x32_bf16 v[108:111], a[156:159], v[132:135], v[108:111]
v_add_f32 v177, v177, v185
v_add_f32 v178, v178, v186
v_mfma_f32_16x16x32_bf16 v[128:131], v[60:63], v[132:135], 0
s_waitcnt lgkmcnt(3)
v_add_f32 v179, v179, v187
v_add_f32 v176, v176, v188
v_mfma_f32_16x16x32_bf16 v[96:99], a[160:163], v[136:139], v[96:99]
ds_read_b128 v[132:135], v25 offset:13312
v_add_f32 v177, v177, v189
v_mfma_f32_16x16x32_bf16 v[100:103], a[164:167], v[136:139], v[100:103]
v_add_f32 v178, v178, v190
v_add_f32 v179, v179, v191
v_mfma_f32_16x16x32_bf16 v[104:107], a[168:171], v[136:139], v[104:107]
s_waitcnt lgkmcnt(3)
v_add_f32 v180, v180, v192
v_add_f32 v181, v181, v193
v_mfma_f32_16x16x32_bf16 v[108:111], a[172:175], v[136:139], v[108:111]
v_add_f32 v182, v182, v194
v_add_f32 v183, v183, v195
v_mfma_f32_16x16x32_bf16 v[128:131], v[64:67], v[136:139], v[128:131]
s_waitcnt lgkmcnt(2)
v_cvt_pk_bf16_f32 v172, v176, v177
v_cvt_pk_bf16_f32 v173, v178, v179
v_mfma_f32_16x16x32_bf16 v[96:99], a[176:179], v[140:143], v[96:99]
s_cmp_eq_u32 s70, 32
ds_read_b128 v[136:139], v25 offset:14336
v_cvt_pk_bf16_f32 v174, v180, v181
v_mfma_f32_16x16x32_bf16 v[100:103], a[180:183], v[140:143], v[100:103]
s_cselect_b64 s[42:43], s[44:45], s[42:43]
v_cvt_pk_bf16_f32 v175, v182, v183
v_cvt_pk_bf16_f32 v164, v112, v113
v_mfma_f32_16x16x32_bf16 v[104:107], a[184:187], v[140:143], v[104:107]
s_cmp_eq_u32 s70, 16
global_store_dwordx4 v14, v[172:175], s[38:39] sc1
v_mfma_f32_16x16x32_bf16 v[108:111], a[188:191], v[140:143], v[108:111]
s_cselect_b64 s[42:43], s[46:47], s[42:43]
v_add_u32 v14, 18688, v14
v_cvt_pk_bf16_f32 v165, v114, v115
v_mfma_f32_16x16x32_bf16 v[128:131], v[68:71], v[140:143], v[128:131]
s_waitcnt lgkmcnt(2)
v_cvt_pk_bf16_f32 v166, v116, v117
v_cvt_pk_bf16_f32 v167, v118, v119
v_mfma_f32_16x16x32_bf16 v[96:99], a[192:195], v[144:147], v[96:99]
s_xor_b32 s49, s49, 0x10000
ds_read_b128 v[140:143], v25 offset:15360
v_add_f32 v254, v112, v113
v_mfma_f32_16x16x32_bf16 v[100:103], a[196:199], v[144:147], v[100:103]
s_mov_b32 m0, 53248
global_store_dwordx4 v13, v[164:167], s[38:39] offset:0 sc1
v_mfma_f32_16x16x32_bf16 v[104:107], a[200:203], v[144:147], v[104:107]
v_cvt_pk_bf16_f32 v168, v120, v121
v_cvt_pk_bf16_f32 v169, v122, v123
v_mfma_f32_16x16x32_bf16 v[108:111], a[204:207], v[144:147], v[108:111]
v_cvt_pk_bf16_f32 v170, v124, v125
v_cvt_pk_bf16_f32 v171, v126, v127
v_mfma_f32_16x16x32_bf16 v[128:131], v[72:75], v[144:147], v[128:131]
s_waitcnt lgkmcnt(2)
global_store_dwordx4 v13, v[168:171], s[38:39] offset:1024 sc1
v_mfma_f32_16x16x32_bf16 v[96:99], a[208:211], v[132:135], v[96:99]
ds_read_b128 v[144:147], v25 offset:16384
v_add_u32 v13, 18688, v13
v_mfma_f32_16x16x32_bf16 v[100:103], a[212:215], v[132:135], v[100:103]
v_mul_f32 v255, v112, v112
v_fmac_f32 v255, v113, v113
v_mfma_f32_16x16x32_bf16 v[104:107], a[216:219], v[132:135], v[104:107]
v_add_f32 v254, v254, v114
v_fmac_f32 v255, v114, v114
v_mfma_f32_16x16x32_bf16 v[108:111], a[220:223], v[132:135], v[108:111]
v_add_f32 v254, v254, v115
v_fmac_f32 v255, v115, v115
v_mfma_f32_16x16x32_bf16 v[128:131], v[76:79], v[132:135], v[128:131]
s_waitcnt vmcnt(7)
global_load_lds_dwordx4 v16, s[42:43] offset:0 sc1
ds_read_b128 v[148:151], v19 offset:8192
s_waitcnt lgkmcnt(3)
v_mfma_f32_16x16x32_bf16 v[96:99], a[224:227], v[136:139], v[96:99]
ds_read_b128 v[132:135], v25 offset:17408
ds_read_b128 v[152:155], v20 offset:8192
v_mfma_f32_16x16x32_bf16 v[100:103], a[228:231], v[136:139], v[100:103]
v_add_f32 v254, v254, v116
v_fmac_f32 v255, v116, v116
v_mfma_f32_16x16x32_bf16 v[104:107], a[232:235], v[136:139], v[104:107]
v_add_f32 v254, v254, v117
v_fmac_f32 v255, v117, v117
v_mfma_f32_16x16x32_bf16 v[108:111], a[236:239], v[136:139], v[108:111]
v_add_f32 v254, v254, v118
v_fmac_f32 v255, v118, v118
v_mfma_f32_16x16x32_bf16 v[128:131], v[80:83], v[136:139], v[128:131]
s_waitcnt lgkmcnt(4)
v_add_f32 v254, v254, v119
v_fmac_f32 v255, v119, v119
v_mfma_f32_16x16x32_bf16 v[96:99], a[240:243], v[140:143], v[96:99]
ds_read_b128 v[136:139], v25 offset:0
v_add_f32 v254, v254, v120
v_mfma_f32_16x16x32_bf16 v[100:103], a[244:247], v[140:143], v[100:103]
global_load_lds_dwordx4 v17, s[42:43] offset:1024 sc1
v_fmac_f32 v255, v120, v120
v_mfma_f32_16x16x32_bf16 v[104:107], a[248:251], v[140:143], v[104:107]
v_add_f32 v254, v254, v121
v_fmac_f32 v255, v121, v121
v_mfma_f32_16x16x32_bf16 v[108:111], a[252:255], v[140:143], v[108:111]
v_add_f32 v254, v254, v122
v_fmac_f32 v255, v122, v122
v_mfma_f32_16x16x32_bf16 v[128:131], v[84:87], v[140:143], v[128:131]
s_waitcnt lgkmcnt(4)
v_add_f32 v254, v254, v123
v_fmac_f32 v255, v123, v123
v_mfma_f32_16x16x32_bf16 v[96:99], v[28:31], v[144:147], v[96:99]
ds_read_b128 v[140:143], v25 offset:1024
v_add_f32 v254, v254, v124
v_mfma_f32_16x16x32_bf16 v[100:103], v[32:35], v[144:147], v[100:103]
s_add_u32 m0, s49, 0
v_fmac_f32 v255, v124, v124
v_add_f32 v254, v254, v125
v_mfma_f32_16x16x32_bf16 v[104:107], v[36:39], v[144:147], v[104:107]
v_fmac_f32 v255, v125, v125
v_add_f32 v254, v254, v126
v_mfma_f32_16x16x32_bf16 v[108:111], v[40:43], v[144:147], v[108:111]
v_fmac_f32 v255, v126, v126
v_add_f32 v254, v254, v127
v_mfma_f32_16x16x32_bf16 v[128:131], v[88:91], v[144:147], v[128:131]
s_waitcnt lgkmcnt(3)
v_fmac_f32 v255, v127, v127
v_add_f32 v254, v254, v176
v_mfma_f32_16x16x32_bf16 v[96:99], v[44:47], v[132:135], v[96:99]
ds_read_b128 v[144:147], v25 offset:2048
v_fmac_f32 v255, v176, v176
v_mfma_f32_16x16x32_bf16 v[100:103], v[48:51], v[132:135], v[100:103]
v_add_f32 v254, v254, v177
v_fmac_f32 v255, v177, v177
v_mfma_f32_16x16x32_bf16 v[104:107], v[52:55], v[132:135], v[104:107]
v_add_f32 v254, v254, v178
v_fmac_f32 v255, v178, v178
v_mfma_f32_16x16x32_bf16 v[108:111], v[56:59], v[132:135], v[108:111]
v_add_f32 v254, v254, v179
v_fmac_f32 v255, v179, v179
v_mfma_f32_16x16x32_bf16 v[128:131], v[92:95], v[132:135], v[128:131]
s_waitcnt lgkmcnt(2)
v_add_f32 v254, v254, v180
v_fmac_f32 v255, v180, v180
v_mfma_f32_16x16x32_bf16 v[96:99], a[0:3], v[136:139], v[96:99]
ds_read_b128 v[132:135], v25 offset:3072
v_add_f32 v254, v254, v181
v_mfma_f32_16x16x32_bf16 v[100:103], a[4:7], v[136:139], v[100:103]
v_fmac_f32 v255, v181, v181
v_add_f32 v254, v254, v182
v_mfma_f32_16x16x32_bf16 v[104:107], a[8:11], v[136:139], v[104:107]
v_fmac_f32 v255, v182, v182
v_add_f32 v254, v254, v183
v_mfma_f32_16x16x32_bf16 v[108:111], a[12:15], v[136:139], v[108:111]
s_waitcnt lgkmcnt(2)
v_fmac_f32 v255, v183, v183
v_mfma_f32_16x16x32_bf16 v[96:99], a[16:19], v[140:143], v[96:99]
ds_read_b128 v[136:139], v25 offset:4096
v_mfma_f32_16x16x32_bf16 v[100:103], a[20:23], v[140:143], v[100:103]
ds_write_b64 v27, v[254:255] offset:46592
v_mfma_f32_16x16x32_bf16 v[104:107], a[24:27], v[140:143], v[104:107]
v_lshlrev_b32 v176, 16, v156
v_and_b32 v177, 0xffff0000, v156
v_mfma_f32_16x16x32_bf16 v[108:111], a[28:31], v[140:143], v[108:111]
s_waitcnt lgkmcnt(3)
v_lshlrev_b32 v178, 16, v157
v_and_b32 v179, 0xffff0000, v157
v_mfma_f32_16x16x32_bf16 v[96:99], a[32:35], v[144:147], v[96:99]
ds_read_b128 v[140:143], v25 offset:5120
v_lshlrev_b32 v180, 16, v158
v_mfma_f32_16x16x32_bf16 v[100:103], a[36:39], v[144:147], v[100:103]
v_mfma_f32_16x16x32_bf16 v[104:107], a[40:43], v[144:147], v[104:107]
s_barrier
v_mfma_f32_16x16x32_bf16 v[108:111], a[44:47], v[144:147], v[108:111]
s_waitcnt lgkmcnt(3)
v_and_b32 v181, 0xffff0000, v158
v_lshlrev_b32 v182, 16, v159
v_mfma_f32_16x16x32_bf16 v[96:99], a[48:51], v[132:135], v[96:99]
ds_read_b128 v[144:147], v25 offset:6144
global_load_lds_dwordx4 v11, s[38:39] offset:-2048 sc1
v_mfma_f32_16x16x32_bf16 v[100:103], a[52:55], v[132:135], v[100:103]
v_and_b32 v183, 0xffff0000, v159
v_add_f32 v180, v180, v128
v_mfma_f32_16x16x32_bf16 v[104:107], a[56:59], v[132:135], v[104:107]
v_add_f32 v181, v181, v129
v_add_f32 v182, v182, v130
v_mfma_f32_16x16x32_bf16 v[108:111], a[60:63], v[132:135], v[108:111]
s_waitcnt lgkmcnt(3)
global_load_lds_dwordx4 v11, s[38:39] offset:-1024 sc1
v_add_f32 v183, v183, v131
v_mfma_f32_16x16x32_bf16 v[96:99], a[64:67], v[136:139], v[96:99]
ds_read_b128 v[132:135], v25 offset:7168
global_load_dwordx4 v[156:159], v18, s[42:43] sc1
v_mfma_f32_16x16x32_bf16 v[100:103], a[68:71], v[136:139], v[100:103]
s_sub_u32 s42, s42, 18432
v_lshlrev_b32 v112, 16, v148
v_and_b32 v113, 0xffff0000, v148
v_mfma_f32_16x16x32_bf16 v[104:107], a[72:75], v[136:139], v[104:107]
s_subb_u32 s43, s43, 0
global_load_lds_dwordx4 v11, s[38:39] offset:0 sc1
v_add_u32 v11, 18432, v11
v_mfma_f32_16x16x32_bf16 v[108:111], a[76:79], v[136:139], v[108:111]
s_waitcnt lgkmcnt(2)
v_min_u32 v11, v11, v12
v_lshlrev_b32 v114, 16, v149
v_mfma_f32_16x16x32_bf16 v[96:99], a[80:83], v[140:143], v[96:99]
ds_read_b128 v[136:139], v25 offset:8192
v_and_b32 v115, 0xffff0000, v149
v_mfma_f32_16x16x32_bf16 v[100:103], a[84:87], v[140:143], v[100:103]
v_lshlrev_b32 v116, 16, v150
v_and_b32 v117, 0xffff0000, v150
v_mfma_f32_16x16x32_bf16 v[104:107], a[88:91], v[140:143], v[104:107]
)"
R"(v_lshlrev_b32 v118, 16, v151
v_and_b32 v119, 0xffff0000, v151
v_mfma_f32_16x16x32_bf16 v[108:111], a[92:95], v[140:143], v[108:111]
s_waitcnt lgkmcnt(2)
v_lshlrev_b32 v120, 16, v152
v_and_b32 v121, 0xffff0000, v152
v_mfma_f32_16x16x32_bf16 v[96:99], a[96:99], v[144:147], v[96:99]
v_lshlrev_b32 v122, 16, v153
v_and_b32 v123, 0xffff0000, v153
v_mfma_f32_16x16x32_bf16 v[100:103], a[100:103], v[144:147], v[100:103]
v_mfma_f32_16x16x32_bf16 v[104:107], a[104:107], v[144:147], v[104:107]
s_waitcnt vmcnt(9)
s_barrier
v_mfma_f32_16x16x32_bf16 v[108:111], a[108:111], v[144:147], v[108:111]
s_waitcnt lgkmcnt(1)
ds_read_b128 v[140:143], v25 offset:27648
ds_read_b128 v[184:187], v26 offset:36864
v_mfma_f32_16x16x32_bf16 v[96:99], a[112:115], v[132:135], v[96:99]
ds_read_b128 v[144:147], v25 offset:28672
ds_read_b128 v[188:191], v26 offset:37888
v_mfma_f32_16x16x32_bf16 v[100:103], a[116:119], v[132:135], v[100:103]
ds_read_b128 v[192:195], v26 offset:38912
v_lshlrev_b32 v124, 16, v154
v_mfma_f32_16x16x32_bf16 v[104:107], a[120:123], v[132:135], v[104:107]
v_and_b32 v125, 0xffff0000, v154
v_lshlrev_b32 v126, 16, v155
v_mfma_f32_16x16x32_bf16 v[108:111], a[124:127], v[132:135], v[108:111]
s_waitcnt lgkmcnt(5)
v_and_b32 v127, 0xffff0000, v155
v_mfma_f32_16x16x32_bf16 v[96:99], a[128:131], v[136:139], v[96:99]
ds_read_b128 v[132:135], v25 offset:29696
v_mfma_f32_16x16x32_bf16 v[100:103], a[132:135], v[136:139], v[100:103]
v_mfma_f32_16x16x32_bf16 v[104:107], a[136:139], v[136:139], v[104:107]
s_waitcnt lgkmcnt(4)
v_add_f32 v176, v176, v184
v_add_f32 v177, v177, v185
v_mfma_f32_16x16x32_bf16 v[108:111], a[140:143], v[136:139], v[108:111]
v_add_f32 v178, v178, v186
v_add_f32 v179, v179, v187
v_mfma_f32_16x16x32_bf16 v[112:115], a[144:147], v[140:143], v[112:115]
s_waitcnt lgkmcnt(2)
ds_read_b128 v[136:139], v25 offset:30720
v_add_f32 v176, v176, v188
v_mfma_f32_16x16x32_bf16 v[116:119], a[148:151], v[140:143], v[116:119]
v_mfma_f32_16x16x32_bf16 v[120:123], a[152:155], v[140:143], v[120:123]
s_waitcnt vmcnt(6)
s_barrier
v_mfma_f32_16x16x32_bf16 v[124:127], a[156:159], v[140:143], v[124:127]
v_add_f32 v177, v177, v189
v_add_f32 v178, v178, v190
v_mfma_f32_16x16x32_bf16 v[128:131], v[60:63], v[140:143], 0
s_waitcnt lgkmcnt(2)
v_add_f32 v179, v179, v191
v_add_f32 v180, v180, v192
v_mfma_f32_16x16x32_bf16 v[112:115], a[160:163], v[144:147], v[112:115]
ds_read_b128 v[140:143], v25 offset:31744
v_add_f32 v181, v181, v193
v_mfma_f32_16x16x32_bf16 v[116:119], a[164:167], v[144:147], v[116:119]
v_add_f32 v182, v182, v194
v_add_f32 v183, v183, v195
v_mfma_f32_16x16x32_bf16 v[120:123], a[168:171], v[144:147], v[120:123]
v_cvt_pk_bf16_f32 v172, v176, v177
v_cvt_pk_bf16_f32 v173, v178, v179
v_mfma_f32_16x16x32_bf16 v[124:127], a[172:175], v[144:147], v[124:127]
v_cvt_pk_bf16_f32 v174, v180, v181
v_cvt_pk_bf16_f32 v175, v182, v183
v_mfma_f32_16x16x32_bf16 v[128:131], v[64:67], v[144:147], v[128:131]
s_waitcnt lgkmcnt(2)
global_store_dwordx4 v14, v[172:175], s[38:39] sc1
v_mfma_f32_16x16x32_bf16 v[112:115], a[176:179], v[132:135], v[112:115]
s_mov_b32 m0, 61440
ds_read_b128 v[144:147], v25 offset:32768
v_add_u32 v14, 18688, v14
v_mfma_f32_16x16x32_bf16 v[116:119], a[180:183], v[132:135], v[116:119]
v_cvt_pk_bf16_f32 v164, v96, v97
v_cvt_pk_bf16_f32 v165, v98, v99
v_mfma_f32_16x16x32_bf16 v[120:123], a[184:187], v[132:135], v[120:123]
v_cvt_pk_bf16_f32 v166, v100, v101
v_cvt_pk_bf16_f32 v167, v102, v103
v_mfma_f32_16x16x32_bf16 v[124:127], a[188:191], v[132:135], v[124:127]
global_store_dwordx4 v13, v[164:167], s[38:39] offset:0 sc1
v_mfma_f32_16x16x32_bf16 v[128:131], v[68:71], v[132:135], v[128:131]
s_waitcnt lgkmcnt(2)
v_cvt_pk_bf16_f32 v168, v104, v105
v_cvt_pk_bf16_f32 v169, v106, v107
v_mfma_f32_16x16x32_bf16 v[112:115], a[192:195], v[136:139], v[112:115]
ds_read_b128 v[132:135], v25 offset:33792
v_cvt_pk_bf16_f32 v170, v108, v109
v_mfma_f32_16x16x32_bf16 v[116:119], a[196:199], v[136:139], v[116:119]
v_cvt_pk_bf16_f32 v171, v110, v111
v_add_f32 v254, v96, v97
v_mfma_f32_16x16x32_bf16 v[120:123], a[200:203], v[136:139], v[120:123]
global_store_dwordx4 v13, v[168:171], s[38:39] offset:1024 sc1
v_mfma_f32_16x16x32_bf16 v[124:127], a[204:207], v[136:139], v[124:127]
v_add_u32 v13, 18688, v13
v_mul_f32 v255, v96, v96
v_mfma_f32_16x16x32_bf16 v[128:131], v[72:75], v[136:139], v[128:131]
s_waitcnt lgkmcnt(2)
v_fmac_f32 v255, v97, v97
v_add_f32 v254, v254, v98
v_mfma_f32_16x16x32_bf16 v[112:115], a[208:211], v[140:143], v[112:115]
ds_read_b128 v[136:139], v25 offset:34816
v_fmac_f32 v255, v98, v98
v_mfma_f32_16x16x32_bf16 v[116:119], a[212:215], v[140:143], v[116:119]
v_add_f32 v254, v254, v99
v_fmac_f32 v255, v99, v99
v_mfma_f32_16x16x32_bf16 v[120:123], a[216:219], v[140:143], v[120:123]
v_add_f32 v254, v254, v100
v_fmac_f32 v255, v100, v100
v_mfma_f32_16x16x32_bf16 v[124:127], a[220:223], v[140:143], v[124:127]
v_add_f32 v254, v254, v101
v_fmac_f32 v255, v101, v101
v_mfma_f32_16x16x32_bf16 v[128:131], v[76:79], v[140:143], v[128:131]
s_waitcnt vmcnt(7)
global_load_lds_dwordx4 v16, s[42:43] offset:0 sc1
ds_read_b128 v[148:151], v19 offset:0
s_waitcnt lgkmcnt(3)
v_mfma_f32_16x16x32_bf16 v[112:115], a[224:227], v[144:147], v[112:115]
ds_read_b128 v[140:143], v25 offset:35840
ds_read_b128 v[152:155], v20 offset:0
v_mfma_f32_16x16x32_bf16 v[116:119], a[228:231], v[144:147], v[116:119]
v_add_f32 v254, v254, v102
v_fmac_f32 v255, v102, v102
v_mfma_f32_16x16x32_bf16 v[120:123], a[232:235], v[144:147], v[120:123]
v_add_f32 v254, v254, v103
v_fmac_f32 v255, v103, v103
v_mfma_f32_16x16x32_bf16 v[124:127], a[236:239], v[144:147], v[124:127]
v_add_f32 v254, v254, v104
v_fmac_f32 v255, v104, v104
v_mfma_f32_16x16x32_bf16 v[128:131], v[80:83], v[144:147], v[128:131]
s_waitcnt lgkmcnt(4)
v_add_f32 v254, v254, v105
v_fmac_f32 v255, v105, v105
v_mfma_f32_16x16x32_bf16 v[112:115], a[240:243], v[132:135], v[112:115]
ds_read_b128 v[144:147], v25 offset:18432
v_add_f32 v254, v254, v106
v_mfma_f32_16x16x32_bf16 v[116:119], a[244:247], v[132:135], v[116:119]
global_load_lds_dwordx4 v17, s[42:43] offset:1024 sc1
v_fmac_f32 v255, v106, v106
v_mfma_f32_16x16x32_bf16 v[120:123], a[248:251], v[132:135], v[120:123]
v_add_f32 v254, v254, v107
v_fmac_f32 v255, v107, v107
v_mfma_f32_16x16x32_bf16 v[124:127], a[252:255], v[132:135], v[124:127]
v_add_f32 v254, v254, v108
v_fmac_f32 v255, v108, v108
v_mfma_f32_16x16x32_bf16 v[128:131], v[84:87], v[132:135], v[128:131]
s_waitcnt lgkmcnt(4)
v_add_f32 v254, v254, v109
v_fmac_f32 v255, v109, v109
v_mfma_f32_16x16x32_bf16 v[112:115], v[28:31], v[136:139], v[112:115]
ds_read_b128 v[132:135], v25 offset:19456
v_add_f32 v254, v254, v110
v_mfma_f32_16x16x32_bf16 v[116:119], v[32:35], v[136:139], v[116:119]
s_add_u32 m0, s49, 18432
v_fmac_f32 v255, v110, v110
v_add_f32 v254, v254, v111
v_mfma_f32_16x16x32_bf16 v[120:123], v[36:39], v[136:139], v[120:123]
v_fmac_f32 v255, v111, v111
v_add_f32 v254, v254, v176
v_mfma_f32_16x16x32_bf16 v[124:127], v[40:43], v[136:139], v[124:127]
v_fmac_f32 v255, v176, v176
v_add_f32 v254, v254, v177
v_mfma_f32_16x16x32_bf16 v[128:131], v[88:91], v[136:139], v[128:131]
s_waitcnt lgkmcnt(3)
v_fmac_f32 v255, v177, v177
v_add_f32 v254, v254, v178
v_mfma_f32_16x16x32_bf16 v[112:115], v[44:47], v[140:143], v[112:115]
ds_read_b128 v[136:139], v25 offset:20480
v_fmac_f32 v255, v178, v178
v_mfma_f32_16x16x32_bf16 v[116:119], v[48:51], v[140:143], v[116:119]
v_add_f32 v254, v254, v179
v_fmac_f32 v255, v179, v179
v_mfma_f32_16x16x32_bf16 v[120:123], v[52:55], v[140:143], v[120:123]
v_add_f32 v254, v254, v180
v_fmac_f32 v255, v180, v180
v_mfma_f32_16x16x32_bf16 v[124:127], v[56:59], v[140:143], v[124:127]
v_add_f32 v254, v254, v181
v_fmac_f32 v255, v181, v181
v_mfma_f32_16x16x32_bf16 v[128:131], v[92:95], v[140:143], v[128:131]
s_waitcnt lgkmcnt(2)
v_add_f32 v254, v254, v182
v_fmac_f32 v255, v182, v182
v_mfma_f32_16x16x32_bf16 v[112:115], a[0:3], v[144:147], v[112:115]
ds_read_b128 v[140:143], v25 offset:21504
v_add_f32 v254, v254, v183
v_mfma_f32_16x16x32_bf16 v[116:119], a[4:7], v[144:147], v[116:119]
v_fmac_f32 v255, v183, v183
v_mfma_f32_16x16x32_bf16 v[120:123], a[8:11], v[144:147], v[120:123]
ds_write_b64 v27, v[254:255] offset:44544
v_mfma_f32_16x16x32_bf16 v[124:127], a[12:15], v[144:147], v[124:127]
s_waitcnt lgkmcnt(3)
v_lshlrev_b32 v176, 16, v160
v_and_b32 v177, 0xffff0000, v160
v_mfma_f32_16x16x32_bf16 v[112:115], a[16:19], v[132:135], v[112:115]
ds_read_b128 v[144:147], v25 offset:22528
v_lshlrev_b32 v178, 16, v161
v_mfma_f32_16x16x32_bf16 v[116:119], a[20:23], v[132:135], v[116:119]
v_and_b32 v179, 0xffff0000, v161
v_lshlrev_b32 v180, 16, v162
v_mfma_f32_16x16x32_bf16 v[120:123], a[24:27], v[132:135], v[120:123]
v_and_b32 v181, 0xffff0000, v162
v_lshlrev_b32 v182, 16, v163
v_mfma_f32_16x16x32_bf16 v[124:127], a[28:31], v[132:135], v[124:127]
s_waitcnt lgkmcnt(3)
v_and_b32 v183, 0xffff0000, v163
v_add_f32 v180, v180, v128
v_mfma_f32_16x16x32_bf16 v[112:115], a[32:35], v[136:139], v[112:115]
ds_read_b128 v[132:135], v25 offset:23552
v_add_f32 v181, v181, v129
v_mfma_f32_16x16x32_bf16 v[116:119], a[36:39], v[136:139], v[116:119]
v_mfma_f32_16x16x32_bf16 v[120:123], a[40:43], v[136:139], v[120:123]
s_barrier
v_mfma_f32_16x16x32_bf16 v[124:127], a[44:47], v[136:139], v[124:127]
s_waitcnt lgkmcnt(3)
v_add_f32 v182, v182, v130
v_add_f32 v183, v183, v131
v_mfma_f32_16x16x32_bf16 v[112:115], a[48:51], v[140:143], v[112:115]
s_mov_b64 s[76:77], -1
ds_read_b128 v[136:139], v25 offset:24576
global_load_lds_dwordx4 v11, s[38:39] offset:-2048 sc1
v_mfma_f32_16x16x32_bf16 v[116:119], a[52:55], v[140:143], v[116:119]
global_load_dwordx4 v[160:163], v18, s[42:43] sc1
v_lshlrev_b32 v96, 16, v148
v_mfma_f32_16x16x32_bf16 v[120:123], a[56:59], v[140:143], v[120:123]
s_sub_u32 s42, s42, 18432
v_and_b32 v97, 0xffff0000, v148
v_lshlrev_b32 v98, 16, v149
v_mfma_f32_16x16x32_bf16 v[124:127], a[60:63], v[140:143], v[124:127]
s_waitcnt lgkmcnt(2)
global_load_lds_dwordx4 v11, s[38:39] offset:-1024 sc1
v_and_b32 v99, 0xffff0000, v149
v_mfma_f32_16x16x32_bf16 v[112:115], a[64:67], v[144:147], v[112:115]
s_subb_u32 s43, s43, 0
ds_read_b128 v[140:143], v25 offset:25600
v_lshlrev_b32 v100, 16, v150
v_mfma_f32_16x16x32_bf16 v[116:119], a[68:71], v[144:147], v[116:119]
v_and_b32 v101, 0xffff0000, v150
v_lshlrev_b32 v102, 16, v151
v_mfma_f32_16x16x32_bf16 v[120:123], a[72:75], v[144:147], v[120:123]
global_load_lds_dwordx4 v11, s[38:39] offset:0 sc1
v_add_u32 v11, 18432, v11
v_mfma_f32_16x16x32_bf16 v[124:127], a[76:79], v[144:147], v[124:127]
s_waitcnt lgkmcnt(2)
v_min_u32 v11, v11, v12
v_and_b32 v103, 0xffff0000, v151
v_mfma_f32_16x16x32_bf16 v[112:115], a[80:83], v[132:135], v[112:115]
ds_read_b128 v[144:147], v25 offset:26624
v_lshlrev_b32 v104, 16, v152
v_mfma_f32_16x16x32_bf16 v[116:119], a[84:87], v[132:135], v[116:119]
v_and_b32 v105, 0xffff0000, v152
v_lshlrev_b32 v106, 16, v153
v_mfma_f32_16x16x32_bf16 v[120:123], a[88:91], v[132:135], v[120:123]
v_and_b32 v107, 0xffff0000, v153
v_lshlrev_b32 v108, 16, v154
v_mfma_f32_16x16x32_bf16 v[124:127], a[92:95], v[132:135], v[124:127]
s_waitcnt lgkmcnt(2)
v_xor_b32 v25, 0x10000, v25
v_and_b32 v109, 0xffff0000, v154
v_mfma_f32_16x16x32_bf16 v[112:115], a[96:99], v[136:139], v[112:115]
v_lshlrev_b32 v110, 16, v155
v_and_b32 v111, 0xffff0000, v155
v_mfma_f32_16x16x32_bf16 v[116:119], a[100:103], v[136:139], v[116:119]
v_mfma_f32_16x16x32_bf16 v[120:123], a[104:107], v[136:139], v[120:123]
s_waitcnt vmcnt(9)
s_barrier
v_mfma_f32_16x16x32_bf16 v[124:127], a[108:111], v[136:139], v[124:127]
s_waitcnt lgkmcnt(1)
ds_read_b128 v[132:135], v25 offset:9216
ds_read_b128 v[184:187], v26 offset:39936
v_mfma_f32_16x16x32_bf16 v[112:115], a[112:115], v[140:143], v[112:115]
ds_read_b128 v[136:139], v25 offset:10240
ds_read_b128 v[188:191], v26 offset:40960
v_mfma_f32_16x16x32_bf16 v[116:119], a[116:119], v[140:143], v[116:119]
ds_read_b128 v[192:195], v26 offset:41984
v_mfma_f32_16x16x32_bf16 v[120:123], a[120:123], v[140:143], v[120:123]
v_mfma_f32_16x16x32_bf16 v[124:127], a[124:127], v[140:143], v[124:127]
s_waitcnt lgkmcnt(5)
v_mfma_f32_16x16x32_bf16 v[112:115], a[128:131], v[144:147], v[112:115]
ds_read_b128 v[140:143], v25 offset:11264
v_mfma_f32_16x16x32_bf16 v[116:119], a[132:135], v[144:147], v[116:119]
v_mfma_f32_16x16x32_bf16 v[120:123], a[136:139], v[144:147], v[120:123]
v_mfma_f32_16x16x32_bf16 v[124:127], a[140:143], v[144:147], v[124:127]
s_sub_u32 s70, s70, 1
s_cbranch_scc0 .Lwo_top14
s_waitcnt vmcnt(6)
s_barrier
s_waitcnt lgkmcnt(4)
v_add_f32 v176, v176, v184
v_add_f32 v177, v177, v185
v_add_f32 v178, v178, v186
v_add_f32 v179, v179, v187
s_waitcnt lgkmcnt(2)
v_add_f32 v176, v176, v188
v_add_f32 v177, v177, v189
v_add_f32 v178, v178, v190
v_add_f32 v179, v179, v191
s_waitcnt lgkmcnt(1)
v_add_f32 v180, v180, v192
v_add_f32 v181, v181, v193
v_add_f32 v182, v182, v194
v_add_f32 v183, v183, v195
v_cvt_pk_bf16_f32 v172, v176, v177
v_cvt_pk_bf16_f32 v173, v178, v179
v_cvt_pk_bf16_f32 v174, v180, v181
v_cvt_pk_bf16_f32 v175, v182, v183
v_cvt_pk_bf16_f32 v164, v112, v113
global_store_dwordx4 v14, v[172:175], s[38:39] sc1
v_cvt_pk_bf16_f32 v165, v114, v115
v_cvt_pk_bf16_f32 v166, v116, v117
v_cvt_pk_bf16_f32 v167, v118, v119
global_store_dwordx4 v13, v[164:167], s[38:39] offset:0 sc1
v_cvt_pk_bf16_f32 v168, v120, v121
v_cvt_pk_bf16_f32 v169, v122, v123
v_cvt_pk_bf16_f32 v170, v124, v125
v_cvt_pk_bf16_f32 v171, v126, v127
global_store_dwordx4 v13, v[168:171], s[38:39] offset:1024 sc1
v_add_f32 v254, v112, v113
v_mul_f32 v255, v112, v112
v_fmac_f32 v255, v113, v113
v_add_f32 v254, v254, v114
v_fmac_f32 v255, v114, v114
v_add_f32 v254, v254, v115
v_fmac_f32 v255, v115, v115
v_add_f32 v254, v254, v116
v_fmac_f32 v255, v116, v116
v_add_f32 v254, v254, v117
v_fmac_f32 v255, v117, v117
v_add_f32 v254, v254, v118
v_fmac_f32 v255, v118, v118
v_add_f32 v254, v254, v119
v_fmac_f32 v255, v119, v119
v_add_f32 v254, v254, v120
v_fmac_f32 v255, v120, v120
v_add_f32 v254, v254, v121
v_fmac_f32 v255, v121, v121
v_add_f32 v254, v254, v122
v_fmac_f32 v255, v122, v122
v_add_f32 v254, v254, v123
v_fmac_f32 v255, v123, v123
v_add_f32 v254, v254, v124
v_fmac_f32 v255, v124, v124
v_add_f32 v254, v254, v125
v_fmac_f32 v255, v125, v125
v_add_f32 v254, v254, v126
v_fmac_f32 v255, v126, v126
v_add_f32 v254, v254, v127
v_fmac_f32 v255, v127, v127
v_add_f32 v254, v254, v176
v_fmac_f32 v255, v176, v176
v_add_f32 v254, v254, v177
v_fmac_f32 v255, v177, v177
v_add_f32 v254, v254, v178
v_fmac_f32 v255, v178, v178
v_add_f32 v254, v254, v179
v_fmac_f32 v255, v179, v179
v_add_f32 v254, v254, v180
v_fmac_f32 v255, v180, v180
v_add_f32 v254, v254, v181
v_fmac_f32 v255, v181, v181
v_add_f32 v254, v254, v182
v_fmac_f32 v255, v182, v182
v_add_f32 v254, v254, v183
v_fmac_f32 v255, v183, v183
ds_write_b64 v27, v[254:255] offset:46592
s_waitcnt vmcnt(0) lgkmcnt(0)
s_barrier
s_barrier
s_endpgm
.Lwo_sp6:
s_sleep 1
global_load_dword v201, v21, s[38:39] sc1
s_add_u32 s71, s71, 1
s_waitcnt vmcnt(0)
v_sub_u32 v201, v201, v22
v_cmp_lt_u32 vcc, 0x7f, v201
s_and_b64 vcc, vcc, s[76:77]
s_cbranch_vccz .Lwo_rt7
s_cmp_lt_u32 s71, 3000000
s_cbranch_scc1 .Lwo_sp6
s_branch .Lwo_rt7
.Lwo_fr8:
s_waitcnt vmcnt(5)
v_mov_b32 v201, v200
v_sub_u32 v201, v201, v22
v_cmp_lt_u32 vcc, 0x7f, v201
s_and_b64 vcc, vcc, s[76:77]
s_cbranch_vccz .Lwo_rt7
s_branch .Lwo_sp6
.Lwo_sp9:
s_sleep 1
global_load_dword v200, v21, s[38:39] sc1
s_add_u32 s71, s71, 1
s_waitcnt vmcnt(0)
v_sub_u32 v200, v200, v22
v_cmp_lt_u32 vcc, 0x7f, v200
s_and_b64 vcc, vcc, s[76:77]
s_cbranch_vccz .Lwo_rt10
s_cmp_lt_u32 s71, 3000000
s_cbranch_scc1 .Lwo_sp9
s_branch .Lwo_rt10
)"
R"(.Lwo_fr11:
s_waitcnt vmcnt(5)
v_mov_b32 v200, v201
v_sub_u32 v200, v200, v22
v_cmp_lt_u32 vcc, 0x7f, v200
s_and_b64 vcc, vcc, s[76:77]
s_cbranch_vccz .Lwo_rt10
s_branch .Lwo_sp9
.Lrole2:
s_sleep 50
s_mov_b32 m0, 0x27f00
s_nop 0
global_load_lds_dword v18, s[40:41]
v_add_u32 v18, 294912, v18
global_load_lds_dword v18, s[40:41]
v_add_u32 v18, 294912, v18
global_load_lds_dword v18, s[40:41]
v_add_u32 v18, 294912, v18
global_load_lds_dword v18, s[40:41]
v_add_u32 v18, 294912, v18
s_waitcnt vmcnt(0)
s_sleep 30
s_add_u32 m0, s44, 4096
s_nop 0
global_load_lds_dwordx4 v0, s[42:43] offset:-4096 nt
global_load_lds_dwordx4 v1, s[42:43] offset:-3072 nt
global_load_lds_dwordx4 v2, s[42:43] offset:-2048 nt
global_load_lds_dwordx4 v3, s[42:43] offset:-1024 nt
global_load_lds_dwordx4 v4, s[42:43] offset:0 nt
global_load_lds_dwordx4 v5, s[42:43] offset:1024 nt
s_add_u32 s42, s42, 73728
s_addc_u32 s43, s43, 0
s_add_u32 m0, s44, 28672
s_nop 0
global_load_lds_dwordx4 v0, s[42:43] offset:-4096 nt
global_load_lds_dwordx4 v1, s[42:43] offset:-3072 nt
global_load_lds_dwordx4 v2, s[42:43] offset:-2048 nt
global_load_lds_dwordx4 v3, s[42:43] offset:-1024 nt
global_load_lds_dwordx4 v4, s[42:43] offset:0 nt
global_load_lds_dwordx4 v5, s[42:43] offset:1024 nt
s_add_u32 s42, s42, 73728
s_addc_u32 s43, s43, 0
s_add_u32 m0, s44, 53248
s_nop 0
global_load_lds_dwordx4 v0, s[42:43] offset:-4096 nt
global_load_lds_dwordx4 v1, s[42:43] offset:-3072 nt
global_load_lds_dwordx4 v2, s[42:43] offset:-2048 nt
global_load_lds_dwordx4 v3, s[42:43] offset:-1024 nt
global_load_lds_dwordx4 v4, s[42:43] offset:0 nt
global_load_lds_dwordx4 v5, s[42:43] offset:1024 nt
s_add_u32 s42, s42, 73728
s_addc_u32 s43, s43, 0
s_add_u32 m0, s44, 77824
s_nop 0
global_load_lds_dwordx4 v0, s[42:43] offset:-4096 nt
global_load_lds_dwordx4 v1, s[42:43] offset:-3072 nt
global_load_lds_dwordx4 v2, s[42:43] offset:-2048 nt
global_load_lds_dwordx4 v3, s[42:43] offset:-1024 nt
global_load_lds_dwordx4 v4, s[42:43] offset:0 nt
global_load_lds_dwordx4 v5, s[42:43] offset:1024 nt
s_add_u32 s42, s42, 73728
s_addc_u32 s43, s43, 0
s_add_u32 m0, s44, 102400
s_nop 0
global_load_lds_dwordx4 v0, s[42:43] offset:-4096 nt
global_load_lds_dwordx4 v1, s[42:43] offset:-3072 nt
global_load_lds_dwordx4 v2, s[42:43] offset:-2048 nt
global_load_lds_dwordx4 v3, s[42:43] offset:-1024 nt
global_load_lds_dwordx4 v4, s[42:43] offset:0 nt
global_load_lds_dwordx4 v5, s[42:43] offset:1024 nt
s_add_u32 s42, s42, 73728
s_addc_u32 s43, s43, 0
v_add_u32 v254, 0x10000, v6
v_add_u32 v255, 0x20000, v6
v_add_u32 v132, 0x10000, v7
v_add_u32 v133, 0x20000, v7
v_add_u32 v134, 0x10000, v8
v_add_u32 v135, 0x20000, v8
s_waitcnt lgkmcnt(0)
s_mul_i32 s37, s37, 0x9E3779B1
s_add_u32 s37, s37, 0x7F4A7C15
v_add_u32 v14, s37, v14
v_add_u32 v16, s37, v16
s_waitcnt vmcnt(24)
s_barrier
s_add_u32 m0, s44, 126976
ds_read_b64_tr_b16 a[0:1], v6 offset:0
ds_read_b64_tr_b16 a[2:3], v6 offset:1536
global_load_lds_dwordx4 v0, s[42:43] offset:-4096 nt
ds_read_b64_tr_b16 a[4:5], v7 offset:0
global_load_lds_dwordx4 v1, s[42:43] offset:-3072 nt
ds_read_b64_tr_b16 a[6:7], v7 offset:1536
global_load_lds_dwordx4 v2, s[42:43] offset:-2048 nt
ds_read_b64_tr_b16 a[8:9], v8 offset:0
ds_read_b64_tr_b16 a[10:11], v8 offset:1536
global_load_lds_dwordx4 v3, s[42:43] offset:-1024 nt
ds_read_b64_tr_b16 a[12:13], v6 offset:12288
global_load_lds_dwordx4 v4, s[42:43] offset:0 nt
ds_read_b64_tr_b16 a[14:15], v6 offset:13824
global_load_lds_dwordx4 v5, s[42:43] offset:1024 nt
ds_read_b64_tr_b16 a[16:17], v7 offset:12288
ds_read_b64_tr_b16 a[18:19], v7 offset:13824
s_add_u32 s42, s42, 73728
ds_read_b64_tr_b16 a[20:21], v8 offset:12288
s_addc_u32 s43, s43, 0
ds_read_b64_tr_b16 a[22:23], v8 offset:13824
s_waitcnt vmcnt(24) lgkmcnt(0)
s_barrier
s_add_u32 m0, s44, 4096
ds_read_b64_tr_b16 a[24:25], v6 offset:24576
ds_read_b64_tr_b16 a[26:27], v6 offset:26112
global_load_lds_dwordx4 v0, s[42:43] offset:-4096 nt
ds_read_b64_tr_b16 a[28:29], v7 offset:24576
global_load_lds_dwordx4 v1, s[42:43] offset:-3072 nt
ds_read_b64_tr_b16 a[30:31], v7 offset:26112
global_load_lds_dwordx4 v2, s[42:43] offset:-2048 nt
ds_read_b64_tr_b16 a[32:33], v8 offset:24576
ds_read_b64_tr_b16 a[34:35], v8 offset:26112
global_load_lds_dwordx4 v3, s[42:43] offset:-1024 nt
ds_read_b64_tr_b16 a[36:37], v6 offset:36864
global_load_lds_dwordx4 v4, s[42:43] offset:0 nt
ds_read_b64_tr_b16 a[38:39], v6 offset:38400
global_load_lds_dwordx4 v5, s[42:43] offset:1024 nt
ds_read_b64_tr_b16 a[40:41], v7 offset:36864
ds_read_b64_tr_b16 a[42:43], v7 offset:38400
s_add_u32 s42, s42, 73728
ds_read_b64_tr_b16 a[44:45], v8 offset:36864
s_addc_u32 s43, s43, 0
ds_read_b64_tr_b16 a[46:47], v8 offset:38400
s_waitcnt vmcnt(24) lgkmcnt(0)
s_barrier
s_add_u32 m0, s44, 28672
ds_read_b64_tr_b16 a[48:49], v6 offset:49152
ds_read_b64_tr_b16 a[50:51], v6 offset:50688
global_load_lds_dwordx4 v0, s[42:43] offset:-4096 nt
ds_read_b64_tr_b16 a[52:53], v7 offset:49152
global_load_lds_dwordx4 v1, s[42:43] offset:-3072 nt
ds_read_b64_tr_b16 a[54:55], v7 offset:50688
global_load_lds_dwordx4 v2, s[42:43] offset:-2048 nt
ds_read_b64_tr_b16 a[56:57], v8 offset:49152
ds_read_b64_tr_b16 a[58:59], v8 offset:50688
global_load_lds_dwordx4 v3, s[42:43] offset:-1024 nt
ds_read_b64_tr_b16 a[60:61], v6 offset:61440
global_load_lds_dwordx4 v4, s[42:43] offset:0 nt
ds_read_b64_tr_b16 a[62:63], v6 offset:62976
global_load_lds_dwordx4 v5, s[42:43] offset:1024 nt
ds_read_b64_tr_b16 a[64:65], v7 offset:61440
ds_read_b64_tr_b16 a[66:67], v7 offset:62976
s_add_u32 s42, s42, 73728
ds_read_b64_tr_b16 a[68:69], v8 offset:61440
s_addc_u32 s43, s43, 0
ds_read_b64_tr_b16 a[70:71], v8 offset:62976
s_waitcnt vmcnt(24) lgkmcnt(0)
s_barrier
s_add_u32 m0, s44, 53248
ds_read_b64_tr_b16 a[72:73], v254 offset:8192
ds_read_b64_tr_b16 a[74:75], v254 offset:9728
global_load_lds_dwordx4 v0, s[42:43] offset:-4096 nt
ds_read_b64_tr_b16 a[76:77], v132 offset:8192
global_load_lds_dwordx4 v1, s[42:43] offset:-3072 nt
ds_read_b64_tr_b16 a[78:79], v132 offset:9728
global_load_lds_dwordx4 v2, s[42:43] offset:-2048 nt
ds_read_b64_tr_b16 a[80:81], v134 offset:8192
ds_read_b64_tr_b16 a[82:83], v134 offset:9728
global_load_lds_dwordx4 v3, s[42:43] offset:-1024 nt
ds_read_b64_tr_b16 a[84:85], v254 offset:20480
global_load_lds_dwordx4 v4, s[42:43] offset:0 nt
ds_read_b64_tr_b16 a[86:87], v254 offset:22016
global_load_lds_dwordx4 v5, s[42:43] offset:1024 nt
ds_read_b64_tr_b16 a[88:89], v132 offset:20480
ds_read_b64_tr_b16 a[90:91], v132 offset:22016
s_add_u32 s42, s42, 73728
ds_read_b64_tr_b16 a[92:93], v134 offset:20480
s_addc_u32 s43, s43, 0
ds_read_b64_tr_b16 a[94:95], v134 offset:22016
s_waitcnt vmcnt(24) lgkmcnt(0)
s_barrier
s_add_u32 m0, s44, 77824
ds_read_b64_tr_b16 a[96:97], v254 offset:32768
ds_read_b64_tr_b16 a[98:99], v254 offset:34304
global_load_lds_dwordx4 v0, s[42:43] offset:-4096 nt
ds_read_b64_tr_b16 a[100:101], v132 offset:32768
global_load_lds_dwordx4 v1, s[42:43] offset:-3072 nt
ds_read_b64_tr_b16 a[102:103], v132 offset:34304
global_load_lds_dwordx4 v2, s[42:43] offset:-2048 nt
ds_read_b64_tr_b16 a[104:105], v134 offset:32768
ds_read_b64_tr_b16 a[106:107], v134 offset:34304
global_load_lds_dwordx4 v3, s[42:43] offset:-1024 nt
ds_read_b64_tr_b16 a[108:109], v254 offset:45056
global_load_lds_dwordx4 v4, s[42:43] offset:0 nt
ds_read_b64_tr_b16 a[110:111], v254 offset:46592
global_load_lds_dwordx4 v5, s[42:43] offset:1024 nt
ds_read_b64_tr_b16 a[112:113], v132 offset:45056
ds_read_b64_tr_b16 a[114:115], v132 offset:46592
s_add_u32 s42, s42, 73728
ds_read_b64_tr_b16 a[116:117], v134 offset:45056
s_addc_u32 s43, s43, 0
ds_read_b64_tr_b16 a[118:119], v134 offset:46592
s_waitcnt vmcnt(24) lgkmcnt(0)
s_barrier
s_add_u32 m0, s44, 102400
ds_read_b64_tr_b16 a[120:121], v254 offset:57344
ds_read_b64_tr_b16 a[122:123], v254 offset:58880
global_load_lds_dwordx4 v0, s[42:43] offset:-4096 nt
ds_read_b64_tr_b16 a[124:125], v132 offset:57344
global_load_lds_dwordx4 v1, s[42:43] offset:-3072 nt
ds_read_b64_tr_b16 a[126:127], v132 offset:58880
global_load_lds_dwordx4 v2, s[42:43] offset:-2048 nt
ds_read_b64_tr_b16 a[128:129], v134 offset:57344
ds_read_b64_tr_b16 a[130:131], v134 offset:58880
global_load_lds_dwordx4 v3, s[42:43] offset:-1024 nt
ds_read_b64_tr_b16 a[132:133], v255 offset:4096
global_load_lds_dwordx4 v4, s[42:43] offset:0 nt
ds_read_b64_tr_b16 a[134:135], v255 offset:5632
global_load_lds_dwordx4 v5, s[42:43] offset:1024 nt
ds_read_b64_tr_b16 a[136:137], v133 offset:4096
ds_read_b64_tr_b16 a[138:139], v133 offset:5632
s_add_u32 s42, s42, 73728
ds_read_b64_tr_b16 a[140:141], v135 offset:4096
s_addc_u32 s43, s43, 0
ds_read_b64_tr_b16 a[142:143], v135 offset:5632
s_waitcnt vmcnt(24) lgkmcnt(0)
s_barrier
s_add_u32 m0, s44, 126976
ds_read_b64_tr_b16 a[144:145], v6 offset:0
ds_read_b64_tr_b16 a[146:147], v6 offset:1536
global_load_lds_dwordx4 v0, s[42:43] offset:-4096 nt
ds_read_b64_tr_b16 a[148:149], v7 offset:0
global_load_lds_dwordx4 v1, s[42:43] offset:-3072 nt
ds_read_b64_tr_b16 a[150:151], v7 offset:1536
global_load_lds_dwordx4 v2, s[42:43] offset:-2048 nt
ds_read_b64_tr_b16 a[152:153], v8 offset:0
ds_read_b64_tr_b16 a[154:155], v8 offset:1536
global_load_lds_dwordx4 v3, s[42:43] offset:-1024 nt
ds_read_b64_tr_b16 a[156:157], v6 offset:12288
global_load_lds_dwordx4 v4, s[42:43] offset:0 nt
ds_read_b64_tr_b16 a[158:159], v6 offset:13824
global_load_lds_dwordx4 v5, s[42:43] offset:1024 nt
ds_read_b64_tr_b16 a[160:161], v7 offset:12288
ds_read_b64_tr_b16 a[162:163], v7 offset:13824
s_sub_u32 s42, s42, 811008
ds_read_b64_tr_b16 a[164:165], v8 offset:12288
s_subb_u32 s43, s43, 0
ds_read_b64_tr_b16 a[166:167], v8 offset:13824
s_waitcnt vmcnt(24) lgkmcnt(0)
s_barrier
ds_read_b64_tr_b16 a[168:169], v6 offset:24576
ds_read_b64_tr_b16 a[170:171], v6 offset:26112
ds_read_b64_tr_b16 a[172:173], v7 offset:24576
ds_read_b64_tr_b16 a[174:175], v7 offset:26112
ds_read_b64_tr_b16 a[176:177], v8 offset:24576
ds_read_b64_tr_b16 a[178:179], v8 offset:26112
ds_read_b64_tr_b16 a[180:181], v6 offset:36864
ds_read_b64_tr_b16 a[182:183], v6 offset:38400
ds_read_b64_tr_b16 a[184:185], v7 offset:36864
ds_read_b64_tr_b16 a[186:187], v7 offset:38400
ds_read_b64_tr_b16 a[188:189], v8 offset:36864
ds_read_b64_tr_b16 a[190:191], v8 offset:38400
s_waitcnt vmcnt(18) lgkmcnt(0)
s_barrier
ds_read_b64_tr_b16 a[192:193], v6 offset:49152
ds_read_b64_tr_b16 a[194:195], v6 offset:50688
ds_read_b64_tr_b16 a[196:197], v7 offset:49152
ds_read_b64_tr_b16 a[198:199], v7 offset:50688
ds_read_b64_tr_b16 a[200:201], v8 offset:49152
ds_read_b64_tr_b16 a[202:203], v8 offset:50688
ds_read_b64_tr_b16 a[204:205], v6 offset:61440
ds_read_b64_tr_b16 a[206:207], v6 offset:62976
ds_read_b64_tr_b16 a[208:209], v7 offset:61440
ds_read_b64_tr_b16 a[210:211], v7 offset:62976
ds_read_b64_tr_b16 a[212:213], v8 offset:61440
ds_read_b64_tr_b16 a[214:215], v8 offset:62976
s_waitcnt vmcnt(12) lgkmcnt(0)
s_barrier
ds_read_b64_tr_b16 a[216:217], v254 offset:8192
ds_read_b64_tr_b16 a[218:219], v254 offset:9728
ds_read_b64_tr_b16 a[220:221], v132 offset:8192
ds_read_b64_tr_b16 a[222:223], v132 offset:9728
ds_read_b64_tr_b16 a[224:225], v134 offset:8192
ds_read_b64_tr_b16 a[226:227], v134 offset:9728
ds_read_b64_tr_b16 a[228:229], v254 offset:20480
ds_read_b64_tr_b16 a[230:231], v254 offset:22016
ds_read_b64_tr_b16 a[232:233], v132 offset:20480
ds_read_b64_tr_b16 a[234:235], v132 offset:22016
ds_read_b64_tr_b16 a[236:237], v134 offset:20480
ds_read_b64_tr_b16 a[238:239], v134 offset:22016
s_waitcnt vmcnt(6) lgkmcnt(0)
s_barrier
ds_read_b64_tr_b16 a[240:241], v254 offset:32768
ds_read_b64_tr_b16 a[242:243], v254 offset:34304
ds_read_b64_tr_b16 a[244:245], v132 offset:32768
ds_read_b64_tr_b16 a[246:247], v132 offset:34304
ds_read_b64_tr_b16 a[248:249], v134 offset:32768
ds_read_b64_tr_b16 a[250:251], v134 offset:34304
ds_read_b64_tr_b16 a[252:253], v254 offset:45056
ds_read_b64_tr_b16 a[254:255], v254 offset:46592
ds_read_b64_tr_b16 v[20:21], v132 offset:45056
ds_read_b64_tr_b16 v[22:23], v132 offset:46592
ds_read_b64_tr_b16 v[24:25], v134 offset:45056
ds_read_b64_tr_b16 v[26:27], v134 offset:46592
s_waitcnt vmcnt(0) lgkmcnt(0)
s_barrier
ds_read_b64_tr_b16 v[28:29], v254 offset:57344
ds_read_b64_tr_b16 v[30:31], v254 offset:58880
ds_read_b64_tr_b16 v[32:33], v132 offset:57344
ds_read_b64_tr_b16 v[34:35], v132 offset:58880
ds_read_b64_tr_b16 v[36:37], v134 offset:57344
ds_read_b64_tr_b16 v[38:39], v134 offset:58880
ds_read_b64_tr_b16 v[40:41], v255 offset:4096
ds_read_b64_tr_b16 v[42:43], v255 offset:5632
ds_read_b64_tr_b16 v[44:45], v133 offset:4096
ds_read_b64_tr_b16 v[46:47], v133 offset:5632
ds_read_b64_tr_b16 v[48:49], v135 offset:4096
ds_read_b64_tr_b16 v[50:51], v135 offset:5632
s_waitcnt lgkmcnt(0)
s_barrier
s_and_b32 s75, s36, 3
s_cmp_eq_u32 s75, 1
s_cbranch_scc1 .Ldn_w2
s_cmp_eq_u32 s75, 2
s_cbranch_scc1 .Ldn_w3
s_cmp_eq_u32 s75, 3
s_cbranch_scc1 .Ldn_w4
.Ldn_w1:
v_add_u32 v19, -1, v14
s_mov_b32 s76, 0xffff
s_mov_b32 s77, 0xffff
s_mov_b32 s70, 32
s_waitcnt vmcnt(0) lgkmcnt(0)
s_nop 7
.p2align 6
.Ldn_top5:
s_waitcnt lgkmcnt(5)
v_mfma_f32_16x16x32_bf16 v[52:55], a[0:3], v[76:79], 0
s_waitcnt lgkmcnt(4)
v_mfma_f32_16x16x32_bf16 v[52:55], a[12:15], v[80:83], v[52:55]
s_waitcnt lgkmcnt(3)
v_mfma_f32_16x16x32_bf16 v[52:55], a[24:27], v[84:87], v[52:55]
s_waitcnt lgkmcnt(2)
v_mfma_f32_16x16x32_bf16 v[52:55], a[36:39], v[88:91], v[52:55]
s_waitcnt lgkmcnt(1)
ds_read_b128 v[100:103], v17 offset:6144
v_cvt_pk_bf16_f32 v128, v72, v73
v_mfma_f32_16x16x32_bf16 v[52:55], a[48:51], v[92:95], v[52:55]
s_waitcnt lgkmcnt(1)
v_mfma_f32_16x16x32_bf16 v[52:55], a[60:63], v[96:99], v[52:55]
s_waitcnt vmcnt(6)
s_barrier
v_mfma_f32_16x16x32_bf16 v[56:59], a[4:7], v[76:79], 0
ds_read_b128 v[104:107], v17 offset:7168
v_cvt_pk_bf16_f32 v130, v72, v73
v_mfma_f32_16x16x32_bf16 v[56:59], a[16:19], v[80:83], v[56:59]
global_store_dwordx4 v11, v[124:127], s[38:39] sc1
v_mfma_f32_16x16x32_bf16 v[56:59], a[28:31], v[84:87], v[56:59]
ds_read_b128 v[108:111], v17 offset:8192
v_add_u32 v11, 110592, v11
v_mfma_f32_16x16x32_bf16 v[56:59], a[40:43], v[88:91], v[56:59]
v_cvt_pk_bf16_f32 v129, v74, v75
v_cvt_pk_bf16_f32 v131, v74, v75
v_mfma_f32_16x16x32_bf16 v[56:59], a[52:55], v[92:95], v[56:59]
ds_read_b128 v[112:115], v17 offset:9216
v_mfma_f32_16x16x32_bf16 v[56:59], a[64:67], v[96:99], v[56:59]
v_permlane16_swap_b32 v128, v130
v_mfma_f32_16x16x32_bf16 v[60:63], a[8:11], v[76:79], 0
ds_read_b128 v[116:119], v17 offset:10240
v_mfma_f32_16x16x32_bf16 v[60:63], a[20:23], v[80:83], v[60:63]
v_permlane16_swap_b32 v129, v131
v_mfma_f32_16x16x32_bf16 v[60:63], a[32:35], v[84:87], v[60:63]
ds_read_b128 v[120:123], v17 offset:11264
v_mfma_f32_16x16x32_bf16 v[60:63], a[44:47], v[88:91], v[60:63]
s_mov_b64 exec, s[76:77]
global_store_dwordx4 v12, v[128:131], s[38:39] sc1
s_mov_b64 exec, -1
v_mfma_f32_16x16x32_bf16 v[60:63], a[56:59], v[92:95], v[60:63]
v_add_u32 v12, 110592, v12
v_mfma_f32_16x16x32_bf16 v[60:63], a[68:71], v[96:99], v[60:63]
s_waitcnt lgkmcnt(5)
v_mfma_f32_16x16x32_bf16 v[52:55], a[72:75], v[100:103], v[52:55]
s_waitcnt lgkmcnt(4)
v_mfma_f32_16x16x32_bf16 v[52:55], a[84:87], v[104:107], v[52:55]
s_waitcnt lgkmcnt(3)
v_mfma_f32_16x16x32_bf16 v[52:55], a[96:99], v[108:111], v[52:55]
s_waitcnt lgkmcnt(2)
v_mfma_f32_16x16x32_bf16 v[52:55], a[108:111], v[112:115], v[52:55]
s_waitcnt lgkmcnt(1)
v_mfma_f32_16x16x32_bf16 v[52:55], a[120:123], v[116:119], v[52:55]
s_waitcnt lgkmcnt(0)
ds_read_b128 v[76:79], v17 offset:12288
v_mfma_f32_16x16x32_bf16 v[52:55], a[132:135], v[120:123], v[52:55]
v_mfma_f32_16x16x32_bf16 v[56:59], a[76:79], v[100:103], v[56:59]
ds_read_b128 v[80:83], v17 offset:13312
v_mfma_f32_16x16x32_bf16 v[56:59], a[88:91], v[104:107], v[56:59]
v_mfma_f32_16x16x32_bf16 v[56:59], a[100:103], v[108:111], v[56:59]
ds_read_b128 v[84:87], v17 offset:14336
v_mfma_f32_16x16x32_bf16 v[56:59], a[112:115], v[112:115], v[56:59]
v_mfma_f32_16x16x32_bf16 v[56:59], a[124:127], v[116:119], v[56:59]
ds_read_b128 v[88:91], v17 offset:15360
v_mfma_f32_16x16x32_bf16 v[56:59], a[136:139], v[120:123], v[56:59]
v_mfma_f32_16x16x32_bf16 v[60:63], a[80:83], v[100:103], v[60:63]
ds_read_b128 v[92:95], v17 offset:16384
)"
R"(v_mfma_f32_16x16x32_bf16 v[60:63], a[92:95], v[104:107], v[60:63]
v_mfma_f32_16x16x32_bf16 v[60:63], a[104:107], v[108:111], v[60:63]
ds_read_b128 v[96:99], v17 offset:17408
v_mfma_f32_16x16x32_bf16 v[60:63], a[116:119], v[112:115], v[60:63]
v_mfma_f32_16x16x32_bf16 v[60:63], a[128:131], v[116:119], v[60:63]
v_mfma_f32_16x16x32_bf16 v[60:63], a[140:143], v[120:123], v[60:63]
s_waitcnt lgkmcnt(5)
v_mfma_f32_16x16x32_bf16 v[52:55], a[144:147], v[76:79], v[52:55]
s_waitcnt lgkmcnt(4)
v_mfma_f32_16x16x32_bf16 v[52:55], a[156:159], v[80:83], v[52:55]
s_waitcnt lgkmcnt(3)
v_mfma_f32_16x16x32_bf16 v[52:55], a[168:171], v[84:87], v[52:55]
s_waitcnt lgkmcnt(2)
v_mfma_f32_16x16x32_bf16 v[52:55], a[180:183], v[88:91], v[52:55]
s_waitcnt lgkmcnt(1)
v_mfma_f32_16x16x32_bf16 v[52:55], a[192:195], v[92:95], v[52:55]
s_xor_b32 s45, s45, 0x10000
ds_read_b128 v[100:103], v17 offset:18432
s_waitcnt lgkmcnt(1)
v_mfma_f32_16x16x32_bf16 v[52:55], a[204:207], v[96:99], v[52:55]
s_add_u32 m0, s45, 0
v_mfma_f32_16x16x32_bf16 v[56:59], a[148:151], v[76:79], v[56:59]
ds_read_b128 v[104:107], v17 offset:19456
ds_read_b128 v[108:111], v17 offset:20480
v_mfma_f32_16x16x32_bf16 v[56:59], a[160:163], v[80:83], v[56:59]
v_mfma_f32_16x16x32_bf16 v[56:59], a[172:175], v[84:87], v[56:59]
s_barrier
v_mfma_f32_16x16x32_bf16 v[56:59], a[184:187], v[88:91], v[56:59]
global_load_lds_dwordx4 v9, s[38:39] offset:-2048 sc1
v_mfma_f32_16x16x32_bf16 v[56:59], a[196:199], v[92:95], v[56:59]
ds_read_b128 v[112:115], v17 offset:21504
v_mfma_f32_16x16x32_bf16 v[56:59], a[208:211], v[96:99], v[56:59]
v_mfma_f32_16x16x32_bf16 v[60:63], a[152:155], v[76:79], v[60:63]
ds_read_b128 v[116:119], v17 offset:22528
global_load_lds_dwordx4 v9, s[38:39] offset:-1024 sc1
v_mfma_f32_16x16x32_bf16 v[60:63], a[164:167], v[80:83], v[60:63]
v_mfma_f32_16x16x32_bf16 v[60:63], a[176:179], v[84:87], v[60:63]
ds_read_b128 v[120:123], v17 offset:23552
v_mfma_f32_16x16x32_bf16 v[60:63], a[188:191], v[88:91], v[60:63]
global_load_lds_dwordx4 v9, s[38:39] offset:0 sc1
v_mfma_f32_16x16x32_bf16 v[60:63], a[200:203], v[92:95], v[60:63]
v_mfma_f32_16x16x32_bf16 v[60:63], a[212:215], v[96:99], v[60:63]
s_waitcnt lgkmcnt(5)
v_mfma_f32_16x16x32_bf16 v[52:55], a[216:219], v[100:103], v[52:55]
s_waitcnt lgkmcnt(4)
global_load_lds_dwordx4 v9, s[38:39] offset:1024 sc1
v_mfma_f32_16x16x32_bf16 v[52:55], a[228:231], v[104:107], v[52:55]
s_waitcnt lgkmcnt(3)
v_mfma_f32_16x16x32_bf16 v[52:55], a[240:243], v[108:111], v[52:55]
s_waitcnt lgkmcnt(2)
v_mfma_f32_16x16x32_bf16 v[52:55], a[252:255], v[112:115], v[52:55]
s_waitcnt lgkmcnt(1)
global_load_lds_dwordx4 v9, s[38:39] offset:2048 sc1
v_mfma_f32_16x16x32_bf16 v[52:55], v[28:31], v[116:119], v[52:55]
s_waitcnt lgkmcnt(0)
v_mfma_f32_16x16x32_bf16 v[52:55], v[40:43], v[120:123], v[52:55]
v_mfma_f32_16x16x32_bf16 v[56:59], a[220:223], v[100:103], v[56:59]
global_load_lds_dwordx4 v9, s[38:39] offset:3072 sc1
v_add_u32 v9, 147456, v9
v_mfma_f32_16x16x32_bf16 v[56:59], a[232:235], v[104:107], v[56:59]
v_mfma_f32_16x16x32_bf16 v[56:59], a[244:247], v[108:111], v[56:59]
s_waitcnt vmcnt(8)
s_barrier
v_mfma_f32_16x16x32_bf16 v[56:59], v[20:23], v[112:115], v[56:59]
ds_read_b128 v[76:79], v17 offset:24576
v_min_u32 v9, v9, v10
v_mfma_f32_16x16x32_bf16 v[56:59], v[32:35], v[116:119], v[56:59]
ds_read_b128 v[80:83], v17 offset:25600
v_cvt_pk_bf16_f32 v124, v52, v53
v_mfma_f32_16x16x32_bf16 v[56:59], v[44:47], v[120:123], v[56:59]
ds_read_b128 v[84:87], v17 offset:26624
v_cvt_pk_bf16_f32 v125, v54, v55
v_mfma_f32_16x16x32_bf16 v[60:63], a[224:227], v[100:103], v[60:63]
ds_read_b128 v[88:91], v17 offset:27648
v_mfma_f32_16x16x32_bf16 v[60:63], a[236:239], v[104:107], v[60:63]
ds_read_b128 v[92:95], v17 offset:28672
v_mfma_f32_16x16x32_bf16 v[60:63], a[248:251], v[108:111], v[60:63]
ds_read_b128 v[96:99], v17 offset:29696
v_mfma_f32_16x16x32_bf16 v[60:63], v[24:27], v[112:115], v[60:63]
v_cvt_pk_bf16_f32 v126, v56, v57
v_cvt_pk_bf16_f32 v127, v58, v59
v_mfma_f32_16x16x32_bf16 v[60:63], v[36:39], v[116:119], v[60:63]
v_mfma_f32_16x16x32_bf16 v[60:63], v[48:51], v[120:123], v[60:63]
s_waitcnt lgkmcnt(5)
v_mfma_f32_16x16x32_bf16 v[64:67], a[0:3], v[76:79], 0
s_waitcnt lgkmcnt(4)
v_mfma_f32_16x16x32_bf16 v[64:67], a[12:15], v[80:83], v[64:67]
s_waitcnt lgkmcnt(3)
v_mfma_f32_16x16x32_bf16 v[64:67], a[24:27], v[84:87], v[64:67]
s_waitcnt lgkmcnt(2)
v_mfma_f32_16x16x32_bf16 v[64:67], a[36:39], v[88:91], v[64:67]
s_waitcnt lgkmcnt(1)
ds_read_b128 v[100:103], v17 offset:30720
v_cvt_pk_bf16_f32 v128, v60, v61
v_mfma_f32_16x16x32_bf16 v[64:67], a[48:51], v[92:95], v[64:67]
s_waitcnt lgkmcnt(1)
v_mfma_f32_16x16x32_bf16 v[64:67], a[60:63], v[96:99], v[64:67]
s_waitcnt vmcnt(6)
s_barrier
v_mfma_f32_16x16x32_bf16 v[68:71], a[4:7], v[76:79], 0
ds_read_b128 v[104:107], v17 offset:31744
v_cvt_pk_bf16_f32 v130, v60, v61
v_mfma_f32_16x16x32_bf16 v[68:71], a[16:19], v[80:83], v[68:71]
global_store_dwordx4 v11, v[124:127], s[38:39] sc1
v_mfma_f32_16x16x32_bf16 v[68:71], a[28:31], v[84:87], v[68:71]
ds_read_b128 v[108:111], v17 offset:32768
v_add_u32 v11, 110592, v11
v_mfma_f32_16x16x32_bf16 v[68:71], a[40:43], v[88:91], v[68:71]
v_cvt_pk_bf16_f32 v129, v62, v63
v_cvt_pk_bf16_f32 v131, v62, v63
v_mfma_f32_16x16x32_bf16 v[68:71], a[52:55], v[92:95], v[68:71]
ds_read_b128 v[112:115], v17 offset:33792
v_mfma_f32_16x16x32_bf16 v[68:71], a[64:67], v[96:99], v[68:71]
v_permlane16_swap_b32 v128, v130
v_mfma_f32_16x16x32_bf16 v[72:75], a[8:11], v[76:79], 0
ds_read_b128 v[116:119], v17 offset:34816
v_mfma_f32_16x16x32_bf16 v[72:75], a[20:23], v[80:83], v[72:75]
v_permlane16_swap_b32 v129, v131
v_mfma_f32_16x16x32_bf16 v[72:75], a[32:35], v[84:87], v[72:75]
ds_read_b128 v[120:123], v17 offset:35840
v_mfma_f32_16x16x32_bf16 v[72:75], a[44:47], v[88:91], v[72:75]
s_mov_b64 exec, s[76:77]
global_store_dwordx4 v12, v[128:131], s[38:39] sc1
s_mov_b64 exec, -1
v_mfma_f32_16x16x32_bf16 v[72:75], a[56:59], v[92:95], v[72:75]
v_add_u32 v12, 110592, v12
v_mfma_f32_16x16x32_bf16 v[72:75], a[68:71], v[96:99], v[72:75]
s_waitcnt lgkmcnt(5)
v_mfma_f32_16x16x32_bf16 v[64:67], a[72:75], v[100:103], v[64:67]
s_waitcnt lgkmcnt(4)
v_mfma_f32_16x16x32_bf16 v[64:67], a[84:87], v[104:107], v[64:67]
s_waitcnt lgkmcnt(3)
v_mfma_f32_16x16x32_bf16 v[64:67], a[96:99], v[108:111], v[64:67]
s_waitcnt lgkmcnt(2)
v_mfma_f32_16x16x32_bf16 v[64:67], a[108:111], v[112:115], v[64:67]
s_waitcnt lgkmcnt(1)
v_mfma_f32_16x16x32_bf16 v[64:67], a[120:123], v[116:119], v[64:67]
s_waitcnt lgkmcnt(0)
ds_read_b128 v[76:79], v17 offset:36864
v_mfma_f32_16x16x32_bf16 v[64:67], a[132:135], v[120:123], v[64:67]
v_mfma_f32_16x16x32_bf16 v[68:71], a[76:79], v[100:103], v[68:71]
ds_read_b128 v[80:83], v17 offset:37888
v_mfma_f32_16x16x32_bf16 v[68:71], a[88:91], v[104:107], v[68:71]
v_mfma_f32_16x16x32_bf16 v[68:71], a[100:103], v[108:111], v[68:71]
ds_read_b128 v[84:87], v17 offset:38912
v_mfma_f32_16x16x32_bf16 v[68:71], a[112:115], v[112:115], v[68:71]
v_mfma_f32_16x16x32_bf16 v[68:71], a[124:127], v[116:119], v[68:71]
ds_read_b128 v[88:91], v17 offset:39936
v_mfma_f32_16x16x32_bf16 v[68:71], a[136:139], v[120:123], v[68:71]
v_mfma_f32_16x16x32_bf16 v[72:75], a[80:83], v[100:103], v[72:75]
ds_read_b128 v[92:95], v17 offset:40960
v_mfma_f32_16x16x32_bf16 v[72:75], a[92:95], v[104:107], v[72:75]
v_mfma_f32_16x16x32_bf16 v[72:75], a[104:107], v[108:111], v[72:75]
ds_read_b128 v[96:99], v17 offset:41984
v_mfma_f32_16x16x32_bf16 v[72:75], a[116:119], v[112:115], v[72:75]
v_mfma_f32_16x16x32_bf16 v[72:75], a[128:131], v[116:119], v[72:75]
v_mfma_f32_16x16x32_bf16 v[72:75], a[140:143], v[120:123], v[72:75]
s_waitcnt lgkmcnt(5)
v_mfma_f32_16x16x32_bf16 v[64:67], a[144:147], v[76:79], v[64:67]
s_waitcnt lgkmcnt(4)
v_mfma_f32_16x16x32_bf16 v[64:67], a[156:159], v[80:83], v[64:67]
s_waitcnt lgkmcnt(3)
v_mfma_f32_16x16x32_bf16 v[64:67], a[168:171], v[84:87], v[64:67]
s_waitcnt lgkmcnt(2)
v_mfma_f32_16x16x32_bf16 v[64:67], a[180:183], v[88:91], v[64:67]
s_waitcnt lgkmcnt(1)
v_mfma_f32_16x16x32_bf16 v[64:67], a[192:195], v[92:95], v[64:67]
s_waitcnt lgkmcnt(0)
ds_read_b128 v[100:103], v17 offset:43008
v_mfma_f32_16x16x32_bf16 v[64:67], a[204:207], v[96:99], v[64:67]
s_add_u32 m0, s45, 24576
v_mfma_f32_16x16x32_bf16 v[68:71], a[148:151], v[76:79], v[68:71]
ds_read_b128 v[104:107], v17 offset:44032
ds_read_b128 v[108:111], v17 offset:45056
v_mfma_f32_16x16x32_bf16 v[68:71], a[160:163], v[80:83], v[68:71]
v_mfma_f32_16x16x32_bf16 v[68:71], a[172:175], v[84:87], v[68:71]
s_barrier
v_mfma_f32_16x16x32_bf16 v[68:71], a[184:187], v[88:91], v[68:71]
global_load_lds_dwordx4 v9, s[38:39] offset:-2048 sc1
v_mfma_f32_16x16x32_bf16 v[68:71], a[196:199], v[92:95], v[68:71]
ds_read_b128 v[112:115], v17 offset:46080
v_mfma_f32_16x16x32_bf16 v[68:71], a[208:211], v[96:99], v[68:71]
v_mfma_f32_16x16x32_bf16 v[72:75], a[152:155], v[76:79], v[72:75]
ds_read_b128 v[116:119], v17 offset:47104
global_load_lds_dwordx4 v9, s[38:39] offset:-1024 sc1
v_mfma_f32_16x16x32_bf16 v[72:75], a[164:167], v[80:83], v[72:75]
v_mfma_f32_16x16x32_bf16 v[72:75], a[176:179], v[84:87], v[72:75]
ds_read_b128 v[120:123], v17 offset:48128
v_mfma_f32_16x16x32_bf16 v[72:75], a[188:191], v[88:91], v[72:75]
global_load_lds_dwordx4 v9, s[38:39] offset:0 sc1
v_mfma_f32_16x16x32_bf16 v[72:75], a[200:203], v[92:95], v[72:75]
v_mfma_f32_16x16x32_bf16 v[72:75], a[212:215], v[96:99], v[72:75]
s_waitcnt lgkmcnt(5)
v_mfma_f32_16x16x32_bf16 v[64:67], a[216:219], v[100:103], v[64:67]
s_waitcnt lgkmcnt(4)
global_load_lds_dwordx4 v9, s[38:39] offset:1024 sc1
v_mfma_f32_16x16x32_bf16 v[64:67], a[228:231], v[104:107], v[64:67]
s_waitcnt lgkmcnt(3)
v_mfma_f32_16x16x32_bf16 v[64:67], a[240:243], v[108:111], v[64:67]
s_waitcnt lgkmcnt(2)
v_mfma_f32_16x16x32_bf16 v[64:67], a[252:255], v[112:115], v[64:67]
s_waitcnt lgkmcnt(1)
global_load_lds_dwordx4 v9, s[38:39] offset:2048 sc1
v_mfma_f32_16x16x32_bf16 v[64:67], v[28:31], v[116:119], v[64:67]
s_waitcnt lgkmcnt(0)
v_mfma_f32_16x16x32_bf16 v[64:67], v[40:43], v[120:123], v[64:67]
v_mfma_f32_16x16x32_bf16 v[68:71], a[220:223], v[100:103], v[68:71]
v_xor_b32 v17, 0x10000, v17
global_load_lds_dwordx4 v9, s[38:39] offset:3072 sc1
v_mfma_f32_16x16x32_bf16 v[68:71], a[232:235], v[104:107], v[68:71]
v_mfma_f32_16x16x32_bf16 v[68:71], a[244:247], v[108:111], v[68:71]
s_waitcnt vmcnt(8)
s_barrier
v_mfma_f32_16x16x32_bf16 v[68:71], v[20:23], v[112:115], v[68:71]
ds_read_b128 v[76:79], v17 offset:0
v_add_u32 v9, 147456, v9
v_mfma_f32_16x16x32_bf16 v[68:71], v[32:35], v[116:119], v[68:71]
ds_read_b128 v[80:83], v17 offset:1024
v_min_u32 v9, v9, v10
v_mfma_f32_16x16x32_bf16 v[68:71], v[44:47], v[120:123], v[68:71]
ds_read_b128 v[84:87], v17 offset:2048
v_cvt_pk_bf16_f32 v124, v64, v65
v_mfma_f32_16x16x32_bf16 v[72:75], a[224:227], v[100:103], v[72:75]
ds_read_b128 v[88:91], v17 offset:3072
v_cvt_pk_bf16_f32 v125, v66, v67
v_mfma_f32_16x16x32_bf16 v[72:75], a[236:239], v[104:107], v[72:75]
ds_read_b128 v[92:95], v17 offset:4096
v_mfma_f32_16x16x32_bf16 v[72:75], a[248:251], v[108:111], v[72:75]
ds_read_b128 v[96:99], v17 offset:5120
v_mfma_f32_16x16x32_bf16 v[72:75], v[24:27], v[112:115], v[72:75]
v_cvt_pk_bf16_f32 v126, v68, v69
v_cvt_pk_bf16_f32 v127, v70, v71
v_mfma_f32_16x16x32_bf16 v[72:75], v[36:39], v[116:119], v[72:75]
v_mfma_f32_16x16x32_bf16 v[72:75], v[48:51], v[120:123], v[72:75]
s_sub_u32 s70, s70, 1
s_cbranch_scc0 .Ldn_top5
v_cvt_pk_bf16_f32 v124, v64, v65
v_cvt_pk_bf16_f32 v125, v66, v67
v_cvt_pk_bf16_f32 v126, v68, v69
v_cvt_pk_bf16_f32 v127, v70, v71
global_store_dwordx4 v11, v[124:127], s[38:39] sc1
v_add_u32 v11, 110592, v11
v_cvt_pk_bf16_f32 v128, v72, v73
v_cvt_pk_bf16_f32 v130, v72, v73
v_cvt_pk_bf16_f32 v129, v74, v75
v_cvt_pk_bf16_f32 v131, v74, v75
v_permlane16_swap_b32 v128, v130
s_nop 0
v_permlane16_swap_b32 v129, v131
s_mov_b64 exec, s[76:77]
global_store_dwordx4 v12, v[128:131], s[38:39] sc1
s_mov_b64 exec, -1
v_add_u32 v12, 110592, v12
s_waitcnt vmcnt(2)
s_barrier
s_waitcnt vmcnt(0)
s_barrier
s_endpgm
.Ldn_w2:
v_add_u32 v19, -1, v14
s_mov_b32 s76, 0xffff
s_mov_b32 s77, 0xffff
s_mov_b32 s70, 32
s_waitcnt vmcnt(0) lgkmcnt(0)
s_nop 7
.p2align 6
.Ldn_top6:
s_waitcnt lgkmcnt(5)
v_mfma_f32_16x16x32_bf16 v[52:55], a[0:3], v[76:79], 0
s_waitcnt lgkmcnt(4)
v_mfma_f32_16x16x32_bf16 v[52:55], a[12:15], v[80:83], v[52:55]
s_waitcnt lgkmcnt(3)
v_mfma_f32_16x16x32_bf16 v[52:55], a[24:27], v[84:87], v[52:55]
s_waitcnt lgkmcnt(2)
v_mfma_f32_16x16x32_bf16 v[52:55], a[36:39], v[88:91], v[52:55]
s_waitcnt lgkmcnt(1)
ds_read_b128 v[100:103], v17 offset:6144
v_cvt_pk_bf16_f32 v128, v72, v73
v_mfma_f32_16x16x32_bf16 v[52:55], a[48:51], v[92:95], v[52:55]
s_waitcnt lgkmcnt(1)
v_mfma_f32_16x16x32_bf16 v[52:55], a[60:63], v[96:99], v[52:55]
s_waitcnt vmcnt(6)
s_barrier
v_mfma_f32_16x16x32_bf16 v[56:59], a[4:7], v[76:79], 0
ds_read_b128 v[104:107], v17 offset:7168
v_cvt_pk_bf16_f32 v130, v72, v73
v_mfma_f32_16x16x32_bf16 v[56:59], a[16:19], v[80:83], v[56:59]
v_cvt_pk_bf16_f32 v129, v74, v75
v_cvt_pk_bf16_f32 v131, v74, v75
v_mfma_f32_16x16x32_bf16 v[56:59], a[28:31], v[84:87], v[56:59]
ds_read_b128 v[108:111], v17 offset:8192
v_mfma_f32_16x16x32_bf16 v[56:59], a[40:43], v[88:91], v[56:59]
global_store_dwordx4 v11, v[124:127], s[38:39] sc1
v_mfma_f32_16x16x32_bf16 v[56:59], a[52:55], v[92:95], v[56:59]
ds_read_b128 v[112:115], v17 offset:9216
v_add_u32 v11, 110592, v11
v_mfma_f32_16x16x32_bf16 v[56:59], a[64:67], v[96:99], v[56:59]
v_permlane16_swap_b32 v128, v130
v_mfma_f32_16x16x32_bf16 v[60:63], a[8:11], v[76:79], 0
ds_read_b128 v[116:119], v17 offset:10240
v_mfma_f32_16x16x32_bf16 v[60:63], a[20:23], v[80:83], v[60:63]
v_permlane16_swap_b32 v129, v131
v_mfma_f32_16x16x32_bf16 v[60:63], a[32:35], v[84:87], v[60:63]
ds_read_b128 v[120:123], v17 offset:11264
v_mfma_f32_16x16x32_bf16 v[60:63], a[44:47], v[88:91], v[60:63]
s_mov_b64 exec, s[76:77]
global_store_dwordx4 v12, v[128:131], s[38:39] sc1
s_mov_b64 exec, -1
v_mfma_f32_16x16x32_bf16 v[60:63], a[56:59], v[92:95], v[60:63]
v_add_u32 v12, 110592, v12
v_mfma_f32_16x16x32_bf16 v[60:63], a[68:71], v[96:99], v[60:63]
s_waitcnt lgkmcnt(5)
v_mfma_f32_16x16x32_bf16 v[52:55], a[72:75], v[100:103], v[52:55]
s_waitcnt lgkmcnt(4)
v_mfma_f32_16x16x32_bf16 v[52:55], a[84:87], v[104:107], v[52:55]
s_waitcnt lgkmcnt(3)
v_mfma_f32_16x16x32_bf16 v[52:55], a[96:99], v[108:111], v[52:55]
s_waitcnt lgkmcnt(2)
v_mfma_f32_16x16x32_bf16 v[52:55], a[108:111], v[112:115], v[52:55]
s_waitcnt lgkmcnt(1)
v_mfma_f32_16x16x32_bf16 v[52:55], a[120:123], v[116:119], v[52:55]
s_waitcnt lgkmcnt(0)
ds_read_b128 v[76:79], v17 offset:12288
v_mfma_f32_16x16x32_bf16 v[52:55], a[132:135], v[120:123], v[52:55]
v_mfma_f32_16x16x32_bf16 v[56:59], a[76:79], v[100:103], v[56:59]
ds_read_b128 v[80:83], v17 offset:13312
v_mfma_f32_16x16x32_bf16 v[56:59], a[88:91], v[104:107], v[56:59]
v_mfma_f32_16x16x32_bf16 v[56:59], a[100:103], v[108:111], v[56:59]
ds_read_b128 v[84:87], v17 offset:14336
v_mfma_f32_16x16x32_bf16 v[56:59], a[112:115], v[112:115], v[56:59]
v_mfma_f32_16x16x32_bf16 v[56:59], a[124:127], v[116:119], v[56:59]
ds_read_b128 v[88:91], v17 offset:15360
v_mfma_f32_16x16x32_bf16 v[56:59], a[136:139], v[120:123], v[56:59]
v_mfma_f32_16x16x32_bf16 v[60:63], a[80:83], v[100:103], v[60:63]
ds_read_b128 v[92:95], v17 offset:16384
v_mfma_f32_16x16x32_bf16 v[60:63], a[92:95], v[104:107], v[60:63]
v_mfma_f32_16x16x32_bf16 v[60:63], a[104:107], v[108:111], v[60:63]
ds_read_b128 v[96:99], v17 offset:17408
v_mfma_f32_16x16x32_bf16 v[60:63], a[116:119], v[112:115], v[60:63]
v_mfma_f32_16x16x32_bf16 v[60:63], a[128:131], v[116:119], v[60:63]
v_mfma_f32_16x16x32_bf16 v[60:63], a[140:143], v[120:123], v[60:63]
s_waitcnt lgkmcnt(5)
v_mfma_f32_16x16x32_bf16 v[52:55], a[144:147], v[76:79], v[52:55]
s_waitcnt lgkmcnt(4)
v_mfma_f32_16x16x32_bf16 v[52:55], a[156:159], v[80:83], v[52:55]
s_waitcnt lgkmcnt(3)
v_mfma_f32_16x16x32_bf16 v[52:55], a[168:171], v[84:87], v[52:55]
s_waitcnt lgkmcnt(2)
v_mfma_f32_16x16x32_bf16 v[52:55], a[180:183], v[88:91], v[52:55]
s_waitcnt lgkmcnt(1)
v_mfma_f32_16x16x32_bf16 v[52:55], a[192:195], v[92:95], v[52:55]
s_xor_b32 s45, s45, 0x10000
ds_read_b128 v[100:103], v17 offset:18432
s_waitcnt lgkmcnt(1)
v_mfma_f32_16x16x32_bf16 v[52:55], a[204:207], v[96:99], v[52:55]
s_add_u32 m0, s45, 0
v_mfma_f32_16x16x32_bf16 v[56:59], a[148:151], v[76:79], v[56:59]
ds_read_b128 v[104:107], v17 offset:19456
ds_read_b128 v[108:111], v17 offset:20480
v_mfma_f32_16x16x32_bf16 v[56:59], a[160:163], v[80:83], v[56:59]
v_mfma_f32_16x16x32_bf16 v[56:59], a[172:175], v[84:87], v[56:59]
s_barrier
v_mfma_f32_16x16x32_bf16 v[56:59], a[184:187], v[88:91], v[56:59]
v_mfma_f32_16x16x32_bf16 v[56:59], a[196:199], v[92:95], v[56:59]
ds_read_b128 v[112:115], v17 offset:21504
global_load_lds_dwordx4 v9, s[38:39] offset:-2048 sc1
v_mfma_f32_16x16x32_bf16 v[56:59], a[208:211], v[96:99], v[56:59]
v_mfma_f32_16x16x32_bf16 v[60:63], a[152:155], v[76:79], v[60:63]
ds_read_b128 v[116:119], v17 offset:22528
v_mfma_f32_16x16x32_bf16 v[60:63], a[164:167], v[80:83], v[60:63]
global_load_lds_dwordx4 v9, s[38:39] offset:-1024 sc1
v_mfma_f32_16x16x32_bf16 v[60:63], a[176:179], v[84:87], v[60:63]
ds_read_b128 v[120:123], v17 offset:23552
v_mfma_f32_16x16x32_bf16 v[60:63], a[188:191], v[88:91], v[60:63]
v_mfma_f32_16x16x32_bf16 v[60:63], a[200:203], v[92:95], v[60:63]
global_load_lds_dwordx4 v9, s[38:39] offset:0 sc1
v_mfma_f32_16x16x32_bf16 v[60:63], a[212:215], v[96:99], v[60:63]
s_waitcnt lgkmcnt(5)
v_mfma_f32_16x16x32_bf16 v[52:55], a[216:219], v[100:103], v[52:55]
s_waitcnt lgkmcnt(4)
v_mfma_f32_16x16x32_bf16 v[52:55], a[228:231], v[104:107], v[52:55]
s_waitcnt lgkmcnt(3)
global_load_lds_dwordx4 v9, s[38:39] offset:1024 sc1
v_mfma_f32_16x16x32_bf16 v[52:55], a[240:243], v[108:111], v[52:55]
s_waitcnt lgkmcnt(2)
v_mfma_f32_16x16x32_bf16 v[52:55], a[252:255], v[112:115], v[52:55]
s_waitcnt lgkmcnt(1)
v_mfma_f32_16x16x32_bf16 v[52:55], v[28:31], v[116:119], v[52:55]
s_waitcnt lgkmcnt(0)
global_load_lds_dwordx4 v9, s[38:39] offset:2048 sc1
v_mfma_f32_16x16x32_bf16 v[52:55], v[40:43], v[120:123], v[52:55]
v_mfma_f32_16x16x32_bf16 v[56:59], a[220:223], v[100:103], v[56:59]
v_mfma_f32_16x16x32_bf16 v[56:59], a[232:235], v[104:107], v[56:59]
v_mfma_f32_16x16x32_bf16 v[56:59], a[244:247], v[108:111], v[56:59]
s_waitcnt vmcnt(7)
s_barrier
v_mfma_f32_16x16x32_bf16 v[56:59], v[20:23], v[112:115], v[56:59]
ds_read_b128 v[76:79], v17 offset:24576
global_load_lds_dwordx4 v9, s[38:39] offset:3072 sc1
v_mfma_f32_16x16x32_bf16 v[56:59], v[32:35], v[116:119], v[56:59]
)"
R"(ds_read_b128 v[80:83], v17 offset:25600
v_add_u32 v9, 147456, v9
v_mfma_f32_16x16x32_bf16 v[56:59], v[44:47], v[120:123], v[56:59]
ds_read_b128 v[84:87], v17 offset:26624
v_min_u32 v9, v9, v10
v_mfma_f32_16x16x32_bf16 v[60:63], a[224:227], v[100:103], v[60:63]
ds_read_b128 v[88:91], v17 offset:27648
v_cvt_pk_bf16_f32 v124, v52, v53
v_mfma_f32_16x16x32_bf16 v[60:63], a[236:239], v[104:107], v[60:63]
ds_read_b128 v[92:95], v17 offset:28672
v_cvt_pk_bf16_f32 v125, v54, v55
v_mfma_f32_16x16x32_bf16 v[60:63], a[248:251], v[108:111], v[60:63]
ds_read_b128 v[96:99], v17 offset:29696
v_mfma_f32_16x16x32_bf16 v[60:63], v[24:27], v[112:115], v[60:63]
v_cvt_pk_bf16_f32 v126, v56, v57
v_cvt_pk_bf16_f32 v127, v58, v59
v_mfma_f32_16x16x32_bf16 v[60:63], v[36:39], v[116:119], v[60:63]
v_mfma_f32_16x16x32_bf16 v[60:63], v[48:51], v[120:123], v[60:63]
s_waitcnt lgkmcnt(5)
v_mfma_f32_16x16x32_bf16 v[64:67], a[0:3], v[76:79], 0
s_waitcnt lgkmcnt(4)
v_mfma_f32_16x16x32_bf16 v[64:67], a[12:15], v[80:83], v[64:67]
s_waitcnt lgkmcnt(3)
v_mfma_f32_16x16x32_bf16 v[64:67], a[24:27], v[84:87], v[64:67]
s_waitcnt lgkmcnt(2)
v_mfma_f32_16x16x32_bf16 v[64:67], a[36:39], v[88:91], v[64:67]
s_waitcnt lgkmcnt(1)
ds_read_b128 v[100:103], v17 offset:30720
v_cvt_pk_bf16_f32 v128, v60, v61
v_mfma_f32_16x16x32_bf16 v[64:67], a[48:51], v[92:95], v[64:67]
s_waitcnt lgkmcnt(1)
v_mfma_f32_16x16x32_bf16 v[64:67], a[60:63], v[96:99], v[64:67]
s_waitcnt vmcnt(6)
s_barrier
v_mfma_f32_16x16x32_bf16 v[68:71], a[4:7], v[76:79], 0
ds_read_b128 v[104:107], v17 offset:31744
v_cvt_pk_bf16_f32 v130, v60, v61
v_mfma_f32_16x16x32_bf16 v[68:71], a[16:19], v[80:83], v[68:71]
v_cvt_pk_bf16_f32 v129, v62, v63
v_cvt_pk_bf16_f32 v131, v62, v63
v_mfma_f32_16x16x32_bf16 v[68:71], a[28:31], v[84:87], v[68:71]
ds_read_b128 v[108:111], v17 offset:32768
v_mfma_f32_16x16x32_bf16 v[68:71], a[40:43], v[88:91], v[68:71]
global_store_dwordx4 v11, v[124:127], s[38:39] sc1
v_mfma_f32_16x16x32_bf16 v[68:71], a[52:55], v[92:95], v[68:71]
ds_read_b128 v[112:115], v17 offset:33792
v_add_u32 v11, 110592, v11
v_mfma_f32_16x16x32_bf16 v[68:71], a[64:67], v[96:99], v[68:71]
v_permlane16_swap_b32 v128, v130
v_mfma_f32_16x16x32_bf16 v[72:75], a[8:11], v[76:79], 0
ds_read_b128 v[116:119], v17 offset:34816
v_mfma_f32_16x16x32_bf16 v[72:75], a[20:23], v[80:83], v[72:75]
v_permlane16_swap_b32 v129, v131
v_mfma_f32_16x16x32_bf16 v[72:75], a[32:35], v[84:87], v[72:75]
ds_read_b128 v[120:123], v17 offset:35840
v_mfma_f32_16x16x32_bf16 v[72:75], a[44:47], v[88:91], v[72:75]
s_mov_b64 exec, s[76:77]
global_store_dwordx4 v12, v[128:131], s[38:39] sc1
s_mov_b64 exec, -1
v_mfma_f32_16x16x32_bf16 v[72:75], a[56:59], v[92:95], v[72:75]
v_add_u32 v12, 110592, v12
v_mfma_f32_16x16x32_bf16 v[72:75], a[68:71], v[96:99], v[72:75]
s_waitcnt lgkmcnt(5)
v_mfma_f32_16x16x32_bf16 v[64:67], a[72:75], v[100:103], v[64:67]
s_waitcnt lgkmcnt(4)
v_mfma_f32_16x16x32_bf16 v[64:67], a[84:87], v[104:107], v[64:67]
s_waitcnt lgkmcnt(3)
v_mfma_f32_16x16x32_bf16 v[64:67], a[96:99], v[108:111], v[64:67]
s_waitcnt lgkmcnt(2)
v_mfma_f32_16x16x32_bf16 v[64:67], a[108:111], v[112:115], v[64:67]
s_waitcnt lgkmcnt(1)
v_mfma_f32_16x16x32_bf16 v[64:67], a[120:123], v[116:119], v[64:67]
s_waitcnt lgkmcnt(0)
ds_read_b128 v[76:79], v17 offset:36864
v_mfma_f32_16x16x32_bf16 v[64:67], a[132:135], v[120:123], v[64:67]
v_mfma_f32_16x16x32_bf16 v[68:71], a[76:79], v[100:103], v[68:71]
ds_read_b128 v[80:83], v17 offset:37888
v_mfma_f32_16x16x32_bf16 v[68:71], a[88:91], v[104:107], v[68:71]
v_mfma_f32_16x16x32_bf16 v[68:71], a[100:103], v[108:111], v[68:71]
ds_read_b128 v[84:87], v17 offset:38912
v_mfma_f32_16x16x32_bf16 v[68:71], a[112:115], v[112:115], v[68:71]
v_mfma_f32_16x16x32_bf16 v[68:71], a[124:127], v[116:119], v[68:71]
ds_read_b128 v[88:91], v17 offset:39936
v_mfma_f32_16x16x32_bf16 v[68:71], a[136:139], v[120:123], v[68:71]
v_mfma_f32_16x16x32_bf16 v[72:75], a[80:83], v[100:103], v[72:75]
ds_read_b128 v[92:95], v17 offset:40960
v_mfma_f32_16x16x32_bf16 v[72:75], a[92:95], v[104:107], v[72:75]
v_mfma_f32_16x16x32_bf16 v[72:75], a[104:107], v[108:111], v[72:75]
ds_read_b128 v[96:99], v17 offset:41984
v_mfma_f32_16x16x32_bf16 v[72:75], a[116:119], v[112:115], v[72:75]
v_mfma_f32_16x16x32_bf16 v[72:75], a[128:131], v[116:119], v[72:75]
v_mfma_f32_16x16x32_bf16 v[72:75], a[140:143], v[120:123], v[72:75]
s_waitcnt lgkmcnt(5)
v_mfma_f32_16x16x32_bf16 v[64:67], a[144:147], v[76:79], v[64:67]
s_waitcnt lgkmcnt(4)
v_mfma_f32_16x16x32_bf16 v[64:67], a[156:159], v[80:83], v[64:67]
s_waitcnt lgkmcnt(3)
v_mfma_f32_16x16x32_bf16 v[64:67], a[168:171], v[84:87], v[64:67]
s_waitcnt lgkmcnt(2)
v_mfma_f32_16x16x32_bf16 v[64:67], a[180:183], v[88:91], v[64:67]
s_waitcnt lgkmcnt(1)
v_mfma_f32_16x16x32_bf16 v[64:67], a[192:195], v[92:95], v[64:67]
s_waitcnt lgkmcnt(0)
ds_read_b128 v[100:103], v17 offset:43008
v_mfma_f32_16x16x32_bf16 v[64:67], a[204:207], v[96:99], v[64:67]
s_add_u32 m0, s45, 24576
v_mfma_f32_16x16x32_bf16 v[68:71], a[148:151], v[76:79], v[68:71]
ds_read_b128 v[104:107], v17 offset:44032
ds_read_b128 v[108:111], v17 offset:45056
v_mfma_f32_16x16x32_bf16 v[68:71], a[160:163], v[80:83], v[68:71]
v_mfma_f32_16x16x32_bf16 v[68:71], a[172:175], v[84:87], v[68:71]
s_barrier
v_mfma_f32_16x16x32_bf16 v[68:71], a[184:187], v[88:91], v[68:71]
v_mfma_f32_16x16x32_bf16 v[68:71], a[196:199], v[92:95], v[68:71]
ds_read_b128 v[112:115], v17 offset:46080
global_load_lds_dwordx4 v9, s[38:39] offset:-2048 sc1
v_mfma_f32_16x16x32_bf16 v[68:71], a[208:211], v[96:99], v[68:71]
v_mfma_f32_16x16x32_bf16 v[72:75], a[152:155], v[76:79], v[72:75]
ds_read_b128 v[116:119], v17 offset:47104
v_mfma_f32_16x16x32_bf16 v[72:75], a[164:167], v[80:83], v[72:75]
global_load_lds_dwordx4 v9, s[38:39] offset:-1024 sc1
v_mfma_f32_16x16x32_bf16 v[72:75], a[176:179], v[84:87], v[72:75]
ds_read_b128 v[120:123], v17 offset:48128
v_mfma_f32_16x16x32_bf16 v[72:75], a[188:191], v[88:91], v[72:75]
v_mfma_f32_16x16x32_bf16 v[72:75], a[200:203], v[92:95], v[72:75]
global_load_lds_dwordx4 v9, s[38:39] offset:0 sc1
v_mfma_f32_16x16x32_bf16 v[72:75], a[212:215], v[96:99], v[72:75]
s_waitcnt lgkmcnt(5)
v_mfma_f32_16x16x32_bf16 v[64:67], a[216:219], v[100:103], v[64:67]
s_waitcnt lgkmcnt(4)
v_mfma_f32_16x16x32_bf16 v[64:67], a[228:231], v[104:107], v[64:67]
s_waitcnt lgkmcnt(3)
global_load_lds_dwordx4 v9, s[38:39] offset:1024 sc1
v_mfma_f32_16x16x32_bf16 v[64:67], a[240:243], v[108:111], v[64:67]
s_waitcnt lgkmcnt(2)
v_mfma_f32_16x16x32_bf16 v[64:67], a[252:255], v[112:115], v[64:67]
s_waitcnt lgkmcnt(1)
v_mfma_f32_16x16x32_bf16 v[64:67], v[28:31], v[116:119], v[64:67]
s_waitcnt lgkmcnt(0)
global_load_lds_dwordx4 v9, s[38:39] offset:2048 sc1
v_mfma_f32_16x16x32_bf16 v[64:67], v[40:43], v[120:123], v[64:67]
v_mfma_f32_16x16x32_bf16 v[68:71], a[220:223], v[100:103], v[68:71]
v_xor_b32 v17, 0x10000, v17
v_mfma_f32_16x16x32_bf16 v[68:71], a[232:235], v[104:107], v[68:71]
v_mfma_f32_16x16x32_bf16 v[68:71], a[244:247], v[108:111], v[68:71]
s_waitcnt vmcnt(7)
s_barrier
v_mfma_f32_16x16x32_bf16 v[68:71], v[20:23], v[112:115], v[68:71]
ds_read_b128 v[76:79], v17 offset:0
global_load_lds_dwordx4 v9, s[38:39] offset:3072 sc1
v_mfma_f32_16x16x32_bf16 v[68:71], v[32:35], v[116:119], v[68:71]
ds_read_b128 v[80:83], v17 offset:1024
v_add_u32 v9, 147456, v9
v_mfma_f32_16x16x32_bf16 v[68:71], v[44:47], v[120:123], v[68:71]
ds_read_b128 v[84:87], v17 offset:2048
v_min_u32 v9, v9, v10
v_mfma_f32_16x16x32_bf16 v[72:75], a[224:227], v[100:103], v[72:75]
ds_read_b128 v[88:91], v17 offset:3072
v_cvt_pk_bf16_f32 v124, v64, v65
v_mfma_f32_16x16x32_bf16 v[72:75], a[236:239], v[104:107], v[72:75]
ds_read_b128 v[92:95], v17 offset:4096
v_cvt_pk_bf16_f32 v125, v66, v67
v_mfma_f32_16x16x32_bf16 v[72:75], a[248:251], v[108:111], v[72:75]
ds_read_b128 v[96:99], v17 offset:5120
v_mfma_f32_16x16x32_bf16 v[72:75], v[24:27], v[112:115], v[72:75]
v_cvt_pk_bf16_f32 v126, v68, v69
v_cvt_pk_bf16_f32 v127, v70, v71
v_mfma_f32_16x16x32_bf16 v[72:75], v[36:39], v[116:119], v[72:75]
v_mfma_f32_16x16x32_bf16 v[72:75], v[48:51], v[120:123], v[72:75]
s_sub_u32 s70, s70, 1
s_cbranch_scc0 .Ldn_top6
v_cvt_pk_bf16_f32 v124, v64, v65
v_cvt_pk_bf16_f32 v125, v66, v67
v_cvt_pk_bf16_f32 v126, v68, v69
v_cvt_pk_bf16_f32 v127, v70, v71
global_store_dwordx4 v11, v[124:127], s[38:39] sc1
v_add_u32 v11, 110592, v11
v_cvt_pk_bf16_f32 v128, v72, v73
v_cvt_pk_bf16_f32 v130, v72, v73
v_cvt_pk_bf16_f32 v129, v74, v75
v_cvt_pk_bf16_f32 v131, v74, v75
v_permlane16_swap_b32 v128, v130
s_nop 0
v_permlane16_swap_b32 v129, v131
s_mov_b64 exec, s[76:77]
global_store_dwordx4 v12, v[128:131], s[38:39] sc1
s_mov_b64 exec, -1
v_add_u32 v12, 110592, v12
s_waitcnt vmcnt(2)
s_barrier
s_waitcnt vmcnt(0)
s_barrier
s_endpgm
.Ldn_w3:
v_add_u32 v19, -1, v14
s_mov_b32 s76, 0xffff
s_mov_b32 s77, 0xffff
s_mov_b32 s70, 32
s_waitcnt vmcnt(0) lgkmcnt(0)
s_nop 7
.p2align 6
.Ldn_top7:
s_waitcnt lgkmcnt(5)
v_mfma_f32_16x16x32_bf16 v[52:55], a[0:3], v[76:79], 0
s_waitcnt lgkmcnt(4)
v_mfma_f32_16x16x32_bf16 v[52:55], a[12:15], v[80:83], v[52:55]
s_waitcnt lgkmcnt(3)
v_mfma_f32_16x16x32_bf16 v[52:55], a[24:27], v[84:87], v[52:55]
s_waitcnt lgkmcnt(2)
v_mfma_f32_16x16x32_bf16 v[52:55], a[36:39], v[88:91], v[52:55]
s_waitcnt lgkmcnt(1)
ds_read_b128 v[100:103], v17 offset:6144
v_cvt_pk_bf16_f32 v128, v72, v73
v_mfma_f32_16x16x32_bf16 v[52:55], a[48:51], v[92:95], v[52:55]
s_waitcnt lgkmcnt(1)
v_mfma_f32_16x16x32_bf16 v[52:55], a[60:63], v[96:99], v[52:55]
s_waitcnt vmcnt(7)
s_barrier
v_mfma_f32_16x16x32_bf16 v[56:59], a[4:7], v[76:79], 0
ds_read_b128 v[104:107], v17 offset:7168
v_cvt_pk_bf16_f32 v130, v72, v73
v_mfma_f32_16x16x32_bf16 v[56:59], a[16:19], v[80:83], v[56:59]
s_mov_b64 exec, 1
global_store_dword v15, v16, s[38:39] sc1
s_mov_b64 exec, -1
v_mfma_f32_16x16x32_bf16 v[56:59], a[28:31], v[84:87], v[56:59]
ds_read_b128 v[108:111], v17 offset:8192
v_add_u32 v16, 1, v16
v_mfma_f32_16x16x32_bf16 v[56:59], a[40:43], v[88:91], v[56:59]
global_store_dwordx4 v11, v[124:127], s[38:39] sc1
v_mfma_f32_16x16x32_bf16 v[56:59], a[52:55], v[92:95], v[56:59]
ds_read_b128 v[112:115], v17 offset:9216
v_add_u32 v11, 110592, v11
v_mfma_f32_16x16x32_bf16 v[56:59], a[64:67], v[96:99], v[56:59]
v_cvt_pk_bf16_f32 v129, v74, v75
v_cvt_pk_bf16_f32 v131, v74, v75
v_mfma_f32_16x16x32_bf16 v[60:63], a[8:11], v[76:79], 0
ds_read_b128 v[116:119], v17 offset:10240
v_mfma_f32_16x16x32_bf16 v[60:63], a[20:23], v[80:83], v[60:63]
v_permlane16_swap_b32 v128, v130
v_mfma_f32_16x16x32_bf16 v[60:63], a[32:35], v[84:87], v[60:63]
ds_read_b128 v[120:123], v17 offset:11264
v_mfma_f32_16x16x32_bf16 v[60:63], a[44:47], v[88:91], v[60:63]
v_permlane16_swap_b32 v129, v131
v_mfma_f32_16x16x32_bf16 v[60:63], a[56:59], v[92:95], v[60:63]
s_mov_b64 exec, s[76:77]
global_store_dwordx4 v12, v[128:131], s[38:39] sc1
s_mov_b64 exec, -1
v_mfma_f32_16x16x32_bf16 v[60:63], a[68:71], v[96:99], v[60:63]
s_waitcnt lgkmcnt(5)
v_add_u32 v12, 110592, v12
v_mfma_f32_16x16x32_bf16 v[52:55], a[72:75], v[100:103], v[52:55]
s_waitcnt lgkmcnt(4)
v_mfma_f32_16x16x32_bf16 v[52:55], a[84:87], v[104:107], v[52:55]
s_waitcnt lgkmcnt(3)
v_mfma_f32_16x16x32_bf16 v[52:55], a[96:99], v[108:111], v[52:55]
s_waitcnt lgkmcnt(2)
v_mfma_f32_16x16x32_bf16 v[52:55], a[108:111], v[112:115], v[52:55]
s_waitcnt lgkmcnt(1)
v_mfma_f32_16x16x32_bf16 v[52:55], a[120:123], v[116:119], v[52:55]
s_waitcnt lgkmcnt(0)
ds_read_b128 v[76:79], v17 offset:12288
v_mfma_f32_16x16x32_bf16 v[52:55], a[132:135], v[120:123], v[52:55]
v_mfma_f32_16x16x32_bf16 v[56:59], a[76:79], v[100:103], v[56:59]
ds_read_b128 v[80:83], v17 offset:13312
v_mfma_f32_16x16x32_bf16 v[56:59], a[88:91], v[104:107], v[56:59]
v_mfma_f32_16x16x32_bf16 v[56:59], a[100:103], v[108:111], v[56:59]
ds_read_b128 v[84:87], v17 offset:14336
v_mfma_f32_16x16x32_bf16 v[56:59], a[112:115], v[112:115], v[56:59]
v_mfma_f32_16x16x32_bf16 v[56:59], a[124:127], v[116:119], v[56:59]
ds_read_b128 v[88:91], v17 offset:15360
v_mfma_f32_16x16x32_bf16 v[56:59], a[136:139], v[120:123], v[56:59]
v_mfma_f32_16x16x32_bf16 v[60:63], a[80:83], v[100:103], v[60:63]
ds_read_b128 v[92:95], v17 offset:16384
v_mfma_f32_16x16x32_bf16 v[60:63], a[92:95], v[104:107], v[60:63]
v_mfma_f32_16x16x32_bf16 v[60:63], a[104:107], v[108:111], v[60:63]
ds_read_b128 v[96:99], v17 offset:17408
v_mfma_f32_16x16x32_bf16 v[60:63], a[116:119], v[112:115], v[60:63]
v_mfma_f32_16x16x32_bf16 v[60:63], a[128:131], v[116:119], v[60:63]
v_mfma_f32_16x16x32_bf16 v[60:63], a[140:143], v[120:123], v[60:63]
s_waitcnt lgkmcnt(5)
v_mfma_f32_16x16x32_bf16 v[52:55], a[144:147], v[76:79], v[52:55]
s_waitcnt lgkmcnt(4)
v_mfma_f32_16x16x32_bf16 v[52:55], a[156:159], v[80:83], v[52:55]
s_waitcnt lgkmcnt(3)
v_mfma_f32_16x16x32_bf16 v[52:55], a[168:171], v[84:87], v[52:55]
s_waitcnt lgkmcnt(2)
v_mfma_f32_16x16x32_bf16 v[52:55], a[180:183], v[88:91], v[52:55]
s_waitcnt lgkmcnt(1)
v_mfma_f32_16x16x32_bf16 v[52:55], a[192:195], v[92:95], v[52:55]
s_xor_b32 s45, s45, 0x10000
ds_read_b128 v[100:103], v17 offset:18432
s_waitcnt lgkmcnt(1)
v_mfma_f32_16x16x32_bf16 v[52:55], a[204:207], v[96:99], v[52:55]
s_add_u32 m0, s45, 0
v_mfma_f32_16x16x32_bf16 v[56:59], a[148:151], v[76:79], v[56:59]
ds_read_b128 v[104:107], v17 offset:19456
ds_read_b128 v[108:111], v17 offset:20480
v_mfma_f32_16x16x32_bf16 v[56:59], a[160:163], v[80:83], v[56:59]
v_mfma_f32_16x16x32_bf16 v[56:59], a[172:175], v[84:87], v[56:59]
s_barrier
v_mfma_f32_16x16x32_bf16 v[56:59], a[184:187], v[88:91], v[56:59]
v_mfma_f32_16x16x32_bf16 v[56:59], a[196:199], v[92:95], v[56:59]
ds_read_b128 v[112:115], v17 offset:21504
v_mfma_f32_16x16x32_bf16 v[56:59], a[208:211], v[96:99], v[56:59]
global_load_lds_dwordx4 v9, s[38:39] offset:-2048 sc1
v_mfma_f32_16x16x32_bf16 v[60:63], a[152:155], v[76:79], v[60:63]
ds_read_b128 v[116:119], v17 offset:22528
v_mfma_f32_16x16x32_bf16 v[60:63], a[164:167], v[80:83], v[60:63]
v_mfma_f32_16x16x32_bf16 v[60:63], a[176:179], v[84:87], v[60:63]
ds_read_b128 v[120:123], v17 offset:23552
global_load_lds_dwordx4 v9, s[38:39] offset:-1024 sc1
v_mfma_f32_16x16x32_bf16 v[60:63], a[188:191], v[88:91], v[60:63]
v_mfma_f32_16x16x32_bf16 v[60:63], a[200:203], v[92:95], v[60:63]
v_mfma_f32_16x16x32_bf16 v[60:63], a[212:215], v[96:99], v[60:63]
s_waitcnt lgkmcnt(5)
global_load_lds_dwordx4 v9, s[38:39] offset:0 sc1
v_mfma_f32_16x16x32_bf16 v[52:55], a[216:219], v[100:103], v[52:55]
s_waitcnt lgkmcnt(4)
v_mfma_f32_16x16x32_bf16 v[52:55], a[228:231], v[104:107], v[52:55]
s_waitcnt lgkmcnt(3)
v_mfma_f32_16x16x32_bf16 v[52:55], a[240:243], v[108:111], v[52:55]
s_waitcnt lgkmcnt(2)
global_load_lds_dwordx4 v9, s[38:39] offset:1024 sc1
v_mfma_f32_16x16x32_bf16 v[52:55], a[252:255], v[112:115], v[52:55]
s_waitcnt lgkmcnt(1)
v_mfma_f32_16x16x32_bf16 v[52:55], v[28:31], v[116:119], v[52:55]
s_waitcnt lgkmcnt(0)
v_mfma_f32_16x16x32_bf16 v[52:55], v[40:43], v[120:123], v[52:55]
global_load_lds_dwordx4 v9, s[38:39] offset:2048 sc1
v_mfma_f32_16x16x32_bf16 v[56:59], a[220:223], v[100:103], v[56:59]
v_mfma_f32_16x16x32_bf16 v[56:59], a[232:235], v[104:107], v[56:59]
v_mfma_f32_16x16x32_bf16 v[56:59], a[244:247], v[108:111], v[56:59]
s_waitcnt vmcnt(8)
s_barrier
v_mfma_f32_16x16x32_bf16 v[56:59], v[20:23], v[112:115], v[56:59]
ds_read_b128 v[76:79], v17 offset:24576
global_load_lds_dwordx4 v9, s[38:39] offset:3072 sc1
v_mfma_f32_16x16x32_bf16 v[56:59], v[32:35], v[116:119], v[56:59]
ds_read_b128 v[80:83], v17 offset:25600
v_add_u32 v9, 147456, v9
v_mfma_f32_16x16x32_bf16 v[56:59], v[44:47], v[120:123], v[56:59]
ds_read_b128 v[84:87], v17 offset:26624
v_min_u32 v9, v9, v10
v_mfma_f32_16x16x32_bf16 v[60:63], a[224:227], v[100:103], v[60:63]
ds_read_b128 v[88:91], v17 offset:27648
v_cvt_pk_bf16_f32 v124, v52, v53
v_mfma_f32_16x16x32_bf16 v[60:63], a[236:239], v[104:107], v[60:63]
ds_read_b128 v[92:95], v17 offset:28672
v_cvt_pk_bf16_f32 v125, v54, v55
v_mfma_f32_16x16x32_bf16 v[60:63], a[248:251], v[108:111], v[60:63]
ds_read_b128 v[96:99], v17 offset:29696
v_mfma_f32_16x16x32_bf16 v[60:63], v[24:27], v[112:115], v[60:63]
v_cvt_pk_bf16_f32 v126, v56, v57
v_cvt_pk_bf16_f32 v127, v58, v59
v_mfma_f32_16x16x32_bf16 v[60:63], v[36:39], v[116:119], v[60:63]
v_mfma_f32_16x16x32_bf16 v[60:63], v[48:51], v[120:123], v[60:63]
s_waitcnt lgkmcnt(5)
v_mfma_f32_16x16x32_bf16 v[64:67], a[0:3], v[76:79], 0
s_waitcnt lgkmcnt(4)
v_mfma_f32_16x16x32_bf16 v[64:67], a[12:15], v[80:83], v[64:67]
s_waitcnt lgkmcnt(3)
v_mfma_f32_16x16x32_bf16 v[64:67], a[24:27], v[84:87], v[64:67]
s_waitcnt lgkmcnt(2)
v_mfma_f32_16x16x32_bf16 v[64:67], a[36:39], v[88:91], v[64:67]
s_waitcnt lgkmcnt(1)
ds_read_b128 v[100:103], v17 offset:30720
v_cvt_pk_bf16_f32 v128, v60, v61
v_mfma_f32_16x16x32_bf16 v[64:67], a[48:51], v[92:95], v[64:67]
s_waitcnt lgkmcnt(1)
v_mfma_f32_16x16x32_bf16 v[64:67], a[60:63], v[96:99], v[64:67]
s_waitcnt vmcnt(6)
s_barrier
v_mfma_f32_16x16x32_bf16 v[68:71], a[4:7], v[76:79], 0
ds_read_b128 v[104:107], v17 offset:31744
v_cvt_pk_bf16_f32 v130, v60, v61
v_mfma_f32_16x16x32_bf16 v[68:71], a[16:19], v[80:83], v[68:71]
v_cvt_pk_bf16_f32 v129, v62, v63
v_cvt_pk_bf16_f32 v131, v62, v63
v_mfma_f32_16x16x32_bf16 v[68:71], a[28:31], v[84:87], v[68:71]
ds_read_b128 v[108:111], v17 offset:32768
v_mfma_f32_16x16x32_bf16 v[68:71], a[40:43], v[88:91], v[68:71]
global_store_dwordx4 v11, v[124:127], s[38:39] sc1
v_mfma_f32_16x16x32_bf16 v[68:71], a[52:55], v[92:95], v[68:71]
ds_read_b128 v[112:115], v17 offset:33792
v_add_u32 v11, 110592, v11
v_mfma_f32_16x16x32_bf16 v[68:71], a[64:67], v[96:99], v[68:71]
v_permlane16_swap_b32 v128, v130
v_mfma_f32_16x16x32_bf16 v[72:75], a[8:11], v[76:79], 0
ds_read_b128 v[116:119], v17 offset:34816
v_mfma_f32_16x16x32_bf16 v[72:75], a[20:23], v[80:83], v[72:75]
v_permlane16_swap_b32 v129, v131
v_mfma_f32_16x16x32_bf16 v[72:75], a[32:35], v[84:87], v[72:75]
ds_read_b128 v[120:123], v17 offset:35840
v_mfma_f32_16x16x32_bf16 v[72:75], a[44:47], v[88:91], v[72:75]
s_mov_b64 exec, s[76:77]
global_store_dwordx4 v12, v[128:131], s[38:39] sc1
s_mov_b64 exec, -1
v_mfma_f32_16x16x32_bf16 v[72:75], a[56:59], v[92:95], v[72:75]
v_add_u32 v12, 110592, v12
)"
R"(v_mfma_f32_16x16x32_bf16 v[72:75], a[68:71], v[96:99], v[72:75]
s_waitcnt lgkmcnt(5)
s_mov_b64 exec, 1
global_store_dword v15, v16, s[38:39] sc1
s_mov_b64 exec, -1
v_mfma_f32_16x16x32_bf16 v[64:67], a[72:75], v[100:103], v[64:67]
s_waitcnt lgkmcnt(4)
v_add_u32 v16, 1, v16
v_mfma_f32_16x16x32_bf16 v[64:67], a[84:87], v[104:107], v[64:67]
s_waitcnt lgkmcnt(3)
v_mfma_f32_16x16x32_bf16 v[64:67], a[96:99], v[108:111], v[64:67]
s_waitcnt lgkmcnt(2)
v_mfma_f32_16x16x32_bf16 v[64:67], a[108:111], v[112:115], v[64:67]
s_waitcnt lgkmcnt(1)
v_mfma_f32_16x16x32_bf16 v[64:67], a[120:123], v[116:119], v[64:67]
s_waitcnt lgkmcnt(0)
ds_read_b128 v[76:79], v17 offset:36864
v_mfma_f32_16x16x32_bf16 v[64:67], a[132:135], v[120:123], v[64:67]
v_mfma_f32_16x16x32_bf16 v[68:71], a[76:79], v[100:103], v[68:71]
ds_read_b128 v[80:83], v17 offset:37888
v_mfma_f32_16x16x32_bf16 v[68:71], a[88:91], v[104:107], v[68:71]
v_mfma_f32_16x16x32_bf16 v[68:71], a[100:103], v[108:111], v[68:71]
ds_read_b128 v[84:87], v17 offset:38912
v_mfma_f32_16x16x32_bf16 v[68:71], a[112:115], v[112:115], v[68:71]
v_mfma_f32_16x16x32_bf16 v[68:71], a[124:127], v[116:119], v[68:71]
ds_read_b128 v[88:91], v17 offset:39936
v_mfma_f32_16x16x32_bf16 v[68:71], a[136:139], v[120:123], v[68:71]
v_mfma_f32_16x16x32_bf16 v[72:75], a[80:83], v[100:103], v[72:75]
ds_read_b128 v[92:95], v17 offset:40960
v_mfma_f32_16x16x32_bf16 v[72:75], a[92:95], v[104:107], v[72:75]
v_mfma_f32_16x16x32_bf16 v[72:75], a[104:107], v[108:111], v[72:75]
ds_read_b128 v[96:99], v17 offset:41984
v_mfma_f32_16x16x32_bf16 v[72:75], a[116:119], v[112:115], v[72:75]
v_mfma_f32_16x16x32_bf16 v[72:75], a[128:131], v[116:119], v[72:75]
v_mfma_f32_16x16x32_bf16 v[72:75], a[140:143], v[120:123], v[72:75]
s_waitcnt lgkmcnt(5)
v_mfma_f32_16x16x32_bf16 v[64:67], a[144:147], v[76:79], v[64:67]
s_waitcnt lgkmcnt(4)
v_mfma_f32_16x16x32_bf16 v[64:67], a[156:159], v[80:83], v[64:67]
s_waitcnt lgkmcnt(3)
v_mfma_f32_16x16x32_bf16 v[64:67], a[168:171], v[84:87], v[64:67]
s_waitcnt lgkmcnt(2)
v_mfma_f32_16x16x32_bf16 v[64:67], a[180:183], v[88:91], v[64:67]
s_waitcnt lgkmcnt(1)
v_mfma_f32_16x16x32_bf16 v[64:67], a[192:195], v[92:95], v[64:67]
s_waitcnt lgkmcnt(0)
ds_read_b128 v[100:103], v17 offset:43008
v_mfma_f32_16x16x32_bf16 v[64:67], a[204:207], v[96:99], v[64:67]
s_add_u32 m0, s45, 24576
v_mfma_f32_16x16x32_bf16 v[68:71], a[148:151], v[76:79], v[68:71]
ds_read_b128 v[104:107], v17 offset:44032
ds_read_b128 v[108:111], v17 offset:45056
v_mfma_f32_16x16x32_bf16 v[68:71], a[160:163], v[80:83], v[68:71]
v_mfma_f32_16x16x32_bf16 v[68:71], a[172:175], v[84:87], v[68:71]
s_barrier
v_mfma_f32_16x16x32_bf16 v[68:71], a[184:187], v[88:91], v[68:71]
v_mfma_f32_16x16x32_bf16 v[68:71], a[196:199], v[92:95], v[68:71]
ds_read_b128 v[112:115], v17 offset:46080
v_mfma_f32_16x16x32_bf16 v[68:71], a[208:211], v[96:99], v[68:71]
global_load_lds_dwordx4 v9, s[38:39] offset:-2048 sc1
v_mfma_f32_16x16x32_bf16 v[72:75], a[152:155], v[76:79], v[72:75]
ds_read_b128 v[116:119], v17 offset:47104
v_mfma_f32_16x16x32_bf16 v[72:75], a[164:167], v[80:83], v[72:75]
v_mfma_f32_16x16x32_bf16 v[72:75], a[176:179], v[84:87], v[72:75]
ds_read_b128 v[120:123], v17 offset:48128
global_load_lds_dwordx4 v9, s[38:39] offset:-1024 sc1
v_mfma_f32_16x16x32_bf16 v[72:75], a[188:191], v[88:91], v[72:75]
v_mfma_f32_16x16x32_bf16 v[72:75], a[200:203], v[92:95], v[72:75]
v_mfma_f32_16x16x32_bf16 v[72:75], a[212:215], v[96:99], v[72:75]
s_waitcnt lgkmcnt(5)
global_load_lds_dwordx4 v9, s[38:39] offset:0 sc1
v_mfma_f32_16x16x32_bf16 v[64:67], a[216:219], v[100:103], v[64:67]
s_waitcnt lgkmcnt(4)
v_mfma_f32_16x16x32_bf16 v[64:67], a[228:231], v[104:107], v[64:67]
s_waitcnt lgkmcnt(3)
v_mfma_f32_16x16x32_bf16 v[64:67], a[240:243], v[108:111], v[64:67]
s_waitcnt lgkmcnt(2)
global_load_lds_dwordx4 v9, s[38:39] offset:1024 sc1
v_mfma_f32_16x16x32_bf16 v[64:67], a[252:255], v[112:115], v[64:67]
s_waitcnt lgkmcnt(1)
v_mfma_f32_16x16x32_bf16 v[64:67], v[28:31], v[116:119], v[64:67]
s_waitcnt lgkmcnt(0)
v_mfma_f32_16x16x32_bf16 v[64:67], v[40:43], v[120:123], v[64:67]
global_load_lds_dwordx4 v9, s[38:39] offset:2048 sc1
v_mfma_f32_16x16x32_bf16 v[68:71], a[220:223], v[100:103], v[68:71]
v_xor_b32 v17, 0x10000, v17
v_mfma_f32_16x16x32_bf16 v[68:71], a[232:235], v[104:107], v[68:71]
v_mfma_f32_16x16x32_bf16 v[68:71], a[244:247], v[108:111], v[68:71]
s_waitcnt vmcnt(8)
s_barrier
v_mfma_f32_16x16x32_bf16 v[68:71], v[20:23], v[112:115], v[68:71]
ds_read_b128 v[76:79], v17 offset:0
global_load_lds_dwordx4 v9, s[38:39] offset:3072 sc1
v_mfma_f32_16x16x32_bf16 v[68:71], v[32:35], v[116:119], v[68:71]
ds_read_b128 v[80:83], v17 offset:1024
v_add_u32 v9, 147456, v9
v_mfma_f32_16x16x32_bf16 v[68:71], v[44:47], v[120:123], v[68:71]
ds_read_b128 v[84:87], v17 offset:2048
v_min_u32 v9, v9, v10
v_mfma_f32_16x16x32_bf16 v[72:75], a[224:227], v[100:103], v[72:75]
ds_read_b128 v[88:91], v17 offset:3072
v_cvt_pk_bf16_f32 v124, v64, v65
v_mfma_f32_16x16x32_bf16 v[72:75], a[236:239], v[104:107], v[72:75]
ds_read_b128 v[92:95], v17 offset:4096
v_cvt_pk_bf16_f32 v125, v66, v67
v_mfma_f32_16x16x32_bf16 v[72:75], a[248:251], v[108:111], v[72:75]
ds_read_b128 v[96:99], v17 offset:5120
v_mfma_f32_16x16x32_bf16 v[72:75], v[24:27], v[112:115], v[72:75]
v_cvt_pk_bf16_f32 v126, v68, v69
v_cvt_pk_bf16_f32 v127, v70, v71
v_mfma_f32_16x16x32_bf16 v[72:75], v[36:39], v[116:119], v[72:75]
v_mfma_f32_16x16x32_bf16 v[72:75], v[48:51], v[120:123], v[72:75]
s_sub_u32 s70, s70, 1
s_cbranch_scc0 .Ldn_top7
v_cvt_pk_bf16_f32 v124, v64, v65
v_cvt_pk_bf16_f32 v125, v66, v67
v_cvt_pk_bf16_f32 v126, v68, v69
v_cvt_pk_bf16_f32 v127, v70, v71
global_store_dwordx4 v11, v[124:127], s[38:39] sc1
v_add_u32 v11, 110592, v11
v_cvt_pk_bf16_f32 v128, v72, v73
v_cvt_pk_bf16_f32 v130, v72, v73
v_cvt_pk_bf16_f32 v129, v74, v75
v_cvt_pk_bf16_f32 v131, v74, v75
v_permlane16_swap_b32 v128, v130
s_nop 0
v_permlane16_swap_b32 v129, v131
s_mov_b64 exec, s[76:77]
global_store_dwordx4 v12, v[128:131], s[38:39] sc1
s_mov_b64 exec, -1
v_add_u32 v12, 110592, v12
s_waitcnt vmcnt(2)
s_barrier
s_mov_b64 exec, 1
global_store_dword v15, v16, s[38:39] sc1
s_mov_b64 exec, -1
v_add_u32 v16, 1, v16
s_waitcnt vmcnt(1)
s_barrier
s_mov_b64 exec, 1
global_store_dword v15, v16, s[38:39] sc1
s_mov_b64 exec, -1
s_endpgm
.Ldn_w4:
v_add_u32 v19, -1, v14
s_mov_b32 s76, 0xffff
s_mov_b32 s77, 0xffff
s_mov_b32 s70, 32
s_waitcnt vmcnt(0) lgkmcnt(0)
s_nop 7
.p2align 6
.Ldn_top12:
s_waitcnt lgkmcnt(5)
v_mfma_f32_16x16x32_bf16 v[52:55], a[0:3], v[76:79], 0
s_waitcnt lgkmcnt(4)
v_mfma_f32_16x16x32_bf16 v[52:55], a[12:15], v[80:83], v[52:55]
s_waitcnt lgkmcnt(3)
global_load_dword v19, v13, s[38:39] sc1
v_mfma_f32_16x16x32_bf16 v[52:55], a[24:27], v[84:87], v[52:55]
s_waitcnt lgkmcnt(2)
v_mfma_f32_16x16x32_bf16 v[52:55], a[36:39], v[88:91], v[52:55]
s_waitcnt lgkmcnt(1)
ds_read_b128 v[100:103], v17 offset:6144
v_cvt_pk_bf16_f32 v128, v72, v73
v_mfma_f32_16x16x32_bf16 v[52:55], a[48:51], v[92:95], v[52:55]
s_waitcnt lgkmcnt(1)
v_mfma_f32_16x16x32_bf16 v[52:55], a[60:63], v[96:99], v[52:55]
s_waitcnt vmcnt(7)
s_barrier
v_mfma_f32_16x16x32_bf16 v[56:59], a[4:7], v[76:79], 0
ds_read_b128 v[104:107], v17 offset:7168
v_cvt_pk_bf16_f32 v130, v72, v73
v_mfma_f32_16x16x32_bf16 v[56:59], a[16:19], v[80:83], v[56:59]
v_cvt_pk_bf16_f32 v129, v74, v75
v_cvt_pk_bf16_f32 v131, v74, v75
v_mfma_f32_16x16x32_bf16 v[56:59], a[28:31], v[84:87], v[56:59]
ds_read_b128 v[108:111], v17 offset:8192
v_mfma_f32_16x16x32_bf16 v[56:59], a[40:43], v[88:91], v[56:59]
v_permlane16_swap_b32 v128, v130
v_mfma_f32_16x16x32_bf16 v[56:59], a[52:55], v[92:95], v[56:59]
ds_read_b128 v[112:115], v17 offset:9216
v_mfma_f32_16x16x32_bf16 v[56:59], a[64:67], v[96:99], v[56:59]
global_store_dwordx4 v11, v[124:127], s[38:39] sc1
v_mfma_f32_16x16x32_bf16 v[60:63], a[8:11], v[76:79], 0
ds_read_b128 v[116:119], v17 offset:10240
v_add_u32 v11, 110592, v11
v_mfma_f32_16x16x32_bf16 v[60:63], a[20:23], v[80:83], v[60:63]
v_permlane16_swap_b32 v129, v131
v_mfma_f32_16x16x32_bf16 v[60:63], a[32:35], v[84:87], v[60:63]
ds_read_b128 v[120:123], v17 offset:11264
v_mfma_f32_16x16x32_bf16 v[60:63], a[44:47], v[88:91], v[60:63]
s_mov_b64 exec, s[76:77]
global_store_dwordx4 v12, v[128:131], s[38:39] sc1
s_mov_b64 exec, -1
v_mfma_f32_16x16x32_bf16 v[60:63], a[56:59], v[92:95], v[60:63]
v_add_u32 v12, 110592, v12
v_mfma_f32_16x16x32_bf16 v[60:63], a[68:71], v[96:99], v[60:63]
s_waitcnt lgkmcnt(5)
v_mfma_f32_16x16x32_bf16 v[52:55], a[72:75], v[100:103], v[52:55]
s_waitcnt lgkmcnt(4)
v_mfma_f32_16x16x32_bf16 v[52:55], a[84:87], v[104:107], v[52:55]
s_waitcnt lgkmcnt(3)
v_mfma_f32_16x16x32_bf16 v[52:55], a[96:99], v[108:111], v[52:55]
s_waitcnt lgkmcnt(2)
v_mfma_f32_16x16x32_bf16 v[52:55], a[108:111], v[112:115], v[52:55]
s_waitcnt lgkmcnt(1)
v_mfma_f32_16x16x32_bf16 v[52:55], a[120:123], v[116:119], v[52:55]
s_waitcnt lgkmcnt(0)
ds_read_b128 v[76:79], v17 offset:12288
v_mfma_f32_16x16x32_bf16 v[52:55], a[132:135], v[120:123], v[52:55]
v_mfma_f32_16x16x32_bf16 v[56:59], a[76:79], v[100:103], v[56:59]
ds_read_b128 v[80:83], v17 offset:13312
v_mfma_f32_16x16x32_bf16 v[56:59], a[88:91], v[104:107], v[56:59]
v_mfma_f32_16x16x32_bf16 v[56:59], a[100:103], v[108:111], v[56:59]
ds_read_b128 v[84:87], v17 offset:14336
v_mfma_f32_16x16x32_bf16 v[56:59], a[112:115], v[112:115], v[56:59]
v_mfma_f32_16x16x32_bf16 v[56:59], a[124:127], v[116:119], v[56:59]
ds_read_b128 v[88:91], v17 offset:15360
v_mfma_f32_16x16x32_bf16 v[56:59], a[136:139], v[120:123], v[56:59]
v_mfma_f32_16x16x32_bf16 v[60:63], a[80:83], v[100:103], v[60:63]
ds_read_b128 v[92:95], v17 offset:16384
v_mfma_f32_16x16x32_bf16 v[60:63], a[92:95], v[104:107], v[60:63]
v_mfma_f32_16x16x32_bf16 v[60:63], a[104:107], v[108:111], v[60:63]
ds_read_b128 v[96:99], v17 offset:17408
v_mfma_f32_16x16x32_bf16 v[60:63], a[116:119], v[112:115], v[60:63]
v_mfma_f32_16x16x32_bf16 v[60:63], a[128:131], v[116:119], v[60:63]
v_mfma_f32_16x16x32_bf16 v[60:63], a[140:143], v[120:123], v[60:63]
s_waitcnt lgkmcnt(5)
v_mfma_f32_16x16x32_bf16 v[52:55], a[144:147], v[76:79], v[52:55]
s_waitcnt lgkmcnt(4)
v_mfma_f32_16x16x32_bf16 v[52:55], a[156:159], v[80:83], v[52:55]
s_waitcnt lgkmcnt(3)
v_mfma_f32_16x16x32_bf16 v[52:55], a[168:171], v[84:87], v[52:55]
s_waitcnt lgkmcnt(2)
v_mfma_f32_16x16x32_bf16 v[52:55], a[180:183], v[88:91], v[52:55]
s_waitcnt vmcnt(2)
v_sub_u32 v19, v19, v14
v_cmp_lt_u32 vcc, 0x7f, v19
s_waitcnt lgkmcnt(1)
v_mfma_f32_16x16x32_bf16 v[52:55], a[192:195], v[92:95], v[52:55]
s_xor_b32 s45, s45, 0x10000
ds_read_b128 v[100:103], v17 offset:18432
s_waitcnt lgkmcnt(1)
v_mfma_f32_16x16x32_bf16 v[52:55], a[204:207], v[96:99], v[52:55]
s_add_u32 m0, s45, 0
v_mfma_f32_16x16x32_bf16 v[56:59], a[148:151], v[76:79], v[56:59]
s_cbranch_vccnz .Ldn_sp8
.Ldn_rt9:
ds_read_b128 v[104:107], v17 offset:19456
ds_read_b128 v[108:111], v17 offset:20480
v_mfma_f32_16x16x32_bf16 v[56:59], a[160:163], v[80:83], v[56:59]
v_mfma_f32_16x16x32_bf16 v[56:59], a[172:175], v[84:87], v[56:59]
s_barrier
v_mfma_f32_16x16x32_bf16 v[56:59], a[184:187], v[88:91], v[56:59]
v_add_u32 v14, 1, v14
v_mfma_f32_16x16x32_bf16 v[56:59], a[196:199], v[92:95], v[56:59]
ds_read_b128 v[112:115], v17 offset:21504
v_mfma_f32_16x16x32_bf16 v[56:59], a[208:211], v[96:99], v[56:59]
v_mfma_f32_16x16x32_bf16 v[60:63], a[152:155], v[76:79], v[60:63]
ds_read_b128 v[116:119], v17 offset:22528
global_load_lds_dwordx4 v9, s[38:39] offset:-2048 sc1
v_mfma_f32_16x16x32_bf16 v[60:63], a[164:167], v[80:83], v[60:63]
v_mfma_f32_16x16x32_bf16 v[60:63], a[176:179], v[84:87], v[60:63]
ds_read_b128 v[120:123], v17 offset:23552
v_mfma_f32_16x16x32_bf16 v[60:63], a[188:191], v[88:91], v[60:63]
global_load_lds_dwordx4 v9, s[38:39] offset:-1024 sc1
v_mfma_f32_16x16x32_bf16 v[60:63], a[200:203], v[92:95], v[60:63]
v_mfma_f32_16x16x32_bf16 v[60:63], a[212:215], v[96:99], v[60:63]
s_waitcnt lgkmcnt(5)
v_mfma_f32_16x16x32_bf16 v[52:55], a[216:219], v[100:103], v[52:55]
s_waitcnt lgkmcnt(4)
global_load_lds_dwordx4 v9, s[38:39] offset:0 sc1
v_mfma_f32_16x16x32_bf16 v[52:55], a[228:231], v[104:107], v[52:55]
s_waitcnt lgkmcnt(3)
v_mfma_f32_16x16x32_bf16 v[52:55], a[240:243], v[108:111], v[52:55]
s_waitcnt lgkmcnt(2)
v_mfma_f32_16x16x32_bf16 v[52:55], a[252:255], v[112:115], v[52:55]
s_waitcnt lgkmcnt(1)
global_load_lds_dwordx4 v9, s[38:39] offset:1024 sc1
v_mfma_f32_16x16x32_bf16 v[52:55], v[28:31], v[116:119], v[52:55]
s_waitcnt lgkmcnt(0)
v_mfma_f32_16x16x32_bf16 v[52:55], v[40:43], v[120:123], v[52:55]
v_mfma_f32_16x16x32_bf16 v[56:59], a[220:223], v[100:103], v[56:59]
global_load_lds_dwordx4 v9, s[38:39] offset:2048 sc1
v_mfma_f32_16x16x32_bf16 v[56:59], a[232:235], v[104:107], v[56:59]
v_mfma_f32_16x16x32_bf16 v[56:59], a[244:247], v[108:111], v[56:59]
s_barrier
v_mfma_f32_16x16x32_bf16 v[56:59], v[20:23], v[112:115], v[56:59]
ds_read_b128 v[76:79], v17 offset:24576
global_load_lds_dwordx4 v9, s[38:39] offset:3072 sc1
v_mfma_f32_16x16x32_bf16 v[56:59], v[32:35], v[116:119], v[56:59]
ds_read_b128 v[80:83], v17 offset:25600
v_add_u32 v9, 147456, v9
v_mfma_f32_16x16x32_bf16 v[56:59], v[44:47], v[120:123], v[56:59]
ds_read_b128 v[84:87], v17 offset:26624
v_min_u32 v9, v9, v10
v_mfma_f32_16x16x32_bf16 v[60:63], a[224:227], v[100:103], v[60:63]
ds_read_b128 v[88:91], v17 offset:27648
v_cvt_pk_bf16_f32 v124, v52, v53
v_mfma_f32_16x16x32_bf16 v[60:63], a[236:239], v[104:107], v[60:63]
ds_read_b128 v[92:95], v17 offset:28672
v_cvt_pk_bf16_f32 v125, v54, v55
v_mfma_f32_16x16x32_bf16 v[60:63], a[248:251], v[108:111], v[60:63]
ds_read_b128 v[96:99], v17 offset:29696
v_mfma_f32_16x16x32_bf16 v[60:63], v[24:27], v[112:115], v[60:63]
v_cvt_pk_bf16_f32 v126, v56, v57
v_cvt_pk_bf16_f32 v127, v58, v59
v_mfma_f32_16x16x32_bf16 v[60:63], v[36:39], v[116:119], v[60:63]
v_mfma_f32_16x16x32_bf16 v[60:63], v[48:51], v[120:123], v[60:63]
s_waitcnt lgkmcnt(5)
v_mfma_f32_16x16x32_bf16 v[64:67], a[0:3], v[76:79], 0
s_waitcnt lgkmcnt(4)
v_mfma_f32_16x16x32_bf16 v[64:67], a[12:15], v[80:83], v[64:67]
s_waitcnt lgkmcnt(3)
global_load_dword v19, v13, s[38:39] sc1
v_mfma_f32_16x16x32_bf16 v[64:67], a[24:27], v[84:87], v[64:67]
s_waitcnt lgkmcnt(2)
v_mfma_f32_16x16x32_bf16 v[64:67], a[36:39], v[88:91], v[64:67]
s_waitcnt lgkmcnt(1)
ds_read_b128 v[100:103], v17 offset:30720
v_cvt_pk_bf16_f32 v128, v60, v61
v_mfma_f32_16x16x32_bf16 v[64:67], a[48:51], v[92:95], v[64:67]
s_waitcnt lgkmcnt(1)
v_mfma_f32_16x16x32_bf16 v[64:67], a[60:63], v[96:99], v[64:67]
s_waitcnt vmcnt(7)
s_barrier
v_mfma_f32_16x16x32_bf16 v[68:71], a[4:7], v[76:79], 0
ds_read_b128 v[104:107], v17 offset:31744
v_cvt_pk_bf16_f32 v130, v60, v61
v_mfma_f32_16x16x32_bf16 v[68:71], a[16:19], v[80:83], v[68:71]
v_cvt_pk_bf16_f32 v129, v62, v63
v_cvt_pk_bf16_f32 v131, v62, v63
v_mfma_f32_16x16x32_bf16 v[68:71], a[28:31], v[84:87], v[68:71]
ds_read_b128 v[108:111], v17 offset:32768
v_mfma_f32_16x16x32_bf16 v[68:71], a[40:43], v[88:91], v[68:71]
v_permlane16_swap_b32 v128, v130
v_mfma_f32_16x16x32_bf16 v[68:71], a[52:55], v[92:95], v[68:71]
ds_read_b128 v[112:115], v17 offset:33792
v_mfma_f32_16x16x32_bf16 v[68:71], a[64:67], v[96:99], v[68:71]
global_store_dwordx4 v11, v[124:127], s[38:39] sc1
v_mfma_f32_16x16x32_bf16 v[72:75], a[8:11], v[76:79], 0
ds_read_b128 v[116:119], v17 offset:34816
v_add_u32 v11, 110592, v11
v_mfma_f32_16x16x32_bf16 v[72:75], a[20:23], v[80:83], v[72:75]
v_permlane16_swap_b32 v129, v131
v_mfma_f32_16x16x32_bf16 v[72:75], a[32:35], v[84:87], v[72:75]
ds_read_b128 v[120:123], v17 offset:35840
v_mfma_f32_16x16x32_bf16 v[72:75], a[44:47], v[88:91], v[72:75]
s_mov_b64 exec, s[76:77]
global_store_dwordx4 v12, v[128:131], s[38:39] sc1
s_mov_b64 exec, -1
v_mfma_f32_16x16x32_bf16 v[72:75], a[56:59], v[92:95], v[72:75]
v_add_u32 v12, 110592, v12
v_mfma_f32_16x16x32_bf16 v[72:75], a[68:71], v[96:99], v[72:75]
s_waitcnt lgkmcnt(5)
v_mfma_f32_16x16x32_bf16 v[64:67], a[72:75], v[100:103], v[64:67]
s_waitcnt lgkmcnt(4)
v_mfma_f32_16x16x32_bf16 v[64:67], a[84:87], v[104:107], v[64:67]
s_waitcnt lgkmcnt(3)
v_mfma_f32_16x16x32_bf16 v[64:67], a[96:99], v[108:111], v[64:67]
s_waitcnt lgkmcnt(2)
v_mfma_f32_16x16x32_bf16 v[64:67], a[108:111], v[112:115], v[64:67]
s_waitcnt lgkmcnt(1)
v_mfma_f32_16x16x32_bf16 v[64:67], a[120:123], v[116:119], v[64:67]
s_waitcnt lgkmcnt(0)
ds_read_b128 v[76:79], v17 offset:36864
v_mfma_f32_16x16x32_bf16 v[64:67], a[132:135], v[120:123], v[64:67]
v_mfma_f32_16x16x32_bf16 v[68:71], a[76:79], v[100:103], v[68:71]
ds_read_b128 v[80:83], v17 offset:37888
v_mfma_f32_16x16x32_bf16 v[68:71], a[88:91], v[104:107], v[68:71]
v_mfma_f32_16x16x32_bf16 v[68:71], a[100:103], v[108:111], v[68:71]
ds_read_b128 v[84:87], v17 offset:38912
v_mfma_f32_16x16x32_bf16 v[68:71], a[112:115], v[112:115], v[68:71]
v_mfma_f32_16x16x32_bf16 v[68:71], a[124:127], v[116:119], v[68:71]
ds_read_b128 v[88:91], v17 offset:39936
v_mfma_f32_16x16x32_bf16 v[68:71], a[136:139], v[120:123], v[68:71]
v_mfma_f32_16x16x32_bf16 v[72:75], a[80:83], v[100:103], v[72:75]
ds_read_b128 v[92:95], v17 offset:40960
v_mfma_f32_16x16x32_bf16 v[72:75], a[92:95], v[104:107], v[72:75]
v_mfma_f32_16x16x32_bf16 v[72:75], a[104:107], v[108:111], v[72:75]
ds_read_b128 v[96:99], v17 offset:41984
v_mfma_f32_16x16x32_bf16 v[72:75], a[116:119], v[112:115], v[72:75]
v_mfma_f32_16x16x32_bf16 v[72:75], a[128:131], v[116:119], v[72:75]
v_mfma_f32_16x16x32_bf16 v[72:75], a[140:143], v[120:123], v[72:75]
s_waitcnt lgkmcnt(5)
v_mfma_f32_16x16x32_bf16 v[64:67], a[144:147], v[76:79], v[64:67]
s_waitcnt lgkmcnt(4)
v_mfma_f32_16x16x32_bf16 v[64:67], a[156:159], v[80:83], v[64:67]
s_waitcnt lgkmcnt(3)
v_mfma_f32_16x16x32_bf16 v[64:67], a[168:171], v[84:87], v[64:67]
s_waitcnt lgkmcnt(2)
v_mfma_f32_16x16x32_bf16 v[64:67], a[180:183], v[88:91], v[64:67]
s_waitcnt vmcnt(2)
v_sub_u32 v19, v19, v14
v_cmp_lt_u32 vcc, 0x7f, v19
s_waitcnt lgkmcnt(1)
v_mfma_f32_16x16x32_bf16 v[64:67], a[192:195], v[92:95], v[64:67]
s_waitcnt lgkmcnt(0)
ds_read_b128 v[100:103], v17 offset:43008
v_mfma_f32_16x16x32_bf16 v[64:67], a[204:207], v[96:99], v[64:67]
)"
R"(s_add_u32 m0, s45, 24576
v_mfma_f32_16x16x32_bf16 v[68:71], a[148:151], v[76:79], v[68:71]
s_cbranch_vccnz .Ldn_sp10
.Ldn_rt11:
ds_read_b128 v[104:107], v17 offset:44032
ds_read_b128 v[108:111], v17 offset:45056
v_mfma_f32_16x16x32_bf16 v[68:71], a[160:163], v[80:83], v[68:71]
v_mfma_f32_16x16x32_bf16 v[68:71], a[172:175], v[84:87], v[68:71]
s_barrier
v_mfma_f32_16x16x32_bf16 v[68:71], a[184:187], v[88:91], v[68:71]
v_add_u32 v14, 1, v14
v_mfma_f32_16x16x32_bf16 v[68:71], a[196:199], v[92:95], v[68:71]
ds_read_b128 v[112:115], v17 offset:46080
v_mfma_f32_16x16x32_bf16 v[68:71], a[208:211], v[96:99], v[68:71]
v_mfma_f32_16x16x32_bf16 v[72:75], a[152:155], v[76:79], v[72:75]
ds_read_b128 v[116:119], v17 offset:47104
global_load_lds_dwordx4 v9, s[38:39] offset:-2048 sc1
v_mfma_f32_16x16x32_bf16 v[72:75], a[164:167], v[80:83], v[72:75]
v_mfma_f32_16x16x32_bf16 v[72:75], a[176:179], v[84:87], v[72:75]
ds_read_b128 v[120:123], v17 offset:48128
v_mfma_f32_16x16x32_bf16 v[72:75], a[188:191], v[88:91], v[72:75]
global_load_lds_dwordx4 v9, s[38:39] offset:-1024 sc1
v_mfma_f32_16x16x32_bf16 v[72:75], a[200:203], v[92:95], v[72:75]
v_mfma_f32_16x16x32_bf16 v[72:75], a[212:215], v[96:99], v[72:75]
s_waitcnt lgkmcnt(5)
v_mfma_f32_16x16x32_bf16 v[64:67], a[216:219], v[100:103], v[64:67]
s_waitcnt lgkmcnt(4)
global_load_lds_dwordx4 v9, s[38:39] offset:0 sc1
v_mfma_f32_16x16x32_bf16 v[64:67], a[228:231], v[104:107], v[64:67]
s_waitcnt lgkmcnt(3)
v_mfma_f32_16x16x32_bf16 v[64:67], a[240:243], v[108:111], v[64:67]
s_waitcnt lgkmcnt(2)
v_mfma_f32_16x16x32_bf16 v[64:67], a[252:255], v[112:115], v[64:67]
s_waitcnt lgkmcnt(1)
global_load_lds_dwordx4 v9, s[38:39] offset:1024 sc1
v_mfma_f32_16x16x32_bf16 v[64:67], v[28:31], v[116:119], v[64:67]
s_waitcnt lgkmcnt(0)
v_mfma_f32_16x16x32_bf16 v[64:67], v[40:43], v[120:123], v[64:67]
v_mfma_f32_16x16x32_bf16 v[68:71], a[220:223], v[100:103], v[68:71]
v_xor_b32 v17, 0x10000, v17
global_load_lds_dwordx4 v9, s[38:39] offset:2048 sc1
v_mfma_f32_16x16x32_bf16 v[68:71], a[232:235], v[104:107], v[68:71]
v_mfma_f32_16x16x32_bf16 v[68:71], a[244:247], v[108:111], v[68:71]
s_barrier
v_mfma_f32_16x16x32_bf16 v[68:71], v[20:23], v[112:115], v[68:71]
ds_read_b128 v[76:79], v17 offset:0
global_load_lds_dwordx4 v9, s[38:39] offset:3072 sc1
v_mfma_f32_16x16x32_bf16 v[68:71], v[32:35], v[116:119], v[68:71]
ds_read_b128 v[80:83], v17 offset:1024
v_add_u32 v9, 147456, v9
v_mfma_f32_16x16x32_bf16 v[68:71], v[44:47], v[120:123], v[68:71]
ds_read_b128 v[84:87], v17 offset:2048
v_min_u32 v9, v9, v10
v_mfma_f32_16x16x32_bf16 v[72:75], a[224:227], v[100:103], v[72:75]
ds_read_b128 v[88:91], v17 offset:3072
v_cvt_pk_bf16_f32 v124, v64, v65
v_mfma_f32_16x16x32_bf16 v[72:75], a[236:239], v[104:107], v[72:75]
ds_read_b128 v[92:95], v17 offset:4096
v_cvt_pk_bf16_f32 v125, v66, v67
v_mfma_f32_16x16x32_bf16 v[72:75], a[248:251], v[108:111], v[72:75]
ds_read_b128 v[96:99], v17 offset:5120
v_mfma_f32_16x16x32_bf16 v[72:75], v[24:27], v[112:115], v[72:75]
v_cvt_pk_bf16_f32 v126, v68, v69
v_cvt_pk_bf16_f32 v127, v70, v71
v_mfma_f32_16x16x32_bf16 v[72:75], v[36:39], v[116:119], v[72:75]
v_mfma_f32_16x16x32_bf16 v[72:75], v[48:51], v[120:123], v[72:75]
s_sub_u32 s70, s70, 1
s_cbranch_scc0 .Ldn_top12
v_cvt_pk_bf16_f32 v124, v64, v65
v_cvt_pk_bf16_f32 v125, v66, v67
v_cvt_pk_bf16_f32 v126, v68, v69
v_cvt_pk_bf16_f32 v127, v70, v71
global_store_dwordx4 v11, v[124:127], s[38:39] sc1
v_add_u32 v11, 110592, v11
v_cvt_pk_bf16_f32 v128, v72, v73
v_cvt_pk_bf16_f32 v130, v72, v73
v_cvt_pk_bf16_f32 v129, v74, v75
v_cvt_pk_bf16_f32 v131, v74, v75
v_permlane16_swap_b32 v128, v130
s_nop 0
v_permlane16_swap_b32 v129, v131
s_mov_b64 exec, s[76:77]
global_store_dwordx4 v12, v[128:131], s[38:39] sc1
s_mov_b64 exec, -1
v_add_u32 v12, 110592, v12
s_waitcnt vmcnt(2)
s_barrier
s_waitcnt vmcnt(0)
s_barrier
s_endpgm
.Ldn_sp8:
s_sleep 1
global_load_dword v19, v13, s[38:39] sc1
s_add_u32 s71, s71, 1
s_waitcnt vmcnt(0)
v_sub_u32 v19, v19, v14
v_cmp_lt_u32 vcc, 0x7f, v19
s_cbranch_vccz .Ldn_rt9
s_cmp_lt_u32 s71, 3000000
s_cbranch_scc1 .Ldn_sp8
s_branch .Ldn_rt9
.Ldn_sp10:
s_sleep 1
global_load_dword v19, v13, s[38:39] sc1
s_add_u32 s71, s71, 1
s_waitcnt vmcnt(0)
v_sub_u32 v19, v19, v14
v_cmp_lt_u32 vcc, 0x7f, v19
s_cbranch_vccz .Ldn_rt11
s_cmp_lt_u32 s71, 3000000
s_cbranch_scc1 .Ldn_sp10
s_branch .Ldn_rt11
.Lrole3:
v_add_u32 v255, 35840, v0
v_add_u32 v150, 71680, v0
v_add_u32 v151, 107520, v0
v_add_u32 v152, 143360, v0
v_add_u32 v153, 179200, v0
v_add_u32 v154, 215040, v0
v_add_u32 v155, 250880, v0
s_sleep 20
s_add_u32 m0, s42, 4096
s_nop 0
global_load_lds_dwordx4 v0, s[40:41] offset:-4096 nt
global_load_lds_dwordx4 v255, s[40:41] offset:-3072 nt
global_load_lds_dwordx4 v150, s[40:41] offset:-2048 nt
global_load_lds_dwordx4 v151, s[40:41] offset:-1024 nt
global_load_lds_dwordx4 v152, s[40:41] offset:0 nt
global_load_lds_dwordx4 v153, s[40:41] offset:1024 nt
global_load_lds_dwordx4 v154, s[40:41] offset:2048 nt
global_load_lds_dwordx4 v155, s[40:41] offset:3072 nt
s_add_u32 s40, s40, 589824
s_addc_u32 s41, s41, 0
s_add_u32 m0, s42, 36864
s_nop 0
global_load_lds_dwordx4 v0, s[40:41] offset:-4096 nt
global_load_lds_dwordx4 v255, s[40:41] offset:-3072 nt
global_load_lds_dwordx4 v150, s[40:41] offset:-2048 nt
global_load_lds_dwordx4 v151, s[40:41] offset:-1024 nt
global_load_lds_dwordx4 v152, s[40:41] offset:0 nt
global_load_lds_dwordx4 v153, s[40:41] offset:1024 nt
global_load_lds_dwordx4 v154, s[40:41] offset:2048 nt
global_load_lds_dwordx4 v155, s[40:41] offset:3072 nt
s_add_u32 s40, s40, 589824
s_addc_u32 s41, s41, 0
s_add_u32 m0, s42, 69632
s_nop 0
global_load_lds_dwordx4 v0, s[40:41] offset:-4096 nt
global_load_lds_dwordx4 v255, s[40:41] offset:-3072 nt
global_load_lds_dwordx4 v150, s[40:41] offset:-2048 nt
global_load_lds_dwordx4 v151, s[40:41] offset:-1024 nt
global_load_lds_dwordx4 v152, s[40:41] offset:0 nt
global_load_lds_dwordx4 v153, s[40:41] offset:1024 nt
global_load_lds_dwordx4 v154, s[40:41] offset:2048 nt
global_load_lds_dwordx4 v155, s[40:41] offset:3072 nt
s_add_u32 s40, s40, 589824
s_addc_u32 s41, s41, 0
v_add_u32 v156, 0x10000, v1
v_mov_b32 v174, 0x3F803F80
v_mov_b32 v175, 0x3F803F80
v_mov_b32 v176, 0x3F803F80
v_mov_b32 v177, 0x3F803F80
s_waitcnt lgkmcnt(0)
s_mul_i32 s37, s37, 0x9E3779B1
s_add_u32 s37, s37, 0x7F4A7C15
v_add_u32 v5, s37, v5
v_add_u32 v7, s37, v7
v_add_u32 v17, s37, v17
s_waitcnt vmcnt(16)
s_barrier
s_add_u32 m0, s42, 102400
ds_read_b64_tr_b16 a[0:1], v1 offset:0
ds_read_b64_tr_b16 a[2:3], v1 offset:1024
global_load_lds_dwordx4 v0, s[40:41] offset:-4096 nt
ds_read_b64_tr_b16 a[4:5], v1 offset:8
global_load_lds_dwordx4 v255, s[40:41] offset:-3072 nt
ds_read_b64_tr_b16 a[6:7], v1 offset:1032
ds_read_b64_tr_b16 a[8:9], v1 offset:16384
global_load_lds_dwordx4 v150, s[40:41] offset:-2048 nt
ds_read_b64_tr_b16 a[10:11], v1 offset:17408
global_load_lds_dwordx4 v151, s[40:41] offset:-1024 nt
ds_read_b64_tr_b16 a[12:13], v1 offset:16392
ds_read_b64_tr_b16 a[14:15], v1 offset:17416
global_load_lds_dwordx4 v152, s[40:41] offset:0 nt
ds_read_b64_tr_b16 a[16:17], v1 offset:8192
global_load_lds_dwordx4 v153, s[40:41] offset:1024 nt
ds_read_b64_tr_b16 a[18:19], v1 offset:9216
ds_read_b64_tr_b16 a[20:21], v1 offset:8200
global_load_lds_dwordx4 v154, s[40:41] offset:2048 nt
ds_read_b64_tr_b16 a[22:23], v1 offset:9224
global_load_lds_dwordx4 v155, s[40:41] offset:3072 nt
ds_read_b64_tr_b16 a[24:25], v1 offset:24576
ds_read_b64_tr_b16 a[26:27], v1 offset:25600
s_add_u32 s40, s40, 589824
ds_read_b64_tr_b16 a[28:29], v1 offset:24584
s_addc_u32 s41, s41, 0
ds_read_b64_tr_b16 a[30:31], v1 offset:25608
s_waitcnt vmcnt(16) lgkmcnt(0)
s_barrier
s_add_u32 m0, s42, 4096
ds_read_b64_tr_b16 a[32:33], v1 offset:32768
v_mfma_f32_16x16x32_bf16 v[158:161], v[174:177], a[0:3], 0
ds_read_b64_tr_b16 a[34:35], v1 offset:33792
global_load_lds_dwordx4 v0, s[40:41] offset:-4096 nt
ds_read_b64_tr_b16 a[36:37], v1 offset:32776
ds_read_b64_tr_b16 a[38:39], v1 offset:33800
global_load_lds_dwordx4 v255, s[40:41] offset:-3072 nt
ds_read_b64_tr_b16 a[40:41], v1 offset:49152
v_mfma_f32_16x16x32_bf16 v[162:165], v[174:177], a[4:7], 0
global_load_lds_dwordx4 v150, s[40:41] offset:-2048 nt
ds_read_b64_tr_b16 a[42:43], v1 offset:50176
ds_read_b64_tr_b16 a[44:45], v1 offset:49160
global_load_lds_dwordx4 v151, s[40:41] offset:-1024 nt
ds_read_b64_tr_b16 a[46:47], v1 offset:50184
ds_read_b64_tr_b16 a[48:49], v1 offset:40960
global_load_lds_dwordx4 v152, s[40:41] offset:0 nt
v_mfma_f32_16x16x32_bf16 v[166:169], v[174:177], a[8:11], 0
ds_read_b64_tr_b16 a[50:51], v1 offset:41984
ds_read_b64_tr_b16 a[52:53], v1 offset:40968
global_load_lds_dwordx4 v153, s[40:41] offset:1024 nt
ds_read_b64_tr_b16 a[54:55], v1 offset:41992
ds_read_b64_tr_b16 a[56:57], v1 offset:57344
global_load_lds_dwordx4 v154, s[40:41] offset:2048 nt
v_mfma_f32_16x16x32_bf16 v[170:173], v[174:177], a[12:15], 0
ds_read_b64_tr_b16 a[58:59], v1 offset:58368
global_load_lds_dwordx4 v155, s[40:41] offset:3072 nt
ds_read_b64_tr_b16 a[60:61], v1 offset:57352
ds_read_b64_tr_b16 a[62:63], v1 offset:58376
s_add_u32 s40, s40, 589824
v_mfma_f32_16x16x32_bf16 v[158:161], v[174:177], a[16:19], v[158:161]
v_mfma_f32_16x16x32_bf16 v[162:165], v[174:177], a[20:23], v[162:165]
s_addc_u32 s41, s41, 0
v_mfma_f32_16x16x32_bf16 v[166:169], v[174:177], a[24:27], v[166:169]
v_mfma_f32_16x16x32_bf16 v[170:173], v[174:177], a[28:31], v[170:173]
s_waitcnt vmcnt(16) lgkmcnt(0)
s_barrier
s_add_u32 m0, s42, 36864
ds_read_b64_tr_b16 a[64:65], v156 offset:0
v_mfma_f32_16x16x32_bf16 v[158:161], v[174:177], a[32:35], v[158:161]
ds_read_b64_tr_b16 a[66:67], v156 offset:1024
global_load_lds_dwordx4 v0, s[40:41] offset:-4096 nt
ds_read_b64_tr_b16 a[68:69], v156 offset:8
ds_read_b64_tr_b16 a[70:71], v156 offset:1032
global_load_lds_dwordx4 v255, s[40:41] offset:-3072 nt
ds_read_b64_tr_b16 a[72:73], v156 offset:16384
v_mfma_f32_16x16x32_bf16 v[162:165], v[174:177], a[36:39], v[162:165]
global_load_lds_dwordx4 v150, s[40:41] offset:-2048 nt
ds_read_b64_tr_b16 a[74:75], v156 offset:17408
ds_read_b64_tr_b16 a[76:77], v156 offset:16392
global_load_lds_dwordx4 v151, s[40:41] offset:-1024 nt
ds_read_b64_tr_b16 a[78:79], v156 offset:17416
ds_read_b64_tr_b16 a[80:81], v156 offset:8192
global_load_lds_dwordx4 v152, s[40:41] offset:0 nt
v_mfma_f32_16x16x32_bf16 v[166:169], v[174:177], a[40:43], v[166:169]
ds_read_b64_tr_b16 a[82:83], v156 offset:9216
ds_read_b64_tr_b16 a[84:85], v156 offset:8200
global_load_lds_dwordx4 v153, s[40:41] offset:1024 nt
ds_read_b64_tr_b16 a[86:87], v156 offset:9224
ds_read_b64_tr_b16 a[88:89], v156 offset:24576
global_load_lds_dwordx4 v154, s[40:41] offset:2048 nt
v_mfma_f32_16x16x32_bf16 v[170:173], v[174:177], a[44:47], v[170:173]
ds_read_b64_tr_b16 a[90:91], v156 offset:25600
global_load_lds_dwordx4 v155, s[40:41] offset:3072 nt
ds_read_b64_tr_b16 a[92:93], v156 offset:24584
ds_read_b64_tr_b16 a[94:95], v156 offset:25608
s_add_u32 s40, s40, 589824
v_mfma_f32_16x16x32_bf16 v[158:161], v[174:177], a[48:51], v[158:161]
v_mfma_f32_16x16x32_bf16 v[162:165], v[174:177], a[52:55], v[162:165]
s_addc_u32 s41, s41, 0
v_mfma_f32_16x16x32_bf16 v[166:169], v[174:177], a[56:59], v[166:169]
v_mfma_f32_16x16x32_bf16 v[170:173], v[174:177], a[60:63], v[170:173]
s_waitcnt vmcnt(16) lgkmcnt(0)
s_barrier
s_add_u32 m0, s42, 69632
ds_read_b64_tr_b16 a[96:97], v156 offset:32768
v_mfma_f32_16x16x32_bf16 v[158:161], v[174:177], a[64:67], v[158:161]
ds_read_b64_tr_b16 a[98:99], v156 offset:33792
global_load_lds_dwordx4 v0, s[40:41] offset:-4096 nt
ds_read_b64_tr_b16 a[100:101], v156 offset:32776
ds_read_b64_tr_b16 a[102:103], v156 offset:33800
global_load_lds_dwordx4 v255, s[40:41] offset:-3072 nt
ds_read_b64_tr_b16 a[104:105], v156 offset:49152
v_mfma_f32_16x16x32_bf16 v[162:165], v[174:177], a[68:71], v[162:165]
global_load_lds_dwordx4 v150, s[40:41] offset:-2048 nt
ds_read_b64_tr_b16 a[106:107], v156 offset:50176
ds_read_b64_tr_b16 a[108:109], v156 offset:49160
global_load_lds_dwordx4 v151, s[40:41] offset:-1024 nt
ds_read_b64_tr_b16 a[110:111], v156 offset:50184
ds_read_b64_tr_b16 a[112:113], v156 offset:40960
global_load_lds_dwordx4 v152, s[40:41] offset:0 nt
v_mfma_f32_16x16x32_bf16 v[166:169], v[174:177], a[72:75], v[166:169]
ds_read_b64_tr_b16 a[114:115], v156 offset:41984
ds_read_b64_tr_b16 a[116:117], v156 offset:40968
global_load_lds_dwordx4 v153, s[40:41] offset:1024 nt
ds_read_b64_tr_b16 a[118:119], v156 offset:41992
ds_read_b64_tr_b16 a[120:121], v156 offset:57344
global_load_lds_dwordx4 v154, s[40:41] offset:2048 nt
v_mfma_f32_16x16x32_bf16 v[170:173], v[174:177], a[76:79], v[170:173]
ds_read_b64_tr_b16 a[122:123], v156 offset:58368
global_load_lds_dwordx4 v155, s[40:41] offset:3072 nt
ds_read_b64_tr_b16 a[124:125], v156 offset:57352
ds_read_b64_tr_b16 a[126:127], v156 offset:58376
s_add_u32 s40, s40, 589824
v_mfma_f32_16x16x32_bf16 v[158:161], v[174:177], a[80:83], v[158:161]
v_mfma_f32_16x16x32_bf16 v[162:165], v[174:177], a[84:87], v[162:165]
s_addc_u32 s41, s41, 0
v_mfma_f32_16x16x32_bf16 v[166:169], v[174:177], a[88:91], v[166:169]
v_mfma_f32_16x16x32_bf16 v[170:173], v[174:177], a[92:95], v[170:173]
s_waitcnt vmcnt(16) lgkmcnt(0)
s_barrier
s_add_u32 m0, s42, 102400
ds_read_b64_tr_b16 a[128:129], v1 offset:0
v_mfma_f32_16x16x32_bf16 v[158:161], v[174:177], a[96:99], v[158:161]
ds_read_b64_tr_b16 a[130:131], v1 offset:1024
global_load_lds_dwordx4 v0, s[40:41] offset:-4096 nt
ds_read_b64_tr_b16 a[132:133], v1 offset:8
ds_read_b64_tr_b16 a[134:135], v1 offset:1032
global_load_lds_dwordx4 v255, s[40:41] offset:-3072 nt
ds_read_b64_tr_b16 a[136:137], v1 offset:16384
v_mfma_f32_16x16x32_bf16 v[162:165], v[174:177], a[100:103], v[162:165]
global_load_lds_dwordx4 v150, s[40:41] offset:-2048 nt
ds_read_b64_tr_b16 a[138:139], v1 offset:17408
ds_read_b64_tr_b16 a[140:141], v1 offset:16392
global_load_lds_dwordx4 v151, s[40:41] offset:-1024 nt
ds_read_b64_tr_b16 a[142:143], v1 offset:17416
ds_read_b64_tr_b16 a[144:145], v1 offset:8192
global_load_lds_dwordx4 v152, s[40:41] offset:0 nt
v_mfma_f32_16x16x32_bf16 v[166:169], v[174:177], a[104:107], v[166:169]
ds_read_b64_tr_b16 a[146:147], v1 offset:9216
ds_read_b64_tr_b16 a[148:149], v1 offset:8200
global_load_lds_dwordx4 v153, s[40:41] offset:1024 nt
ds_read_b64_tr_b16 a[150:151], v1 offset:9224
ds_read_b64_tr_b16 a[152:153], v1 offset:24576
global_load_lds_dwordx4 v154, s[40:41] offset:2048 nt
v_mfma_f32_16x16x32_bf16 v[170:173], v[174:177], a[108:111], v[170:173]
ds_read_b64_tr_b16 a[154:155], v1 offset:25600
global_load_lds_dwordx4 v155, s[40:41] offset:3072 nt
ds_read_b64_tr_b16 a[156:157], v1 offset:24584
ds_read_b64_tr_b16 a[158:159], v1 offset:25608
s_add_u32 s40, s40, 589824
v_mfma_f32_16x16x32_bf16 v[158:161], v[174:177], a[112:115], v[158:161]
v_mfma_f32_16x16x32_bf16 v[162:165], v[174:177], a[116:119], v[162:165]
s_addc_u32 s41, s41, 0
v_mfma_f32_16x16x32_bf16 v[166:169], v[174:177], a[120:123], v[166:169]
v_mfma_f32_16x16x32_bf16 v[170:173], v[174:177], a[124:127], v[170:173]
s_waitcnt vmcnt(16) lgkmcnt(0)
s_barrier
s_add_u32 m0, s42, 4096
ds_read_b64_tr_b16 a[160:161], v1 offset:32768
v_mfma_f32_16x16x32_bf16 v[158:161], v[174:177], a[128:131], v[158:161]
ds_read_b64_tr_b16 a[162:163], v1 offset:33792
global_load_lds_dwordx4 v0, s[40:41] offset:-4096 nt
ds_read_b64_tr_b16 a[164:165], v1 offset:32776
ds_read_b64_tr_b16 a[166:167], v1 offset:33800
global_load_lds_dwordx4 v255, s[40:41] offset:-3072 nt
ds_read_b64_tr_b16 a[168:169], v1 offset:49152
v_mfma_f32_16x16x32_bf16 v[162:165], v[174:177], a[132:135], v[162:165]
global_load_lds_dwordx4 v150, s[40:41] offset:-2048 nt
ds_read_b64_tr_b16 a[170:171], v1 offset:50176
ds_read_b64_tr_b16 a[172:173], v1 offset:49160
global_load_lds_dwordx4 v151, s[40:41] offset:-1024 nt
ds_read_b64_tr_b16 a[174:175], v1 offset:50184
ds_read_b64_tr_b16 a[176:177], v1 offset:40960
global_load_lds_dwordx4 v152, s[40:41] offset:0 nt
v_mfma_f32_16x16x32_bf16 v[166:169], v[174:177], a[136:139], v[166:169]
ds_read_b64_tr_b16 a[178:179], v1 offset:41984
ds_read_b64_tr_b16 a[180:181], v1 offset:40968
global_load_lds_dwordx4 v153, s[40:41] offset:1024 nt
ds_read_b64_tr_b16 a[182:183], v1 offset:41992
ds_read_b64_tr_b16 a[184:185], v1 offset:57344
global_load_lds_dwordx4 v154, s[40:41] offset:2048 nt
v_mfma_f32_16x16x32_bf16 v[170:173], v[174:177], a[140:143], v[170:173]
ds_read_b64_tr_b16 a[186:187], v1 offset:58368
global_load_lds_dwordx4 v155, s[40:41] offset:3072 nt
ds_read_b64_tr_b16 a[188:189], v1 offset:57352
ds_read_b64_tr_b16 a[190:191], v1 offset:58376
s_sub_u32 s40, s40, 4718592
v_mfma_f32_16x16x32_bf16 v[158:161], v[174:177], a[144:147], v[158:161]
v_mfma_f32_16x16x32_bf16 v[162:165], v[174:177], a[148:151], v[162:165]
s_subb_u32 s41, s41, 0
v_mfma_f32_16x16x32_bf16 v[166:169], v[174:177], a[152:155], v[166:169]
v_mfma_f32_16x16x32_bf16 v[170:173], v[174:177], a[156:159], v[170:173]
s_waitcnt vmcnt(16) lgkmcnt(0)
s_barrier
ds_read_b64_tr_b16 a[192:193], v156 offset:0
v_mfma_f32_16x16x32_bf16 v[158:161], v[174:177], a[160:163], v[158:161]
ds_read_b64_tr_b16 a[194:195], v156 offset:1024
ds_read_b64_tr_b16 a[196:197], v156 offset:8
ds_read_b64_tr_b16 a[198:199], v156 offset:1032
ds_read_b64_tr_b16 a[200:201], v156 offset:16384
v_mfma_f32_16x16x32_bf16 v[162:165], v[174:177], a[164:167], v[162:165]
ds_read_b64_tr_b16 a[202:203], v156 offset:17408
ds_read_b64_tr_b16 a[204:205], v156 offset:16392
ds_read_b64_tr_b16 a[206:207], v156 offset:17416
ds_read_b64_tr_b16 a[208:209], v156 offset:8192
v_mfma_f32_16x16x32_bf16 v[166:169], v[174:177], a[168:171], v[166:169]
ds_read_b64_tr_b16 a[210:211], v156 offset:9216
ds_read_b64_tr_b16 a[212:213], v156 offset:8200
ds_read_b64_tr_b16 a[214:215], v156 offset:9224
ds_read_b64_tr_b16 a[216:217], v156 offset:24576
)"
R"(v_mfma_f32_16x16x32_bf16 v[170:173], v[174:177], a[172:175], v[170:173]
ds_read_b64_tr_b16 a[218:219], v156 offset:25600
ds_read_b64_tr_b16 a[220:221], v156 offset:24584
ds_read_b64_tr_b16 a[222:223], v156 offset:25608
v_mfma_f32_16x16x32_bf16 v[158:161], v[174:177], a[176:179], v[158:161]
v_mfma_f32_16x16x32_bf16 v[162:165], v[174:177], a[180:183], v[162:165]
v_mfma_f32_16x16x32_bf16 v[166:169], v[174:177], a[184:187], v[166:169]
v_mfma_f32_16x16x32_bf16 v[170:173], v[174:177], a[188:191], v[170:173]
s_waitcnt vmcnt(8) lgkmcnt(0)
s_barrier
ds_read_b64_tr_b16 a[224:225], v156 offset:32768
v_mfma_f32_16x16x32_bf16 v[158:161], v[174:177], a[192:195], v[158:161]
ds_read_b64_tr_b16 a[226:227], v156 offset:33792
ds_read_b64_tr_b16 a[228:229], v156 offset:32776
ds_read_b64_tr_b16 a[230:231], v156 offset:33800
ds_read_b64_tr_b16 a[232:233], v156 offset:49152
v_mfma_f32_16x16x32_bf16 v[162:165], v[174:177], a[196:199], v[162:165]
ds_read_b64_tr_b16 a[234:235], v156 offset:50176
ds_read_b64_tr_b16 a[236:237], v156 offset:49160
ds_read_b64_tr_b16 a[238:239], v156 offset:50184
ds_read_b64_tr_b16 a[240:241], v156 offset:40960
v_mfma_f32_16x16x32_bf16 v[166:169], v[174:177], a[200:203], v[166:169]
ds_read_b64_tr_b16 a[242:243], v156 offset:41984
ds_read_b64_tr_b16 a[244:245], v156 offset:40968
ds_read_b64_tr_b16 a[246:247], v156 offset:41992
ds_read_b64_tr_b16 a[248:249], v156 offset:57344
v_mfma_f32_16x16x32_bf16 v[170:173], v[174:177], a[204:207], v[170:173]
ds_read_b64_tr_b16 a[250:251], v156 offset:58368
ds_read_b64_tr_b16 a[252:253], v156 offset:57352
ds_read_b64_tr_b16 a[254:255], v156 offset:58376
v_mfma_f32_16x16x32_bf16 v[158:161], v[174:177], a[208:211], v[158:161]
v_mfma_f32_16x16x32_bf16 v[162:165], v[174:177], a[212:215], v[162:165]
v_mfma_f32_16x16x32_bf16 v[166:169], v[174:177], a[216:219], v[166:169]
v_mfma_f32_16x16x32_bf16 v[170:173], v[174:177], a[220:223], v[170:173]
s_waitcnt vmcnt(0) lgkmcnt(0)
s_barrier
ds_read_b64_tr_b16 v[26:27], v1 offset:0
v_mfma_f32_16x16x32_bf16 v[158:161], v[174:177], a[224:227], v[158:161]
ds_read_b64_tr_b16 v[28:29], v1 offset:1024
ds_read_b64_tr_b16 v[30:31], v1 offset:8
ds_read_b64_tr_b16 v[32:33], v1 offset:1032
ds_read_b64_tr_b16 v[34:35], v1 offset:16384
v_mfma_f32_16x16x32_bf16 v[162:165], v[174:177], a[228:231], v[162:165]
ds_read_b64_tr_b16 v[36:37], v1 offset:17408
ds_read_b64_tr_b16 v[38:39], v1 offset:16392
ds_read_b64_tr_b16 v[40:41], v1 offset:17416
ds_read_b64_tr_b16 v[42:43], v1 offset:8192
v_mfma_f32_16x16x32_bf16 v[166:169], v[174:177], a[232:235], v[166:169]
ds_read_b64_tr_b16 v[44:45], v1 offset:9216
ds_read_b64_tr_b16 v[46:47], v1 offset:8200
ds_read_b64_tr_b16 v[48:49], v1 offset:9224
ds_read_b64_tr_b16 v[50:51], v1 offset:24576
v_mfma_f32_16x16x32_bf16 v[170:173], v[174:177], a[236:239], v[170:173]
ds_read_b64_tr_b16 v[52:53], v1 offset:25600
ds_read_b64_tr_b16 v[54:55], v1 offset:24584
ds_read_b64_tr_b16 v[56:57], v1 offset:25608
v_mfma_f32_16x16x32_bf16 v[158:161], v[174:177], a[240:243], v[158:161]
v_mfma_f32_16x16x32_bf16 v[162:165], v[174:177], a[244:247], v[162:165]
v_mfma_f32_16x16x32_bf16 v[166:169], v[174:177], a[248:251], v[166:169]
v_mfma_f32_16x16x32_bf16 v[170:173], v[174:177], a[252:255], v[170:173]
s_waitcnt lgkmcnt(14)
v_mfma_f32_16x16x32_bf16 v[158:161], v[174:177], v[26:29], v[158:161]
s_waitcnt lgkmcnt(12)
v_mfma_f32_16x16x32_bf16 v[162:165], v[174:177], v[30:33], v[162:165]
s_waitcnt lgkmcnt(10)
v_mfma_f32_16x16x32_bf16 v[166:169], v[174:177], v[34:37], v[166:169]
s_waitcnt lgkmcnt(8)
v_mfma_f32_16x16x32_bf16 v[170:173], v[174:177], v[38:41], v[170:173]
s_waitcnt lgkmcnt(6)
v_mfma_f32_16x16x32_bf16 v[158:161], v[174:177], v[42:45], v[158:161]
s_waitcnt lgkmcnt(4)
v_mfma_f32_16x16x32_bf16 v[162:165], v[174:177], v[46:49], v[162:165]
s_waitcnt lgkmcnt(2)
v_mfma_f32_16x16x32_bf16 v[166:169], v[174:177], v[50:53], v[166:169]
s_waitcnt lgkmcnt(0)
v_mfma_f32_16x16x32_bf16 v[170:173], v[174:177], v[54:57], v[170:173]
s_nop 1
v_mul_f32 v158, 0xbae38e39, v158
v_mul_f32 v159, 0xbae38e39, v159
v_mul_f32 v160, 0xbae38e39, v160
v_mul_f32 v161, 0xbae38e39, v161
v_mul_f32 v162, 0xbae38e39, v162
v_mul_f32 v163, 0xbae38e39, v163
v_mul_f32 v164, 0xbae38e39, v164
v_mul_f32 v165, 0xbae38e39, v165
v_mul_f32 v166, 0xbae38e39, v166
v_mul_f32 v167, 0xbae38e39, v167
v_mul_f32 v168, 0xbae38e39, v168
v_mul_f32 v169, 0xbae38e39, v169
v_mul_f32 v170, 0xbae38e39, v170
v_mul_f32 v171, 0xbae38e39, v171
v_mul_f32 v172, 0xbae38e39, v172
v_mul_f32 v173, 0xbae38e39, v173
v_mfma_f32_16x16x32_bf16 v[178:181], v[22:25], a[0:3], v[158:161]
v_mfma_f32_16x16x32_bf16 v[182:185], v[20:23], a[0:3], v[158:161]
v_mfma_f32_16x16x32_bf16 v[186:189], v[22:25], a[4:7], v[162:165]
v_mfma_f32_16x16x32_bf16 v[190:193], v[20:23], a[4:7], v[162:165]
v_mfma_f32_16x16x32_bf16 v[194:197], v[22:25], a[8:11], v[166:169]
s_nop 3
v_cvt_pk_bf16_f32 v210, v178, v179
v_cvt_pk_bf16_f32 v211, v180, v181
v_cvt_pk_bf16_f32 v212, v182, v183
v_cvt_pk_bf16_f32 v213, v184, v185
v_mfma_f32_16x16x32_bf16 v[198:201], v[20:23], a[8:11], v[166:169]
v_accvgpr_write_b32 a0, v210
v_accvgpr_write_b32 a1, v211
v_accvgpr_write_b32 a2, v212
v_accvgpr_write_b32 a3, v213
v_mfma_f32_16x16x32_bf16 v[202:205], v[22:25], a[12:15], v[170:173]
v_cvt_pk_bf16_f32 v214, v186, v187
v_cvt_pk_bf16_f32 v215, v188, v189
v_cvt_pk_bf16_f32 v216, v190, v191
v_cvt_pk_bf16_f32 v217, v192, v193
v_mfma_f32_16x16x32_bf16 v[206:209], v[20:23], a[12:15], v[170:173]
v_accvgpr_write_b32 a4, v214
v_accvgpr_write_b32 a5, v215
v_accvgpr_write_b32 a6, v216
v_accvgpr_write_b32 a7, v217
v_mfma_f32_16x16x32_bf16 v[178:181], v[22:25], a[16:19], v[158:161]
v_cvt_pk_bf16_f32 v210, v194, v195
v_cvt_pk_bf16_f32 v211, v196, v197
v_cvt_pk_bf16_f32 v212, v198, v199
v_cvt_pk_bf16_f32 v213, v200, v201
v_mfma_f32_16x16x32_bf16 v[182:185], v[20:23], a[16:19], v[158:161]
v_accvgpr_write_b32 a8, v210
v_accvgpr_write_b32 a9, v211
v_accvgpr_write_b32 a10, v212
v_accvgpr_write_b32 a11, v213
v_mfma_f32_16x16x32_bf16 v[186:189], v[22:25], a[20:23], v[162:165]
v_cvt_pk_bf16_f32 v214, v202, v203
v_cvt_pk_bf16_f32 v215, v204, v205
v_cvt_pk_bf16_f32 v216, v206, v207
v_cvt_pk_bf16_f32 v217, v208, v209
v_mfma_f32_16x16x32_bf16 v[190:193], v[20:23], a[20:23], v[162:165]
v_accvgpr_write_b32 a12, v214
v_accvgpr_write_b32 a13, v215
v_accvgpr_write_b32 a14, v216
v_accvgpr_write_b32 a15, v217
v_mfma_f32_16x16x32_bf16 v[194:197], v[22:25], a[24:27], v[166:169]
v_cvt_pk_bf16_f32 v210, v178, v179
v_cvt_pk_bf16_f32 v211, v180, v181
v_cvt_pk_bf16_f32 v212, v182, v183
v_cvt_pk_bf16_f32 v213, v184, v185
v_mfma_f32_16x16x32_bf16 v[198:201], v[20:23], a[24:27], v[166:169]
v_accvgpr_write_b32 a16, v210
v_accvgpr_write_b32 a17, v211
v_accvgpr_write_b32 a18, v212
v_accvgpr_write_b32 a19, v213
v_mfma_f32_16x16x32_bf16 v[202:205], v[22:25], a[28:31], v[170:173]
v_cvt_pk_bf16_f32 v214, v186, v187
v_cvt_pk_bf16_f32 v215, v188, v189
v_cvt_pk_bf16_f32 v216, v190, v191
v_cvt_pk_bf16_f32 v217, v192, v193
v_mfma_f32_16x16x32_bf16 v[206:209], v[20:23], a[28:31], v[170:173]
v_accvgpr_write_b32 a20, v214
v_accvgpr_write_b32 a21, v215
v_accvgpr_write_b32 a22, v216
v_accvgpr_write_b32 a23, v217
v_mfma_f32_16x16x32_bf16 v[178:181], v[22:25], a[32:35], v[158:161]
v_cvt_pk_bf16_f32 v210, v194, v195
v_cvt_pk_bf16_f32 v211, v196, v197
v_cvt_pk_bf16_f32 v212, v198, v199
v_cvt_pk_bf16_f32 v213, v200, v201
v_mfma_f32_16x16x32_bf16 v[182:185], v[20:23], a[32:35], v[158:161]
v_accvgpr_write_b32 a24, v210
v_accvgpr_write_b32 a25, v211
v_accvgpr_write_b32 a26, v212
v_accvgpr_write_b32 a27, v213
v_mfma_f32_16x16x32_bf16 v[186:189], v[22:25], a[36:39], v[162:165]
v_cvt_pk_bf16_f32 v214, v202, v203
v_cvt_pk_bf16_f32 v215, v204, v205
v_cvt_pk_bf16_f32 v216, v206, v207
v_cvt_pk_bf16_f32 v217, v208, v209
v_mfma_f32_16x16x32_bf16 v[190:193], v[20:23], a[36:39], v[162:165]
v_accvgpr_write_b32 a28, v214
v_accvgpr_write_b32 a29, v215
v_accvgpr_write_b32 a30, v216
v_accvgpr_write_b32 a31, v217
v_mfma_f32_16x16x32_bf16 v[194:197], v[22:25], a[40:43], v[166:169]
v_cvt_pk_bf16_f32 v210, v178, v179
v_cvt_pk_bf16_f32 v211, v180, v181
v_cvt_pk_bf16_f32 v212, v182, v183
v_cvt_pk_bf16_f32 v213, v184, v185
v_mfma_f32_16x16x32_bf16 v[198:201], v[20:23], a[40:43], v[166:169]
v_accvgpr_write_b32 a32, v210
v_accvgpr_write_b32 a33, v211
v_accvgpr_write_b32 a34, v212
v_accvgpr_write_b32 a35, v213
v_mfma_f32_16x16x32_bf16 v[202:205], v[22:25], a[44:47], v[170:173]
v_cvt_pk_bf16_f32 v214, v186, v187
v_cvt_pk_bf16_f32 v215, v188, v189
v_cvt_pk_bf16_f32 v216, v190, v191
v_cvt_pk_bf16_f32 v217, v192, v193
v_mfma_f32_16x16x32_bf16 v[206:209], v[20:23], a[44:47], v[170:173]
v_accvgpr_write_b32 a36, v214
v_accvgpr_write_b32 a37, v215
v_accvgpr_write_b32 a38, v216
v_accvgpr_write_b32 a39, v217
v_mfma_f32_16x16x32_bf16 v[178:181], v[22:25], a[48:51], v[158:161]
v_cvt_pk_bf16_f32 v210, v194, v195
v_cvt_pk_bf16_f32 v211, v196, v197
v_cvt_pk_bf16_f32 v212, v198, v199
v_cvt_pk_bf16_f32 v213, v200, v201
v_mfma_f32_16x16x32_bf16 v[182:185], v[20:23], a[48:51], v[158:161]
v_accvgpr_write_b32 a40, v210
v_accvgpr_write_b32 a41, v211
v_accvgpr_write_b32 a42, v212
v_accvgpr_write_b32 a43, v213
v_mfma_f32_16x16x32_bf16 v[186:189], v[22:25], a[52:55], v[162:165]
v_cvt_pk_bf16_f32 v214, v202, v203
v_cvt_pk_bf16_f32 v215, v204, v205
v_cvt_pk_bf16_f32 v216, v206, v207
v_cvt_pk_bf16_f32 v217, v208, v209
v_mfma_f32_16x16x32_bf16 v[190:193], v[20:23], a[52:55], v[162:165]
v_accvgpr_write_b32 a44, v214
v_accvgpr_write_b32 a45, v215
v_accvgpr_write_b32 a46, v216
v_accvgpr_write_b32 a47, v217
v_mfma_f32_16x16x32_bf16 v[194:197], v[22:25], a[56:59], v[166:169]
v_cvt_pk_bf16_f32 v210, v178, v179
v_cvt_pk_bf16_f32 v211, v180, v181
v_cvt_pk_bf16_f32 v212, v182, v183
v_cvt_pk_bf16_f32 v213, v184, v185
v_mfma_f32_16x16x32_bf16 v[198:201], v[20:23], a[56:59], v[166:169]
v_accvgpr_write_b32 a48, v210
v_accvgpr_write_b32 a49, v211
v_accvgpr_write_b32 a50, v212
v_accvgpr_write_b32 a51, v213
v_mfma_f32_16x16x32_bf16 v[202:205], v[22:25], a[60:63], v[170:173]
v_cvt_pk_bf16_f32 v214, v186, v187
v_cvt_pk_bf16_f32 v215, v188, v189
v_cvt_pk_bf16_f32 v216, v190, v191
v_cvt_pk_bf16_f32 v217, v192, v193
v_mfma_f32_16x16x32_bf16 v[206:209], v[20:23], a[60:63], v[170:173]
v_accvgpr_write_b32 a52, v214
v_accvgpr_write_b32 a53, v215
v_accvgpr_write_b32 a54, v216
v_accvgpr_write_b32 a55, v217
v_mfma_f32_16x16x32_bf16 v[178:181], v[22:25], a[64:67], v[158:161]
v_cvt_pk_bf16_f32 v210, v194, v195
v_cvt_pk_bf16_f32 v211, v196, v197
v_cvt_pk_bf16_f32 v212, v198, v199
v_cvt_pk_bf16_f32 v213, v200, v201
v_mfma_f32_16x16x32_bf16 v[182:185], v[20:23], a[64:67], v[158:161]
v_accvgpr_write_b32 a56, v210
v_accvgpr_write_b32 a57, v211
v_accvgpr_write_b32 a58, v212
v_accvgpr_write_b32 a59, v213
v_mfma_f32_16x16x32_bf16 v[186:189], v[22:25], a[68:71], v[162:165]
v_cvt_pk_bf16_f32 v214, v202, v203
v_cvt_pk_bf16_f32 v215, v204, v205
v_cvt_pk_bf16_f32 v216, v206, v207
v_cvt_pk_bf16_f32 v217, v208, v209
v_mfma_f32_16x16x32_bf16 v[190:193], v[20:23], a[68:71], v[162:165]
v_accvgpr_write_b32 a60, v214
v_accvgpr_write_b32 a61, v215
v_accvgpr_write_b32 a62, v216
v_accvgpr_write_b32 a63, v217
v_mfma_f32_16x16x32_bf16 v[194:197], v[22:25], a[72:75], v[166:169]
v_cvt_pk_bf16_f32 v210, v178, v179
v_cvt_pk_bf16_f32 v211, v180, v181
v_cvt_pk_bf16_f32 v212, v182, v183
v_cvt_pk_bf16_f32 v213, v184, v185
v_mfma_f32_16x16x32_bf16 v[198:201], v[20:23], a[72:75], v[166:169]
v_accvgpr_write_b32 a64, v210
v_accvgpr_write_b32 a65, v211
v_accvgpr_write_b32 a66, v212
v_accvgpr_write_b32 a67, v213
v_mfma_f32_16x16x32_bf16 v[202:205], v[22:25], a[76:79], v[170:173]
v_cvt_pk_bf16_f32 v214, v186, v187
v_cvt_pk_bf16_f32 v215, v188, v189
v_cvt_pk_bf16_f32 v216, v190, v191
v_cvt_pk_bf16_f32 v217, v192, v193
v_mfma_f32_16x16x32_bf16 v[206:209], v[20:23], a[76:79], v[170:173]
v_accvgpr_write_b32 a68, v214
v_accvgpr_write_b32 a69, v215
v_accvgpr_write_b32 a70, v216
v_accvgpr_write_b32 a71, v217
v_mfma_f32_16x16x32_bf16 v[178:181], v[22:25], a[80:83], v[158:161]
v_cvt_pk_bf16_f32 v210, v194, v195
v_cvt_pk_bf16_f32 v211, v196, v197
v_cvt_pk_bf16_f32 v212, v198, v199
v_cvt_pk_bf16_f32 v213, v200, v201
v_mfma_f32_16x16x32_bf16 v[182:185], v[20:23], a[80:83], v[158:161]
v_accvgpr_write_b32 a72, v210
v_accvgpr_write_b32 a73, v211
v_accvgpr_write_b32 a74, v212
v_accvgpr_write_b32 a75, v213
v_mfma_f32_16x16x32_bf16 v[186:189], v[22:25], a[84:87], v[162:165]
v_cvt_pk_bf16_f32 v214, v202, v203
v_cvt_pk_bf16_f32 v215, v204, v205
v_cvt_pk_bf16_f32 v216, v206, v207
v_cvt_pk_bf16_f32 v217, v208, v209
v_mfma_f32_16x16x32_bf16 v[190:193], v[20:23], a[84:87], v[162:165]
v_accvgpr_write_b32 a76, v214
v_accvgpr_write_b32 a77, v215
v_accvgpr_write_b32 a78, v216
v_accvgpr_write_b32 a79, v217
v_mfma_f32_16x16x32_bf16 v[194:197], v[22:25], a[88:91], v[166:169]
v_cvt_pk_bf16_f32 v210, v178, v179
v_cvt_pk_bf16_f32 v211, v180, v181
v_cvt_pk_bf16_f32 v212, v182, v183
v_cvt_pk_bf16_f32 v213, v184, v185
v_mfma_f32_16x16x32_bf16 v[198:201], v[20:23], a[88:91], v[166:169]
v_accvgpr_write_b32 a80, v210
v_accvgpr_write_b32 a81, v211
v_accvgpr_write_b32 a82, v212
v_accvgpr_write_b32 a83, v213
v_mfma_f32_16x16x32_bf16 v[202:205], v[22:25], a[92:95], v[170:173]
v_cvt_pk_bf16_f32 v214, v186, v187
v_cvt_pk_bf16_f32 v215, v188, v189
v_cvt_pk_bf16_f32 v216, v190, v191
v_cvt_pk_bf16_f32 v217, v192, v193
v_mfma_f32_16x16x32_bf16 v[206:209], v[20:23], a[92:95], v[170:173]
v_accvgpr_write_b32 a84, v214
v_accvgpr_write_b32 a85, v215
v_accvgpr_write_b32 a86, v216
v_accvgpr_write_b32 a87, v217
v_mfma_f32_16x16x32_bf16 v[178:181], v[22:25], a[96:99], v[158:161]
v_cvt_pk_bf16_f32 v210, v194, v195
v_cvt_pk_bf16_f32 v211, v196, v197
v_cvt_pk_bf16_f32 v212, v198, v199
v_cvt_pk_bf16_f32 v213, v200, v201
v_mfma_f32_16x16x32_bf16 v[182:185], v[20:23], a[96:99], v[158:161]
v_accvgpr_write_b32 a88, v210
v_accvgpr_write_b32 a89, v211
v_accvgpr_write_b32 a90, v212
v_accvgpr_write_b32 a91, v213
v_mfma_f32_16x16x32_bf16 v[186:189], v[22:25], a[100:103], v[162:165]
v_cvt_pk_bf16_f32 v214, v202, v203
v_cvt_pk_bf16_f32 v215, v204, v205
v_cvt_pk_bf16_f32 v216, v206, v207
v_cvt_pk_bf16_f32 v217, v208, v209
v_mfma_f32_16x16x32_bf16 v[190:193], v[20:23], a[100:103], v[162:165]
v_accvgpr_write_b32 a92, v214
v_accvgpr_write_b32 a93, v215
v_accvgpr_write_b32 a94, v216
v_accvgpr_write_b32 a95, v217
v_mfma_f32_16x16x32_bf16 v[194:197], v[22:25], a[104:107], v[166:169]
v_cvt_pk_bf16_f32 v210, v178, v179
v_cvt_pk_bf16_f32 v211, v180, v181
v_cvt_pk_bf16_f32 v212, v182, v183
v_cvt_pk_bf16_f32 v213, v184, v185
v_mfma_f32_16x16x32_bf16 v[198:201], v[20:23], a[104:107], v[166:169]
v_accvgpr_write_b32 a96, v210
v_accvgpr_write_b32 a97, v211
v_accvgpr_write_b32 a98, v212
v_accvgpr_write_b32 a99, v213
v_mfma_f32_16x16x32_bf16 v[202:205], v[22:25], a[108:111], v[170:173]
v_cvt_pk_bf16_f32 v214, v186, v187
v_cvt_pk_bf16_f32 v215, v188, v189
v_cvt_pk_bf16_f32 v216, v190, v191
v_cvt_pk_bf16_f32 v217, v192, v193
v_mfma_f32_16x16x32_bf16 v[206:209], v[20:23], a[108:111], v[170:173]
v_accvgpr_write_b32 a100, v214
v_accvgpr_write_b32 a101, v215
v_accvgpr_write_b32 a102, v216
v_accvgpr_write_b32 a103, v217
v_mfma_f32_16x16x32_bf16 v[178:181], v[22:25], a[112:115], v[158:161]
v_cvt_pk_bf16_f32 v210, v194, v195
v_cvt_pk_bf16_f32 v211, v196, v197
v_cvt_pk_bf16_f32 v212, v198, v199
v_cvt_pk_bf16_f32 v213, v200, v201
v_mfma_f32_16x16x32_bf16 v[182:185], v[20:23], a[112:115], v[158:161]
v_accvgpr_write_b32 a104, v210
v_accvgpr_write_b32 a105, v211
v_accvgpr_write_b32 a106, v212
v_accvgpr_write_b32 a107, v213
v_mfma_f32_16x16x32_bf16 v[186:189], v[22:25], a[116:119], v[162:165]
v_cvt_pk_bf16_f32 v214, v202, v203
v_cvt_pk_bf16_f32 v215, v204, v205
v_cvt_pk_bf16_f32 v216, v206, v207
v_cvt_pk_bf16_f32 v217, v208, v209
v_mfma_f32_16x16x32_bf16 v[190:193], v[20:23], a[116:119], v[162:165]
v_accvgpr_write_b32 a108, v214
v_accvgpr_write_b32 a109, v215
v_accvgpr_write_b32 a110, v216
v_accvgpr_write_b32 a111, v217
v_mfma_f32_16x16x32_bf16 v[194:197], v[22:25], a[120:123], v[166:169]
v_cvt_pk_bf16_f32 v210, v178, v179
v_cvt_pk_bf16_f32 v211, v180, v181
v_cvt_pk_bf16_f32 v212, v182, v183
v_cvt_pk_bf16_f32 v213, v184, v185
v_mfma_f32_16x16x32_bf16 v[198:201], v[20:23], a[120:123], v[166:169]
v_accvgpr_write_b32 a112, v210
v_accvgpr_write_b32 a113, v211
v_accvgpr_write_b32 a114, v212
v_accvgpr_write_b32 a115, v213
v_mfma_f32_16x16x32_bf16 v[202:205], v[22:25], a[124:127], v[170:173]
v_cvt_pk_bf16_f32 v214, v186, v187
v_cvt_pk_bf16_f32 v215, v188, v189
v_cvt_pk_bf16_f32 v216, v190, v191
v_cvt_pk_bf16_f32 v217, v192, v193
v_mfma_f32_16x16x32_bf16 v[206:209], v[20:23], a[124:127], v[170:173]
v_accvgpr_write_b32 a116, v214
v_accvgpr_write_b32 a117, v215
v_accvgpr_write_b32 a118, v216
v_accvgpr_write_b32 a119, v217
v_mfma_f32_16x16x32_bf16 v[178:181], v[22:25], a[128:131], v[158:161]
v_cvt_pk_bf16_f32 v210, v194, v195
)"
R"(v_cvt_pk_bf16_f32 v211, v196, v197
v_cvt_pk_bf16_f32 v212, v198, v199
v_cvt_pk_bf16_f32 v213, v200, v201
v_mfma_f32_16x16x32_bf16 v[182:185], v[20:23], a[128:131], v[158:161]
v_accvgpr_write_b32 a120, v210
v_accvgpr_write_b32 a121, v211
v_accvgpr_write_b32 a122, v212
v_accvgpr_write_b32 a123, v213
v_mfma_f32_16x16x32_bf16 v[186:189], v[22:25], a[132:135], v[162:165]
v_cvt_pk_bf16_f32 v214, v202, v203
v_cvt_pk_bf16_f32 v215, v204, v205
v_cvt_pk_bf16_f32 v216, v206, v207
v_cvt_pk_bf16_f32 v217, v208, v209
v_mfma_f32_16x16x32_bf16 v[190:193], v[20:23], a[132:135], v[162:165]
v_accvgpr_write_b32 a124, v214
v_accvgpr_write_b32 a125, v215
v_accvgpr_write_b32 a126, v216
v_accvgpr_write_b32 a127, v217
v_mfma_f32_16x16x32_bf16 v[194:197], v[22:25], a[136:139], v[166:169]
v_cvt_pk_bf16_f32 v210, v178, v179
v_cvt_pk_bf16_f32 v211, v180, v181
v_cvt_pk_bf16_f32 v212, v182, v183
v_cvt_pk_bf16_f32 v213, v184, v185
v_mfma_f32_16x16x32_bf16 v[198:201], v[20:23], a[136:139], v[166:169]
v_accvgpr_write_b32 a128, v210
v_accvgpr_write_b32 a129, v211
v_accvgpr_write_b32 a130, v212
v_accvgpr_write_b32 a131, v213
v_mfma_f32_16x16x32_bf16 v[202:205], v[22:25], a[140:143], v[170:173]
v_cvt_pk_bf16_f32 v214, v186, v187
v_cvt_pk_bf16_f32 v215, v188, v189
v_cvt_pk_bf16_f32 v216, v190, v191
v_cvt_pk_bf16_f32 v217, v192, v193
v_mfma_f32_16x16x32_bf16 v[206:209], v[20:23], a[140:143], v[170:173]
v_accvgpr_write_b32 a132, v214
v_accvgpr_write_b32 a133, v215
v_accvgpr_write_b32 a134, v216
v_accvgpr_write_b32 a135, v217
v_mfma_f32_16x16x32_bf16 v[178:181], v[22:25], a[144:147], v[158:161]
v_cvt_pk_bf16_f32 v210, v194, v195
v_cvt_pk_bf16_f32 v211, v196, v197
v_cvt_pk_bf16_f32 v212, v198, v199
v_cvt_pk_bf16_f32 v213, v200, v201
v_mfma_f32_16x16x32_bf16 v[182:185], v[20:23], a[144:147], v[158:161]
v_accvgpr_write_b32 a136, v210
v_accvgpr_write_b32 a137, v211
v_accvgpr_write_b32 a138, v212
v_accvgpr_write_b32 a139, v213
v_mfma_f32_16x16x32_bf16 v[186:189], v[22:25], a[148:151], v[162:165]
v_cvt_pk_bf16_f32 v214, v202, v203
v_cvt_pk_bf16_f32 v215, v204, v205
v_cvt_pk_bf16_f32 v216, v206, v207
v_cvt_pk_bf16_f32 v217, v208, v209
v_mfma_f32_16x16x32_bf16 v[190:193], v[20:23], a[148:151], v[162:165]
v_accvgpr_write_b32 a140, v214
v_accvgpr_write_b32 a141, v215
v_accvgpr_write_b32 a142, v216
v_accvgpr_write_b32 a143, v217
v_mfma_f32_16x16x32_bf16 v[194:197], v[22:25], a[152:155], v[166:169]
v_cvt_pk_bf16_f32 v210, v178, v179
v_cvt_pk_bf16_f32 v211, v180, v181
v_cvt_pk_bf16_f32 v212, v182, v183
v_cvt_pk_bf16_f32 v213, v184, v185
v_mfma_f32_16x16x32_bf16 v[198:201], v[20:23], a[152:155], v[166:169]
v_accvgpr_write_b32 a144, v210
v_accvgpr_write_b32 a145, v211
v_accvgpr_write_b32 a146, v212
v_accvgpr_write_b32 a147, v213
v_mfma_f32_16x16x32_bf16 v[202:205], v[22:25], a[156:159], v[170:173]
v_cvt_pk_bf16_f32 v214, v186, v187
v_cvt_pk_bf16_f32 v215, v188, v189
v_cvt_pk_bf16_f32 v216, v190, v191
v_cvt_pk_bf16_f32 v217, v192, v193
v_mfma_f32_16x16x32_bf16 v[206:209], v[20:23], a[156:159], v[170:173]
v_accvgpr_write_b32 a148, v214
v_accvgpr_write_b32 a149, v215
v_accvgpr_write_b32 a150, v216
v_accvgpr_write_b32 a151, v217
v_mfma_f32_16x16x32_bf16 v[178:181], v[22:25], a[160:163], v[158:161]
v_cvt_pk_bf16_f32 v210, v194, v195
v_cvt_pk_bf16_f32 v211, v196, v197
v_cvt_pk_bf16_f32 v212, v198, v199
v_cvt_pk_bf16_f32 v213, v200, v201
v_mfma_f32_16x16x32_bf16 v[182:185], v[20:23], a[160:163], v[158:161]
v_accvgpr_write_b32 a152, v210
v_accvgpr_write_b32 a153, v211
v_accvgpr_write_b32 a154, v212
v_accvgpr_write_b32 a155, v213
v_mfma_f32_16x16x32_bf16 v[186:189], v[22:25], a[164:167], v[162:165]
v_cvt_pk_bf16_f32 v214, v202, v203
v_cvt_pk_bf16_f32 v215, v204, v205
v_cvt_pk_bf16_f32 v216, v206, v207
v_cvt_pk_bf16_f32 v217, v208, v209
v_mfma_f32_16x16x32_bf16 v[190:193], v[20:23], a[164:167], v[162:165]
v_accvgpr_write_b32 a156, v214
v_accvgpr_write_b32 a157, v215
v_accvgpr_write_b32 a158, v216
v_accvgpr_write_b32 a159, v217
v_mfma_f32_16x16x32_bf16 v[194:197], v[22:25], a[168:171], v[166:169]
v_cvt_pk_bf16_f32 v210, v178, v179
v_cvt_pk_bf16_f32 v211, v180, v181
v_cvt_pk_bf16_f32 v212, v182, v183
v_cvt_pk_bf16_f32 v213, v184, v185
v_mfma_f32_16x16x32_bf16 v[198:201], v[20:23], a[168:171], v[166:169]
v_accvgpr_write_b32 a160, v210
v_accvgpr_write_b32 a161, v211
v_accvgpr_write_b32 a162, v212
v_accvgpr_write_b32 a163, v213
v_mfma_f32_16x16x32_bf16 v[202:205], v[22:25], a[172:175], v[170:173]
v_cvt_pk_bf16_f32 v214, v186, v187
v_cvt_pk_bf16_f32 v215, v188, v189
v_cvt_pk_bf16_f32 v216, v190, v191
v_cvt_pk_bf16_f32 v217, v192, v193
v_mfma_f32_16x16x32_bf16 v[206:209], v[20:23], a[172:175], v[170:173]
v_accvgpr_write_b32 a164, v214
v_accvgpr_write_b32 a165, v215
v_accvgpr_write_b32 a166, v216
v_accvgpr_write_b32 a167, v217
v_mfma_f32_16x16x32_bf16 v[178:181], v[22:25], a[176:179], v[158:161]
v_cvt_pk_bf16_f32 v210, v194, v195
v_cvt_pk_bf16_f32 v211, v196, v197
v_cvt_pk_bf16_f32 v212, v198, v199
v_cvt_pk_bf16_f32 v213, v200, v201
v_mfma_f32_16x16x32_bf16 v[182:185], v[20:23], a[176:179], v[158:161]
v_accvgpr_write_b32 a168, v210
v_accvgpr_write_b32 a169, v211
v_accvgpr_write_b32 a170, v212
v_accvgpr_write_b32 a171, v213
v_mfma_f32_16x16x32_bf16 v[186:189], v[22:25], a[180:183], v[162:165]
v_cvt_pk_bf16_f32 v214, v202, v203
v_cvt_pk_bf16_f32 v215, v204, v205
v_cvt_pk_bf16_f32 v216, v206, v207
v_cvt_pk_bf16_f32 v217, v208, v209
v_mfma_f32_16x16x32_bf16 v[190:193], v[20:23], a[180:183], v[162:165]
v_accvgpr_write_b32 a172, v214
v_accvgpr_write_b32 a173, v215
v_accvgpr_write_b32 a174, v216
v_accvgpr_write_b32 a175, v217
v_mfma_f32_16x16x32_bf16 v[194:197], v[22:25], a[184:187], v[166:169]
v_cvt_pk_bf16_f32 v210, v178, v179
v_cvt_pk_bf16_f32 v211, v180, v181
v_cvt_pk_bf16_f32 v212, v182, v183
v_cvt_pk_bf16_f32 v213, v184, v185
v_mfma_f32_16x16x32_bf16 v[198:201], v[20:23], a[184:187], v[166:169]
v_accvgpr_write_b32 a176, v210
v_accvgpr_write_b32 a177, v211
v_accvgpr_write_b32 a178, v212
v_accvgpr_write_b32 a179, v213
v_mfma_f32_16x16x32_bf16 v[202:205], v[22:25], a[188:191], v[170:173]
v_cvt_pk_bf16_f32 v214, v186, v187
v_cvt_pk_bf16_f32 v215, v188, v189
v_cvt_pk_bf16_f32 v216, v190, v191
v_cvt_pk_bf16_f32 v217, v192, v193
v_mfma_f32_16x16x32_bf16 v[206:209], v[20:23], a[188:191], v[170:173]
v_accvgpr_write_b32 a180, v214
v_accvgpr_write_b32 a181, v215
v_accvgpr_write_b32 a182, v216
v_accvgpr_write_b32 a183, v217
v_mfma_f32_16x16x32_bf16 v[178:181], v[22:25], a[192:195], v[158:161]
v_cvt_pk_bf16_f32 v210, v194, v195
v_cvt_pk_bf16_f32 v211, v196, v197
v_cvt_pk_bf16_f32 v212, v198, v199
v_cvt_pk_bf16_f32 v213, v200, v201
v_mfma_f32_16x16x32_bf16 v[182:185], v[20:23], a[192:195], v[158:161]
v_accvgpr_write_b32 a184, v210
v_accvgpr_write_b32 a185, v211
v_accvgpr_write_b32 a186, v212
v_accvgpr_write_b32 a187, v213
v_mfma_f32_16x16x32_bf16 v[186:189], v[22:25], a[196:199], v[162:165]
v_cvt_pk_bf16_f32 v214, v202, v203
v_cvt_pk_bf16_f32 v215, v204, v205
v_cvt_pk_bf16_f32 v216, v206, v207
v_cvt_pk_bf16_f32 v217, v208, v209
v_mfma_f32_16x16x32_bf16 v[190:193], v[20:23], a[196:199], v[162:165]
v_accvgpr_write_b32 a188, v214
v_accvgpr_write_b32 a189, v215
v_accvgpr_write_b32 a190, v216
v_accvgpr_write_b32 a191, v217
v_mfma_f32_16x16x32_bf16 v[194:197], v[22:25], a[200:203], v[166:169]
v_cvt_pk_bf16_f32 v210, v178, v179
v_cvt_pk_bf16_f32 v211, v180, v181
v_cvt_pk_bf16_f32 v212, v182, v183
v_cvt_pk_bf16_f32 v213, v184, v185
v_mfma_f32_16x16x32_bf16 v[198:201], v[20:23], a[200:203], v[166:169]
v_accvgpr_write_b32 a192, v210
v_accvgpr_write_b32 a193, v211
v_accvgpr_write_b32 a194, v212
v_accvgpr_write_b32 a195, v213
v_mfma_f32_16x16x32_bf16 v[202:205], v[22:25], a[204:207], v[170:173]
v_cvt_pk_bf16_f32 v214, v186, v187
v_cvt_pk_bf16_f32 v215, v188, v189
v_cvt_pk_bf16_f32 v216, v190, v191
v_cvt_pk_bf16_f32 v217, v192, v193
v_mfma_f32_16x16x32_bf16 v[206:209], v[20:23], a[204:207], v[170:173]
v_accvgpr_write_b32 a196, v214
v_accvgpr_write_b32 a197, v215
v_accvgpr_write_b32 a198, v216
v_accvgpr_write_b32 a199, v217
v_mfma_f32_16x16x32_bf16 v[178:181], v[22:25], a[208:211], v[158:161]
v_cvt_pk_bf16_f32 v210, v194, v195
v_cvt_pk_bf16_f32 v211, v196, v197
v_cvt_pk_bf16_f32 v212, v198, v199
v_cvt_pk_bf16_f32 v213, v200, v201
v_mfma_f32_16x16x32_bf16 v[182:185], v[20:23], a[208:211], v[158:161]
v_accvgpr_write_b32 a200, v210
v_accvgpr_write_b32 a201, v211
v_accvgpr_write_b32 a202, v212
v_accvgpr_write_b32 a203, v213
v_mfma_f32_16x16x32_bf16 v[186:189], v[22:25], a[212:215], v[162:165]
v_cvt_pk_bf16_f32 v214, v202, v203
v_cvt_pk_bf16_f32 v215, v204, v205
v_cvt_pk_bf16_f32 v216, v206, v207
v_cvt_pk_bf16_f32 v217, v208, v209
v_mfma_f32_16x16x32_bf16 v[190:193], v[20:23], a[212:215], v[162:165]
v_accvgpr_write_b32 a204, v214
v_accvgpr_write_b32 a205, v215
v_accvgpr_write_b32 a206, v216
v_accvgpr_write_b32 a207, v217
v_mfma_f32_16x16x32_bf16 v[194:197], v[22:25], a[216:219], v[166:169]
v_cvt_pk_bf16_f32 v210, v178, v179
v_cvt_pk_bf16_f32 v211, v180, v181
v_cvt_pk_bf16_f32 v212, v182, v183
v_cvt_pk_bf16_f32 v213, v184, v185
v_mfma_f32_16x16x32_bf16 v[198:201], v[20:23], a[216:219], v[166:169]
v_accvgpr_write_b32 a208, v210
v_accvgpr_write_b32 a209, v211
v_accvgpr_write_b32 a210, v212
v_accvgpr_write_b32 a211, v213
v_mfma_f32_16x16x32_bf16 v[202:205], v[22:25], a[220:223], v[170:173]
v_cvt_pk_bf16_f32 v214, v186, v187
v_cvt_pk_bf16_f32 v215, v188, v189
v_cvt_pk_bf16_f32 v216, v190, v191
v_cvt_pk_bf16_f32 v217, v192, v193
v_mfma_f32_16x16x32_bf16 v[206:209], v[20:23], a[220:223], v[170:173]
v_accvgpr_write_b32 a212, v214
v_accvgpr_write_b32 a213, v215
v_accvgpr_write_b32 a214, v216
v_accvgpr_write_b32 a215, v217
v_mfma_f32_16x16x32_bf16 v[178:181], v[22:25], a[224:227], v[158:161]
v_cvt_pk_bf16_f32 v210, v194, v195
v_cvt_pk_bf16_f32 v211, v196, v197
v_cvt_pk_bf16_f32 v212, v198, v199
v_cvt_pk_bf16_f32 v213, v200, v201
v_mfma_f32_16x16x32_bf16 v[182:185], v[20:23], a[224:227], v[158:161]
v_accvgpr_write_b32 a216, v210
v_accvgpr_write_b32 a217, v211
v_accvgpr_write_b32 a218, v212
v_accvgpr_write_b32 a219, v213
v_mfma_f32_16x16x32_bf16 v[186:189], v[22:25], a[228:231], v[162:165]
v_cvt_pk_bf16_f32 v214, v202, v203
v_cvt_pk_bf16_f32 v215, v204, v205
v_cvt_pk_bf16_f32 v216, v206, v207
v_cvt_pk_bf16_f32 v217, v208, v209
v_mfma_f32_16x16x32_bf16 v[190:193], v[20:23], a[228:231], v[162:165]
v_accvgpr_write_b32 a220, v214
v_accvgpr_write_b32 a221, v215
v_accvgpr_write_b32 a222, v216
v_accvgpr_write_b32 a223, v217
v_mfma_f32_16x16x32_bf16 v[194:197], v[22:25], a[232:235], v[166:169]
v_cvt_pk_bf16_f32 v210, v178, v179
v_cvt_pk_bf16_f32 v211, v180, v181
v_cvt_pk_bf16_f32 v212, v182, v183
v_cvt_pk_bf16_f32 v213, v184, v185
v_mfma_f32_16x16x32_bf16 v[198:201], v[20:23], a[232:235], v[166:169]
v_accvgpr_write_b32 a224, v210
v_accvgpr_write_b32 a225, v211
v_accvgpr_write_b32 a226, v212
v_accvgpr_write_b32 a227, v213
v_mfma_f32_16x16x32_bf16 v[202:205], v[22:25], a[236:239], v[170:173]
v_cvt_pk_bf16_f32 v214, v186, v187
v_cvt_pk_bf16_f32 v215, v188, v189
v_cvt_pk_bf16_f32 v216, v190, v191
v_cvt_pk_bf16_f32 v217, v192, v193
v_mfma_f32_16x16x32_bf16 v[206:209], v[20:23], a[236:239], v[170:173]
v_accvgpr_write_b32 a228, v214
v_accvgpr_write_b32 a229, v215
v_accvgpr_write_b32 a230, v216
v_accvgpr_write_b32 a231, v217
v_mfma_f32_16x16x32_bf16 v[178:181], v[22:25], a[240:243], v[158:161]
v_cvt_pk_bf16_f32 v210, v194, v195
v_cvt_pk_bf16_f32 v211, v196, v197
v_cvt_pk_bf16_f32 v212, v198, v199
v_cvt_pk_bf16_f32 v213, v200, v201
v_mfma_f32_16x16x32_bf16 v[182:185], v[20:23], a[240:243], v[158:161]
v_accvgpr_write_b32 a232, v210
v_accvgpr_write_b32 a233, v211
v_accvgpr_write_b32 a234, v212
v_accvgpr_write_b32 a235, v213
v_mfma_f32_16x16x32_bf16 v[186:189], v[22:25], a[244:247], v[162:165]
v_cvt_pk_bf16_f32 v214, v202, v203
v_cvt_pk_bf16_f32 v215, v204, v205
v_cvt_pk_bf16_f32 v216, v206, v207
v_cvt_pk_bf16_f32 v217, v208, v209
v_mfma_f32_16x16x32_bf16 v[190:193], v[20:23], a[244:247], v[162:165]
v_accvgpr_write_b32 a236, v214
v_accvgpr_write_b32 a237, v215
v_accvgpr_write_b32 a238, v216
v_accvgpr_write_b32 a239, v217
v_mfma_f32_16x16x32_bf16 v[194:197], v[22:25], a[248:251], v[166:169]
v_cvt_pk_bf16_f32 v210, v178, v179
v_cvt_pk_bf16_f32 v211, v180, v181
v_cvt_pk_bf16_f32 v212, v182, v183
v_cvt_pk_bf16_f32 v213, v184, v185
v_mfma_f32_16x16x32_bf16 v[198:201], v[20:23], a[248:251], v[166:169]
v_accvgpr_write_b32 a240, v210
v_accvgpr_write_b32 a241, v211
v_accvgpr_write_b32 a242, v212
v_accvgpr_write_b32 a243, v213
v_mfma_f32_16x16x32_bf16 v[202:205], v[22:25], a[252:255], v[170:173]
v_cvt_pk_bf16_f32 v214, v186, v187
v_cvt_pk_bf16_f32 v215, v188, v189
v_cvt_pk_bf16_f32 v216, v190, v191
v_cvt_pk_bf16_f32 v217, v192, v193
v_mfma_f32_16x16x32_bf16 v[206:209], v[20:23], a[252:255], v[170:173]
v_accvgpr_write_b32 a244, v214
v_accvgpr_write_b32 a245, v215
v_accvgpr_write_b32 a246, v216
v_accvgpr_write_b32 a247, v217
v_mfma_f32_16x16x32_bf16 v[178:181], v[22:25], v[26:29], v[158:161]
v_cvt_pk_bf16_f32 v210, v194, v195
v_cvt_pk_bf16_f32 v211, v196, v197
v_cvt_pk_bf16_f32 v212, v198, v199
v_cvt_pk_bf16_f32 v213, v200, v201
v_mfma_f32_16x16x32_bf16 v[182:185], v[20:23], v[26:29], v[158:161]
v_accvgpr_write_b32 a248, v210
v_accvgpr_write_b32 a249, v211
v_accvgpr_write_b32 a250, v212
v_accvgpr_write_b32 a251, v213
v_mfma_f32_16x16x32_bf16 v[186:189], v[22:25], v[30:33], v[162:165]
v_cvt_pk_bf16_f32 v214, v202, v203
v_cvt_pk_bf16_f32 v215, v204, v205
v_cvt_pk_bf16_f32 v216, v206, v207
v_cvt_pk_bf16_f32 v217, v208, v209
v_mfma_f32_16x16x32_bf16 v[190:193], v[20:23], v[30:33], v[162:165]
v_accvgpr_write_b32 a252, v214
v_accvgpr_write_b32 a253, v215
v_accvgpr_write_b32 a254, v216
v_accvgpr_write_b32 a255, v217
v_mfma_f32_16x16x32_bf16 v[194:197], v[22:25], v[34:37], v[166:169]
v_cvt_pk_bf16_f32 v26, v178, v179
v_cvt_pk_bf16_f32 v27, v180, v181
v_cvt_pk_bf16_f32 v28, v182, v183
v_cvt_pk_bf16_f32 v29, v184, v185
v_mfma_f32_16x16x32_bf16 v[198:201], v[20:23], v[34:37], v[166:169]
v_mfma_f32_16x16x32_bf16 v[202:205], v[22:25], v[38:41], v[170:173]
v_cvt_pk_bf16_f32 v30, v186, v187
v_cvt_pk_bf16_f32 v31, v188, v189
v_cvt_pk_bf16_f32 v32, v190, v191
v_cvt_pk_bf16_f32 v33, v192, v193
v_mfma_f32_16x16x32_bf16 v[206:209], v[20:23], v[38:41], v[170:173]
v_mfma_f32_16x16x32_bf16 v[178:181], v[22:25], v[42:45], v[158:161]
v_cvt_pk_bf16_f32 v34, v194, v195
v_cvt_pk_bf16_f32 v35, v196, v197
v_cvt_pk_bf16_f32 v36, v198, v199
v_cvt_pk_bf16_f32 v37, v200, v201
v_mfma_f32_16x16x32_bf16 v[182:185], v[20:23], v[42:45], v[158:161]
v_mfma_f32_16x16x32_bf16 v[186:189], v[22:25], v[46:49], v[162:165]
v_cvt_pk_bf16_f32 v38, v202, v203
v_cvt_pk_bf16_f32 v39, v204, v205
v_cvt_pk_bf16_f32 v40, v206, v207
v_cvt_pk_bf16_f32 v41, v208, v209
v_mfma_f32_16x16x32_bf16 v[190:193], v[20:23], v[46:49], v[162:165]
v_mfma_f32_16x16x32_bf16 v[194:197], v[22:25], v[50:53], v[166:169]
v_cvt_pk_bf16_f32 v42, v178, v179
v_cvt_pk_bf16_f32 v43, v180, v181
v_cvt_pk_bf16_f32 v44, v182, v183
v_cvt_pk_bf16_f32 v45, v184, v185
v_mfma_f32_16x16x32_bf16 v[198:201], v[20:23], v[50:53], v[166:169]
v_mfma_f32_16x16x32_bf16 v[202:205], v[22:25], v[54:57], v[170:173]
v_cvt_pk_bf16_f32 v46, v186, v187
v_cvt_pk_bf16_f32 v47, v188, v189
v_cvt_pk_bf16_f32 v48, v190, v191
v_cvt_pk_bf16_f32 v49, v192, v193
v_mfma_f32_16x16x32_bf16 v[206:209], v[20:23], v[54:57], v[170:173]
v_cvt_pk_bf16_f32 v50, v194, v195
v_cvt_pk_bf16_f32 v51, v196, v197
v_cvt_pk_bf16_f32 v52, v198, v199
v_cvt_pk_bf16_f32 v53, v200, v201
v_cvt_pk_bf16_f32 v54, v202, v203
v_cvt_pk_bf16_f32 v55, v204, v205
s_nop 1
v_cvt_pk_bf16_f32 v56, v206, v207
v_cvt_pk_bf16_f32 v57, v208, v209
s_barrier
s_and_b32 s75, s36, 3
s_cmp_eq_u32 s75, 1
s_cbranch_scc1 .Lgu_w2
s_cmp_eq_u32 s75, 2
s_cbranch_scc1 .Lgu_w3
s_cmp_eq_u32 s75, 3
s_cbranch_scc1 .Lgu_w4
.Lgu_w1:
v_add_u32 v254, -1, v5
s_mov_b32 s70, 32
s_waitcnt vmcnt(0) lgkmcnt(0)
s_nop 7
.p2align 6
.Lgu_top5:
s_waitcnt lgkmcnt(5)
v_mfma_f32_16x16x32_bf16 v[58:61], a[0:3], v[90:93], 0
)"
R"(s_waitcnt lgkmcnt(4)
v_cvt_pk_bf16_f32 v146, v138, v139
v_cvt_pk_bf16_f32 v147, v140, v141
v_mfma_f32_16x16x32_bf16 v[58:61], a[16:19], v[94:97], v[58:61]
s_waitcnt lgkmcnt(3)
v_mfma_f32_16x16x32_bf16 v[58:61], a[32:35], v[98:101], v[58:61]
s_waitcnt lgkmcnt(2)
v_mfma_f32_16x16x32_bf16 v[58:61], a[48:51], v[102:105], v[58:61]
s_waitcnt lgkmcnt(1)
v_mul_f32 v142, v78, v86
v_mul_f32 v143, v79, v87
v_mfma_f32_16x16x32_bf16 v[58:61], a[64:67], v[106:109], v[58:61]
s_waitcnt lgkmcnt(0)
v_mul_f32 v144, v80, v88
v_mul_f32 v145, v81, v89
v_mfma_f32_16x16x32_bf16 v[58:61], a[80:83], v[110:113], v[58:61]
ds_read_b128 v[114:117], v8 offset:6144
v_cvt_pk_bf16_f32 v148, v142, v143
v_mfma_f32_16x16x32_bf16 v[66:69], a[8:11], v[90:93], 0
ds_read_b128 v[118:121], v8 offset:7168
v_cvt_pk_bf16_f32 v149, v144, v145
v_mfma_f32_16x16x32_bf16 v[66:69], a[24:27], v[94:97], v[66:69]
v_mfma_f32_16x16x32_bf16 v[66:69], a[40:43], v[98:101], v[66:69]
s_waitcnt vmcnt(6)
s_barrier
v_mfma_f32_16x16x32_bf16 v[66:69], a[56:59], v[102:105], v[66:69]
v_mfma_f32_16x16x32_bf16 v[66:69], a[72:75], v[106:109], v[66:69]
global_store_dwordx4 v3, v[146:149], s[38:39] sc1
v_mfma_f32_16x16x32_bf16 v[66:69], a[88:91], v[110:113], v[66:69]
ds_read_b128 v[122:125], v8 offset:8192
v_add_u32 v3, 147456, v3
v_mfma_f32_16x16x32_bf16 v[62:65], a[4:7], v[90:93], 0
v_mfma_f32_16x16x32_bf16 v[62:65], a[20:23], v[94:97], v[62:65]
v_mfma_f32_16x16x32_bf16 v[62:65], a[36:39], v[98:101], v[62:65]
ds_read_b128 v[126:129], v8 offset:9216
v_mfma_f32_16x16x32_bf16 v[62:65], a[52:55], v[102:105], v[62:65]
v_mfma_f32_16x16x32_bf16 v[62:65], a[68:71], v[106:109], v[62:65]
v_mfma_f32_16x16x32_bf16 v[62:65], a[84:87], v[110:113], v[62:65]
ds_read_b128 v[130:133], v8 offset:10240
v_mfma_f32_16x16x32_bf16 v[70:73], a[12:15], v[90:93], 0
v_mfma_f32_16x16x32_bf16 v[70:73], a[28:31], v[94:97], v[70:73]
v_mfma_f32_16x16x32_bf16 v[70:73], a[44:47], v[98:101], v[70:73]
ds_read_b128 v[134:137], v8 offset:11264
v_mfma_f32_16x16x32_bf16 v[70:73], a[60:63], v[102:105], v[70:73]
v_mfma_f32_16x16x32_bf16 v[70:73], a[76:79], v[106:109], v[70:73]
v_mfma_f32_16x16x32_bf16 v[70:73], a[92:95], v[110:113], v[70:73]
s_waitcnt lgkmcnt(5)
v_mfma_f32_16x16x32_bf16 v[58:61], a[96:99], v[114:117], v[58:61]
s_waitcnt lgkmcnt(4)
v_mfma_f32_16x16x32_bf16 v[58:61], a[112:115], v[118:121], v[58:61]
s_waitcnt lgkmcnt(3)
v_mfma_f32_16x16x32_bf16 v[58:61], a[128:131], v[122:125], v[58:61]
s_waitcnt lgkmcnt(2)
v_mfma_f32_16x16x32_bf16 v[58:61], a[144:147], v[126:129], v[58:61]
s_waitcnt lgkmcnt(1)
v_mfma_f32_16x16x32_bf16 v[58:61], a[160:163], v[130:133], v[58:61]
s_waitcnt lgkmcnt(0)
v_mfma_f32_16x16x32_bf16 v[58:61], a[176:179], v[134:137], v[58:61]
ds_read_b128 v[90:93], v8 offset:12288
v_mfma_f32_16x16x32_bf16 v[66:69], a[104:107], v[114:117], v[66:69]
v_mfma_f32_16x16x32_bf16 v[66:69], a[120:123], v[118:121], v[66:69]
v_mfma_f32_16x16x32_bf16 v[66:69], a[136:139], v[122:125], v[66:69]
ds_read_b128 v[94:97], v8 offset:13312
v_mfma_f32_16x16x32_bf16 v[66:69], a[152:155], v[126:129], v[66:69]
v_mfma_f32_16x16x32_bf16 v[66:69], a[168:171], v[130:133], v[66:69]
v_mfma_f32_16x16x32_bf16 v[66:69], a[184:187], v[134:137], v[66:69]
ds_read_b128 v[98:101], v8 offset:14336
v_mfma_f32_16x16x32_bf16 v[62:65], a[100:103], v[114:117], v[62:65]
v_mfma_f32_16x16x32_bf16 v[62:65], a[116:119], v[118:121], v[62:65]
v_mfma_f32_16x16x32_bf16 v[62:65], a[132:135], v[122:125], v[62:65]
ds_read_b128 v[102:105], v8 offset:15360
v_mfma_f32_16x16x32_bf16 v[62:65], a[148:151], v[126:129], v[62:65]
v_mfma_f32_16x16x32_bf16 v[62:65], a[164:167], v[130:133], v[62:65]
v_mfma_f32_16x16x32_bf16 v[62:65], a[180:183], v[134:137], v[62:65]
ds_read_b128 v[106:109], v8 offset:16384
v_mfma_f32_16x16x32_bf16 v[70:73], a[108:111], v[114:117], v[70:73]
v_mfma_f32_16x16x32_bf16 v[70:73], a[124:127], v[118:121], v[70:73]
v_mfma_f32_16x16x32_bf16 v[70:73], a[140:143], v[122:125], v[70:73]
ds_read_b128 v[110:113], v8 offset:17408
v_mfma_f32_16x16x32_bf16 v[70:73], a[156:159], v[126:129], v[70:73]
s_xor_b32 s43, s43, 0x10000
v_mfma_f32_16x16x32_bf16 v[70:73], a[172:175], v[130:133], v[70:73]
s_add_u32 m0, s43, 0
v_mfma_f32_16x16x32_bf16 v[70:73], a[188:191], v[134:137], v[70:73]
s_waitcnt lgkmcnt(5)
v_mfma_f32_16x16x32_bf16 v[58:61], a[192:195], v[90:93], v[58:61]
s_barrier
s_waitcnt lgkmcnt(4)
v_mfma_f32_16x16x32_bf16 v[58:61], a[208:211], v[94:97], v[58:61]
s_waitcnt lgkmcnt(3)
global_load_lds_dwordx4 v2, s[38:39] offset:-2048 sc1
v_mfma_f32_16x16x32_bf16 v[58:61], a[224:227], v[98:101], v[58:61]
s_waitcnt lgkmcnt(2)
v_mfma_f32_16x16x32_bf16 v[58:61], a[240:243], v[102:105], v[58:61]
s_waitcnt lgkmcnt(1)
v_mfma_f32_16x16x32_bf16 v[58:61], v[26:29], v[106:109], v[58:61]
s_waitcnt lgkmcnt(0)
global_load_lds_dwordx4 v2, s[38:39] offset:-1024 sc1
v_mfma_f32_16x16x32_bf16 v[58:61], v[42:45], v[110:113], v[58:61]
v_mfma_f32_16x16x32_bf16 v[66:69], a[200:203], v[90:93], v[66:69]
v_mfma_f32_16x16x32_bf16 v[66:69], a[216:219], v[94:97], v[66:69]
global_load_lds_dwordx4 v2, s[38:39] offset:0 sc1
v_mfma_f32_16x16x32_bf16 v[66:69], a[232:235], v[98:101], v[66:69]
v_mfma_f32_16x16x32_bf16 v[66:69], a[248:251], v[102:105], v[66:69]
v_mfma_f32_16x16x32_bf16 v[66:69], v[34:37], v[106:109], v[66:69]
global_load_lds_dwordx4 v2, s[38:39] offset:1024 sc1
v_mfma_f32_16x16x32_bf16 v[66:69], v[50:53], v[110:113], v[66:69]
v_mfma_f32_16x16x32_bf16 v[62:65], a[196:199], v[90:93], v[62:65]
s_waitcnt vmcnt(5)
s_barrier
v_mfma_f32_16x16x32_bf16 v[62:65], a[212:215], v[94:97], v[62:65]
global_load_lds_dwordx4 v2, s[38:39] offset:2048 sc1
v_mfma_f32_16x16x32_bf16 v[62:65], a[228:231], v[98:101], v[62:65]
v_mfma_f32_16x16x32_bf16 v[62:65], a[244:247], v[102:105], v[62:65]
ds_read_b128 v[114:117], v8 offset:18432
v_mfma_f32_16x16x32_bf16 v[62:65], v[30:33], v[106:109], v[62:65]
ds_read_b128 v[118:121], v8 offset:19456
global_load_lds_dwordx4 v2, s[38:39] offset:3072 sc1
v_mfma_f32_16x16x32_bf16 v[62:65], v[46:49], v[110:113], v[62:65]
ds_read_b128 v[122:125], v8 offset:20480
v_add_u32 v2, 18688, v2
v_mfma_f32_16x16x32_bf16 v[70:73], a[204:207], v[90:93], v[70:73]
ds_read_b128 v[126:129], v8 offset:21504
v_min_u32 v2, v2, v9
v_mfma_f32_16x16x32_bf16 v[70:73], a[220:223], v[94:97], v[70:73]
ds_read_b128 v[130:133], v8 offset:22528
v_mul_f32 v138, v58, v66
v_mfma_f32_16x16x32_bf16 v[70:73], a[236:239], v[98:101], v[70:73]
ds_read_b128 v[134:137], v8 offset:23552
v_mul_f32 v139, v59, v67
v_mfma_f32_16x16x32_bf16 v[70:73], a[252:255], v[102:105], v[70:73]
v_mul_f32 v140, v60, v68
v_mul_f32 v141, v61, v69
v_mfma_f32_16x16x32_bf16 v[70:73], v[38:41], v[106:109], v[70:73]
v_cvt_pk_bf16_f32 v146, v138, v139
v_cvt_pk_bf16_f32 v147, v140, v141
v_mfma_f32_16x16x32_bf16 v[70:73], v[54:57], v[110:113], v[70:73]
s_waitcnt lgkmcnt(5)
v_mfma_f32_16x16x32_bf16 v[74:77], a[0:3], v[114:117], 0
s_waitcnt lgkmcnt(4)
v_mfma_f32_16x16x32_bf16 v[74:77], a[16:19], v[118:121], v[74:77]
s_waitcnt lgkmcnt(3)
v_mfma_f32_16x16x32_bf16 v[74:77], a[32:35], v[122:125], v[74:77]
s_waitcnt lgkmcnt(2)
v_mfma_f32_16x16x32_bf16 v[74:77], a[48:51], v[126:129], v[74:77]
s_waitcnt lgkmcnt(1)
v_mul_f32 v142, v62, v70
v_mul_f32 v143, v63, v71
v_mfma_f32_16x16x32_bf16 v[74:77], a[64:67], v[130:133], v[74:77]
s_waitcnt lgkmcnt(0)
v_mul_f32 v144, v64, v72
v_mul_f32 v145, v65, v73
v_mfma_f32_16x16x32_bf16 v[74:77], a[80:83], v[134:137], v[74:77]
ds_read_b128 v[90:93], v8 offset:24576
v_cvt_pk_bf16_f32 v148, v142, v143
v_mfma_f32_16x16x32_bf16 v[82:85], a[8:11], v[114:117], 0
ds_read_b128 v[94:97], v8 offset:25600
v_cvt_pk_bf16_f32 v149, v144, v145
v_mfma_f32_16x16x32_bf16 v[82:85], a[24:27], v[118:121], v[82:85]
v_mfma_f32_16x16x32_bf16 v[82:85], a[40:43], v[122:125], v[82:85]
s_waitcnt vmcnt(6)
s_barrier
v_mfma_f32_16x16x32_bf16 v[82:85], a[56:59], v[126:129], v[82:85]
v_mfma_f32_16x16x32_bf16 v[82:85], a[72:75], v[130:133], v[82:85]
global_store_dwordx4 v3, v[146:149], s[38:39] sc1
v_mfma_f32_16x16x32_bf16 v[82:85], a[88:91], v[134:137], v[82:85]
ds_read_b128 v[98:101], v8 offset:26624
v_add_u32 v3, 147456, v3
v_mfma_f32_16x16x32_bf16 v[78:81], a[4:7], v[114:117], 0
v_mfma_f32_16x16x32_bf16 v[78:81], a[20:23], v[118:121], v[78:81]
v_mfma_f32_16x16x32_bf16 v[78:81], a[36:39], v[122:125], v[78:81]
ds_read_b128 v[102:105], v8 offset:27648
v_mfma_f32_16x16x32_bf16 v[78:81], a[52:55], v[126:129], v[78:81]
v_mfma_f32_16x16x32_bf16 v[78:81], a[68:71], v[130:133], v[78:81]
v_mfma_f32_16x16x32_bf16 v[78:81], a[84:87], v[134:137], v[78:81]
ds_read_b128 v[106:109], v8 offset:28672
v_mfma_f32_16x16x32_bf16 v[86:89], a[12:15], v[114:117], 0
v_mfma_f32_16x16x32_bf16 v[86:89], a[28:31], v[118:121], v[86:89]
v_mfma_f32_16x16x32_bf16 v[86:89], a[44:47], v[122:125], v[86:89]
ds_read_b128 v[110:113], v8 offset:29696
v_mfma_f32_16x16x32_bf16 v[86:89], a[60:63], v[126:129], v[86:89]
v_mfma_f32_16x16x32_bf16 v[86:89], a[76:79], v[130:133], v[86:89]
v_mfma_f32_16x16x32_bf16 v[86:89], a[92:95], v[134:137], v[86:89]
s_waitcnt lgkmcnt(5)
v_mfma_f32_16x16x32_bf16 v[74:77], a[96:99], v[90:93], v[74:77]
s_waitcnt lgkmcnt(4)
v_mfma_f32_16x16x32_bf16 v[74:77], a[112:115], v[94:97], v[74:77]
s_waitcnt lgkmcnt(3)
v_mfma_f32_16x16x32_bf16 v[74:77], a[128:131], v[98:101], v[74:77]
s_waitcnt lgkmcnt(2)
v_mfma_f32_16x16x32_bf16 v[74:77], a[144:147], v[102:105], v[74:77]
s_waitcnt lgkmcnt(1)
v_mfma_f32_16x16x32_bf16 v[74:77], a[160:163], v[106:109], v[74:77]
s_waitcnt lgkmcnt(0)
v_mfma_f32_16x16x32_bf16 v[74:77], a[176:179], v[110:113], v[74:77]
ds_read_b128 v[114:117], v8 offset:30720
v_mfma_f32_16x16x32_bf16 v[82:85], a[104:107], v[90:93], v[82:85]
v_mfma_f32_16x16x32_bf16 v[82:85], a[120:123], v[94:97], v[82:85]
v_mfma_f32_16x16x32_bf16 v[82:85], a[136:139], v[98:101], v[82:85]
ds_read_b128 v[118:121], v8 offset:31744
v_mfma_f32_16x16x32_bf16 v[82:85], a[152:155], v[102:105], v[82:85]
v_mfma_f32_16x16x32_bf16 v[82:85], a[168:171], v[106:109], v[82:85]
v_mfma_f32_16x16x32_bf16 v[82:85], a[184:187], v[110:113], v[82:85]
ds_read_b128 v[122:125], v8 offset:32768
v_mfma_f32_16x16x32_bf16 v[78:81], a[100:103], v[90:93], v[78:81]
v_mfma_f32_16x16x32_bf16 v[78:81], a[116:119], v[94:97], v[78:81]
v_mfma_f32_16x16x32_bf16 v[78:81], a[132:135], v[98:101], v[78:81]
ds_read_b128 v[126:129], v8 offset:33792
v_mfma_f32_16x16x32_bf16 v[78:81], a[148:151], v[102:105], v[78:81]
v_mfma_f32_16x16x32_bf16 v[78:81], a[164:167], v[106:109], v[78:81]
v_mfma_f32_16x16x32_bf16 v[78:81], a[180:183], v[110:113], v[78:81]
ds_read_b128 v[130:133], v8 offset:34816
v_mfma_f32_16x16x32_bf16 v[86:89], a[108:111], v[90:93], v[86:89]
v_mfma_f32_16x16x32_bf16 v[86:89], a[124:127], v[94:97], v[86:89]
v_mfma_f32_16x16x32_bf16 v[86:89], a[140:143], v[98:101], v[86:89]
ds_read_b128 v[134:137], v8 offset:35840
v_mfma_f32_16x16x32_bf16 v[86:89], a[156:159], v[102:105], v[86:89]
v_mfma_f32_16x16x32_bf16 v[86:89], a[172:175], v[106:109], v[86:89]
s_add_u32 m0, s43, 18432
v_mfma_f32_16x16x32_bf16 v[86:89], a[188:191], v[110:113], v[86:89]
s_waitcnt lgkmcnt(5)
v_mfma_f32_16x16x32_bf16 v[74:77], a[192:195], v[114:117], v[74:77]
s_barrier
s_waitcnt lgkmcnt(4)
v_mfma_f32_16x16x32_bf16 v[74:77], a[208:211], v[118:121], v[74:77]
s_waitcnt lgkmcnt(3)
global_load_lds_dwordx4 v2, s[38:39] offset:-2048 sc1
v_mfma_f32_16x16x32_bf16 v[74:77], a[224:227], v[122:125], v[74:77]
s_waitcnt lgkmcnt(2)
v_mfma_f32_16x16x32_bf16 v[74:77], a[240:243], v[126:129], v[74:77]
s_waitcnt lgkmcnt(1)
v_mfma_f32_16x16x32_bf16 v[74:77], v[26:29], v[130:133], v[74:77]
s_waitcnt lgkmcnt(0)
global_load_lds_dwordx4 v2, s[38:39] offset:-1024 sc1
v_mfma_f32_16x16x32_bf16 v[74:77], v[42:45], v[134:137], v[74:77]
v_mfma_f32_16x16x32_bf16 v[82:85], a[200:203], v[114:117], v[82:85]
v_mfma_f32_16x16x32_bf16 v[82:85], a[216:219], v[118:121], v[82:85]
global_load_lds_dwordx4 v2, s[38:39] offset:0 sc1
v_mfma_f32_16x16x32_bf16 v[82:85], a[232:235], v[122:125], v[82:85]
v_mfma_f32_16x16x32_bf16 v[82:85], a[248:251], v[126:129], v[82:85]
v_mfma_f32_16x16x32_bf16 v[82:85], v[34:37], v[130:133], v[82:85]
v_xor_b32 v8, 0x10000, v8
global_load_lds_dwordx4 v2, s[38:39] offset:1024 sc1
v_mfma_f32_16x16x32_bf16 v[82:85], v[50:53], v[134:137], v[82:85]
v_mfma_f32_16x16x32_bf16 v[78:81], a[196:199], v[114:117], v[78:81]
s_waitcnt vmcnt(5)
s_barrier
v_mfma_f32_16x16x32_bf16 v[78:81], a[212:215], v[118:121], v[78:81]
global_load_lds_dwordx4 v2, s[38:39] offset:2048 sc1
v_mfma_f32_16x16x32_bf16 v[78:81], a[228:231], v[122:125], v[78:81]
v_mfma_f32_16x16x32_bf16 v[78:81], a[244:247], v[126:129], v[78:81]
ds_read_b128 v[90:93], v8 offset:0
v_mfma_f32_16x16x32_bf16 v[78:81], v[30:33], v[130:133], v[78:81]
ds_read_b128 v[94:97], v8 offset:1024
global_load_lds_dwordx4 v2, s[38:39] offset:3072 sc1
v_mfma_f32_16x16x32_bf16 v[78:81], v[46:49], v[134:137], v[78:81]
ds_read_b128 v[98:101], v8 offset:2048
v_add_u32 v2, 18688, v2
v_mfma_f32_16x16x32_bf16 v[86:89], a[204:207], v[114:117], v[86:89]
ds_read_b128 v[102:105], v8 offset:3072
v_min_u32 v2, v2, v9
v_mfma_f32_16x16x32_bf16 v[86:89], a[220:223], v[118:121], v[86:89]
ds_read_b128 v[106:109], v8 offset:4096
v_mul_f32 v138, v74, v82
v_mfma_f32_16x16x32_bf16 v[86:89], a[236:239], v[122:125], v[86:89]
ds_read_b128 v[110:113], v8 offset:5120
v_mul_f32 v139, v75, v83
v_mfma_f32_16x16x32_bf16 v[86:89], a[252:255], v[126:129], v[86:89]
v_mul_f32 v140, v76, v84
v_mul_f32 v141, v77, v85
v_mfma_f32_16x16x32_bf16 v[86:89], v[38:41], v[130:133], v[86:89]
v_mfma_f32_16x16x32_bf16 v[86:89], v[54:57], v[134:137], v[86:89]
s_sub_u32 s70, s70, 1
s_cbranch_scc0 .Lgu_top5
v_mul_f32 v138, v74, v82
v_mul_f32 v139, v75, v83
v_mul_f32 v140, v76, v84
v_mul_f32 v141, v77, v85
v_cvt_pk_bf16_f32 v146, v138, v139
v_cvt_pk_bf16_f32 v147, v140, v141
v_mul_f32 v142, v78, v86
v_mul_f32 v143, v79, v87
v_mul_f32 v144, v80, v88
v_mul_f32 v145, v81, v89
v_cvt_pk_bf16_f32 v148, v142, v143
v_cvt_pk_bf16_f32 v149, v144, v145
global_store_dwordx4 v3, v[146:149], s[38:39] sc1
s_waitcnt vmcnt(1) lgkmcnt(0)
s_barrier
v_mov_b32 v218, 192
ds_read_b32 v218, v218 offset:36864
s_waitcnt vmcnt(0)
s_barrier
s_waitcnt lgkmcnt(0)
v_readfirstlane_b32 s60, v218
s_nop 3
s_max_i32 s60, s60, 2
s_min_i32 s60, s60, 66
s_add_u32 s60, s60, -2
s_branch .Lgu_fin
.Lgu_w2:
v_add_u32 v254, -1, v5
s_mov_b32 s70, 32
s_waitcnt vmcnt(0) lgkmcnt(0)
s_nop 7
.p2align 6
.Lgu_top6:
s_waitcnt lgkmcnt(5)
v_mfma_f32_16x16x32_bf16 v[58:61], a[0:3], v[90:93], 0
s_waitcnt lgkmcnt(4)
v_mul_f32 v141, v77, v85
v_cvt_pk_bf16_f32 v146, v138, v139
v_mfma_f32_16x16x32_bf16 v[58:61], a[16:19], v[94:97], v[58:61]
s_waitcnt lgkmcnt(3)
v_cvt_pk_bf16_f32 v147, v140, v141
v_mfma_f32_16x16x32_bf16 v[58:61], a[32:35], v[98:101], v[58:61]
s_waitcnt lgkmcnt(2)
v_mfma_f32_16x16x32_bf16 v[58:61], a[48:51], v[102:105], v[58:61]
s_waitcnt lgkmcnt(1)
v_mul_f32 v142, v78, v86
v_mul_f32 v143, v79, v87
v_mfma_f32_16x16x32_bf16 v[58:61], a[64:67], v[106:109], v[58:61]
s_waitcnt lgkmcnt(0)
v_mul_f32 v144, v80, v88
v_mul_f32 v145, v81, v89
v_mfma_f32_16x16x32_bf16 v[58:61], a[80:83], v[110:113], v[58:61]
ds_read_b128 v[114:117], v8 offset:6144
v_cvt_pk_bf16_f32 v148, v142, v143
v_mfma_f32_16x16x32_bf16 v[66:69], a[8:11], v[90:93], 0
ds_read_b128 v[118:121], v8 offset:7168
v_cvt_pk_bf16_f32 v149, v144, v145
v_mfma_f32_16x16x32_bf16 v[66:69], a[24:27], v[94:97], v[66:69]
v_mfma_f32_16x16x32_bf16 v[66:69], a[40:43], v[98:101], v[66:69]
s_waitcnt vmcnt(6)
s_barrier
v_mfma_f32_16x16x32_bf16 v[66:69], a[56:59], v[102:105], v[66:69]
s_mov_b64 exec, 1
global_store_dword v6, v7, s[38:39] sc1
s_mov_b64 exec, -1
v_mfma_f32_16x16x32_bf16 v[66:69], a[72:75], v[106:109], v[66:69]
v_add_u32 v7, 1, v7
v_mfma_f32_16x16x32_bf16 v[66:69], a[88:91], v[110:113], v[66:69]
ds_read_b128 v[122:125], v8 offset:8192
v_mfma_f32_16x16x32_bf16 v[62:65], a[4:7], v[90:93], 0
global_store_dwordx4 v3, v[146:149], s[38:39] sc1
v_mfma_f32_16x16x32_bf16 v[62:65], a[20:23], v[94:97], v[62:65]
v_add_u32 v3, 147456, v3
v_mfma_f32_16x16x32_bf16 v[62:65], a[36:39], v[98:101], v[62:65]
ds_read_b128 v[126:129], v8 offset:9216
v_mfma_f32_16x16x32_bf16 v[62:65], a[52:55], v[102:105], v[62:65]
v_mfma_f32_16x16x32_bf16 v[62:65], a[68:71], v[106:109], v[62:65]
v_mfma_f32_16x16x32_bf16 v[62:65], a[84:87], v[110:113], v[62:65]
ds_read_b128 v[130:133], v8 offset:10240
v_mfma_f32_16x16x32_bf16 v[70:73], a[12:15], v[90:93], 0
v_mfma_f32_16x16x32_bf16 v[70:73], a[28:31], v[94:97], v[70:73]
v_mfma_f32_16x16x32_bf16 v[70:73], a[44:47], v[98:101], v[70:73]
ds_read_b128 v[134:137], v8 offset:11264
v_mfma_f32_16x16x32_bf16 v[70:73], a[60:63], v[102:105], v[70:73]
v_mfma_f32_16x16x32_bf16 v[70:73], a[76:79], v[106:109], v[70:73]
v_mfma_f32_16x16x32_bf16 v[70:73], a[92:95], v[110:113], v[70:73]
s_waitcnt lgkmcnt(5)
v_mfma_f32_16x16x32_bf16 v[58:61], a[96:99], v[114:117], v[58:61]
s_waitcnt lgkmcnt(4)
v_mfma_f32_16x16x32_bf16 v[58:61], a[112:115], v[118:121], v[58:61]
s_waitcnt lgkmcnt(3)
v_mfma_f32_16x16x32_bf16 v[58:61], a[128:131], v[122:125], v[58:61]
s_waitcnt lgkmcnt(2)
v_mfma_f32_16x16x32_bf16 v[58:61], a[144:147], v[126:129], v[58:61]
s_waitcnt lgkmcnt(1)
v_mfma_f32_16x16x32_bf16 v[58:61], a[160:163], v[130:133], v[58:61]
s_waitcnt lgkmcnt(0)
v_mfma_f32_16x16x32_bf16 v[58:61], a[176:179], v[134:137], v[58:61]
ds_read_b128 v[90:93], v8 offset:12288
v_mfma_f32_16x16x32_bf16 v[66:69], a[104:107], v[114:117], v[66:69]
v_mfma_f32_16x16x32_bf16 v[66:69], a[120:123], v[118:121], v[66:69]
v_mfma_f32_16x16x32_bf16 v[66:69], a[136:139], v[122:125], v[66:69]
ds_read_b128 v[94:97], v8 offset:13312
v_mfma_f32_16x16x32_bf16 v[66:69], a[152:155], v[126:129], v[66:69]
v_mfma_f32_16x16x32_bf16 v[66:69], a[168:171], v[130:133], v[66:69]
v_mfma_f32_16x16x32_bf16 v[66:69], a[184:187], v[134:137], v[66:69]
ds_read_b128 v[98:101], v8 offset:14336
v_mfma_f32_16x16x32_bf16 v[62:65], a[100:103], v[114:117], v[62:65]
v_mfma_f32_16x16x32_bf16 v[62:65], a[116:119], v[118:121], v[62:65]
v_mfma_f32_16x16x32_bf16 v[62:65], a[132:135], v[122:125], v[62:65]
ds_read_b128 v[102:105], v8 offset:15360
v_mfma_f32_16x16x32_bf16 v[62:65], a[148:151], v[126:129], v[62:65]
v_mfma_f32_16x16x32_bf16 v[62:65], a[164:167], v[130:133], v[62:65]
v_mfma_f32_16x16x32_bf16 v[62:65], a[180:183], v[134:137], v[62:65]
ds_read_b128 v[106:109], v8 offset:16384
v_mfma_f32_16x16x32_bf16 v[70:73], a[108:111], v[114:117], v[70:73]
v_mfma_f32_16x16x32_bf16 v[70:73], a[124:127], v[118:121], v[70:73]
v_mfma_f32_16x16x32_bf16 v[70:73], a[140:143], v[122:125], v[70:73]
ds_read_b128 v[110:113], v8 offset:17408
v_mfma_f32_16x16x32_bf16 v[70:73], a[156:159], v[126:129], v[70:73]
s_xor_b32 s43, s43, 0x10000
v_mfma_f32_16x16x32_bf16 v[70:73], a[172:175], v[130:133], v[70:73]
s_add_u32 m0, s43, 0
v_mfma_f32_16x16x32_bf16 v[70:73], a[188:191], v[134:137], v[70:73]
s_waitcnt lgkmcnt(5)
v_mfma_f32_16x16x32_bf16 v[58:61], a[192:195], v[90:93], v[58:61]
)"
R"(s_barrier
s_waitcnt lgkmcnt(4)
v_mfma_f32_16x16x32_bf16 v[58:61], a[208:211], v[94:97], v[58:61]
s_waitcnt lgkmcnt(3)
v_mfma_f32_16x16x32_bf16 v[58:61], a[224:227], v[98:101], v[58:61]
s_waitcnt lgkmcnt(2)
global_load_lds_dwordx4 v2, s[38:39] offset:-2048 sc1
v_mfma_f32_16x16x32_bf16 v[58:61], a[240:243], v[102:105], v[58:61]
s_waitcnt lgkmcnt(1)
v_mfma_f32_16x16x32_bf16 v[58:61], v[26:29], v[106:109], v[58:61]
s_waitcnt lgkmcnt(0)
v_mfma_f32_16x16x32_bf16 v[58:61], v[42:45], v[110:113], v[58:61]
global_load_lds_dwordx4 v2, s[38:39] offset:-1024 sc1
v_mfma_f32_16x16x32_bf16 v[66:69], a[200:203], v[90:93], v[66:69]
v_mfma_f32_16x16x32_bf16 v[66:69], a[216:219], v[94:97], v[66:69]
v_mfma_f32_16x16x32_bf16 v[66:69], a[232:235], v[98:101], v[66:69]
global_load_lds_dwordx4 v2, s[38:39] offset:0 sc1
v_mfma_f32_16x16x32_bf16 v[66:69], a[248:251], v[102:105], v[66:69]
v_mfma_f32_16x16x32_bf16 v[66:69], v[34:37], v[106:109], v[66:69]
v_mfma_f32_16x16x32_bf16 v[66:69], v[50:53], v[110:113], v[66:69]
v_mfma_f32_16x16x32_bf16 v[62:65], a[196:199], v[90:93], v[62:65]
s_waitcnt vmcnt(5)
s_barrier
v_mfma_f32_16x16x32_bf16 v[62:65], a[212:215], v[94:97], v[62:65]
global_load_lds_dwordx4 v2, s[38:39] offset:1024 sc1
v_mfma_f32_16x16x32_bf16 v[62:65], a[228:231], v[98:101], v[62:65]
v_mfma_f32_16x16x32_bf16 v[62:65], a[244:247], v[102:105], v[62:65]
ds_read_b128 v[114:117], v8 offset:18432
v_mfma_f32_16x16x32_bf16 v[62:65], v[30:33], v[106:109], v[62:65]
ds_read_b128 v[118:121], v8 offset:19456
global_load_lds_dwordx4 v2, s[38:39] offset:2048 sc1
v_mfma_f32_16x16x32_bf16 v[62:65], v[46:49], v[110:113], v[62:65]
ds_read_b128 v[122:125], v8 offset:20480
v_mul_f32 v138, v58, v66
v_mfma_f32_16x16x32_bf16 v[70:73], a[204:207], v[90:93], v[70:73]
ds_read_b128 v[126:129], v8 offset:21504
v_mul_f32 v139, v59, v67
v_mfma_f32_16x16x32_bf16 v[70:73], a[220:223], v[94:97], v[70:73]
ds_read_b128 v[130:133], v8 offset:22528
global_load_lds_dwordx4 v2, s[38:39] offset:3072 sc1
v_mfma_f32_16x16x32_bf16 v[70:73], a[236:239], v[98:101], v[70:73]
ds_read_b128 v[134:137], v8 offset:23552
v_add_u32 v2, 18688, v2
v_mfma_f32_16x16x32_bf16 v[70:73], a[252:255], v[102:105], v[70:73]
v_min_u32 v2, v2, v9
v_mul_f32 v140, v60, v68
v_mfma_f32_16x16x32_bf16 v[70:73], v[38:41], v[106:109], v[70:73]
v_mul_f32 v141, v61, v69
v_cvt_pk_bf16_f32 v146, v138, v139
v_mfma_f32_16x16x32_bf16 v[70:73], v[54:57], v[110:113], v[70:73]
s_waitcnt lgkmcnt(5)
v_cvt_pk_bf16_f32 v147, v140, v141
v_mfma_f32_16x16x32_bf16 v[74:77], a[0:3], v[114:117], 0
s_waitcnt lgkmcnt(4)
v_mfma_f32_16x16x32_bf16 v[74:77], a[16:19], v[118:121], v[74:77]
s_waitcnt lgkmcnt(3)
v_mfma_f32_16x16x32_bf16 v[74:77], a[32:35], v[122:125], v[74:77]
s_waitcnt lgkmcnt(2)
v_mfma_f32_16x16x32_bf16 v[74:77], a[48:51], v[126:129], v[74:77]
s_waitcnt lgkmcnt(1)
v_mul_f32 v142, v62, v70
v_mul_f32 v143, v63, v71
v_mfma_f32_16x16x32_bf16 v[74:77], a[64:67], v[130:133], v[74:77]
s_waitcnt lgkmcnt(0)
v_mul_f32 v144, v64, v72
v_mul_f32 v145, v65, v73
v_mfma_f32_16x16x32_bf16 v[74:77], a[80:83], v[134:137], v[74:77]
ds_read_b128 v[90:93], v8 offset:24576
v_cvt_pk_bf16_f32 v148, v142, v143
v_mfma_f32_16x16x32_bf16 v[82:85], a[8:11], v[114:117], 0
ds_read_b128 v[94:97], v8 offset:25600
v_cvt_pk_bf16_f32 v149, v144, v145
v_mfma_f32_16x16x32_bf16 v[82:85], a[24:27], v[118:121], v[82:85]
v_mfma_f32_16x16x32_bf16 v[82:85], a[40:43], v[122:125], v[82:85]
s_waitcnt vmcnt(6)
s_barrier
v_mfma_f32_16x16x32_bf16 v[82:85], a[56:59], v[126:129], v[82:85]
s_mov_b64 exec, 1
global_store_dword v6, v7, s[38:39] sc1
s_mov_b64 exec, -1
v_mfma_f32_16x16x32_bf16 v[82:85], a[72:75], v[130:133], v[82:85]
v_add_u32 v7, 1, v7
v_mfma_f32_16x16x32_bf16 v[82:85], a[88:91], v[134:137], v[82:85]
ds_read_b128 v[98:101], v8 offset:26624
v_mfma_f32_16x16x32_bf16 v[78:81], a[4:7], v[114:117], 0
global_store_dwordx4 v3, v[146:149], s[38:39] sc1
v_mfma_f32_16x16x32_bf16 v[78:81], a[20:23], v[118:121], v[78:81]
v_add_u32 v3, 147456, v3
v_mfma_f32_16x16x32_bf16 v[78:81], a[36:39], v[122:125], v[78:81]
ds_read_b128 v[102:105], v8 offset:27648
v_mfma_f32_16x16x32_bf16 v[78:81], a[52:55], v[126:129], v[78:81]
v_mfma_f32_16x16x32_bf16 v[78:81], a[68:71], v[130:133], v[78:81]
v_mfma_f32_16x16x32_bf16 v[78:81], a[84:87], v[134:137], v[78:81]
ds_read_b128 v[106:109], v8 offset:28672
v_mfma_f32_16x16x32_bf16 v[86:89], a[12:15], v[114:117], 0
v_mfma_f32_16x16x32_bf16 v[86:89], a[28:31], v[118:121], v[86:89]
v_mfma_f32_16x16x32_bf16 v[86:89], a[44:47], v[122:125], v[86:89]
ds_read_b128 v[110:113], v8 offset:29696
v_mfma_f32_16x16x32_bf16 v[86:89], a[60:63], v[126:129], v[86:89]
v_mfma_f32_16x16x32_bf16 v[86:89], a[76:79], v[130:133], v[86:89]
v_mfma_f32_16x16x32_bf16 v[86:89], a[92:95], v[134:137], v[86:89]
s_waitcnt lgkmcnt(5)
v_mfma_f32_16x16x32_bf16 v[74:77], a[96:99], v[90:93], v[74:77]
s_waitcnt lgkmcnt(4)
v_mfma_f32_16x16x32_bf16 v[74:77], a[112:115], v[94:97], v[74:77]
s_waitcnt lgkmcnt(3)
v_mfma_f32_16x16x32_bf16 v[74:77], a[128:131], v[98:101], v[74:77]
s_waitcnt lgkmcnt(2)
v_mfma_f32_16x16x32_bf16 v[74:77], a[144:147], v[102:105], v[74:77]
s_waitcnt lgkmcnt(1)
v_mfma_f32_16x16x32_bf16 v[74:77], a[160:163], v[106:109], v[74:77]
s_waitcnt lgkmcnt(0)
v_mfma_f32_16x16x32_bf16 v[74:77], a[176:179], v[110:113], v[74:77]
ds_read_b128 v[114:117], v8 offset:30720
v_mfma_f32_16x16x32_bf16 v[82:85], a[104:107], v[90:93], v[82:85]
v_mfma_f32_16x16x32_bf16 v[82:85], a[120:123], v[94:97], v[82:85]
v_mfma_f32_16x16x32_bf16 v[82:85], a[136:139], v[98:101], v[82:85]
ds_read_b128 v[118:121], v8 offset:31744
v_mfma_f32_16x16x32_bf16 v[82:85], a[152:155], v[102:105], v[82:85]
v_mfma_f32_16x16x32_bf16 v[82:85], a[168:171], v[106:109], v[82:85]
v_mfma_f32_16x16x32_bf16 v[82:85], a[184:187], v[110:113], v[82:85]
ds_read_b128 v[122:125], v8 offset:32768
v_mfma_f32_16x16x32_bf16 v[78:81], a[100:103], v[90:93], v[78:81]
v_mfma_f32_16x16x32_bf16 v[78:81], a[116:119], v[94:97], v[78:81]
v_mfma_f32_16x16x32_bf16 v[78:81], a[132:135], v[98:101], v[78:81]
ds_read_b128 v[126:129], v8 offset:33792
v_mfma_f32_16x16x32_bf16 v[78:81], a[148:151], v[102:105], v[78:81]
v_mfma_f32_16x16x32_bf16 v[78:81], a[164:167], v[106:109], v[78:81]
v_mfma_f32_16x16x32_bf16 v[78:81], a[180:183], v[110:113], v[78:81]
ds_read_b128 v[130:133], v8 offset:34816
v_mfma_f32_16x16x32_bf16 v[86:89], a[108:111], v[90:93], v[86:89]
v_mfma_f32_16x16x32_bf16 v[86:89], a[124:127], v[94:97], v[86:89]
v_mfma_f32_16x16x32_bf16 v[86:89], a[140:143], v[98:101], v[86:89]
ds_read_b128 v[134:137], v8 offset:35840
v_mfma_f32_16x16x32_bf16 v[86:89], a[156:159], v[102:105], v[86:89]
v_mfma_f32_16x16x32_bf16 v[86:89], a[172:175], v[106:109], v[86:89]
s_add_u32 m0, s43, 18432
v_mfma_f32_16x16x32_bf16 v[86:89], a[188:191], v[110:113], v[86:89]
s_waitcnt lgkmcnt(5)
v_mfma_f32_16x16x32_bf16 v[74:77], a[192:195], v[114:117], v[74:77]
s_barrier
s_waitcnt lgkmcnt(4)
v_mfma_f32_16x16x32_bf16 v[74:77], a[208:211], v[118:121], v[74:77]
s_waitcnt lgkmcnt(3)
v_mfma_f32_16x16x32_bf16 v[74:77], a[224:227], v[122:125], v[74:77]
s_waitcnt lgkmcnt(2)
global_load_lds_dwordx4 v2, s[38:39] offset:-2048 sc1
v_mfma_f32_16x16x32_bf16 v[74:77], a[240:243], v[126:129], v[74:77]
s_waitcnt lgkmcnt(1)
v_mfma_f32_16x16x32_bf16 v[74:77], v[26:29], v[130:133], v[74:77]
s_waitcnt lgkmcnt(0)
v_mfma_f32_16x16x32_bf16 v[74:77], v[42:45], v[134:137], v[74:77]
global_load_lds_dwordx4 v2, s[38:39] offset:-1024 sc1
v_mfma_f32_16x16x32_bf16 v[82:85], a[200:203], v[114:117], v[82:85]
v_mfma_f32_16x16x32_bf16 v[82:85], a[216:219], v[118:121], v[82:85]
v_mfma_f32_16x16x32_bf16 v[82:85], a[232:235], v[122:125], v[82:85]
global_load_lds_dwordx4 v2, s[38:39] offset:0 sc1
v_mfma_f32_16x16x32_bf16 v[82:85], a[248:251], v[126:129], v[82:85]
v_mfma_f32_16x16x32_bf16 v[82:85], v[34:37], v[130:133], v[82:85]
v_xor_b32 v8, 0x10000, v8
v_mfma_f32_16x16x32_bf16 v[82:85], v[50:53], v[134:137], v[82:85]
v_mfma_f32_16x16x32_bf16 v[78:81], a[196:199], v[114:117], v[78:81]
s_waitcnt vmcnt(5)
s_barrier
v_mfma_f32_16x16x32_bf16 v[78:81], a[212:215], v[118:121], v[78:81]
global_load_lds_dwordx4 v2, s[38:39] offset:1024 sc1
v_mfma_f32_16x16x32_bf16 v[78:81], a[228:231], v[122:125], v[78:81]
v_mfma_f32_16x16x32_bf16 v[78:81], a[244:247], v[126:129], v[78:81]
ds_read_b128 v[90:93], v8 offset:0
v_mfma_f32_16x16x32_bf16 v[78:81], v[30:33], v[130:133], v[78:81]
ds_read_b128 v[94:97], v8 offset:1024
global_load_lds_dwordx4 v2, s[38:39] offset:2048 sc1
v_mfma_f32_16x16x32_bf16 v[78:81], v[46:49], v[134:137], v[78:81]
ds_read_b128 v[98:101], v8 offset:2048
v_mul_f32 v138, v74, v82
v_mfma_f32_16x16x32_bf16 v[86:89], a[204:207], v[114:117], v[86:89]
ds_read_b128 v[102:105], v8 offset:3072
v_mul_f32 v139, v75, v83
v_mfma_f32_16x16x32_bf16 v[86:89], a[220:223], v[118:121], v[86:89]
ds_read_b128 v[106:109], v8 offset:4096
global_load_lds_dwordx4 v2, s[38:39] offset:3072 sc1
v_mfma_f32_16x16x32_bf16 v[86:89], a[236:239], v[122:125], v[86:89]
ds_read_b128 v[110:113], v8 offset:5120
v_add_u32 v2, 18688, v2
v_mfma_f32_16x16x32_bf16 v[86:89], a[252:255], v[126:129], v[86:89]
v_min_u32 v2, v2, v9
v_mul_f32 v140, v76, v84
v_mfma_f32_16x16x32_bf16 v[86:89], v[38:41], v[130:133], v[86:89]
v_mfma_f32_16x16x32_bf16 v[86:89], v[54:57], v[134:137], v[86:89]
s_sub_u32 s70, s70, 1
s_cbranch_scc0 .Lgu_top6
v_mul_f32 v138, v74, v82
v_mul_f32 v139, v75, v83
v_mul_f32 v140, v76, v84
v_mul_f32 v141, v77, v85
v_cvt_pk_bf16_f32 v146, v138, v139
v_cvt_pk_bf16_f32 v147, v140, v141
v_mul_f32 v142, v78, v86
v_mul_f32 v143, v79, v87
v_mul_f32 v144, v80, v88
v_mul_f32 v145, v81, v89
v_cvt_pk_bf16_f32 v148, v142, v143
v_cvt_pk_bf16_f32 v149, v144, v145
global_store_dwordx4 v3, v[146:149], s[38:39] sc1
s_waitcnt vmcnt(1) lgkmcnt(0)
s_barrier
s_mov_b64 exec, 1
global_store_dword v6, v7, s[38:39] sc1
s_mov_b64 exec, -1
v_add_u32 v7, 3, v7
v_mov_b32 v218, 192
ds_read_b32 v218, v218 offset:36864
s_waitcnt vmcnt(1)
s_barrier
s_mov_b64 exec, 1
global_store_dword v6, v7, s[38:39] sc1
s_mov_b64 exec, -1
s_waitcnt lgkmcnt(0)
v_readfirstlane_b32 s60, v218
s_nop 3
s_max_i32 s60, s60, 2
s_min_i32 s60, s60, 66
s_add_u32 s60, s60, -1
s_branch .Lgu_fin
.Lgu_w3:
v_add_u32 v254, -1, v5
s_mov_b32 s70, 32
s_waitcnt vmcnt(0) lgkmcnt(0)
s_nop 7
.p2align 6
.Lgu_top11:
s_waitcnt lgkmcnt(5)
v_mfma_f32_16x16x32_bf16 v[58:61], a[0:3], v[90:93], 0
s_waitcnt lgkmcnt(4)
v_mul_f32 v141, v77, v85
v_cvt_pk_bf16_f32 v146, v138, v139
v_mfma_f32_16x16x32_bf16 v[58:61], a[16:19], v[94:97], v[58:61]
s_waitcnt lgkmcnt(3)
v_cvt_pk_bf16_f32 v147, v140, v141
v_mfma_f32_16x16x32_bf16 v[58:61], a[32:35], v[98:101], v[58:61]
s_waitcnt lgkmcnt(2)
global_load_dword v254, v4, s[38:39] sc1
v_mfma_f32_16x16x32_bf16 v[58:61], a[48:51], v[102:105], v[58:61]
s_waitcnt lgkmcnt(1)
v_mul_f32 v142, v78, v86
v_mul_f32 v143, v79, v87
v_mfma_f32_16x16x32_bf16 v[58:61], a[64:67], v[106:109], v[58:61]
s_waitcnt lgkmcnt(0)
v_mul_f32 v144, v80, v88
v_mul_f32 v145, v81, v89
v_mfma_f32_16x16x32_bf16 v[58:61], a[80:83], v[110:113], v[58:61]
ds_read_b128 v[114:117], v8 offset:6144
v_cvt_pk_bf16_f32 v148, v142, v143
v_mfma_f32_16x16x32_bf16 v[66:69], a[8:11], v[90:93], 0
ds_read_b128 v[118:121], v8 offset:7168
v_cvt_pk_bf16_f32 v149, v144, v145
v_mfma_f32_16x16x32_bf16 v[66:69], a[24:27], v[94:97], v[66:69]
v_mfma_f32_16x16x32_bf16 v[66:69], a[40:43], v[98:101], v[66:69]
s_waitcnt vmcnt(7)
s_barrier
v_mfma_f32_16x16x32_bf16 v[66:69], a[56:59], v[102:105], v[66:69]
v_mfma_f32_16x16x32_bf16 v[66:69], a[72:75], v[106:109], v[66:69]
v_mfma_f32_16x16x32_bf16 v[66:69], a[88:91], v[110:113], v[66:69]
ds_read_b128 v[122:125], v8 offset:8192
v_mfma_f32_16x16x32_bf16 v[62:65], a[4:7], v[90:93], 0
global_store_dwordx4 v3, v[146:149], s[38:39] sc1
v_mfma_f32_16x16x32_bf16 v[62:65], a[20:23], v[94:97], v[62:65]
v_add_u32 v3, 147456, v3
v_mfma_f32_16x16x32_bf16 v[62:65], a[36:39], v[98:101], v[62:65]
ds_read_b128 v[126:129], v8 offset:9216
v_mfma_f32_16x16x32_bf16 v[62:65], a[52:55], v[102:105], v[62:65]
v_mfma_f32_16x16x32_bf16 v[62:65], a[68:71], v[106:109], v[62:65]
v_mfma_f32_16x16x32_bf16 v[62:65], a[84:87], v[110:113], v[62:65]
ds_read_b128 v[130:133], v8 offset:10240
v_mfma_f32_16x16x32_bf16 v[70:73], a[12:15], v[90:93], 0
v_mfma_f32_16x16x32_bf16 v[70:73], a[28:31], v[94:97], v[70:73]
v_mfma_f32_16x16x32_bf16 v[70:73], a[44:47], v[98:101], v[70:73]
ds_read_b128 v[134:137], v8 offset:11264
v_mfma_f32_16x16x32_bf16 v[70:73], a[60:63], v[102:105], v[70:73]
v_mfma_f32_16x16x32_bf16 v[70:73], a[76:79], v[106:109], v[70:73]
v_mfma_f32_16x16x32_bf16 v[70:73], a[92:95], v[110:113], v[70:73]
s_waitcnt lgkmcnt(5)
v_mfma_f32_16x16x32_bf16 v[58:61], a[96:99], v[114:117], v[58:61]
s_waitcnt lgkmcnt(4)
v_mfma_f32_16x16x32_bf16 v[58:61], a[112:115], v[118:121], v[58:61]
s_waitcnt lgkmcnt(3)
v_mfma_f32_16x16x32_bf16 v[58:61], a[128:131], v[122:125], v[58:61]
s_waitcnt lgkmcnt(2)
v_mfma_f32_16x16x32_bf16 v[58:61], a[144:147], v[126:129], v[58:61]
s_waitcnt lgkmcnt(1)
v_mfma_f32_16x16x32_bf16 v[58:61], a[160:163], v[130:133], v[58:61]
s_waitcnt lgkmcnt(0)
v_mfma_f32_16x16x32_bf16 v[58:61], a[176:179], v[134:137], v[58:61]
ds_read_b128 v[90:93], v8 offset:12288
v_mfma_f32_16x16x32_bf16 v[66:69], a[104:107], v[114:117], v[66:69]
v_mfma_f32_16x16x32_bf16 v[66:69], a[120:123], v[118:121], v[66:69]
v_mfma_f32_16x16x32_bf16 v[66:69], a[136:139], v[122:125], v[66:69]
ds_read_b128 v[94:97], v8 offset:13312
v_mfma_f32_16x16x32_bf16 v[66:69], a[152:155], v[126:129], v[66:69]
v_mfma_f32_16x16x32_bf16 v[66:69], a[168:171], v[130:133], v[66:69]
v_mfma_f32_16x16x32_bf16 v[66:69], a[184:187], v[134:137], v[66:69]
ds_read_b128 v[98:101], v8 offset:14336
v_mfma_f32_16x16x32_bf16 v[62:65], a[100:103], v[114:117], v[62:65]
v_mfma_f32_16x16x32_bf16 v[62:65], a[116:119], v[118:121], v[62:65]
v_mfma_f32_16x16x32_bf16 v[62:65], a[132:135], v[122:125], v[62:65]
ds_read_b128 v[102:105], v8 offset:15360
v_mfma_f32_16x16x32_bf16 v[62:65], a[148:151], v[126:129], v[62:65]
v_mfma_f32_16x16x32_bf16 v[62:65], a[164:167], v[130:133], v[62:65]
v_mfma_f32_16x16x32_bf16 v[62:65], a[180:183], v[134:137], v[62:65]
ds_read_b128 v[106:109], v8 offset:16384
v_mfma_f32_16x16x32_bf16 v[70:73], a[108:111], v[114:117], v[70:73]
v_mfma_f32_16x16x32_bf16 v[70:73], a[124:127], v[118:121], v[70:73]
s_waitcnt vmcnt(1)
v_sub_u32 v254, v254, v5
v_cmp_lt_u32 vcc, 0x7f, v254
v_mfma_f32_16x16x32_bf16 v[70:73], a[140:143], v[122:125], v[70:73]
ds_read_b128 v[110:113], v8 offset:17408
v_mfma_f32_16x16x32_bf16 v[70:73], a[156:159], v[126:129], v[70:73]
s_xor_b32 s43, s43, 0x10000
v_mfma_f32_16x16x32_bf16 v[70:73], a[172:175], v[130:133], v[70:73]
s_cbranch_vccnz .Lgu_sp7
.Lgu_rt8:
s_add_u32 m0, s43, 0
v_mfma_f32_16x16x32_bf16 v[70:73], a[188:191], v[134:137], v[70:73]
s_waitcnt lgkmcnt(5)
v_mfma_f32_16x16x32_bf16 v[58:61], a[192:195], v[90:93], v[58:61]
s_barrier
s_waitcnt lgkmcnt(4)
v_mfma_f32_16x16x32_bf16 v[58:61], a[208:211], v[94:97], v[58:61]
s_waitcnt lgkmcnt(3)
v_add_u32 v5, 1, v5
v_mfma_f32_16x16x32_bf16 v[58:61], a[224:227], v[98:101], v[58:61]
s_waitcnt lgkmcnt(2)
v_mfma_f32_16x16x32_bf16 v[58:61], a[240:243], v[102:105], v[58:61]
s_waitcnt lgkmcnt(1)
global_load_lds_dwordx4 v2, s[38:39] offset:-2048 sc1
v_mfma_f32_16x16x32_bf16 v[58:61], v[26:29], v[106:109], v[58:61]
s_waitcnt lgkmcnt(0)
v_mfma_f32_16x16x32_bf16 v[58:61], v[42:45], v[110:113], v[58:61]
v_mfma_f32_16x16x32_bf16 v[66:69], a[200:203], v[90:93], v[66:69]
global_load_lds_dwordx4 v2, s[38:39] offset:-1024 sc1
v_mfma_f32_16x16x32_bf16 v[66:69], a[216:219], v[94:97], v[66:69]
v_mfma_f32_16x16x32_bf16 v[66:69], a[232:235], v[98:101], v[66:69]
v_mfma_f32_16x16x32_bf16 v[66:69], a[248:251], v[102:105], v[66:69]
global_load_lds_dwordx4 v2, s[38:39] offset:0 sc1
v_mfma_f32_16x16x32_bf16 v[66:69], v[34:37], v[106:109], v[66:69]
v_mfma_f32_16x16x32_bf16 v[66:69], v[50:53], v[110:113], v[66:69]
v_mfma_f32_16x16x32_bf16 v[62:65], a[196:199], v[90:93], v[62:65]
s_barrier
v_mfma_f32_16x16x32_bf16 v[62:65], a[212:215], v[94:97], v[62:65]
global_load_lds_dwordx4 v2, s[38:39] offset:1024 sc1
v_mfma_f32_16x16x32_bf16 v[62:65], a[228:231], v[98:101], v[62:65]
v_mfma_f32_16x16x32_bf16 v[62:65], a[244:247], v[102:105], v[62:65]
ds_read_b128 v[114:117], v8 offset:18432
v_mfma_f32_16x16x32_bf16 v[62:65], v[30:33], v[106:109], v[62:65]
ds_read_b128 v[118:121], v8 offset:19456
global_load_lds_dwordx4 v2, s[38:39] offset:2048 sc1
v_mfma_f32_16x16x32_bf16 v[62:65], v[46:49], v[110:113], v[62:65]
ds_read_b128 v[122:125], v8 offset:20480
v_mul_f32 v138, v58, v66
v_mfma_f32_16x16x32_bf16 v[70:73], a[204:207], v[90:93], v[70:73]
ds_read_b128 v[126:129], v8 offset:21504
v_mul_f32 v139, v59, v67
v_mfma_f32_16x16x32_bf16 v[70:73], a[220:223], v[94:97], v[70:73]
ds_read_b128 v[130:133], v8 offset:22528
global_load_lds_dwordx4 v2, s[38:39] offset:3072 sc1
v_mfma_f32_16x16x32_bf16 v[70:73], a[236:239], v[98:101], v[70:73]
ds_read_b128 v[134:137], v8 offset:23552
v_add_u32 v2, 18688, v2
v_mfma_f32_16x16x32_bf16 v[70:73], a[252:255], v[102:105], v[70:73]
v_min_u32 v2, v2, v9
v_mul_f32 v140, v60, v68
v_mfma_f32_16x16x32_bf16 v[70:73], v[38:41], v[106:109], v[70:73]
v_mul_f32 v141, v61, v69
v_cvt_pk_bf16_f32 v146, v138, v139
v_mfma_f32_16x16x32_bf16 v[70:73], v[54:57], v[110:113], v[70:73]
s_waitcnt lgkmcnt(5)
v_cvt_pk_bf16_f32 v147, v140, v141
v_mfma_f32_16x16x32_bf16 v[74:77], a[0:3], v[114:117], 0
s_waitcnt lgkmcnt(4)
v_mfma_f32_16x16x32_bf16 v[74:77], a[16:19], v[118:121], v[74:77]
s_waitcnt lgkmcnt(3)
v_mfma_f32_16x16x32_bf16 v[74:77], a[32:35], v[122:125], v[74:77]
s_waitcnt lgkmcnt(2)
global_load_dword v254, v4, s[38:39] sc1
v_mfma_f32_16x16x32_bf16 v[74:77], a[48:51], v[126:129], v[74:77]
s_waitcnt lgkmcnt(1)
v_mul_f32 v142, v62, v70
v_mul_f32 v143, v63, v71
v_mfma_f32_16x16x32_bf16 v[74:77], a[64:67], v[130:133], v[74:77]
s_waitcnt lgkmcnt(0)
v_mul_f32 v144, v64, v72
v_mul_f32 v145, v65, v73
v_mfma_f32_16x16x32_bf16 v[74:77], a[80:83], v[134:137], v[74:77]
ds_read_b128 v[90:93], v8 offset:24576
v_cvt_pk_bf16_f32 v148, v142, v143
v_mfma_f32_16x16x32_bf16 v[82:85], a[8:11], v[114:117], 0
ds_read_b128 v[94:97], v8 offset:25600
v_cvt_pk_bf16_f32 v149, v144, v145
v_mfma_f32_16x16x32_bf16 v[82:85], a[24:27], v[118:121], v[82:85]
)"
R"(v_mfma_f32_16x16x32_bf16 v[82:85], a[40:43], v[122:125], v[82:85]
s_waitcnt vmcnt(7)
s_barrier
v_mfma_f32_16x16x32_bf16 v[82:85], a[56:59], v[126:129], v[82:85]
v_mfma_f32_16x16x32_bf16 v[82:85], a[72:75], v[130:133], v[82:85]
v_mfma_f32_16x16x32_bf16 v[82:85], a[88:91], v[134:137], v[82:85]
ds_read_b128 v[98:101], v8 offset:26624
v_mfma_f32_16x16x32_bf16 v[78:81], a[4:7], v[114:117], 0
global_store_dwordx4 v3, v[146:149], s[38:39] sc1
v_mfma_f32_16x16x32_bf16 v[78:81], a[20:23], v[118:121], v[78:81]
v_add_u32 v3, 147456, v3
v_mfma_f32_16x16x32_bf16 v[78:81], a[36:39], v[122:125], v[78:81]
ds_read_b128 v[102:105], v8 offset:27648
v_mfma_f32_16x16x32_bf16 v[78:81], a[52:55], v[126:129], v[78:81]
v_mfma_f32_16x16x32_bf16 v[78:81], a[68:71], v[130:133], v[78:81]
v_mfma_f32_16x16x32_bf16 v[78:81], a[84:87], v[134:137], v[78:81]
ds_read_b128 v[106:109], v8 offset:28672
v_mfma_f32_16x16x32_bf16 v[86:89], a[12:15], v[114:117], 0
v_mfma_f32_16x16x32_bf16 v[86:89], a[28:31], v[118:121], v[86:89]
v_mfma_f32_16x16x32_bf16 v[86:89], a[44:47], v[122:125], v[86:89]
ds_read_b128 v[110:113], v8 offset:29696
v_mfma_f32_16x16x32_bf16 v[86:89], a[60:63], v[126:129], v[86:89]
v_mfma_f32_16x16x32_bf16 v[86:89], a[76:79], v[130:133], v[86:89]
v_mfma_f32_16x16x32_bf16 v[86:89], a[92:95], v[134:137], v[86:89]
s_waitcnt lgkmcnt(5)
v_mfma_f32_16x16x32_bf16 v[74:77], a[96:99], v[90:93], v[74:77]
s_waitcnt lgkmcnt(4)
v_mfma_f32_16x16x32_bf16 v[74:77], a[112:115], v[94:97], v[74:77]
s_waitcnt lgkmcnt(3)
v_mfma_f32_16x16x32_bf16 v[74:77], a[128:131], v[98:101], v[74:77]
s_waitcnt lgkmcnt(2)
v_mfma_f32_16x16x32_bf16 v[74:77], a[144:147], v[102:105], v[74:77]
s_waitcnt lgkmcnt(1)
v_mfma_f32_16x16x32_bf16 v[74:77], a[160:163], v[106:109], v[74:77]
s_waitcnt lgkmcnt(0)
v_mfma_f32_16x16x32_bf16 v[74:77], a[176:179], v[110:113], v[74:77]
ds_read_b128 v[114:117], v8 offset:30720
v_mfma_f32_16x16x32_bf16 v[82:85], a[104:107], v[90:93], v[82:85]
v_mfma_f32_16x16x32_bf16 v[82:85], a[120:123], v[94:97], v[82:85]
v_mfma_f32_16x16x32_bf16 v[82:85], a[136:139], v[98:101], v[82:85]
ds_read_b128 v[118:121], v8 offset:31744
v_mfma_f32_16x16x32_bf16 v[82:85], a[152:155], v[102:105], v[82:85]
v_mfma_f32_16x16x32_bf16 v[82:85], a[168:171], v[106:109], v[82:85]
v_mfma_f32_16x16x32_bf16 v[82:85], a[184:187], v[110:113], v[82:85]
ds_read_b128 v[122:125], v8 offset:32768
v_mfma_f32_16x16x32_bf16 v[78:81], a[100:103], v[90:93], v[78:81]
v_mfma_f32_16x16x32_bf16 v[78:81], a[116:119], v[94:97], v[78:81]
v_mfma_f32_16x16x32_bf16 v[78:81], a[132:135], v[98:101], v[78:81]
ds_read_b128 v[126:129], v8 offset:33792
v_mfma_f32_16x16x32_bf16 v[78:81], a[148:151], v[102:105], v[78:81]
v_mfma_f32_16x16x32_bf16 v[78:81], a[164:167], v[106:109], v[78:81]
v_mfma_f32_16x16x32_bf16 v[78:81], a[180:183], v[110:113], v[78:81]
ds_read_b128 v[130:133], v8 offset:34816
v_mfma_f32_16x16x32_bf16 v[86:89], a[108:111], v[90:93], v[86:89]
v_mfma_f32_16x16x32_bf16 v[86:89], a[124:127], v[94:97], v[86:89]
s_waitcnt vmcnt(1)
v_sub_u32 v254, v254, v5
v_cmp_lt_u32 vcc, 0x7f, v254
v_mfma_f32_16x16x32_bf16 v[86:89], a[140:143], v[98:101], v[86:89]
ds_read_b128 v[134:137], v8 offset:35840
v_mfma_f32_16x16x32_bf16 v[86:89], a[156:159], v[102:105], v[86:89]
v_mfma_f32_16x16x32_bf16 v[86:89], a[172:175], v[106:109], v[86:89]
s_cbranch_vccnz .Lgu_sp9
.Lgu_rt10:
s_add_u32 m0, s43, 18432
v_mfma_f32_16x16x32_bf16 v[86:89], a[188:191], v[110:113], v[86:89]
s_waitcnt lgkmcnt(5)
v_mfma_f32_16x16x32_bf16 v[74:77], a[192:195], v[114:117], v[74:77]
s_barrier
s_waitcnt lgkmcnt(4)
v_mfma_f32_16x16x32_bf16 v[74:77], a[208:211], v[118:121], v[74:77]
s_waitcnt lgkmcnt(3)
v_add_u32 v5, 1, v5
v_mfma_f32_16x16x32_bf16 v[74:77], a[224:227], v[122:125], v[74:77]
s_waitcnt lgkmcnt(2)
v_mfma_f32_16x16x32_bf16 v[74:77], a[240:243], v[126:129], v[74:77]
s_waitcnt lgkmcnt(1)
global_load_lds_dwordx4 v2, s[38:39] offset:-2048 sc1
v_mfma_f32_16x16x32_bf16 v[74:77], v[26:29], v[130:133], v[74:77]
s_waitcnt lgkmcnt(0)
v_mfma_f32_16x16x32_bf16 v[74:77], v[42:45], v[134:137], v[74:77]
v_mfma_f32_16x16x32_bf16 v[82:85], a[200:203], v[114:117], v[82:85]
global_load_lds_dwordx4 v2, s[38:39] offset:-1024 sc1
v_mfma_f32_16x16x32_bf16 v[82:85], a[216:219], v[118:121], v[82:85]
v_mfma_f32_16x16x32_bf16 v[82:85], a[232:235], v[122:125], v[82:85]
v_mfma_f32_16x16x32_bf16 v[82:85], a[248:251], v[126:129], v[82:85]
global_load_lds_dwordx4 v2, s[38:39] offset:0 sc1
v_mfma_f32_16x16x32_bf16 v[82:85], v[34:37], v[130:133], v[82:85]
v_xor_b32 v8, 0x10000, v8
v_mfma_f32_16x16x32_bf16 v[82:85], v[50:53], v[134:137], v[82:85]
v_mfma_f32_16x16x32_bf16 v[78:81], a[196:199], v[114:117], v[78:81]
s_barrier
v_mfma_f32_16x16x32_bf16 v[78:81], a[212:215], v[118:121], v[78:81]
global_load_lds_dwordx4 v2, s[38:39] offset:1024 sc1
v_mfma_f32_16x16x32_bf16 v[78:81], a[228:231], v[122:125], v[78:81]
v_mfma_f32_16x16x32_bf16 v[78:81], a[244:247], v[126:129], v[78:81]
ds_read_b128 v[90:93], v8 offset:0
v_mfma_f32_16x16x32_bf16 v[78:81], v[30:33], v[130:133], v[78:81]
ds_read_b128 v[94:97], v8 offset:1024
global_load_lds_dwordx4 v2, s[38:39] offset:2048 sc1
v_mfma_f32_16x16x32_bf16 v[78:81], v[46:49], v[134:137], v[78:81]
ds_read_b128 v[98:101], v8 offset:2048
v_mul_f32 v138, v74, v82
v_mfma_f32_16x16x32_bf16 v[86:89], a[204:207], v[114:117], v[86:89]
ds_read_b128 v[102:105], v8 offset:3072
v_mul_f32 v139, v75, v83
v_mfma_f32_16x16x32_bf16 v[86:89], a[220:223], v[118:121], v[86:89]
ds_read_b128 v[106:109], v8 offset:4096
global_load_lds_dwordx4 v2, s[38:39] offset:3072 sc1
v_mfma_f32_16x16x32_bf16 v[86:89], a[236:239], v[122:125], v[86:89]
ds_read_b128 v[110:113], v8 offset:5120
v_add_u32 v2, 18688, v2
v_mfma_f32_16x16x32_bf16 v[86:89], a[252:255], v[126:129], v[86:89]
v_min_u32 v2, v2, v9
v_mul_f32 v140, v76, v84
v_mfma_f32_16x16x32_bf16 v[86:89], v[38:41], v[130:133], v[86:89]
v_mfma_f32_16x16x32_bf16 v[86:89], v[54:57], v[134:137], v[86:89]
s_sub_u32 s70, s70, 1
s_cbranch_scc0 .Lgu_top11
v_mul_f32 v138, v74, v82
v_mul_f32 v139, v75, v83
v_mul_f32 v140, v76, v84
v_mul_f32 v141, v77, v85
v_cvt_pk_bf16_f32 v146, v138, v139
v_cvt_pk_bf16_f32 v147, v140, v141
v_mul_f32 v142, v78, v86
v_mul_f32 v143, v79, v87
v_mul_f32 v144, v80, v88
v_mul_f32 v145, v81, v89
v_cvt_pk_bf16_f32 v148, v142, v143
v_cvt_pk_bf16_f32 v149, v144, v145
global_store_dwordx4 v3, v[146:149], s[38:39] sc1
s_waitcnt vmcnt(1) lgkmcnt(0)
s_barrier
v_mov_b32 v218, 192
ds_read_b32 v218, v218 offset:36864
s_waitcnt vmcnt(0)
s_barrier
s_waitcnt lgkmcnt(0)
v_readfirstlane_b32 s60, v218
s_nop 3
s_max_i32 s60, s60, 2
s_min_i32 s60, s60, 66
s_add_u32 s60, s60, 0
s_branch .Lgu_fin
.Lgu_w4:
s_add_u32 s84, s52, 18688
s_addc_u32 s85, s53, 0
s_add_u32 s86, s50, 110592
s_addc_u32 s87, s51, 0
s_mov_b32 s80, 0
s_mov_b64 s[76:77], 0
s_mov_b32 s72, -14
v_add_u32 v217, -1, v17
v_add_u32 v157, -1, s37
v_add_u32 v214, -1, s37
v_add_u32 v215, -1, s37
v_add_u32 v216, -1, s37
v_add_u32 v254, -1, v5
s_mov_b32 s70, 32
s_waitcnt vmcnt(0) lgkmcnt(0)
s_nop 7
.p2align 6
.Lgu_top16:
s_waitcnt lgkmcnt(5)
v_mfma_f32_16x16x32_bf16 v[58:61], a[0:3], v[90:93], 0
s_waitcnt lgkmcnt(4)
v_lshlrev_b32 v204, 16, v175
v_and_b32 v205, 0xffff0000, v175
v_mfma_f32_16x16x32_bf16 v[58:61], a[16:19], v[94:97], v[58:61]
s_waitcnt lgkmcnt(3)
v_add_f32_dpp v194, v198, v194 row_ror:8 row_mask:0xf bank_mask:0xf
v_add_f32_dpp v195, v199, v195 row_ror:8 row_mask:0xf bank_mask:0xf
v_mfma_f32_16x16x32_bf16 v[58:61], a[32:35], v[98:101], v[58:61]
s_waitcnt lgkmcnt(2)
v_add_f32_dpp v196, v200, v196 row_ror:8 row_mask:0xf bank_mask:0xf
v_add_f32_dpp v197, v201, v197 row_ror:8 row_mask:0xf bank_mask:0xf
v_mfma_f32_16x16x32_bf16 v[58:61], a[48:51], v[102:105], v[58:61]
s_waitcnt lgkmcnt(1)
v_mul_f32 v142, v78, v86
v_mul_f32 v143, v79, v87
v_mfma_f32_16x16x32_bf16 v[58:61], a[64:67], v[106:109], v[58:61]
s_waitcnt lgkmcnt(0)
v_mul_f32 v144, v80, v88
v_mul_f32 v145, v81, v89
v_mfma_f32_16x16x32_bf16 v[58:61], a[80:83], v[110:113], v[58:61]
ds_read_b128 v[114:117], v8 offset:6144
v_cvt_pk_bf16_f32 v148, v142, v143
v_mfma_f32_16x16x32_bf16 v[66:69], a[8:11], v[90:93], 0
ds_read_b128 v[118:121], v8 offset:7168
v_cvt_pk_bf16_f32 v149, v144, v145
v_mfma_f32_16x16x32_bf16 v[66:69], a[24:27], v[94:97], v[66:69]
v_mfma_f32_16x16x32_bf16 v[66:69], a[40:43], v[98:101], v[66:69]
s_waitcnt vmcnt(8)
s_barrier
v_mfma_f32_16x16x32_bf16 v[66:69], a[56:59], v[102:105], v[66:69]
v_fma_f32 v194, v194, v218, v202
v_fma_f32 v195, v195, v218, v203
v_mfma_f32_16x16x32_bf16 v[66:69], a[72:75], v[106:109], v[66:69]
v_fma_f32 v196, v196, v218, v204
v_fma_f32 v197, v197, v218, v205
v_mfma_f32_16x16x32_bf16 v[66:69], a[88:91], v[110:113], v[66:69]
ds_read_b128 v[122:125], v8 offset:8192
v_cvt_pk_bf16_f32 v210, v194, v195
v_mfma_f32_16x16x32_bf16 v[62:65], a[4:7], v[90:93], 0
s_waitcnt vmcnt(7)
v_cvt_pk_bf16_f32 v211, v196, v197
s_mov_b64 exec, 1
global_store_dword v18, v216, s[38:39] sc1
s_mov_b64 exec, -1
v_mfma_f32_16x16x32_bf16 v[62:65], a[20:23], v[94:97], v[62:65]
global_store_dwordx4 v3, v[146:149], s[38:39] sc1
v_mfma_f32_16x16x32_bf16 v[62:65], a[36:39], v[98:101], v[62:65]
ds_read_b128 v[126:129], v8 offset:9216
v_add_u32 v3, 147456, v3
v_mfma_f32_16x16x32_bf16 v[62:65], a[52:55], v[102:105], v[62:65]
ds_write_b64 v14, v[210:211]
v_mfma_f32_16x16x32_bf16 v[62:65], a[68:71], v[106:109], v[62:65]
ds_read_b64 v[212:213], v15
v_mfma_f32_16x16x32_bf16 v[62:65], a[84:87], v[110:113], v[62:65]
ds_read_b128 v[130:133], v8 offset:10240
v_mfma_f32_16x16x32_bf16 v[70:73], a[12:15], v[90:93], 0
v_mfma_f32_16x16x32_bf16 v[70:73], a[28:31], v[94:97], v[70:73]
v_mfma_f32_16x16x32_bf16 v[70:73], a[44:47], v[98:101], v[70:73]
ds_read_b128 v[134:137], v8 offset:11264
v_mfma_f32_16x16x32_bf16 v[70:73], a[60:63], v[102:105], v[70:73]
v_mfma_f32_16x16x32_bf16 v[70:73], a[76:79], v[106:109], v[70:73]
v_mfma_f32_16x16x32_bf16 v[70:73], a[92:95], v[110:113], v[70:73]
s_waitcnt lgkmcnt(2)
global_store_dwordx2 v13, v[212:213], s[56:57] sc1
v_mov_b32 v215, v157
v_mfma_f32_16x16x32_bf16 v[58:61], a[96:99], v[114:117], v[58:61]
v_mfma_f32_16x16x32_bf16 v[58:61], a[112:115], v[118:121], v[58:61]
v_mfma_f32_16x16x32_bf16 v[58:61], a[128:131], v[122:125], v[58:61]
v_mfma_f32_16x16x32_bf16 v[58:61], a[144:147], v[126:129], v[58:61]
s_waitcnt vmcnt(8)
v_add_f32 v188, v188, v190
s_waitcnt vmcnt(7)
v_lshlrev_b32 v194, 16, v176
v_add_f32 v189, v189, v191
s_waitcnt lgkmcnt(1)
v_mfma_f32_16x16x32_bf16 v[58:61], a[160:163], v[130:133], v[58:61]
s_waitcnt lgkmcnt(0)
v_and_b32 v195, 0xffff0000, v176
v_mul_f32 v188, 0x3ae38e39, v188
v_lshlrev_b32 v196, 16, v177
v_mfma_f32_16x16x32_bf16 v[58:61], a[176:179], v[134:137], v[58:61]
ds_read_b128 v[90:93], v8 offset:12288
v_mul_f32 v189, 0x3ae38e39, v189
v_and_b32 v197, 0xffff0000, v177
v_mfma_f32_16x16x32_bf16 v[66:69], a[104:107], v[114:117], v[66:69]
v_fma_f32 v219, -v188, v188, v189
v_lshlrev_b32 v198, 16, v178
v_max_f32 v219, 0, v219
v_mfma_f32_16x16x32_bf16 v[66:69], a[120:123], v[118:121], v[66:69]
v_and_b32 v199, 0xffff0000, v178
v_add_f32 v219, 0x3727c5ac, v219
v_mfma_f32_16x16x32_bf16 v[66:69], a[136:139], v[122:125], v[66:69]
ds_read_b128 v[94:97], v8 offset:13312
v_lshlrev_b32 v200, 16, v179
v_mfma_f32_16x16x32_bf16 v[66:69], a[152:155], v[126:129], v[66:69]
s_waitcnt vmcnt(3)
v_sub_u32 v217, v217, v17
v_cmp_lt_u32 vcc, 0x7f, v217
v_mfma_f32_16x16x32_bf16 v[66:69], a[168:171], v[130:133], v[66:69]
v_rcp_f32 v218, v219
v_mfma_f32_16x16x32_bf16 v[66:69], a[184:187], v[134:137], v[66:69]
ds_read_b128 v[98:101], v8 offset:14336
v_and_b32 v201, 0xffff0000, v179
v_mfma_f32_16x16x32_bf16 v[62:65], a[100:103], v[114:117], v[62:65]
s_and_b64 s[88:89], vcc, s[76:77]
s_cbranch_scc1 .Lgu_sp12
.Lgu_rt13:
v_mfma_f32_16x16x32_bf16 v[62:65], a[116:119], v[118:121], v[62:65]
s_cmp_eq_u64 vcc, 0
v_lshlrev_b32 v202, 16, v180
v_and_b32 v203, 0xffff0000, v180
v_mfma_f32_16x16x32_bf16 v[62:65], a[132:135], v[122:125], v[62:65]
s_cselect_b64 s[52:53], s[84:85], s[52:53]
ds_read_b128 v[102:105], v8 offset:15360
v_lshlrev_b32 v204, 16, v181
v_mfma_f32_16x16x32_bf16 v[62:65], a[148:151], v[126:129], v[62:65]
s_cselect_b64 s[50:51], s[86:87], s[50:51]
global_load_dwordx2 v[170:171], v12, s[52:53] offset:0 sc1
v_and_b32 v205, 0xffff0000, v181
v_mfma_f32_16x16x32_bf16 v[62:65], a[164:167], v[130:133], v[62:65]
s_cselect_b64 s[56:57], s[54:55], s[48:49]
global_load_dwordx2 v[172:173], v12, s[52:53] offset:128 sc1
v_lshlrev_b32 v206, 16, v182
v_mfma_f32_16x16x32_bf16 v[62:65], a[180:183], v[134:137], v[62:65]
s_cselect_b32 s82, 1, 0
ds_read_b128 v[106:109], v8 offset:16384
global_load_dwordx4 v[158:161], v10, s[50:51] offset:0 sc1
v_mfma_f32_16x16x32_bf16 v[70:73], a[108:111], v[114:117], v[70:73]
s_add_u32 s60, s60, s82
global_load_dwordx4 v[162:165], v10, s[50:51] offset:1024 sc1
v_and_b32 v207, 0xffff0000, v182
v_mfma_f32_16x16x32_bf16 v[70:73], a[124:127], v[118:121], v[70:73]
s_add_u32 s83, s60, s37
global_load_dwordx4 v[166:169], v10, s[50:51] offset:2048 sc1
v_add_u32 v17, s82, v17
v_add_u32 v157, -1, s83
v_mfma_f32_16x16x32_bf16 v[70:73], a[140:143], v[122:125], v[70:73]
s_cmp_lt_i32 s60, 32
ds_read_b128 v[110:113], v8 offset:17408
global_load_dwordx2 v[174:175], v11, s[52:53] sc1
v_mfma_f32_16x16x32_bf16 v[70:73], a[156:159], v[126:129], v[70:73]
s_cselect_b64 s[54:55], s[44:45], s[46:47]
v_lshlrev_b32 v208, 16, v183
v_and_b32 v209, 0xffff0000, v183
v_mfma_f32_16x16x32_bf16 v[70:73], a[172:175], v[130:133], v[70:73]
s_mul_i32 s80, s60, 18432
v_add_f32 v194, v194, v202
v_add_f32 v195, v195, v203
v_mfma_f32_16x16x32_bf16 v[70:73], a[188:191], v[134:137], v[70:73]
s_waitcnt lgkmcnt(5)
v_mfma_f32_16x16x32_bf16 v[58:61], a[192:195], v[90:93], v[58:61]
s_barrier
s_waitcnt lgkmcnt(4)
v_mfma_f32_16x16x32_bf16 v[58:61], a[208:211], v[94:97], v[58:61]
s_waitcnt lgkmcnt(3)
v_add_f32 v196, v196, v204
v_add_f32 v197, v197, v205
v_mfma_f32_16x16x32_bf16 v[58:61], a[224:227], v[98:101], v[58:61]
s_waitcnt lgkmcnt(2)
v_add_f32 v198, v198, v206
v_add_f32 v199, v199, v207
v_mfma_f32_16x16x32_bf16 v[58:61], a[240:243], v[102:105], v[58:61]
s_waitcnt lgkmcnt(1)
v_add_f32 v200, v200, v208
v_add_f32 v201, v201, v209
v_mfma_f32_16x16x32_bf16 v[58:61], v[26:29], v[106:109], v[58:61]
s_waitcnt lgkmcnt(0)
v_lshlrev_b32 v202, 16, v184
v_and_b32 v203, 0xffff0000, v184
v_mfma_f32_16x16x32_bf16 v[58:61], v[42:45], v[110:113], v[58:61]
s_sub_u32 s54, s54, s80
v_lshlrev_b32 v204, 16, v185
v_and_b32 v205, 0xffff0000, v185
v_mfma_f32_16x16x32_bf16 v[66:69], a[200:203], v[90:93], v[66:69]
s_subb_u32 s55, s55, 0
v_lshlrev_b32 v206, 16, v186
v_and_b32 v207, 0xffff0000, v186
v_mfma_f32_16x16x32_bf16 v[66:69], a[216:219], v[94:97], v[66:69]
s_add_u32 s84, s52, 18688
v_lshlrev_b32 v208, 16, v187
v_and_b32 v209, 0xffff0000, v187
v_mfma_f32_16x16x32_bf16 v[66:69], a[232:235], v[98:101], v[66:69]
s_addc_u32 s85, s53, 0
v_add_f32 v194, v194, v202
v_add_f32 v195, v195, v203
v_mfma_f32_16x16x32_bf16 v[66:69], a[248:251], v[102:105], v[66:69]
s_add_u32 s86, s50, 110592
v_add_f32 v196, v196, v204
v_add_f32 v197, v197, v205
v_mfma_f32_16x16x32_bf16 v[66:69], v[34:37], v[106:109], v[66:69]
s_addc_u32 s87, s51, 0
v_add_f32 v198, v198, v206
v_add_f32 v199, v199, v207
v_mfma_f32_16x16x32_bf16 v[66:69], v[50:53], v[110:113], v[66:69]
v_mfma_f32_16x16x32_bf16 v[62:65], a[196:199], v[90:93], v[62:65]
s_barrier
v_mfma_f32_16x16x32_bf16 v[62:65], a[212:215], v[94:97], v[62:65]
s_add_u32 s72, s72, 1
v_add_f32 v200, v200, v208
v_add_f32 v201, v201, v209
v_mfma_f32_16x16x32_bf16 v[62:65], a[228:231], v[98:101], v[62:65]
s_cmp_le_i32 s60, s72
v_lshlrev_b32 v202, 16, v192
v_and_b32 v203, 0xffff0000, v192
v_mfma_f32_16x16x32_bf16 v[62:65], a[244:247], v[102:105], v[62:65]
s_cselect_b64 s[76:77], -1, 0
ds_read_b128 v[114:117], v8 offset:18432
v_lshlrev_b32 v204, 16, v193
v_mfma_f32_16x16x32_bf16 v[62:65], v[30:33], v[106:109], v[62:65]
ds_read_b128 v[118:121], v8 offset:19456
v_mul_f32 v138, v58, v66
v_mfma_f32_16x16x32_bf16 v[62:65], v[46:49], v[110:113], v[62:65]
ds_read_b128 v[122:125], v8 offset:20480
v_mul_f32 v139, v59, v67
v_mfma_f32_16x16x32_bf16 v[70:73], a[204:207], v[90:93], v[70:73]
ds_read_b128 v[126:129], v8 offset:21504
v_mul_f32 v140, v60, v68
v_mfma_f32_16x16x32_bf16 v[70:73], a[220:223], v[94:97], v[70:73]
ds_read_b128 v[130:133], v8 offset:22528
v_mul_f32 v141, v61, v69
v_mfma_f32_16x16x32_bf16 v[70:73], a[236:239], v[98:101], v[70:73]
ds_read_b128 v[134:137], v8 offset:23552
v_cvt_pk_bf16_f32 v146, v138, v139
v_mfma_f32_16x16x32_bf16 v[70:73], a[252:255], v[102:105], v[70:73]
v_cvt_pk_bf16_f32 v147, v140, v141
global_load_dword v217, v16, s[38:39] sc1
v_mfma_f32_16x16x32_bf16 v[70:73], v[38:41], v[106:109], v[70:73]
v_and_b32 v205, 0xffff0000, v193
v_add_f32_dpp v194, v198, v194 row_ror:8 row_mask:0xf bank_mask:0xf
v_mfma_f32_16x16x32_bf16 v[70:73], v[54:57], v[110:113], v[70:73]
s_waitcnt lgkmcnt(5)
v_add_f32_dpp v195, v199, v195 row_ror:8 row_mask:0xf bank_mask:0xf
v_add_f32_dpp v196, v200, v196 row_ror:8 row_mask:0xf bank_mask:0xf
v_mfma_f32_16x16x32_bf16 v[74:77], a[0:3], v[114:117], 0
)"
R"(s_waitcnt lgkmcnt(4)
v_add_f32_dpp v197, v201, v197 row_ror:8 row_mask:0xf bank_mask:0xf
v_fma_f32 v194, v194, v218, v202
v_mfma_f32_16x16x32_bf16 v[74:77], a[16:19], v[118:121], v[74:77]
s_waitcnt lgkmcnt(3)
v_fma_f32 v195, v195, v218, v203
v_fma_f32 v196, v196, v218, v204
v_mfma_f32_16x16x32_bf16 v[74:77], a[32:35], v[122:125], v[74:77]
s_waitcnt lgkmcnt(2)
v_fma_f32 v197, v197, v218, v205
v_cvt_pk_bf16_f32 v210, v194, v195
v_mfma_f32_16x16x32_bf16 v[74:77], a[48:51], v[126:129], v[74:77]
s_waitcnt lgkmcnt(1)
v_mul_f32 v142, v62, v70
v_mul_f32 v143, v63, v71
v_mfma_f32_16x16x32_bf16 v[74:77], a[64:67], v[130:133], v[74:77]
s_waitcnt lgkmcnt(0)
v_mul_f32 v144, v64, v72
v_mul_f32 v145, v65, v73
v_mfma_f32_16x16x32_bf16 v[74:77], a[80:83], v[134:137], v[74:77]
ds_read_b128 v[90:93], v8 offset:24576
v_cvt_pk_bf16_f32 v148, v142, v143
v_mfma_f32_16x16x32_bf16 v[82:85], a[8:11], v[114:117], 0
ds_read_b128 v[94:97], v8 offset:25600
v_cvt_pk_bf16_f32 v149, v144, v145
v_mfma_f32_16x16x32_bf16 v[82:85], a[24:27], v[118:121], v[82:85]
v_mfma_f32_16x16x32_bf16 v[82:85], a[40:43], v[122:125], v[82:85]
s_waitcnt vmcnt(8)
s_barrier
v_mfma_f32_16x16x32_bf16 v[82:85], a[56:59], v[126:129], v[82:85]
s_waitcnt vmcnt(7)
s_mov_b64 exec, 1
global_store_dword v18, v215, s[38:39] sc1
s_mov_b64 exec, -1
v_mfma_f32_16x16x32_bf16 v[82:85], a[72:75], v[130:133], v[82:85]
v_cvt_pk_bf16_f32 v211, v196, v197
v_mfma_f32_16x16x32_bf16 v[82:85], a[88:91], v[134:137], v[82:85]
ds_read_b128 v[98:101], v8 offset:26624
v_mfma_f32_16x16x32_bf16 v[78:81], a[4:7], v[114:117], 0
ds_write_b64 v14, v[210:211]
v_mfma_f32_16x16x32_bf16 v[78:81], a[20:23], v[118:121], v[78:81]
global_store_dwordx4 v3, v[146:149], s[38:39] sc1
v_mfma_f32_16x16x32_bf16 v[78:81], a[36:39], v[122:125], v[78:81]
ds_read_b128 v[102:105], v8 offset:27648
v_add_u32 v3, 147456, v3
v_mfma_f32_16x16x32_bf16 v[78:81], a[52:55], v[126:129], v[78:81]
ds_read_b64 v[212:213], v15
v_mfma_f32_16x16x32_bf16 v[78:81], a[68:71], v[130:133], v[78:81]
v_mfma_f32_16x16x32_bf16 v[78:81], a[84:87], v[134:137], v[78:81]
ds_read_b128 v[106:109], v8 offset:28672
v_mfma_f32_16x16x32_bf16 v[86:89], a[12:15], v[114:117], 0
v_mfma_f32_16x16x32_bf16 v[86:89], a[28:31], v[118:121], v[86:89]
v_mfma_f32_16x16x32_bf16 v[86:89], a[44:47], v[122:125], v[86:89]
ds_read_b128 v[110:113], v8 offset:29696
v_mfma_f32_16x16x32_bf16 v[86:89], a[60:63], v[126:129], v[86:89]
v_mfma_f32_16x16x32_bf16 v[86:89], a[76:79], v[130:133], v[86:89]
s_waitcnt lgkmcnt(2)
global_store_dwordx2 v13, v[212:213], s[58:59] sc1
v_mov_b32 v216, v214
v_mfma_f32_16x16x32_bf16 v[86:89], a[92:95], v[134:137], v[86:89]
v_mfma_f32_16x16x32_bf16 v[74:77], a[96:99], v[90:93], v[74:77]
v_mfma_f32_16x16x32_bf16 v[74:77], a[112:115], v[94:97], v[74:77]
v_mfma_f32_16x16x32_bf16 v[74:77], a[128:131], v[98:101], v[74:77]
v_mfma_f32_16x16x32_bf16 v[74:77], a[144:147], v[102:105], v[74:77]
s_waitcnt vmcnt(8)
v_add_f32 v170, v170, v172
s_waitcnt vmcnt(7)
v_lshlrev_b32 v194, 16, v158
v_add_f32 v171, v171, v173
s_waitcnt lgkmcnt(1)
v_mfma_f32_16x16x32_bf16 v[74:77], a[160:163], v[106:109], v[74:77]
s_waitcnt lgkmcnt(0)
v_and_b32 v195, 0xffff0000, v158
v_mul_f32 v170, 0x3ae38e39, v170
v_lshlrev_b32 v196, 16, v159
v_mfma_f32_16x16x32_bf16 v[74:77], a[176:179], v[110:113], v[74:77]
ds_read_b128 v[114:117], v8 offset:30720
v_mul_f32 v171, 0x3ae38e39, v171
v_and_b32 v197, 0xffff0000, v159
v_mfma_f32_16x16x32_bf16 v[82:85], a[104:107], v[90:93], v[82:85]
v_fma_f32 v219, -v170, v170, v171
v_lshlrev_b32 v198, 16, v160
v_max_f32 v219, 0, v219
v_mfma_f32_16x16x32_bf16 v[82:85], a[120:123], v[94:97], v[82:85]
v_and_b32 v199, 0xffff0000, v160
v_add_f32 v219, 0x3727c5ac, v219
v_mfma_f32_16x16x32_bf16 v[82:85], a[136:139], v[98:101], v[82:85]
ds_read_b128 v[118:121], v8 offset:31744
v_lshlrev_b32 v200, 16, v161
v_mfma_f32_16x16x32_bf16 v[82:85], a[152:155], v[102:105], v[82:85]
s_waitcnt vmcnt(3)
v_sub_u32 v217, v217, v17
v_cmp_lt_u32 vcc, 0x7f, v217
v_mfma_f32_16x16x32_bf16 v[82:85], a[168:171], v[106:109], v[82:85]
v_rcp_f32 v218, v219
v_mfma_f32_16x16x32_bf16 v[82:85], a[184:187], v[110:113], v[82:85]
ds_read_b128 v[122:125], v8 offset:32768
v_and_b32 v201, 0xffff0000, v161
v_mfma_f32_16x16x32_bf16 v[78:81], a[100:103], v[90:93], v[78:81]
s_and_b64 s[88:89], vcc, s[76:77]
s_cbranch_scc1 .Lgu_sp14
.Lgu_rt15:
v_mfma_f32_16x16x32_bf16 v[78:81], a[116:119], v[94:97], v[78:81]
s_cmp_eq_u64 vcc, 0
v_lshlrev_b32 v202, 16, v162
v_and_b32 v203, 0xffff0000, v162
v_mfma_f32_16x16x32_bf16 v[78:81], a[132:135], v[98:101], v[78:81]
s_cselect_b64 s[52:53], s[84:85], s[52:53]
ds_read_b128 v[126:129], v8 offset:33792
v_lshlrev_b32 v204, 16, v163
v_mfma_f32_16x16x32_bf16 v[78:81], a[148:151], v[102:105], v[78:81]
s_cselect_b64 s[50:51], s[86:87], s[50:51]
global_load_dwordx2 v[188:189], v12, s[52:53] offset:0 sc1
v_and_b32 v205, 0xffff0000, v163
v_mfma_f32_16x16x32_bf16 v[78:81], a[164:167], v[106:109], v[78:81]
s_cselect_b64 s[58:59], s[54:55], s[48:49]
global_load_dwordx2 v[190:191], v12, s[52:53] offset:128 sc1
v_lshlrev_b32 v206, 16, v164
v_mfma_f32_16x16x32_bf16 v[78:81], a[180:183], v[110:113], v[78:81]
s_cselect_b32 s82, 1, 0
ds_read_b128 v[130:133], v8 offset:34816
global_load_dwordx4 v[176:179], v10, s[50:51] offset:0 sc1
v_mfma_f32_16x16x32_bf16 v[86:89], a[108:111], v[90:93], v[86:89]
s_add_u32 s60, s60, s82
global_load_dwordx4 v[180:183], v10, s[50:51] offset:1024 sc1
v_and_b32 v207, 0xffff0000, v164
v_mfma_f32_16x16x32_bf16 v[86:89], a[124:127], v[94:97], v[86:89]
s_add_u32 s83, s60, s37
global_load_dwordx4 v[184:187], v10, s[50:51] offset:2048 sc1
v_add_u32 v17, s82, v17
v_add_u32 v214, -1, s83
v_mfma_f32_16x16x32_bf16 v[86:89], a[140:143], v[98:101], v[86:89]
s_cmp_lt_i32 s60, 32
ds_read_b128 v[134:137], v8 offset:35840
global_load_dwordx2 v[192:193], v11, s[52:53] sc1
v_mfma_f32_16x16x32_bf16 v[86:89], a[156:159], v[102:105], v[86:89]
s_cselect_b64 s[54:55], s[44:45], s[46:47]
v_lshlrev_b32 v208, 16, v165
v_and_b32 v209, 0xffff0000, v165
v_mfma_f32_16x16x32_bf16 v[86:89], a[172:175], v[106:109], v[86:89]
s_mul_i32 s80, s60, 18432
v_add_f32 v194, v194, v202
v_add_f32 v195, v195, v203
v_mfma_f32_16x16x32_bf16 v[86:89], a[188:191], v[110:113], v[86:89]
s_waitcnt lgkmcnt(5)
v_mfma_f32_16x16x32_bf16 v[74:77], a[192:195], v[114:117], v[74:77]
s_barrier
s_waitcnt lgkmcnt(4)
v_mfma_f32_16x16x32_bf16 v[74:77], a[208:211], v[118:121], v[74:77]
s_waitcnt lgkmcnt(3)
v_add_f32 v196, v196, v204
v_add_f32 v197, v197, v205
v_mfma_f32_16x16x32_bf16 v[74:77], a[224:227], v[122:125], v[74:77]
s_waitcnt lgkmcnt(2)
v_add_f32 v198, v198, v206
v_add_f32 v199, v199, v207
v_mfma_f32_16x16x32_bf16 v[74:77], a[240:243], v[126:129], v[74:77]
s_waitcnt lgkmcnt(1)
v_add_f32 v200, v200, v208
v_add_f32 v201, v201, v209
v_mfma_f32_16x16x32_bf16 v[74:77], v[26:29], v[130:133], v[74:77]
s_waitcnt lgkmcnt(0)
v_lshlrev_b32 v202, 16, v166
v_and_b32 v203, 0xffff0000, v166
v_mfma_f32_16x16x32_bf16 v[74:77], v[42:45], v[134:137], v[74:77]
s_sub_u32 s54, s54, s80
v_lshlrev_b32 v204, 16, v167
v_and_b32 v205, 0xffff0000, v167
v_mfma_f32_16x16x32_bf16 v[82:85], a[200:203], v[114:117], v[82:85]
s_subb_u32 s55, s55, 0
v_lshlrev_b32 v206, 16, v168
v_and_b32 v207, 0xffff0000, v168
v_mfma_f32_16x16x32_bf16 v[82:85], a[216:219], v[118:121], v[82:85]
s_add_u32 s84, s52, 18688
v_lshlrev_b32 v208, 16, v169
v_and_b32 v209, 0xffff0000, v169
v_mfma_f32_16x16x32_bf16 v[82:85], a[232:235], v[122:125], v[82:85]
s_addc_u32 s85, s53, 0
v_add_f32 v194, v194, v202
v_add_f32 v195, v195, v203
v_mfma_f32_16x16x32_bf16 v[82:85], a[248:251], v[126:129], v[82:85]
s_add_u32 s86, s50, 110592
v_add_f32 v196, v196, v204
v_add_f32 v197, v197, v205
v_mfma_f32_16x16x32_bf16 v[82:85], v[34:37], v[130:133], v[82:85]
s_addc_u32 s87, s51, 0
v_xor_b32 v8, 0x10000, v8
v_add_f32 v198, v198, v206
v_mfma_f32_16x16x32_bf16 v[82:85], v[50:53], v[134:137], v[82:85]
v_mfma_f32_16x16x32_bf16 v[78:81], a[196:199], v[114:117], v[78:81]
s_barrier
v_mfma_f32_16x16x32_bf16 v[78:81], a[212:215], v[118:121], v[78:81]
s_add_u32 s72, s72, 1
v_add_f32 v199, v199, v207
v_add_f32 v200, v200, v208
v_mfma_f32_16x16x32_bf16 v[78:81], a[228:231], v[122:125], v[78:81]
s_cmp_le_i32 s60, s72
v_add_f32 v201, v201, v209
v_lshlrev_b32 v202, 16, v174
v_mfma_f32_16x16x32_bf16 v[78:81], a[244:247], v[126:129], v[78:81]
s_cselect_b64 s[76:77], -1, 0
ds_read_b128 v[90:93], v8 offset:0
v_and_b32 v203, 0xffff0000, v174
v_mfma_f32_16x16x32_bf16 v[78:81], v[30:33], v[130:133], v[78:81]
ds_read_b128 v[94:97], v8 offset:1024
v_mul_f32 v138, v74, v82
v_mfma_f32_16x16x32_bf16 v[78:81], v[46:49], v[134:137], v[78:81]
ds_read_b128 v[98:101], v8 offset:2048
v_mul_f32 v139, v75, v83
v_mfma_f32_16x16x32_bf16 v[86:89], a[204:207], v[114:117], v[86:89]
ds_read_b128 v[102:105], v8 offset:3072
v_mul_f32 v140, v76, v84
v_mfma_f32_16x16x32_bf16 v[86:89], a[220:223], v[118:121], v[86:89]
ds_read_b128 v[106:109], v8 offset:4096
v_mul_f32 v141, v77, v85
v_mfma_f32_16x16x32_bf16 v[86:89], a[236:239], v[122:125], v[86:89]
ds_read_b128 v[110:113], v8 offset:5120
v_cvt_pk_bf16_f32 v146, v138, v139
v_mfma_f32_16x16x32_bf16 v[86:89], a[252:255], v[126:129], v[86:89]
v_cvt_pk_bf16_f32 v147, v140, v141
global_load_dword v217, v16, s[38:39] sc1
v_mfma_f32_16x16x32_bf16 v[86:89], v[38:41], v[130:133], v[86:89]
v_mfma_f32_16x16x32_bf16 v[86:89], v[54:57], v[134:137], v[86:89]
s_sub_u32 s70, s70, 1
s_cbranch_scc0 .Lgu_top16
v_mul_f32 v138, v74, v82
v_mul_f32 v139, v75, v83
v_mul_f32 v140, v76, v84
v_mul_f32 v141, v77, v85
v_cvt_pk_bf16_f32 v146, v138, v139
v_cvt_pk_bf16_f32 v147, v140, v141
v_mul_f32 v142, v78, v86
v_mul_f32 v143, v79, v87
v_mul_f32 v144, v80, v88
v_mul_f32 v145, v81, v89
v_cvt_pk_bf16_f32 v148, v142, v143
v_cvt_pk_bf16_f32 v149, v144, v145
global_store_dwordx4 v3, v[146:149], s[38:39] sc1
v_mov_b32 v219, s60
ds_write_b32 v1, v219 offset:36864
v_subrev_u32 v17, s60, v17
s_waitcnt vmcnt(1) lgkmcnt(0)
s_barrier
v_mov_b32 v218, 192
ds_read_b32 v218, v218 offset:36864
s_waitcnt vmcnt(0)
s_barrier
s_waitcnt lgkmcnt(0)
v_readfirstlane_b32 s60, v218
s_nop 3
s_max_i32 s60, s60, 2
s_min_i32 s60, s60, 66
s_add_u32 s60, s60, 1
s_branch .Lgu_fin
.Lgu_fin:
s_cmp_gt_i32 s60, 63
s_cbranch_scc1 .Lgu_end
v_add_u32 v220, s60, v17
global_load_dword v217, v16, s[38:39] sc1
s_cmp_lt_i32 s60, 32
s_cselect_b64 s[54:55], s[44:45], s[46:47]
s_mul_i32 s80, s60, 18432
s_sub_u32 s54, s54, s80
s_subb_u32 s55, s55, 0
s_mov_b64 s[56:57], s[54:55]
s_mul_i32 s80, s60, 110592
s_mul_i32 s81, s60, 18688
s_mov_b64 s[50:51], s[38:39]
s_mov_b64 s[52:53], s[38:39]
s_add_u32 s50, s50, s80
s_addc_u32 s51, s51, 0
s_add_u32 s52, s52, s81
s_addc_u32 s53, s53, 0
s_waitcnt vmcnt(0)
v_sub_u32 v217, v217, v220
v_cmp_lt_u32 vcc, 0x7f, v217
s_cbranch_vccnz .Lgu_sp17
.Lgu_rt18:
global_load_dwordx2 v[170:171], v12, s[52:53] offset:0 sc1
global_load_dwordx2 v[172:173], v12, s[52:53] offset:128 sc1
global_load_dwordx4 v[158:161], v10, s[50:51] offset:0 sc1
global_load_dwordx4 v[162:165], v10, s[50:51] offset:1024 sc1
global_load_dwordx4 v[166:169], v10, s[50:51] offset:2048 sc1
global_load_dwordx2 v[174:175], v11, s[52:53] sc1
s_waitcnt vmcnt(4)
v_add_f32 v170, v170, v172
s_waitcnt vmcnt(3)
v_lshlrev_b32 v194, 16, v158
v_add_f32 v171, v171, v173
v_and_b32 v195, 0xffff0000, v158
v_mul_f32 v170, 0x3ae38e39, v170
v_lshlrev_b32 v196, 16, v159
v_mul_f32 v171, 0x3ae38e39, v171
v_and_b32 v197, 0xffff0000, v159
v_fma_f32 v219, -v170, v170, v171
v_lshlrev_b32 v198, 16, v160
v_max_f32 v219, 0, v219
v_and_b32 v199, 0xffff0000, v160
v_add_f32 v219, 0x3727c5ac, v219
v_lshlrev_b32 v200, 16, v161
v_rcp_f32 v218, v219
v_and_b32 v201, 0xffff0000, v161
s_waitcnt vmcnt(2)
v_lshlrev_b32 v202, 16, v162
v_and_b32 v203, 0xffff0000, v162
v_lshlrev_b32 v204, 16, v163
v_and_b32 v205, 0xffff0000, v163
v_lshlrev_b32 v206, 16, v164
v_and_b32 v207, 0xffff0000, v164
v_lshlrev_b32 v208, 16, v165
v_and_b32 v209, 0xffff0000, v165
v_add_f32 v194, v194, v202
v_add_f32 v195, v195, v203
v_add_f32 v196, v196, v204
v_add_f32 v197, v197, v205
v_add_f32 v198, v198, v206
v_add_f32 v199, v199, v207
v_add_f32 v200, v200, v208
v_add_f32 v201, v201, v209
s_waitcnt vmcnt(1)
v_lshlrev_b32 v202, 16, v166
v_and_b32 v203, 0xffff0000, v166
v_lshlrev_b32 v204, 16, v167
v_and_b32 v205, 0xffff0000, v167
v_lshlrev_b32 v206, 16, v168
v_and_b32 v207, 0xffff0000, v168
v_lshlrev_b32 v208, 16, v169
v_and_b32 v209, 0xffff0000, v169
v_add_f32 v194, v194, v202
v_add_f32 v195, v195, v203
v_add_f32 v196, v196, v204
v_add_f32 v197, v197, v205
v_add_f32 v198, v198, v206
v_add_f32 v199, v199, v207
v_add_f32 v200, v200, v208
v_add_f32 v201, v201, v209
s_waitcnt vmcnt(0)
v_lshlrev_b32 v202, 16, v174
v_and_b32 v203, 0xffff0000, v174
v_lshlrev_b32 v204, 16, v175
v_and_b32 v205, 0xffff0000, v175
v_add_f32_dpp v194, v198, v194 row_ror:8 row_mask:0xf bank_mask:0xf
v_add_f32_dpp v195, v199, v195 row_ror:8 row_mask:0xf bank_mask:0xf
v_add_f32_dpp v196, v200, v196 row_ror:8 row_mask:0xf bank_mask:0xf
v_add_f32_dpp v197, v201, v197 row_ror:8 row_mask:0xf bank_mask:0xf
v_fma_f32 v194, v194, v218, v202
v_fma_f32 v195, v195, v218, v203
v_fma_f32 v196, v196, v218, v204
v_fma_f32 v197, v197, v218, v205
v_cvt_pk_bf16_f32 v210, v194, v195
v_cvt_pk_bf16_f32 v211, v196, v197
ds_write_b64 v14, v[210:211]
ds_read_b64 v[212:213], v15
s_waitcnt lgkmcnt(0)
global_store_dwordx2 v13, v[212:213], s[56:57] sc1
s_add_u32 s60, s60, 4
s_branch .Lgu_fin
.Lgu_end:
s_endpgm
.Lgu_sp7:
s_sleep 1
global_load_dword v254, v4, s[38:39] sc1
s_add_u32 s71, s71, 1
s_waitcnt vmcnt(0)
v_sub_u32 v254, v254, v5
v_cmp_lt_u32 vcc, 0x7f, v254
s_cbranch_vccz .Lgu_rt8
s_cmp_lt_u32 s71, 3000000
s_cbranch_scc1 .Lgu_sp7
s_branch .Lgu_rt8
.Lgu_sp9:
s_sleep 1
global_load_dword v254, v4, s[38:39] sc1
s_add_u32 s71, s71, 1
s_waitcnt vmcnt(0)
v_sub_u32 v254, v254, v5
v_cmp_lt_u32 vcc, 0x7f, v254
s_cbranch_vccz .Lgu_rt10
s_cmp_lt_u32 s71, 3000000
s_cbranch_scc1 .Lgu_sp9
s_branch .Lgu_rt10
.Lgu_sp12:
s_sleep 1
global_load_dword v217, v16, s[38:39] sc1
s_add_u32 s71, s71, 1
s_waitcnt vmcnt(0)
v_sub_u32 v217, v217, v17
v_cmp_lt_u32 vcc, 0x7f, v217
s_and_b64 vcc, vcc, s[76:77]
s_cbranch_vccz .Lgu_rt13
s_cmp_lt_u32 s71, 3000000
s_cbranch_scc1 .Lgu_sp12
s_branch .Lgu_rt13
.Lgu_sp14:
s_sleep 1
global_load_dword v217, v16, s[38:39] sc1
s_add_u32 s71, s71, 1
s_waitcnt vmcnt(0)
v_sub_u32 v217, v217, v17
v_cmp_lt_u32 vcc, 0x7f, v217
)"
R"(s_and_b64 vcc, vcc, s[76:77]
s_cbranch_vccz .Lgu_rt15
s_cmp_lt_u32 s71, 3000000
s_cbranch_scc1 .Lgu_sp14
s_branch .Lgu_rt15
.Lgu_sp17:
s_sleep 1
global_load_dword v217, v16, s[38:39] sc1
s_add_u32 s71, s71, 1
s_waitcnt vmcnt(0)
v_sub_u32 v217, v217, v220
v_cmp_lt_u32 vcc, 0x7f, v217
s_cbranch_vccz .Lgu_rt18
s_cmp_lt_u32 s71, 3000000
s_cbranch_scc1 .Lgu_sp17
s_branch .Lgu_rt18
.Lkend:
)"
  :: "{v[0:31]}"(va), "{v[32:63]}"(vb), "{s[36:51]}"(sa), "{s[52:67]}"(sb) : "v255", "a255", "s99", "vcc", "memory");
}

extern "C" void kernel_launch(void* const* d_in, const int* in_sizes, int n_in,
                              void* d_out, int out_size, void* d_ws, size_t ws_size,
                              hipStream_t stream) {
  k_main<<<256, 256, 0, stream>>>((const char*)d_in[0], (const char*)d_in[1], (const char*)d_in[2], (const char*)d_in[4],
                                  (const char*)d_in[5], (const char*)d_in[6], (char*)d_ws, (char*)d_out);
}
```

```cpp
#include <hip/hip_runtime.h>
#include <hip/hip_bf16.h>
#include <stdint.h>
typedef __attribute__((ext_vector_type(32))) unsigned u32x32;
typedef __attribute__((ext_vector_type(16))) unsigned u32x16;
constexpr unsigned RP = 80u;
constexpr unsigned PAD = 12u;
constexpr unsigned XT = 18432u;
constexpr unsigned X1P = 18688u;
constexpr unsigned HT = 147456u;
constexpr unsigned PT = 110592u;
constexpr unsigned FL = 1024u;
constexpr unsigned FR = 102400u;
constexpr unsigned WS_FL = 16384u;
constexpr unsigned WS_ST = 425984u;
constexpr unsigned WS_O = 1048576u;
constexpr unsigned WS_X1 = 6946816u;
constexpr unsigned WS_X2 = 13000704u;
constexpr unsigned WS_XD = 15396864u;
constexpr unsigned WS_P = 17977344u;
constexpr unsigned WS_H = 53366784u;
constexpr unsigned WS_END = 100552704u;
constexpr unsigned FO = 0u;
constexpr unsigned FX1 = 8192u;
constexpr unsigned FX2 = 65536u;
constexpr unsigned FP = 10240u;
constexpr unsigned FH = 28672u;
__device__ __forceinline__ void touch(const void* p) {
  asm volatile("s_mov_b32 m0, 0x27f00\n\ts_nop 0\n\tglobal_load_lds_dword %0, off" :: "v"(p) : "memory");
}
__global__ __launch_bounds__(256, 1) void k_main(const char* x0, const char* wqkv, const char* wo, const char* wg, const char* wu, const char* wd,
                                                 char* ws, char* out) {
  __shared__ __attribute__((aligned(1024))) char smem[163840];
  asm volatile("" :: "s"((unsigned)(size_t)(__attribute__((address_space(3))) const char*)smem) : "memory");
  unsigned lane = threadIdx.x & 63; const unsigned wave = __builtin_amdgcn_readfirstlane(threadIdx.x >> 6);
  const unsigned xc = blockIdx.x & 7, jj = blockIdx.x >> 3;
  const unsigned role = jj < 4 ? 0 : (jj == 4 || jj == 31) ? ((xc & 1) == 0 ? 1 : 3) : jj < 14 ? 2 : 3;
  const unsigned qa_hg = xc, qa_rho = jj, wo_rho = xc >> 1, wo_cw = jj & 1, dn_i = xc * 9 + jj - 5, gu_i = jj == 4 ? (xc ^ 1) * 18 + 17 : xc * 18 + jj - 14;
  unsigned qv[9] = {}, qx[5] = {};
  if (__builtin_expect(jj < 4, 1)) {
#pragma unroll
    for (unsigned j = 0; j < 9; ++j) {
      const unsigned c = j * 64 + lane, sg = (c * 5462u) >> 16;
      qv[j] = sg * (1152 - 192) + c * 16 + 4096 - (j & 7) * 1024;
    }
    const unsigned pc0 = wave < 2 ? wave * 5 : 2 + wave * 4;
#pragma unroll
    for (unsigned j = 0; j < 5; ++j)
      qx[j] = ((jj * 32 + 31) * 16 + ((pc0 + j) & 1) * 8 + (lane >> 3)) * 1152 + ((pc0 + j) >> 1) * 128 + (((lane ^ (lane >> 3)) & 7) << 4) + 4096 - j * 1024;
    const unsigned long long xb = (unsigned long long)(x0 - 2048);
    const unsigned long long wb = (unsigned long long)(wqkv + ((xc * 144) & ~63u) + wave * (16 * 3456));
    asm volatile("s_add_u32 m0, %18, 0\n\ts_sub_u32 vcc_lo, %19, 0\n\ts_subb_u32 vcc_hi, %20, 0\n\tglobal_load_lds_dwordx4 %13, vcc offset:-2048 sc1\n\tglobal_load_lds_dwordx4 %14, vcc offset:-1024 sc1\n\tglobal_load_lds_dwordx4 %15, vcc offset:0 sc1\n\tglobal_load_lds_dwordx4 %16, vcc offset:1024 sc1\n\ts_cmp_gt_u32 %21, 1\n\ts_cbranch_scc1 .Lqx0\n\tglobal_load_lds_dwordx4 %17, vcc offset:2048 sc1\n.Lqx0:\n\ts_add_u32 m0, %18, 18432\n\ts_sub_u32 vcc_lo, %19, 18432\n\ts_subb_u32 vcc_hi, %20, 0\n\tglobal_load_lds_dwordx4 %13, vcc offset:-2048 sc1\n\tglobal_load_lds_dwordx4 %14, vcc offset:-1024 sc1\n\tglobal_load_lds_dwordx4 %15, vcc offset:0 sc1\n\tglobal_load_lds_dwordx4 %16, vcc offset:1024 sc1\n\ts_cmp_gt_u32 %21, 1\n\ts_cbranch_scc1 .Lqx1\n\tglobal_load_lds_dwordx4 %17, vcc offset:2048 sc1\n.Lqx1:\n\t" : "+v"(lane) : "v"(qv[0]), "v"(qv[1]), "v"(qv[2]), "v"(qv[3]), "v"(qv[4]), "v"(qv[5]), "v"(qv[6]), "v"(qv[7]), "v"(qv[8]), "s"(47104 + wave * 9216),
                 "s"((unsigned)wb), "s"((unsigned)(wb >> 32)), "v"(qx[0]), "v"(qx[1]), "v"(qx[2]), "v"(qx[3]), "v"(qx[4]), "s"(pc0 * 1024 + 2048),
                 "s"((unsigned)xb), "s"((unsigned)(xb >> 32)), "s"(wave) : "vcc", "scc", "memory");
  } else {
    if (wave < 2) { const unsigned u = min((jj - 4) * 124 + threadIdx.x, 3455u), sg = u >> 1, k = sg / 3; touch(wqkv + k * 3456 + (sg - 3 * k) * 1152 + xc * 144 + (u & 1) * 140); }
    else if (wave == 2) {
      const unsigned o = min((lane & 7) * 1048576u, 5308412u);
      touch(wg + o); touch(wu + o); touch(wd + o); touch(out + min(o, 2359292u));
      touch(ws + min(lane * 2097152u, WS_END - 4));
      for (unsigned k = blockIdx.x - 40; k < 400 * FL / 8192; k += 216) touch(ws + WS_FL + (k * 64 + lane) * 128);
    } else {
      unsigned long long pc; unsigned len;
      asm volatile("s_getpc_b64 %0\n.Lkbeg:\n\ts_mov_b32 %1, .Lkend-.Lkbeg" : "=s"(pc), "=s"(len));
      touch((const char*)pc + min(((jj - 4) * 64 + lane) * 128, len));
    }
    if (wave < 3 && (xc & 1) == 0) touch(wo + min((jj - 4) * 186 + threadIdx.x, 5183u) * 128);
    if (jj >= 14 && wave < 2) touch(x0 + ((blockIdx.x - 112) * 128 + threadIdx.x) * 128);
  }

  const unsigned r = lane & 15, g = lane >> 4, q = r >> 2, p = lane & 3;
  u32x32 va = {}, vb = {};
  u32x16 sa = {}, sb = {};
  unsigned sv[28] = {};
  sa[0] = __builtin_amdgcn_readfirstlane(role * 4 + wave); sa[2] = (unsigned)(unsigned long long)ws; sa[3] = (unsigned)((unsigned long long)ws >> 32);
  if (__builtin_expect(role == 0, 1)) {
    const unsigned hg = qa_hg, rho = qa_rho, row0 = rho * RP + PAD, pc0 = wave < 2 ? wave * 5 : 2 + wave * 4, hb = wave * 36 + (wave & 1) * 4;
    auto oa = [&](unsigned b) { return 43520 + (b >> 4) * 256 + r * 16 + (b & 15); };
    const unsigned m0 = 4 * g + (r & 1), m1 = m0 + 2, hm = (r & 1) ? 0xFFFF0000u : 0xFFFFu, ninf = 0xFF800000u;
    va[0] = qv[0];
    va[1] = qv[1];
    va[2] = qv[2];
    va[3] = qv[3];
    va[4] = qv[4];
    va[5] = qv[5];
    va[6] = qv[6];
    va[7] = qv[7];
    va[8] = qv[8];
    va[9] = (8 * g + q) * 576 + (hg & 3) * 16 + hb + p * 8;
    va[10] = (8 * g + q) * 576 + (hg & 3) * 16 + 32 + (p & 1) * 72 + (wave == 0 ? (p >> 1) * 192 : 384);
    va[11] = qx[0];
    va[12] = qx[1];
    va[13] = qx[2];
    va[14] = qx[3];
    va[15] = qx[4];
    va[16] = WS_O + (row0 - 1) * XT + hg * 2304 + wave * 1024 + (wave < 2 ? lane : r) * 16;
    va[17] = 43520 + wave * 1024 + (wave < 2 ? lane : r) * 16;
    va[18] = oa(hb + g * 8);
    va[19] = ((g & 1) == (wave >> 1)) ? oa(32 + (wave & 1) * 4 + (wave >> 1) * 72) : 46336 + lane * 4;
    va[20] = g == 0 ? 40960 + ((wave >> 1) * 16 + r) * 8 + (wave & 1) * 4 : 46080;
    va[21] = 41216 + ((wave >> 1) * 16 + r) * 8 + (wave & 1) * 4;
    va[22] = lane * 8;
    va[23] = lane * 16;
    va[24] = 43008 + r * 4;
    va[25] = 43008 + g * 16;
    va[26] = 41984 + (q * 16 + r) * 16 + p * 4;
    va[27] = 43136 + (((m0 > r) ? 0 : 1) * 16 + m0) * 4;
    va[28] = 43136 + (((m1 > r) ? 0 : 1) * 16 + m1) * 4;
    va[29] = 43136 + (((m0 > r) ? 1 : 0) * 16 + m0) * 4;
    va[30] = 43136 + (((m1 > r) ? 1 : 0) * 16 + m1) * 4;
    va[31] = 0u;
    vb[0] = (4 * g + 0 > r && !((r ^ 0) & 1)) ? 0u : ninf;
    vb[1] = (4 * g + 1 > r && !((r ^ 1) & 1)) ? 0u : ninf;
    vb[2] = (4 * g + 2 > r && !((r ^ 2) & 1)) ? 0u : ninf;
    vb[3] = (4 * g + 3 > r && !((r ^ 3) & 1)) ? 0u : ninf;
    vb[4] = (4 * g + 0 <= r && !((r ^ 0) & 1)) ? 0u : ninf;
    vb[5] = (4 * g + 1 <= r && !((r ^ 1) & 1)) ? 0u : ninf;
    vb[6] = (4 * g + 2 <= r && !((r ^ 2) & 1)) ? 0u : ninf;
    vb[7] = (4 * g + 3 <= r && !((r ^ 3) & 1)) ? 0u : ninf;
    vb[8] = m0 > r ? hm : 0u;
    vb[9] = m1 > r ? hm : 0u;
    vb[10] = m0 > r ? 0u : hm;
    vb[11] = m1 > r ? 0u : hm;
    vb[12] = WS_FL + rho * FR + FX2 + min(lane, 35u) * FL;
    vb[13] = WS_FL + rho * FR + FX2 + min(lane, 35u) * FL;
    vb[14] = 0u - 29;
    vb[15] = WS_FL + rho * FR + FO + hg * FL;
    vb[16] = 0u - 2;
    vb[17] = WS_FL + lane * FL;
    vb[18] = 0u;
    vb[19] = r * 128 + (((0 + g) ^ (r & 7)) << 4);
    vb[20] = r * 128 + (((4 + g) ^ (r & 7)) << 4);
    sv[0] = (unsigned)(unsigned long long)(wqkv + ((hg * 144) & ~63u) + wave * (16 * 3456) + 0 * 64 * 3456);
    sv[1] = (unsigned)((unsigned long long)(wqkv + ((hg * 144) & ~63u) + wave * (16 * 3456) + 0 * 64 * 3456) >> 32);
    sv[2] = (unsigned)(unsigned long long)(x0 - 2048 - 2 * XT);
    sv[3] = (unsigned)((unsigned long long)(x0 - 2048 - 2 * XT) >> 32);
    sv[4] = (unsigned)(unsigned long long)((ws + WS_X2) - x0 + (rho ? 32 : 31) * (long long)XT);
    sv[5] = (unsigned)((unsigned long long)((ws + WS_X2) - x0 + (rho ? 32 : 31) * (long long)XT) >> 32);
    sv[6] = (unsigned)(unsigned long long)(-(long long)XT);
    sv[7] = (unsigned)((unsigned long long)(-(long long)XT) >> 32);
    sv[8] = 47104 + wave * 9216;
    sv[9] = pc0 * 1024 + 2048 + 0x10000;
    sv[10] = rho ? 0u : XT;
    sv[11] = rho ? 0u - FR : 0u;
    sv[12] = rho ? 0u : ninf;
    sv[13] = hg * 4 + rho;
  } else if (__builtin_expect(role == 1, 0)) {
    const unsigned rho = wo_rho, cw = wo_cw, row0 = rho * RP + PAD, pc0 = wave * 5;
    auto wv = [&](unsigned j) { const unsigned b = j * 1024 + lane * 16, rw = b / 576; return rw * 576 + b + 4096 - (j & 7) * 1024; };
    va[0] = wv(0);
    va[1] = wv(1);
    va[2] = wv(2);
    va[3] = wv(3);
    va[4] = wv(4);
    va[5] = wv(5);
    va[6] = wv(6);
    va[7] = wv(7);
    va[8] = wv(8);
    va[9] = (8 * g + q) * 576 + cw * 64 + wave * 128 + p * 16;
    va[10] = (8 * g + q) * 576 + (cw ? 0 : 512) + p * 16 + (wave >> 1) * 8;
    va[11] = WS_O + (row0 - 2) * XT + pc0 * 1024 + 2048 + lane * 16;
    va[12] = WS_O + (row0 + 63) * XT + pc0 * 1024 + 2048 + lane * 16;
    va[13] = WS_X1 + (row0 - 5) * X1P + (cw * 10 + wave * 2) * 1024 + lane * 16;
    va[14] = WS_X1 + (row0 - 5) * X1P + (8 + cw) * 1024 + lane * 16;
    va[15] = WS_X1 + (row0 - 7) * X1P + XT + cw * 128 + r * 8;
    va[16] = ((rho * 32 + 31) * 16 + 0 + (lane >> 3)) * 1152 + (cw * 5 + wave) * 128 + (((lane ^ (lane >> 3)) & 7) << 4) - 0;
    va[17] = ((rho * 32 + 31) * 16 + 8 + (lane >> 3)) * 1152 + (cw * 5 + wave) * 128 + (((lane ^ (lane >> 3)) & 7) << 4) - 1024;
    va[18] = ((rho * 32 + 31) * 16 + r) * 1152 + (8 + cw) * 64 + g * 16;
    va[19] = 47104 + wave * 2048 + r * 128 + (((0 + g) ^ (r & 7)) << 4);
    va[20] = 47104 + wave * 2048 + r * 128 + (((4 + g) ^ (r & 7)) << 4);
    va[21] = WS_FL + rho * FR + FO + (lane & 7) * FL;
    va[22] = 0u - 2;
    va[23] = WS_FL + rho * FR + FX1 + cw * FL;
    va[24] = 0u - 6;
    va[25] = lane * 16;
    va[26] = lane * 16;
    va[27] = lane * 8;
    sv[0] = (unsigned)(unsigned long long)(wo + wave * 16 * 1152 + cw * 576);
    sv[1] = (unsigned)((unsigned long long)(wo + wave * 16 * 1152 + cw * 576) >> 32);
    sv[2] = (unsigned)(unsigned long long)(x0);
    sv[3] = (unsigned)((unsigned long long)(x0) >> 32);
    sv[4] = (unsigned)(unsigned long long)(x0);
    sv[5] = (unsigned)((unsigned long long)(x0) >> 32);
    sv[6] = (unsigned)(unsigned long long)(ws + WS_X2);
    sv[7] = (unsigned)((unsigned long long)(ws + WS_X2) >> 32);
    sv[8] = wave * 9216;
    sv[9] = pc0 * 1024 + 2048;
  } else if (__builtin_expect(role == 2, 0)) {
    const unsigned tri = dn_i / 3, nu = dn_i % 3, kap = tri >> 2, rho = tri & 3, row0 = rho * RP + PAD, pg = dn_i % 36;
    auto wv = [&](unsigned j) { const unsigned b = j * 1024 + lane * 16, rw = b / 384; return rw * (1152 - 384) + b + 4096 - j * 1024; };
    va[0] = wv(0);
    va[1] = wv(1);
    va[2] = wv(2);
    va[3] = wv(3);
    va[4] = wv(4);
    va[5] = wv(5);
    va[6] = (8 * g + q) * 384 + wave * 64 + p * 16 + (kap & 1) * 8;
    va[7] = (8 * g + q) * 384 + wave * 64 + p * 16 + 8 - (kap & 1) * 8;
    va[8] = (8 * g + q) * 384 + 256 + wave * 32 + (p ^ (kap & 1)) * 8;
    va[9] = WS_H + row0 * HT + (kap * 24 + wave * 6 + 2) * 1024 + lane * 16;
    va[10] = WS_H + (row0 + 63) * HT + (kap * 24 + wave * 6 + 2) * 1024 + lane * 16;
    va[11] = WS_P + (row0 - 3) * PT + (((nu * 3 + (wave >> 1)) * 4 + q) * 6 + kap) * 512 + (wave & 1) * 256 + g * 64 + p * 16;
    va[12] = WS_P + (row0 - 3) * PT + (((nu * 3 + 2) * 4 + q) * 6 + kap) * 512 + wave * 128 + (g >> 1) * 64 + p * 16;
    va[13] = WS_FL + rho * FR + FH + (kap * 6 + min(lane, 5u)) * FL;
    va[14] = 0u;
    va[15] = WS_FL + rho * FR + FP + (nu * 6 + kap) * FL;
    va[16] = 0u - 4;
    va[17] = lane * 16;
    va[18] = (320 + (wave & 1) * 128 + (lane >> 1)) * 9216 + (lane & 1) * 768;
    sv[0] = (unsigned)(unsigned long long)((wave < 2 ? wg : wu) + pg / 6 * 1536 + (pg % 6) * 128);
    sv[1] = (unsigned)((unsigned long long)((wave < 2 ? wg : wu) + pg / 6 * 1536 + (pg % 6) * 128) >> 32);
    sv[2] = (unsigned)(unsigned long long)(wd + (kap * 768 + wave * 16) * 1152 + nu * 384);
    sv[3] = (unsigned)((unsigned long long)(wd + (kap * 768 + wave * 16) * 1152 + nu * 384) >> 32);
    sv[4] = wave * 6144;
    sv[5] = wave * 6144 + 2048;
  } else {
    const unsigned ii = gu_i, gam = (ii % 72) >> 1, rho = ii / 72 * 2 + (ii & 1), row0 = rho * RP + PAD, pc0 = wave * 6;
    const unsigned bk = gam >> 2, qd = gam & 3, od = (lane >> 3) & 1, un = g * 8 + (lane & 7), jr = qd * 4 + p, jc = (un >> 2) * 8 + od * 4;
    va[0] = g * 9216 + ((r ^ (g << 2)) << 4) + 4096 + (((r ^ (g << 2)) >> 3) & 1) * 640;
    va[1] = (8 * g + q) * 256 + (((4 * wave + p) ^ (q << 2)) << 4);
    va[2] = WS_X1 + row0 * X1P + pc0 * 1024 + 2048 + lane * 16;
    va[3] = WS_H + (row0 - 3) * HT + (gam / 6 * 24 + (gam % 6) * 2 + (wave >> 1) * 12 + (wave & 1)) * 1024 + lane * 16;
    va[4] = WS_FL + rho * FR + FX1 + (lane & 1) * FL;
    va[5] = 0u;
    va[6] = WS_FL + rho * FR + FH + gam * FL;
    va[7] = 0u - 4;
    va[8] = lane * 16;
    va[9] = WS_X1 + (row0 + 63) * X1P + pc0 * 1024 + 2048 + lane * 16;
    va[10] = WS_P + row0 * PT + gam * 3072 + od * 512 + un * 16;
    va[11] = WS_X1 + row0 * X1P + bk * 2048 + (un >> 2) * 256 + jr * 16 + od * 8;
    va[12] = WS_X1 + row0 * X1P + XT + jr * 8;
    va[13] = ((rho * 32 + 31) * 16 + qd * 4 + g) * 1152 + bk * 128 + r * 8;
    va[14] = 49152 + wave * 1024 + p * 144 + jc * 2;
    va[15] = 49152 + wave * 1024 + g * 144 + r * 8;
    va[16] = WS_FL + rho * FR + FP + (bk / 3 * 6 + min(lane, 5u)) * FL;
    va[17] = 0u;
    va[18] = WS_FL + rho * FR + FX2 + gam * FL;
    va[19] = (rho * 144 + (wave & 1) * 40 + (lane >> 1)) * 9216 + (lane & 1) * 128;
    va[20] = 0u;
    va[21] = 0u;
    va[22] = (g == q && p < 2) ? (0x3F80u << (16 * p)) : 0u;
    va[23] = (g == q && p >= 2) ? (0x3F80u << (16 * (p - 2))) : 0u;
    va[24] = 0u;
    va[25] = 0u;
    sv[0] = (unsigned)(unsigned long long)((wave < 2 ? wg : wu) + gam / 6 * 1536 + (gam % 6) * 128 + (wave & 1) * (32 * 9216) + 0 * 64 * 9216);
    sv[1] = (unsigned)((unsigned long long)((wave < 2 ? wg : wu) + gam / 6 * 1536 + (gam % 6) * 128 + (wave & 1) * (32 * 9216) + 0 * 64 * 9216) >> 32);
    sv[2] = wave * 8192;
    sv[3] = pc0 * 1024 + 2048;
    sv[4] = (unsigned)(unsigned long long)(ws + WS_X2);
    sv[5] = (unsigned)((unsigned long long)(ws + WS_X2) >> 32);
    sv[6] = (unsigned)(unsigned long long)(out + 32 * XT);
    sv[7] = (unsigned)((unsigned long long)(out + 32 * XT) >> 32);
    sv[8] = (unsigned)(unsigned long long)(ws + WS_XD);
    sv[9] = (unsigned)((unsigned long long)(ws + WS_XD) >> 32);
    sv[10] = (unsigned)(unsigned long long)(ws - PT);
    sv[11] = (unsigned)((unsigned long long)(ws - PT) >> 32);
    sv[12] = (unsigned)(unsigned long long)(ws - X1P);
    sv[13] = (unsigned)((unsigned long long)(ws - X1P) >> 32);
    sv[14] = (unsigned)(unsigned long long)(ws + WS_X2);
    sv[15] = (unsigned)((unsigned long long)(ws + WS_X2) >> 32);
    sv[16] = (unsigned)(unsigned long long)(ws + WS_XD);
    sv[17] = (unsigned)((unsigned long long)(ws + WS_XD) >> 32);
    sv[18] = (unsigned)(unsigned long long)(ws + WS_XD);
    sv[19] = (unsigned)((unsigned long long)(ws + WS_XD) >> 32);
    sv[20] = 0u;
    sv[21] = rho & 1;
  }
#pragma unroll
  for (int i = 0; i < 28; ++i) { const unsigned u = __builtin_amdgcn_readfirstlane(sv[i]); if (i < 12) sa[i + 4] = u; else sb[i - 12] = u; }

  asm volatile(
R"(s_movk_i32 s69, 0x380
s_mov_b32 s71, 0
s_mov_b32 s74, 0
s_load_dword s37, s[38:39], 0x0
s_lshr_b32 s75, s36, 2
s_cmp_eq_u32 s75, 1
s_cbranch_scc1 .Lrole1
s_cmp_eq_u32 s75, 2
s_cbranch_scc1 .Lrole2
s_cmp_eq_u32 s75, 3
s_cbranch_scc1 .Lrole3
.Lrole0:
s_and_b32 s75, s36, 3
s_cmp_eq_u32 s75, 1
s_cbranch_scc1 .Lqa_w2
s_cmp_eq_u32 s75, 2
s_cbranch_scc1 .Lqa_w3
s_cmp_eq_u32 s75, 3
s_cbranch_scc1 .Lqa_w4
.Lqa_w1:
s_add_u32 m0, s48, 4096
s_nop 0
global_load_lds_dwordx4 v0, s[40:41] offset:-4096
global_load_lds_dwordx4 v1, s[40:41] offset:-3072
global_load_lds_dwordx4 v2, s[40:41] offset:-2048
global_load_lds_dwordx4 v3, s[40:41] offset:-1024
global_load_lds_dwordx4 v4, s[40:41] offset:0
global_load_lds_dwordx4 v5, s[40:41] offset:1024
global_load_lds_dwordx4 v6, s[40:41] offset:2048
global_load_lds_dwordx4 v7, s[40:41] offset:3072
s_add_u32 m0, s48, 12288
s_nop 0
global_load_lds_dwordx4 v8, s[40:41] offset:-4096
s_add_u32 s40, s40, 221184
s_addc_u32 s41, s41, 0
s_add_u32 m0, s48, 40960
s_nop 0
global_load_lds_dwordx4 v0, s[40:41] offset:-4096
global_load_lds_dwordx4 v1, s[40:41] offset:-3072
global_load_lds_dwordx4 v2, s[40:41] offset:-2048
global_load_lds_dwordx4 v3, s[40:41] offset:-1024
global_load_lds_dwordx4 v4, s[40:41] offset:0
global_load_lds_dwordx4 v5, s[40:41] offset:1024
global_load_lds_dwordx4 v6, s[40:41] offset:2048
global_load_lds_dwordx4 v7, s[40:41] offset:3072
s_add_u32 m0, s48, 49152
s_nop 0
global_load_lds_dwordx4 v8, s[40:41] offset:-4096
s_add_u32 s40, s40, 221184
s_addc_u32 s41, s41, 0
v_add_u32 v255, 0x10000, v9
v_add_u32 v162, 0x20000, v9
v_add_u32 v163, 0x10000, v10
v_add_u32 v164, 0x20000, v10
v_mov_b32 v106, 0x3F803F80
v_mov_b32 v107, 0x3F803F80
v_mov_b32 v108, 0x3F803F80
v_mov_b32 v109, 0x3F803F80
v_mov_b32 v141, 0
v_mov_b32 v145, 0
v_mov_b32 v149, 0
v_mov_b32 v153, 0
v_mov_b32 v165, 46080
s_waitcnt lgkmcnt(0)
s_mul_i32 s37, s37, 0x9E3779B1
s_add_u32 s37, s37, 0x7F4A7C15
v_add_u32 v46, s37, v46
v_add_u32 v48, s37, v48
v_add_u32 v50, s37, v50
ds_write_b32 v165, v141
s_mov_b64 s[76:77], 0
s_mov_b32 s73, 18
s_waitcnt vmcnt(9) lgkmcnt(0)
s_barrier
s_add_u32 m0, s48, 77824
ds_read_b64_tr_b16 a[0:1], v9 offset:47104
ds_read_b64_tr_b16 a[2:3], v9 offset:49408
global_load_lds_dwordx4 v0, s[40:41] offset:-4096
ds_read_b64_tr_b16 a[4:5], v9 offset:47296
ds_read_b64_tr_b16 a[6:7], v9 offset:49600
global_load_lds_dwordx4 v1, s[40:41] offset:-3072
ds_read_b64_tr_b16 a[8:9], v9 offset:47488
global_load_lds_dwordx4 v2, s[40:41] offset:-2048
ds_read_b64_tr_b16 a[10:11], v9 offset:49792
ds_read_b64_tr_b16 a[216:217], v10 offset:47104
global_load_lds_dwordx4 v3, s[40:41] offset:-1024
ds_read_b64_tr_b16 a[218:219], v10 offset:49408
global_load_lds_dwordx4 v4, s[40:41] offset:0
ds_read_b128 v[166:169], v51 offset:0
ds_read_b128 v[174:177], v51 offset:18432
global_load_lds_dwordx4 v5, s[40:41] offset:1024
ds_read_b64_tr_b16 a[12:13], v255 offset:0
global_load_lds_dwordx4 v6, s[40:41] offset:2048
ds_read_b64_tr_b16 a[14:15], v255 offset:2304
ds_read_b64_tr_b16 a[16:17], v255 offset:192
global_load_lds_dwordx4 v7, s[40:41] offset:3072
ds_read_b64_tr_b16 a[18:19], v255 offset:2496
s_add_u32 m0, s48, 86016
ds_read_b64_tr_b16 a[20:21], v255 offset:384
ds_read_b64_tr_b16 a[22:23], v255 offset:2688
global_load_lds_dwordx4 v8, s[40:41] offset:-4096
ds_read_b64_tr_b16 a[220:221], v163 offset:0
s_add_u32 s40, s40, 221184
ds_read_b64_tr_b16 a[222:223], v163 offset:2304
ds_read_b128 v[170:173], v52 offset:0
s_addc_u32 s41, s41, 0
ds_read_b128 v[178:181], v52 offset:18432
s_waitcnt vmcnt(9) lgkmcnt(0)
s_barrier
s_add_u32 m0, s48, 4096
ds_read_b64_tr_b16 a[24:25], v255 offset:18432
v_mfma_f32_16x16x32_bf16 v[86:89], a[0:3], v[106:109], 0
ds_read_b64_tr_b16 a[26:27], v255 offset:20736
v_mfma_f32_16x16x32_bf16 v[90:93], a[4:7], v[106:109], 0
global_load_lds_dwordx4 v0, s[40:41] offset:-4096
ds_read_b64_tr_b16 a[28:29], v255 offset:18624
v_mfma_f32_16x16x32_bf16 v[94:97], v[106:109], a[8:11], 0
ds_read_b64_tr_b16 a[30:31], v255 offset:20928
global_load_lds_dwordx4 v1, s[40:41] offset:-3072
v_mfma_f32_16x16x32_bf16 v[98:101], a[216:219], v[106:109], 0
ds_read_b64_tr_b16 a[32:33], v255 offset:18816
v_mfma_f32_16x16x32_bf16 v[110:113], a[0:3], v[166:169], 0
ds_read_b64_tr_b16 a[34:35], v255 offset:21120
global_load_lds_dwordx4 v2, s[40:41] offset:-2048
v_mfma_f32_16x16x32_bf16 v[114:117], a[4:7], v[166:169], 0
ds_read_b64_tr_b16 a[224:225], v163 offset:18432
v_mfma_f32_16x16x32_bf16 v[118:121], v[166:169], a[8:11], 0
global_load_lds_dwordx4 v3, s[40:41] offset:-1024
ds_read_b64_tr_b16 a[226:227], v163 offset:20736
v_mfma_f32_16x16x32_bf16 v[134:137], a[216:219], v[166:169], 0
ds_read_b128 v[182:185], v51 offset:2048
global_load_lds_dwordx4 v4, s[40:41] offset:0
v_mfma_f32_16x16x32_bf16 v[122:125], a[0:3], v[174:177], 0
ds_read_b128 v[190:193], v51 offset:20480
v_mfma_f32_16x16x32_bf16 v[126:129], a[4:7], v[174:177], 0
ds_read_b64_tr_b16 a[36:37], v255 offset:36864
global_load_lds_dwordx4 v5, s[40:41] offset:1024
v_mfma_f32_16x16x32_bf16 v[130:133], v[174:177], a[8:11], 0
ds_read_b64_tr_b16 a[38:39], v255 offset:39168
v_mfma_f32_16x16x32_bf16 v[158:161], a[216:219], v[174:177], 0
global_load_lds_dwordx4 v6, s[40:41] offset:2048
ds_read_b64_tr_b16 a[40:41], v255 offset:37056
v_mfma_f32_16x16x32_bf16 v[86:89], a[12:15], v[106:109], v[86:89]
ds_read_b64_tr_b16 a[42:43], v255 offset:39360
v_mfma_f32_16x16x32_bf16 v[90:93], a[16:19], v[106:109], v[90:93]
global_load_lds_dwordx4 v7, s[40:41] offset:3072
ds_read_b64_tr_b16 a[44:45], v255 offset:37248
v_mfma_f32_16x16x32_bf16 v[94:97], v[106:109], a[20:23], v[94:97]
s_add_u32 m0, s48, 12288
ds_read_b64_tr_b16 a[46:47], v255 offset:39552
v_mfma_f32_16x16x32_bf16 v[98:101], a[220:223], v[106:109], v[98:101]
ds_read_b64_tr_b16 a[228:229], v163 offset:36864
v_mfma_f32_16x16x32_bf16 v[110:113], a[12:15], v[170:173], v[110:113]
global_load_lds_dwordx4 v8, s[40:41] offset:-4096
ds_read_b64_tr_b16 a[230:231], v163 offset:39168
v_mfma_f32_16x16x32_bf16 v[114:117], a[16:19], v[170:173], v[114:117]
ds_read_b128 v[186:189], v52 offset:2048
v_mfma_f32_16x16x32_bf16 v[118:121], v[170:173], a[20:23], v[118:121]
s_add_u32 s40, s40, 221184
ds_read_b128 v[194:197], v52 offset:20480
v_mfma_f32_16x16x32_bf16 v[134:137], a[220:223], v[170:173], v[134:137]
v_mfma_f32_16x16x32_bf16 v[122:125], a[12:15], v[178:181], v[122:125]
s_addc_u32 s41, s41, 0
v_mfma_f32_16x16x32_bf16 v[126:129], a[16:19], v[178:181], v[126:129]
v_mfma_f32_16x16x32_bf16 v[130:133], v[178:181], a[20:23], v[130:133]
v_mfma_f32_16x16x32_bf16 v[158:161], a[220:223], v[178:181], v[158:161]
s_waitcnt vmcnt(9) lgkmcnt(0)
s_barrier
s_add_u32 m0, s48, 40960
ds_read_b64_tr_b16 a[48:49], v255 offset:55296
v_mfma_f32_16x16x32_bf16 v[86:89], a[24:27], v[106:109], v[86:89]
ds_read_b64_tr_b16 a[50:51], v255 offset:57600
v_mfma_f32_16x16x32_bf16 v[90:93], a[28:31], v[106:109], v[90:93]
global_load_lds_dwordx4 v0, s[40:41] offset:-4096
ds_read_b64_tr_b16 a[52:53], v255 offset:55488
v_mfma_f32_16x16x32_bf16 v[94:97], v[106:109], a[32:35], v[94:97]
ds_read_b64_tr_b16 a[54:55], v255 offset:57792
global_load_lds_dwordx4 v1, s[40:41] offset:-3072
v_mfma_f32_16x16x32_bf16 v[98:101], a[224:227], v[106:109], v[98:101]
ds_read_b64_tr_b16 a[56:57], v255 offset:55680
v_mfma_f32_16x16x32_bf16 v[110:113], a[24:27], v[182:185], v[110:113]
ds_read_b64_tr_b16 a[58:59], v255 offset:57984
global_load_lds_dwordx4 v2, s[40:41] offset:-2048
v_mfma_f32_16x16x32_bf16 v[114:117], a[28:31], v[182:185], v[114:117]
ds_read_b64_tr_b16 a[232:233], v163 offset:55296
v_mfma_f32_16x16x32_bf16 v[118:121], v[182:185], a[32:35], v[118:121]
global_load_lds_dwordx4 v3, s[40:41] offset:-1024
ds_read_b64_tr_b16 a[234:235], v163 offset:57600
v_mfma_f32_16x16x32_bf16 v[134:137], a[224:227], v[182:185], v[134:137]
ds_read_b128 v[166:169], v51 offset:4096
global_load_lds_dwordx4 v4, s[40:41] offset:0
v_mfma_f32_16x16x32_bf16 v[122:125], a[24:27], v[190:193], v[122:125]
ds_read_b128 v[174:177], v51 offset:22528
v_mfma_f32_16x16x32_bf16 v[126:129], a[28:31], v[190:193], v[126:129]
ds_read_b64_tr_b16 a[60:61], v162 offset:8192
global_load_lds_dwordx4 v5, s[40:41] offset:1024
v_mfma_f32_16x16x32_bf16 v[130:133], v[190:193], a[32:35], v[130:133]
ds_read_b64_tr_b16 a[62:63], v162 offset:10496
v_mfma_f32_16x16x32_bf16 v[158:161], a[224:227], v[190:193], v[158:161]
global_load_lds_dwordx4 v6, s[40:41] offset:2048
ds_read_b64_tr_b16 a[64:65], v162 offset:8384
v_mfma_f32_16x16x32_bf16 v[86:89], a[36:39], v[106:109], v[86:89]
ds_read_b64_tr_b16 a[66:67], v162 offset:10688
v_mfma_f32_16x16x32_bf16 v[90:93], a[40:43], v[106:109], v[90:93]
global_load_lds_dwordx4 v7, s[40:41] offset:3072
ds_read_b64_tr_b16 a[68:69], v162 offset:8576
v_mfma_f32_16x16x32_bf16 v[94:97], v[106:109], a[44:47], v[94:97]
s_add_u32 m0, s48, 49152
ds_read_b64_tr_b16 a[70:71], v162 offset:10880
v_mfma_f32_16x16x32_bf16 v[98:101], a[228:231], v[106:109], v[98:101]
ds_read_b64_tr_b16 a[236:237], v164 offset:8192
v_mfma_f32_16x16x32_bf16 v[110:113], a[36:39], v[186:189], v[110:113]
global_load_lds_dwordx4 v8, s[40:41] offset:-4096
ds_read_b64_tr_b16 a[238:239], v164 offset:10496
v_mfma_f32_16x16x32_bf16 v[114:117], a[40:43], v[186:189], v[114:117]
ds_read_b128 v[170:173], v52 offset:4096
v_mfma_f32_16x16x32_bf16 v[118:121], v[186:189], a[44:47], v[118:121]
s_add_u32 s40, s40, 221184
ds_read_b128 v[178:181], v52 offset:22528
v_mfma_f32_16x16x32_bf16 v[134:137], a[228:231], v[186:189], v[134:137]
v_mfma_f32_16x16x32_bf16 v[122:125], a[36:39], v[194:197], v[122:125]
s_addc_u32 s41, s41, 0
v_mfma_f32_16x16x32_bf16 v[126:129], a[40:43], v[194:197], v[126:129]
v_mfma_f32_16x16x32_bf16 v[130:133], v[194:197], a[44:47], v[130:133]
v_mfma_f32_16x16x32_bf16 v[158:161], a[228:231], v[194:197], v[158:161]
s_waitcnt vmcnt(9) lgkmcnt(0)
s_barrier
s_add_u32 m0, s48, 77824
ds_read_b64_tr_b16 a[72:73], v9 offset:47104
v_mfma_f32_16x16x32_bf16 v[86:89], a[48:51], v[106:109], v[86:89]
ds_read_b64_tr_b16 a[74:75], v9 offset:49408
v_mfma_f32_16x16x32_bf16 v[90:93], a[52:55], v[106:109], v[90:93]
global_load_lds_dwordx4 v0, s[40:41] offset:-4096
ds_read_b64_tr_b16 a[76:77], v9 offset:47296
v_mfma_f32_16x16x32_bf16 v[94:97], v[106:109], a[56:59], v[94:97]
ds_read_b64_tr_b16 a[78:79], v9 offset:49600
global_load_lds_dwordx4 v1, s[40:41] offset:-3072
v_mfma_f32_16x16x32_bf16 v[98:101], a[232:235], v[106:109], v[98:101]
ds_read_b64_tr_b16 a[80:81], v9 offset:47488
v_mfma_f32_16x16x32_bf16 v[110:113], a[48:51], v[166:169], v[110:113]
ds_read_b64_tr_b16 a[82:83], v9 offset:49792
global_load_lds_dwordx4 v2, s[40:41] offset:-2048
v_mfma_f32_16x16x32_bf16 v[114:117], a[52:55], v[166:169], v[114:117]
ds_read_b64_tr_b16 a[240:241], v10 offset:47104
v_mfma_f32_16x16x32_bf16 v[118:121], v[166:169], a[56:59], v[118:121]
global_load_lds_dwordx4 v3, s[40:41] offset:-1024
ds_read_b64_tr_b16 a[242:243], v10 offset:49408
v_mfma_f32_16x16x32_bf16 v[134:137], a[232:235], v[166:169], v[134:137]
ds_read_b128 v[182:185], v51 offset:6144
global_load_lds_dwordx4 v4, s[40:41] offset:0
v_mfma_f32_16x16x32_bf16 v[122:125], a[48:51], v[174:177], v[122:125]
ds_read_b128 v[190:193], v51 offset:24576
v_mfma_f32_16x16x32_bf16 v[126:129], a[52:55], v[174:177], v[126:129]
ds_read_b64_tr_b16 a[84:85], v255 offset:0
global_load_lds_dwordx4 v5, s[40:41] offset:1024
v_mfma_f32_16x16x32_bf16 v[130:133], v[174:177], a[56:59], v[130:133]
ds_read_b64_tr_b16 a[86:87], v255 offset:2304
v_mfma_f32_16x16x32_bf16 v[158:161], a[232:235], v[174:177], v[158:161]
global_load_lds_dwordx4 v6, s[40:41] offset:2048
ds_read_b64_tr_b16 a[88:89], v255 offset:192
v_mfma_f32_16x16x32_bf16 v[86:89], a[60:63], v[106:109], v[86:89]
ds_read_b64_tr_b16 a[90:91], v255 offset:2496
v_mfma_f32_16x16x32_bf16 v[90:93], a[64:67], v[106:109], v[90:93]
global_load_lds_dwordx4 v7, s[40:41] offset:3072
ds_read_b64_tr_b16 a[92:93], v255 offset:384
v_mfma_f32_16x16x32_bf16 v[94:97], v[106:109], a[68:71], v[94:97]
s_add_u32 m0, s48, 86016
ds_read_b64_tr_b16 a[94:95], v255 offset:2688
v_mfma_f32_16x16x32_bf16 v[98:101], a[236:239], v[106:109], v[98:101]
ds_read_b64_tr_b16 a[244:245], v163 offset:0
v_mfma_f32_16x16x32_bf16 v[110:113], a[60:63], v[170:173], v[110:113]
global_load_lds_dwordx4 v8, s[40:41] offset:-4096
ds_read_b64_tr_b16 a[246:247], v163 offset:2304
v_mfma_f32_16x16x32_bf16 v[114:117], a[64:67], v[170:173], v[114:117]
ds_read_b128 v[186:189], v52 offset:6144
v_mfma_f32_16x16x32_bf16 v[118:121], v[170:173], a[68:71], v[118:121]
s_add_u32 s40, s40, 221184
ds_read_b128 v[194:197], v52 offset:24576
v_mfma_f32_16x16x32_bf16 v[134:137], a[236:239], v[170:173], v[134:137]
v_mfma_f32_16x16x32_bf16 v[122:125], a[60:63], v[178:181], v[122:125]
s_addc_u32 s41, s41, 0
v_mfma_f32_16x16x32_bf16 v[126:129], a[64:67], v[178:181], v[126:129]
v_mfma_f32_16x16x32_bf16 v[130:133], v[178:181], a[68:71], v[130:133]
v_mfma_f32_16x16x32_bf16 v[158:161], a[236:239], v[178:181], v[158:161]
s_waitcnt vmcnt(9) lgkmcnt(0)
s_barrier
s_add_u32 m0, s48, 4096
ds_read_b64_tr_b16 a[96:97], v255 offset:18432
v_mfma_f32_16x16x32_bf16 v[86:89], a[72:75], v[106:109], v[86:89]
ds_read_b64_tr_b16 a[98:99], v255 offset:20736
v_mfma_f32_16x16x32_bf16 v[90:93], a[76:79], v[106:109], v[90:93]
global_load_lds_dwordx4 v0, s[40:41] offset:-4096
ds_read_b64_tr_b16 a[100:101], v255 offset:18624
v_mfma_f32_16x16x32_bf16 v[94:97], v[106:109], a[80:83], v[94:97]
ds_read_b64_tr_b16 a[102:103], v255 offset:20928
global_load_lds_dwordx4 v1, s[40:41] offset:-3072
v_mfma_f32_16x16x32_bf16 v[98:101], a[240:243], v[106:109], v[98:101]
ds_read_b64_tr_b16 a[104:105], v255 offset:18816
v_mfma_f32_16x16x32_bf16 v[110:113], a[72:75], v[182:185], v[110:113]
ds_read_b64_tr_b16 a[106:107], v255 offset:21120
global_load_lds_dwordx4 v2, s[40:41] offset:-2048
v_mfma_f32_16x16x32_bf16 v[114:117], a[76:79], v[182:185], v[114:117]
ds_read_b64_tr_b16 a[248:249], v163 offset:18432
v_mfma_f32_16x16x32_bf16 v[118:121], v[182:185], a[80:83], v[118:121]
global_load_lds_dwordx4 v3, s[40:41] offset:-1024
ds_read_b64_tr_b16 a[250:251], v163 offset:20736
v_mfma_f32_16x16x32_bf16 v[134:137], a[240:243], v[182:185], v[134:137]
ds_read_b128 v[166:169], v51 offset:8192
global_load_lds_dwordx4 v4, s[40:41] offset:0
v_mfma_f32_16x16x32_bf16 v[122:125], a[72:75], v[190:193], v[122:125]
ds_read_b128 v[174:177], v51 offset:26624
v_mfma_f32_16x16x32_bf16 v[126:129], a[76:79], v[190:193], v[126:129]
ds_read_b64_tr_b16 a[108:109], v255 offset:36864
global_load_lds_dwordx4 v5, s[40:41] offset:1024
v_mfma_f32_16x16x32_bf16 v[130:133], v[190:193], a[80:83], v[130:133]
ds_read_b64_tr_b16 a[110:111], v255 offset:39168
v_mfma_f32_16x16x32_bf16 v[158:161], a[240:243], v[190:193], v[158:161]
global_load_lds_dwordx4 v6, s[40:41] offset:2048
ds_read_b64_tr_b16 a[112:113], v255 offset:37056
v_mfma_f32_16x16x32_bf16 v[86:89], a[84:87], v[106:109], v[86:89]
ds_read_b64_tr_b16 a[114:115], v255 offset:39360
v_mfma_f32_16x16x32_bf16 v[90:93], a[88:91], v[106:109], v[90:93]
global_load_lds_dwordx4 v7, s[40:41] offset:3072
ds_read_b64_tr_b16 a[116:117], v255 offset:37248
v_mfma_f32_16x16x32_bf16 v[94:97], v[106:109], a[92:95], v[94:97]
s_add_u32 m0, s48, 12288
ds_read_b64_tr_b16 a[118:119], v255 offset:39552
v_mfma_f32_16x16x32_bf16 v[98:101], a[244:247], v[106:109], v[98:101]
ds_read_b64_tr_b16 a[252:253], v163 offset:36864
v_mfma_f32_16x16x32_bf16 v[110:113], a[84:87], v[186:189], v[110:113]
global_load_lds_dwordx4 v8, s[40:41] offset:-4096
ds_read_b64_tr_b16 a[254:255], v163 offset:39168
v_mfma_f32_16x16x32_bf16 v[114:117], a[88:91], v[186:189], v[114:117]
ds_read_b128 v[170:173], v52 offset:8192
v_mfma_f32_16x16x32_bf16 v[118:121], v[186:189], a[92:95], v[118:121]
s_add_u32 s40, s40, 221184
ds_read_b128 v[178:181], v52 offset:26624
v_mfma_f32_16x16x32_bf16 v[134:137], a[244:247], v[186:189], v[134:137]
v_mfma_f32_16x16x32_bf16 v[122:125], a[84:87], v[194:197], v[122:125]
s_addc_u32 s41, s41, 0
v_mfma_f32_16x16x32_bf16 v[126:129], a[88:91], v[194:197], v[126:129]
v_mfma_f32_16x16x32_bf16 v[130:133], v[194:197], a[92:95], v[130:133]
v_mfma_f32_16x16x32_bf16 v[158:161], a[244:247], v[194:197], v[158:161]
s_waitcnt vmcnt(9) lgkmcnt(0)
s_barrier
s_add_u32 m0, s48, 40960
ds_read_b64_tr_b16 a[120:121], v255 offset:55296
v_mfma_f32_16x16x32_bf16 v[86:89], a[96:99], v[106:109], v[86:89]
ds_read_b64_tr_b16 a[122:123], v255 offset:57600
v_mfma_f32_16x16x32_bf16 v[90:93], a[100:103], v[106:109], v[90:93]
global_load_lds_dwordx4 v0, s[40:41] offset:-4096
ds_read_b64_tr_b16 a[124:125], v255 offset:55488
v_mfma_f32_16x16x32_bf16 v[94:97], v[106:109], a[104:107], v[94:97]
ds_read_b64_tr_b16 a[126:127], v255 offset:57792
global_load_lds_dwordx4 v1, s[40:41] offset:-3072
v_mfma_f32_16x16x32_bf16 v[98:101], a[248:251], v[106:109], v[98:101]
ds_read_b64_tr_b16 a[128:129], v255 offset:55680
v_mfma_f32_16x16x32_bf16 v[110:113], a[96:99], v[166:169], v[110:113]
ds_read_b64_tr_b16 a[130:131], v255 offset:57984
global_load_lds_dwordx4 v2, s[40:41] offset:-2048
v_mfma_f32_16x16x32_bf16 v[114:117], a[100:103], v[166:169], v[114:117]
ds_read_b64_tr_b16 v[54:55], v163 offset:55296
v_mfma_f32_16x16x32_bf16 v[118:121], v[166:169], a[104:107], v[118:121]
global_load_lds_dwordx4 v3, s[40:41] offset:-1024
ds_read_b64_tr_b16 v[56:57], v163 offset:57600
v_mfma_f32_16x16x32_bf16 v[134:137], a[248:251], v[166:169], v[134:137]
ds_read_b128 v[182:185], v51 offset:10240
global_load_lds_dwordx4 v4, s[40:41] offset:0
v_mfma_f32_16x16x32_bf16 v[122:125], a[96:99], v[174:177], v[122:125]
ds_read_b128 v[190:193], v51 offset:28672
v_mfma_f32_16x16x32_bf16 v[126:129], a[100:103], v[174:177], v[126:129]
ds_read_b64_tr_b16 a[132:133], v162 offset:8192
global_load_lds_dwordx4 v5, s[40:41] offset:1024
v_mfma_f32_16x16x32_bf16 v[130:133], v[174:177], a[104:107], v[130:133]
ds_read_b64_tr_b16 a[134:135], v162 offset:10496
v_mfma_f32_16x16x32_bf16 v[158:161], a[248:251], v[174:177], v[158:161]
global_load_lds_dwordx4 v6, s[40:41] offset:2048
ds_read_b64_tr_b16 a[136:137], v162 offset:8384
v_mfma_f32_16x16x32_bf16 v[86:89], a[108:111], v[106:109], v[86:89]
ds_read_b64_tr_b16 a[138:139], v162 offset:10688
v_mfma_f32_16x16x32_bf16 v[90:93], a[112:115], v[106:109], v[90:93]
global_load_lds_dwordx4 v7, s[40:41] offset:3072
ds_read_b64_tr_b16 a[140:141], v162 offset:8576
v_mfma_f32_16x16x32_bf16 v[94:97], v[106:109], a[116:119], v[94:97]
s_add_u32 m0, s48, 49152
ds_read_b64_tr_b16 a[142:143], v162 offset:10880
v_mfma_f32_16x16x32_bf16 v[98:101], a[252:255], v[106:109], v[98:101]
ds_read_b64_tr_b16 v[58:59], v164 offset:8192
v_mfma_f32_16x16x32_bf16 v[110:113], a[108:111], v[170:173], v[110:113]
global_load_lds_dwordx4 v8, s[40:41] offset:-4096
ds_read_b64_tr_b16 v[60:61], v164 offset:10496
v_mfma_f32_16x16x32_bf16 v[114:117], a[112:115], v[170:173], v[114:117]
ds_read_b128 v[186:189], v52 offset:10240
v_mfma_f32_16x16x32_bf16 v[118:121], v[170:173], a[116:119], v[118:121]
s_add_u32 s40, s40, 221184
ds_read_b128 v[194:197], v52 offset:28672
v_mfma_f32_16x16x32_bf16 v[134:137], a[252:255], v[170:173], v[134:137]
v_mfma_f32_16x16x32_bf16 v[122:125], a[108:111], v[178:181], v[122:125]
s_addc_u32 s41, s41, 0
v_mfma_f32_16x16x32_bf16 v[126:129], a[112:115], v[178:181], v[126:129]
)"
R"(v_mfma_f32_16x16x32_bf16 v[130:133], v[178:181], a[116:119], v[130:133]
v_mfma_f32_16x16x32_bf16 v[158:161], a[252:255], v[178:181], v[158:161]
s_waitcnt vmcnt(9) lgkmcnt(0)
s_barrier
s_add_u32 m0, s48, 77824
ds_read_b64_tr_b16 a[144:145], v9 offset:47104
v_mfma_f32_16x16x32_bf16 v[86:89], a[120:123], v[106:109], v[86:89]
ds_read_b64_tr_b16 a[146:147], v9 offset:49408
v_mfma_f32_16x16x32_bf16 v[90:93], a[124:127], v[106:109], v[90:93]
global_load_lds_dwordx4 v0, s[40:41] offset:-4096
ds_read_b64_tr_b16 a[148:149], v9 offset:47296
v_mfma_f32_16x16x32_bf16 v[94:97], v[106:109], a[128:131], v[94:97]
ds_read_b64_tr_b16 a[150:151], v9 offset:49600
global_load_lds_dwordx4 v1, s[40:41] offset:-3072
v_mfma_f32_16x16x32_bf16 v[98:101], v[54:57], v[106:109], v[98:101]
ds_read_b64_tr_b16 a[152:153], v9 offset:47488
v_mfma_f32_16x16x32_bf16 v[110:113], a[120:123], v[182:185], v[110:113]
ds_read_b64_tr_b16 a[154:155], v9 offset:49792
global_load_lds_dwordx4 v2, s[40:41] offset:-2048
v_mfma_f32_16x16x32_bf16 v[114:117], a[124:127], v[182:185], v[114:117]
ds_read_b64_tr_b16 v[62:63], v10 offset:47104
v_mfma_f32_16x16x32_bf16 v[118:121], v[182:185], a[128:131], v[118:121]
global_load_lds_dwordx4 v3, s[40:41] offset:-1024
ds_read_b64_tr_b16 v[64:65], v10 offset:49408
v_mfma_f32_16x16x32_bf16 v[134:137], v[54:57], v[182:185], v[134:137]
ds_read_b128 v[166:169], v51 offset:12288
global_load_lds_dwordx4 v4, s[40:41] offset:0
v_mfma_f32_16x16x32_bf16 v[122:125], a[120:123], v[190:193], v[122:125]
ds_read_b128 v[174:177], v51 offset:30720
v_mfma_f32_16x16x32_bf16 v[126:129], a[124:127], v[190:193], v[126:129]
ds_read_b64_tr_b16 a[156:157], v255 offset:0
global_load_lds_dwordx4 v5, s[40:41] offset:1024
v_mfma_f32_16x16x32_bf16 v[130:133], v[190:193], a[128:131], v[130:133]
ds_read_b64_tr_b16 a[158:159], v255 offset:2304
v_mfma_f32_16x16x32_bf16 v[158:161], v[54:57], v[190:193], v[158:161]
global_load_lds_dwordx4 v6, s[40:41] offset:2048
ds_read_b64_tr_b16 a[160:161], v255 offset:192
v_mfma_f32_16x16x32_bf16 v[86:89], a[132:135], v[106:109], v[86:89]
ds_read_b64_tr_b16 a[162:163], v255 offset:2496
v_mfma_f32_16x16x32_bf16 v[90:93], a[136:139], v[106:109], v[90:93]
global_load_lds_dwordx4 v7, s[40:41] offset:3072
ds_read_b64_tr_b16 a[164:165], v255 offset:384
v_mfma_f32_16x16x32_bf16 v[94:97], v[106:109], a[140:143], v[94:97]
s_add_u32 m0, s48, 86016
ds_read_b64_tr_b16 a[166:167], v255 offset:2688
v_mfma_f32_16x16x32_bf16 v[98:101], v[58:61], v[106:109], v[98:101]
ds_read_b64_tr_b16 v[66:67], v163 offset:0
v_mfma_f32_16x16x32_bf16 v[110:113], a[132:135], v[186:189], v[110:113]
global_load_lds_dwordx4 v8, s[40:41] offset:-4096
ds_read_b64_tr_b16 v[68:69], v163 offset:2304
v_mfma_f32_16x16x32_bf16 v[114:117], a[136:139], v[186:189], v[114:117]
ds_read_b128 v[170:173], v52 offset:12288
v_mfma_f32_16x16x32_bf16 v[118:121], v[186:189], a[140:143], v[118:121]
s_sub_u32 s40, s40, 1769472
ds_read_b128 v[178:181], v52 offset:30720
v_mfma_f32_16x16x32_bf16 v[134:137], v[58:61], v[186:189], v[134:137]
v_mfma_f32_16x16x32_bf16 v[122:125], a[132:135], v[194:197], v[122:125]
s_subb_u32 s41, s41, 0
v_mfma_f32_16x16x32_bf16 v[126:129], a[136:139], v[194:197], v[126:129]
v_mfma_f32_16x16x32_bf16 v[130:133], v[194:197], a[140:143], v[130:133]
v_mfma_f32_16x16x32_bf16 v[158:161], v[58:61], v[194:197], v[158:161]
s_waitcnt vmcnt(9) lgkmcnt(0)
s_barrier
s_add_u32 m0, s49, 0
ds_read_b64_tr_b16 a[168:169], v255 offset:18432
v_mfma_f32_16x16x32_bf16 v[86:89], a[144:147], v[106:109], v[86:89]
ds_read_b64_tr_b16 a[170:171], v255 offset:20736
v_mfma_f32_16x16x32_bf16 v[90:93], a[148:151], v[106:109], v[90:93]
ds_read_b64_tr_b16 a[172:173], v255 offset:18624
v_mfma_f32_16x16x32_bf16 v[94:97], v[106:109], a[152:155], v[94:97]
ds_read_b64_tr_b16 a[174:175], v255 offset:20928
global_load_lds_dwordx4 v11, s[42:43] offset:-2048 sc1
v_mfma_f32_16x16x32_bf16 v[98:101], v[62:65], v[106:109], v[98:101]
ds_read_b64_tr_b16 a[176:177], v255 offset:18816
v_mfma_f32_16x16x32_bf16 v[110:113], a[144:147], v[166:169], v[110:113]
ds_read_b64_tr_b16 a[178:179], v255 offset:21120
v_mfma_f32_16x16x32_bf16 v[114:117], a[148:151], v[166:169], v[114:117]
ds_read_b64_tr_b16 v[70:71], v163 offset:18432
global_load_lds_dwordx4 v12, s[42:43] offset:-1024 sc1
v_mfma_f32_16x16x32_bf16 v[118:121], v[166:169], a[152:155], v[118:121]
ds_read_b64_tr_b16 v[72:73], v163 offset:20736
v_mfma_f32_16x16x32_bf16 v[134:137], v[62:65], v[166:169], v[134:137]
ds_read_b128 v[182:185], v51 offset:14336
v_mfma_f32_16x16x32_bf16 v[122:125], a[144:147], v[174:177], v[122:125]
ds_read_b128 v[190:193], v51 offset:32768
global_load_lds_dwordx4 v13, s[42:43] offset:0 sc1
v_mfma_f32_16x16x32_bf16 v[126:129], a[148:151], v[174:177], v[126:129]
ds_read_b64_tr_b16 a[180:181], v255 offset:36864
v_mfma_f32_16x16x32_bf16 v[130:133], v[174:177], a[152:155], v[130:133]
ds_read_b64_tr_b16 a[182:183], v255 offset:39168
v_mfma_f32_16x16x32_bf16 v[158:161], v[62:65], v[174:177], v[158:161]
ds_read_b64_tr_b16 a[184:185], v255 offset:37056
v_mfma_f32_16x16x32_bf16 v[86:89], a[156:159], v[106:109], v[86:89]
global_load_lds_dwordx4 v14, s[42:43] offset:1024 sc1
ds_read_b64_tr_b16 a[186:187], v255 offset:39360
v_mfma_f32_16x16x32_bf16 v[90:93], a[160:163], v[106:109], v[90:93]
ds_read_b64_tr_b16 a[188:189], v255 offset:37248
v_mfma_f32_16x16x32_bf16 v[94:97], v[106:109], a[164:167], v[94:97]
ds_read_b64_tr_b16 a[190:191], v255 offset:39552
v_mfma_f32_16x16x32_bf16 v[98:101], v[66:69], v[106:109], v[98:101]
global_load_lds_dwordx4 v15, s[42:43] offset:2048 sc1
ds_read_b64_tr_b16 v[74:75], v163 offset:36864
v_mfma_f32_16x16x32_bf16 v[110:113], a[156:159], v[170:173], v[110:113]
ds_read_b64_tr_b16 v[76:77], v163 offset:39168
v_mfma_f32_16x16x32_bf16 v[114:117], a[160:163], v[170:173], v[114:117]
ds_read_b128 v[186:189], v52 offset:14336
v_mfma_f32_16x16x32_bf16 v[118:121], v[170:173], a[164:167], v[118:121]
s_sub_u32 s42, s42, 18432
s_subb_u32 s43, s43, 0
ds_read_b128 v[194:197], v52 offset:32768
v_mfma_f32_16x16x32_bf16 v[134:137], v[66:69], v[170:173], v[134:137]
v_mfma_f32_16x16x32_bf16 v[122:125], a[156:159], v[178:181], v[122:125]
v_mfma_f32_16x16x32_bf16 v[126:129], a[160:163], v[178:181], v[126:129]
v_mfma_f32_16x16x32_bf16 v[130:133], v[178:181], a[164:167], v[130:133]
v_mfma_f32_16x16x32_bf16 v[158:161], v[66:69], v[178:181], v[158:161]
s_waitcnt vmcnt(5) lgkmcnt(0)
s_barrier
s_add_u32 m0, s49, 18432
ds_read_b64_tr_b16 a[192:193], v255 offset:55296
v_mfma_f32_16x16x32_bf16 v[86:89], a[168:171], v[106:109], v[86:89]
ds_read_b64_tr_b16 a[194:195], v255 offset:57600
v_mfma_f32_16x16x32_bf16 v[90:93], a[172:175], v[106:109], v[90:93]
ds_read_b64_tr_b16 a[196:197], v255 offset:55488
v_mfma_f32_16x16x32_bf16 v[94:97], v[106:109], a[176:179], v[94:97]
ds_read_b64_tr_b16 a[198:199], v255 offset:57792
global_load_lds_dwordx4 v11, s[42:43] offset:-2048 sc1
v_mfma_f32_16x16x32_bf16 v[98:101], v[70:73], v[106:109], v[98:101]
ds_read_b64_tr_b16 a[200:201], v255 offset:55680
v_mfma_f32_16x16x32_bf16 v[110:113], a[168:171], v[182:185], v[110:113]
ds_read_b64_tr_b16 a[202:203], v255 offset:57984
v_mfma_f32_16x16x32_bf16 v[114:117], a[172:175], v[182:185], v[114:117]
ds_read_b64_tr_b16 v[78:79], v163 offset:55296
global_load_lds_dwordx4 v12, s[42:43] offset:-1024 sc1
v_mfma_f32_16x16x32_bf16 v[118:121], v[182:185], a[176:179], v[118:121]
ds_read_b64_tr_b16 v[80:81], v163 offset:57600
v_mfma_f32_16x16x32_bf16 v[134:137], v[70:73], v[182:185], v[134:137]
ds_read_b128 v[166:169], v51 offset:16384
v_mfma_f32_16x16x32_bf16 v[122:125], a[168:171], v[190:193], v[122:125]
ds_read_b128 v[174:177], v51 offset:34816
global_load_lds_dwordx4 v13, s[42:43] offset:0 sc1
v_mfma_f32_16x16x32_bf16 v[126:129], a[172:175], v[190:193], v[126:129]
ds_read_b64_tr_b16 a[204:205], v162 offset:8192
v_mfma_f32_16x16x32_bf16 v[130:133], v[190:193], a[176:179], v[130:133]
ds_read_b64_tr_b16 a[206:207], v162 offset:10496
v_mfma_f32_16x16x32_bf16 v[158:161], v[70:73], v[190:193], v[158:161]
ds_read_b64_tr_b16 a[208:209], v162 offset:8384
global_load_lds_dwordx4 v14, s[42:43] offset:1024 sc1
v_mfma_f32_16x16x32_bf16 v[86:89], a[180:183], v[106:109], v[86:89]
ds_read_b64_tr_b16 a[210:211], v162 offset:10688
v_mfma_f32_16x16x32_bf16 v[90:93], a[184:187], v[106:109], v[90:93]
ds_read_b64_tr_b16 a[212:213], v162 offset:8576
v_mfma_f32_16x16x32_bf16 v[94:97], v[106:109], a[188:191], v[94:97]
ds_read_b64_tr_b16 a[214:215], v162 offset:10880
global_load_lds_dwordx4 v15, s[42:43] offset:2048 sc1
v_mfma_f32_16x16x32_bf16 v[98:101], v[74:77], v[106:109], v[98:101]
ds_read_b64_tr_b16 v[82:83], v164 offset:8192
v_mfma_f32_16x16x32_bf16 v[110:113], a[180:183], v[186:189], v[110:113]
ds_read_b64_tr_b16 v[84:85], v164 offset:10496
v_mfma_f32_16x16x32_bf16 v[114:117], a[184:187], v[186:189], v[114:117]
ds_read_b128 v[170:173], v52 offset:16384
s_sub_u32 s42, s42, 18432
s_subb_u32 s43, s43, 0
v_mfma_f32_16x16x32_bf16 v[118:121], v[186:189], a[188:191], v[118:121]
ds_read_b128 v[178:181], v52 offset:34816
v_mfma_f32_16x16x32_bf16 v[134:137], v[74:77], v[186:189], v[134:137]
v_mfma_f32_16x16x32_bf16 v[122:125], a[180:183], v[194:197], v[122:125]
v_mfma_f32_16x16x32_bf16 v[126:129], a[184:187], v[194:197], v[126:129]
v_mfma_f32_16x16x32_bf16 v[158:161], v[74:77], v[194:197], v[158:161]
s_waitcnt lgkmcnt(15)
v_mfma_f32_16x16x32_bf16 v[86:89], a[192:195], v[106:109], v[86:89]
v_mfma_f32_16x16x32_bf16 v[90:93], a[196:199], v[106:109], v[90:93]
s_waitcnt lgkmcnt(14)
v_mfma_f32_16x16x32_bf16 v[94:97], v[106:109], a[200:203], v[94:97]
s_waitcnt lgkmcnt(12)
v_mfma_f32_16x16x32_bf16 v[98:101], v[78:81], v[106:109], v[98:101]
s_waitcnt lgkmcnt(11)
v_mfma_f32_16x16x32_bf16 v[110:113], a[192:195], v[166:169], v[110:113]
v_mfma_f32_16x16x32_bf16 v[114:117], a[196:199], v[166:169], v[114:117]
v_mfma_f32_16x16x32_bf16 v[118:121], v[166:169], a[200:203], v[118:121]
v_mfma_f32_16x16x32_bf16 v[134:137], v[78:81], v[166:169], v[134:137]
s_waitcnt lgkmcnt(10)
v_mfma_f32_16x16x32_bf16 v[158:161], v[78:81], v[174:177], v[158:161]
s_waitcnt lgkmcnt(8)
v_mfma_f32_16x16x32_bf16 v[86:89], a[204:207], v[106:109], v[86:89]
s_waitcnt lgkmcnt(6)
v_mfma_f32_16x16x32_bf16 v[90:93], a[208:211], v[106:109], v[90:93]
s_waitcnt lgkmcnt(4)
v_mfma_f32_16x16x32_bf16 v[94:97], v[106:109], a[212:215], v[94:97]
s_waitcnt lgkmcnt(2)
v_mfma_f32_16x16x32_bf16 v[98:101], v[82:85], v[106:109], v[98:101]
s_waitcnt lgkmcnt(1)
v_mfma_f32_16x16x32_bf16 v[110:113], a[204:207], v[170:173], v[110:113]
v_mfma_f32_16x16x32_bf16 v[114:117], a[208:211], v[170:173], v[114:117]
v_mfma_f32_16x16x32_bf16 v[118:121], v[170:173], a[212:215], v[118:121]
v_mfma_f32_16x16x32_bf16 v[134:137], v[82:85], v[170:173], v[134:137]
s_waitcnt lgkmcnt(0)
v_mfma_f32_16x16x32_bf16 v[158:161], v[82:85], v[178:181], v[158:161]
s_barrier
ds_read_b32 v242, v24 offset:0
s_waitcnt lgkmcnt(0)
s_nop 2
v_fma_f32 v230, v98, v242, v134
v_fma_f32 v231, v99, v242, v135
v_fma_f32 v232, v100, v242, v136
v_fma_f32 v233, v101, v242, v137
v_cvt_pk_bf16_f32 v230, v230, v231
v_cvt_pk_bf16_f32 v231, v232, v233
ds_write_b64 v22, v[230:231] offset:40960
ds_read_b128 v[198:201], v51 offset:32768
ds_read_b128 v[202:205], v52 offset:32768
ds_read_b128 v[206:209], v51 offset:34816
ds_read_b128 v[210:213], v52 offset:34816
v_mov_b32 v234, v32
v_mov_b32 v235, v33
v_mov_b32 v236, v34
v_mov_b32 v237, v35
v_mov_b32 v134, v158
v_mov_b32 v135, v159
v_mov_b32 v136, v160
v_mov_b32 v137, v161
s_waitcnt lgkmcnt(4)
s_barrier
ds_read_b32 v23, v24 offset:0
ds_read_b32 v148, v21
ds_read_b32 v26, v24 offset:192
ds_read_b32 v53, v27
ds_read_b32 v8, v28
ds_read_b128 v[226:229], v25 offset:0
ds_read_b64 v[158:159], v22 offset:41472
ds_read_b32 v140, v20
v_xor_b32 v51, 0x10000, v51
v_xor_b32 v52, 0x10000, v52
s_waitcnt lgkmcnt(7)
v_fma_f32 v238, v90, v23, v114
v_fma_f32 v239, v91, v23, v115
v_fma_f32 v240, v92, v23, v116
v_fma_f32 v241, v93, v23, v117
v_cvt_pk_bf16_f32 v146, v238, v239
v_cvt_pk_bf16_f32 v147, v240, v241
s_waitcnt lgkmcnt(0)
s_barrier
ds_read_b32 v242, v24 offset:64
v_fma_f32 v238, v94, v226, v118
v_fma_f32 v239, v94, v227, v119
v_fma_f32 v240, v94, v228, v120
v_fma_f32 v241, v94, v229, v121
v_cvt_pk_bf16_f32 v154, v238, v239
v_cvt_pk_bf16_f32 v155, v240, v241
v_fma_f32 v238, v86, v23, v110
v_fma_f32 v239, v87, v23, v111
v_fma_f32 v240, v88, v23, v112
v_fma_f32 v241, v89, v23, v113
v_cvt_pk_bf16_f32 v138, v238, v239
v_cvt_pk_bf16_f32 v139, v240, v241
s_waitcnt lgkmcnt(0)
v_fma_f32 v230, v98, v242, v134
v_fma_f32 v231, v99, v242, v135
v_fma_f32 v232, v100, v242, v136
v_fma_f32 v233, v101, v242, v137
v_cvt_pk_bf16_f32 v230, v230, v231
v_cvt_pk_bf16_f32 v231, v232, v233
ds_write_b64 v22, v[230:231] offset:40960
s_waitcnt vmcnt(5) lgkmcnt(0)
s_barrier
s_mov_b32 s70, 32
s_waitcnt lgkmcnt(0)
s_nop 1
s_branch .Lqa_in5
.p2align 6
.Lqa_top15:
s_waitcnt lgkmcnt(9)
v_mfma_f32_16x16x32_bf16 v[134:137], a[216:219], v[44:47], 0
s_waitcnt lgkmcnt(8)
ds_read_b128 v[162:165], v52 offset:4096
ds_read_b32 v8, v30
v_mfma_f32_16x16x32_bf16 v[134:137], a[220:223], v[102:105], v[134:137]
s_waitcnt lgkmcnt(9)
global_store_dwordx4 v16, v[230:233], s[38:39] sc1
v_mfma_f32_16x16x32_bf16 v[110:113], a[0:3], v[44:47], 0
s_waitcnt lgkmcnt(6)
ds_read_b128 v[166:169], v51 offset:6144
v_add_u32 v16, 18432, v16
v_mfma_f32_16x16x32_bf16 v[134:137], a[224:227], v[106:109], v[134:137]
s_waitcnt lgkmcnt(5)
ds_read_b128 v[170:173], v52 offset:6144
ds_read_b128 v[226:229], v25 offset:64
v_mfma_f32_16x16x32_bf16 v[134:137], a[228:231], v[0:3], v[134:137]
ds_read_b64 v[160:161], v22 offset:41472
ds_read_b32 v144, v20
v_mfma_f32_16x16x32_bf16 v[114:117], a[4:7], v[44:47], 0
s_waitcnt lgkmcnt(7)
ds_read_b128 v[174:177], v51 offset:8192
v_fma_f32 v238, v90, v23, v126
v_mfma_f32_16x16x32_bf16 v[134:137], a[232:235], v[4:7], v[134:137]
s_waitcnt lgkmcnt(7)
ds_read_b128 v[178:181], v52 offset:8192
v_fma_f32 v239, v91, v23, v127
v_mfma_f32_16x16x32_bf16 v[134:137], a[236:239], v[162:165], v[134:137]
v_fma_f32 v240, v92, v23, v128
v_fma_f32 v241, v93, v23, v129
v_mfma_f32_16x16x32_bf16 v[118:121], v[44:47], a[8:11], 0
s_waitcnt lgkmcnt(6)
ds_read_b128 v[182:185], v51 offset:10240
v_cvt_pk_bf16_f32 v150, v238, v239
v_mfma_f32_16x16x32_bf16 v[134:137], a[240:243], v[166:169], v[134:137]
s_waitcnt lgkmcnt(6)
ds_read_b128 v[186:189], v52 offset:10240
v_cvt_pk_bf16_f32 v151, v240, v241
v_mfma_f32_16x16x32_bf16 v[134:137], a[244:247], v[170:173], v[134:137]
s_nop 0
v_mfma_f32_16x16x32_bf16 v[214:217], v[150:153], v[138:141], v[32:35]
v_max_f32 v48, v218, v219
v_max_f32 v49, v220, v221
v_mfma_f32_16x16x32_bf16 v[110:113], a[12:15], v[102:105], v[110:113]
s_waitcnt lgkmcnt(6)
ds_read_b128 v[190:193], v51 offset:12288
v_fma_f32 v238, v94, v226, v130
s_waitcnt lgkmcnt(4)
v_mfma_f32_16x16x32_bf16 v[134:137], a[248:251], v[174:177], v[134:137]
s_xor_b32 s49, s49, 0x10000
ds_read_b128 v[194:197], v52 offset:12288
s_waitcnt lgkmcnt(4)
v_mfma_f32_16x16x32_bf16 v[134:137], a[252:255], v[178:181], v[134:137]
s_add_u32 m0, s49, 0
v_mul_f32 v26, 0x3eae1a93, v26
v_mul_f32 v9, v53, v26
v_mfma_f32_16x16x32_bf16 v[114:117], a[16:19], v[102:105], v[114:117]
s_waitcnt lgkmcnt(3)
ds_read_b128 v[198:201], v51 offset:14336
global_load_lds_dwordx4 v11, s[42:43] offset:-2048 sc1
v_mfma_f32_16x16x32_bf16 v[134:137], v[54:57], v[182:185], v[134:137]
s_waitcnt lgkmcnt(3)
ds_read_b128 v[202:205], v52 offset:14336
v_mul_f32 v10, v8, v26
v_mfma_f32_16x16x32_bf16 v[134:137], v[58:61], v[186:189], v[134:137]
v_max3_f32 v48, v214, v215, v48
v_max3_f32 v49, v216, v217, v49
v_mfma_f32_16x16x32_bf16 v[118:121], v[102:105], a[20:23], v[118:121]
s_waitcnt lgkmcnt(3)
ds_read_b128 v[206:209], v51 offset:16384
v_mul_f32 v48, v48, v9
v_mfma_f32_16x16x32_bf16 v[134:137], v[62:65], v[190:193], v[134:137]
s_waitcnt lgkmcnt(3)
ds_read_b128 v[210:213], v52 offset:16384
v_mul_f32 v49, v49, v10
v_mfma_f32_16x16x32_bf16 v[134:137], v[66:69], v[194:197], v[134:137]
v_max_f32 v50, v48, v49
v_mov_b32 v95, v50
v_mfma_f32_16x16x32_bf16 v[110:113], a[24:27], v[106:109], v[110:113]
s_waitcnt lgkmcnt(3)
v_permlane16_swap_b32 v50, v95
v_mfma_f32_16x16x32_bf16 v[134:137], v[70:73], v[198:201], v[134:137]
s_waitcnt lgkmcnt(2)
v_max_f32 v50, v50, v95
v_mov_b32 v95, v50
v_mfma_f32_16x16x32_bf16 v[134:137], v[74:77], v[202:205], v[134:137]
global_load_lds_dwordx4 v12, s[42:43] offset:-1024 sc1
v_fma_f32 v239, v94, v227, v131
v_mfma_f32_16x16x32_bf16 v[114:117], a[28:31], v[106:109], v[114:117]
s_waitcnt lgkmcnt(1)
v_permlane32_swap_b32 v50, v95
v_mfma_f32_16x16x32_bf16 v[134:137], v[78:81], v[206:209], v[134:137]
s_waitcnt lgkmcnt(0)
v_max_f32 v50, v50, v95
v_sub_f32 v48, v48, v50
v_mfma_f32_16x16x32_bf16 v[134:137], v[82:85], v[210:213], v[134:137]
v_sub_f32 v49, v49, v50
v_fma_f32 v240, v94, v228, v132
v_mfma_f32_16x16x32_bf16 v[118:121], v[106:109], a[32:35], v[118:121]
v_exp_f32 v96, v48
v_mfma_f32_16x16x32_bf16 v[110:113], a[36:39], v[0:3], v[110:113]
v_exp_f32 v97, v49
v_mfma_f32_16x16x32_bf16 v[114:117], a[40:43], v[0:3], v[114:117]
v_add_f32 v50, v96, v97
v_mov_b32 v95, v50
v_mfma_f32_16x16x32_bf16 v[118:121], v[0:3], a[44:47], v[118:121]
v_mul_f32 v96, v96, v53
v_fma_f32 v241, v94, v229, v133
v_mfma_f32_16x16x32_bf16 v[110:113], a[48:51], v[4:7], v[110:113]
global_load_lds_dwordx4 v13, s[42:43] offset:0 sc1
v_cvt_pk_bf16_f32 v156, v238, v239
v_mfma_f32_16x16x32_bf16 v[114:117], a[52:55], v[4:7], v[114:117]
v_permlane16_swap_b32 v50, v95
v_mfma_f32_16x16x32_bf16 v[118:121], v[4:7], a[56:59], v[118:121]
v_add_f32 v50, v50, v95
v_mov_b32 v95, v50
v_mfma_f32_16x16x32_bf16 v[110:113], a[60:63], v[162:165], v[110:113]
v_mul_f32 v97, v97, v8
v_cvt_pk_bf16_f32 v157, v240, v241
v_mfma_f32_16x16x32_bf16 v[114:117], a[64:67], v[162:165], v[114:117]
v_permlane32_swap_b32 v50, v95
v_mfma_f32_16x16x32_bf16 v[118:121], v[162:165], a[68:71], v[118:121]
v_mfma_f32_16x16x32_bf16 v[110:113], a[72:75], v[166:169], v[110:113]
s_barrier
v_mfma_f32_16x16x32_bf16 v[114:117], a[76:79], v[166:169], v[114:117]
ds_read_b32 v242, v24 offset:0
v_add_f32 v50, v50, v95
v_mfma_f32_16x16x32_bf16 v[118:121], v[166:169], a[80:83], v[118:121]
global_load_lds_dwordx4 v14, s[42:43] offset:1024 sc1
v_fma_f32 v238, v86, v23, v122
)"
R"(v_mfma_f32_16x16x32_bf16 v[110:113], a[84:87], v[170:173], v[110:113]
v_rcp_f32 v50, v50
v_mfma_f32_16x16x32_bf16 v[114:117], a[88:91], v[170:173], v[114:117]
v_mul_f32 v96, v96, v50
v_mul_f32 v97, v97, v50
v_mfma_f32_16x16x32_bf16 v[118:121], v[170:173], a[92:95], v[118:121]
v_cvt_pk_bf16_f32 v96, v96, v96
v_cvt_pk_bf16_f32 v97, v97, v97
v_mfma_f32_16x16x32_bf16 v[110:113], a[96:99], v[174:177], v[110:113]
v_and_b32 v224, v96, v40
v_and_b32 v225, v97, v41
v_mfma_f32_16x16x32_bf16 v[114:117], a[100:103], v[174:177], v[114:117]
v_and_b32 v222, v96, v42
v_and_b32 v223, v97, v43
v_mfma_f32_16x16x32_bf16 v[118:121], v[174:177], a[104:107], v[118:121]
s_nop 0
v_mfma_f32_16x16x32_bf16 v[218:221], v[154:157], v[222:225], 0
v_mfma_f32_16x16x32_bf16 v[214:217], v[158:161], v[222:225], 0
s_waitcnt lgkmcnt(0)
v_fma_f32 v230, v98, v242, v134
v_fma_f32 v231, v99, v242, v135
v_mfma_f32_16x16x32_bf16 v[110:113], a[108:111], v[178:181], v[110:113]
v_fma_f32 v232, v100, v242, v136
v_fma_f32 v233, v101, v242, v137
v_mfma_f32_16x16x32_bf16 v[114:117], a[112:115], v[178:181], v[114:117]
global_load_lds_dwordx4 v15, s[42:43] offset:2048 sc1
s_add_u32 s42, s42, s46
s_addc_u32 s43, s43, s47
v_mfma_f32_16x16x32_bf16 v[118:121], v[178:181], a[116:119], v[118:121]
v_cvt_pk_bf16_f32 v230, v230, v231
v_cvt_pk_bf16_f32 v231, v232, v233
v_mfma_f32_16x16x32_bf16 v[110:113], a[120:123], v[182:185], v[110:113]
v_cvt_pk_bf16_f32 v254, v218, v219
v_cvt_pk_bf16_f32 v255, v220, v221
v_mfma_f32_16x16x32_bf16 v[114:117], a[124:127], v[182:185], v[114:117]
ds_write_b64 v18, v[254:255]
v_mfma_f32_16x16x32_bf16 v[118:121], v[182:185], a[128:131], v[118:121]
v_cvt_pk_bf16_f32 v96, v214, v215
v_fma_f32 v239, v87, v23, v123
v_mfma_f32_16x16x32_bf16 v[110:113], a[132:135], v[186:189], v[110:113]
ds_write_b32 v19, v96
v_mfma_f32_16x16x32_bf16 v[114:117], a[136:139], v[186:189], v[114:117]
ds_write_b64 v22, v[230:231] offset:40960
v_mfma_f32_16x16x32_bf16 v[118:121], v[186:189], a[140:143], v[118:121]
v_fma_f32 v240, v88, v23, v124
v_fma_f32 v241, v89, v23, v125
v_mfma_f32_16x16x32_bf16 v[110:113], a[144:147], v[190:193], v[110:113]
v_cvt_pk_bf16_f32 v142, v238, v239
v_cvt_pk_bf16_f32 v143, v240, v241
v_mfma_f32_16x16x32_bf16 v[114:117], a[148:151], v[190:193], v[114:117]
v_mfma_f32_16x16x32_bf16 v[118:121], v[190:193], a[152:155], v[118:121]
v_mfma_f32_16x16x32_bf16 v[110:113], a[156:159], v[194:197], v[110:113]
v_mfma_f32_16x16x32_bf16 v[114:117], a[160:163], v[194:197], v[114:117]
v_mfma_f32_16x16x32_bf16 v[118:121], v[194:197], a[164:167], v[118:121]
v_mfma_f32_16x16x32_bf16 v[110:113], a[168:171], v[198:201], v[110:113]
v_mfma_f32_16x16x32_bf16 v[114:117], a[172:175], v[198:201], v[114:117]
v_mfma_f32_16x16x32_bf16 v[118:121], v[198:201], a[176:179], v[118:121]
v_mfma_f32_16x16x32_bf16 v[110:113], a[180:183], v[202:205], v[110:113]
v_mfma_f32_16x16x32_bf16 v[114:117], a[184:187], v[202:205], v[114:117]
s_waitcnt vmcnt(5) lgkmcnt(0)
s_barrier
v_mfma_f32_16x16x32_bf16 v[118:121], v[202:205], a[188:191], v[118:121]
ds_read_b128 v[44:47], v51 offset:18432
v_mfma_f32_16x16x32_bf16 v[110:113], a[192:195], v[206:209], v[110:113]
ds_read_b128 v[102:105], v52 offset:18432
ds_read_b128 v[230:233], v17 offset:0
v_mfma_f32_16x16x32_bf16 v[114:117], a[196:199], v[206:209], v[114:117]
ds_read_b32 v23, v24 offset:0
ds_read_b32 v148, v21
v_mfma_f32_16x16x32_bf16 v[218:221], v[150:153], v[142:145], v[36:39]
v_mfma_f32_16x16x32_bf16 v[118:121], v[206:209], a[200:203], v[118:121]
ds_read_b128 v[106:109], v51 offset:20480
ds_read_b32 v26, v24 offset:192
v_mfma_f32_16x16x32_bf16 v[110:113], a[204:207], v[210:213], v[110:113]
ds_read_b128 v[0:3], v52 offset:20480
ds_read_b32 v53, v27
v_mfma_f32_16x16x32_bf16 v[114:117], a[208:211], v[210:213], v[114:117]
ds_read_b32 v8, v28
ds_read_b128 v[226:229], v25 offset:0
v_mfma_f32_16x16x32_bf16 v[118:121], v[210:213], a[212:215], v[118:121]
s_waitcnt lgkmcnt(10)
ds_read_b128 v[4:7], v51 offset:22528
ds_read_b64 v[158:159], v22 offset:41472
v_mfma_f32_16x16x32_bf16 v[134:137], a[216:219], v[44:47], 0
s_waitcnt lgkmcnt(11)
ds_read_b128 v[162:165], v52 offset:22528
ds_read_b32 v140, v20
v_mfma_f32_16x16x32_bf16 v[134:137], a[220:223], v[102:105], v[134:137]
s_waitcnt lgkmcnt(12)
global_store_dwordx4 v16, v[230:233], s[38:39] sc1
v_mfma_f32_16x16x32_bf16 v[122:125], a[0:3], v[44:47], 0
s_waitcnt lgkmcnt(9)
ds_read_b128 v[166:169], v51 offset:24576
v_add_u32 v16, 18432, v16
v_mfma_f32_16x16x32_bf16 v[134:137], a[224:227], v[106:109], v[134:137]
s_waitcnt lgkmcnt(8)
ds_read_b128 v[170:173], v52 offset:24576
v_fma_f32 v238, v90, v23, v114
v_mfma_f32_16x16x32_bf16 v[134:137], a[228:231], v[0:3], v[134:137]
v_fma_f32 v239, v91, v23, v115
v_fma_f32 v240, v92, v23, v116
v_mfma_f32_16x16x32_bf16 v[126:129], a[4:7], v[44:47], 0
s_waitcnt lgkmcnt(5)
ds_read_b128 v[174:177], v51 offset:26624
v_fma_f32 v241, v93, v23, v117
v_mfma_f32_16x16x32_bf16 v[134:137], a[232:235], v[4:7], v[134:137]
s_waitcnt lgkmcnt(4)
ds_read_b128 v[178:181], v52 offset:26624
v_cvt_pk_bf16_f32 v146, v238, v239
v_mfma_f32_16x16x32_bf16 v[134:137], a[236:239], v[162:165], v[134:137]
v_cvt_pk_bf16_f32 v147, v240, v241
v_max_f32 v48, v218, v219
v_mfma_f32_16x16x32_bf16 v[130:133], v[44:47], a[8:11], 0
ds_read_b128 v[182:185], v51 offset:28672
v_mfma_f32_16x16x32_bf16 v[214:217], v[146:149], v[142:145], v[234:237]
s_waitcnt lgkmcnt(4)
v_max_f32 v49, v220, v221
v_fma_f32 v238, v94, v226, v118
v_mfma_f32_16x16x32_bf16 v[134:137], a[240:243], v[166:169], v[134:137]
s_waitcnt lgkmcnt(3)
ds_read_b128 v[186:189], v52 offset:28672
v_fma_f32 v239, v94, v227, v119
v_mfma_f32_16x16x32_bf16 v[134:137], a[244:247], v[170:173], v[134:137]
v_mul_f32 v26, 0x3eae1a93, v26
v_mul_f32 v9, v53, v26
v_mfma_f32_16x16x32_bf16 v[122:125], a[12:15], v[102:105], v[122:125]
s_waitcnt lgkmcnt(3)
ds_read_b128 v[190:193], v51 offset:30720
v_mul_f32 v10, v8, v26
v_mfma_f32_16x16x32_bf16 v[134:137], a[248:251], v[174:177], v[134:137]
s_add_u32 m0, s49, 18432
ds_read_b128 v[194:197], v52 offset:30720
s_waitcnt lgkmcnt(4)
v_mfma_f32_16x16x32_bf16 v[134:137], a[252:255], v[178:181], v[134:137]
global_load_lds_dwordx4 v11, s[42:43] offset:-2048 sc1
v_max3_f32 v48, v214, v215, v48
v_mfma_f32_16x16x32_bf16 v[126:129], a[16:19], v[102:105], v[126:129]
s_waitcnt lgkmcnt(3)
ds_read_b128 v[198:201], v51 offset:32768
v_max3_f32 v49, v216, v217, v49
v_mfma_f32_16x16x32_bf16 v[134:137], v[54:57], v[182:185], v[134:137]
s_waitcnt lgkmcnt(3)
ds_read_b128 v[202:205], v52 offset:32768
v_mul_f32 v48, v48, v9
v_mfma_f32_16x16x32_bf16 v[134:137], v[58:61], v[186:189], v[134:137]
v_mul_f32 v49, v49, v10
v_max_f32 v50, v48, v49
v_mfma_f32_16x16x32_bf16 v[130:133], v[102:105], a[20:23], v[130:133]
s_waitcnt lgkmcnt(3)
ds_read_b128 v[206:209], v51 offset:34816
v_mov_b32 v95, v50
v_mfma_f32_16x16x32_bf16 v[134:137], v[62:65], v[190:193], v[134:137]
s_waitcnt lgkmcnt(3)
ds_read_b128 v[210:213], v52 offset:34816
v_fma_f32 v240, v94, v228, v120
v_mfma_f32_16x16x32_bf16 v[134:137], v[66:69], v[194:197], v[134:137]
v_permlane16_swap_b32 v50, v95
v_mfma_f32_16x16x32_bf16 v[122:125], a[24:27], v[106:109], v[122:125]
s_waitcnt lgkmcnt(3)
v_max_f32 v50, v50, v95
v_mov_b32 v95, v50
v_mfma_f32_16x16x32_bf16 v[134:137], v[70:73], v[198:201], v[134:137]
s_waitcnt lgkmcnt(2)
global_load_lds_dwordx4 v12, s[42:43] offset:-1024 sc1
v_fma_f32 v241, v94, v229, v121
v_mfma_f32_16x16x32_bf16 v[134:137], v[74:77], v[202:205], v[134:137]
v_permlane32_swap_b32 v50, v95
v_mfma_f32_16x16x32_bf16 v[126:129], a[28:31], v[106:109], v[126:129]
s_waitcnt lgkmcnt(1)
v_max_f32 v50, v50, v95
v_sub_f32 v48, v48, v50
v_mfma_f32_16x16x32_bf16 v[134:137], v[78:81], v[206:209], v[134:137]
s_waitcnt lgkmcnt(0)
v_sub_f32 v49, v49, v50
v_cvt_pk_bf16_f32 v154, v238, v239
v_mfma_f32_16x16x32_bf16 v[134:137], v[82:85], v[210:213], v[134:137]
v_exp_f32 v96, v48
v_mfma_f32_16x16x32_bf16 v[130:133], v[106:109], a[32:35], v[130:133]
v_exp_f32 v97, v49
v_mfma_f32_16x16x32_bf16 v[122:125], a[36:39], v[0:3], v[122:125]
v_add_f32 v50, v96, v97
v_mov_b32 v95, v50
v_mfma_f32_16x16x32_bf16 v[126:129], a[40:43], v[0:3], v[126:129]
v_mul_f32 v96, v96, v53
v_cvt_pk_bf16_f32 v155, v240, v241
v_mfma_f32_16x16x32_bf16 v[130:133], v[0:3], a[44:47], v[130:133]
global_load_lds_dwordx4 v13, s[42:43] offset:0 sc1
v_fma_f32 v238, v86, v23, v110
v_mfma_f32_16x16x32_bf16 v[122:125], a[48:51], v[4:7], v[122:125]
v_permlane16_swap_b32 v50, v95
v_mfma_f32_16x16x32_bf16 v[126:129], a[52:55], v[4:7], v[126:129]
v_add_f32 v50, v50, v95
v_mov_b32 v95, v50
v_mfma_f32_16x16x32_bf16 v[130:133], v[4:7], a[56:59], v[130:133]
v_mul_f32 v97, v97, v8
v_fma_f32 v239, v87, v23, v111
v_mfma_f32_16x16x32_bf16 v[122:125], a[60:63], v[162:165], v[122:125]
v_permlane32_swap_b32 v50, v95
v_mfma_f32_16x16x32_bf16 v[126:129], a[64:67], v[162:165], v[126:129]
v_add_f32 v50, v50, v95
v_fma_f32 v240, v88, v23, v112
v_mfma_f32_16x16x32_bf16 v[130:133], v[162:165], a[68:71], v[130:133]
v_mfma_f32_16x16x32_bf16 v[122:125], a[72:75], v[166:169], v[122:125]
s_barrier
v_mfma_f32_16x16x32_bf16 v[126:129], a[76:79], v[166:169], v[126:129]
global_load_lds_dwordx4 v14, s[42:43] offset:1024 sc1
ds_read_b32 v242, v24 offset:64
v_mfma_f32_16x16x32_bf16 v[130:133], v[166:169], a[80:83], v[130:133]
v_rcp_f32 v50, v50
v_mfma_f32_16x16x32_bf16 v[122:125], a[84:87], v[170:173], v[122:125]
v_mul_f32 v96, v96, v50
v_mul_f32 v97, v97, v50
v_mfma_f32_16x16x32_bf16 v[126:129], a[88:91], v[170:173], v[126:129]
v_cvt_pk_bf16_f32 v96, v96, v96
v_cvt_pk_bf16_f32 v97, v97, v97
v_mfma_f32_16x16x32_bf16 v[130:133], v[170:173], a[92:95], v[130:133]
v_and_b32 v222, v96, v40
v_and_b32 v223, v97, v41
v_mfma_f32_16x16x32_bf16 v[122:125], a[96:99], v[174:177], v[122:125]
v_and_b32 v224, v96, v42
v_and_b32 v225, v97, v43
v_mfma_f32_16x16x32_bf16 v[126:129], a[100:103], v[174:177], v[126:129]
s_nop 0
v_mfma_f32_16x16x32_bf16 v[218:221], v[154:157], v[222:225], 0
v_mfma_f32_16x16x32_bf16 v[214:217], v[158:161], v[222:225], 0
s_waitcnt lgkmcnt(0)
v_fma_f32 v230, v98, v242, v134
v_fma_f32 v231, v99, v242, v135
v_mfma_f32_16x16x32_bf16 v[130:133], v[174:177], a[104:107], v[130:133]
v_fma_f32 v232, v100, v242, v136
v_fma_f32 v233, v101, v242, v137
v_mfma_f32_16x16x32_bf16 v[122:125], a[108:111], v[178:181], v[122:125]
global_load_lds_dwordx4 v15, s[42:43] offset:2048 sc1
s_sub_u32 s42, s42, 18432
s_subb_u32 s43, s43, 0
v_mfma_f32_16x16x32_bf16 v[126:129], a[112:115], v[178:181], v[126:129]
v_cvt_pk_bf16_f32 v230, v230, v231
v_cvt_pk_bf16_f32 v231, v232, v233
v_mfma_f32_16x16x32_bf16 v[130:133], v[178:181], a[116:119], v[130:133]
v_cvt_pk_bf16_f32 v254, v218, v219
v_cvt_pk_bf16_f32 v255, v220, v221
v_mfma_f32_16x16x32_bf16 v[122:125], a[120:123], v[182:185], v[122:125]
ds_write_b64 v18, v[254:255]
v_mfma_f32_16x16x32_bf16 v[126:129], a[124:127], v[182:185], v[126:129]
v_cvt_pk_bf16_f32 v96, v214, v215
v_fma_f32 v241, v89, v23, v113
v_mfma_f32_16x16x32_bf16 v[130:133], v[182:185], a[128:131], v[130:133]
ds_write_b32 v19, v96
v_mfma_f32_16x16x32_bf16 v[122:125], a[132:135], v[186:189], v[122:125]
ds_write_b64 v22, v[230:231] offset:40960
v_mfma_f32_16x16x32_bf16 v[126:129], a[136:139], v[186:189], v[126:129]
v_cvt_pk_bf16_f32 v138, v238, v239
v_cvt_pk_bf16_f32 v139, v240, v241
v_mfma_f32_16x16x32_bf16 v[130:133], v[186:189], a[140:143], v[130:133]
v_mfma_f32_16x16x32_bf16 v[122:125], a[144:147], v[190:193], v[122:125]
v_mfma_f32_16x16x32_bf16 v[126:129], a[148:151], v[190:193], v[126:129]
v_mfma_f32_16x16x32_bf16 v[130:133], v[190:193], a[152:155], v[130:133]
v_mfma_f32_16x16x32_bf16 v[122:125], a[156:159], v[194:197], v[122:125]
v_mfma_f32_16x16x32_bf16 v[126:129], a[160:163], v[194:197], v[126:129]
v_mfma_f32_16x16x32_bf16 v[130:133], v[194:197], a[164:167], v[130:133]
v_mfma_f32_16x16x32_bf16 v[122:125], a[168:171], v[198:201], v[122:125]
v_mfma_f32_16x16x32_bf16 v[126:129], a[172:175], v[198:201], v[126:129]
v_xor_b32 v51, 0x10000, v51
v_xor_b32 v52, 0x10000, v52
v_mfma_f32_16x16x32_bf16 v[130:133], v[198:201], a[176:179], v[130:133]
v_mfma_f32_16x16x32_bf16 v[122:125], a[180:183], v[202:205], v[122:125]
v_mfma_f32_16x16x32_bf16 v[126:129], a[184:187], v[202:205], v[126:129]
s_waitcnt vmcnt(5) lgkmcnt(0)
s_barrier
.Lqa_in5:
v_mfma_f32_16x16x32_bf16 v[130:133], v[202:205], a[188:191], v[130:133]
s_cmp_eq_u32 s70, s73
s_cbranch_scc1 .Lqa_hk6
.Lqa_hr7:
ds_read_b128 v[44:47], v51 offset:0
v_mfma_f32_16x16x32_bf16 v[122:125], a[192:195], v[206:209], v[122:125]
ds_read_b128 v[102:105], v52 offset:0
ds_read_b128 v[230:233], v17 offset:0
v_mfma_f32_16x16x32_bf16 v[126:129], a[196:199], v[206:209], v[126:129]
ds_read_b32 v23, v24 offset:64
ds_read_b32 v152, v21
v_mfma_f32_16x16x32_bf16 v[218:221], v[146:149], v[138:141], v[36:39]
v_mfma_f32_16x16x32_bf16 v[130:133], v[206:209], a[200:203], v[130:133]
ds_read_b128 v[106:109], v51 offset:2048
ds_read_b32 v26, v24 offset:128
v_mfma_f32_16x16x32_bf16 v[122:125], a[204:207], v[210:213], v[122:125]
ds_read_b128 v[0:3], v52 offset:2048
ds_read_b32 v53, v29
v_mfma_f32_16x16x32_bf16 v[126:129], a[208:211], v[210:213], v[126:129]
v_mfma_f32_16x16x32_bf16 v[130:133], v[210:213], a[212:215], v[130:133]
s_sub_u32 s70, s70, 1
ds_read_b128 v[4:7], v51 offset:4096
s_cbranch_scc0 .Lqa_top15
s_waitcnt lgkmcnt(0)
ds_read_b32 v8, v30
global_store_dwordx4 v16, v[230:233], s[38:39] sc1
v_add_u32 v16, 18432, v16
ds_read_b128 v[226:229], v25 offset:64
ds_read_b64 v[160:161], v22 offset:41472
ds_read_b32 v144, v20
v_fma_f32 v238, v90, v23, v126
v_fma_f32 v239, v91, v23, v127
v_fma_f32 v240, v92, v23, v128
v_fma_f32 v241, v93, v23, v129
v_cvt_pk_bf16_f32 v150, v238, v239
v_cvt_pk_bf16_f32 v151, v240, v241
s_nop 1
v_mfma_f32_16x16x32_bf16 v[214:217], v[150:153], v[138:141], v[32:35]
s_waitcnt lgkmcnt(2)
v_max_f32 v48, v218, v219
v_max_f32 v49, v220, v221
v_fma_f32 v238, v94, v226, v130
v_mul_f32 v26, 0x3eae1a93, v26
v_mul_f32 v9, v53, v26
v_mul_f32 v10, v8, v26
s_nop 0
v_max3_f32 v48, v214, v215, v48
v_max3_f32 v49, v216, v217, v49
v_mul_f32 v48, v48, v9
v_mul_f32 v49, v49, v10
v_max_f32 v50, v48, v49
v_mov_b32 v95, v50
s_nop 1
v_permlane16_swap_b32 v50, v95
v_max_f32 v50, v50, v95
v_mov_b32 v95, v50
v_fma_f32 v239, v94, v227, v131
s_nop 0
v_permlane32_swap_b32 v50, v95
v_max_f32 v50, v50, v95
v_sub_f32 v48, v48, v50
v_sub_f32 v49, v49, v50
v_fma_f32 v240, v94, v228, v132
v_exp_f32 v96, v48
v_exp_f32 v97, v49
s_nop 0
v_add_f32 v50, v96, v97
v_mov_b32 v95, v50
v_mul_f32 v96, v96, v53
v_fma_f32 v241, v94, v229, v133
v_cvt_pk_bf16_f32 v156, v238, v239
v_permlane16_swap_b32 v50, v95
v_add_f32 v50, v50, v95
v_mov_b32 v95, v50
v_mul_f32 v97, v97, v8
v_cvt_pk_bf16_f32 v157, v240, v241
v_permlane32_swap_b32 v50, v95
s_waitcnt lgkmcnt(0)
s_barrier
v_add_f32 v50, v50, v95
v_fma_f32 v238, v86, v23, v122
v_rcp_f32 v50, v50
s_nop 0
v_mul_f32 v96, v96, v50
v_mul_f32 v97, v97, v50
v_cvt_pk_bf16_f32 v96, v96, v96
v_cvt_pk_bf16_f32 v97, v97, v97
v_and_b32 v224, v96, v40
v_and_b32 v225, v97, v41
v_and_b32 v222, v96, v42
v_and_b32 v223, v97, v43
s_nop 1
v_mfma_f32_16x16x32_bf16 v[218:221], v[154:157], v[222:225], 0
v_mfma_f32_16x16x32_bf16 v[214:217], v[158:161], v[222:225], 0
s_nop 6
v_cvt_pk_bf16_f32 v254, v218, v219
v_cvt_pk_bf16_f32 v255, v220, v221
ds_write_b64 v18, v[254:255]
v_cvt_pk_bf16_f32 v96, v214, v215
v_fma_f32 v239, v87, v23, v123
ds_write_b32 v19, v96
v_fma_f32 v240, v88, v23, v124
v_fma_f32 v241, v89, v23, v125
v_cvt_pk_bf16_f32 v142, v238, v239
v_cvt_pk_bf16_f32 v143, v240, v241
s_waitcnt vmcnt(0)
s_waitcnt lgkmcnt(0)
s_barrier
ds_read_b128 v[230:233], v17 offset:0
s_waitcnt lgkmcnt(0)
global_store_dwordx4 v16, v[230:233], s[38:39] sc1
s_waitcnt vmcnt(0)
s_barrier
s_endpgm
.Lqa_w2:
s_add_u32 m0, s48, 4096
s_nop 0
global_load_lds_dwordx4 v0, s[40:41] offset:-4096
global_load_lds_dwordx4 v1, s[40:41] offset:-3072
global_load_lds_dwordx4 v2, s[40:41] offset:-2048
global_load_lds_dwordx4 v3, s[40:41] offset:-1024
global_load_lds_dwordx4 v4, s[40:41] offset:0
global_load_lds_dwordx4 v5, s[40:41] offset:1024
global_load_lds_dwordx4 v6, s[40:41] offset:2048
global_load_lds_dwordx4 v7, s[40:41] offset:3072
s_add_u32 m0, s48, 12288
s_nop 0
global_load_lds_dwordx4 v8, s[40:41] offset:-4096
s_add_u32 s40, s40, 221184
s_addc_u32 s41, s41, 0
)"
R"(s_add_u32 m0, s48, 40960
s_nop 0
global_load_lds_dwordx4 v0, s[40:41] offset:-4096
global_load_lds_dwordx4 v1, s[40:41] offset:-3072
global_load_lds_dwordx4 v2, s[40:41] offset:-2048
global_load_lds_dwordx4 v3, s[40:41] offset:-1024
global_load_lds_dwordx4 v4, s[40:41] offset:0
global_load_lds_dwordx4 v5, s[40:41] offset:1024
global_load_lds_dwordx4 v6, s[40:41] offset:2048
global_load_lds_dwordx4 v7, s[40:41] offset:3072
s_add_u32 m0, s48, 49152
s_nop 0
global_load_lds_dwordx4 v8, s[40:41] offset:-4096
s_add_u32 s40, s40, 221184
s_addc_u32 s41, s41, 0
v_add_u32 v255, 0x10000, v9
v_add_u32 v162, 0x20000, v9
v_add_u32 v163, 0x10000, v10
v_add_u32 v164, 0x20000, v10
v_mov_b32 v106, 0x3F803F80
v_mov_b32 v107, 0x3F803F80
v_mov_b32 v108, 0x3F803F80
v_mov_b32 v109, 0x3F803F80
v_mov_b32 v141, 0
v_mov_b32 v145, 0
v_mov_b32 v149, 0
v_mov_b32 v153, 0
v_mov_b32 v165, 46080
s_waitcnt lgkmcnt(0)
s_mul_i32 s37, s37, 0x9E3779B1
s_add_u32 s37, s37, 0x7F4A7C15
v_add_u32 v46, s37, v46
v_add_u32 v48, s37, v48
v_add_u32 v50, s37, v50
ds_write_b32 v165, v141
s_mov_b64 s[76:77], 0
s_mov_b32 s73, 18
s_waitcnt vmcnt(9) lgkmcnt(0)
s_barrier
s_add_u32 m0, s48, 77824
ds_read_b64_tr_b16 a[0:1], v9 offset:47104
ds_read_b64_tr_b16 a[2:3], v9 offset:49408
global_load_lds_dwordx4 v0, s[40:41] offset:-4096
ds_read_b64_tr_b16 a[4:5], v9 offset:47296
ds_read_b64_tr_b16 a[6:7], v9 offset:49600
global_load_lds_dwordx4 v1, s[40:41] offset:-3072
ds_read_b64_tr_b16 a[8:9], v9 offset:47488
global_load_lds_dwordx4 v2, s[40:41] offset:-2048
ds_read_b64_tr_b16 a[10:11], v9 offset:49792
ds_read_b64_tr_b16 a[216:217], v10 offset:47104
global_load_lds_dwordx4 v3, s[40:41] offset:-1024
ds_read_b64_tr_b16 a[218:219], v10 offset:49408
global_load_lds_dwordx4 v4, s[40:41] offset:0
ds_read_b128 v[166:169], v51 offset:0
ds_read_b128 v[174:177], v51 offset:18432
global_load_lds_dwordx4 v5, s[40:41] offset:1024
ds_read_b64_tr_b16 a[12:13], v255 offset:0
global_load_lds_dwordx4 v6, s[40:41] offset:2048
ds_read_b64_tr_b16 a[14:15], v255 offset:2304
ds_read_b64_tr_b16 a[16:17], v255 offset:192
global_load_lds_dwordx4 v7, s[40:41] offset:3072
ds_read_b64_tr_b16 a[18:19], v255 offset:2496
s_add_u32 m0, s48, 86016
ds_read_b64_tr_b16 a[20:21], v255 offset:384
ds_read_b64_tr_b16 a[22:23], v255 offset:2688
global_load_lds_dwordx4 v8, s[40:41] offset:-4096
ds_read_b64_tr_b16 a[220:221], v163 offset:0
s_add_u32 s40, s40, 221184
ds_read_b64_tr_b16 a[222:223], v163 offset:2304
ds_read_b128 v[170:173], v52 offset:0
s_addc_u32 s41, s41, 0
ds_read_b128 v[178:181], v52 offset:18432
s_waitcnt vmcnt(9) lgkmcnt(0)
s_barrier
s_add_u32 m0, s48, 4096
ds_read_b64_tr_b16 a[24:25], v255 offset:18432
v_mfma_f32_16x16x32_bf16 v[86:89], a[0:3], v[106:109], 0
ds_read_b64_tr_b16 a[26:27], v255 offset:20736
v_mfma_f32_16x16x32_bf16 v[90:93], a[4:7], v[106:109], 0
global_load_lds_dwordx4 v0, s[40:41] offset:-4096
ds_read_b64_tr_b16 a[28:29], v255 offset:18624
v_mfma_f32_16x16x32_bf16 v[94:97], v[106:109], a[8:11], 0
ds_read_b64_tr_b16 a[30:31], v255 offset:20928
global_load_lds_dwordx4 v1, s[40:41] offset:-3072
v_mfma_f32_16x16x32_bf16 v[102:105], v[106:109], a[216:219], 0
ds_read_b64_tr_b16 a[32:33], v255 offset:18816
v_mfma_f32_16x16x32_bf16 v[110:113], a[0:3], v[166:169], 0
ds_read_b64_tr_b16 a[34:35], v255 offset:21120
global_load_lds_dwordx4 v2, s[40:41] offset:-2048
v_mfma_f32_16x16x32_bf16 v[114:117], a[4:7], v[166:169], 0
ds_read_b64_tr_b16 a[224:225], v163 offset:18432
v_mfma_f32_16x16x32_bf16 v[118:121], v[166:169], a[8:11], 0
global_load_lds_dwordx4 v3, s[40:41] offset:-1024
ds_read_b64_tr_b16 a[226:227], v163 offset:20736
v_mfma_f32_16x16x32_bf16 v[134:137], v[166:169], a[216:219], 0
ds_read_b128 v[182:185], v51 offset:2048
global_load_lds_dwordx4 v4, s[40:41] offset:0
v_mfma_f32_16x16x32_bf16 v[122:125], a[0:3], v[174:177], 0
ds_read_b128 v[190:193], v51 offset:20480
v_mfma_f32_16x16x32_bf16 v[126:129], a[4:7], v[174:177], 0
ds_read_b64_tr_b16 a[36:37], v255 offset:36864
global_load_lds_dwordx4 v5, s[40:41] offset:1024
v_mfma_f32_16x16x32_bf16 v[130:133], v[174:177], a[8:11], 0
ds_read_b64_tr_b16 a[38:39], v255 offset:39168
v_mfma_f32_16x16x32_bf16 v[158:161], v[174:177], a[216:219], 0
global_load_lds_dwordx4 v6, s[40:41] offset:2048
ds_read_b64_tr_b16 a[40:41], v255 offset:37056
v_mfma_f32_16x16x32_bf16 v[86:89], a[12:15], v[106:109], v[86:89]
ds_read_b64_tr_b16 a[42:43], v255 offset:39360
v_mfma_f32_16x16x32_bf16 v[90:93], a[16:19], v[106:109], v[90:93]
global_load_lds_dwordx4 v7, s[40:41] offset:3072
ds_read_b64_tr_b16 a[44:45], v255 offset:37248
v_mfma_f32_16x16x32_bf16 v[94:97], v[106:109], a[20:23], v[94:97]
s_add_u32 m0, s48, 12288
ds_read_b64_tr_b16 a[46:47], v255 offset:39552
v_mfma_f32_16x16x32_bf16 v[102:105], v[106:109], a[220:223], v[102:105]
ds_read_b64_tr_b16 a[228:229], v163 offset:36864
v_mfma_f32_16x16x32_bf16 v[110:113], a[12:15], v[170:173], v[110:113]
global_load_lds_dwordx4 v8, s[40:41] offset:-4096
ds_read_b64_tr_b16 a[230:231], v163 offset:39168
v_mfma_f32_16x16x32_bf16 v[114:117], a[16:19], v[170:173], v[114:117]
ds_read_b128 v[186:189], v52 offset:2048
v_mfma_f32_16x16x32_bf16 v[118:121], v[170:173], a[20:23], v[118:121]
s_add_u32 s40, s40, 221184
ds_read_b128 v[194:197], v52 offset:20480
v_mfma_f32_16x16x32_bf16 v[134:137], v[170:173], a[220:223], v[134:137]
v_mfma_f32_16x16x32_bf16 v[122:125], a[12:15], v[178:181], v[122:125]
s_addc_u32 s41, s41, 0
v_mfma_f32_16x16x32_bf16 v[126:129], a[16:19], v[178:181], v[126:129]
v_mfma_f32_16x16x32_bf16 v[130:133], v[178:181], a[20:23], v[130:133]
v_mfma_f32_16x16x32_bf16 v[158:161], v[178:181], a[220:223], v[158:161]
s_waitcnt vmcnt(9) lgkmcnt(0)
s_barrier
s_add_u32 m0, s48, 40960
ds_read_b64_tr_b16 a[48:49], v255 offset:55296
v_mfma_f32_16x16x32_bf16 v[86:89], a[24:27], v[106:109], v[86:89]
ds_read_b64_tr_b16 a[50:51], v255 offset:57600
v_mfma_f32_16x16x32_bf16 v[90:93], a[28:31], v[106:109], v[90:93]
global_load_lds_dwordx4 v0, s[40:41] offset:-4096
ds_read_b64_tr_b16 a[52:53], v255 offset:55488
v_mfma_f32_16x16x32_bf16 v[94:97], v[106:109], a[32:35], v[94:97]
ds_read_b64_tr_b16 a[54:55], v255 offset:57792
global_load_lds_dwordx4 v1, s[40:41] offset:-3072
v_mfma_f32_16x16x32_bf16 v[102:105], v[106:109], a[224:227], v[102:105]
ds_read_b64_tr_b16 a[56:57], v255 offset:55680
v_mfma_f32_16x16x32_bf16 v[110:113], a[24:27], v[182:185], v[110:113]
ds_read_b64_tr_b16 a[58:59], v255 offset:57984
global_load_lds_dwordx4 v2, s[40:41] offset:-2048
v_mfma_f32_16x16x32_bf16 v[114:117], a[28:31], v[182:185], v[114:117]
ds_read_b64_tr_b16 a[232:233], v163 offset:55296
v_mfma_f32_16x16x32_bf16 v[118:121], v[182:185], a[32:35], v[118:121]
global_load_lds_dwordx4 v3, s[40:41] offset:-1024
ds_read_b64_tr_b16 a[234:235], v163 offset:57600
v_mfma_f32_16x16x32_bf16 v[134:137], v[182:185], a[224:227], v[134:137]
ds_read_b128 v[166:169], v51 offset:4096
global_load_lds_dwordx4 v4, s[40:41] offset:0
v_mfma_f32_16x16x32_bf16 v[122:125], a[24:27], v[190:193], v[122:125]
ds_read_b128 v[174:177], v51 offset:22528
v_mfma_f32_16x16x32_bf16 v[126:129], a[28:31], v[190:193], v[126:129]
ds_read_b64_tr_b16 a[60:61], v162 offset:8192
global_load_lds_dwordx4 v5, s[40:41] offset:1024
v_mfma_f32_16x16x32_bf16 v[130:133], v[190:193], a[32:35], v[130:133]
ds_read_b64_tr_b16 a[62:63], v162 offset:10496
v_mfma_f32_16x16x32_bf16 v[158:161], v[190:193], a[224:227], v[158:161]
global_load_lds_dwordx4 v6, s[40:41] offset:2048
ds_read_b64_tr_b16 a[64:65], v162 offset:8384
v_mfma_f32_16x16x32_bf16 v[86:89], a[36:39], v[106:109], v[86:89]
ds_read_b64_tr_b16 a[66:67], v162 offset:10688
v_mfma_f32_16x16x32_bf16 v[90:93], a[40:43], v[106:109], v[90:93]
global_load_lds_dwordx4 v7, s[40:41] offset:3072
ds_read_b64_tr_b16 a[68:69], v162 offset:8576
v_mfma_f32_16x16x32_bf16 v[94:97], v[106:109], a[44:47], v[94:97]
s_add_u32 m0, s48, 49152
ds_read_b64_tr_b16 a[70:71], v162 offset:10880
v_mfma_f32_16x16x32_bf16 v[102:105], v[106:109], a[228:231], v[102:105]
ds_read_b64_tr_b16 a[236:237], v164 offset:8192
v_mfma_f32_16x16x32_bf16 v[110:113], a[36:39], v[186:189], v[110:113]
global_load_lds_dwordx4 v8, s[40:41] offset:-4096
ds_read_b64_tr_b16 a[238:239], v164 offset:10496
v_mfma_f32_16x16x32_bf16 v[114:117], a[40:43], v[186:189], v[114:117]
ds_read_b128 v[170:173], v52 offset:4096
v_mfma_f32_16x16x32_bf16 v[118:121], v[186:189], a[44:47], v[118:121]
s_add_u32 s40, s40, 221184
ds_read_b128 v[178:181], v52 offset:22528
v_mfma_f32_16x16x32_bf16 v[134:137], v[186:189], a[228:231], v[134:137]
v_mfma_f32_16x16x32_bf16 v[122:125], a[36:39], v[194:197], v[122:125]
s_addc_u32 s41, s41, 0
v_mfma_f32_16x16x32_bf16 v[126:129], a[40:43], v[194:197], v[126:129]
v_mfma_f32_16x16x32_bf16 v[130:133], v[194:197], a[44:47], v[130:133]
v_mfma_f32_16x16x32_bf16 v[158:161], v[194:197], a[228:231], v[158:161]
s_waitcnt vmcnt(9) lgkmcnt(0)
s_barrier
s_add_u32 m0, s48, 77824
ds_read_b64_tr_b16 a[72:73], v9 offset:47104
v_mfma_f32_16x16x32_bf16 v[86:89], a[48:51], v[106:109], v[86:89]
ds_read_b64_tr_b16 a[74:75], v9 offset:49408
v_mfma_f32_16x16x32_bf16 v[90:93], a[52:55], v[106:109], v[90:93]
global_load_lds_dwordx4 v0, s[40:41] offset:-4096
ds_read_b64_tr_b16 a[76:77], v9 offset:47296
v_mfma_f32_16x16x32_bf16 v[94:97], v[106:109], a[56:59], v[94:97]
ds_read_b64_tr_b16 a[78:79], v9 offset:49600
global_load_lds_dwordx4 v1, s[40:41] offset:-3072
v_mfma_f32_16x16x32_bf16 v[102:105], v[106:109], a[232:235], v[102:105]
ds_read_b64_tr_b16 a[80:81], v9 offset:47488
v_mfma_f32_16x16x32_bf16 v[110:113], a[48:51], v[166:169], v[110:113]
ds_read_b64_tr_b16 a[82:83], v9 offset:49792
global_load_lds_dwordx4 v2, s[40:41] offset:-2048
v_mfma_f32_16x16x32_bf16 v[114:117], a[52:55], v[166:169], v[114:117]
ds_read_b64_tr_b16 a[240:241], v10 offset:47104
v_mfma_f32_16x16x32_bf16 v[118:121], v[166:169], a[56:59], v[118:121]
global_load_lds_dwordx4 v3, s[40:41] offset:-1024
ds_read_b64_tr_b16 a[242:243], v10 offset:49408
v_mfma_f32_16x16x32_bf16 v[134:137], v[166:169], a[232:235], v[134:137]
ds_read_b128 v[182:185], v51 offset:6144
global_load_lds_dwordx4 v4, s[40:41] offset:0
v_mfma_f32_16x16x32_bf16 v[122:125], a[48:51], v[174:177], v[122:125]
ds_read_b128 v[190:193], v51 offset:24576
v_mfma_f32_16x16x32_bf16 v[126:129], a[52:55], v[174:177], v[126:129]
ds_read_b64_tr_b16 a[84:85], v255 offset:0
global_load_lds_dwordx4 v5, s[40:41] offset:1024
v_mfma_f32_16x16x32_bf16 v[130:133], v[174:177], a[56:59], v[130:133]
ds_read_b64_tr_b16 a[86:87], v255 offset:2304
v_mfma_f32_16x16x32_bf16 v[158:161], v[174:177], a[232:235], v[158:161]
global_load_lds_dwordx4 v6, s[40:41] offset:2048
ds_read_b64_tr_b16 a[88:89], v255 offset:192
v_mfma_f32_16x16x32_bf16 v[86:89], a[60:63], v[106:109], v[86:89]
ds_read_b64_tr_b16 a[90:91], v255 offset:2496
v_mfma_f32_16x16x32_bf16 v[90:93], a[64:67], v[106:109], v[90:93]
global_load_lds_dwordx4 v7, s[40:41] offset:3072
ds_read_b64_tr_b16 a[92:93], v255 offset:384
v_mfma_f32_16x16x32_bf16 v[94:97], v[106:109], a[68:71], v[94:97]
s_add_u32 m0, s48, 86016
ds_read_b64_tr_b16 a[94:95], v255 offset:2688
v_mfma_f32_16x16x32_bf16 v[102:105], v[106:109], a[236:239], v[102:105]
ds_read_b64_tr_b16 a[244:245], v163 offset:0
v_mfma_f32_16x16x32_bf16 v[110:113], a[60:63], v[170:173], v[110:113]
global_load_lds_dwordx4 v8, s[40:41] offset:-4096
ds_read_b64_tr_b16 a[246:247], v163 offset:2304
v_mfma_f32_16x16x32_bf16 v[114:117], a[64:67], v[170:173], v[114:117]
ds_read_b128 v[186:189], v52 offset:6144
v_mfma_f32_16x16x32_bf16 v[118:121], v[170:173], a[68:71], v[118:121]
s_add_u32 s40, s40, 221184
ds_read_b128 v[194:197], v52 offset:24576
v_mfma_f32_16x16x32_bf16 v[134:137], v[170:173], a[236:239], v[134:137]
v_mfma_f32_16x16x32_bf16 v[122:125], a[60:63], v[178:181], v[122:125]
s_addc_u32 s41, s41, 0
v_mfma_f32_16x16x32_bf16 v[126:129], a[64:67], v[178:181], v[126:129]
v_mfma_f32_16x16x32_bf16 v[130:133], v[178:181], a[68:71], v[130:133]
v_mfma_f32_16x16x32_bf16 v[158:161], v[178:181], a[236:239], v[158:161]
s_waitcnt vmcnt(9) lgkmcnt(0)
s_barrier
s_add_u32 m0, s48, 4096
ds_read_b64_tr_b16 a[96:97], v255 offset:18432
v_mfma_f32_16x16x32_bf16 v[86:89], a[72:75], v[106:109], v[86:89]
ds_read_b64_tr_b16 a[98:99], v255 offset:20736
v_mfma_f32_16x16x32_bf16 v[90:93], a[76:79], v[106:109], v[90:93]
global_load_lds_dwordx4 v0, s[40:41] offset:-4096
ds_read_b64_tr_b16 a[100:101], v255 offset:18624
v_mfma_f32_16x16x32_bf16 v[94:97], v[106:109], a[80:83], v[94:97]
ds_read_b64_tr_b16 a[102:103], v255 offset:20928
global_load_lds_dwordx4 v1, s[40:41] offset:-3072
v_mfma_f32_16x16x32_bf16 v[102:105], v[106:109], a[240:243], v[102:105]
ds_read_b64_tr_b16 a[104:105], v255 offset:18816
v_mfma_f32_16x16x32_bf16 v[110:113], a[72:75], v[182:185], v[110:113]
ds_read_b64_tr_b16 a[106:107], v255 offset:21120
global_load_lds_dwordx4 v2, s[40:41] offset:-2048
v_mfma_f32_16x16x32_bf16 v[114:117], a[76:79], v[182:185], v[114:117]
ds_read_b64_tr_b16 a[248:249], v163 offset:18432
v_mfma_f32_16x16x32_bf16 v[118:121], v[182:185], a[80:83], v[118:121]
global_load_lds_dwordx4 v3, s[40:41] offset:-1024
ds_read_b64_tr_b16 a[250:251], v163 offset:20736
v_mfma_f32_16x16x32_bf16 v[134:137], v[182:185], a[240:243], v[134:137]
ds_read_b128 v[166:169], v51 offset:8192
global_load_lds_dwordx4 v4, s[40:41] offset:0
v_mfma_f32_16x16x32_bf16 v[122:125], a[72:75], v[190:193], v[122:125]
ds_read_b128 v[174:177], v51 offset:26624
v_mfma_f32_16x16x32_bf16 v[126:129], a[76:79], v[190:193], v[126:129]
ds_read_b64_tr_b16 a[108:109], v255 offset:36864
global_load_lds_dwordx4 v5, s[40:41] offset:1024
v_mfma_f32_16x16x32_bf16 v[130:133], v[190:193], a[80:83], v[130:133]
ds_read_b64_tr_b16 a[110:111], v255 offset:39168
v_mfma_f32_16x16x32_bf16 v[158:161], v[190:193], a[240:243], v[158:161]
global_load_lds_dwordx4 v6, s[40:41] offset:2048
ds_read_b64_tr_b16 a[112:113], v255 offset:37056
v_mfma_f32_16x16x32_bf16 v[86:89], a[84:87], v[106:109], v[86:89]
ds_read_b64_tr_b16 a[114:115], v255 offset:39360
v_mfma_f32_16x16x32_bf16 v[90:93], a[88:91], v[106:109], v[90:93]
global_load_lds_dwordx4 v7, s[40:41] offset:3072
ds_read_b64_tr_b16 a[116:117], v255 offset:37248
v_mfma_f32_16x16x32_bf16 v[94:97], v[106:109], a[92:95], v[94:97]
s_add_u32 m0, s48, 12288
ds_read_b64_tr_b16 a[118:119], v255 offset:39552
v_mfma_f32_16x16x32_bf16 v[102:105], v[106:109], a[244:247], v[102:105]
ds_read_b64_tr_b16 a[252:253], v163 offset:36864
v_mfma_f32_16x16x32_bf16 v[110:113], a[84:87], v[186:189], v[110:113]
global_load_lds_dwordx4 v8, s[40:41] offset:-4096
ds_read_b64_tr_b16 a[254:255], v163 offset:39168
v_mfma_f32_16x16x32_bf16 v[114:117], a[88:91], v[186:189], v[114:117]
ds_read_b128 v[170:173], v52 offset:8192
v_mfma_f32_16x16x32_bf16 v[118:121], v[186:189], a[92:95], v[118:121]
s_add_u32 s40, s40, 221184
ds_read_b128 v[178:181], v52 offset:26624
v_mfma_f32_16x16x32_bf16 v[134:137], v[186:189], a[244:247], v[134:137]
v_mfma_f32_16x16x32_bf16 v[122:125], a[84:87], v[194:197], v[122:125]
s_addc_u32 s41, s41, 0
v_mfma_f32_16x16x32_bf16 v[126:129], a[88:91], v[194:197], v[126:129]
v_mfma_f32_16x16x32_bf16 v[130:133], v[194:197], a[92:95], v[130:133]
v_mfma_f32_16x16x32_bf16 v[158:161], v[194:197], a[244:247], v[158:161]
s_waitcnt vmcnt(9) lgkmcnt(0)
s_barrier
s_add_u32 m0, s48, 40960
ds_read_b64_tr_b16 a[120:121], v255 offset:55296
v_mfma_f32_16x16x32_bf16 v[86:89], a[96:99], v[106:109], v[86:89]
ds_read_b64_tr_b16 a[122:123], v255 offset:57600
v_mfma_f32_16x16x32_bf16 v[90:93], a[100:103], v[106:109], v[90:93]
global_load_lds_dwordx4 v0, s[40:41] offset:-4096
ds_read_b64_tr_b16 a[124:125], v255 offset:55488
v_mfma_f32_16x16x32_bf16 v[94:97], v[106:109], a[104:107], v[94:97]
ds_read_b64_tr_b16 a[126:127], v255 offset:57792
global_load_lds_dwordx4 v1, s[40:41] offset:-3072
v_mfma_f32_16x16x32_bf16 v[102:105], v[106:109], a[248:251], v[102:105]
ds_read_b64_tr_b16 a[128:129], v255 offset:55680
v_mfma_f32_16x16x32_bf16 v[110:113], a[96:99], v[166:169], v[110:113]
ds_read_b64_tr_b16 a[130:131], v255 offset:57984
global_load_lds_dwordx4 v2, s[40:41] offset:-2048
v_mfma_f32_16x16x32_bf16 v[114:117], a[100:103], v[166:169], v[114:117]
ds_read_b64_tr_b16 v[54:55], v163 offset:55296
v_mfma_f32_16x16x32_bf16 v[118:121], v[166:169], a[104:107], v[118:121]
global_load_lds_dwordx4 v3, s[40:41] offset:-1024
ds_read_b64_tr_b16 v[56:57], v163 offset:57600
v_mfma_f32_16x16x32_bf16 v[134:137], v[166:169], a[248:251], v[134:137]
ds_read_b128 v[182:185], v51 offset:10240
global_load_lds_dwordx4 v4, s[40:41] offset:0
v_mfma_f32_16x16x32_bf16 v[122:125], a[96:99], v[174:177], v[122:125]
ds_read_b128 v[190:193], v51 offset:28672
v_mfma_f32_16x16x32_bf16 v[126:129], a[100:103], v[174:177], v[126:129]
ds_read_b64_tr_b16 a[132:133], v162 offset:8192
global_load_lds_dwordx4 v5, s[40:41] offset:1024
v_mfma_f32_16x16x32_bf16 v[130:133], v[174:177], a[104:107], v[130:133]
ds_read_b64_tr_b16 a[134:135], v162 offset:10496
v_mfma_f32_16x16x32_bf16 v[158:161], v[174:177], a[248:251], v[158:161]
global_load_lds_dwordx4 v6, s[40:41] offset:2048
ds_read_b64_tr_b16 a[136:137], v162 offset:8384
v_mfma_f32_16x16x32_bf16 v[86:89], a[108:111], v[106:109], v[86:89]
ds_read_b64_tr_b16 a[138:139], v162 offset:10688
v_mfma_f32_16x16x32_bf16 v[90:93], a[112:115], v[106:109], v[90:93]
global_load_lds_dwordx4 v7, s[40:41] offset:3072
ds_read_b64_tr_b16 a[140:141], v162 offset:8576
v_mfma_f32_16x16x32_bf16 v[94:97], v[106:109], a[116:119], v[94:97]
s_add_u32 m0, s48, 49152
ds_read_b64_tr_b16 a[142:143], v162 offset:10880
v_mfma_f32_16x16x32_bf16 v[102:105], v[106:109], a[252:255], v[102:105]
ds_read_b64_tr_b16 v[58:59], v164 offset:8192
v_mfma_f32_16x16x32_bf16 v[110:113], a[108:111], v[170:173], v[110:113]
global_load_lds_dwordx4 v8, s[40:41] offset:-4096
ds_read_b64_tr_b16 v[60:61], v164 offset:10496
v_mfma_f32_16x16x32_bf16 v[114:117], a[112:115], v[170:173], v[114:117]
ds_read_b128 v[186:189], v52 offset:10240
v_mfma_f32_16x16x32_bf16 v[118:121], v[170:173], a[116:119], v[118:121]
s_add_u32 s40, s40, 221184
ds_read_b128 v[194:197], v52 offset:28672
v_mfma_f32_16x16x32_bf16 v[134:137], v[170:173], a[252:255], v[134:137]
v_mfma_f32_16x16x32_bf16 v[122:125], a[108:111], v[178:181], v[122:125]
s_addc_u32 s41, s41, 0
v_mfma_f32_16x16x32_bf16 v[126:129], a[112:115], v[178:181], v[126:129]
v_mfma_f32_16x16x32_bf16 v[130:133], v[178:181], a[116:119], v[130:133]
v_mfma_f32_16x16x32_bf16 v[158:161], v[178:181], a[252:255], v[158:161]
s_waitcnt vmcnt(9) lgkmcnt(0)
s_barrier
s_add_u32 m0, s48, 77824
ds_read_b64_tr_b16 a[144:145], v9 offset:47104
v_mfma_f32_16x16x32_bf16 v[86:89], a[120:123], v[106:109], v[86:89]
ds_read_b64_tr_b16 a[146:147], v9 offset:49408
v_mfma_f32_16x16x32_bf16 v[90:93], a[124:127], v[106:109], v[90:93]
global_load_lds_dwordx4 v0, s[40:41] offset:-4096
ds_read_b64_tr_b16 a[148:149], v9 offset:47296
v_mfma_f32_16x16x32_bf16 v[94:97], v[106:109], a[128:131], v[94:97]
ds_read_b64_tr_b16 a[150:151], v9 offset:49600
global_load_lds_dwordx4 v1, s[40:41] offset:-3072
v_mfma_f32_16x16x32_bf16 v[102:105], v[106:109], v[54:57], v[102:105]
ds_read_b64_tr_b16 a[152:153], v9 offset:47488
v_mfma_f32_16x16x32_bf16 v[110:113], a[120:123], v[182:185], v[110:113]
ds_read_b64_tr_b16 a[154:155], v9 offset:49792
global_load_lds_dwordx4 v2, s[40:41] offset:-2048
v_mfma_f32_16x16x32_bf16 v[114:117], a[124:127], v[182:185], v[114:117]
ds_read_b64_tr_b16 v[62:63], v10 offset:47104
v_mfma_f32_16x16x32_bf16 v[118:121], v[182:185], a[128:131], v[118:121]
global_load_lds_dwordx4 v3, s[40:41] offset:-1024
ds_read_b64_tr_b16 v[64:65], v10 offset:49408
v_mfma_f32_16x16x32_bf16 v[134:137], v[182:185], v[54:57], v[134:137]
ds_read_b128 v[166:169], v51 offset:12288
global_load_lds_dwordx4 v4, s[40:41] offset:0
v_mfma_f32_16x16x32_bf16 v[122:125], a[120:123], v[190:193], v[122:125]
ds_read_b128 v[174:177], v51 offset:30720
v_mfma_f32_16x16x32_bf16 v[126:129], a[124:127], v[190:193], v[126:129]
ds_read_b64_tr_b16 a[156:157], v255 offset:0
global_load_lds_dwordx4 v5, s[40:41] offset:1024
v_mfma_f32_16x16x32_bf16 v[130:133], v[190:193], a[128:131], v[130:133]
ds_read_b64_tr_b16 a[158:159], v255 offset:2304
v_mfma_f32_16x16x32_bf16 v[158:161], v[190:193], v[54:57], v[158:161]
)"
R"(global_load_lds_dwordx4 v6, s[40:41] offset:2048
ds_read_b64_tr_b16 a[160:161], v255 offset:192
v_mfma_f32_16x16x32_bf16 v[86:89], a[132:135], v[106:109], v[86:89]
ds_read_b64_tr_b16 a[162:163], v255 offset:2496
v_mfma_f32_16x16x32_bf16 v[90:93], a[136:139], v[106:109], v[90:93]
global_load_lds_dwordx4 v7, s[40:41] offset:3072
ds_read_b64_tr_b16 a[164:165], v255 offset:384
v_mfma_f32_16x16x32_bf16 v[94:97], v[106:109], a[140:143], v[94:97]
s_add_u32 m0, s48, 86016
ds_read_b64_tr_b16 a[166:167], v255 offset:2688
v_mfma_f32_16x16x32_bf16 v[102:105], v[106:109], v[58:61], v[102:105]
ds_read_b64_tr_b16 v[66:67], v163 offset:0
v_mfma_f32_16x16x32_bf16 v[110:113], a[132:135], v[186:189], v[110:113]
global_load_lds_dwordx4 v8, s[40:41] offset:-4096
ds_read_b64_tr_b16 v[68:69], v163 offset:2304
v_mfma_f32_16x16x32_bf16 v[114:117], a[136:139], v[186:189], v[114:117]
ds_read_b128 v[170:173], v52 offset:12288
v_mfma_f32_16x16x32_bf16 v[118:121], v[186:189], a[140:143], v[118:121]
s_sub_u32 s40, s40, 1769472
ds_read_b128 v[178:181], v52 offset:30720
v_mfma_f32_16x16x32_bf16 v[134:137], v[186:189], v[58:61], v[134:137]
v_mfma_f32_16x16x32_bf16 v[122:125], a[132:135], v[194:197], v[122:125]
s_subb_u32 s41, s41, 0
v_mfma_f32_16x16x32_bf16 v[126:129], a[136:139], v[194:197], v[126:129]
v_mfma_f32_16x16x32_bf16 v[130:133], v[194:197], a[140:143], v[130:133]
v_mfma_f32_16x16x32_bf16 v[158:161], v[194:197], v[58:61], v[158:161]
s_waitcnt vmcnt(9) lgkmcnt(0)
s_barrier
s_add_u32 m0, s49, 0
ds_read_b64_tr_b16 a[168:169], v255 offset:18432
v_mfma_f32_16x16x32_bf16 v[86:89], a[144:147], v[106:109], v[86:89]
ds_read_b64_tr_b16 a[170:171], v255 offset:20736
v_mfma_f32_16x16x32_bf16 v[90:93], a[148:151], v[106:109], v[90:93]
ds_read_b64_tr_b16 a[172:173], v255 offset:18624
v_mfma_f32_16x16x32_bf16 v[94:97], v[106:109], a[152:155], v[94:97]
ds_read_b64_tr_b16 a[174:175], v255 offset:20928
global_load_lds_dwordx4 v11, s[42:43] offset:-2048 sc1
v_mfma_f32_16x16x32_bf16 v[102:105], v[106:109], v[62:65], v[102:105]
ds_read_b64_tr_b16 a[176:177], v255 offset:18816
v_mfma_f32_16x16x32_bf16 v[110:113], a[144:147], v[166:169], v[110:113]
ds_read_b64_tr_b16 a[178:179], v255 offset:21120
v_mfma_f32_16x16x32_bf16 v[114:117], a[148:151], v[166:169], v[114:117]
ds_read_b64_tr_b16 v[70:71], v163 offset:18432
global_load_lds_dwordx4 v12, s[42:43] offset:-1024 sc1
v_mfma_f32_16x16x32_bf16 v[118:121], v[166:169], a[152:155], v[118:121]
ds_read_b64_tr_b16 v[72:73], v163 offset:20736
v_mfma_f32_16x16x32_bf16 v[134:137], v[166:169], v[62:65], v[134:137]
ds_read_b128 v[182:185], v51 offset:14336
v_mfma_f32_16x16x32_bf16 v[122:125], a[144:147], v[174:177], v[122:125]
ds_read_b128 v[190:193], v51 offset:32768
global_load_lds_dwordx4 v13, s[42:43] offset:0 sc1
v_mfma_f32_16x16x32_bf16 v[126:129], a[148:151], v[174:177], v[126:129]
ds_read_b64_tr_b16 a[180:181], v255 offset:36864
v_mfma_f32_16x16x32_bf16 v[130:133], v[174:177], a[152:155], v[130:133]
ds_read_b64_tr_b16 a[182:183], v255 offset:39168
v_mfma_f32_16x16x32_bf16 v[158:161], v[174:177], v[62:65], v[158:161]
ds_read_b64_tr_b16 a[184:185], v255 offset:37056
v_mfma_f32_16x16x32_bf16 v[86:89], a[156:159], v[106:109], v[86:89]
global_load_lds_dwordx4 v14, s[42:43] offset:1024 sc1
ds_read_b64_tr_b16 a[186:187], v255 offset:39360
v_mfma_f32_16x16x32_bf16 v[90:93], a[160:163], v[106:109], v[90:93]
ds_read_b64_tr_b16 a[188:189], v255 offset:37248
v_mfma_f32_16x16x32_bf16 v[94:97], v[106:109], a[164:167], v[94:97]
ds_read_b64_tr_b16 a[190:191], v255 offset:39552
v_mfma_f32_16x16x32_bf16 v[102:105], v[106:109], v[66:69], v[102:105]
global_load_lds_dwordx4 v15, s[42:43] offset:2048 sc1
ds_read_b64_tr_b16 v[74:75], v163 offset:36864
v_mfma_f32_16x16x32_bf16 v[110:113], a[156:159], v[170:173], v[110:113]
ds_read_b64_tr_b16 v[76:77], v163 offset:39168
v_mfma_f32_16x16x32_bf16 v[114:117], a[160:163], v[170:173], v[114:117]
ds_read_b128 v[186:189], v52 offset:14336
v_mfma_f32_16x16x32_bf16 v[118:121], v[170:173], a[164:167], v[118:121]
s_sub_u32 s42, s42, 18432
s_subb_u32 s43, s43, 0
ds_read_b128 v[194:197], v52 offset:32768
v_mfma_f32_16x16x32_bf16 v[134:137], v[170:173], v[66:69], v[134:137]
v_mfma_f32_16x16x32_bf16 v[122:125], a[156:159], v[178:181], v[122:125]
v_mfma_f32_16x16x32_bf16 v[126:129], a[160:163], v[178:181], v[126:129]
v_mfma_f32_16x16x32_bf16 v[130:133], v[178:181], a[164:167], v[130:133]
v_mfma_f32_16x16x32_bf16 v[158:161], v[178:181], v[66:69], v[158:161]
s_waitcnt vmcnt(5) lgkmcnt(0)
s_barrier
s_add_u32 m0, s49, 18432
ds_read_b64_tr_b16 a[192:193], v255 offset:55296
v_mfma_f32_16x16x32_bf16 v[86:89], a[168:171], v[106:109], v[86:89]
ds_read_b64_tr_b16 a[194:195], v255 offset:57600
v_mfma_f32_16x16x32_bf16 v[90:93], a[172:175], v[106:109], v[90:93]
ds_read_b64_tr_b16 a[196:197], v255 offset:55488
v_mfma_f32_16x16x32_bf16 v[94:97], v[106:109], a[176:179], v[94:97]
ds_read_b64_tr_b16 a[198:199], v255 offset:57792
global_load_lds_dwordx4 v11, s[42:43] offset:-2048 sc1
v_mfma_f32_16x16x32_bf16 v[102:105], v[106:109], v[70:73], v[102:105]
ds_read_b64_tr_b16 a[200:201], v255 offset:55680
v_mfma_f32_16x16x32_bf16 v[110:113], a[168:171], v[182:185], v[110:113]
ds_read_b64_tr_b16 a[202:203], v255 offset:57984
v_mfma_f32_16x16x32_bf16 v[114:117], a[172:175], v[182:185], v[114:117]
ds_read_b64_tr_b16 v[78:79], v163 offset:55296
global_load_lds_dwordx4 v12, s[42:43] offset:-1024 sc1
v_mfma_f32_16x16x32_bf16 v[118:121], v[182:185], a[176:179], v[118:121]
ds_read_b64_tr_b16 v[80:81], v163 offset:57600
v_mfma_f32_16x16x32_bf16 v[134:137], v[182:185], v[70:73], v[134:137]
ds_read_b128 v[166:169], v51 offset:16384
v_mfma_f32_16x16x32_bf16 v[122:125], a[168:171], v[190:193], v[122:125]
ds_read_b128 v[174:177], v51 offset:34816
global_load_lds_dwordx4 v13, s[42:43] offset:0 sc1
v_mfma_f32_16x16x32_bf16 v[126:129], a[172:175], v[190:193], v[126:129]
ds_read_b64_tr_b16 a[204:205], v162 offset:8192
v_mfma_f32_16x16x32_bf16 v[130:133], v[190:193], a[176:179], v[130:133]
ds_read_b64_tr_b16 a[206:207], v162 offset:10496
v_mfma_f32_16x16x32_bf16 v[158:161], v[190:193], v[70:73], v[158:161]
ds_read_b64_tr_b16 a[208:209], v162 offset:8384
global_load_lds_dwordx4 v14, s[42:43] offset:1024 sc1
v_mfma_f32_16x16x32_bf16 v[86:89], a[180:183], v[106:109], v[86:89]
ds_read_b64_tr_b16 a[210:211], v162 offset:10688
v_mfma_f32_16x16x32_bf16 v[90:93], a[184:187], v[106:109], v[90:93]
ds_read_b64_tr_b16 a[212:213], v162 offset:8576
v_mfma_f32_16x16x32_bf16 v[94:97], v[106:109], a[188:191], v[94:97]
ds_read_b64_tr_b16 a[214:215], v162 offset:10880
global_load_lds_dwordx4 v15, s[42:43] offset:2048 sc1
v_mfma_f32_16x16x32_bf16 v[102:105], v[106:109], v[74:77], v[102:105]
ds_read_b64_tr_b16 v[82:83], v164 offset:8192
v_mfma_f32_16x16x32_bf16 v[110:113], a[180:183], v[186:189], v[110:113]
ds_read_b64_tr_b16 v[84:85], v164 offset:10496
v_mfma_f32_16x16x32_bf16 v[114:117], a[184:187], v[186:189], v[114:117]
ds_read_b128 v[170:173], v52 offset:16384
s_sub_u32 s42, s42, 18432
s_subb_u32 s43, s43, 0
v_mfma_f32_16x16x32_bf16 v[118:121], v[186:189], a[188:191], v[118:121]
ds_read_b128 v[178:181], v52 offset:34816
v_mfma_f32_16x16x32_bf16 v[134:137], v[186:189], v[74:77], v[134:137]
v_mfma_f32_16x16x32_bf16 v[122:125], a[180:183], v[194:197], v[122:125]
v_mfma_f32_16x16x32_bf16 v[126:129], a[184:187], v[194:197], v[126:129]
v_mfma_f32_16x16x32_bf16 v[158:161], v[194:197], v[74:77], v[158:161]
s_waitcnt lgkmcnt(15)
v_mfma_f32_16x16x32_bf16 v[86:89], a[192:195], v[106:109], v[86:89]
v_mfma_f32_16x16x32_bf16 v[90:93], a[196:199], v[106:109], v[90:93]
s_waitcnt lgkmcnt(14)
v_mfma_f32_16x16x32_bf16 v[94:97], v[106:109], a[200:203], v[94:97]
s_waitcnt lgkmcnt(12)
v_mfma_f32_16x16x32_bf16 v[102:105], v[106:109], v[78:81], v[102:105]
s_waitcnt lgkmcnt(11)
v_mfma_f32_16x16x32_bf16 v[110:113], a[192:195], v[166:169], v[110:113]
v_mfma_f32_16x16x32_bf16 v[114:117], a[196:199], v[166:169], v[114:117]
v_mfma_f32_16x16x32_bf16 v[118:121], v[166:169], a[200:203], v[118:121]
v_mfma_f32_16x16x32_bf16 v[134:137], v[166:169], v[78:81], v[134:137]
s_waitcnt lgkmcnt(10)
v_mfma_f32_16x16x32_bf16 v[158:161], v[174:177], v[78:81], v[158:161]
s_waitcnt lgkmcnt(8)
v_mfma_f32_16x16x32_bf16 v[86:89], a[204:207], v[106:109], v[86:89]
s_waitcnt lgkmcnt(6)
v_mfma_f32_16x16x32_bf16 v[90:93], a[208:211], v[106:109], v[90:93]
s_waitcnt lgkmcnt(4)
v_mfma_f32_16x16x32_bf16 v[94:97], v[106:109], a[212:215], v[94:97]
s_waitcnt lgkmcnt(2)
v_mfma_f32_16x16x32_bf16 v[102:105], v[106:109], v[82:85], v[102:105]
s_waitcnt lgkmcnt(1)
v_mfma_f32_16x16x32_bf16 v[110:113], a[204:207], v[170:173], v[110:113]
v_mfma_f32_16x16x32_bf16 v[114:117], a[208:211], v[170:173], v[114:117]
v_mfma_f32_16x16x32_bf16 v[118:121], v[170:173], a[212:215], v[118:121]
v_mfma_f32_16x16x32_bf16 v[134:137], v[170:173], v[82:85], v[134:137]
s_waitcnt lgkmcnt(0)
v_mfma_f32_16x16x32_bf16 v[158:161], v[178:181], v[82:85], v[158:161]
s_barrier
ds_read_b128 v[242:245], v25 offset:0
s_waitcnt lgkmcnt(0)
s_nop 2
v_fma_f32 v230, v102, v242, v134
v_fma_f32 v231, v102, v243, v135
v_fma_f32 v232, v102, v244, v136
v_fma_f32 v233, v102, v245, v137
v_cvt_pk_bf16_f32 v230, v230, v231
v_cvt_pk_bf16_f32 v231, v232, v233
ds_write_b64 v22, v[230:231] offset:41472
ds_read_b128 v[198:201], v51 offset:32768
ds_read_b128 v[202:205], v52 offset:32768
ds_read_b128 v[206:209], v51 offset:34816
ds_read_b128 v[210:213], v52 offset:34816
v_mov_b32 v234, v32
v_mov_b32 v235, v33
v_mov_b32 v236, v34
v_mov_b32 v237, v35
v_mov_b32 v134, v158
v_mov_b32 v135, v159
v_mov_b32 v136, v160
v_mov_b32 v137, v161
s_waitcnt lgkmcnt(4)
s_barrier
ds_read_b32 v23, v24 offset:0
ds_read_b32 v148, v21
ds_read_b32 v26, v24 offset:192
ds_read_b32 v53, v27
ds_read_b32 v95, v28
ds_read_b128 v[226:229], v25 offset:0
ds_read_b64 v[158:159], v22 offset:41472
ds_read_b32 v140, v20
v_xor_b32 v51, 0x10000, v51
v_xor_b32 v52, 0x10000, v52
s_waitcnt lgkmcnt(7)
v_fma_f32 v238, v90, v23, v114
v_fma_f32 v239, v91, v23, v115
v_fma_f32 v240, v92, v23, v116
v_fma_f32 v241, v93, v23, v117
v_cvt_pk_bf16_f32 v146, v238, v239
v_cvt_pk_bf16_f32 v147, v240, v241
s_waitcnt lgkmcnt(0)
s_barrier
ds_read_b128 v[242:245], v25 offset:64
v_fma_f32 v238, v94, v226, v118
v_fma_f32 v239, v94, v227, v119
v_fma_f32 v240, v94, v228, v120
v_fma_f32 v241, v94, v229, v121
v_cvt_pk_bf16_f32 v154, v238, v239
v_cvt_pk_bf16_f32 v155, v240, v241
v_fma_f32 v238, v86, v23, v110
v_fma_f32 v239, v87, v23, v111
v_fma_f32 v240, v88, v23, v112
v_fma_f32 v241, v89, v23, v113
v_cvt_pk_bf16_f32 v138, v238, v239
v_cvt_pk_bf16_f32 v139, v240, v241
s_waitcnt lgkmcnt(0)
v_fma_f32 v230, v102, v242, v134
v_fma_f32 v231, v102, v243, v135
v_fma_f32 v232, v102, v244, v136
v_fma_f32 v233, v102, v245, v137
v_cvt_pk_bf16_f32 v230, v230, v231
v_cvt_pk_bf16_f32 v231, v232, v233
ds_write_b64 v22, v[230:231] offset:41472
s_waitcnt vmcnt(5) lgkmcnt(0)
s_barrier
s_mov_b32 s70, 32
s_waitcnt lgkmcnt(0)
s_nop 1
s_branch .Lqa_in16
.p2align 6
.Lqa_top26:
s_waitcnt lgkmcnt(9)
v_mfma_f32_16x16x32_bf16 v[134:137], v[44:47], a[216:219], 0
s_waitcnt lgkmcnt(8)
ds_read_b128 v[162:165], v52 offset:4096
ds_read_b32 v95, v30
v_mfma_f32_16x16x32_bf16 v[134:137], v[96:99], a[220:223], v[134:137]
ds_read_b128 v[226:229], v25 offset:64
ds_read_b64 v[160:161], v22 offset:41472
v_mfma_f32_16x16x32_bf16 v[110:113], a[0:3], v[44:47], 0
s_waitcnt lgkmcnt(8)
ds_read_b128 v[166:169], v51 offset:6144
ds_read_b32 v144, v20
v_mfma_f32_16x16x32_bf16 v[134:137], v[104:107], a[224:227], v[134:137]
s_waitcnt lgkmcnt(8)
ds_read_b128 v[170:173], v52 offset:6144
v_fma_f32 v238, v90, v23, v126
v_mfma_f32_16x16x32_bf16 v[134:137], v[0:3], a[228:231], v[134:137]
global_store_dwordx4 v16, v[230:233], s[38:39] sc1
v_mfma_f32_16x16x32_bf16 v[114:117], a[4:7], v[44:47], 0
s_waitcnt lgkmcnt(7)
ds_read_b128 v[174:177], v51 offset:8192
v_add_u32 v16, 18432, v16
v_mfma_f32_16x16x32_bf16 v[134:137], v[4:7], a[232:235], v[134:137]
s_waitcnt lgkmcnt(7)
ds_read_b128 v[178:181], v52 offset:8192
v_fma_f32 v239, v91, v23, v127
v_mfma_f32_16x16x32_bf16 v[134:137], v[162:165], a[236:239], v[134:137]
v_fma_f32 v240, v92, v23, v128
v_fma_f32 v241, v93, v23, v129
v_mfma_f32_16x16x32_bf16 v[118:121], v[44:47], a[8:11], 0
s_waitcnt lgkmcnt(4)
ds_read_b128 v[182:185], v51 offset:10240
v_cvt_pk_bf16_f32 v150, v238, v239
v_mfma_f32_16x16x32_bf16 v[134:137], v[166:169], a[240:243], v[134:137]
s_waitcnt lgkmcnt(3)
ds_read_b128 v[186:189], v52 offset:10240
v_cvt_pk_bf16_f32 v151, v240, v241
v_mfma_f32_16x16x32_bf16 v[134:137], v[170:173], a[244:247], v[134:137]
s_nop 0
v_mfma_f32_16x16x32_bf16 v[214:217], v[150:153], v[138:141], v[32:35]
v_max_f32 v109, v218, v219
v_max_f32 v254, v220, v221
v_mfma_f32_16x16x32_bf16 v[110:113], a[12:15], v[96:99], v[110:113]
s_waitcnt lgkmcnt(3)
ds_read_b128 v[190:193], v51 offset:12288
v_fma_f32 v238, v94, v226, v130
v_mfma_f32_16x16x32_bf16 v[134:137], v[174:177], a[248:251], v[134:137]
s_xor_b32 s49, s49, 0x10000
ds_read_b128 v[194:197], v52 offset:12288
s_waitcnt lgkmcnt(4)
v_mfma_f32_16x16x32_bf16 v[134:137], v[178:181], a[252:255], v[134:137]
s_add_u32 m0, s49, 0
v_mul_f32 v26, 0x3eae1a93, v26
v_mul_f32 v103, v53, v26
v_mfma_f32_16x16x32_bf16 v[114:117], a[16:19], v[96:99], v[114:117]
s_waitcnt lgkmcnt(3)
ds_read_b128 v[198:201], v51 offset:14336
v_mul_f32 v108, v95, v26
v_mfma_f32_16x16x32_bf16 v[134:137], v[182:185], v[54:57], v[134:137]
s_waitcnt lgkmcnt(3)
ds_read_b128 v[202:205], v52 offset:14336
v_max3_f32 v109, v214, v215, v109
v_mfma_f32_16x16x32_bf16 v[134:137], v[186:189], v[58:61], v[134:137]
global_load_lds_dwordx4 v11, s[42:43] offset:-2048 sc1
v_max3_f32 v254, v216, v217, v254
v_mfma_f32_16x16x32_bf16 v[118:121], v[96:99], a[20:23], v[118:121]
s_waitcnt lgkmcnt(3)
ds_read_b128 v[206:209], v51 offset:16384
v_mul_f32 v109, v109, v103
v_mfma_f32_16x16x32_bf16 v[134:137], v[190:193], v[62:65], v[134:137]
s_waitcnt lgkmcnt(3)
ds_read_b128 v[210:213], v52 offset:16384
v_mul_f32 v254, v254, v108
v_mfma_f32_16x16x32_bf16 v[134:137], v[194:197], v[66:69], v[134:137]
v_max_f32 v255, v109, v254
v_mov_b32 v8, v255
v_mfma_f32_16x16x32_bf16 v[110:113], a[24:27], v[104:107], v[110:113]
s_waitcnt lgkmcnt(3)
v_permlane16_swap_b32 v255, v8
v_mfma_f32_16x16x32_bf16 v[134:137], v[198:201], v[70:73], v[134:137]
s_waitcnt lgkmcnt(2)
v_max_f32 v255, v255, v8
v_mov_b32 v8, v255
v_mfma_f32_16x16x32_bf16 v[134:137], v[202:205], v[74:77], v[134:137]
s_nop 0
v_permlane32_swap_b32 v255, v8
v_mfma_f32_16x16x32_bf16 v[114:117], a[28:31], v[104:107], v[114:117]
s_waitcnt lgkmcnt(1)
v_max_f32 v255, v255, v8
v_sub_f32 v109, v109, v255
v_mfma_f32_16x16x32_bf16 v[134:137], v[206:209], v[78:81], v[134:137]
s_waitcnt lgkmcnt(0)
global_load_lds_dwordx4 v12, s[42:43] offset:-1024 sc1
v_sub_f32 v254, v254, v255
v_mfma_f32_16x16x32_bf16 v[134:137], v[210:213], v[82:85], v[134:137]
v_exp_f32 v9, v109
v_mfma_f32_16x16x32_bf16 v[118:121], v[104:107], a[32:35], v[118:121]
v_exp_f32 v10, v254
v_mfma_f32_16x16x32_bf16 v[110:113], a[36:39], v[0:3], v[110:113]
v_add_f32 v255, v9, v10
v_mov_b32 v8, v255
v_mfma_f32_16x16x32_bf16 v[114:117], a[40:43], v[0:3], v[114:117]
v_mul_f32 v9, v9, v53
v_fma_f32 v239, v94, v227, v131
v_mfma_f32_16x16x32_bf16 v[118:121], v[0:3], a[44:47], v[118:121]
v_permlane16_swap_b32 v255, v8
v_mfma_f32_16x16x32_bf16 v[110:113], a[48:51], v[4:7], v[110:113]
v_add_f32 v255, v255, v8
v_mov_b32 v8, v255
v_mfma_f32_16x16x32_bf16 v[114:117], a[52:55], v[4:7], v[114:117]
v_mul_f32 v10, v10, v95
v_fma_f32 v240, v94, v228, v132
v_mfma_f32_16x16x32_bf16 v[118:121], v[4:7], a[56:59], v[118:121]
global_load_lds_dwordx4 v13, s[42:43] offset:0 sc1
v_fma_f32 v241, v94, v229, v133
v_mfma_f32_16x16x32_bf16 v[110:113], a[60:63], v[162:165], v[110:113]
v_permlane32_swap_b32 v255, v8
v_mfma_f32_16x16x32_bf16 v[114:117], a[64:67], v[162:165], v[114:117]
v_add_f32 v255, v255, v8
v_cvt_pk_bf16_f32 v156, v238, v239
v_mfma_f32_16x16x32_bf16 v[118:121], v[162:165], a[68:71], v[118:121]
v_mfma_f32_16x16x32_bf16 v[110:113], a[72:75], v[166:169], v[110:113]
s_barrier
v_mfma_f32_16x16x32_bf16 v[114:117], a[76:79], v[166:169], v[114:117]
ds_read_b128 v[242:245], v25 offset:0
v_cvt_pk_bf16_f32 v157, v240, v241
v_mfma_f32_16x16x32_bf16 v[118:121], v[166:169], a[80:83], v[118:121]
v_rcp_f32 v255, v255
v_mfma_f32_16x16x32_bf16 v[110:113], a[84:87], v[170:173], v[110:113]
v_mul_f32 v9, v9, v255
v_mul_f32 v10, v10, v255
v_mfma_f32_16x16x32_bf16 v[114:117], a[88:91], v[170:173], v[114:117]
global_load_lds_dwordx4 v14, s[42:43] offset:1024 sc1
v_cvt_pk_bf16_f32 v9, v9, v9
v_mfma_f32_16x16x32_bf16 v[118:121], v[170:173], a[92:95], v[118:121]
v_cvt_pk_bf16_f32 v10, v10, v10
v_and_b32 v224, v9, v40
v_mfma_f32_16x16x32_bf16 v[110:113], a[96:99], v[174:177], v[110:113]
v_and_b32 v225, v10, v41
v_and_b32 v222, v9, v42
v_mfma_f32_16x16x32_bf16 v[114:117], a[100:103], v[174:177], v[114:117]
s_waitcnt lgkmcnt(0)
v_and_b32 v223, v10, v43
v_fma_f32 v230, v102, v242, v134
v_mfma_f32_16x16x32_bf16 v[118:121], v[174:177], a[104:107], v[118:121]
v_mfma_f32_16x16x32_bf16 v[218:221], v[154:157], v[222:225], 0
v_mfma_f32_16x16x32_bf16 v[214:217], v[158:161], v[222:225], 0
v_fma_f32 v231, v102, v243, v135
v_fma_f32 v232, v102, v244, v136
v_mfma_f32_16x16x32_bf16 v[110:113], a[108:111], v[178:181], v[110:113]
v_fma_f32 v233, v102, v245, v137
v_cvt_pk_bf16_f32 v230, v230, v231
v_mfma_f32_16x16x32_bf16 v[114:117], a[112:115], v[178:181], v[114:117]
v_cvt_pk_bf16_f32 v231, v232, v233
v_fma_f32 v238, v86, v23, v122
v_mfma_f32_16x16x32_bf16 v[118:121], v[178:181], a[116:119], v[118:121]
ds_write_b64 v22, v[230:231] offset:41472
v_mfma_f32_16x16x32_bf16 v[110:113], a[120:123], v[182:185], v[110:113]
global_load_lds_dwordx4 v15, s[42:43] offset:2048 sc1
s_add_u32 s42, s42, s46
s_addc_u32 s43, s43, s47
v_mfma_f32_16x16x32_bf16 v[114:117], a[124:127], v[182:185], v[114:117]
v_cvt_pk_bf16_f32 v100, v218, v219
v_cvt_pk_bf16_f32 v101, v220, v221
v_mfma_f32_16x16x32_bf16 v[118:121], v[182:185], a[128:131], v[118:121]
)"
R"(ds_write_b64 v18, v[100:101]
v_mfma_f32_16x16x32_bf16 v[110:113], a[132:135], v[186:189], v[110:113]
v_cvt_pk_bf16_f32 v9, v216, v217
v_fma_f32 v239, v87, v23, v123
v_mfma_f32_16x16x32_bf16 v[114:117], a[136:139], v[186:189], v[114:117]
ds_write_b32 v19, v9
v_mfma_f32_16x16x32_bf16 v[118:121], v[186:189], a[140:143], v[118:121]
v_fma_f32 v240, v88, v23, v124
v_fma_f32 v241, v89, v23, v125
v_mfma_f32_16x16x32_bf16 v[110:113], a[144:147], v[190:193], v[110:113]
v_cvt_pk_bf16_f32 v142, v238, v239
v_cvt_pk_bf16_f32 v143, v240, v241
v_mfma_f32_16x16x32_bf16 v[114:117], a[148:151], v[190:193], v[114:117]
v_mfma_f32_16x16x32_bf16 v[118:121], v[190:193], a[152:155], v[118:121]
v_mfma_f32_16x16x32_bf16 v[110:113], a[156:159], v[194:197], v[110:113]
v_mfma_f32_16x16x32_bf16 v[114:117], a[160:163], v[194:197], v[114:117]
v_mfma_f32_16x16x32_bf16 v[118:121], v[194:197], a[164:167], v[118:121]
v_mfma_f32_16x16x32_bf16 v[110:113], a[168:171], v[198:201], v[110:113]
v_mfma_f32_16x16x32_bf16 v[114:117], a[172:175], v[198:201], v[114:117]
v_mfma_f32_16x16x32_bf16 v[118:121], v[198:201], a[176:179], v[118:121]
v_mfma_f32_16x16x32_bf16 v[110:113], a[180:183], v[202:205], v[110:113]
v_mfma_f32_16x16x32_bf16 v[114:117], a[184:187], v[202:205], v[114:117]
s_waitcnt vmcnt(5) lgkmcnt(0)
s_barrier
v_mfma_f32_16x16x32_bf16 v[118:121], v[202:205], a[188:191], v[118:121]
ds_read_b128 v[44:47], v51 offset:18432
v_mfma_f32_16x16x32_bf16 v[110:113], a[192:195], v[206:209], v[110:113]
ds_read_b128 v[96:99], v52 offset:18432
ds_read_b128 v[230:233], v17 offset:0
v_mfma_f32_16x16x32_bf16 v[114:117], a[196:199], v[206:209], v[114:117]
ds_read_b32 v23, v24 offset:0
ds_read_b32 v148, v21
v_mfma_f32_16x16x32_bf16 v[218:221], v[150:153], v[142:145], v[36:39]
v_mfma_f32_16x16x32_bf16 v[118:121], v[206:209], a[200:203], v[118:121]
ds_read_b128 v[104:107], v51 offset:20480
ds_read_b32 v26, v24 offset:192
v_mfma_f32_16x16x32_bf16 v[110:113], a[204:207], v[210:213], v[110:113]
ds_read_b128 v[0:3], v52 offset:20480
ds_read_b32 v53, v27
v_mfma_f32_16x16x32_bf16 v[114:117], a[208:211], v[210:213], v[114:117]
ds_read_b32 v95, v28
ds_read_b128 v[226:229], v25 offset:0
v_mfma_f32_16x16x32_bf16 v[118:121], v[210:213], a[212:215], v[118:121]
s_waitcnt lgkmcnt(10)
ds_read_b128 v[4:7], v51 offset:22528
ds_read_b64 v[158:159], v22 offset:41472
v_mfma_f32_16x16x32_bf16 v[134:137], v[44:47], a[216:219], 0
s_waitcnt lgkmcnt(11)
ds_read_b128 v[162:165], v52 offset:22528
ds_read_b32 v140, v20
v_mfma_f32_16x16x32_bf16 v[134:137], v[96:99], a[220:223], v[134:137]
v_max_f32 v109, v218, v219
v_max_f32 v254, v220, v221
v_mfma_f32_16x16x32_bf16 v[122:125], a[0:3], v[44:47], 0
s_waitcnt lgkmcnt(11)
ds_read_b128 v[166:169], v51 offset:24576
v_fma_f32 v238, v90, v23, v114
s_waitcnt lgkmcnt(10)
v_mfma_f32_16x16x32_bf16 v[134:137], v[104:107], a[224:227], v[134:137]
s_waitcnt lgkmcnt(8)
ds_read_b128 v[170:173], v52 offset:24576
v_fma_f32 v239, v91, v23, v115
v_mfma_f32_16x16x32_bf16 v[134:137], v[0:3], a[228:231], v[134:137]
global_store_dwordx4 v16, v[230:233], s[38:39] sc1
v_mfma_f32_16x16x32_bf16 v[126:129], a[4:7], v[44:47], 0
s_waitcnt lgkmcnt(5)
ds_read_b128 v[174:177], v51 offset:26624
v_add_u32 v16, 18432, v16
v_mfma_f32_16x16x32_bf16 v[134:137], v[4:7], a[232:235], v[134:137]
s_waitcnt lgkmcnt(4)
ds_read_b128 v[178:181], v52 offset:26624
v_fma_f32 v240, v92, v23, v116
v_mfma_f32_16x16x32_bf16 v[134:137], v[162:165], a[236:239], v[134:137]
v_fma_f32 v241, v93, v23, v117
v_cvt_pk_bf16_f32 v146, v238, v239
v_mfma_f32_16x16x32_bf16 v[130:133], v[44:47], a[8:11], 0
s_waitcnt lgkmcnt(3)
ds_read_b128 v[182:185], v51 offset:28672
v_cvt_pk_bf16_f32 v147, v240, v241
v_mfma_f32_16x16x32_bf16 v[134:137], v[166:169], a[240:243], v[134:137]
ds_read_b128 v[186:189], v52 offset:28672
v_mfma_f32_16x16x32_bf16 v[214:217], v[146:149], v[142:145], v[234:237]
s_waitcnt lgkmcnt(4)
v_mul_f32 v26, 0x3eae1a93, v26
v_mul_f32 v103, v53, v26
v_mfma_f32_16x16x32_bf16 v[134:137], v[170:173], a[244:247], v[134:137]
v_mul_f32 v108, v95, v26
v_fma_f32 v238, v94, v226, v118
v_mfma_f32_16x16x32_bf16 v[122:125], a[12:15], v[96:99], v[122:125]
s_waitcnt lgkmcnt(3)
ds_read_b128 v[190:193], v51 offset:30720
v_fma_f32 v239, v94, v227, v119
v_mfma_f32_16x16x32_bf16 v[134:137], v[174:177], a[248:251], v[134:137]
s_add_u32 m0, s49, 18432
ds_read_b128 v[194:197], v52 offset:30720
s_waitcnt lgkmcnt(4)
v_mfma_f32_16x16x32_bf16 v[134:137], v[178:181], a[252:255], v[134:137]
v_max3_f32 v109, v214, v215, v109
v_max3_f32 v254, v216, v217, v254
v_mfma_f32_16x16x32_bf16 v[126:129], a[16:19], v[96:99], v[126:129]
s_waitcnt lgkmcnt(3)
ds_read_b128 v[198:201], v51 offset:32768
v_mul_f32 v109, v109, v103
v_mfma_f32_16x16x32_bf16 v[134:137], v[182:185], v[54:57], v[134:137]
s_waitcnt lgkmcnt(3)
ds_read_b128 v[202:205], v52 offset:32768
global_load_lds_dwordx4 v11, s[42:43] offset:-2048 sc1
v_mfma_f32_16x16x32_bf16 v[134:137], v[186:189], v[58:61], v[134:137]
v_mul_f32 v254, v254, v108
v_max_f32 v255, v109, v254
v_mfma_f32_16x16x32_bf16 v[130:133], v[96:99], a[20:23], v[130:133]
s_waitcnt lgkmcnt(3)
ds_read_b128 v[206:209], v51 offset:34816
v_mov_b32 v8, v255
v_mfma_f32_16x16x32_bf16 v[134:137], v[190:193], v[62:65], v[134:137]
s_waitcnt lgkmcnt(3)
ds_read_b128 v[210:213], v52 offset:34816
v_fma_f32 v240, v94, v228, v120
v_mfma_f32_16x16x32_bf16 v[134:137], v[194:197], v[66:69], v[134:137]
v_permlane16_swap_b32 v255, v8
v_mfma_f32_16x16x32_bf16 v[122:125], a[24:27], v[104:107], v[122:125]
s_waitcnt lgkmcnt(3)
v_max_f32 v255, v255, v8
v_mov_b32 v8, v255
v_mfma_f32_16x16x32_bf16 v[134:137], v[198:201], v[70:73], v[134:137]
s_waitcnt lgkmcnt(2)
v_permlane32_swap_b32 v255, v8
v_mfma_f32_16x16x32_bf16 v[134:137], v[202:205], v[74:77], v[134:137]
v_max_f32 v255, v255, v8
v_sub_f32 v109, v109, v255
v_mfma_f32_16x16x32_bf16 v[126:129], a[28:31], v[104:107], v[126:129]
s_waitcnt lgkmcnt(1)
global_load_lds_dwordx4 v12, s[42:43] offset:-1024 sc1
v_sub_f32 v254, v254, v255
v_mfma_f32_16x16x32_bf16 v[134:137], v[206:209], v[78:81], v[134:137]
s_waitcnt lgkmcnt(0)
v_exp_f32 v9, v109
v_mfma_f32_16x16x32_bf16 v[134:137], v[210:213], v[82:85], v[134:137]
v_exp_f32 v10, v254
v_mfma_f32_16x16x32_bf16 v[130:133], v[104:107], a[32:35], v[130:133]
v_add_f32 v255, v9, v10
v_mov_b32 v8, v255
v_mfma_f32_16x16x32_bf16 v[122:125], a[36:39], v[0:3], v[122:125]
v_mul_f32 v9, v9, v53
v_fma_f32 v241, v94, v229, v121
v_mfma_f32_16x16x32_bf16 v[126:129], a[40:43], v[0:3], v[126:129]
v_permlane16_swap_b32 v255, v8
v_mfma_f32_16x16x32_bf16 v[130:133], v[0:3], a[44:47], v[130:133]
v_add_f32 v255, v255, v8
v_mov_b32 v8, v255
v_mfma_f32_16x16x32_bf16 v[122:125], a[48:51], v[4:7], v[122:125]
v_mul_f32 v10, v10, v95
v_cvt_pk_bf16_f32 v154, v238, v239
v_mfma_f32_16x16x32_bf16 v[126:129], a[52:55], v[4:7], v[126:129]
global_load_lds_dwordx4 v13, s[42:43] offset:0 sc1
v_cvt_pk_bf16_f32 v155, v240, v241
v_mfma_f32_16x16x32_bf16 v[130:133], v[4:7], a[56:59], v[130:133]
v_permlane32_swap_b32 v255, v8
v_mfma_f32_16x16x32_bf16 v[122:125], a[60:63], v[162:165], v[122:125]
v_add_f32 v255, v255, v8
v_fma_f32 v238, v86, v23, v110
v_mfma_f32_16x16x32_bf16 v[126:129], a[64:67], v[162:165], v[126:129]
v_rcp_f32 v255, v255
v_mfma_f32_16x16x32_bf16 v[130:133], v[162:165], a[68:71], v[130:133]
v_mfma_f32_16x16x32_bf16 v[122:125], a[72:75], v[166:169], v[122:125]
s_barrier
v_mfma_f32_16x16x32_bf16 v[126:129], a[76:79], v[166:169], v[126:129]
ds_read_b128 v[242:245], v25 offset:64
v_mul_f32 v9, v9, v255
v_mfma_f32_16x16x32_bf16 v[130:133], v[166:169], a[80:83], v[130:133]
v_mul_f32 v10, v10, v255
v_cvt_pk_bf16_f32 v9, v9, v9
v_mfma_f32_16x16x32_bf16 v[122:125], a[84:87], v[170:173], v[122:125]
global_load_lds_dwordx4 v14, s[42:43] offset:1024 sc1
v_cvt_pk_bf16_f32 v10, v10, v10
v_mfma_f32_16x16x32_bf16 v[126:129], a[88:91], v[170:173], v[126:129]
v_and_b32 v222, v9, v40
v_and_b32 v223, v10, v41
v_mfma_f32_16x16x32_bf16 v[130:133], v[170:173], a[92:95], v[130:133]
v_and_b32 v224, v9, v42
v_and_b32 v225, v10, v43
v_mfma_f32_16x16x32_bf16 v[122:125], a[96:99], v[174:177], v[122:125]
s_nop 0
v_mfma_f32_16x16x32_bf16 v[218:221], v[154:157], v[222:225], 0
v_mfma_f32_16x16x32_bf16 v[214:217], v[158:161], v[222:225], 0
v_fma_f32 v239, v87, v23, v111
v_fma_f32 v240, v88, v23, v112
v_mfma_f32_16x16x32_bf16 v[126:129], a[100:103], v[174:177], v[126:129]
s_waitcnt lgkmcnt(0)
v_fma_f32 v230, v102, v242, v134
v_fma_f32 v231, v102, v243, v135
v_mfma_f32_16x16x32_bf16 v[130:133], v[174:177], a[104:107], v[130:133]
v_fma_f32 v232, v102, v244, v136
v_fma_f32 v233, v102, v245, v137
v_mfma_f32_16x16x32_bf16 v[122:125], a[108:111], v[178:181], v[122:125]
v_cvt_pk_bf16_f32 v230, v230, v231
v_cvt_pk_bf16_f32 v231, v232, v233
v_mfma_f32_16x16x32_bf16 v[126:129], a[112:115], v[178:181], v[126:129]
v_cvt_pk_bf16_f32 v100, v218, v219
v_cvt_pk_bf16_f32 v101, v220, v221
v_mfma_f32_16x16x32_bf16 v[130:133], v[178:181], a[116:119], v[130:133]
global_load_lds_dwordx4 v15, s[42:43] offset:2048 sc1
s_sub_u32 s42, s42, 18432
s_subb_u32 s43, s43, 0
v_mfma_f32_16x16x32_bf16 v[122:125], a[120:123], v[182:185], v[122:125]
ds_write_b64 v18, v[100:101]
v_mfma_f32_16x16x32_bf16 v[126:129], a[124:127], v[182:185], v[126:129]
v_cvt_pk_bf16_f32 v9, v216, v217
v_fma_f32 v241, v89, v23, v113
v_mfma_f32_16x16x32_bf16 v[130:133], v[182:185], a[128:131], v[130:133]
ds_write_b32 v19, v9
v_mfma_f32_16x16x32_bf16 v[122:125], a[132:135], v[186:189], v[122:125]
ds_write_b64 v22, v[230:231] offset:41472
v_mfma_f32_16x16x32_bf16 v[126:129], a[136:139], v[186:189], v[126:129]
v_cvt_pk_bf16_f32 v138, v238, v239
v_cvt_pk_bf16_f32 v139, v240, v241
v_mfma_f32_16x16x32_bf16 v[130:133], v[186:189], a[140:143], v[130:133]
v_mfma_f32_16x16x32_bf16 v[122:125], a[144:147], v[190:193], v[122:125]
v_mfma_f32_16x16x32_bf16 v[126:129], a[148:151], v[190:193], v[126:129]
v_mfma_f32_16x16x32_bf16 v[130:133], v[190:193], a[152:155], v[130:133]
v_mfma_f32_16x16x32_bf16 v[122:125], a[156:159], v[194:197], v[122:125]
v_mfma_f32_16x16x32_bf16 v[126:129], a[160:163], v[194:197], v[126:129]
v_mfma_f32_16x16x32_bf16 v[130:133], v[194:197], a[164:167], v[130:133]
v_mfma_f32_16x16x32_bf16 v[122:125], a[168:171], v[198:201], v[122:125]
v_mfma_f32_16x16x32_bf16 v[126:129], a[172:175], v[198:201], v[126:129]
v_xor_b32 v51, 0x10000, v51
v_xor_b32 v52, 0x10000, v52
v_mfma_f32_16x16x32_bf16 v[130:133], v[198:201], a[176:179], v[130:133]
v_mfma_f32_16x16x32_bf16 v[122:125], a[180:183], v[202:205], v[122:125]
v_mfma_f32_16x16x32_bf16 v[126:129], a[184:187], v[202:205], v[126:129]
s_waitcnt vmcnt(5) lgkmcnt(0)
s_barrier
.Lqa_in16:
v_mfma_f32_16x16x32_bf16 v[130:133], v[202:205], a[188:191], v[130:133]
s_cmp_eq_u32 s70, s73
s_cbranch_scc1 .Lqa_hk17
.Lqa_hr18:
ds_read_b128 v[44:47], v51 offset:0
v_mfma_f32_16x16x32_bf16 v[122:125], a[192:195], v[206:209], v[122:125]
ds_read_b128 v[96:99], v52 offset:0
ds_read_b128 v[230:233], v17 offset:0
v_mfma_f32_16x16x32_bf16 v[126:129], a[196:199], v[206:209], v[126:129]
ds_read_b32 v23, v24 offset:64
ds_read_b32 v152, v21
v_mfma_f32_16x16x32_bf16 v[218:221], v[146:149], v[138:141], v[36:39]
v_mfma_f32_16x16x32_bf16 v[130:133], v[206:209], a[200:203], v[130:133]
ds_read_b128 v[104:107], v51 offset:2048
ds_read_b32 v26, v24 offset:128
v_mfma_f32_16x16x32_bf16 v[122:125], a[204:207], v[210:213], v[122:125]
ds_read_b128 v[0:3], v52 offset:2048
ds_read_b32 v53, v29
v_mfma_f32_16x16x32_bf16 v[126:129], a[208:211], v[210:213], v[126:129]
v_mfma_f32_16x16x32_bf16 v[130:133], v[210:213], a[212:215], v[130:133]
s_sub_u32 s70, s70, 1
ds_read_b128 v[4:7], v51 offset:4096
s_cbranch_scc0 .Lqa_top26
s_waitcnt lgkmcnt(0)
ds_read_b32 v95, v30
ds_read_b128 v[226:229], v25 offset:64
ds_read_b64 v[160:161], v22 offset:41472
ds_read_b32 v144, v20
v_fma_f32 v238, v90, v23, v126
global_store_dwordx4 v16, v[230:233], s[38:39] sc1
v_add_u32 v16, 18432, v16
v_fma_f32 v239, v91, v23, v127
v_fma_f32 v240, v92, v23, v128
v_fma_f32 v241, v93, v23, v129
v_cvt_pk_bf16_f32 v150, v238, v239
v_cvt_pk_bf16_f32 v151, v240, v241
s_nop 1
v_mfma_f32_16x16x32_bf16 v[214:217], v[150:153], v[138:141], v[32:35]
s_waitcnt lgkmcnt(2)
v_max_f32 v109, v218, v219
v_max_f32 v254, v220, v221
v_fma_f32 v238, v94, v226, v130
v_mul_f32 v26, 0x3eae1a93, v26
v_mul_f32 v103, v53, v26
v_mul_f32 v108, v95, v26
s_nop 0
v_max3_f32 v109, v214, v215, v109
v_max3_f32 v254, v216, v217, v254
v_mul_f32 v109, v109, v103
v_mul_f32 v254, v254, v108
v_max_f32 v255, v109, v254
v_mov_b32 v8, v255
s_nop 1
v_permlane16_swap_b32 v255, v8
v_max_f32 v255, v255, v8
v_mov_b32 v8, v255
s_nop 1
v_permlane32_swap_b32 v255, v8
v_max_f32 v255, v255, v8
v_sub_f32 v109, v109, v255
v_sub_f32 v254, v254, v255
v_exp_f32 v9, v109
v_exp_f32 v10, v254
s_nop 0
v_add_f32 v255, v9, v10
v_mov_b32 v8, v255
v_mul_f32 v9, v9, v53
v_fma_f32 v239, v94, v227, v131
v_permlane16_swap_b32 v255, v8
v_add_f32 v255, v255, v8
v_mov_b32 v8, v255
v_mul_f32 v10, v10, v95
v_fma_f32 v240, v94, v228, v132
v_fma_f32 v241, v94, v229, v133
v_permlane32_swap_b32 v255, v8
v_add_f32 v255, v255, v8
v_cvt_pk_bf16_f32 v156, v238, v239
s_waitcnt lgkmcnt(0)
s_barrier
v_cvt_pk_bf16_f32 v157, v240, v241
v_rcp_f32 v255, v255
s_nop 0
v_mul_f32 v9, v9, v255
v_mul_f32 v10, v10, v255
v_cvt_pk_bf16_f32 v9, v9, v9
v_cvt_pk_bf16_f32 v10, v10, v10
v_and_b32 v224, v9, v40
v_and_b32 v225, v10, v41
v_and_b32 v222, v9, v42
v_and_b32 v223, v10, v43
s_nop 1
v_mfma_f32_16x16x32_bf16 v[218:221], v[154:157], v[222:225], 0
v_mfma_f32_16x16x32_bf16 v[214:217], v[158:161], v[222:225], 0
v_fma_f32 v238, v86, v23, v122
s_nop 5
v_cvt_pk_bf16_f32 v100, v218, v219
v_cvt_pk_bf16_f32 v101, v220, v221
ds_write_b64 v18, v[100:101]
v_cvt_pk_bf16_f32 v9, v216, v217
v_fma_f32 v239, v87, v23, v123
ds_write_b32 v19, v9
v_fma_f32 v240, v88, v23, v124
v_fma_f32 v241, v89, v23, v125
v_cvt_pk_bf16_f32 v142, v238, v239
v_cvt_pk_bf16_f32 v143, v240, v241
s_waitcnt vmcnt(0)
s_waitcnt lgkmcnt(0)
s_barrier
ds_read_b128 v[230:233], v17 offset:0
s_waitcnt lgkmcnt(0)
global_store_dwordx4 v16, v[230:233], s[38:39] sc1
s_waitcnt vmcnt(0)
s_barrier
s_endpgm
.Lqa_w3:
s_add_u32 m0, s48, 4096
s_nop 0
global_load_lds_dwordx4 v0, s[40:41] offset:-4096
global_load_lds_dwordx4 v1, s[40:41] offset:-3072
global_load_lds_dwordx4 v2, s[40:41] offset:-2048
global_load_lds_dwordx4 v3, s[40:41] offset:-1024
global_load_lds_dwordx4 v4, s[40:41] offset:0
global_load_lds_dwordx4 v5, s[40:41] offset:1024
global_load_lds_dwordx4 v6, s[40:41] offset:2048
global_load_lds_dwordx4 v7, s[40:41] offset:3072
s_add_u32 m0, s48, 12288
s_nop 0
global_load_lds_dwordx4 v8, s[40:41] offset:-4096
s_add_u32 s40, s40, 221184
s_addc_u32 s41, s41, 0
s_add_u32 m0, s48, 40960
s_nop 0
global_load_lds_dwordx4 v0, s[40:41] offset:-4096
global_load_lds_dwordx4 v1, s[40:41] offset:-3072
global_load_lds_dwordx4 v2, s[40:41] offset:-2048
global_load_lds_dwordx4 v3, s[40:41] offset:-1024
global_load_lds_dwordx4 v4, s[40:41] offset:0
global_load_lds_dwordx4 v5, s[40:41] offset:1024
global_load_lds_dwordx4 v6, s[40:41] offset:2048
global_load_lds_dwordx4 v7, s[40:41] offset:3072
s_add_u32 m0, s48, 49152
s_nop 0
global_load_lds_dwordx4 v8, s[40:41] offset:-4096
s_add_u32 s40, s40, 221184
s_addc_u32 s41, s41, 0
v_add_u32 v255, 0x10000, v9
v_add_u32 v162, 0x20000, v9
v_add_u32 v163, 0x10000, v10
v_add_u32 v164, 0x20000, v10
v_mov_b32 v106, 0x3F803F80
v_mov_b32 v107, 0x3F803F80
v_mov_b32 v108, 0x3F803F80
v_mov_b32 v109, 0x3F803F80
v_mov_b32 v141, 0
v_mov_b32 v145, 0
v_mov_b32 v149, 0
v_mov_b32 v153, 0
v_mov_b32 v165, 46080
s_waitcnt lgkmcnt(0)
s_mul_i32 s37, s37, 0x9E3779B1
s_add_u32 s37, s37, 0x7F4A7C15
v_add_u32 v46, s37, v46
v_add_u32 v48, s37, v48
v_add_u32 v50, s37, v50
ds_write_b32 v165, v141
s_mov_b64 s[76:77], 0
s_mov_b32 s73, 18
)"
R"(s_waitcnt vmcnt(9) lgkmcnt(0)
s_barrier
s_add_u32 m0, s48, 77824
ds_read_b64_tr_b16 a[0:1], v9 offset:47104
ds_read_b64_tr_b16 a[2:3], v9 offset:49408
global_load_lds_dwordx4 v0, s[40:41] offset:-4096
ds_read_b64_tr_b16 a[4:5], v9 offset:47296
global_load_lds_dwordx4 v1, s[40:41] offset:-3072
ds_read_b64_tr_b16 a[6:7], v9 offset:49600
global_load_lds_dwordx4 v2, s[40:41] offset:-2048
ds_read_b64_tr_b16 a[8:9], v9 offset:47488
global_load_lds_dwordx4 v3, s[40:41] offset:-1024
ds_read_b64_tr_b16 a[10:11], v9 offset:49792
ds_read_b128 v[166:169], v51 offset:0
global_load_lds_dwordx4 v4, s[40:41] offset:0
ds_read_b128 v[174:177], v51 offset:18432
global_load_lds_dwordx4 v5, s[40:41] offset:1024
ds_read_b64_tr_b16 a[12:13], v255 offset:0
global_load_lds_dwordx4 v6, s[40:41] offset:2048
ds_read_b64_tr_b16 a[14:15], v255 offset:2304
global_load_lds_dwordx4 v7, s[40:41] offset:3072
ds_read_b64_tr_b16 a[16:17], v255 offset:192
ds_read_b64_tr_b16 a[18:19], v255 offset:2496
s_add_u32 m0, s48, 86016
ds_read_b64_tr_b16 a[20:21], v255 offset:384
global_load_lds_dwordx4 v8, s[40:41] offset:-4096
ds_read_b64_tr_b16 a[22:23], v255 offset:2688
s_add_u32 s40, s40, 221184
ds_read_b128 v[170:173], v52 offset:0
s_addc_u32 s41, s41, 0
ds_read_b128 v[178:181], v52 offset:18432
s_waitcnt vmcnt(9) lgkmcnt(0)
s_barrier
s_add_u32 m0, s48, 4096
ds_read_b64_tr_b16 a[24:25], v255 offset:18432
v_mfma_f32_16x16x32_bf16 v[86:89], a[0:3], v[106:109], 0
v_mfma_f32_16x16x32_bf16 v[90:93], a[4:7], v[106:109], 0
global_load_lds_dwordx4 v0, s[40:41] offset:-4096
ds_read_b64_tr_b16 a[26:27], v255 offset:20736
v_mfma_f32_16x16x32_bf16 v[94:97], v[106:109], a[8:11], 0
ds_read_b64_tr_b16 a[28:29], v255 offset:18624
global_load_lds_dwordx4 v1, s[40:41] offset:-3072
v_mfma_f32_16x16x32_bf16 v[110:113], a[0:3], v[166:169], 0
ds_read_b64_tr_b16 a[30:31], v255 offset:20928
v_mfma_f32_16x16x32_bf16 v[114:117], a[4:7], v[166:169], 0
global_load_lds_dwordx4 v2, s[40:41] offset:-2048
ds_read_b64_tr_b16 a[32:33], v255 offset:18816
v_mfma_f32_16x16x32_bf16 v[118:121], v[166:169], a[8:11], 0
ds_read_b64_tr_b16 a[34:35], v255 offset:21120
global_load_lds_dwordx4 v3, s[40:41] offset:-1024
v_mfma_f32_16x16x32_bf16 v[134:137], v[166:169], v[166:169], 0
ds_read_b128 v[182:185], v51 offset:2048
v_mfma_f32_16x16x32_bf16 v[122:125], a[0:3], v[174:177], 0
global_load_lds_dwordx4 v4, s[40:41] offset:0
ds_read_b128 v[190:193], v51 offset:20480
v_mfma_f32_16x16x32_bf16 v[126:129], a[4:7], v[174:177], 0
ds_read_b64_tr_b16 a[36:37], v255 offset:36864
global_load_lds_dwordx4 v5, s[40:41] offset:1024
v_mfma_f32_16x16x32_bf16 v[130:133], v[174:177], a[8:11], 0
v_mfma_f32_16x16x32_bf16 v[158:161], v[174:177], v[174:177], 0
ds_read_b64_tr_b16 a[38:39], v255 offset:39168
global_load_lds_dwordx4 v6, s[40:41] offset:2048
v_mfma_f32_16x16x32_bf16 v[86:89], a[12:15], v[106:109], v[86:89]
ds_read_b64_tr_b16 a[40:41], v255 offset:37056
v_mfma_f32_16x16x32_bf16 v[90:93], a[16:19], v[106:109], v[90:93]
global_load_lds_dwordx4 v7, s[40:41] offset:3072
ds_read_b64_tr_b16 a[42:43], v255 offset:39360
v_mfma_f32_16x16x32_bf16 v[94:97], v[106:109], a[20:23], v[94:97]
s_add_u32 m0, s48, 12288
ds_read_b64_tr_b16 a[44:45], v255 offset:37248
v_mfma_f32_16x16x32_bf16 v[110:113], a[12:15], v[170:173], v[110:113]
ds_read_b64_tr_b16 a[46:47], v255 offset:39552
v_mfma_f32_16x16x32_bf16 v[114:117], a[16:19], v[170:173], v[114:117]
global_load_lds_dwordx4 v8, s[40:41] offset:-4096
ds_read_b128 v[186:189], v52 offset:2048
v_mfma_f32_16x16x32_bf16 v[118:121], v[170:173], a[20:23], v[118:121]
s_add_u32 s40, s40, 221184
ds_read_b128 v[194:197], v52 offset:20480
v_mfma_f32_16x16x32_bf16 v[134:137], v[170:173], v[170:173], v[134:137]
v_mfma_f32_16x16x32_bf16 v[122:125], a[12:15], v[178:181], v[122:125]
v_mfma_f32_16x16x32_bf16 v[126:129], a[16:19], v[178:181], v[126:129]
s_addc_u32 s41, s41, 0
v_mfma_f32_16x16x32_bf16 v[130:133], v[178:181], a[20:23], v[130:133]
v_mfma_f32_16x16x32_bf16 v[158:161], v[178:181], v[178:181], v[158:161]
s_waitcnt vmcnt(9) lgkmcnt(0)
s_barrier
s_add_u32 m0, s48, 40960
ds_read_b64_tr_b16 a[48:49], v255 offset:55296
v_mfma_f32_16x16x32_bf16 v[86:89], a[24:27], v[106:109], v[86:89]
v_mfma_f32_16x16x32_bf16 v[90:93], a[28:31], v[106:109], v[90:93]
global_load_lds_dwordx4 v0, s[40:41] offset:-4096
ds_read_b64_tr_b16 a[50:51], v255 offset:57600
v_mfma_f32_16x16x32_bf16 v[94:97], v[106:109], a[32:35], v[94:97]
ds_read_b64_tr_b16 a[52:53], v255 offset:55488
global_load_lds_dwordx4 v1, s[40:41] offset:-3072
v_mfma_f32_16x16x32_bf16 v[110:113], a[24:27], v[182:185], v[110:113]
ds_read_b64_tr_b16 a[54:55], v255 offset:57792
v_mfma_f32_16x16x32_bf16 v[114:117], a[28:31], v[182:185], v[114:117]
global_load_lds_dwordx4 v2, s[40:41] offset:-2048
ds_read_b64_tr_b16 a[56:57], v255 offset:55680
v_mfma_f32_16x16x32_bf16 v[118:121], v[182:185], a[32:35], v[118:121]
ds_read_b64_tr_b16 a[58:59], v255 offset:57984
global_load_lds_dwordx4 v3, s[40:41] offset:-1024
v_mfma_f32_16x16x32_bf16 v[134:137], v[182:185], v[182:185], v[134:137]
ds_read_b128 v[166:169], v51 offset:4096
v_mfma_f32_16x16x32_bf16 v[122:125], a[24:27], v[190:193], v[122:125]
global_load_lds_dwordx4 v4, s[40:41] offset:0
ds_read_b128 v[174:177], v51 offset:22528
v_mfma_f32_16x16x32_bf16 v[126:129], a[28:31], v[190:193], v[126:129]
ds_read_b64_tr_b16 a[60:61], v162 offset:8192
global_load_lds_dwordx4 v5, s[40:41] offset:1024
v_mfma_f32_16x16x32_bf16 v[130:133], v[190:193], a[32:35], v[130:133]
v_mfma_f32_16x16x32_bf16 v[158:161], v[190:193], v[190:193], v[158:161]
ds_read_b64_tr_b16 a[62:63], v162 offset:10496
global_load_lds_dwordx4 v6, s[40:41] offset:2048
v_mfma_f32_16x16x32_bf16 v[86:89], a[36:39], v[106:109], v[86:89]
ds_read_b64_tr_b16 a[64:65], v162 offset:8384
v_mfma_f32_16x16x32_bf16 v[90:93], a[40:43], v[106:109], v[90:93]
global_load_lds_dwordx4 v7, s[40:41] offset:3072
ds_read_b64_tr_b16 a[66:67], v162 offset:10688
v_mfma_f32_16x16x32_bf16 v[94:97], v[106:109], a[44:47], v[94:97]
s_add_u32 m0, s48, 49152
ds_read_b64_tr_b16 a[68:69], v162 offset:8576
v_mfma_f32_16x16x32_bf16 v[110:113], a[36:39], v[186:189], v[110:113]
ds_read_b64_tr_b16 a[70:71], v162 offset:10880
v_mfma_f32_16x16x32_bf16 v[114:117], a[40:43], v[186:189], v[114:117]
global_load_lds_dwordx4 v8, s[40:41] offset:-4096
ds_read_b128 v[170:173], v52 offset:4096
v_mfma_f32_16x16x32_bf16 v[118:121], v[186:189], a[44:47], v[118:121]
s_add_u32 s40, s40, 221184
ds_read_b128 v[178:181], v52 offset:22528
v_mfma_f32_16x16x32_bf16 v[134:137], v[186:189], v[186:189], v[134:137]
v_mfma_f32_16x16x32_bf16 v[122:125], a[36:39], v[194:197], v[122:125]
v_mfma_f32_16x16x32_bf16 v[126:129], a[40:43], v[194:197], v[126:129]
s_addc_u32 s41, s41, 0
v_mfma_f32_16x16x32_bf16 v[130:133], v[194:197], a[44:47], v[130:133]
v_mfma_f32_16x16x32_bf16 v[158:161], v[194:197], v[194:197], v[158:161]
s_waitcnt vmcnt(9) lgkmcnt(0)
s_barrier
s_add_u32 m0, s48, 77824
ds_read_b64_tr_b16 a[72:73], v9 offset:47104
v_mfma_f32_16x16x32_bf16 v[86:89], a[48:51], v[106:109], v[86:89]
v_mfma_f32_16x16x32_bf16 v[90:93], a[52:55], v[106:109], v[90:93]
global_load_lds_dwordx4 v0, s[40:41] offset:-4096
ds_read_b64_tr_b16 a[74:75], v9 offset:49408
v_mfma_f32_16x16x32_bf16 v[94:97], v[106:109], a[56:59], v[94:97]
ds_read_b64_tr_b16 a[76:77], v9 offset:47296
global_load_lds_dwordx4 v1, s[40:41] offset:-3072
v_mfma_f32_16x16x32_bf16 v[110:113], a[48:51], v[166:169], v[110:113]
ds_read_b64_tr_b16 a[78:79], v9 offset:49600
v_mfma_f32_16x16x32_bf16 v[114:117], a[52:55], v[166:169], v[114:117]
global_load_lds_dwordx4 v2, s[40:41] offset:-2048
ds_read_b64_tr_b16 a[80:81], v9 offset:47488
v_mfma_f32_16x16x32_bf16 v[118:121], v[166:169], a[56:59], v[118:121]
ds_read_b64_tr_b16 a[82:83], v9 offset:49792
global_load_lds_dwordx4 v3, s[40:41] offset:-1024
v_mfma_f32_16x16x32_bf16 v[134:137], v[166:169], v[166:169], v[134:137]
ds_read_b128 v[182:185], v51 offset:6144
v_mfma_f32_16x16x32_bf16 v[122:125], a[48:51], v[174:177], v[122:125]
global_load_lds_dwordx4 v4, s[40:41] offset:0
ds_read_b128 v[190:193], v51 offset:24576
v_mfma_f32_16x16x32_bf16 v[126:129], a[52:55], v[174:177], v[126:129]
ds_read_b64_tr_b16 a[84:85], v255 offset:0
global_load_lds_dwordx4 v5, s[40:41] offset:1024
v_mfma_f32_16x16x32_bf16 v[130:133], v[174:177], a[56:59], v[130:133]
v_mfma_f32_16x16x32_bf16 v[158:161], v[174:177], v[174:177], v[158:161]
ds_read_b64_tr_b16 a[86:87], v255 offset:2304
global_load_lds_dwordx4 v6, s[40:41] offset:2048
v_mfma_f32_16x16x32_bf16 v[86:89], a[60:63], v[106:109], v[86:89]
ds_read_b64_tr_b16 a[88:89], v255 offset:192
v_mfma_f32_16x16x32_bf16 v[90:93], a[64:67], v[106:109], v[90:93]
global_load_lds_dwordx4 v7, s[40:41] offset:3072
ds_read_b64_tr_b16 a[90:91], v255 offset:2496
v_mfma_f32_16x16x32_bf16 v[94:97], v[106:109], a[68:71], v[94:97]
s_add_u32 m0, s48, 86016
ds_read_b64_tr_b16 a[92:93], v255 offset:384
v_mfma_f32_16x16x32_bf16 v[110:113], a[60:63], v[170:173], v[110:113]
ds_read_b64_tr_b16 a[94:95], v255 offset:2688
v_mfma_f32_16x16x32_bf16 v[114:117], a[64:67], v[170:173], v[114:117]
global_load_lds_dwordx4 v8, s[40:41] offset:-4096
ds_read_b128 v[186:189], v52 offset:6144
v_mfma_f32_16x16x32_bf16 v[118:121], v[170:173], a[68:71], v[118:121]
s_add_u32 s40, s40, 221184
ds_read_b128 v[194:197], v52 offset:24576
v_mfma_f32_16x16x32_bf16 v[134:137], v[170:173], v[170:173], v[134:137]
v_mfma_f32_16x16x32_bf16 v[122:125], a[60:63], v[178:181], v[122:125]
v_mfma_f32_16x16x32_bf16 v[126:129], a[64:67], v[178:181], v[126:129]
s_addc_u32 s41, s41, 0
v_mfma_f32_16x16x32_bf16 v[130:133], v[178:181], a[68:71], v[130:133]
v_mfma_f32_16x16x32_bf16 v[158:161], v[178:181], v[178:181], v[158:161]
s_waitcnt vmcnt(9) lgkmcnt(0)
s_barrier
s_add_u32 m0, s48, 4096
ds_read_b64_tr_b16 a[96:97], v255 offset:18432
v_mfma_f32_16x16x32_bf16 v[86:89], a[72:75], v[106:109], v[86:89]
v_mfma_f32_16x16x32_bf16 v[90:93], a[76:79], v[106:109], v[90:93]
global_load_lds_dwordx4 v0, s[40:41] offset:-4096
ds_read_b64_tr_b16 a[98:99], v255 offset:20736
v_mfma_f32_16x16x32_bf16 v[94:97], v[106:109], a[80:83], v[94:97]
ds_read_b64_tr_b16 a[100:101], v255 offset:18624
global_load_lds_dwordx4 v1, s[40:41] offset:-3072
v_mfma_f32_16x16x32_bf16 v[110:113], a[72:75], v[182:185], v[110:113]
ds_read_b64_tr_b16 a[102:103], v255 offset:20928
v_mfma_f32_16x16x32_bf16 v[114:117], a[76:79], v[182:185], v[114:117]
global_load_lds_dwordx4 v2, s[40:41] offset:-2048
ds_read_b64_tr_b16 a[104:105], v255 offset:18816
v_mfma_f32_16x16x32_bf16 v[118:121], v[182:185], a[80:83], v[118:121]
ds_read_b64_tr_b16 a[106:107], v255 offset:21120
global_load_lds_dwordx4 v3, s[40:41] offset:-1024
v_mfma_f32_16x16x32_bf16 v[134:137], v[182:185], v[182:185], v[134:137]
ds_read_b128 v[166:169], v51 offset:8192
v_mfma_f32_16x16x32_bf16 v[122:125], a[72:75], v[190:193], v[122:125]
global_load_lds_dwordx4 v4, s[40:41] offset:0
ds_read_b128 v[174:177], v51 offset:26624
v_mfma_f32_16x16x32_bf16 v[126:129], a[76:79], v[190:193], v[126:129]
ds_read_b64_tr_b16 a[108:109], v255 offset:36864
global_load_lds_dwordx4 v5, s[40:41] offset:1024
v_mfma_f32_16x16x32_bf16 v[130:133], v[190:193], a[80:83], v[130:133]
v_mfma_f32_16x16x32_bf16 v[158:161], v[190:193], v[190:193], v[158:161]
ds_read_b64_tr_b16 a[110:111], v255 offset:39168
global_load_lds_dwordx4 v6, s[40:41] offset:2048
v_mfma_f32_16x16x32_bf16 v[86:89], a[84:87], v[106:109], v[86:89]
ds_read_b64_tr_b16 a[112:113], v255 offset:37056
v_mfma_f32_16x16x32_bf16 v[90:93], a[88:91], v[106:109], v[90:93]
global_load_lds_dwordx4 v7, s[40:41] offset:3072
ds_read_b64_tr_b16 a[114:115], v255 offset:39360
v_mfma_f32_16x16x32_bf16 v[94:97], v[106:109], a[92:95], v[94:97]
s_add_u32 m0, s48, 12288
ds_read_b64_tr_b16 a[116:117], v255 offset:37248
v_mfma_f32_16x16x32_bf16 v[110:113], a[84:87], v[186:189], v[110:113]
ds_read_b64_tr_b16 a[118:119], v255 offset:39552
v_mfma_f32_16x16x32_bf16 v[114:117], a[88:91], v[186:189], v[114:117]
global_load_lds_dwordx4 v8, s[40:41] offset:-4096
ds_read_b128 v[170:173], v52 offset:8192
v_mfma_f32_16x16x32_bf16 v[118:121], v[186:189], a[92:95], v[118:121]
s_add_u32 s40, s40, 221184
ds_read_b128 v[178:181], v52 offset:26624
v_mfma_f32_16x16x32_bf16 v[134:137], v[186:189], v[186:189], v[134:137]
v_mfma_f32_16x16x32_bf16 v[122:125], a[84:87], v[194:197], v[122:125]
v_mfma_f32_16x16x32_bf16 v[126:129], a[88:91], v[194:197], v[126:129]
s_addc_u32 s41, s41, 0
v_mfma_f32_16x16x32_bf16 v[130:133], v[194:197], a[92:95], v[130:133]
v_mfma_f32_16x16x32_bf16 v[158:161], v[194:197], v[194:197], v[158:161]
s_waitcnt vmcnt(9) lgkmcnt(0)
s_barrier
s_add_u32 m0, s48, 40960
ds_read_b64_tr_b16 a[120:121], v255 offset:55296
v_mfma_f32_16x16x32_bf16 v[86:89], a[96:99], v[106:109], v[86:89]
v_mfma_f32_16x16x32_bf16 v[90:93], a[100:103], v[106:109], v[90:93]
global_load_lds_dwordx4 v0, s[40:41] offset:-4096
ds_read_b64_tr_b16 a[122:123], v255 offset:57600
v_mfma_f32_16x16x32_bf16 v[94:97], v[106:109], a[104:107], v[94:97]
ds_read_b64_tr_b16 a[124:125], v255 offset:55488
global_load_lds_dwordx4 v1, s[40:41] offset:-3072
v_mfma_f32_16x16x32_bf16 v[110:113], a[96:99], v[166:169], v[110:113]
ds_read_b64_tr_b16 a[126:127], v255 offset:57792
v_mfma_f32_16x16x32_bf16 v[114:117], a[100:103], v[166:169], v[114:117]
global_load_lds_dwordx4 v2, s[40:41] offset:-2048
ds_read_b64_tr_b16 a[128:129], v255 offset:55680
v_mfma_f32_16x16x32_bf16 v[118:121], v[166:169], a[104:107], v[118:121]
ds_read_b64_tr_b16 a[130:131], v255 offset:57984
global_load_lds_dwordx4 v3, s[40:41] offset:-1024
v_mfma_f32_16x16x32_bf16 v[134:137], v[166:169], v[166:169], v[134:137]
ds_read_b128 v[182:185], v51 offset:10240
v_mfma_f32_16x16x32_bf16 v[122:125], a[96:99], v[174:177], v[122:125]
global_load_lds_dwordx4 v4, s[40:41] offset:0
ds_read_b128 v[190:193], v51 offset:28672
v_mfma_f32_16x16x32_bf16 v[126:129], a[100:103], v[174:177], v[126:129]
ds_read_b64_tr_b16 a[132:133], v162 offset:8192
global_load_lds_dwordx4 v5, s[40:41] offset:1024
v_mfma_f32_16x16x32_bf16 v[130:133], v[174:177], a[104:107], v[130:133]
v_mfma_f32_16x16x32_bf16 v[158:161], v[174:177], v[174:177], v[158:161]
ds_read_b64_tr_b16 a[134:135], v162 offset:10496
global_load_lds_dwordx4 v6, s[40:41] offset:2048
v_mfma_f32_16x16x32_bf16 v[86:89], a[108:111], v[106:109], v[86:89]
ds_read_b64_tr_b16 a[136:137], v162 offset:8384
v_mfma_f32_16x16x32_bf16 v[90:93], a[112:115], v[106:109], v[90:93]
global_load_lds_dwordx4 v7, s[40:41] offset:3072
ds_read_b64_tr_b16 a[138:139], v162 offset:10688
v_mfma_f32_16x16x32_bf16 v[94:97], v[106:109], a[116:119], v[94:97]
s_add_u32 m0, s48, 49152
ds_read_b64_tr_b16 a[140:141], v162 offset:8576
v_mfma_f32_16x16x32_bf16 v[110:113], a[108:111], v[170:173], v[110:113]
ds_read_b64_tr_b16 a[142:143], v162 offset:10880
v_mfma_f32_16x16x32_bf16 v[114:117], a[112:115], v[170:173], v[114:117]
global_load_lds_dwordx4 v8, s[40:41] offset:-4096
ds_read_b128 v[186:189], v52 offset:10240
v_mfma_f32_16x16x32_bf16 v[118:121], v[170:173], a[116:119], v[118:121]
s_add_u32 s40, s40, 221184
ds_read_b128 v[194:197], v52 offset:28672
v_mfma_f32_16x16x32_bf16 v[134:137], v[170:173], v[170:173], v[134:137]
v_mfma_f32_16x16x32_bf16 v[122:125], a[108:111], v[178:181], v[122:125]
v_mfma_f32_16x16x32_bf16 v[126:129], a[112:115], v[178:181], v[126:129]
s_addc_u32 s41, s41, 0
v_mfma_f32_16x16x32_bf16 v[130:133], v[178:181], a[116:119], v[130:133]
v_mfma_f32_16x16x32_bf16 v[158:161], v[178:181], v[178:181], v[158:161]
s_waitcnt vmcnt(9) lgkmcnt(0)
s_barrier
s_add_u32 m0, s48, 77824
ds_read_b64_tr_b16 a[144:145], v9 offset:47104
v_mfma_f32_16x16x32_bf16 v[86:89], a[120:123], v[106:109], v[86:89]
v_mfma_f32_16x16x32_bf16 v[90:93], a[124:127], v[106:109], v[90:93]
global_load_lds_dwordx4 v0, s[40:41] offset:-4096
ds_read_b64_tr_b16 a[146:147], v9 offset:49408
v_mfma_f32_16x16x32_bf16 v[94:97], v[106:109], a[128:131], v[94:97]
ds_read_b64_tr_b16 a[148:149], v9 offset:47296
global_load_lds_dwordx4 v1, s[40:41] offset:-3072
v_mfma_f32_16x16x32_bf16 v[110:113], a[120:123], v[182:185], v[110:113]
ds_read_b64_tr_b16 a[150:151], v9 offset:49600
v_mfma_f32_16x16x32_bf16 v[114:117], a[124:127], v[182:185], v[114:117]
global_load_lds_dwordx4 v2, s[40:41] offset:-2048
ds_read_b64_tr_b16 a[152:153], v9 offset:47488
v_mfma_f32_16x16x32_bf16 v[118:121], v[182:185], a[128:131], v[118:121]
ds_read_b64_tr_b16 a[154:155], v9 offset:49792
global_load_lds_dwordx4 v3, s[40:41] offset:-1024
v_mfma_f32_16x16x32_bf16 v[134:137], v[182:185], v[182:185], v[134:137]
ds_read_b128 v[166:169], v51 offset:12288
v_mfma_f32_16x16x32_bf16 v[122:125], a[120:123], v[190:193], v[122:125]
global_load_lds_dwordx4 v4, s[40:41] offset:0
ds_read_b128 v[174:177], v51 offset:30720
v_mfma_f32_16x16x32_bf16 v[126:129], a[124:127], v[190:193], v[126:129]
ds_read_b64_tr_b16 a[156:157], v255 offset:0
global_load_lds_dwordx4 v5, s[40:41] offset:1024
v_mfma_f32_16x16x32_bf16 v[130:133], v[190:193], a[128:131], v[130:133]
v_mfma_f32_16x16x32_bf16 v[158:161], v[190:193], v[190:193], v[158:161]
ds_read_b64_tr_b16 a[158:159], v255 offset:2304
global_load_lds_dwordx4 v6, s[40:41] offset:2048
v_mfma_f32_16x16x32_bf16 v[86:89], a[132:135], v[106:109], v[86:89]
ds_read_b64_tr_b16 a[160:161], v255 offset:192
v_mfma_f32_16x16x32_bf16 v[90:93], a[136:139], v[106:109], v[90:93]
global_load_lds_dwordx4 v7, s[40:41] offset:3072
ds_read_b64_tr_b16 a[162:163], v255 offset:2496
v_mfma_f32_16x16x32_bf16 v[94:97], v[106:109], a[140:143], v[94:97]
s_add_u32 m0, s48, 86016
ds_read_b64_tr_b16 a[164:165], v255 offset:384
v_mfma_f32_16x16x32_bf16 v[110:113], a[132:135], v[186:189], v[110:113]
ds_read_b64_tr_b16 a[166:167], v255 offset:2688
v_mfma_f32_16x16x32_bf16 v[114:117], a[136:139], v[186:189], v[114:117]
global_load_lds_dwordx4 v8, s[40:41] offset:-4096
ds_read_b128 v[170:173], v52 offset:12288
v_mfma_f32_16x16x32_bf16 v[118:121], v[186:189], a[140:143], v[118:121]
s_sub_u32 s40, s40, 1769472
ds_read_b128 v[178:181], v52 offset:30720
v_mfma_f32_16x16x32_bf16 v[134:137], v[186:189], v[186:189], v[134:137]
v_mfma_f32_16x16x32_bf16 v[122:125], a[132:135], v[194:197], v[122:125]
v_mfma_f32_16x16x32_bf16 v[126:129], a[136:139], v[194:197], v[126:129]
s_subb_u32 s41, s41, 0
v_mfma_f32_16x16x32_bf16 v[130:133], v[194:197], a[140:143], v[130:133]
v_mfma_f32_16x16x32_bf16 v[158:161], v[194:197], v[194:197], v[158:161]
s_waitcnt vmcnt(9) lgkmcnt(0)
s_barrier
s_add_u32 m0, s49, 0
ds_read_b64_tr_b16 a[168:169], v255 offset:18432
v_mfma_f32_16x16x32_bf16 v[86:89], a[144:147], v[106:109], v[86:89]
v_mfma_f32_16x16x32_bf16 v[90:93], a[148:151], v[106:109], v[90:93]
ds_read_b64_tr_b16 a[170:171], v255 offset:20736
v_mfma_f32_16x16x32_bf16 v[94:97], v[106:109], a[152:155], v[94:97]
ds_read_b64_tr_b16 a[172:173], v255 offset:18624
v_mfma_f32_16x16x32_bf16 v[110:113], a[144:147], v[166:169], v[110:113]
global_load_lds_dwordx4 v11, s[42:43] offset:-2048 sc1
ds_read_b64_tr_b16 a[174:175], v255 offset:20928
v_mfma_f32_16x16x32_bf16 v[114:117], a[148:151], v[166:169], v[114:117]
ds_read_b64_tr_b16 a[176:177], v255 offset:18816
v_mfma_f32_16x16x32_bf16 v[118:121], v[166:169], a[152:155], v[118:121]
ds_read_b64_tr_b16 a[178:179], v255 offset:21120
v_mfma_f32_16x16x32_bf16 v[134:137], v[166:169], v[166:169], v[134:137]
global_load_lds_dwordx4 v12, s[42:43] offset:-1024 sc1
ds_read_b128 v[182:185], v51 offset:14336
v_mfma_f32_16x16x32_bf16 v[122:125], a[144:147], v[174:177], v[122:125]
ds_read_b128 v[190:193], v51 offset:32768
v_mfma_f32_16x16x32_bf16 v[126:129], a[148:151], v[174:177], v[126:129]
ds_read_b64_tr_b16 a[180:181], v255 offset:36864
v_mfma_f32_16x16x32_bf16 v[130:133], v[174:177], a[152:155], v[130:133]
global_load_lds_dwordx4 v13, s[42:43] offset:0 sc1
v_mfma_f32_16x16x32_bf16 v[158:161], v[174:177], v[174:177], v[158:161]
ds_read_b64_tr_b16 a[182:183], v255 offset:39168
v_mfma_f32_16x16x32_bf16 v[86:89], a[156:159], v[106:109], v[86:89]
ds_read_b64_tr_b16 a[184:185], v255 offset:37056
v_mfma_f32_16x16x32_bf16 v[90:93], a[160:163], v[106:109], v[90:93]
ds_read_b64_tr_b16 a[186:187], v255 offset:39360
v_mfma_f32_16x16x32_bf16 v[94:97], v[106:109], a[164:167], v[94:97]
global_load_lds_dwordx4 v14, s[42:43] offset:1024 sc1
ds_read_b64_tr_b16 a[188:189], v255 offset:37248
v_mfma_f32_16x16x32_bf16 v[110:113], a[156:159], v[170:173], v[110:113]
ds_read_b64_tr_b16 a[190:191], v255 offset:39552
v_mfma_f32_16x16x32_bf16 v[114:117], a[160:163], v[170:173], v[114:117]
ds_read_b128 v[186:189], v52 offset:14336
v_mfma_f32_16x16x32_bf16 v[118:121], v[170:173], a[164:167], v[118:121]
s_sub_u32 s42, s42, 18432
s_subb_u32 s43, s43, 0
ds_read_b128 v[194:197], v52 offset:32768
v_mfma_f32_16x16x32_bf16 v[134:137], v[170:173], v[170:173], v[134:137]
v_mfma_f32_16x16x32_bf16 v[122:125], a[156:159], v[178:181], v[122:125]
v_mfma_f32_16x16x32_bf16 v[126:129], a[160:163], v[178:181], v[126:129]
v_mfma_f32_16x16x32_bf16 v[130:133], v[178:181], a[164:167], v[130:133]
v_mfma_f32_16x16x32_bf16 v[158:161], v[178:181], v[178:181], v[158:161]
s_waitcnt vmcnt(4) lgkmcnt(0)
s_barrier
s_add_u32 m0, s49, 18432
ds_read_b64_tr_b16 a[192:193], v255 offset:55296
)"
R"(v_mfma_f32_16x16x32_bf16 v[86:89], a[168:171], v[106:109], v[86:89]
v_mfma_f32_16x16x32_bf16 v[90:93], a[172:175], v[106:109], v[90:93]
ds_read_b64_tr_b16 a[194:195], v255 offset:57600
v_mfma_f32_16x16x32_bf16 v[94:97], v[106:109], a[176:179], v[94:97]
ds_read_b64_tr_b16 a[196:197], v255 offset:55488
v_mfma_f32_16x16x32_bf16 v[110:113], a[168:171], v[182:185], v[110:113]
global_load_lds_dwordx4 v11, s[42:43] offset:-2048 sc1
ds_read_b64_tr_b16 a[198:199], v255 offset:57792
v_mfma_f32_16x16x32_bf16 v[114:117], a[172:175], v[182:185], v[114:117]
ds_read_b64_tr_b16 a[200:201], v255 offset:55680
v_mfma_f32_16x16x32_bf16 v[118:121], v[182:185], a[176:179], v[118:121]
ds_read_b64_tr_b16 a[202:203], v255 offset:57984
v_mfma_f32_16x16x32_bf16 v[134:137], v[182:185], v[182:185], v[134:137]
global_load_lds_dwordx4 v12, s[42:43] offset:-1024 sc1
ds_read_b128 v[166:169], v51 offset:16384
v_mfma_f32_16x16x32_bf16 v[122:125], a[168:171], v[190:193], v[122:125]
ds_read_b128 v[174:177], v51 offset:34816
v_mfma_f32_16x16x32_bf16 v[126:129], a[172:175], v[190:193], v[126:129]
ds_read_b64_tr_b16 a[204:205], v162 offset:8192
v_mfma_f32_16x16x32_bf16 v[130:133], v[190:193], a[176:179], v[130:133]
global_load_lds_dwordx4 v13, s[42:43] offset:0 sc1
ds_read_b64_tr_b16 a[206:207], v162 offset:10496
v_mfma_f32_16x16x32_bf16 v[158:161], v[190:193], v[190:193], v[158:161]
ds_read_b64_tr_b16 a[208:209], v162 offset:8384
v_mfma_f32_16x16x32_bf16 v[86:89], a[180:183], v[106:109], v[86:89]
ds_read_b64_tr_b16 a[210:211], v162 offset:10688
v_mfma_f32_16x16x32_bf16 v[90:93], a[184:187], v[106:109], v[90:93]
global_load_lds_dwordx4 v14, s[42:43] offset:1024 sc1
ds_read_b64_tr_b16 a[212:213], v162 offset:8576
v_mfma_f32_16x16x32_bf16 v[94:97], v[106:109], a[188:191], v[94:97]
ds_read_b64_tr_b16 a[214:215], v162 offset:10880
v_mfma_f32_16x16x32_bf16 v[110:113], a[180:183], v[186:189], v[110:113]
ds_read_b128 v[170:173], v52 offset:16384
v_mfma_f32_16x16x32_bf16 v[114:117], a[184:187], v[186:189], v[114:117]
s_sub_u32 s42, s42, 18432
s_subb_u32 s43, s43, 0
ds_read_b128 v[178:181], v52 offset:34816
v_mfma_f32_16x16x32_bf16 v[118:121], v[186:189], a[188:191], v[118:121]
v_mfma_f32_16x16x32_bf16 v[134:137], v[186:189], v[186:189], v[134:137]
v_mfma_f32_16x16x32_bf16 v[122:125], a[180:183], v[194:197], v[122:125]
v_mfma_f32_16x16x32_bf16 v[126:129], a[184:187], v[194:197], v[126:129]
v_mfma_f32_16x16x32_bf16 v[158:161], v[194:197], v[194:197], v[158:161]
s_waitcnt lgkmcnt(14)
v_mfma_f32_16x16x32_bf16 v[86:89], a[192:195], v[106:109], v[86:89]
s_waitcnt lgkmcnt(12)
v_mfma_f32_16x16x32_bf16 v[90:93], a[196:199], v[106:109], v[90:93]
s_waitcnt lgkmcnt(10)
v_mfma_f32_16x16x32_bf16 v[94:97], v[106:109], a[200:203], v[94:97]
s_waitcnt lgkmcnt(9)
v_mfma_f32_16x16x32_bf16 v[110:113], a[192:195], v[166:169], v[110:113]
v_mfma_f32_16x16x32_bf16 v[114:117], a[196:199], v[166:169], v[114:117]
v_mfma_f32_16x16x32_bf16 v[118:121], v[166:169], a[200:203], v[118:121]
v_mfma_f32_16x16x32_bf16 v[134:137], v[166:169], v[166:169], v[134:137]
s_waitcnt lgkmcnt(8)
v_mfma_f32_16x16x32_bf16 v[158:161], v[174:177], v[174:177], v[158:161]
s_waitcnt lgkmcnt(6)
v_mfma_f32_16x16x32_bf16 v[86:89], a[204:207], v[106:109], v[86:89]
s_waitcnt lgkmcnt(4)
v_mfma_f32_16x16x32_bf16 v[90:93], a[208:211], v[106:109], v[90:93]
s_waitcnt lgkmcnt(2)
v_mfma_f32_16x16x32_bf16 v[94:97], v[106:109], a[212:215], v[94:97]
s_waitcnt lgkmcnt(1)
v_mfma_f32_16x16x32_bf16 v[110:113], a[204:207], v[170:173], v[110:113]
v_mfma_f32_16x16x32_bf16 v[114:117], a[208:211], v[170:173], v[114:117]
v_mfma_f32_16x16x32_bf16 v[118:121], v[170:173], a[212:215], v[118:121]
v_mfma_f32_16x16x32_bf16 v[134:137], v[170:173], v[170:173], v[134:137]
s_waitcnt lgkmcnt(0)
v_mfma_f32_16x16x32_bf16 v[158:161], v[178:181], v[178:181], v[158:161]
s_nop 5
ds_write_b128 v23, v[134:137] offset:41984
s_waitcnt lgkmcnt(0)
s_barrier
ds_read_b128 v[166:169], v51 offset:32768
ds_read_b128 v[170:173], v52 offset:32768
ds_read_b128 v[174:177], v51 offset:34816
ds_read_b128 v[178:181], v52 offset:34816
v_mov_b32 v202, v32
v_mov_b32 v203, v33
v_mov_b32 v204, v34
v_mov_b32 v205, v35
v_mov_b32 v134, v158
v_mov_b32 v135, v159
v_mov_b32 v136, v160
v_mov_b32 v137, v161
s_barrier
ds_read_b32 v26, v24 offset:0
ds_read_b32 v148, v21
ds_read_b32 v95, v24 offset:192
ds_read_b32 v255, v27
ds_read_b32 v8, v28
ds_read_b128 v[194:197], v25 offset:0
ds_read_b64 v[158:159], v22 offset:41472
ds_read_b32 v140, v20
v_xor_b32 v51, 0x10000, v51
v_xor_b32 v52, 0x10000, v52
ds_write_b128 v23, v[134:137] offset:41984
s_waitcnt lgkmcnt(8)
v_fma_f32 v206, v90, v26, v114
v_fma_f32 v207, v91, v26, v115
v_fma_f32 v208, v92, v26, v116
v_fma_f32 v209, v93, v26, v117
v_cvt_pk_bf16_f32 v146, v206, v207
v_cvt_pk_bf16_f32 v147, v208, v209
s_waitcnt lgkmcnt(0)
s_barrier
v_fma_f32 v206, v94, v194, v118
v_fma_f32 v207, v94, v195, v119
v_fma_f32 v208, v94, v196, v120
v_fma_f32 v209, v94, v197, v121
v_cvt_pk_bf16_f32 v154, v206, v207
v_cvt_pk_bf16_f32 v155, v208, v209
v_fma_f32 v206, v86, v26, v110
v_fma_f32 v207, v87, v26, v111
v_fma_f32 v208, v88, v26, v112
v_fma_f32 v209, v89, v26, v113
v_cvt_pk_bf16_f32 v138, v206, v207
v_cvt_pk_bf16_f32 v139, v208, v209
s_waitcnt vmcnt(4) lgkmcnt(0)
s_barrier
s_mov_b32 s70, 32
s_waitcnt lgkmcnt(0)
s_nop 1
s_branch .Lqa_in27
.p2align 6
.Lqa_top41:
s_waitcnt lgkmcnt(9)
v_mfma_f32_16x16x32_bf16 v[134:137], v[0:3], v[0:3], 0
s_waitcnt lgkmcnt(8)
ds_read_b128 v[54:57], v52 offset:4096
ds_read_b32 v8, v30
v_mfma_f32_16x16x32_bf16 v[134:137], v[4:7], v[4:7], v[134:137]
ds_read_b128 v[194:197], v25 offset:64
ds_read_b64 v[160:161], v22 offset:41472
v_mfma_f32_16x16x32_bf16 v[110:113], a[0:3], v[0:3], 0
s_waitcnt lgkmcnt(8)
ds_read_b128 v[58:61], v51 offset:6144
ds_read_b32 v144, v20
v_mfma_f32_16x16x32_bf16 v[134:137], v[96:99], v[96:99], v[134:137]
s_waitcnt lgkmcnt(8)
ds_read_b128 v[62:65], v52 offset:6144
v_add_u32 v46, 1, v46
v_mfma_f32_16x16x32_bf16 v[134:137], v[100:103], v[100:103], v[134:137]
s_mov_b64 exec, 65535
global_store_dwordx4 v16, v[198:201], s[38:39] sc1
s_mov_b64 exec, -1
v_mfma_f32_16x16x32_bf16 v[114:117], a[4:7], v[0:3], 0
s_waitcnt lgkmcnt(7)
ds_read_b128 v[66:69], v51 offset:8192
v_add_u32 v16, 18432, v16
v_mfma_f32_16x16x32_bf16 v[134:137], v[104:107], v[104:107], v[134:137]
s_waitcnt lgkmcnt(7)
ds_read_b128 v[70:73], v52 offset:8192
v_fma_f32 v206, v90, v26, v126
v_mfma_f32_16x16x32_bf16 v[134:137], v[54:57], v[54:57], v[134:137]
v_fma_f32 v207, v91, v26, v127
v_fma_f32 v208, v92, v26, v128
v_mfma_f32_16x16x32_bf16 v[118:121], v[0:3], a[8:11], 0
s_waitcnt lgkmcnt(4)
ds_read_b128 v[74:77], v51 offset:10240
v_fma_f32 v209, v93, v26, v129
v_mfma_f32_16x16x32_bf16 v[134:137], v[58:61], v[58:61], v[134:137]
s_waitcnt lgkmcnt(3)
ds_read_b128 v[78:81], v52 offset:10240
v_cvt_pk_bf16_f32 v150, v206, v207
v_mfma_f32_16x16x32_bf16 v[134:137], v[62:65], v[62:65], v[134:137]
v_cvt_pk_bf16_f32 v151, v208, v209
v_max_f32 v47, v186, v187
v_mfma_f32_16x16x32_bf16 v[110:113], a[12:15], v[4:7], v[110:113]
ds_read_b128 v[82:85], v51 offset:12288
v_mfma_f32_16x16x32_bf16 v[182:185], v[150:153], v[138:141], v[32:35]
s_waitcnt lgkmcnt(4)
v_max_f32 v48, v188, v189
v_fma_f32 v206, v94, v194, v130
v_mfma_f32_16x16x32_bf16 v[134:137], v[66:69], v[66:69], v[134:137]
s_xor_b32 s49, s49, 0x10000
ds_read_b128 v[162:165], v52 offset:12288
s_waitcnt lgkmcnt(4)
v_mfma_f32_16x16x32_bf16 v[134:137], v[70:73], v[70:73], v[134:137]
s_add_u32 m0, s49, 0
v_mul_f32 v95, 0x3eae1a93, v95
v_mul_f32 v9, v255, v95
v_mfma_f32_16x16x32_bf16 v[114:117], a[16:19], v[4:7], v[114:117]
s_waitcnt lgkmcnt(3)
ds_read_b128 v[166:169], v51 offset:14336
v_mul_f32 v10, v8, v95
v_mfma_f32_16x16x32_bf16 v[134:137], v[74:77], v[74:77], v[134:137]
s_waitcnt lgkmcnt(3)
ds_read_b128 v[170:173], v52 offset:14336
v_max3_f32 v47, v182, v183, v47
v_mfma_f32_16x16x32_bf16 v[134:137], v[78:81], v[78:81], v[134:137]
v_max3_f32 v48, v184, v185, v48
v_mul_f32 v47, v47, v9
v_mfma_f32_16x16x32_bf16 v[118:121], v[4:7], a[20:23], v[118:121]
s_waitcnt lgkmcnt(3)
ds_read_b128 v[174:177], v51 offset:16384
v_mul_f32 v48, v48, v10
v_mfma_f32_16x16x32_bf16 v[134:137], v[82:85], v[82:85], v[134:137]
s_waitcnt lgkmcnt(3)
ds_read_b128 v[178:181], v52 offset:16384
global_load_lds_dwordx4 v11, s[42:43] offset:-2048 sc1
v_mfma_f32_16x16x32_bf16 v[134:137], v[162:165], v[162:165], v[134:137]
v_max_f32 v49, v47, v48
v_mov_b32 v50, v49
v_mfma_f32_16x16x32_bf16 v[110:113], a[24:27], v[96:99], v[110:113]
s_waitcnt lgkmcnt(3)
v_permlane16_swap_b32 v49, v50
v_mfma_f32_16x16x32_bf16 v[134:137], v[166:169], v[166:169], v[134:137]
s_waitcnt lgkmcnt(2)
v_max_f32 v49, v49, v50
v_mov_b32 v50, v49
v_mfma_f32_16x16x32_bf16 v[134:137], v[170:173], v[170:173], v[134:137]
s_nop 0
v_permlane32_swap_b32 v49, v50
v_mfma_f32_16x16x32_bf16 v[114:117], a[28:31], v[96:99], v[114:117]
s_waitcnt lgkmcnt(1)
v_max_f32 v49, v49, v50
v_sub_f32 v47, v47, v49
v_mfma_f32_16x16x32_bf16 v[134:137], v[174:177], v[174:177], v[134:137]
s_waitcnt lgkmcnt(0)
v_sub_f32 v48, v48, v49
v_fma_f32 v207, v94, v195, v131
v_mfma_f32_16x16x32_bf16 v[134:137], v[178:181], v[178:181], v[134:137]
v_exp_f32 v210, v47
v_mfma_f32_16x16x32_bf16 v[118:121], v[96:99], a[32:35], v[118:121]
global_load_lds_dwordx4 v12, s[42:43] offset:-1024 sc1
v_fma_f32 v208, v94, v196, v132
v_mfma_f32_16x16x32_bf16 v[110:113], a[36:39], v[100:103], v[110:113]
v_exp_f32 v211, v48
v_mfma_f32_16x16x32_bf16 v[114:117], a[40:43], v[100:103], v[114:117]
v_add_f32 v49, v210, v211
v_mov_b32 v50, v49
v_mfma_f32_16x16x32_bf16 v[118:121], v[100:103], a[44:47], v[118:121]
ds_write_b128 v23, v[134:137] offset:41984
v_mfma_f32_16x16x32_bf16 v[110:113], a[48:51], v[104:107], v[110:113]
v_mul_f32 v210, v210, v255
v_fma_f32 v209, v94, v197, v133
v_mfma_f32_16x16x32_bf16 v[114:117], a[52:55], v[104:107], v[114:117]
v_permlane16_swap_b32 v49, v50
v_mfma_f32_16x16x32_bf16 v[118:121], v[104:107], a[56:59], v[118:121]
v_add_f32 v49, v49, v50
v_mov_b32 v50, v49
v_mfma_f32_16x16x32_bf16 v[110:113], a[60:63], v[54:57], v[110:113]
v_mul_f32 v211, v211, v8
v_cvt_pk_bf16_f32 v156, v206, v207
v_mfma_f32_16x16x32_bf16 v[114:117], a[64:67], v[54:57], v[114:117]
global_load_lds_dwordx4 v13, s[42:43] offset:0 sc1
v_cvt_pk_bf16_f32 v157, v208, v209
v_mfma_f32_16x16x32_bf16 v[118:121], v[54:57], a[68:71], v[118:121]
v_mfma_f32_16x16x32_bf16 v[110:113], a[72:75], v[58:61], v[110:113]
s_waitcnt lgkmcnt(0)
s_barrier
v_mfma_f32_16x16x32_bf16 v[114:117], a[76:79], v[58:61], v[114:117]
v_permlane32_swap_b32 v49, v50
v_mfma_f32_16x16x32_bf16 v[118:121], v[58:61], a[80:83], v[118:121]
v_add_f32 v49, v49, v50
v_fma_f32 v206, v86, v26, v122
v_mfma_f32_16x16x32_bf16 v[110:113], a[84:87], v[62:65], v[110:113]
v_rcp_f32 v49, v49
v_mfma_f32_16x16x32_bf16 v[114:117], a[88:91], v[62:65], v[114:117]
v_mul_f32 v210, v210, v49
v_mul_f32 v211, v211, v49
v_mfma_f32_16x16x32_bf16 v[118:121], v[62:65], a[92:95], v[118:121]
v_cvt_pk_bf16_f32 v210, v210, v210
v_cvt_pk_bf16_f32 v211, v211, v211
v_mfma_f32_16x16x32_bf16 v[110:113], a[96:99], v[66:69], v[110:113]
global_load_lds_dwordx4 v14, s[42:43] offset:1024 sc1
s_add_u32 s42, s42, s46
s_addc_u32 s43, s43, s47
v_mfma_f32_16x16x32_bf16 v[114:117], a[100:103], v[66:69], v[114:117]
v_and_b32 v192, v210, v40
v_and_b32 v193, v211, v41
v_mfma_f32_16x16x32_bf16 v[118:121], v[66:69], a[104:107], v[118:121]
v_and_b32 v190, v210, v42
v_and_b32 v191, v211, v43
v_mfma_f32_16x16x32_bf16 v[110:113], a[108:111], v[70:73], v[110:113]
s_nop 0
v_mfma_f32_16x16x32_bf16 v[186:189], v[154:157], v[190:193], 0
v_mfma_f32_16x16x32_bf16 v[182:185], v[158:161], v[190:193], 0
v_fma_f32 v207, v87, v26, v123
v_fma_f32 v208, v88, v26, v124
v_mfma_f32_16x16x32_bf16 v[114:117], a[112:115], v[70:73], v[114:117]
v_fma_f32 v209, v89, v26, v125
v_cvt_pk_bf16_f32 v142, v206, v207
v_mfma_f32_16x16x32_bf16 v[118:121], v[70:73], a[116:119], v[118:121]
v_cvt_pk_bf16_f32 v143, v208, v209
v_mfma_f32_16x16x32_bf16 v[110:113], a[120:123], v[74:77], v[110:113]
v_mfma_f32_16x16x32_bf16 v[114:117], a[124:127], v[74:77], v[114:117]
v_cvt_pk_bf16_f32 v108, v186, v187
v_cvt_pk_bf16_f32 v109, v188, v189
v_mfma_f32_16x16x32_bf16 v[118:121], v[74:77], a[128:131], v[118:121]
ds_write_b64 v18, v[108:109]
v_mfma_f32_16x16x32_bf16 v[110:113], a[132:135], v[78:81], v[110:113]
v_cvt_pk_bf16_f32 v210, v182, v183
v_mfma_f32_16x16x32_bf16 v[114:117], a[136:139], v[78:81], v[114:117]
ds_write_b32 v19, v210
v_mfma_f32_16x16x32_bf16 v[118:121], v[78:81], a[140:143], v[118:121]
v_mfma_f32_16x16x32_bf16 v[110:113], a[144:147], v[82:85], v[110:113]
v_mfma_f32_16x16x32_bf16 v[114:117], a[148:151], v[82:85], v[114:117]
v_mfma_f32_16x16x32_bf16 v[118:121], v[82:85], a[152:155], v[118:121]
v_mfma_f32_16x16x32_bf16 v[110:113], a[156:159], v[162:165], v[110:113]
v_add_lshl_u32 v253, v46, 1, 7
v_bfi_b32 v253, s69, v253, v44
v_mfma_f32_16x16x32_bf16 v[114:117], a[160:163], v[162:165], v[114:117]
global_load_dword v254, v253, s[38:39] sc1
v_mfma_f32_16x16x32_bf16 v[118:121], v[162:165], a[164:167], v[118:121]
s_waitcnt vmcnt(6)
v_sub_u32 v53, v53, v46
v_cmp_lt_u32 vcc, 0x7f, v53
v_mfma_f32_16x16x32_bf16 v[110:113], a[168:171], v[166:169], v[110:113]
v_mfma_f32_16x16x32_bf16 v[114:117], a[172:175], v[166:169], v[114:117]
v_mfma_f32_16x16x32_bf16 v[118:121], v[166:169], a[176:179], v[118:121]
s_and_b64 vcc, vcc, s[76:77]
s_cbranch_vccnz .Lqa_sp30
.Lqa_rt31:
v_mfma_f32_16x16x32_bf16 v[110:113], a[180:183], v[170:173], v[110:113]
v_mfma_f32_16x16x32_bf16 v[114:117], a[184:187], v[170:173], v[114:117]
s_waitcnt vmcnt(5) lgkmcnt(0)
s_barrier
v_mfma_f32_16x16x32_bf16 v[118:121], v[170:173], a[188:191], v[118:121]
ds_read_b128 v[0:3], v51 offset:18432
v_mfma_f32_16x16x32_bf16 v[110:113], a[192:195], v[174:177], v[110:113]
ds_read_b128 v[4:7], v52 offset:18432
ds_read_b128 v[198:201], v17 offset:0
v_mfma_f32_16x16x32_bf16 v[114:117], a[196:199], v[174:177], v[114:117]
ds_read_b32 v26, v24 offset:0
ds_read_b32 v148, v21
v_mfma_f32_16x16x32_bf16 v[186:189], v[150:153], v[142:145], v[36:39]
v_mfma_f32_16x16x32_bf16 v[118:121], v[174:177], a[200:203], v[118:121]
ds_read_b128 v[96:99], v51 offset:20480
ds_read_b32 v95, v24 offset:192
v_mfma_f32_16x16x32_bf16 v[110:113], a[204:207], v[178:181], v[110:113]
ds_read_b128 v[100:103], v52 offset:20480
ds_read_b32 v255, v27
v_mfma_f32_16x16x32_bf16 v[114:117], a[208:211], v[178:181], v[114:117]
ds_read_b32 v8, v28
ds_read_b128 v[194:197], v25 offset:0
v_mfma_f32_16x16x32_bf16 v[118:121], v[178:181], a[212:215], v[118:121]
s_waitcnt lgkmcnt(10)
ds_read_b128 v[104:107], v51 offset:22528
ds_read_b64 v[158:159], v22 offset:41472
v_mfma_f32_16x16x32_bf16 v[134:137], v[0:3], v[0:3], 0
s_waitcnt lgkmcnt(11)
ds_read_b128 v[54:57], v52 offset:22528
ds_read_b32 v140, v20
v_mfma_f32_16x16x32_bf16 v[134:137], v[4:7], v[4:7], v[134:137]
v_add_u32 v46, 1, v46
v_max_f32 v47, v186, v187
v_mfma_f32_16x16x32_bf16 v[122:125], a[0:3], v[0:3], 0
s_waitcnt lgkmcnt(11)
ds_read_b128 v[58:61], v51 offset:24576
v_fma_f32 v206, v90, v26, v114
s_waitcnt lgkmcnt(10)
v_mfma_f32_16x16x32_bf16 v[134:137], v[96:99], v[96:99], v[134:137]
s_waitcnt lgkmcnt(8)
ds_read_b128 v[62:65], v52 offset:24576
v_fma_f32 v207, v91, v26, v115
v_mfma_f32_16x16x32_bf16 v[134:137], v[100:103], v[100:103], v[134:137]
s_mov_b64 exec, 65535
global_store_dwordx4 v16, v[198:201], s[38:39] sc1
s_mov_b64 exec, -1
v_mfma_f32_16x16x32_bf16 v[126:129], a[4:7], v[0:3], 0
s_waitcnt lgkmcnt(5)
ds_read_b128 v[66:69], v51 offset:26624
v_add_u32 v16, 18432, v16
v_mfma_f32_16x16x32_bf16 v[134:137], v[104:107], v[104:107], v[134:137]
s_waitcnt lgkmcnt(4)
ds_read_b128 v[70:73], v52 offset:26624
v_fma_f32 v208, v92, v26, v116
v_mfma_f32_16x16x32_bf16 v[134:137], v[54:57], v[54:57], v[134:137]
v_fma_f32 v209, v93, v26, v117
v_cvt_pk_bf16_f32 v146, v206, v207
v_mfma_f32_16x16x32_bf16 v[130:133], v[0:3], a[8:11], 0
s_waitcnt lgkmcnt(3)
ds_read_b128 v[74:77], v51 offset:28672
v_cvt_pk_bf16_f32 v147, v208, v209
v_mfma_f32_16x16x32_bf16 v[134:137], v[58:61], v[58:61], v[134:137]
ds_read_b128 v[78:81], v52 offset:28672
v_mfma_f32_16x16x32_bf16 v[182:185], v[146:149], v[142:145], v[202:205]
s_waitcnt lgkmcnt(4)
v_max_f32 v48, v188, v189
v_fma_f32 v206, v94, v194, v118
v_mfma_f32_16x16x32_bf16 v[134:137], v[62:65], v[62:65], v[134:137]
v_fma_f32 v207, v94, v195, v119
v_fma_f32 v208, v94, v196, v120
v_mfma_f32_16x16x32_bf16 v[122:125], a[12:15], v[4:7], v[122:125]
s_waitcnt lgkmcnt(3)
ds_read_b128 v[82:85], v51 offset:30720
v_mul_f32 v95, 0x3eae1a93, v95
v_mfma_f32_16x16x32_bf16 v[134:137], v[66:69], v[66:69], v[134:137]
s_add_u32 m0, s49, 18432
ds_read_b128 v[162:165], v52 offset:30720
s_waitcnt lgkmcnt(4)
v_mfma_f32_16x16x32_bf16 v[134:137], v[70:73], v[70:73], v[134:137]
v_mul_f32 v9, v255, v95
v_mul_f32 v10, v8, v95
v_mfma_f32_16x16x32_bf16 v[126:129], a[16:19], v[4:7], v[126:129]
s_waitcnt lgkmcnt(3)
ds_read_b128 v[166:169], v51 offset:32768
v_max3_f32 v47, v182, v183, v47
v_mfma_f32_16x16x32_bf16 v[134:137], v[74:77], v[74:77], v[134:137]
)"
R"(s_waitcnt lgkmcnt(3)
ds_read_b128 v[170:173], v52 offset:32768
v_max3_f32 v48, v184, v185, v48
v_mfma_f32_16x16x32_bf16 v[134:137], v[78:81], v[78:81], v[134:137]
v_mul_f32 v47, v47, v9
v_mul_f32 v48, v48, v10
v_mfma_f32_16x16x32_bf16 v[130:133], v[4:7], a[20:23], v[130:133]
s_waitcnt lgkmcnt(3)
ds_read_b128 v[174:177], v51 offset:34816
global_load_lds_dwordx4 v11, s[42:43] offset:-2048 sc1
v_mfma_f32_16x16x32_bf16 v[134:137], v[82:85], v[82:85], v[134:137]
s_waitcnt lgkmcnt(3)
ds_read_b128 v[178:181], v52 offset:34816
v_max_f32 v49, v47, v48
v_mfma_f32_16x16x32_bf16 v[134:137], v[162:165], v[162:165], v[134:137]
v_mov_b32 v50, v49
v_fma_f32 v209, v94, v197, v121
v_mfma_f32_16x16x32_bf16 v[122:125], a[24:27], v[96:99], v[122:125]
s_waitcnt lgkmcnt(3)
v_permlane16_swap_b32 v49, v50
v_mfma_f32_16x16x32_bf16 v[134:137], v[166:169], v[166:169], v[134:137]
s_waitcnt lgkmcnt(2)
v_max_f32 v49, v49, v50
v_mov_b32 v50, v49
v_mfma_f32_16x16x32_bf16 v[134:137], v[170:173], v[170:173], v[134:137]
s_nop 0
v_permlane32_swap_b32 v49, v50
v_mfma_f32_16x16x32_bf16 v[126:129], a[28:31], v[96:99], v[126:129]
s_waitcnt lgkmcnt(1)
v_max_f32 v49, v49, v50
v_sub_f32 v47, v47, v49
v_mfma_f32_16x16x32_bf16 v[134:137], v[174:177], v[174:177], v[134:137]
s_waitcnt lgkmcnt(0)
v_sub_f32 v48, v48, v49
v_cvt_pk_bf16_f32 v154, v206, v207
v_mfma_f32_16x16x32_bf16 v[134:137], v[178:181], v[178:181], v[134:137]
global_load_lds_dwordx4 v12, s[42:43] offset:-1024 sc1
v_cvt_pk_bf16_f32 v155, v208, v209
v_mfma_f32_16x16x32_bf16 v[130:133], v[96:99], a[32:35], v[130:133]
v_exp_f32 v210, v47
v_mfma_f32_16x16x32_bf16 v[122:125], a[36:39], v[100:103], v[122:125]
v_exp_f32 v211, v48
v_mfma_f32_16x16x32_bf16 v[126:129], a[40:43], v[100:103], v[126:129]
v_add_f32 v49, v210, v211
v_mov_b32 v50, v49
v_mfma_f32_16x16x32_bf16 v[130:133], v[100:103], a[44:47], v[130:133]
ds_write_b128 v23, v[134:137] offset:41984
v_mfma_f32_16x16x32_bf16 v[122:125], a[48:51], v[104:107], v[122:125]
v_mul_f32 v210, v210, v255
v_fma_f32 v206, v86, v26, v110
v_mfma_f32_16x16x32_bf16 v[126:129], a[52:55], v[104:107], v[126:129]
v_permlane16_swap_b32 v49, v50
v_mfma_f32_16x16x32_bf16 v[130:133], v[104:107], a[56:59], v[130:133]
v_add_f32 v49, v49, v50
v_mov_b32 v50, v49
v_mfma_f32_16x16x32_bf16 v[122:125], a[60:63], v[54:57], v[122:125]
global_load_lds_dwordx4 v13, s[42:43] offset:0 sc1
v_mul_f32 v211, v211, v8
v_mfma_f32_16x16x32_bf16 v[126:129], a[64:67], v[54:57], v[126:129]
v_permlane32_swap_b32 v49, v50
v_mfma_f32_16x16x32_bf16 v[130:133], v[54:57], a[68:71], v[130:133]
v_mfma_f32_16x16x32_bf16 v[122:125], a[72:75], v[58:61], v[122:125]
s_waitcnt lgkmcnt(0)
s_barrier
v_mfma_f32_16x16x32_bf16 v[126:129], a[76:79], v[58:61], v[126:129]
v_add_f32 v49, v49, v50
v_fma_f32 v207, v87, v26, v111
v_mfma_f32_16x16x32_bf16 v[130:133], v[58:61], a[80:83], v[130:133]
v_rcp_f32 v49, v49
v_mfma_f32_16x16x32_bf16 v[122:125], a[84:87], v[62:65], v[122:125]
v_mul_f32 v210, v210, v49
v_mul_f32 v211, v211, v49
v_mfma_f32_16x16x32_bf16 v[126:129], a[88:91], v[62:65], v[126:129]
v_cvt_pk_bf16_f32 v210, v210, v210
v_cvt_pk_bf16_f32 v211, v211, v211
v_mfma_f32_16x16x32_bf16 v[130:133], v[62:65], a[92:95], v[130:133]
global_load_lds_dwordx4 v14, s[42:43] offset:1024 sc1
s_sub_u32 s42, s42, 18432
s_subb_u32 s43, s43, 0
v_mfma_f32_16x16x32_bf16 v[122:125], a[96:99], v[66:69], v[122:125]
v_and_b32 v190, v210, v40
v_and_b32 v191, v211, v41
v_mfma_f32_16x16x32_bf16 v[126:129], a[100:103], v[66:69], v[126:129]
v_and_b32 v192, v210, v42
v_and_b32 v193, v211, v43
v_mfma_f32_16x16x32_bf16 v[130:133], v[66:69], a[104:107], v[130:133]
s_nop 0
v_mfma_f32_16x16x32_bf16 v[186:189], v[154:157], v[190:193], 0
v_mfma_f32_16x16x32_bf16 v[182:185], v[158:161], v[190:193], 0
v_fma_f32 v208, v88, v26, v112
v_fma_f32 v209, v89, v26, v113
v_mfma_f32_16x16x32_bf16 v[122:125], a[108:111], v[70:73], v[122:125]
v_cvt_pk_bf16_f32 v138, v206, v207
v_cvt_pk_bf16_f32 v139, v208, v209
v_mfma_f32_16x16x32_bf16 v[126:129], a[112:115], v[70:73], v[126:129]
v_mfma_f32_16x16x32_bf16 v[130:133], v[70:73], a[116:119], v[130:133]
v_mfma_f32_16x16x32_bf16 v[122:125], a[120:123], v[74:77], v[122:125]
v_cvt_pk_bf16_f32 v108, v186, v187
v_cvt_pk_bf16_f32 v109, v188, v189
v_mfma_f32_16x16x32_bf16 v[126:129], a[124:127], v[74:77], v[126:129]
ds_write_b64 v18, v[108:109]
v_mfma_f32_16x16x32_bf16 v[130:133], v[74:77], a[128:131], v[130:133]
v_cvt_pk_bf16_f32 v210, v182, v183
v_mfma_f32_16x16x32_bf16 v[122:125], a[132:135], v[78:81], v[122:125]
ds_write_b32 v19, v210
v_mfma_f32_16x16x32_bf16 v[126:129], a[136:139], v[78:81], v[126:129]
v_mfma_f32_16x16x32_bf16 v[130:133], v[78:81], a[140:143], v[130:133]
v_mfma_f32_16x16x32_bf16 v[122:125], a[144:147], v[82:85], v[122:125]
v_mfma_f32_16x16x32_bf16 v[126:129], a[148:151], v[82:85], v[126:129]
v_mfma_f32_16x16x32_bf16 v[130:133], v[82:85], a[152:155], v[130:133]
v_mfma_f32_16x16x32_bf16 v[122:125], a[156:159], v[162:165], v[122:125]
v_add_lshl_u32 v253, v46, 1, 7
v_bfi_b32 v253, s69, v253, v45
v_mfma_f32_16x16x32_bf16 v[126:129], a[160:163], v[162:165], v[126:129]
global_load_dword v53, v253, s[38:39] sc1
v_mfma_f32_16x16x32_bf16 v[130:133], v[162:165], a[164:167], v[130:133]
s_waitcnt vmcnt(6)
v_sub_u32 v254, v254, v46
v_cmp_lt_u32 vcc, 0x7f, v254
v_mfma_f32_16x16x32_bf16 v[122:125], a[168:171], v[166:169], v[122:125]
v_mfma_f32_16x16x32_bf16 v[126:129], a[172:175], v[166:169], v[126:129]
v_xor_b32 v51, 0x10000, v51
v_xor_b32 v52, 0x10000, v52
v_mfma_f32_16x16x32_bf16 v[130:133], v[166:169], a[176:179], v[130:133]
s_and_b64 vcc, vcc, s[76:77]
s_cbranch_vccnz .Lqa_sp32
.Lqa_rt33:
v_mfma_f32_16x16x32_bf16 v[122:125], a[180:183], v[170:173], v[122:125]
v_mfma_f32_16x16x32_bf16 v[126:129], a[184:187], v[170:173], v[126:129]
s_waitcnt vmcnt(5) lgkmcnt(0)
s_barrier
.Lqa_in27:
v_mfma_f32_16x16x32_bf16 v[130:133], v[170:173], a[188:191], v[130:133]
s_cmp_eq_u32 s70, s73
s_cbranch_scc1 .Lqa_hk28
.Lqa_hr29:
ds_read_b128 v[0:3], v51 offset:0
v_mfma_f32_16x16x32_bf16 v[122:125], a[192:195], v[174:177], v[122:125]
ds_read_b128 v[4:7], v52 offset:0
ds_read_b128 v[198:201], v17 offset:0
v_mfma_f32_16x16x32_bf16 v[126:129], a[196:199], v[174:177], v[126:129]
ds_read_b32 v26, v24 offset:64
ds_read_b32 v152, v21
v_mfma_f32_16x16x32_bf16 v[186:189], v[146:149], v[138:141], v[36:39]
v_mfma_f32_16x16x32_bf16 v[130:133], v[174:177], a[200:203], v[130:133]
ds_read_b128 v[96:99], v51 offset:2048
ds_read_b32 v95, v24 offset:128
v_mfma_f32_16x16x32_bf16 v[122:125], a[204:207], v[178:181], v[122:125]
ds_read_b128 v[100:103], v52 offset:2048
ds_read_b32 v255, v29
v_mfma_f32_16x16x32_bf16 v[126:129], a[208:211], v[178:181], v[126:129]
v_mfma_f32_16x16x32_bf16 v[130:133], v[178:181], a[212:215], v[130:133]
s_sub_u32 s70, s70, 1
ds_read_b128 v[104:107], v51 offset:4096
s_cbranch_scc0 .Lqa_top41
s_waitcnt lgkmcnt(0)
ds_read_b32 v8, v30
ds_read_b128 v[194:197], v25 offset:64
ds_read_b64 v[160:161], v22 offset:41472
ds_read_b32 v144, v20
s_mov_b64 exec, 65535
global_store_dwordx4 v16, v[198:201], s[38:39] sc1
s_mov_b64 exec, -1
v_add_u32 v16, 18432, v16
v_fma_f32 v206, v90, v26, v126
v_fma_f32 v207, v91, v26, v127
v_fma_f32 v208, v92, v26, v128
v_fma_f32 v209, v93, v26, v129
v_cvt_pk_bf16_f32 v150, v206, v207
v_cvt_pk_bf16_f32 v151, v208, v209
v_max_f32 v47, v186, v187
s_nop 0
v_mfma_f32_16x16x32_bf16 v[182:185], v[150:153], v[138:141], v[32:35]
s_waitcnt lgkmcnt(2)
v_max_f32 v48, v188, v189
v_fma_f32 v206, v94, v194, v130
v_mul_f32 v95, 0x3eae1a93, v95
v_mul_f32 v9, v255, v95
v_mul_f32 v10, v8, v95
s_nop 1
v_max3_f32 v47, v182, v183, v47
v_max3_f32 v48, v184, v185, v48
v_mul_f32 v47, v47, v9
v_mul_f32 v48, v48, v10
v_max_f32 v49, v47, v48
v_mov_b32 v50, v49
s_nop 1
v_permlane16_swap_b32 v49, v50
v_max_f32 v49, v49, v50
v_mov_b32 v50, v49
s_nop 1
v_permlane32_swap_b32 v49, v50
v_max_f32 v49, v49, v50
v_sub_f32 v47, v47, v49
v_sub_f32 v48, v48, v49
v_fma_f32 v207, v94, v195, v131
v_exp_f32 v210, v47
v_fma_f32 v208, v94, v196, v132
v_exp_f32 v211, v48
s_nop 0
v_add_f32 v49, v210, v211
v_mov_b32 v50, v49
v_mul_f32 v210, v210, v255
v_fma_f32 v209, v94, v197, v133
v_permlane16_swap_b32 v49, v50
v_add_f32 v49, v49, v50
v_mov_b32 v50, v49
v_mul_f32 v211, v211, v8
v_cvt_pk_bf16_f32 v156, v206, v207
v_cvt_pk_bf16_f32 v157, v208, v209
s_waitcnt lgkmcnt(0)
s_barrier
v_permlane32_swap_b32 v49, v50
v_add_f32 v49, v49, v50
v_fma_f32 v206, v86, v26, v122
v_rcp_f32 v49, v49
s_nop 0
v_mul_f32 v210, v210, v49
v_mul_f32 v211, v211, v49
v_cvt_pk_bf16_f32 v210, v210, v210
v_cvt_pk_bf16_f32 v211, v211, v211
v_and_b32 v192, v210, v40
v_and_b32 v193, v211, v41
v_and_b32 v190, v210, v42
v_and_b32 v191, v211, v43
s_nop 1
v_mfma_f32_16x16x32_bf16 v[186:189], v[154:157], v[190:193], 0
v_mfma_f32_16x16x32_bf16 v[182:185], v[158:161], v[190:193], 0
v_fma_f32 v207, v87, v26, v123
v_fma_f32 v208, v88, v26, v124
v_fma_f32 v209, v89, v26, v125
v_cvt_pk_bf16_f32 v142, v206, v207
v_cvt_pk_bf16_f32 v143, v208, v209
s_nop 1
v_cvt_pk_bf16_f32 v108, v186, v187
v_cvt_pk_bf16_f32 v109, v188, v189
ds_write_b64 v18, v[108:109]
v_cvt_pk_bf16_f32 v210, v182, v183
ds_write_b32 v19, v210
s_waitcnt vmcnt(0)
s_waitcnt lgkmcnt(0)
s_barrier
ds_read_b128 v[198:201], v17 offset:0
s_waitcnt lgkmcnt(0)
global_store_dwordx4 v16, v[198:201], s[38:39] sc1
s_waitcnt vmcnt(0)
s_barrier
s_endpgm
.Lqa_w4:
s_add_u32 m0, s48, 4096
s_nop 0
global_load_lds_dwordx4 v0, s[40:41] offset:-4096
global_load_lds_dwordx4 v1, s[40:41] offset:-3072
global_load_lds_dwordx4 v2, s[40:41] offset:-2048
global_load_lds_dwordx4 v3, s[40:41] offset:-1024
global_load_lds_dwordx4 v4, s[40:41] offset:0
global_load_lds_dwordx4 v5, s[40:41] offset:1024
global_load_lds_dwordx4 v6, s[40:41] offset:2048
global_load_lds_dwordx4 v7, s[40:41] offset:3072
s_add_u32 m0, s48, 12288
s_nop 0
global_load_lds_dwordx4 v8, s[40:41] offset:-4096
s_add_u32 s40, s40, 221184
s_addc_u32 s41, s41, 0
s_add_u32 m0, s48, 40960
s_nop 0
global_load_lds_dwordx4 v0, s[40:41] offset:-4096
global_load_lds_dwordx4 v1, s[40:41] offset:-3072
global_load_lds_dwordx4 v2, s[40:41] offset:-2048
global_load_lds_dwordx4 v3, s[40:41] offset:-1024
global_load_lds_dwordx4 v4, s[40:41] offset:0
global_load_lds_dwordx4 v5, s[40:41] offset:1024
global_load_lds_dwordx4 v6, s[40:41] offset:2048
global_load_lds_dwordx4 v7, s[40:41] offset:3072
s_add_u32 m0, s48, 49152
s_nop 0
global_load_lds_dwordx4 v8, s[40:41] offset:-4096
s_add_u32 s40, s40, 221184
s_addc_u32 s41, s41, 0
v_add_u32 v255, 0x10000, v9
v_add_u32 v162, 0x20000, v9
v_add_u32 v163, 0x10000, v10
v_add_u32 v164, 0x20000, v10
v_mov_b32 v106, 0x3F803F80
v_mov_b32 v107, 0x3F803F80
v_mov_b32 v108, 0x3F803F80
v_mov_b32 v109, 0x3F803F80
v_mov_b32 v141, 0
v_mov_b32 v145, 0
v_mov_b32 v149, 0
v_mov_b32 v153, 0
v_mov_b32 v165, 46080
s_waitcnt lgkmcnt(0)
s_mul_i32 s37, s37, 0x9E3779B1
s_add_u32 s37, s37, 0x7F4A7C15
v_add_u32 v46, s37, v46
v_add_u32 v48, s37, v48
v_add_u32 v50, s37, v50
ds_write_b32 v165, v141
s_mov_b64 s[76:77], 0
s_mov_b32 s73, 18
s_waitcnt vmcnt(9) lgkmcnt(0)
s_barrier
s_add_u32 m0, s48, 77824
ds_read_b64_tr_b16 a[0:1], v9 offset:47104
ds_read_b64_tr_b16 a[2:3], v9 offset:49408
global_load_lds_dwordx4 v0, s[40:41] offset:-4096
ds_read_b64_tr_b16 a[4:5], v9 offset:47296
global_load_lds_dwordx4 v1, s[40:41] offset:-3072
ds_read_b64_tr_b16 a[6:7], v9 offset:49600
global_load_lds_dwordx4 v2, s[40:41] offset:-2048
ds_read_b64_tr_b16 a[8:9], v9 offset:47488
global_load_lds_dwordx4 v3, s[40:41] offset:-1024
ds_read_b64_tr_b16 a[10:11], v9 offset:49792
ds_read_b128 v[166:169], v51 offset:0
global_load_lds_dwordx4 v4, s[40:41] offset:0
ds_read_b128 v[174:177], v51 offset:18432
global_load_lds_dwordx4 v5, s[40:41] offset:1024
ds_read_b64_tr_b16 a[12:13], v255 offset:0
global_load_lds_dwordx4 v6, s[40:41] offset:2048
ds_read_b64_tr_b16 a[14:15], v255 offset:2304
global_load_lds_dwordx4 v7, s[40:41] offset:3072
ds_read_b64_tr_b16 a[16:17], v255 offset:192
ds_read_b64_tr_b16 a[18:19], v255 offset:2496
s_add_u32 m0, s48, 86016
ds_read_b64_tr_b16 a[20:21], v255 offset:384
global_load_lds_dwordx4 v8, s[40:41] offset:-4096
ds_read_b64_tr_b16 a[22:23], v255 offset:2688
s_add_u32 s40, s40, 221184
ds_read_b128 v[170:173], v52 offset:0
s_addc_u32 s41, s41, 0
ds_read_b128 v[178:181], v52 offset:18432
s_waitcnt vmcnt(9) lgkmcnt(0)
s_barrier
s_add_u32 m0, s48, 4096
ds_read_b64_tr_b16 a[24:25], v255 offset:18432
v_mfma_f32_16x16x32_bf16 v[86:89], a[0:3], v[106:109], 0
v_mfma_f32_16x16x32_bf16 v[90:93], a[4:7], v[106:109], 0
global_load_lds_dwordx4 v0, s[40:41] offset:-4096
ds_read_b64_tr_b16 a[26:27], v255 offset:20736
v_mfma_f32_16x16x32_bf16 v[94:97], v[106:109], a[8:11], 0
ds_read_b64_tr_b16 a[28:29], v255 offset:18624
global_load_lds_dwordx4 v1, s[40:41] offset:-3072
v_mfma_f32_16x16x32_bf16 v[110:113], a[0:3], v[166:169], 0
ds_read_b64_tr_b16 a[30:31], v255 offset:20928
v_mfma_f32_16x16x32_bf16 v[114:117], a[4:7], v[166:169], 0
global_load_lds_dwordx4 v2, s[40:41] offset:-2048
ds_read_b64_tr_b16 a[32:33], v255 offset:18816
v_mfma_f32_16x16x32_bf16 v[118:121], v[166:169], a[8:11], 0
ds_read_b64_tr_b16 a[34:35], v255 offset:21120
global_load_lds_dwordx4 v3, s[40:41] offset:-1024
v_mfma_f32_16x16x32_bf16 v[134:137], v[106:109], v[166:169], 0
ds_read_b128 v[182:185], v51 offset:2048
v_mfma_f32_16x16x32_bf16 v[122:125], a[0:3], v[174:177], 0
global_load_lds_dwordx4 v4, s[40:41] offset:0
ds_read_b128 v[190:193], v51 offset:20480
v_mfma_f32_16x16x32_bf16 v[126:129], a[4:7], v[174:177], 0
ds_read_b64_tr_b16 a[36:37], v255 offset:36864
global_load_lds_dwordx4 v5, s[40:41] offset:1024
v_mfma_f32_16x16x32_bf16 v[130:133], v[174:177], a[8:11], 0
v_mfma_f32_16x16x32_bf16 v[158:161], v[106:109], v[174:177], 0
ds_read_b64_tr_b16 a[38:39], v255 offset:39168
global_load_lds_dwordx4 v6, s[40:41] offset:2048
v_mfma_f32_16x16x32_bf16 v[86:89], a[12:15], v[106:109], v[86:89]
ds_read_b64_tr_b16 a[40:41], v255 offset:37056
v_mfma_f32_16x16x32_bf16 v[90:93], a[16:19], v[106:109], v[90:93]
global_load_lds_dwordx4 v7, s[40:41] offset:3072
ds_read_b64_tr_b16 a[42:43], v255 offset:39360
v_mfma_f32_16x16x32_bf16 v[94:97], v[106:109], a[20:23], v[94:97]
s_add_u32 m0, s48, 12288
ds_read_b64_tr_b16 a[44:45], v255 offset:37248
v_mfma_f32_16x16x32_bf16 v[110:113], a[12:15], v[170:173], v[110:113]
ds_read_b64_tr_b16 a[46:47], v255 offset:39552
v_mfma_f32_16x16x32_bf16 v[114:117], a[16:19], v[170:173], v[114:117]
global_load_lds_dwordx4 v8, s[40:41] offset:-4096
ds_read_b128 v[186:189], v52 offset:2048
v_mfma_f32_16x16x32_bf16 v[118:121], v[170:173], a[20:23], v[118:121]
s_add_u32 s40, s40, 221184
ds_read_b128 v[194:197], v52 offset:20480
v_mfma_f32_16x16x32_bf16 v[134:137], v[106:109], v[170:173], v[134:137]
v_mfma_f32_16x16x32_bf16 v[122:125], a[12:15], v[178:181], v[122:125]
v_mfma_f32_16x16x32_bf16 v[126:129], a[16:19], v[178:181], v[126:129]
s_addc_u32 s41, s41, 0
v_mfma_f32_16x16x32_bf16 v[130:133], v[178:181], a[20:23], v[130:133]
v_mfma_f32_16x16x32_bf16 v[158:161], v[106:109], v[178:181], v[158:161]
s_waitcnt vmcnt(9) lgkmcnt(0)
s_barrier
s_add_u32 m0, s48, 40960
ds_read_b64_tr_b16 a[48:49], v255 offset:55296
v_mfma_f32_16x16x32_bf16 v[86:89], a[24:27], v[106:109], v[86:89]
v_mfma_f32_16x16x32_bf16 v[90:93], a[28:31], v[106:109], v[90:93]
global_load_lds_dwordx4 v0, s[40:41] offset:-4096
ds_read_b64_tr_b16 a[50:51], v255 offset:57600
v_mfma_f32_16x16x32_bf16 v[94:97], v[106:109], a[32:35], v[94:97]
ds_read_b64_tr_b16 a[52:53], v255 offset:55488
global_load_lds_dwordx4 v1, s[40:41] offset:-3072
v_mfma_f32_16x16x32_bf16 v[110:113], a[24:27], v[182:185], v[110:113]
ds_read_b64_tr_b16 a[54:55], v255 offset:57792
v_mfma_f32_16x16x32_bf16 v[114:117], a[28:31], v[182:185], v[114:117]
global_load_lds_dwordx4 v2, s[40:41] offset:-2048
)"
R"(ds_read_b64_tr_b16 a[56:57], v255 offset:55680
v_mfma_f32_16x16x32_bf16 v[118:121], v[182:185], a[32:35], v[118:121]
ds_read_b64_tr_b16 a[58:59], v255 offset:57984
global_load_lds_dwordx4 v3, s[40:41] offset:-1024
v_mfma_f32_16x16x32_bf16 v[134:137], v[106:109], v[182:185], v[134:137]
ds_read_b128 v[166:169], v51 offset:4096
v_mfma_f32_16x16x32_bf16 v[122:125], a[24:27], v[190:193], v[122:125]
global_load_lds_dwordx4 v4, s[40:41] offset:0
ds_read_b128 v[174:177], v51 offset:22528
v_mfma_f32_16x16x32_bf16 v[126:129], a[28:31], v[190:193], v[126:129]
ds_read_b64_tr_b16 a[60:61], v162 offset:8192
global_load_lds_dwordx4 v5, s[40:41] offset:1024
v_mfma_f32_16x16x32_bf16 v[130:133], v[190:193], a[32:35], v[130:133]
v_mfma_f32_16x16x32_bf16 v[158:161], v[106:109], v[190:193], v[158:161]
ds_read_b64_tr_b16 a[62:63], v162 offset:10496
global_load_lds_dwordx4 v6, s[40:41] offset:2048
v_mfma_f32_16x16x32_bf16 v[86:89], a[36:39], v[106:109], v[86:89]
ds_read_b64_tr_b16 a[64:65], v162 offset:8384
v_mfma_f32_16x16x32_bf16 v[90:93], a[40:43], v[106:109], v[90:93]
global_load_lds_dwordx4 v7, s[40:41] offset:3072
ds_read_b64_tr_b16 a[66:67], v162 offset:10688
v_mfma_f32_16x16x32_bf16 v[94:97], v[106:109], a[44:47], v[94:97]
s_add_u32 m0, s48, 49152
ds_read_b64_tr_b16 a[68:69], v162 offset:8576
v_mfma_f32_16x16x32_bf16 v[110:113], a[36:39], v[186:189], v[110:113]
ds_read_b64_tr_b16 a[70:71], v162 offset:10880
v_mfma_f32_16x16x32_bf16 v[114:117], a[40:43], v[186:189], v[114:117]
global_load_lds_dwordx4 v8, s[40:41] offset:-4096
ds_read_b128 v[170:173], v52 offset:4096
v_mfma_f32_16x16x32_bf16 v[118:121], v[186:189], a[44:47], v[118:121]
s_add_u32 s40, s40, 221184
ds_read_b128 v[178:181], v52 offset:22528
v_mfma_f32_16x16x32_bf16 v[134:137], v[106:109], v[186:189], v[134:137]
v_mfma_f32_16x16x32_bf16 v[122:125], a[36:39], v[194:197], v[122:125]
v_mfma_f32_16x16x32_bf16 v[126:129], a[40:43], v[194:197], v[126:129]
s_addc_u32 s41, s41, 0
v_mfma_f32_16x16x32_bf16 v[130:133], v[194:197], a[44:47], v[130:133]
v_mfma_f32_16x16x32_bf16 v[158:161], v[106:109], v[194:197], v[158:161]
s_waitcnt vmcnt(9) lgkmcnt(0)
s_barrier
s_add_u32 m0, s48, 77824
ds_read_b64_tr_b16 a[72:73], v9 offset:47104
v_mfma_f32_16x16x32_bf16 v[86:89], a[48:51], v[106:109], v[86:89]
v_mfma_f32_16x16x32_bf16 v[90:93], a[52:55], v[106:109], v[90:93]
global_load_lds_dwordx4 v0, s[40:41] offset:-4096
ds_read_b64_tr_b16 a[74:75], v9 offset:49408
v_mfma_f32_16x16x32_bf16 v[94:97], v[106:109], a[56:59], v[94:97]
ds_read_b64_tr_b16 a[76:77], v9 offset:47296
global_load_lds_dwordx4 v1, s[40:41] offset:-3072
v_mfma_f32_16x16x32_bf16 v[110:113], a[48:51], v[166:169], v[110:113]
ds_read_b64_tr_b16 a[78:79], v9 offset:49600
v_mfma_f32_16x16x32_bf16 v[114:117], a[52:55], v[166:169], v[114:117]
global_load_lds_dwordx4 v2, s[40:41] offset:-2048
ds_read_b64_tr_b16 a[80:81], v9 offset:47488
v_mfma_f32_16x16x32_bf16 v[118:121], v[166:169], a[56:59], v[118:121]
ds_read_b64_tr_b16 a[82:83], v9 offset:49792
global_load_lds_dwordx4 v3, s[40:41] offset:-1024
v_mfma_f32_16x16x32_bf16 v[134:137], v[106:109], v[166:169], v[134:137]
ds_read_b128 v[182:185], v51 offset:6144
v_mfma_f32_16x16x32_bf16 v[122:125], a[48:51], v[174:177], v[122:125]
global_load_lds_dwordx4 v4, s[40:41] offset:0
ds_read_b128 v[190:193], v51 offset:24576
v_mfma_f32_16x16x32_bf16 v[126:129], a[52:55], v[174:177], v[126:129]
ds_read_b64_tr_b16 a[84:85], v255 offset:0
global_load_lds_dwordx4 v5, s[40:41] offset:1024
v_mfma_f32_16x16x32_bf16 v[130:133], v[174:177], a[56:59], v[130:133]
v_mfma_f32_16x16x32_bf16 v[158:161], v[106:109], v[174:177], v[158:161]
ds_read_b64_tr_b16 a[86:87], v255 offset:2304
global_load_lds_dwordx4 v6, s[40:41] offset:2048
v_mfma_f32_16x16x32_bf16 v[86:89], a[60:63], v[106:109], v[86:89]
ds_read_b64_tr_b16 a[88:89], v255 offset:192
v_mfma_f32_16x16x32_bf16 v[90:93], a[64:67], v[106:109], v[90:93]
global_load_lds_dwordx4 v7, s[40:41] offset:3072
ds_read_b64_tr_b16 a[90:91], v255 offset:2496
v_mfma_f32_16x16x32_bf16 v[94:97], v[106:109], a[68:71], v[94:97]
s_add_u32 m0, s48, 86016
ds_read_b64_tr_b16 a[92:93], v255 offset:384
v_mfma_f32_16x16x32_bf16 v[110:113], a[60:63], v[170:173], v[110:113]
ds_read_b64_tr_b16 a[94:95], v255 offset:2688
v_mfma_f32_16x16x32_bf16 v[114:117], a[64:67], v[170:173], v[114:117]
global_load_lds_dwordx4 v8, s[40:41] offset:-4096
ds_read_b128 v[186:189], v52 offset:6144
v_mfma_f32_16x16x32_bf16 v[118:121], v[170:173], a[68:71], v[118:121]
s_add_u32 s40, s40, 221184
ds_read_b128 v[194:197], v52 offset:24576
v_mfma_f32_16x16x32_bf16 v[134:137], v[106:109], v[170:173], v[134:137]
v_mfma_f32_16x16x32_bf16 v[122:125], a[60:63], v[178:181], v[122:125]
v_mfma_f32_16x16x32_bf16 v[126:129], a[64:67], v[178:181], v[126:129]
s_addc_u32 s41, s41, 0
v_mfma_f32_16x16x32_bf16 v[130:133], v[178:181], a[68:71], v[130:133]
v_mfma_f32_16x16x32_bf16 v[158:161], v[106:109], v[178:181], v[158:161]
s_waitcnt vmcnt(9) lgkmcnt(0)
s_barrier
s_add_u32 m0, s48, 4096
ds_read_b64_tr_b16 a[96:97], v255 offset:18432
v_mfma_f32_16x16x32_bf16 v[86:89], a[72:75], v[106:109], v[86:89]
v_mfma_f32_16x16x32_bf16 v[90:93], a[76:79], v[106:109], v[90:93]
global_load_lds_dwordx4 v0, s[40:41] offset:-4096
ds_read_b64_tr_b16 a[98:99], v255 offset:20736
v_mfma_f32_16x16x32_bf16 v[94:97], v[106:109], a[80:83], v[94:97]
ds_read_b64_tr_b16 a[100:101], v255 offset:18624
global_load_lds_dwordx4 v1, s[40:41] offset:-3072
v_mfma_f32_16x16x32_bf16 v[110:113], a[72:75], v[182:185], v[110:113]
ds_read_b64_tr_b16 a[102:103], v255 offset:20928
v_mfma_f32_16x16x32_bf16 v[114:117], a[76:79], v[182:185], v[114:117]
global_load_lds_dwordx4 v2, s[40:41] offset:-2048
ds_read_b64_tr_b16 a[104:105], v255 offset:18816
v_mfma_f32_16x16x32_bf16 v[118:121], v[182:185], a[80:83], v[118:121]
ds_read_b64_tr_b16 a[106:107], v255 offset:21120
global_load_lds_dwordx4 v3, s[40:41] offset:-1024
v_mfma_f32_16x16x32_bf16 v[134:137], v[106:109], v[182:185], v[134:137]
ds_read_b128 v[166:169], v51 offset:8192
v_mfma_f32_16x16x32_bf16 v[122:125], a[72:75], v[190:193], v[122:125]
global_load_lds_dwordx4 v4, s[40:41] offset:0
ds_read_b128 v[174:177], v51 offset:26624
v_mfma_f32_16x16x32_bf16 v[126:129], a[76:79], v[190:193], v[126:129]
ds_read_b64_tr_b16 a[108:109], v255 offset:36864
global_load_lds_dwordx4 v5, s[40:41] offset:1024
v_mfma_f32_16x16x32_bf16 v[130:133], v[190:193], a[80:83], v[130:133]
v_mfma_f32_16x16x32_bf16 v[158:161], v[106:109], v[190:193], v[158:161]
ds_read_b64_tr_b16 a[110:111], v255 offset:39168
global_load_lds_dwordx4 v6, s[40:41] offset:2048
v_mfma_f32_16x16x32_bf16 v[86:89], a[84:87], v[106:109], v[86:89]
ds_read_b64_tr_b16 a[112:113], v255 offset:37056
v_mfma_f32_16x16x32_bf16 v[90:93], a[88:91], v[106:109], v[90:93]
global_load_lds_dwordx4 v7, s[40:41] offset:3072
ds_read_b64_tr_b16 a[114:115], v255 offset:39360
v_mfma_f32_16x16x32_bf16 v[94:97], v[106:109], a[92:95], v[94:97]
s_add_u32 m0, s48, 12288
ds_read_b64_tr_b16 a[116:117], v255 offset:37248
v_mfma_f32_16x16x32_bf16 v[110:113], a[84:87], v[186:189], v[110:113]
ds_read_b64_tr_b16 a[118:119], v255 offset:39552
v_mfma_f32_16x16x32_bf16 v[114:117], a[88:91], v[186:189], v[114:117]
global_load_lds_dwordx4 v8, s[40:41] offset:-4096
ds_read_b128 v[170:173], v52 offset:8192
v_mfma_f32_16x16x32_bf16 v[118:121], v[186:189], a[92:95], v[118:121]
s_add_u32 s40, s40, 221184
ds_read_b128 v[178:181], v52 offset:26624
v_mfma_f32_16x16x32_bf16 v[134:137], v[106:109], v[186:189], v[134:137]
v_mfma_f32_16x16x32_bf16 v[122:125], a[84:87], v[194:197], v[122:125]
v_mfma_f32_16x16x32_bf16 v[126:129], a[88:91], v[194:197], v[126:129]
s_addc_u32 s41, s41, 0
v_mfma_f32_16x16x32_bf16 v[130:133], v[194:197], a[92:95], v[130:133]
v_mfma_f32_16x16x32_bf16 v[158:161], v[106:109], v[194:197], v[158:161]
s_waitcnt vmcnt(9) lgkmcnt(0)
s_barrier
s_add_u32 m0, s48, 40960
ds_read_b64_tr_b16 a[120:121], v255 offset:55296
v_mfma_f32_16x16x32_bf16 v[86:89], a[96:99], v[106:109], v[86:89]
v_mfma_f32_16x16x32_bf16 v[90:93], a[100:103], v[106:109], v[90:93]
global_load_lds_dwordx4 v0, s[40:41] offset:-4096
ds_read_b64_tr_b16 a[122:123], v255 offset:57600
v_mfma_f32_16x16x32_bf16 v[94:97], v[106:109], a[104:107], v[94:97]
ds_read_b64_tr_b16 a[124:125], v255 offset:55488
global_load_lds_dwordx4 v1, s[40:41] offset:-3072
v_mfma_f32_16x16x32_bf16 v[110:113], a[96:99], v[166:169], v[110:113]
ds_read_b64_tr_b16 a[126:127], v255 offset:57792
v_mfma_f32_16x16x32_bf16 v[114:117], a[100:103], v[166:169], v[114:117]
global_load_lds_dwordx4 v2, s[40:41] offset:-2048
ds_read_b64_tr_b16 a[128:129], v255 offset:55680
v_mfma_f32_16x16x32_bf16 v[118:121], v[166:169], a[104:107], v[118:121]
ds_read_b64_tr_b16 a[130:131], v255 offset:57984
global_load_lds_dwordx4 v3, s[40:41] offset:-1024
v_mfma_f32_16x16x32_bf16 v[134:137], v[106:109], v[166:169], v[134:137]
ds_read_b128 v[182:185], v51 offset:10240
v_mfma_f32_16x16x32_bf16 v[122:125], a[96:99], v[174:177], v[122:125]
global_load_lds_dwordx4 v4, s[40:41] offset:0
ds_read_b128 v[190:193], v51 offset:28672
v_mfma_f32_16x16x32_bf16 v[126:129], a[100:103], v[174:177], v[126:129]
ds_read_b64_tr_b16 a[132:133], v162 offset:8192
global_load_lds_dwordx4 v5, s[40:41] offset:1024
v_mfma_f32_16x16x32_bf16 v[130:133], v[174:177], a[104:107], v[130:133]
v_mfma_f32_16x16x32_bf16 v[158:161], v[106:109], v[174:177], v[158:161]
ds_read_b64_tr_b16 a[134:135], v162 offset:10496
global_load_lds_dwordx4 v6, s[40:41] offset:2048
v_mfma_f32_16x16x32_bf16 v[86:89], a[108:111], v[106:109], v[86:89]
ds_read_b64_tr_b16 a[136:137], v162 offset:8384
v_mfma_f32_16x16x32_bf16 v[90:93], a[112:115], v[106:109], v[90:93]
global_load_lds_dwordx4 v7, s[40:41] offset:3072
ds_read_b64_tr_b16 a[138:139], v162 offset:10688
v_mfma_f32_16x16x32_bf16 v[94:97], v[106:109], a[116:119], v[94:97]
s_add_u32 m0, s48, 49152
ds_read_b64_tr_b16 a[140:141], v162 offset:8576
v_mfma_f32_16x16x32_bf16 v[110:113], a[108:111], v[170:173], v[110:113]
ds_read_b64_tr_b16 a[142:143], v162 offset:10880
v_mfma_f32_16x16x32_bf16 v[114:117], a[112:115], v[170:173], v[114:117]
global_load_lds_dwordx4 v8, s[40:41] offset:-4096
ds_read_b128 v[186:189], v52 offset:10240
v_mfma_f32_16x16x32_bf16 v[118:121], v[170:173], a[116:119], v[118:121]
s_add_u32 s40, s40, 221184
ds_read_b128 v[194:197], v52 offset:28672
v_mfma_f32_16x16x32_bf16 v[134:137], v[106:109], v[170:173], v[134:137]
v_mfma_f32_16x16x32_bf16 v[122:125], a[108:111], v[178:181], v[122:125]
v_mfma_f32_16x16x32_bf16 v[126:129], a[112:115], v[178:181], v[126:129]
s_addc_u32 s41, s41, 0
v_mfma_f32_16x16x32_bf16 v[130:133], v[178:181], a[116:119], v[130:133]
v_mfma_f32_16x16x32_bf16 v[158:161], v[106:109], v[178:181], v[158:161]
s_waitcnt vmcnt(9) lgkmcnt(0)
s_barrier
s_add_u32 m0, s48, 77824
ds_read_b64_tr_b16 a[144:145], v9 offset:47104
v_mfma_f32_16x16x32_bf16 v[86:89], a[120:123], v[106:109], v[86:89]
v_mfma_f32_16x16x32_bf16 v[90:93], a[124:127], v[106:109], v[90:93]
global_load_lds_dwordx4 v0, s[40:41] offset:-4096
ds_read_b64_tr_b16 a[146:147], v9 offset:49408
v_mfma_f32_16x16x32_bf16 v[94:97], v[106:109], a[128:131], v[94:97]
ds_read_b64_tr_b16 a[148:149], v9 offset:47296
global_load_lds_dwordx4 v1, s[40:41] offset:-3072
v_mfma_f32_16x16x32_bf16 v[110:113], a[120:123], v[182:185], v[110:113]
ds_read_b64_tr_b16 a[150:151], v9 offset:49600
v_mfma_f32_16x16x32_bf16 v[114:117], a[124:127], v[182:185], v[114:117]
global_load_lds_dwordx4 v2, s[40:41] offset:-2048
ds_read_b64_tr_b16 a[152:153], v9 offset:47488
v_mfma_f32_16x16x32_bf16 v[118:121], v[182:185], a[128:131], v[118:121]
ds_read_b64_tr_b16 a[154:155], v9 offset:49792
global_load_lds_dwordx4 v3, s[40:41] offset:-1024
v_mfma_f32_16x16x32_bf16 v[134:137], v[106:109], v[182:185], v[134:137]
ds_read_b128 v[166:169], v51 offset:12288
v_mfma_f32_16x16x32_bf16 v[122:125], a[120:123], v[190:193], v[122:125]
global_load_lds_dwordx4 v4, s[40:41] offset:0
ds_read_b128 v[174:177], v51 offset:30720
v_mfma_f32_16x16x32_bf16 v[126:129], a[124:127], v[190:193], v[126:129]
ds_read_b64_tr_b16 a[156:157], v255 offset:0
global_load_lds_dwordx4 v5, s[40:41] offset:1024
v_mfma_f32_16x16x32_bf16 v[130:133], v[190:193], a[128:131], v[130:133]
v_mfma_f32_16x16x32_bf16 v[158:161], v[106:109], v[190:193], v[158:161]
ds_read_b64_tr_b16 a[158:159], v255 offset:2304
global_load_lds_dwordx4 v6, s[40:41] offset:2048
v_mfma_f32_16x16x32_bf16 v[86:89], a[132:135], v[106:109], v[86:89]
ds_read_b64_tr_b16 a[160:161], v255 offset:192
v_mfma_f32_16x16x32_bf16 v[90:93], a[136:139], v[106:109], v[90:93]
global_load_lds_dwordx4 v7, s[40:41] offset:3072
ds_read_b64_tr_b16 a[162:163], v255 offset:2496
v_mfma_f32_16x16x32_bf16 v[94:97], v[106:109], a[140:143], v[94:97]
s_add_u32 m0, s48, 86016
ds_read_b64_tr_b16 a[164:165], v255 offset:384
v_mfma_f32_16x16x32_bf16 v[110:113], a[132:135], v[186:189], v[110:113]
ds_read_b64_tr_b16 a[166:167], v255 offset:2688
v_mfma_f32_16x16x32_bf16 v[114:117], a[136:139], v[186:189], v[114:117]
global_load_lds_dwordx4 v8, s[40:41] offset:-4096
ds_read_b128 v[170:173], v52 offset:12288
v_mfma_f32_16x16x32_bf16 v[118:121], v[186:189], a[140:143], v[118:121]
s_sub_u32 s40, s40, 1769472
ds_read_b128 v[178:181], v52 offset:30720
v_mfma_f32_16x16x32_bf16 v[134:137], v[106:109], v[186:189], v[134:137]
v_mfma_f32_16x16x32_bf16 v[122:125], a[132:135], v[194:197], v[122:125]
v_mfma_f32_16x16x32_bf16 v[126:129], a[136:139], v[194:197], v[126:129]
s_subb_u32 s41, s41, 0
v_mfma_f32_16x16x32_bf16 v[130:133], v[194:197], a[140:143], v[130:133]
v_mfma_f32_16x16x32_bf16 v[158:161], v[106:109], v[194:197], v[158:161]
s_waitcnt vmcnt(9) lgkmcnt(0)
s_barrier
s_add_u32 m0, s49, 0
ds_read_b64_tr_b16 a[168:169], v255 offset:18432
v_mfma_f32_16x16x32_bf16 v[86:89], a[144:147], v[106:109], v[86:89]
v_mfma_f32_16x16x32_bf16 v[90:93], a[148:151], v[106:109], v[90:93]
ds_read_b64_tr_b16 a[170:171], v255 offset:20736
v_mfma_f32_16x16x32_bf16 v[94:97], v[106:109], a[152:155], v[94:97]
ds_read_b64_tr_b16 a[172:173], v255 offset:18624
v_mfma_f32_16x16x32_bf16 v[110:113], a[144:147], v[166:169], v[110:113]
global_load_lds_dwordx4 v11, s[42:43] offset:-2048 sc1
ds_read_b64_tr_b16 a[174:175], v255 offset:20928
v_mfma_f32_16x16x32_bf16 v[114:117], a[148:151], v[166:169], v[114:117]
ds_read_b64_tr_b16 a[176:177], v255 offset:18816
v_mfma_f32_16x16x32_bf16 v[118:121], v[166:169], a[152:155], v[118:121]
ds_read_b64_tr_b16 a[178:179], v255 offset:21120
v_mfma_f32_16x16x32_bf16 v[134:137], v[106:109], v[166:169], v[134:137]
global_load_lds_dwordx4 v12, s[42:43] offset:-1024 sc1
ds_read_b128 v[182:185], v51 offset:14336
v_mfma_f32_16x16x32_bf16 v[122:125], a[144:147], v[174:177], v[122:125]
ds_read_b128 v[190:193], v51 offset:32768
v_mfma_f32_16x16x32_bf16 v[126:129], a[148:151], v[174:177], v[126:129]
ds_read_b64_tr_b16 a[180:181], v255 offset:36864
v_mfma_f32_16x16x32_bf16 v[130:133], v[174:177], a[152:155], v[130:133]
global_load_lds_dwordx4 v13, s[42:43] offset:0 sc1
v_mfma_f32_16x16x32_bf16 v[158:161], v[106:109], v[174:177], v[158:161]
ds_read_b64_tr_b16 a[182:183], v255 offset:39168
v_mfma_f32_16x16x32_bf16 v[86:89], a[156:159], v[106:109], v[86:89]
ds_read_b64_tr_b16 a[184:185], v255 offset:37056
v_mfma_f32_16x16x32_bf16 v[90:93], a[160:163], v[106:109], v[90:93]
ds_read_b64_tr_b16 a[186:187], v255 offset:39360
v_mfma_f32_16x16x32_bf16 v[94:97], v[106:109], a[164:167], v[94:97]
global_load_lds_dwordx4 v14, s[42:43] offset:1024 sc1
ds_read_b64_tr_b16 a[188:189], v255 offset:37248
v_mfma_f32_16x16x32_bf16 v[110:113], a[156:159], v[170:173], v[110:113]
ds_read_b64_tr_b16 a[190:191], v255 offset:39552
v_mfma_f32_16x16x32_bf16 v[114:117], a[160:163], v[170:173], v[114:117]
ds_read_b128 v[186:189], v52 offset:14336
v_mfma_f32_16x16x32_bf16 v[118:121], v[170:173], a[164:167], v[118:121]
s_sub_u32 s42, s42, 18432
s_subb_u32 s43, s43, 0
ds_read_b128 v[194:197], v52 offset:32768
v_mfma_f32_16x16x32_bf16 v[134:137], v[106:109], v[170:173], v[134:137]
v_mfma_f32_16x16x32_bf16 v[122:125], a[156:159], v[178:181], v[122:125]
v_mfma_f32_16x16x32_bf16 v[126:129], a[160:163], v[178:181], v[126:129]
v_mfma_f32_16x16x32_bf16 v[130:133], v[178:181], a[164:167], v[130:133]
v_mfma_f32_16x16x32_bf16 v[158:161], v[106:109], v[178:181], v[158:161]
s_waitcnt vmcnt(4) lgkmcnt(0)
s_barrier
s_add_u32 m0, s49, 18432
ds_read_b64_tr_b16 a[192:193], v255 offset:55296
v_mfma_f32_16x16x32_bf16 v[86:89], a[168:171], v[106:109], v[86:89]
v_mfma_f32_16x16x32_bf16 v[90:93], a[172:175], v[106:109], v[90:93]
ds_read_b64_tr_b16 a[194:195], v255 offset:57600
v_mfma_f32_16x16x32_bf16 v[94:97], v[106:109], a[176:179], v[94:97]
ds_read_b64_tr_b16 a[196:197], v255 offset:55488
v_mfma_f32_16x16x32_bf16 v[110:113], a[168:171], v[182:185], v[110:113]
global_load_lds_dwordx4 v11, s[42:43] offset:-2048 sc1
ds_read_b64_tr_b16 a[198:199], v255 offset:57792
v_mfma_f32_16x16x32_bf16 v[114:117], a[172:175], v[182:185], v[114:117]
ds_read_b64_tr_b16 a[200:201], v255 offset:55680
v_mfma_f32_16x16x32_bf16 v[118:121], v[182:185], a[176:179], v[118:121]
ds_read_b64_tr_b16 a[202:203], v255 offset:57984
v_mfma_f32_16x16x32_bf16 v[134:137], v[106:109], v[182:185], v[134:137]
global_load_lds_dwordx4 v12, s[42:43] offset:-1024 sc1
ds_read_b128 v[166:169], v51 offset:16384
v_mfma_f32_16x16x32_bf16 v[122:125], a[168:171], v[190:193], v[122:125]
ds_read_b128 v[174:177], v51 offset:34816
v_mfma_f32_16x16x32_bf16 v[126:129], a[172:175], v[190:193], v[126:129]
ds_read_b64_tr_b16 a[204:205], v162 offset:8192
v_mfma_f32_16x16x32_bf16 v[130:133], v[190:193], a[176:179], v[130:133]
global_load_lds_dwordx4 v13, s[42:43] offset:0 sc1
ds_read_b64_tr_b16 a[206:207], v162 offset:10496
v_mfma_f32_16x16x32_bf16 v[158:161], v[106:109], v[190:193], v[158:161]
ds_read_b64_tr_b16 a[208:209], v162 offset:8384
v_mfma_f32_16x16x32_bf16 v[86:89], a[180:183], v[106:109], v[86:89]
ds_read_b64_tr_b16 a[210:211], v162 offset:10688
v_mfma_f32_16x16x32_bf16 v[90:93], a[184:187], v[106:109], v[90:93]
global_load_lds_dwordx4 v14, s[42:43] offset:1024 sc1
ds_read_b64_tr_b16 a[212:213], v162 offset:8576
v_mfma_f32_16x16x32_bf16 v[94:97], v[106:109], a[188:191], v[94:97]
ds_read_b64_tr_b16 a[214:215], v162 offset:10880
v_mfma_f32_16x16x32_bf16 v[110:113], a[180:183], v[186:189], v[110:113]
ds_read_b128 v[170:173], v52 offset:16384
v_mfma_f32_16x16x32_bf16 v[114:117], a[184:187], v[186:189], v[114:117]
s_sub_u32 s42, s42, 18432
s_subb_u32 s43, s43, 0
ds_read_b128 v[178:181], v52 offset:34816
v_mfma_f32_16x16x32_bf16 v[118:121], v[186:189], a[188:191], v[118:121]
v_mfma_f32_16x16x32_bf16 v[134:137], v[106:109], v[186:189], v[134:137]
v_mfma_f32_16x16x32_bf16 v[122:125], a[180:183], v[194:197], v[122:125]
v_mfma_f32_16x16x32_bf16 v[126:129], a[184:187], v[194:197], v[126:129]
v_mfma_f32_16x16x32_bf16 v[158:161], v[106:109], v[194:197], v[158:161]
s_waitcnt lgkmcnt(14)
v_mfma_f32_16x16x32_bf16 v[86:89], a[192:195], v[106:109], v[86:89]
s_waitcnt lgkmcnt(12)
v_mfma_f32_16x16x32_bf16 v[90:93], a[196:199], v[106:109], v[90:93]
s_waitcnt lgkmcnt(10)
v_mfma_f32_16x16x32_bf16 v[94:97], v[106:109], a[200:203], v[94:97]
s_waitcnt lgkmcnt(9)
v_mfma_f32_16x16x32_bf16 v[110:113], a[192:195], v[166:169], v[110:113]
v_mfma_f32_16x16x32_bf16 v[114:117], a[196:199], v[166:169], v[114:117]
v_mfma_f32_16x16x32_bf16 v[118:121], v[166:169], a[200:203], v[118:121]
v_mfma_f32_16x16x32_bf16 v[134:137], v[106:109], v[166:169], v[134:137]
s_waitcnt lgkmcnt(8)
v_mfma_f32_16x16x32_bf16 v[158:161], v[106:109], v[174:177], v[158:161]
s_waitcnt lgkmcnt(6)
v_mfma_f32_16x16x32_bf16 v[86:89], a[204:207], v[106:109], v[86:89]
s_waitcnt lgkmcnt(4)
v_mfma_f32_16x16x32_bf16 v[90:93], a[208:211], v[106:109], v[90:93]
s_waitcnt lgkmcnt(2)
v_mfma_f32_16x16x32_bf16 v[94:97], v[106:109], a[212:215], v[94:97]
s_waitcnt lgkmcnt(1)
v_mfma_f32_16x16x32_bf16 v[110:113], a[204:207], v[170:173], v[110:113]
v_mfma_f32_16x16x32_bf16 v[114:117], a[208:211], v[170:173], v[114:117]
v_mfma_f32_16x16x32_bf16 v[118:121], v[170:173], a[212:215], v[118:121]
v_mfma_f32_16x16x32_bf16 v[134:137], v[106:109], v[170:173], v[134:137]
s_waitcnt lgkmcnt(0)
v_mfma_f32_16x16x32_bf16 v[158:161], v[106:109], v[178:181], v[158:161]
s_nop 5
v_mul_f32 v202, 0x3ae38e39, v134
v_cvt_pk_bf16_f32 v216, -v202, -v202
v_lshlrev_b32 v216, 16, v216
ds_write_b32 v24, v216 offset:0
s_waitcnt lgkmcnt(0)
s_barrier
ds_read_b32 v216, v26
s_waitcnt lgkmcnt(0)
v_mul_f32 v216, 0x3ae38e39, v216
v_fma_f32 v203, -v202, v202, v216
v_max_f32 v203, 0, v203
v_cvt_pk_bf16_f32 v203, v203, v203
v_lshlrev_b32 v203, 16, v203
v_add_f32 v203, 0x3727c5ac, v203
v_cvt_pk_bf16_f32 v203, v203, v203
v_lshlrev_b32 v203, 16, v203
v_sqrt_f32 v203, v203
s_nop 0
v_cvt_pk_bf16_f32 v203, v203, v203
v_lshlrev_b32 v203, 16, v203
v_rcp_f32 v216, v203
s_nop 0
ds_write_b32 v24, v216 offset:128
ds_read_b128 v[170:173], v51 offset:32768
ds_read_b128 v[174:177], v52 offset:32768
ds_read_b128 v[178:181], v51 offset:34816
ds_read_b128 v[182:185], v52 offset:34816
v_mov_b32 v206, v32
v_mov_b32 v207, v33
v_mov_b32 v208, v34
)"
R"(v_mov_b32 v209, v35
v_mov_b32 v134, v158
v_mov_b32 v135, v159
v_mov_b32 v136, v160
v_mov_b32 v137, v161
s_waitcnt lgkmcnt(4)
s_barrier
ds_read_b32 v23, v24 offset:0
ds_read_b32 v148, v21
ds_read_b32 v95, v24 offset:192
ds_read_b32 v254, v27
ds_read_b32 v255, v28
ds_read_b128 v[198:201], v25 offset:0
ds_read_b64 v[158:159], v22 offset:41472
ds_read_b32 v140, v20
v_xor_b32 v51, 0x10000, v51
v_xor_b32 v52, 0x10000, v52
v_mul_f32 v202, 0x3ae38e39, v134
v_cvt_pk_bf16_f32 v216, -v202, -v202
v_lshlrev_b32 v216, 16, v216
ds_write_b32 v24, v216 offset:64
s_waitcnt lgkmcnt(8)
v_fma_f32 v210, v90, v23, v114
v_fma_f32 v211, v91, v23, v115
v_fma_f32 v212, v92, v23, v116
v_fma_f32 v213, v93, v23, v117
v_cvt_pk_bf16_f32 v146, v210, v211
v_cvt_pk_bf16_f32 v147, v212, v213
s_waitcnt lgkmcnt(0)
s_barrier
ds_read_b32 v216, v26
v_fma_f32 v210, v94, v198, v118
v_fma_f32 v211, v94, v199, v119
v_fma_f32 v212, v94, v200, v120
v_fma_f32 v213, v94, v201, v121
v_cvt_pk_bf16_f32 v154, v210, v211
v_cvt_pk_bf16_f32 v155, v212, v213
v_fma_f32 v210, v86, v23, v110
v_fma_f32 v211, v87, v23, v111
v_fma_f32 v212, v88, v23, v112
v_fma_f32 v213, v89, v23, v113
v_cvt_pk_bf16_f32 v138, v210, v211
v_cvt_pk_bf16_f32 v139, v212, v213
s_waitcnt lgkmcnt(0)
v_mul_f32 v216, 0x3ae38e39, v216
v_fma_f32 v203, -v202, v202, v216
v_max_f32 v203, 0, v203
v_cvt_pk_bf16_f32 v203, v203, v203
v_lshlrev_b32 v203, 16, v203
v_add_f32 v203, 0x3727c5ac, v203
v_cvt_pk_bf16_f32 v203, v203, v203
v_lshlrev_b32 v203, 16, v203
v_sqrt_f32 v203, v203
s_nop 0
v_cvt_pk_bf16_f32 v203, v203, v203
v_lshlrev_b32 v203, 16, v203
v_rcp_f32 v216, v203
s_nop 0
ds_write_b32 v24, v216 offset:192
v_add_lshl_u32 v252, v48, 0, 7
v_bfi_b32 v252, s69, v252, v47
s_waitcnt vmcnt(4) lgkmcnt(0)
s_barrier
s_mov_b32 s70, 32
s_waitcnt lgkmcnt(0)
s_nop 1
s_branch .Lqa_in42
.p2align 6
.Lqa_top52:
s_waitcnt lgkmcnt(5)
v_mfma_f32_16x16x32_bf16 v[134:137], v[106:109], v[0:3], 0
s_waitcnt lgkmcnt(4)
ds_read_b128 v[58:61], v52 offset:4096
v_bfi_b32 v252, s69, v252, v47
v_mfma_f32_16x16x32_bf16 v[134:137], v[106:109], v[4:7], v[134:137]
ds_read_b32 v152, v21
ds_read_b32 v95, v24 offset:128
v_mfma_f32_16x16x32_bf16 v[110:113], a[0:3], v[0:3], 0
s_waitcnt lgkmcnt(5)
ds_read_b128 v[62:65], v51 offset:6144
ds_read_b32 v254, v29
v_mfma_f32_16x16x32_bf16 v[134:137], v[106:109], v[96:99], v[134:137]
s_waitcnt lgkmcnt(6)
ds_read_b128 v[66:69], v52 offset:6144
ds_read_b32 v255, v30
v_mfma_f32_16x16x32_bf16 v[134:137], v[106:109], v[100:103], v[134:137]
ds_read_b128 v[198:201], v25 offset:64
ds_read_b64 v[160:161], v22 offset:41472
v_mfma_f32_16x16x32_bf16 v[114:117], a[4:7], v[0:3], 0
s_waitcnt lgkmcnt(9)
ds_read_b128 v[70:73], v51 offset:8192
ds_read_b32 v144, v20
v_mfma_f32_16x16x32_bf16 v[134:137], v[106:109], v[54:57], v[134:137]
s_waitcnt lgkmcnt(10)
ds_read_b128 v[74:77], v52 offset:8192
v_fma_f32 v210, v90, v23, v126
v_mfma_f32_16x16x32_bf16 v[134:137], v[106:109], v[58:61], v[134:137]
v_fma_f32 v211, v91, v23, v127
v_fma_f32 v212, v92, v23, v128
v_mfma_f32_16x16x32_bf16 v[118:121], v[0:3], a[8:11], 0
s_waitcnt lgkmcnt(8)
ds_read_b128 v[78:81], v51 offset:10240
v_fma_f32 v213, v93, v23, v129
v_mfma_f32_16x16x32_bf16 v[134:137], v[106:109], v[62:65], v[134:137]
s_waitcnt lgkmcnt(7)
ds_read_b128 v[82:85], v52 offset:10240
v_cvt_pk_bf16_f32 v150, v210, v211
v_mfma_f32_16x16x32_bf16 v[134:137], v[106:109], v[66:69], v[134:137]
v_cvt_pk_bf16_f32 v151, v212, v213
v_max_f32 v10, v190, v191
v_mfma_f32_16x16x32_bf16 v[110:113], a[12:15], v[4:7], v[110:113]
ds_read_b128 v[162:165], v51 offset:12288
v_mfma_f32_16x16x32_bf16 v[186:189], v[150:153], v[138:141], v[32:35]
s_waitcnt lgkmcnt(7)
v_max_f32 v44, v192, v193
v_fma_f32 v210, v94, v198, v130
s_waitcnt lgkmcnt(5)
v_mfma_f32_16x16x32_bf16 v[134:137], v[106:109], v[70:73], v[134:137]
s_xor_b32 s49, s49, 0x10000
ds_read_b128 v[166:169], v52 offset:12288
s_waitcnt lgkmcnt(4)
v_mfma_f32_16x16x32_bf16 v[134:137], v[106:109], v[74:77], v[134:137]
s_add_u32 m0, s49, 0
v_mul_f32 v95, 0x3eae1a93, v95
v_mul_f32 v8, v254, v95
v_mfma_f32_16x16x32_bf16 v[114:117], a[16:19], v[4:7], v[114:117]
s_waitcnt lgkmcnt(3)
ds_read_b128 v[170:173], v51 offset:14336
v_mul_f32 v9, v255, v95
v_mfma_f32_16x16x32_bf16 v[134:137], v[106:109], v[78:81], v[134:137]
s_waitcnt lgkmcnt(3)
ds_read_b128 v[174:177], v52 offset:14336
v_max3_f32 v10, v186, v187, v10
v_mfma_f32_16x16x32_bf16 v[134:137], v[106:109], v[82:85], v[134:137]
v_max3_f32 v44, v188, v189, v44
v_mul_f32 v10, v10, v8
v_mfma_f32_16x16x32_bf16 v[118:121], v[4:7], a[20:23], v[118:121]
s_waitcnt lgkmcnt(3)
ds_read_b128 v[178:181], v51 offset:16384
v_mul_f32 v44, v44, v9
v_mfma_f32_16x16x32_bf16 v[134:137], v[106:109], v[162:165], v[134:137]
s_waitcnt lgkmcnt(3)
ds_read_b128 v[182:185], v52 offset:16384
v_max_f32 v45, v10, v44
v_mfma_f32_16x16x32_bf16 v[134:137], v[106:109], v[166:169], v[134:137]
v_mov_b32 v46, v45
v_fma_f32 v211, v94, v199, v131
v_mfma_f32_16x16x32_bf16 v[110:113], a[24:27], v[96:99], v[110:113]
s_waitcnt lgkmcnt(3)
global_load_lds_dwordx4 v11, s[42:43] offset:-2048 sc1
v_fma_f32 v212, v94, v200, v132
v_mfma_f32_16x16x32_bf16 v[134:137], v[106:109], v[170:173], v[134:137]
s_waitcnt lgkmcnt(2)
v_permlane16_swap_b32 v45, v46
v_mfma_f32_16x16x32_bf16 v[134:137], v[106:109], v[174:177], v[134:137]
v_max_f32 v45, v45, v46
v_mov_b32 v46, v45
v_mfma_f32_16x16x32_bf16 v[114:117], a[28:31], v[96:99], v[114:117]
s_waitcnt lgkmcnt(1)
v_permlane32_swap_b32 v45, v46
v_mfma_f32_16x16x32_bf16 v[134:137], v[106:109], v[178:181], v[134:137]
s_waitcnt lgkmcnt(0)
v_max_f32 v45, v45, v46
v_sub_f32 v10, v10, v45
v_mfma_f32_16x16x32_bf16 v[134:137], v[106:109], v[182:185], v[134:137]
v_sub_f32 v44, v44, v45
v_fma_f32 v213, v94, v201, v133
v_mfma_f32_16x16x32_bf16 v[118:121], v[96:99], a[32:35], v[118:121]
v_exp_f32 v214, v10
v_mfma_f32_16x16x32_bf16 v[110:113], a[36:39], v[100:103], v[110:113]
v_exp_f32 v215, v44
v_mfma_f32_16x16x32_bf16 v[114:117], a[40:43], v[100:103], v[114:117]
s_nop 0
v_mul_f32 v202, 0x3ae38e39, v134
v_cvt_pk_bf16_f32 v216, -v202, -v202
v_mfma_f32_16x16x32_bf16 v[118:121], v[100:103], a[44:47], v[118:121]
v_lshlrev_b32 v216, 16, v216
global_load_lds_dwordx4 v12, s[42:43] offset:-1024 sc1
v_mfma_f32_16x16x32_bf16 v[110:113], a[48:51], v[54:57], v[110:113]
ds_write_b32 v24, v216 offset:0
v_mfma_f32_16x16x32_bf16 v[114:117], a[52:55], v[54:57], v[114:117]
v_add_f32 v45, v214, v215
v_mov_b32 v46, v45
v_mfma_f32_16x16x32_bf16 v[118:121], v[54:57], a[56:59], v[118:121]
v_mul_f32 v214, v214, v254
v_cvt_pk_bf16_f32 v156, v210, v211
v_mfma_f32_16x16x32_bf16 v[110:113], a[60:63], v[58:61], v[110:113]
v_permlane16_swap_b32 v45, v46
v_mfma_f32_16x16x32_bf16 v[114:117], a[64:67], v[58:61], v[114:117]
v_add_f32 v45, v45, v46
v_mov_b32 v46, v45
v_mfma_f32_16x16x32_bf16 v[118:121], v[58:61], a[68:71], v[118:121]
v_mfma_f32_16x16x32_bf16 v[110:113], a[72:75], v[62:65], v[110:113]
s_waitcnt lgkmcnt(0)
s_barrier
v_mfma_f32_16x16x32_bf16 v[114:117], a[76:79], v[62:65], v[114:117]
global_load_lds_dwordx4 v13, s[42:43] offset:0 sc1
ds_read_b32 v216, v26
v_mfma_f32_16x16x32_bf16 v[118:121], v[62:65], a[80:83], v[118:121]
v_mul_f32 v215, v215, v255
v_cvt_pk_bf16_f32 v157, v212, v213
v_mfma_f32_16x16x32_bf16 v[110:113], a[84:87], v[66:69], v[110:113]
v_permlane32_swap_b32 v45, v46
v_mfma_f32_16x16x32_bf16 v[114:117], a[88:91], v[66:69], v[114:117]
v_add_f32 v45, v45, v46
v_fma_f32 v210, v86, v23, v122
v_mfma_f32_16x16x32_bf16 v[118:121], v[66:69], a[92:95], v[118:121]
v_rcp_f32 v45, v45
v_mfma_f32_16x16x32_bf16 v[110:113], a[96:99], v[70:73], v[110:113]
v_mul_f32 v214, v214, v45
v_mul_f32 v215, v215, v45
v_mfma_f32_16x16x32_bf16 v[114:117], a[100:103], v[70:73], v[114:117]
v_cvt_pk_bf16_f32 v214, v214, v214
v_cvt_pk_bf16_f32 v215, v215, v215
v_mfma_f32_16x16x32_bf16 v[118:121], v[70:73], a[104:107], v[118:121]
v_and_b32 v196, v214, v40
v_and_b32 v197, v215, v41
v_mfma_f32_16x16x32_bf16 v[110:113], a[108:111], v[74:77], v[110:113]
global_load_lds_dwordx4 v14, s[42:43] offset:1024 sc1
s_add_u32 s42, s42, s46
s_addc_u32 s43, s43, s47
v_mfma_f32_16x16x32_bf16 v[114:117], a[112:115], v[74:77], v[114:117]
v_and_b32 v194, v214, v42
v_and_b32 v195, v215, v43
v_mfma_f32_16x16x32_bf16 v[118:121], v[74:77], a[116:119], v[118:121]
s_nop 0
v_mfma_f32_16x16x32_bf16 v[190:193], v[154:157], v[194:197], 0
v_mfma_f32_16x16x32_bf16 v[186:189], v[158:161], v[194:197], 0
s_waitcnt lgkmcnt(0)
v_mul_f32 v216, 0x3ae38e39, v216
v_fma_f32 v203, -v202, v202, v216
v_mfma_f32_16x16x32_bf16 v[110:113], a[120:123], v[78:81], v[110:113]
v_max_f32 v203, 0, v203
v_cvt_pk_bf16_f32 v203, v203, v203
v_mfma_f32_16x16x32_bf16 v[114:117], a[124:127], v[78:81], v[114:117]
v_lshlrev_b32 v203, 16, v203
v_add_f32 v203, 0x3727c5ac, v203
v_mfma_f32_16x16x32_bf16 v[118:121], v[78:81], a[128:131], v[118:121]
v_cvt_pk_bf16_f32 v203, v203, v203
v_lshlrev_b32 v203, 16, v203
v_mfma_f32_16x16x32_bf16 v[110:113], a[132:135], v[82:85], v[110:113]
v_cvt_pk_bf16_f32 v104, v190, v191
v_cvt_pk_bf16_f32 v105, v192, v193
v_mfma_f32_16x16x32_bf16 v[114:117], a[136:139], v[82:85], v[114:117]
ds_write_b64 v18, v[104:105]
v_mfma_f32_16x16x32_bf16 v[118:121], v[82:85], a[140:143], v[118:121]
v_cvt_pk_bf16_f32 v214, v188, v189
v_fma_f32 v211, v87, v23, v123
v_mfma_f32_16x16x32_bf16 v[110:113], a[144:147], v[162:165], v[110:113]
ds_write_b32 v19, v214
v_mfma_f32_16x16x32_bf16 v[114:117], a[148:151], v[162:165], v[114:117]
v_sqrt_f32 v203, v203
v_mfma_f32_16x16x32_bf16 v[118:121], v[162:165], a[152:155], v[118:121]
v_cvt_pk_bf16_f32 v203, v203, v203
v_lshlrev_b32 v203, 16, v203
v_mfma_f32_16x16x32_bf16 v[110:113], a[156:159], v[166:169], v[110:113]
v_rcp_f32 v216, v203
v_mfma_f32_16x16x32_bf16 v[114:117], a[160:163], v[166:169], v[114:117]
ds_write_b32 v24, v216 offset:128
v_mfma_f32_16x16x32_bf16 v[118:121], v[166:169], a[164:167], v[118:121]
v_fma_f32 v212, v88, v23, v124
v_fma_f32 v213, v89, v23, v125
v_mfma_f32_16x16x32_bf16 v[110:113], a[168:171], v[170:173], v[110:113]
v_cvt_pk_bf16_f32 v142, v210, v211
v_cvt_pk_bf16_f32 v143, v212, v213
v_mfma_f32_16x16x32_bf16 v[114:117], a[172:175], v[170:173], v[114:117]
v_mfma_f32_16x16x32_bf16 v[118:121], v[170:173], a[176:179], v[118:121]
v_mfma_f32_16x16x32_bf16 v[110:113], a[180:183], v[174:177], v[110:113]
v_mfma_f32_16x16x32_bf16 v[114:117], a[184:187], v[174:177], v[114:117]
s_waitcnt vmcnt(5) lgkmcnt(0)
s_barrier
v_mfma_f32_16x16x32_bf16 v[118:121], v[174:177], a[188:191], v[118:121]
ds_read_b128 v[0:3], v51 offset:18432
v_mfma_f32_16x16x32_bf16 v[110:113], a[192:195], v[178:181], v[110:113]
ds_read_b128 v[4:7], v52 offset:18432
ds_read_b32 v23, v24 offset:0
v_mfma_f32_16x16x32_bf16 v[114:117], a[196:199], v[178:181], v[114:117]
s_mov_b64 exec, 1
global_store_dword v252, v48, s[38:39] sc1
s_mov_b64 exec, -1
v_mfma_f32_16x16x32_bf16 v[190:193], v[150:153], v[142:145], v[36:39]
v_mfma_f32_16x16x32_bf16 v[118:121], v[178:181], a[200:203], v[118:121]
ds_read_b128 v[96:99], v51 offset:20480
v_add_u32 v48, 1, v48
v_mfma_f32_16x16x32_bf16 v[110:113], a[204:207], v[182:185], v[110:113]
ds_read_b128 v[100:103], v52 offset:20480
v_add_lshl_u32 v252, v48, 0, 7
v_mfma_f32_16x16x32_bf16 v[114:117], a[208:211], v[182:185], v[114:117]
v_bfi_b32 v252, s69, v252, v47
ds_read_b32 v148, v21
v_mfma_f32_16x16x32_bf16 v[118:121], v[182:185], a[212:215], v[118:121]
s_waitcnt lgkmcnt(5)
ds_read_b128 v[54:57], v51 offset:22528
ds_read_b32 v95, v24 offset:192
v_mfma_f32_16x16x32_bf16 v[134:137], v[106:109], v[0:3], 0
s_waitcnt lgkmcnt(6)
ds_read_b128 v[58:61], v52 offset:22528
ds_read_b32 v254, v27
v_mfma_f32_16x16x32_bf16 v[134:137], v[106:109], v[4:7], v[134:137]
ds_read_b32 v255, v28
ds_read_b128 v[198:201], v25 offset:0
v_mfma_f32_16x16x32_bf16 v[122:125], a[0:3], v[0:3], 0
s_waitcnt lgkmcnt(8)
ds_read_b128 v[62:65], v51 offset:24576
ds_read_b64 v[158:159], v22 offset:41472
v_mfma_f32_16x16x32_bf16 v[134:137], v[106:109], v[96:99], v[134:137]
s_waitcnt lgkmcnt(9)
ds_read_b128 v[66:69], v52 offset:24576
ds_read_b32 v140, v20
v_mfma_f32_16x16x32_bf16 v[134:137], v[106:109], v[100:103], v[134:137]
v_fma_f32 v210, v90, v23, v114
v_fma_f32 v211, v91, v23, v115
v_mfma_f32_16x16x32_bf16 v[126:129], a[4:7], v[0:3], 0
s_waitcnt lgkmcnt(9)
ds_read_b128 v[70:73], v51 offset:26624
v_fma_f32 v212, v92, v23, v116
v_mfma_f32_16x16x32_bf16 v[134:137], v[106:109], v[54:57], v[134:137]
s_waitcnt lgkmcnt(8)
ds_read_b128 v[74:77], v52 offset:26624
v_fma_f32 v213, v93, v23, v117
v_mfma_f32_16x16x32_bf16 v[134:137], v[106:109], v[58:61], v[134:137]
v_cvt_pk_bf16_f32 v146, v210, v211
v_cvt_pk_bf16_f32 v147, v212, v213
v_mfma_f32_16x16x32_bf16 v[130:133], v[0:3], a[8:11], 0
ds_read_b128 v[78:81], v51 offset:28672
v_mfma_f32_16x16x32_bf16 v[186:189], v[146:149], v[142:145], v[206:209]
s_waitcnt lgkmcnt(6)
v_max_f32 v10, v190, v191
v_max_f32 v44, v192, v193
v_mfma_f32_16x16x32_bf16 v[134:137], v[106:109], v[62:65], v[134:137]
s_waitcnt lgkmcnt(4)
ds_read_b128 v[82:85], v52 offset:28672
v_fma_f32 v210, v94, v198, v118
v_mfma_f32_16x16x32_bf16 v[134:137], v[106:109], v[66:69], v[134:137]
v_mul_f32 v95, 0x3eae1a93, v95
v_mul_f32 v8, v254, v95
v_mfma_f32_16x16x32_bf16 v[122:125], a[12:15], v[4:7], v[122:125]
s_waitcnt lgkmcnt(3)
ds_read_b128 v[162:165], v51 offset:30720
v_mul_f32 v9, v255, v95
v_mfma_f32_16x16x32_bf16 v[134:137], v[106:109], v[70:73], v[134:137]
s_add_u32 m0, s49, 18432
ds_read_b128 v[166:169], v52 offset:30720
s_waitcnt lgkmcnt(4)
v_mfma_f32_16x16x32_bf16 v[134:137], v[106:109], v[74:77], v[134:137]
v_max3_f32 v10, v186, v187, v10
v_max3_f32 v44, v188, v189, v44
v_mfma_f32_16x16x32_bf16 v[126:129], a[16:19], v[4:7], v[126:129]
s_waitcnt lgkmcnt(3)
ds_read_b128 v[170:173], v51 offset:32768
v_mul_f32 v10, v10, v8
v_mfma_f32_16x16x32_bf16 v[134:137], v[106:109], v[78:81], v[134:137]
s_waitcnt lgkmcnt(3)
ds_read_b128 v[174:177], v52 offset:32768
v_mul_f32 v44, v44, v9
v_mfma_f32_16x16x32_bf16 v[134:137], v[106:109], v[82:85], v[134:137]
v_max_f32 v45, v10, v44
v_mov_b32 v46, v45
v_mfma_f32_16x16x32_bf16 v[130:133], v[4:7], a[20:23], v[130:133]
s_waitcnt lgkmcnt(3)
ds_read_b128 v[178:181], v51 offset:34816
v_fma_f32 v211, v94, v199, v119
v_mfma_f32_16x16x32_bf16 v[134:137], v[106:109], v[162:165], v[134:137]
s_waitcnt lgkmcnt(3)
ds_read_b128 v[182:185], v52 offset:34816
v_fma_f32 v212, v94, v200, v120
v_mfma_f32_16x16x32_bf16 v[134:137], v[106:109], v[166:169], v[134:137]
global_load_lds_dwordx4 v11, s[42:43] offset:-2048 sc1
v_fma_f32 v213, v94, v201, v121
v_mfma_f32_16x16x32_bf16 v[122:125], a[24:27], v[96:99], v[122:125]
s_waitcnt lgkmcnt(3)
v_permlane16_swap_b32 v45, v46
v_mfma_f32_16x16x32_bf16 v[134:137], v[106:109], v[170:173], v[134:137]
s_waitcnt lgkmcnt(2)
v_max_f32 v45, v45, v46
v_mov_b32 v46, v45
v_mfma_f32_16x16x32_bf16 v[134:137], v[106:109], v[174:177], v[134:137]
s_nop 0
v_permlane32_swap_b32 v45, v46
v_mfma_f32_16x16x32_bf16 v[126:129], a[28:31], v[96:99], v[126:129]
s_waitcnt lgkmcnt(1)
v_max_f32 v45, v45, v46
v_sub_f32 v10, v10, v45
v_mfma_f32_16x16x32_bf16 v[134:137], v[106:109], v[178:181], v[134:137]
s_waitcnt lgkmcnt(0)
v_sub_f32 v44, v44, v45
v_cvt_pk_bf16_f32 v154, v210, v211
v_mfma_f32_16x16x32_bf16 v[134:137], v[106:109], v[182:185], v[134:137]
v_exp_f32 v214, v10
v_mfma_f32_16x16x32_bf16 v[130:133], v[96:99], a[32:35], v[130:133]
v_exp_f32 v215, v44
v_mfma_f32_16x16x32_bf16 v[122:125], a[36:39], v[100:103], v[122:125]
global_load_lds_dwordx4 v12, s[42:43] offset:-1024 sc1
v_add_f32 v45, v214, v215
v_mfma_f32_16x16x32_bf16 v[126:129], a[40:43], v[100:103], v[126:129]
s_nop 0
v_mul_f32 v202, 0x3ae38e39, v134
v_cvt_pk_bf16_f32 v216, -v202, -v202
v_mfma_f32_16x16x32_bf16 v[130:133], v[100:103], a[44:47], v[130:133]
v_lshlrev_b32 v216, 16, v216
)"
R"(v_mov_b32 v46, v45
v_mfma_f32_16x16x32_bf16 v[122:125], a[48:51], v[54:57], v[122:125]
ds_write_b32 v24, v216 offset:64
v_mfma_f32_16x16x32_bf16 v[126:129], a[52:55], v[54:57], v[126:129]
v_mul_f32 v214, v214, v254
v_cvt_pk_bf16_f32 v155, v212, v213
v_mfma_f32_16x16x32_bf16 v[130:133], v[54:57], a[56:59], v[130:133]
v_permlane16_swap_b32 v45, v46
v_mfma_f32_16x16x32_bf16 v[122:125], a[60:63], v[58:61], v[122:125]
v_add_f32 v45, v45, v46
v_mov_b32 v46, v45
v_mfma_f32_16x16x32_bf16 v[126:129], a[64:67], v[58:61], v[126:129]
v_mul_f32 v215, v215, v255
v_fma_f32 v210, v86, v23, v110
v_mfma_f32_16x16x32_bf16 v[130:133], v[58:61], a[68:71], v[130:133]
v_mfma_f32_16x16x32_bf16 v[122:125], a[72:75], v[62:65], v[122:125]
s_waitcnt lgkmcnt(0)
s_barrier
v_mfma_f32_16x16x32_bf16 v[126:129], a[76:79], v[62:65], v[126:129]
global_load_lds_dwordx4 v13, s[42:43] offset:0 sc1
ds_read_b32 v216, v26
v_mfma_f32_16x16x32_bf16 v[130:133], v[62:65], a[80:83], v[130:133]
v_permlane32_swap_b32 v45, v46
v_mfma_f32_16x16x32_bf16 v[122:125], a[84:87], v[66:69], v[122:125]
v_add_f32 v45, v45, v46
v_fma_f32 v211, v87, v23, v111
v_mfma_f32_16x16x32_bf16 v[126:129], a[88:91], v[66:69], v[126:129]
v_rcp_f32 v45, v45
v_mfma_f32_16x16x32_bf16 v[130:133], v[66:69], a[92:95], v[130:133]
v_mul_f32 v214, v214, v45
v_mul_f32 v215, v215, v45
v_mfma_f32_16x16x32_bf16 v[122:125], a[96:99], v[70:73], v[122:125]
v_cvt_pk_bf16_f32 v214, v214, v214
v_cvt_pk_bf16_f32 v215, v215, v215
v_mfma_f32_16x16x32_bf16 v[126:129], a[100:103], v[70:73], v[126:129]
v_and_b32 v194, v214, v40
v_and_b32 v195, v215, v41
v_mfma_f32_16x16x32_bf16 v[130:133], v[70:73], a[104:107], v[130:133]
v_and_b32 v196, v214, v42
v_and_b32 v197, v215, v43
v_mfma_f32_16x16x32_bf16 v[122:125], a[108:111], v[74:77], v[122:125]
global_load_lds_dwordx4 v14, s[42:43] offset:1024 sc1
s_sub_u32 s42, s42, 18432
s_subb_u32 s43, s43, 0
v_mfma_f32_16x16x32_bf16 v[190:193], v[154:157], v[194:197], 0
v_mfma_f32_16x16x32_bf16 v[186:189], v[158:161], v[194:197], 0
s_waitcnt lgkmcnt(0)
v_mul_f32 v216, 0x3ae38e39, v216
v_fma_f32 v203, -v202, v202, v216
v_mfma_f32_16x16x32_bf16 v[126:129], a[112:115], v[74:77], v[126:129]
v_max_f32 v203, 0, v203
v_cvt_pk_bf16_f32 v203, v203, v203
v_mfma_f32_16x16x32_bf16 v[130:133], v[74:77], a[116:119], v[130:133]
v_lshlrev_b32 v203, 16, v203
v_add_f32 v203, 0x3727c5ac, v203
v_mfma_f32_16x16x32_bf16 v[122:125], a[120:123], v[78:81], v[122:125]
v_cvt_pk_bf16_f32 v203, v203, v203
v_lshlrev_b32 v203, 16, v203
v_mfma_f32_16x16x32_bf16 v[126:129], a[124:127], v[78:81], v[126:129]
v_cvt_pk_bf16_f32 v104, v190, v191
v_cvt_pk_bf16_f32 v105, v192, v193
v_mfma_f32_16x16x32_bf16 v[130:133], v[78:81], a[128:131], v[130:133]
ds_write_b64 v18, v[104:105]
v_mfma_f32_16x16x32_bf16 v[122:125], a[132:135], v[82:85], v[122:125]
v_cvt_pk_bf16_f32 v214, v188, v189
v_fma_f32 v212, v88, v23, v112
v_mfma_f32_16x16x32_bf16 v[126:129], a[136:139], v[82:85], v[126:129]
ds_write_b32 v19, v214
v_mfma_f32_16x16x32_bf16 v[130:133], v[82:85], a[140:143], v[130:133]
v_sqrt_f32 v203, v203
v_mfma_f32_16x16x32_bf16 v[122:125], a[144:147], v[162:165], v[122:125]
v_cvt_pk_bf16_f32 v203, v203, v203
v_lshlrev_b32 v203, 16, v203
v_mfma_f32_16x16x32_bf16 v[126:129], a[148:151], v[162:165], v[126:129]
v_rcp_f32 v216, v203
v_mfma_f32_16x16x32_bf16 v[130:133], v[162:165], a[152:155], v[130:133]
ds_write_b32 v24, v216 offset:192
v_mfma_f32_16x16x32_bf16 v[122:125], a[156:159], v[166:169], v[122:125]
v_fma_f32 v213, v89, v23, v113
v_cvt_pk_bf16_f32 v138, v210, v211
v_mfma_f32_16x16x32_bf16 v[126:129], a[160:163], v[166:169], v[126:129]
v_cvt_pk_bf16_f32 v139, v212, v213
v_mfma_f32_16x16x32_bf16 v[130:133], v[166:169], a[164:167], v[130:133]
v_mfma_f32_16x16x32_bf16 v[122:125], a[168:171], v[170:173], v[122:125]
v_mfma_f32_16x16x32_bf16 v[126:129], a[172:175], v[170:173], v[126:129]
v_xor_b32 v51, 0x10000, v51
v_xor_b32 v52, 0x10000, v52
v_mfma_f32_16x16x32_bf16 v[130:133], v[170:173], a[176:179], v[130:133]
v_mfma_f32_16x16x32_bf16 v[122:125], a[180:183], v[174:177], v[122:125]
v_mfma_f32_16x16x32_bf16 v[126:129], a[184:187], v[174:177], v[126:129]
s_waitcnt vmcnt(5) lgkmcnt(0)
s_barrier
.Lqa_in42:
v_mfma_f32_16x16x32_bf16 v[130:133], v[174:177], a[188:191], v[130:133]
s_cmp_eq_u32 s70, s73
s_cbranch_scc1 .Lqa_hk43
.Lqa_hr44:
ds_read_b128 v[0:3], v51 offset:0
v_mfma_f32_16x16x32_bf16 v[122:125], a[192:195], v[178:181], v[122:125]
ds_read_b128 v[4:7], v52 offset:0
ds_read_b32 v23, v24 offset:64
v_mfma_f32_16x16x32_bf16 v[126:129], a[196:199], v[178:181], v[126:129]
s_mov_b64 exec, 1
global_store_dword v252, v48, s[38:39] sc1
s_mov_b64 exec, -1
v_mfma_f32_16x16x32_bf16 v[190:193], v[146:149], v[138:141], v[36:39]
v_mfma_f32_16x16x32_bf16 v[130:133], v[178:181], a[200:203], v[130:133]
ds_read_b128 v[96:99], v51 offset:2048
v_add_u32 v48, 1, v48
v_mfma_f32_16x16x32_bf16 v[122:125], a[204:207], v[182:185], v[122:125]
ds_read_b128 v[100:103], v52 offset:2048
v_add_lshl_u32 v252, v48, 0, 7
v_mfma_f32_16x16x32_bf16 v[126:129], a[208:211], v[182:185], v[126:129]
v_mfma_f32_16x16x32_bf16 v[130:133], v[182:185], a[212:215], v[130:133]
s_sub_u32 s70, s70, 1
ds_read_b128 v[54:57], v51 offset:4096
s_cbranch_scc0 .Lqa_top52
s_waitcnt lgkmcnt(0)
v_bfi_b32 v252, s69, v252, v47
ds_read_b32 v152, v21
ds_read_b32 v95, v24 offset:128
ds_read_b32 v254, v29
ds_read_b32 v255, v30
ds_read_b128 v[198:201], v25 offset:64
ds_read_b64 v[160:161], v22 offset:41472
ds_read_b32 v144, v20
v_fma_f32 v210, v90, v23, v126
v_fma_f32 v211, v91, v23, v127
v_fma_f32 v212, v92, v23, v128
v_fma_f32 v213, v93, v23, v129
v_cvt_pk_bf16_f32 v150, v210, v211
v_cvt_pk_bf16_f32 v151, v212, v213
v_max_f32 v10, v190, v191
s_waitcnt lgkmcnt(6)
v_mfma_f32_16x16x32_bf16 v[186:189], v[150:153], v[138:141], v[32:35]
s_waitcnt lgkmcnt(2)
v_max_f32 v44, v192, v193
v_fma_f32 v210, v94, v198, v130
v_mul_f32 v95, 0x3eae1a93, v95
v_mul_f32 v8, v254, v95
v_mul_f32 v9, v255, v95
s_nop 1
v_max3_f32 v10, v186, v187, v10
v_max3_f32 v44, v188, v189, v44
v_mul_f32 v10, v10, v8
v_mul_f32 v44, v44, v9
v_max_f32 v45, v10, v44
v_mov_b32 v46, v45
v_fma_f32 v211, v94, v199, v131
v_fma_f32 v212, v94, v200, v132
v_permlane16_swap_b32 v45, v46
v_max_f32 v45, v45, v46
v_mov_b32 v46, v45
s_nop 1
v_permlane32_swap_b32 v45, v46
v_max_f32 v45, v45, v46
v_sub_f32 v10, v10, v45
v_sub_f32 v44, v44, v45
v_fma_f32 v213, v94, v201, v133
v_exp_f32 v214, v10
v_exp_f32 v215, v44
s_nop 0
v_add_f32 v45, v214, v215
v_mov_b32 v46, v45
v_mul_f32 v214, v214, v254
v_cvt_pk_bf16_f32 v156, v210, v211
v_permlane16_swap_b32 v45, v46
v_add_f32 v45, v45, v46
v_mov_b32 v46, v45
s_waitcnt lgkmcnt(0)
s_barrier
v_mul_f32 v215, v215, v255
v_cvt_pk_bf16_f32 v157, v212, v213
v_permlane32_swap_b32 v45, v46
v_add_f32 v45, v45, v46
v_fma_f32 v210, v86, v23, v122
v_rcp_f32 v45, v45
s_nop 0
v_mul_f32 v214, v214, v45
v_mul_f32 v215, v215, v45
v_cvt_pk_bf16_f32 v214, v214, v214
v_cvt_pk_bf16_f32 v215, v215, v215
v_and_b32 v196, v214, v40
v_and_b32 v197, v215, v41
v_and_b32 v194, v214, v42
v_and_b32 v195, v215, v43
s_nop 1
v_mfma_f32_16x16x32_bf16 v[190:193], v[154:157], v[194:197], 0
v_mfma_f32_16x16x32_bf16 v[186:189], v[158:161], v[194:197], 0
s_nop 6
v_cvt_pk_bf16_f32 v104, v190, v191
v_cvt_pk_bf16_f32 v105, v192, v193
ds_write_b64 v18, v[104:105]
v_cvt_pk_bf16_f32 v214, v188, v189
v_fma_f32 v211, v87, v23, v123
ds_write_b32 v19, v214
v_fma_f32 v212, v88, v23, v124
v_fma_f32 v213, v89, v23, v125
v_cvt_pk_bf16_f32 v142, v210, v211
v_cvt_pk_bf16_f32 v143, v212, v213
s_waitcnt vmcnt(0)
s_waitcnt lgkmcnt(0)
s_barrier
s_mov_b64 exec, 1
global_store_dword v252, v48, s[38:39] sc1
s_mov_b64 exec, -1
s_waitcnt vmcnt(1)
s_barrier
v_add_u32 v48, 8, v48
v_mbcnt_lo_u32_b32 v252, -1, 0
v_mbcnt_hi_u32_b32 v252, -1, v252
v_add_lshl_u32 v252, v252, 0, 7
v_bfi_b32 v252, s69, v252, v47
s_mov_b64 exec, 0xff
global_store_dword v252, v48, s[38:39] sc1
s_mov_b64 exec, -1
s_cmp_lg_u32 s53, 0
s_cbranch_scc1 .Lqa_n53
v_add_lshl_u32 v253, v50, 0, 7
v_bfi_b32 v253, s69, v253, v49
global_load_dword v53, v253, s[38:39] sc1
s_waitcnt vmcnt(0)
v_sub_u32 v53, v53, v50
v_cmp_lt_u32 vcc, 0x7f, v53
s_cbranch_vccnz .Lqa_sp54
.Lqa_rt55:
v_add_u32 v49, 102400, v49
v_add_lshl_u32 v253, v50, 0, 7
v_bfi_b32 v253, s69, v253, v49
global_load_dword v53, v253, s[38:39] sc1
s_waitcnt vmcnt(0)
v_sub_u32 v53, v53, v50
v_cmp_lt_u32 vcc, 0x7f, v53
s_cbranch_vccnz .Lqa_sp56
.Lqa_rt57:
v_add_u32 v49, 102400, v49
v_add_lshl_u32 v253, v50, 0, 7
v_bfi_b32 v253, s69, v253, v49
global_load_dword v53, v253, s[38:39] sc1
s_waitcnt vmcnt(0)
v_sub_u32 v53, v53, v50
v_cmp_lt_u32 vcc, 0x7f, v53
s_cbranch_vccnz .Lqa_sp58
.Lqa_rt59:
v_add_u32 v49, 102400, v49
v_add_lshl_u32 v253, v50, 0, 7
v_bfi_b32 v253, s69, v253, v49
global_load_dword v53, v253, s[38:39] sc1
s_waitcnt vmcnt(0)
v_sub_u32 v53, v53, v50
v_cmp_lt_u32 vcc, 0x7f, v53
s_cbranch_vccnz .Lqa_sp60
.Lqa_rt61:
v_add_u32 v49, 102400, v49
s_load_dword s75, s[38:39], 0x0
s_waitcnt vmcnt(0) lgkmcnt(0)
s_add_u32 s75, s75, 1
v_mov_b32 v53, s75
v_mov_b32 v49, 0
global_store_dword v49, v53, s[38:39] sc1
.Lqa_n53:
s_endpgm
.Lqa_hk6:
s_cmp_lg_u32 s70, 18
s_cbranch_scc1 .Lqa_e8
s_add_u32 s42, s42, s50
s_addc_u32 s43, s43, 0
s_mov_b64 s[76:77], -1
s_mov_b64 s[46:47], s[44:45]
s_mov_b32 s73, 17
s_nop 1
s_branch .Lqa_hr7
.Lqa_e8:
s_cmp_lg_u32 s70, 17
s_cbranch_scc1 .Lqa_e9
s_mov_b32 s46, -18432
s_mov_b32 s47, -1
v_or_b32 v234, s52, v234
v_or_b32 v235, s52, v235
v_or_b32 v236, s52, v236
v_or_b32 v237, s52, v237
s_mov_b32 s73, 16
s_nop 1
s_branch .Lqa_hr7
.Lqa_e9:
s_cmp_lg_u32 s70, 16
s_cbranch_scc1 .Lqa_e10
v_mov_b32 v234, v32
v_mov_b32 v235, v33
v_mov_b32 v236, v34
v_mov_b32 v237, v35
s_mov_b32 s73, 15
s_nop 1
s_branch .Lqa_hr7
.Lqa_e10:
s_cmp_lg_u32 s70, 15
s_cbranch_scc1 .Lqa_e11
v_subrev_u32 v16, 18432, v16
s_mov_b32 s73, 3
s_nop 1
s_branch .Lqa_hr7
.Lqa_e11:
s_cmp_lg_u32 s70, 3
s_cbranch_scc1 .Lqa_e12
s_mov_b32 s73, 2
s_nop 1
s_branch .Lqa_hr7
.Lqa_e12:
s_cmp_lg_u32 s70, 2
s_cbranch_scc1 .Lqa_e13
s_add_u32 s46, s46, s50
s_addc_u32 s47, s47, 0
s_mov_b32 s73, 0
s_nop 1
s_branch .Lqa_hr7
.Lqa_e13:
s_cmp_lg_u32 s70, 0
s_cbranch_scc1 .Lqa_e14
v_or_b32 v32, s52, v32
v_or_b32 v33, s52, v33
v_or_b32 v34, s52, v34
v_or_b32 v35, s52, v35
s_mov_b32 s73, 999
s_nop 1
s_branch .Lqa_hr7
.Lqa_e14:
s_branch .Lqa_hr7
.Lqa_hk17:
s_cmp_lg_u32 s70, 18
s_cbranch_scc1 .Lqa_e19
s_add_u32 s42, s42, s50
s_addc_u32 s43, s43, 0
s_mov_b64 s[76:77], -1
s_mov_b64 s[46:47], s[44:45]
s_mov_b32 s73, 17
s_nop 1
s_branch .Lqa_hr18
.Lqa_e19:
s_cmp_lg_u32 s70, 17
s_cbranch_scc1 .Lqa_e20
s_mov_b32 s46, -18432
s_mov_b32 s47, -1
v_or_b32 v234, s52, v234
v_or_b32 v235, s52, v235
v_or_b32 v236, s52, v236
v_or_b32 v237, s52, v237
s_mov_b32 s73, 16
s_nop 1
s_branch .Lqa_hr18
.Lqa_e20:
s_cmp_lg_u32 s70, 16
s_cbranch_scc1 .Lqa_e21
v_mov_b32 v234, v32
v_mov_b32 v235, v33
v_mov_b32 v236, v34
v_mov_b32 v237, v35
s_mov_b32 s73, 15
s_nop 1
s_branch .Lqa_hr18
.Lqa_e21:
s_cmp_lg_u32 s70, 15
s_cbranch_scc1 .Lqa_e22
v_subrev_u32 v16, 18432, v16
s_mov_b32 s73, 3
s_nop 1
s_branch .Lqa_hr18
.Lqa_e22:
s_cmp_lg_u32 s70, 3
s_cbranch_scc1 .Lqa_e23
s_mov_b32 s73, 2
s_nop 1
s_branch .Lqa_hr18
.Lqa_e23:
s_cmp_lg_u32 s70, 2
s_cbranch_scc1 .Lqa_e24
s_add_u32 s46, s46, s50
s_addc_u32 s47, s47, 0
s_mov_b32 s73, 0
s_nop 1
s_branch .Lqa_hr18
.Lqa_e24:
s_cmp_lg_u32 s70, 0
s_cbranch_scc1 .Lqa_e25
v_or_b32 v32, s52, v32
v_or_b32 v33, s52, v33
v_or_b32 v34, s52, v34
v_or_b32 v35, s52, v35
s_mov_b32 s73, 999
s_nop 1
s_branch .Lqa_hr18
.Lqa_e25:
s_branch .Lqa_hr18
.Lqa_sp30:
s_sleep 1
v_add_lshl_u32 v253, v46, 0, 7
v_bfi_b32 v253, s69, v253, v44
global_load_dword v53, v253, s[38:39] sc1
s_add_u32 s71, s71, 1
s_waitcnt vmcnt(0)
v_sub_u32 v53, v53, v46
)"
R"(v_cmp_lt_u32 vcc, 0x7f, v53
s_and_b64 vcc, vcc, s[76:77]
s_cbranch_vccz .Lqa_rt31
s_cmp_lt_u32 s71, 3000000
s_cbranch_scc1 .Lqa_sp30
s_branch .Lqa_rt31
.Lqa_sp32:
s_sleep 1
v_add_lshl_u32 v253, v46, 0, 7
v_bfi_b32 v253, s69, v253, v44
global_load_dword v254, v253, s[38:39] sc1
s_add_u32 s71, s71, 1
s_waitcnt vmcnt(0)
v_sub_u32 v254, v254, v46
v_cmp_lt_u32 vcc, 0x7f, v254
s_and_b64 vcc, vcc, s[76:77]
s_cbranch_vccz .Lqa_rt33
s_cmp_lt_u32 s71, 3000000
s_cbranch_scc1 .Lqa_sp32
s_branch .Lqa_rt33
.Lqa_hk28:
s_cmp_lg_u32 s70, 18
s_cbranch_scc1 .Lqa_e34
s_add_u32 s42, s42, s50
s_addc_u32 s43, s43, 0
s_mov_b64 s[76:77], -1
s_mov_b64 s[46:47], s[44:45]
s_mov_b32 s73, 17
s_nop 1
s_branch .Lqa_hr29
.Lqa_e34:
s_cmp_lg_u32 s70, 17
s_cbranch_scc1 .Lqa_e35
s_mov_b32 s46, -18432
s_mov_b32 s47, -1
v_or_b32 v202, s52, v202
v_or_b32 v203, s52, v203
v_or_b32 v204, s52, v204
v_or_b32 v205, s52, v205
s_mov_b32 s73, 16
s_nop 1
s_branch .Lqa_hr29
.Lqa_e35:
s_cmp_lg_u32 s70, 16
s_cbranch_scc1 .Lqa_e36
v_mov_b32 v202, v32
v_mov_b32 v203, v33
v_mov_b32 v204, v34
v_mov_b32 v205, v35
s_mov_b32 s73, 15
s_nop 1
s_branch .Lqa_hr29
.Lqa_e36:
s_cmp_lg_u32 s70, 15
s_cbranch_scc1 .Lqa_e37
v_subrev_u32 v16, 18432, v16
s_mov_b32 s73, 3
s_nop 1
s_branch .Lqa_hr29
.Lqa_e37:
s_cmp_lg_u32 s70, 3
s_cbranch_scc1 .Lqa_e38
v_add_u32 v45, s51, v45
s_mov_b32 s73, 2
s_nop 1
s_branch .Lqa_hr29
.Lqa_e38:
s_cmp_lg_u32 s70, 2
s_cbranch_scc1 .Lqa_e39
v_add_u32 v44, s51, v44
v_add_u32 v46, -32, v46
s_add_u32 s46, s46, s50
s_addc_u32 s47, s47, 0
s_mov_b32 s73, 0
s_nop 1
s_branch .Lqa_hr29
.Lqa_e39:
s_cmp_lg_u32 s70, 0
s_cbranch_scc1 .Lqa_e40
v_or_b32 v32, s52, v32
v_or_b32 v33, s52, v33
v_or_b32 v34, s52, v34
v_or_b32 v35, s52, v35
s_mov_b32 s73, 999
s_nop 1
s_branch .Lqa_hr29
.Lqa_e40:
s_branch .Lqa_hr29
.Lqa_hk43:
s_cmp_lg_u32 s70, 18
s_cbranch_scc1 .Lqa_e45
s_add_u32 s42, s42, s50
s_addc_u32 s43, s43, 0
s_mov_b64 s[76:77], -1
s_mov_b64 s[46:47], s[44:45]
s_mov_b32 s73, 17
s_nop 1
s_branch .Lqa_hr44
.Lqa_e45:
s_cmp_lg_u32 s70, 17
s_cbranch_scc1 .Lqa_e46
s_mov_b32 s46, -18432
s_mov_b32 s47, -1
v_or_b32 v206, s52, v206
v_or_b32 v207, s52, v207
v_or_b32 v208, s52, v208
v_or_b32 v209, s52, v209
s_mov_b32 s73, 16
s_nop 1
s_branch .Lqa_hr44
.Lqa_e46:
s_cmp_lg_u32 s70, 16
s_cbranch_scc1 .Lqa_e47
v_mov_b32 v206, v32
v_mov_b32 v207, v33
v_mov_b32 v208, v34
v_mov_b32 v209, v35
s_mov_b32 s73, 15
s_nop 1
s_branch .Lqa_hr44
.Lqa_e47:
s_cmp_lg_u32 s70, 15
s_cbranch_scc1 .Lqa_e48
v_add_u32 v48, -1, v48
v_add_lshl_u32 v252, v48, 0, 7
v_bfi_b32 v252, s69, v252, v47
s_mov_b32 s73, 3
s_nop 1
s_branch .Lqa_hr44
.Lqa_e48:
s_cmp_lg_u32 s70, 3
s_cbranch_scc1 .Lqa_e49
s_mov_b32 s73, 2
s_nop 1
s_branch .Lqa_hr44
.Lqa_e49:
s_cmp_lg_u32 s70, 2
s_cbranch_scc1 .Lqa_e50
s_add_u32 s46, s46, s50
s_addc_u32 s47, s47, 0
s_mov_b32 s73, 0
s_nop 1
s_branch .Lqa_hr44
.Lqa_e50:
s_cmp_lg_u32 s70, 0
s_cbranch_scc1 .Lqa_e51
v_or_b32 v32, s52, v32
v_or_b32 v33, s52, v33
v_or_b32 v34, s52, v34
v_or_b32 v35, s52, v35
s_mov_b32 s73, 999
s_nop 1
s_branch .Lqa_hr44
.Lqa_e51:
s_branch .Lqa_hr44
.Lqa_sp54:
s_sleep 1
v_add_lshl_u32 v253, v50, 0, 7
v_bfi_b32 v253, s69, v253, v49
global_load_dword v53, v253, s[38:39] sc1
s_add_u32 s71, s71, 1
s_waitcnt vmcnt(0)
v_sub_u32 v53, v53, v50
v_cmp_lt_u32 vcc, 0x7f, v53
s_cbranch_vccz .Lqa_rt55
s_cmp_lt_u32 s71, 3000000
s_cbranch_scc1 .Lqa_sp54
s_branch .Lqa_rt55
.Lqa_sp56:
s_sleep 1
v_add_lshl_u32 v253, v50, 0, 7
v_bfi_b32 v253, s69, v253, v49
global_load_dword v53, v253, s[38:39] sc1
s_add_u32 s71, s71, 1
s_waitcnt vmcnt(0)
v_sub_u32 v53, v53, v50
v_cmp_lt_u32 vcc, 0x7f, v53
s_cbranch_vccz .Lqa_rt57
s_cmp_lt_u32 s71, 3000000
s_cbranch_scc1 .Lqa_sp56
s_branch .Lqa_rt57
.Lqa_sp58:
s_sleep 1
v_add_lshl_u32 v253, v50, 0, 7
v_bfi_b32 v253, s69, v253, v49
global_load_dword v53, v253, s[38:39] sc1
s_add_u32 s71, s71, 1
s_waitcnt vmcnt(0)
v_sub_u32 v53, v53, v50
v_cmp_lt_u32 vcc, 0x7f, v53
s_cbranch_vccz .Lqa_rt59
s_cmp_lt_u32 s71, 3000000
s_cbranch_scc1 .Lqa_sp58
s_branch .Lqa_rt59
.Lqa_sp60:
s_sleep 1
v_add_lshl_u32 v253, v50, 0, 7
v_bfi_b32 v253, s69, v253, v49
global_load_dword v53, v253, s[38:39] sc1
s_add_u32 s71, s71, 1
s_waitcnt vmcnt(0)
v_sub_u32 v53, v53, v50
v_cmp_lt_u32 vcc, 0x7f, v53
s_cbranch_vccz .Lqa_rt61
s_cmp_lt_u32 s71, 3000000
s_cbranch_scc1 .Lqa_sp60
s_branch .Lqa_rt61
.Lrole1:
s_and_b32 s75, s36, 3
s_cmp_eq_u32 s75, 1
s_cbranch_scc1 .Lwo_w2
s_cmp_eq_u32 s75, 2
s_cbranch_scc1 .Lwo_w3
s_cmp_eq_u32 s75, 3
s_cbranch_scc1 .Lwo_w4
.Lwo_w1:
s_add_u32 m0, s48, 4096
s_nop 0
global_load_lds_dwordx4 v0, s[40:41] offset:-4096
global_load_lds_dwordx4 v1, s[40:41] offset:-3072
global_load_lds_dwordx4 v2, s[40:41] offset:-2048
global_load_lds_dwordx4 v3, s[40:41] offset:-1024
global_load_lds_dwordx4 v4, s[40:41] offset:0
global_load_lds_dwordx4 v5, s[40:41] offset:1024
global_load_lds_dwordx4 v6, s[40:41] offset:2048
global_load_lds_dwordx4 v7, s[40:41] offset:3072
s_add_u32 m0, s48, 12288
s_nop 0
global_load_lds_dwordx4 v8, s[40:41] offset:-4096
s_add_u32 s40, s40, 73728
s_addc_u32 s41, s41, 0
s_add_u32 m0, s48, 40960
s_nop 0
global_load_lds_dwordx4 v0, s[40:41] offset:-4096
global_load_lds_dwordx4 v1, s[40:41] offset:-3072
global_load_lds_dwordx4 v2, s[40:41] offset:-2048
global_load_lds_dwordx4 v3, s[40:41] offset:-1024
global_load_lds_dwordx4 v4, s[40:41] offset:0
global_load_lds_dwordx4 v5, s[40:41] offset:1024
global_load_lds_dwordx4 v6, s[40:41] offset:2048
global_load_lds_dwordx4 v7, s[40:41] offset:3072
s_add_u32 m0, s48, 49152
s_nop 0
global_load_lds_dwordx4 v8, s[40:41] offset:-4096
s_add_u32 s40, s40, 73728
s_addc_u32 s41, s41, 0
s_add_u32 m0, s48, 77824
s_nop 0
global_load_lds_dwordx4 v0, s[40:41] offset:-4096
global_load_lds_dwordx4 v1, s[40:41] offset:-3072
global_load_lds_dwordx4 v2, s[40:41] offset:-2048
global_load_lds_dwordx4 v3, s[40:41] offset:-1024
global_load_lds_dwordx4 v4, s[40:41] offset:0
global_load_lds_dwordx4 v5, s[40:41] offset:1024
global_load_lds_dwordx4 v6, s[40:41] offset:2048
global_load_lds_dwordx4 v7, s[40:41] offset:3072
s_add_u32 m0, s48, 86016
s_nop 0
global_load_lds_dwordx4 v8, s[40:41] offset:-4096
s_add_u32 s40, s40, 73728
s_addc_u32 s41, s41, 0
v_add_u32 v204, 0x10000, v9
v_add_u32 v205, 0x20000, v9
v_add_u32 v206, 0x10000, v10
v_add_u32 v207, 0x20000, v10
s_waitcnt lgkmcnt(0)
s_mul_i32 s37, s37, 0x9E3779B1
s_add_u32 s37, s37, 0x7F4A7C15
v_add_u32 v22, s37, v22
v_add_u32 v24, s37, v24
s_waitcnt vmcnt(18)
s_barrier
s_add_u32 m0, s48, 114688
ds_read_b64_tr_b16 a[0:1], v9 offset:0
ds_read_b64_tr_b16 a[2:3], v9 offset:2304
global_load_lds_dwordx4 v0, s[40:41] offset:-4096
ds_read_b64_tr_b16 a[4:5], v9 offset:8
ds_read_b64_tr_b16 a[6:7], v9 offset:2312
global_load_lds_dwordx4 v1, s[40:41] offset:-3072
ds_read_b64_tr_b16 a[8:9], v9 offset:64
global_load_lds_dwordx4 v2, s[40:41] offset:-2048
ds_read_b64_tr_b16 a[10:11], v9 offset:2368
ds_read_b64_tr_b16 a[12:13], v9 offset:72
global_load_lds_dwordx4 v3, s[40:41] offset:-1024
ds_read_b64_tr_b16 a[14:15], v9 offset:2376
global_load_lds_dwordx4 v4, s[40:41] offset:0
ds_read_b64_tr_b16 v[60:61], v10 offset:0
ds_read_b64_tr_b16 v[62:63], v10 offset:2304
global_load_lds_dwordx4 v5, s[40:41] offset:1024
ds_read_b64_tr_b16 a[16:17], v9 offset:18432
global_load_lds_dwordx4 v6, s[40:41] offset:2048
ds_read_b64_tr_b16 a[18:19], v9 offset:20736
ds_read_b64_tr_b16 a[20:21], v9 offset:18440
global_load_lds_dwordx4 v7, s[40:41] offset:3072
ds_read_b64_tr_b16 a[22:23], v9 offset:20744
s_add_u32 m0, s48, 122880
ds_read_b64_tr_b16 a[24:25], v9 offset:18496
ds_read_b64_tr_b16 a[26:27], v9 offset:20800
global_load_lds_dwordx4 v8, s[40:41] offset:-4096
ds_read_b64_tr_b16 a[28:29], v9 offset:18504
s_add_u32 s40, s40, 73728
ds_read_b64_tr_b16 a[30:31], v9 offset:20808
ds_read_b64_tr_b16 v[64:65], v10 offset:18432
s_addc_u32 s41, s41, 0
ds_read_b64_tr_b16 v[66:67], v10 offset:20736
s_waitcnt vmcnt(18) lgkmcnt(0)
s_barrier
s_add_u32 m0, s48, 4096
ds_read_b64_tr_b16 a[32:33], v9 offset:36864
ds_read_b64_tr_b16 a[34:35], v9 offset:39168
global_load_lds_dwordx4 v0, s[40:41] offset:-4096
ds_read_b64_tr_b16 a[36:37], v9 offset:36872
ds_read_b64_tr_b16 a[38:39], v9 offset:39176
global_load_lds_dwordx4 v1, s[40:41] offset:-3072
ds_read_b64_tr_b16 a[40:41], v9 offset:36928
global_load_lds_dwordx4 v2, s[40:41] offset:-2048
ds_read_b64_tr_b16 a[42:43], v9 offset:39232
ds_read_b64_tr_b16 a[44:45], v9 offset:36936
global_load_lds_dwordx4 v3, s[40:41] offset:-1024
ds_read_b64_tr_b16 a[46:47], v9 offset:39240
global_load_lds_dwordx4 v4, s[40:41] offset:0
ds_read_b64_tr_b16 v[68:69], v10 offset:36864
ds_read_b64_tr_b16 v[70:71], v10 offset:39168
global_load_lds_dwordx4 v5, s[40:41] offset:1024
ds_read_b64_tr_b16 a[48:49], v9 offset:55296
global_load_lds_dwordx4 v6, s[40:41] offset:2048
ds_read_b64_tr_b16 a[50:51], v9 offset:57600
ds_read_b64_tr_b16 a[52:53], v9 offset:55304
global_load_lds_dwordx4 v7, s[40:41] offset:3072
ds_read_b64_tr_b16 a[54:55], v9 offset:57608
s_add_u32 m0, s48, 12288
ds_read_b64_tr_b16 a[56:57], v9 offset:55360
ds_read_b64_tr_b16 a[58:59], v9 offset:57664
global_load_lds_dwordx4 v8, s[40:41] offset:-4096
ds_read_b64_tr_b16 a[60:61], v9 offset:55368
s_add_u32 s40, s40, 73728
ds_read_b64_tr_b16 a[62:63], v9 offset:57672
ds_read_b64_tr_b16 v[72:73], v10 offset:55296
s_addc_u32 s41, s41, 0
ds_read_b64_tr_b16 v[74:75], v10 offset:57600
s_waitcnt vmcnt(18) lgkmcnt(0)
s_barrier
s_add_u32 m0, s48, 40960
ds_read_b64_tr_b16 a[64:65], v204 offset:8192
ds_read_b64_tr_b16 a[66:67], v204 offset:10496
global_load_lds_dwordx4 v0, s[40:41] offset:-4096
ds_read_b64_tr_b16 a[68:69], v204 offset:8200
ds_read_b64_tr_b16 a[70:71], v204 offset:10504
global_load_lds_dwordx4 v1, s[40:41] offset:-3072
ds_read_b64_tr_b16 a[72:73], v204 offset:8256
global_load_lds_dwordx4 v2, s[40:41] offset:-2048
ds_read_b64_tr_b16 a[74:75], v204 offset:10560
ds_read_b64_tr_b16 a[76:77], v204 offset:8264
global_load_lds_dwordx4 v3, s[40:41] offset:-1024
ds_read_b64_tr_b16 a[78:79], v204 offset:10568
global_load_lds_dwordx4 v4, s[40:41] offset:0
ds_read_b64_tr_b16 v[76:77], v206 offset:8192
ds_read_b64_tr_b16 v[78:79], v206 offset:10496
global_load_lds_dwordx4 v5, s[40:41] offset:1024
ds_read_b64_tr_b16 a[80:81], v204 offset:26624
global_load_lds_dwordx4 v6, s[40:41] offset:2048
ds_read_b64_tr_b16 a[82:83], v204 offset:28928
ds_read_b64_tr_b16 a[84:85], v204 offset:26632
global_load_lds_dwordx4 v7, s[40:41] offset:3072
ds_read_b64_tr_b16 a[86:87], v204 offset:28936
s_add_u32 m0, s48, 49152
ds_read_b64_tr_b16 a[88:89], v204 offset:26688
ds_read_b64_tr_b16 a[90:91], v204 offset:28992
global_load_lds_dwordx4 v8, s[40:41] offset:-4096
ds_read_b64_tr_b16 a[92:93], v204 offset:26696
s_add_u32 s40, s40, 73728
ds_read_b64_tr_b16 a[94:95], v204 offset:29000
ds_read_b64_tr_b16 v[80:81], v206 offset:26624
s_addc_u32 s41, s41, 0
ds_read_b64_tr_b16 v[82:83], v206 offset:28928
s_waitcnt vmcnt(18) lgkmcnt(0)
s_barrier
s_add_u32 m0, s48, 77824
ds_read_b64_tr_b16 a[96:97], v204 offset:45056
ds_read_b64_tr_b16 a[98:99], v204 offset:47360
global_load_lds_dwordx4 v0, s[40:41] offset:-4096
ds_read_b64_tr_b16 a[100:101], v204 offset:45064
ds_read_b64_tr_b16 a[102:103], v204 offset:47368
global_load_lds_dwordx4 v1, s[40:41] offset:-3072
ds_read_b64_tr_b16 a[104:105], v204 offset:45120
global_load_lds_dwordx4 v2, s[40:41] offset:-2048
ds_read_b64_tr_b16 a[106:107], v204 offset:47424
ds_read_b64_tr_b16 a[108:109], v204 offset:45128
global_load_lds_dwordx4 v3, s[40:41] offset:-1024
ds_read_b64_tr_b16 a[110:111], v204 offset:47432
global_load_lds_dwordx4 v4, s[40:41] offset:0
ds_read_b64_tr_b16 v[84:85], v206 offset:45056
ds_read_b64_tr_b16 v[86:87], v206 offset:47360
global_load_lds_dwordx4 v5, s[40:41] offset:1024
ds_read_b64_tr_b16 a[112:113], v204 offset:63488
global_load_lds_dwordx4 v6, s[40:41] offset:2048
ds_read_b64_tr_b16 a[114:115], v205 offset:256
ds_read_b64_tr_b16 a[116:117], v204 offset:63496
global_load_lds_dwordx4 v7, s[40:41] offset:3072
ds_read_b64_tr_b16 a[118:119], v205 offset:264
)"
R"(s_add_u32 m0, s48, 86016
ds_read_b64_tr_b16 a[120:121], v204 offset:63552
ds_read_b64_tr_b16 a[122:123], v205 offset:320
global_load_lds_dwordx4 v8, s[40:41] offset:-4096
ds_read_b64_tr_b16 a[124:125], v204 offset:63560
s_add_u32 s40, s40, 73728
ds_read_b64_tr_b16 a[126:127], v205 offset:328
ds_read_b64_tr_b16 v[88:89], v206 offset:63488
s_addc_u32 s41, s41, 0
ds_read_b64_tr_b16 v[90:91], v207 offset:256
s_waitcnt vmcnt(18) lgkmcnt(0)
s_barrier
s_add_u32 m0, s48, 114688
ds_read_b64_tr_b16 a[128:129], v9 offset:0
ds_read_b64_tr_b16 a[130:131], v9 offset:2304
global_load_lds_dwordx4 v0, s[40:41] offset:-4096
ds_read_b64_tr_b16 a[132:133], v9 offset:8
global_load_lds_dwordx4 v1, s[40:41] offset:-3072
ds_read_b64_tr_b16 a[134:135], v9 offset:2312
ds_read_b64_tr_b16 a[136:137], v9 offset:64
global_load_lds_dwordx4 v2, s[40:41] offset:-2048
ds_read_b64_tr_b16 a[138:139], v9 offset:2368
global_load_lds_dwordx4 v3, s[40:41] offset:-1024
ds_read_b64_tr_b16 a[140:141], v9 offset:72
global_load_lds_dwordx4 v4, s[40:41] offset:0
ds_read_b64_tr_b16 a[142:143], v9 offset:2376
ds_read_b64_tr_b16 v[92:93], v10 offset:0
global_load_lds_dwordx4 v5, s[40:41] offset:1024
ds_read_b64_tr_b16 v[94:95], v10 offset:2304
global_load_lds_dwordx4 v6, s[40:41] offset:2048
ds_read_b64_tr_b16 a[144:145], v9 offset:18432
ds_read_b64_tr_b16 a[146:147], v9 offset:20736
global_load_lds_dwordx4 v7, s[40:41] offset:3072
ds_read_b64_tr_b16 a[148:149], v9 offset:18440
s_add_u32 m0, s48, 122880
ds_read_b64_tr_b16 a[150:151], v9 offset:20744
global_load_lds_dwordx4 v8, s[40:41] offset:-4096
ds_read_b64_tr_b16 a[152:153], v9 offset:18496
ds_read_b64_tr_b16 a[154:155], v9 offset:20800
s_add_u32 s40, s40, 73728
ds_read_b64_tr_b16 a[156:157], v9 offset:18504
s_addc_u32 s41, s41, 0
ds_read_b64_tr_b16 a[158:159], v9 offset:20808
s_waitcnt vmcnt(18) lgkmcnt(0)
s_barrier
s_add_u32 m0, s48, 4096
ds_read_b64_tr_b16 a[160:161], v9 offset:36864
ds_read_b64_tr_b16 a[162:163], v9 offset:39168
global_load_lds_dwordx4 v0, s[40:41] offset:-4096
ds_read_b64_tr_b16 a[164:165], v9 offset:36872
global_load_lds_dwordx4 v1, s[40:41] offset:-3072
ds_read_b64_tr_b16 a[166:167], v9 offset:39176
global_load_lds_dwordx4 v2, s[40:41] offset:-2048
ds_read_b64_tr_b16 a[168:169], v9 offset:36928
global_load_lds_dwordx4 v3, s[40:41] offset:-1024
ds_read_b64_tr_b16 a[170:171], v9 offset:39232
ds_read_b64_tr_b16 a[172:173], v9 offset:36936
global_load_lds_dwordx4 v4, s[40:41] offset:0
ds_read_b64_tr_b16 a[174:175], v9 offset:39240
global_load_lds_dwordx4 v5, s[40:41] offset:1024
ds_read_b64_tr_b16 a[176:177], v9 offset:55296
global_load_lds_dwordx4 v6, s[40:41] offset:2048
ds_read_b64_tr_b16 a[178:179], v9 offset:57600
global_load_lds_dwordx4 v7, s[40:41] offset:3072
ds_read_b64_tr_b16 a[180:181], v9 offset:55304
ds_read_b64_tr_b16 a[182:183], v9 offset:57608
s_add_u32 m0, s48, 12288
ds_read_b64_tr_b16 a[184:185], v9 offset:55360
global_load_lds_dwordx4 v8, s[40:41] offset:-4096
ds_read_b64_tr_b16 a[186:187], v9 offset:57664
s_sub_u32 s40, s40, 589824
ds_read_b64_tr_b16 a[188:189], v9 offset:55368
s_subb_u32 s41, s41, 0
ds_read_b64_tr_b16 a[190:191], v9 offset:57672
s_waitcnt vmcnt(18) lgkmcnt(0)
s_barrier
ds_read_b64_tr_b16 a[192:193], v204 offset:8192
ds_read_b64_tr_b16 a[194:195], v204 offset:10496
ds_read_b64_tr_b16 a[196:197], v204 offset:8200
ds_read_b64_tr_b16 a[198:199], v204 offset:10504
ds_read_b64_tr_b16 a[200:201], v204 offset:8256
ds_read_b64_tr_b16 a[202:203], v204 offset:10560
ds_read_b64_tr_b16 a[204:205], v204 offset:8264
ds_read_b64_tr_b16 a[206:207], v204 offset:10568
ds_read_b64_tr_b16 a[208:209], v204 offset:26624
ds_read_b64_tr_b16 a[210:211], v204 offset:28928
ds_read_b64_tr_b16 a[212:213], v204 offset:26632
ds_read_b64_tr_b16 a[214:215], v204 offset:28936
ds_read_b64_tr_b16 a[216:217], v204 offset:26688
ds_read_b64_tr_b16 a[218:219], v204 offset:28992
ds_read_b64_tr_b16 a[220:221], v204 offset:26696
ds_read_b64_tr_b16 a[222:223], v204 offset:29000
s_waitcnt vmcnt(9) lgkmcnt(0)
s_barrier
ds_read_b64_tr_b16 a[224:225], v204 offset:45056
ds_read_b64_tr_b16 a[226:227], v204 offset:47360
ds_read_b64_tr_b16 a[228:229], v204 offset:45064
ds_read_b64_tr_b16 a[230:231], v204 offset:47368
ds_read_b64_tr_b16 a[232:233], v204 offset:45120
ds_read_b64_tr_b16 a[234:235], v204 offset:47424
ds_read_b64_tr_b16 a[236:237], v204 offset:45128
ds_read_b64_tr_b16 a[238:239], v204 offset:47432
ds_read_b64_tr_b16 a[240:241], v204 offset:63488
ds_read_b64_tr_b16 a[242:243], v205 offset:256
ds_read_b64_tr_b16 a[244:245], v204 offset:63496
ds_read_b64_tr_b16 a[246:247], v205 offset:264
ds_read_b64_tr_b16 a[248:249], v204 offset:63552
ds_read_b64_tr_b16 a[250:251], v205 offset:320
ds_read_b64_tr_b16 a[252:253], v204 offset:63560
ds_read_b64_tr_b16 a[254:255], v205 offset:328
s_waitcnt vmcnt(0) lgkmcnt(0)
s_barrier
ds_read_b64_tr_b16 v[28:29], v9 offset:0
ds_read_b64_tr_b16 v[30:31], v9 offset:2304
ds_read_b64_tr_b16 v[32:33], v9 offset:8
ds_read_b64_tr_b16 v[34:35], v9 offset:2312
ds_read_b64_tr_b16 v[36:37], v9 offset:64
ds_read_b64_tr_b16 v[38:39], v9 offset:2368
ds_read_b64_tr_b16 v[40:41], v9 offset:72
ds_read_b64_tr_b16 v[42:43], v9 offset:2376
ds_read_b64_tr_b16 v[44:45], v9 offset:18432
ds_read_b64_tr_b16 v[46:47], v9 offset:20736
ds_read_b64_tr_b16 v[48:49], v9 offset:18440
ds_read_b64_tr_b16 v[50:51], v9 offset:20744
ds_read_b64_tr_b16 v[52:53], v9 offset:18496
ds_read_b64_tr_b16 v[54:55], v9 offset:20800
ds_read_b64_tr_b16 v[56:57], v9 offset:18504
ds_read_b64_tr_b16 v[58:59], v9 offset:20808
s_waitcnt lgkmcnt(0)
s_barrier
v_add_lshl_u32 v252, v24, 0, 7
v_bfi_b32 v252, s69, v252, v23
v_add_lshl_u32 v253, v22, 0, 7
v_bfi_b32 v253, s69, v253, v21
s_mov_b64 s[76:77], 0
s_add_u32 m0, s49, 18432
s_mov_b32 s70, 33
s_waitcnt vmcnt(0) lgkmcnt(0)
s_nop 7
.p2align 6
.Lwo_top5:
s_waitcnt lgkmcnt(2)
v_mfma_f32_16x16x32_bf16 v[96:99], a[0:3], v[132:135], v[96:99]
ds_read_b128 v[144:147], v25 offset:3072
v_mfma_f32_16x16x32_bf16 v[100:103], a[4:7], v[132:135], v[100:103]
v_mfma_f32_16x16x32_bf16 v[104:107], a[8:11], v[132:135], v[104:107]
s_waitcnt vmcnt(7)
s_barrier
v_mfma_f32_16x16x32_bf16 v[108:111], a[12:15], v[132:135], v[108:111]
v_cvt_pk_bf16_f32 v164, v112, v113
v_cvt_pk_bf16_f32 v165, v114, v115
s_mov_b64 exec, 1
global_store_dword v252, v24, s[38:39] sc1
s_mov_b64 exec, -1
v_mfma_f32_16x16x32_bf16 v[128:131], v[60:63], v[132:135], 0
s_waitcnt lgkmcnt(2)
v_cvt_pk_bf16_f32 v166, v116, v117
v_cvt_pk_bf16_f32 v167, v118, v119
v_add_u32 v24, 1, v24
v_add_lshl_u32 v252, v24, 0, 7
v_mfma_f32_16x16x32_bf16 v[96:99], a[16:19], v[136:139], v[96:99]
ds_read_b128 v[132:135], v25 offset:4096
v_bfi_b32 v252, s69, v252, v23
v_mfma_f32_16x16x32_bf16 v[100:103], a[20:23], v[136:139], v[100:103]
global_store_dwordx4 v13, v[164:167], s[38:39] offset:0 sc1
v_mfma_f32_16x16x32_bf16 v[104:107], a[24:27], v[136:139], v[104:107]
v_cvt_pk_bf16_f32 v168, v120, v121
v_cvt_pk_bf16_f32 v169, v122, v123
v_mfma_f32_16x16x32_bf16 v[108:111], a[28:31], v[136:139], v[108:111]
v_cvt_pk_bf16_f32 v170, v124, v125
v_cvt_pk_bf16_f32 v171, v126, v127
v_mfma_f32_16x16x32_bf16 v[128:131], v[64:67], v[136:139], v[128:131]
s_waitcnt lgkmcnt(2)
global_store_dwordx4 v13, v[168:171], s[38:39] offset:1024 sc1
v_mfma_f32_16x16x32_bf16 v[96:99], a[32:35], v[140:143], v[96:99]
s_cmp_eq_u32 s70, 32
ds_read_b128 v[136:139], v25 offset:5120
v_add_u32 v13, 18688, v13
v_mfma_f32_16x16x32_bf16 v[100:103], a[36:39], v[140:143], v[100:103]
s_cselect_b64 s[42:43], s[44:45], s[42:43]
v_add_f32 v254, v112, v113
v_mul_f32 v255, v112, v112
v_mfma_f32_16x16x32_bf16 v[104:107], a[40:43], v[140:143], v[104:107]
s_cmp_eq_u32 s70, 16
v_fmac_f32 v255, v113, v113
v_add_f32 v254, v254, v114
v_mfma_f32_16x16x32_bf16 v[108:111], a[44:47], v[140:143], v[108:111]
s_cselect_b64 s[42:43], s[46:47], s[42:43]
v_fmac_f32 v255, v114, v114
v_add_f32 v254, v254, v115
v_mfma_f32_16x16x32_bf16 v[128:131], v[68:71], v[140:143], v[128:131]
s_waitcnt lgkmcnt(2)
v_fmac_f32 v255, v115, v115
v_add_f32 v254, v254, v116
v_mfma_f32_16x16x32_bf16 v[96:99], a[48:51], v[144:147], v[96:99]
s_xor_b32 s49, s49, 0x10000
ds_read_b128 v[140:143], v25 offset:6144
v_fmac_f32 v255, v116, v116
v_mfma_f32_16x16x32_bf16 v[100:103], a[52:55], v[144:147], v[100:103]
s_mov_b32 m0, 47104
v_add_f32 v254, v254, v117
v_fmac_f32 v255, v117, v117
v_mfma_f32_16x16x32_bf16 v[104:107], a[56:59], v[144:147], v[104:107]
v_add_f32 v254, v254, v118
v_fmac_f32 v255, v118, v118
v_mfma_f32_16x16x32_bf16 v[108:111], a[60:63], v[144:147], v[108:111]
global_load_lds_dwordx4 v16, s[42:43] offset:0 sc1
v_add_f32 v254, v254, v119
v_mfma_f32_16x16x32_bf16 v[128:131], v[72:75], v[144:147], v[128:131]
s_waitcnt lgkmcnt(2)
v_fmac_f32 v255, v119, v119
v_add_f32 v254, v254, v120
v_mfma_f32_16x16x32_bf16 v[96:99], a[64:67], v[132:135], v[96:99]
ds_read_b128 v[144:147], v25 offset:7168
v_fmac_f32 v255, v120, v120
v_mfma_f32_16x16x32_bf16 v[100:103], a[68:71], v[132:135], v[100:103]
v_add_f32 v254, v254, v121
v_fmac_f32 v255, v121, v121
v_mfma_f32_16x16x32_bf16 v[104:107], a[72:75], v[132:135], v[104:107]
v_add_f32 v254, v254, v122
v_fmac_f32 v255, v122, v122
v_mfma_f32_16x16x32_bf16 v[108:111], a[76:79], v[132:135], v[108:111]
v_add_f32 v254, v254, v123
v_fmac_f32 v255, v123, v123
v_mfma_f32_16x16x32_bf16 v[128:131], v[76:79], v[132:135], v[128:131]
s_waitcnt vmcnt(9)
ds_read_b128 v[148:151], v19 offset:8192
ds_read_b128 v[152:155], v20 offset:8192
s_waitcnt lgkmcnt(4)
v_mfma_f32_16x16x32_bf16 v[96:99], a[80:83], v[136:139], v[96:99]
ds_read_b128 v[132:135], v25 offset:8192
global_load_lds_dwordx4 v17, s[42:43] offset:1024 sc1
v_mfma_f32_16x16x32_bf16 v[100:103], a[84:87], v[136:139], v[100:103]
v_add_f32 v254, v254, v124
v_fmac_f32 v255, v124, v124
v_mfma_f32_16x16x32_bf16 v[104:107], a[88:91], v[136:139], v[104:107]
v_add_f32 v254, v254, v125
v_fmac_f32 v255, v125, v125
v_mfma_f32_16x16x32_bf16 v[108:111], a[92:95], v[136:139], v[108:111]
v_add_f32 v254, v254, v126
v_fmac_f32 v255, v126, v126
v_mfma_f32_16x16x32_bf16 v[128:131], v[80:83], v[136:139], v[128:131]
s_waitcnt lgkmcnt(4)
v_add_f32 v254, v254, v127
v_fmac_f32 v255, v127, v127
v_mfma_f32_16x16x32_bf16 v[96:99], a[96:99], v[140:143], v[96:99]
s_add_u32 m0, s49, 0
ds_read_b128 v[136:139], v25 offset:9216
v_mfma_f32_16x16x32_bf16 v[100:103], a[100:103], v[140:143], v[100:103]
ds_write_b64 v27, v[254:255] offset:45056
v_mfma_f32_16x16x32_bf16 v[104:107], a[104:107], v[140:143], v[104:107]
s_waitcnt lgkmcnt(4)
v_lshlrev_b32 v112, 16, v148
v_and_b32 v113, 0xffff0000, v148
v_mfma_f32_16x16x32_bf16 v[108:111], a[108:111], v[140:143], v[108:111]
s_sub_u32 s42, s42, 18432
v_lshlrev_b32 v114, 16, v149
v_and_b32 v115, 0xffff0000, v149
v_mfma_f32_16x16x32_bf16 v[128:131], v[84:87], v[140:143], v[128:131]
v_lshlrev_b32 v116, 16, v150
v_and_b32 v117, 0xffff0000, v150
v_mfma_f32_16x16x32_bf16 v[96:99], a[112:115], v[144:147], v[96:99]
s_subb_u32 s43, s43, 0
ds_read_b128 v[140:143], v25 offset:10240
v_lshlrev_b32 v118, 16, v151
v_mfma_f32_16x16x32_bf16 v[100:103], a[116:119], v[144:147], v[100:103]
s_waitcnt lgkmcnt(4)
v_and_b32 v119, 0xffff0000, v151
v_lshlrev_b32 v120, 16, v152
v_mfma_f32_16x16x32_bf16 v[104:107], a[120:123], v[144:147], v[104:107]
v_and_b32 v121, 0xffff0000, v152
v_lshlrev_b32 v122, 16, v153
v_mfma_f32_16x16x32_bf16 v[108:111], a[124:127], v[144:147], v[108:111]
v_and_b32 v123, 0xffff0000, v153
v_lshlrev_b32 v124, 16, v154
v_mfma_f32_16x16x32_bf16 v[128:131], v[88:91], v[144:147], v[128:131]
s_waitcnt lgkmcnt(3)
v_and_b32 v125, 0xffff0000, v154
v_lshlrev_b32 v126, 16, v155
v_mfma_f32_16x16x32_bf16 v[96:99], a[128:131], v[132:135], v[96:99]
ds_read_b128 v[144:147], v25 offset:11264
v_and_b32 v127, 0xffff0000, v155
v_mfma_f32_16x16x32_bf16 v[100:103], a[132:135], v[132:135], v[100:103]
v_mfma_f32_16x16x32_bf16 v[104:107], a[136:139], v[132:135], v[104:107]
v_mfma_f32_16x16x32_bf16 v[108:111], a[140:143], v[132:135], v[108:111]
v_mfma_f32_16x16x32_bf16 v[128:131], v[92:95], v[132:135], v[128:131]
s_waitcnt lgkmcnt(3)
v_mfma_f32_16x16x32_bf16 v[96:99], a[144:147], v[136:139], v[96:99]
ds_read_b128 v[132:135], v25 offset:12288
v_mfma_f32_16x16x32_bf16 v[100:103], a[148:151], v[136:139], v[100:103]
v_mfma_f32_16x16x32_bf16 v[104:107], a[152:155], v[136:139], v[104:107]
v_mfma_f32_16x16x32_bf16 v[108:111], a[156:159], v[136:139], v[108:111]
s_waitcnt lgkmcnt(2)
v_mfma_f32_16x16x32_bf16 v[96:99], a[160:163], v[140:143], v[96:99]
ds_read_b128 v[136:139], v25 offset:13312
v_mfma_f32_16x16x32_bf16 v[100:103], a[164:167], v[140:143], v[100:103]
v_mfma_f32_16x16x32_bf16 v[104:107], a[168:171], v[140:143], v[104:107]
ds_write_b128 v26, v[128:131] offset:36864
v_mfma_f32_16x16x32_bf16 v[108:111], a[172:175], v[140:143], v[108:111]
s_waitcnt lgkmcnt(3)
v_mfma_f32_16x16x32_bf16 v[96:99], a[176:179], v[144:147], v[96:99]
ds_read_b128 v[140:143], v25 offset:14336
v_mfma_f32_16x16x32_bf16 v[100:103], a[180:183], v[144:147], v[100:103]
v_mfma_f32_16x16x32_bf16 v[104:107], a[184:187], v[144:147], v[104:107]
s_barrier
v_mfma_f32_16x16x32_bf16 v[108:111], a[188:191], v[144:147], v[108:111]
s_waitcnt lgkmcnt(3)
global_load_lds_dwordx4 v11, s[38:39] offset:-2048 sc1
v_mfma_f32_16x16x32_bf16 v[96:99], a[192:195], v[132:135], v[96:99]
ds_read_b128 v[144:147], v25 offset:15360
v_mfma_f32_16x16x32_bf16 v[100:103], a[196:199], v[132:135], v[100:103]
v_mfma_f32_16x16x32_bf16 v[104:107], a[200:203], v[132:135], v[104:107]
global_load_lds_dwordx4 v11, s[38:39] offset:-1024 sc1
v_mfma_f32_16x16x32_bf16 v[108:111], a[204:207], v[132:135], v[108:111]
s_waitcnt lgkmcnt(3)
v_mfma_f32_16x16x32_bf16 v[96:99], a[208:211], v[136:139], v[96:99]
ds_read_b128 v[132:135], v25 offset:16384
v_mfma_f32_16x16x32_bf16 v[100:103], a[212:215], v[136:139], v[100:103]
global_load_lds_dwordx4 v11, s[38:39] offset:0 sc1
v_mfma_f32_16x16x32_bf16 v[104:107], a[216:219], v[136:139], v[104:107]
v_mfma_f32_16x16x32_bf16 v[108:111], a[220:223], v[136:139], v[108:111]
s_waitcnt lgkmcnt(2)
v_mfma_f32_16x16x32_bf16 v[96:99], a[224:227], v[140:143], v[96:99]
ds_read_b128 v[136:139], v25 offset:17408
global_load_lds_dwordx4 v11, s[38:39] offset:1024 sc1
v_mfma_f32_16x16x32_bf16 v[100:103], a[228:231], v[140:143], v[100:103]
v_mfma_f32_16x16x32_bf16 v[104:107], a[232:235], v[140:143], v[104:107]
v_mfma_f32_16x16x32_bf16 v[108:111], a[236:239], v[140:143], v[108:111]
s_waitcnt lgkmcnt(2)
global_load_lds_dwordx4 v11, s[38:39] offset:2048 sc1
v_add_u32 v11, 18432, v11
v_mfma_f32_16x16x32_bf16 v[96:99], a[240:243], v[144:147], v[96:99]
v_min_u32 v11, v11, v12
v_mfma_f32_16x16x32_bf16 v[100:103], a[244:247], v[144:147], v[100:103]
v_mfma_f32_16x16x32_bf16 v[104:107], a[248:251], v[144:147], v[104:107]
s_waitcnt vmcnt(10)
s_barrier
v_mfma_f32_16x16x32_bf16 v[108:111], a[252:255], v[144:147], v[108:111]
s_waitcnt lgkmcnt(1)
ds_read_b128 v[140:143], v25 offset:18432
v_mfma_f32_16x16x32_bf16 v[96:99], v[28:31], v[132:135], v[96:99]
ds_read_b128 v[144:147], v25 offset:19456
v_mfma_f32_16x16x32_bf16 v[100:103], v[32:35], v[132:135], v[100:103]
v_mfma_f32_16x16x32_bf16 v[104:107], v[36:39], v[132:135], v[104:107]
v_mfma_f32_16x16x32_bf16 v[108:111], v[40:43], v[132:135], v[108:111]
s_waitcnt lgkmcnt(2)
v_mfma_f32_16x16x32_bf16 v[96:99], v[44:47], v[136:139], v[96:99]
ds_read_b128 v[132:135], v25 offset:20480
v_mfma_f32_16x16x32_bf16 v[100:103], v[48:51], v[136:139], v[100:103]
v_mfma_f32_16x16x32_bf16 v[104:107], v[52:55], v[136:139], v[104:107]
v_mfma_f32_16x16x32_bf16 v[108:111], v[56:59], v[136:139], v[108:111]
s_waitcnt lgkmcnt(2)
v_mfma_f32_16x16x32_bf16 v[112:115], a[0:3], v[140:143], v[112:115]
ds_read_b128 v[136:139], v25 offset:21504
v_mfma_f32_16x16x32_bf16 v[116:119], a[4:7], v[140:143], v[116:119]
v_mfma_f32_16x16x32_bf16 v[120:123], a[8:11], v[140:143], v[120:123]
s_waitcnt vmcnt(7)
s_barrier
v_mfma_f32_16x16x32_bf16 v[124:127], a[12:15], v[140:143], v[124:127]
s_mov_b64 exec, 1
global_store_dword v252, v24, s[38:39] sc1
s_mov_b64 exec, -1
v_add_u32 v24, 1, v24
v_add_lshl_u32 v252, v24, 0, 7
v_mfma_f32_16x16x32_bf16 v[128:131], v[60:63], v[140:143], 0
s_waitcnt lgkmcnt(2)
v_bfi_b32 v252, s69, v252, v23
v_cvt_pk_bf16_f32 v164, v96, v97
v_mfma_f32_16x16x32_bf16 v[112:115], a[16:19], v[144:147], v[112:115]
ds_read_b128 v[140:143], v25 offset:22528
v_cvt_pk_bf16_f32 v165, v98, v99
v_mfma_f32_16x16x32_bf16 v[116:119], a[20:23], v[144:147], v[116:119]
v_cvt_pk_bf16_f32 v166, v100, v101
v_cvt_pk_bf16_f32 v167, v102, v103
v_mfma_f32_16x16x32_bf16 v[120:123], a[24:27], v[144:147], v[120:123]
global_store_dwordx4 v13, v[164:167], s[38:39] offset:0 sc1
v_mfma_f32_16x16x32_bf16 v[124:127], a[28:31], v[144:147], v[124:127]
v_cvt_pk_bf16_f32 v168, v104, v105
v_cvt_pk_bf16_f32 v169, v106, v107
v_mfma_f32_16x16x32_bf16 v[128:131], v[64:67], v[144:147], v[128:131]
s_waitcnt lgkmcnt(2)
v_cvt_pk_bf16_f32 v170, v108, v109
v_cvt_pk_bf16_f32 v171, v110, v111
v_mfma_f32_16x16x32_bf16 v[112:115], a[32:35], v[132:135], v[112:115]
s_mov_b32 m0, 55296
ds_read_b128 v[144:147], v25 offset:23552
v_add_f32 v254, v96, v97
v_mfma_f32_16x16x32_bf16 v[116:119], a[36:39], v[132:135], v[116:119]
global_store_dwordx4 v13, v[168:171], s[38:39] offset:1024 sc1
v_mfma_f32_16x16x32_bf16 v[120:123], a[40:43], v[132:135], v[120:123]
v_add_u32 v13, 18688, v13
v_mul_f32 v255, v96, v96
v_mfma_f32_16x16x32_bf16 v[124:127], a[44:47], v[132:135], v[124:127]
v_fmac_f32 v255, v97, v97
v_add_f32 v254, v254, v98
v_mfma_f32_16x16x32_bf16 v[128:131], v[68:71], v[132:135], v[128:131]
s_waitcnt lgkmcnt(2)
v_fmac_f32 v255, v98, v98
v_add_f32 v254, v254, v99
v_mfma_f32_16x16x32_bf16 v[112:115], a[48:51], v[136:139], v[112:115]
)"
R"(ds_read_b128 v[132:135], v25 offset:24576
v_fmac_f32 v255, v99, v99
v_mfma_f32_16x16x32_bf16 v[116:119], a[52:55], v[136:139], v[116:119]
v_add_f32 v254, v254, v100
v_fmac_f32 v255, v100, v100
v_mfma_f32_16x16x32_bf16 v[120:123], a[56:59], v[136:139], v[120:123]
v_add_f32 v254, v254, v101
v_fmac_f32 v255, v101, v101
v_mfma_f32_16x16x32_bf16 v[124:127], a[60:63], v[136:139], v[124:127]
global_load_lds_dwordx4 v16, s[42:43] offset:0 sc1
v_add_f32 v254, v254, v102
v_mfma_f32_16x16x32_bf16 v[128:131], v[72:75], v[136:139], v[128:131]
s_waitcnt lgkmcnt(2)
v_fmac_f32 v255, v102, v102
v_add_f32 v254, v254, v103
v_mfma_f32_16x16x32_bf16 v[112:115], a[64:67], v[140:143], v[112:115]
ds_read_b128 v[136:139], v25 offset:25600
v_fmac_f32 v255, v103, v103
v_mfma_f32_16x16x32_bf16 v[116:119], a[68:71], v[140:143], v[116:119]
v_add_f32 v254, v254, v104
v_fmac_f32 v255, v104, v104
v_mfma_f32_16x16x32_bf16 v[120:123], a[72:75], v[140:143], v[120:123]
v_add_f32 v254, v254, v105
v_fmac_f32 v255, v105, v105
v_mfma_f32_16x16x32_bf16 v[124:127], a[76:79], v[140:143], v[124:127]
v_add_f32 v254, v254, v106
v_fmac_f32 v255, v106, v106
v_mfma_f32_16x16x32_bf16 v[128:131], v[76:79], v[140:143], v[128:131]
s_waitcnt vmcnt(9)
ds_read_b128 v[148:151], v19 offset:0
ds_read_b128 v[152:155], v20 offset:0
s_waitcnt lgkmcnt(4)
v_mfma_f32_16x16x32_bf16 v[112:115], a[80:83], v[144:147], v[112:115]
ds_read_b128 v[140:143], v25 offset:26624
global_load_lds_dwordx4 v17, s[42:43] offset:1024 sc1
v_mfma_f32_16x16x32_bf16 v[116:119], a[84:87], v[144:147], v[116:119]
v_add_f32 v254, v254, v107
v_fmac_f32 v255, v107, v107
v_mfma_f32_16x16x32_bf16 v[120:123], a[88:91], v[144:147], v[120:123]
v_add_f32 v254, v254, v108
v_fmac_f32 v255, v108, v108
v_mfma_f32_16x16x32_bf16 v[124:127], a[92:95], v[144:147], v[124:127]
v_add_f32 v254, v254, v109
v_fmac_f32 v255, v109, v109
v_mfma_f32_16x16x32_bf16 v[128:131], v[80:83], v[144:147], v[128:131]
s_waitcnt lgkmcnt(4)
v_add_f32 v254, v254, v110
v_fmac_f32 v255, v110, v110
v_mfma_f32_16x16x32_bf16 v[112:115], a[96:99], v[132:135], v[112:115]
s_add_u32 m0, s49, 18432
ds_read_b128 v[144:147], v25 offset:27648
v_add_f32 v254, v254, v111
v_mfma_f32_16x16x32_bf16 v[116:119], a[100:103], v[132:135], v[116:119]
v_fmac_f32 v255, v111, v111
v_mfma_f32_16x16x32_bf16 v[120:123], a[104:107], v[132:135], v[120:123]
ds_write_b64 v27, v[254:255] offset:43008
v_mfma_f32_16x16x32_bf16 v[124:127], a[108:111], v[132:135], v[124:127]
s_sub_u32 s42, s42, 18432
s_waitcnt lgkmcnt(4)
v_lshlrev_b32 v96, 16, v148
v_and_b32 v97, 0xffff0000, v148
v_mfma_f32_16x16x32_bf16 v[128:131], v[84:87], v[132:135], v[128:131]
v_lshlrev_b32 v98, 16, v149
v_and_b32 v99, 0xffff0000, v149
v_mfma_f32_16x16x32_bf16 v[112:115], a[112:115], v[136:139], v[112:115]
s_subb_u32 s43, s43, 0
ds_read_b128 v[132:135], v25 offset:28672
v_lshlrev_b32 v100, 16, v150
v_mfma_f32_16x16x32_bf16 v[116:119], a[116:119], v[136:139], v[116:119]
v_and_b32 v101, 0xffff0000, v150
v_lshlrev_b32 v102, 16, v151
v_mfma_f32_16x16x32_bf16 v[120:123], a[120:123], v[136:139], v[120:123]
s_waitcnt lgkmcnt(4)
v_and_b32 v103, 0xffff0000, v151
v_lshlrev_b32 v104, 16, v152
v_mfma_f32_16x16x32_bf16 v[124:127], a[124:127], v[136:139], v[124:127]
v_and_b32 v105, 0xffff0000, v152
v_lshlrev_b32 v106, 16, v153
v_mfma_f32_16x16x32_bf16 v[128:131], v[88:91], v[136:139], v[128:131]
s_waitcnt lgkmcnt(3)
v_and_b32 v107, 0xffff0000, v153
v_lshlrev_b32 v108, 16, v154
v_mfma_f32_16x16x32_bf16 v[112:115], a[128:131], v[140:143], v[112:115]
ds_read_b128 v[136:139], v25 offset:29696
v_and_b32 v109, 0xffff0000, v154
v_mfma_f32_16x16x32_bf16 v[116:119], a[132:135], v[140:143], v[116:119]
v_lshlrev_b32 v110, 16, v155
v_and_b32 v111, 0xffff0000, v155
v_mfma_f32_16x16x32_bf16 v[120:123], a[136:139], v[140:143], v[120:123]
v_mfma_f32_16x16x32_bf16 v[124:127], a[140:143], v[140:143], v[124:127]
v_mfma_f32_16x16x32_bf16 v[128:131], v[92:95], v[140:143], v[128:131]
s_waitcnt lgkmcnt(3)
v_mfma_f32_16x16x32_bf16 v[112:115], a[144:147], v[144:147], v[112:115]
ds_read_b128 v[140:143], v25 offset:30720
v_mfma_f32_16x16x32_bf16 v[116:119], a[148:151], v[144:147], v[116:119]
v_mfma_f32_16x16x32_bf16 v[120:123], a[152:155], v[144:147], v[120:123]
v_mfma_f32_16x16x32_bf16 v[124:127], a[156:159], v[144:147], v[124:127]
s_waitcnt lgkmcnt(2)
v_mfma_f32_16x16x32_bf16 v[112:115], a[160:163], v[132:135], v[112:115]
ds_read_b128 v[144:147], v25 offset:31744
v_mfma_f32_16x16x32_bf16 v[116:119], a[164:167], v[132:135], v[116:119]
v_mfma_f32_16x16x32_bf16 v[120:123], a[168:171], v[132:135], v[120:123]
ds_write_b128 v26, v[128:131] offset:39936
v_mfma_f32_16x16x32_bf16 v[124:127], a[172:175], v[132:135], v[124:127]
s_waitcnt lgkmcnt(3)
v_mfma_f32_16x16x32_bf16 v[112:115], a[176:179], v[136:139], v[112:115]
ds_read_b128 v[132:135], v25 offset:32768
v_mfma_f32_16x16x32_bf16 v[116:119], a[180:183], v[136:139], v[116:119]
v_mfma_f32_16x16x32_bf16 v[120:123], a[184:187], v[136:139], v[120:123]
s_barrier
v_mfma_f32_16x16x32_bf16 v[124:127], a[188:191], v[136:139], v[124:127]
s_waitcnt lgkmcnt(3)
global_load_lds_dwordx4 v11, s[38:39] offset:-2048 sc1
v_mfma_f32_16x16x32_bf16 v[112:115], a[192:195], v[140:143], v[112:115]
s_mov_b64 s[76:77], -1
ds_read_b128 v[136:139], v25 offset:33792
v_mfma_f32_16x16x32_bf16 v[116:119], a[196:199], v[140:143], v[116:119]
v_mfma_f32_16x16x32_bf16 v[120:123], a[200:203], v[140:143], v[120:123]
global_load_lds_dwordx4 v11, s[38:39] offset:-1024 sc1
v_mfma_f32_16x16x32_bf16 v[124:127], a[204:207], v[140:143], v[124:127]
s_waitcnt lgkmcnt(3)
v_mfma_f32_16x16x32_bf16 v[112:115], a[208:211], v[144:147], v[112:115]
ds_read_b128 v[140:143], v25 offset:34816
v_mfma_f32_16x16x32_bf16 v[116:119], a[212:215], v[144:147], v[116:119]
global_load_lds_dwordx4 v11, s[38:39] offset:0 sc1
v_mfma_f32_16x16x32_bf16 v[120:123], a[216:219], v[144:147], v[120:123]
v_mfma_f32_16x16x32_bf16 v[124:127], a[220:223], v[144:147], v[124:127]
s_waitcnt lgkmcnt(2)
v_mfma_f32_16x16x32_bf16 v[112:115], a[224:227], v[132:135], v[112:115]
ds_read_b128 v[144:147], v25 offset:35840
global_load_lds_dwordx4 v11, s[38:39] offset:1024 sc1
v_mfma_f32_16x16x32_bf16 v[116:119], a[228:231], v[132:135], v[116:119]
v_mfma_f32_16x16x32_bf16 v[120:123], a[232:235], v[132:135], v[120:123]
v_mfma_f32_16x16x32_bf16 v[124:127], a[236:239], v[132:135], v[124:127]
s_waitcnt lgkmcnt(2)
v_xor_b32 v25, 0x10000, v25
global_load_lds_dwordx4 v11, s[38:39] offset:2048 sc1
v_mfma_f32_16x16x32_bf16 v[112:115], a[240:243], v[136:139], v[112:115]
v_add_u32 v11, 18432, v11
v_min_u32 v11, v11, v12
v_mfma_f32_16x16x32_bf16 v[116:119], a[244:247], v[136:139], v[116:119]
v_mfma_f32_16x16x32_bf16 v[120:123], a[248:251], v[136:139], v[120:123]
s_waitcnt vmcnt(10)
s_barrier
v_mfma_f32_16x16x32_bf16 v[124:127], a[252:255], v[136:139], v[124:127]
s_waitcnt lgkmcnt(1)
ds_read_b128 v[132:135], v25 offset:0
v_mfma_f32_16x16x32_bf16 v[112:115], v[28:31], v[140:143], v[112:115]
ds_read_b128 v[136:139], v25 offset:1024
v_mfma_f32_16x16x32_bf16 v[116:119], v[32:35], v[140:143], v[116:119]
v_mfma_f32_16x16x32_bf16 v[120:123], v[36:39], v[140:143], v[120:123]
v_mfma_f32_16x16x32_bf16 v[124:127], v[40:43], v[140:143], v[124:127]
s_waitcnt lgkmcnt(2)
v_mfma_f32_16x16x32_bf16 v[112:115], v[44:47], v[144:147], v[112:115]
ds_read_b128 v[140:143], v25 offset:2048
v_mfma_f32_16x16x32_bf16 v[116:119], v[48:51], v[144:147], v[116:119]
v_mfma_f32_16x16x32_bf16 v[120:123], v[52:55], v[144:147], v[120:123]
v_mfma_f32_16x16x32_bf16 v[124:127], v[56:59], v[144:147], v[124:127]
s_sub_u32 s70, s70, 1
s_cbranch_scc0 .Lwo_top5
s_waitcnt vmcnt(7)
s_barrier
s_mov_b64 exec, 1
global_store_dword v252, v24, s[38:39] sc1
s_mov_b64 exec, -1
v_add_u32 v24, 1, v24
v_add_lshl_u32 v252, v24, 0, 7
v_bfi_b32 v252, s69, v252, v23
v_cvt_pk_bf16_f32 v164, v112, v113
v_cvt_pk_bf16_f32 v165, v114, v115
v_cvt_pk_bf16_f32 v166, v116, v117
v_cvt_pk_bf16_f32 v167, v118, v119
global_store_dwordx4 v13, v[164:167], s[38:39] offset:0 sc1
v_cvt_pk_bf16_f32 v168, v120, v121
v_cvt_pk_bf16_f32 v169, v122, v123
v_cvt_pk_bf16_f32 v170, v124, v125
v_cvt_pk_bf16_f32 v171, v126, v127
global_store_dwordx4 v13, v[168:171], s[38:39] offset:1024 sc1
v_add_f32 v254, v112, v113
v_mul_f32 v255, v112, v112
v_fmac_f32 v255, v113, v113
v_add_f32 v254, v254, v114
v_fmac_f32 v255, v114, v114
v_add_f32 v254, v254, v115
v_fmac_f32 v255, v115, v115
v_add_f32 v254, v254, v116
v_fmac_f32 v255, v116, v116
v_add_f32 v254, v254, v117
v_fmac_f32 v255, v117, v117
v_add_f32 v254, v254, v118
v_fmac_f32 v255, v118, v118
v_add_f32 v254, v254, v119
v_fmac_f32 v255, v119, v119
v_add_f32 v254, v254, v120
v_fmac_f32 v255, v120, v120
v_add_f32 v254, v254, v121
v_fmac_f32 v255, v121, v121
v_add_f32 v254, v254, v122
v_fmac_f32 v255, v122, v122
v_add_f32 v254, v254, v123
v_fmac_f32 v255, v123, v123
v_add_f32 v254, v254, v124
v_fmac_f32 v255, v124, v124
v_add_f32 v254, v254, v125
v_fmac_f32 v255, v125, v125
v_add_f32 v254, v254, v126
v_fmac_f32 v255, v126, v126
v_add_f32 v254, v254, v127
v_fmac_f32 v255, v127, v127
ds_write_b64 v27, v[254:255] offset:45056
s_waitcnt vmcnt(0) lgkmcnt(0)
s_barrier
s_mov_b64 exec, 1
global_store_dword v252, v24, s[38:39] sc1
s_mov_b64 exec, -1
s_waitcnt vmcnt(0)
s_barrier
v_add_u32 v24, 8, v24
v_mbcnt_lo_u32_b32 v252, -1, 0
v_mbcnt_hi_u32_b32 v252, -1, v252
v_add_lshl_u32 v252, v252, 0, 7
v_bfi_b32 v252, s69, v252, v23
s_mov_b64 exec, 0xff
global_store_dword v252, v24, s[38:39] sc1
s_mov_b64 exec, -1
s_endpgm
.Lwo_w2:
s_add_u32 m0, s48, 4096
s_nop 0
global_load_lds_dwordx4 v0, s[40:41] offset:-4096
global_load_lds_dwordx4 v1, s[40:41] offset:-3072
global_load_lds_dwordx4 v2, s[40:41] offset:-2048
global_load_lds_dwordx4 v3, s[40:41] offset:-1024
global_load_lds_dwordx4 v4, s[40:41] offset:0
global_load_lds_dwordx4 v5, s[40:41] offset:1024
global_load_lds_dwordx4 v6, s[40:41] offset:2048
global_load_lds_dwordx4 v7, s[40:41] offset:3072
s_add_u32 m0, s48, 12288
s_nop 0
global_load_lds_dwordx4 v8, s[40:41] offset:-4096
s_add_u32 s40, s40, 73728
s_addc_u32 s41, s41, 0
s_add_u32 m0, s48, 40960
s_nop 0
global_load_lds_dwordx4 v0, s[40:41] offset:-4096
global_load_lds_dwordx4 v1, s[40:41] offset:-3072
global_load_lds_dwordx4 v2, s[40:41] offset:-2048
global_load_lds_dwordx4 v3, s[40:41] offset:-1024
global_load_lds_dwordx4 v4, s[40:41] offset:0
global_load_lds_dwordx4 v5, s[40:41] offset:1024
global_load_lds_dwordx4 v6, s[40:41] offset:2048
global_load_lds_dwordx4 v7, s[40:41] offset:3072
s_add_u32 m0, s48, 49152
s_nop 0
global_load_lds_dwordx4 v8, s[40:41] offset:-4096
s_add_u32 s40, s40, 73728
s_addc_u32 s41, s41, 0
s_add_u32 m0, s48, 77824
s_nop 0
global_load_lds_dwordx4 v0, s[40:41] offset:-4096
global_load_lds_dwordx4 v1, s[40:41] offset:-3072
global_load_lds_dwordx4 v2, s[40:41] offset:-2048
global_load_lds_dwordx4 v3, s[40:41] offset:-1024
global_load_lds_dwordx4 v4, s[40:41] offset:0
global_load_lds_dwordx4 v5, s[40:41] offset:1024
global_load_lds_dwordx4 v6, s[40:41] offset:2048
global_load_lds_dwordx4 v7, s[40:41] offset:3072
s_add_u32 m0, s48, 86016
s_nop 0
global_load_lds_dwordx4 v8, s[40:41] offset:-4096
s_add_u32 s40, s40, 73728
s_addc_u32 s41, s41, 0
v_add_u32 v204, 0x10000, v9
v_add_u32 v205, 0x20000, v9
v_add_u32 v206, 0x10000, v10
v_add_u32 v207, 0x20000, v10
s_waitcnt lgkmcnt(0)
s_mul_i32 s37, s37, 0x9E3779B1
s_add_u32 s37, s37, 0x7F4A7C15
v_add_u32 v22, s37, v22
v_add_u32 v24, s37, v24
s_waitcnt vmcnt(18)
s_barrier
s_add_u32 m0, s48, 114688
ds_read_b64_tr_b16 a[0:1], v9 offset:0
ds_read_b64_tr_b16 a[2:3], v9 offset:2304
global_load_lds_dwordx4 v0, s[40:41] offset:-4096
ds_read_b64_tr_b16 a[4:5], v9 offset:8
global_load_lds_dwordx4 v1, s[40:41] offset:-3072
ds_read_b64_tr_b16 a[6:7], v9 offset:2312
global_load_lds_dwordx4 v2, s[40:41] offset:-2048
ds_read_b64_tr_b16 a[8:9], v9 offset:64
global_load_lds_dwordx4 v3, s[40:41] offset:-1024
ds_read_b64_tr_b16 a[10:11], v9 offset:2368
ds_read_b64_tr_b16 a[12:13], v9 offset:72
global_load_lds_dwordx4 v4, s[40:41] offset:0
ds_read_b64_tr_b16 a[14:15], v9 offset:2376
global_load_lds_dwordx4 v5, s[40:41] offset:1024
ds_read_b64_tr_b16 a[16:17], v9 offset:18432
global_load_lds_dwordx4 v6, s[40:41] offset:2048
ds_read_b64_tr_b16 a[18:19], v9 offset:20736
global_load_lds_dwordx4 v7, s[40:41] offset:3072
ds_read_b64_tr_b16 a[20:21], v9 offset:18440
ds_read_b64_tr_b16 a[22:23], v9 offset:20744
s_add_u32 m0, s48, 122880
ds_read_b64_tr_b16 a[24:25], v9 offset:18496
global_load_lds_dwordx4 v8, s[40:41] offset:-4096
ds_read_b64_tr_b16 a[26:27], v9 offset:20800
s_add_u32 s40, s40, 73728
ds_read_b64_tr_b16 a[28:29], v9 offset:18504
s_addc_u32 s41, s41, 0
ds_read_b64_tr_b16 a[30:31], v9 offset:20808
s_waitcnt vmcnt(18) lgkmcnt(0)
s_barrier
s_add_u32 m0, s48, 4096
ds_read_b64_tr_b16 a[32:33], v9 offset:36864
ds_read_b64_tr_b16 a[34:35], v9 offset:39168
global_load_lds_dwordx4 v0, s[40:41] offset:-4096
ds_read_b64_tr_b16 a[36:37], v9 offset:36872
global_load_lds_dwordx4 v1, s[40:41] offset:-3072
ds_read_b64_tr_b16 a[38:39], v9 offset:39176
global_load_lds_dwordx4 v2, s[40:41] offset:-2048
ds_read_b64_tr_b16 a[40:41], v9 offset:36928
global_load_lds_dwordx4 v3, s[40:41] offset:-1024
ds_read_b64_tr_b16 a[42:43], v9 offset:39232
ds_read_b64_tr_b16 a[44:45], v9 offset:36936
global_load_lds_dwordx4 v4, s[40:41] offset:0
ds_read_b64_tr_b16 a[46:47], v9 offset:39240
global_load_lds_dwordx4 v5, s[40:41] offset:1024
ds_read_b64_tr_b16 a[48:49], v9 offset:55296
global_load_lds_dwordx4 v6, s[40:41] offset:2048
ds_read_b64_tr_b16 a[50:51], v9 offset:57600
global_load_lds_dwordx4 v7, s[40:41] offset:3072
ds_read_b64_tr_b16 a[52:53], v9 offset:55304
ds_read_b64_tr_b16 a[54:55], v9 offset:57608
s_add_u32 m0, s48, 12288
ds_read_b64_tr_b16 a[56:57], v9 offset:55360
global_load_lds_dwordx4 v8, s[40:41] offset:-4096
ds_read_b64_tr_b16 a[58:59], v9 offset:57664
s_add_u32 s40, s40, 73728
ds_read_b64_tr_b16 a[60:61], v9 offset:55368
s_addc_u32 s41, s41, 0
ds_read_b64_tr_b16 a[62:63], v9 offset:57672
s_waitcnt vmcnt(18) lgkmcnt(0)
s_barrier
s_add_u32 m0, s48, 40960
ds_read_b64_tr_b16 a[64:65], v204 offset:8192
ds_read_b64_tr_b16 a[66:67], v204 offset:10496
global_load_lds_dwordx4 v0, s[40:41] offset:-4096
ds_read_b64_tr_b16 a[68:69], v204 offset:8200
global_load_lds_dwordx4 v1, s[40:41] offset:-3072
ds_read_b64_tr_b16 a[70:71], v204 offset:10504
global_load_lds_dwordx4 v2, s[40:41] offset:-2048
ds_read_b64_tr_b16 a[72:73], v204 offset:8256
global_load_lds_dwordx4 v3, s[40:41] offset:-1024
ds_read_b64_tr_b16 a[74:75], v204 offset:10560
ds_read_b64_tr_b16 a[76:77], v204 offset:8264
global_load_lds_dwordx4 v4, s[40:41] offset:0
ds_read_b64_tr_b16 a[78:79], v204 offset:10568
global_load_lds_dwordx4 v5, s[40:41] offset:1024
ds_read_b64_tr_b16 a[80:81], v204 offset:26624
global_load_lds_dwordx4 v6, s[40:41] offset:2048
ds_read_b64_tr_b16 a[82:83], v204 offset:28928
global_load_lds_dwordx4 v7, s[40:41] offset:3072
ds_read_b64_tr_b16 a[84:85], v204 offset:26632
ds_read_b64_tr_b16 a[86:87], v204 offset:28936
s_add_u32 m0, s48, 49152
ds_read_b64_tr_b16 a[88:89], v204 offset:26688
global_load_lds_dwordx4 v8, s[40:41] offset:-4096
ds_read_b64_tr_b16 a[90:91], v204 offset:28992
s_add_u32 s40, s40, 73728
ds_read_b64_tr_b16 a[92:93], v204 offset:26696
s_addc_u32 s41, s41, 0
ds_read_b64_tr_b16 a[94:95], v204 offset:29000
s_waitcnt vmcnt(18) lgkmcnt(0)
s_barrier
s_add_u32 m0, s48, 77824
ds_read_b64_tr_b16 a[96:97], v204 offset:45056
ds_read_b64_tr_b16 a[98:99], v204 offset:47360
global_load_lds_dwordx4 v0, s[40:41] offset:-4096
ds_read_b64_tr_b16 a[100:101], v204 offset:45064
global_load_lds_dwordx4 v1, s[40:41] offset:-3072
ds_read_b64_tr_b16 a[102:103], v204 offset:47368
global_load_lds_dwordx4 v2, s[40:41] offset:-2048
ds_read_b64_tr_b16 a[104:105], v204 offset:45120
global_load_lds_dwordx4 v3, s[40:41] offset:-1024
ds_read_b64_tr_b16 a[106:107], v204 offset:47424
ds_read_b64_tr_b16 a[108:109], v204 offset:45128
global_load_lds_dwordx4 v4, s[40:41] offset:0
ds_read_b64_tr_b16 a[110:111], v204 offset:47432
global_load_lds_dwordx4 v5, s[40:41] offset:1024
ds_read_b64_tr_b16 a[112:113], v204 offset:63488
global_load_lds_dwordx4 v6, s[40:41] offset:2048
ds_read_b64_tr_b16 a[114:115], v205 offset:256
global_load_lds_dwordx4 v7, s[40:41] offset:3072
ds_read_b64_tr_b16 a[116:117], v204 offset:63496
ds_read_b64_tr_b16 a[118:119], v205 offset:264
s_add_u32 m0, s48, 86016
ds_read_b64_tr_b16 a[120:121], v204 offset:63552
global_load_lds_dwordx4 v8, s[40:41] offset:-4096
)"
R"(ds_read_b64_tr_b16 a[122:123], v205 offset:320
s_add_u32 s40, s40, 73728
ds_read_b64_tr_b16 a[124:125], v204 offset:63560
s_addc_u32 s41, s41, 0
ds_read_b64_tr_b16 a[126:127], v205 offset:328
s_waitcnt vmcnt(18) lgkmcnt(0)
s_barrier
s_add_u32 m0, s48, 114688
ds_read_b64_tr_b16 a[128:129], v9 offset:0
ds_read_b64_tr_b16 a[130:131], v9 offset:2304
global_load_lds_dwordx4 v0, s[40:41] offset:-4096
ds_read_b64_tr_b16 a[132:133], v9 offset:8
global_load_lds_dwordx4 v1, s[40:41] offset:-3072
ds_read_b64_tr_b16 a[134:135], v9 offset:2312
ds_read_b64_tr_b16 a[136:137], v9 offset:64
global_load_lds_dwordx4 v2, s[40:41] offset:-2048
ds_read_b64_tr_b16 a[138:139], v9 offset:2368
global_load_lds_dwordx4 v3, s[40:41] offset:-1024
ds_read_b64_tr_b16 a[140:141], v9 offset:72
global_load_lds_dwordx4 v4, s[40:41] offset:0
ds_read_b64_tr_b16 a[142:143], v9 offset:2376
ds_read_b64_tr_b16 a[144:145], v9 offset:18432
global_load_lds_dwordx4 v5, s[40:41] offset:1024
ds_read_b64_tr_b16 a[146:147], v9 offset:20736
global_load_lds_dwordx4 v6, s[40:41] offset:2048
ds_read_b64_tr_b16 a[148:149], v9 offset:18440
ds_read_b64_tr_b16 a[150:151], v9 offset:20744
global_load_lds_dwordx4 v7, s[40:41] offset:3072
ds_read_b64_tr_b16 a[152:153], v9 offset:18496
s_add_u32 m0, s48, 122880
ds_read_b64_tr_b16 a[154:155], v9 offset:20800
global_load_lds_dwordx4 v8, s[40:41] offset:-4096
ds_read_b64_tr_b16 a[156:157], v9 offset:18504
ds_read_b64_tr_b16 a[158:159], v9 offset:20808
s_add_u32 s40, s40, 73728
ds_read_b64_tr_b16 v[60:61], v10 offset:18432
s_addc_u32 s41, s41, 0
ds_read_b64_tr_b16 v[62:63], v10 offset:20736
s_waitcnt vmcnt(18) lgkmcnt(0)
s_barrier
s_add_u32 m0, s48, 4096
ds_read_b64_tr_b16 a[160:161], v9 offset:36864
ds_read_b64_tr_b16 a[162:163], v9 offset:39168
global_load_lds_dwordx4 v0, s[40:41] offset:-4096
ds_read_b64_tr_b16 a[164:165], v9 offset:36872
ds_read_b64_tr_b16 a[166:167], v9 offset:39176
global_load_lds_dwordx4 v1, s[40:41] offset:-3072
ds_read_b64_tr_b16 a[168:169], v9 offset:36928
global_load_lds_dwordx4 v2, s[40:41] offset:-2048
ds_read_b64_tr_b16 a[170:171], v9 offset:39232
ds_read_b64_tr_b16 a[172:173], v9 offset:36936
global_load_lds_dwordx4 v3, s[40:41] offset:-1024
ds_read_b64_tr_b16 a[174:175], v9 offset:39240
global_load_lds_dwordx4 v4, s[40:41] offset:0
ds_read_b64_tr_b16 v[64:65], v10 offset:36864
ds_read_b64_tr_b16 v[66:67], v10 offset:39168
global_load_lds_dwordx4 v5, s[40:41] offset:1024
ds_read_b64_tr_b16 a[176:177], v9 offset:55296
global_load_lds_dwordx4 v6, s[40:41] offset:2048
ds_read_b64_tr_b16 a[178:179], v9 offset:57600
ds_read_b64_tr_b16 a[180:181], v9 offset:55304
global_load_lds_dwordx4 v7, s[40:41] offset:3072
ds_read_b64_tr_b16 a[182:183], v9 offset:57608
s_add_u32 m0, s48, 12288
ds_read_b64_tr_b16 a[184:185], v9 offset:55360
ds_read_b64_tr_b16 a[186:187], v9 offset:57664
global_load_lds_dwordx4 v8, s[40:41] offset:-4096
ds_read_b64_tr_b16 a[188:189], v9 offset:55368
s_sub_u32 s40, s40, 589824
ds_read_b64_tr_b16 a[190:191], v9 offset:57672
ds_read_b64_tr_b16 v[68:69], v10 offset:55296
s_subb_u32 s41, s41, 0
ds_read_b64_tr_b16 v[70:71], v10 offset:57600
s_waitcnt vmcnt(18) lgkmcnt(0)
s_barrier
ds_read_b64_tr_b16 a[192:193], v204 offset:8192
ds_read_b64_tr_b16 a[194:195], v204 offset:10496
ds_read_b64_tr_b16 a[196:197], v204 offset:8200
ds_read_b64_tr_b16 a[198:199], v204 offset:10504
ds_read_b64_tr_b16 a[200:201], v204 offset:8256
ds_read_b64_tr_b16 a[202:203], v204 offset:10560
ds_read_b64_tr_b16 a[204:205], v204 offset:8264
ds_read_b64_tr_b16 a[206:207], v204 offset:10568
ds_read_b64_tr_b16 v[72:73], v206 offset:8192
ds_read_b64_tr_b16 v[74:75], v206 offset:10496
ds_read_b64_tr_b16 a[208:209], v204 offset:26624
ds_read_b64_tr_b16 a[210:211], v204 offset:28928
ds_read_b64_tr_b16 a[212:213], v204 offset:26632
ds_read_b64_tr_b16 a[214:215], v204 offset:28936
ds_read_b64_tr_b16 a[216:217], v204 offset:26688
ds_read_b64_tr_b16 a[218:219], v204 offset:28992
ds_read_b64_tr_b16 a[220:221], v204 offset:26696
ds_read_b64_tr_b16 a[222:223], v204 offset:29000
ds_read_b64_tr_b16 v[76:77], v206 offset:26624
ds_read_b64_tr_b16 v[78:79], v206 offset:28928
s_waitcnt vmcnt(9) lgkmcnt(0)
s_barrier
ds_read_b64_tr_b16 a[224:225], v204 offset:45056
ds_read_b64_tr_b16 a[226:227], v204 offset:47360
ds_read_b64_tr_b16 a[228:229], v204 offset:45064
ds_read_b64_tr_b16 a[230:231], v204 offset:47368
ds_read_b64_tr_b16 a[232:233], v204 offset:45120
ds_read_b64_tr_b16 a[234:235], v204 offset:47424
ds_read_b64_tr_b16 a[236:237], v204 offset:45128
ds_read_b64_tr_b16 a[238:239], v204 offset:47432
ds_read_b64_tr_b16 v[80:81], v206 offset:45056
ds_read_b64_tr_b16 v[82:83], v206 offset:47360
ds_read_b64_tr_b16 a[240:241], v204 offset:63488
ds_read_b64_tr_b16 a[242:243], v205 offset:256
ds_read_b64_tr_b16 a[244:245], v204 offset:63496
ds_read_b64_tr_b16 a[246:247], v205 offset:264
ds_read_b64_tr_b16 a[248:249], v204 offset:63552
ds_read_b64_tr_b16 a[250:251], v205 offset:320
ds_read_b64_tr_b16 a[252:253], v204 offset:63560
ds_read_b64_tr_b16 a[254:255], v205 offset:328
ds_read_b64_tr_b16 v[84:85], v206 offset:63488
ds_read_b64_tr_b16 v[86:87], v207 offset:256
s_waitcnt vmcnt(0) lgkmcnt(0)
s_barrier
ds_read_b64_tr_b16 v[28:29], v9 offset:0
ds_read_b64_tr_b16 v[30:31], v9 offset:2304
ds_read_b64_tr_b16 v[32:33], v9 offset:8
ds_read_b64_tr_b16 v[34:35], v9 offset:2312
ds_read_b64_tr_b16 v[36:37], v9 offset:64
ds_read_b64_tr_b16 v[38:39], v9 offset:2368
ds_read_b64_tr_b16 v[40:41], v9 offset:72
ds_read_b64_tr_b16 v[42:43], v9 offset:2376
ds_read_b64_tr_b16 v[88:89], v10 offset:0
ds_read_b64_tr_b16 v[90:91], v10 offset:2304
ds_read_b64_tr_b16 v[44:45], v9 offset:18432
ds_read_b64_tr_b16 v[46:47], v9 offset:20736
ds_read_b64_tr_b16 v[48:49], v9 offset:18440
ds_read_b64_tr_b16 v[50:51], v9 offset:20744
ds_read_b64_tr_b16 v[52:53], v9 offset:18496
ds_read_b64_tr_b16 v[54:55], v9 offset:20800
ds_read_b64_tr_b16 v[56:57], v9 offset:18504
ds_read_b64_tr_b16 v[58:59], v9 offset:20808
ds_read_b64_tr_b16 v[92:93], v10 offset:18432
ds_read_b64_tr_b16 v[94:95], v10 offset:20736
s_waitcnt lgkmcnt(0)
s_barrier
v_add_lshl_u32 v252, v24, 0, 7
v_bfi_b32 v252, s69, v252, v23
v_add_lshl_u32 v253, v22, 0, 7
v_bfi_b32 v253, s69, v253, v21
s_mov_b64 s[76:77], 0
s_add_u32 m0, s49, 18432
s_mov_b32 s70, 33
s_waitcnt vmcnt(0) lgkmcnt(0)
s_nop 7
.p2align 6
.Lwo_top10:
s_waitcnt lgkmcnt(2)
v_mfma_f32_16x16x32_bf16 v[96:99], a[144:147], v[132:135], v[96:99]
ds_read_b128 v[144:147], v25 offset:12288
v_mfma_f32_16x16x32_bf16 v[100:103], a[148:151], v[132:135], v[100:103]
v_mfma_f32_16x16x32_bf16 v[104:107], a[152:155], v[132:135], v[104:107]
s_waitcnt vmcnt(8)
s_barrier
v_mfma_f32_16x16x32_bf16 v[108:111], a[156:159], v[132:135], v[108:111]
v_cvt_pk_bf16_f32 v164, v112, v113
v_cvt_pk_bf16_f32 v165, v114, v115
v_mfma_f32_16x16x32_bf16 v[128:131], v[60:63], v[132:135], 0
s_waitcnt lgkmcnt(2)
v_cvt_pk_bf16_f32 v166, v116, v117
v_cvt_pk_bf16_f32 v167, v118, v119
v_mfma_f32_16x16x32_bf16 v[96:99], a[160:163], v[136:139], v[96:99]
ds_read_b128 v[132:135], v25 offset:13312
v_add_f32 v254, v112, v113
v_mfma_f32_16x16x32_bf16 v[100:103], a[164:167], v[136:139], v[100:103]
global_store_dwordx4 v13, v[164:167], s[38:39] offset:0 sc1
v_mfma_f32_16x16x32_bf16 v[104:107], a[168:171], v[136:139], v[104:107]
v_cvt_pk_bf16_f32 v168, v120, v121
v_cvt_pk_bf16_f32 v169, v122, v123
v_mfma_f32_16x16x32_bf16 v[108:111], a[172:175], v[136:139], v[108:111]
v_cvt_pk_bf16_f32 v170, v124, v125
v_cvt_pk_bf16_f32 v171, v126, v127
v_mfma_f32_16x16x32_bf16 v[128:131], v[64:67], v[136:139], v[128:131]
s_waitcnt lgkmcnt(2)
v_mul_f32 v255, v112, v112
v_fmac_f32 v255, v113, v113
v_mfma_f32_16x16x32_bf16 v[96:99], a[176:179], v[140:143], v[96:99]
s_cmp_eq_u32 s70, 32
ds_read_b128 v[136:139], v25 offset:14336
v_add_f32 v254, v254, v114
v_mfma_f32_16x16x32_bf16 v[100:103], a[180:183], v[140:143], v[100:103]
s_cselect_b64 s[42:43], s[44:45], s[42:43]
global_store_dwordx4 v13, v[168:171], s[38:39] offset:1024 sc1
v_mfma_f32_16x16x32_bf16 v[104:107], a[184:187], v[140:143], v[104:107]
s_cmp_eq_u32 s70, 16
v_add_u32 v13, 18688, v13
v_fmac_f32 v255, v114, v114
v_mfma_f32_16x16x32_bf16 v[108:111], a[188:191], v[140:143], v[108:111]
s_cselect_b64 s[42:43], s[46:47], s[42:43]
v_add_f32 v254, v254, v115
v_fmac_f32 v255, v115, v115
v_mfma_f32_16x16x32_bf16 v[128:131], v[68:71], v[140:143], v[128:131]
s_waitcnt lgkmcnt(2)
v_add_f32 v254, v254, v116
v_fmac_f32 v255, v116, v116
v_mfma_f32_16x16x32_bf16 v[96:99], a[192:195], v[144:147], v[96:99]
s_xor_b32 s49, s49, 0x10000
ds_read_b128 v[140:143], v25 offset:15360
v_add_f32 v254, v254, v117
v_mfma_f32_16x16x32_bf16 v[100:103], a[196:199], v[144:147], v[100:103]
s_mov_b32 m0, 49152
v_fmac_f32 v255, v117, v117
v_add_f32 v254, v254, v118
v_mfma_f32_16x16x32_bf16 v[104:107], a[200:203], v[144:147], v[104:107]
v_fmac_f32 v255, v118, v118
v_add_f32 v254, v254, v119
v_mfma_f32_16x16x32_bf16 v[108:111], a[204:207], v[144:147], v[108:111]
v_fmac_f32 v255, v119, v119
v_add_f32 v254, v254, v120
v_mfma_f32_16x16x32_bf16 v[128:131], v[72:75], v[144:147], v[128:131]
s_waitcnt lgkmcnt(2)
v_fmac_f32 v255, v120, v120
v_add_f32 v254, v254, v121
v_mfma_f32_16x16x32_bf16 v[96:99], a[208:211], v[132:135], v[96:99]
ds_read_b128 v[144:147], v25 offset:16384
global_load_lds_dwordx4 v16, s[42:43] offset:0 sc1
v_mfma_f32_16x16x32_bf16 v[100:103], a[212:215], v[132:135], v[100:103]
v_fmac_f32 v255, v121, v121
v_add_f32 v254, v254, v122
v_mfma_f32_16x16x32_bf16 v[104:107], a[216:219], v[132:135], v[104:107]
v_fmac_f32 v255, v122, v122
v_add_f32 v254, v254, v123
v_mfma_f32_16x16x32_bf16 v[108:111], a[220:223], v[132:135], v[108:111]
v_fmac_f32 v255, v123, v123
v_add_f32 v254, v254, v124
v_mfma_f32_16x16x32_bf16 v[128:131], v[76:79], v[132:135], v[128:131]
s_waitcnt vmcnt(9)
ds_read_b128 v[148:151], v19 offset:8192
ds_read_b128 v[152:155], v20 offset:8192
s_waitcnt lgkmcnt(4)
v_mfma_f32_16x16x32_bf16 v[96:99], a[224:227], v[136:139], v[96:99]
ds_read_b128 v[132:135], v25 offset:17408
v_fmac_f32 v255, v124, v124
v_mfma_f32_16x16x32_bf16 v[100:103], a[228:231], v[136:139], v[100:103]
v_add_f32 v254, v254, v125
v_fmac_f32 v255, v125, v125
v_mfma_f32_16x16x32_bf16 v[104:107], a[232:235], v[136:139], v[104:107]
global_load_lds_dwordx4 v17, s[42:43] offset:1024 sc1
v_add_f32 v254, v254, v126
v_mfma_f32_16x16x32_bf16 v[108:111], a[236:239], v[136:139], v[108:111]
v_fmac_f32 v255, v126, v126
v_add_f32 v254, v254, v127
v_mfma_f32_16x16x32_bf16 v[128:131], v[80:83], v[136:139], v[128:131]
s_waitcnt lgkmcnt(4)
v_fmac_f32 v255, v127, v127
v_mfma_f32_16x16x32_bf16 v[96:99], a[240:243], v[140:143], v[96:99]
ds_read_b128 v[136:139], v25 offset:0
v_mfma_f32_16x16x32_bf16 v[100:103], a[244:247], v[140:143], v[100:103]
ds_write_b64 v27, v[254:255] offset:45568
v_mfma_f32_16x16x32_bf16 v[104:107], a[248:251], v[140:143], v[104:107]
s_add_u32 m0, s49, 0
s_waitcnt lgkmcnt(4)
v_lshlrev_b32 v112, 16, v148
v_and_b32 v113, 0xffff0000, v148
v_mfma_f32_16x16x32_bf16 v[108:111], a[252:255], v[140:143], v[108:111]
v_lshlrev_b32 v114, 16, v149
v_and_b32 v115, 0xffff0000, v149
v_mfma_f32_16x16x32_bf16 v[128:131], v[84:87], v[140:143], v[128:131]
v_lshlrev_b32 v116, 16, v150
v_and_b32 v117, 0xffff0000, v150
v_mfma_f32_16x16x32_bf16 v[96:99], v[28:31], v[144:147], v[96:99]
s_sub_u32 s42, s42, 18432
ds_read_b128 v[140:143], v25 offset:1024
v_lshlrev_b32 v118, 16, v151
v_mfma_f32_16x16x32_bf16 v[100:103], v[32:35], v[144:147], v[100:103]
s_subb_u32 s43, s43, 0
v_and_b32 v119, 0xffff0000, v151
s_waitcnt lgkmcnt(4)
v_lshlrev_b32 v120, 16, v152
v_mfma_f32_16x16x32_bf16 v[104:107], v[36:39], v[144:147], v[104:107]
v_and_b32 v121, 0xffff0000, v152
v_lshlrev_b32 v122, 16, v153
v_mfma_f32_16x16x32_bf16 v[108:111], v[40:43], v[144:147], v[108:111]
v_and_b32 v123, 0xffff0000, v153
v_lshlrev_b32 v124, 16, v154
v_mfma_f32_16x16x32_bf16 v[128:131], v[88:91], v[144:147], v[128:131]
s_waitcnt lgkmcnt(3)
v_and_b32 v125, 0xffff0000, v154
v_lshlrev_b32 v126, 16, v155
v_mfma_f32_16x16x32_bf16 v[96:99], v[44:47], v[132:135], v[96:99]
ds_read_b128 v[144:147], v25 offset:2048
v_and_b32 v127, 0xffff0000, v155
v_mfma_f32_16x16x32_bf16 v[100:103], v[48:51], v[132:135], v[100:103]
v_mfma_f32_16x16x32_bf16 v[104:107], v[52:55], v[132:135], v[104:107]
v_mfma_f32_16x16x32_bf16 v[108:111], v[56:59], v[132:135], v[108:111]
v_mfma_f32_16x16x32_bf16 v[128:131], v[92:95], v[132:135], v[128:131]
s_waitcnt lgkmcnt(3)
v_mfma_f32_16x16x32_bf16 v[96:99], a[0:3], v[136:139], v[96:99]
ds_read_b128 v[132:135], v25 offset:3072
v_mfma_f32_16x16x32_bf16 v[100:103], a[4:7], v[136:139], v[100:103]
v_mfma_f32_16x16x32_bf16 v[104:107], a[8:11], v[136:139], v[104:107]
v_mfma_f32_16x16x32_bf16 v[108:111], a[12:15], v[136:139], v[108:111]
s_waitcnt lgkmcnt(2)
v_mfma_f32_16x16x32_bf16 v[96:99], a[16:19], v[140:143], v[96:99]
ds_read_b128 v[136:139], v25 offset:4096
v_mfma_f32_16x16x32_bf16 v[100:103], a[20:23], v[140:143], v[100:103]
s_waitcnt vmcnt(6)
v_sub_u32 v200, v200, v22
v_cmp_lt_u32 vcc, 0x7f, v200
v_mfma_f32_16x16x32_bf16 v[104:107], a[24:27], v[140:143], v[104:107]
v_mfma_f32_16x16x32_bf16 v[108:111], a[28:31], v[140:143], v[108:111]
s_waitcnt lgkmcnt(2)
v_mfma_f32_16x16x32_bf16 v[96:99], a[32:35], v[144:147], v[96:99]
s_and_b64 vcc, vcc, s[76:77]
s_cbranch_vccnz .Lwo_sp6
.Lwo_rt7:
ds_read_b128 v[140:143], v25 offset:5120
v_mfma_f32_16x16x32_bf16 v[100:103], a[36:39], v[144:147], v[100:103]
v_mfma_f32_16x16x32_bf16 v[104:107], a[40:43], v[144:147], v[104:107]
s_barrier
v_mfma_f32_16x16x32_bf16 v[108:111], a[44:47], v[144:147], v[108:111]
s_waitcnt lgkmcnt(2)
ds_write_b128 v26, v[128:131] offset:37888
v_mfma_f32_16x16x32_bf16 v[96:99], a[48:51], v[132:135], v[96:99]
ds_read_b128 v[144:147], v25 offset:6144
v_add_u32 v22, 1, v22
v_mfma_f32_16x16x32_bf16 v[100:103], a[52:55], v[132:135], v[100:103]
v_add_lshl_u32 v253, v22, 0, 7
v_bfi_b32 v253, s69, v253, v21
v_mfma_f32_16x16x32_bf16 v[104:107], a[56:59], v[132:135], v[104:107]
global_load_lds_dwordx4 v11, s[38:39] offset:-2048 sc1
v_mfma_f32_16x16x32_bf16 v[108:111], a[60:63], v[132:135], v[108:111]
s_waitcnt lgkmcnt(3)
v_mfma_f32_16x16x32_bf16 v[96:99], a[64:67], v[136:139], v[96:99]
ds_read_b128 v[132:135], v25 offset:7168
v_mfma_f32_16x16x32_bf16 v[100:103], a[68:71], v[136:139], v[100:103]
global_load_lds_dwordx4 v11, s[38:39] offset:-1024 sc1
v_mfma_f32_16x16x32_bf16 v[104:107], a[72:75], v[136:139], v[104:107]
v_mfma_f32_16x16x32_bf16 v[108:111], a[76:79], v[136:139], v[108:111]
s_waitcnt lgkmcnt(3)
v_mfma_f32_16x16x32_bf16 v[96:99], a[80:83], v[140:143], v[96:99]
ds_read_b128 v[136:139], v25 offset:8192
global_load_lds_dwordx4 v11, s[38:39] offset:0 sc1
v_mfma_f32_16x16x32_bf16 v[100:103], a[84:87], v[140:143], v[100:103]
v_mfma_f32_16x16x32_bf16 v[104:107], a[88:91], v[140:143], v[104:107]
v_mfma_f32_16x16x32_bf16 v[108:111], a[92:95], v[140:143], v[108:111]
s_waitcnt lgkmcnt(2)
global_load_dword v201, v253, s[38:39] sc1
global_load_lds_dwordx4 v11, s[38:39] offset:1024 sc1
v_mfma_f32_16x16x32_bf16 v[96:99], a[96:99], v[144:147], v[96:99]
v_mfma_f32_16x16x32_bf16 v[100:103], a[100:103], v[144:147], v[100:103]
v_mfma_f32_16x16x32_bf16 v[104:107], a[104:107], v[144:147], v[104:107]
s_waitcnt vmcnt(9)
s_barrier
v_mfma_f32_16x16x32_bf16 v[108:111], a[108:111], v[144:147], v[108:111]
s_waitcnt lgkmcnt(1)
ds_read_b128 v[140:143], v25 offset:27648
global_load_lds_dwordx4 v11, s[38:39] offset:2048 sc1
v_mfma_f32_16x16x32_bf16 v[96:99], a[112:115], v[132:135], v[96:99]
ds_read_b128 v[144:147], v25 offset:28672
v_add_u32 v11, 18432, v11
v_mfma_f32_16x16x32_bf16 v[100:103], a[116:119], v[132:135], v[100:103]
v_min_u32 v11, v11, v12
v_mfma_f32_16x16x32_bf16 v[104:107], a[120:123], v[132:135], v[104:107]
v_mfma_f32_16x16x32_bf16 v[108:111], a[124:127], v[132:135], v[108:111]
s_waitcnt lgkmcnt(2)
v_mfma_f32_16x16x32_bf16 v[96:99], a[128:131], v[136:139], v[96:99]
ds_read_b128 v[132:135], v25 offset:29696
v_mfma_f32_16x16x32_bf16 v[100:103], a[132:135], v[136:139], v[100:103]
v_mfma_f32_16x16x32_bf16 v[104:107], a[136:139], v[136:139], v[104:107]
v_mfma_f32_16x16x32_bf16 v[108:111], a[140:143], v[136:139], v[108:111]
s_waitcnt lgkmcnt(2)
v_mfma_f32_16x16x32_bf16 v[112:115], a[144:147], v[140:143], v[112:115]
ds_read_b128 v[136:139], v25 offset:30720
v_mfma_f32_16x16x32_bf16 v[116:119], a[148:151], v[140:143], v[116:119]
v_mfma_f32_16x16x32_bf16 v[120:123], a[152:155], v[140:143], v[120:123]
s_waitcnt vmcnt(8)
s_barrier
v_mfma_f32_16x16x32_bf16 v[124:127], a[156:159], v[140:143], v[124:127]
v_cvt_pk_bf16_f32 v164, v96, v97
v_cvt_pk_bf16_f32 v165, v98, v99
v_mfma_f32_16x16x32_bf16 v[128:131], v[60:63], v[140:143], 0
s_waitcnt lgkmcnt(2)
v_cvt_pk_bf16_f32 v166, v100, v101
v_cvt_pk_bf16_f32 v167, v102, v103
v_mfma_f32_16x16x32_bf16 v[112:115], a[160:163], v[144:147], v[112:115]
ds_read_b128 v[140:143], v25 offset:31744
v_add_f32 v254, v96, v97
v_mfma_f32_16x16x32_bf16 v[116:119], a[164:167], v[144:147], v[116:119]
global_store_dwordx4 v13, v[164:167], s[38:39] offset:0 sc1
v_mfma_f32_16x16x32_bf16 v[120:123], a[168:171], v[144:147], v[120:123]
v_cvt_pk_bf16_f32 v168, v104, v105
v_cvt_pk_bf16_f32 v169, v106, v107
v_mfma_f32_16x16x32_bf16 v[124:127], a[172:175], v[144:147], v[124:127]
v_cvt_pk_bf16_f32 v170, v108, v109
v_cvt_pk_bf16_f32 v171, v110, v111
v_mfma_f32_16x16x32_bf16 v[128:131], v[64:67], v[144:147], v[128:131]
s_waitcnt lgkmcnt(2)
v_mul_f32 v255, v96, v96
v_fmac_f32 v255, v97, v97
v_mfma_f32_16x16x32_bf16 v[112:115], a[176:179], v[132:135], v[112:115]
s_mov_b32 m0, 57344
ds_read_b128 v[144:147], v25 offset:32768
v_add_f32 v254, v254, v98
v_mfma_f32_16x16x32_bf16 v[116:119], a[180:183], v[132:135], v[116:119]
)"
R"(global_store_dwordx4 v13, v[168:171], s[38:39] offset:1024 sc1
v_mfma_f32_16x16x32_bf16 v[120:123], a[184:187], v[132:135], v[120:123]
v_add_u32 v13, 18688, v13
v_fmac_f32 v255, v98, v98
v_mfma_f32_16x16x32_bf16 v[124:127], a[188:191], v[132:135], v[124:127]
v_add_f32 v254, v254, v99
v_fmac_f32 v255, v99, v99
v_mfma_f32_16x16x32_bf16 v[128:131], v[68:71], v[132:135], v[128:131]
s_waitcnt lgkmcnt(2)
v_add_f32 v254, v254, v100
v_fmac_f32 v255, v100, v100
v_mfma_f32_16x16x32_bf16 v[112:115], a[192:195], v[136:139], v[112:115]
ds_read_b128 v[132:135], v25 offset:33792
v_add_f32 v254, v254, v101
v_mfma_f32_16x16x32_bf16 v[116:119], a[196:199], v[136:139], v[116:119]
v_fmac_f32 v255, v101, v101
v_add_f32 v254, v254, v102
v_mfma_f32_16x16x32_bf16 v[120:123], a[200:203], v[136:139], v[120:123]
v_fmac_f32 v255, v102, v102
v_add_f32 v254, v254, v103
v_mfma_f32_16x16x32_bf16 v[124:127], a[204:207], v[136:139], v[124:127]
v_fmac_f32 v255, v103, v103
v_add_f32 v254, v254, v104
v_mfma_f32_16x16x32_bf16 v[128:131], v[72:75], v[136:139], v[128:131]
s_waitcnt lgkmcnt(2)
v_fmac_f32 v255, v104, v104
v_add_f32 v254, v254, v105
v_mfma_f32_16x16x32_bf16 v[112:115], a[208:211], v[140:143], v[112:115]
ds_read_b128 v[136:139], v25 offset:34816
global_load_lds_dwordx4 v16, s[42:43] offset:0 sc1
v_mfma_f32_16x16x32_bf16 v[116:119], a[212:215], v[140:143], v[116:119]
v_fmac_f32 v255, v105, v105
v_add_f32 v254, v254, v106
v_mfma_f32_16x16x32_bf16 v[120:123], a[216:219], v[140:143], v[120:123]
v_fmac_f32 v255, v106, v106
v_add_f32 v254, v254, v107
v_mfma_f32_16x16x32_bf16 v[124:127], a[220:223], v[140:143], v[124:127]
v_fmac_f32 v255, v107, v107
v_add_f32 v254, v254, v108
v_mfma_f32_16x16x32_bf16 v[128:131], v[76:79], v[140:143], v[128:131]
s_waitcnt vmcnt(9)
ds_read_b128 v[148:151], v19 offset:0
ds_read_b128 v[152:155], v20 offset:0
s_waitcnt lgkmcnt(4)
v_mfma_f32_16x16x32_bf16 v[112:115], a[224:227], v[144:147], v[112:115]
ds_read_b128 v[140:143], v25 offset:35840
v_fmac_f32 v255, v108, v108
v_mfma_f32_16x16x32_bf16 v[116:119], a[228:231], v[144:147], v[116:119]
v_add_f32 v254, v254, v109
v_fmac_f32 v255, v109, v109
v_mfma_f32_16x16x32_bf16 v[120:123], a[232:235], v[144:147], v[120:123]
global_load_lds_dwordx4 v17, s[42:43] offset:1024 sc1
v_add_f32 v254, v254, v110
v_mfma_f32_16x16x32_bf16 v[124:127], a[236:239], v[144:147], v[124:127]
v_fmac_f32 v255, v110, v110
v_add_f32 v254, v254, v111
v_mfma_f32_16x16x32_bf16 v[128:131], v[80:83], v[144:147], v[128:131]
s_waitcnt lgkmcnt(4)
v_fmac_f32 v255, v111, v111
v_mfma_f32_16x16x32_bf16 v[112:115], a[240:243], v[132:135], v[112:115]
ds_read_b128 v[144:147], v25 offset:18432
v_mfma_f32_16x16x32_bf16 v[116:119], a[244:247], v[132:135], v[116:119]
ds_write_b64 v27, v[254:255] offset:43520
v_mfma_f32_16x16x32_bf16 v[120:123], a[248:251], v[132:135], v[120:123]
s_add_u32 m0, s49, 18432
s_waitcnt lgkmcnt(4)
v_lshlrev_b32 v96, 16, v148
v_and_b32 v97, 0xffff0000, v148
v_mfma_f32_16x16x32_bf16 v[124:127], a[252:255], v[132:135], v[124:127]
v_lshlrev_b32 v98, 16, v149
v_and_b32 v99, 0xffff0000, v149
v_mfma_f32_16x16x32_bf16 v[128:131], v[84:87], v[132:135], v[128:131]
v_lshlrev_b32 v100, 16, v150
v_and_b32 v101, 0xffff0000, v150
v_mfma_f32_16x16x32_bf16 v[112:115], v[28:31], v[136:139], v[112:115]
s_sub_u32 s42, s42, 18432
ds_read_b128 v[132:135], v25 offset:19456
v_lshlrev_b32 v102, 16, v151
v_mfma_f32_16x16x32_bf16 v[116:119], v[32:35], v[136:139], v[116:119]
s_subb_u32 s43, s43, 0
v_and_b32 v103, 0xffff0000, v151
s_waitcnt lgkmcnt(4)
v_lshlrev_b32 v104, 16, v152
v_mfma_f32_16x16x32_bf16 v[120:123], v[36:39], v[136:139], v[120:123]
v_and_b32 v105, 0xffff0000, v152
v_lshlrev_b32 v106, 16, v153
v_mfma_f32_16x16x32_bf16 v[124:127], v[40:43], v[136:139], v[124:127]
v_and_b32 v107, 0xffff0000, v153
v_lshlrev_b32 v108, 16, v154
v_mfma_f32_16x16x32_bf16 v[128:131], v[88:91], v[136:139], v[128:131]
s_waitcnt lgkmcnt(3)
v_and_b32 v109, 0xffff0000, v154
v_lshlrev_b32 v110, 16, v155
v_mfma_f32_16x16x32_bf16 v[112:115], v[44:47], v[140:143], v[112:115]
ds_read_b128 v[136:139], v25 offset:20480
v_and_b32 v111, 0xffff0000, v155
v_mfma_f32_16x16x32_bf16 v[116:119], v[48:51], v[140:143], v[116:119]
v_mfma_f32_16x16x32_bf16 v[120:123], v[52:55], v[140:143], v[120:123]
v_mfma_f32_16x16x32_bf16 v[124:127], v[56:59], v[140:143], v[124:127]
v_mfma_f32_16x16x32_bf16 v[128:131], v[92:95], v[140:143], v[128:131]
s_waitcnt lgkmcnt(3)
v_mfma_f32_16x16x32_bf16 v[112:115], a[0:3], v[144:147], v[112:115]
ds_read_b128 v[140:143], v25 offset:21504
v_mfma_f32_16x16x32_bf16 v[116:119], a[4:7], v[144:147], v[116:119]
v_mfma_f32_16x16x32_bf16 v[120:123], a[8:11], v[144:147], v[120:123]
v_mfma_f32_16x16x32_bf16 v[124:127], a[12:15], v[144:147], v[124:127]
s_waitcnt lgkmcnt(2)
v_mfma_f32_16x16x32_bf16 v[112:115], a[16:19], v[132:135], v[112:115]
ds_read_b128 v[144:147], v25 offset:22528
v_mfma_f32_16x16x32_bf16 v[116:119], a[20:23], v[132:135], v[116:119]
s_waitcnt vmcnt(6)
v_sub_u32 v201, v201, v22
v_cmp_lt_u32 vcc, 0x7f, v201
v_mfma_f32_16x16x32_bf16 v[120:123], a[24:27], v[132:135], v[120:123]
v_mfma_f32_16x16x32_bf16 v[124:127], a[28:31], v[132:135], v[124:127]
s_waitcnt lgkmcnt(2)
v_mfma_f32_16x16x32_bf16 v[112:115], a[32:35], v[136:139], v[112:115]
s_and_b64 vcc, vcc, s[76:77]
s_cbranch_vccnz .Lwo_sp8
.Lwo_rt9:
ds_read_b128 v[132:135], v25 offset:23552
v_mfma_f32_16x16x32_bf16 v[116:119], a[36:39], v[136:139], v[116:119]
v_mfma_f32_16x16x32_bf16 v[120:123], a[40:43], v[136:139], v[120:123]
s_barrier
v_mfma_f32_16x16x32_bf16 v[124:127], a[44:47], v[136:139], v[124:127]
s_waitcnt lgkmcnt(2)
ds_write_b128 v26, v[128:131] offset:40960
v_mfma_f32_16x16x32_bf16 v[112:115], a[48:51], v[140:143], v[112:115]
s_mov_b64 s[76:77], -1
ds_read_b128 v[136:139], v25 offset:24576
v_add_u32 v22, 1, v22
v_mfma_f32_16x16x32_bf16 v[116:119], a[52:55], v[140:143], v[116:119]
v_add_lshl_u32 v253, v22, 0, 7
v_bfi_b32 v253, s69, v253, v21
v_mfma_f32_16x16x32_bf16 v[120:123], a[56:59], v[140:143], v[120:123]
global_load_lds_dwordx4 v11, s[38:39] offset:-2048 sc1
v_mfma_f32_16x16x32_bf16 v[124:127], a[60:63], v[140:143], v[124:127]
s_waitcnt lgkmcnt(3)
v_mfma_f32_16x16x32_bf16 v[112:115], a[64:67], v[144:147], v[112:115]
ds_read_b128 v[140:143], v25 offset:25600
v_mfma_f32_16x16x32_bf16 v[116:119], a[68:71], v[144:147], v[116:119]
global_load_lds_dwordx4 v11, s[38:39] offset:-1024 sc1
v_mfma_f32_16x16x32_bf16 v[120:123], a[72:75], v[144:147], v[120:123]
v_mfma_f32_16x16x32_bf16 v[124:127], a[76:79], v[144:147], v[124:127]
s_waitcnt lgkmcnt(3)
v_mfma_f32_16x16x32_bf16 v[112:115], a[80:83], v[132:135], v[112:115]
ds_read_b128 v[144:147], v25 offset:26624
global_load_lds_dwordx4 v11, s[38:39] offset:0 sc1
v_mfma_f32_16x16x32_bf16 v[116:119], a[84:87], v[132:135], v[116:119]
v_mfma_f32_16x16x32_bf16 v[120:123], a[88:91], v[132:135], v[120:123]
v_mfma_f32_16x16x32_bf16 v[124:127], a[92:95], v[132:135], v[124:127]
s_waitcnt lgkmcnt(2)
v_xor_b32 v25, 0x10000, v25
global_load_dword v200, v253, s[38:39] sc1
v_mfma_f32_16x16x32_bf16 v[112:115], a[96:99], v[136:139], v[112:115]
global_load_lds_dwordx4 v11, s[38:39] offset:1024 sc1
v_mfma_f32_16x16x32_bf16 v[116:119], a[100:103], v[136:139], v[116:119]
v_mfma_f32_16x16x32_bf16 v[120:123], a[104:107], v[136:139], v[120:123]
s_waitcnt vmcnt(9)
s_barrier
v_mfma_f32_16x16x32_bf16 v[124:127], a[108:111], v[136:139], v[124:127]
s_waitcnt lgkmcnt(1)
ds_read_b128 v[132:135], v25 offset:9216
global_load_lds_dwordx4 v11, s[38:39] offset:2048 sc1
v_mfma_f32_16x16x32_bf16 v[112:115], a[112:115], v[140:143], v[112:115]
ds_read_b128 v[136:139], v25 offset:10240
v_add_u32 v11, 18432, v11
v_mfma_f32_16x16x32_bf16 v[116:119], a[116:119], v[140:143], v[116:119]
v_min_u32 v11, v11, v12
v_mfma_f32_16x16x32_bf16 v[120:123], a[120:123], v[140:143], v[120:123]
v_mfma_f32_16x16x32_bf16 v[124:127], a[124:127], v[140:143], v[124:127]
s_waitcnt lgkmcnt(2)
v_mfma_f32_16x16x32_bf16 v[112:115], a[128:131], v[144:147], v[112:115]
ds_read_b128 v[140:143], v25 offset:11264
v_mfma_f32_16x16x32_bf16 v[116:119], a[132:135], v[144:147], v[116:119]
v_mfma_f32_16x16x32_bf16 v[120:123], a[136:139], v[144:147], v[120:123]
v_mfma_f32_16x16x32_bf16 v[124:127], a[140:143], v[144:147], v[124:127]
s_sub_u32 s70, s70, 1
s_cbranch_scc0 .Lwo_top10
s_waitcnt vmcnt(8)
s_barrier
v_cvt_pk_bf16_f32 v164, v112, v113
v_cvt_pk_bf16_f32 v165, v114, v115
v_cvt_pk_bf16_f32 v166, v116, v117
v_cvt_pk_bf16_f32 v167, v118, v119
global_store_dwordx4 v13, v[164:167], s[38:39] offset:0 sc1
v_cvt_pk_bf16_f32 v168, v120, v121
v_cvt_pk_bf16_f32 v169, v122, v123
v_cvt_pk_bf16_f32 v170, v124, v125
v_cvt_pk_bf16_f32 v171, v126, v127
global_store_dwordx4 v13, v[168:171], s[38:39] offset:1024 sc1
v_add_f32 v254, v112, v113
v_mul_f32 v255, v112, v112
v_fmac_f32 v255, v113, v113
v_add_f32 v254, v254, v114
v_fmac_f32 v255, v114, v114
v_add_f32 v254, v254, v115
v_fmac_f32 v255, v115, v115
v_add_f32 v254, v254, v116
v_fmac_f32 v255, v116, v116
v_add_f32 v254, v254, v117
v_fmac_f32 v255, v117, v117
v_add_f32 v254, v254, v118
v_fmac_f32 v255, v118, v118
v_add_f32 v254, v254, v119
v_fmac_f32 v255, v119, v119
v_add_f32 v254, v254, v120
v_fmac_f32 v255, v120, v120
v_add_f32 v254, v254, v121
v_fmac_f32 v255, v121, v121
v_add_f32 v254, v254, v122
v_fmac_f32 v255, v122, v122
v_add_f32 v254, v254, v123
v_fmac_f32 v255, v123, v123
v_add_f32 v254, v254, v124
v_fmac_f32 v255, v124, v124
v_add_f32 v254, v254, v125
v_fmac_f32 v255, v125, v125
v_add_f32 v254, v254, v126
v_fmac_f32 v255, v126, v126
v_add_f32 v254, v254, v127
v_fmac_f32 v255, v127, v127
ds_write_b64 v27, v[254:255] offset:45568
s_waitcnt vmcnt(0) lgkmcnt(0)
s_barrier
s_barrier
s_endpgm
.Lwo_w3:
s_add_u32 m0, s48, 4096
s_nop 0
global_load_lds_dwordx4 v0, s[40:41] offset:-4096
global_load_lds_dwordx4 v1, s[40:41] offset:-3072
global_load_lds_dwordx4 v2, s[40:41] offset:-2048
global_load_lds_dwordx4 v3, s[40:41] offset:-1024
global_load_lds_dwordx4 v4, s[40:41] offset:0
global_load_lds_dwordx4 v5, s[40:41] offset:1024
global_load_lds_dwordx4 v6, s[40:41] offset:2048
global_load_lds_dwordx4 v7, s[40:41] offset:3072
s_add_u32 m0, s48, 12288
s_nop 0
global_load_lds_dwordx4 v8, s[40:41] offset:-4096
s_add_u32 s40, s40, 73728
s_addc_u32 s41, s41, 0
s_add_u32 m0, s48, 40960
s_nop 0
global_load_lds_dwordx4 v0, s[40:41] offset:-4096
global_load_lds_dwordx4 v1, s[40:41] offset:-3072
global_load_lds_dwordx4 v2, s[40:41] offset:-2048
global_load_lds_dwordx4 v3, s[40:41] offset:-1024
global_load_lds_dwordx4 v4, s[40:41] offset:0
global_load_lds_dwordx4 v5, s[40:41] offset:1024
global_load_lds_dwordx4 v6, s[40:41] offset:2048
global_load_lds_dwordx4 v7, s[40:41] offset:3072
s_add_u32 m0, s48, 49152
s_nop 0
global_load_lds_dwordx4 v8, s[40:41] offset:-4096
s_add_u32 s40, s40, 73728
s_addc_u32 s41, s41, 0
s_add_u32 m0, s48, 77824
s_nop 0
global_load_lds_dwordx4 v0, s[40:41] offset:-4096
global_load_lds_dwordx4 v1, s[40:41] offset:-3072
global_load_lds_dwordx4 v2, s[40:41] offset:-2048
global_load_lds_dwordx4 v3, s[40:41] offset:-1024
global_load_lds_dwordx4 v4, s[40:41] offset:0
global_load_lds_dwordx4 v5, s[40:41] offset:1024
global_load_lds_dwordx4 v6, s[40:41] offset:2048
global_load_lds_dwordx4 v7, s[40:41] offset:3072
s_add_u32 m0, s48, 86016
s_nop 0
global_load_lds_dwordx4 v8, s[40:41] offset:-4096
s_add_u32 s40, s40, 73728
s_addc_u32 s41, s41, 0
v_add_u32 v204, 0x10000, v9
v_add_u32 v205, 0x20000, v9
v_add_u32 v206, 0x10000, v10
v_add_u32 v207, 0x20000, v10
s_waitcnt lgkmcnt(0)
s_mul_i32 s37, s37, 0x9E3779B1
s_add_u32 s37, s37, 0x7F4A7C15
v_add_u32 v22, s37, v22
v_add_u32 v24, s37, v24
s_waitcnt vmcnt(18)
s_barrier
s_add_u32 m0, s48, 114688
ds_read_b64_tr_b16 a[0:1], v9 offset:0
ds_read_b64_tr_b16 a[2:3], v9 offset:2304
global_load_lds_dwordx4 v0, s[40:41] offset:-4096
ds_read_b64_tr_b16 a[4:5], v9 offset:8
ds_read_b64_tr_b16 a[6:7], v9 offset:2312
global_load_lds_dwordx4 v1, s[40:41] offset:-3072
ds_read_b64_tr_b16 a[8:9], v9 offset:64
global_load_lds_dwordx4 v2, s[40:41] offset:-2048
ds_read_b64_tr_b16 a[10:11], v9 offset:2368
ds_read_b64_tr_b16 a[12:13], v9 offset:72
global_load_lds_dwordx4 v3, s[40:41] offset:-1024
ds_read_b64_tr_b16 a[14:15], v9 offset:2376
global_load_lds_dwordx4 v4, s[40:41] offset:0
ds_read_b64_tr_b16 v[60:61], v10 offset:0
ds_read_b64_tr_b16 v[62:63], v10 offset:2304
global_load_lds_dwordx4 v5, s[40:41] offset:1024
ds_read_b64_tr_b16 a[16:17], v9 offset:18432
global_load_lds_dwordx4 v6, s[40:41] offset:2048
ds_read_b64_tr_b16 a[18:19], v9 offset:20736
ds_read_b64_tr_b16 a[20:21], v9 offset:18440
global_load_lds_dwordx4 v7, s[40:41] offset:3072
ds_read_b64_tr_b16 a[22:23], v9 offset:20744
s_add_u32 m0, s48, 122880
ds_read_b64_tr_b16 a[24:25], v9 offset:18496
ds_read_b64_tr_b16 a[26:27], v9 offset:20800
global_load_lds_dwordx4 v8, s[40:41] offset:-4096
ds_read_b64_tr_b16 a[28:29], v9 offset:18504
s_add_u32 s40, s40, 73728
ds_read_b64_tr_b16 a[30:31], v9 offset:20808
ds_read_b64_tr_b16 v[64:65], v10 offset:18432
s_addc_u32 s41, s41, 0
ds_read_b64_tr_b16 v[66:67], v10 offset:20736
s_waitcnt vmcnt(18) lgkmcnt(0)
s_barrier
s_add_u32 m0, s48, 4096
ds_read_b64_tr_b16 a[32:33], v9 offset:36864
ds_read_b64_tr_b16 a[34:35], v9 offset:39168
global_load_lds_dwordx4 v0, s[40:41] offset:-4096
ds_read_b64_tr_b16 a[36:37], v9 offset:36872
ds_read_b64_tr_b16 a[38:39], v9 offset:39176
global_load_lds_dwordx4 v1, s[40:41] offset:-3072
ds_read_b64_tr_b16 a[40:41], v9 offset:36928
global_load_lds_dwordx4 v2, s[40:41] offset:-2048
ds_read_b64_tr_b16 a[42:43], v9 offset:39232
ds_read_b64_tr_b16 a[44:45], v9 offset:36936
global_load_lds_dwordx4 v3, s[40:41] offset:-1024
ds_read_b64_tr_b16 a[46:47], v9 offset:39240
global_load_lds_dwordx4 v4, s[40:41] offset:0
ds_read_b64_tr_b16 v[68:69], v10 offset:36864
ds_read_b64_tr_b16 v[70:71], v10 offset:39168
global_load_lds_dwordx4 v5, s[40:41] offset:1024
ds_read_b64_tr_b16 a[48:49], v9 offset:55296
global_load_lds_dwordx4 v6, s[40:41] offset:2048
ds_read_b64_tr_b16 a[50:51], v9 offset:57600
ds_read_b64_tr_b16 a[52:53], v9 offset:55304
global_load_lds_dwordx4 v7, s[40:41] offset:3072
ds_read_b64_tr_b16 a[54:55], v9 offset:57608
s_add_u32 m0, s48, 12288
ds_read_b64_tr_b16 a[56:57], v9 offset:55360
ds_read_b64_tr_b16 a[58:59], v9 offset:57664
global_load_lds_dwordx4 v8, s[40:41] offset:-4096
ds_read_b64_tr_b16 a[60:61], v9 offset:55368
s_add_u32 s40, s40, 73728
ds_read_b64_tr_b16 a[62:63], v9 offset:57672
ds_read_b64_tr_b16 v[72:73], v10 offset:55296
s_addc_u32 s41, s41, 0
ds_read_b64_tr_b16 v[74:75], v10 offset:57600
s_waitcnt vmcnt(18) lgkmcnt(0)
s_barrier
s_add_u32 m0, s48, 40960
ds_read_b64_tr_b16 a[64:65], v204 offset:8192
ds_read_b64_tr_b16 a[66:67], v204 offset:10496
global_load_lds_dwordx4 v0, s[40:41] offset:-4096
ds_read_b64_tr_b16 a[68:69], v204 offset:8200
ds_read_b64_tr_b16 a[70:71], v204 offset:10504
global_load_lds_dwordx4 v1, s[40:41] offset:-3072
ds_read_b64_tr_b16 a[72:73], v204 offset:8256
global_load_lds_dwordx4 v2, s[40:41] offset:-2048
ds_read_b64_tr_b16 a[74:75], v204 offset:10560
ds_read_b64_tr_b16 a[76:77], v204 offset:8264
global_load_lds_dwordx4 v3, s[40:41] offset:-1024
ds_read_b64_tr_b16 a[78:79], v204 offset:10568
global_load_lds_dwordx4 v4, s[40:41] offset:0
ds_read_b64_tr_b16 v[76:77], v206 offset:8192
ds_read_b64_tr_b16 v[78:79], v206 offset:10496
global_load_lds_dwordx4 v5, s[40:41] offset:1024
ds_read_b64_tr_b16 a[80:81], v204 offset:26624
global_load_lds_dwordx4 v6, s[40:41] offset:2048
ds_read_b64_tr_b16 a[82:83], v204 offset:28928
ds_read_b64_tr_b16 a[84:85], v204 offset:26632
global_load_lds_dwordx4 v7, s[40:41] offset:3072
ds_read_b64_tr_b16 a[86:87], v204 offset:28936
s_add_u32 m0, s48, 49152
ds_read_b64_tr_b16 a[88:89], v204 offset:26688
ds_read_b64_tr_b16 a[90:91], v204 offset:28992
global_load_lds_dwordx4 v8, s[40:41] offset:-4096
ds_read_b64_tr_b16 a[92:93], v204 offset:26696
s_add_u32 s40, s40, 73728
ds_read_b64_tr_b16 a[94:95], v204 offset:29000
ds_read_b64_tr_b16 v[80:81], v206 offset:26624
s_addc_u32 s41, s41, 0
ds_read_b64_tr_b16 v[82:83], v206 offset:28928
s_waitcnt vmcnt(18) lgkmcnt(0)
s_barrier
s_add_u32 m0, s48, 77824
ds_read_b64_tr_b16 a[96:97], v204 offset:45056
ds_read_b64_tr_b16 a[98:99], v204 offset:47360
global_load_lds_dwordx4 v0, s[40:41] offset:-4096
ds_read_b64_tr_b16 a[100:101], v204 offset:45064
ds_read_b64_tr_b16 a[102:103], v204 offset:47368
global_load_lds_dwordx4 v1, s[40:41] offset:-3072
ds_read_b64_tr_b16 a[104:105], v204 offset:45120
global_load_lds_dwordx4 v2, s[40:41] offset:-2048
ds_read_b64_tr_b16 a[106:107], v204 offset:47424
ds_read_b64_tr_b16 a[108:109], v204 offset:45128
)"
R"(global_load_lds_dwordx4 v3, s[40:41] offset:-1024
ds_read_b64_tr_b16 a[110:111], v204 offset:47432
global_load_lds_dwordx4 v4, s[40:41] offset:0
ds_read_b64_tr_b16 v[84:85], v206 offset:45056
ds_read_b64_tr_b16 v[86:87], v206 offset:47360
global_load_lds_dwordx4 v5, s[40:41] offset:1024
ds_read_b64_tr_b16 a[112:113], v204 offset:63488
global_load_lds_dwordx4 v6, s[40:41] offset:2048
ds_read_b64_tr_b16 a[114:115], v205 offset:256
ds_read_b64_tr_b16 a[116:117], v204 offset:63496
global_load_lds_dwordx4 v7, s[40:41] offset:3072
ds_read_b64_tr_b16 a[118:119], v205 offset:264
s_add_u32 m0, s48, 86016
ds_read_b64_tr_b16 a[120:121], v204 offset:63552
ds_read_b64_tr_b16 a[122:123], v205 offset:320
global_load_lds_dwordx4 v8, s[40:41] offset:-4096
ds_read_b64_tr_b16 a[124:125], v204 offset:63560
s_add_u32 s40, s40, 73728
ds_read_b64_tr_b16 a[126:127], v205 offset:328
ds_read_b64_tr_b16 v[88:89], v206 offset:63488
s_addc_u32 s41, s41, 0
ds_read_b64_tr_b16 v[90:91], v207 offset:256
s_waitcnt vmcnt(18) lgkmcnt(0)
s_barrier
s_add_u32 m0, s48, 114688
ds_read_b64_tr_b16 a[128:129], v9 offset:0
ds_read_b64_tr_b16 a[130:131], v9 offset:2304
global_load_lds_dwordx4 v0, s[40:41] offset:-4096
ds_read_b64_tr_b16 a[132:133], v9 offset:8
global_load_lds_dwordx4 v1, s[40:41] offset:-3072
ds_read_b64_tr_b16 a[134:135], v9 offset:2312
ds_read_b64_tr_b16 a[136:137], v9 offset:64
global_load_lds_dwordx4 v2, s[40:41] offset:-2048
ds_read_b64_tr_b16 a[138:139], v9 offset:2368
global_load_lds_dwordx4 v3, s[40:41] offset:-1024
ds_read_b64_tr_b16 a[140:141], v9 offset:72
global_load_lds_dwordx4 v4, s[40:41] offset:0
ds_read_b64_tr_b16 a[142:143], v9 offset:2376
ds_read_b64_tr_b16 v[92:93], v10 offset:0
global_load_lds_dwordx4 v5, s[40:41] offset:1024
ds_read_b64_tr_b16 v[94:95], v10 offset:2304
global_load_lds_dwordx4 v6, s[40:41] offset:2048
ds_read_b64_tr_b16 a[144:145], v9 offset:18432
ds_read_b64_tr_b16 a[146:147], v9 offset:20736
global_load_lds_dwordx4 v7, s[40:41] offset:3072
ds_read_b64_tr_b16 a[148:149], v9 offset:18440
s_add_u32 m0, s48, 122880
ds_read_b64_tr_b16 a[150:151], v9 offset:20744
global_load_lds_dwordx4 v8, s[40:41] offset:-4096
ds_read_b64_tr_b16 a[152:153], v9 offset:18496
ds_read_b64_tr_b16 a[154:155], v9 offset:20800
s_add_u32 s40, s40, 73728
ds_read_b64_tr_b16 a[156:157], v9 offset:18504
s_addc_u32 s41, s41, 0
ds_read_b64_tr_b16 a[158:159], v9 offset:20808
s_waitcnt vmcnt(18) lgkmcnt(0)
s_barrier
s_add_u32 m0, s48, 4096
ds_read_b64_tr_b16 a[160:161], v9 offset:36864
ds_read_b64_tr_b16 a[162:163], v9 offset:39168
global_load_lds_dwordx4 v0, s[40:41] offset:-4096
ds_read_b64_tr_b16 a[164:165], v9 offset:36872
global_load_lds_dwordx4 v1, s[40:41] offset:-3072
ds_read_b64_tr_b16 a[166:167], v9 offset:39176
global_load_lds_dwordx4 v2, s[40:41] offset:-2048
ds_read_b64_tr_b16 a[168:169], v9 offset:36928
global_load_lds_dwordx4 v3, s[40:41] offset:-1024
ds_read_b64_tr_b16 a[170:171], v9 offset:39232
ds_read_b64_tr_b16 a[172:173], v9 offset:36936
global_load_lds_dwordx4 v4, s[40:41] offset:0
ds_read_b64_tr_b16 a[174:175], v9 offset:39240
global_load_lds_dwordx4 v5, s[40:41] offset:1024
ds_read_b64_tr_b16 a[176:177], v9 offset:55296
global_load_lds_dwordx4 v6, s[40:41] offset:2048
ds_read_b64_tr_b16 a[178:179], v9 offset:57600
global_load_lds_dwordx4 v7, s[40:41] offset:3072
ds_read_b64_tr_b16 a[180:181], v9 offset:55304
ds_read_b64_tr_b16 a[182:183], v9 offset:57608
s_add_u32 m0, s48, 12288
ds_read_b64_tr_b16 a[184:185], v9 offset:55360
global_load_lds_dwordx4 v8, s[40:41] offset:-4096
ds_read_b64_tr_b16 a[186:187], v9 offset:57664
s_sub_u32 s40, s40, 589824
ds_read_b64_tr_b16 a[188:189], v9 offset:55368
s_subb_u32 s41, s41, 0
ds_read_b64_tr_b16 a[190:191], v9 offset:57672
s_waitcnt vmcnt(18) lgkmcnt(0)
s_barrier
ds_read_b64_tr_b16 a[192:193], v204 offset:8192
ds_read_b64_tr_b16 a[194:195], v204 offset:10496
ds_read_b64_tr_b16 a[196:197], v204 offset:8200
ds_read_b64_tr_b16 a[198:199], v204 offset:10504
ds_read_b64_tr_b16 a[200:201], v204 offset:8256
ds_read_b64_tr_b16 a[202:203], v204 offset:10560
ds_read_b64_tr_b16 a[204:205], v204 offset:8264
ds_read_b64_tr_b16 a[206:207], v204 offset:10568
ds_read_b64_tr_b16 a[208:209], v204 offset:26624
ds_read_b64_tr_b16 a[210:211], v204 offset:28928
ds_read_b64_tr_b16 a[212:213], v204 offset:26632
ds_read_b64_tr_b16 a[214:215], v204 offset:28936
ds_read_b64_tr_b16 a[216:217], v204 offset:26688
ds_read_b64_tr_b16 a[218:219], v204 offset:28992
ds_read_b64_tr_b16 a[220:221], v204 offset:26696
ds_read_b64_tr_b16 a[222:223], v204 offset:29000
s_waitcnt vmcnt(9) lgkmcnt(0)
s_barrier
ds_read_b64_tr_b16 a[224:225], v204 offset:45056
ds_read_b64_tr_b16 a[226:227], v204 offset:47360
ds_read_b64_tr_b16 a[228:229], v204 offset:45064
ds_read_b64_tr_b16 a[230:231], v204 offset:47368
ds_read_b64_tr_b16 a[232:233], v204 offset:45120
ds_read_b64_tr_b16 a[234:235], v204 offset:47424
ds_read_b64_tr_b16 a[236:237], v204 offset:45128
ds_read_b64_tr_b16 a[238:239], v204 offset:47432
ds_read_b64_tr_b16 a[240:241], v204 offset:63488
ds_read_b64_tr_b16 a[242:243], v205 offset:256
ds_read_b64_tr_b16 a[244:245], v204 offset:63496
ds_read_b64_tr_b16 a[246:247], v205 offset:264
ds_read_b64_tr_b16 a[248:249], v204 offset:63552
ds_read_b64_tr_b16 a[250:251], v205 offset:320
ds_read_b64_tr_b16 a[252:253], v204 offset:63560
ds_read_b64_tr_b16 a[254:255], v205 offset:328
s_waitcnt vmcnt(0) lgkmcnt(0)
s_barrier
ds_read_b64_tr_b16 v[28:29], v9 offset:0
ds_read_b64_tr_b16 v[30:31], v9 offset:2304
ds_read_b64_tr_b16 v[32:33], v9 offset:8
ds_read_b64_tr_b16 v[34:35], v9 offset:2312
ds_read_b64_tr_b16 v[36:37], v9 offset:64
ds_read_b64_tr_b16 v[38:39], v9 offset:2368
ds_read_b64_tr_b16 v[40:41], v9 offset:72
ds_read_b64_tr_b16 v[42:43], v9 offset:2376
ds_read_b64_tr_b16 v[44:45], v9 offset:18432
ds_read_b64_tr_b16 v[46:47], v9 offset:20736
ds_read_b64_tr_b16 v[48:49], v9 offset:18440
ds_read_b64_tr_b16 v[50:51], v9 offset:20744
ds_read_b64_tr_b16 v[52:53], v9 offset:18496
ds_read_b64_tr_b16 v[54:55], v9 offset:20800
ds_read_b64_tr_b16 v[56:57], v9 offset:18504
ds_read_b64_tr_b16 v[58:59], v9 offset:20808
s_waitcnt lgkmcnt(0)
s_barrier
v_add_lshl_u32 v252, v24, 0, 7
v_bfi_b32 v252, s69, v252, v23
v_add_lshl_u32 v253, v22, 0, 7
v_bfi_b32 v253, s69, v253, v21
s_mov_b64 s[76:77], 0
s_add_u32 m0, s49, 18432
s_mov_b32 s70, 33
s_waitcnt vmcnt(0) lgkmcnt(0)
s_nop 7
.p2align 6
.Lwo_top11:
s_waitcnt lgkmcnt(2)
v_mfma_f32_16x16x32_bf16 v[96:99], a[0:3], v[132:135], v[96:99]
ds_read_b128 v[144:147], v25 offset:3072
v_mfma_f32_16x16x32_bf16 v[100:103], a[4:7], v[132:135], v[100:103]
v_mfma_f32_16x16x32_bf16 v[104:107], a[8:11], v[132:135], v[104:107]
s_waitcnt vmcnt(7)
s_barrier
v_mfma_f32_16x16x32_bf16 v[108:111], a[12:15], v[132:135], v[108:111]
v_cvt_pk_bf16_f32 v164, v112, v113
v_cvt_pk_bf16_f32 v165, v114, v115
v_mfma_f32_16x16x32_bf16 v[128:131], v[60:63], v[132:135], 0
s_waitcnt lgkmcnt(2)
v_cvt_pk_bf16_f32 v166, v116, v117
v_cvt_pk_bf16_f32 v167, v118, v119
s_mov_b64 exec, 65535
global_store_dwordx2 v15, v[176:177], s[38:39] sc1
s_mov_b64 exec, -1
v_mfma_f32_16x16x32_bf16 v[96:99], a[16:19], v[136:139], v[96:99]
ds_read_b128 v[132:135], v25 offset:4096
v_add_f32 v254, v112, v113
v_add_u32 v15, 18688, v15
v_mfma_f32_16x16x32_bf16 v[100:103], a[20:23], v[136:139], v[100:103]
v_mul_f32 v255, v112, v112
v_fmac_f32 v255, v113, v113
v_mfma_f32_16x16x32_bf16 v[104:107], a[24:27], v[136:139], v[104:107]
global_store_dwordx4 v13, v[164:167], s[38:39] offset:0 sc1
v_mfma_f32_16x16x32_bf16 v[108:111], a[28:31], v[136:139], v[108:111]
v_cvt_pk_bf16_f32 v168, v120, v121
v_cvt_pk_bf16_f32 v169, v122, v123
v_mfma_f32_16x16x32_bf16 v[128:131], v[64:67], v[136:139], v[128:131]
s_waitcnt lgkmcnt(2)
v_cvt_pk_bf16_f32 v170, v124, v125
v_cvt_pk_bf16_f32 v171, v126, v127
v_mfma_f32_16x16x32_bf16 v[96:99], a[32:35], v[140:143], v[96:99]
s_cmp_eq_u32 s70, 32
ds_read_b128 v[136:139], v25 offset:5120
v_add_f32 v254, v254, v114
v_mfma_f32_16x16x32_bf16 v[100:103], a[36:39], v[140:143], v[100:103]
s_cselect_b64 s[42:43], s[44:45], s[42:43]
global_store_dwordx4 v13, v[168:171], s[38:39] offset:1024 sc1
v_mfma_f32_16x16x32_bf16 v[104:107], a[40:43], v[140:143], v[104:107]
s_cmp_eq_u32 s70, 16
v_add_u32 v13, 18688, v13
v_fmac_f32 v255, v114, v114
v_mfma_f32_16x16x32_bf16 v[108:111], a[44:47], v[140:143], v[108:111]
s_cselect_b64 s[42:43], s[46:47], s[42:43]
v_add_f32 v254, v254, v115
v_fmac_f32 v255, v115, v115
v_mfma_f32_16x16x32_bf16 v[128:131], v[68:71], v[140:143], v[128:131]
s_waitcnt lgkmcnt(2)
v_add_f32 v254, v254, v116
v_fmac_f32 v255, v116, v116
v_mfma_f32_16x16x32_bf16 v[96:99], a[48:51], v[144:147], v[96:99]
s_xor_b32 s49, s49, 0x10000
ds_read_b128 v[140:143], v25 offset:6144
v_add_f32 v254, v254, v117
v_mfma_f32_16x16x32_bf16 v[100:103], a[52:55], v[144:147], v[100:103]
s_mov_b32 m0, 51200
v_fmac_f32 v255, v117, v117
v_add_f32 v254, v254, v118
v_mfma_f32_16x16x32_bf16 v[104:107], a[56:59], v[144:147], v[104:107]
v_fmac_f32 v255, v118, v118
v_add_f32 v254, v254, v119
v_mfma_f32_16x16x32_bf16 v[108:111], a[60:63], v[144:147], v[108:111]
v_fmac_f32 v255, v119, v119
v_add_f32 v254, v254, v120
v_mfma_f32_16x16x32_bf16 v[128:131], v[72:75], v[144:147], v[128:131]
s_waitcnt lgkmcnt(2)
v_fmac_f32 v255, v120, v120
v_add_f32 v254, v254, v121
v_mfma_f32_16x16x32_bf16 v[96:99], a[64:67], v[132:135], v[96:99]
ds_read_b128 v[144:147], v25 offset:7168
v_fmac_f32 v255, v121, v121
v_mfma_f32_16x16x32_bf16 v[100:103], a[68:71], v[132:135], v[100:103]
v_add_f32 v254, v254, v122
v_fmac_f32 v255, v122, v122
v_mfma_f32_16x16x32_bf16 v[104:107], a[72:75], v[132:135], v[104:107]
global_load_lds_dwordx4 v16, s[42:43] offset:0 sc1
v_add_f32 v254, v254, v123
v_mfma_f32_16x16x32_bf16 v[108:111], a[76:79], v[132:135], v[108:111]
v_fmac_f32 v255, v123, v123
v_add_f32 v254, v254, v124
v_mfma_f32_16x16x32_bf16 v[128:131], v[76:79], v[132:135], v[128:131]
s_waitcnt vmcnt(9)
ds_read_b128 v[148:151], v19 offset:8192
ds_read_b128 v[152:155], v20 offset:8192
s_waitcnt lgkmcnt(4)
v_mfma_f32_16x16x32_bf16 v[96:99], a[80:83], v[136:139], v[96:99]
ds_read_b128 v[132:135], v25 offset:8192
v_fmac_f32 v255, v124, v124
v_mfma_f32_16x16x32_bf16 v[100:103], a[84:87], v[136:139], v[100:103]
v_add_f32 v254, v254, v125
v_fmac_f32 v255, v125, v125
v_mfma_f32_16x16x32_bf16 v[104:107], a[88:91], v[136:139], v[104:107]
v_add_f32 v254, v254, v126
v_fmac_f32 v255, v126, v126
v_mfma_f32_16x16x32_bf16 v[108:111], a[92:95], v[136:139], v[108:111]
v_add_f32 v254, v254, v127
v_fmac_f32 v255, v127, v127
v_mfma_f32_16x16x32_bf16 v[128:131], v[80:83], v[136:139], v[128:131]
s_waitcnt lgkmcnt(4)
global_load_lds_dwordx4 v17, s[42:43] offset:1024 sc1
v_mfma_f32_16x16x32_bf16 v[96:99], a[96:99], v[140:143], v[96:99]
ds_read_b128 v[136:139], v25 offset:9216
v_mfma_f32_16x16x32_bf16 v[100:103], a[100:103], v[140:143], v[100:103]
ds_write_b64 v27, v[254:255] offset:46080
v_mfma_f32_16x16x32_bf16 v[104:107], a[104:107], v[140:143], v[104:107]
s_waitcnt lgkmcnt(4)
v_lshlrev_b32 v112, 16, v148
v_and_b32 v113, 0xffff0000, v148
v_mfma_f32_16x16x32_bf16 v[108:111], a[108:111], v[140:143], v[108:111]
v_lshlrev_b32 v114, 16, v149
v_and_b32 v115, 0xffff0000, v149
v_mfma_f32_16x16x32_bf16 v[128:131], v[84:87], v[140:143], v[128:131]
v_lshlrev_b32 v116, 16, v150
v_and_b32 v117, 0xffff0000, v150
v_mfma_f32_16x16x32_bf16 v[96:99], a[112:115], v[144:147], v[96:99]
s_add_u32 m0, s49, 0
ds_read_b128 v[140:143], v25 offset:10240
v_lshlrev_b32 v118, 16, v151
v_mfma_f32_16x16x32_bf16 v[100:103], a[116:119], v[144:147], v[100:103]
s_waitcnt lgkmcnt(4)
v_and_b32 v119, 0xffff0000, v151
v_lshlrev_b32 v120, 16, v152
v_mfma_f32_16x16x32_bf16 v[104:107], a[120:123], v[144:147], v[104:107]
s_sub_u32 s42, s42, 18432
v_and_b32 v121, 0xffff0000, v152
v_lshlrev_b32 v122, 16, v153
v_mfma_f32_16x16x32_bf16 v[108:111], a[124:127], v[144:147], v[108:111]
s_subb_u32 s43, s43, 0
v_and_b32 v123, 0xffff0000, v153
v_lshlrev_b32 v124, 16, v154
v_mfma_f32_16x16x32_bf16 v[128:131], v[88:91], v[144:147], v[128:131]
s_waitcnt lgkmcnt(3)
v_and_b32 v125, 0xffff0000, v154
v_lshlrev_b32 v126, 16, v155
v_mfma_f32_16x16x32_bf16 v[96:99], a[128:131], v[132:135], v[96:99]
ds_read_b128 v[144:147], v25 offset:11264
v_and_b32 v127, 0xffff0000, v155
v_mfma_f32_16x16x32_bf16 v[100:103], a[132:135], v[132:135], v[100:103]
v_mfma_f32_16x16x32_bf16 v[104:107], a[136:139], v[132:135], v[104:107]
v_mfma_f32_16x16x32_bf16 v[108:111], a[140:143], v[132:135], v[108:111]
v_mfma_f32_16x16x32_bf16 v[128:131], v[92:95], v[132:135], v[128:131]
s_waitcnt lgkmcnt(3)
ds_read_b64 v[176:177], v27 offset:43008
ds_read_b64 v[180:181], v27 offset:43520
v_mfma_f32_16x16x32_bf16 v[96:99], a[144:147], v[136:139], v[96:99]
ds_read_b128 v[132:135], v25 offset:12288
ds_read_b64 v[184:185], v27 offset:44032
v_mfma_f32_16x16x32_bf16 v[100:103], a[148:151], v[136:139], v[100:103]
ds_read_b64 v[188:189], v27 offset:44544
v_mfma_f32_16x16x32_bf16 v[104:107], a[152:155], v[136:139], v[104:107]
v_mfma_f32_16x16x32_bf16 v[108:111], a[156:159], v[136:139], v[108:111]
s_waitcnt lgkmcnt(6)
v_mfma_f32_16x16x32_bf16 v[96:99], a[160:163], v[140:143], v[96:99]
ds_read_b128 v[136:139], v25 offset:13312
v_mfma_f32_16x16x32_bf16 v[100:103], a[164:167], v[140:143], v[100:103]
v_mfma_f32_16x16x32_bf16 v[104:107], a[168:171], v[140:143], v[104:107]
v_mfma_f32_16x16x32_bf16 v[108:111], a[172:175], v[140:143], v[108:111]
s_waitcnt lgkmcnt(4)
v_add_f32 v176, v176, v180
v_add_f32 v177, v177, v181
v_mfma_f32_16x16x32_bf16 v[96:99], a[176:179], v[144:147], v[96:99]
s_waitcnt lgkmcnt(1)
ds_read_b128 v[140:143], v25 offset:14336
v_add_f32 v184, v184, v188
v_mfma_f32_16x16x32_bf16 v[100:103], a[180:183], v[144:147], v[100:103]
v_mfma_f32_16x16x32_bf16 v[104:107], a[184:187], v[144:147], v[104:107]
s_barrier
v_mfma_f32_16x16x32_bf16 v[108:111], a[188:191], v[144:147], v[108:111]
global_load_lds_dwordx4 v11, s[38:39] offset:-2048 sc1
v_add_f32 v185, v185, v189
v_mfma_f32_16x16x32_bf16 v[96:99], a[192:195], v[132:135], v[96:99]
ds_read_b128 v[144:147], v25 offset:15360
v_add_f32 v176, v176, v184
v_mfma_f32_16x16x32_bf16 v[100:103], a[196:199], v[132:135], v[100:103]
v_add_f32 v177, v177, v185
v_mov_b32 v202, v176
v_mfma_f32_16x16x32_bf16 v[104:107], a[200:203], v[132:135], v[104:107]
ds_write_b128 v26, v[128:131] offset:38912
v_mfma_f32_16x16x32_bf16 v[108:111], a[204:207], v[132:135], v[108:111]
s_waitcnt lgkmcnt(3)
global_load_lds_dwordx4 v11, s[38:39] offset:-1024 sc1
v_mov_b32 v203, v177
v_mfma_f32_16x16x32_bf16 v[96:99], a[208:211], v[136:139], v[96:99]
ds_read_b128 v[132:135], v25 offset:16384
v_mfma_f32_16x16x32_bf16 v[100:103], a[212:215], v[136:139], v[100:103]
v_permlane16_swap_b32 v176, v202
v_mfma_f32_16x16x32_bf16 v[104:107], a[216:219], v[136:139], v[104:107]
global_load_lds_dwordx4 v11, s[38:39] offset:0 sc1
v_mfma_f32_16x16x32_bf16 v[108:111], a[220:223], v[136:139], v[108:111]
s_waitcnt lgkmcnt(3)
v_permlane16_swap_b32 v177, v203
v_mfma_f32_16x16x32_bf16 v[96:99], a[224:227], v[140:143], v[96:99]
ds_read_b128 v[136:139], v25 offset:17408
v_add_f32 v176, v176, v202
v_mfma_f32_16x16x32_bf16 v[100:103], a[228:231], v[140:143], v[100:103]
global_load_lds_dwordx4 v11, s[38:39] offset:1024 sc1
v_add_f32 v177, v177, v203
v_mfma_f32_16x16x32_bf16 v[104:107], a[232:235], v[140:143], v[104:107]
v_mov_b32 v202, v176
v_mov_b32 v203, v177
v_mfma_f32_16x16x32_bf16 v[108:111], a[236:239], v[140:143], v[108:111]
s_waitcnt lgkmcnt(3)
v_permlane32_swap_b32 v176, v202
v_mfma_f32_16x16x32_bf16 v[96:99], a[240:243], v[144:147], v[96:99]
global_load_lds_dwordx4 v11, s[38:39] offset:2048 sc1
v_add_u32 v11, 18432, v11
v_mfma_f32_16x16x32_bf16 v[100:103], a[244:247], v[144:147], v[100:103]
v_mfma_f32_16x16x32_bf16 v[104:107], a[248:251], v[144:147], v[104:107]
s_waitcnt vmcnt(10) lgkmcnt(2)
s_barrier
v_mfma_f32_16x16x32_bf16 v[108:111], a[252:255], v[144:147], v[108:111]
s_waitcnt lgkmcnt(1)
ds_read_b128 v[140:143], v25 offset:18432
v_min_u32 v11, v11, v12
v_mfma_f32_16x16x32_bf16 v[96:99], v[28:31], v[132:135], v[96:99]
ds_read_b128 v[144:147], v25 offset:19456
v_mfma_f32_16x16x32_bf16 v[100:103], v[32:35], v[132:135], v[100:103]
v_permlane32_swap_b32 v177, v203
v_mfma_f32_16x16x32_bf16 v[104:107], v[36:39], v[132:135], v[104:107]
v_add_f32 v176, v176, v202
v_add_f32 v177, v177, v203
v_mfma_f32_16x16x32_bf16 v[108:111], v[40:43], v[132:135], v[108:111]
s_waitcnt lgkmcnt(2)
v_mfma_f32_16x16x32_bf16 v[96:99], v[44:47], v[136:139], v[96:99]
ds_read_b128 v[132:135], v25 offset:20480
v_mfma_f32_16x16x32_bf16 v[100:103], v[48:51], v[136:139], v[100:103]
v_mfma_f32_16x16x32_bf16 v[104:107], v[52:55], v[136:139], v[104:107]
v_mfma_f32_16x16x32_bf16 v[108:111], v[56:59], v[136:139], v[108:111]
s_waitcnt lgkmcnt(2)
v_mfma_f32_16x16x32_bf16 v[112:115], a[0:3], v[140:143], v[112:115]
ds_read_b128 v[136:139], v25 offset:21504
v_mfma_f32_16x16x32_bf16 v[116:119], a[4:7], v[140:143], v[116:119]
v_mfma_f32_16x16x32_bf16 v[120:123], a[8:11], v[140:143], v[120:123]
s_waitcnt vmcnt(7)
s_barrier
v_mfma_f32_16x16x32_bf16 v[124:127], a[12:15], v[140:143], v[124:127]
v_cvt_pk_bf16_f32 v164, v96, v97
v_cvt_pk_bf16_f32 v165, v98, v99
v_mfma_f32_16x16x32_bf16 v[128:131], v[60:63], v[140:143], 0
s_waitcnt lgkmcnt(2)
v_cvt_pk_bf16_f32 v166, v100, v101
v_cvt_pk_bf16_f32 v167, v102, v103
s_mov_b64 exec, 65535
global_store_dwordx2 v15, v[176:177], s[38:39] sc1
s_mov_b64 exec, -1
)"
R"(v_mfma_f32_16x16x32_bf16 v[112:115], a[16:19], v[144:147], v[112:115]
ds_read_b128 v[140:143], v25 offset:22528
v_add_u32 v15, 18688, v15
v_mfma_f32_16x16x32_bf16 v[116:119], a[20:23], v[144:147], v[116:119]
v_add_f32 v254, v96, v97
v_mul_f32 v255, v96, v96
v_mfma_f32_16x16x32_bf16 v[120:123], a[24:27], v[144:147], v[120:123]
global_store_dwordx4 v13, v[164:167], s[38:39] offset:0 sc1
v_mfma_f32_16x16x32_bf16 v[124:127], a[28:31], v[144:147], v[124:127]
v_cvt_pk_bf16_f32 v168, v104, v105
v_cvt_pk_bf16_f32 v169, v106, v107
v_mfma_f32_16x16x32_bf16 v[128:131], v[64:67], v[144:147], v[128:131]
s_waitcnt lgkmcnt(2)
v_cvt_pk_bf16_f32 v170, v108, v109
v_cvt_pk_bf16_f32 v171, v110, v111
v_mfma_f32_16x16x32_bf16 v[112:115], a[32:35], v[132:135], v[112:115]
s_mov_b32 m0, 59392
ds_read_b128 v[144:147], v25 offset:23552
v_fmac_f32 v255, v97, v97
v_mfma_f32_16x16x32_bf16 v[116:119], a[36:39], v[132:135], v[116:119]
global_store_dwordx4 v13, v[168:171], s[38:39] offset:1024 sc1
v_mfma_f32_16x16x32_bf16 v[120:123], a[40:43], v[132:135], v[120:123]
v_add_u32 v13, 18688, v13
v_add_f32 v254, v254, v98
v_mfma_f32_16x16x32_bf16 v[124:127], a[44:47], v[132:135], v[124:127]
v_fmac_f32 v255, v98, v98
v_add_f32 v254, v254, v99
v_mfma_f32_16x16x32_bf16 v[128:131], v[68:71], v[132:135], v[128:131]
s_waitcnt lgkmcnt(2)
v_fmac_f32 v255, v99, v99
v_add_f32 v254, v254, v100
v_mfma_f32_16x16x32_bf16 v[112:115], a[48:51], v[136:139], v[112:115]
ds_read_b128 v[132:135], v25 offset:24576
v_fmac_f32 v255, v100, v100
v_mfma_f32_16x16x32_bf16 v[116:119], a[52:55], v[136:139], v[116:119]
v_add_f32 v254, v254, v101
v_fmac_f32 v255, v101, v101
v_mfma_f32_16x16x32_bf16 v[120:123], a[56:59], v[136:139], v[120:123]
v_add_f32 v254, v254, v102
v_fmac_f32 v255, v102, v102
v_mfma_f32_16x16x32_bf16 v[124:127], a[60:63], v[136:139], v[124:127]
v_add_f32 v254, v254, v103
v_fmac_f32 v255, v103, v103
v_mfma_f32_16x16x32_bf16 v[128:131], v[72:75], v[136:139], v[128:131]
s_waitcnt lgkmcnt(2)
v_add_f32 v254, v254, v104
v_fmac_f32 v255, v104, v104
v_mfma_f32_16x16x32_bf16 v[112:115], a[64:67], v[140:143], v[112:115]
ds_read_b128 v[136:139], v25 offset:25600
v_add_f32 v254, v254, v105
v_mfma_f32_16x16x32_bf16 v[116:119], a[68:71], v[140:143], v[116:119]
v_fmac_f32 v255, v105, v105
v_add_f32 v254, v254, v106
v_mfma_f32_16x16x32_bf16 v[120:123], a[72:75], v[140:143], v[120:123]
global_load_lds_dwordx4 v16, s[42:43] offset:0 sc1
v_fmac_f32 v255, v106, v106
v_mfma_f32_16x16x32_bf16 v[124:127], a[76:79], v[140:143], v[124:127]
v_add_f32 v254, v254, v107
v_fmac_f32 v255, v107, v107
v_mfma_f32_16x16x32_bf16 v[128:131], v[76:79], v[140:143], v[128:131]
s_waitcnt vmcnt(9)
ds_read_b128 v[148:151], v19 offset:0
ds_read_b128 v[152:155], v20 offset:0
s_waitcnt lgkmcnt(4)
v_mfma_f32_16x16x32_bf16 v[112:115], a[80:83], v[144:147], v[112:115]
ds_read_b128 v[140:143], v25 offset:26624
v_add_f32 v254, v254, v108
v_mfma_f32_16x16x32_bf16 v[116:119], a[84:87], v[144:147], v[116:119]
v_fmac_f32 v255, v108, v108
v_add_f32 v254, v254, v109
v_mfma_f32_16x16x32_bf16 v[120:123], a[88:91], v[144:147], v[120:123]
v_fmac_f32 v255, v109, v109
v_add_f32 v254, v254, v110
v_mfma_f32_16x16x32_bf16 v[124:127], a[92:95], v[144:147], v[124:127]
v_fmac_f32 v255, v110, v110
v_add_f32 v254, v254, v111
v_mfma_f32_16x16x32_bf16 v[128:131], v[80:83], v[144:147], v[128:131]
s_waitcnt lgkmcnt(4)
global_load_lds_dwordx4 v17, s[42:43] offset:1024 sc1
v_fmac_f32 v255, v111, v111
v_mfma_f32_16x16x32_bf16 v[112:115], a[96:99], v[132:135], v[112:115]
ds_read_b128 v[144:147], v25 offset:27648
v_mfma_f32_16x16x32_bf16 v[116:119], a[100:103], v[132:135], v[116:119]
ds_write_b64 v27, v[254:255] offset:44032
v_mfma_f32_16x16x32_bf16 v[120:123], a[104:107], v[132:135], v[120:123]
s_waitcnt lgkmcnt(4)
v_lshlrev_b32 v96, 16, v148
v_and_b32 v97, 0xffff0000, v148
v_mfma_f32_16x16x32_bf16 v[124:127], a[108:111], v[132:135], v[124:127]
v_lshlrev_b32 v98, 16, v149
v_and_b32 v99, 0xffff0000, v149
v_mfma_f32_16x16x32_bf16 v[128:131], v[84:87], v[132:135], v[128:131]
v_lshlrev_b32 v100, 16, v150
v_and_b32 v101, 0xffff0000, v150
v_mfma_f32_16x16x32_bf16 v[112:115], a[112:115], v[136:139], v[112:115]
s_add_u32 m0, s49, 18432
ds_read_b128 v[132:135], v25 offset:28672
v_lshlrev_b32 v102, 16, v151
v_mfma_f32_16x16x32_bf16 v[116:119], a[116:119], v[136:139], v[116:119]
s_waitcnt lgkmcnt(4)
v_and_b32 v103, 0xffff0000, v151
v_lshlrev_b32 v104, 16, v152
v_mfma_f32_16x16x32_bf16 v[120:123], a[120:123], v[136:139], v[120:123]
s_sub_u32 s42, s42, 18432
v_and_b32 v105, 0xffff0000, v152
v_lshlrev_b32 v106, 16, v153
v_mfma_f32_16x16x32_bf16 v[124:127], a[124:127], v[136:139], v[124:127]
s_subb_u32 s43, s43, 0
v_and_b32 v107, 0xffff0000, v153
v_lshlrev_b32 v108, 16, v154
v_mfma_f32_16x16x32_bf16 v[128:131], v[88:91], v[136:139], v[128:131]
s_waitcnt lgkmcnt(3)
v_and_b32 v109, 0xffff0000, v154
v_lshlrev_b32 v110, 16, v155
v_mfma_f32_16x16x32_bf16 v[112:115], a[128:131], v[140:143], v[112:115]
ds_read_b128 v[136:139], v25 offset:29696
v_and_b32 v111, 0xffff0000, v155
v_mfma_f32_16x16x32_bf16 v[116:119], a[132:135], v[140:143], v[116:119]
v_mfma_f32_16x16x32_bf16 v[120:123], a[136:139], v[140:143], v[120:123]
v_mfma_f32_16x16x32_bf16 v[124:127], a[140:143], v[140:143], v[124:127]
v_mfma_f32_16x16x32_bf16 v[128:131], v[92:95], v[140:143], v[128:131]
s_waitcnt lgkmcnt(3)
ds_read_b64 v[176:177], v27 offset:45056
ds_read_b64 v[180:181], v27 offset:45568
v_mfma_f32_16x16x32_bf16 v[112:115], a[144:147], v[144:147], v[112:115]
ds_read_b128 v[140:143], v25 offset:30720
ds_read_b64 v[184:185], v27 offset:46080
v_mfma_f32_16x16x32_bf16 v[116:119], a[148:151], v[144:147], v[116:119]
ds_read_b64 v[188:189], v27 offset:46592
v_mfma_f32_16x16x32_bf16 v[120:123], a[152:155], v[144:147], v[120:123]
v_mfma_f32_16x16x32_bf16 v[124:127], a[156:159], v[144:147], v[124:127]
s_waitcnt lgkmcnt(6)
v_mfma_f32_16x16x32_bf16 v[112:115], a[160:163], v[132:135], v[112:115]
ds_read_b128 v[144:147], v25 offset:31744
v_mfma_f32_16x16x32_bf16 v[116:119], a[164:167], v[132:135], v[116:119]
v_mfma_f32_16x16x32_bf16 v[120:123], a[168:171], v[132:135], v[120:123]
v_mfma_f32_16x16x32_bf16 v[124:127], a[172:175], v[132:135], v[124:127]
s_waitcnt lgkmcnt(4)
v_add_f32 v176, v176, v180
v_add_f32 v177, v177, v181
v_mfma_f32_16x16x32_bf16 v[112:115], a[176:179], v[136:139], v[112:115]
s_waitcnt lgkmcnt(1)
ds_read_b128 v[132:135], v25 offset:32768
v_add_f32 v184, v184, v188
v_mfma_f32_16x16x32_bf16 v[116:119], a[180:183], v[136:139], v[116:119]
v_mfma_f32_16x16x32_bf16 v[120:123], a[184:187], v[136:139], v[120:123]
s_barrier
v_mfma_f32_16x16x32_bf16 v[124:127], a[188:191], v[136:139], v[124:127]
global_load_lds_dwordx4 v11, s[38:39] offset:-2048 sc1
v_add_f32 v185, v185, v189
v_mfma_f32_16x16x32_bf16 v[112:115], a[192:195], v[140:143], v[112:115]
s_mov_b64 s[76:77], -1
ds_read_b128 v[136:139], v25 offset:33792
v_add_f32 v176, v176, v184
v_mfma_f32_16x16x32_bf16 v[116:119], a[196:199], v[140:143], v[116:119]
v_add_f32 v177, v177, v185
v_mov_b32 v202, v176
v_mfma_f32_16x16x32_bf16 v[120:123], a[200:203], v[140:143], v[120:123]
ds_write_b128 v26, v[128:131] offset:41984
v_mfma_f32_16x16x32_bf16 v[124:127], a[204:207], v[140:143], v[124:127]
s_waitcnt lgkmcnt(3)
global_load_lds_dwordx4 v11, s[38:39] offset:-1024 sc1
v_mov_b32 v203, v177
v_mfma_f32_16x16x32_bf16 v[112:115], a[208:211], v[144:147], v[112:115]
ds_read_b128 v[140:143], v25 offset:34816
v_mfma_f32_16x16x32_bf16 v[116:119], a[212:215], v[144:147], v[116:119]
v_permlane16_swap_b32 v176, v202
v_mfma_f32_16x16x32_bf16 v[120:123], a[216:219], v[144:147], v[120:123]
global_load_lds_dwordx4 v11, s[38:39] offset:0 sc1
v_mfma_f32_16x16x32_bf16 v[124:127], a[220:223], v[144:147], v[124:127]
s_waitcnt lgkmcnt(3)
v_permlane16_swap_b32 v177, v203
v_mfma_f32_16x16x32_bf16 v[112:115], a[224:227], v[132:135], v[112:115]
ds_read_b128 v[144:147], v25 offset:35840
v_add_f32 v176, v176, v202
v_mfma_f32_16x16x32_bf16 v[116:119], a[228:231], v[132:135], v[116:119]
global_load_lds_dwordx4 v11, s[38:39] offset:1024 sc1
v_add_f32 v177, v177, v203
v_mfma_f32_16x16x32_bf16 v[120:123], a[232:235], v[132:135], v[120:123]
v_mov_b32 v202, v176
v_mov_b32 v203, v177
v_mfma_f32_16x16x32_bf16 v[124:127], a[236:239], v[132:135], v[124:127]
s_waitcnt lgkmcnt(3)
v_xor_b32 v25, 0x10000, v25
v_mfma_f32_16x16x32_bf16 v[112:115], a[240:243], v[136:139], v[112:115]
global_load_lds_dwordx4 v11, s[38:39] offset:2048 sc1
v_add_u32 v11, 18432, v11
v_mfma_f32_16x16x32_bf16 v[116:119], a[244:247], v[136:139], v[116:119]
v_mfma_f32_16x16x32_bf16 v[120:123], a[248:251], v[136:139], v[120:123]
s_waitcnt vmcnt(10) lgkmcnt(2)
s_barrier
v_mfma_f32_16x16x32_bf16 v[124:127], a[252:255], v[136:139], v[124:127]
s_waitcnt lgkmcnt(1)
ds_read_b128 v[132:135], v25 offset:0
v_min_u32 v11, v11, v12
v_mfma_f32_16x16x32_bf16 v[112:115], v[28:31], v[140:143], v[112:115]
ds_read_b128 v[136:139], v25 offset:1024
v_mfma_f32_16x16x32_bf16 v[116:119], v[32:35], v[140:143], v[116:119]
v_permlane32_swap_b32 v176, v202
v_mfma_f32_16x16x32_bf16 v[120:123], v[36:39], v[140:143], v[120:123]
v_permlane32_swap_b32 v177, v203
v_mfma_f32_16x16x32_bf16 v[124:127], v[40:43], v[140:143], v[124:127]
s_waitcnt lgkmcnt(2)
v_add_f32 v176, v176, v202
v_add_f32 v177, v177, v203
v_mfma_f32_16x16x32_bf16 v[112:115], v[44:47], v[144:147], v[112:115]
ds_read_b128 v[140:143], v25 offset:2048
v_mfma_f32_16x16x32_bf16 v[116:119], v[48:51], v[144:147], v[116:119]
v_mfma_f32_16x16x32_bf16 v[120:123], v[52:55], v[144:147], v[120:123]
v_mfma_f32_16x16x32_bf16 v[124:127], v[56:59], v[144:147], v[124:127]
s_sub_u32 s70, s70, 1
s_cbranch_scc0 .Lwo_top11
s_waitcnt vmcnt(7)
s_barrier
s_mov_b64 exec, 65535
global_store_dwordx2 v15, v[176:177], s[38:39] sc1
s_mov_b64 exec, -1
v_add_u32 v15, 18688, v15
v_cvt_pk_bf16_f32 v164, v112, v113
v_cvt_pk_bf16_f32 v165, v114, v115
v_cvt_pk_bf16_f32 v166, v116, v117
v_cvt_pk_bf16_f32 v167, v118, v119
global_store_dwordx4 v13, v[164:167], s[38:39] offset:0 sc1
v_cvt_pk_bf16_f32 v168, v120, v121
v_cvt_pk_bf16_f32 v169, v122, v123
v_cvt_pk_bf16_f32 v170, v124, v125
v_cvt_pk_bf16_f32 v171, v126, v127
global_store_dwordx4 v13, v[168:171], s[38:39] offset:1024 sc1
v_add_f32 v254, v112, v113
v_mul_f32 v255, v112, v112
v_fmac_f32 v255, v113, v113
v_add_f32 v254, v254, v114
v_fmac_f32 v255, v114, v114
v_add_f32 v254, v254, v115
v_fmac_f32 v255, v115, v115
v_add_f32 v254, v254, v116
v_fmac_f32 v255, v116, v116
v_add_f32 v254, v254, v117
v_fmac_f32 v255, v117, v117
v_add_f32 v254, v254, v118
v_fmac_f32 v255, v118, v118
v_add_f32 v254, v254, v119
v_fmac_f32 v255, v119, v119
v_add_f32 v254, v254, v120
v_fmac_f32 v255, v120, v120
v_add_f32 v254, v254, v121
v_fmac_f32 v255, v121, v121
v_add_f32 v254, v254, v122
v_fmac_f32 v255, v122, v122
v_add_f32 v254, v254, v123
v_fmac_f32 v255, v123, v123
v_add_f32 v254, v254, v124
v_fmac_f32 v255, v124, v124
v_add_f32 v254, v254, v125
v_fmac_f32 v255, v125, v125
v_add_f32 v254, v254, v126
v_fmac_f32 v255, v126, v126
v_add_f32 v254, v254, v127
v_fmac_f32 v255, v127, v127
ds_write_b64 v27, v[254:255] offset:46080
s_waitcnt vmcnt(0) lgkmcnt(0)
s_barrier
ds_read_b64 v[176:177], v27 offset:43008
ds_read_b64 v[180:181], v27 offset:43520
ds_read_b64 v[184:185], v27 offset:44032
ds_read_b64 v[188:189], v27 offset:44544
s_waitcnt lgkmcnt(2)
v_add_f32 v176, v176, v180
v_add_f32 v177, v177, v181
s_waitcnt lgkmcnt(0)
v_add_f32 v184, v184, v188
v_add_f32 v185, v185, v189
v_add_f32 v176, v176, v184
v_add_f32 v177, v177, v185
v_mov_b32 v202, v176
v_mov_b32 v203, v177
s_nop 0
v_permlane16_swap_b32 v176, v202
v_permlane16_swap_b32 v177, v203
v_add_f32 v176, v176, v202
v_add_f32 v177, v177, v203
v_mov_b32 v202, v176
v_mov_b32 v203, v177
s_nop 0
v_permlane32_swap_b32 v176, v202
v_permlane32_swap_b32 v177, v203
v_add_f32 v176, v176, v202
v_add_f32 v177, v177, v203
s_mov_b64 exec, 65535
global_store_dwordx2 v15, v[176:177], s[38:39] sc1
s_mov_b64 exec, -1
v_add_u32 v15, 18688, v15
ds_read_b64 v[176:177], v27 offset:45056
ds_read_b64 v[180:181], v27 offset:45568
ds_read_b64 v[184:185], v27 offset:46080
ds_read_b64 v[188:189], v27 offset:46592
s_waitcnt lgkmcnt(2)
v_add_f32 v176, v176, v180
v_add_f32 v177, v177, v181
s_waitcnt lgkmcnt(0)
v_add_f32 v184, v184, v188
v_add_f32 v185, v185, v189
v_add_f32 v176, v176, v184
v_add_f32 v177, v177, v185
v_mov_b32 v202, v176
v_mov_b32 v203, v177
s_nop 0
v_permlane16_swap_b32 v176, v202
v_permlane16_swap_b32 v177, v203
v_add_f32 v176, v176, v202
v_add_f32 v177, v177, v203
v_mov_b32 v202, v176
v_mov_b32 v203, v177
s_nop 0
v_permlane32_swap_b32 v176, v202
v_permlane32_swap_b32 v177, v203
v_add_f32 v176, v176, v202
v_add_f32 v177, v177, v203
s_mov_b64 exec, 65535
global_store_dwordx2 v15, v[176:177], s[38:39] sc1
s_mov_b64 exec, -1
v_add_u32 v15, 18688, v15
s_waitcnt vmcnt(0)
s_barrier
s_endpgm
.Lwo_w4:
s_add_u32 m0, s48, 4096
s_nop 0
global_load_lds_dwordx4 v0, s[40:41] offset:-4096
global_load_lds_dwordx4 v1, s[40:41] offset:-3072
global_load_lds_dwordx4 v2, s[40:41] offset:-2048
global_load_lds_dwordx4 v3, s[40:41] offset:-1024
global_load_lds_dwordx4 v4, s[40:41] offset:0
global_load_lds_dwordx4 v5, s[40:41] offset:1024
global_load_lds_dwordx4 v6, s[40:41] offset:2048
global_load_lds_dwordx4 v7, s[40:41] offset:3072
s_add_u32 m0, s48, 12288
s_nop 0
global_load_lds_dwordx4 v8, s[40:41] offset:-4096
s_add_u32 s40, s40, 73728
s_addc_u32 s41, s41, 0
s_add_u32 m0, s48, 40960
s_nop 0
global_load_lds_dwordx4 v0, s[40:41] offset:-4096
global_load_lds_dwordx4 v1, s[40:41] offset:-3072
global_load_lds_dwordx4 v2, s[40:41] offset:-2048
global_load_lds_dwordx4 v3, s[40:41] offset:-1024
global_load_lds_dwordx4 v4, s[40:41] offset:0
global_load_lds_dwordx4 v5, s[40:41] offset:1024
global_load_lds_dwordx4 v6, s[40:41] offset:2048
global_load_lds_dwordx4 v7, s[40:41] offset:3072
s_add_u32 m0, s48, 49152
s_nop 0
global_load_lds_dwordx4 v8, s[40:41] offset:-4096
s_add_u32 s40, s40, 73728
s_addc_u32 s41, s41, 0
s_add_u32 m0, s48, 77824
s_nop 0
global_load_lds_dwordx4 v0, s[40:41] offset:-4096
global_load_lds_dwordx4 v1, s[40:41] offset:-3072
global_load_lds_dwordx4 v2, s[40:41] offset:-2048
global_load_lds_dwordx4 v3, s[40:41] offset:-1024
global_load_lds_dwordx4 v4, s[40:41] offset:0
global_load_lds_dwordx4 v5, s[40:41] offset:1024
global_load_lds_dwordx4 v6, s[40:41] offset:2048
global_load_lds_dwordx4 v7, s[40:41] offset:3072
s_add_u32 m0, s48, 86016
s_nop 0
global_load_lds_dwordx4 v8, s[40:41] offset:-4096
s_add_u32 s40, s40, 73728
s_addc_u32 s41, s41, 0
v_add_u32 v204, 0x10000, v9
v_add_u32 v205, 0x20000, v9
v_add_u32 v206, 0x10000, v10
v_add_u32 v207, 0x20000, v10
s_waitcnt lgkmcnt(0)
s_mul_i32 s37, s37, 0x9E3779B1
s_add_u32 s37, s37, 0x7F4A7C15
v_add_u32 v22, s37, v22
v_add_u32 v24, s37, v24
s_waitcnt vmcnt(18)
s_barrier
s_add_u32 m0, s48, 114688
ds_read_b64_tr_b16 a[0:1], v9 offset:0
ds_read_b64_tr_b16 a[2:3], v9 offset:2304
global_load_lds_dwordx4 v0, s[40:41] offset:-4096
ds_read_b64_tr_b16 a[4:5], v9 offset:8
global_load_lds_dwordx4 v1, s[40:41] offset:-3072
ds_read_b64_tr_b16 a[6:7], v9 offset:2312
global_load_lds_dwordx4 v2, s[40:41] offset:-2048
ds_read_b64_tr_b16 a[8:9], v9 offset:64
global_load_lds_dwordx4 v3, s[40:41] offset:-1024
ds_read_b64_tr_b16 a[10:11], v9 offset:2368
ds_read_b64_tr_b16 a[12:13], v9 offset:72
global_load_lds_dwordx4 v4, s[40:41] offset:0
ds_read_b64_tr_b16 a[14:15], v9 offset:2376
global_load_lds_dwordx4 v5, s[40:41] offset:1024
ds_read_b64_tr_b16 a[16:17], v9 offset:18432
global_load_lds_dwordx4 v6, s[40:41] offset:2048
ds_read_b64_tr_b16 a[18:19], v9 offset:20736
)"
R"(global_load_lds_dwordx4 v7, s[40:41] offset:3072
ds_read_b64_tr_b16 a[20:21], v9 offset:18440
ds_read_b64_tr_b16 a[22:23], v9 offset:20744
s_add_u32 m0, s48, 122880
ds_read_b64_tr_b16 a[24:25], v9 offset:18496
global_load_lds_dwordx4 v8, s[40:41] offset:-4096
ds_read_b64_tr_b16 a[26:27], v9 offset:20800
s_add_u32 s40, s40, 73728
ds_read_b64_tr_b16 a[28:29], v9 offset:18504
s_addc_u32 s41, s41, 0
ds_read_b64_tr_b16 a[30:31], v9 offset:20808
s_waitcnt vmcnt(18) lgkmcnt(0)
s_barrier
s_add_u32 m0, s48, 4096
ds_read_b64_tr_b16 a[32:33], v9 offset:36864
ds_read_b64_tr_b16 a[34:35], v9 offset:39168
global_load_lds_dwordx4 v0, s[40:41] offset:-4096
ds_read_b64_tr_b16 a[36:37], v9 offset:36872
global_load_lds_dwordx4 v1, s[40:41] offset:-3072
ds_read_b64_tr_b16 a[38:39], v9 offset:39176
global_load_lds_dwordx4 v2, s[40:41] offset:-2048
ds_read_b64_tr_b16 a[40:41], v9 offset:36928
global_load_lds_dwordx4 v3, s[40:41] offset:-1024
ds_read_b64_tr_b16 a[42:43], v9 offset:39232
ds_read_b64_tr_b16 a[44:45], v9 offset:36936
global_load_lds_dwordx4 v4, s[40:41] offset:0
ds_read_b64_tr_b16 a[46:47], v9 offset:39240
global_load_lds_dwordx4 v5, s[40:41] offset:1024
ds_read_b64_tr_b16 a[48:49], v9 offset:55296
global_load_lds_dwordx4 v6, s[40:41] offset:2048
ds_read_b64_tr_b16 a[50:51], v9 offset:57600
global_load_lds_dwordx4 v7, s[40:41] offset:3072
ds_read_b64_tr_b16 a[52:53], v9 offset:55304
ds_read_b64_tr_b16 a[54:55], v9 offset:57608
s_add_u32 m0, s48, 12288
ds_read_b64_tr_b16 a[56:57], v9 offset:55360
global_load_lds_dwordx4 v8, s[40:41] offset:-4096
ds_read_b64_tr_b16 a[58:59], v9 offset:57664
s_add_u32 s40, s40, 73728
ds_read_b64_tr_b16 a[60:61], v9 offset:55368
s_addc_u32 s41, s41, 0
ds_read_b64_tr_b16 a[62:63], v9 offset:57672
s_waitcnt vmcnt(18) lgkmcnt(0)
s_barrier
s_add_u32 m0, s48, 40960
ds_read_b64_tr_b16 a[64:65], v204 offset:8192
ds_read_b64_tr_b16 a[66:67], v204 offset:10496
global_load_lds_dwordx4 v0, s[40:41] offset:-4096
ds_read_b64_tr_b16 a[68:69], v204 offset:8200
global_load_lds_dwordx4 v1, s[40:41] offset:-3072
ds_read_b64_tr_b16 a[70:71], v204 offset:10504
global_load_lds_dwordx4 v2, s[40:41] offset:-2048
ds_read_b64_tr_b16 a[72:73], v204 offset:8256
global_load_lds_dwordx4 v3, s[40:41] offset:-1024
ds_read_b64_tr_b16 a[74:75], v204 offset:10560
ds_read_b64_tr_b16 a[76:77], v204 offset:8264
global_load_lds_dwordx4 v4, s[40:41] offset:0
ds_read_b64_tr_b16 a[78:79], v204 offset:10568
global_load_lds_dwordx4 v5, s[40:41] offset:1024
ds_read_b64_tr_b16 a[80:81], v204 offset:26624
global_load_lds_dwordx4 v6, s[40:41] offset:2048
ds_read_b64_tr_b16 a[82:83], v204 offset:28928
global_load_lds_dwordx4 v7, s[40:41] offset:3072
ds_read_b64_tr_b16 a[84:85], v204 offset:26632
ds_read_b64_tr_b16 a[86:87], v204 offset:28936
s_add_u32 m0, s48, 49152
ds_read_b64_tr_b16 a[88:89], v204 offset:26688
global_load_lds_dwordx4 v8, s[40:41] offset:-4096
ds_read_b64_tr_b16 a[90:91], v204 offset:28992
s_add_u32 s40, s40, 73728
ds_read_b64_tr_b16 a[92:93], v204 offset:26696
s_addc_u32 s41, s41, 0
ds_read_b64_tr_b16 a[94:95], v204 offset:29000
s_waitcnt vmcnt(18) lgkmcnt(0)
s_barrier
s_add_u32 m0, s48, 77824
ds_read_b64_tr_b16 a[96:97], v204 offset:45056
ds_read_b64_tr_b16 a[98:99], v204 offset:47360
global_load_lds_dwordx4 v0, s[40:41] offset:-4096
ds_read_b64_tr_b16 a[100:101], v204 offset:45064
global_load_lds_dwordx4 v1, s[40:41] offset:-3072
ds_read_b64_tr_b16 a[102:103], v204 offset:47368
global_load_lds_dwordx4 v2, s[40:41] offset:-2048
ds_read_b64_tr_b16 a[104:105], v204 offset:45120
global_load_lds_dwordx4 v3, s[40:41] offset:-1024
ds_read_b64_tr_b16 a[106:107], v204 offset:47424
ds_read_b64_tr_b16 a[108:109], v204 offset:45128
global_load_lds_dwordx4 v4, s[40:41] offset:0
ds_read_b64_tr_b16 a[110:111], v204 offset:47432
global_load_lds_dwordx4 v5, s[40:41] offset:1024
ds_read_b64_tr_b16 a[112:113], v204 offset:63488
global_load_lds_dwordx4 v6, s[40:41] offset:2048
ds_read_b64_tr_b16 a[114:115], v205 offset:256
global_load_lds_dwordx4 v7, s[40:41] offset:3072
ds_read_b64_tr_b16 a[116:117], v204 offset:63496
ds_read_b64_tr_b16 a[118:119], v205 offset:264
s_add_u32 m0, s48, 86016
ds_read_b64_tr_b16 a[120:121], v204 offset:63552
global_load_lds_dwordx4 v8, s[40:41] offset:-4096
ds_read_b64_tr_b16 a[122:123], v205 offset:320
s_add_u32 s40, s40, 73728
ds_read_b64_tr_b16 a[124:125], v204 offset:63560
s_addc_u32 s41, s41, 0
ds_read_b64_tr_b16 a[126:127], v205 offset:328
s_waitcnt vmcnt(18) lgkmcnt(0)
s_barrier
s_add_u32 m0, s48, 114688
ds_read_b64_tr_b16 a[128:129], v9 offset:0
ds_read_b64_tr_b16 a[130:131], v9 offset:2304
global_load_lds_dwordx4 v0, s[40:41] offset:-4096
ds_read_b64_tr_b16 a[132:133], v9 offset:8
global_load_lds_dwordx4 v1, s[40:41] offset:-3072
ds_read_b64_tr_b16 a[134:135], v9 offset:2312
ds_read_b64_tr_b16 a[136:137], v9 offset:64
global_load_lds_dwordx4 v2, s[40:41] offset:-2048
ds_read_b64_tr_b16 a[138:139], v9 offset:2368
global_load_lds_dwordx4 v3, s[40:41] offset:-1024
ds_read_b64_tr_b16 a[140:141], v9 offset:72
global_load_lds_dwordx4 v4, s[40:41] offset:0
ds_read_b64_tr_b16 a[142:143], v9 offset:2376
ds_read_b64_tr_b16 a[144:145], v9 offset:18432
global_load_lds_dwordx4 v5, s[40:41] offset:1024
ds_read_b64_tr_b16 a[146:147], v9 offset:20736
global_load_lds_dwordx4 v6, s[40:41] offset:2048
ds_read_b64_tr_b16 a[148:149], v9 offset:18440
ds_read_b64_tr_b16 a[150:151], v9 offset:20744
global_load_lds_dwordx4 v7, s[40:41] offset:3072
ds_read_b64_tr_b16 a[152:153], v9 offset:18496
s_add_u32 m0, s48, 122880
ds_read_b64_tr_b16 a[154:155], v9 offset:20800
global_load_lds_dwordx4 v8, s[40:41] offset:-4096
ds_read_b64_tr_b16 a[156:157], v9 offset:18504
ds_read_b64_tr_b16 a[158:159], v9 offset:20808
s_add_u32 s40, s40, 73728
ds_read_b64_tr_b16 v[60:61], v10 offset:18432
s_addc_u32 s41, s41, 0
ds_read_b64_tr_b16 v[62:63], v10 offset:20736
s_waitcnt vmcnt(18) lgkmcnt(0)
s_barrier
s_add_u32 m0, s48, 4096
ds_read_b64_tr_b16 a[160:161], v9 offset:36864
ds_read_b64_tr_b16 a[162:163], v9 offset:39168
global_load_lds_dwordx4 v0, s[40:41] offset:-4096
ds_read_b64_tr_b16 a[164:165], v9 offset:36872
ds_read_b64_tr_b16 a[166:167], v9 offset:39176
global_load_lds_dwordx4 v1, s[40:41] offset:-3072
ds_read_b64_tr_b16 a[168:169], v9 offset:36928
global_load_lds_dwordx4 v2, s[40:41] offset:-2048
ds_read_b64_tr_b16 a[170:171], v9 offset:39232
ds_read_b64_tr_b16 a[172:173], v9 offset:36936
global_load_lds_dwordx4 v3, s[40:41] offset:-1024
ds_read_b64_tr_b16 a[174:175], v9 offset:39240
global_load_lds_dwordx4 v4, s[40:41] offset:0
ds_read_b64_tr_b16 v[64:65], v10 offset:36864
ds_read_b64_tr_b16 v[66:67], v10 offset:39168
global_load_lds_dwordx4 v5, s[40:41] offset:1024
ds_read_b64_tr_b16 a[176:177], v9 offset:55296
global_load_lds_dwordx4 v6, s[40:41] offset:2048
ds_read_b64_tr_b16 a[178:179], v9 offset:57600
ds_read_b64_tr_b16 a[180:181], v9 offset:55304
global_load_lds_dwordx4 v7, s[40:41] offset:3072
ds_read_b64_tr_b16 a[182:183], v9 offset:57608
s_add_u32 m0, s48, 12288
ds_read_b64_tr_b16 a[184:185], v9 offset:55360
ds_read_b64_tr_b16 a[186:187], v9 offset:57664
global_load_lds_dwordx4 v8, s[40:41] offset:-4096
ds_read_b64_tr_b16 a[188:189], v9 offset:55368
s_sub_u32 s40, s40, 589824
ds_read_b64_tr_b16 a[190:191], v9 offset:57672
ds_read_b64_tr_b16 v[68:69], v10 offset:55296
s_subb_u32 s41, s41, 0
ds_read_b64_tr_b16 v[70:71], v10 offset:57600
s_waitcnt vmcnt(18) lgkmcnt(0)
s_barrier
ds_read_b64_tr_b16 a[192:193], v204 offset:8192
ds_read_b64_tr_b16 a[194:195], v204 offset:10496
ds_read_b64_tr_b16 a[196:197], v204 offset:8200
ds_read_b64_tr_b16 a[198:199], v204 offset:10504
ds_read_b64_tr_b16 a[200:201], v204 offset:8256
ds_read_b64_tr_b16 a[202:203], v204 offset:10560
ds_read_b64_tr_b16 a[204:205], v204 offset:8264
ds_read_b64_tr_b16 a[206:207], v204 offset:10568
ds_read_b64_tr_b16 v[72:73], v206 offset:8192
ds_read_b64_tr_b16 v[74:75], v206 offset:10496
ds_read_b64_tr_b16 a[208:209], v204 offset:26624
ds_read_b64_tr_b16 a[210:211], v204 offset:28928
ds_read_b64_tr_b16 a[212:213], v204 offset:26632
ds_read_b64_tr_b16 a[214:215], v204 offset:28936
ds_read_b64_tr_b16 a[216:217], v204 offset:26688
ds_read_b64_tr_b16 a[218:219], v204 offset:28992
ds_read_b64_tr_b16 a[220:221], v204 offset:26696
ds_read_b64_tr_b16 a[222:223], v204 offset:29000
ds_read_b64_tr_b16 v[76:77], v206 offset:26624
ds_read_b64_tr_b16 v[78:79], v206 offset:28928
s_waitcnt vmcnt(9) lgkmcnt(0)
s_barrier
ds_read_b64_tr_b16 a[224:225], v204 offset:45056
ds_read_b64_tr_b16 a[226:227], v204 offset:47360
ds_read_b64_tr_b16 a[228:229], v204 offset:45064
ds_read_b64_tr_b16 a[230:231], v204 offset:47368
ds_read_b64_tr_b16 a[232:233], v204 offset:45120
ds_read_b64_tr_b16 a[234:235], v204 offset:47424
ds_read_b64_tr_b16 a[236:237], v204 offset:45128
ds_read_b64_tr_b16 a[238:239], v204 offset:47432
ds_read_b64_tr_b16 v[80:81], v206 offset:45056
ds_read_b64_tr_b16 v[82:83], v206 offset:47360
ds_read_b64_tr_b16 a[240:241], v204 offset:63488
ds_read_b64_tr_b16 a[242:243], v205 offset:256
ds_read_b64_tr_b16 a[244:245], v204 offset:63496
ds_read_b64_tr_b16 a[246:247], v205 offset:264
ds_read_b64_tr_b16 a[248:249], v204 offset:63552
ds_read_b64_tr_b16 a[250:251], v205 offset:320
ds_read_b64_tr_b16 a[252:253], v204 offset:63560
ds_read_b64_tr_b16 a[254:255], v205 offset:328
ds_read_b64_tr_b16 v[84:85], v206 offset:63488
ds_read_b64_tr_b16 v[86:87], v207 offset:256
s_waitcnt vmcnt(0) lgkmcnt(0)
s_barrier
ds_read_b64_tr_b16 v[28:29], v9 offset:0
ds_read_b64_tr_b16 v[30:31], v9 offset:2304
ds_read_b64_tr_b16 v[32:33], v9 offset:8
ds_read_b64_tr_b16 v[34:35], v9 offset:2312
ds_read_b64_tr_b16 v[36:37], v9 offset:64
ds_read_b64_tr_b16 v[38:39], v9 offset:2368
ds_read_b64_tr_b16 v[40:41], v9 offset:72
ds_read_b64_tr_b16 v[42:43], v9 offset:2376
ds_read_b64_tr_b16 v[88:89], v10 offset:0
ds_read_b64_tr_b16 v[90:91], v10 offset:2304
ds_read_b64_tr_b16 v[44:45], v9 offset:18432
ds_read_b64_tr_b16 v[46:47], v9 offset:20736
ds_read_b64_tr_b16 v[48:49], v9 offset:18440
ds_read_b64_tr_b16 v[50:51], v9 offset:20744
ds_read_b64_tr_b16 v[52:53], v9 offset:18496
ds_read_b64_tr_b16 v[54:55], v9 offset:20800
ds_read_b64_tr_b16 v[56:57], v9 offset:18504
ds_read_b64_tr_b16 v[58:59], v9 offset:20808
ds_read_b64_tr_b16 v[92:93], v10 offset:18432
ds_read_b64_tr_b16 v[94:95], v10 offset:20736
s_waitcnt lgkmcnt(0)
s_barrier
v_add_lshl_u32 v252, v24, 0, 7
v_bfi_b32 v252, s69, v252, v23
v_add_lshl_u32 v253, v22, 0, 7
v_bfi_b32 v253, s69, v253, v21
s_mov_b64 s[76:77], 0
s_add_u32 m0, s49, 18432
s_mov_b32 s70, 33
s_waitcnt vmcnt(0) lgkmcnt(0)
s_nop 7
.p2align 6
.Lwo_top12:
s_waitcnt lgkmcnt(5)
v_mfma_f32_16x16x32_bf16 v[96:99], a[144:147], v[132:135], v[96:99]
s_waitcnt lgkmcnt(4)
ds_read_b128 v[144:147], v25 offset:12288
v_add_f32 v176, v176, v184
v_mfma_f32_16x16x32_bf16 v[100:103], a[148:151], v[132:135], v[100:103]
v_mfma_f32_16x16x32_bf16 v[104:107], a[152:155], v[132:135], v[104:107]
s_waitcnt vmcnt(6)
s_barrier
v_mfma_f32_16x16x32_bf16 v[108:111], a[156:159], v[132:135], v[108:111]
v_add_f32 v177, v177, v185
v_add_f32 v178, v178, v186
v_mfma_f32_16x16x32_bf16 v[128:131], v[60:63], v[132:135], 0
s_waitcnt lgkmcnt(3)
v_add_f32 v179, v179, v187
v_add_f32 v176, v176, v188
v_mfma_f32_16x16x32_bf16 v[96:99], a[160:163], v[136:139], v[96:99]
ds_read_b128 v[132:135], v25 offset:13312
v_add_f32 v177, v177, v189
v_mfma_f32_16x16x32_bf16 v[100:103], a[164:167], v[136:139], v[100:103]
v_add_f32 v178, v178, v190
v_add_f32 v179, v179, v191
v_mfma_f32_16x16x32_bf16 v[104:107], a[168:171], v[136:139], v[104:107]
s_waitcnt lgkmcnt(3)
v_add_f32 v180, v180, v192
v_add_f32 v181, v181, v193
v_mfma_f32_16x16x32_bf16 v[108:111], a[172:175], v[136:139], v[108:111]
v_add_f32 v182, v182, v194
v_add_f32 v183, v183, v195
v_mfma_f32_16x16x32_bf16 v[128:131], v[64:67], v[136:139], v[128:131]
s_waitcnt lgkmcnt(2)
v_cvt_pk_bf16_f32 v172, v176, v177
v_cvt_pk_bf16_f32 v173, v178, v179
v_mfma_f32_16x16x32_bf16 v[96:99], a[176:179], v[140:143], v[96:99]
s_cmp_eq_u32 s70, 32
ds_read_b128 v[136:139], v25 offset:14336
v_cvt_pk_bf16_f32 v174, v180, v181
v_mfma_f32_16x16x32_bf16 v[100:103], a[180:183], v[140:143], v[100:103]
s_cselect_b64 s[42:43], s[44:45], s[42:43]
v_cvt_pk_bf16_f32 v175, v182, v183
v_cvt_pk_bf16_f32 v164, v112, v113
v_mfma_f32_16x16x32_bf16 v[104:107], a[184:187], v[140:143], v[104:107]
s_cmp_eq_u32 s70, 16
global_store_dwordx4 v14, v[172:175], s[38:39] sc1
v_mfma_f32_16x16x32_bf16 v[108:111], a[188:191], v[140:143], v[108:111]
s_cselect_b64 s[42:43], s[46:47], s[42:43]
v_add_u32 v14, 18688, v14
v_cvt_pk_bf16_f32 v165, v114, v115
v_mfma_f32_16x16x32_bf16 v[128:131], v[68:71], v[140:143], v[128:131]
s_waitcnt lgkmcnt(2)
v_cvt_pk_bf16_f32 v166, v116, v117
v_cvt_pk_bf16_f32 v167, v118, v119
v_mfma_f32_16x16x32_bf16 v[96:99], a[192:195], v[144:147], v[96:99]
s_xor_b32 s49, s49, 0x10000
ds_read_b128 v[140:143], v25 offset:15360
v_add_f32 v254, v112, v113
v_mfma_f32_16x16x32_bf16 v[100:103], a[196:199], v[144:147], v[100:103]
s_mov_b32 m0, 53248
global_store_dwordx4 v13, v[164:167], s[38:39] offset:0 sc1
v_mfma_f32_16x16x32_bf16 v[104:107], a[200:203], v[144:147], v[104:107]
v_cvt_pk_bf16_f32 v168, v120, v121
v_cvt_pk_bf16_f32 v169, v122, v123
v_mfma_f32_16x16x32_bf16 v[108:111], a[204:207], v[144:147], v[108:111]
v_cvt_pk_bf16_f32 v170, v124, v125
v_cvt_pk_bf16_f32 v171, v126, v127
v_mfma_f32_16x16x32_bf16 v[128:131], v[72:75], v[144:147], v[128:131]
s_waitcnt lgkmcnt(2)
global_store_dwordx4 v13, v[168:171], s[38:39] offset:1024 sc1
v_mfma_f32_16x16x32_bf16 v[96:99], a[208:211], v[132:135], v[96:99]
ds_read_b128 v[144:147], v25 offset:16384
v_add_u32 v13, 18688, v13
v_mfma_f32_16x16x32_bf16 v[100:103], a[212:215], v[132:135], v[100:103]
v_mul_f32 v255, v112, v112
v_fmac_f32 v255, v113, v113
v_mfma_f32_16x16x32_bf16 v[104:107], a[216:219], v[132:135], v[104:107]
v_add_f32 v254, v254, v114
v_fmac_f32 v255, v114, v114
v_mfma_f32_16x16x32_bf16 v[108:111], a[220:223], v[132:135], v[108:111]
v_add_f32 v254, v254, v115
v_fmac_f32 v255, v115, v115
v_mfma_f32_16x16x32_bf16 v[128:131], v[76:79], v[132:135], v[128:131]
s_waitcnt vmcnt(7)
global_load_lds_dwordx4 v16, s[42:43] offset:0 sc1
ds_read_b128 v[148:151], v19 offset:8192
s_waitcnt lgkmcnt(3)
v_mfma_f32_16x16x32_bf16 v[96:99], a[224:227], v[136:139], v[96:99]
ds_read_b128 v[132:135], v25 offset:17408
ds_read_b128 v[152:155], v20 offset:8192
v_mfma_f32_16x16x32_bf16 v[100:103], a[228:231], v[136:139], v[100:103]
v_add_f32 v254, v254, v116
v_fmac_f32 v255, v116, v116
v_mfma_f32_16x16x32_bf16 v[104:107], a[232:235], v[136:139], v[104:107]
v_add_f32 v254, v254, v117
v_fmac_f32 v255, v117, v117
v_mfma_f32_16x16x32_bf16 v[108:111], a[236:239], v[136:139], v[108:111]
v_add_f32 v254, v254, v118
v_fmac_f32 v255, v118, v118
v_mfma_f32_16x16x32_bf16 v[128:131], v[80:83], v[136:139], v[128:131]
s_waitcnt lgkmcnt(4)
v_add_f32 v254, v254, v119
v_fmac_f32 v255, v119, v119
v_mfma_f32_16x16x32_bf16 v[96:99], a[240:243], v[140:143], v[96:99]
ds_read_b128 v[136:139], v25 offset:0
v_add_f32 v254, v254, v120
v_mfma_f32_16x16x32_bf16 v[100:103], a[244:247], v[140:143], v[100:103]
global_load_lds_dwordx4 v17, s[42:43] offset:1024 sc1
v_fmac_f32 v255, v120, v120
v_mfma_f32_16x16x32_bf16 v[104:107], a[248:251], v[140:143], v[104:107]
v_add_f32 v254, v254, v121
v_fmac_f32 v255, v121, v121
v_mfma_f32_16x16x32_bf16 v[108:111], a[252:255], v[140:143], v[108:111]
v_add_f32 v254, v254, v122
v_fmac_f32 v255, v122, v122
v_mfma_f32_16x16x32_bf16 v[128:131], v[84:87], v[140:143], v[128:131]
s_waitcnt lgkmcnt(4)
v_add_f32 v254, v254, v123
v_fmac_f32 v255, v123, v123
v_mfma_f32_16x16x32_bf16 v[96:99], v[28:31], v[144:147], v[96:99]
ds_read_b128 v[140:143], v25 offset:1024
v_add_f32 v254, v254, v124
v_mfma_f32_16x16x32_bf16 v[100:103], v[32:35], v[144:147], v[100:103]
s_add_u32 m0, s49, 0
v_fmac_f32 v255, v124, v124
v_add_f32 v254, v254, v125
v_mfma_f32_16x16x32_bf16 v[104:107], v[36:39], v[144:147], v[104:107]
v_fmac_f32 v255, v125, v125
v_add_f32 v254, v254, v126
v_mfma_f32_16x16x32_bf16 v[108:111], v[40:43], v[144:147], v[108:111]
v_fmac_f32 v255, v126, v126
v_add_f32 v254, v254, v127
v_mfma_f32_16x16x32_bf16 v[128:131], v[88:91], v[144:147], v[128:131]
s_waitcnt lgkmcnt(3)
v_fmac_f32 v255, v127, v127
v_add_f32 v254, v254, v176
v_mfma_f32_16x16x32_bf16 v[96:99], v[44:47], v[132:135], v[96:99]
ds_read_b128 v[144:147], v25 offset:2048
v_fmac_f32 v255, v176, v176
v_mfma_f32_16x16x32_bf16 v[100:103], v[48:51], v[132:135], v[100:103]
v_add_f32 v254, v254, v177
v_fmac_f32 v255, v177, v177
v_mfma_f32_16x16x32_bf16 v[104:107], v[52:55], v[132:135], v[104:107]
v_add_f32 v254, v254, v178
v_fmac_f32 v255, v178, v178
v_mfma_f32_16x16x32_bf16 v[108:111], v[56:59], v[132:135], v[108:111]
v_add_f32 v254, v254, v179
v_fmac_f32 v255, v179, v179
v_mfma_f32_16x16x32_bf16 v[128:131], v[92:95], v[132:135], v[128:131]
s_waitcnt lgkmcnt(2)
v_add_f32 v254, v254, v180
)"
R"(v_fmac_f32 v255, v180, v180
v_mfma_f32_16x16x32_bf16 v[96:99], a[0:3], v[136:139], v[96:99]
ds_read_b128 v[132:135], v25 offset:3072
v_add_f32 v254, v254, v181
v_mfma_f32_16x16x32_bf16 v[100:103], a[4:7], v[136:139], v[100:103]
v_fmac_f32 v255, v181, v181
v_add_f32 v254, v254, v182
v_mfma_f32_16x16x32_bf16 v[104:107], a[8:11], v[136:139], v[104:107]
v_fmac_f32 v255, v182, v182
v_add_f32 v254, v254, v183
v_mfma_f32_16x16x32_bf16 v[108:111], a[12:15], v[136:139], v[108:111]
s_waitcnt lgkmcnt(2)
v_fmac_f32 v255, v183, v183
v_mfma_f32_16x16x32_bf16 v[96:99], a[16:19], v[140:143], v[96:99]
ds_read_b128 v[136:139], v25 offset:4096
v_mfma_f32_16x16x32_bf16 v[100:103], a[20:23], v[140:143], v[100:103]
ds_write_b64 v27, v[254:255] offset:46592
v_mfma_f32_16x16x32_bf16 v[104:107], a[24:27], v[140:143], v[104:107]
v_lshlrev_b32 v176, 16, v156
v_and_b32 v177, 0xffff0000, v156
v_mfma_f32_16x16x32_bf16 v[108:111], a[28:31], v[140:143], v[108:111]
s_waitcnt lgkmcnt(3)
v_lshlrev_b32 v178, 16, v157
v_and_b32 v179, 0xffff0000, v157
v_mfma_f32_16x16x32_bf16 v[96:99], a[32:35], v[144:147], v[96:99]
ds_read_b128 v[140:143], v25 offset:5120
v_lshlrev_b32 v180, 16, v158
v_mfma_f32_16x16x32_bf16 v[100:103], a[36:39], v[144:147], v[100:103]
v_mfma_f32_16x16x32_bf16 v[104:107], a[40:43], v[144:147], v[104:107]
s_barrier
v_mfma_f32_16x16x32_bf16 v[108:111], a[44:47], v[144:147], v[108:111]
s_waitcnt lgkmcnt(3)
v_and_b32 v181, 0xffff0000, v158
v_lshlrev_b32 v182, 16, v159
v_mfma_f32_16x16x32_bf16 v[96:99], a[48:51], v[132:135], v[96:99]
ds_read_b128 v[144:147], v25 offset:6144
global_load_lds_dwordx4 v11, s[38:39] offset:-2048 sc1
v_mfma_f32_16x16x32_bf16 v[100:103], a[52:55], v[132:135], v[100:103]
v_and_b32 v183, 0xffff0000, v159
v_add_f32 v180, v180, v128
v_mfma_f32_16x16x32_bf16 v[104:107], a[56:59], v[132:135], v[104:107]
v_add_f32 v181, v181, v129
v_add_f32 v182, v182, v130
v_mfma_f32_16x16x32_bf16 v[108:111], a[60:63], v[132:135], v[108:111]
s_waitcnt lgkmcnt(3)
global_load_lds_dwordx4 v11, s[38:39] offset:-1024 sc1
v_add_f32 v183, v183, v131
v_mfma_f32_16x16x32_bf16 v[96:99], a[64:67], v[136:139], v[96:99]
ds_read_b128 v[132:135], v25 offset:7168
global_load_dwordx4 v[156:159], v18, s[42:43] sc1
v_mfma_f32_16x16x32_bf16 v[100:103], a[68:71], v[136:139], v[100:103]
s_sub_u32 s42, s42, 18432
v_lshlrev_b32 v112, 16, v148
v_and_b32 v113, 0xffff0000, v148
v_mfma_f32_16x16x32_bf16 v[104:107], a[72:75], v[136:139], v[104:107]
s_subb_u32 s43, s43, 0
global_load_lds_dwordx4 v11, s[38:39] offset:0 sc1
v_add_u32 v11, 18432, v11
v_mfma_f32_16x16x32_bf16 v[108:111], a[76:79], v[136:139], v[108:111]
s_waitcnt lgkmcnt(2)
v_min_u32 v11, v11, v12
v_lshlrev_b32 v114, 16, v149
v_mfma_f32_16x16x32_bf16 v[96:99], a[80:83], v[140:143], v[96:99]
ds_read_b128 v[136:139], v25 offset:8192
v_and_b32 v115, 0xffff0000, v149
v_mfma_f32_16x16x32_bf16 v[100:103], a[84:87], v[140:143], v[100:103]
v_lshlrev_b32 v116, 16, v150
v_and_b32 v117, 0xffff0000, v150
v_mfma_f32_16x16x32_bf16 v[104:107], a[88:91], v[140:143], v[104:107]
v_lshlrev_b32 v118, 16, v151
v_and_b32 v119, 0xffff0000, v151
v_mfma_f32_16x16x32_bf16 v[108:111], a[92:95], v[140:143], v[108:111]
s_waitcnt lgkmcnt(2)
v_lshlrev_b32 v120, 16, v152
v_and_b32 v121, 0xffff0000, v152
v_mfma_f32_16x16x32_bf16 v[96:99], a[96:99], v[144:147], v[96:99]
v_lshlrev_b32 v122, 16, v153
v_and_b32 v123, 0xffff0000, v153
v_mfma_f32_16x16x32_bf16 v[100:103], a[100:103], v[144:147], v[100:103]
v_mfma_f32_16x16x32_bf16 v[104:107], a[104:107], v[144:147], v[104:107]
s_waitcnt vmcnt(9)
s_barrier
v_mfma_f32_16x16x32_bf16 v[108:111], a[108:111], v[144:147], v[108:111]
s_waitcnt lgkmcnt(1)
ds_read_b128 v[140:143], v25 offset:27648
ds_read_b128 v[184:187], v26 offset:36864
v_mfma_f32_16x16x32_bf16 v[96:99], a[112:115], v[132:135], v[96:99]
ds_read_b128 v[144:147], v25 offset:28672
ds_read_b128 v[188:191], v26 offset:37888
v_mfma_f32_16x16x32_bf16 v[100:103], a[116:119], v[132:135], v[100:103]
ds_read_b128 v[192:195], v26 offset:38912
v_lshlrev_b32 v124, 16, v154
v_mfma_f32_16x16x32_bf16 v[104:107], a[120:123], v[132:135], v[104:107]
v_and_b32 v125, 0xffff0000, v154
v_lshlrev_b32 v126, 16, v155
v_mfma_f32_16x16x32_bf16 v[108:111], a[124:127], v[132:135], v[108:111]
s_waitcnt lgkmcnt(5)
v_and_b32 v127, 0xffff0000, v155
v_mfma_f32_16x16x32_bf16 v[96:99], a[128:131], v[136:139], v[96:99]
ds_read_b128 v[132:135], v25 offset:29696
v_mfma_f32_16x16x32_bf16 v[100:103], a[132:135], v[136:139], v[100:103]
v_mfma_f32_16x16x32_bf16 v[104:107], a[136:139], v[136:139], v[104:107]
s_waitcnt lgkmcnt(4)
v_add_f32 v176, v176, v184
v_add_f32 v177, v177, v185
v_mfma_f32_16x16x32_bf16 v[108:111], a[140:143], v[136:139], v[108:111]
v_add_f32 v178, v178, v186
v_add_f32 v179, v179, v187
v_mfma_f32_16x16x32_bf16 v[112:115], a[144:147], v[140:143], v[112:115]
s_waitcnt lgkmcnt(2)
ds_read_b128 v[136:139], v25 offset:30720
v_add_f32 v176, v176, v188
v_mfma_f32_16x16x32_bf16 v[116:119], a[148:151], v[140:143], v[116:119]
v_mfma_f32_16x16x32_bf16 v[120:123], a[152:155], v[140:143], v[120:123]
s_waitcnt vmcnt(6)
s_barrier
v_mfma_f32_16x16x32_bf16 v[124:127], a[156:159], v[140:143], v[124:127]
v_add_f32 v177, v177, v189
v_add_f32 v178, v178, v190
v_mfma_f32_16x16x32_bf16 v[128:131], v[60:63], v[140:143], 0
s_waitcnt lgkmcnt(2)
v_add_f32 v179, v179, v191
v_add_f32 v180, v180, v192
v_mfma_f32_16x16x32_bf16 v[112:115], a[160:163], v[144:147], v[112:115]
ds_read_b128 v[140:143], v25 offset:31744
v_add_f32 v181, v181, v193
v_mfma_f32_16x16x32_bf16 v[116:119], a[164:167], v[144:147], v[116:119]
v_add_f32 v182, v182, v194
v_add_f32 v183, v183, v195
v_mfma_f32_16x16x32_bf16 v[120:123], a[168:171], v[144:147], v[120:123]
v_cvt_pk_bf16_f32 v172, v176, v177
v_cvt_pk_bf16_f32 v173, v178, v179
v_mfma_f32_16x16x32_bf16 v[124:127], a[172:175], v[144:147], v[124:127]
v_cvt_pk_bf16_f32 v174, v180, v181
v_cvt_pk_bf16_f32 v175, v182, v183
v_mfma_f32_16x16x32_bf16 v[128:131], v[64:67], v[144:147], v[128:131]
s_waitcnt lgkmcnt(2)
global_store_dwordx4 v14, v[172:175], s[38:39] sc1
v_mfma_f32_16x16x32_bf16 v[112:115], a[176:179], v[132:135], v[112:115]
s_mov_b32 m0, 61440
ds_read_b128 v[144:147], v25 offset:32768
v_add_u32 v14, 18688, v14
v_mfma_f32_16x16x32_bf16 v[116:119], a[180:183], v[132:135], v[116:119]
v_cvt_pk_bf16_f32 v164, v96, v97
v_cvt_pk_bf16_f32 v165, v98, v99
v_mfma_f32_16x16x32_bf16 v[120:123], a[184:187], v[132:135], v[120:123]
v_cvt_pk_bf16_f32 v166, v100, v101
v_cvt_pk_bf16_f32 v167, v102, v103
v_mfma_f32_16x16x32_bf16 v[124:127], a[188:191], v[132:135], v[124:127]
global_store_dwordx4 v13, v[164:167], s[38:39] offset:0 sc1
v_mfma_f32_16x16x32_bf16 v[128:131], v[68:71], v[132:135], v[128:131]
s_waitcnt lgkmcnt(2)
v_cvt_pk_bf16_f32 v168, v104, v105
v_cvt_pk_bf16_f32 v169, v106, v107
v_mfma_f32_16x16x32_bf16 v[112:115], a[192:195], v[136:139], v[112:115]
ds_read_b128 v[132:135], v25 offset:33792
v_cvt_pk_bf16_f32 v170, v108, v109
v_mfma_f32_16x16x32_bf16 v[116:119], a[196:199], v[136:139], v[116:119]
v_cvt_pk_bf16_f32 v171, v110, v111
v_add_f32 v254, v96, v97
v_mfma_f32_16x16x32_bf16 v[120:123], a[200:203], v[136:139], v[120:123]
global_store_dwordx4 v13, v[168:171], s[38:39] offset:1024 sc1
v_mfma_f32_16x16x32_bf16 v[124:127], a[204:207], v[136:139], v[124:127]
v_add_u32 v13, 18688, v13
v_mul_f32 v255, v96, v96
v_mfma_f32_16x16x32_bf16 v[128:131], v[72:75], v[136:139], v[128:131]
s_waitcnt lgkmcnt(2)
v_fmac_f32 v255, v97, v97
v_add_f32 v254, v254, v98
v_mfma_f32_16x16x32_bf16 v[112:115], a[208:211], v[140:143], v[112:115]
ds_read_b128 v[136:139], v25 offset:34816
v_fmac_f32 v255, v98, v98
v_mfma_f32_16x16x32_bf16 v[116:119], a[212:215], v[140:143], v[116:119]
v_add_f32 v254, v254, v99
v_fmac_f32 v255, v99, v99
v_mfma_f32_16x16x32_bf16 v[120:123], a[216:219], v[140:143], v[120:123]
v_add_f32 v254, v254, v100
v_fmac_f32 v255, v100, v100
v_mfma_f32_16x16x32_bf16 v[124:127], a[220:223], v[140:143], v[124:127]
v_add_f32 v254, v254, v101
v_fmac_f32 v255, v101, v101
v_mfma_f32_16x16x32_bf16 v[128:131], v[76:79], v[140:143], v[128:131]
s_waitcnt vmcnt(7)
global_load_lds_dwordx4 v16, s[42:43] offset:0 sc1
ds_read_b128 v[148:151], v19 offset:0
s_waitcnt lgkmcnt(3)
v_mfma_f32_16x16x32_bf16 v[112:115], a[224:227], v[144:147], v[112:115]
ds_read_b128 v[140:143], v25 offset:35840
ds_read_b128 v[152:155], v20 offset:0
v_mfma_f32_16x16x32_bf16 v[116:119], a[228:231], v[144:147], v[116:119]
v_add_f32 v254, v254, v102
v_fmac_f32 v255, v102, v102
v_mfma_f32_16x16x32_bf16 v[120:123], a[232:235], v[144:147], v[120:123]
v_add_f32 v254, v254, v103
v_fmac_f32 v255, v103, v103
v_mfma_f32_16x16x32_bf16 v[124:127], a[236:239], v[144:147], v[124:127]
v_add_f32 v254, v254, v104
v_fmac_f32 v255, v104, v104
v_mfma_f32_16x16x32_bf16 v[128:131], v[80:83], v[144:147], v[128:131]
s_waitcnt lgkmcnt(4)
v_add_f32 v254, v254, v105
v_fmac_f32 v255, v105, v105
v_mfma_f32_16x16x32_bf16 v[112:115], a[240:243], v[132:135], v[112:115]
ds_read_b128 v[144:147], v25 offset:18432
v_add_f32 v254, v254, v106
v_mfma_f32_16x16x32_bf16 v[116:119], a[244:247], v[132:135], v[116:119]
global_load_lds_dwordx4 v17, s[42:43] offset:1024 sc1
v_fmac_f32 v255, v106, v106
v_mfma_f32_16x16x32_bf16 v[120:123], a[248:251], v[132:135], v[120:123]
v_add_f32 v254, v254, v107
v_fmac_f32 v255, v107, v107
v_mfma_f32_16x16x32_bf16 v[124:127], a[252:255], v[132:135], v[124:127]
v_add_f32 v254, v254, v108
v_fmac_f32 v255, v108, v108
v_mfma_f32_16x16x32_bf16 v[128:131], v[84:87], v[132:135], v[128:131]
s_waitcnt lgkmcnt(4)
v_add_f32 v254, v254, v109
v_fmac_f32 v255, v109, v109
v_mfma_f32_16x16x32_bf16 v[112:115], v[28:31], v[136:139], v[112:115]
ds_read_b128 v[132:135], v25 offset:19456
v_add_f32 v254, v254, v110
v_mfma_f32_16x16x32_bf16 v[116:119], v[32:35], v[136:139], v[116:119]
s_add_u32 m0, s49, 18432
v_fmac_f32 v255, v110, v110
v_add_f32 v254, v254, v111
v_mfma_f32_16x16x32_bf16 v[120:123], v[36:39], v[136:139], v[120:123]
v_fmac_f32 v255, v111, v111
v_add_f32 v254, v254, v176
v_mfma_f32_16x16x32_bf16 v[124:127], v[40:43], v[136:139], v[124:127]
v_fmac_f32 v255, v176, v176
v_add_f32 v254, v254, v177
v_mfma_f32_16x16x32_bf16 v[128:131], v[88:91], v[136:139], v[128:131]
s_waitcnt lgkmcnt(3)
v_fmac_f32 v255, v177, v177
v_add_f32 v254, v254, v178
v_mfma_f32_16x16x32_bf16 v[112:115], v[44:47], v[140:143], v[112:115]
ds_read_b128 v[136:139], v25 offset:20480
v_fmac_f32 v255, v178, v178
v_mfma_f32_16x16x32_bf16 v[116:119], v[48:51], v[140:143], v[116:119]
v_add_f32 v254, v254, v179
v_fmac_f32 v255, v179, v179
v_mfma_f32_16x16x32_bf16 v[120:123], v[52:55], v[140:143], v[120:123]
v_add_f32 v254, v254, v180
v_fmac_f32 v255, v180, v180
v_mfma_f32_16x16x32_bf16 v[124:127], v[56:59], v[140:143], v[124:127]
v_add_f32 v254, v254, v181
v_fmac_f32 v255, v181, v181
v_mfma_f32_16x16x32_bf16 v[128:131], v[92:95], v[140:143], v[128:131]
s_waitcnt lgkmcnt(2)
v_add_f32 v254, v254, v182
v_fmac_f32 v255, v182, v182
v_mfma_f32_16x16x32_bf16 v[112:115], a[0:3], v[144:147], v[112:115]
ds_read_b128 v[140:143], v25 offset:21504
v_add_f32 v254, v254, v183
v_mfma_f32_16x16x32_bf16 v[116:119], a[4:7], v[144:147], v[116:119]
v_fmac_f32 v255, v183, v183
v_mfma_f32_16x16x32_bf16 v[120:123], a[8:11], v[144:147], v[120:123]
ds_write_b64 v27, v[254:255] offset:44544
v_mfma_f32_16x16x32_bf16 v[124:127], a[12:15], v[144:147], v[124:127]
s_waitcnt lgkmcnt(3)
v_lshlrev_b32 v176, 16, v160
v_and_b32 v177, 0xffff0000, v160
v_mfma_f32_16x16x32_bf16 v[112:115], a[16:19], v[132:135], v[112:115]
ds_read_b128 v[144:147], v25 offset:22528
v_lshlrev_b32 v178, 16, v161
v_mfma_f32_16x16x32_bf16 v[116:119], a[20:23], v[132:135], v[116:119]
v_and_b32 v179, 0xffff0000, v161
v_lshlrev_b32 v180, 16, v162
v_mfma_f32_16x16x32_bf16 v[120:123], a[24:27], v[132:135], v[120:123]
v_and_b32 v181, 0xffff0000, v162
v_lshlrev_b32 v182, 16, v163
v_mfma_f32_16x16x32_bf16 v[124:127], a[28:31], v[132:135], v[124:127]
s_waitcnt lgkmcnt(3)
v_and_b32 v183, 0xffff0000, v163
v_add_f32 v180, v180, v128
v_mfma_f32_16x16x32_bf16 v[112:115], a[32:35], v[136:139], v[112:115]
ds_read_b128 v[132:135], v25 offset:23552
v_add_f32 v181, v181, v129
v_mfma_f32_16x16x32_bf16 v[116:119], a[36:39], v[136:139], v[116:119]
v_mfma_f32_16x16x32_bf16 v[120:123], a[40:43], v[136:139], v[120:123]
s_barrier
v_mfma_f32_16x16x32_bf16 v[124:127], a[44:47], v[136:139], v[124:127]
s_waitcnt lgkmcnt(3)
v_add_f32 v182, v182, v130
v_add_f32 v183, v183, v131
v_mfma_f32_16x16x32_bf16 v[112:115], a[48:51], v[140:143], v[112:115]
s_mov_b64 s[76:77], -1
ds_read_b128 v[136:139], v25 offset:24576
global_load_lds_dwordx4 v11, s[38:39] offset:-2048 sc1
v_mfma_f32_16x16x32_bf16 v[116:119], a[52:55], v[140:143], v[116:119]
global_load_dwordx4 v[160:163], v18, s[42:43] sc1
v_lshlrev_b32 v96, 16, v148
v_mfma_f32_16x16x32_bf16 v[120:123], a[56:59], v[140:143], v[120:123]
s_sub_u32 s42, s42, 18432
v_and_b32 v97, 0xffff0000, v148
v_lshlrev_b32 v98, 16, v149
v_mfma_f32_16x16x32_bf16 v[124:127], a[60:63], v[140:143], v[124:127]
s_waitcnt lgkmcnt(2)
global_load_lds_dwordx4 v11, s[38:39] offset:-1024 sc1
v_and_b32 v99, 0xffff0000, v149
v_mfma_f32_16x16x32_bf16 v[112:115], a[64:67], v[144:147], v[112:115]
s_subb_u32 s43, s43, 0
ds_read_b128 v[140:143], v25 offset:25600
v_lshlrev_b32 v100, 16, v150
v_mfma_f32_16x16x32_bf16 v[116:119], a[68:71], v[144:147], v[116:119]
v_and_b32 v101, 0xffff0000, v150
v_lshlrev_b32 v102, 16, v151
v_mfma_f32_16x16x32_bf16 v[120:123], a[72:75], v[144:147], v[120:123]
global_load_lds_dwordx4 v11, s[38:39] offset:0 sc1
v_add_u32 v11, 18432, v11
v_mfma_f32_16x16x32_bf16 v[124:127], a[76:79], v[144:147], v[124:127]
s_waitcnt lgkmcnt(2)
v_min_u32 v11, v11, v12
v_and_b32 v103, 0xffff0000, v151
v_mfma_f32_16x16x32_bf16 v[112:115], a[80:83], v[132:135], v[112:115]
ds_read_b128 v[144:147], v25 offset:26624
v_lshlrev_b32 v104, 16, v152
v_mfma_f32_16x16x32_bf16 v[116:119], a[84:87], v[132:135], v[116:119]
v_and_b32 v105, 0xffff0000, v152
v_lshlrev_b32 v106, 16, v153
v_mfma_f32_16x16x32_bf16 v[120:123], a[88:91], v[132:135], v[120:123]
v_and_b32 v107, 0xffff0000, v153
v_lshlrev_b32 v108, 16, v154
v_mfma_f32_16x16x32_bf16 v[124:127], a[92:95], v[132:135], v[124:127]
s_waitcnt lgkmcnt(2)
v_xor_b32 v25, 0x10000, v25
v_and_b32 v109, 0xffff0000, v154
v_mfma_f32_16x16x32_bf16 v[112:115], a[96:99], v[136:139], v[112:115]
v_lshlrev_b32 v110, 16, v155
v_and_b32 v111, 0xffff0000, v155
v_mfma_f32_16x16x32_bf16 v[116:119], a[100:103], v[136:139], v[116:119]
v_mfma_f32_16x16x32_bf16 v[120:123], a[104:107], v[136:139], v[120:123]
s_waitcnt vmcnt(9)
s_barrier
v_mfma_f32_16x16x32_bf16 v[124:127], a[108:111], v[136:139], v[124:127]
s_waitcnt lgkmcnt(1)
ds_read_b128 v[132:135], v25 offset:9216
ds_read_b128 v[184:187], v26 offset:39936
v_mfma_f32_16x16x32_bf16 v[112:115], a[112:115], v[140:143], v[112:115]
ds_read_b128 v[136:139], v25 offset:10240
ds_read_b128 v[188:191], v26 offset:40960
v_mfma_f32_16x16x32_bf16 v[116:119], a[116:119], v[140:143], v[116:119]
ds_read_b128 v[192:195], v26 offset:41984
v_mfma_f32_16x16x32_bf16 v[120:123], a[120:123], v[140:143], v[120:123]
v_mfma_f32_16x16x32_bf16 v[124:127], a[124:127], v[140:143], v[124:127]
s_waitcnt lgkmcnt(5)
v_mfma_f32_16x16x32_bf16 v[112:115], a[128:131], v[144:147], v[112:115]
ds_read_b128 v[140:143], v25 offset:11264
v_mfma_f32_16x16x32_bf16 v[116:119], a[132:135], v[144:147], v[116:119]
v_mfma_f32_16x16x32_bf16 v[120:123], a[136:139], v[144:147], v[120:123]
v_mfma_f32_16x16x32_bf16 v[124:127], a[140:143], v[144:147], v[124:127]
s_sub_u32 s70, s70, 1
s_cbranch_scc0 .Lwo_top12
s_waitcnt vmcnt(6)
s_barrier
s_waitcnt lgkmcnt(4)
v_add_f32 v176, v176, v184
v_add_f32 v177, v177, v185
v_add_f32 v178, v178, v186
v_add_f32 v179, v179, v187
s_waitcnt lgkmcnt(2)
v_add_f32 v176, v176, v188
v_add_f32 v177, v177, v189
v_add_f32 v178, v178, v190
v_add_f32 v179, v179, v191
s_waitcnt lgkmcnt(1)
v_add_f32 v180, v180, v192
v_add_f32 v181, v181, v193
v_add_f32 v182, v182, v194
v_add_f32 v183, v183, v195
v_cvt_pk_bf16_f32 v172, v176, v177
v_cvt_pk_bf16_f32 v173, v178, v179
v_cvt_pk_bf16_f32 v174, v180, v181
v_cvt_pk_bf16_f32 v175, v182, v183
v_cvt_pk_bf16_f32 v164, v112, v113
global_store_dwordx4 v14, v[172:175], s[38:39] sc1
v_cvt_pk_bf16_f32 v165, v114, v115
v_cvt_pk_bf16_f32 v166, v116, v117
v_cvt_pk_bf16_f32 v167, v118, v119
global_store_dwordx4 v13, v[164:167], s[38:39] offset:0 sc1
v_cvt_pk_bf16_f32 v168, v120, v121
v_cvt_pk_bf16_f32 v169, v122, v123
v_cvt_pk_bf16_f32 v170, v124, v125
v_cvt_pk_bf16_f32 v171, v126, v127
global_store_dwordx4 v13, v[168:171], s[38:39] offset:1024 sc1
v_add_f32 v254, v112, v113
v_mul_f32 v255, v112, v112
v_fmac_f32 v255, v113, v113
v_add_f32 v254, v254, v114
v_fmac_f32 v255, v114, v114
v_add_f32 v254, v254, v115
v_fmac_f32 v255, v115, v115
v_add_f32 v254, v254, v116
v_fmac_f32 v255, v116, v116
v_add_f32 v254, v254, v117
v_fmac_f32 v255, v117, v117
v_add_f32 v254, v254, v118
v_fmac_f32 v255, v118, v118
v_add_f32 v254, v254, v119
v_fmac_f32 v255, v119, v119
)"
R"(v_add_f32 v254, v254, v120
v_fmac_f32 v255, v120, v120
v_add_f32 v254, v254, v121
v_fmac_f32 v255, v121, v121
v_add_f32 v254, v254, v122
v_fmac_f32 v255, v122, v122
v_add_f32 v254, v254, v123
v_fmac_f32 v255, v123, v123
v_add_f32 v254, v254, v124
v_fmac_f32 v255, v124, v124
v_add_f32 v254, v254, v125
v_fmac_f32 v255, v125, v125
v_add_f32 v254, v254, v126
v_fmac_f32 v255, v126, v126
v_add_f32 v254, v254, v127
v_fmac_f32 v255, v127, v127
v_add_f32 v254, v254, v176
v_fmac_f32 v255, v176, v176
v_add_f32 v254, v254, v177
v_fmac_f32 v255, v177, v177
v_add_f32 v254, v254, v178
v_fmac_f32 v255, v178, v178
v_add_f32 v254, v254, v179
v_fmac_f32 v255, v179, v179
v_add_f32 v254, v254, v180
v_fmac_f32 v255, v180, v180
v_add_f32 v254, v254, v181
v_fmac_f32 v255, v181, v181
v_add_f32 v254, v254, v182
v_fmac_f32 v255, v182, v182
v_add_f32 v254, v254, v183
v_fmac_f32 v255, v183, v183
ds_write_b64 v27, v[254:255] offset:46592
s_waitcnt vmcnt(0) lgkmcnt(0)
s_barrier
s_barrier
s_endpgm
.Lwo_sp6:
s_sleep 1
v_add_lshl_u32 v253, v22, 0, 7
v_bfi_b32 v253, s69, v253, v21
global_load_dword v200, v253, s[38:39] sc1
s_add_u32 s71, s71, 1
s_waitcnt vmcnt(0)
v_sub_u32 v200, v200, v22
v_cmp_lt_u32 vcc, 0x7f, v200
s_and_b64 vcc, vcc, s[76:77]
s_cbranch_vccz .Lwo_rt7
s_cmp_lt_u32 s71, 3000000
s_cbranch_scc1 .Lwo_sp6
s_branch .Lwo_rt7
.Lwo_sp8:
s_sleep 1
v_add_lshl_u32 v253, v22, 0, 7
v_bfi_b32 v253, s69, v253, v21
global_load_dword v201, v253, s[38:39] sc1
s_add_u32 s71, s71, 1
s_waitcnt vmcnt(0)
v_sub_u32 v201, v201, v22
v_cmp_lt_u32 vcc, 0x7f, v201
s_and_b64 vcc, vcc, s[76:77]
s_cbranch_vccz .Lwo_rt9
s_cmp_lt_u32 s71, 3000000
s_cbranch_scc1 .Lwo_sp8
s_branch .Lwo_rt9
.Lrole2:
s_sleep 50
s_mov_b32 m0, 0x27f00
s_nop 0
global_load_lds_dword v18, s[40:41]
v_add_u32 v18, 294912, v18
global_load_lds_dword v18, s[40:41]
v_add_u32 v18, 294912, v18
global_load_lds_dword v18, s[40:41]
v_add_u32 v18, 294912, v18
global_load_lds_dword v18, s[40:41]
v_add_u32 v18, 294912, v18
s_waitcnt vmcnt(0)
s_sleep 30
s_add_u32 m0, s44, 4096
s_nop 0
global_load_lds_dwordx4 v0, s[42:43] offset:-4096 nt
global_load_lds_dwordx4 v1, s[42:43] offset:-3072 nt
global_load_lds_dwordx4 v2, s[42:43] offset:-2048 nt
global_load_lds_dwordx4 v3, s[42:43] offset:-1024 nt
global_load_lds_dwordx4 v4, s[42:43] offset:0 nt
global_load_lds_dwordx4 v5, s[42:43] offset:1024 nt
s_add_u32 s42, s42, 73728
s_addc_u32 s43, s43, 0
s_add_u32 m0, s44, 28672
s_nop 0
global_load_lds_dwordx4 v0, s[42:43] offset:-4096 nt
global_load_lds_dwordx4 v1, s[42:43] offset:-3072 nt
global_load_lds_dwordx4 v2, s[42:43] offset:-2048 nt
global_load_lds_dwordx4 v3, s[42:43] offset:-1024 nt
global_load_lds_dwordx4 v4, s[42:43] offset:0 nt
global_load_lds_dwordx4 v5, s[42:43] offset:1024 nt
s_add_u32 s42, s42, 73728
s_addc_u32 s43, s43, 0
s_add_u32 m0, s44, 53248
s_nop 0
global_load_lds_dwordx4 v0, s[42:43] offset:-4096 nt
global_load_lds_dwordx4 v1, s[42:43] offset:-3072 nt
global_load_lds_dwordx4 v2, s[42:43] offset:-2048 nt
global_load_lds_dwordx4 v3, s[42:43] offset:-1024 nt
global_load_lds_dwordx4 v4, s[42:43] offset:0 nt
global_load_lds_dwordx4 v5, s[42:43] offset:1024 nt
s_add_u32 s42, s42, 73728
s_addc_u32 s43, s43, 0
s_add_u32 m0, s44, 77824
s_nop 0
global_load_lds_dwordx4 v0, s[42:43] offset:-4096 nt
global_load_lds_dwordx4 v1, s[42:43] offset:-3072 nt
global_load_lds_dwordx4 v2, s[42:43] offset:-2048 nt
global_load_lds_dwordx4 v3, s[42:43] offset:-1024 nt
global_load_lds_dwordx4 v4, s[42:43] offset:0 nt
global_load_lds_dwordx4 v5, s[42:43] offset:1024 nt
s_add_u32 s42, s42, 73728
s_addc_u32 s43, s43, 0
s_add_u32 m0, s44, 102400
s_nop 0
global_load_lds_dwordx4 v0, s[42:43] offset:-4096 nt
global_load_lds_dwordx4 v1, s[42:43] offset:-3072 nt
global_load_lds_dwordx4 v2, s[42:43] offset:-2048 nt
global_load_lds_dwordx4 v3, s[42:43] offset:-1024 nt
global_load_lds_dwordx4 v4, s[42:43] offset:0 nt
global_load_lds_dwordx4 v5, s[42:43] offset:1024 nt
s_add_u32 s42, s42, 73728
s_addc_u32 s43, s43, 0
v_add_u32 v255, 0x10000, v6
v_add_u32 v132, 0x20000, v6
v_add_u32 v133, 0x10000, v7
v_add_u32 v134, 0x20000, v7
v_add_u32 v135, 0x10000, v8
v_add_u32 v136, 0x20000, v8
s_waitcnt lgkmcnt(0)
s_mul_i32 s37, s37, 0x9E3779B1
s_add_u32 s37, s37, 0x7F4A7C15
v_add_u32 v14, s37, v14
v_add_u32 v16, s37, v16
s_waitcnt vmcnt(24)
s_barrier
s_add_u32 m0, s44, 126976
ds_read_b64_tr_b16 a[0:1], v6 offset:0
ds_read_b64_tr_b16 a[2:3], v6 offset:1536
global_load_lds_dwordx4 v0, s[42:43] offset:-4096 nt
ds_read_b64_tr_b16 a[4:5], v7 offset:0
global_load_lds_dwordx4 v1, s[42:43] offset:-3072 nt
ds_read_b64_tr_b16 a[6:7], v7 offset:1536
global_load_lds_dwordx4 v2, s[42:43] offset:-2048 nt
ds_read_b64_tr_b16 a[8:9], v8 offset:0
ds_read_b64_tr_b16 a[10:11], v8 offset:1536
global_load_lds_dwordx4 v3, s[42:43] offset:-1024 nt
ds_read_b64_tr_b16 a[12:13], v6 offset:12288
global_load_lds_dwordx4 v4, s[42:43] offset:0 nt
ds_read_b64_tr_b16 a[14:15], v6 offset:13824
global_load_lds_dwordx4 v5, s[42:43] offset:1024 nt
ds_read_b64_tr_b16 a[16:17], v7 offset:12288
ds_read_b64_tr_b16 a[18:19], v7 offset:13824
s_add_u32 s42, s42, 73728
ds_read_b64_tr_b16 a[20:21], v8 offset:12288
s_addc_u32 s43, s43, 0
ds_read_b64_tr_b16 a[22:23], v8 offset:13824
s_waitcnt vmcnt(24) lgkmcnt(0)
s_barrier
s_add_u32 m0, s44, 4096
ds_read_b64_tr_b16 a[24:25], v6 offset:24576
ds_read_b64_tr_b16 a[26:27], v6 offset:26112
global_load_lds_dwordx4 v0, s[42:43] offset:-4096 nt
ds_read_b64_tr_b16 a[28:29], v7 offset:24576
global_load_lds_dwordx4 v1, s[42:43] offset:-3072 nt
ds_read_b64_tr_b16 a[30:31], v7 offset:26112
global_load_lds_dwordx4 v2, s[42:43] offset:-2048 nt
ds_read_b64_tr_b16 a[32:33], v8 offset:24576
ds_read_b64_tr_b16 a[34:35], v8 offset:26112
global_load_lds_dwordx4 v3, s[42:43] offset:-1024 nt
ds_read_b64_tr_b16 a[36:37], v6 offset:36864
global_load_lds_dwordx4 v4, s[42:43] offset:0 nt
ds_read_b64_tr_b16 a[38:39], v6 offset:38400
global_load_lds_dwordx4 v5, s[42:43] offset:1024 nt
ds_read_b64_tr_b16 a[40:41], v7 offset:36864
ds_read_b64_tr_b16 a[42:43], v7 offset:38400
s_add_u32 s42, s42, 73728
ds_read_b64_tr_b16 a[44:45], v8 offset:36864
s_addc_u32 s43, s43, 0
ds_read_b64_tr_b16 a[46:47], v8 offset:38400
s_waitcnt vmcnt(24) lgkmcnt(0)
s_barrier
s_add_u32 m0, s44, 28672
ds_read_b64_tr_b16 a[48:49], v6 offset:49152
ds_read_b64_tr_b16 a[50:51], v6 offset:50688
global_load_lds_dwordx4 v0, s[42:43] offset:-4096 nt
ds_read_b64_tr_b16 a[52:53], v7 offset:49152
global_load_lds_dwordx4 v1, s[42:43] offset:-3072 nt
ds_read_b64_tr_b16 a[54:55], v7 offset:50688
global_load_lds_dwordx4 v2, s[42:43] offset:-2048 nt
ds_read_b64_tr_b16 a[56:57], v8 offset:49152
ds_read_b64_tr_b16 a[58:59], v8 offset:50688
global_load_lds_dwordx4 v3, s[42:43] offset:-1024 nt
ds_read_b64_tr_b16 a[60:61], v6 offset:61440
global_load_lds_dwordx4 v4, s[42:43] offset:0 nt
ds_read_b64_tr_b16 a[62:63], v6 offset:62976
global_load_lds_dwordx4 v5, s[42:43] offset:1024 nt
ds_read_b64_tr_b16 a[64:65], v7 offset:61440
ds_read_b64_tr_b16 a[66:67], v7 offset:62976
s_add_u32 s42, s42, 73728
ds_read_b64_tr_b16 a[68:69], v8 offset:61440
s_addc_u32 s43, s43, 0
ds_read_b64_tr_b16 a[70:71], v8 offset:62976
s_waitcnt vmcnt(24) lgkmcnt(0)
s_barrier
s_add_u32 m0, s44, 53248
ds_read_b64_tr_b16 a[72:73], v255 offset:8192
ds_read_b64_tr_b16 a[74:75], v255 offset:9728
global_load_lds_dwordx4 v0, s[42:43] offset:-4096 nt
ds_read_b64_tr_b16 a[76:77], v133 offset:8192
global_load_lds_dwordx4 v1, s[42:43] offset:-3072 nt
ds_read_b64_tr_b16 a[78:79], v133 offset:9728
global_load_lds_dwordx4 v2, s[42:43] offset:-2048 nt
ds_read_b64_tr_b16 a[80:81], v135 offset:8192
ds_read_b64_tr_b16 a[82:83], v135 offset:9728
global_load_lds_dwordx4 v3, s[42:43] offset:-1024 nt
ds_read_b64_tr_b16 a[84:85], v255 offset:20480
global_load_lds_dwordx4 v4, s[42:43] offset:0 nt
ds_read_b64_tr_b16 a[86:87], v255 offset:22016
global_load_lds_dwordx4 v5, s[42:43] offset:1024 nt
ds_read_b64_tr_b16 a[88:89], v133 offset:20480
ds_read_b64_tr_b16 a[90:91], v133 offset:22016
s_add_u32 s42, s42, 73728
ds_read_b64_tr_b16 a[92:93], v135 offset:20480
s_addc_u32 s43, s43, 0
ds_read_b64_tr_b16 a[94:95], v135 offset:22016
s_waitcnt vmcnt(24) lgkmcnt(0)
s_barrier
s_add_u32 m0, s44, 77824
ds_read_b64_tr_b16 a[96:97], v255 offset:32768
ds_read_b64_tr_b16 a[98:99], v255 offset:34304
global_load_lds_dwordx4 v0, s[42:43] offset:-4096 nt
ds_read_b64_tr_b16 a[100:101], v133 offset:32768
global_load_lds_dwordx4 v1, s[42:43] offset:-3072 nt
ds_read_b64_tr_b16 a[102:103], v133 offset:34304
global_load_lds_dwordx4 v2, s[42:43] offset:-2048 nt
ds_read_b64_tr_b16 a[104:105], v135 offset:32768
ds_read_b64_tr_b16 a[106:107], v135 offset:34304
global_load_lds_dwordx4 v3, s[42:43] offset:-1024 nt
ds_read_b64_tr_b16 a[108:109], v255 offset:45056
global_load_lds_dwordx4 v4, s[42:43] offset:0 nt
ds_read_b64_tr_b16 a[110:111], v255 offset:46592
global_load_lds_dwordx4 v5, s[42:43] offset:1024 nt
ds_read_b64_tr_b16 a[112:113], v133 offset:45056
ds_read_b64_tr_b16 a[114:115], v133 offset:46592
s_add_u32 s42, s42, 73728
ds_read_b64_tr_b16 a[116:117], v135 offset:45056
s_addc_u32 s43, s43, 0
ds_read_b64_tr_b16 a[118:119], v135 offset:46592
s_waitcnt vmcnt(24) lgkmcnt(0)
s_barrier
s_add_u32 m0, s44, 102400
ds_read_b64_tr_b16 a[120:121], v255 offset:57344
ds_read_b64_tr_b16 a[122:123], v255 offset:58880
global_load_lds_dwordx4 v0, s[42:43] offset:-4096 nt
ds_read_b64_tr_b16 a[124:125], v133 offset:57344
global_load_lds_dwordx4 v1, s[42:43] offset:-3072 nt
ds_read_b64_tr_b16 a[126:127], v133 offset:58880
global_load_lds_dwordx4 v2, s[42:43] offset:-2048 nt
ds_read_b64_tr_b16 a[128:129], v135 offset:57344
ds_read_b64_tr_b16 a[130:131], v135 offset:58880
global_load_lds_dwordx4 v3, s[42:43] offset:-1024 nt
ds_read_b64_tr_b16 a[132:133], v132 offset:4096
global_load_lds_dwordx4 v4, s[42:43] offset:0 nt
ds_read_b64_tr_b16 a[134:135], v132 offset:5632
global_load_lds_dwordx4 v5, s[42:43] offset:1024 nt
ds_read_b64_tr_b16 a[136:137], v134 offset:4096
ds_read_b64_tr_b16 a[138:139], v134 offset:5632
s_add_u32 s42, s42, 73728
ds_read_b64_tr_b16 a[140:141], v136 offset:4096
s_addc_u32 s43, s43, 0
ds_read_b64_tr_b16 a[142:143], v136 offset:5632
s_waitcnt vmcnt(24) lgkmcnt(0)
s_barrier
s_add_u32 m0, s44, 126976
ds_read_b64_tr_b16 a[144:145], v6 offset:0
ds_read_b64_tr_b16 a[146:147], v6 offset:1536
global_load_lds_dwordx4 v0, s[42:43] offset:-4096 nt
ds_read_b64_tr_b16 a[148:149], v7 offset:0
global_load_lds_dwordx4 v1, s[42:43] offset:-3072 nt
ds_read_b64_tr_b16 a[150:151], v7 offset:1536
global_load_lds_dwordx4 v2, s[42:43] offset:-2048 nt
ds_read_b64_tr_b16 a[152:153], v8 offset:0
ds_read_b64_tr_b16 a[154:155], v8 offset:1536
global_load_lds_dwordx4 v3, s[42:43] offset:-1024 nt
ds_read_b64_tr_b16 a[156:157], v6 offset:12288
global_load_lds_dwordx4 v4, s[42:43] offset:0 nt
ds_read_b64_tr_b16 a[158:159], v6 offset:13824
global_load_lds_dwordx4 v5, s[42:43] offset:1024 nt
ds_read_b64_tr_b16 a[160:161], v7 offset:12288
ds_read_b64_tr_b16 a[162:163], v7 offset:13824
s_sub_u32 s42, s42, 811008
ds_read_b64_tr_b16 a[164:165], v8 offset:12288
s_subb_u32 s43, s43, 0
ds_read_b64_tr_b16 a[166:167], v8 offset:13824
s_waitcnt vmcnt(24) lgkmcnt(0)
s_barrier
ds_read_b64_tr_b16 a[168:169], v6 offset:24576
ds_read_b64_tr_b16 a[170:171], v6 offset:26112
ds_read_b64_tr_b16 a[172:173], v7 offset:24576
ds_read_b64_tr_b16 a[174:175], v7 offset:26112
ds_read_b64_tr_b16 a[176:177], v8 offset:24576
ds_read_b64_tr_b16 a[178:179], v8 offset:26112
ds_read_b64_tr_b16 a[180:181], v6 offset:36864
ds_read_b64_tr_b16 a[182:183], v6 offset:38400
ds_read_b64_tr_b16 a[184:185], v7 offset:36864
ds_read_b64_tr_b16 a[186:187], v7 offset:38400
ds_read_b64_tr_b16 a[188:189], v8 offset:36864
ds_read_b64_tr_b16 a[190:191], v8 offset:38400
s_waitcnt vmcnt(18) lgkmcnt(0)
s_barrier
ds_read_b64_tr_b16 a[192:193], v6 offset:49152
ds_read_b64_tr_b16 a[194:195], v6 offset:50688
ds_read_b64_tr_b16 a[196:197], v7 offset:49152
ds_read_b64_tr_b16 a[198:199], v7 offset:50688
ds_read_b64_tr_b16 a[200:201], v8 offset:49152
ds_read_b64_tr_b16 a[202:203], v8 offset:50688
ds_read_b64_tr_b16 a[204:205], v6 offset:61440
ds_read_b64_tr_b16 a[206:207], v6 offset:62976
ds_read_b64_tr_b16 a[208:209], v7 offset:61440
ds_read_b64_tr_b16 a[210:211], v7 offset:62976
ds_read_b64_tr_b16 a[212:213], v8 offset:61440
ds_read_b64_tr_b16 a[214:215], v8 offset:62976
s_waitcnt vmcnt(12) lgkmcnt(0)
s_barrier
ds_read_b64_tr_b16 a[216:217], v255 offset:8192
ds_read_b64_tr_b16 a[218:219], v255 offset:9728
ds_read_b64_tr_b16 a[220:221], v133 offset:8192
ds_read_b64_tr_b16 a[222:223], v133 offset:9728
ds_read_b64_tr_b16 a[224:225], v135 offset:8192
ds_read_b64_tr_b16 a[226:227], v135 offset:9728
ds_read_b64_tr_b16 a[228:229], v255 offset:20480
ds_read_b64_tr_b16 a[230:231], v255 offset:22016
ds_read_b64_tr_b16 a[232:233], v133 offset:20480
ds_read_b64_tr_b16 a[234:235], v133 offset:22016
ds_read_b64_tr_b16 a[236:237], v135 offset:20480
ds_read_b64_tr_b16 a[238:239], v135 offset:22016
s_waitcnt vmcnt(6) lgkmcnt(0)
s_barrier
ds_read_b64_tr_b16 a[240:241], v255 offset:32768
ds_read_b64_tr_b16 a[242:243], v255 offset:34304
ds_read_b64_tr_b16 a[244:245], v133 offset:32768
ds_read_b64_tr_b16 a[246:247], v133 offset:34304
ds_read_b64_tr_b16 a[248:249], v135 offset:32768
ds_read_b64_tr_b16 a[250:251], v135 offset:34304
ds_read_b64_tr_b16 a[252:253], v255 offset:45056
ds_read_b64_tr_b16 a[254:255], v255 offset:46592
ds_read_b64_tr_b16 v[20:21], v133 offset:45056
ds_read_b64_tr_b16 v[22:23], v133 offset:46592
ds_read_b64_tr_b16 v[24:25], v135 offset:45056
ds_read_b64_tr_b16 v[26:27], v135 offset:46592
s_waitcnt vmcnt(0) lgkmcnt(0)
s_barrier
ds_read_b64_tr_b16 v[28:29], v255 offset:57344
ds_read_b64_tr_b16 v[30:31], v255 offset:58880
ds_read_b64_tr_b16 v[32:33], v133 offset:57344
ds_read_b64_tr_b16 v[34:35], v133 offset:58880
ds_read_b64_tr_b16 v[36:37], v135 offset:57344
ds_read_b64_tr_b16 v[38:39], v135 offset:58880
ds_read_b64_tr_b16 v[40:41], v132 offset:4096
ds_read_b64_tr_b16 v[42:43], v132 offset:5632
ds_read_b64_tr_b16 v[44:45], v134 offset:4096
ds_read_b64_tr_b16 v[46:47], v134 offset:5632
ds_read_b64_tr_b16 v[48:49], v136 offset:4096
ds_read_b64_tr_b16 v[50:51], v136 offset:5632
s_waitcnt lgkmcnt(0)
s_barrier
s_and_b32 s75, s36, 3
s_cmp_eq_u32 s75, 1
s_cbranch_scc1 .Ldn_w2
s_cmp_eq_u32 s75, 2
s_cbranch_scc1 .Ldn_w3
s_cmp_eq_u32 s75, 3
s_cbranch_scc1 .Ldn_w4
.Ldn_w1:
v_add_u32 v19, -1, v14
v_add_u32 v254, -1, v14
v_add_lshl_u32 v252, v16, 0, 7
v_bfi_b32 v252, s69, v252, v15
v_add_lshl_u32 v253, v14, 0, 7
v_bfi_b32 v253, s69, v253, v13
s_mov_b32 s76, 0xffff
s_mov_b32 s77, 0xffff
s_add_u32 m0, s45, 24576
s_mov_b32 s70, 32
s_waitcnt vmcnt(0) lgkmcnt(0)
s_nop 7
.p2align 6
.Ldn_top5:
s_waitcnt lgkmcnt(5)
v_mfma_f32_16x16x32_bf16 v[52:55], a[0:3], v[76:79], 0
s_waitcnt lgkmcnt(4)
v_mfma_f32_16x16x32_bf16 v[52:55], a[12:15], v[80:83], v[52:55]
s_waitcnt lgkmcnt(3)
)"
R"(v_mfma_f32_16x16x32_bf16 v[52:55], a[24:27], v[84:87], v[52:55]
s_waitcnt lgkmcnt(2)
v_mfma_f32_16x16x32_bf16 v[52:55], a[36:39], v[88:91], v[52:55]
s_waitcnt lgkmcnt(1)
ds_read_b128 v[100:103], v17 offset:6144
v_cvt_pk_bf16_f32 v128, v72, v73
v_mfma_f32_16x16x32_bf16 v[52:55], a[48:51], v[92:95], v[52:55]
s_waitcnt lgkmcnt(1)
v_mfma_f32_16x16x32_bf16 v[52:55], a[60:63], v[96:99], v[52:55]
s_waitcnt vmcnt(6)
s_barrier
v_mfma_f32_16x16x32_bf16 v[56:59], a[4:7], v[76:79], 0
ds_read_b128 v[104:107], v17 offset:7168
v_cvt_pk_bf16_f32 v130, v72, v73
v_mfma_f32_16x16x32_bf16 v[56:59], a[16:19], v[80:83], v[56:59]
global_store_dwordx4 v11, v[124:127], s[38:39] sc1
v_mfma_f32_16x16x32_bf16 v[56:59], a[28:31], v[84:87], v[56:59]
ds_read_b128 v[108:111], v17 offset:8192
v_add_u32 v11, 110592, v11
v_mfma_f32_16x16x32_bf16 v[56:59], a[40:43], v[88:91], v[56:59]
v_cvt_pk_bf16_f32 v129, v74, v75
v_cvt_pk_bf16_f32 v131, v74, v75
v_mfma_f32_16x16x32_bf16 v[56:59], a[52:55], v[92:95], v[56:59]
ds_read_b128 v[112:115], v17 offset:9216
v_mfma_f32_16x16x32_bf16 v[56:59], a[64:67], v[96:99], v[56:59]
v_permlane16_swap_b32 v128, v130
v_mfma_f32_16x16x32_bf16 v[60:63], a[8:11], v[76:79], 0
ds_read_b128 v[116:119], v17 offset:10240
v_mfma_f32_16x16x32_bf16 v[60:63], a[20:23], v[80:83], v[60:63]
v_permlane16_swap_b32 v129, v131
v_mfma_f32_16x16x32_bf16 v[60:63], a[32:35], v[84:87], v[60:63]
ds_read_b128 v[120:123], v17 offset:11264
v_mfma_f32_16x16x32_bf16 v[60:63], a[44:47], v[88:91], v[60:63]
s_mov_b64 exec, s[76:77]
global_store_dwordx4 v12, v[128:131], s[38:39] sc1
s_mov_b64 exec, -1
v_mfma_f32_16x16x32_bf16 v[60:63], a[56:59], v[92:95], v[60:63]
v_add_u32 v12, 110592, v12
v_mfma_f32_16x16x32_bf16 v[60:63], a[68:71], v[96:99], v[60:63]
s_waitcnt lgkmcnt(5)
v_mfma_f32_16x16x32_bf16 v[52:55], a[72:75], v[100:103], v[52:55]
s_waitcnt lgkmcnt(4)
v_mfma_f32_16x16x32_bf16 v[52:55], a[84:87], v[104:107], v[52:55]
s_waitcnt lgkmcnt(3)
v_mfma_f32_16x16x32_bf16 v[52:55], a[96:99], v[108:111], v[52:55]
s_waitcnt lgkmcnt(2)
v_mfma_f32_16x16x32_bf16 v[52:55], a[108:111], v[112:115], v[52:55]
s_waitcnt lgkmcnt(1)
v_mfma_f32_16x16x32_bf16 v[52:55], a[120:123], v[116:119], v[52:55]
s_waitcnt lgkmcnt(0)
ds_read_b128 v[76:79], v17 offset:12288
v_mfma_f32_16x16x32_bf16 v[52:55], a[132:135], v[120:123], v[52:55]
v_mfma_f32_16x16x32_bf16 v[56:59], a[76:79], v[100:103], v[56:59]
ds_read_b128 v[80:83], v17 offset:13312
v_mfma_f32_16x16x32_bf16 v[56:59], a[88:91], v[104:107], v[56:59]
v_mfma_f32_16x16x32_bf16 v[56:59], a[100:103], v[108:111], v[56:59]
ds_read_b128 v[84:87], v17 offset:14336
v_mfma_f32_16x16x32_bf16 v[56:59], a[112:115], v[112:115], v[56:59]
v_mfma_f32_16x16x32_bf16 v[56:59], a[124:127], v[116:119], v[56:59]
ds_read_b128 v[88:91], v17 offset:15360
v_mfma_f32_16x16x32_bf16 v[56:59], a[136:139], v[120:123], v[56:59]
v_mfma_f32_16x16x32_bf16 v[60:63], a[80:83], v[100:103], v[60:63]
ds_read_b128 v[92:95], v17 offset:16384
v_mfma_f32_16x16x32_bf16 v[60:63], a[92:95], v[104:107], v[60:63]
v_mfma_f32_16x16x32_bf16 v[60:63], a[104:107], v[108:111], v[60:63]
ds_read_b128 v[96:99], v17 offset:17408
v_mfma_f32_16x16x32_bf16 v[60:63], a[116:119], v[112:115], v[60:63]
v_mfma_f32_16x16x32_bf16 v[60:63], a[128:131], v[116:119], v[60:63]
v_mfma_f32_16x16x32_bf16 v[60:63], a[140:143], v[120:123], v[60:63]
s_waitcnt lgkmcnt(5)
v_mfma_f32_16x16x32_bf16 v[52:55], a[144:147], v[76:79], v[52:55]
s_waitcnt lgkmcnt(4)
v_mfma_f32_16x16x32_bf16 v[52:55], a[156:159], v[80:83], v[52:55]
s_waitcnt lgkmcnt(3)
v_mfma_f32_16x16x32_bf16 v[52:55], a[168:171], v[84:87], v[52:55]
s_waitcnt lgkmcnt(2)
v_mfma_f32_16x16x32_bf16 v[52:55], a[180:183], v[88:91], v[52:55]
s_waitcnt lgkmcnt(1)
v_mfma_f32_16x16x32_bf16 v[52:55], a[192:195], v[92:95], v[52:55]
s_xor_b32 s45, s45, 0x10000
ds_read_b128 v[100:103], v17 offset:18432
s_waitcnt lgkmcnt(1)
v_mfma_f32_16x16x32_bf16 v[52:55], a[204:207], v[96:99], v[52:55]
s_add_u32 m0, s45, 0
v_mfma_f32_16x16x32_bf16 v[56:59], a[148:151], v[76:79], v[56:59]
ds_read_b128 v[104:107], v17 offset:19456
ds_read_b128 v[108:111], v17 offset:20480
v_mfma_f32_16x16x32_bf16 v[56:59], a[160:163], v[80:83], v[56:59]
v_mfma_f32_16x16x32_bf16 v[56:59], a[172:175], v[84:87], v[56:59]
s_barrier
v_mfma_f32_16x16x32_bf16 v[56:59], a[184:187], v[88:91], v[56:59]
global_load_lds_dwordx4 v9, s[38:39] offset:-2048 sc1
v_mfma_f32_16x16x32_bf16 v[56:59], a[196:199], v[92:95], v[56:59]
ds_read_b128 v[112:115], v17 offset:21504
v_mfma_f32_16x16x32_bf16 v[56:59], a[208:211], v[96:99], v[56:59]
v_mfma_f32_16x16x32_bf16 v[60:63], a[152:155], v[76:79], v[60:63]
ds_read_b128 v[116:119], v17 offset:22528
global_load_lds_dwordx4 v9, s[38:39] offset:-1024 sc1
v_mfma_f32_16x16x32_bf16 v[60:63], a[164:167], v[80:83], v[60:63]
v_mfma_f32_16x16x32_bf16 v[60:63], a[176:179], v[84:87], v[60:63]
ds_read_b128 v[120:123], v17 offset:23552
v_mfma_f32_16x16x32_bf16 v[60:63], a[188:191], v[88:91], v[60:63]
global_load_lds_dwordx4 v9, s[38:39] offset:0 sc1
v_mfma_f32_16x16x32_bf16 v[60:63], a[200:203], v[92:95], v[60:63]
v_mfma_f32_16x16x32_bf16 v[60:63], a[212:215], v[96:99], v[60:63]
s_waitcnt lgkmcnt(5)
v_mfma_f32_16x16x32_bf16 v[52:55], a[216:219], v[100:103], v[52:55]
s_waitcnt lgkmcnt(4)
global_load_lds_dwordx4 v9, s[38:39] offset:1024 sc1
v_mfma_f32_16x16x32_bf16 v[52:55], a[228:231], v[104:107], v[52:55]
s_waitcnt lgkmcnt(3)
v_mfma_f32_16x16x32_bf16 v[52:55], a[240:243], v[108:111], v[52:55]
s_waitcnt lgkmcnt(2)
v_mfma_f32_16x16x32_bf16 v[52:55], a[252:255], v[112:115], v[52:55]
s_waitcnt lgkmcnt(1)
global_load_lds_dwordx4 v9, s[38:39] offset:2048 sc1
v_mfma_f32_16x16x32_bf16 v[52:55], v[28:31], v[116:119], v[52:55]
s_waitcnt lgkmcnt(0)
v_mfma_f32_16x16x32_bf16 v[52:55], v[40:43], v[120:123], v[52:55]
v_mfma_f32_16x16x32_bf16 v[56:59], a[220:223], v[100:103], v[56:59]
global_load_lds_dwordx4 v9, s[38:39] offset:3072 sc1
v_add_u32 v9, 147456, v9
v_mfma_f32_16x16x32_bf16 v[56:59], a[232:235], v[104:107], v[56:59]
v_mfma_f32_16x16x32_bf16 v[56:59], a[244:247], v[108:111], v[56:59]
s_waitcnt vmcnt(8)
s_barrier
v_mfma_f32_16x16x32_bf16 v[56:59], v[20:23], v[112:115], v[56:59]
ds_read_b128 v[76:79], v17 offset:24576
v_min_u32 v9, v9, v10
v_mfma_f32_16x16x32_bf16 v[56:59], v[32:35], v[116:119], v[56:59]
ds_read_b128 v[80:83], v17 offset:25600
v_cvt_pk_bf16_f32 v124, v52, v53
v_mfma_f32_16x16x32_bf16 v[56:59], v[44:47], v[120:123], v[56:59]
ds_read_b128 v[84:87], v17 offset:26624
v_cvt_pk_bf16_f32 v125, v54, v55
v_mfma_f32_16x16x32_bf16 v[60:63], a[224:227], v[100:103], v[60:63]
ds_read_b128 v[88:91], v17 offset:27648
v_mfma_f32_16x16x32_bf16 v[60:63], a[236:239], v[104:107], v[60:63]
ds_read_b128 v[92:95], v17 offset:28672
v_mfma_f32_16x16x32_bf16 v[60:63], a[248:251], v[108:111], v[60:63]
ds_read_b128 v[96:99], v17 offset:29696
v_mfma_f32_16x16x32_bf16 v[60:63], v[24:27], v[112:115], v[60:63]
v_cvt_pk_bf16_f32 v126, v56, v57
v_cvt_pk_bf16_f32 v127, v58, v59
v_mfma_f32_16x16x32_bf16 v[60:63], v[36:39], v[116:119], v[60:63]
v_mfma_f32_16x16x32_bf16 v[60:63], v[48:51], v[120:123], v[60:63]
s_waitcnt lgkmcnt(5)
v_mfma_f32_16x16x32_bf16 v[64:67], a[0:3], v[76:79], 0
s_waitcnt lgkmcnt(4)
v_mfma_f32_16x16x32_bf16 v[64:67], a[12:15], v[80:83], v[64:67]
s_waitcnt lgkmcnt(3)
v_mfma_f32_16x16x32_bf16 v[64:67], a[24:27], v[84:87], v[64:67]
s_waitcnt lgkmcnt(2)
v_mfma_f32_16x16x32_bf16 v[64:67], a[36:39], v[88:91], v[64:67]
s_waitcnt lgkmcnt(1)
ds_read_b128 v[100:103], v17 offset:30720
v_cvt_pk_bf16_f32 v128, v60, v61
v_mfma_f32_16x16x32_bf16 v[64:67], a[48:51], v[92:95], v[64:67]
s_waitcnt lgkmcnt(1)
v_mfma_f32_16x16x32_bf16 v[64:67], a[60:63], v[96:99], v[64:67]
s_waitcnt vmcnt(6)
s_barrier
v_mfma_f32_16x16x32_bf16 v[68:71], a[4:7], v[76:79], 0
ds_read_b128 v[104:107], v17 offset:31744
v_cvt_pk_bf16_f32 v130, v60, v61
v_mfma_f32_16x16x32_bf16 v[68:71], a[16:19], v[80:83], v[68:71]
global_store_dwordx4 v11, v[124:127], s[38:39] sc1
v_mfma_f32_16x16x32_bf16 v[68:71], a[28:31], v[84:87], v[68:71]
ds_read_b128 v[108:111], v17 offset:32768
v_add_u32 v11, 110592, v11
v_mfma_f32_16x16x32_bf16 v[68:71], a[40:43], v[88:91], v[68:71]
v_cvt_pk_bf16_f32 v129, v62, v63
v_cvt_pk_bf16_f32 v131, v62, v63
v_mfma_f32_16x16x32_bf16 v[68:71], a[52:55], v[92:95], v[68:71]
ds_read_b128 v[112:115], v17 offset:33792
v_mfma_f32_16x16x32_bf16 v[68:71], a[64:67], v[96:99], v[68:71]
v_permlane16_swap_b32 v128, v130
v_mfma_f32_16x16x32_bf16 v[72:75], a[8:11], v[76:79], 0
ds_read_b128 v[116:119], v17 offset:34816
v_mfma_f32_16x16x32_bf16 v[72:75], a[20:23], v[80:83], v[72:75]
v_permlane16_swap_b32 v129, v131
v_mfma_f32_16x16x32_bf16 v[72:75], a[32:35], v[84:87], v[72:75]
ds_read_b128 v[120:123], v17 offset:35840
v_mfma_f32_16x16x32_bf16 v[72:75], a[44:47], v[88:91], v[72:75]
s_mov_b64 exec, s[76:77]
global_store_dwordx4 v12, v[128:131], s[38:39] sc1
s_mov_b64 exec, -1
v_mfma_f32_16x16x32_bf16 v[72:75], a[56:59], v[92:95], v[72:75]
v_add_u32 v12, 110592, v12
v_mfma_f32_16x16x32_bf16 v[72:75], a[68:71], v[96:99], v[72:75]
s_waitcnt lgkmcnt(5)
v_mfma_f32_16x16x32_bf16 v[64:67], a[72:75], v[100:103], v[64:67]
s_waitcnt lgkmcnt(4)
v_mfma_f32_16x16x32_bf16 v[64:67], a[84:87], v[104:107], v[64:67]
s_waitcnt lgkmcnt(3)
v_mfma_f32_16x16x32_bf16 v[64:67], a[96:99], v[108:111], v[64:67]
s_waitcnt lgkmcnt(2)
v_mfma_f32_16x16x32_bf16 v[64:67], a[108:111], v[112:115], v[64:67]
s_waitcnt lgkmcnt(1)
v_mfma_f32_16x16x32_bf16 v[64:67], a[120:123], v[116:119], v[64:67]
s_waitcnt lgkmcnt(0)
ds_read_b128 v[76:79], v17 offset:36864
v_mfma_f32_16x16x32_bf16 v[64:67], a[132:135], v[120:123], v[64:67]
v_mfma_f32_16x16x32_bf16 v[68:71], a[76:79], v[100:103], v[68:71]
ds_read_b128 v[80:83], v17 offset:37888
v_mfma_f32_16x16x32_bf16 v[68:71], a[88:91], v[104:107], v[68:71]
v_mfma_f32_16x16x32_bf16 v[68:71], a[100:103], v[108:111], v[68:71]
ds_read_b128 v[84:87], v17 offset:38912
v_mfma_f32_16x16x32_bf16 v[68:71], a[112:115], v[112:115], v[68:71]
v_mfma_f32_16x16x32_bf16 v[68:71], a[124:127], v[116:119], v[68:71]
ds_read_b128 v[88:91], v17 offset:39936
v_mfma_f32_16x16x32_bf16 v[68:71], a[136:139], v[120:123], v[68:71]
v_mfma_f32_16x16x32_bf16 v[72:75], a[80:83], v[100:103], v[72:75]
ds_read_b128 v[92:95], v17 offset:40960
v_mfma_f32_16x16x32_bf16 v[72:75], a[92:95], v[104:107], v[72:75]
v_mfma_f32_16x16x32_bf16 v[72:75], a[104:107], v[108:111], v[72:75]
ds_read_b128 v[96:99], v17 offset:41984
v_mfma_f32_16x16x32_bf16 v[72:75], a[116:119], v[112:115], v[72:75]
v_mfma_f32_16x16x32_bf16 v[72:75], a[128:131], v[116:119], v[72:75]
v_mfma_f32_16x16x32_bf16 v[72:75], a[140:143], v[120:123], v[72:75]
s_waitcnt lgkmcnt(5)
v_mfma_f32_16x16x32_bf16 v[64:67], a[144:147], v[76:79], v[64:67]
s_waitcnt lgkmcnt(4)
v_mfma_f32_16x16x32_bf16 v[64:67], a[156:159], v[80:83], v[64:67]
s_waitcnt lgkmcnt(3)
v_mfma_f32_16x16x32_bf16 v[64:67], a[168:171], v[84:87], v[64:67]
s_waitcnt lgkmcnt(2)
v_mfma_f32_16x16x32_bf16 v[64:67], a[180:183], v[88:91], v[64:67]
s_waitcnt lgkmcnt(1)
v_mfma_f32_16x16x32_bf16 v[64:67], a[192:195], v[92:95], v[64:67]
s_waitcnt lgkmcnt(0)
ds_read_b128 v[100:103], v17 offset:43008
v_mfma_f32_16x16x32_bf16 v[64:67], a[204:207], v[96:99], v[64:67]
s_add_u32 m0, s45, 24576
v_mfma_f32_16x16x32_bf16 v[68:71], a[148:151], v[76:79], v[68:71]
ds_read_b128 v[104:107], v17 offset:44032
ds_read_b128 v[108:111], v17 offset:45056
v_mfma_f32_16x16x32_bf16 v[68:71], a[160:163], v[80:83], v[68:71]
v_mfma_f32_16x16x32_bf16 v[68:71], a[172:175], v[84:87], v[68:71]
s_barrier
v_mfma_f32_16x16x32_bf16 v[68:71], a[184:187], v[88:91], v[68:71]
global_load_lds_dwordx4 v9, s[38:39] offset:-2048 sc1
v_mfma_f32_16x16x32_bf16 v[68:71], a[196:199], v[92:95], v[68:71]
ds_read_b128 v[112:115], v17 offset:46080
v_mfma_f32_16x16x32_bf16 v[68:71], a[208:211], v[96:99], v[68:71]
v_mfma_f32_16x16x32_bf16 v[72:75], a[152:155], v[76:79], v[72:75]
ds_read_b128 v[116:119], v17 offset:47104
global_load_lds_dwordx4 v9, s[38:39] offset:-1024 sc1
v_mfma_f32_16x16x32_bf16 v[72:75], a[164:167], v[80:83], v[72:75]
v_mfma_f32_16x16x32_bf16 v[72:75], a[176:179], v[84:87], v[72:75]
ds_read_b128 v[120:123], v17 offset:48128
v_mfma_f32_16x16x32_bf16 v[72:75], a[188:191], v[88:91], v[72:75]
global_load_lds_dwordx4 v9, s[38:39] offset:0 sc1
v_mfma_f32_16x16x32_bf16 v[72:75], a[200:203], v[92:95], v[72:75]
v_mfma_f32_16x16x32_bf16 v[72:75], a[212:215], v[96:99], v[72:75]
s_waitcnt lgkmcnt(5)
v_mfma_f32_16x16x32_bf16 v[64:67], a[216:219], v[100:103], v[64:67]
s_waitcnt lgkmcnt(4)
global_load_lds_dwordx4 v9, s[38:39] offset:1024 sc1
v_mfma_f32_16x16x32_bf16 v[64:67], a[228:231], v[104:107], v[64:67]
s_waitcnt lgkmcnt(3)
v_mfma_f32_16x16x32_bf16 v[64:67], a[240:243], v[108:111], v[64:67]
s_waitcnt lgkmcnt(2)
v_mfma_f32_16x16x32_bf16 v[64:67], a[252:255], v[112:115], v[64:67]
s_waitcnt lgkmcnt(1)
global_load_lds_dwordx4 v9, s[38:39] offset:2048 sc1
v_mfma_f32_16x16x32_bf16 v[64:67], v[28:31], v[116:119], v[64:67]
s_waitcnt lgkmcnt(0)
v_mfma_f32_16x16x32_bf16 v[64:67], v[40:43], v[120:123], v[64:67]
v_xor_b32 v17, 0x10000, v17
v_mfma_f32_16x16x32_bf16 v[68:71], a[220:223], v[100:103], v[68:71]
global_load_lds_dwordx4 v9, s[38:39] offset:3072 sc1
v_add_u32 v9, 147456, v9
v_mfma_f32_16x16x32_bf16 v[68:71], a[232:235], v[104:107], v[68:71]
v_mfma_f32_16x16x32_bf16 v[68:71], a[244:247], v[108:111], v[68:71]
s_waitcnt vmcnt(8)
s_barrier
v_mfma_f32_16x16x32_bf16 v[68:71], v[20:23], v[112:115], v[68:71]
ds_read_b128 v[76:79], v17 offset:0
v_min_u32 v9, v9, v10
v_mfma_f32_16x16x32_bf16 v[68:71], v[32:35], v[116:119], v[68:71]
ds_read_b128 v[80:83], v17 offset:1024
v_cvt_pk_bf16_f32 v124, v64, v65
v_mfma_f32_16x16x32_bf16 v[68:71], v[44:47], v[120:123], v[68:71]
ds_read_b128 v[84:87], v17 offset:2048
v_cvt_pk_bf16_f32 v125, v66, v67
v_mfma_f32_16x16x32_bf16 v[72:75], a[224:227], v[100:103], v[72:75]
ds_read_b128 v[88:91], v17 offset:3072
v_mfma_f32_16x16x32_bf16 v[72:75], a[236:239], v[104:107], v[72:75]
ds_read_b128 v[92:95], v17 offset:4096
v_mfma_f32_16x16x32_bf16 v[72:75], a[248:251], v[108:111], v[72:75]
ds_read_b128 v[96:99], v17 offset:5120
v_mfma_f32_16x16x32_bf16 v[72:75], v[24:27], v[112:115], v[72:75]
v_cvt_pk_bf16_f32 v126, v68, v69
v_cvt_pk_bf16_f32 v127, v70, v71
v_mfma_f32_16x16x32_bf16 v[72:75], v[36:39], v[116:119], v[72:75]
v_mfma_f32_16x16x32_bf16 v[72:75], v[48:51], v[120:123], v[72:75]
s_sub_u32 s70, s70, 1
s_cbranch_scc0 .Ldn_top5
v_cvt_pk_bf16_f32 v124, v64, v65
v_cvt_pk_bf16_f32 v125, v66, v67
v_cvt_pk_bf16_f32 v126, v68, v69
v_cvt_pk_bf16_f32 v127, v70, v71
global_store_dwordx4 v11, v[124:127], s[38:39] sc1
v_add_u32 v11, 110592, v11
v_cvt_pk_bf16_f32 v128, v72, v73
v_cvt_pk_bf16_f32 v130, v72, v73
v_cvt_pk_bf16_f32 v129, v74, v75
v_cvt_pk_bf16_f32 v131, v74, v75
v_permlane16_swap_b32 v128, v130
s_nop 0
v_permlane16_swap_b32 v129, v131
s_mov_b64 exec, s[76:77]
global_store_dwordx4 v12, v[128:131], s[38:39] sc1
s_mov_b64 exec, -1
v_add_u32 v12, 110592, v12
s_waitcnt vmcnt(2)
s_barrier
s_waitcnt vmcnt(0)
s_barrier
s_endpgm
.Ldn_w2:
v_add_u32 v19, -1, v14
v_add_u32 v254, -1, v14
v_add_lshl_u32 v252, v16, 0, 7
v_bfi_b32 v252, s69, v252, v15
v_add_lshl_u32 v253, v14, 0, 7
v_bfi_b32 v253, s69, v253, v13
s_mov_b32 s76, 0xffff
s_mov_b32 s77, 0xffff
s_add_u32 m0, s45, 24576
s_mov_b32 s70, 32
s_waitcnt vmcnt(0) lgkmcnt(0)
s_nop 7
.p2align 6
.Ldn_top6:
s_waitcnt lgkmcnt(5)
v_mfma_f32_16x16x32_bf16 v[52:55], a[0:3], v[76:79], 0
s_waitcnt lgkmcnt(4)
v_mfma_f32_16x16x32_bf16 v[52:55], a[12:15], v[80:83], v[52:55]
s_waitcnt lgkmcnt(3)
v_mfma_f32_16x16x32_bf16 v[52:55], a[24:27], v[84:87], v[52:55]
s_waitcnt lgkmcnt(2)
v_mfma_f32_16x16x32_bf16 v[52:55], a[36:39], v[88:91], v[52:55]
s_waitcnt lgkmcnt(1)
ds_read_b128 v[100:103], v17 offset:6144
v_cvt_pk_bf16_f32 v128, v72, v73
v_mfma_f32_16x16x32_bf16 v[52:55], a[48:51], v[92:95], v[52:55]
s_waitcnt lgkmcnt(1)
v_mfma_f32_16x16x32_bf16 v[52:55], a[60:63], v[96:99], v[52:55]
s_waitcnt vmcnt(6)
s_barrier
v_mfma_f32_16x16x32_bf16 v[56:59], a[4:7], v[76:79], 0
ds_read_b128 v[104:107], v17 offset:7168
v_cvt_pk_bf16_f32 v130, v72, v73
v_mfma_f32_16x16x32_bf16 v[56:59], a[16:19], v[80:83], v[56:59]
v_cvt_pk_bf16_f32 v129, v74, v75
v_cvt_pk_bf16_f32 v131, v74, v75
v_mfma_f32_16x16x32_bf16 v[56:59], a[28:31], v[84:87], v[56:59]
ds_read_b128 v[108:111], v17 offset:8192
v_mfma_f32_16x16x32_bf16 v[56:59], a[40:43], v[88:91], v[56:59]
global_store_dwordx4 v11, v[124:127], s[38:39] sc1
v_mfma_f32_16x16x32_bf16 v[56:59], a[52:55], v[92:95], v[56:59]
ds_read_b128 v[112:115], v17 offset:9216
v_add_u32 v11, 110592, v11
v_mfma_f32_16x16x32_bf16 v[56:59], a[64:67], v[96:99], v[56:59]
v_permlane16_swap_b32 v128, v130
v_mfma_f32_16x16x32_bf16 v[60:63], a[8:11], v[76:79], 0
ds_read_b128 v[116:119], v17 offset:10240
v_mfma_f32_16x16x32_bf16 v[60:63], a[20:23], v[80:83], v[60:63]
v_permlane16_swap_b32 v129, v131
v_mfma_f32_16x16x32_bf16 v[60:63], a[32:35], v[84:87], v[60:63]
ds_read_b128 v[120:123], v17 offset:11264
v_mfma_f32_16x16x32_bf16 v[60:63], a[44:47], v[88:91], v[60:63]
s_mov_b64 exec, s[76:77]
global_store_dwordx4 v12, v[128:131], s[38:39] sc1
s_mov_b64 exec, -1
v_mfma_f32_16x16x32_bf16 v[60:63], a[56:59], v[92:95], v[60:63]
v_add_u32 v12, 110592, v12
v_mfma_f32_16x16x32_bf16 v[60:63], a[68:71], v[96:99], v[60:63]
s_waitcnt lgkmcnt(5)
v_mfma_f32_16x16x32_bf16 v[52:55], a[72:75], v[100:103], v[52:55]
s_waitcnt lgkmcnt(4)
v_mfma_f32_16x16x32_bf16 v[52:55], a[84:87], v[104:107], v[52:55]
s_waitcnt lgkmcnt(3)
v_mfma_f32_16x16x32_bf16 v[52:55], a[96:99], v[108:111], v[52:55]
s_waitcnt lgkmcnt(2)
v_mfma_f32_16x16x32_bf16 v[52:55], a[108:111], v[112:115], v[52:55]
s_waitcnt lgkmcnt(1)
v_mfma_f32_16x16x32_bf16 v[52:55], a[120:123], v[116:119], v[52:55]
s_waitcnt lgkmcnt(0)
ds_read_b128 v[76:79], v17 offset:12288
v_mfma_f32_16x16x32_bf16 v[52:55], a[132:135], v[120:123], v[52:55]
v_mfma_f32_16x16x32_bf16 v[56:59], a[76:79], v[100:103], v[56:59]
ds_read_b128 v[80:83], v17 offset:13312
v_mfma_f32_16x16x32_bf16 v[56:59], a[88:91], v[104:107], v[56:59]
v_mfma_f32_16x16x32_bf16 v[56:59], a[100:103], v[108:111], v[56:59]
ds_read_b128 v[84:87], v17 offset:14336
v_mfma_f32_16x16x32_bf16 v[56:59], a[112:115], v[112:115], v[56:59]
v_mfma_f32_16x16x32_bf16 v[56:59], a[124:127], v[116:119], v[56:59]
)"
R"(ds_read_b128 v[88:91], v17 offset:15360
v_mfma_f32_16x16x32_bf16 v[56:59], a[136:139], v[120:123], v[56:59]
v_mfma_f32_16x16x32_bf16 v[60:63], a[80:83], v[100:103], v[60:63]
ds_read_b128 v[92:95], v17 offset:16384
v_mfma_f32_16x16x32_bf16 v[60:63], a[92:95], v[104:107], v[60:63]
v_mfma_f32_16x16x32_bf16 v[60:63], a[104:107], v[108:111], v[60:63]
ds_read_b128 v[96:99], v17 offset:17408
v_mfma_f32_16x16x32_bf16 v[60:63], a[116:119], v[112:115], v[60:63]
v_mfma_f32_16x16x32_bf16 v[60:63], a[128:131], v[116:119], v[60:63]
v_mfma_f32_16x16x32_bf16 v[60:63], a[140:143], v[120:123], v[60:63]
s_waitcnt lgkmcnt(5)
v_mfma_f32_16x16x32_bf16 v[52:55], a[144:147], v[76:79], v[52:55]
s_waitcnt lgkmcnt(4)
v_mfma_f32_16x16x32_bf16 v[52:55], a[156:159], v[80:83], v[52:55]
s_waitcnt lgkmcnt(3)
v_mfma_f32_16x16x32_bf16 v[52:55], a[168:171], v[84:87], v[52:55]
s_waitcnt lgkmcnt(2)
v_mfma_f32_16x16x32_bf16 v[52:55], a[180:183], v[88:91], v[52:55]
s_waitcnt lgkmcnt(1)
v_mfma_f32_16x16x32_bf16 v[52:55], a[192:195], v[92:95], v[52:55]
s_xor_b32 s45, s45, 0x10000
ds_read_b128 v[100:103], v17 offset:18432
s_waitcnt lgkmcnt(1)
v_mfma_f32_16x16x32_bf16 v[52:55], a[204:207], v[96:99], v[52:55]
s_add_u32 m0, s45, 0
v_mfma_f32_16x16x32_bf16 v[56:59], a[148:151], v[76:79], v[56:59]
ds_read_b128 v[104:107], v17 offset:19456
ds_read_b128 v[108:111], v17 offset:20480
v_mfma_f32_16x16x32_bf16 v[56:59], a[160:163], v[80:83], v[56:59]
v_mfma_f32_16x16x32_bf16 v[56:59], a[172:175], v[84:87], v[56:59]
s_barrier
v_mfma_f32_16x16x32_bf16 v[56:59], a[184:187], v[88:91], v[56:59]
v_mfma_f32_16x16x32_bf16 v[56:59], a[196:199], v[92:95], v[56:59]
ds_read_b128 v[112:115], v17 offset:21504
global_load_lds_dwordx4 v9, s[38:39] offset:-2048 sc1
v_mfma_f32_16x16x32_bf16 v[56:59], a[208:211], v[96:99], v[56:59]
v_mfma_f32_16x16x32_bf16 v[60:63], a[152:155], v[76:79], v[60:63]
ds_read_b128 v[116:119], v17 offset:22528
v_mfma_f32_16x16x32_bf16 v[60:63], a[164:167], v[80:83], v[60:63]
global_load_lds_dwordx4 v9, s[38:39] offset:-1024 sc1
v_mfma_f32_16x16x32_bf16 v[60:63], a[176:179], v[84:87], v[60:63]
ds_read_b128 v[120:123], v17 offset:23552
v_mfma_f32_16x16x32_bf16 v[60:63], a[188:191], v[88:91], v[60:63]
v_mfma_f32_16x16x32_bf16 v[60:63], a[200:203], v[92:95], v[60:63]
global_load_lds_dwordx4 v9, s[38:39] offset:0 sc1
v_mfma_f32_16x16x32_bf16 v[60:63], a[212:215], v[96:99], v[60:63]
s_waitcnt lgkmcnt(5)
v_mfma_f32_16x16x32_bf16 v[52:55], a[216:219], v[100:103], v[52:55]
s_waitcnt lgkmcnt(4)
v_mfma_f32_16x16x32_bf16 v[52:55], a[228:231], v[104:107], v[52:55]
s_waitcnt lgkmcnt(3)
global_load_lds_dwordx4 v9, s[38:39] offset:1024 sc1
v_mfma_f32_16x16x32_bf16 v[52:55], a[240:243], v[108:111], v[52:55]
s_waitcnt lgkmcnt(2)
v_mfma_f32_16x16x32_bf16 v[52:55], a[252:255], v[112:115], v[52:55]
s_waitcnt lgkmcnt(1)
v_mfma_f32_16x16x32_bf16 v[52:55], v[28:31], v[116:119], v[52:55]
s_waitcnt lgkmcnt(0)
global_load_lds_dwordx4 v9, s[38:39] offset:2048 sc1
v_mfma_f32_16x16x32_bf16 v[52:55], v[40:43], v[120:123], v[52:55]
v_mfma_f32_16x16x32_bf16 v[56:59], a[220:223], v[100:103], v[56:59]
v_mfma_f32_16x16x32_bf16 v[56:59], a[232:235], v[104:107], v[56:59]
v_mfma_f32_16x16x32_bf16 v[56:59], a[244:247], v[108:111], v[56:59]
s_waitcnt vmcnt(7)
s_barrier
v_mfma_f32_16x16x32_bf16 v[56:59], v[20:23], v[112:115], v[56:59]
ds_read_b128 v[76:79], v17 offset:24576
global_load_lds_dwordx4 v9, s[38:39] offset:3072 sc1
v_mfma_f32_16x16x32_bf16 v[56:59], v[32:35], v[116:119], v[56:59]
ds_read_b128 v[80:83], v17 offset:25600
v_add_u32 v9, 147456, v9
v_mfma_f32_16x16x32_bf16 v[56:59], v[44:47], v[120:123], v[56:59]
ds_read_b128 v[84:87], v17 offset:26624
v_min_u32 v9, v9, v10
v_mfma_f32_16x16x32_bf16 v[60:63], a[224:227], v[100:103], v[60:63]
ds_read_b128 v[88:91], v17 offset:27648
v_cvt_pk_bf16_f32 v124, v52, v53
v_mfma_f32_16x16x32_bf16 v[60:63], a[236:239], v[104:107], v[60:63]
ds_read_b128 v[92:95], v17 offset:28672
v_cvt_pk_bf16_f32 v125, v54, v55
v_mfma_f32_16x16x32_bf16 v[60:63], a[248:251], v[108:111], v[60:63]
ds_read_b128 v[96:99], v17 offset:29696
v_mfma_f32_16x16x32_bf16 v[60:63], v[24:27], v[112:115], v[60:63]
v_cvt_pk_bf16_f32 v126, v56, v57
v_cvt_pk_bf16_f32 v127, v58, v59
v_mfma_f32_16x16x32_bf16 v[60:63], v[36:39], v[116:119], v[60:63]
v_mfma_f32_16x16x32_bf16 v[60:63], v[48:51], v[120:123], v[60:63]
s_waitcnt lgkmcnt(5)
v_mfma_f32_16x16x32_bf16 v[64:67], a[0:3], v[76:79], 0
s_waitcnt lgkmcnt(4)
v_mfma_f32_16x16x32_bf16 v[64:67], a[12:15], v[80:83], v[64:67]
s_waitcnt lgkmcnt(3)
v_mfma_f32_16x16x32_bf16 v[64:67], a[24:27], v[84:87], v[64:67]
s_waitcnt lgkmcnt(2)
v_mfma_f32_16x16x32_bf16 v[64:67], a[36:39], v[88:91], v[64:67]
s_waitcnt lgkmcnt(1)
ds_read_b128 v[100:103], v17 offset:30720
v_cvt_pk_bf16_f32 v128, v60, v61
v_mfma_f32_16x16x32_bf16 v[64:67], a[48:51], v[92:95], v[64:67]
s_waitcnt lgkmcnt(1)
v_mfma_f32_16x16x32_bf16 v[64:67], a[60:63], v[96:99], v[64:67]
s_waitcnt vmcnt(6)
s_barrier
v_mfma_f32_16x16x32_bf16 v[68:71], a[4:7], v[76:79], 0
ds_read_b128 v[104:107], v17 offset:31744
v_cvt_pk_bf16_f32 v130, v60, v61
v_mfma_f32_16x16x32_bf16 v[68:71], a[16:19], v[80:83], v[68:71]
v_cvt_pk_bf16_f32 v129, v62, v63
v_cvt_pk_bf16_f32 v131, v62, v63
v_mfma_f32_16x16x32_bf16 v[68:71], a[28:31], v[84:87], v[68:71]
ds_read_b128 v[108:111], v17 offset:32768
v_mfma_f32_16x16x32_bf16 v[68:71], a[40:43], v[88:91], v[68:71]
global_store_dwordx4 v11, v[124:127], s[38:39] sc1
v_mfma_f32_16x16x32_bf16 v[68:71], a[52:55], v[92:95], v[68:71]
ds_read_b128 v[112:115], v17 offset:33792
v_add_u32 v11, 110592, v11
v_mfma_f32_16x16x32_bf16 v[68:71], a[64:67], v[96:99], v[68:71]
v_permlane16_swap_b32 v128, v130
v_mfma_f32_16x16x32_bf16 v[72:75], a[8:11], v[76:79], 0
ds_read_b128 v[116:119], v17 offset:34816
v_mfma_f32_16x16x32_bf16 v[72:75], a[20:23], v[80:83], v[72:75]
v_permlane16_swap_b32 v129, v131
v_mfma_f32_16x16x32_bf16 v[72:75], a[32:35], v[84:87], v[72:75]
ds_read_b128 v[120:123], v17 offset:35840
v_mfma_f32_16x16x32_bf16 v[72:75], a[44:47], v[88:91], v[72:75]
s_mov_b64 exec, s[76:77]
global_store_dwordx4 v12, v[128:131], s[38:39] sc1
s_mov_b64 exec, -1
v_mfma_f32_16x16x32_bf16 v[72:75], a[56:59], v[92:95], v[72:75]
v_add_u32 v12, 110592, v12
v_mfma_f32_16x16x32_bf16 v[72:75], a[68:71], v[96:99], v[72:75]
s_waitcnt lgkmcnt(5)
v_mfma_f32_16x16x32_bf16 v[64:67], a[72:75], v[100:103], v[64:67]
s_waitcnt lgkmcnt(4)
v_mfma_f32_16x16x32_bf16 v[64:67], a[84:87], v[104:107], v[64:67]
s_waitcnt lgkmcnt(3)
v_mfma_f32_16x16x32_bf16 v[64:67], a[96:99], v[108:111], v[64:67]
s_waitcnt lgkmcnt(2)
v_mfma_f32_16x16x32_bf16 v[64:67], a[108:111], v[112:115], v[64:67]
s_waitcnt lgkmcnt(1)
v_mfma_f32_16x16x32_bf16 v[64:67], a[120:123], v[116:119], v[64:67]
s_waitcnt lgkmcnt(0)
ds_read_b128 v[76:79], v17 offset:36864
v_mfma_f32_16x16x32_bf16 v[64:67], a[132:135], v[120:123], v[64:67]
v_mfma_f32_16x16x32_bf16 v[68:71], a[76:79], v[100:103], v[68:71]
ds_read_b128 v[80:83], v17 offset:37888
v_mfma_f32_16x16x32_bf16 v[68:71], a[88:91], v[104:107], v[68:71]
v_mfma_f32_16x16x32_bf16 v[68:71], a[100:103], v[108:111], v[68:71]
ds_read_b128 v[84:87], v17 offset:38912
v_mfma_f32_16x16x32_bf16 v[68:71], a[112:115], v[112:115], v[68:71]
v_mfma_f32_16x16x32_bf16 v[68:71], a[124:127], v[116:119], v[68:71]
ds_read_b128 v[88:91], v17 offset:39936
v_mfma_f32_16x16x32_bf16 v[68:71], a[136:139], v[120:123], v[68:71]
v_mfma_f32_16x16x32_bf16 v[72:75], a[80:83], v[100:103], v[72:75]
ds_read_b128 v[92:95], v17 offset:40960
v_mfma_f32_16x16x32_bf16 v[72:75], a[92:95], v[104:107], v[72:75]
v_mfma_f32_16x16x32_bf16 v[72:75], a[104:107], v[108:111], v[72:75]
ds_read_b128 v[96:99], v17 offset:41984
v_mfma_f32_16x16x32_bf16 v[72:75], a[116:119], v[112:115], v[72:75]
v_mfma_f32_16x16x32_bf16 v[72:75], a[128:131], v[116:119], v[72:75]
v_mfma_f32_16x16x32_bf16 v[72:75], a[140:143], v[120:123], v[72:75]
s_waitcnt lgkmcnt(5)
v_mfma_f32_16x16x32_bf16 v[64:67], a[144:147], v[76:79], v[64:67]
s_waitcnt lgkmcnt(4)
v_mfma_f32_16x16x32_bf16 v[64:67], a[156:159], v[80:83], v[64:67]
s_waitcnt lgkmcnt(3)
v_mfma_f32_16x16x32_bf16 v[64:67], a[168:171], v[84:87], v[64:67]
s_waitcnt lgkmcnt(2)
v_mfma_f32_16x16x32_bf16 v[64:67], a[180:183], v[88:91], v[64:67]
s_waitcnt lgkmcnt(1)
v_mfma_f32_16x16x32_bf16 v[64:67], a[192:195], v[92:95], v[64:67]
s_waitcnt lgkmcnt(0)
ds_read_b128 v[100:103], v17 offset:43008
v_mfma_f32_16x16x32_bf16 v[64:67], a[204:207], v[96:99], v[64:67]
s_add_u32 m0, s45, 24576
v_mfma_f32_16x16x32_bf16 v[68:71], a[148:151], v[76:79], v[68:71]
ds_read_b128 v[104:107], v17 offset:44032
ds_read_b128 v[108:111], v17 offset:45056
v_mfma_f32_16x16x32_bf16 v[68:71], a[160:163], v[80:83], v[68:71]
v_mfma_f32_16x16x32_bf16 v[68:71], a[172:175], v[84:87], v[68:71]
s_barrier
v_mfma_f32_16x16x32_bf16 v[68:71], a[184:187], v[88:91], v[68:71]
v_mfma_f32_16x16x32_bf16 v[68:71], a[196:199], v[92:95], v[68:71]
ds_read_b128 v[112:115], v17 offset:46080
global_load_lds_dwordx4 v9, s[38:39] offset:-2048 sc1
v_mfma_f32_16x16x32_bf16 v[68:71], a[208:211], v[96:99], v[68:71]
v_mfma_f32_16x16x32_bf16 v[72:75], a[152:155], v[76:79], v[72:75]
ds_read_b128 v[116:119], v17 offset:47104
v_mfma_f32_16x16x32_bf16 v[72:75], a[164:167], v[80:83], v[72:75]
global_load_lds_dwordx4 v9, s[38:39] offset:-1024 sc1
v_mfma_f32_16x16x32_bf16 v[72:75], a[176:179], v[84:87], v[72:75]
ds_read_b128 v[120:123], v17 offset:48128
v_mfma_f32_16x16x32_bf16 v[72:75], a[188:191], v[88:91], v[72:75]
v_mfma_f32_16x16x32_bf16 v[72:75], a[200:203], v[92:95], v[72:75]
global_load_lds_dwordx4 v9, s[38:39] offset:0 sc1
v_mfma_f32_16x16x32_bf16 v[72:75], a[212:215], v[96:99], v[72:75]
s_waitcnt lgkmcnt(5)
v_mfma_f32_16x16x32_bf16 v[64:67], a[216:219], v[100:103], v[64:67]
s_waitcnt lgkmcnt(4)
v_mfma_f32_16x16x32_bf16 v[64:67], a[228:231], v[104:107], v[64:67]
s_waitcnt lgkmcnt(3)
global_load_lds_dwordx4 v9, s[38:39] offset:1024 sc1
v_mfma_f32_16x16x32_bf16 v[64:67], a[240:243], v[108:111], v[64:67]
s_waitcnt lgkmcnt(2)
v_mfma_f32_16x16x32_bf16 v[64:67], a[252:255], v[112:115], v[64:67]
s_waitcnt lgkmcnt(1)
v_mfma_f32_16x16x32_bf16 v[64:67], v[28:31], v[116:119], v[64:67]
s_waitcnt lgkmcnt(0)
global_load_lds_dwordx4 v9, s[38:39] offset:2048 sc1
v_mfma_f32_16x16x32_bf16 v[64:67], v[40:43], v[120:123], v[64:67]
v_xor_b32 v17, 0x10000, v17
v_mfma_f32_16x16x32_bf16 v[68:71], a[220:223], v[100:103], v[68:71]
v_mfma_f32_16x16x32_bf16 v[68:71], a[232:235], v[104:107], v[68:71]
v_mfma_f32_16x16x32_bf16 v[68:71], a[244:247], v[108:111], v[68:71]
s_waitcnt vmcnt(7)
s_barrier
v_mfma_f32_16x16x32_bf16 v[68:71], v[20:23], v[112:115], v[68:71]
ds_read_b128 v[76:79], v17 offset:0
global_load_lds_dwordx4 v9, s[38:39] offset:3072 sc1
v_mfma_f32_16x16x32_bf16 v[68:71], v[32:35], v[116:119], v[68:71]
ds_read_b128 v[80:83], v17 offset:1024
v_add_u32 v9, 147456, v9
v_mfma_f32_16x16x32_bf16 v[68:71], v[44:47], v[120:123], v[68:71]
ds_read_b128 v[84:87], v17 offset:2048
v_min_u32 v9, v9, v10
v_mfma_f32_16x16x32_bf16 v[72:75], a[224:227], v[100:103], v[72:75]
ds_read_b128 v[88:91], v17 offset:3072
v_cvt_pk_bf16_f32 v124, v64, v65
v_mfma_f32_16x16x32_bf16 v[72:75], a[236:239], v[104:107], v[72:75]
ds_read_b128 v[92:95], v17 offset:4096
v_cvt_pk_bf16_f32 v125, v66, v67
v_mfma_f32_16x16x32_bf16 v[72:75], a[248:251], v[108:111], v[72:75]
ds_read_b128 v[96:99], v17 offset:5120
v_mfma_f32_16x16x32_bf16 v[72:75], v[24:27], v[112:115], v[72:75]
v_cvt_pk_bf16_f32 v126, v68, v69
v_cvt_pk_bf16_f32 v127, v70, v71
v_mfma_f32_16x16x32_bf16 v[72:75], v[36:39], v[116:119], v[72:75]
v_mfma_f32_16x16x32_bf16 v[72:75], v[48:51], v[120:123], v[72:75]
s_sub_u32 s70, s70, 1
s_cbranch_scc0 .Ldn_top6
v_cvt_pk_bf16_f32 v124, v64, v65
v_cvt_pk_bf16_f32 v125, v66, v67
v_cvt_pk_bf16_f32 v126, v68, v69
v_cvt_pk_bf16_f32 v127, v70, v71
global_store_dwordx4 v11, v[124:127], s[38:39] sc1
v_add_u32 v11, 110592, v11
v_cvt_pk_bf16_f32 v128, v72, v73
v_cvt_pk_bf16_f32 v130, v72, v73
v_cvt_pk_bf16_f32 v129, v74, v75
v_cvt_pk_bf16_f32 v131, v74, v75
v_permlane16_swap_b32 v128, v130
s_nop 0
v_permlane16_swap_b32 v129, v131
s_mov_b64 exec, s[76:77]
global_store_dwordx4 v12, v[128:131], s[38:39] sc1
s_mov_b64 exec, -1
v_add_u32 v12, 110592, v12
s_waitcnt vmcnt(2)
s_barrier
s_waitcnt vmcnt(0)
s_barrier
s_endpgm
.Ldn_w3:
v_add_u32 v19, -1, v14
v_add_u32 v254, -1, v14
v_add_lshl_u32 v252, v16, 0, 7
v_bfi_b32 v252, s69, v252, v15
v_add_lshl_u32 v253, v14, 0, 7
v_bfi_b32 v253, s69, v253, v13
s_mov_b32 s76, 0xffff
s_mov_b32 s77, 0xffff
s_add_u32 m0, s45, 24576
s_mov_b32 s70, 32
s_waitcnt vmcnt(0) lgkmcnt(0)
s_nop 7
.p2align 6
.Ldn_top7:
s_waitcnt lgkmcnt(5)
v_mfma_f32_16x16x32_bf16 v[52:55], a[0:3], v[76:79], 0
s_waitcnt lgkmcnt(4)
v_mfma_f32_16x16x32_bf16 v[52:55], a[12:15], v[80:83], v[52:55]
s_waitcnt lgkmcnt(3)
v_mfma_f32_16x16x32_bf16 v[52:55], a[24:27], v[84:87], v[52:55]
s_waitcnt lgkmcnt(2)
v_mfma_f32_16x16x32_bf16 v[52:55], a[36:39], v[88:91], v[52:55]
s_waitcnt lgkmcnt(1)
ds_read_b128 v[100:103], v17 offset:6144
v_cvt_pk_bf16_f32 v128, v72, v73
v_mfma_f32_16x16x32_bf16 v[52:55], a[48:51], v[92:95], v[52:55]
s_waitcnt lgkmcnt(1)
v_mfma_f32_16x16x32_bf16 v[52:55], a[60:63], v[96:99], v[52:55]
s_waitcnt vmcnt(7)
s_barrier
v_mfma_f32_16x16x32_bf16 v[56:59], a[4:7], v[76:79], 0
ds_read_b128 v[104:107], v17 offset:7168
v_cvt_pk_bf16_f32 v130, v72, v73
v_mfma_f32_16x16x32_bf16 v[56:59], a[16:19], v[80:83], v[56:59]
s_mov_b64 exec, 1
global_store_dword v252, v16, s[38:39] sc1
s_mov_b64 exec, -1
v_mfma_f32_16x16x32_bf16 v[56:59], a[28:31], v[84:87], v[56:59]
ds_read_b128 v[108:111], v17 offset:8192
v_add_u32 v16, 1, v16
v_mfma_f32_16x16x32_bf16 v[56:59], a[40:43], v[88:91], v[56:59]
v_add_lshl_u32 v252, v16, 0, 7
v_bfi_b32 v252, s69, v252, v15
v_mfma_f32_16x16x32_bf16 v[56:59], a[52:55], v[92:95], v[56:59]
ds_read_b128 v[112:115], v17 offset:9216
v_cvt_pk_bf16_f32 v129, v74, v75
v_mfma_f32_16x16x32_bf16 v[56:59], a[64:67], v[96:99], v[56:59]
global_store_dwordx4 v11, v[124:127], s[38:39] sc1
v_mfma_f32_16x16x32_bf16 v[60:63], a[8:11], v[76:79], 0
ds_read_b128 v[116:119], v17 offset:10240
v_add_u32 v11, 110592, v11
v_mfma_f32_16x16x32_bf16 v[60:63], a[20:23], v[80:83], v[60:63]
v_cvt_pk_bf16_f32 v131, v74, v75
v_mfma_f32_16x16x32_bf16 v[60:63], a[32:35], v[84:87], v[60:63]
ds_read_b128 v[120:123], v17 offset:11264
v_mfma_f32_16x16x32_bf16 v[60:63], a[44:47], v[88:91], v[60:63]
v_permlane16_swap_b32 v128, v130
v_mfma_f32_16x16x32_bf16 v[60:63], a[56:59], v[92:95], v[60:63]
v_permlane16_swap_b32 v129, v131
v_mfma_f32_16x16x32_bf16 v[60:63], a[68:71], v[96:99], v[60:63]
s_waitcnt lgkmcnt(5)
s_mov_b64 exec, s[76:77]
global_store_dwordx4 v12, v[128:131], s[38:39] sc1
s_mov_b64 exec, -1
v_mfma_f32_16x16x32_bf16 v[52:55], a[72:75], v[100:103], v[52:55]
s_waitcnt lgkmcnt(4)
v_add_u32 v12, 110592, v12
v_mfma_f32_16x16x32_bf16 v[52:55], a[84:87], v[104:107], v[52:55]
s_waitcnt lgkmcnt(3)
v_mfma_f32_16x16x32_bf16 v[52:55], a[96:99], v[108:111], v[52:55]
s_waitcnt lgkmcnt(2)
v_mfma_f32_16x16x32_bf16 v[52:55], a[108:111], v[112:115], v[52:55]
s_waitcnt lgkmcnt(1)
v_mfma_f32_16x16x32_bf16 v[52:55], a[120:123], v[116:119], v[52:55]
s_waitcnt lgkmcnt(0)
ds_read_b128 v[76:79], v17 offset:12288
v_mfma_f32_16x16x32_bf16 v[52:55], a[132:135], v[120:123], v[52:55]
v_mfma_f32_16x16x32_bf16 v[56:59], a[76:79], v[100:103], v[56:59]
ds_read_b128 v[80:83], v17 offset:13312
v_mfma_f32_16x16x32_bf16 v[56:59], a[88:91], v[104:107], v[56:59]
v_mfma_f32_16x16x32_bf16 v[56:59], a[100:103], v[108:111], v[56:59]
ds_read_b128 v[84:87], v17 offset:14336
v_mfma_f32_16x16x32_bf16 v[56:59], a[112:115], v[112:115], v[56:59]
v_mfma_f32_16x16x32_bf16 v[56:59], a[124:127], v[116:119], v[56:59]
ds_read_b128 v[88:91], v17 offset:15360
v_mfma_f32_16x16x32_bf16 v[56:59], a[136:139], v[120:123], v[56:59]
v_mfma_f32_16x16x32_bf16 v[60:63], a[80:83], v[100:103], v[60:63]
ds_read_b128 v[92:95], v17 offset:16384
v_mfma_f32_16x16x32_bf16 v[60:63], a[92:95], v[104:107], v[60:63]
v_mfma_f32_16x16x32_bf16 v[60:63], a[104:107], v[108:111], v[60:63]
ds_read_b128 v[96:99], v17 offset:17408
v_mfma_f32_16x16x32_bf16 v[60:63], a[116:119], v[112:115], v[60:63]
v_mfma_f32_16x16x32_bf16 v[60:63], a[128:131], v[116:119], v[60:63]
v_mfma_f32_16x16x32_bf16 v[60:63], a[140:143], v[120:123], v[60:63]
s_waitcnt lgkmcnt(5)
v_mfma_f32_16x16x32_bf16 v[52:55], a[144:147], v[76:79], v[52:55]
s_waitcnt lgkmcnt(4)
v_mfma_f32_16x16x32_bf16 v[52:55], a[156:159], v[80:83], v[52:55]
s_waitcnt lgkmcnt(3)
v_mfma_f32_16x16x32_bf16 v[52:55], a[168:171], v[84:87], v[52:55]
s_waitcnt lgkmcnt(2)
v_mfma_f32_16x16x32_bf16 v[52:55], a[180:183], v[88:91], v[52:55]
s_waitcnt lgkmcnt(1)
v_mfma_f32_16x16x32_bf16 v[52:55], a[192:195], v[92:95], v[52:55]
s_xor_b32 s45, s45, 0x10000
ds_read_b128 v[100:103], v17 offset:18432
s_waitcnt lgkmcnt(1)
v_mfma_f32_16x16x32_bf16 v[52:55], a[204:207], v[96:99], v[52:55]
s_add_u32 m0, s45, 0
v_mfma_f32_16x16x32_bf16 v[56:59], a[148:151], v[76:79], v[56:59]
ds_read_b128 v[104:107], v17 offset:19456
ds_read_b128 v[108:111], v17 offset:20480
v_mfma_f32_16x16x32_bf16 v[56:59], a[160:163], v[80:83], v[56:59]
v_mfma_f32_16x16x32_bf16 v[56:59], a[172:175], v[84:87], v[56:59]
s_barrier
v_mfma_f32_16x16x32_bf16 v[56:59], a[184:187], v[88:91], v[56:59]
v_mfma_f32_16x16x32_bf16 v[56:59], a[196:199], v[92:95], v[56:59]
ds_read_b128 v[112:115], v17 offset:21504
v_mfma_f32_16x16x32_bf16 v[56:59], a[208:211], v[96:99], v[56:59]
global_load_lds_dwordx4 v9, s[38:39] offset:-2048 sc1
v_mfma_f32_16x16x32_bf16 v[60:63], a[152:155], v[76:79], v[60:63]
ds_read_b128 v[116:119], v17 offset:22528
v_mfma_f32_16x16x32_bf16 v[60:63], a[164:167], v[80:83], v[60:63]
v_mfma_f32_16x16x32_bf16 v[60:63], a[176:179], v[84:87], v[60:63]
ds_read_b128 v[120:123], v17 offset:23552
global_load_lds_dwordx4 v9, s[38:39] offset:-1024 sc1
v_mfma_f32_16x16x32_bf16 v[60:63], a[188:191], v[88:91], v[60:63]
v_mfma_f32_16x16x32_bf16 v[60:63], a[200:203], v[92:95], v[60:63]
v_mfma_f32_16x16x32_bf16 v[60:63], a[212:215], v[96:99], v[60:63]
s_waitcnt lgkmcnt(5)
global_load_lds_dwordx4 v9, s[38:39] offset:0 sc1
v_mfma_f32_16x16x32_bf16 v[52:55], a[216:219], v[100:103], v[52:55]
s_waitcnt lgkmcnt(4)
v_mfma_f32_16x16x32_bf16 v[52:55], a[228:231], v[104:107], v[52:55]
s_waitcnt lgkmcnt(3)
v_mfma_f32_16x16x32_bf16 v[52:55], a[240:243], v[108:111], v[52:55]
s_waitcnt lgkmcnt(2)
)"
R"(global_load_lds_dwordx4 v9, s[38:39] offset:1024 sc1
v_mfma_f32_16x16x32_bf16 v[52:55], a[252:255], v[112:115], v[52:55]
s_waitcnt lgkmcnt(1)
v_mfma_f32_16x16x32_bf16 v[52:55], v[28:31], v[116:119], v[52:55]
s_waitcnt lgkmcnt(0)
v_mfma_f32_16x16x32_bf16 v[52:55], v[40:43], v[120:123], v[52:55]
global_load_lds_dwordx4 v9, s[38:39] offset:2048 sc1
v_mfma_f32_16x16x32_bf16 v[56:59], a[220:223], v[100:103], v[56:59]
v_mfma_f32_16x16x32_bf16 v[56:59], a[232:235], v[104:107], v[56:59]
v_mfma_f32_16x16x32_bf16 v[56:59], a[244:247], v[108:111], v[56:59]
s_waitcnt vmcnt(8)
s_barrier
v_mfma_f32_16x16x32_bf16 v[56:59], v[20:23], v[112:115], v[56:59]
ds_read_b128 v[76:79], v17 offset:24576
global_load_lds_dwordx4 v9, s[38:39] offset:3072 sc1
v_mfma_f32_16x16x32_bf16 v[56:59], v[32:35], v[116:119], v[56:59]
ds_read_b128 v[80:83], v17 offset:25600
v_add_u32 v9, 147456, v9
v_mfma_f32_16x16x32_bf16 v[56:59], v[44:47], v[120:123], v[56:59]
ds_read_b128 v[84:87], v17 offset:26624
v_min_u32 v9, v9, v10
v_mfma_f32_16x16x32_bf16 v[60:63], a[224:227], v[100:103], v[60:63]
ds_read_b128 v[88:91], v17 offset:27648
v_cvt_pk_bf16_f32 v124, v52, v53
v_mfma_f32_16x16x32_bf16 v[60:63], a[236:239], v[104:107], v[60:63]
ds_read_b128 v[92:95], v17 offset:28672
v_cvt_pk_bf16_f32 v125, v54, v55
v_mfma_f32_16x16x32_bf16 v[60:63], a[248:251], v[108:111], v[60:63]
ds_read_b128 v[96:99], v17 offset:29696
v_mfma_f32_16x16x32_bf16 v[60:63], v[24:27], v[112:115], v[60:63]
v_cvt_pk_bf16_f32 v126, v56, v57
v_cvt_pk_bf16_f32 v127, v58, v59
v_mfma_f32_16x16x32_bf16 v[60:63], v[36:39], v[116:119], v[60:63]
v_mfma_f32_16x16x32_bf16 v[60:63], v[48:51], v[120:123], v[60:63]
s_waitcnt lgkmcnt(5)
v_mfma_f32_16x16x32_bf16 v[64:67], a[0:3], v[76:79], 0
s_waitcnt lgkmcnt(4)
v_mfma_f32_16x16x32_bf16 v[64:67], a[12:15], v[80:83], v[64:67]
s_waitcnt lgkmcnt(3)
v_mfma_f32_16x16x32_bf16 v[64:67], a[24:27], v[84:87], v[64:67]
s_waitcnt lgkmcnt(2)
v_mfma_f32_16x16x32_bf16 v[64:67], a[36:39], v[88:91], v[64:67]
s_waitcnt lgkmcnt(1)
ds_read_b128 v[100:103], v17 offset:30720
v_cvt_pk_bf16_f32 v128, v60, v61
v_mfma_f32_16x16x32_bf16 v[64:67], a[48:51], v[92:95], v[64:67]
s_waitcnt lgkmcnt(1)
v_mfma_f32_16x16x32_bf16 v[64:67], a[60:63], v[96:99], v[64:67]
s_waitcnt vmcnt(6)
s_barrier
v_mfma_f32_16x16x32_bf16 v[68:71], a[4:7], v[76:79], 0
ds_read_b128 v[104:107], v17 offset:31744
v_cvt_pk_bf16_f32 v130, v60, v61
v_mfma_f32_16x16x32_bf16 v[68:71], a[16:19], v[80:83], v[68:71]
v_cvt_pk_bf16_f32 v129, v62, v63
v_cvt_pk_bf16_f32 v131, v62, v63
v_mfma_f32_16x16x32_bf16 v[68:71], a[28:31], v[84:87], v[68:71]
ds_read_b128 v[108:111], v17 offset:32768
v_mfma_f32_16x16x32_bf16 v[68:71], a[40:43], v[88:91], v[68:71]
global_store_dwordx4 v11, v[124:127], s[38:39] sc1
v_mfma_f32_16x16x32_bf16 v[68:71], a[52:55], v[92:95], v[68:71]
ds_read_b128 v[112:115], v17 offset:33792
v_add_u32 v11, 110592, v11
v_mfma_f32_16x16x32_bf16 v[68:71], a[64:67], v[96:99], v[68:71]
v_permlane16_swap_b32 v128, v130
v_mfma_f32_16x16x32_bf16 v[72:75], a[8:11], v[76:79], 0
ds_read_b128 v[116:119], v17 offset:34816
v_mfma_f32_16x16x32_bf16 v[72:75], a[20:23], v[80:83], v[72:75]
v_permlane16_swap_b32 v129, v131
v_mfma_f32_16x16x32_bf16 v[72:75], a[32:35], v[84:87], v[72:75]
ds_read_b128 v[120:123], v17 offset:35840
v_mfma_f32_16x16x32_bf16 v[72:75], a[44:47], v[88:91], v[72:75]
s_mov_b64 exec, s[76:77]
global_store_dwordx4 v12, v[128:131], s[38:39] sc1
s_mov_b64 exec, -1
v_mfma_f32_16x16x32_bf16 v[72:75], a[56:59], v[92:95], v[72:75]
v_add_u32 v12, 110592, v12
v_mfma_f32_16x16x32_bf16 v[72:75], a[68:71], v[96:99], v[72:75]
s_waitcnt lgkmcnt(5)
s_mov_b64 exec, 1
global_store_dword v252, v16, s[38:39] sc1
s_mov_b64 exec, -1
v_mfma_f32_16x16x32_bf16 v[64:67], a[72:75], v[100:103], v[64:67]
s_waitcnt lgkmcnt(4)
v_add_u32 v16, 1, v16
v_add_lshl_u32 v252, v16, 0, 7
v_mfma_f32_16x16x32_bf16 v[64:67], a[84:87], v[104:107], v[64:67]
s_waitcnt lgkmcnt(3)
v_bfi_b32 v252, s69, v252, v15
v_mfma_f32_16x16x32_bf16 v[64:67], a[96:99], v[108:111], v[64:67]
s_waitcnt lgkmcnt(2)
v_mfma_f32_16x16x32_bf16 v[64:67], a[108:111], v[112:115], v[64:67]
s_waitcnt lgkmcnt(1)
v_mfma_f32_16x16x32_bf16 v[64:67], a[120:123], v[116:119], v[64:67]
s_waitcnt lgkmcnt(0)
ds_read_b128 v[76:79], v17 offset:36864
v_mfma_f32_16x16x32_bf16 v[64:67], a[132:135], v[120:123], v[64:67]
v_mfma_f32_16x16x32_bf16 v[68:71], a[76:79], v[100:103], v[68:71]
ds_read_b128 v[80:83], v17 offset:37888
v_mfma_f32_16x16x32_bf16 v[68:71], a[88:91], v[104:107], v[68:71]
v_mfma_f32_16x16x32_bf16 v[68:71], a[100:103], v[108:111], v[68:71]
ds_read_b128 v[84:87], v17 offset:38912
v_mfma_f32_16x16x32_bf16 v[68:71], a[112:115], v[112:115], v[68:71]
v_mfma_f32_16x16x32_bf16 v[68:71], a[124:127], v[116:119], v[68:71]
ds_read_b128 v[88:91], v17 offset:39936
v_mfma_f32_16x16x32_bf16 v[68:71], a[136:139], v[120:123], v[68:71]
v_mfma_f32_16x16x32_bf16 v[72:75], a[80:83], v[100:103], v[72:75]
ds_read_b128 v[92:95], v17 offset:40960
v_mfma_f32_16x16x32_bf16 v[72:75], a[92:95], v[104:107], v[72:75]
v_mfma_f32_16x16x32_bf16 v[72:75], a[104:107], v[108:111], v[72:75]
ds_read_b128 v[96:99], v17 offset:41984
v_mfma_f32_16x16x32_bf16 v[72:75], a[116:119], v[112:115], v[72:75]
v_mfma_f32_16x16x32_bf16 v[72:75], a[128:131], v[116:119], v[72:75]
v_mfma_f32_16x16x32_bf16 v[72:75], a[140:143], v[120:123], v[72:75]
s_waitcnt lgkmcnt(5)
v_mfma_f32_16x16x32_bf16 v[64:67], a[144:147], v[76:79], v[64:67]
s_waitcnt lgkmcnt(4)
v_mfma_f32_16x16x32_bf16 v[64:67], a[156:159], v[80:83], v[64:67]
s_waitcnt lgkmcnt(3)
v_mfma_f32_16x16x32_bf16 v[64:67], a[168:171], v[84:87], v[64:67]
s_waitcnt lgkmcnt(2)
v_mfma_f32_16x16x32_bf16 v[64:67], a[180:183], v[88:91], v[64:67]
s_waitcnt lgkmcnt(1)
v_mfma_f32_16x16x32_bf16 v[64:67], a[192:195], v[92:95], v[64:67]
s_waitcnt lgkmcnt(0)
ds_read_b128 v[100:103], v17 offset:43008
v_mfma_f32_16x16x32_bf16 v[64:67], a[204:207], v[96:99], v[64:67]
s_add_u32 m0, s45, 24576
v_mfma_f32_16x16x32_bf16 v[68:71], a[148:151], v[76:79], v[68:71]
ds_read_b128 v[104:107], v17 offset:44032
ds_read_b128 v[108:111], v17 offset:45056
v_mfma_f32_16x16x32_bf16 v[68:71], a[160:163], v[80:83], v[68:71]
v_mfma_f32_16x16x32_bf16 v[68:71], a[172:175], v[84:87], v[68:71]
s_barrier
v_mfma_f32_16x16x32_bf16 v[68:71], a[184:187], v[88:91], v[68:71]
v_mfma_f32_16x16x32_bf16 v[68:71], a[196:199], v[92:95], v[68:71]
ds_read_b128 v[112:115], v17 offset:46080
v_mfma_f32_16x16x32_bf16 v[68:71], a[208:211], v[96:99], v[68:71]
global_load_lds_dwordx4 v9, s[38:39] offset:-2048 sc1
v_mfma_f32_16x16x32_bf16 v[72:75], a[152:155], v[76:79], v[72:75]
ds_read_b128 v[116:119], v17 offset:47104
v_mfma_f32_16x16x32_bf16 v[72:75], a[164:167], v[80:83], v[72:75]
v_mfma_f32_16x16x32_bf16 v[72:75], a[176:179], v[84:87], v[72:75]
ds_read_b128 v[120:123], v17 offset:48128
global_load_lds_dwordx4 v9, s[38:39] offset:-1024 sc1
v_mfma_f32_16x16x32_bf16 v[72:75], a[188:191], v[88:91], v[72:75]
v_mfma_f32_16x16x32_bf16 v[72:75], a[200:203], v[92:95], v[72:75]
v_mfma_f32_16x16x32_bf16 v[72:75], a[212:215], v[96:99], v[72:75]
s_waitcnt lgkmcnt(5)
global_load_lds_dwordx4 v9, s[38:39] offset:0 sc1
v_mfma_f32_16x16x32_bf16 v[64:67], a[216:219], v[100:103], v[64:67]
s_waitcnt lgkmcnt(4)
v_mfma_f32_16x16x32_bf16 v[64:67], a[228:231], v[104:107], v[64:67]
s_waitcnt lgkmcnt(3)
v_mfma_f32_16x16x32_bf16 v[64:67], a[240:243], v[108:111], v[64:67]
s_waitcnt lgkmcnt(2)
global_load_lds_dwordx4 v9, s[38:39] offset:1024 sc1
v_mfma_f32_16x16x32_bf16 v[64:67], a[252:255], v[112:115], v[64:67]
s_waitcnt lgkmcnt(1)
v_mfma_f32_16x16x32_bf16 v[64:67], v[28:31], v[116:119], v[64:67]
s_waitcnt lgkmcnt(0)
v_mfma_f32_16x16x32_bf16 v[64:67], v[40:43], v[120:123], v[64:67]
v_xor_b32 v17, 0x10000, v17
global_load_lds_dwordx4 v9, s[38:39] offset:2048 sc1
v_mfma_f32_16x16x32_bf16 v[68:71], a[220:223], v[100:103], v[68:71]
v_mfma_f32_16x16x32_bf16 v[68:71], a[232:235], v[104:107], v[68:71]
v_mfma_f32_16x16x32_bf16 v[68:71], a[244:247], v[108:111], v[68:71]
s_waitcnt vmcnt(8)
s_barrier
v_mfma_f32_16x16x32_bf16 v[68:71], v[20:23], v[112:115], v[68:71]
ds_read_b128 v[76:79], v17 offset:0
global_load_lds_dwordx4 v9, s[38:39] offset:3072 sc1
v_mfma_f32_16x16x32_bf16 v[68:71], v[32:35], v[116:119], v[68:71]
ds_read_b128 v[80:83], v17 offset:1024
v_add_u32 v9, 147456, v9
v_mfma_f32_16x16x32_bf16 v[68:71], v[44:47], v[120:123], v[68:71]
ds_read_b128 v[84:87], v17 offset:2048
v_min_u32 v9, v9, v10
v_mfma_f32_16x16x32_bf16 v[72:75], a[224:227], v[100:103], v[72:75]
ds_read_b128 v[88:91], v17 offset:3072
v_cvt_pk_bf16_f32 v124, v64, v65
v_mfma_f32_16x16x32_bf16 v[72:75], a[236:239], v[104:107], v[72:75]
ds_read_b128 v[92:95], v17 offset:4096
v_cvt_pk_bf16_f32 v125, v66, v67
v_mfma_f32_16x16x32_bf16 v[72:75], a[248:251], v[108:111], v[72:75]
ds_read_b128 v[96:99], v17 offset:5120
v_mfma_f32_16x16x32_bf16 v[72:75], v[24:27], v[112:115], v[72:75]
v_cvt_pk_bf16_f32 v126, v68, v69
v_cvt_pk_bf16_f32 v127, v70, v71
v_mfma_f32_16x16x32_bf16 v[72:75], v[36:39], v[116:119], v[72:75]
v_mfma_f32_16x16x32_bf16 v[72:75], v[48:51], v[120:123], v[72:75]
s_sub_u32 s70, s70, 1
s_cbranch_scc0 .Ldn_top7
v_cvt_pk_bf16_f32 v124, v64, v65
v_cvt_pk_bf16_f32 v125, v66, v67
v_cvt_pk_bf16_f32 v126, v68, v69
v_cvt_pk_bf16_f32 v127, v70, v71
global_store_dwordx4 v11, v[124:127], s[38:39] sc1
v_add_u32 v11, 110592, v11
v_cvt_pk_bf16_f32 v128, v72, v73
v_cvt_pk_bf16_f32 v130, v72, v73
v_cvt_pk_bf16_f32 v129, v74, v75
v_cvt_pk_bf16_f32 v131, v74, v75
v_permlane16_swap_b32 v128, v130
s_nop 0
v_permlane16_swap_b32 v129, v131
s_mov_b64 exec, s[76:77]
global_store_dwordx4 v12, v[128:131], s[38:39] sc1
s_mov_b64 exec, -1
v_add_u32 v12, 110592, v12
s_waitcnt vmcnt(2)
s_barrier
s_mov_b64 exec, 1
global_store_dword v252, v16, s[38:39] sc1
s_mov_b64 exec, -1
v_add_u32 v16, 1, v16
v_add_lshl_u32 v252, v16, 0, 7
v_bfi_b32 v252, s69, v252, v15
s_waitcnt vmcnt(1)
s_barrier
s_mov_b64 exec, 1
global_store_dword v252, v16, s[38:39] sc1
s_mov_b64 exec, -1
s_endpgm
.Ldn_w4:
v_add_u32 v19, -1, v14
v_add_u32 v254, -1, v14
v_add_lshl_u32 v252, v16, 0, 7
v_bfi_b32 v252, s69, v252, v15
v_add_lshl_u32 v253, v14, 0, 7
v_bfi_b32 v253, s69, v253, v13
s_mov_b32 s76, 0xffff
s_mov_b32 s77, 0xffff
s_add_u32 m0, s45, 24576
s_mov_b32 s70, 32
s_waitcnt vmcnt(0) lgkmcnt(0)
s_nop 7
.p2align 6
.Ldn_top12:
s_waitcnt lgkmcnt(5)
v_mfma_f32_16x16x32_bf16 v[52:55], a[0:3], v[76:79], 0
s_waitcnt lgkmcnt(4)
v_mfma_f32_16x16x32_bf16 v[52:55], a[12:15], v[80:83], v[52:55]
s_waitcnt lgkmcnt(3)
global_load_dword v19, v253, s[38:39] sc1
v_mfma_f32_16x16x32_bf16 v[52:55], a[24:27], v[84:87], v[52:55]
s_waitcnt lgkmcnt(2)
v_mfma_f32_16x16x32_bf16 v[52:55], a[36:39], v[88:91], v[52:55]
s_waitcnt lgkmcnt(1)
ds_read_b128 v[100:103], v17 offset:6144
v_cvt_pk_bf16_f32 v128, v72, v73
v_mfma_f32_16x16x32_bf16 v[52:55], a[48:51], v[92:95], v[52:55]
s_waitcnt lgkmcnt(1)
v_mfma_f32_16x16x32_bf16 v[52:55], a[60:63], v[96:99], v[52:55]
s_waitcnt vmcnt(7)
s_barrier
v_mfma_f32_16x16x32_bf16 v[56:59], a[4:7], v[76:79], 0
ds_read_b128 v[104:107], v17 offset:7168
v_cvt_pk_bf16_f32 v130, v72, v73
v_mfma_f32_16x16x32_bf16 v[56:59], a[16:19], v[80:83], v[56:59]
v_cvt_pk_bf16_f32 v129, v74, v75
v_cvt_pk_bf16_f32 v131, v74, v75
v_mfma_f32_16x16x32_bf16 v[56:59], a[28:31], v[84:87], v[56:59]
ds_read_b128 v[108:111], v17 offset:8192
v_mfma_f32_16x16x32_bf16 v[56:59], a[40:43], v[88:91], v[56:59]
v_permlane16_swap_b32 v128, v130
v_mfma_f32_16x16x32_bf16 v[56:59], a[52:55], v[92:95], v[56:59]
ds_read_b128 v[112:115], v17 offset:9216
v_mfma_f32_16x16x32_bf16 v[56:59], a[64:67], v[96:99], v[56:59]
global_store_dwordx4 v11, v[124:127], s[38:39] sc1
v_mfma_f32_16x16x32_bf16 v[60:63], a[8:11], v[76:79], 0
ds_read_b128 v[116:119], v17 offset:10240
v_add_u32 v11, 110592, v11
v_mfma_f32_16x16x32_bf16 v[60:63], a[20:23], v[80:83], v[60:63]
v_permlane16_swap_b32 v129, v131
v_mfma_f32_16x16x32_bf16 v[60:63], a[32:35], v[84:87], v[60:63]
ds_read_b128 v[120:123], v17 offset:11264
v_mfma_f32_16x16x32_bf16 v[60:63], a[44:47], v[88:91], v[60:63]
s_mov_b64 exec, s[76:77]
global_store_dwordx4 v12, v[128:131], s[38:39] sc1
s_mov_b64 exec, -1
v_mfma_f32_16x16x32_bf16 v[60:63], a[56:59], v[92:95], v[60:63]
v_add_u32 v12, 110592, v12
v_mfma_f32_16x16x32_bf16 v[60:63], a[68:71], v[96:99], v[60:63]
s_waitcnt lgkmcnt(5)
v_mfma_f32_16x16x32_bf16 v[52:55], a[72:75], v[100:103], v[52:55]
s_waitcnt lgkmcnt(4)
v_mfma_f32_16x16x32_bf16 v[52:55], a[84:87], v[104:107], v[52:55]
s_waitcnt lgkmcnt(3)
v_mfma_f32_16x16x32_bf16 v[52:55], a[96:99], v[108:111], v[52:55]
s_waitcnt lgkmcnt(2)
v_mfma_f32_16x16x32_bf16 v[52:55], a[108:111], v[112:115], v[52:55]
s_waitcnt lgkmcnt(1)
v_mfma_f32_16x16x32_bf16 v[52:55], a[120:123], v[116:119], v[52:55]
s_waitcnt lgkmcnt(0)
ds_read_b128 v[76:79], v17 offset:12288
v_mfma_f32_16x16x32_bf16 v[52:55], a[132:135], v[120:123], v[52:55]
v_mfma_f32_16x16x32_bf16 v[56:59], a[76:79], v[100:103], v[56:59]
ds_read_b128 v[80:83], v17 offset:13312
v_mfma_f32_16x16x32_bf16 v[56:59], a[88:91], v[104:107], v[56:59]
v_mfma_f32_16x16x32_bf16 v[56:59], a[100:103], v[108:111], v[56:59]
ds_read_b128 v[84:87], v17 offset:14336
v_mfma_f32_16x16x32_bf16 v[56:59], a[112:115], v[112:115], v[56:59]
v_mfma_f32_16x16x32_bf16 v[56:59], a[124:127], v[116:119], v[56:59]
ds_read_b128 v[88:91], v17 offset:15360
v_mfma_f32_16x16x32_bf16 v[56:59], a[136:139], v[120:123], v[56:59]
v_mfma_f32_16x16x32_bf16 v[60:63], a[80:83], v[100:103], v[60:63]
ds_read_b128 v[92:95], v17 offset:16384
v_mfma_f32_16x16x32_bf16 v[60:63], a[92:95], v[104:107], v[60:63]
v_mfma_f32_16x16x32_bf16 v[60:63], a[104:107], v[108:111], v[60:63]
ds_read_b128 v[96:99], v17 offset:17408
v_mfma_f32_16x16x32_bf16 v[60:63], a[116:119], v[112:115], v[60:63]
v_mfma_f32_16x16x32_bf16 v[60:63], a[128:131], v[116:119], v[60:63]
v_mfma_f32_16x16x32_bf16 v[60:63], a[140:143], v[120:123], v[60:63]
s_waitcnt lgkmcnt(5)
v_mfma_f32_16x16x32_bf16 v[52:55], a[144:147], v[76:79], v[52:55]
s_waitcnt lgkmcnt(4)
v_mfma_f32_16x16x32_bf16 v[52:55], a[156:159], v[80:83], v[52:55]
s_waitcnt lgkmcnt(3)
v_mfma_f32_16x16x32_bf16 v[52:55], a[168:171], v[84:87], v[52:55]
s_waitcnt lgkmcnt(2)
v_mfma_f32_16x16x32_bf16 v[52:55], a[180:183], v[88:91], v[52:55]
s_waitcnt vmcnt(2)
v_sub_u32 v19, v19, v14
v_cmp_lt_u32 vcc, 0x7f, v19
s_waitcnt lgkmcnt(1)
v_mfma_f32_16x16x32_bf16 v[52:55], a[192:195], v[92:95], v[52:55]
s_xor_b32 s45, s45, 0x10000
ds_read_b128 v[100:103], v17 offset:18432
s_waitcnt lgkmcnt(1)
v_mfma_f32_16x16x32_bf16 v[52:55], a[204:207], v[96:99], v[52:55]
s_add_u32 m0, s45, 0
v_mfma_f32_16x16x32_bf16 v[56:59], a[148:151], v[76:79], v[56:59]
s_cbranch_vccnz .Ldn_sp8
.Ldn_rt9:
ds_read_b128 v[104:107], v17 offset:19456
ds_read_b128 v[108:111], v17 offset:20480
v_mfma_f32_16x16x32_bf16 v[56:59], a[160:163], v[80:83], v[56:59]
v_mfma_f32_16x16x32_bf16 v[56:59], a[172:175], v[84:87], v[56:59]
s_barrier
v_mfma_f32_16x16x32_bf16 v[56:59], a[184:187], v[88:91], v[56:59]
v_add_u32 v14, 1, v14
v_add_lshl_u32 v253, v14, 0, 7
v_mfma_f32_16x16x32_bf16 v[56:59], a[196:199], v[92:95], v[56:59]
ds_read_b128 v[112:115], v17 offset:21504
v_bfi_b32 v253, s69, v253, v13
v_mfma_f32_16x16x32_bf16 v[56:59], a[208:211], v[96:99], v[56:59]
v_mfma_f32_16x16x32_bf16 v[60:63], a[152:155], v[76:79], v[60:63]
ds_read_b128 v[116:119], v17 offset:22528
global_load_lds_dwordx4 v9, s[38:39] offset:-2048 sc1
v_mfma_f32_16x16x32_bf16 v[60:63], a[164:167], v[80:83], v[60:63]
v_mfma_f32_16x16x32_bf16 v[60:63], a[176:179], v[84:87], v[60:63]
ds_read_b128 v[120:123], v17 offset:23552
v_mfma_f32_16x16x32_bf16 v[60:63], a[188:191], v[88:91], v[60:63]
global_load_lds_dwordx4 v9, s[38:39] offset:-1024 sc1
v_mfma_f32_16x16x32_bf16 v[60:63], a[200:203], v[92:95], v[60:63]
v_mfma_f32_16x16x32_bf16 v[60:63], a[212:215], v[96:99], v[60:63]
s_waitcnt lgkmcnt(5)
v_mfma_f32_16x16x32_bf16 v[52:55], a[216:219], v[100:103], v[52:55]
s_waitcnt lgkmcnt(4)
global_load_lds_dwordx4 v9, s[38:39] offset:0 sc1
v_mfma_f32_16x16x32_bf16 v[52:55], a[228:231], v[104:107], v[52:55]
s_waitcnt lgkmcnt(3)
v_mfma_f32_16x16x32_bf16 v[52:55], a[240:243], v[108:111], v[52:55]
s_waitcnt lgkmcnt(2)
v_mfma_f32_16x16x32_bf16 v[52:55], a[252:255], v[112:115], v[52:55]
s_waitcnt lgkmcnt(1)
global_load_lds_dwordx4 v9, s[38:39] offset:1024 sc1
v_mfma_f32_16x16x32_bf16 v[52:55], v[28:31], v[116:119], v[52:55]
s_waitcnt lgkmcnt(0)
v_mfma_f32_16x16x32_bf16 v[52:55], v[40:43], v[120:123], v[52:55]
v_mfma_f32_16x16x32_bf16 v[56:59], a[220:223], v[100:103], v[56:59]
global_load_lds_dwordx4 v9, s[38:39] offset:2048 sc1
v_mfma_f32_16x16x32_bf16 v[56:59], a[232:235], v[104:107], v[56:59]
v_mfma_f32_16x16x32_bf16 v[56:59], a[244:247], v[108:111], v[56:59]
s_barrier
v_mfma_f32_16x16x32_bf16 v[56:59], v[20:23], v[112:115], v[56:59]
ds_read_b128 v[76:79], v17 offset:24576
global_load_lds_dwordx4 v9, s[38:39] offset:3072 sc1
v_mfma_f32_16x16x32_bf16 v[56:59], v[32:35], v[116:119], v[56:59]
ds_read_b128 v[80:83], v17 offset:25600
v_add_u32 v9, 147456, v9
v_mfma_f32_16x16x32_bf16 v[56:59], v[44:47], v[120:123], v[56:59]
ds_read_b128 v[84:87], v17 offset:26624
v_min_u32 v9, v9, v10
v_mfma_f32_16x16x32_bf16 v[60:63], a[224:227], v[100:103], v[60:63]
ds_read_b128 v[88:91], v17 offset:27648
v_cvt_pk_bf16_f32 v124, v52, v53
v_mfma_f32_16x16x32_bf16 v[60:63], a[236:239], v[104:107], v[60:63]
ds_read_b128 v[92:95], v17 offset:28672
v_cvt_pk_bf16_f32 v125, v54, v55
v_mfma_f32_16x16x32_bf16 v[60:63], a[248:251], v[108:111], v[60:63]
ds_read_b128 v[96:99], v17 offset:29696
v_mfma_f32_16x16x32_bf16 v[60:63], v[24:27], v[112:115], v[60:63]
v_cvt_pk_bf16_f32 v126, v56, v57
v_cvt_pk_bf16_f32 v127, v58, v59
v_mfma_f32_16x16x32_bf16 v[60:63], v[36:39], v[116:119], v[60:63]
v_mfma_f32_16x16x32_bf16 v[60:63], v[48:51], v[120:123], v[60:63]
s_waitcnt lgkmcnt(5)
v_mfma_f32_16x16x32_bf16 v[64:67], a[0:3], v[76:79], 0
)"
R"(s_waitcnt lgkmcnt(4)
v_mfma_f32_16x16x32_bf16 v[64:67], a[12:15], v[80:83], v[64:67]
s_waitcnt lgkmcnt(3)
global_load_dword v254, v253, s[38:39] sc1
v_mfma_f32_16x16x32_bf16 v[64:67], a[24:27], v[84:87], v[64:67]
s_waitcnt lgkmcnt(2)
v_mfma_f32_16x16x32_bf16 v[64:67], a[36:39], v[88:91], v[64:67]
s_waitcnt lgkmcnt(1)
ds_read_b128 v[100:103], v17 offset:30720
v_cvt_pk_bf16_f32 v128, v60, v61
v_mfma_f32_16x16x32_bf16 v[64:67], a[48:51], v[92:95], v[64:67]
s_waitcnt lgkmcnt(1)
v_mfma_f32_16x16x32_bf16 v[64:67], a[60:63], v[96:99], v[64:67]
s_waitcnt vmcnt(7)
s_barrier
v_mfma_f32_16x16x32_bf16 v[68:71], a[4:7], v[76:79], 0
ds_read_b128 v[104:107], v17 offset:31744
v_cvt_pk_bf16_f32 v130, v60, v61
v_mfma_f32_16x16x32_bf16 v[68:71], a[16:19], v[80:83], v[68:71]
v_cvt_pk_bf16_f32 v129, v62, v63
v_cvt_pk_bf16_f32 v131, v62, v63
v_mfma_f32_16x16x32_bf16 v[68:71], a[28:31], v[84:87], v[68:71]
ds_read_b128 v[108:111], v17 offset:32768
v_mfma_f32_16x16x32_bf16 v[68:71], a[40:43], v[88:91], v[68:71]
v_permlane16_swap_b32 v128, v130
v_mfma_f32_16x16x32_bf16 v[68:71], a[52:55], v[92:95], v[68:71]
ds_read_b128 v[112:115], v17 offset:33792
v_mfma_f32_16x16x32_bf16 v[68:71], a[64:67], v[96:99], v[68:71]
global_store_dwordx4 v11, v[124:127], s[38:39] sc1
v_mfma_f32_16x16x32_bf16 v[72:75], a[8:11], v[76:79], 0
ds_read_b128 v[116:119], v17 offset:34816
v_add_u32 v11, 110592, v11
v_mfma_f32_16x16x32_bf16 v[72:75], a[20:23], v[80:83], v[72:75]
v_permlane16_swap_b32 v129, v131
v_mfma_f32_16x16x32_bf16 v[72:75], a[32:35], v[84:87], v[72:75]
ds_read_b128 v[120:123], v17 offset:35840
v_mfma_f32_16x16x32_bf16 v[72:75], a[44:47], v[88:91], v[72:75]
s_mov_b64 exec, s[76:77]
global_store_dwordx4 v12, v[128:131], s[38:39] sc1
s_mov_b64 exec, -1
v_mfma_f32_16x16x32_bf16 v[72:75], a[56:59], v[92:95], v[72:75]
v_add_u32 v12, 110592, v12
v_mfma_f32_16x16x32_bf16 v[72:75], a[68:71], v[96:99], v[72:75]
s_waitcnt lgkmcnt(5)
v_mfma_f32_16x16x32_bf16 v[64:67], a[72:75], v[100:103], v[64:67]
s_waitcnt lgkmcnt(4)
v_mfma_f32_16x16x32_bf16 v[64:67], a[84:87], v[104:107], v[64:67]
s_waitcnt lgkmcnt(3)
v_mfma_f32_16x16x32_bf16 v[64:67], a[96:99], v[108:111], v[64:67]
s_waitcnt lgkmcnt(2)
v_mfma_f32_16x16x32_bf16 v[64:67], a[108:111], v[112:115], v[64:67]
s_waitcnt lgkmcnt(1)
v_mfma_f32_16x16x32_bf16 v[64:67], a[120:123], v[116:119], v[64:67]
s_waitcnt lgkmcnt(0)
ds_read_b128 v[76:79], v17 offset:36864
v_mfma_f32_16x16x32_bf16 v[64:67], a[132:135], v[120:123], v[64:67]
v_mfma_f32_16x16x32_bf16 v[68:71], a[76:79], v[100:103], v[68:71]
ds_read_b128 v[80:83], v17 offset:37888
v_mfma_f32_16x16x32_bf16 v[68:71], a[88:91], v[104:107], v[68:71]
v_mfma_f32_16x16x32_bf16 v[68:71], a[100:103], v[108:111], v[68:71]
ds_read_b128 v[84:87], v17 offset:38912
v_mfma_f32_16x16x32_bf16 v[68:71], a[112:115], v[112:115], v[68:71]
v_mfma_f32_16x16x32_bf16 v[68:71], a[124:127], v[116:119], v[68:71]
ds_read_b128 v[88:91], v17 offset:39936
v_mfma_f32_16x16x32_bf16 v[68:71], a[136:139], v[120:123], v[68:71]
v_mfma_f32_16x16x32_bf16 v[72:75], a[80:83], v[100:103], v[72:75]
ds_read_b128 v[92:95], v17 offset:40960
v_mfma_f32_16x16x32_bf16 v[72:75], a[92:95], v[104:107], v[72:75]
v_mfma_f32_16x16x32_bf16 v[72:75], a[104:107], v[108:111], v[72:75]
ds_read_b128 v[96:99], v17 offset:41984
v_mfma_f32_16x16x32_bf16 v[72:75], a[116:119], v[112:115], v[72:75]
v_mfma_f32_16x16x32_bf16 v[72:75], a[128:131], v[116:119], v[72:75]
v_mfma_f32_16x16x32_bf16 v[72:75], a[140:143], v[120:123], v[72:75]
s_waitcnt lgkmcnt(5)
v_mfma_f32_16x16x32_bf16 v[64:67], a[144:147], v[76:79], v[64:67]
s_waitcnt lgkmcnt(4)
v_mfma_f32_16x16x32_bf16 v[64:67], a[156:159], v[80:83], v[64:67]
s_waitcnt lgkmcnt(3)
v_mfma_f32_16x16x32_bf16 v[64:67], a[168:171], v[84:87], v[64:67]
s_waitcnt lgkmcnt(2)
v_mfma_f32_16x16x32_bf16 v[64:67], a[180:183], v[88:91], v[64:67]
s_waitcnt vmcnt(2)
v_sub_u32 v254, v254, v14
v_cmp_lt_u32 vcc, 0x7f, v254
s_waitcnt lgkmcnt(1)
v_mfma_f32_16x16x32_bf16 v[64:67], a[192:195], v[92:95], v[64:67]
s_waitcnt lgkmcnt(0)
ds_read_b128 v[100:103], v17 offset:43008
v_mfma_f32_16x16x32_bf16 v[64:67], a[204:207], v[96:99], v[64:67]
s_add_u32 m0, s45, 24576
v_mfma_f32_16x16x32_bf16 v[68:71], a[148:151], v[76:79], v[68:71]
s_cbranch_vccnz .Ldn_sp10
.Ldn_rt11:
ds_read_b128 v[104:107], v17 offset:44032
ds_read_b128 v[108:111], v17 offset:45056
v_mfma_f32_16x16x32_bf16 v[68:71], a[160:163], v[80:83], v[68:71]
v_mfma_f32_16x16x32_bf16 v[68:71], a[172:175], v[84:87], v[68:71]
s_barrier
v_mfma_f32_16x16x32_bf16 v[68:71], a[184:187], v[88:91], v[68:71]
v_add_u32 v14, 1, v14
v_add_lshl_u32 v253, v14, 0, 7
v_mfma_f32_16x16x32_bf16 v[68:71], a[196:199], v[92:95], v[68:71]
ds_read_b128 v[112:115], v17 offset:46080
v_bfi_b32 v253, s69, v253, v13
v_mfma_f32_16x16x32_bf16 v[68:71], a[208:211], v[96:99], v[68:71]
v_mfma_f32_16x16x32_bf16 v[72:75], a[152:155], v[76:79], v[72:75]
ds_read_b128 v[116:119], v17 offset:47104
global_load_lds_dwordx4 v9, s[38:39] offset:-2048 sc1
v_mfma_f32_16x16x32_bf16 v[72:75], a[164:167], v[80:83], v[72:75]
v_mfma_f32_16x16x32_bf16 v[72:75], a[176:179], v[84:87], v[72:75]
ds_read_b128 v[120:123], v17 offset:48128
v_mfma_f32_16x16x32_bf16 v[72:75], a[188:191], v[88:91], v[72:75]
global_load_lds_dwordx4 v9, s[38:39] offset:-1024 sc1
v_mfma_f32_16x16x32_bf16 v[72:75], a[200:203], v[92:95], v[72:75]
v_mfma_f32_16x16x32_bf16 v[72:75], a[212:215], v[96:99], v[72:75]
s_waitcnt lgkmcnt(5)
v_mfma_f32_16x16x32_bf16 v[64:67], a[216:219], v[100:103], v[64:67]
s_waitcnt lgkmcnt(4)
global_load_lds_dwordx4 v9, s[38:39] offset:0 sc1
v_mfma_f32_16x16x32_bf16 v[64:67], a[228:231], v[104:107], v[64:67]
s_waitcnt lgkmcnt(3)
v_mfma_f32_16x16x32_bf16 v[64:67], a[240:243], v[108:111], v[64:67]
s_waitcnt lgkmcnt(2)
v_mfma_f32_16x16x32_bf16 v[64:67], a[252:255], v[112:115], v[64:67]
s_waitcnt lgkmcnt(1)
global_load_lds_dwordx4 v9, s[38:39] offset:1024 sc1
v_mfma_f32_16x16x32_bf16 v[64:67], v[28:31], v[116:119], v[64:67]
s_waitcnt lgkmcnt(0)
v_mfma_f32_16x16x32_bf16 v[64:67], v[40:43], v[120:123], v[64:67]
v_xor_b32 v17, 0x10000, v17
v_mfma_f32_16x16x32_bf16 v[68:71], a[220:223], v[100:103], v[68:71]
global_load_lds_dwordx4 v9, s[38:39] offset:2048 sc1
v_mfma_f32_16x16x32_bf16 v[68:71], a[232:235], v[104:107], v[68:71]
v_mfma_f32_16x16x32_bf16 v[68:71], a[244:247], v[108:111], v[68:71]
s_barrier
v_mfma_f32_16x16x32_bf16 v[68:71], v[20:23], v[112:115], v[68:71]
ds_read_b128 v[76:79], v17 offset:0
global_load_lds_dwordx4 v9, s[38:39] offset:3072 sc1
v_mfma_f32_16x16x32_bf16 v[68:71], v[32:35], v[116:119], v[68:71]
ds_read_b128 v[80:83], v17 offset:1024
v_add_u32 v9, 147456, v9
v_mfma_f32_16x16x32_bf16 v[68:71], v[44:47], v[120:123], v[68:71]
ds_read_b128 v[84:87], v17 offset:2048
v_min_u32 v9, v9, v10
v_mfma_f32_16x16x32_bf16 v[72:75], a[224:227], v[100:103], v[72:75]
ds_read_b128 v[88:91], v17 offset:3072
v_cvt_pk_bf16_f32 v124, v64, v65
v_mfma_f32_16x16x32_bf16 v[72:75], a[236:239], v[104:107], v[72:75]
ds_read_b128 v[92:95], v17 offset:4096
v_cvt_pk_bf16_f32 v125, v66, v67
v_mfma_f32_16x16x32_bf16 v[72:75], a[248:251], v[108:111], v[72:75]
ds_read_b128 v[96:99], v17 offset:5120
v_mfma_f32_16x16x32_bf16 v[72:75], v[24:27], v[112:115], v[72:75]
v_cvt_pk_bf16_f32 v126, v68, v69
v_cvt_pk_bf16_f32 v127, v70, v71
v_mfma_f32_16x16x32_bf16 v[72:75], v[36:39], v[116:119], v[72:75]
v_mfma_f32_16x16x32_bf16 v[72:75], v[48:51], v[120:123], v[72:75]
s_sub_u32 s70, s70, 1
s_cbranch_scc0 .Ldn_top12
v_cvt_pk_bf16_f32 v124, v64, v65
v_cvt_pk_bf16_f32 v125, v66, v67
v_cvt_pk_bf16_f32 v126, v68, v69
v_cvt_pk_bf16_f32 v127, v70, v71
global_store_dwordx4 v11, v[124:127], s[38:39] sc1
v_add_u32 v11, 110592, v11
v_cvt_pk_bf16_f32 v128, v72, v73
v_cvt_pk_bf16_f32 v130, v72, v73
v_cvt_pk_bf16_f32 v129, v74, v75
v_cvt_pk_bf16_f32 v131, v74, v75
v_permlane16_swap_b32 v128, v130
s_nop 0
v_permlane16_swap_b32 v129, v131
s_mov_b64 exec, s[76:77]
global_store_dwordx4 v12, v[128:131], s[38:39] sc1
s_mov_b64 exec, -1
v_add_u32 v12, 110592, v12
s_waitcnt vmcnt(2)
s_barrier
s_waitcnt vmcnt(0)
s_barrier
s_endpgm
.Ldn_sp8:
s_sleep 1
v_add_lshl_u32 v253, v14, 0, 7
v_bfi_b32 v253, s69, v253, v13
global_load_dword v19, v253, s[38:39] sc1
s_add_u32 s71, s71, 1
s_waitcnt vmcnt(0)
v_sub_u32 v19, v19, v14
v_cmp_lt_u32 vcc, 0x7f, v19
s_cbranch_vccz .Ldn_rt9
s_cmp_lt_u32 s71, 3000000
s_cbranch_scc1 .Ldn_sp8
s_branch .Ldn_rt9
.Ldn_sp10:
s_sleep 1
v_add_lshl_u32 v253, v14, 0, 7
v_bfi_b32 v253, s69, v253, v13
global_load_dword v254, v253, s[38:39] sc1
s_add_u32 s71, s71, 1
s_waitcnt vmcnt(0)
v_sub_u32 v254, v254, v14
v_cmp_lt_u32 vcc, 0x7f, v254
s_cbranch_vccz .Ldn_rt11
s_cmp_lt_u32 s71, 3000000
s_cbranch_scc1 .Ldn_sp10
s_branch .Ldn_rt11
.Lrole3:
v_add_u32 v150, 35840, v0
v_add_u32 v151, 71680, v0
v_add_u32 v152, 107520, v0
v_add_u32 v153, 143360, v0
v_add_u32 v154, 179200, v0
v_add_u32 v155, 215040, v0
v_add_u32 v156, 250880, v0
s_sleep 20
s_add_u32 m0, s42, 4096
s_nop 0
global_load_lds_dwordx4 v0, s[40:41] offset:-4096 nt
global_load_lds_dwordx4 v150, s[40:41] offset:-3072 nt
global_load_lds_dwordx4 v151, s[40:41] offset:-2048 nt
global_load_lds_dwordx4 v152, s[40:41] offset:-1024 nt
global_load_lds_dwordx4 v153, s[40:41] offset:0 nt
global_load_lds_dwordx4 v154, s[40:41] offset:1024 nt
global_load_lds_dwordx4 v155, s[40:41] offset:2048 nt
global_load_lds_dwordx4 v156, s[40:41] offset:3072 nt
s_add_u32 s40, s40, 589824
s_addc_u32 s41, s41, 0
s_add_u32 m0, s42, 36864
s_nop 0
global_load_lds_dwordx4 v0, s[40:41] offset:-4096 nt
global_load_lds_dwordx4 v150, s[40:41] offset:-3072 nt
global_load_lds_dwordx4 v151, s[40:41] offset:-2048 nt
global_load_lds_dwordx4 v152, s[40:41] offset:-1024 nt
global_load_lds_dwordx4 v153, s[40:41] offset:0 nt
global_load_lds_dwordx4 v154, s[40:41] offset:1024 nt
global_load_lds_dwordx4 v155, s[40:41] offset:2048 nt
global_load_lds_dwordx4 v156, s[40:41] offset:3072 nt
s_add_u32 s40, s40, 589824
s_addc_u32 s41, s41, 0
s_add_u32 m0, s42, 69632
s_nop 0
global_load_lds_dwordx4 v0, s[40:41] offset:-4096 nt
global_load_lds_dwordx4 v150, s[40:41] offset:-3072 nt
global_load_lds_dwordx4 v151, s[40:41] offset:-2048 nt
global_load_lds_dwordx4 v152, s[40:41] offset:-1024 nt
global_load_lds_dwordx4 v153, s[40:41] offset:0 nt
global_load_lds_dwordx4 v154, s[40:41] offset:1024 nt
global_load_lds_dwordx4 v155, s[40:41] offset:2048 nt
global_load_lds_dwordx4 v156, s[40:41] offset:3072 nt
s_add_u32 s40, s40, 589824
s_addc_u32 s41, s41, 0
v_add_u32 v157, 0x10000, v1
v_mov_b32 v174, 0x3F803F80
v_mov_b32 v175, 0x3F803F80
v_mov_b32 v176, 0x3F803F80
v_mov_b32 v177, 0x3F803F80
s_waitcnt lgkmcnt(0)
s_mul_i32 s37, s37, 0x9E3779B1
s_add_u32 s37, s37, 0x7F4A7C15
v_add_u32 v5, s37, v5
v_add_u32 v7, s37, v7
v_add_u32 v17, s37, v17
s_waitcnt vmcnt(16)
s_barrier
s_add_u32 m0, s42, 102400
ds_read_b64_tr_b16 a[0:1], v1 offset:0
ds_read_b64_tr_b16 a[2:3], v1 offset:1024
global_load_lds_dwordx4 v0, s[40:41] offset:-4096 nt
ds_read_b64_tr_b16 a[4:5], v1 offset:8
global_load_lds_dwordx4 v150, s[40:41] offset:-3072 nt
ds_read_b64_tr_b16 a[6:7], v1 offset:1032
ds_read_b64_tr_b16 a[8:9], v1 offset:16384
global_load_lds_dwordx4 v151, s[40:41] offset:-2048 nt
ds_read_b64_tr_b16 a[10:11], v1 offset:17408
global_load_lds_dwordx4 v152, s[40:41] offset:-1024 nt
ds_read_b64_tr_b16 a[12:13], v1 offset:16392
ds_read_b64_tr_b16 a[14:15], v1 offset:17416
global_load_lds_dwordx4 v153, s[40:41] offset:0 nt
ds_read_b64_tr_b16 a[16:17], v1 offset:8192
global_load_lds_dwordx4 v154, s[40:41] offset:1024 nt
ds_read_b64_tr_b16 a[18:19], v1 offset:9216
ds_read_b64_tr_b16 a[20:21], v1 offset:8200
global_load_lds_dwordx4 v155, s[40:41] offset:2048 nt
ds_read_b64_tr_b16 a[22:23], v1 offset:9224
global_load_lds_dwordx4 v156, s[40:41] offset:3072 nt
ds_read_b64_tr_b16 a[24:25], v1 offset:24576
ds_read_b64_tr_b16 a[26:27], v1 offset:25600
s_add_u32 s40, s40, 589824
ds_read_b64_tr_b16 a[28:29], v1 offset:24584
s_addc_u32 s41, s41, 0
ds_read_b64_tr_b16 a[30:31], v1 offset:25608
s_waitcnt vmcnt(16) lgkmcnt(0)
s_barrier
s_add_u32 m0, s42, 4096
ds_read_b64_tr_b16 a[32:33], v1 offset:32768
v_mfma_f32_16x16x32_bf16 v[158:161], v[174:177], a[0:3], 0
ds_read_b64_tr_b16 a[34:35], v1 offset:33792
global_load_lds_dwordx4 v0, s[40:41] offset:-4096 nt
ds_read_b64_tr_b16 a[36:37], v1 offset:32776
ds_read_b64_tr_b16 a[38:39], v1 offset:33800
global_load_lds_dwordx4 v150, s[40:41] offset:-3072 nt
ds_read_b64_tr_b16 a[40:41], v1 offset:49152
v_mfma_f32_16x16x32_bf16 v[162:165], v[174:177], a[4:7], 0
global_load_lds_dwordx4 v151, s[40:41] offset:-2048 nt
ds_read_b64_tr_b16 a[42:43], v1 offset:50176
ds_read_b64_tr_b16 a[44:45], v1 offset:49160
global_load_lds_dwordx4 v152, s[40:41] offset:-1024 nt
ds_read_b64_tr_b16 a[46:47], v1 offset:50184
ds_read_b64_tr_b16 a[48:49], v1 offset:40960
global_load_lds_dwordx4 v153, s[40:41] offset:0 nt
v_mfma_f32_16x16x32_bf16 v[166:169], v[174:177], a[8:11], 0
ds_read_b64_tr_b16 a[50:51], v1 offset:41984
ds_read_b64_tr_b16 a[52:53], v1 offset:40968
global_load_lds_dwordx4 v154, s[40:41] offset:1024 nt
ds_read_b64_tr_b16 a[54:55], v1 offset:41992
ds_read_b64_tr_b16 a[56:57], v1 offset:57344
global_load_lds_dwordx4 v155, s[40:41] offset:2048 nt
v_mfma_f32_16x16x32_bf16 v[170:173], v[174:177], a[12:15], 0
ds_read_b64_tr_b16 a[58:59], v1 offset:58368
global_load_lds_dwordx4 v156, s[40:41] offset:3072 nt
ds_read_b64_tr_b16 a[60:61], v1 offset:57352
ds_read_b64_tr_b16 a[62:63], v1 offset:58376
s_add_u32 s40, s40, 589824
v_mfma_f32_16x16x32_bf16 v[158:161], v[174:177], a[16:19], v[158:161]
v_mfma_f32_16x16x32_bf16 v[162:165], v[174:177], a[20:23], v[162:165]
s_addc_u32 s41, s41, 0
v_mfma_f32_16x16x32_bf16 v[166:169], v[174:177], a[24:27], v[166:169]
v_mfma_f32_16x16x32_bf16 v[170:173], v[174:177], a[28:31], v[170:173]
s_waitcnt vmcnt(16) lgkmcnt(0)
s_barrier
s_add_u32 m0, s42, 36864
ds_read_b64_tr_b16 a[64:65], v157 offset:0
v_mfma_f32_16x16x32_bf16 v[158:161], v[174:177], a[32:35], v[158:161]
ds_read_b64_tr_b16 a[66:67], v157 offset:1024
global_load_lds_dwordx4 v0, s[40:41] offset:-4096 nt
ds_read_b64_tr_b16 a[68:69], v157 offset:8
ds_read_b64_tr_b16 a[70:71], v157 offset:1032
global_load_lds_dwordx4 v150, s[40:41] offset:-3072 nt
ds_read_b64_tr_b16 a[72:73], v157 offset:16384
v_mfma_f32_16x16x32_bf16 v[162:165], v[174:177], a[36:39], v[162:165]
global_load_lds_dwordx4 v151, s[40:41] offset:-2048 nt
ds_read_b64_tr_b16 a[74:75], v157 offset:17408
ds_read_b64_tr_b16 a[76:77], v157 offset:16392
global_load_lds_dwordx4 v152, s[40:41] offset:-1024 nt
ds_read_b64_tr_b16 a[78:79], v157 offset:17416
ds_read_b64_tr_b16 a[80:81], v157 offset:8192
global_load_lds_dwordx4 v153, s[40:41] offset:0 nt
v_mfma_f32_16x16x32_bf16 v[166:169], v[174:177], a[40:43], v[166:169]
ds_read_b64_tr_b16 a[82:83], v157 offset:9216
ds_read_b64_tr_b16 a[84:85], v157 offset:8200
global_load_lds_dwordx4 v154, s[40:41] offset:1024 nt
ds_read_b64_tr_b16 a[86:87], v157 offset:9224
ds_read_b64_tr_b16 a[88:89], v157 offset:24576
global_load_lds_dwordx4 v155, s[40:41] offset:2048 nt
v_mfma_f32_16x16x32_bf16 v[170:173], v[174:177], a[44:47], v[170:173]
ds_read_b64_tr_b16 a[90:91], v157 offset:25600
global_load_lds_dwordx4 v156, s[40:41] offset:3072 nt
ds_read_b64_tr_b16 a[92:93], v157 offset:24584
ds_read_b64_tr_b16 a[94:95], v157 offset:25608
s_add_u32 s40, s40, 589824
v_mfma_f32_16x16x32_bf16 v[158:161], v[174:177], a[48:51], v[158:161]
v_mfma_f32_16x16x32_bf16 v[162:165], v[174:177], a[52:55], v[162:165]
s_addc_u32 s41, s41, 0
v_mfma_f32_16x16x32_bf16 v[166:169], v[174:177], a[56:59], v[166:169]
v_mfma_f32_16x16x32_bf16 v[170:173], v[174:177], a[60:63], v[170:173]
s_waitcnt vmcnt(16) lgkmcnt(0)
s_barrier
s_add_u32 m0, s42, 69632
ds_read_b64_tr_b16 a[96:97], v157 offset:32768
v_mfma_f32_16x16x32_bf16 v[158:161], v[174:177], a[64:67], v[158:161]
ds_read_b64_tr_b16 a[98:99], v157 offset:33792
global_load_lds_dwordx4 v0, s[40:41] offset:-4096 nt
ds_read_b64_tr_b16 a[100:101], v157 offset:32776
ds_read_b64_tr_b16 a[102:103], v157 offset:33800
global_load_lds_dwordx4 v150, s[40:41] offset:-3072 nt
ds_read_b64_tr_b16 a[104:105], v157 offset:49152
v_mfma_f32_16x16x32_bf16 v[162:165], v[174:177], a[68:71], v[162:165]
global_load_lds_dwordx4 v151, s[40:41] offset:-2048 nt
ds_read_b64_tr_b16 a[106:107], v157 offset:50176
ds_read_b64_tr_b16 a[108:109], v157 offset:49160
global_load_lds_dwordx4 v152, s[40:41] offset:-1024 nt
ds_read_b64_tr_b16 a[110:111], v157 offset:50184
ds_read_b64_tr_b16 a[112:113], v157 offset:40960
global_load_lds_dwordx4 v153, s[40:41] offset:0 nt
v_mfma_f32_16x16x32_bf16 v[166:169], v[174:177], a[72:75], v[166:169]
ds_read_b64_tr_b16 a[114:115], v157 offset:41984
ds_read_b64_tr_b16 a[116:117], v157 offset:40968
global_load_lds_dwordx4 v154, s[40:41] offset:1024 nt
ds_read_b64_tr_b16 a[118:119], v157 offset:41992
ds_read_b64_tr_b16 a[120:121], v157 offset:57344
global_load_lds_dwordx4 v155, s[40:41] offset:2048 nt
v_mfma_f32_16x16x32_bf16 v[170:173], v[174:177], a[76:79], v[170:173]
ds_read_b64_tr_b16 a[122:123], v157 offset:58368
global_load_lds_dwordx4 v156, s[40:41] offset:3072 nt
ds_read_b64_tr_b16 a[124:125], v157 offset:57352
ds_read_b64_tr_b16 a[126:127], v157 offset:58376
s_add_u32 s40, s40, 589824
v_mfma_f32_16x16x32_bf16 v[158:161], v[174:177], a[80:83], v[158:161]
v_mfma_f32_16x16x32_bf16 v[162:165], v[174:177], a[84:87], v[162:165]
)"
R"(s_addc_u32 s41, s41, 0
v_mfma_f32_16x16x32_bf16 v[166:169], v[174:177], a[88:91], v[166:169]
v_mfma_f32_16x16x32_bf16 v[170:173], v[174:177], a[92:95], v[170:173]
s_waitcnt vmcnt(16) lgkmcnt(0)
s_barrier
s_add_u32 m0, s42, 102400
ds_read_b64_tr_b16 a[128:129], v1 offset:0
v_mfma_f32_16x16x32_bf16 v[158:161], v[174:177], a[96:99], v[158:161]
ds_read_b64_tr_b16 a[130:131], v1 offset:1024
global_load_lds_dwordx4 v0, s[40:41] offset:-4096 nt
ds_read_b64_tr_b16 a[132:133], v1 offset:8
ds_read_b64_tr_b16 a[134:135], v1 offset:1032
global_load_lds_dwordx4 v150, s[40:41] offset:-3072 nt
ds_read_b64_tr_b16 a[136:137], v1 offset:16384
v_mfma_f32_16x16x32_bf16 v[162:165], v[174:177], a[100:103], v[162:165]
global_load_lds_dwordx4 v151, s[40:41] offset:-2048 nt
ds_read_b64_tr_b16 a[138:139], v1 offset:17408
ds_read_b64_tr_b16 a[140:141], v1 offset:16392
global_load_lds_dwordx4 v152, s[40:41] offset:-1024 nt
ds_read_b64_tr_b16 a[142:143], v1 offset:17416
ds_read_b64_tr_b16 a[144:145], v1 offset:8192
global_load_lds_dwordx4 v153, s[40:41] offset:0 nt
v_mfma_f32_16x16x32_bf16 v[166:169], v[174:177], a[104:107], v[166:169]
ds_read_b64_tr_b16 a[146:147], v1 offset:9216
ds_read_b64_tr_b16 a[148:149], v1 offset:8200
global_load_lds_dwordx4 v154, s[40:41] offset:1024 nt
ds_read_b64_tr_b16 a[150:151], v1 offset:9224
ds_read_b64_tr_b16 a[152:153], v1 offset:24576
global_load_lds_dwordx4 v155, s[40:41] offset:2048 nt
v_mfma_f32_16x16x32_bf16 v[170:173], v[174:177], a[108:111], v[170:173]
ds_read_b64_tr_b16 a[154:155], v1 offset:25600
global_load_lds_dwordx4 v156, s[40:41] offset:3072 nt
ds_read_b64_tr_b16 a[156:157], v1 offset:24584
ds_read_b64_tr_b16 a[158:159], v1 offset:25608
s_add_u32 s40, s40, 589824
v_mfma_f32_16x16x32_bf16 v[158:161], v[174:177], a[112:115], v[158:161]
v_mfma_f32_16x16x32_bf16 v[162:165], v[174:177], a[116:119], v[162:165]
s_addc_u32 s41, s41, 0
v_mfma_f32_16x16x32_bf16 v[166:169], v[174:177], a[120:123], v[166:169]
v_mfma_f32_16x16x32_bf16 v[170:173], v[174:177], a[124:127], v[170:173]
s_waitcnt vmcnt(16) lgkmcnt(0)
s_barrier
s_add_u32 m0, s42, 4096
ds_read_b64_tr_b16 a[160:161], v1 offset:32768
v_mfma_f32_16x16x32_bf16 v[158:161], v[174:177], a[128:131], v[158:161]
ds_read_b64_tr_b16 a[162:163], v1 offset:33792
global_load_lds_dwordx4 v0, s[40:41] offset:-4096 nt
ds_read_b64_tr_b16 a[164:165], v1 offset:32776
ds_read_b64_tr_b16 a[166:167], v1 offset:33800
global_load_lds_dwordx4 v150, s[40:41] offset:-3072 nt
ds_read_b64_tr_b16 a[168:169], v1 offset:49152
v_mfma_f32_16x16x32_bf16 v[162:165], v[174:177], a[132:135], v[162:165]
global_load_lds_dwordx4 v151, s[40:41] offset:-2048 nt
ds_read_b64_tr_b16 a[170:171], v1 offset:50176
ds_read_b64_tr_b16 a[172:173], v1 offset:49160
global_load_lds_dwordx4 v152, s[40:41] offset:-1024 nt
ds_read_b64_tr_b16 a[174:175], v1 offset:50184
ds_read_b64_tr_b16 a[176:177], v1 offset:40960
global_load_lds_dwordx4 v153, s[40:41] offset:0 nt
v_mfma_f32_16x16x32_bf16 v[166:169], v[174:177], a[136:139], v[166:169]
ds_read_b64_tr_b16 a[178:179], v1 offset:41984
ds_read_b64_tr_b16 a[180:181], v1 offset:40968
global_load_lds_dwordx4 v154, s[40:41] offset:1024 nt
ds_read_b64_tr_b16 a[182:183], v1 offset:41992
ds_read_b64_tr_b16 a[184:185], v1 offset:57344
global_load_lds_dwordx4 v155, s[40:41] offset:2048 nt
v_mfma_f32_16x16x32_bf16 v[170:173], v[174:177], a[140:143], v[170:173]
ds_read_b64_tr_b16 a[186:187], v1 offset:58368
global_load_lds_dwordx4 v156, s[40:41] offset:3072 nt
ds_read_b64_tr_b16 a[188:189], v1 offset:57352
ds_read_b64_tr_b16 a[190:191], v1 offset:58376
s_sub_u32 s40, s40, 4718592
v_mfma_f32_16x16x32_bf16 v[158:161], v[174:177], a[144:147], v[158:161]
v_mfma_f32_16x16x32_bf16 v[162:165], v[174:177], a[148:151], v[162:165]
s_subb_u32 s41, s41, 0
v_mfma_f32_16x16x32_bf16 v[166:169], v[174:177], a[152:155], v[166:169]
v_mfma_f32_16x16x32_bf16 v[170:173], v[174:177], a[156:159], v[170:173]
s_waitcnt vmcnt(16) lgkmcnt(0)
s_barrier
ds_read_b64_tr_b16 a[192:193], v157 offset:0
v_mfma_f32_16x16x32_bf16 v[158:161], v[174:177], a[160:163], v[158:161]
ds_read_b64_tr_b16 a[194:195], v157 offset:1024
ds_read_b64_tr_b16 a[196:197], v157 offset:8
ds_read_b64_tr_b16 a[198:199], v157 offset:1032
ds_read_b64_tr_b16 a[200:201], v157 offset:16384
v_mfma_f32_16x16x32_bf16 v[162:165], v[174:177], a[164:167], v[162:165]
ds_read_b64_tr_b16 a[202:203], v157 offset:17408
ds_read_b64_tr_b16 a[204:205], v157 offset:16392
ds_read_b64_tr_b16 a[206:207], v157 offset:17416
ds_read_b64_tr_b16 a[208:209], v157 offset:8192
v_mfma_f32_16x16x32_bf16 v[166:169], v[174:177], a[168:171], v[166:169]
ds_read_b64_tr_b16 a[210:211], v157 offset:9216
ds_read_b64_tr_b16 a[212:213], v157 offset:8200
ds_read_b64_tr_b16 a[214:215], v157 offset:9224
ds_read_b64_tr_b16 a[216:217], v157 offset:24576
v_mfma_f32_16x16x32_bf16 v[170:173], v[174:177], a[172:175], v[170:173]
ds_read_b64_tr_b16 a[218:219], v157 offset:25600
ds_read_b64_tr_b16 a[220:221], v157 offset:24584
ds_read_b64_tr_b16 a[222:223], v157 offset:25608
v_mfma_f32_16x16x32_bf16 v[158:161], v[174:177], a[176:179], v[158:161]
v_mfma_f32_16x16x32_bf16 v[162:165], v[174:177], a[180:183], v[162:165]
v_mfma_f32_16x16x32_bf16 v[166:169], v[174:177], a[184:187], v[166:169]
v_mfma_f32_16x16x32_bf16 v[170:173], v[174:177], a[188:191], v[170:173]
s_waitcnt vmcnt(8) lgkmcnt(0)
s_barrier
ds_read_b64_tr_b16 a[224:225], v157 offset:32768
v_mfma_f32_16x16x32_bf16 v[158:161], v[174:177], a[192:195], v[158:161]
ds_read_b64_tr_b16 a[226:227], v157 offset:33792
ds_read_b64_tr_b16 a[228:229], v157 offset:32776
ds_read_b64_tr_b16 a[230:231], v157 offset:33800
ds_read_b64_tr_b16 a[232:233], v157 offset:49152
v_mfma_f32_16x16x32_bf16 v[162:165], v[174:177], a[196:199], v[162:165]
ds_read_b64_tr_b16 a[234:235], v157 offset:50176
ds_read_b64_tr_b16 a[236:237], v157 offset:49160
ds_read_b64_tr_b16 a[238:239], v157 offset:50184
ds_read_b64_tr_b16 a[240:241], v157 offset:40960
v_mfma_f32_16x16x32_bf16 v[166:169], v[174:177], a[200:203], v[166:169]
ds_read_b64_tr_b16 a[242:243], v157 offset:41984
ds_read_b64_tr_b16 a[244:245], v157 offset:40968
ds_read_b64_tr_b16 a[246:247], v157 offset:41992
ds_read_b64_tr_b16 a[248:249], v157 offset:57344
v_mfma_f32_16x16x32_bf16 v[170:173], v[174:177], a[204:207], v[170:173]
ds_read_b64_tr_b16 a[250:251], v157 offset:58368
ds_read_b64_tr_b16 a[252:253], v157 offset:57352
ds_read_b64_tr_b16 a[254:255], v157 offset:58376
v_mfma_f32_16x16x32_bf16 v[158:161], v[174:177], a[208:211], v[158:161]
v_mfma_f32_16x16x32_bf16 v[162:165], v[174:177], a[212:215], v[162:165]
v_mfma_f32_16x16x32_bf16 v[166:169], v[174:177], a[216:219], v[166:169]
v_mfma_f32_16x16x32_bf16 v[170:173], v[174:177], a[220:223], v[170:173]
s_waitcnt vmcnt(0) lgkmcnt(0)
s_barrier
ds_read_b64_tr_b16 v[26:27], v1 offset:0
v_mfma_f32_16x16x32_bf16 v[158:161], v[174:177], a[224:227], v[158:161]
ds_read_b64_tr_b16 v[28:29], v1 offset:1024
ds_read_b64_tr_b16 v[30:31], v1 offset:8
ds_read_b64_tr_b16 v[32:33], v1 offset:1032
ds_read_b64_tr_b16 v[34:35], v1 offset:16384
v_mfma_f32_16x16x32_bf16 v[162:165], v[174:177], a[228:231], v[162:165]
ds_read_b64_tr_b16 v[36:37], v1 offset:17408
ds_read_b64_tr_b16 v[38:39], v1 offset:16392
ds_read_b64_tr_b16 v[40:41], v1 offset:17416
ds_read_b64_tr_b16 v[42:43], v1 offset:8192
v_mfma_f32_16x16x32_bf16 v[166:169], v[174:177], a[232:235], v[166:169]
ds_read_b64_tr_b16 v[44:45], v1 offset:9216
ds_read_b64_tr_b16 v[46:47], v1 offset:8200
ds_read_b64_tr_b16 v[48:49], v1 offset:9224
ds_read_b64_tr_b16 v[50:51], v1 offset:24576
v_mfma_f32_16x16x32_bf16 v[170:173], v[174:177], a[236:239], v[170:173]
ds_read_b64_tr_b16 v[52:53], v1 offset:25600
ds_read_b64_tr_b16 v[54:55], v1 offset:24584
ds_read_b64_tr_b16 v[56:57], v1 offset:25608
v_mfma_f32_16x16x32_bf16 v[158:161], v[174:177], a[240:243], v[158:161]
v_mfma_f32_16x16x32_bf16 v[162:165], v[174:177], a[244:247], v[162:165]
v_mfma_f32_16x16x32_bf16 v[166:169], v[174:177], a[248:251], v[166:169]
v_mfma_f32_16x16x32_bf16 v[170:173], v[174:177], a[252:255], v[170:173]
s_waitcnt lgkmcnt(14)
v_mfma_f32_16x16x32_bf16 v[158:161], v[174:177], v[26:29], v[158:161]
s_waitcnt lgkmcnt(12)
v_mfma_f32_16x16x32_bf16 v[162:165], v[174:177], v[30:33], v[162:165]
s_waitcnt lgkmcnt(10)
v_mfma_f32_16x16x32_bf16 v[166:169], v[174:177], v[34:37], v[166:169]
s_waitcnt lgkmcnt(8)
v_mfma_f32_16x16x32_bf16 v[170:173], v[174:177], v[38:41], v[170:173]
s_waitcnt lgkmcnt(6)
v_mfma_f32_16x16x32_bf16 v[158:161], v[174:177], v[42:45], v[158:161]
s_waitcnt lgkmcnt(4)
v_mfma_f32_16x16x32_bf16 v[162:165], v[174:177], v[46:49], v[162:165]
s_waitcnt lgkmcnt(2)
v_mfma_f32_16x16x32_bf16 v[166:169], v[174:177], v[50:53], v[166:169]
s_waitcnt lgkmcnt(0)
v_mfma_f32_16x16x32_bf16 v[170:173], v[174:177], v[54:57], v[170:173]
s_nop 1
v_mul_f32 v158, 0xbae38e39, v158
v_mul_f32 v159, 0xbae38e39, v159
v_mul_f32 v160, 0xbae38e39, v160
v_mul_f32 v161, 0xbae38e39, v161
v_mul_f32 v162, 0xbae38e39, v162
v_mul_f32 v163, 0xbae38e39, v163
v_mul_f32 v164, 0xbae38e39, v164
v_mul_f32 v165, 0xbae38e39, v165
v_mul_f32 v166, 0xbae38e39, v166
v_mul_f32 v167, 0xbae38e39, v167
v_mul_f32 v168, 0xbae38e39, v168
v_mul_f32 v169, 0xbae38e39, v169
v_mul_f32 v170, 0xbae38e39, v170
v_mul_f32 v171, 0xbae38e39, v171
v_mul_f32 v172, 0xbae38e39, v172
v_mul_f32 v173, 0xbae38e39, v173
v_mfma_f32_16x16x32_bf16 v[178:181], v[22:25], a[0:3], v[158:161]
v_mfma_f32_16x16x32_bf16 v[182:185], v[20:23], a[0:3], v[158:161]
v_mfma_f32_16x16x32_bf16 v[186:189], v[22:25], a[4:7], v[162:165]
v_mfma_f32_16x16x32_bf16 v[190:193], v[20:23], a[4:7], v[162:165]
v_mfma_f32_16x16x32_bf16 v[194:197], v[22:25], a[8:11], v[166:169]
s_nop 3
v_cvt_pk_bf16_f32 v210, v178, v179
v_cvt_pk_bf16_f32 v211, v180, v181
v_cvt_pk_bf16_f32 v212, v182, v183
v_cvt_pk_bf16_f32 v213, v184, v185
v_mfma_f32_16x16x32_bf16 v[198:201], v[20:23], a[8:11], v[166:169]
v_accvgpr_write_b32 a0, v210
v_accvgpr_write_b32 a1, v211
v_accvgpr_write_b32 a2, v212
v_accvgpr_write_b32 a3, v213
v_mfma_f32_16x16x32_bf16 v[202:205], v[22:25], a[12:15], v[170:173]
v_cvt_pk_bf16_f32 v214, v186, v187
v_cvt_pk_bf16_f32 v215, v188, v189
v_cvt_pk_bf16_f32 v216, v190, v191
v_cvt_pk_bf16_f32 v217, v192, v193
v_mfma_f32_16x16x32_bf16 v[206:209], v[20:23], a[12:15], v[170:173]
v_accvgpr_write_b32 a4, v214
v_accvgpr_write_b32 a5, v215
v_accvgpr_write_b32 a6, v216
v_accvgpr_write_b32 a7, v217
v_mfma_f32_16x16x32_bf16 v[178:181], v[22:25], a[16:19], v[158:161]
v_cvt_pk_bf16_f32 v210, v194, v195
v_cvt_pk_bf16_f32 v211, v196, v197
v_cvt_pk_bf16_f32 v212, v198, v199
v_cvt_pk_bf16_f32 v213, v200, v201
v_mfma_f32_16x16x32_bf16 v[182:185], v[20:23], a[16:19], v[158:161]
v_accvgpr_write_b32 a8, v210
v_accvgpr_write_b32 a9, v211
v_accvgpr_write_b32 a10, v212
v_accvgpr_write_b32 a11, v213
v_mfma_f32_16x16x32_bf16 v[186:189], v[22:25], a[20:23], v[162:165]
v_cvt_pk_bf16_f32 v214, v202, v203
v_cvt_pk_bf16_f32 v215, v204, v205
v_cvt_pk_bf16_f32 v216, v206, v207
v_cvt_pk_bf16_f32 v217, v208, v209
v_mfma_f32_16x16x32_bf16 v[190:193], v[20:23], a[20:23], v[162:165]
v_accvgpr_write_b32 a12, v214
v_accvgpr_write_b32 a13, v215
v_accvgpr_write_b32 a14, v216
v_accvgpr_write_b32 a15, v217
v_mfma_f32_16x16x32_bf16 v[194:197], v[22:25], a[24:27], v[166:169]
v_cvt_pk_bf16_f32 v210, v178, v179
v_cvt_pk_bf16_f32 v211, v180, v181
v_cvt_pk_bf16_f32 v212, v182, v183
v_cvt_pk_bf16_f32 v213, v184, v185
v_mfma_f32_16x16x32_bf16 v[198:201], v[20:23], a[24:27], v[166:169]
v_accvgpr_write_b32 a16, v210
v_accvgpr_write_b32 a17, v211
v_accvgpr_write_b32 a18, v212
v_accvgpr_write_b32 a19, v213
v_mfma_f32_16x16x32_bf16 v[202:205], v[22:25], a[28:31], v[170:173]
v_cvt_pk_bf16_f32 v214, v186, v187
v_cvt_pk_bf16_f32 v215, v188, v189
v_cvt_pk_bf16_f32 v216, v190, v191
v_cvt_pk_bf16_f32 v217, v192, v193
v_mfma_f32_16x16x32_bf16 v[206:209], v[20:23], a[28:31], v[170:173]
v_accvgpr_write_b32 a20, v214
v_accvgpr_write_b32 a21, v215
v_accvgpr_write_b32 a22, v216
v_accvgpr_write_b32 a23, v217
v_mfma_f32_16x16x32_bf16 v[178:181], v[22:25], a[32:35], v[158:161]
v_cvt_pk_bf16_f32 v210, v194, v195
v_cvt_pk_bf16_f32 v211, v196, v197
v_cvt_pk_bf16_f32 v212, v198, v199
v_cvt_pk_bf16_f32 v213, v200, v201
v_mfma_f32_16x16x32_bf16 v[182:185], v[20:23], a[32:35], v[158:161]
v_accvgpr_write_b32 a24, v210
v_accvgpr_write_b32 a25, v211
v_accvgpr_write_b32 a26, v212
v_accvgpr_write_b32 a27, v213
v_mfma_f32_16x16x32_bf16 v[186:189], v[22:25], a[36:39], v[162:165]
v_cvt_pk_bf16_f32 v214, v202, v203
v_cvt_pk_bf16_f32 v215, v204, v205
v_cvt_pk_bf16_f32 v216, v206, v207
v_cvt_pk_bf16_f32 v217, v208, v209
v_mfma_f32_16x16x32_bf16 v[190:193], v[20:23], a[36:39], v[162:165]
v_accvgpr_write_b32 a28, v214
v_accvgpr_write_b32 a29, v215
v_accvgpr_write_b32 a30, v216
v_accvgpr_write_b32 a31, v217
v_mfma_f32_16x16x32_bf16 v[194:197], v[22:25], a[40:43], v[166:169]
v_cvt_pk_bf16_f32 v210, v178, v179
v_cvt_pk_bf16_f32 v211, v180, v181
v_cvt_pk_bf16_f32 v212, v182, v183
v_cvt_pk_bf16_f32 v213, v184, v185
v_mfma_f32_16x16x32_bf16 v[198:201], v[20:23], a[40:43], v[166:169]
v_accvgpr_write_b32 a32, v210
v_accvgpr_write_b32 a33, v211
v_accvgpr_write_b32 a34, v212
v_accvgpr_write_b32 a35, v213
v_mfma_f32_16x16x32_bf16 v[202:205], v[22:25], a[44:47], v[170:173]
v_cvt_pk_bf16_f32 v214, v186, v187
v_cvt_pk_bf16_f32 v215, v188, v189
v_cvt_pk_bf16_f32 v216, v190, v191
v_cvt_pk_bf16_f32 v217, v192, v193
v_mfma_f32_16x16x32_bf16 v[206:209], v[20:23], a[44:47], v[170:173]
v_accvgpr_write_b32 a36, v214
v_accvgpr_write_b32 a37, v215
v_accvgpr_write_b32 a38, v216
v_accvgpr_write_b32 a39, v217
v_mfma_f32_16x16x32_bf16 v[178:181], v[22:25], a[48:51], v[158:161]
v_cvt_pk_bf16_f32 v210, v194, v195
v_cvt_pk_bf16_f32 v211, v196, v197
v_cvt_pk_bf16_f32 v212, v198, v199
v_cvt_pk_bf16_f32 v213, v200, v201
v_mfma_f32_16x16x32_bf16 v[182:185], v[20:23], a[48:51], v[158:161]
v_accvgpr_write_b32 a40, v210
v_accvgpr_write_b32 a41, v211
v_accvgpr_write_b32 a42, v212
v_accvgpr_write_b32 a43, v213
v_mfma_f32_16x16x32_bf16 v[186:189], v[22:25], a[52:55], v[162:165]
v_cvt_pk_bf16_f32 v214, v202, v203
v_cvt_pk_bf16_f32 v215, v204, v205
v_cvt_pk_bf16_f32 v216, v206, v207
v_cvt_pk_bf16_f32 v217, v208, v209
v_mfma_f32_16x16x32_bf16 v[190:193], v[20:23], a[52:55], v[162:165]
v_accvgpr_write_b32 a44, v214
v_accvgpr_write_b32 a45, v215
v_accvgpr_write_b32 a46, v216
v_accvgpr_write_b32 a47, v217
v_mfma_f32_16x16x32_bf16 v[194:197], v[22:25], a[56:59], v[166:169]
v_cvt_pk_bf16_f32 v210, v178, v179
v_cvt_pk_bf16_f32 v211, v180, v181
v_cvt_pk_bf16_f32 v212, v182, v183
v_cvt_pk_bf16_f32 v213, v184, v185
v_mfma_f32_16x16x32_bf16 v[198:201], v[20:23], a[56:59], v[166:169]
v_accvgpr_write_b32 a48, v210
v_accvgpr_write_b32 a49, v211
v_accvgpr_write_b32 a50, v212
v_accvgpr_write_b32 a51, v213
v_mfma_f32_16x16x32_bf16 v[202:205], v[22:25], a[60:63], v[170:173]
v_cvt_pk_bf16_f32 v214, v186, v187
v_cvt_pk_bf16_f32 v215, v188, v189
v_cvt_pk_bf16_f32 v216, v190, v191
v_cvt_pk_bf16_f32 v217, v192, v193
v_mfma_f32_16x16x32_bf16 v[206:209], v[20:23], a[60:63], v[170:173]
v_accvgpr_write_b32 a52, v214
v_accvgpr_write_b32 a53, v215
v_accvgpr_write_b32 a54, v216
v_accvgpr_write_b32 a55, v217
v_mfma_f32_16x16x32_bf16 v[178:181], v[22:25], a[64:67], v[158:161]
v_cvt_pk_bf16_f32 v210, v194, v195
v_cvt_pk_bf16_f32 v211, v196, v197
v_cvt_pk_bf16_f32 v212, v198, v199
v_cvt_pk_bf16_f32 v213, v200, v201
v_mfma_f32_16x16x32_bf16 v[182:185], v[20:23], a[64:67], v[158:161]
v_accvgpr_write_b32 a56, v210
v_accvgpr_write_b32 a57, v211
v_accvgpr_write_b32 a58, v212
v_accvgpr_write_b32 a59, v213
v_mfma_f32_16x16x32_bf16 v[186:189], v[22:25], a[68:71], v[162:165]
v_cvt_pk_bf16_f32 v214, v202, v203
v_cvt_pk_bf16_f32 v215, v204, v205
v_cvt_pk_bf16_f32 v216, v206, v207
v_cvt_pk_bf16_f32 v217, v208, v209
v_mfma_f32_16x16x32_bf16 v[190:193], v[20:23], a[68:71], v[162:165]
v_accvgpr_write_b32 a60, v214
v_accvgpr_write_b32 a61, v215
v_accvgpr_write_b32 a62, v216
v_accvgpr_write_b32 a63, v217
v_mfma_f32_16x16x32_bf16 v[194:197], v[22:25], a[72:75], v[166:169]
v_cvt_pk_bf16_f32 v210, v178, v179
v_cvt_pk_bf16_f32 v211, v180, v181
v_cvt_pk_bf16_f32 v212, v182, v183
v_cvt_pk_bf16_f32 v213, v184, v185
v_mfma_f32_16x16x32_bf16 v[198:201], v[20:23], a[72:75], v[166:169]
v_accvgpr_write_b32 a64, v210
v_accvgpr_write_b32 a65, v211
v_accvgpr_write_b32 a66, v212
v_accvgpr_write_b32 a67, v213
v_mfma_f32_16x16x32_bf16 v[202:205], v[22:25], a[76:79], v[170:173]
v_cvt_pk_bf16_f32 v214, v186, v187
v_cvt_pk_bf16_f32 v215, v188, v189
v_cvt_pk_bf16_f32 v216, v190, v191
v_cvt_pk_bf16_f32 v217, v192, v193
v_mfma_f32_16x16x32_bf16 v[206:209], v[20:23], a[76:79], v[170:173]
v_accvgpr_write_b32 a68, v214
v_accvgpr_write_b32 a69, v215
v_accvgpr_write_b32 a70, v216
v_accvgpr_write_b32 a71, v217
v_mfma_f32_16x16x32_bf16 v[178:181], v[22:25], a[80:83], v[158:161]
v_cvt_pk_bf16_f32 v210, v194, v195
v_cvt_pk_bf16_f32 v211, v196, v197
v_cvt_pk_bf16_f32 v212, v198, v199
v_cvt_pk_bf16_f32 v213, v200, v201
v_mfma_f32_16x16x32_bf16 v[182:185], v[20:23], a[80:83], v[158:161]
v_accvgpr_write_b32 a72, v210
v_accvgpr_write_b32 a73, v211
v_accvgpr_write_b32 a74, v212
v_accvgpr_write_b32 a75, v213
v_mfma_f32_16x16x32_bf16 v[186:189], v[22:25], a[84:87], v[162:165]
v_cvt_pk_bf16_f32 v214, v202, v203
v_cvt_pk_bf16_f32 v215, v204, v205
v_cvt_pk_bf16_f32 v216, v206, v207
v_cvt_pk_bf16_f32 v217, v208, v209
v_mfma_f32_16x16x32_bf16 v[190:193], v[20:23], a[84:87], v[162:165]
v_accvgpr_write_b32 a76, v214
v_accvgpr_write_b32 a77, v215
v_accvgpr_write_b32 a78, v216
v_accvgpr_write_b32 a79, v217
v_mfma_f32_16x16x32_bf16 v[194:197], v[22:25], a[88:91], v[166:169]
v_cvt_pk_bf16_f32 v210, v178, v179
v_cvt_pk_bf16_f32 v211, v180, v181
v_cvt_pk_bf16_f32 v212, v182, v183
v_cvt_pk_bf16_f32 v213, v184, v185
v_mfma_f32_16x16x32_bf16 v[198:201], v[20:23], a[88:91], v[166:169]
v_accvgpr_write_b32 a80, v210
)"
R"(v_accvgpr_write_b32 a81, v211
v_accvgpr_write_b32 a82, v212
v_accvgpr_write_b32 a83, v213
v_mfma_f32_16x16x32_bf16 v[202:205], v[22:25], a[92:95], v[170:173]
v_cvt_pk_bf16_f32 v214, v186, v187
v_cvt_pk_bf16_f32 v215, v188, v189
v_cvt_pk_bf16_f32 v216, v190, v191
v_cvt_pk_bf16_f32 v217, v192, v193
v_mfma_f32_16x16x32_bf16 v[206:209], v[20:23], a[92:95], v[170:173]
v_accvgpr_write_b32 a84, v214
v_accvgpr_write_b32 a85, v215
v_accvgpr_write_b32 a86, v216
v_accvgpr_write_b32 a87, v217
v_mfma_f32_16x16x32_bf16 v[178:181], v[22:25], a[96:99], v[158:161]
v_cvt_pk_bf16_f32 v210, v194, v195
v_cvt_pk_bf16_f32 v211, v196, v197
v_cvt_pk_bf16_f32 v212, v198, v199
v_cvt_pk_bf16_f32 v213, v200, v201
v_mfma_f32_16x16x32_bf16 v[182:185], v[20:23], a[96:99], v[158:161]
v_accvgpr_write_b32 a88, v210
v_accvgpr_write_b32 a89, v211
v_accvgpr_write_b32 a90, v212
v_accvgpr_write_b32 a91, v213
v_mfma_f32_16x16x32_bf16 v[186:189], v[22:25], a[100:103], v[162:165]
v_cvt_pk_bf16_f32 v214, v202, v203
v_cvt_pk_bf16_f32 v215, v204, v205
v_cvt_pk_bf16_f32 v216, v206, v207
v_cvt_pk_bf16_f32 v217, v208, v209
v_mfma_f32_16x16x32_bf16 v[190:193], v[20:23], a[100:103], v[162:165]
v_accvgpr_write_b32 a92, v214
v_accvgpr_write_b32 a93, v215
v_accvgpr_write_b32 a94, v216
v_accvgpr_write_b32 a95, v217
v_mfma_f32_16x16x32_bf16 v[194:197], v[22:25], a[104:107], v[166:169]
v_cvt_pk_bf16_f32 v210, v178, v179
v_cvt_pk_bf16_f32 v211, v180, v181
v_cvt_pk_bf16_f32 v212, v182, v183
v_cvt_pk_bf16_f32 v213, v184, v185
v_mfma_f32_16x16x32_bf16 v[198:201], v[20:23], a[104:107], v[166:169]
v_accvgpr_write_b32 a96, v210
v_accvgpr_write_b32 a97, v211
v_accvgpr_write_b32 a98, v212
v_accvgpr_write_b32 a99, v213
v_mfma_f32_16x16x32_bf16 v[202:205], v[22:25], a[108:111], v[170:173]
v_cvt_pk_bf16_f32 v214, v186, v187
v_cvt_pk_bf16_f32 v215, v188, v189
v_cvt_pk_bf16_f32 v216, v190, v191
v_cvt_pk_bf16_f32 v217, v192, v193
v_mfma_f32_16x16x32_bf16 v[206:209], v[20:23], a[108:111], v[170:173]
v_accvgpr_write_b32 a100, v214
v_accvgpr_write_b32 a101, v215
v_accvgpr_write_b32 a102, v216
v_accvgpr_write_b32 a103, v217
v_mfma_f32_16x16x32_bf16 v[178:181], v[22:25], a[112:115], v[158:161]
v_cvt_pk_bf16_f32 v210, v194, v195
v_cvt_pk_bf16_f32 v211, v196, v197
v_cvt_pk_bf16_f32 v212, v198, v199
v_cvt_pk_bf16_f32 v213, v200, v201
v_mfma_f32_16x16x32_bf16 v[182:185], v[20:23], a[112:115], v[158:161]
v_accvgpr_write_b32 a104, v210
v_accvgpr_write_b32 a105, v211
v_accvgpr_write_b32 a106, v212
v_accvgpr_write_b32 a107, v213
v_mfma_f32_16x16x32_bf16 v[186:189], v[22:25], a[116:119], v[162:165]
v_cvt_pk_bf16_f32 v214, v202, v203
v_cvt_pk_bf16_f32 v215, v204, v205
v_cvt_pk_bf16_f32 v216, v206, v207
v_cvt_pk_bf16_f32 v217, v208, v209
v_mfma_f32_16x16x32_bf16 v[190:193], v[20:23], a[116:119], v[162:165]
v_accvgpr_write_b32 a108, v214
v_accvgpr_write_b32 a109, v215
v_accvgpr_write_b32 a110, v216
v_accvgpr_write_b32 a111, v217
v_mfma_f32_16x16x32_bf16 v[194:197], v[22:25], a[120:123], v[166:169]
v_cvt_pk_bf16_f32 v210, v178, v179
v_cvt_pk_bf16_f32 v211, v180, v181
v_cvt_pk_bf16_f32 v212, v182, v183
v_cvt_pk_bf16_f32 v213, v184, v185
v_mfma_f32_16x16x32_bf16 v[198:201], v[20:23], a[120:123], v[166:169]
v_accvgpr_write_b32 a112, v210
v_accvgpr_write_b32 a113, v211
v_accvgpr_write_b32 a114, v212
v_accvgpr_write_b32 a115, v213
v_mfma_f32_16x16x32_bf16 v[202:205], v[22:25], a[124:127], v[170:173]
v_cvt_pk_bf16_f32 v214, v186, v187
v_cvt_pk_bf16_f32 v215, v188, v189
v_cvt_pk_bf16_f32 v216, v190, v191
v_cvt_pk_bf16_f32 v217, v192, v193
v_mfma_f32_16x16x32_bf16 v[206:209], v[20:23], a[124:127], v[170:173]
v_accvgpr_write_b32 a116, v214
v_accvgpr_write_b32 a117, v215
v_accvgpr_write_b32 a118, v216
v_accvgpr_write_b32 a119, v217
v_mfma_f32_16x16x32_bf16 v[178:181], v[22:25], a[128:131], v[158:161]
v_cvt_pk_bf16_f32 v210, v194, v195
v_cvt_pk_bf16_f32 v211, v196, v197
v_cvt_pk_bf16_f32 v212, v198, v199
v_cvt_pk_bf16_f32 v213, v200, v201
v_mfma_f32_16x16x32_bf16 v[182:185], v[20:23], a[128:131], v[158:161]
v_accvgpr_write_b32 a120, v210
v_accvgpr_write_b32 a121, v211
v_accvgpr_write_b32 a122, v212
v_accvgpr_write_b32 a123, v213
v_mfma_f32_16x16x32_bf16 v[186:189], v[22:25], a[132:135], v[162:165]
v_cvt_pk_bf16_f32 v214, v202, v203
v_cvt_pk_bf16_f32 v215, v204, v205
v_cvt_pk_bf16_f32 v216, v206, v207
v_cvt_pk_bf16_f32 v217, v208, v209
v_mfma_f32_16x16x32_bf16 v[190:193], v[20:23], a[132:135], v[162:165]
v_accvgpr_write_b32 a124, v214
v_accvgpr_write_b32 a125, v215
v_accvgpr_write_b32 a126, v216
v_accvgpr_write_b32 a127, v217
v_mfma_f32_16x16x32_bf16 v[194:197], v[22:25], a[136:139], v[166:169]
v_cvt_pk_bf16_f32 v210, v178, v179
v_cvt_pk_bf16_f32 v211, v180, v181
v_cvt_pk_bf16_f32 v212, v182, v183
v_cvt_pk_bf16_f32 v213, v184, v185
v_mfma_f32_16x16x32_bf16 v[198:201], v[20:23], a[136:139], v[166:169]
v_accvgpr_write_b32 a128, v210
v_accvgpr_write_b32 a129, v211
v_accvgpr_write_b32 a130, v212
v_accvgpr_write_b32 a131, v213
v_mfma_f32_16x16x32_bf16 v[202:205], v[22:25], a[140:143], v[170:173]
v_cvt_pk_bf16_f32 v214, v186, v187
v_cvt_pk_bf16_f32 v215, v188, v189
v_cvt_pk_bf16_f32 v216, v190, v191
v_cvt_pk_bf16_f32 v217, v192, v193
v_mfma_f32_16x16x32_bf16 v[206:209], v[20:23], a[140:143], v[170:173]
v_accvgpr_write_b32 a132, v214
v_accvgpr_write_b32 a133, v215
v_accvgpr_write_b32 a134, v216
v_accvgpr_write_b32 a135, v217
v_mfma_f32_16x16x32_bf16 v[178:181], v[22:25], a[144:147], v[158:161]
v_cvt_pk_bf16_f32 v210, v194, v195
v_cvt_pk_bf16_f32 v211, v196, v197
v_cvt_pk_bf16_f32 v212, v198, v199
v_cvt_pk_bf16_f32 v213, v200, v201
v_mfma_f32_16x16x32_bf16 v[182:185], v[20:23], a[144:147], v[158:161]
v_accvgpr_write_b32 a136, v210
v_accvgpr_write_b32 a137, v211
v_accvgpr_write_b32 a138, v212
v_accvgpr_write_b32 a139, v213
v_mfma_f32_16x16x32_bf16 v[186:189], v[22:25], a[148:151], v[162:165]
v_cvt_pk_bf16_f32 v214, v202, v203
v_cvt_pk_bf16_f32 v215, v204, v205
v_cvt_pk_bf16_f32 v216, v206, v207
v_cvt_pk_bf16_f32 v217, v208, v209
v_mfma_f32_16x16x32_bf16 v[190:193], v[20:23], a[148:151], v[162:165]
v_accvgpr_write_b32 a140, v214
v_accvgpr_write_b32 a141, v215
v_accvgpr_write_b32 a142, v216
v_accvgpr_write_b32 a143, v217
v_mfma_f32_16x16x32_bf16 v[194:197], v[22:25], a[152:155], v[166:169]
v_cvt_pk_bf16_f32 v210, v178, v179
v_cvt_pk_bf16_f32 v211, v180, v181
v_cvt_pk_bf16_f32 v212, v182, v183
v_cvt_pk_bf16_f32 v213, v184, v185
v_mfma_f32_16x16x32_bf16 v[198:201], v[20:23], a[152:155], v[166:169]
v_accvgpr_write_b32 a144, v210
v_accvgpr_write_b32 a145, v211
v_accvgpr_write_b32 a146, v212
v_accvgpr_write_b32 a147, v213
v_mfma_f32_16x16x32_bf16 v[202:205], v[22:25], a[156:159], v[170:173]
v_cvt_pk_bf16_f32 v214, v186, v187
v_cvt_pk_bf16_f32 v215, v188, v189
v_cvt_pk_bf16_f32 v216, v190, v191
v_cvt_pk_bf16_f32 v217, v192, v193
v_mfma_f32_16x16x32_bf16 v[206:209], v[20:23], a[156:159], v[170:173]
v_accvgpr_write_b32 a148, v214
v_accvgpr_write_b32 a149, v215
v_accvgpr_write_b32 a150, v216
v_accvgpr_write_b32 a151, v217
v_mfma_f32_16x16x32_bf16 v[178:181], v[22:25], a[160:163], v[158:161]
v_cvt_pk_bf16_f32 v210, v194, v195
v_cvt_pk_bf16_f32 v211, v196, v197
v_cvt_pk_bf16_f32 v212, v198, v199
v_cvt_pk_bf16_f32 v213, v200, v201
v_mfma_f32_16x16x32_bf16 v[182:185], v[20:23], a[160:163], v[158:161]
v_accvgpr_write_b32 a152, v210
v_accvgpr_write_b32 a153, v211
v_accvgpr_write_b32 a154, v212
v_accvgpr_write_b32 a155, v213
v_mfma_f32_16x16x32_bf16 v[186:189], v[22:25], a[164:167], v[162:165]
v_cvt_pk_bf16_f32 v214, v202, v203
v_cvt_pk_bf16_f32 v215, v204, v205
v_cvt_pk_bf16_f32 v216, v206, v207
v_cvt_pk_bf16_f32 v217, v208, v209
v_mfma_f32_16x16x32_bf16 v[190:193], v[20:23], a[164:167], v[162:165]
v_accvgpr_write_b32 a156, v214
v_accvgpr_write_b32 a157, v215
v_accvgpr_write_b32 a158, v216
v_accvgpr_write_b32 a159, v217
v_mfma_f32_16x16x32_bf16 v[194:197], v[22:25], a[168:171], v[166:169]
v_cvt_pk_bf16_f32 v210, v178, v179
v_cvt_pk_bf16_f32 v211, v180, v181
v_cvt_pk_bf16_f32 v212, v182, v183
v_cvt_pk_bf16_f32 v213, v184, v185
v_mfma_f32_16x16x32_bf16 v[198:201], v[20:23], a[168:171], v[166:169]
v_accvgpr_write_b32 a160, v210
v_accvgpr_write_b32 a161, v211
v_accvgpr_write_b32 a162, v212
v_accvgpr_write_b32 a163, v213
v_mfma_f32_16x16x32_bf16 v[202:205], v[22:25], a[172:175], v[170:173]
v_cvt_pk_bf16_f32 v214, v186, v187
v_cvt_pk_bf16_f32 v215, v188, v189
v_cvt_pk_bf16_f32 v216, v190, v191
v_cvt_pk_bf16_f32 v217, v192, v193
v_mfma_f32_16x16x32_bf16 v[206:209], v[20:23], a[172:175], v[170:173]
v_accvgpr_write_b32 a164, v214
v_accvgpr_write_b32 a165, v215
v_accvgpr_write_b32 a166, v216
v_accvgpr_write_b32 a167, v217
v_mfma_f32_16x16x32_bf16 v[178:181], v[22:25], a[176:179], v[158:161]
v_cvt_pk_bf16_f32 v210, v194, v195
v_cvt_pk_bf16_f32 v211, v196, v197
v_cvt_pk_bf16_f32 v212, v198, v199
v_cvt_pk_bf16_f32 v213, v200, v201
v_mfma_f32_16x16x32_bf16 v[182:185], v[20:23], a[176:179], v[158:161]
v_accvgpr_write_b32 a168, v210
v_accvgpr_write_b32 a169, v211
v_accvgpr_write_b32 a170, v212
v_accvgpr_write_b32 a171, v213
v_mfma_f32_16x16x32_bf16 v[186:189], v[22:25], a[180:183], v[162:165]
v_cvt_pk_bf16_f32 v214, v202, v203
v_cvt_pk_bf16_f32 v215, v204, v205
v_cvt_pk_bf16_f32 v216, v206, v207
v_cvt_pk_bf16_f32 v217, v208, v209
v_mfma_f32_16x16x32_bf16 v[190:193], v[20:23], a[180:183], v[162:165]
v_accvgpr_write_b32 a172, v214
v_accvgpr_write_b32 a173, v215
v_accvgpr_write_b32 a174, v216
v_accvgpr_write_b32 a175, v217
v_mfma_f32_16x16x32_bf16 v[194:197], v[22:25], a[184:187], v[166:169]
v_cvt_pk_bf16_f32 v210, v178, v179
v_cvt_pk_bf16_f32 v211, v180, v181
v_cvt_pk_bf16_f32 v212, v182, v183
v_cvt_pk_bf16_f32 v213, v184, v185
v_mfma_f32_16x16x32_bf16 v[198:201], v[20:23], a[184:187], v[166:169]
v_accvgpr_write_b32 a176, v210
v_accvgpr_write_b32 a177, v211
v_accvgpr_write_b32 a178, v212
v_accvgpr_write_b32 a179, v213
v_mfma_f32_16x16x32_bf16 v[202:205], v[22:25], a[188:191], v[170:173]
v_cvt_pk_bf16_f32 v214, v186, v187
v_cvt_pk_bf16_f32 v215, v188, v189
v_cvt_pk_bf16_f32 v216, v190, v191
v_cvt_pk_bf16_f32 v217, v192, v193
v_mfma_f32_16x16x32_bf16 v[206:209], v[20:23], a[188:191], v[170:173]
v_accvgpr_write_b32 a180, v214
v_accvgpr_write_b32 a181, v215
v_accvgpr_write_b32 a182, v216
v_accvgpr_write_b32 a183, v217
v_mfma_f32_16x16x32_bf16 v[178:181], v[22:25], a[192:195], v[158:161]
v_cvt_pk_bf16_f32 v210, v194, v195
v_cvt_pk_bf16_f32 v211, v196, v197
v_cvt_pk_bf16_f32 v212, v198, v199
v_cvt_pk_bf16_f32 v213, v200, v201
v_mfma_f32_16x16x32_bf16 v[182:185], v[20:23], a[192:195], v[158:161]
v_accvgpr_write_b32 a184, v210
v_accvgpr_write_b32 a185, v211
v_accvgpr_write_b32 a186, v212
v_accvgpr_write_b32 a187, v213
v_mfma_f32_16x16x32_bf16 v[186:189], v[22:25], a[196:199], v[162:165]
v_cvt_pk_bf16_f32 v214, v202, v203
v_cvt_pk_bf16_f32 v215, v204, v205
v_cvt_pk_bf16_f32 v216, v206, v207
v_cvt_pk_bf16_f32 v217, v208, v209
v_mfma_f32_16x16x32_bf16 v[190:193], v[20:23], a[196:199], v[162:165]
v_accvgpr_write_b32 a188, v214
v_accvgpr_write_b32 a189, v215
v_accvgpr_write_b32 a190, v216
v_accvgpr_write_b32 a191, v217
v_mfma_f32_16x16x32_bf16 v[194:197], v[22:25], a[200:203], v[166:169]
v_cvt_pk_bf16_f32 v210, v178, v179
v_cvt_pk_bf16_f32 v211, v180, v181
v_cvt_pk_bf16_f32 v212, v182, v183
v_cvt_pk_bf16_f32 v213, v184, v185
v_mfma_f32_16x16x32_bf16 v[198:201], v[20:23], a[200:203], v[166:169]
v_accvgpr_write_b32 a192, v210
v_accvgpr_write_b32 a193, v211
v_accvgpr_write_b32 a194, v212
v_accvgpr_write_b32 a195, v213
v_mfma_f32_16x16x32_bf16 v[202:205], v[22:25], a[204:207], v[170:173]
v_cvt_pk_bf16_f32 v214, v186, v187
v_cvt_pk_bf16_f32 v215, v188, v189
v_cvt_pk_bf16_f32 v216, v190, v191
v_cvt_pk_bf16_f32 v217, v192, v193
v_mfma_f32_16x16x32_bf16 v[206:209], v[20:23], a[204:207], v[170:173]
v_accvgpr_write_b32 a196, v214
v_accvgpr_write_b32 a197, v215
v_accvgpr_write_b32 a198, v216
v_accvgpr_write_b32 a199, v217
v_mfma_f32_16x16x32_bf16 v[178:181], v[22:25], a[208:211], v[158:161]
v_cvt_pk_bf16_f32 v210, v194, v195
v_cvt_pk_bf16_f32 v211, v196, v197
v_cvt_pk_bf16_f32 v212, v198, v199
v_cvt_pk_bf16_f32 v213, v200, v201
v_mfma_f32_16x16x32_bf16 v[182:185], v[20:23], a[208:211], v[158:161]
v_accvgpr_write_b32 a200, v210
v_accvgpr_write_b32 a201, v211
v_accvgpr_write_b32 a202, v212
v_accvgpr_write_b32 a203, v213
v_mfma_f32_16x16x32_bf16 v[186:189], v[22:25], a[212:215], v[162:165]
v_cvt_pk_bf16_f32 v214, v202, v203
v_cvt_pk_bf16_f32 v215, v204, v205
v_cvt_pk_bf16_f32 v216, v206, v207
v_cvt_pk_bf16_f32 v217, v208, v209
v_mfma_f32_16x16x32_bf16 v[190:193], v[20:23], a[212:215], v[162:165]
v_accvgpr_write_b32 a204, v214
v_accvgpr_write_b32 a205, v215
v_accvgpr_write_b32 a206, v216
v_accvgpr_write_b32 a207, v217
v_mfma_f32_16x16x32_bf16 v[194:197], v[22:25], a[216:219], v[166:169]
v_cvt_pk_bf16_f32 v210, v178, v179
v_cvt_pk_bf16_f32 v211, v180, v181
v_cvt_pk_bf16_f32 v212, v182, v183
v_cvt_pk_bf16_f32 v213, v184, v185
v_mfma_f32_16x16x32_bf16 v[198:201], v[20:23], a[216:219], v[166:169]
v_accvgpr_write_b32 a208, v210
v_accvgpr_write_b32 a209, v211
v_accvgpr_write_b32 a210, v212
v_accvgpr_write_b32 a211, v213
v_mfma_f32_16x16x32_bf16 v[202:205], v[22:25], a[220:223], v[170:173]
v_cvt_pk_bf16_f32 v214, v186, v187
v_cvt_pk_bf16_f32 v215, v188, v189
v_cvt_pk_bf16_f32 v216, v190, v191
v_cvt_pk_bf16_f32 v217, v192, v193
v_mfma_f32_16x16x32_bf16 v[206:209], v[20:23], a[220:223], v[170:173]
v_accvgpr_write_b32 a212, v214
v_accvgpr_write_b32 a213, v215
v_accvgpr_write_b32 a214, v216
v_accvgpr_write_b32 a215, v217
v_mfma_f32_16x16x32_bf16 v[178:181], v[22:25], a[224:227], v[158:161]
v_cvt_pk_bf16_f32 v210, v194, v195
v_cvt_pk_bf16_f32 v211, v196, v197
v_cvt_pk_bf16_f32 v212, v198, v199
v_cvt_pk_bf16_f32 v213, v200, v201
v_mfma_f32_16x16x32_bf16 v[182:185], v[20:23], a[224:227], v[158:161]
v_accvgpr_write_b32 a216, v210
v_accvgpr_write_b32 a217, v211
v_accvgpr_write_b32 a218, v212
v_accvgpr_write_b32 a219, v213
v_mfma_f32_16x16x32_bf16 v[186:189], v[22:25], a[228:231], v[162:165]
v_cvt_pk_bf16_f32 v214, v202, v203
v_cvt_pk_bf16_f32 v215, v204, v205
v_cvt_pk_bf16_f32 v216, v206, v207
v_cvt_pk_bf16_f32 v217, v208, v209
v_mfma_f32_16x16x32_bf16 v[190:193], v[20:23], a[228:231], v[162:165]
v_accvgpr_write_b32 a220, v214
v_accvgpr_write_b32 a221, v215
v_accvgpr_write_b32 a222, v216
v_accvgpr_write_b32 a223, v217
v_mfma_f32_16x16x32_bf16 v[194:197], v[22:25], a[232:235], v[166:169]
v_cvt_pk_bf16_f32 v210, v178, v179
v_cvt_pk_bf16_f32 v211, v180, v181
v_cvt_pk_bf16_f32 v212, v182, v183
v_cvt_pk_bf16_f32 v213, v184, v185
v_mfma_f32_16x16x32_bf16 v[198:201], v[20:23], a[232:235], v[166:169]
v_accvgpr_write_b32 a224, v210
v_accvgpr_write_b32 a225, v211
v_accvgpr_write_b32 a226, v212
v_accvgpr_write_b32 a227, v213
v_mfma_f32_16x16x32_bf16 v[202:205], v[22:25], a[236:239], v[170:173]
v_cvt_pk_bf16_f32 v214, v186, v187
v_cvt_pk_bf16_f32 v215, v188, v189
v_cvt_pk_bf16_f32 v216, v190, v191
v_cvt_pk_bf16_f32 v217, v192, v193
v_mfma_f32_16x16x32_bf16 v[206:209], v[20:23], a[236:239], v[170:173]
v_accvgpr_write_b32 a228, v214
v_accvgpr_write_b32 a229, v215
v_accvgpr_write_b32 a230, v216
v_accvgpr_write_b32 a231, v217
v_mfma_f32_16x16x32_bf16 v[178:181], v[22:25], a[240:243], v[158:161]
v_cvt_pk_bf16_f32 v210, v194, v195
v_cvt_pk_bf16_f32 v211, v196, v197
v_cvt_pk_bf16_f32 v212, v198, v199
v_cvt_pk_bf16_f32 v213, v200, v201
v_mfma_f32_16x16x32_bf16 v[182:185], v[20:23], a[240:243], v[158:161]
v_accvgpr_write_b32 a232, v210
v_accvgpr_write_b32 a233, v211
v_accvgpr_write_b32 a234, v212
v_accvgpr_write_b32 a235, v213
v_mfma_f32_16x16x32_bf16 v[186:189], v[22:25], a[244:247], v[162:165]
v_cvt_pk_bf16_f32 v214, v202, v203
v_cvt_pk_bf16_f32 v215, v204, v205
v_cvt_pk_bf16_f32 v216, v206, v207
v_cvt_pk_bf16_f32 v217, v208, v209
v_mfma_f32_16x16x32_bf16 v[190:193], v[20:23], a[244:247], v[162:165]
v_accvgpr_write_b32 a236, v214
v_accvgpr_write_b32 a237, v215
v_accvgpr_write_b32 a238, v216
v_accvgpr_write_b32 a239, v217
v_mfma_f32_16x16x32_bf16 v[194:197], v[22:25], a[248:251], v[166:169]
v_cvt_pk_bf16_f32 v210, v178, v179
v_cvt_pk_bf16_f32 v211, v180, v181
v_cvt_pk_bf16_f32 v212, v182, v183
v_cvt_pk_bf16_f32 v213, v184, v185
v_mfma_f32_16x16x32_bf16 v[198:201], v[20:23], a[248:251], v[166:169]
v_accvgpr_write_b32 a240, v210
)"
R"(v_accvgpr_write_b32 a241, v211
v_accvgpr_write_b32 a242, v212
v_accvgpr_write_b32 a243, v213
v_mfma_f32_16x16x32_bf16 v[202:205], v[22:25], a[252:255], v[170:173]
v_cvt_pk_bf16_f32 v214, v186, v187
v_cvt_pk_bf16_f32 v215, v188, v189
v_cvt_pk_bf16_f32 v216, v190, v191
v_cvt_pk_bf16_f32 v217, v192, v193
v_mfma_f32_16x16x32_bf16 v[206:209], v[20:23], a[252:255], v[170:173]
v_accvgpr_write_b32 a244, v214
v_accvgpr_write_b32 a245, v215
v_accvgpr_write_b32 a246, v216
v_accvgpr_write_b32 a247, v217
v_mfma_f32_16x16x32_bf16 v[178:181], v[22:25], v[26:29], v[158:161]
v_cvt_pk_bf16_f32 v210, v194, v195
v_cvt_pk_bf16_f32 v211, v196, v197
v_cvt_pk_bf16_f32 v212, v198, v199
v_cvt_pk_bf16_f32 v213, v200, v201
v_mfma_f32_16x16x32_bf16 v[182:185], v[20:23], v[26:29], v[158:161]
v_accvgpr_write_b32 a248, v210
v_accvgpr_write_b32 a249, v211
v_accvgpr_write_b32 a250, v212
v_accvgpr_write_b32 a251, v213
v_mfma_f32_16x16x32_bf16 v[186:189], v[22:25], v[30:33], v[162:165]
v_cvt_pk_bf16_f32 v214, v202, v203
v_cvt_pk_bf16_f32 v215, v204, v205
v_cvt_pk_bf16_f32 v216, v206, v207
v_cvt_pk_bf16_f32 v217, v208, v209
v_mfma_f32_16x16x32_bf16 v[190:193], v[20:23], v[30:33], v[162:165]
v_accvgpr_write_b32 a252, v214
v_accvgpr_write_b32 a253, v215
v_accvgpr_write_b32 a254, v216
v_accvgpr_write_b32 a255, v217
v_mfma_f32_16x16x32_bf16 v[194:197], v[22:25], v[34:37], v[166:169]
v_cvt_pk_bf16_f32 v26, v178, v179
v_cvt_pk_bf16_f32 v27, v180, v181
v_cvt_pk_bf16_f32 v28, v182, v183
v_cvt_pk_bf16_f32 v29, v184, v185
v_mfma_f32_16x16x32_bf16 v[198:201], v[20:23], v[34:37], v[166:169]
v_mfma_f32_16x16x32_bf16 v[202:205], v[22:25], v[38:41], v[170:173]
v_cvt_pk_bf16_f32 v30, v186, v187
v_cvt_pk_bf16_f32 v31, v188, v189
v_cvt_pk_bf16_f32 v32, v190, v191
v_cvt_pk_bf16_f32 v33, v192, v193
v_mfma_f32_16x16x32_bf16 v[206:209], v[20:23], v[38:41], v[170:173]
v_mfma_f32_16x16x32_bf16 v[178:181], v[22:25], v[42:45], v[158:161]
v_cvt_pk_bf16_f32 v34, v194, v195
v_cvt_pk_bf16_f32 v35, v196, v197
v_cvt_pk_bf16_f32 v36, v198, v199
v_cvt_pk_bf16_f32 v37, v200, v201
v_mfma_f32_16x16x32_bf16 v[182:185], v[20:23], v[42:45], v[158:161]
v_mfma_f32_16x16x32_bf16 v[186:189], v[22:25], v[46:49], v[162:165]
v_cvt_pk_bf16_f32 v38, v202, v203
v_cvt_pk_bf16_f32 v39, v204, v205
v_cvt_pk_bf16_f32 v40, v206, v207
v_cvt_pk_bf16_f32 v41, v208, v209
v_mfma_f32_16x16x32_bf16 v[190:193], v[20:23], v[46:49], v[162:165]
v_mfma_f32_16x16x32_bf16 v[194:197], v[22:25], v[50:53], v[166:169]
v_cvt_pk_bf16_f32 v42, v178, v179
v_cvt_pk_bf16_f32 v43, v180, v181
v_cvt_pk_bf16_f32 v44, v182, v183
v_cvt_pk_bf16_f32 v45, v184, v185
v_mfma_f32_16x16x32_bf16 v[198:201], v[20:23], v[50:53], v[166:169]
v_mfma_f32_16x16x32_bf16 v[202:205], v[22:25], v[54:57], v[170:173]
v_cvt_pk_bf16_f32 v46, v186, v187
v_cvt_pk_bf16_f32 v47, v188, v189
v_cvt_pk_bf16_f32 v48, v190, v191
v_cvt_pk_bf16_f32 v49, v192, v193
v_mfma_f32_16x16x32_bf16 v[206:209], v[20:23], v[54:57], v[170:173]
v_cvt_pk_bf16_f32 v50, v194, v195
v_cvt_pk_bf16_f32 v51, v196, v197
v_cvt_pk_bf16_f32 v52, v198, v199
v_cvt_pk_bf16_f32 v53, v200, v201
v_cvt_pk_bf16_f32 v54, v202, v203
v_cvt_pk_bf16_f32 v55, v204, v205
s_nop 1
v_cvt_pk_bf16_f32 v56, v206, v207
v_cvt_pk_bf16_f32 v57, v208, v209
s_barrier
s_and_b32 s75, s36, 3
s_cmp_eq_u32 s75, 1
s_cbranch_scc1 .Lgu_w2
s_cmp_eq_u32 s75, 2
s_cbranch_scc1 .Lgu_w3
s_cmp_eq_u32 s75, 3
s_cbranch_scc1 .Lgu_w4
.Lgu_w1:
v_add_u32 v254, -1, v5
v_add_u32 v255, -1, v5
v_add_lshl_u32 v252, v7, 0, 7
v_bfi_b32 v252, s69, v252, v6
v_add_lshl_u32 v253, v5, 0, 7
v_bfi_b32 v253, s69, v253, v4
s_add_u32 m0, s43, 18432
s_mov_b32 s70, 32
s_waitcnt vmcnt(0) lgkmcnt(0)
s_nop 7
.p2align 6
.Lgu_top5:
s_waitcnt lgkmcnt(5)
v_mfma_f32_16x16x32_bf16 v[58:61], a[0:3], v[90:93], 0
s_waitcnt lgkmcnt(4)
v_cvt_pk_bf16_f32 v146, v138, v139
v_cvt_pk_bf16_f32 v147, v140, v141
v_mfma_f32_16x16x32_bf16 v[58:61], a[16:19], v[94:97], v[58:61]
s_waitcnt lgkmcnt(3)
v_mfma_f32_16x16x32_bf16 v[58:61], a[32:35], v[98:101], v[58:61]
s_waitcnt lgkmcnt(2)
v_mfma_f32_16x16x32_bf16 v[58:61], a[48:51], v[102:105], v[58:61]
s_waitcnt lgkmcnt(1)
v_mul_f32 v142, v78, v86
v_mul_f32 v143, v79, v87
v_mfma_f32_16x16x32_bf16 v[58:61], a[64:67], v[106:109], v[58:61]
s_waitcnt lgkmcnt(0)
v_mul_f32 v144, v80, v88
v_mul_f32 v145, v81, v89
v_mfma_f32_16x16x32_bf16 v[58:61], a[80:83], v[110:113], v[58:61]
ds_read_b128 v[114:117], v8 offset:6144
v_cvt_pk_bf16_f32 v148, v142, v143
v_mfma_f32_16x16x32_bf16 v[66:69], a[8:11], v[90:93], 0
ds_read_b128 v[118:121], v8 offset:7168
v_cvt_pk_bf16_f32 v149, v144, v145
v_mfma_f32_16x16x32_bf16 v[66:69], a[24:27], v[94:97], v[66:69]
v_mfma_f32_16x16x32_bf16 v[66:69], a[40:43], v[98:101], v[66:69]
s_waitcnt vmcnt(6)
s_barrier
v_mfma_f32_16x16x32_bf16 v[66:69], a[56:59], v[102:105], v[66:69]
v_mfma_f32_16x16x32_bf16 v[66:69], a[72:75], v[106:109], v[66:69]
global_store_dwordx4 v3, v[146:149], s[38:39] sc1
v_mfma_f32_16x16x32_bf16 v[66:69], a[88:91], v[110:113], v[66:69]
ds_read_b128 v[122:125], v8 offset:8192
v_add_u32 v3, 147456, v3
v_mfma_f32_16x16x32_bf16 v[62:65], a[4:7], v[90:93], 0
v_mfma_f32_16x16x32_bf16 v[62:65], a[20:23], v[94:97], v[62:65]
v_mfma_f32_16x16x32_bf16 v[62:65], a[36:39], v[98:101], v[62:65]
ds_read_b128 v[126:129], v8 offset:9216
v_mfma_f32_16x16x32_bf16 v[62:65], a[52:55], v[102:105], v[62:65]
v_mfma_f32_16x16x32_bf16 v[62:65], a[68:71], v[106:109], v[62:65]
v_mfma_f32_16x16x32_bf16 v[62:65], a[84:87], v[110:113], v[62:65]
ds_read_b128 v[130:133], v8 offset:10240
v_mfma_f32_16x16x32_bf16 v[70:73], a[12:15], v[90:93], 0
v_mfma_f32_16x16x32_bf16 v[70:73], a[28:31], v[94:97], v[70:73]
v_mfma_f32_16x16x32_bf16 v[70:73], a[44:47], v[98:101], v[70:73]
ds_read_b128 v[134:137], v8 offset:11264
v_mfma_f32_16x16x32_bf16 v[70:73], a[60:63], v[102:105], v[70:73]
v_mfma_f32_16x16x32_bf16 v[70:73], a[76:79], v[106:109], v[70:73]
v_mfma_f32_16x16x32_bf16 v[70:73], a[92:95], v[110:113], v[70:73]
s_waitcnt lgkmcnt(5)
v_mfma_f32_16x16x32_bf16 v[58:61], a[96:99], v[114:117], v[58:61]
s_waitcnt lgkmcnt(4)
v_mfma_f32_16x16x32_bf16 v[58:61], a[112:115], v[118:121], v[58:61]
s_waitcnt lgkmcnt(3)
v_mfma_f32_16x16x32_bf16 v[58:61], a[128:131], v[122:125], v[58:61]
s_waitcnt lgkmcnt(2)
v_mfma_f32_16x16x32_bf16 v[58:61], a[144:147], v[126:129], v[58:61]
s_waitcnt lgkmcnt(1)
v_mfma_f32_16x16x32_bf16 v[58:61], a[160:163], v[130:133], v[58:61]
s_waitcnt lgkmcnt(0)
v_mfma_f32_16x16x32_bf16 v[58:61], a[176:179], v[134:137], v[58:61]
ds_read_b128 v[90:93], v8 offset:12288
v_mfma_f32_16x16x32_bf16 v[66:69], a[104:107], v[114:117], v[66:69]
v_mfma_f32_16x16x32_bf16 v[66:69], a[120:123], v[118:121], v[66:69]
v_mfma_f32_16x16x32_bf16 v[66:69], a[136:139], v[122:125], v[66:69]
ds_read_b128 v[94:97], v8 offset:13312
v_mfma_f32_16x16x32_bf16 v[66:69], a[152:155], v[126:129], v[66:69]
v_mfma_f32_16x16x32_bf16 v[66:69], a[168:171], v[130:133], v[66:69]
v_mfma_f32_16x16x32_bf16 v[66:69], a[184:187], v[134:137], v[66:69]
ds_read_b128 v[98:101], v8 offset:14336
v_mfma_f32_16x16x32_bf16 v[62:65], a[100:103], v[114:117], v[62:65]
v_mfma_f32_16x16x32_bf16 v[62:65], a[116:119], v[118:121], v[62:65]
v_mfma_f32_16x16x32_bf16 v[62:65], a[132:135], v[122:125], v[62:65]
ds_read_b128 v[102:105], v8 offset:15360
v_mfma_f32_16x16x32_bf16 v[62:65], a[148:151], v[126:129], v[62:65]
v_mfma_f32_16x16x32_bf16 v[62:65], a[164:167], v[130:133], v[62:65]
v_mfma_f32_16x16x32_bf16 v[62:65], a[180:183], v[134:137], v[62:65]
ds_read_b128 v[106:109], v8 offset:16384
v_mfma_f32_16x16x32_bf16 v[70:73], a[108:111], v[114:117], v[70:73]
v_mfma_f32_16x16x32_bf16 v[70:73], a[124:127], v[118:121], v[70:73]
v_mfma_f32_16x16x32_bf16 v[70:73], a[140:143], v[122:125], v[70:73]
ds_read_b128 v[110:113], v8 offset:17408
v_mfma_f32_16x16x32_bf16 v[70:73], a[156:159], v[126:129], v[70:73]
s_xor_b32 s43, s43, 0x10000
v_mfma_f32_16x16x32_bf16 v[70:73], a[172:175], v[130:133], v[70:73]
s_add_u32 m0, s43, 0
v_mfma_f32_16x16x32_bf16 v[70:73], a[188:191], v[134:137], v[70:73]
s_waitcnt lgkmcnt(5)
v_mfma_f32_16x16x32_bf16 v[58:61], a[192:195], v[90:93], v[58:61]
s_barrier
s_waitcnt lgkmcnt(4)
v_mfma_f32_16x16x32_bf16 v[58:61], a[208:211], v[94:97], v[58:61]
s_waitcnt lgkmcnt(3)
global_load_lds_dwordx4 v2, s[38:39] offset:-2048 sc1
v_mfma_f32_16x16x32_bf16 v[58:61], a[224:227], v[98:101], v[58:61]
s_waitcnt lgkmcnt(2)
v_mfma_f32_16x16x32_bf16 v[58:61], a[240:243], v[102:105], v[58:61]
s_waitcnt lgkmcnt(1)
v_mfma_f32_16x16x32_bf16 v[58:61], v[26:29], v[106:109], v[58:61]
s_waitcnt lgkmcnt(0)
global_load_lds_dwordx4 v2, s[38:39] offset:-1024 sc1
v_mfma_f32_16x16x32_bf16 v[58:61], v[42:45], v[110:113], v[58:61]
v_mfma_f32_16x16x32_bf16 v[66:69], a[200:203], v[90:93], v[66:69]
v_mfma_f32_16x16x32_bf16 v[66:69], a[216:219], v[94:97], v[66:69]
global_load_lds_dwordx4 v2, s[38:39] offset:0 sc1
v_mfma_f32_16x16x32_bf16 v[66:69], a[232:235], v[98:101], v[66:69]
v_mfma_f32_16x16x32_bf16 v[66:69], a[248:251], v[102:105], v[66:69]
v_mfma_f32_16x16x32_bf16 v[66:69], v[34:37], v[106:109], v[66:69]
global_load_lds_dwordx4 v2, s[38:39] offset:1024 sc1
v_mfma_f32_16x16x32_bf16 v[66:69], v[50:53], v[110:113], v[66:69]
v_mfma_f32_16x16x32_bf16 v[62:65], a[196:199], v[90:93], v[62:65]
s_waitcnt vmcnt(5)
s_barrier
v_mfma_f32_16x16x32_bf16 v[62:65], a[212:215], v[94:97], v[62:65]
global_load_lds_dwordx4 v2, s[38:39] offset:2048 sc1
v_mfma_f32_16x16x32_bf16 v[62:65], a[228:231], v[98:101], v[62:65]
v_mfma_f32_16x16x32_bf16 v[62:65], a[244:247], v[102:105], v[62:65]
ds_read_b128 v[114:117], v8 offset:18432
v_mfma_f32_16x16x32_bf16 v[62:65], v[30:33], v[106:109], v[62:65]
ds_read_b128 v[118:121], v8 offset:19456
global_load_lds_dwordx4 v2, s[38:39] offset:3072 sc1
v_mfma_f32_16x16x32_bf16 v[62:65], v[46:49], v[110:113], v[62:65]
ds_read_b128 v[122:125], v8 offset:20480
v_add_u32 v2, 18688, v2
v_mfma_f32_16x16x32_bf16 v[70:73], a[204:207], v[90:93], v[70:73]
ds_read_b128 v[126:129], v8 offset:21504
v_min_u32 v2, v2, v9
v_mfma_f32_16x16x32_bf16 v[70:73], a[220:223], v[94:97], v[70:73]
ds_read_b128 v[130:133], v8 offset:22528
v_mul_f32 v138, v58, v66
v_mfma_f32_16x16x32_bf16 v[70:73], a[236:239], v[98:101], v[70:73]
ds_read_b128 v[134:137], v8 offset:23552
v_mul_f32 v139, v59, v67
v_mfma_f32_16x16x32_bf16 v[70:73], a[252:255], v[102:105], v[70:73]
v_mul_f32 v140, v60, v68
v_mul_f32 v141, v61, v69
v_mfma_f32_16x16x32_bf16 v[70:73], v[38:41], v[106:109], v[70:73]
v_cvt_pk_bf16_f32 v146, v138, v139
v_cvt_pk_bf16_f32 v147, v140, v141
v_mfma_f32_16x16x32_bf16 v[70:73], v[54:57], v[110:113], v[70:73]
s_waitcnt lgkmcnt(5)
v_mfma_f32_16x16x32_bf16 v[74:77], a[0:3], v[114:117], 0
s_waitcnt lgkmcnt(4)
v_mfma_f32_16x16x32_bf16 v[74:77], a[16:19], v[118:121], v[74:77]
s_waitcnt lgkmcnt(3)
v_mfma_f32_16x16x32_bf16 v[74:77], a[32:35], v[122:125], v[74:77]
s_waitcnt lgkmcnt(2)
v_mfma_f32_16x16x32_bf16 v[74:77], a[48:51], v[126:129], v[74:77]
s_waitcnt lgkmcnt(1)
v_mul_f32 v142, v62, v70
v_mul_f32 v143, v63, v71
v_mfma_f32_16x16x32_bf16 v[74:77], a[64:67], v[130:133], v[74:77]
s_waitcnt lgkmcnt(0)
v_mul_f32 v144, v64, v72
v_mul_f32 v145, v65, v73
v_mfma_f32_16x16x32_bf16 v[74:77], a[80:83], v[134:137], v[74:77]
ds_read_b128 v[90:93], v8 offset:24576
v_cvt_pk_bf16_f32 v148, v142, v143
v_mfma_f32_16x16x32_bf16 v[82:85], a[8:11], v[114:117], 0
ds_read_b128 v[94:97], v8 offset:25600
v_cvt_pk_bf16_f32 v149, v144, v145
v_mfma_f32_16x16x32_bf16 v[82:85], a[24:27], v[118:121], v[82:85]
v_mfma_f32_16x16x32_bf16 v[82:85], a[40:43], v[122:125], v[82:85]
s_waitcnt vmcnt(6)
s_barrier
v_mfma_f32_16x16x32_bf16 v[82:85], a[56:59], v[126:129], v[82:85]
v_mfma_f32_16x16x32_bf16 v[82:85], a[72:75], v[130:133], v[82:85]
global_store_dwordx4 v3, v[146:149], s[38:39] sc1
v_mfma_f32_16x16x32_bf16 v[82:85], a[88:91], v[134:137], v[82:85]
ds_read_b128 v[98:101], v8 offset:26624
v_add_u32 v3, 147456, v3
v_mfma_f32_16x16x32_bf16 v[78:81], a[4:7], v[114:117], 0
v_mfma_f32_16x16x32_bf16 v[78:81], a[20:23], v[118:121], v[78:81]
v_mfma_f32_16x16x32_bf16 v[78:81], a[36:39], v[122:125], v[78:81]
ds_read_b128 v[102:105], v8 offset:27648
v_mfma_f32_16x16x32_bf16 v[78:81], a[52:55], v[126:129], v[78:81]
v_mfma_f32_16x16x32_bf16 v[78:81], a[68:71], v[130:133], v[78:81]
v_mfma_f32_16x16x32_bf16 v[78:81], a[84:87], v[134:137], v[78:81]
ds_read_b128 v[106:109], v8 offset:28672
v_mfma_f32_16x16x32_bf16 v[86:89], a[12:15], v[114:117], 0
v_mfma_f32_16x16x32_bf16 v[86:89], a[28:31], v[118:121], v[86:89]
v_mfma_f32_16x16x32_bf16 v[86:89], a[44:47], v[122:125], v[86:89]
ds_read_b128 v[110:113], v8 offset:29696
v_mfma_f32_16x16x32_bf16 v[86:89], a[60:63], v[126:129], v[86:89]
v_mfma_f32_16x16x32_bf16 v[86:89], a[76:79], v[130:133], v[86:89]
v_mfma_f32_16x16x32_bf16 v[86:89], a[92:95], v[134:137], v[86:89]
s_waitcnt lgkmcnt(5)
v_mfma_f32_16x16x32_bf16 v[74:77], a[96:99], v[90:93], v[74:77]
s_waitcnt lgkmcnt(4)
v_mfma_f32_16x16x32_bf16 v[74:77], a[112:115], v[94:97], v[74:77]
s_waitcnt lgkmcnt(3)
v_mfma_f32_16x16x32_bf16 v[74:77], a[128:131], v[98:101], v[74:77]
s_waitcnt lgkmcnt(2)
v_mfma_f32_16x16x32_bf16 v[74:77], a[144:147], v[102:105], v[74:77]
s_waitcnt lgkmcnt(1)
v_mfma_f32_16x16x32_bf16 v[74:77], a[160:163], v[106:109], v[74:77]
s_waitcnt lgkmcnt(0)
v_mfma_f32_16x16x32_bf16 v[74:77], a[176:179], v[110:113], v[74:77]
ds_read_b128 v[114:117], v8 offset:30720
v_mfma_f32_16x16x32_bf16 v[82:85], a[104:107], v[90:93], v[82:85]
v_mfma_f32_16x16x32_bf16 v[82:85], a[120:123], v[94:97], v[82:85]
v_mfma_f32_16x16x32_bf16 v[82:85], a[136:139], v[98:101], v[82:85]
ds_read_b128 v[118:121], v8 offset:31744
v_mfma_f32_16x16x32_bf16 v[82:85], a[152:155], v[102:105], v[82:85]
v_mfma_f32_16x16x32_bf16 v[82:85], a[168:171], v[106:109], v[82:85]
v_mfma_f32_16x16x32_bf16 v[82:85], a[184:187], v[110:113], v[82:85]
ds_read_b128 v[122:125], v8 offset:32768
v_mfma_f32_16x16x32_bf16 v[78:81], a[100:103], v[90:93], v[78:81]
v_mfma_f32_16x16x32_bf16 v[78:81], a[116:119], v[94:97], v[78:81]
v_mfma_f32_16x16x32_bf16 v[78:81], a[132:135], v[98:101], v[78:81]
ds_read_b128 v[126:129], v8 offset:33792
v_mfma_f32_16x16x32_bf16 v[78:81], a[148:151], v[102:105], v[78:81]
v_mfma_f32_16x16x32_bf16 v[78:81], a[164:167], v[106:109], v[78:81]
v_mfma_f32_16x16x32_bf16 v[78:81], a[180:183], v[110:113], v[78:81]
ds_read_b128 v[130:133], v8 offset:34816
v_mfma_f32_16x16x32_bf16 v[86:89], a[108:111], v[90:93], v[86:89]
v_mfma_f32_16x16x32_bf16 v[86:89], a[124:127], v[94:97], v[86:89]
v_mfma_f32_16x16x32_bf16 v[86:89], a[140:143], v[98:101], v[86:89]
ds_read_b128 v[134:137], v8 offset:35840
v_mfma_f32_16x16x32_bf16 v[86:89], a[156:159], v[102:105], v[86:89]
v_mfma_f32_16x16x32_bf16 v[86:89], a[172:175], v[106:109], v[86:89]
s_add_u32 m0, s43, 18432
v_mfma_f32_16x16x32_bf16 v[86:89], a[188:191], v[110:113], v[86:89]
s_waitcnt lgkmcnt(5)
v_mfma_f32_16x16x32_bf16 v[74:77], a[192:195], v[114:117], v[74:77]
s_barrier
s_waitcnt lgkmcnt(4)
v_mfma_f32_16x16x32_bf16 v[74:77], a[208:211], v[118:121], v[74:77]
s_waitcnt lgkmcnt(3)
global_load_lds_dwordx4 v2, s[38:39] offset:-2048 sc1
v_mfma_f32_16x16x32_bf16 v[74:77], a[224:227], v[122:125], v[74:77]
s_waitcnt lgkmcnt(2)
v_mfma_f32_16x16x32_bf16 v[74:77], a[240:243], v[126:129], v[74:77]
s_waitcnt lgkmcnt(1)
v_mfma_f32_16x16x32_bf16 v[74:77], v[26:29], v[130:133], v[74:77]
s_waitcnt lgkmcnt(0)
global_load_lds_dwordx4 v2, s[38:39] offset:-1024 sc1
v_mfma_f32_16x16x32_bf16 v[74:77], v[42:45], v[134:137], v[74:77]
v_mfma_f32_16x16x32_bf16 v[82:85], a[200:203], v[114:117], v[82:85]
v_mfma_f32_16x16x32_bf16 v[82:85], a[216:219], v[118:121], v[82:85]
global_load_lds_dwordx4 v2, s[38:39] offset:0 sc1
v_mfma_f32_16x16x32_bf16 v[82:85], a[232:235], v[122:125], v[82:85]
v_mfma_f32_16x16x32_bf16 v[82:85], a[248:251], v[126:129], v[82:85]
v_xor_b32 v8, 0x10000, v8
v_mfma_f32_16x16x32_bf16 v[82:85], v[34:37], v[130:133], v[82:85]
global_load_lds_dwordx4 v2, s[38:39] offset:1024 sc1
v_mfma_f32_16x16x32_bf16 v[82:85], v[50:53], v[134:137], v[82:85]
v_mfma_f32_16x16x32_bf16 v[78:81], a[196:199], v[114:117], v[78:81]
s_waitcnt vmcnt(5)
s_barrier
v_mfma_f32_16x16x32_bf16 v[78:81], a[212:215], v[118:121], v[78:81]
global_load_lds_dwordx4 v2, s[38:39] offset:2048 sc1
v_mfma_f32_16x16x32_bf16 v[78:81], a[228:231], v[122:125], v[78:81]
v_mfma_f32_16x16x32_bf16 v[78:81], a[244:247], v[126:129], v[78:81]
ds_read_b128 v[90:93], v8 offset:0
v_mfma_f32_16x16x32_bf16 v[78:81], v[30:33], v[130:133], v[78:81]
ds_read_b128 v[94:97], v8 offset:1024
global_load_lds_dwordx4 v2, s[38:39] offset:3072 sc1
v_mfma_f32_16x16x32_bf16 v[78:81], v[46:49], v[134:137], v[78:81]
ds_read_b128 v[98:101], v8 offset:2048
v_add_u32 v2, 18688, v2
v_mfma_f32_16x16x32_bf16 v[86:89], a[204:207], v[114:117], v[86:89]
ds_read_b128 v[102:105], v8 offset:3072
v_min_u32 v2, v2, v9
v_mfma_f32_16x16x32_bf16 v[86:89], a[220:223], v[118:121], v[86:89]
ds_read_b128 v[106:109], v8 offset:4096
v_mul_f32 v138, v74, v82
v_mfma_f32_16x16x32_bf16 v[86:89], a[236:239], v[122:125], v[86:89]
ds_read_b128 v[110:113], v8 offset:5120
v_mul_f32 v139, v75, v83
v_mfma_f32_16x16x32_bf16 v[86:89], a[252:255], v[126:129], v[86:89]
v_mul_f32 v140, v76, v84
v_mul_f32 v141, v77, v85
v_mfma_f32_16x16x32_bf16 v[86:89], v[38:41], v[130:133], v[86:89]
v_mfma_f32_16x16x32_bf16 v[86:89], v[54:57], v[134:137], v[86:89]
s_sub_u32 s70, s70, 1
s_cbranch_scc0 .Lgu_top5
v_mul_f32 v138, v74, v82
v_mul_f32 v139, v75, v83
v_mul_f32 v140, v76, v84
v_mul_f32 v141, v77, v85
v_cvt_pk_bf16_f32 v146, v138, v139
v_cvt_pk_bf16_f32 v147, v140, v141
v_mul_f32 v142, v78, v86
v_mul_f32 v143, v79, v87
v_mul_f32 v144, v80, v88
v_mul_f32 v145, v81, v89
v_cvt_pk_bf16_f32 v148, v142, v143
v_cvt_pk_bf16_f32 v149, v144, v145
global_store_dwordx4 v3, v[146:149], s[38:39] sc1
s_waitcnt vmcnt(1) lgkmcnt(0)
s_barrier
v_mov_b32 v219, 192
ds_read_b32 v219, v219 offset:36864
s_waitcnt vmcnt(0)
s_barrier
s_waitcnt lgkmcnt(0)
v_readfirstlane_b32 s60, v219
s_nop 3
s_max_i32 s60, s60, 2
s_min_i32 s60, s60, 66
s_add_u32 s60, s60, -2
)"
R"(s_branch .Lgu_fin
.Lgu_w2:
v_add_u32 v254, -1, v5
v_add_u32 v255, -1, v5
v_add_lshl_u32 v252, v7, 0, 7
v_bfi_b32 v252, s69, v252, v6
v_add_lshl_u32 v253, v5, 0, 7
v_bfi_b32 v253, s69, v253, v4
s_add_u32 m0, s43, 18432
s_mov_b32 s70, 32
s_waitcnt vmcnt(0) lgkmcnt(0)
s_nop 7
.p2align 6
.Lgu_top6:
s_waitcnt lgkmcnt(5)
v_mfma_f32_16x16x32_bf16 v[58:61], a[0:3], v[90:93], 0
s_waitcnt lgkmcnt(4)
v_mul_f32 v141, v77, v85
v_cvt_pk_bf16_f32 v146, v138, v139
v_mfma_f32_16x16x32_bf16 v[58:61], a[16:19], v[94:97], v[58:61]
s_waitcnt lgkmcnt(3)
v_cvt_pk_bf16_f32 v147, v140, v141
v_mfma_f32_16x16x32_bf16 v[58:61], a[32:35], v[98:101], v[58:61]
s_waitcnt lgkmcnt(2)
v_mfma_f32_16x16x32_bf16 v[58:61], a[48:51], v[102:105], v[58:61]
s_waitcnt lgkmcnt(1)
v_mul_f32 v142, v78, v86
v_mul_f32 v143, v79, v87
v_mfma_f32_16x16x32_bf16 v[58:61], a[64:67], v[106:109], v[58:61]
s_waitcnt lgkmcnt(0)
v_mul_f32 v144, v80, v88
v_mul_f32 v145, v81, v89
v_mfma_f32_16x16x32_bf16 v[58:61], a[80:83], v[110:113], v[58:61]
ds_read_b128 v[114:117], v8 offset:6144
v_cvt_pk_bf16_f32 v148, v142, v143
v_mfma_f32_16x16x32_bf16 v[66:69], a[8:11], v[90:93], 0
ds_read_b128 v[118:121], v8 offset:7168
v_cvt_pk_bf16_f32 v149, v144, v145
v_mfma_f32_16x16x32_bf16 v[66:69], a[24:27], v[94:97], v[66:69]
v_mfma_f32_16x16x32_bf16 v[66:69], a[40:43], v[98:101], v[66:69]
s_waitcnt vmcnt(6)
s_barrier
v_mfma_f32_16x16x32_bf16 v[66:69], a[56:59], v[102:105], v[66:69]
s_mov_b64 exec, 1
global_store_dword v252, v7, s[38:39] sc1
s_mov_b64 exec, -1
v_mfma_f32_16x16x32_bf16 v[66:69], a[72:75], v[106:109], v[66:69]
v_add_u32 v7, 1, v7
v_add_lshl_u32 v252, v7, 0, 7
v_mfma_f32_16x16x32_bf16 v[66:69], a[88:91], v[110:113], v[66:69]
ds_read_b128 v[122:125], v8 offset:8192
v_bfi_b32 v252, s69, v252, v6
v_mfma_f32_16x16x32_bf16 v[62:65], a[4:7], v[90:93], 0
global_store_dwordx4 v3, v[146:149], s[38:39] sc1
v_mfma_f32_16x16x32_bf16 v[62:65], a[20:23], v[94:97], v[62:65]
v_add_u32 v3, 147456, v3
v_mfma_f32_16x16x32_bf16 v[62:65], a[36:39], v[98:101], v[62:65]
ds_read_b128 v[126:129], v8 offset:9216
v_mfma_f32_16x16x32_bf16 v[62:65], a[52:55], v[102:105], v[62:65]
v_mfma_f32_16x16x32_bf16 v[62:65], a[68:71], v[106:109], v[62:65]
v_mfma_f32_16x16x32_bf16 v[62:65], a[84:87], v[110:113], v[62:65]
ds_read_b128 v[130:133], v8 offset:10240
v_mfma_f32_16x16x32_bf16 v[70:73], a[12:15], v[90:93], 0
v_mfma_f32_16x16x32_bf16 v[70:73], a[28:31], v[94:97], v[70:73]
v_mfma_f32_16x16x32_bf16 v[70:73], a[44:47], v[98:101], v[70:73]
ds_read_b128 v[134:137], v8 offset:11264
v_mfma_f32_16x16x32_bf16 v[70:73], a[60:63], v[102:105], v[70:73]
v_mfma_f32_16x16x32_bf16 v[70:73], a[76:79], v[106:109], v[70:73]
v_mfma_f32_16x16x32_bf16 v[70:73], a[92:95], v[110:113], v[70:73]
s_waitcnt lgkmcnt(5)
v_mfma_f32_16x16x32_bf16 v[58:61], a[96:99], v[114:117], v[58:61]
s_waitcnt lgkmcnt(4)
v_mfma_f32_16x16x32_bf16 v[58:61], a[112:115], v[118:121], v[58:61]
s_waitcnt lgkmcnt(3)
v_mfma_f32_16x16x32_bf16 v[58:61], a[128:131], v[122:125], v[58:61]
s_waitcnt lgkmcnt(2)
v_mfma_f32_16x16x32_bf16 v[58:61], a[144:147], v[126:129], v[58:61]
s_waitcnt lgkmcnt(1)
v_mfma_f32_16x16x32_bf16 v[58:61], a[160:163], v[130:133], v[58:61]
s_waitcnt lgkmcnt(0)
v_mfma_f32_16x16x32_bf16 v[58:61], a[176:179], v[134:137], v[58:61]
ds_read_b128 v[90:93], v8 offset:12288
v_mfma_f32_16x16x32_bf16 v[66:69], a[104:107], v[114:117], v[66:69]
v_mfma_f32_16x16x32_bf16 v[66:69], a[120:123], v[118:121], v[66:69]
v_mfma_f32_16x16x32_bf16 v[66:69], a[136:139], v[122:125], v[66:69]
ds_read_b128 v[94:97], v8 offset:13312
v_mfma_f32_16x16x32_bf16 v[66:69], a[152:155], v[126:129], v[66:69]
v_mfma_f32_16x16x32_bf16 v[66:69], a[168:171], v[130:133], v[66:69]
v_mfma_f32_16x16x32_bf16 v[66:69], a[184:187], v[134:137], v[66:69]
ds_read_b128 v[98:101], v8 offset:14336
v_mfma_f32_16x16x32_bf16 v[62:65], a[100:103], v[114:117], v[62:65]
v_mfma_f32_16x16x32_bf16 v[62:65], a[116:119], v[118:121], v[62:65]
v_mfma_f32_16x16x32_bf16 v[62:65], a[132:135], v[122:125], v[62:65]
ds_read_b128 v[102:105], v8 offset:15360
v_mfma_f32_16x16x32_bf16 v[62:65], a[148:151], v[126:129], v[62:65]
v_mfma_f32_16x16x32_bf16 v[62:65], a[164:167], v[130:133], v[62:65]
v_mfma_f32_16x16x32_bf16 v[62:65], a[180:183], v[134:137], v[62:65]
ds_read_b128 v[106:109], v8 offset:16384
v_mfma_f32_16x16x32_bf16 v[70:73], a[108:111], v[114:117], v[70:73]
v_mfma_f32_16x16x32_bf16 v[70:73], a[124:127], v[118:121], v[70:73]
v_mfma_f32_16x16x32_bf16 v[70:73], a[140:143], v[122:125], v[70:73]
ds_read_b128 v[110:113], v8 offset:17408
v_mfma_f32_16x16x32_bf16 v[70:73], a[156:159], v[126:129], v[70:73]
s_xor_b32 s43, s43, 0x10000
v_mfma_f32_16x16x32_bf16 v[70:73], a[172:175], v[130:133], v[70:73]
s_add_u32 m0, s43, 0
v_mfma_f32_16x16x32_bf16 v[70:73], a[188:191], v[134:137], v[70:73]
s_waitcnt lgkmcnt(5)
v_mfma_f32_16x16x32_bf16 v[58:61], a[192:195], v[90:93], v[58:61]
s_barrier
s_waitcnt lgkmcnt(4)
v_mfma_f32_16x16x32_bf16 v[58:61], a[208:211], v[94:97], v[58:61]
s_waitcnt lgkmcnt(3)
v_mfma_f32_16x16x32_bf16 v[58:61], a[224:227], v[98:101], v[58:61]
s_waitcnt lgkmcnt(2)
global_load_lds_dwordx4 v2, s[38:39] offset:-2048 sc1
v_mfma_f32_16x16x32_bf16 v[58:61], a[240:243], v[102:105], v[58:61]
s_waitcnt lgkmcnt(1)
v_mfma_f32_16x16x32_bf16 v[58:61], v[26:29], v[106:109], v[58:61]
s_waitcnt lgkmcnt(0)
v_mfma_f32_16x16x32_bf16 v[58:61], v[42:45], v[110:113], v[58:61]
global_load_lds_dwordx4 v2, s[38:39] offset:-1024 sc1
v_mfma_f32_16x16x32_bf16 v[66:69], a[200:203], v[90:93], v[66:69]
v_mfma_f32_16x16x32_bf16 v[66:69], a[216:219], v[94:97], v[66:69]
v_mfma_f32_16x16x32_bf16 v[66:69], a[232:235], v[98:101], v[66:69]
global_load_lds_dwordx4 v2, s[38:39] offset:0 sc1
v_mfma_f32_16x16x32_bf16 v[66:69], a[248:251], v[102:105], v[66:69]
v_mfma_f32_16x16x32_bf16 v[66:69], v[34:37], v[106:109], v[66:69]
v_mfma_f32_16x16x32_bf16 v[66:69], v[50:53], v[110:113], v[66:69]
v_mfma_f32_16x16x32_bf16 v[62:65], a[196:199], v[90:93], v[62:65]
s_waitcnt vmcnt(5)
s_barrier
v_mfma_f32_16x16x32_bf16 v[62:65], a[212:215], v[94:97], v[62:65]
global_load_lds_dwordx4 v2, s[38:39] offset:1024 sc1
v_mfma_f32_16x16x32_bf16 v[62:65], a[228:231], v[98:101], v[62:65]
v_mfma_f32_16x16x32_bf16 v[62:65], a[244:247], v[102:105], v[62:65]
ds_read_b128 v[114:117], v8 offset:18432
v_mfma_f32_16x16x32_bf16 v[62:65], v[30:33], v[106:109], v[62:65]
ds_read_b128 v[118:121], v8 offset:19456
global_load_lds_dwordx4 v2, s[38:39] offset:2048 sc1
v_mfma_f32_16x16x32_bf16 v[62:65], v[46:49], v[110:113], v[62:65]
ds_read_b128 v[122:125], v8 offset:20480
v_mul_f32 v138, v58, v66
v_mfma_f32_16x16x32_bf16 v[70:73], a[204:207], v[90:93], v[70:73]
ds_read_b128 v[126:129], v8 offset:21504
v_mul_f32 v139, v59, v67
v_mfma_f32_16x16x32_bf16 v[70:73], a[220:223], v[94:97], v[70:73]
ds_read_b128 v[130:133], v8 offset:22528
global_load_lds_dwordx4 v2, s[38:39] offset:3072 sc1
v_mfma_f32_16x16x32_bf16 v[70:73], a[236:239], v[98:101], v[70:73]
ds_read_b128 v[134:137], v8 offset:23552
v_add_u32 v2, 18688, v2
v_mfma_f32_16x16x32_bf16 v[70:73], a[252:255], v[102:105], v[70:73]
v_min_u32 v2, v2, v9
v_mul_f32 v140, v60, v68
v_mfma_f32_16x16x32_bf16 v[70:73], v[38:41], v[106:109], v[70:73]
v_mul_f32 v141, v61, v69
v_cvt_pk_bf16_f32 v146, v138, v139
v_mfma_f32_16x16x32_bf16 v[70:73], v[54:57], v[110:113], v[70:73]
s_waitcnt lgkmcnt(5)
v_cvt_pk_bf16_f32 v147, v140, v141
v_mfma_f32_16x16x32_bf16 v[74:77], a[0:3], v[114:117], 0
s_waitcnt lgkmcnt(4)
v_mfma_f32_16x16x32_bf16 v[74:77], a[16:19], v[118:121], v[74:77]
s_waitcnt lgkmcnt(3)
v_mfma_f32_16x16x32_bf16 v[74:77], a[32:35], v[122:125], v[74:77]
s_waitcnt lgkmcnt(2)
v_mfma_f32_16x16x32_bf16 v[74:77], a[48:51], v[126:129], v[74:77]
s_waitcnt lgkmcnt(1)
v_mul_f32 v142, v62, v70
v_mul_f32 v143, v63, v71
v_mfma_f32_16x16x32_bf16 v[74:77], a[64:67], v[130:133], v[74:77]
s_waitcnt lgkmcnt(0)
v_mul_f32 v144, v64, v72
v_mul_f32 v145, v65, v73
v_mfma_f32_16x16x32_bf16 v[74:77], a[80:83], v[134:137], v[74:77]
ds_read_b128 v[90:93], v8 offset:24576
v_cvt_pk_bf16_f32 v148, v142, v143
v_mfma_f32_16x16x32_bf16 v[82:85], a[8:11], v[114:117], 0
ds_read_b128 v[94:97], v8 offset:25600
v_cvt_pk_bf16_f32 v149, v144, v145
v_mfma_f32_16x16x32_bf16 v[82:85], a[24:27], v[118:121], v[82:85]
v_mfma_f32_16x16x32_bf16 v[82:85], a[40:43], v[122:125], v[82:85]
s_waitcnt vmcnt(6)
s_barrier
v_mfma_f32_16x16x32_bf16 v[82:85], a[56:59], v[126:129], v[82:85]
s_mov_b64 exec, 1
global_store_dword v252, v7, s[38:39] sc1
s_mov_b64 exec, -1
v_mfma_f32_16x16x32_bf16 v[82:85], a[72:75], v[130:133], v[82:85]
v_add_u32 v7, 1, v7
v_add_lshl_u32 v252, v7, 0, 7
v_mfma_f32_16x16x32_bf16 v[82:85], a[88:91], v[134:137], v[82:85]
ds_read_b128 v[98:101], v8 offset:26624
v_bfi_b32 v252, s69, v252, v6
v_mfma_f32_16x16x32_bf16 v[78:81], a[4:7], v[114:117], 0
global_store_dwordx4 v3, v[146:149], s[38:39] sc1
v_mfma_f32_16x16x32_bf16 v[78:81], a[20:23], v[118:121], v[78:81]
v_add_u32 v3, 147456, v3
v_mfma_f32_16x16x32_bf16 v[78:81], a[36:39], v[122:125], v[78:81]
ds_read_b128 v[102:105], v8 offset:27648
v_mfma_f32_16x16x32_bf16 v[78:81], a[52:55], v[126:129], v[78:81]
v_mfma_f32_16x16x32_bf16 v[78:81], a[68:71], v[130:133], v[78:81]
v_mfma_f32_16x16x32_bf16 v[78:81], a[84:87], v[134:137], v[78:81]
ds_read_b128 v[106:109], v8 offset:28672
v_mfma_f32_16x16x32_bf16 v[86:89], a[12:15], v[114:117], 0
v_mfma_f32_16x16x32_bf16 v[86:89], a[28:31], v[118:121], v[86:89]
v_mfma_f32_16x16x32_bf16 v[86:89], a[44:47], v[122:125], v[86:89]
ds_read_b128 v[110:113], v8 offset:29696
v_mfma_f32_16x16x32_bf16 v[86:89], a[60:63], v[126:129], v[86:89]
v_mfma_f32_16x16x32_bf16 v[86:89], a[76:79], v[130:133], v[86:89]
v_mfma_f32_16x16x32_bf16 v[86:89], a[92:95], v[134:137], v[86:89]
s_waitcnt lgkmcnt(5)
v_mfma_f32_16x16x32_bf16 v[74:77], a[96:99], v[90:93], v[74:77]
s_waitcnt lgkmcnt(4)
v_mfma_f32_16x16x32_bf16 v[74:77], a[112:115], v[94:97], v[74:77]
s_waitcnt lgkmcnt(3)
v_mfma_f32_16x16x32_bf16 v[74:77], a[128:131], v[98:101], v[74:77]
s_waitcnt lgkmcnt(2)
v_mfma_f32_16x16x32_bf16 v[74:77], a[144:147], v[102:105], v[74:77]
s_waitcnt lgkmcnt(1)
v_mfma_f32_16x16x32_bf16 v[74:77], a[160:163], v[106:109], v[74:77]
s_waitcnt lgkmcnt(0)
v_mfma_f32_16x16x32_bf16 v[74:77], a[176:179], v[110:113], v[74:77]
ds_read_b128 v[114:117], v8 offset:30720
v_mfma_f32_16x16x32_bf16 v[82:85], a[104:107], v[90:93], v[82:85]
v_mfma_f32_16x16x32_bf16 v[82:85], a[120:123], v[94:97], v[82:85]
v_mfma_f32_16x16x32_bf16 v[82:85], a[136:139], v[98:101], v[82:85]
ds_read_b128 v[118:121], v8 offset:31744
v_mfma_f32_16x16x32_bf16 v[82:85], a[152:155], v[102:105], v[82:85]
v_mfma_f32_16x16x32_bf16 v[82:85], a[168:171], v[106:109], v[82:85]
v_mfma_f32_16x16x32_bf16 v[82:85], a[184:187], v[110:113], v[82:85]
ds_read_b128 v[122:125], v8 offset:32768
v_mfma_f32_16x16x32_bf16 v[78:81], a[100:103], v[90:93], v[78:81]
v_mfma_f32_16x16x32_bf16 v[78:81], a[116:119], v[94:97], v[78:81]
v_mfma_f32_16x16x32_bf16 v[78:81], a[132:135], v[98:101], v[78:81]
ds_read_b128 v[126:129], v8 offset:33792
v_mfma_f32_16x16x32_bf16 v[78:81], a[148:151], v[102:105], v[78:81]
v_mfma_f32_16x16x32_bf16 v[78:81], a[164:167], v[106:109], v[78:81]
v_mfma_f32_16x16x32_bf16 v[78:81], a[180:183], v[110:113], v[78:81]
ds_read_b128 v[130:133], v8 offset:34816
v_mfma_f32_16x16x32_bf16 v[86:89], a[108:111], v[90:93], v[86:89]
v_mfma_f32_16x16x32_bf16 v[86:89], a[124:127], v[94:97], v[86:89]
v_mfma_f32_16x16x32_bf16 v[86:89], a[140:143], v[98:101], v[86:89]
ds_read_b128 v[134:137], v8 offset:35840
v_mfma_f32_16x16x32_bf16 v[86:89], a[156:159], v[102:105], v[86:89]
v_mfma_f32_16x16x32_bf16 v[86:89], a[172:175], v[106:109], v[86:89]
s_add_u32 m0, s43, 18432
v_mfma_f32_16x16x32_bf16 v[86:89], a[188:191], v[110:113], v[86:89]
s_waitcnt lgkmcnt(5)
v_mfma_f32_16x16x32_bf16 v[74:77], a[192:195], v[114:117], v[74:77]
s_barrier
s_waitcnt lgkmcnt(4)
v_mfma_f32_16x16x32_bf16 v[74:77], a[208:211], v[118:121], v[74:77]
s_waitcnt lgkmcnt(3)
v_mfma_f32_16x16x32_bf16 v[74:77], a[224:227], v[122:125], v[74:77]
s_waitcnt lgkmcnt(2)
global_load_lds_dwordx4 v2, s[38:39] offset:-2048 sc1
v_mfma_f32_16x16x32_bf16 v[74:77], a[240:243], v[126:129], v[74:77]
s_waitcnt lgkmcnt(1)
v_mfma_f32_16x16x32_bf16 v[74:77], v[26:29], v[130:133], v[74:77]
s_waitcnt lgkmcnt(0)
v_mfma_f32_16x16x32_bf16 v[74:77], v[42:45], v[134:137], v[74:77]
global_load_lds_dwordx4 v2, s[38:39] offset:-1024 sc1
v_mfma_f32_16x16x32_bf16 v[82:85], a[200:203], v[114:117], v[82:85]
v_mfma_f32_16x16x32_bf16 v[82:85], a[216:219], v[118:121], v[82:85]
v_mfma_f32_16x16x32_bf16 v[82:85], a[232:235], v[122:125], v[82:85]
global_load_lds_dwordx4 v2, s[38:39] offset:0 sc1
v_mfma_f32_16x16x32_bf16 v[82:85], a[248:251], v[126:129], v[82:85]
v_xor_b32 v8, 0x10000, v8
v_mfma_f32_16x16x32_bf16 v[82:85], v[34:37], v[130:133], v[82:85]
v_mfma_f32_16x16x32_bf16 v[82:85], v[50:53], v[134:137], v[82:85]
v_mfma_f32_16x16x32_bf16 v[78:81], a[196:199], v[114:117], v[78:81]
s_waitcnt vmcnt(5)
s_barrier
v_mfma_f32_16x16x32_bf16 v[78:81], a[212:215], v[118:121], v[78:81]
global_load_lds_dwordx4 v2, s[38:39] offset:1024 sc1
v_mfma_f32_16x16x32_bf16 v[78:81], a[228:231], v[122:125], v[78:81]
v_mfma_f32_16x16x32_bf16 v[78:81], a[244:247], v[126:129], v[78:81]
ds_read_b128 v[90:93], v8 offset:0
v_mfma_f32_16x16x32_bf16 v[78:81], v[30:33], v[130:133], v[78:81]
ds_read_b128 v[94:97], v8 offset:1024
global_load_lds_dwordx4 v2, s[38:39] offset:2048 sc1
v_mfma_f32_16x16x32_bf16 v[78:81], v[46:49], v[134:137], v[78:81]
ds_read_b128 v[98:101], v8 offset:2048
v_mul_f32 v138, v74, v82
v_mfma_f32_16x16x32_bf16 v[86:89], a[204:207], v[114:117], v[86:89]
ds_read_b128 v[102:105], v8 offset:3072
v_mul_f32 v139, v75, v83
v_mfma_f32_16x16x32_bf16 v[86:89], a[220:223], v[118:121], v[86:89]
ds_read_b128 v[106:109], v8 offset:4096
global_load_lds_dwordx4 v2, s[38:39] offset:3072 sc1
v_mfma_f32_16x16x32_bf16 v[86:89], a[236:239], v[122:125], v[86:89]
ds_read_b128 v[110:113], v8 offset:5120
v_add_u32 v2, 18688, v2
v_mfma_f32_16x16x32_bf16 v[86:89], a[252:255], v[126:129], v[86:89]
v_min_u32 v2, v2, v9
v_mul_f32 v140, v76, v84
v_mfma_f32_16x16x32_bf16 v[86:89], v[38:41], v[130:133], v[86:89]
v_mfma_f32_16x16x32_bf16 v[86:89], v[54:57], v[134:137], v[86:89]
s_sub_u32 s70, s70, 1
s_cbranch_scc0 .Lgu_top6
v_mul_f32 v138, v74, v82
v_mul_f32 v139, v75, v83
v_mul_f32 v140, v76, v84
v_mul_f32 v141, v77, v85
v_cvt_pk_bf16_f32 v146, v138, v139
v_cvt_pk_bf16_f32 v147, v140, v141
v_mul_f32 v142, v78, v86
v_mul_f32 v143, v79, v87
v_mul_f32 v144, v80, v88
v_mul_f32 v145, v81, v89
v_cvt_pk_bf16_f32 v148, v142, v143
v_cvt_pk_bf16_f32 v149, v144, v145
global_store_dwordx4 v3, v[146:149], s[38:39] sc1
s_waitcnt vmcnt(1) lgkmcnt(0)
s_barrier
s_mov_b64 exec, 1
global_store_dword v252, v7, s[38:39] sc1
s_mov_b64 exec, -1
v_mov_b32 v219, 192
ds_read_b32 v219, v219 offset:36864
s_waitcnt vmcnt(1)
s_barrier
v_add_u32 v7, 8, v7
v_mbcnt_lo_u32_b32 v252, -1, 0
v_mbcnt_hi_u32_b32 v252, -1, v252
v_add_lshl_u32 v252, v252, 0, 7
v_bfi_b32 v252, s69, v252, v6
s_mov_b64 exec, 0xff
global_store_dword v252, v7, s[38:39] sc1
s_mov_b64 exec, -1
s_waitcnt lgkmcnt(0)
v_readfirstlane_b32 s60, v219
s_nop 3
s_max_i32 s60, s60, 2
s_min_i32 s60, s60, 66
s_add_u32 s60, s60, -1
s_branch .Lgu_fin
.Lgu_w3:
v_add_u32 v254, -1, v5
v_add_u32 v255, -1, v5
v_add_lshl_u32 v252, v7, 0, 7
v_bfi_b32 v252, s69, v252, v6
v_add_lshl_u32 v253, v5, 0, 7
v_bfi_b32 v253, s69, v253, v4
s_add_u32 m0, s43, 18432
s_mov_b32 s70, 32
s_waitcnt vmcnt(0) lgkmcnt(0)
s_nop 7
.p2align 6
.Lgu_top11:
s_waitcnt lgkmcnt(5)
v_mfma_f32_16x16x32_bf16 v[58:61], a[0:3], v[90:93], 0
s_waitcnt lgkmcnt(4)
v_mul_f32 v141, v77, v85
v_cvt_pk_bf16_f32 v146, v138, v139
v_mfma_f32_16x16x32_bf16 v[58:61], a[16:19], v[94:97], v[58:61]
s_waitcnt lgkmcnt(3)
v_cvt_pk_bf16_f32 v147, v140, v141
v_mfma_f32_16x16x32_bf16 v[58:61], a[32:35], v[98:101], v[58:61]
s_waitcnt lgkmcnt(2)
global_load_dword v254, v253, s[38:39] sc1
v_mfma_f32_16x16x32_bf16 v[58:61], a[48:51], v[102:105], v[58:61]
s_waitcnt lgkmcnt(1)
v_mul_f32 v142, v78, v86
v_mul_f32 v143, v79, v87
v_mfma_f32_16x16x32_bf16 v[58:61], a[64:67], v[106:109], v[58:61]
s_waitcnt lgkmcnt(0)
v_mul_f32 v144, v80, v88
v_mul_f32 v145, v81, v89
v_mfma_f32_16x16x32_bf16 v[58:61], a[80:83], v[110:113], v[58:61]
ds_read_b128 v[114:117], v8 offset:6144
v_cvt_pk_bf16_f32 v148, v142, v143
v_mfma_f32_16x16x32_bf16 v[66:69], a[8:11], v[90:93], 0
ds_read_b128 v[118:121], v8 offset:7168
v_cvt_pk_bf16_f32 v149, v144, v145
v_mfma_f32_16x16x32_bf16 v[66:69], a[24:27], v[94:97], v[66:69]
v_mfma_f32_16x16x32_bf16 v[66:69], a[40:43], v[98:101], v[66:69]
s_waitcnt vmcnt(7)
s_barrier
v_mfma_f32_16x16x32_bf16 v[66:69], a[56:59], v[102:105], v[66:69]
v_mfma_f32_16x16x32_bf16 v[66:69], a[72:75], v[106:109], v[66:69]
v_mfma_f32_16x16x32_bf16 v[66:69], a[88:91], v[110:113], v[66:69]
ds_read_b128 v[122:125], v8 offset:8192
v_mfma_f32_16x16x32_bf16 v[62:65], a[4:7], v[90:93], 0
global_store_dwordx4 v3, v[146:149], s[38:39] sc1
v_mfma_f32_16x16x32_bf16 v[62:65], a[20:23], v[94:97], v[62:65]
v_add_u32 v3, 147456, v3
v_mfma_f32_16x16x32_bf16 v[62:65], a[36:39], v[98:101], v[62:65]
ds_read_b128 v[126:129], v8 offset:9216
v_mfma_f32_16x16x32_bf16 v[62:65], a[52:55], v[102:105], v[62:65]
v_mfma_f32_16x16x32_bf16 v[62:65], a[68:71], v[106:109], v[62:65]
v_mfma_f32_16x16x32_bf16 v[62:65], a[84:87], v[110:113], v[62:65]
ds_read_b128 v[130:133], v8 offset:10240
v_mfma_f32_16x16x32_bf16 v[70:73], a[12:15], v[90:93], 0
v_mfma_f32_16x16x32_bf16 v[70:73], a[28:31], v[94:97], v[70:73]
v_mfma_f32_16x16x32_bf16 v[70:73], a[44:47], v[98:101], v[70:73]
ds_read_b128 v[134:137], v8 offset:11264
v_mfma_f32_16x16x32_bf16 v[70:73], a[60:63], v[102:105], v[70:73]
)"
R"(v_mfma_f32_16x16x32_bf16 v[70:73], a[76:79], v[106:109], v[70:73]
v_mfma_f32_16x16x32_bf16 v[70:73], a[92:95], v[110:113], v[70:73]
s_waitcnt lgkmcnt(5)
v_mfma_f32_16x16x32_bf16 v[58:61], a[96:99], v[114:117], v[58:61]
s_waitcnt lgkmcnt(4)
v_mfma_f32_16x16x32_bf16 v[58:61], a[112:115], v[118:121], v[58:61]
s_waitcnt lgkmcnt(3)
v_mfma_f32_16x16x32_bf16 v[58:61], a[128:131], v[122:125], v[58:61]
s_waitcnt lgkmcnt(2)
v_mfma_f32_16x16x32_bf16 v[58:61], a[144:147], v[126:129], v[58:61]
s_waitcnt lgkmcnt(1)
v_mfma_f32_16x16x32_bf16 v[58:61], a[160:163], v[130:133], v[58:61]
s_waitcnt lgkmcnt(0)
v_mfma_f32_16x16x32_bf16 v[58:61], a[176:179], v[134:137], v[58:61]
ds_read_b128 v[90:93], v8 offset:12288
v_mfma_f32_16x16x32_bf16 v[66:69], a[104:107], v[114:117], v[66:69]
v_mfma_f32_16x16x32_bf16 v[66:69], a[120:123], v[118:121], v[66:69]
v_mfma_f32_16x16x32_bf16 v[66:69], a[136:139], v[122:125], v[66:69]
ds_read_b128 v[94:97], v8 offset:13312
v_mfma_f32_16x16x32_bf16 v[66:69], a[152:155], v[126:129], v[66:69]
v_mfma_f32_16x16x32_bf16 v[66:69], a[168:171], v[130:133], v[66:69]
v_mfma_f32_16x16x32_bf16 v[66:69], a[184:187], v[134:137], v[66:69]
ds_read_b128 v[98:101], v8 offset:14336
v_mfma_f32_16x16x32_bf16 v[62:65], a[100:103], v[114:117], v[62:65]
v_mfma_f32_16x16x32_bf16 v[62:65], a[116:119], v[118:121], v[62:65]
v_mfma_f32_16x16x32_bf16 v[62:65], a[132:135], v[122:125], v[62:65]
ds_read_b128 v[102:105], v8 offset:15360
v_mfma_f32_16x16x32_bf16 v[62:65], a[148:151], v[126:129], v[62:65]
v_mfma_f32_16x16x32_bf16 v[62:65], a[164:167], v[130:133], v[62:65]
v_mfma_f32_16x16x32_bf16 v[62:65], a[180:183], v[134:137], v[62:65]
ds_read_b128 v[106:109], v8 offset:16384
v_mfma_f32_16x16x32_bf16 v[70:73], a[108:111], v[114:117], v[70:73]
v_mfma_f32_16x16x32_bf16 v[70:73], a[124:127], v[118:121], v[70:73]
s_waitcnt vmcnt(1)
v_sub_u32 v254, v254, v5
v_cmp_lt_u32 vcc, 0x7f, v254
v_mfma_f32_16x16x32_bf16 v[70:73], a[140:143], v[122:125], v[70:73]
ds_read_b128 v[110:113], v8 offset:17408
v_mfma_f32_16x16x32_bf16 v[70:73], a[156:159], v[126:129], v[70:73]
s_xor_b32 s43, s43, 0x10000
v_mfma_f32_16x16x32_bf16 v[70:73], a[172:175], v[130:133], v[70:73]
s_cbranch_vccnz .Lgu_sp7
.Lgu_rt8:
s_add_u32 m0, s43, 0
v_mfma_f32_16x16x32_bf16 v[70:73], a[188:191], v[134:137], v[70:73]
s_waitcnt lgkmcnt(5)
v_mfma_f32_16x16x32_bf16 v[58:61], a[192:195], v[90:93], v[58:61]
s_barrier
s_waitcnt lgkmcnt(4)
v_mfma_f32_16x16x32_bf16 v[58:61], a[208:211], v[94:97], v[58:61]
s_waitcnt lgkmcnt(3)
v_add_u32 v5, 1, v5
v_add_lshl_u32 v253, v5, 0, 7
v_mfma_f32_16x16x32_bf16 v[58:61], a[224:227], v[98:101], v[58:61]
s_waitcnt lgkmcnt(2)
v_bfi_b32 v253, s69, v253, v4
v_mfma_f32_16x16x32_bf16 v[58:61], a[240:243], v[102:105], v[58:61]
s_waitcnt lgkmcnt(1)
global_load_lds_dwordx4 v2, s[38:39] offset:-2048 sc1
v_mfma_f32_16x16x32_bf16 v[58:61], v[26:29], v[106:109], v[58:61]
s_waitcnt lgkmcnt(0)
v_mfma_f32_16x16x32_bf16 v[58:61], v[42:45], v[110:113], v[58:61]
v_mfma_f32_16x16x32_bf16 v[66:69], a[200:203], v[90:93], v[66:69]
global_load_lds_dwordx4 v2, s[38:39] offset:-1024 sc1
v_mfma_f32_16x16x32_bf16 v[66:69], a[216:219], v[94:97], v[66:69]
v_mfma_f32_16x16x32_bf16 v[66:69], a[232:235], v[98:101], v[66:69]
v_mfma_f32_16x16x32_bf16 v[66:69], a[248:251], v[102:105], v[66:69]
global_load_lds_dwordx4 v2, s[38:39] offset:0 sc1
v_mfma_f32_16x16x32_bf16 v[66:69], v[34:37], v[106:109], v[66:69]
v_mfma_f32_16x16x32_bf16 v[66:69], v[50:53], v[110:113], v[66:69]
v_mfma_f32_16x16x32_bf16 v[62:65], a[196:199], v[90:93], v[62:65]
s_barrier
v_mfma_f32_16x16x32_bf16 v[62:65], a[212:215], v[94:97], v[62:65]
global_load_lds_dwordx4 v2, s[38:39] offset:1024 sc1
v_mfma_f32_16x16x32_bf16 v[62:65], a[228:231], v[98:101], v[62:65]
v_mfma_f32_16x16x32_bf16 v[62:65], a[244:247], v[102:105], v[62:65]
ds_read_b128 v[114:117], v8 offset:18432
v_mfma_f32_16x16x32_bf16 v[62:65], v[30:33], v[106:109], v[62:65]
ds_read_b128 v[118:121], v8 offset:19456
global_load_lds_dwordx4 v2, s[38:39] offset:2048 sc1
v_mfma_f32_16x16x32_bf16 v[62:65], v[46:49], v[110:113], v[62:65]
ds_read_b128 v[122:125], v8 offset:20480
v_mul_f32 v138, v58, v66
v_mfma_f32_16x16x32_bf16 v[70:73], a[204:207], v[90:93], v[70:73]
ds_read_b128 v[126:129], v8 offset:21504
v_mul_f32 v139, v59, v67
v_mfma_f32_16x16x32_bf16 v[70:73], a[220:223], v[94:97], v[70:73]
ds_read_b128 v[130:133], v8 offset:22528
global_load_lds_dwordx4 v2, s[38:39] offset:3072 sc1
v_mfma_f32_16x16x32_bf16 v[70:73], a[236:239], v[98:101], v[70:73]
ds_read_b128 v[134:137], v8 offset:23552
v_add_u32 v2, 18688, v2
v_mfma_f32_16x16x32_bf16 v[70:73], a[252:255], v[102:105], v[70:73]
v_min_u32 v2, v2, v9
v_mul_f32 v140, v60, v68
v_mfma_f32_16x16x32_bf16 v[70:73], v[38:41], v[106:109], v[70:73]
v_mul_f32 v141, v61, v69
v_cvt_pk_bf16_f32 v146, v138, v139
v_mfma_f32_16x16x32_bf16 v[70:73], v[54:57], v[110:113], v[70:73]
s_waitcnt lgkmcnt(5)
v_cvt_pk_bf16_f32 v147, v140, v141
v_mfma_f32_16x16x32_bf16 v[74:77], a[0:3], v[114:117], 0
s_waitcnt lgkmcnt(4)
v_mfma_f32_16x16x32_bf16 v[74:77], a[16:19], v[118:121], v[74:77]
s_waitcnt lgkmcnt(3)
v_mfma_f32_16x16x32_bf16 v[74:77], a[32:35], v[122:125], v[74:77]
s_waitcnt lgkmcnt(2)
global_load_dword v255, v253, s[38:39] sc1
v_mfma_f32_16x16x32_bf16 v[74:77], a[48:51], v[126:129], v[74:77]
s_waitcnt lgkmcnt(1)
v_mul_f32 v142, v62, v70
v_mul_f32 v143, v63, v71
v_mfma_f32_16x16x32_bf16 v[74:77], a[64:67], v[130:133], v[74:77]
s_waitcnt lgkmcnt(0)
v_mul_f32 v144, v64, v72
v_mul_f32 v145, v65, v73
v_mfma_f32_16x16x32_bf16 v[74:77], a[80:83], v[134:137], v[74:77]
ds_read_b128 v[90:93], v8 offset:24576
v_cvt_pk_bf16_f32 v148, v142, v143
v_mfma_f32_16x16x32_bf16 v[82:85], a[8:11], v[114:117], 0
ds_read_b128 v[94:97], v8 offset:25600
v_cvt_pk_bf16_f32 v149, v144, v145
v_mfma_f32_16x16x32_bf16 v[82:85], a[24:27], v[118:121], v[82:85]
v_mfma_f32_16x16x32_bf16 v[82:85], a[40:43], v[122:125], v[82:85]
s_waitcnt vmcnt(7)
s_barrier
v_mfma_f32_16x16x32_bf16 v[82:85], a[56:59], v[126:129], v[82:85]
v_mfma_f32_16x16x32_bf16 v[82:85], a[72:75], v[130:133], v[82:85]
v_mfma_f32_16x16x32_bf16 v[82:85], a[88:91], v[134:137], v[82:85]
ds_read_b128 v[98:101], v8 offset:26624
v_mfma_f32_16x16x32_bf16 v[78:81], a[4:7], v[114:117], 0
global_store_dwordx4 v3, v[146:149], s[38:39] sc1
v_mfma_f32_16x16x32_bf16 v[78:81], a[20:23], v[118:121], v[78:81]
v_add_u32 v3, 147456, v3
v_mfma_f32_16x16x32_bf16 v[78:81], a[36:39], v[122:125], v[78:81]
ds_read_b128 v[102:105], v8 offset:27648
v_mfma_f32_16x16x32_bf16 v[78:81], a[52:55], v[126:129], v[78:81]
v_mfma_f32_16x16x32_bf16 v[78:81], a[68:71], v[130:133], v[78:81]
v_mfma_f32_16x16x32_bf16 v[78:81], a[84:87], v[134:137], v[78:81]
ds_read_b128 v[106:109], v8 offset:28672
v_mfma_f32_16x16x32_bf16 v[86:89], a[12:15], v[114:117], 0
v_mfma_f32_16x16x32_bf16 v[86:89], a[28:31], v[118:121], v[86:89]
v_mfma_f32_16x16x32_bf16 v[86:89], a[44:47], v[122:125], v[86:89]
ds_read_b128 v[110:113], v8 offset:29696
v_mfma_f32_16x16x32_bf16 v[86:89], a[60:63], v[126:129], v[86:89]
v_mfma_f32_16x16x32_bf16 v[86:89], a[76:79], v[130:133], v[86:89]
v_mfma_f32_16x16x32_bf16 v[86:89], a[92:95], v[134:137], v[86:89]
s_waitcnt lgkmcnt(5)
v_mfma_f32_16x16x32_bf16 v[74:77], a[96:99], v[90:93], v[74:77]
s_waitcnt lgkmcnt(4)
v_mfma_f32_16x16x32_bf16 v[74:77], a[112:115], v[94:97], v[74:77]
s_waitcnt lgkmcnt(3)
v_mfma_f32_16x16x32_bf16 v[74:77], a[128:131], v[98:101], v[74:77]
s_waitcnt lgkmcnt(2)
v_mfma_f32_16x16x32_bf16 v[74:77], a[144:147], v[102:105], v[74:77]
s_waitcnt lgkmcnt(1)
v_mfma_f32_16x16x32_bf16 v[74:77], a[160:163], v[106:109], v[74:77]
s_waitcnt lgkmcnt(0)
v_mfma_f32_16x16x32_bf16 v[74:77], a[176:179], v[110:113], v[74:77]
ds_read_b128 v[114:117], v8 offset:30720
v_mfma_f32_16x16x32_bf16 v[82:85], a[104:107], v[90:93], v[82:85]
v_mfma_f32_16x16x32_bf16 v[82:85], a[120:123], v[94:97], v[82:85]
v_mfma_f32_16x16x32_bf16 v[82:85], a[136:139], v[98:101], v[82:85]
ds_read_b128 v[118:121], v8 offset:31744
v_mfma_f32_16x16x32_bf16 v[82:85], a[152:155], v[102:105], v[82:85]
v_mfma_f32_16x16x32_bf16 v[82:85], a[168:171], v[106:109], v[82:85]
v_mfma_f32_16x16x32_bf16 v[82:85], a[184:187], v[110:113], v[82:85]
ds_read_b128 v[122:125], v8 offset:32768
v_mfma_f32_16x16x32_bf16 v[78:81], a[100:103], v[90:93], v[78:81]
v_mfma_f32_16x16x32_bf16 v[78:81], a[116:119], v[94:97], v[78:81]
v_mfma_f32_16x16x32_bf16 v[78:81], a[132:135], v[98:101], v[78:81]
ds_read_b128 v[126:129], v8 offset:33792
v_mfma_f32_16x16x32_bf16 v[78:81], a[148:151], v[102:105], v[78:81]
v_mfma_f32_16x16x32_bf16 v[78:81], a[164:167], v[106:109], v[78:81]
v_mfma_f32_16x16x32_bf16 v[78:81], a[180:183], v[110:113], v[78:81]
ds_read_b128 v[130:133], v8 offset:34816
v_mfma_f32_16x16x32_bf16 v[86:89], a[108:111], v[90:93], v[86:89]
v_mfma_f32_16x16x32_bf16 v[86:89], a[124:127], v[94:97], v[86:89]
s_waitcnt vmcnt(1)
v_sub_u32 v255, v255, v5
v_cmp_lt_u32 vcc, 0x7f, v255
v_mfma_f32_16x16x32_bf16 v[86:89], a[140:143], v[98:101], v[86:89]
ds_read_b128 v[134:137], v8 offset:35840
v_mfma_f32_16x16x32_bf16 v[86:89], a[156:159], v[102:105], v[86:89]
v_mfma_f32_16x16x32_bf16 v[86:89], a[172:175], v[106:109], v[86:89]
s_cbranch_vccnz .Lgu_sp9
.Lgu_rt10:
s_add_u32 m0, s43, 18432
v_mfma_f32_16x16x32_bf16 v[86:89], a[188:191], v[110:113], v[86:89]
s_waitcnt lgkmcnt(5)
v_mfma_f32_16x16x32_bf16 v[74:77], a[192:195], v[114:117], v[74:77]
s_barrier
s_waitcnt lgkmcnt(4)
v_mfma_f32_16x16x32_bf16 v[74:77], a[208:211], v[118:121], v[74:77]
s_waitcnt lgkmcnt(3)
v_add_u32 v5, 1, v5
v_add_lshl_u32 v253, v5, 0, 7
v_mfma_f32_16x16x32_bf16 v[74:77], a[224:227], v[122:125], v[74:77]
s_waitcnt lgkmcnt(2)
v_bfi_b32 v253, s69, v253, v4
v_mfma_f32_16x16x32_bf16 v[74:77], a[240:243], v[126:129], v[74:77]
s_waitcnt lgkmcnt(1)
global_load_lds_dwordx4 v2, s[38:39] offset:-2048 sc1
v_mfma_f32_16x16x32_bf16 v[74:77], v[26:29], v[130:133], v[74:77]
s_waitcnt lgkmcnt(0)
v_mfma_f32_16x16x32_bf16 v[74:77], v[42:45], v[134:137], v[74:77]
v_mfma_f32_16x16x32_bf16 v[82:85], a[200:203], v[114:117], v[82:85]
global_load_lds_dwordx4 v2, s[38:39] offset:-1024 sc1
v_mfma_f32_16x16x32_bf16 v[82:85], a[216:219], v[118:121], v[82:85]
v_mfma_f32_16x16x32_bf16 v[82:85], a[232:235], v[122:125], v[82:85]
v_mfma_f32_16x16x32_bf16 v[82:85], a[248:251], v[126:129], v[82:85]
v_xor_b32 v8, 0x10000, v8
global_load_lds_dwordx4 v2, s[38:39] offset:0 sc1
v_mfma_f32_16x16x32_bf16 v[82:85], v[34:37], v[130:133], v[82:85]
v_mfma_f32_16x16x32_bf16 v[82:85], v[50:53], v[134:137], v[82:85]
v_mfma_f32_16x16x32_bf16 v[78:81], a[196:199], v[114:117], v[78:81]
s_barrier
v_mfma_f32_16x16x32_bf16 v[78:81], a[212:215], v[118:121], v[78:81]
global_load_lds_dwordx4 v2, s[38:39] offset:1024 sc1
v_mfma_f32_16x16x32_bf16 v[78:81], a[228:231], v[122:125], v[78:81]
v_mfma_f32_16x16x32_bf16 v[78:81], a[244:247], v[126:129], v[78:81]
ds_read_b128 v[90:93], v8 offset:0
v_mfma_f32_16x16x32_bf16 v[78:81], v[30:33], v[130:133], v[78:81]
ds_read_b128 v[94:97], v8 offset:1024
global_load_lds_dwordx4 v2, s[38:39] offset:2048 sc1
v_mfma_f32_16x16x32_bf16 v[78:81], v[46:49], v[134:137], v[78:81]
ds_read_b128 v[98:101], v8 offset:2048
v_mul_f32 v138, v74, v82
v_mfma_f32_16x16x32_bf16 v[86:89], a[204:207], v[114:117], v[86:89]
ds_read_b128 v[102:105], v8 offset:3072
v_mul_f32 v139, v75, v83
v_mfma_f32_16x16x32_bf16 v[86:89], a[220:223], v[118:121], v[86:89]
ds_read_b128 v[106:109], v8 offset:4096
global_load_lds_dwordx4 v2, s[38:39] offset:3072 sc1
v_mfma_f32_16x16x32_bf16 v[86:89], a[236:239], v[122:125], v[86:89]
ds_read_b128 v[110:113], v8 offset:5120
v_add_u32 v2, 18688, v2
v_mfma_f32_16x16x32_bf16 v[86:89], a[252:255], v[126:129], v[86:89]
v_min_u32 v2, v2, v9
v_mul_f32 v140, v76, v84
v_mfma_f32_16x16x32_bf16 v[86:89], v[38:41], v[130:133], v[86:89]
v_mfma_f32_16x16x32_bf16 v[86:89], v[54:57], v[134:137], v[86:89]
s_sub_u32 s70, s70, 1
s_cbranch_scc0 .Lgu_top11
v_mul_f32 v138, v74, v82
v_mul_f32 v139, v75, v83
v_mul_f32 v140, v76, v84
v_mul_f32 v141, v77, v85
v_cvt_pk_bf16_f32 v146, v138, v139
v_cvt_pk_bf16_f32 v147, v140, v141
v_mul_f32 v142, v78, v86
v_mul_f32 v143, v79, v87
v_mul_f32 v144, v80, v88
v_mul_f32 v145, v81, v89
v_cvt_pk_bf16_f32 v148, v142, v143
v_cvt_pk_bf16_f32 v149, v144, v145
global_store_dwordx4 v3, v[146:149], s[38:39] sc1
s_waitcnt vmcnt(1) lgkmcnt(0)
s_barrier
v_mov_b32 v219, 192
ds_read_b32 v219, v219 offset:36864
s_waitcnt vmcnt(0)
s_barrier
s_waitcnt lgkmcnt(0)
v_readfirstlane_b32 s60, v219
s_nop 3
s_max_i32 s60, s60, 2
s_min_i32 s60, s60, 66
s_add_u32 s60, s60, 0
s_branch .Lgu_fin
.Lgu_w4:
s_add_u32 s84, s52, 18688
s_addc_u32 s85, s53, 0
s_add_u32 s86, s50, 110592
s_addc_u32 s87, s51, 0
s_mov_b32 s80, 0
s_mov_b64 s[76:77], 0
s_mov_b32 s72, -14
v_add_u32 v218, -1, v17
v_add_u32 v214, -1, s37
v_add_u32 v215, -1, s37
v_add_u32 v216, -1, s37
v_add_u32 v217, -1, s37
v_add_lshl_u32 v252, v216, 0, 7
v_bfi_b32 v252, s69, v252, v18
v_add_lshl_u32 v253, v17, 0, 7
v_bfi_b32 v253, s69, v253, v16
v_add_u32 v254, -1, v5
v_add_u32 v255, -1, v5
s_add_u32 m0, s43, 18432
s_mov_b32 s70, 32
s_waitcnt vmcnt(0) lgkmcnt(0)
s_nop 7
.p2align 6
.Lgu_top16:
s_waitcnt lgkmcnt(5)
v_mfma_f32_16x16x32_bf16 v[58:61], a[0:3], v[90:93], 0
s_waitcnt lgkmcnt(4)
global_load_dword v218, v253, s[38:39] sc1
v_lshlrev_b32 v204, 16, v175
v_mfma_f32_16x16x32_bf16 v[58:61], a[16:19], v[94:97], v[58:61]
s_waitcnt lgkmcnt(3)
v_and_b32 v205, 0xffff0000, v175
v_add_f32_dpp v194, v198, v194 row_ror:8 row_mask:0xf bank_mask:0xf
v_mfma_f32_16x16x32_bf16 v[58:61], a[32:35], v[98:101], v[58:61]
s_waitcnt lgkmcnt(2)
v_add_f32_dpp v195, v199, v195 row_ror:8 row_mask:0xf bank_mask:0xf
v_add_f32_dpp v196, v200, v196 row_ror:8 row_mask:0xf bank_mask:0xf
v_mfma_f32_16x16x32_bf16 v[58:61], a[48:51], v[102:105], v[58:61]
s_waitcnt lgkmcnt(1)
v_mul_f32 v142, v78, v86
v_mul_f32 v143, v79, v87
v_mfma_f32_16x16x32_bf16 v[58:61], a[64:67], v[106:109], v[58:61]
s_waitcnt lgkmcnt(0)
v_mul_f32 v144, v80, v88
v_mul_f32 v145, v81, v89
v_mfma_f32_16x16x32_bf16 v[58:61], a[80:83], v[110:113], v[58:61]
ds_read_b128 v[114:117], v8 offset:6144
v_cvt_pk_bf16_f32 v148, v142, v143
v_mfma_f32_16x16x32_bf16 v[66:69], a[8:11], v[90:93], 0
ds_read_b128 v[118:121], v8 offset:7168
v_cvt_pk_bf16_f32 v149, v144, v145
v_mfma_f32_16x16x32_bf16 v[66:69], a[24:27], v[94:97], v[66:69]
v_mfma_f32_16x16x32_bf16 v[66:69], a[40:43], v[98:101], v[66:69]
s_waitcnt vmcnt(8)
s_barrier
v_mfma_f32_16x16x32_bf16 v[66:69], a[56:59], v[102:105], v[66:69]
v_add_f32_dpp v197, v201, v197 row_ror:8 row_mask:0xf bank_mask:0xf
v_fma_f32 v194, v194, v219, v202
v_mfma_f32_16x16x32_bf16 v[66:69], a[72:75], v[106:109], v[66:69]
v_fma_f32 v195, v195, v219, v203
v_fma_f32 v196, v196, v219, v204
v_mfma_f32_16x16x32_bf16 v[66:69], a[88:91], v[110:113], v[66:69]
ds_read_b128 v[122:125], v8 offset:8192
v_fma_f32 v197, v197, v219, v205
v_mfma_f32_16x16x32_bf16 v[62:65], a[4:7], v[90:93], 0
v_cvt_pk_bf16_f32 v210, v194, v195
v_cvt_pk_bf16_f32 v211, v196, v197
v_mfma_f32_16x16x32_bf16 v[62:65], a[20:23], v[94:97], v[62:65]
global_store_dwordx4 v3, v[146:149], s[38:39] sc1
v_mfma_f32_16x16x32_bf16 v[62:65], a[36:39], v[98:101], v[62:65]
ds_read_b128 v[126:129], v8 offset:9216
v_add_u32 v3, 147456, v3
v_mfma_f32_16x16x32_bf16 v[62:65], a[52:55], v[102:105], v[62:65]
ds_write_b64 v14, v[210:211]
v_mfma_f32_16x16x32_bf16 v[62:65], a[68:71], v[106:109], v[62:65]
s_waitcnt vmcnt(8)
ds_read_b64 v[212:213], v15
s_mov_b64 exec, 1
global_store_dword v252, v217, s[38:39] sc1
s_mov_b64 exec, -1
v_mfma_f32_16x16x32_bf16 v[62:65], a[84:87], v[110:113], v[62:65]
ds_read_b128 v[130:133], v8 offset:10240
v_mfma_f32_16x16x32_bf16 v[70:73], a[12:15], v[90:93], 0
v_mfma_f32_16x16x32_bf16 v[70:73], a[28:31], v[94:97], v[70:73]
v_mfma_f32_16x16x32_bf16 v[70:73], a[44:47], v[98:101], v[70:73]
ds_read_b128 v[134:137], v8 offset:11264
v_mfma_f32_16x16x32_bf16 v[70:73], a[60:63], v[102:105], v[70:73]
v_mfma_f32_16x16x32_bf16 v[70:73], a[76:79], v[106:109], v[70:73]
v_mfma_f32_16x16x32_bf16 v[70:73], a[92:95], v[110:113], v[70:73]
s_waitcnt lgkmcnt(2)
global_store_dwordx2 v13, v[212:213], s[56:57] sc1
v_mov_b32 v216, v214
v_mfma_f32_16x16x32_bf16 v[58:61], a[96:99], v[114:117], v[58:61]
v_mfma_f32_16x16x32_bf16 v[58:61], a[112:115], v[118:121], v[58:61]
v_mfma_f32_16x16x32_bf16 v[58:61], a[128:131], v[122:125], v[58:61]
v_mfma_f32_16x16x32_bf16 v[58:61], a[144:147], v[126:129], v[58:61]
s_waitcnt vmcnt(8)
v_add_f32 v188, v188, v190
s_waitcnt vmcnt(7)
v_lshlrev_b32 v194, 16, v176
v_add_f32 v189, v189, v191
s_waitcnt lgkmcnt(1)
v_mfma_f32_16x16x32_bf16 v[58:61], a[160:163], v[130:133], v[58:61]
s_waitcnt lgkmcnt(0)
v_and_b32 v195, 0xffff0000, v176
v_mul_f32 v188, 0x3ae38e39, v188
v_lshlrev_b32 v196, 16, v177
v_mfma_f32_16x16x32_bf16 v[58:61], a[176:179], v[134:137], v[58:61]
ds_read_b128 v[90:93], v8 offset:12288
v_mul_f32 v189, 0x3ae38e39, v189
v_and_b32 v197, 0xffff0000, v177
v_mfma_f32_16x16x32_bf16 v[66:69], a[104:107], v[114:117], v[66:69]
v_fma_f32 v220, -v188, v188, v189
v_lshlrev_b32 v198, 16, v178
v_max_f32 v220, 0, v220
v_mfma_f32_16x16x32_bf16 v[66:69], a[120:123], v[118:121], v[66:69]
v_and_b32 v199, 0xffff0000, v178
v_add_f32 v220, 0x3727c5ac, v220
v_mfma_f32_16x16x32_bf16 v[66:69], a[136:139], v[122:125], v[66:69]
ds_read_b128 v[94:97], v8 offset:13312
v_lshlrev_b32 v200, 16, v179
v_mfma_f32_16x16x32_bf16 v[66:69], a[152:155], v[126:129], v[66:69]
s_waitcnt vmcnt(3)
v_sub_u32 v218, v218, v17
v_cmp_lt_u32 vcc, 0x7f, v218
v_mfma_f32_16x16x32_bf16 v[66:69], a[168:171], v[130:133], v[66:69]
v_rcp_f32 v219, v220
v_mfma_f32_16x16x32_bf16 v[66:69], a[184:187], v[134:137], v[66:69]
ds_read_b128 v[98:101], v8 offset:14336
)"
R"(v_and_b32 v201, 0xffff0000, v179
v_mfma_f32_16x16x32_bf16 v[62:65], a[100:103], v[114:117], v[62:65]
s_and_b64 s[88:89], vcc, s[76:77]
s_cbranch_scc1 .Lgu_sp12
.Lgu_rt13:
v_mfma_f32_16x16x32_bf16 v[62:65], a[116:119], v[118:121], v[62:65]
s_cmp_eq_u64 vcc, 0
v_lshlrev_b32 v202, 16, v180
v_and_b32 v203, 0xffff0000, v180
v_mfma_f32_16x16x32_bf16 v[62:65], a[132:135], v[122:125], v[62:65]
s_cselect_b64 s[52:53], s[84:85], s[52:53]
ds_read_b128 v[102:105], v8 offset:15360
v_lshlrev_b32 v204, 16, v181
v_mfma_f32_16x16x32_bf16 v[62:65], a[148:151], v[126:129], v[62:65]
s_cselect_b64 s[50:51], s[86:87], s[50:51]
global_load_dwordx2 v[170:171], v12, s[52:53] offset:0 sc1
v_and_b32 v205, 0xffff0000, v181
v_mfma_f32_16x16x32_bf16 v[62:65], a[164:167], v[130:133], v[62:65]
s_cselect_b64 s[56:57], s[54:55], s[48:49]
global_load_dwordx2 v[172:173], v12, s[52:53] offset:128 sc1
v_lshlrev_b32 v206, 16, v182
v_mfma_f32_16x16x32_bf16 v[62:65], a[180:183], v[134:137], v[62:65]
s_cselect_b32 s82, 1, 0
ds_read_b128 v[106:109], v8 offset:16384
global_load_dwordx4 v[158:161], v10, s[50:51] offset:0 sc1
v_mfma_f32_16x16x32_bf16 v[70:73], a[108:111], v[114:117], v[70:73]
s_add_u32 s60, s60, s82
global_load_dwordx4 v[162:165], v10, s[50:51] offset:1024 sc1
v_and_b32 v207, 0xffff0000, v182
v_mfma_f32_16x16x32_bf16 v[70:73], a[124:127], v[118:121], v[70:73]
s_add_u32 s83, s60, s37
global_load_dwordx4 v[166:169], v10, s[50:51] offset:2048 sc1
v_add_u32 v17, s82, v17
v_add_u32 v214, -1, s83
v_mfma_f32_16x16x32_bf16 v[70:73], a[140:143], v[122:125], v[70:73]
s_cmp_lt_i32 s60, 32
ds_read_b128 v[110:113], v8 offset:17408
global_load_dwordx2 v[174:175], v11, s[52:53] sc1
v_add_lshl_u32 v253, v17, 0, 7
v_mfma_f32_16x16x32_bf16 v[70:73], a[156:159], v[126:129], v[70:73]
s_cselect_b64 s[54:55], s[44:45], s[46:47]
v_bfi_b32 v253, s69, v253, v16
v_lshlrev_b32 v208, 16, v183
v_mfma_f32_16x16x32_bf16 v[70:73], a[172:175], v[130:133], v[70:73]
s_mul_i32 s80, s60, 18432
v_and_b32 v209, 0xffff0000, v183
v_add_f32 v194, v194, v202
v_mfma_f32_16x16x32_bf16 v[70:73], a[188:191], v[134:137], v[70:73]
s_waitcnt lgkmcnt(5)
v_mfma_f32_16x16x32_bf16 v[58:61], a[192:195], v[90:93], v[58:61]
s_barrier
s_waitcnt lgkmcnt(4)
v_mfma_f32_16x16x32_bf16 v[58:61], a[208:211], v[94:97], v[58:61]
s_waitcnt lgkmcnt(3)
v_add_f32 v195, v195, v203
v_add_f32 v196, v196, v204
v_mfma_f32_16x16x32_bf16 v[58:61], a[224:227], v[98:101], v[58:61]
s_waitcnt lgkmcnt(2)
v_add_f32 v197, v197, v205
v_add_f32 v198, v198, v206
v_mfma_f32_16x16x32_bf16 v[58:61], a[240:243], v[102:105], v[58:61]
s_waitcnt lgkmcnt(1)
v_add_f32 v199, v199, v207
v_add_f32 v200, v200, v208
v_mfma_f32_16x16x32_bf16 v[58:61], v[26:29], v[106:109], v[58:61]
s_waitcnt lgkmcnt(0)
v_add_f32 v201, v201, v209
v_lshlrev_b32 v202, 16, v184
v_mfma_f32_16x16x32_bf16 v[58:61], v[42:45], v[110:113], v[58:61]
s_sub_u32 s54, s54, s80
v_and_b32 v203, 0xffff0000, v184
v_lshlrev_b32 v204, 16, v185
v_mfma_f32_16x16x32_bf16 v[66:69], a[200:203], v[90:93], v[66:69]
s_subb_u32 s55, s55, 0
v_and_b32 v205, 0xffff0000, v185
v_lshlrev_b32 v206, 16, v186
v_mfma_f32_16x16x32_bf16 v[66:69], a[216:219], v[94:97], v[66:69]
s_add_u32 s84, s52, 18688
v_and_b32 v207, 0xffff0000, v186
v_lshlrev_b32 v208, 16, v187
v_mfma_f32_16x16x32_bf16 v[66:69], a[232:235], v[98:101], v[66:69]
s_addc_u32 s85, s53, 0
v_and_b32 v209, 0xffff0000, v187
v_add_f32 v194, v194, v202
v_mfma_f32_16x16x32_bf16 v[66:69], a[248:251], v[102:105], v[66:69]
s_add_u32 s86, s50, 110592
v_add_f32 v195, v195, v203
v_add_f32 v196, v196, v204
v_mfma_f32_16x16x32_bf16 v[66:69], v[34:37], v[106:109], v[66:69]
s_addc_u32 s87, s51, 0
v_add_f32 v197, v197, v205
v_add_f32 v198, v198, v206
v_mfma_f32_16x16x32_bf16 v[66:69], v[50:53], v[110:113], v[66:69]
v_mfma_f32_16x16x32_bf16 v[62:65], a[196:199], v[90:93], v[62:65]
s_barrier
v_add_lshl_u32 v252, v216, 0, 7
v_mfma_f32_16x16x32_bf16 v[62:65], a[212:215], v[94:97], v[62:65]
s_add_u32 s72, s72, 1
v_bfi_b32 v252, s69, v252, v18
v_add_f32 v199, v199, v207
v_mfma_f32_16x16x32_bf16 v[62:65], a[228:231], v[98:101], v[62:65]
s_cmp_le_i32 s60, s72
v_add_f32 v200, v200, v208
v_add_f32 v201, v201, v209
v_mfma_f32_16x16x32_bf16 v[62:65], a[244:247], v[102:105], v[62:65]
s_cselect_b64 s[76:77], -1, 0
ds_read_b128 v[114:117], v8 offset:18432
v_lshlrev_b32 v202, 16, v192
v_mfma_f32_16x16x32_bf16 v[62:65], v[30:33], v[106:109], v[62:65]
ds_read_b128 v[118:121], v8 offset:19456
v_mul_f32 v138, v58, v66
v_mfma_f32_16x16x32_bf16 v[62:65], v[46:49], v[110:113], v[62:65]
ds_read_b128 v[122:125], v8 offset:20480
v_mul_f32 v139, v59, v67
v_mfma_f32_16x16x32_bf16 v[70:73], a[204:207], v[90:93], v[70:73]
ds_read_b128 v[126:129], v8 offset:21504
v_mul_f32 v140, v60, v68
v_mfma_f32_16x16x32_bf16 v[70:73], a[220:223], v[94:97], v[70:73]
ds_read_b128 v[130:133], v8 offset:22528
v_mul_f32 v141, v61, v69
v_mfma_f32_16x16x32_bf16 v[70:73], a[236:239], v[98:101], v[70:73]
ds_read_b128 v[134:137], v8 offset:23552
v_cvt_pk_bf16_f32 v146, v138, v139
v_mfma_f32_16x16x32_bf16 v[70:73], a[252:255], v[102:105], v[70:73]
v_cvt_pk_bf16_f32 v147, v140, v141
v_and_b32 v203, 0xffff0000, v192
v_mfma_f32_16x16x32_bf16 v[70:73], v[38:41], v[106:109], v[70:73]
v_lshlrev_b32 v204, 16, v193
v_and_b32 v205, 0xffff0000, v193
v_mfma_f32_16x16x32_bf16 v[70:73], v[54:57], v[110:113], v[70:73]
s_waitcnt lgkmcnt(5)
global_load_dword v218, v253, s[38:39] sc1
v_add_f32_dpp v194, v198, v194 row_ror:8 row_mask:0xf bank_mask:0xf
v_mfma_f32_16x16x32_bf16 v[74:77], a[0:3], v[114:117], 0
s_waitcnt lgkmcnt(4)
v_add_f32_dpp v195, v199, v195 row_ror:8 row_mask:0xf bank_mask:0xf
v_add_f32_dpp v196, v200, v196 row_ror:8 row_mask:0xf bank_mask:0xf
v_mfma_f32_16x16x32_bf16 v[74:77], a[16:19], v[118:121], v[74:77]
s_waitcnt lgkmcnt(3)
v_add_f32_dpp v197, v201, v197 row_ror:8 row_mask:0xf bank_mask:0xf
v_fma_f32 v194, v194, v219, v202
v_mfma_f32_16x16x32_bf16 v[74:77], a[32:35], v[122:125], v[74:77]
s_waitcnt lgkmcnt(2)
v_fma_f32 v195, v195, v219, v203
v_fma_f32 v196, v196, v219, v204
v_mfma_f32_16x16x32_bf16 v[74:77], a[48:51], v[126:129], v[74:77]
s_waitcnt lgkmcnt(1)
v_mul_f32 v142, v62, v70
v_mul_f32 v143, v63, v71
v_mfma_f32_16x16x32_bf16 v[74:77], a[64:67], v[130:133], v[74:77]
s_waitcnt lgkmcnt(0)
v_mul_f32 v144, v64, v72
v_mul_f32 v145, v65, v73
v_mfma_f32_16x16x32_bf16 v[74:77], a[80:83], v[134:137], v[74:77]
ds_read_b128 v[90:93], v8 offset:24576
v_cvt_pk_bf16_f32 v148, v142, v143
v_mfma_f32_16x16x32_bf16 v[82:85], a[8:11], v[114:117], 0
ds_read_b128 v[94:97], v8 offset:25600
v_cvt_pk_bf16_f32 v149, v144, v145
v_mfma_f32_16x16x32_bf16 v[82:85], a[24:27], v[118:121], v[82:85]
v_mfma_f32_16x16x32_bf16 v[82:85], a[40:43], v[122:125], v[82:85]
s_waitcnt vmcnt(9)
s_barrier
v_mfma_f32_16x16x32_bf16 v[82:85], a[56:59], v[126:129], v[82:85]
s_waitcnt vmcnt(7)
s_mov_b64 exec, 1
global_store_dword v252, v216, s[38:39] sc1
s_mov_b64 exec, -1
v_mfma_f32_16x16x32_bf16 v[82:85], a[72:75], v[130:133], v[82:85]
v_fma_f32 v197, v197, v219, v205
v_cvt_pk_bf16_f32 v210, v194, v195
v_mfma_f32_16x16x32_bf16 v[82:85], a[88:91], v[134:137], v[82:85]
ds_read_b128 v[98:101], v8 offset:26624
v_cvt_pk_bf16_f32 v211, v196, v197
v_mfma_f32_16x16x32_bf16 v[78:81], a[4:7], v[114:117], 0
ds_write_b64 v14, v[210:211]
v_mfma_f32_16x16x32_bf16 v[78:81], a[20:23], v[118:121], v[78:81]
global_store_dwordx4 v3, v[146:149], s[38:39] sc1
v_mfma_f32_16x16x32_bf16 v[78:81], a[36:39], v[122:125], v[78:81]
ds_read_b128 v[102:105], v8 offset:27648
v_add_u32 v3, 147456, v3
v_mfma_f32_16x16x32_bf16 v[78:81], a[52:55], v[126:129], v[78:81]
ds_read_b64 v[212:213], v15
v_mfma_f32_16x16x32_bf16 v[78:81], a[68:71], v[130:133], v[78:81]
v_mfma_f32_16x16x32_bf16 v[78:81], a[84:87], v[134:137], v[78:81]
ds_read_b128 v[106:109], v8 offset:28672
v_mfma_f32_16x16x32_bf16 v[86:89], a[12:15], v[114:117], 0
v_mfma_f32_16x16x32_bf16 v[86:89], a[28:31], v[118:121], v[86:89]
v_mfma_f32_16x16x32_bf16 v[86:89], a[44:47], v[122:125], v[86:89]
ds_read_b128 v[110:113], v8 offset:29696
v_mfma_f32_16x16x32_bf16 v[86:89], a[60:63], v[126:129], v[86:89]
v_mfma_f32_16x16x32_bf16 v[86:89], a[76:79], v[130:133], v[86:89]
s_waitcnt lgkmcnt(2)
global_store_dwordx2 v13, v[212:213], s[58:59] sc1
v_mov_b32 v217, v215
v_mfma_f32_16x16x32_bf16 v[86:89], a[92:95], v[134:137], v[86:89]
v_mfma_f32_16x16x32_bf16 v[74:77], a[96:99], v[90:93], v[74:77]
v_mfma_f32_16x16x32_bf16 v[74:77], a[112:115], v[94:97], v[74:77]
v_mfma_f32_16x16x32_bf16 v[74:77], a[128:131], v[98:101], v[74:77]
v_mfma_f32_16x16x32_bf16 v[74:77], a[144:147], v[102:105], v[74:77]
s_waitcnt vmcnt(8)
v_add_f32 v170, v170, v172
s_waitcnt vmcnt(7)
v_lshlrev_b32 v194, 16, v158
v_add_f32 v171, v171, v173
s_waitcnt lgkmcnt(1)
v_mfma_f32_16x16x32_bf16 v[74:77], a[160:163], v[106:109], v[74:77]
s_waitcnt lgkmcnt(0)
v_and_b32 v195, 0xffff0000, v158
v_mul_f32 v170, 0x3ae38e39, v170
v_lshlrev_b32 v196, 16, v159
v_mfma_f32_16x16x32_bf16 v[74:77], a[176:179], v[110:113], v[74:77]
ds_read_b128 v[114:117], v8 offset:30720
v_mul_f32 v171, 0x3ae38e39, v171
v_and_b32 v197, 0xffff0000, v159
v_mfma_f32_16x16x32_bf16 v[82:85], a[104:107], v[90:93], v[82:85]
v_fma_f32 v220, -v170, v170, v171
v_lshlrev_b32 v198, 16, v160
v_max_f32 v220, 0, v220
v_mfma_f32_16x16x32_bf16 v[82:85], a[120:123], v[94:97], v[82:85]
v_and_b32 v199, 0xffff0000, v160
v_add_f32 v220, 0x3727c5ac, v220
v_mfma_f32_16x16x32_bf16 v[82:85], a[136:139], v[98:101], v[82:85]
ds_read_b128 v[118:121], v8 offset:31744
v_lshlrev_b32 v200, 16, v161
v_mfma_f32_16x16x32_bf16 v[82:85], a[152:155], v[102:105], v[82:85]
s_waitcnt vmcnt(3)
v_sub_u32 v218, v218, v17
v_cmp_lt_u32 vcc, 0x7f, v218
v_mfma_f32_16x16x32_bf16 v[82:85], a[168:171], v[106:109], v[82:85]
v_rcp_f32 v219, v220
v_mfma_f32_16x16x32_bf16 v[82:85], a[184:187], v[110:113], v[82:85]
ds_read_b128 v[122:125], v8 offset:32768
v_and_b32 v201, 0xffff0000, v161
v_mfma_f32_16x16x32_bf16 v[78:81], a[100:103], v[90:93], v[78:81]
s_and_b64 s[88:89], vcc, s[76:77]
s_cbranch_scc1 .Lgu_sp14
.Lgu_rt15:
v_mfma_f32_16x16x32_bf16 v[78:81], a[116:119], v[94:97], v[78:81]
s_cmp_eq_u64 vcc, 0
v_lshlrev_b32 v202, 16, v162
v_and_b32 v203, 0xffff0000, v162
v_mfma_f32_16x16x32_bf16 v[78:81], a[132:135], v[98:101], v[78:81]
s_cselect_b64 s[52:53], s[84:85], s[52:53]
ds_read_b128 v[126:129], v8 offset:33792
v_lshlrev_b32 v204, 16, v163
v_mfma_f32_16x16x32_bf16 v[78:81], a[148:151], v[102:105], v[78:81]
s_cselect_b64 s[50:51], s[86:87], s[50:51]
global_load_dwordx2 v[188:189], v12, s[52:53] offset:0 sc1
v_and_b32 v205, 0xffff0000, v163
v_mfma_f32_16x16x32_bf16 v[78:81], a[164:167], v[106:109], v[78:81]
s_cselect_b64 s[58:59], s[54:55], s[48:49]
global_load_dwordx2 v[190:191], v12, s[52:53] offset:128 sc1
v_lshlrev_b32 v206, 16, v164
v_mfma_f32_16x16x32_bf16 v[78:81], a[180:183], v[110:113], v[78:81]
s_cselect_b32 s82, 1, 0
ds_read_b128 v[130:133], v8 offset:34816
global_load_dwordx4 v[176:179], v10, s[50:51] offset:0 sc1
v_mfma_f32_16x16x32_bf16 v[86:89], a[108:111], v[90:93], v[86:89]
s_add_u32 s60, s60, s82
global_load_dwordx4 v[180:183], v10, s[50:51] offset:1024 sc1
v_and_b32 v207, 0xffff0000, v164
v_mfma_f32_16x16x32_bf16 v[86:89], a[124:127], v[94:97], v[86:89]
s_add_u32 s83, s60, s37
global_load_dwordx4 v[184:187], v10, s[50:51] offset:2048 sc1
v_add_u32 v17, s82, v17
v_add_u32 v215, -1, s83
v_mfma_f32_16x16x32_bf16 v[86:89], a[140:143], v[98:101], v[86:89]
s_cmp_lt_i32 s60, 32
ds_read_b128 v[134:137], v8 offset:35840
global_load_dwordx2 v[192:193], v11, s[52:53] sc1
v_add_lshl_u32 v253, v17, 0, 7
v_mfma_f32_16x16x32_bf16 v[86:89], a[156:159], v[102:105], v[86:89]
s_cselect_b64 s[54:55], s[44:45], s[46:47]
v_bfi_b32 v253, s69, v253, v16
v_lshlrev_b32 v208, 16, v165
v_mfma_f32_16x16x32_bf16 v[86:89], a[172:175], v[106:109], v[86:89]
s_mul_i32 s80, s60, 18432
v_and_b32 v209, 0xffff0000, v165
v_add_f32 v194, v194, v202
v_mfma_f32_16x16x32_bf16 v[86:89], a[188:191], v[110:113], v[86:89]
s_waitcnt lgkmcnt(5)
v_mfma_f32_16x16x32_bf16 v[74:77], a[192:195], v[114:117], v[74:77]
s_barrier
s_waitcnt lgkmcnt(4)
v_mfma_f32_16x16x32_bf16 v[74:77], a[208:211], v[118:121], v[74:77]
s_waitcnt lgkmcnt(3)
v_add_f32 v195, v195, v203
v_add_f32 v196, v196, v204
v_mfma_f32_16x16x32_bf16 v[74:77], a[224:227], v[122:125], v[74:77]
s_waitcnt lgkmcnt(2)
v_add_f32 v197, v197, v205
v_add_f32 v198, v198, v206
v_mfma_f32_16x16x32_bf16 v[74:77], a[240:243], v[126:129], v[74:77]
s_waitcnt lgkmcnt(1)
v_add_f32 v199, v199, v207
v_add_f32 v200, v200, v208
v_mfma_f32_16x16x32_bf16 v[74:77], v[26:29], v[130:133], v[74:77]
s_waitcnt lgkmcnt(0)
v_add_f32 v201, v201, v209
v_lshlrev_b32 v202, 16, v166
v_mfma_f32_16x16x32_bf16 v[74:77], v[42:45], v[134:137], v[74:77]
s_sub_u32 s54, s54, s80
v_and_b32 v203, 0xffff0000, v166
v_lshlrev_b32 v204, 16, v167
v_mfma_f32_16x16x32_bf16 v[82:85], a[200:203], v[114:117], v[82:85]
s_subb_u32 s55, s55, 0
v_and_b32 v205, 0xffff0000, v167
v_lshlrev_b32 v206, 16, v168
v_mfma_f32_16x16x32_bf16 v[82:85], a[216:219], v[118:121], v[82:85]
s_add_u32 s84, s52, 18688
v_and_b32 v207, 0xffff0000, v168
v_lshlrev_b32 v208, 16, v169
v_mfma_f32_16x16x32_bf16 v[82:85], a[232:235], v[122:125], v[82:85]
s_addc_u32 s85, s53, 0
v_and_b32 v209, 0xffff0000, v169
v_add_f32 v194, v194, v202
v_mfma_f32_16x16x32_bf16 v[82:85], a[248:251], v[126:129], v[82:85]
s_add_u32 s86, s50, 110592
v_xor_b32 v8, 0x10000, v8
v_add_f32 v195, v195, v203
v_mfma_f32_16x16x32_bf16 v[82:85], v[34:37], v[130:133], v[82:85]
s_addc_u32 s87, s51, 0
v_add_f32 v196, v196, v204
v_add_f32 v197, v197, v205
v_mfma_f32_16x16x32_bf16 v[82:85], v[50:53], v[134:137], v[82:85]
v_mfma_f32_16x16x32_bf16 v[78:81], a[196:199], v[114:117], v[78:81]
s_barrier
v_add_lshl_u32 v252, v217, 0, 7
v_mfma_f32_16x16x32_bf16 v[78:81], a[212:215], v[118:121], v[78:81]
s_add_u32 s72, s72, 1
v_add_f32 v198, v198, v206
v_add_f32 v199, v199, v207
v_bfi_b32 v252, s69, v252, v18
v_mfma_f32_16x16x32_bf16 v[78:81], a[228:231], v[122:125], v[78:81]
s_cmp_le_i32 s60, s72
v_add_f32 v200, v200, v208
v_add_f32 v201, v201, v209
v_mfma_f32_16x16x32_bf16 v[78:81], a[244:247], v[126:129], v[78:81]
s_cselect_b64 s[76:77], -1, 0
ds_read_b128 v[90:93], v8 offset:0
v_lshlrev_b32 v202, 16, v174
v_mfma_f32_16x16x32_bf16 v[78:81], v[30:33], v[130:133], v[78:81]
ds_read_b128 v[94:97], v8 offset:1024
v_mul_f32 v138, v74, v82
v_mfma_f32_16x16x32_bf16 v[78:81], v[46:49], v[134:137], v[78:81]
ds_read_b128 v[98:101], v8 offset:2048
v_mul_f32 v139, v75, v83
v_mfma_f32_16x16x32_bf16 v[86:89], a[204:207], v[114:117], v[86:89]
ds_read_b128 v[102:105], v8 offset:3072
v_mul_f32 v140, v76, v84
v_mfma_f32_16x16x32_bf16 v[86:89], a[220:223], v[118:121], v[86:89]
ds_read_b128 v[106:109], v8 offset:4096
v_mul_f32 v141, v77, v85
v_mfma_f32_16x16x32_bf16 v[86:89], a[236:239], v[122:125], v[86:89]
ds_read_b128 v[110:113], v8 offset:5120
v_cvt_pk_bf16_f32 v146, v138, v139
v_mfma_f32_16x16x32_bf16 v[86:89], a[252:255], v[126:129], v[86:89]
v_cvt_pk_bf16_f32 v147, v140, v141
v_and_b32 v203, 0xffff0000, v174
v_mfma_f32_16x16x32_bf16 v[86:89], v[38:41], v[130:133], v[86:89]
v_mfma_f32_16x16x32_bf16 v[86:89], v[54:57], v[134:137], v[86:89]
s_sub_u32 s70, s70, 1
s_cbranch_scc0 .Lgu_top16
v_mul_f32 v138, v74, v82
v_mul_f32 v139, v75, v83
v_mul_f32 v140, v76, v84
v_mul_f32 v141, v77, v85
v_cvt_pk_bf16_f32 v146, v138, v139
v_cvt_pk_bf16_f32 v147, v140, v141
v_mul_f32 v142, v78, v86
v_mul_f32 v143, v79, v87
v_mul_f32 v144, v80, v88
v_mul_f32 v145, v81, v89
v_cvt_pk_bf16_f32 v148, v142, v143
v_cvt_pk_bf16_f32 v149, v144, v145
global_store_dwordx4 v3, v[146:149], s[38:39] sc1
v_mov_b32 v220, s60
ds_write_b32 v1, v220 offset:36864
v_subrev_u32 v17, s60, v17
s_waitcnt vmcnt(1) lgkmcnt(0)
s_barrier
v_mov_b32 v219, 192
ds_read_b32 v219, v219 offset:36864
s_waitcnt vmcnt(0)
s_barrier
s_waitcnt lgkmcnt(0)
v_readfirstlane_b32 s60, v219
s_nop 3
s_max_i32 s60, s60, 2
s_min_i32 s60, s60, 66
s_add_u32 s60, s60, 1
s_branch .Lgu_fin
.Lgu_fin:
s_cmp_gt_i32 s60, 63
s_cbranch_scc1 .Lgu_end
v_add_u32 v221, s60, v17
v_add_lshl_u32 v253, v221, 0, 7
v_bfi_b32 v253, s69, v253, v16
global_load_dword v218, v253, s[38:39] sc1
)"
R"(s_cmp_lt_i32 s60, 32
s_cselect_b64 s[54:55], s[44:45], s[46:47]
s_mul_i32 s80, s60, 18432
s_sub_u32 s54, s54, s80
s_subb_u32 s55, s55, 0
s_mov_b64 s[56:57], s[54:55]
s_mul_i32 s80, s60, 110592
s_mul_i32 s81, s60, 18688
s_mov_b64 s[50:51], s[38:39]
s_mov_b64 s[52:53], s[38:39]
s_add_u32 s50, s50, s80
s_addc_u32 s51, s51, 0
s_add_u32 s52, s52, s81
s_addc_u32 s53, s53, 0
s_waitcnt vmcnt(0)
v_sub_u32 v218, v218, v221
v_cmp_lt_u32 vcc, 0x7f, v218
s_cbranch_vccnz .Lgu_sp17
.Lgu_rt18:
global_load_dwordx2 v[170:171], v12, s[52:53] offset:0 sc1
global_load_dwordx2 v[172:173], v12, s[52:53] offset:128 sc1
global_load_dwordx4 v[158:161], v10, s[50:51] offset:0 sc1
global_load_dwordx4 v[162:165], v10, s[50:51] offset:1024 sc1
global_load_dwordx4 v[166:169], v10, s[50:51] offset:2048 sc1
global_load_dwordx2 v[174:175], v11, s[52:53] sc1
s_waitcnt vmcnt(4)
v_add_f32 v170, v170, v172
s_waitcnt vmcnt(3)
v_lshlrev_b32 v194, 16, v158
v_add_f32 v171, v171, v173
v_and_b32 v195, 0xffff0000, v158
v_mul_f32 v170, 0x3ae38e39, v170
v_lshlrev_b32 v196, 16, v159
v_mul_f32 v171, 0x3ae38e39, v171
v_and_b32 v197, 0xffff0000, v159
v_fma_f32 v220, -v170, v170, v171
v_lshlrev_b32 v198, 16, v160
v_max_f32 v220, 0, v220
v_and_b32 v199, 0xffff0000, v160
v_add_f32 v220, 0x3727c5ac, v220
v_lshlrev_b32 v200, 16, v161
v_rcp_f32 v219, v220
v_and_b32 v201, 0xffff0000, v161
s_waitcnt vmcnt(2)
v_lshlrev_b32 v202, 16, v162
v_and_b32 v203, 0xffff0000, v162
v_lshlrev_b32 v204, 16, v163
v_and_b32 v205, 0xffff0000, v163
v_lshlrev_b32 v206, 16, v164
v_and_b32 v207, 0xffff0000, v164
v_lshlrev_b32 v208, 16, v165
v_and_b32 v209, 0xffff0000, v165
v_add_f32 v194, v194, v202
v_add_f32 v195, v195, v203
v_add_f32 v196, v196, v204
v_add_f32 v197, v197, v205
v_add_f32 v198, v198, v206
v_add_f32 v199, v199, v207
v_add_f32 v200, v200, v208
v_add_f32 v201, v201, v209
s_waitcnt vmcnt(1)
v_lshlrev_b32 v202, 16, v166
v_and_b32 v203, 0xffff0000, v166
v_lshlrev_b32 v204, 16, v167
v_and_b32 v205, 0xffff0000, v167
v_lshlrev_b32 v206, 16, v168
v_and_b32 v207, 0xffff0000, v168
v_lshlrev_b32 v208, 16, v169
v_and_b32 v209, 0xffff0000, v169
v_add_f32 v194, v194, v202
v_add_f32 v195, v195, v203
v_add_f32 v196, v196, v204
v_add_f32 v197, v197, v205
v_add_f32 v198, v198, v206
v_add_f32 v199, v199, v207
v_add_f32 v200, v200, v208
v_add_f32 v201, v201, v209
s_waitcnt vmcnt(0)
v_lshlrev_b32 v202, 16, v174
v_and_b32 v203, 0xffff0000, v174
v_lshlrev_b32 v204, 16, v175
v_and_b32 v205, 0xffff0000, v175
v_add_f32_dpp v194, v198, v194 row_ror:8 row_mask:0xf bank_mask:0xf
v_add_f32_dpp v195, v199, v195 row_ror:8 row_mask:0xf bank_mask:0xf
v_add_f32_dpp v196, v200, v196 row_ror:8 row_mask:0xf bank_mask:0xf
v_add_f32_dpp v197, v201, v197 row_ror:8 row_mask:0xf bank_mask:0xf
v_fma_f32 v194, v194, v219, v202
v_fma_f32 v195, v195, v219, v203
v_fma_f32 v196, v196, v219, v204
v_fma_f32 v197, v197, v219, v205
v_cvt_pk_bf16_f32 v210, v194, v195
v_cvt_pk_bf16_f32 v211, v196, v197
ds_write_b64 v14, v[210:211]
ds_read_b64 v[212:213], v15
s_waitcnt lgkmcnt(0)
global_store_dwordx2 v13, v[212:213], s[56:57] sc1
s_add_u32 s60, s60, 4
s_branch .Lgu_fin
.Lgu_end:
s_endpgm
.Lgu_sp7:
s_sleep 1
v_add_lshl_u32 v253, v5, 0, 7
v_bfi_b32 v253, s69, v253, v4
global_load_dword v254, v253, s[38:39] sc1
s_add_u32 s71, s71, 1
s_waitcnt vmcnt(0)
v_sub_u32 v254, v254, v5
v_cmp_lt_u32 vcc, 0x7f, v254
s_cbranch_vccz .Lgu_rt8
s_cmp_lt_u32 s71, 3000000
s_cbranch_scc1 .Lgu_sp7
s_branch .Lgu_rt8
.Lgu_sp9:
s_sleep 1
v_add_lshl_u32 v253, v5, 0, 7
v_bfi_b32 v253, s69, v253, v4
global_load_dword v255, v253, s[38:39] sc1
s_add_u32 s71, s71, 1
s_waitcnt vmcnt(0)
v_sub_u32 v255, v255, v5
v_cmp_lt_u32 vcc, 0x7f, v255
s_cbranch_vccz .Lgu_rt10
s_cmp_lt_u32 s71, 3000000
s_cbranch_scc1 .Lgu_sp9
s_branch .Lgu_rt10
.Lgu_sp12:
s_sleep 1
v_add_lshl_u32 v253, v17, 0, 7
v_bfi_b32 v253, s69, v253, v16
global_load_dword v218, v253, s[38:39] sc1
s_add_u32 s71, s71, 1
s_waitcnt vmcnt(0)
v_sub_u32 v218, v218, v17
v_cmp_lt_u32 vcc, 0x7f, v218
s_and_b64 vcc, vcc, s[76:77]
s_cbranch_vccz .Lgu_rt13
s_cmp_lt_u32 s71, 3000000
s_cbranch_scc1 .Lgu_sp12
s_branch .Lgu_rt13
.Lgu_sp14:
s_sleep 1
v_add_lshl_u32 v253, v17, 0, 7
v_bfi_b32 v253, s69, v253, v16
global_load_dword v218, v253, s[38:39] sc1
s_add_u32 s71, s71, 1
s_waitcnt vmcnt(0)
v_sub_u32 v218, v218, v17
v_cmp_lt_u32 vcc, 0x7f, v218
s_and_b64 vcc, vcc, s[76:77]
s_cbranch_vccz .Lgu_rt15
s_cmp_lt_u32 s71, 3000000
s_cbranch_scc1 .Lgu_sp14
s_branch .Lgu_rt15
.Lgu_sp17:
s_sleep 1
v_add_lshl_u32 v253, v221, 0, 7
v_bfi_b32 v253, s69, v253, v16
global_load_dword v218, v253, s[38:39] sc1
s_add_u32 s71, s71, 1
s_waitcnt vmcnt(0)
v_sub_u32 v218, v218, v221
v_cmp_lt_u32 vcc, 0x7f, v218
s_cbranch_vccz .Lgu_rt18
s_cmp_lt_u32 s71, 3000000
s_cbranch_scc1 .Lgu_sp17
s_branch .Lgu_rt18
.Lkend:
)"
  :: "{v[0:31]}"(va), "{v[32:63]}"(vb), "{s[36:51]}"(sa), "{s[52:67]}"(sb) : "v255", "a255", "s99", "vcc", "memory");
}

extern "C" void kernel_launch(void* const* d_in, const int* in_sizes, int n_in,
                              void* d_out, int out_size, void* d_ws, size_t ws_size,
                              hipStream_t stream) {
  k_main<<<256, 256, 0, stream>>>((const char*)d_in[0], (const char*)d_in[1], (const char*)d_in[2], (const char*)d_in[4],
                                  (const char*)d_in[5], (const char*)d_in[6], (char*)d_ws, (char*)d_out);
}
```

```cpp
#include <hip/hip_runtime.h>
#include <hip/hip_bf16.h>
#include <stdint.h>
typedef __attribute__((ext_vector_type(32))) unsigned u32x32;
typedef __attribute__((ext_vector_type(16))) unsigned u32x16;
constexpr unsigned RP = 80u;
constexpr unsigned PAD = 12u;
constexpr unsigned XT = 18432u;
constexpr unsigned X1P = 18688u;
constexpr unsigned HT = 147456u;
constexpr unsigned PT = 110592u;
constexpr unsigned FL = 1024u;
constexpr unsigned FR = 102400u;
constexpr unsigned WS_FL = 16384u;
constexpr unsigned WS_ST = 425984u;
constexpr unsigned WS_O = 1048576u;
constexpr unsigned WS_X1 = 6946816u;
constexpr unsigned WS_X2 = 13000704u;
constexpr unsigned WS_XD = 15396864u;
constexpr unsigned WS_P = 17977344u;
constexpr unsigned WS_H = 53366784u;
constexpr unsigned WS_END = 100552704u;
constexpr unsigned FO = 0u;
constexpr unsigned FX1 = 8192u;
constexpr unsigned FX2 = 65536u;
constexpr unsigned FP = 10240u;
constexpr unsigned FH = 28672u;
__device__ __forceinline__ void touch(const void* p) {
  asm volatile("s_mov_b32 m0, 0x27f00\n\ts_nop 0\n\tglobal_load_lds_dword %0, off" :: "v"(p) : "memory");
}
__device__ __forceinline__ unsigned uni(unsigned v) { return __builtin_amdgcn_readfirstlane(v); }
#define TOUCH_CODE(K, B, N) do { unsigned long long pc; unsigned len; int rel;                   \
    asm volatile("s_getpc_b64 %0\n.Lpc" #K ":\n\ts_mov_b32 %1, .Le" #K "-(" B ")-4\n\ts_mov_b32 %2, (" B ")-.Lpc" #K : "=s"(pc), "=s"(len), "=s"(rel)); \
    touch((const char*)pc + rel + min(((N) * 64 + lane) * 128, len)); } while (0)
__global__ __launch_bounds__(256, 1) void k_main(const char* x0, const char* wqkv, const char* wo, const char* wg, const char* wu, const char* wd,
                                                 char* ws, char* out) {
  __shared__ __attribute__((aligned(1024))) char smem[163840];
  asm volatile("" :: "s"((unsigned)(size_t)(__attribute__((address_space(3))) const char*)smem) : "memory");
  unsigned lane = threadIdx.x & 63;
  const unsigned wave = uni(threadIdx.x >> 6), xc = blockIdx.x & 7, jj = blockIdx.x >> 3;
  if (wave == 3) { if (jj < 8) TOUCH_CODE(0, ".Lpc0", jj); else if (jj < 16) TOUCH_CODE(1, ".Lb1-2048", jj - 8); else if (jj < 24) TOUCH_CODE(2, ".Lb2-2048", jj - 16); else TOUCH_CODE(3, ".Lb3-2048", jj - 24); };
  if (jj >= 4 && wave < 2) { const unsigned u = min((jj - 4) * 124 + threadIdx.x, 3455u); touch(wqkv + (u >> 1) * 1152 + xc * 144 + (u & 1) * 140); }
  if (__builtin_expect(jj < 4, 1)) {
    const unsigned qa_hg = xc, qa_rho = jj, pq0 = wave < 2 ? wave * 5 : 2 + wave * 4;
    unsigned qx[5];
#pragma unroll
    for (unsigned j = 0; j < 5; ++j)
      qx[j] = ((jj * 32 + 31) * 16 + ((pq0 + j) & 1) * 8 + (lane >> 3)) * 1152 + ((pq0 + j) >> 1) * 128 + (((lane ^ (lane >> 3)) & 7) << 4) + 4096 - j * 1024;
    const unsigned long long xb = (unsigned long long)(x0 - 2048);
    asm volatile("s_add_u32 m0, %6, 0\n\ts_sub_u32 vcc_lo, %7, 0\n\ts_subb_u32 vcc_hi, %8, 0\n\tglobal_load_lds_dwordx4 %1, vcc offset:-2048 sc1\n\tglobal_load_lds_dwordx4 %2, vcc offset:-1024 sc1\n\tglobal_load_lds_dwordx4 %3, vcc offset:0 sc1\n\tglobal_load_lds_dwordx4 %4, vcc offset:1024 sc1\n\ts_cmp_gt_u32 %9, 1\n\ts_cbranch_scc1 .Lqx0\n\tglobal_load_lds_dwordx4 %5, vcc offset:2048 sc1\n.Lqx0:\n\ts_add_u32 m0, %6, 18432\n\ts_sub_u32 vcc_lo, %7, 18432\n\ts_subb_u32 vcc_hi, %8, 0\n\tglobal_load_lds_dwordx4 %1, vcc offset:-2048 sc1\n\tglobal_load_lds_dwordx4 %2, vcc offset:-1024 sc1\n\tglobal_load_lds_dwordx4 %3, vcc offset:0 sc1\n\tglobal_load_lds_dwordx4 %4, vcc offset:1024 sc1\n\ts_cmp_gt_u32 %9, 1\n\ts_cbranch_scc1 .Lqx1\n\tglobal_load_lds_dwordx4 %5, vcc offset:2048 sc1\n.Lqx1:\n\t" : "+v"(lane) : "v"(qx[0]), "v"(qx[1]), "v"(qx[2]), "v"(qx[3]), "v"(qx[4]), "s"(pq0 * 1024 + 2048),
                 "s"((unsigned)xb), "s"((unsigned)(xb >> 32)), "s"(wave) : "vcc", "scc", "memory");
    const unsigned r = lane & 15, g = lane >> 4, q = r >> 2, p = lane & 3;
    const unsigned hg = qa_hg, rho = qa_rho, row0 = rho * RP + PAD, pc0 = wave < 2 ? wave * 5 : 2 + wave * 4, hb = wave * 36 + (wave & 1) * 4;
    auto oa = [&](unsigned b) { return 43520 + (b >> 4) * 256 + r * 16 + (b & 15); };
    const unsigned m0 = 4 * g + (r & 1), m1 = m0 + 2, hm = (r & 1) ? 0xFFFF0000u : 0xFFFFu, ninf = 0xFF800000u;
    u32x32 va, vb; u32x16 sa, sb;
    va[0] = (((0 + lane) * 5462u) >> 16) * (1152 - 192) + lane * 16 + 4096;
    va[1] = (((64 + lane) * 5462u) >> 16) * (1152 - 192) + lane * 16 + 4096;
    va[2] = (((128 + lane) * 5462u) >> 16) * (1152 - 192) + lane * 16 + 4096;
    va[3] = (((192 + lane) * 5462u) >> 16) * (1152 - 192) + lane * 16 + 4096;
    va[4] = (((256 + lane) * 5462u) >> 16) * (1152 - 192) + lane * 16 + 4096;
    va[5] = (((320 + lane) * 5462u) >> 16) * (1152 - 192) + lane * 16 + 4096;
    va[6] = (((384 + lane) * 5462u) >> 16) * (1152 - 192) + lane * 16 + 4096;
    va[7] = (((448 + lane) * 5462u) >> 16) * (1152 - 192) + lane * 16 + 4096;
    va[8] = (((512 + lane) * 5462u) >> 16) * (1152 - 192) + lane * 16 + 12288;
    va[9] = (8 * g + q) * 576 + (hg & 3) * 16 + hb + p * 8;
    va[10] = (8 * g + q) * 576 + (hg & 3) * 16 + 32 + (p & 1) * 72 + (wave == 0 ? (p >> 1) * 192 : 384);
    va[11] = qx[0];
    va[12] = qx[1];
    va[13] = qx[2];
    va[14] = qx[3];
    va[15] = qx[4];
    va[16] = WS_O + (row0 - 1) * XT + hg * 2304 + wave * 1024 + (wave < 2 ? lane : r) * 16;
    va[17] = 43520 + wave * 1024 + (wave < 2 ? lane : r) * 16;
    va[18] = oa(hb + g * 8);
    va[19] = ((g & 1) == (wave >> 1)) ? oa(32 + (wave & 1) * 4 + (wave >> 1) * 72) : 46336 + lane * 4;
    va[20] = g == 0 ? 40960 + ((wave >> 1) * 16 + r) * 8 + (wave & 1) * 4 : 46080;
    va[21] = 41216 + ((wave >> 1) * 16 + r) * 8 + (wave & 1) * 4;
    va[22] = lane * 8;
    va[23] = lane * 16;
    va[24] = 43008 + r * 4;
    va[25] = 43008 + g * 16;
    va[26] = 41984 + (q * 16 + r) * 16 + p * 4;
    va[27] = 43136 + (((m0 > r) ? 0 : 1) * 16 + m0) * 4;
    va[28] = 43136 + (((m1 > r) ? 0 : 1) * 16 + m1) * 4;
    va[29] = 43136 + (((m0 > r) ? 1 : 0) * 16 + m0) * 4;
    va[30] = 43136 + (((m1 > r) ? 1 : 0) * 16 + m1) * 4;
    va[31] = 0u;
    vb[0] = (4 * g + 0 > r && !((r ^ 0) & 1)) ? 0u : ninf;
    vb[1] = (4 * g + 1 > r && !((r ^ 1) & 1)) ? 0u : ninf;
    vb[2] = (4 * g + 2 > r && !((r ^ 2) & 1)) ? 0u : ninf;
    vb[3] = (4 * g + 3 > r && !((r ^ 3) & 1)) ? 0u : ninf;
    vb[4] = (4 * g + 0 <= r && !((r ^ 0) & 1)) ? 0u : ninf;
    vb[5] = (4 * g + 1 <= r && !((r ^ 1) & 1)) ? 0u : ninf;
    vb[6] = (4 * g + 2 <= r && !((r ^ 2) & 1)) ? 0u : ninf;
    vb[7] = (4 * g + 3 <= r && !((r ^ 3) & 1)) ? 0u : ninf;
    vb[8] = m0 > r ? hm : 0u;
    vb[9] = m1 > r ? hm : 0u;
    vb[10] = m0 > r ? 0u : hm;
    vb[11] = m1 > r ? 0u : hm;
    vb[12] = WS_FL + rho * FR + FX2 + min(lane, 35u) * FL;
    vb[13] = WS_FL + rho * FR + FX2 + min(lane, 35u) * FL;
    vb[14] = 0u - 29;
    vb[15] = WS_FL + rho * FR + FO + hg * FL;
    vb[16] = 0u - 2;
    vb[17] = WS_FL + lane * FL;
    vb[18] = 0u;
    vb[19] = r * 128 + (((0 + g) ^ (r & 7)) << 4);
    vb[20] = r * 128 + (((4 + g) ^ (r & 7)) << 4);
    sa[0] = 0 + wave; sa[2] = (unsigned)(unsigned long long)ws; sa[3] = (unsigned)((unsigned long long)ws >> 32);
    sa[4] = uni((unsigned)(unsigned long long)(wqkv + ((hg * 144) & ~63u) + wave * (16 * 3456) + 0 * 64 * 3456));
    sa[5] = uni((unsigned)((unsigned long long)(wqkv + ((hg * 144) & ~63u) + wave * (16 * 3456) + 0 * 64 * 3456) >> 32));
    sa[6] = uni((unsigned)(unsigned long long)(x0 - 2048 - 2 * XT));
    sa[7] = uni((unsigned)((unsigned long long)(x0 - 2048 - 2 * XT) >> 32));
    sa[8] = uni((unsigned)(unsigned long long)((ws + WS_X2) - x0 + (rho ? 32 : 31) * (long long)XT));
    sa[9] = uni((unsigned)((unsigned long long)((ws + WS_X2) - x0 + (rho ? 32 : 31) * (long long)XT) >> 32));
    sa[10] = uni((unsigned)(unsigned long long)(-(long long)XT));
    sa[11] = uni((unsigned)((unsigned long long)(-(long long)XT) >> 32));
    sa[12] = uni(47104 + wave * 9216);
    sa[13] = uni(pc0 * 1024 + 2048 + 0x10000);
    sa[14] = uni(rho ? 0u : XT);
    sa[15] = uni(rho ? 0u - FR : 0u);
    sb[0] = uni(rho ? 0u : ninf);
    sb[1] = uni(hg * 4 + rho);
    asm volatile(
R"(.Lb0:
s_movk_i32 s69, 0x380
s_mov_b32 s71, 0
s_mov_b32 s74, 0
s_load_dword s37, s[38:39], 0x0
s_nop 1
s_and_b32 s75, s36, 3
s_cmp_eq_u32 s75, 1
s_cbranch_scc1 .Lqa_w2
s_cmp_eq_u32 s75, 2
s_cbranch_scc1 .Lqa_w3
s_cmp_eq_u32 s75, 3
s_cbranch_scc1 .Lqa_w4
.Lqa_w1:
s_mov_b32 s70, 500
s_branch .Lqa_hk6
.Lqa_bk15:
v_add_u32 v255, 0x10000, v9
v_add_u32 v162, 0x20000, v9
v_add_u32 v163, 0x10000, v10
v_add_u32 v164, 0x20000, v10
v_mov_b32 v106, 0x3F803F80
v_mov_b32 v107, 0x3F803F80
v_mov_b32 v108, 0x3F803F80
v_mov_b32 v109, 0x3F803F80
v_mov_b32 v141, 0
v_mov_b32 v145, 0
v_mov_b32 v149, 0
v_mov_b32 v153, 0
v_mov_b32 v165, 46080
s_waitcnt lgkmcnt(0)
s_mul_i32 s37, s37, 0x9E3779B1
s_add_u32 s37, s37, 0x7F4A7C15
v_add_u32 v46, s37, v46
v_add_u32 v48, s37, v48
v_add_u32 v50, s37, v50
ds_write_b32 v165, v141
s_mov_b64 s[76:77], 0
s_mov_b32 s73, 18
s_add_u32 m0, s48, 4096
s_nop 0
global_load_lds_dwordx4 v0, s[40:41] offset:-4096
global_load_lds_dwordx4 v1, s[40:41] offset:-3072
global_load_lds_dwordx4 v2, s[40:41] offset:-2048
global_load_lds_dwordx4 v3, s[40:41] offset:-1024
global_load_lds_dwordx4 v4, s[40:41] offset:0
global_load_lds_dwordx4 v5, s[40:41] offset:1024
global_load_lds_dwordx4 v6, s[40:41] offset:2048
global_load_lds_dwordx4 v7, s[40:41] offset:3072
s_add_u32 m0, s48, 12288
s_nop 0
global_load_lds_dwordx4 v8, s[40:41] offset:-4096
s_add_u32 s40, s40, 221184
s_addc_u32 s41, s41, 0
s_add_u32 m0, s48, 40960
s_nop 0
global_load_lds_dwordx4 v0, s[40:41] offset:-4096
global_load_lds_dwordx4 v1, s[40:41] offset:-3072
global_load_lds_dwordx4 v2, s[40:41] offset:-2048
global_load_lds_dwordx4 v3, s[40:41] offset:-1024
global_load_lds_dwordx4 v4, s[40:41] offset:0
global_load_lds_dwordx4 v5, s[40:41] offset:1024
global_load_lds_dwordx4 v6, s[40:41] offset:2048
global_load_lds_dwordx4 v7, s[40:41] offset:3072
s_add_u32 m0, s48, 49152
s_nop 0
global_load_lds_dwordx4 v8, s[40:41] offset:-4096
s_add_u32 s40, s40, 221184
s_addc_u32 s41, s41, 0
s_waitcnt vmcnt(9) lgkmcnt(0)
s_barrier
s_add_u32 m0, s48, 77824
ds_read_b64_tr_b16 a[0:1], v9 offset:47104
ds_read_b64_tr_b16 a[2:3], v9 offset:49408
global_load_lds_dwordx4 v0, s[40:41] offset:-4096
ds_read_b64_tr_b16 a[4:5], v9 offset:47296
ds_read_b64_tr_b16 a[6:7], v9 offset:49600
global_load_lds_dwordx4 v1, s[40:41] offset:-3072
ds_read_b64_tr_b16 a[8:9], v9 offset:47488
global_load_lds_dwordx4 v2, s[40:41] offset:-2048
ds_read_b64_tr_b16 a[10:11], v9 offset:49792
ds_read_b64_tr_b16 a[216:217], v10 offset:47104
global_load_lds_dwordx4 v3, s[40:41] offset:-1024
ds_read_b64_tr_b16 a[218:219], v10 offset:49408
global_load_lds_dwordx4 v4, s[40:41] offset:0
ds_read_b128 v[166:169], v51 offset:0
ds_read_b128 v[174:177], v51 offset:18432
global_load_lds_dwordx4 v5, s[40:41] offset:1024
ds_read_b64_tr_b16 a[12:13], v255 offset:0
global_load_lds_dwordx4 v6, s[40:41] offset:2048
ds_read_b64_tr_b16 a[14:15], v255 offset:2304
ds_read_b64_tr_b16 a[16:17], v255 offset:192
global_load_lds_dwordx4 v7, s[40:41] offset:3072
ds_read_b64_tr_b16 a[18:19], v255 offset:2496
s_add_u32 m0, s48, 86016
ds_read_b64_tr_b16 a[20:21], v255 offset:384
ds_read_b64_tr_b16 a[22:23], v255 offset:2688
global_load_lds_dwordx4 v8, s[40:41] offset:-4096
ds_read_b64_tr_b16 a[220:221], v163 offset:0
s_add_u32 s40, s40, 221184
ds_read_b64_tr_b16 a[222:223], v163 offset:2304
ds_read_b128 v[170:173], v52 offset:0
s_addc_u32 s41, s41, 0
ds_read_b128 v[178:181], v52 offset:18432
s_waitcnt vmcnt(9) lgkmcnt(0)
s_barrier
s_add_u32 m0, s48, 4096
ds_read_b64_tr_b16 a[24:25], v255 offset:18432
v_mfma_f32_16x16x32_bf16 v[86:89], a[0:3], v[106:109], 0
ds_read_b64_tr_b16 a[26:27], v255 offset:20736
v_mfma_f32_16x16x32_bf16 v[90:93], a[4:7], v[106:109], 0
global_load_lds_dwordx4 v0, s[40:41] offset:-4096
ds_read_b64_tr_b16 a[28:29], v255 offset:18624
v_mfma_f32_16x16x32_bf16 v[94:97], v[106:109], a[8:11], 0
ds_read_b64_tr_b16 a[30:31], v255 offset:20928
global_load_lds_dwordx4 v1, s[40:41] offset:-3072
v_mfma_f32_16x16x32_bf16 v[98:101], a[216:219], v[106:109], 0
ds_read_b64_tr_b16 a[32:33], v255 offset:18816
v_mfma_f32_16x16x32_bf16 v[110:113], a[0:3], v[166:169], 0
ds_read_b64_tr_b16 a[34:35], v255 offset:21120
global_load_lds_dwordx4 v2, s[40:41] offset:-2048
v_mfma_f32_16x16x32_bf16 v[114:117], a[4:7], v[166:169], 0
ds_read_b64_tr_b16 a[224:225], v163 offset:18432
v_mfma_f32_16x16x32_bf16 v[118:121], v[166:169], a[8:11], 0
global_load_lds_dwordx4 v3, s[40:41] offset:-1024
ds_read_b64_tr_b16 a[226:227], v163 offset:20736
v_mfma_f32_16x16x32_bf16 v[134:137], a[216:219], v[166:169], 0
ds_read_b128 v[182:185], v51 offset:2048
global_load_lds_dwordx4 v4, s[40:41] offset:0
v_mfma_f32_16x16x32_bf16 v[122:125], a[0:3], v[174:177], 0
ds_read_b128 v[190:193], v51 offset:20480
v_mfma_f32_16x16x32_bf16 v[126:129], a[4:7], v[174:177], 0
ds_read_b64_tr_b16 a[36:37], v255 offset:36864
global_load_lds_dwordx4 v5, s[40:41] offset:1024
v_mfma_f32_16x16x32_bf16 v[130:133], v[174:177], a[8:11], 0
ds_read_b64_tr_b16 a[38:39], v255 offset:39168
v_mfma_f32_16x16x32_bf16 v[158:161], a[216:219], v[174:177], 0
global_load_lds_dwordx4 v6, s[40:41] offset:2048
ds_read_b64_tr_b16 a[40:41], v255 offset:37056
v_mfma_f32_16x16x32_bf16 v[86:89], a[12:15], v[106:109], v[86:89]
ds_read_b64_tr_b16 a[42:43], v255 offset:39360
v_mfma_f32_16x16x32_bf16 v[90:93], a[16:19], v[106:109], v[90:93]
global_load_lds_dwordx4 v7, s[40:41] offset:3072
ds_read_b64_tr_b16 a[44:45], v255 offset:37248
v_mfma_f32_16x16x32_bf16 v[94:97], v[106:109], a[20:23], v[94:97]
s_add_u32 m0, s48, 12288
ds_read_b64_tr_b16 a[46:47], v255 offset:39552
v_mfma_f32_16x16x32_bf16 v[98:101], a[220:223], v[106:109], v[98:101]
ds_read_b64_tr_b16 a[228:229], v163 offset:36864
v_mfma_f32_16x16x32_bf16 v[110:113], a[12:15], v[170:173], v[110:113]
global_load_lds_dwordx4 v8, s[40:41] offset:-4096
ds_read_b64_tr_b16 a[230:231], v163 offset:39168
v_mfma_f32_16x16x32_bf16 v[114:117], a[16:19], v[170:173], v[114:117]
ds_read_b128 v[186:189], v52 offset:2048
v_mfma_f32_16x16x32_bf16 v[118:121], v[170:173], a[20:23], v[118:121]
s_add_u32 s40, s40, 221184
ds_read_b128 v[194:197], v52 offset:20480
v_mfma_f32_16x16x32_bf16 v[134:137], a[220:223], v[170:173], v[134:137]
v_mfma_f32_16x16x32_bf16 v[122:125], a[12:15], v[178:181], v[122:125]
s_addc_u32 s41, s41, 0
v_mfma_f32_16x16x32_bf16 v[126:129], a[16:19], v[178:181], v[126:129]
v_mfma_f32_16x16x32_bf16 v[130:133], v[178:181], a[20:23], v[130:133]
v_mfma_f32_16x16x32_bf16 v[158:161], a[220:223], v[178:181], v[158:161]
s_waitcnt vmcnt(9) lgkmcnt(0)
s_barrier
s_add_u32 m0, s48, 40960
ds_read_b64_tr_b16 a[48:49], v255 offset:55296
v_mfma_f32_16x16x32_bf16 v[86:89], a[24:27], v[106:109], v[86:89]
ds_read_b64_tr_b16 a[50:51], v255 offset:57600
v_mfma_f32_16x16x32_bf16 v[90:93], a[28:31], v[106:109], v[90:93]
global_load_lds_dwordx4 v0, s[40:41] offset:-4096
ds_read_b64_tr_b16 a[52:53], v255 offset:55488
v_mfma_f32_16x16x32_bf16 v[94:97], v[106:109], a[32:35], v[94:97]
ds_read_b64_tr_b16 a[54:55], v255 offset:57792
global_load_lds_dwordx4 v1, s[40:41] offset:-3072
v_mfma_f32_16x16x32_bf16 v[98:101], a[224:227], v[106:109], v[98:101]
ds_read_b64_tr_b16 a[56:57], v255 offset:55680
v_mfma_f32_16x16x32_bf16 v[110:113], a[24:27], v[182:185], v[110:113]
ds_read_b64_tr_b16 a[58:59], v255 offset:57984
global_load_lds_dwordx4 v2, s[40:41] offset:-2048
v_mfma_f32_16x16x32_bf16 v[114:117], a[28:31], v[182:185], v[114:117]
ds_read_b64_tr_b16 a[232:233], v163 offset:55296
v_mfma_f32_16x16x32_bf16 v[118:121], v[182:185], a[32:35], v[118:121]
global_load_lds_dwordx4 v3, s[40:41] offset:-1024
ds_read_b64_tr_b16 a[234:235], v163 offset:57600
v_mfma_f32_16x16x32_bf16 v[134:137], a[224:227], v[182:185], v[134:137]
ds_read_b128 v[166:169], v51 offset:4096
global_load_lds_dwordx4 v4, s[40:41] offset:0
v_mfma_f32_16x16x32_bf16 v[122:125], a[24:27], v[190:193], v[122:125]
ds_read_b128 v[174:177], v51 offset:22528
v_mfma_f32_16x16x32_bf16 v[126:129], a[28:31], v[190:193], v[126:129]
ds_read_b64_tr_b16 a[60:61], v162 offset:8192
global_load_lds_dwordx4 v5, s[40:41] offset:1024
v_mfma_f32_16x16x32_bf16 v[130:133], v[190:193], a[32:35], v[130:133]
ds_read_b64_tr_b16 a[62:63], v162 offset:10496
v_mfma_f32_16x16x32_bf16 v[158:161], a[224:227], v[190:193], v[158:161]
global_load_lds_dwordx4 v6, s[40:41] offset:2048
ds_read_b64_tr_b16 a[64:65], v162 offset:8384
v_mfma_f32_16x16x32_bf16 v[86:89], a[36:39], v[106:109], v[86:89]
ds_read_b64_tr_b16 a[66:67], v162 offset:10688
v_mfma_f32_16x16x32_bf16 v[90:93], a[40:43], v[106:109], v[90:93]
global_load_lds_dwordx4 v7, s[40:41] offset:3072
ds_read_b64_tr_b16 a[68:69], v162 offset:8576
v_mfma_f32_16x16x32_bf16 v[94:97], v[106:109], a[44:47], v[94:97]
s_add_u32 m0, s48, 49152
ds_read_b64_tr_b16 a[70:71], v162 offset:10880
v_mfma_f32_16x16x32_bf16 v[98:101], a[228:231], v[106:109], v[98:101]
ds_read_b64_tr_b16 a[236:237], v164 offset:8192
v_mfma_f32_16x16x32_bf16 v[110:113], a[36:39], v[186:189], v[110:113]
global_load_lds_dwordx4 v8, s[40:41] offset:-4096
ds_read_b64_tr_b16 a[238:239], v164 offset:10496
v_mfma_f32_16x16x32_bf16 v[114:117], a[40:43], v[186:189], v[114:117]
ds_read_b128 v[170:173], v52 offset:4096
v_mfma_f32_16x16x32_bf16 v[118:121], v[186:189], a[44:47], v[118:121]
s_add_u32 s40, s40, 221184
ds_read_b128 v[178:181], v52 offset:22528
v_mfma_f32_16x16x32_bf16 v[134:137], a[228:231], v[186:189], v[134:137]
v_mfma_f32_16x16x32_bf16 v[122:125], a[36:39], v[194:197], v[122:125]
s_addc_u32 s41, s41, 0
v_mfma_f32_16x16x32_bf16 v[126:129], a[40:43], v[194:197], v[126:129]
v_mfma_f32_16x16x32_bf16 v[130:133], v[194:197], a[44:47], v[130:133]
v_mfma_f32_16x16x32_bf16 v[158:161], a[228:231], v[194:197], v[158:161]
s_waitcnt vmcnt(9) lgkmcnt(0)
s_barrier
s_add_u32 m0, s48, 77824
ds_read_b64_tr_b16 a[72:73], v9 offset:47104
v_mfma_f32_16x16x32_bf16 v[86:89], a[48:51], v[106:109], v[86:89]
ds_read_b64_tr_b16 a[74:75], v9 offset:49408
v_mfma_f32_16x16x32_bf16 v[90:93], a[52:55], v[106:109], v[90:93]
global_load_lds_dwordx4 v0, s[40:41] offset:-4096
ds_read_b64_tr_b16 a[76:77], v9 offset:47296
v_mfma_f32_16x16x32_bf16 v[94:97], v[106:109], a[56:59], v[94:97]
ds_read_b64_tr_b16 a[78:79], v9 offset:49600
global_load_lds_dwordx4 v1, s[40:41] offset:-3072
v_mfma_f32_16x16x32_bf16 v[98:101], a[232:235], v[106:109], v[98:101]
ds_read_b64_tr_b16 a[80:81], v9 offset:47488
v_mfma_f32_16x16x32_bf16 v[110:113], a[48:51], v[166:169], v[110:113]
ds_read_b64_tr_b16 a[82:83], v9 offset:49792
global_load_lds_dwordx4 v2, s[40:41] offset:-2048
v_mfma_f32_16x16x32_bf16 v[114:117], a[52:55], v[166:169], v[114:117]
ds_read_b64_tr_b16 a[240:241], v10 offset:47104
v_mfma_f32_16x16x32_bf16 v[118:121], v[166:169], a[56:59], v[118:121]
global_load_lds_dwordx4 v3, s[40:41] offset:-1024
ds_read_b64_tr_b16 a[242:243], v10 offset:49408
v_mfma_f32_16x16x32_bf16 v[134:137], a[232:235], v[166:169], v[134:137]
ds_read_b128 v[182:185], v51 offset:6144
global_load_lds_dwordx4 v4, s[40:41] offset:0
v_mfma_f32_16x16x32_bf16 v[122:125], a[48:51], v[174:177], v[122:125]
ds_read_b128 v[190:193], v51 offset:24576
v_mfma_f32_16x16x32_bf16 v[126:129], a[52:55], v[174:177], v[126:129]
ds_read_b64_tr_b16 a[84:85], v255 offset:0
global_load_lds_dwordx4 v5, s[40:41] offset:1024
v_mfma_f32_16x16x32_bf16 v[130:133], v[174:177], a[56:59], v[130:133]
ds_read_b64_tr_b16 a[86:87], v255 offset:2304
v_mfma_f32_16x16x32_bf16 v[158:161], a[232:235], v[174:177], v[158:161]
global_load_lds_dwordx4 v6, s[40:41] offset:2048
ds_read_b64_tr_b16 a[88:89], v255 offset:192
v_mfma_f32_16x16x32_bf16 v[86:89], a[60:63], v[106:109], v[86:89]
ds_read_b64_tr_b16 a[90:91], v255 offset:2496
v_mfma_f32_16x16x32_bf16 v[90:93], a[64:67], v[106:109], v[90:93]
global_load_lds_dwordx4 v7, s[40:41] offset:3072
ds_read_b64_tr_b16 a[92:93], v255 offset:384
v_mfma_f32_16x16x32_bf16 v[94:97], v[106:109], a[68:71], v[94:97]
s_add_u32 m0, s48, 86016
ds_read_b64_tr_b16 a[94:95], v255 offset:2688
v_mfma_f32_16x16x32_bf16 v[98:101], a[236:239], v[106:109], v[98:101]
ds_read_b64_tr_b16 a[244:245], v163 offset:0
v_mfma_f32_16x16x32_bf16 v[110:113], a[60:63], v[170:173], v[110:113]
global_load_lds_dwordx4 v8, s[40:41] offset:-4096
ds_read_b64_tr_b16 a[246:247], v163 offset:2304
v_mfma_f32_16x16x32_bf16 v[114:117], a[64:67], v[170:173], v[114:117]
ds_read_b128 v[186:189], v52 offset:6144
v_mfma_f32_16x16x32_bf16 v[118:121], v[170:173], a[68:71], v[118:121]
s_add_u32 s40, s40, 221184
ds_read_b128 v[194:197], v52 offset:24576
v_mfma_f32_16x16x32_bf16 v[134:137], a[236:239], v[170:173], v[134:137]
v_mfma_f32_16x16x32_bf16 v[122:125], a[60:63], v[178:181], v[122:125]
s_addc_u32 s41, s41, 0
v_mfma_f32_16x16x32_bf16 v[126:129], a[64:67], v[178:181], v[126:129]
v_mfma_f32_16x16x32_bf16 v[130:133], v[178:181], a[68:71], v[130:133]
v_mfma_f32_16x16x32_bf16 v[158:161], a[236:239], v[178:181], v[158:161]
s_waitcnt vmcnt(9) lgkmcnt(0)
s_barrier
s_add_u32 m0, s48, 4096
ds_read_b64_tr_b16 a[96:97], v255 offset:18432
v_mfma_f32_16x16x32_bf16 v[86:89], a[72:75], v[106:109], v[86:89]
ds_read_b64_tr_b16 a[98:99], v255 offset:20736
v_mfma_f32_16x16x32_bf16 v[90:93], a[76:79], v[106:109], v[90:93]
global_load_lds_dwordx4 v0, s[40:41] offset:-4096
ds_read_b64_tr_b16 a[100:101], v255 offset:18624
v_mfma_f32_16x16x32_bf16 v[94:97], v[106:109], a[80:83], v[94:97]
ds_read_b64_tr_b16 a[102:103], v255 offset:20928
global_load_lds_dwordx4 v1, s[40:41] offset:-3072
v_mfma_f32_16x16x32_bf16 v[98:101], a[240:243], v[106:109], v[98:101]
ds_read_b64_tr_b16 a[104:105], v255 offset:18816
v_mfma_f32_16x16x32_bf16 v[110:113], a[72:75], v[182:185], v[110:113]
ds_read_b64_tr_b16 a[106:107], v255 offset:21120
global_load_lds_dwordx4 v2, s[40:41] offset:-2048
v_mfma_f32_16x16x32_bf16 v[114:117], a[76:79], v[182:185], v[114:117]
ds_read_b64_tr_b16 a[248:249], v163 offset:18432
v_mfma_f32_16x16x32_bf16 v[118:121], v[182:185], a[80:83], v[118:121]
global_load_lds_dwordx4 v3, s[40:41] offset:-1024
ds_read_b64_tr_b16 a[250:251], v163 offset:20736
v_mfma_f32_16x16x32_bf16 v[134:137], a[240:243], v[182:185], v[134:137]
ds_read_b128 v[166:169], v51 offset:8192
global_load_lds_dwordx4 v4, s[40:41] offset:0
v_mfma_f32_16x16x32_bf16 v[122:125], a[72:75], v[190:193], v[122:125]
ds_read_b128 v[174:177], v51 offset:26624
v_mfma_f32_16x16x32_bf16 v[126:129], a[76:79], v[190:193], v[126:129]
ds_read_b64_tr_b16 a[108:109], v255 offset:36864
global_load_lds_dwordx4 v5, s[40:41] offset:1024
v_mfma_f32_16x16x32_bf16 v[130:133], v[190:193], a[80:83], v[130:133]
ds_read_b64_tr_b16 a[110:111], v255 offset:39168
v_mfma_f32_16x16x32_bf16 v[158:161], a[240:243], v[190:193], v[158:161]
global_load_lds_dwordx4 v6, s[40:41] offset:2048
ds_read_b64_tr_b16 a[112:113], v255 offset:37056
v_mfma_f32_16x16x32_bf16 v[86:89], a[84:87], v[106:109], v[86:89]
ds_read_b64_tr_b16 a[114:115], v255 offset:39360
v_mfma_f32_16x16x32_bf16 v[90:93], a[88:91], v[106:109], v[90:93]
global_load_lds_dwordx4 v7, s[40:41] offset:3072
ds_read_b64_tr_b16 a[116:117], v255 offset:37248
v_mfma_f32_16x16x32_bf16 v[94:97], v[106:109], a[92:95], v[94:97]
s_add_u32 m0, s48, 12288
ds_read_b64_tr_b16 a[118:119], v255 offset:39552
v_mfma_f32_16x16x32_bf16 v[98:101], a[244:247], v[106:109], v[98:101]
ds_read_b64_tr_b16 a[252:253], v163 offset:36864
v_mfma_f32_16x16x32_bf16 v[110:113], a[84:87], v[186:189], v[110:113]
global_load_lds_dwordx4 v8, s[40:41] offset:-4096
ds_read_b64_tr_b16 a[254:255], v163 offset:39168
v_mfma_f32_16x16x32_bf16 v[114:117], a[88:91], v[186:189], v[114:117]
ds_read_b128 v[170:173], v52 offset:8192
v_mfma_f32_16x16x32_bf16 v[118:121], v[186:189], a[92:95], v[118:121]
s_add_u32 s40, s40, 221184
ds_read_b128 v[178:181], v52 offset:26624
v_mfma_f32_16x16x32_bf16 v[134:137], a[244:247], v[186:189], v[134:137]
v_mfma_f32_16x16x32_bf16 v[122:125], a[84:87], v[194:197], v[122:125]
s_addc_u32 s41, s41, 0
v_mfma_f32_16x16x32_bf16 v[126:129], a[88:91], v[194:197], v[126:129]
v_mfma_f32_16x16x32_bf16 v[130:133], v[194:197], a[92:95], v[130:133]
v_mfma_f32_16x16x32_bf16 v[158:161], a[244:247], v[194:197], v[158:161]
s_waitcnt vmcnt(9) lgkmcnt(0)
s_barrier
s_add_u32 m0, s48, 40960
ds_read_b64_tr_b16 a[120:121], v255 offset:55296
v_mfma_f32_16x16x32_bf16 v[86:89], a[96:99], v[106:109], v[86:89]
ds_read_b64_tr_b16 a[122:123], v255 offset:57600
v_mfma_f32_16x16x32_bf16 v[90:93], a[100:103], v[106:109], v[90:93]
global_load_lds_dwordx4 v0, s[40:41] offset:-4096
ds_read_b64_tr_b16 a[124:125], v255 offset:55488
v_mfma_f32_16x16x32_bf16 v[94:97], v[106:109], a[104:107], v[94:97]
ds_read_b64_tr_b16 a[126:127], v255 offset:57792
global_load_lds_dwordx4 v1, s[40:41] offset:-3072
v_mfma_f32_16x16x32_bf16 v[98:101], a[248:251], v[106:109], v[98:101]
ds_read_b64_tr_b16 a[128:129], v255 offset:55680
v_mfma_f32_16x16x32_bf16 v[110:113], a[96:99], v[166:169], v[110:113]
ds_read_b64_tr_b16 a[130:131], v255 offset:57984
global_load_lds_dwordx4 v2, s[40:41] offset:-2048
v_mfma_f32_16x16x32_bf16 v[114:117], a[100:103], v[166:169], v[114:117]
ds_read_b64_tr_b16 v[54:55], v163 offset:55296
v_mfma_f32_16x16x32_bf16 v[118:121], v[166:169], a[104:107], v[118:121]
global_load_lds_dwordx4 v3, s[40:41] offset:-1024
ds_read_b64_tr_b16 v[56:57], v163 offset:57600
v_mfma_f32_16x16x32_bf16 v[134:137], a[248:251], v[166:169], v[134:137]
ds_read_b128 v[182:185], v51 offset:10240
global_load_lds_dwordx4 v4, s[40:41] offset:0
v_mfma_f32_16x16x32_bf16 v[122:125], a[96:99], v[174:177], v[122:125]
ds_read_b128 v[190:193], v51 offset:28672
v_mfma_f32_16x16x32_bf16 v[126:129], a[100:103], v[174:177], v[126:129]
ds_read_b64_tr_b16 a[132:133], v162 offset:8192
global_load_lds_dwordx4 v5, s[40:41] offset:1024
v_mfma_f32_16x16x32_bf16 v[130:133], v[174:177], a[104:107], v[130:133]
ds_read_b64_tr_b16 a[134:135], v162 offset:10496
v_mfma_f32_16x16x32_bf16 v[158:161], a[248:251], v[174:177], v[158:161]
global_load_lds_dwordx4 v6, s[40:41] offset:2048
ds_read_b64_tr_b16 a[136:137], v162 offset:8384
v_mfma_f32_16x16x32_bf16 v[86:89], a[108:111], v[106:109], v[86:89]
ds_read_b64_tr_b16 a[138:139], v162 offset:10688
v_mfma_f32_16x16x32_bf16 v[90:93], a[112:115], v[106:109], v[90:93]
global_load_lds_dwordx4 v7, s[40:41] offset:3072
ds_read_b64_tr_b16 a[140:141], v162 offset:8576
v_mfma_f32_16x16x32_bf16 v[94:97], v[106:109], a[116:119], v[94:97]
s_add_u32 m0, s48, 49152
ds_read_b64_tr_b16 a[142:143], v162 offset:10880
v_mfma_f32_16x16x32_bf16 v[98:101], a[252:255], v[106:109], v[98:101]
ds_read_b64_tr_b16 v[58:59], v164 offset:8192
v_mfma_f32_16x16x32_bf16 v[110:113], a[108:111], v[170:173], v[110:113]
global_load_lds_dwordx4 v8, s[40:41] offset:-4096
ds_read_b64_tr_b16 v[60:61], v164 offset:10496
v_mfma_f32_16x16x32_bf16 v[114:117], a[112:115], v[170:173], v[114:117]
ds_read_b128 v[186:189], v52 offset:10240
v_mfma_f32_16x16x32_bf16 v[118:121], v[170:173], a[116:119], v[118:121]
s_add_u32 s40, s40, 221184
ds_read_b128 v[194:197], v52 offset:28672
v_mfma_f32_16x16x32_bf16 v[134:137], a[252:255], v[170:173], v[134:137]
v_mfma_f32_16x16x32_bf16 v[122:125], a[108:111], v[178:181], v[122:125]
s_addc_u32 s41, s41, 0
v_mfma_f32_16x16x32_bf16 v[126:129], a[112:115], v[178:181], v[126:129]
v_mfma_f32_16x16x32_bf16 v[130:133], v[178:181], a[116:119], v[130:133]
v_mfma_f32_16x16x32_bf16 v[158:161], a[252:255], v[178:181], v[158:161]
s_waitcnt vmcnt(9) lgkmcnt(0)
)"
R"(s_barrier
s_add_u32 m0, s48, 77824
ds_read_b64_tr_b16 a[144:145], v9 offset:47104
v_mfma_f32_16x16x32_bf16 v[86:89], a[120:123], v[106:109], v[86:89]
ds_read_b64_tr_b16 a[146:147], v9 offset:49408
v_mfma_f32_16x16x32_bf16 v[90:93], a[124:127], v[106:109], v[90:93]
global_load_lds_dwordx4 v0, s[40:41] offset:-4096
ds_read_b64_tr_b16 a[148:149], v9 offset:47296
v_mfma_f32_16x16x32_bf16 v[94:97], v[106:109], a[128:131], v[94:97]
ds_read_b64_tr_b16 a[150:151], v9 offset:49600
global_load_lds_dwordx4 v1, s[40:41] offset:-3072
v_mfma_f32_16x16x32_bf16 v[98:101], v[54:57], v[106:109], v[98:101]
ds_read_b64_tr_b16 a[152:153], v9 offset:47488
v_mfma_f32_16x16x32_bf16 v[110:113], a[120:123], v[182:185], v[110:113]
ds_read_b64_tr_b16 a[154:155], v9 offset:49792
global_load_lds_dwordx4 v2, s[40:41] offset:-2048
v_mfma_f32_16x16x32_bf16 v[114:117], a[124:127], v[182:185], v[114:117]
ds_read_b64_tr_b16 v[62:63], v10 offset:47104
v_mfma_f32_16x16x32_bf16 v[118:121], v[182:185], a[128:131], v[118:121]
global_load_lds_dwordx4 v3, s[40:41] offset:-1024
ds_read_b64_tr_b16 v[64:65], v10 offset:49408
v_mfma_f32_16x16x32_bf16 v[134:137], v[54:57], v[182:185], v[134:137]
ds_read_b128 v[166:169], v51 offset:12288
global_load_lds_dwordx4 v4, s[40:41] offset:0
v_mfma_f32_16x16x32_bf16 v[122:125], a[120:123], v[190:193], v[122:125]
ds_read_b128 v[174:177], v51 offset:30720
v_mfma_f32_16x16x32_bf16 v[126:129], a[124:127], v[190:193], v[126:129]
ds_read_b64_tr_b16 a[156:157], v255 offset:0
global_load_lds_dwordx4 v5, s[40:41] offset:1024
v_mfma_f32_16x16x32_bf16 v[130:133], v[190:193], a[128:131], v[130:133]
ds_read_b64_tr_b16 a[158:159], v255 offset:2304
v_mfma_f32_16x16x32_bf16 v[158:161], v[54:57], v[190:193], v[158:161]
global_load_lds_dwordx4 v6, s[40:41] offset:2048
ds_read_b64_tr_b16 a[160:161], v255 offset:192
v_mfma_f32_16x16x32_bf16 v[86:89], a[132:135], v[106:109], v[86:89]
ds_read_b64_tr_b16 a[162:163], v255 offset:2496
v_mfma_f32_16x16x32_bf16 v[90:93], a[136:139], v[106:109], v[90:93]
global_load_lds_dwordx4 v7, s[40:41] offset:3072
ds_read_b64_tr_b16 a[164:165], v255 offset:384
v_mfma_f32_16x16x32_bf16 v[94:97], v[106:109], a[140:143], v[94:97]
s_add_u32 m0, s48, 86016
ds_read_b64_tr_b16 a[166:167], v255 offset:2688
v_mfma_f32_16x16x32_bf16 v[98:101], v[58:61], v[106:109], v[98:101]
ds_read_b64_tr_b16 v[66:67], v163 offset:0
v_mfma_f32_16x16x32_bf16 v[110:113], a[132:135], v[186:189], v[110:113]
global_load_lds_dwordx4 v8, s[40:41] offset:-4096
ds_read_b64_tr_b16 v[68:69], v163 offset:2304
v_mfma_f32_16x16x32_bf16 v[114:117], a[136:139], v[186:189], v[114:117]
ds_read_b128 v[170:173], v52 offset:12288
v_mfma_f32_16x16x32_bf16 v[118:121], v[186:189], a[140:143], v[118:121]
s_sub_u32 s40, s40, 1769472
ds_read_b128 v[178:181], v52 offset:30720
v_mfma_f32_16x16x32_bf16 v[134:137], v[58:61], v[186:189], v[134:137]
v_mfma_f32_16x16x32_bf16 v[122:125], a[132:135], v[194:197], v[122:125]
s_subb_u32 s41, s41, 0
v_mfma_f32_16x16x32_bf16 v[126:129], a[136:139], v[194:197], v[126:129]
v_mfma_f32_16x16x32_bf16 v[130:133], v[194:197], a[140:143], v[130:133]
v_mfma_f32_16x16x32_bf16 v[158:161], v[58:61], v[194:197], v[158:161]
s_waitcnt vmcnt(9) lgkmcnt(0)
s_barrier
s_add_u32 m0, s49, 0
ds_read_b64_tr_b16 a[168:169], v255 offset:18432
v_mfma_f32_16x16x32_bf16 v[86:89], a[144:147], v[106:109], v[86:89]
ds_read_b64_tr_b16 a[170:171], v255 offset:20736
v_mfma_f32_16x16x32_bf16 v[90:93], a[148:151], v[106:109], v[90:93]
ds_read_b64_tr_b16 a[172:173], v255 offset:18624
v_mfma_f32_16x16x32_bf16 v[94:97], v[106:109], a[152:155], v[94:97]
ds_read_b64_tr_b16 a[174:175], v255 offset:20928
global_load_lds_dwordx4 v11, s[42:43] offset:-2048 sc1
v_mfma_f32_16x16x32_bf16 v[98:101], v[62:65], v[106:109], v[98:101]
ds_read_b64_tr_b16 a[176:177], v255 offset:18816
v_mfma_f32_16x16x32_bf16 v[110:113], a[144:147], v[166:169], v[110:113]
ds_read_b64_tr_b16 a[178:179], v255 offset:21120
v_mfma_f32_16x16x32_bf16 v[114:117], a[148:151], v[166:169], v[114:117]
ds_read_b64_tr_b16 v[70:71], v163 offset:18432
global_load_lds_dwordx4 v12, s[42:43] offset:-1024 sc1
v_mfma_f32_16x16x32_bf16 v[118:121], v[166:169], a[152:155], v[118:121]
ds_read_b64_tr_b16 v[72:73], v163 offset:20736
v_mfma_f32_16x16x32_bf16 v[134:137], v[62:65], v[166:169], v[134:137]
ds_read_b128 v[182:185], v51 offset:14336
v_mfma_f32_16x16x32_bf16 v[122:125], a[144:147], v[174:177], v[122:125]
ds_read_b128 v[190:193], v51 offset:32768
global_load_lds_dwordx4 v13, s[42:43] offset:0 sc1
v_mfma_f32_16x16x32_bf16 v[126:129], a[148:151], v[174:177], v[126:129]
ds_read_b64_tr_b16 a[180:181], v255 offset:36864
v_mfma_f32_16x16x32_bf16 v[130:133], v[174:177], a[152:155], v[130:133]
ds_read_b64_tr_b16 a[182:183], v255 offset:39168
v_mfma_f32_16x16x32_bf16 v[158:161], v[62:65], v[174:177], v[158:161]
ds_read_b64_tr_b16 a[184:185], v255 offset:37056
v_mfma_f32_16x16x32_bf16 v[86:89], a[156:159], v[106:109], v[86:89]
global_load_lds_dwordx4 v14, s[42:43] offset:1024 sc1
ds_read_b64_tr_b16 a[186:187], v255 offset:39360
v_mfma_f32_16x16x32_bf16 v[90:93], a[160:163], v[106:109], v[90:93]
ds_read_b64_tr_b16 a[188:189], v255 offset:37248
v_mfma_f32_16x16x32_bf16 v[94:97], v[106:109], a[164:167], v[94:97]
ds_read_b64_tr_b16 a[190:191], v255 offset:39552
v_mfma_f32_16x16x32_bf16 v[98:101], v[66:69], v[106:109], v[98:101]
global_load_lds_dwordx4 v15, s[42:43] offset:2048 sc1
ds_read_b64_tr_b16 v[74:75], v163 offset:36864
v_mfma_f32_16x16x32_bf16 v[110:113], a[156:159], v[170:173], v[110:113]
ds_read_b64_tr_b16 v[76:77], v163 offset:39168
v_mfma_f32_16x16x32_bf16 v[114:117], a[160:163], v[170:173], v[114:117]
ds_read_b128 v[186:189], v52 offset:14336
v_mfma_f32_16x16x32_bf16 v[118:121], v[170:173], a[164:167], v[118:121]
s_sub_u32 s42, s42, 18432
s_subb_u32 s43, s43, 0
ds_read_b128 v[194:197], v52 offset:32768
v_mfma_f32_16x16x32_bf16 v[134:137], v[66:69], v[170:173], v[134:137]
v_mfma_f32_16x16x32_bf16 v[122:125], a[156:159], v[178:181], v[122:125]
v_mfma_f32_16x16x32_bf16 v[126:129], a[160:163], v[178:181], v[126:129]
v_mfma_f32_16x16x32_bf16 v[130:133], v[178:181], a[164:167], v[130:133]
v_mfma_f32_16x16x32_bf16 v[158:161], v[66:69], v[178:181], v[158:161]
s_waitcnt vmcnt(5) lgkmcnt(0)
s_barrier
s_add_u32 m0, s49, 18432
ds_read_b64_tr_b16 a[192:193], v255 offset:55296
v_mfma_f32_16x16x32_bf16 v[86:89], a[168:171], v[106:109], v[86:89]
ds_read_b64_tr_b16 a[194:195], v255 offset:57600
v_mfma_f32_16x16x32_bf16 v[90:93], a[172:175], v[106:109], v[90:93]
ds_read_b64_tr_b16 a[196:197], v255 offset:55488
v_mfma_f32_16x16x32_bf16 v[94:97], v[106:109], a[176:179], v[94:97]
ds_read_b64_tr_b16 a[198:199], v255 offset:57792
global_load_lds_dwordx4 v11, s[42:43] offset:-2048 sc1
v_mfma_f32_16x16x32_bf16 v[98:101], v[70:73], v[106:109], v[98:101]
ds_read_b64_tr_b16 a[200:201], v255 offset:55680
v_mfma_f32_16x16x32_bf16 v[110:113], a[168:171], v[182:185], v[110:113]
ds_read_b64_tr_b16 a[202:203], v255 offset:57984
v_mfma_f32_16x16x32_bf16 v[114:117], a[172:175], v[182:185], v[114:117]
ds_read_b64_tr_b16 v[78:79], v163 offset:55296
global_load_lds_dwordx4 v12, s[42:43] offset:-1024 sc1
v_mfma_f32_16x16x32_bf16 v[118:121], v[182:185], a[176:179], v[118:121]
ds_read_b64_tr_b16 v[80:81], v163 offset:57600
v_mfma_f32_16x16x32_bf16 v[134:137], v[70:73], v[182:185], v[134:137]
ds_read_b128 v[166:169], v51 offset:16384
v_mfma_f32_16x16x32_bf16 v[122:125], a[168:171], v[190:193], v[122:125]
ds_read_b128 v[174:177], v51 offset:34816
global_load_lds_dwordx4 v13, s[42:43] offset:0 sc1
v_mfma_f32_16x16x32_bf16 v[126:129], a[172:175], v[190:193], v[126:129]
ds_read_b64_tr_b16 a[204:205], v162 offset:8192
v_mfma_f32_16x16x32_bf16 v[130:133], v[190:193], a[176:179], v[130:133]
ds_read_b64_tr_b16 a[206:207], v162 offset:10496
v_mfma_f32_16x16x32_bf16 v[158:161], v[70:73], v[190:193], v[158:161]
ds_read_b64_tr_b16 a[208:209], v162 offset:8384
global_load_lds_dwordx4 v14, s[42:43] offset:1024 sc1
v_mfma_f32_16x16x32_bf16 v[86:89], a[180:183], v[106:109], v[86:89]
ds_read_b64_tr_b16 a[210:211], v162 offset:10688
v_mfma_f32_16x16x32_bf16 v[90:93], a[184:187], v[106:109], v[90:93]
ds_read_b64_tr_b16 a[212:213], v162 offset:8576
v_mfma_f32_16x16x32_bf16 v[94:97], v[106:109], a[188:191], v[94:97]
ds_read_b64_tr_b16 a[214:215], v162 offset:10880
global_load_lds_dwordx4 v15, s[42:43] offset:2048 sc1
v_mfma_f32_16x16x32_bf16 v[98:101], v[74:77], v[106:109], v[98:101]
ds_read_b64_tr_b16 v[82:83], v164 offset:8192
v_mfma_f32_16x16x32_bf16 v[110:113], a[180:183], v[186:189], v[110:113]
ds_read_b64_tr_b16 v[84:85], v164 offset:10496
v_mfma_f32_16x16x32_bf16 v[114:117], a[184:187], v[186:189], v[114:117]
ds_read_b128 v[170:173], v52 offset:16384
s_sub_u32 s42, s42, 18432
s_subb_u32 s43, s43, 0
v_mfma_f32_16x16x32_bf16 v[118:121], v[186:189], a[188:191], v[118:121]
ds_read_b128 v[178:181], v52 offset:34816
v_mfma_f32_16x16x32_bf16 v[134:137], v[74:77], v[186:189], v[134:137]
v_mfma_f32_16x16x32_bf16 v[122:125], a[180:183], v[194:197], v[122:125]
v_mfma_f32_16x16x32_bf16 v[126:129], a[184:187], v[194:197], v[126:129]
v_mfma_f32_16x16x32_bf16 v[158:161], v[74:77], v[194:197], v[158:161]
s_waitcnt lgkmcnt(15)
v_mfma_f32_16x16x32_bf16 v[86:89], a[192:195], v[106:109], v[86:89]
v_mfma_f32_16x16x32_bf16 v[90:93], a[196:199], v[106:109], v[90:93]
s_waitcnt lgkmcnt(14)
v_mfma_f32_16x16x32_bf16 v[94:97], v[106:109], a[200:203], v[94:97]
s_waitcnt lgkmcnt(12)
v_mfma_f32_16x16x32_bf16 v[98:101], v[78:81], v[106:109], v[98:101]
s_waitcnt lgkmcnt(11)
v_mfma_f32_16x16x32_bf16 v[110:113], a[192:195], v[166:169], v[110:113]
v_mfma_f32_16x16x32_bf16 v[114:117], a[196:199], v[166:169], v[114:117]
v_mfma_f32_16x16x32_bf16 v[118:121], v[166:169], a[200:203], v[118:121]
v_mfma_f32_16x16x32_bf16 v[134:137], v[78:81], v[166:169], v[134:137]
s_waitcnt lgkmcnt(10)
v_mfma_f32_16x16x32_bf16 v[158:161], v[78:81], v[174:177], v[158:161]
s_waitcnt lgkmcnt(8)
v_mfma_f32_16x16x32_bf16 v[86:89], a[204:207], v[106:109], v[86:89]
s_waitcnt lgkmcnt(6)
v_mfma_f32_16x16x32_bf16 v[90:93], a[208:211], v[106:109], v[90:93]
s_waitcnt lgkmcnt(4)
v_mfma_f32_16x16x32_bf16 v[94:97], v[106:109], a[212:215], v[94:97]
s_waitcnt lgkmcnt(2)
v_mfma_f32_16x16x32_bf16 v[98:101], v[82:85], v[106:109], v[98:101]
s_waitcnt lgkmcnt(1)
v_mfma_f32_16x16x32_bf16 v[110:113], a[204:207], v[170:173], v[110:113]
v_mfma_f32_16x16x32_bf16 v[114:117], a[208:211], v[170:173], v[114:117]
v_mfma_f32_16x16x32_bf16 v[118:121], v[170:173], a[212:215], v[118:121]
v_mfma_f32_16x16x32_bf16 v[134:137], v[82:85], v[170:173], v[134:137]
s_waitcnt lgkmcnt(0)
v_mfma_f32_16x16x32_bf16 v[158:161], v[82:85], v[178:181], v[158:161]
s_barrier
ds_read_b32 v242, v24 offset:0
s_waitcnt lgkmcnt(0)
s_nop 2
v_fma_f32 v230, v98, v242, v134
v_fma_f32 v231, v99, v242, v135
v_fma_f32 v232, v100, v242, v136
v_fma_f32 v233, v101, v242, v137
v_cvt_pk_bf16_f32 v230, v230, v231
v_cvt_pk_bf16_f32 v231, v232, v233
ds_write_b64 v22, v[230:231] offset:40960
ds_read_b128 v[198:201], v51 offset:32768
ds_read_b128 v[202:205], v52 offset:32768
ds_read_b128 v[206:209], v51 offset:34816
ds_read_b128 v[210:213], v52 offset:34816
v_mov_b32 v234, v32
v_mov_b32 v235, v33
v_mov_b32 v236, v34
v_mov_b32 v237, v35
v_mov_b32 v134, v158
v_mov_b32 v135, v159
v_mov_b32 v136, v160
v_mov_b32 v137, v161
s_waitcnt lgkmcnt(4)
s_barrier
ds_read_b32 v23, v24 offset:0
ds_read_b32 v148, v21
ds_read_b32 v26, v24 offset:192
ds_read_b32 v53, v27
ds_read_b32 v8, v28
ds_read_b128 v[226:229], v25 offset:0
ds_read_b64 v[158:159], v22 offset:41472
ds_read_b32 v140, v20
v_xor_b32 v51, 0x10000, v51
v_xor_b32 v52, 0x10000, v52
s_waitcnt lgkmcnt(7)
v_fma_f32 v238, v90, v23, v114
v_fma_f32 v239, v91, v23, v115
v_fma_f32 v240, v92, v23, v116
v_fma_f32 v241, v93, v23, v117
v_cvt_pk_bf16_f32 v146, v238, v239
v_cvt_pk_bf16_f32 v147, v240, v241
s_waitcnt lgkmcnt(0)
s_barrier
ds_read_b32 v242, v24 offset:64
v_fma_f32 v238, v94, v226, v118
v_fma_f32 v239, v94, v227, v119
v_fma_f32 v240, v94, v228, v120
v_fma_f32 v241, v94, v229, v121
v_cvt_pk_bf16_f32 v154, v238, v239
v_cvt_pk_bf16_f32 v155, v240, v241
v_fma_f32 v238, v86, v23, v110
v_fma_f32 v239, v87, v23, v111
v_fma_f32 v240, v88, v23, v112
v_fma_f32 v241, v89, v23, v113
v_cvt_pk_bf16_f32 v138, v238, v239
v_cvt_pk_bf16_f32 v139, v240, v241
s_waitcnt lgkmcnt(0)
v_fma_f32 v230, v98, v242, v134
v_fma_f32 v231, v99, v242, v135
v_fma_f32 v232, v100, v242, v136
v_fma_f32 v233, v101, v242, v137
v_cvt_pk_bf16_f32 v230, v230, v231
v_cvt_pk_bf16_f32 v231, v232, v233
ds_write_b64 v22, v[230:231] offset:40960
s_waitcnt vmcnt(5) lgkmcnt(0)
s_barrier
s_mov_b32 s70, 32
s_waitcnt lgkmcnt(0)
s_nop 1
s_branch .Lqa_in5
.p2align 6
.Lqa_top16:
s_waitcnt lgkmcnt(9)
v_mfma_f32_16x16x32_bf16 v[134:137], a[216:219], v[44:47], 0
s_waitcnt lgkmcnt(8)
ds_read_b128 v[162:165], v52 offset:4096
ds_read_b32 v8, v30
v_mfma_f32_16x16x32_bf16 v[134:137], a[220:223], v[102:105], v[134:137]
s_waitcnt lgkmcnt(9)
global_store_dwordx4 v16, v[230:233], s[38:39] sc1
v_mfma_f32_16x16x32_bf16 v[110:113], a[0:3], v[44:47], 0
s_waitcnt lgkmcnt(6)
ds_read_b128 v[166:169], v51 offset:6144
v_add_u32 v16, 18432, v16
v_mfma_f32_16x16x32_bf16 v[134:137], a[224:227], v[106:109], v[134:137]
s_waitcnt lgkmcnt(5)
ds_read_b128 v[170:173], v52 offset:6144
ds_read_b128 v[226:229], v25 offset:64
v_mfma_f32_16x16x32_bf16 v[134:137], a[228:231], v[0:3], v[134:137]
ds_read_b64 v[160:161], v22 offset:41472
ds_read_b32 v144, v20
v_mfma_f32_16x16x32_bf16 v[114:117], a[4:7], v[44:47], 0
s_waitcnt lgkmcnt(7)
ds_read_b128 v[174:177], v51 offset:8192
v_fma_f32 v238, v90, v23, v126
v_mfma_f32_16x16x32_bf16 v[134:137], a[232:235], v[4:7], v[134:137]
s_waitcnt lgkmcnt(7)
ds_read_b128 v[178:181], v52 offset:8192
v_fma_f32 v239, v91, v23, v127
v_mfma_f32_16x16x32_bf16 v[134:137], a[236:239], v[162:165], v[134:137]
v_fma_f32 v240, v92, v23, v128
v_fma_f32 v241, v93, v23, v129
v_mfma_f32_16x16x32_bf16 v[118:121], v[44:47], a[8:11], 0
s_waitcnt lgkmcnt(6)
ds_read_b128 v[182:185], v51 offset:10240
v_cvt_pk_bf16_f32 v150, v238, v239
v_mfma_f32_16x16x32_bf16 v[134:137], a[240:243], v[166:169], v[134:137]
s_waitcnt lgkmcnt(6)
ds_read_b128 v[186:189], v52 offset:10240
v_cvt_pk_bf16_f32 v151, v240, v241
v_mfma_f32_16x16x32_bf16 v[134:137], a[244:247], v[170:173], v[134:137]
s_nop 0
v_mfma_f32_16x16x32_bf16 v[214:217], v[150:153], v[138:141], v[32:35]
v_max_f32 v48, v218, v219
v_max_f32 v49, v220, v221
v_mfma_f32_16x16x32_bf16 v[110:113], a[12:15], v[102:105], v[110:113]
s_waitcnt lgkmcnt(6)
ds_read_b128 v[190:193], v51 offset:12288
v_fma_f32 v238, v94, v226, v130
s_waitcnt lgkmcnt(4)
v_mfma_f32_16x16x32_bf16 v[134:137], a[248:251], v[174:177], v[134:137]
s_xor_b32 s49, s49, 0x10000
ds_read_b128 v[194:197], v52 offset:12288
s_waitcnt lgkmcnt(4)
v_mfma_f32_16x16x32_bf16 v[134:137], a[252:255], v[178:181], v[134:137]
s_add_u32 m0, s49, 0
v_mul_f32 v26, 0x3eae1a93, v26
v_mul_f32 v9, v53, v26
v_mfma_f32_16x16x32_bf16 v[114:117], a[16:19], v[102:105], v[114:117]
s_waitcnt lgkmcnt(3)
ds_read_b128 v[198:201], v51 offset:14336
global_load_lds_dwordx4 v11, s[42:43] offset:-2048 sc1
v_mfma_f32_16x16x32_bf16 v[134:137], v[54:57], v[182:185], v[134:137]
s_waitcnt lgkmcnt(3)
ds_read_b128 v[202:205], v52 offset:14336
v_mul_f32 v10, v8, v26
v_mfma_f32_16x16x32_bf16 v[134:137], v[58:61], v[186:189], v[134:137]
v_max3_f32 v48, v214, v215, v48
v_max3_f32 v49, v216, v217, v49
v_mfma_f32_16x16x32_bf16 v[118:121], v[102:105], a[20:23], v[118:121]
s_waitcnt lgkmcnt(3)
ds_read_b128 v[206:209], v51 offset:16384
v_mul_f32 v48, v48, v9
v_mfma_f32_16x16x32_bf16 v[134:137], v[62:65], v[190:193], v[134:137]
s_waitcnt lgkmcnt(3)
ds_read_b128 v[210:213], v52 offset:16384
v_mul_f32 v49, v49, v10
v_mfma_f32_16x16x32_bf16 v[134:137], v[66:69], v[194:197], v[134:137]
v_max_f32 v50, v48, v49
v_mov_b32 v95, v50
v_mfma_f32_16x16x32_bf16 v[110:113], a[24:27], v[106:109], v[110:113]
s_waitcnt lgkmcnt(3)
v_permlane16_swap_b32 v50, v95
v_mfma_f32_16x16x32_bf16 v[134:137], v[70:73], v[198:201], v[134:137]
s_waitcnt lgkmcnt(2)
v_max_f32 v50, v50, v95
v_mov_b32 v95, v50
v_mfma_f32_16x16x32_bf16 v[134:137], v[74:77], v[202:205], v[134:137]
global_load_lds_dwordx4 v12, s[42:43] offset:-1024 sc1
v_fma_f32 v239, v94, v227, v131
v_mfma_f32_16x16x32_bf16 v[114:117], a[28:31], v[106:109], v[114:117]
s_waitcnt lgkmcnt(1)
v_permlane32_swap_b32 v50, v95
v_mfma_f32_16x16x32_bf16 v[134:137], v[78:81], v[206:209], v[134:137]
s_waitcnt lgkmcnt(0)
v_max_f32 v50, v50, v95
v_sub_f32 v48, v48, v50
v_mfma_f32_16x16x32_bf16 v[134:137], v[82:85], v[210:213], v[134:137]
v_sub_f32 v49, v49, v50
v_fma_f32 v240, v94, v228, v132
v_mfma_f32_16x16x32_bf16 v[118:121], v[106:109], a[32:35], v[118:121]
v_exp_f32 v96, v48
v_mfma_f32_16x16x32_bf16 v[110:113], a[36:39], v[0:3], v[110:113]
v_exp_f32 v97, v49
v_mfma_f32_16x16x32_bf16 v[114:117], a[40:43], v[0:3], v[114:117]
v_add_f32 v50, v96, v97
v_mov_b32 v95, v50
v_mfma_f32_16x16x32_bf16 v[118:121], v[0:3], a[44:47], v[118:121]
v_mul_f32 v96, v96, v53
v_fma_f32 v241, v94, v229, v133
v_mfma_f32_16x16x32_bf16 v[110:113], a[48:51], v[4:7], v[110:113]
global_load_lds_dwordx4 v13, s[42:43] offset:0 sc1
v_cvt_pk_bf16_f32 v156, v238, v239
v_mfma_f32_16x16x32_bf16 v[114:117], a[52:55], v[4:7], v[114:117]
v_permlane16_swap_b32 v50, v95
v_mfma_f32_16x16x32_bf16 v[118:121], v[4:7], a[56:59], v[118:121]
v_add_f32 v50, v50, v95
v_mov_b32 v95, v50
v_mfma_f32_16x16x32_bf16 v[110:113], a[60:63], v[162:165], v[110:113]
v_mul_f32 v97, v97, v8
v_cvt_pk_bf16_f32 v157, v240, v241
v_mfma_f32_16x16x32_bf16 v[114:117], a[64:67], v[162:165], v[114:117]
v_permlane32_swap_b32 v50, v95
v_mfma_f32_16x16x32_bf16 v[118:121], v[162:165], a[68:71], v[118:121]
v_mfma_f32_16x16x32_bf16 v[110:113], a[72:75], v[166:169], v[110:113]
s_barrier
v_mfma_f32_16x16x32_bf16 v[114:117], a[76:79], v[166:169], v[114:117]
ds_read_b32 v242, v24 offset:0
v_add_f32 v50, v50, v95
v_mfma_f32_16x16x32_bf16 v[118:121], v[166:169], a[80:83], v[118:121]
global_load_lds_dwordx4 v14, s[42:43] offset:1024 sc1
v_fma_f32 v238, v86, v23, v122
v_mfma_f32_16x16x32_bf16 v[110:113], a[84:87], v[170:173], v[110:113]
v_rcp_f32 v50, v50
v_mfma_f32_16x16x32_bf16 v[114:117], a[88:91], v[170:173], v[114:117]
)"
R"(v_mul_f32 v96, v96, v50
v_mul_f32 v97, v97, v50
v_mfma_f32_16x16x32_bf16 v[118:121], v[170:173], a[92:95], v[118:121]
v_cvt_pk_bf16_f32 v96, v96, v96
v_cvt_pk_bf16_f32 v97, v97, v97
v_mfma_f32_16x16x32_bf16 v[110:113], a[96:99], v[174:177], v[110:113]
v_and_b32 v224, v96, v40
v_and_b32 v225, v97, v41
v_mfma_f32_16x16x32_bf16 v[114:117], a[100:103], v[174:177], v[114:117]
v_and_b32 v222, v96, v42
v_and_b32 v223, v97, v43
v_mfma_f32_16x16x32_bf16 v[118:121], v[174:177], a[104:107], v[118:121]
s_nop 0
v_mfma_f32_16x16x32_bf16 v[218:221], v[154:157], v[222:225], 0
v_mfma_f32_16x16x32_bf16 v[214:217], v[158:161], v[222:225], 0
s_waitcnt lgkmcnt(0)
v_fma_f32 v230, v98, v242, v134
v_fma_f32 v231, v99, v242, v135
v_mfma_f32_16x16x32_bf16 v[110:113], a[108:111], v[178:181], v[110:113]
v_fma_f32 v232, v100, v242, v136
v_fma_f32 v233, v101, v242, v137
v_mfma_f32_16x16x32_bf16 v[114:117], a[112:115], v[178:181], v[114:117]
global_load_lds_dwordx4 v15, s[42:43] offset:2048 sc1
s_add_u32 s42, s42, s46
s_addc_u32 s43, s43, s47
v_mfma_f32_16x16x32_bf16 v[118:121], v[178:181], a[116:119], v[118:121]
v_cvt_pk_bf16_f32 v230, v230, v231
v_cvt_pk_bf16_f32 v231, v232, v233
v_mfma_f32_16x16x32_bf16 v[110:113], a[120:123], v[182:185], v[110:113]
v_cvt_pk_bf16_f32 v254, v218, v219
v_cvt_pk_bf16_f32 v255, v220, v221
v_mfma_f32_16x16x32_bf16 v[114:117], a[124:127], v[182:185], v[114:117]
ds_write_b64 v18, v[254:255]
v_mfma_f32_16x16x32_bf16 v[118:121], v[182:185], a[128:131], v[118:121]
v_cvt_pk_bf16_f32 v96, v214, v215
v_fma_f32 v239, v87, v23, v123
v_mfma_f32_16x16x32_bf16 v[110:113], a[132:135], v[186:189], v[110:113]
ds_write_b32 v19, v96
v_mfma_f32_16x16x32_bf16 v[114:117], a[136:139], v[186:189], v[114:117]
ds_write_b64 v22, v[230:231] offset:40960
v_mfma_f32_16x16x32_bf16 v[118:121], v[186:189], a[140:143], v[118:121]
v_fma_f32 v240, v88, v23, v124
v_fma_f32 v241, v89, v23, v125
v_mfma_f32_16x16x32_bf16 v[110:113], a[144:147], v[190:193], v[110:113]
v_cvt_pk_bf16_f32 v142, v238, v239
v_cvt_pk_bf16_f32 v143, v240, v241
v_mfma_f32_16x16x32_bf16 v[114:117], a[148:151], v[190:193], v[114:117]
v_mfma_f32_16x16x32_bf16 v[118:121], v[190:193], a[152:155], v[118:121]
v_mfma_f32_16x16x32_bf16 v[110:113], a[156:159], v[194:197], v[110:113]
v_mfma_f32_16x16x32_bf16 v[114:117], a[160:163], v[194:197], v[114:117]
v_mfma_f32_16x16x32_bf16 v[118:121], v[194:197], a[164:167], v[118:121]
v_mfma_f32_16x16x32_bf16 v[110:113], a[168:171], v[198:201], v[110:113]
v_mfma_f32_16x16x32_bf16 v[114:117], a[172:175], v[198:201], v[114:117]
v_mfma_f32_16x16x32_bf16 v[118:121], v[198:201], a[176:179], v[118:121]
v_mfma_f32_16x16x32_bf16 v[110:113], a[180:183], v[202:205], v[110:113]
v_mfma_f32_16x16x32_bf16 v[114:117], a[184:187], v[202:205], v[114:117]
s_waitcnt vmcnt(5) lgkmcnt(0)
s_barrier
v_mfma_f32_16x16x32_bf16 v[118:121], v[202:205], a[188:191], v[118:121]
ds_read_b128 v[44:47], v51 offset:18432
v_mfma_f32_16x16x32_bf16 v[110:113], a[192:195], v[206:209], v[110:113]
ds_read_b128 v[102:105], v52 offset:18432
ds_read_b128 v[230:233], v17 offset:0
v_mfma_f32_16x16x32_bf16 v[114:117], a[196:199], v[206:209], v[114:117]
ds_read_b32 v23, v24 offset:0
ds_read_b32 v148, v21
v_mfma_f32_16x16x32_bf16 v[218:221], v[150:153], v[142:145], v[36:39]
v_mfma_f32_16x16x32_bf16 v[118:121], v[206:209], a[200:203], v[118:121]
ds_read_b128 v[106:109], v51 offset:20480
ds_read_b32 v26, v24 offset:192
v_mfma_f32_16x16x32_bf16 v[110:113], a[204:207], v[210:213], v[110:113]
ds_read_b128 v[0:3], v52 offset:20480
ds_read_b32 v53, v27
v_mfma_f32_16x16x32_bf16 v[114:117], a[208:211], v[210:213], v[114:117]
ds_read_b32 v8, v28
ds_read_b128 v[226:229], v25 offset:0
v_mfma_f32_16x16x32_bf16 v[118:121], v[210:213], a[212:215], v[118:121]
s_waitcnt lgkmcnt(10)
ds_read_b128 v[4:7], v51 offset:22528
ds_read_b64 v[158:159], v22 offset:41472
v_mfma_f32_16x16x32_bf16 v[134:137], a[216:219], v[44:47], 0
s_waitcnt lgkmcnt(11)
ds_read_b128 v[162:165], v52 offset:22528
ds_read_b32 v140, v20
v_mfma_f32_16x16x32_bf16 v[134:137], a[220:223], v[102:105], v[134:137]
s_waitcnt lgkmcnt(12)
global_store_dwordx4 v16, v[230:233], s[38:39] sc1
v_mfma_f32_16x16x32_bf16 v[122:125], a[0:3], v[44:47], 0
s_waitcnt lgkmcnt(9)
ds_read_b128 v[166:169], v51 offset:24576
v_add_u32 v16, 18432, v16
v_mfma_f32_16x16x32_bf16 v[134:137], a[224:227], v[106:109], v[134:137]
s_waitcnt lgkmcnt(8)
ds_read_b128 v[170:173], v52 offset:24576
v_fma_f32 v238, v90, v23, v114
v_mfma_f32_16x16x32_bf16 v[134:137], a[228:231], v[0:3], v[134:137]
v_fma_f32 v239, v91, v23, v115
v_fma_f32 v240, v92, v23, v116
v_mfma_f32_16x16x32_bf16 v[126:129], a[4:7], v[44:47], 0
s_waitcnt lgkmcnt(5)
ds_read_b128 v[174:177], v51 offset:26624
v_fma_f32 v241, v93, v23, v117
v_mfma_f32_16x16x32_bf16 v[134:137], a[232:235], v[4:7], v[134:137]
s_waitcnt lgkmcnt(4)
ds_read_b128 v[178:181], v52 offset:26624
v_cvt_pk_bf16_f32 v146, v238, v239
v_mfma_f32_16x16x32_bf16 v[134:137], a[236:239], v[162:165], v[134:137]
v_cvt_pk_bf16_f32 v147, v240, v241
v_max_f32 v48, v218, v219
v_mfma_f32_16x16x32_bf16 v[130:133], v[44:47], a[8:11], 0
ds_read_b128 v[182:185], v51 offset:28672
v_mfma_f32_16x16x32_bf16 v[214:217], v[146:149], v[142:145], v[234:237]
s_waitcnt lgkmcnt(4)
v_max_f32 v49, v220, v221
v_fma_f32 v238, v94, v226, v118
v_mfma_f32_16x16x32_bf16 v[134:137], a[240:243], v[166:169], v[134:137]
s_waitcnt lgkmcnt(3)
ds_read_b128 v[186:189], v52 offset:28672
v_fma_f32 v239, v94, v227, v119
v_mfma_f32_16x16x32_bf16 v[134:137], a[244:247], v[170:173], v[134:137]
v_mul_f32 v26, 0x3eae1a93, v26
v_mul_f32 v9, v53, v26
v_mfma_f32_16x16x32_bf16 v[122:125], a[12:15], v[102:105], v[122:125]
s_waitcnt lgkmcnt(3)
ds_read_b128 v[190:193], v51 offset:30720
v_mul_f32 v10, v8, v26
v_mfma_f32_16x16x32_bf16 v[134:137], a[248:251], v[174:177], v[134:137]
s_add_u32 m0, s49, 18432
ds_read_b128 v[194:197], v52 offset:30720
s_waitcnt lgkmcnt(4)
v_mfma_f32_16x16x32_bf16 v[134:137], a[252:255], v[178:181], v[134:137]
global_load_lds_dwordx4 v11, s[42:43] offset:-2048 sc1
v_max3_f32 v48, v214, v215, v48
v_mfma_f32_16x16x32_bf16 v[126:129], a[16:19], v[102:105], v[126:129]
s_waitcnt lgkmcnt(3)
ds_read_b128 v[198:201], v51 offset:32768
v_max3_f32 v49, v216, v217, v49
v_mfma_f32_16x16x32_bf16 v[134:137], v[54:57], v[182:185], v[134:137]
s_waitcnt lgkmcnt(3)
ds_read_b128 v[202:205], v52 offset:32768
v_mul_f32 v48, v48, v9
v_mfma_f32_16x16x32_bf16 v[134:137], v[58:61], v[186:189], v[134:137]
v_mul_f32 v49, v49, v10
v_max_f32 v50, v48, v49
v_mfma_f32_16x16x32_bf16 v[130:133], v[102:105], a[20:23], v[130:133]
s_waitcnt lgkmcnt(3)
ds_read_b128 v[206:209], v51 offset:34816
v_mov_b32 v95, v50
v_mfma_f32_16x16x32_bf16 v[134:137], v[62:65], v[190:193], v[134:137]
s_waitcnt lgkmcnt(3)
ds_read_b128 v[210:213], v52 offset:34816
v_fma_f32 v240, v94, v228, v120
v_mfma_f32_16x16x32_bf16 v[134:137], v[66:69], v[194:197], v[134:137]
v_permlane16_swap_b32 v50, v95
v_mfma_f32_16x16x32_bf16 v[122:125], a[24:27], v[106:109], v[122:125]
s_waitcnt lgkmcnt(3)
v_max_f32 v50, v50, v95
v_mov_b32 v95, v50
v_mfma_f32_16x16x32_bf16 v[134:137], v[70:73], v[198:201], v[134:137]
s_waitcnt lgkmcnt(2)
global_load_lds_dwordx4 v12, s[42:43] offset:-1024 sc1
v_fma_f32 v241, v94, v229, v121
v_mfma_f32_16x16x32_bf16 v[134:137], v[74:77], v[202:205], v[134:137]
v_permlane32_swap_b32 v50, v95
v_mfma_f32_16x16x32_bf16 v[126:129], a[28:31], v[106:109], v[126:129]
s_waitcnt lgkmcnt(1)
v_max_f32 v50, v50, v95
v_sub_f32 v48, v48, v50
v_mfma_f32_16x16x32_bf16 v[134:137], v[78:81], v[206:209], v[134:137]
s_waitcnt lgkmcnt(0)
v_sub_f32 v49, v49, v50
v_cvt_pk_bf16_f32 v154, v238, v239
v_mfma_f32_16x16x32_bf16 v[134:137], v[82:85], v[210:213], v[134:137]
v_exp_f32 v96, v48
v_mfma_f32_16x16x32_bf16 v[130:133], v[106:109], a[32:35], v[130:133]
v_exp_f32 v97, v49
v_mfma_f32_16x16x32_bf16 v[122:125], a[36:39], v[0:3], v[122:125]
v_add_f32 v50, v96, v97
v_mov_b32 v95, v50
v_mfma_f32_16x16x32_bf16 v[126:129], a[40:43], v[0:3], v[126:129]
v_mul_f32 v96, v96, v53
v_cvt_pk_bf16_f32 v155, v240, v241
v_mfma_f32_16x16x32_bf16 v[130:133], v[0:3], a[44:47], v[130:133]
global_load_lds_dwordx4 v13, s[42:43] offset:0 sc1
v_fma_f32 v238, v86, v23, v110
v_mfma_f32_16x16x32_bf16 v[122:125], a[48:51], v[4:7], v[122:125]
v_permlane16_swap_b32 v50, v95
v_mfma_f32_16x16x32_bf16 v[126:129], a[52:55], v[4:7], v[126:129]
v_add_f32 v50, v50, v95
v_mov_b32 v95, v50
v_mfma_f32_16x16x32_bf16 v[130:133], v[4:7], a[56:59], v[130:133]
v_mul_f32 v97, v97, v8
v_fma_f32 v239, v87, v23, v111
v_mfma_f32_16x16x32_bf16 v[122:125], a[60:63], v[162:165], v[122:125]
v_permlane32_swap_b32 v50, v95
v_mfma_f32_16x16x32_bf16 v[126:129], a[64:67], v[162:165], v[126:129]
v_add_f32 v50, v50, v95
v_fma_f32 v240, v88, v23, v112
v_mfma_f32_16x16x32_bf16 v[130:133], v[162:165], a[68:71], v[130:133]
v_mfma_f32_16x16x32_bf16 v[122:125], a[72:75], v[166:169], v[122:125]
s_barrier
v_mfma_f32_16x16x32_bf16 v[126:129], a[76:79], v[166:169], v[126:129]
global_load_lds_dwordx4 v14, s[42:43] offset:1024 sc1
ds_read_b32 v242, v24 offset:64
v_mfma_f32_16x16x32_bf16 v[130:133], v[166:169], a[80:83], v[130:133]
v_rcp_f32 v50, v50
v_mfma_f32_16x16x32_bf16 v[122:125], a[84:87], v[170:173], v[122:125]
v_mul_f32 v96, v96, v50
v_mul_f32 v97, v97, v50
v_mfma_f32_16x16x32_bf16 v[126:129], a[88:91], v[170:173], v[126:129]
v_cvt_pk_bf16_f32 v96, v96, v96
v_cvt_pk_bf16_f32 v97, v97, v97
v_mfma_f32_16x16x32_bf16 v[130:133], v[170:173], a[92:95], v[130:133]
v_and_b32 v222, v96, v40
v_and_b32 v223, v97, v41
v_mfma_f32_16x16x32_bf16 v[122:125], a[96:99], v[174:177], v[122:125]
v_and_b32 v224, v96, v42
v_and_b32 v225, v97, v43
v_mfma_f32_16x16x32_bf16 v[126:129], a[100:103], v[174:177], v[126:129]
s_nop 0
v_mfma_f32_16x16x32_bf16 v[218:221], v[154:157], v[222:225], 0
v_mfma_f32_16x16x32_bf16 v[214:217], v[158:161], v[222:225], 0
s_waitcnt lgkmcnt(0)
v_fma_f32 v230, v98, v242, v134
v_fma_f32 v231, v99, v242, v135
v_mfma_f32_16x16x32_bf16 v[130:133], v[174:177], a[104:107], v[130:133]
v_fma_f32 v232, v100, v242, v136
v_fma_f32 v233, v101, v242, v137
v_mfma_f32_16x16x32_bf16 v[122:125], a[108:111], v[178:181], v[122:125]
global_load_lds_dwordx4 v15, s[42:43] offset:2048 sc1
s_sub_u32 s42, s42, 18432
s_subb_u32 s43, s43, 0
v_mfma_f32_16x16x32_bf16 v[126:129], a[112:115], v[178:181], v[126:129]
v_cvt_pk_bf16_f32 v230, v230, v231
v_cvt_pk_bf16_f32 v231, v232, v233
v_mfma_f32_16x16x32_bf16 v[130:133], v[178:181], a[116:119], v[130:133]
v_cvt_pk_bf16_f32 v254, v218, v219
v_cvt_pk_bf16_f32 v255, v220, v221
v_mfma_f32_16x16x32_bf16 v[122:125], a[120:123], v[182:185], v[122:125]
ds_write_b64 v18, v[254:255]
v_mfma_f32_16x16x32_bf16 v[126:129], a[124:127], v[182:185], v[126:129]
v_cvt_pk_bf16_f32 v96, v214, v215
v_fma_f32 v241, v89, v23, v113
v_mfma_f32_16x16x32_bf16 v[130:133], v[182:185], a[128:131], v[130:133]
ds_write_b32 v19, v96
v_mfma_f32_16x16x32_bf16 v[122:125], a[132:135], v[186:189], v[122:125]
ds_write_b64 v22, v[230:231] offset:40960
v_mfma_f32_16x16x32_bf16 v[126:129], a[136:139], v[186:189], v[126:129]
v_cvt_pk_bf16_f32 v138, v238, v239
v_cvt_pk_bf16_f32 v139, v240, v241
v_mfma_f32_16x16x32_bf16 v[130:133], v[186:189], a[140:143], v[130:133]
v_mfma_f32_16x16x32_bf16 v[122:125], a[144:147], v[190:193], v[122:125]
v_mfma_f32_16x16x32_bf16 v[126:129], a[148:151], v[190:193], v[126:129]
v_mfma_f32_16x16x32_bf16 v[130:133], v[190:193], a[152:155], v[130:133]
v_mfma_f32_16x16x32_bf16 v[122:125], a[156:159], v[194:197], v[122:125]
v_mfma_f32_16x16x32_bf16 v[126:129], a[160:163], v[194:197], v[126:129]
v_mfma_f32_16x16x32_bf16 v[130:133], v[194:197], a[164:167], v[130:133]
v_mfma_f32_16x16x32_bf16 v[122:125], a[168:171], v[198:201], v[122:125]
v_mfma_f32_16x16x32_bf16 v[126:129], a[172:175], v[198:201], v[126:129]
v_xor_b32 v51, 0x10000, v51
v_xor_b32 v52, 0x10000, v52
v_mfma_f32_16x16x32_bf16 v[130:133], v[198:201], a[176:179], v[130:133]
v_mfma_f32_16x16x32_bf16 v[122:125], a[180:183], v[202:205], v[122:125]
v_mfma_f32_16x16x32_bf16 v[126:129], a[184:187], v[202:205], v[126:129]
s_waitcnt vmcnt(5) lgkmcnt(0)
s_barrier
.Lqa_in5:
v_mfma_f32_16x16x32_bf16 v[130:133], v[202:205], a[188:191], v[130:133]
s_cmp_eq_u32 s70, s73
s_cbranch_scc1 .Lqa_hk6
.Lqa_hr7:
ds_read_b128 v[44:47], v51 offset:0
v_mfma_f32_16x16x32_bf16 v[122:125], a[192:195], v[206:209], v[122:125]
ds_read_b128 v[102:105], v52 offset:0
ds_read_b128 v[230:233], v17 offset:0
v_mfma_f32_16x16x32_bf16 v[126:129], a[196:199], v[206:209], v[126:129]
ds_read_b32 v23, v24 offset:64
ds_read_b32 v152, v21
v_mfma_f32_16x16x32_bf16 v[218:221], v[146:149], v[138:141], v[36:39]
v_mfma_f32_16x16x32_bf16 v[130:133], v[206:209], a[200:203], v[130:133]
ds_read_b128 v[106:109], v51 offset:2048
ds_read_b32 v26, v24 offset:128
v_mfma_f32_16x16x32_bf16 v[122:125], a[204:207], v[210:213], v[122:125]
ds_read_b128 v[0:3], v52 offset:2048
ds_read_b32 v53, v29
v_mfma_f32_16x16x32_bf16 v[126:129], a[208:211], v[210:213], v[126:129]
v_mfma_f32_16x16x32_bf16 v[130:133], v[210:213], a[212:215], v[130:133]
s_sub_u32 s70, s70, 1
ds_read_b128 v[4:7], v51 offset:4096
s_cbranch_scc0 .Lqa_top16
s_waitcnt lgkmcnt(0)
ds_read_b32 v8, v30
global_store_dwordx4 v16, v[230:233], s[38:39] sc1
v_add_u32 v16, 18432, v16
ds_read_b128 v[226:229], v25 offset:64
ds_read_b64 v[160:161], v22 offset:41472
ds_read_b32 v144, v20
v_fma_f32 v238, v90, v23, v126
v_fma_f32 v239, v91, v23, v127
v_fma_f32 v240, v92, v23, v128
v_fma_f32 v241, v93, v23, v129
v_cvt_pk_bf16_f32 v150, v238, v239
v_cvt_pk_bf16_f32 v151, v240, v241
s_nop 1
v_mfma_f32_16x16x32_bf16 v[214:217], v[150:153], v[138:141], v[32:35]
s_waitcnt lgkmcnt(2)
v_max_f32 v48, v218, v219
v_max_f32 v49, v220, v221
v_fma_f32 v238, v94, v226, v130
v_mul_f32 v26, 0x3eae1a93, v26
v_mul_f32 v9, v53, v26
v_mul_f32 v10, v8, v26
s_nop 0
v_max3_f32 v48, v214, v215, v48
v_max3_f32 v49, v216, v217, v49
v_mul_f32 v48, v48, v9
v_mul_f32 v49, v49, v10
v_max_f32 v50, v48, v49
v_mov_b32 v95, v50
s_nop 1
v_permlane16_swap_b32 v50, v95
v_max_f32 v50, v50, v95
v_mov_b32 v95, v50
v_fma_f32 v239, v94, v227, v131
s_nop 0
v_permlane32_swap_b32 v50, v95
v_max_f32 v50, v50, v95
v_sub_f32 v48, v48, v50
v_sub_f32 v49, v49, v50
v_fma_f32 v240, v94, v228, v132
v_exp_f32 v96, v48
v_exp_f32 v97, v49
s_nop 0
v_add_f32 v50, v96, v97
v_mov_b32 v95, v50
v_mul_f32 v96, v96, v53
v_fma_f32 v241, v94, v229, v133
v_cvt_pk_bf16_f32 v156, v238, v239
v_permlane16_swap_b32 v50, v95
v_add_f32 v50, v50, v95
v_mov_b32 v95, v50
v_mul_f32 v97, v97, v8
v_cvt_pk_bf16_f32 v157, v240, v241
v_permlane32_swap_b32 v50, v95
s_waitcnt lgkmcnt(0)
s_barrier
v_add_f32 v50, v50, v95
v_fma_f32 v238, v86, v23, v122
v_rcp_f32 v50, v50
s_nop 0
v_mul_f32 v96, v96, v50
v_mul_f32 v97, v97, v50
v_cvt_pk_bf16_f32 v96, v96, v96
v_cvt_pk_bf16_f32 v97, v97, v97
v_and_b32 v224, v96, v40
v_and_b32 v225, v97, v41
v_and_b32 v222, v96, v42
v_and_b32 v223, v97, v43
s_nop 1
v_mfma_f32_16x16x32_bf16 v[218:221], v[154:157], v[222:225], 0
v_mfma_f32_16x16x32_bf16 v[214:217], v[158:161], v[222:225], 0
s_nop 6
v_cvt_pk_bf16_f32 v254, v218, v219
v_cvt_pk_bf16_f32 v255, v220, v221
ds_write_b64 v18, v[254:255]
v_cvt_pk_bf16_f32 v96, v214, v215
v_fma_f32 v239, v87, v23, v123
ds_write_b32 v19, v96
v_fma_f32 v240, v88, v23, v124
v_fma_f32 v241, v89, v23, v125
v_cvt_pk_bf16_f32 v142, v238, v239
v_cvt_pk_bf16_f32 v143, v240, v241
s_waitcnt vmcnt(0)
s_waitcnt lgkmcnt(0)
s_barrier
ds_read_b128 v[230:233], v17 offset:0
s_waitcnt lgkmcnt(0)
global_store_dwordx4 v16, v[230:233], s[38:39] sc1
s_waitcnt vmcnt(0)
s_barrier
s_endpgm
.Lqa_w2:
s_mov_b32 s70, 500
s_branch .Lqa_hk19
.Lqa_bk28:
v_add_u32 v255, 0x10000, v9
v_add_u32 v162, 0x20000, v9
v_add_u32 v163, 0x10000, v10
v_add_u32 v164, 0x20000, v10
v_mov_b32 v106, 0x3F803F80
v_mov_b32 v107, 0x3F803F80
v_mov_b32 v108, 0x3F803F80
v_mov_b32 v109, 0x3F803F80
v_mov_b32 v141, 0
v_mov_b32 v145, 0
v_mov_b32 v149, 0
v_mov_b32 v153, 0
v_mov_b32 v165, 46080
s_waitcnt lgkmcnt(0)
s_mul_i32 s37, s37, 0x9E3779B1
)"
R"(s_add_u32 s37, s37, 0x7F4A7C15
v_add_u32 v46, s37, v46
v_add_u32 v48, s37, v48
v_add_u32 v50, s37, v50
ds_write_b32 v165, v141
s_mov_b64 s[76:77], 0
s_mov_b32 s73, 18
s_add_u32 m0, s48, 4096
s_nop 0
global_load_lds_dwordx4 v0, s[40:41] offset:-4096
global_load_lds_dwordx4 v1, s[40:41] offset:-3072
global_load_lds_dwordx4 v2, s[40:41] offset:-2048
global_load_lds_dwordx4 v3, s[40:41] offset:-1024
global_load_lds_dwordx4 v4, s[40:41] offset:0
global_load_lds_dwordx4 v5, s[40:41] offset:1024
global_load_lds_dwordx4 v6, s[40:41] offset:2048
global_load_lds_dwordx4 v7, s[40:41] offset:3072
s_add_u32 m0, s48, 12288
s_nop 0
global_load_lds_dwordx4 v8, s[40:41] offset:-4096
s_add_u32 s40, s40, 221184
s_addc_u32 s41, s41, 0
s_add_u32 m0, s48, 40960
s_nop 0
global_load_lds_dwordx4 v0, s[40:41] offset:-4096
global_load_lds_dwordx4 v1, s[40:41] offset:-3072
global_load_lds_dwordx4 v2, s[40:41] offset:-2048
global_load_lds_dwordx4 v3, s[40:41] offset:-1024
global_load_lds_dwordx4 v4, s[40:41] offset:0
global_load_lds_dwordx4 v5, s[40:41] offset:1024
global_load_lds_dwordx4 v6, s[40:41] offset:2048
global_load_lds_dwordx4 v7, s[40:41] offset:3072
s_add_u32 m0, s48, 49152
s_nop 0
global_load_lds_dwordx4 v8, s[40:41] offset:-4096
s_add_u32 s40, s40, 221184
s_addc_u32 s41, s41, 0
s_waitcnt vmcnt(9) lgkmcnt(0)
s_barrier
s_add_u32 m0, s48, 77824
ds_read_b64_tr_b16 a[0:1], v9 offset:47104
ds_read_b64_tr_b16 a[2:3], v9 offset:49408
global_load_lds_dwordx4 v0, s[40:41] offset:-4096
ds_read_b64_tr_b16 a[4:5], v9 offset:47296
ds_read_b64_tr_b16 a[6:7], v9 offset:49600
global_load_lds_dwordx4 v1, s[40:41] offset:-3072
ds_read_b64_tr_b16 a[8:9], v9 offset:47488
global_load_lds_dwordx4 v2, s[40:41] offset:-2048
ds_read_b64_tr_b16 a[10:11], v9 offset:49792
ds_read_b64_tr_b16 a[216:217], v10 offset:47104
global_load_lds_dwordx4 v3, s[40:41] offset:-1024
ds_read_b64_tr_b16 a[218:219], v10 offset:49408
global_load_lds_dwordx4 v4, s[40:41] offset:0
ds_read_b128 v[166:169], v51 offset:0
ds_read_b128 v[174:177], v51 offset:18432
global_load_lds_dwordx4 v5, s[40:41] offset:1024
ds_read_b64_tr_b16 a[12:13], v255 offset:0
global_load_lds_dwordx4 v6, s[40:41] offset:2048
ds_read_b64_tr_b16 a[14:15], v255 offset:2304
ds_read_b64_tr_b16 a[16:17], v255 offset:192
global_load_lds_dwordx4 v7, s[40:41] offset:3072
ds_read_b64_tr_b16 a[18:19], v255 offset:2496
s_add_u32 m0, s48, 86016
ds_read_b64_tr_b16 a[20:21], v255 offset:384
ds_read_b64_tr_b16 a[22:23], v255 offset:2688
global_load_lds_dwordx4 v8, s[40:41] offset:-4096
ds_read_b64_tr_b16 a[220:221], v163 offset:0
s_add_u32 s40, s40, 221184
ds_read_b64_tr_b16 a[222:223], v163 offset:2304
ds_read_b128 v[170:173], v52 offset:0
s_addc_u32 s41, s41, 0
ds_read_b128 v[178:181], v52 offset:18432
s_waitcnt vmcnt(9) lgkmcnt(0)
s_barrier
s_add_u32 m0, s48, 4096
ds_read_b64_tr_b16 a[24:25], v255 offset:18432
v_mfma_f32_16x16x32_bf16 v[86:89], a[0:3], v[106:109], 0
ds_read_b64_tr_b16 a[26:27], v255 offset:20736
v_mfma_f32_16x16x32_bf16 v[90:93], a[4:7], v[106:109], 0
global_load_lds_dwordx4 v0, s[40:41] offset:-4096
ds_read_b64_tr_b16 a[28:29], v255 offset:18624
v_mfma_f32_16x16x32_bf16 v[94:97], v[106:109], a[8:11], 0
ds_read_b64_tr_b16 a[30:31], v255 offset:20928
global_load_lds_dwordx4 v1, s[40:41] offset:-3072
v_mfma_f32_16x16x32_bf16 v[102:105], v[106:109], a[216:219], 0
ds_read_b64_tr_b16 a[32:33], v255 offset:18816
v_mfma_f32_16x16x32_bf16 v[110:113], a[0:3], v[166:169], 0
ds_read_b64_tr_b16 a[34:35], v255 offset:21120
global_load_lds_dwordx4 v2, s[40:41] offset:-2048
v_mfma_f32_16x16x32_bf16 v[114:117], a[4:7], v[166:169], 0
ds_read_b64_tr_b16 a[224:225], v163 offset:18432
v_mfma_f32_16x16x32_bf16 v[118:121], v[166:169], a[8:11], 0
global_load_lds_dwordx4 v3, s[40:41] offset:-1024
ds_read_b64_tr_b16 a[226:227], v163 offset:20736
v_mfma_f32_16x16x32_bf16 v[134:137], v[166:169], a[216:219], 0
ds_read_b128 v[182:185], v51 offset:2048
global_load_lds_dwordx4 v4, s[40:41] offset:0
v_mfma_f32_16x16x32_bf16 v[122:125], a[0:3], v[174:177], 0
ds_read_b128 v[190:193], v51 offset:20480
v_mfma_f32_16x16x32_bf16 v[126:129], a[4:7], v[174:177], 0
ds_read_b64_tr_b16 a[36:37], v255 offset:36864
global_load_lds_dwordx4 v5, s[40:41] offset:1024
v_mfma_f32_16x16x32_bf16 v[130:133], v[174:177], a[8:11], 0
ds_read_b64_tr_b16 a[38:39], v255 offset:39168
v_mfma_f32_16x16x32_bf16 v[158:161], v[174:177], a[216:219], 0
global_load_lds_dwordx4 v6, s[40:41] offset:2048
ds_read_b64_tr_b16 a[40:41], v255 offset:37056
v_mfma_f32_16x16x32_bf16 v[86:89], a[12:15], v[106:109], v[86:89]
ds_read_b64_tr_b16 a[42:43], v255 offset:39360
v_mfma_f32_16x16x32_bf16 v[90:93], a[16:19], v[106:109], v[90:93]
global_load_lds_dwordx4 v7, s[40:41] offset:3072
ds_read_b64_tr_b16 a[44:45], v255 offset:37248
v_mfma_f32_16x16x32_bf16 v[94:97], v[106:109], a[20:23], v[94:97]
s_add_u32 m0, s48, 12288
ds_read_b64_tr_b16 a[46:47], v255 offset:39552
v_mfma_f32_16x16x32_bf16 v[102:105], v[106:109], a[220:223], v[102:105]
ds_read_b64_tr_b16 a[228:229], v163 offset:36864
v_mfma_f32_16x16x32_bf16 v[110:113], a[12:15], v[170:173], v[110:113]
global_load_lds_dwordx4 v8, s[40:41] offset:-4096
ds_read_b64_tr_b16 a[230:231], v163 offset:39168
v_mfma_f32_16x16x32_bf16 v[114:117], a[16:19], v[170:173], v[114:117]
ds_read_b128 v[186:189], v52 offset:2048
v_mfma_f32_16x16x32_bf16 v[118:121], v[170:173], a[20:23], v[118:121]
s_add_u32 s40, s40, 221184
ds_read_b128 v[194:197], v52 offset:20480
v_mfma_f32_16x16x32_bf16 v[134:137], v[170:173], a[220:223], v[134:137]
v_mfma_f32_16x16x32_bf16 v[122:125], a[12:15], v[178:181], v[122:125]
s_addc_u32 s41, s41, 0
v_mfma_f32_16x16x32_bf16 v[126:129], a[16:19], v[178:181], v[126:129]
v_mfma_f32_16x16x32_bf16 v[130:133], v[178:181], a[20:23], v[130:133]
v_mfma_f32_16x16x32_bf16 v[158:161], v[178:181], a[220:223], v[158:161]
s_waitcnt vmcnt(9) lgkmcnt(0)
s_barrier
s_add_u32 m0, s48, 40960
ds_read_b64_tr_b16 a[48:49], v255 offset:55296
v_mfma_f32_16x16x32_bf16 v[86:89], a[24:27], v[106:109], v[86:89]
ds_read_b64_tr_b16 a[50:51], v255 offset:57600
v_mfma_f32_16x16x32_bf16 v[90:93], a[28:31], v[106:109], v[90:93]
global_load_lds_dwordx4 v0, s[40:41] offset:-4096
ds_read_b64_tr_b16 a[52:53], v255 offset:55488
v_mfma_f32_16x16x32_bf16 v[94:97], v[106:109], a[32:35], v[94:97]
ds_read_b64_tr_b16 a[54:55], v255 offset:57792
global_load_lds_dwordx4 v1, s[40:41] offset:-3072
v_mfma_f32_16x16x32_bf16 v[102:105], v[106:109], a[224:227], v[102:105]
ds_read_b64_tr_b16 a[56:57], v255 offset:55680
v_mfma_f32_16x16x32_bf16 v[110:113], a[24:27], v[182:185], v[110:113]
ds_read_b64_tr_b16 a[58:59], v255 offset:57984
global_load_lds_dwordx4 v2, s[40:41] offset:-2048
v_mfma_f32_16x16x32_bf16 v[114:117], a[28:31], v[182:185], v[114:117]
ds_read_b64_tr_b16 a[232:233], v163 offset:55296
v_mfma_f32_16x16x32_bf16 v[118:121], v[182:185], a[32:35], v[118:121]
global_load_lds_dwordx4 v3, s[40:41] offset:-1024
ds_read_b64_tr_b16 a[234:235], v163 offset:57600
v_mfma_f32_16x16x32_bf16 v[134:137], v[182:185], a[224:227], v[134:137]
ds_read_b128 v[166:169], v51 offset:4096
global_load_lds_dwordx4 v4, s[40:41] offset:0
v_mfma_f32_16x16x32_bf16 v[122:125], a[24:27], v[190:193], v[122:125]
ds_read_b128 v[174:177], v51 offset:22528
v_mfma_f32_16x16x32_bf16 v[126:129], a[28:31], v[190:193], v[126:129]
ds_read_b64_tr_b16 a[60:61], v162 offset:8192
global_load_lds_dwordx4 v5, s[40:41] offset:1024
v_mfma_f32_16x16x32_bf16 v[130:133], v[190:193], a[32:35], v[130:133]
ds_read_b64_tr_b16 a[62:63], v162 offset:10496
v_mfma_f32_16x16x32_bf16 v[158:161], v[190:193], a[224:227], v[158:161]
global_load_lds_dwordx4 v6, s[40:41] offset:2048
ds_read_b64_tr_b16 a[64:65], v162 offset:8384
v_mfma_f32_16x16x32_bf16 v[86:89], a[36:39], v[106:109], v[86:89]
ds_read_b64_tr_b16 a[66:67], v162 offset:10688
v_mfma_f32_16x16x32_bf16 v[90:93], a[40:43], v[106:109], v[90:93]
global_load_lds_dwordx4 v7, s[40:41] offset:3072
ds_read_b64_tr_b16 a[68:69], v162 offset:8576
v_mfma_f32_16x16x32_bf16 v[94:97], v[106:109], a[44:47], v[94:97]
s_add_u32 m0, s48, 49152
ds_read_b64_tr_b16 a[70:71], v162 offset:10880
v_mfma_f32_16x16x32_bf16 v[102:105], v[106:109], a[228:231], v[102:105]
ds_read_b64_tr_b16 a[236:237], v164 offset:8192
v_mfma_f32_16x16x32_bf16 v[110:113], a[36:39], v[186:189], v[110:113]
global_load_lds_dwordx4 v8, s[40:41] offset:-4096
ds_read_b64_tr_b16 a[238:239], v164 offset:10496
v_mfma_f32_16x16x32_bf16 v[114:117], a[40:43], v[186:189], v[114:117]
ds_read_b128 v[170:173], v52 offset:4096
v_mfma_f32_16x16x32_bf16 v[118:121], v[186:189], a[44:47], v[118:121]
s_add_u32 s40, s40, 221184
ds_read_b128 v[178:181], v52 offset:22528
v_mfma_f32_16x16x32_bf16 v[134:137], v[186:189], a[228:231], v[134:137]
v_mfma_f32_16x16x32_bf16 v[122:125], a[36:39], v[194:197], v[122:125]
s_addc_u32 s41, s41, 0
v_mfma_f32_16x16x32_bf16 v[126:129], a[40:43], v[194:197], v[126:129]
v_mfma_f32_16x16x32_bf16 v[130:133], v[194:197], a[44:47], v[130:133]
v_mfma_f32_16x16x32_bf16 v[158:161], v[194:197], a[228:231], v[158:161]
s_waitcnt vmcnt(9) lgkmcnt(0)
s_barrier
s_add_u32 m0, s48, 77824
ds_read_b64_tr_b16 a[72:73], v9 offset:47104
v_mfma_f32_16x16x32_bf16 v[86:89], a[48:51], v[106:109], v[86:89]
ds_read_b64_tr_b16 a[74:75], v9 offset:49408
v_mfma_f32_16x16x32_bf16 v[90:93], a[52:55], v[106:109], v[90:93]
global_load_lds_dwordx4 v0, s[40:41] offset:-4096
ds_read_b64_tr_b16 a[76:77], v9 offset:47296
v_mfma_f32_16x16x32_bf16 v[94:97], v[106:109], a[56:59], v[94:97]
ds_read_b64_tr_b16 a[78:79], v9 offset:49600
global_load_lds_dwordx4 v1, s[40:41] offset:-3072
v_mfma_f32_16x16x32_bf16 v[102:105], v[106:109], a[232:235], v[102:105]
ds_read_b64_tr_b16 a[80:81], v9 offset:47488
v_mfma_f32_16x16x32_bf16 v[110:113], a[48:51], v[166:169], v[110:113]
ds_read_b64_tr_b16 a[82:83], v9 offset:49792
global_load_lds_dwordx4 v2, s[40:41] offset:-2048
v_mfma_f32_16x16x32_bf16 v[114:117], a[52:55], v[166:169], v[114:117]
ds_read_b64_tr_b16 a[240:241], v10 offset:47104
v_mfma_f32_16x16x32_bf16 v[118:121], v[166:169], a[56:59], v[118:121]
global_load_lds_dwordx4 v3, s[40:41] offset:-1024
ds_read_b64_tr_b16 a[242:243], v10 offset:49408
v_mfma_f32_16x16x32_bf16 v[134:137], v[166:169], a[232:235], v[134:137]
ds_read_b128 v[182:185], v51 offset:6144
global_load_lds_dwordx4 v4, s[40:41] offset:0
v_mfma_f32_16x16x32_bf16 v[122:125], a[48:51], v[174:177], v[122:125]
ds_read_b128 v[190:193], v51 offset:24576
v_mfma_f32_16x16x32_bf16 v[126:129], a[52:55], v[174:177], v[126:129]
ds_read_b64_tr_b16 a[84:85], v255 offset:0
global_load_lds_dwordx4 v5, s[40:41] offset:1024
v_mfma_f32_16x16x32_bf16 v[130:133], v[174:177], a[56:59], v[130:133]
ds_read_b64_tr_b16 a[86:87], v255 offset:2304
v_mfma_f32_16x16x32_bf16 v[158:161], v[174:177], a[232:235], v[158:161]
global_load_lds_dwordx4 v6, s[40:41] offset:2048
ds_read_b64_tr_b16 a[88:89], v255 offset:192
v_mfma_f32_16x16x32_bf16 v[86:89], a[60:63], v[106:109], v[86:89]
ds_read_b64_tr_b16 a[90:91], v255 offset:2496
v_mfma_f32_16x16x32_bf16 v[90:93], a[64:67], v[106:109], v[90:93]
global_load_lds_dwordx4 v7, s[40:41] offset:3072
ds_read_b64_tr_b16 a[92:93], v255 offset:384
v_mfma_f32_16x16x32_bf16 v[94:97], v[106:109], a[68:71], v[94:97]
s_add_u32 m0, s48, 86016
ds_read_b64_tr_b16 a[94:95], v255 offset:2688
v_mfma_f32_16x16x32_bf16 v[102:105], v[106:109], a[236:239], v[102:105]
ds_read_b64_tr_b16 a[244:245], v163 offset:0
v_mfma_f32_16x16x32_bf16 v[110:113], a[60:63], v[170:173], v[110:113]
global_load_lds_dwordx4 v8, s[40:41] offset:-4096
ds_read_b64_tr_b16 a[246:247], v163 offset:2304
v_mfma_f32_16x16x32_bf16 v[114:117], a[64:67], v[170:173], v[114:117]
ds_read_b128 v[186:189], v52 offset:6144
v_mfma_f32_16x16x32_bf16 v[118:121], v[170:173], a[68:71], v[118:121]
s_add_u32 s40, s40, 221184
ds_read_b128 v[194:197], v52 offset:24576
v_mfma_f32_16x16x32_bf16 v[134:137], v[170:173], a[236:239], v[134:137]
v_mfma_f32_16x16x32_bf16 v[122:125], a[60:63], v[178:181], v[122:125]
s_addc_u32 s41, s41, 0
v_mfma_f32_16x16x32_bf16 v[126:129], a[64:67], v[178:181], v[126:129]
v_mfma_f32_16x16x32_bf16 v[130:133], v[178:181], a[68:71], v[130:133]
v_mfma_f32_16x16x32_bf16 v[158:161], v[178:181], a[236:239], v[158:161]
s_waitcnt vmcnt(9) lgkmcnt(0)
s_barrier
s_add_u32 m0, s48, 4096
ds_read_b64_tr_b16 a[96:97], v255 offset:18432
v_mfma_f32_16x16x32_bf16 v[86:89], a[72:75], v[106:109], v[86:89]
ds_read_b64_tr_b16 a[98:99], v255 offset:20736
v_mfma_f32_16x16x32_bf16 v[90:93], a[76:79], v[106:109], v[90:93]
global_load_lds_dwordx4 v0, s[40:41] offset:-4096
ds_read_b64_tr_b16 a[100:101], v255 offset:18624
v_mfma_f32_16x16x32_bf16 v[94:97], v[106:109], a[80:83], v[94:97]
ds_read_b64_tr_b16 a[102:103], v255 offset:20928
global_load_lds_dwordx4 v1, s[40:41] offset:-3072
v_mfma_f32_16x16x32_bf16 v[102:105], v[106:109], a[240:243], v[102:105]
ds_read_b64_tr_b16 a[104:105], v255 offset:18816
v_mfma_f32_16x16x32_bf16 v[110:113], a[72:75], v[182:185], v[110:113]
ds_read_b64_tr_b16 a[106:107], v255 offset:21120
global_load_lds_dwordx4 v2, s[40:41] offset:-2048
v_mfma_f32_16x16x32_bf16 v[114:117], a[76:79], v[182:185], v[114:117]
ds_read_b64_tr_b16 a[248:249], v163 offset:18432
v_mfma_f32_16x16x32_bf16 v[118:121], v[182:185], a[80:83], v[118:121]
global_load_lds_dwordx4 v3, s[40:41] offset:-1024
ds_read_b64_tr_b16 a[250:251], v163 offset:20736
v_mfma_f32_16x16x32_bf16 v[134:137], v[182:185], a[240:243], v[134:137]
ds_read_b128 v[166:169], v51 offset:8192
global_load_lds_dwordx4 v4, s[40:41] offset:0
v_mfma_f32_16x16x32_bf16 v[122:125], a[72:75], v[190:193], v[122:125]
ds_read_b128 v[174:177], v51 offset:26624
v_mfma_f32_16x16x32_bf16 v[126:129], a[76:79], v[190:193], v[126:129]
ds_read_b64_tr_b16 a[108:109], v255 offset:36864
global_load_lds_dwordx4 v5, s[40:41] offset:1024
v_mfma_f32_16x16x32_bf16 v[130:133], v[190:193], a[80:83], v[130:133]
ds_read_b64_tr_b16 a[110:111], v255 offset:39168
v_mfma_f32_16x16x32_bf16 v[158:161], v[190:193], a[240:243], v[158:161]
global_load_lds_dwordx4 v6, s[40:41] offset:2048
ds_read_b64_tr_b16 a[112:113], v255 offset:37056
v_mfma_f32_16x16x32_bf16 v[86:89], a[84:87], v[106:109], v[86:89]
ds_read_b64_tr_b16 a[114:115], v255 offset:39360
v_mfma_f32_16x16x32_bf16 v[90:93], a[88:91], v[106:109], v[90:93]
global_load_lds_dwordx4 v7, s[40:41] offset:3072
ds_read_b64_tr_b16 a[116:117], v255 offset:37248
v_mfma_f32_16x16x32_bf16 v[94:97], v[106:109], a[92:95], v[94:97]
s_add_u32 m0, s48, 12288
ds_read_b64_tr_b16 a[118:119], v255 offset:39552
v_mfma_f32_16x16x32_bf16 v[102:105], v[106:109], a[244:247], v[102:105]
ds_read_b64_tr_b16 a[252:253], v163 offset:36864
v_mfma_f32_16x16x32_bf16 v[110:113], a[84:87], v[186:189], v[110:113]
global_load_lds_dwordx4 v8, s[40:41] offset:-4096
ds_read_b64_tr_b16 a[254:255], v163 offset:39168
v_mfma_f32_16x16x32_bf16 v[114:117], a[88:91], v[186:189], v[114:117]
ds_read_b128 v[170:173], v52 offset:8192
v_mfma_f32_16x16x32_bf16 v[118:121], v[186:189], a[92:95], v[118:121]
s_add_u32 s40, s40, 221184
ds_read_b128 v[178:181], v52 offset:26624
v_mfma_f32_16x16x32_bf16 v[134:137], v[186:189], a[244:247], v[134:137]
v_mfma_f32_16x16x32_bf16 v[122:125], a[84:87], v[194:197], v[122:125]
s_addc_u32 s41, s41, 0
v_mfma_f32_16x16x32_bf16 v[126:129], a[88:91], v[194:197], v[126:129]
v_mfma_f32_16x16x32_bf16 v[130:133], v[194:197], a[92:95], v[130:133]
v_mfma_f32_16x16x32_bf16 v[158:161], v[194:197], a[244:247], v[158:161]
s_waitcnt vmcnt(9) lgkmcnt(0)
s_barrier
s_add_u32 m0, s48, 40960
ds_read_b64_tr_b16 a[120:121], v255 offset:55296
v_mfma_f32_16x16x32_bf16 v[86:89], a[96:99], v[106:109], v[86:89]
ds_read_b64_tr_b16 a[122:123], v255 offset:57600
v_mfma_f32_16x16x32_bf16 v[90:93], a[100:103], v[106:109], v[90:93]
global_load_lds_dwordx4 v0, s[40:41] offset:-4096
ds_read_b64_tr_b16 a[124:125], v255 offset:55488
v_mfma_f32_16x16x32_bf16 v[94:97], v[106:109], a[104:107], v[94:97]
ds_read_b64_tr_b16 a[126:127], v255 offset:57792
global_load_lds_dwordx4 v1, s[40:41] offset:-3072
v_mfma_f32_16x16x32_bf16 v[102:105], v[106:109], a[248:251], v[102:105]
ds_read_b64_tr_b16 a[128:129], v255 offset:55680
v_mfma_f32_16x16x32_bf16 v[110:113], a[96:99], v[166:169], v[110:113]
ds_read_b64_tr_b16 a[130:131], v255 offset:57984
global_load_lds_dwordx4 v2, s[40:41] offset:-2048
v_mfma_f32_16x16x32_bf16 v[114:117], a[100:103], v[166:169], v[114:117]
ds_read_b64_tr_b16 v[54:55], v163 offset:55296
v_mfma_f32_16x16x32_bf16 v[118:121], v[166:169], a[104:107], v[118:121]
global_load_lds_dwordx4 v3, s[40:41] offset:-1024
ds_read_b64_tr_b16 v[56:57], v163 offset:57600
v_mfma_f32_16x16x32_bf16 v[134:137], v[166:169], a[248:251], v[134:137]
ds_read_b128 v[182:185], v51 offset:10240
global_load_lds_dwordx4 v4, s[40:41] offset:0
v_mfma_f32_16x16x32_bf16 v[122:125], a[96:99], v[174:177], v[122:125]
ds_read_b128 v[190:193], v51 offset:28672
v_mfma_f32_16x16x32_bf16 v[126:129], a[100:103], v[174:177], v[126:129]
ds_read_b64_tr_b16 a[132:133], v162 offset:8192
global_load_lds_dwordx4 v5, s[40:41] offset:1024
v_mfma_f32_16x16x32_bf16 v[130:133], v[174:177], a[104:107], v[130:133]
ds_read_b64_tr_b16 a[134:135], v162 offset:10496
v_mfma_f32_16x16x32_bf16 v[158:161], v[174:177], a[248:251], v[158:161]
global_load_lds_dwordx4 v6, s[40:41] offset:2048
ds_read_b64_tr_b16 a[136:137], v162 offset:8384
v_mfma_f32_16x16x32_bf16 v[86:89], a[108:111], v[106:109], v[86:89]
ds_read_b64_tr_b16 a[138:139], v162 offset:10688
v_mfma_f32_16x16x32_bf16 v[90:93], a[112:115], v[106:109], v[90:93]
global_load_lds_dwordx4 v7, s[40:41] offset:3072
ds_read_b64_tr_b16 a[140:141], v162 offset:8576
v_mfma_f32_16x16x32_bf16 v[94:97], v[106:109], a[116:119], v[94:97]
s_add_u32 m0, s48, 49152
ds_read_b64_tr_b16 a[142:143], v162 offset:10880
v_mfma_f32_16x16x32_bf16 v[102:105], v[106:109], a[252:255], v[102:105]
ds_read_b64_tr_b16 v[58:59], v164 offset:8192
v_mfma_f32_16x16x32_bf16 v[110:113], a[108:111], v[170:173], v[110:113]
global_load_lds_dwordx4 v8, s[40:41] offset:-4096
ds_read_b64_tr_b16 v[60:61], v164 offset:10496
v_mfma_f32_16x16x32_bf16 v[114:117], a[112:115], v[170:173], v[114:117]
ds_read_b128 v[186:189], v52 offset:10240
v_mfma_f32_16x16x32_bf16 v[118:121], v[170:173], a[116:119], v[118:121]
s_add_u32 s40, s40, 221184
ds_read_b128 v[194:197], v52 offset:28672
v_mfma_f32_16x16x32_bf16 v[134:137], v[170:173], a[252:255], v[134:137]
v_mfma_f32_16x16x32_bf16 v[122:125], a[108:111], v[178:181], v[122:125]
s_addc_u32 s41, s41, 0
v_mfma_f32_16x16x32_bf16 v[126:129], a[112:115], v[178:181], v[126:129]
v_mfma_f32_16x16x32_bf16 v[130:133], v[178:181], a[116:119], v[130:133]
v_mfma_f32_16x16x32_bf16 v[158:161], v[178:181], a[252:255], v[158:161]
s_waitcnt vmcnt(9) lgkmcnt(0)
s_barrier
s_add_u32 m0, s48, 77824
ds_read_b64_tr_b16 a[144:145], v9 offset:47104
v_mfma_f32_16x16x32_bf16 v[86:89], a[120:123], v[106:109], v[86:89]
ds_read_b64_tr_b16 a[146:147], v9 offset:49408
v_mfma_f32_16x16x32_bf16 v[90:93], a[124:127], v[106:109], v[90:93]
global_load_lds_dwordx4 v0, s[40:41] offset:-4096
ds_read_b64_tr_b16 a[148:149], v9 offset:47296
v_mfma_f32_16x16x32_bf16 v[94:97], v[106:109], a[128:131], v[94:97]
ds_read_b64_tr_b16 a[150:151], v9 offset:49600
global_load_lds_dwordx4 v1, s[40:41] offset:-3072
v_mfma_f32_16x16x32_bf16 v[102:105], v[106:109], v[54:57], v[102:105]
ds_read_b64_tr_b16 a[152:153], v9 offset:47488
v_mfma_f32_16x16x32_bf16 v[110:113], a[120:123], v[182:185], v[110:113]
ds_read_b64_tr_b16 a[154:155], v9 offset:49792
global_load_lds_dwordx4 v2, s[40:41] offset:-2048
v_mfma_f32_16x16x32_bf16 v[114:117], a[124:127], v[182:185], v[114:117]
ds_read_b64_tr_b16 v[62:63], v10 offset:47104
v_mfma_f32_16x16x32_bf16 v[118:121], v[182:185], a[128:131], v[118:121]
global_load_lds_dwordx4 v3, s[40:41] offset:-1024
ds_read_b64_tr_b16 v[64:65], v10 offset:49408
v_mfma_f32_16x16x32_bf16 v[134:137], v[182:185], v[54:57], v[134:137]
ds_read_b128 v[166:169], v51 offset:12288
global_load_lds_dwordx4 v4, s[40:41] offset:0
v_mfma_f32_16x16x32_bf16 v[122:125], a[120:123], v[190:193], v[122:125]
ds_read_b128 v[174:177], v51 offset:30720
v_mfma_f32_16x16x32_bf16 v[126:129], a[124:127], v[190:193], v[126:129]
ds_read_b64_tr_b16 a[156:157], v255 offset:0
global_load_lds_dwordx4 v5, s[40:41] offset:1024
v_mfma_f32_16x16x32_bf16 v[130:133], v[190:193], a[128:131], v[130:133]
ds_read_b64_tr_b16 a[158:159], v255 offset:2304
v_mfma_f32_16x16x32_bf16 v[158:161], v[190:193], v[54:57], v[158:161]
)"
R"(global_load_lds_dwordx4 v6, s[40:41] offset:2048
ds_read_b64_tr_b16 a[160:161], v255 offset:192
v_mfma_f32_16x16x32_bf16 v[86:89], a[132:135], v[106:109], v[86:89]
ds_read_b64_tr_b16 a[162:163], v255 offset:2496
v_mfma_f32_16x16x32_bf16 v[90:93], a[136:139], v[106:109], v[90:93]
global_load_lds_dwordx4 v7, s[40:41] offset:3072
ds_read_b64_tr_b16 a[164:165], v255 offset:384
v_mfma_f32_16x16x32_bf16 v[94:97], v[106:109], a[140:143], v[94:97]
s_add_u32 m0, s48, 86016
ds_read_b64_tr_b16 a[166:167], v255 offset:2688
v_mfma_f32_16x16x32_bf16 v[102:105], v[106:109], v[58:61], v[102:105]
ds_read_b64_tr_b16 v[66:67], v163 offset:0
v_mfma_f32_16x16x32_bf16 v[110:113], a[132:135], v[186:189], v[110:113]
global_load_lds_dwordx4 v8, s[40:41] offset:-4096
ds_read_b64_tr_b16 v[68:69], v163 offset:2304
v_mfma_f32_16x16x32_bf16 v[114:117], a[136:139], v[186:189], v[114:117]
ds_read_b128 v[170:173], v52 offset:12288
v_mfma_f32_16x16x32_bf16 v[118:121], v[186:189], a[140:143], v[118:121]
s_sub_u32 s40, s40, 1769472
ds_read_b128 v[178:181], v52 offset:30720
v_mfma_f32_16x16x32_bf16 v[134:137], v[186:189], v[58:61], v[134:137]
v_mfma_f32_16x16x32_bf16 v[122:125], a[132:135], v[194:197], v[122:125]
s_subb_u32 s41, s41, 0
v_mfma_f32_16x16x32_bf16 v[126:129], a[136:139], v[194:197], v[126:129]
v_mfma_f32_16x16x32_bf16 v[130:133], v[194:197], a[140:143], v[130:133]
v_mfma_f32_16x16x32_bf16 v[158:161], v[194:197], v[58:61], v[158:161]
s_waitcnt vmcnt(9) lgkmcnt(0)
s_barrier
s_add_u32 m0, s49, 0
ds_read_b64_tr_b16 a[168:169], v255 offset:18432
v_mfma_f32_16x16x32_bf16 v[86:89], a[144:147], v[106:109], v[86:89]
ds_read_b64_tr_b16 a[170:171], v255 offset:20736
v_mfma_f32_16x16x32_bf16 v[90:93], a[148:151], v[106:109], v[90:93]
ds_read_b64_tr_b16 a[172:173], v255 offset:18624
v_mfma_f32_16x16x32_bf16 v[94:97], v[106:109], a[152:155], v[94:97]
ds_read_b64_tr_b16 a[174:175], v255 offset:20928
global_load_lds_dwordx4 v11, s[42:43] offset:-2048 sc1
v_mfma_f32_16x16x32_bf16 v[102:105], v[106:109], v[62:65], v[102:105]
ds_read_b64_tr_b16 a[176:177], v255 offset:18816
v_mfma_f32_16x16x32_bf16 v[110:113], a[144:147], v[166:169], v[110:113]
ds_read_b64_tr_b16 a[178:179], v255 offset:21120
v_mfma_f32_16x16x32_bf16 v[114:117], a[148:151], v[166:169], v[114:117]
ds_read_b64_tr_b16 v[70:71], v163 offset:18432
global_load_lds_dwordx4 v12, s[42:43] offset:-1024 sc1
v_mfma_f32_16x16x32_bf16 v[118:121], v[166:169], a[152:155], v[118:121]
ds_read_b64_tr_b16 v[72:73], v163 offset:20736
v_mfma_f32_16x16x32_bf16 v[134:137], v[166:169], v[62:65], v[134:137]
ds_read_b128 v[182:185], v51 offset:14336
v_mfma_f32_16x16x32_bf16 v[122:125], a[144:147], v[174:177], v[122:125]
ds_read_b128 v[190:193], v51 offset:32768
global_load_lds_dwordx4 v13, s[42:43] offset:0 sc1
v_mfma_f32_16x16x32_bf16 v[126:129], a[148:151], v[174:177], v[126:129]
ds_read_b64_tr_b16 a[180:181], v255 offset:36864
v_mfma_f32_16x16x32_bf16 v[130:133], v[174:177], a[152:155], v[130:133]
ds_read_b64_tr_b16 a[182:183], v255 offset:39168
v_mfma_f32_16x16x32_bf16 v[158:161], v[174:177], v[62:65], v[158:161]
ds_read_b64_tr_b16 a[184:185], v255 offset:37056
v_mfma_f32_16x16x32_bf16 v[86:89], a[156:159], v[106:109], v[86:89]
global_load_lds_dwordx4 v14, s[42:43] offset:1024 sc1
ds_read_b64_tr_b16 a[186:187], v255 offset:39360
v_mfma_f32_16x16x32_bf16 v[90:93], a[160:163], v[106:109], v[90:93]
ds_read_b64_tr_b16 a[188:189], v255 offset:37248
v_mfma_f32_16x16x32_bf16 v[94:97], v[106:109], a[164:167], v[94:97]
ds_read_b64_tr_b16 a[190:191], v255 offset:39552
v_mfma_f32_16x16x32_bf16 v[102:105], v[106:109], v[66:69], v[102:105]
global_load_lds_dwordx4 v15, s[42:43] offset:2048 sc1
ds_read_b64_tr_b16 v[74:75], v163 offset:36864
v_mfma_f32_16x16x32_bf16 v[110:113], a[156:159], v[170:173], v[110:113]
ds_read_b64_tr_b16 v[76:77], v163 offset:39168
v_mfma_f32_16x16x32_bf16 v[114:117], a[160:163], v[170:173], v[114:117]
ds_read_b128 v[186:189], v52 offset:14336
v_mfma_f32_16x16x32_bf16 v[118:121], v[170:173], a[164:167], v[118:121]
s_sub_u32 s42, s42, 18432
s_subb_u32 s43, s43, 0
ds_read_b128 v[194:197], v52 offset:32768
v_mfma_f32_16x16x32_bf16 v[134:137], v[170:173], v[66:69], v[134:137]
v_mfma_f32_16x16x32_bf16 v[122:125], a[156:159], v[178:181], v[122:125]
v_mfma_f32_16x16x32_bf16 v[126:129], a[160:163], v[178:181], v[126:129]
v_mfma_f32_16x16x32_bf16 v[130:133], v[178:181], a[164:167], v[130:133]
v_mfma_f32_16x16x32_bf16 v[158:161], v[178:181], v[66:69], v[158:161]
s_waitcnt vmcnt(5) lgkmcnt(0)
s_barrier
s_add_u32 m0, s49, 18432
ds_read_b64_tr_b16 a[192:193], v255 offset:55296
v_mfma_f32_16x16x32_bf16 v[86:89], a[168:171], v[106:109], v[86:89]
ds_read_b64_tr_b16 a[194:195], v255 offset:57600
v_mfma_f32_16x16x32_bf16 v[90:93], a[172:175], v[106:109], v[90:93]
ds_read_b64_tr_b16 a[196:197], v255 offset:55488
v_mfma_f32_16x16x32_bf16 v[94:97], v[106:109], a[176:179], v[94:97]
ds_read_b64_tr_b16 a[198:199], v255 offset:57792
global_load_lds_dwordx4 v11, s[42:43] offset:-2048 sc1
v_mfma_f32_16x16x32_bf16 v[102:105], v[106:109], v[70:73], v[102:105]
ds_read_b64_tr_b16 a[200:201], v255 offset:55680
v_mfma_f32_16x16x32_bf16 v[110:113], a[168:171], v[182:185], v[110:113]
ds_read_b64_tr_b16 a[202:203], v255 offset:57984
v_mfma_f32_16x16x32_bf16 v[114:117], a[172:175], v[182:185], v[114:117]
ds_read_b64_tr_b16 v[78:79], v163 offset:55296
global_load_lds_dwordx4 v12, s[42:43] offset:-1024 sc1
v_mfma_f32_16x16x32_bf16 v[118:121], v[182:185], a[176:179], v[118:121]
ds_read_b64_tr_b16 v[80:81], v163 offset:57600
v_mfma_f32_16x16x32_bf16 v[134:137], v[182:185], v[70:73], v[134:137]
ds_read_b128 v[166:169], v51 offset:16384
v_mfma_f32_16x16x32_bf16 v[122:125], a[168:171], v[190:193], v[122:125]
ds_read_b128 v[174:177], v51 offset:34816
global_load_lds_dwordx4 v13, s[42:43] offset:0 sc1
v_mfma_f32_16x16x32_bf16 v[126:129], a[172:175], v[190:193], v[126:129]
ds_read_b64_tr_b16 a[204:205], v162 offset:8192
v_mfma_f32_16x16x32_bf16 v[130:133], v[190:193], a[176:179], v[130:133]
ds_read_b64_tr_b16 a[206:207], v162 offset:10496
v_mfma_f32_16x16x32_bf16 v[158:161], v[190:193], v[70:73], v[158:161]
ds_read_b64_tr_b16 a[208:209], v162 offset:8384
global_load_lds_dwordx4 v14, s[42:43] offset:1024 sc1
v_mfma_f32_16x16x32_bf16 v[86:89], a[180:183], v[106:109], v[86:89]
ds_read_b64_tr_b16 a[210:211], v162 offset:10688
v_mfma_f32_16x16x32_bf16 v[90:93], a[184:187], v[106:109], v[90:93]
ds_read_b64_tr_b16 a[212:213], v162 offset:8576
v_mfma_f32_16x16x32_bf16 v[94:97], v[106:109], a[188:191], v[94:97]
ds_read_b64_tr_b16 a[214:215], v162 offset:10880
global_load_lds_dwordx4 v15, s[42:43] offset:2048 sc1
v_mfma_f32_16x16x32_bf16 v[102:105], v[106:109], v[74:77], v[102:105]
ds_read_b64_tr_b16 v[82:83], v164 offset:8192
v_mfma_f32_16x16x32_bf16 v[110:113], a[180:183], v[186:189], v[110:113]
ds_read_b64_tr_b16 v[84:85], v164 offset:10496
v_mfma_f32_16x16x32_bf16 v[114:117], a[184:187], v[186:189], v[114:117]
ds_read_b128 v[170:173], v52 offset:16384
s_sub_u32 s42, s42, 18432
s_subb_u32 s43, s43, 0
v_mfma_f32_16x16x32_bf16 v[118:121], v[186:189], a[188:191], v[118:121]
ds_read_b128 v[178:181], v52 offset:34816
v_mfma_f32_16x16x32_bf16 v[134:137], v[186:189], v[74:77], v[134:137]
v_mfma_f32_16x16x32_bf16 v[122:125], a[180:183], v[194:197], v[122:125]
v_mfma_f32_16x16x32_bf16 v[126:129], a[184:187], v[194:197], v[126:129]
v_mfma_f32_16x16x32_bf16 v[158:161], v[194:197], v[74:77], v[158:161]
s_waitcnt lgkmcnt(15)
v_mfma_f32_16x16x32_bf16 v[86:89], a[192:195], v[106:109], v[86:89]
v_mfma_f32_16x16x32_bf16 v[90:93], a[196:199], v[106:109], v[90:93]
s_waitcnt lgkmcnt(14)
v_mfma_f32_16x16x32_bf16 v[94:97], v[106:109], a[200:203], v[94:97]
s_waitcnt lgkmcnt(12)
v_mfma_f32_16x16x32_bf16 v[102:105], v[106:109], v[78:81], v[102:105]
s_waitcnt lgkmcnt(11)
v_mfma_f32_16x16x32_bf16 v[110:113], a[192:195], v[166:169], v[110:113]
v_mfma_f32_16x16x32_bf16 v[114:117], a[196:199], v[166:169], v[114:117]
v_mfma_f32_16x16x32_bf16 v[118:121], v[166:169], a[200:203], v[118:121]
v_mfma_f32_16x16x32_bf16 v[134:137], v[166:169], v[78:81], v[134:137]
s_waitcnt lgkmcnt(10)
v_mfma_f32_16x16x32_bf16 v[158:161], v[174:177], v[78:81], v[158:161]
s_waitcnt lgkmcnt(8)
v_mfma_f32_16x16x32_bf16 v[86:89], a[204:207], v[106:109], v[86:89]
s_waitcnt lgkmcnt(6)
v_mfma_f32_16x16x32_bf16 v[90:93], a[208:211], v[106:109], v[90:93]
s_waitcnt lgkmcnt(4)
v_mfma_f32_16x16x32_bf16 v[94:97], v[106:109], a[212:215], v[94:97]
s_waitcnt lgkmcnt(2)
v_mfma_f32_16x16x32_bf16 v[102:105], v[106:109], v[82:85], v[102:105]
s_waitcnt lgkmcnt(1)
v_mfma_f32_16x16x32_bf16 v[110:113], a[204:207], v[170:173], v[110:113]
v_mfma_f32_16x16x32_bf16 v[114:117], a[208:211], v[170:173], v[114:117]
v_mfma_f32_16x16x32_bf16 v[118:121], v[170:173], a[212:215], v[118:121]
v_mfma_f32_16x16x32_bf16 v[134:137], v[170:173], v[82:85], v[134:137]
s_waitcnt lgkmcnt(0)
v_mfma_f32_16x16x32_bf16 v[158:161], v[178:181], v[82:85], v[158:161]
s_barrier
ds_read_b128 v[242:245], v25 offset:0
s_waitcnt lgkmcnt(0)
s_nop 2
v_fma_f32 v230, v102, v242, v134
v_fma_f32 v231, v102, v243, v135
v_fma_f32 v232, v102, v244, v136
v_fma_f32 v233, v102, v245, v137
v_cvt_pk_bf16_f32 v230, v230, v231
v_cvt_pk_bf16_f32 v231, v232, v233
ds_write_b64 v22, v[230:231] offset:41472
ds_read_b128 v[198:201], v51 offset:32768
ds_read_b128 v[202:205], v52 offset:32768
ds_read_b128 v[206:209], v51 offset:34816
ds_read_b128 v[210:213], v52 offset:34816
v_mov_b32 v234, v32
v_mov_b32 v235, v33
v_mov_b32 v236, v34
v_mov_b32 v237, v35
v_mov_b32 v134, v158
v_mov_b32 v135, v159
v_mov_b32 v136, v160
v_mov_b32 v137, v161
s_waitcnt lgkmcnt(4)
s_barrier
ds_read_b32 v23, v24 offset:0
ds_read_b32 v148, v21
ds_read_b32 v26, v24 offset:192
ds_read_b32 v53, v27
ds_read_b32 v95, v28
ds_read_b128 v[226:229], v25 offset:0
ds_read_b64 v[158:159], v22 offset:41472
ds_read_b32 v140, v20
v_xor_b32 v51, 0x10000, v51
v_xor_b32 v52, 0x10000, v52
s_waitcnt lgkmcnt(7)
v_fma_f32 v238, v90, v23, v114
v_fma_f32 v239, v91, v23, v115
v_fma_f32 v240, v92, v23, v116
v_fma_f32 v241, v93, v23, v117
v_cvt_pk_bf16_f32 v146, v238, v239
v_cvt_pk_bf16_f32 v147, v240, v241
s_waitcnt lgkmcnt(0)
s_barrier
ds_read_b128 v[242:245], v25 offset:64
v_fma_f32 v238, v94, v226, v118
v_fma_f32 v239, v94, v227, v119
v_fma_f32 v240, v94, v228, v120
v_fma_f32 v241, v94, v229, v121
v_cvt_pk_bf16_f32 v154, v238, v239
v_cvt_pk_bf16_f32 v155, v240, v241
v_fma_f32 v238, v86, v23, v110
v_fma_f32 v239, v87, v23, v111
v_fma_f32 v240, v88, v23, v112
v_fma_f32 v241, v89, v23, v113
v_cvt_pk_bf16_f32 v138, v238, v239
v_cvt_pk_bf16_f32 v139, v240, v241
s_waitcnt lgkmcnt(0)
v_fma_f32 v230, v102, v242, v134
v_fma_f32 v231, v102, v243, v135
v_fma_f32 v232, v102, v244, v136
v_fma_f32 v233, v102, v245, v137
v_cvt_pk_bf16_f32 v230, v230, v231
v_cvt_pk_bf16_f32 v231, v232, v233
ds_write_b64 v22, v[230:231] offset:41472
s_waitcnt vmcnt(5) lgkmcnt(0)
s_barrier
s_mov_b32 s70, 32
s_waitcnt lgkmcnt(0)
s_nop 1
s_branch .Lqa_in18
.p2align 6
.Lqa_top29:
s_waitcnt lgkmcnt(9)
v_mfma_f32_16x16x32_bf16 v[134:137], v[44:47], a[216:219], 0
s_waitcnt lgkmcnt(8)
ds_read_b128 v[162:165], v52 offset:4096
ds_read_b32 v95, v30
v_mfma_f32_16x16x32_bf16 v[134:137], v[96:99], a[220:223], v[134:137]
ds_read_b128 v[226:229], v25 offset:64
ds_read_b64 v[160:161], v22 offset:41472
v_mfma_f32_16x16x32_bf16 v[110:113], a[0:3], v[44:47], 0
s_waitcnt lgkmcnt(8)
ds_read_b128 v[166:169], v51 offset:6144
ds_read_b32 v144, v20
v_mfma_f32_16x16x32_bf16 v[134:137], v[104:107], a[224:227], v[134:137]
s_waitcnt lgkmcnt(8)
ds_read_b128 v[170:173], v52 offset:6144
v_fma_f32 v238, v90, v23, v126
v_mfma_f32_16x16x32_bf16 v[134:137], v[0:3], a[228:231], v[134:137]
global_store_dwordx4 v16, v[230:233], s[38:39] sc1
v_mfma_f32_16x16x32_bf16 v[114:117], a[4:7], v[44:47], 0
s_waitcnt lgkmcnt(7)
ds_read_b128 v[174:177], v51 offset:8192
v_add_u32 v16, 18432, v16
v_mfma_f32_16x16x32_bf16 v[134:137], v[4:7], a[232:235], v[134:137]
s_waitcnt lgkmcnt(7)
ds_read_b128 v[178:181], v52 offset:8192
v_fma_f32 v239, v91, v23, v127
v_mfma_f32_16x16x32_bf16 v[134:137], v[162:165], a[236:239], v[134:137]
v_fma_f32 v240, v92, v23, v128
v_fma_f32 v241, v93, v23, v129
v_mfma_f32_16x16x32_bf16 v[118:121], v[44:47], a[8:11], 0
s_waitcnt lgkmcnt(4)
ds_read_b128 v[182:185], v51 offset:10240
v_cvt_pk_bf16_f32 v150, v238, v239
v_mfma_f32_16x16x32_bf16 v[134:137], v[166:169], a[240:243], v[134:137]
s_waitcnt lgkmcnt(3)
ds_read_b128 v[186:189], v52 offset:10240
v_cvt_pk_bf16_f32 v151, v240, v241
v_mfma_f32_16x16x32_bf16 v[134:137], v[170:173], a[244:247], v[134:137]
s_nop 0
v_mfma_f32_16x16x32_bf16 v[214:217], v[150:153], v[138:141], v[32:35]
v_max_f32 v109, v218, v219
v_max_f32 v254, v220, v221
v_mfma_f32_16x16x32_bf16 v[110:113], a[12:15], v[96:99], v[110:113]
s_waitcnt lgkmcnt(3)
ds_read_b128 v[190:193], v51 offset:12288
v_fma_f32 v238, v94, v226, v130
v_mfma_f32_16x16x32_bf16 v[134:137], v[174:177], a[248:251], v[134:137]
s_xor_b32 s49, s49, 0x10000
ds_read_b128 v[194:197], v52 offset:12288
s_waitcnt lgkmcnt(4)
v_mfma_f32_16x16x32_bf16 v[134:137], v[178:181], a[252:255], v[134:137]
s_add_u32 m0, s49, 0
v_mul_f32 v26, 0x3eae1a93, v26
v_mul_f32 v103, v53, v26
v_mfma_f32_16x16x32_bf16 v[114:117], a[16:19], v[96:99], v[114:117]
s_waitcnt lgkmcnt(3)
ds_read_b128 v[198:201], v51 offset:14336
v_mul_f32 v108, v95, v26
v_mfma_f32_16x16x32_bf16 v[134:137], v[182:185], v[54:57], v[134:137]
s_waitcnt lgkmcnt(3)
ds_read_b128 v[202:205], v52 offset:14336
v_max3_f32 v109, v214, v215, v109
v_mfma_f32_16x16x32_bf16 v[134:137], v[186:189], v[58:61], v[134:137]
global_load_lds_dwordx4 v11, s[42:43] offset:-2048 sc1
v_max3_f32 v254, v216, v217, v254
v_mfma_f32_16x16x32_bf16 v[118:121], v[96:99], a[20:23], v[118:121]
s_waitcnt lgkmcnt(3)
ds_read_b128 v[206:209], v51 offset:16384
v_mul_f32 v109, v109, v103
v_mfma_f32_16x16x32_bf16 v[134:137], v[190:193], v[62:65], v[134:137]
s_waitcnt lgkmcnt(3)
ds_read_b128 v[210:213], v52 offset:16384
v_mul_f32 v254, v254, v108
v_mfma_f32_16x16x32_bf16 v[134:137], v[194:197], v[66:69], v[134:137]
v_max_f32 v255, v109, v254
v_mov_b32 v8, v255
v_mfma_f32_16x16x32_bf16 v[110:113], a[24:27], v[104:107], v[110:113]
s_waitcnt lgkmcnt(3)
v_permlane16_swap_b32 v255, v8
v_mfma_f32_16x16x32_bf16 v[134:137], v[198:201], v[70:73], v[134:137]
s_waitcnt lgkmcnt(2)
v_max_f32 v255, v255, v8
v_mov_b32 v8, v255
v_mfma_f32_16x16x32_bf16 v[134:137], v[202:205], v[74:77], v[134:137]
s_nop 0
v_permlane32_swap_b32 v255, v8
v_mfma_f32_16x16x32_bf16 v[114:117], a[28:31], v[104:107], v[114:117]
s_waitcnt lgkmcnt(1)
v_max_f32 v255, v255, v8
v_sub_f32 v109, v109, v255
v_mfma_f32_16x16x32_bf16 v[134:137], v[206:209], v[78:81], v[134:137]
s_waitcnt lgkmcnt(0)
global_load_lds_dwordx4 v12, s[42:43] offset:-1024 sc1
v_sub_f32 v254, v254, v255
v_mfma_f32_16x16x32_bf16 v[134:137], v[210:213], v[82:85], v[134:137]
v_exp_f32 v9, v109
v_mfma_f32_16x16x32_bf16 v[118:121], v[104:107], a[32:35], v[118:121]
v_exp_f32 v10, v254
v_mfma_f32_16x16x32_bf16 v[110:113], a[36:39], v[0:3], v[110:113]
v_add_f32 v255, v9, v10
v_mov_b32 v8, v255
v_mfma_f32_16x16x32_bf16 v[114:117], a[40:43], v[0:3], v[114:117]
v_mul_f32 v9, v9, v53
v_fma_f32 v239, v94, v227, v131
v_mfma_f32_16x16x32_bf16 v[118:121], v[0:3], a[44:47], v[118:121]
v_permlane16_swap_b32 v255, v8
v_mfma_f32_16x16x32_bf16 v[110:113], a[48:51], v[4:7], v[110:113]
v_add_f32 v255, v255, v8
v_mov_b32 v8, v255
v_mfma_f32_16x16x32_bf16 v[114:117], a[52:55], v[4:7], v[114:117]
v_mul_f32 v10, v10, v95
v_fma_f32 v240, v94, v228, v132
v_mfma_f32_16x16x32_bf16 v[118:121], v[4:7], a[56:59], v[118:121]
global_load_lds_dwordx4 v13, s[42:43] offset:0 sc1
v_fma_f32 v241, v94, v229, v133
v_mfma_f32_16x16x32_bf16 v[110:113], a[60:63], v[162:165], v[110:113]
v_permlane32_swap_b32 v255, v8
v_mfma_f32_16x16x32_bf16 v[114:117], a[64:67], v[162:165], v[114:117]
v_add_f32 v255, v255, v8
v_cvt_pk_bf16_f32 v156, v238, v239
v_mfma_f32_16x16x32_bf16 v[118:121], v[162:165], a[68:71], v[118:121]
v_mfma_f32_16x16x32_bf16 v[110:113], a[72:75], v[166:169], v[110:113]
s_barrier
v_mfma_f32_16x16x32_bf16 v[114:117], a[76:79], v[166:169], v[114:117]
ds_read_b128 v[242:245], v25 offset:0
v_cvt_pk_bf16_f32 v157, v240, v241
v_mfma_f32_16x16x32_bf16 v[118:121], v[166:169], a[80:83], v[118:121]
v_rcp_f32 v255, v255
v_mfma_f32_16x16x32_bf16 v[110:113], a[84:87], v[170:173], v[110:113]
v_mul_f32 v9, v9, v255
v_mul_f32 v10, v10, v255
v_mfma_f32_16x16x32_bf16 v[114:117], a[88:91], v[170:173], v[114:117]
global_load_lds_dwordx4 v14, s[42:43] offset:1024 sc1
v_cvt_pk_bf16_f32 v9, v9, v9
v_mfma_f32_16x16x32_bf16 v[118:121], v[170:173], a[92:95], v[118:121]
v_cvt_pk_bf16_f32 v10, v10, v10
v_and_b32 v224, v9, v40
v_mfma_f32_16x16x32_bf16 v[110:113], a[96:99], v[174:177], v[110:113]
v_and_b32 v225, v10, v41
v_and_b32 v222, v9, v42
v_mfma_f32_16x16x32_bf16 v[114:117], a[100:103], v[174:177], v[114:117]
s_waitcnt lgkmcnt(0)
v_and_b32 v223, v10, v43
v_fma_f32 v230, v102, v242, v134
v_mfma_f32_16x16x32_bf16 v[118:121], v[174:177], a[104:107], v[118:121]
v_mfma_f32_16x16x32_bf16 v[218:221], v[154:157], v[222:225], 0
v_mfma_f32_16x16x32_bf16 v[214:217], v[158:161], v[222:225], 0
v_fma_f32 v231, v102, v243, v135
v_fma_f32 v232, v102, v244, v136
v_mfma_f32_16x16x32_bf16 v[110:113], a[108:111], v[178:181], v[110:113]
v_fma_f32 v233, v102, v245, v137
v_cvt_pk_bf16_f32 v230, v230, v231
v_mfma_f32_16x16x32_bf16 v[114:117], a[112:115], v[178:181], v[114:117]
v_cvt_pk_bf16_f32 v231, v232, v233
v_fma_f32 v238, v86, v23, v122
v_mfma_f32_16x16x32_bf16 v[118:121], v[178:181], a[116:119], v[118:121]
ds_write_b64 v22, v[230:231] offset:41472
v_mfma_f32_16x16x32_bf16 v[110:113], a[120:123], v[182:185], v[110:113]
global_load_lds_dwordx4 v15, s[42:43] offset:2048 sc1
s_add_u32 s42, s42, s46
s_addc_u32 s43, s43, s47
v_mfma_f32_16x16x32_bf16 v[114:117], a[124:127], v[182:185], v[114:117]
v_cvt_pk_bf16_f32 v100, v218, v219
v_cvt_pk_bf16_f32 v101, v220, v221
v_mfma_f32_16x16x32_bf16 v[118:121], v[182:185], a[128:131], v[118:121]
)"
R"(ds_write_b64 v18, v[100:101]
v_mfma_f32_16x16x32_bf16 v[110:113], a[132:135], v[186:189], v[110:113]
v_cvt_pk_bf16_f32 v9, v216, v217
v_fma_f32 v239, v87, v23, v123
v_mfma_f32_16x16x32_bf16 v[114:117], a[136:139], v[186:189], v[114:117]
ds_write_b32 v19, v9
v_mfma_f32_16x16x32_bf16 v[118:121], v[186:189], a[140:143], v[118:121]
v_fma_f32 v240, v88, v23, v124
v_fma_f32 v241, v89, v23, v125
v_mfma_f32_16x16x32_bf16 v[110:113], a[144:147], v[190:193], v[110:113]
v_cvt_pk_bf16_f32 v142, v238, v239
v_cvt_pk_bf16_f32 v143, v240, v241
v_mfma_f32_16x16x32_bf16 v[114:117], a[148:151], v[190:193], v[114:117]
v_mfma_f32_16x16x32_bf16 v[118:121], v[190:193], a[152:155], v[118:121]
v_mfma_f32_16x16x32_bf16 v[110:113], a[156:159], v[194:197], v[110:113]
v_mfma_f32_16x16x32_bf16 v[114:117], a[160:163], v[194:197], v[114:117]
v_mfma_f32_16x16x32_bf16 v[118:121], v[194:197], a[164:167], v[118:121]
v_mfma_f32_16x16x32_bf16 v[110:113], a[168:171], v[198:201], v[110:113]
v_mfma_f32_16x16x32_bf16 v[114:117], a[172:175], v[198:201], v[114:117]
v_mfma_f32_16x16x32_bf16 v[118:121], v[198:201], a[176:179], v[118:121]
v_mfma_f32_16x16x32_bf16 v[110:113], a[180:183], v[202:205], v[110:113]
v_mfma_f32_16x16x32_bf16 v[114:117], a[184:187], v[202:205], v[114:117]
s_waitcnt vmcnt(5) lgkmcnt(0)
s_barrier
v_mfma_f32_16x16x32_bf16 v[118:121], v[202:205], a[188:191], v[118:121]
ds_read_b128 v[44:47], v51 offset:18432
v_mfma_f32_16x16x32_bf16 v[110:113], a[192:195], v[206:209], v[110:113]
ds_read_b128 v[96:99], v52 offset:18432
ds_read_b128 v[230:233], v17 offset:0
v_mfma_f32_16x16x32_bf16 v[114:117], a[196:199], v[206:209], v[114:117]
ds_read_b32 v23, v24 offset:0
ds_read_b32 v148, v21
v_mfma_f32_16x16x32_bf16 v[218:221], v[150:153], v[142:145], v[36:39]
v_mfma_f32_16x16x32_bf16 v[118:121], v[206:209], a[200:203], v[118:121]
ds_read_b128 v[104:107], v51 offset:20480
ds_read_b32 v26, v24 offset:192
v_mfma_f32_16x16x32_bf16 v[110:113], a[204:207], v[210:213], v[110:113]
ds_read_b128 v[0:3], v52 offset:20480
ds_read_b32 v53, v27
v_mfma_f32_16x16x32_bf16 v[114:117], a[208:211], v[210:213], v[114:117]
ds_read_b32 v95, v28
ds_read_b128 v[226:229], v25 offset:0
v_mfma_f32_16x16x32_bf16 v[118:121], v[210:213], a[212:215], v[118:121]
s_waitcnt lgkmcnt(10)
ds_read_b128 v[4:7], v51 offset:22528
ds_read_b64 v[158:159], v22 offset:41472
v_mfma_f32_16x16x32_bf16 v[134:137], v[44:47], a[216:219], 0
s_waitcnt lgkmcnt(11)
ds_read_b128 v[162:165], v52 offset:22528
ds_read_b32 v140, v20
v_mfma_f32_16x16x32_bf16 v[134:137], v[96:99], a[220:223], v[134:137]
v_max_f32 v109, v218, v219
v_max_f32 v254, v220, v221
v_mfma_f32_16x16x32_bf16 v[122:125], a[0:3], v[44:47], 0
s_waitcnt lgkmcnt(11)
ds_read_b128 v[166:169], v51 offset:24576
v_fma_f32 v238, v90, v23, v114
s_waitcnt lgkmcnt(10)
v_mfma_f32_16x16x32_bf16 v[134:137], v[104:107], a[224:227], v[134:137]
s_waitcnt lgkmcnt(8)
ds_read_b128 v[170:173], v52 offset:24576
v_fma_f32 v239, v91, v23, v115
v_mfma_f32_16x16x32_bf16 v[134:137], v[0:3], a[228:231], v[134:137]
global_store_dwordx4 v16, v[230:233], s[38:39] sc1
v_mfma_f32_16x16x32_bf16 v[126:129], a[4:7], v[44:47], 0
s_waitcnt lgkmcnt(5)
ds_read_b128 v[174:177], v51 offset:26624
v_add_u32 v16, 18432, v16
v_mfma_f32_16x16x32_bf16 v[134:137], v[4:7], a[232:235], v[134:137]
s_waitcnt lgkmcnt(4)
ds_read_b128 v[178:181], v52 offset:26624
v_fma_f32 v240, v92, v23, v116
v_mfma_f32_16x16x32_bf16 v[134:137], v[162:165], a[236:239], v[134:137]
v_fma_f32 v241, v93, v23, v117
v_cvt_pk_bf16_f32 v146, v238, v239
v_mfma_f32_16x16x32_bf16 v[130:133], v[44:47], a[8:11], 0
s_waitcnt lgkmcnt(3)
ds_read_b128 v[182:185], v51 offset:28672
v_cvt_pk_bf16_f32 v147, v240, v241
v_mfma_f32_16x16x32_bf16 v[134:137], v[166:169], a[240:243], v[134:137]
ds_read_b128 v[186:189], v52 offset:28672
v_mfma_f32_16x16x32_bf16 v[214:217], v[146:149], v[142:145], v[234:237]
s_waitcnt lgkmcnt(4)
v_mul_f32 v26, 0x3eae1a93, v26
v_mul_f32 v103, v53, v26
v_mfma_f32_16x16x32_bf16 v[134:137], v[170:173], a[244:247], v[134:137]
v_mul_f32 v108, v95, v26
v_fma_f32 v238, v94, v226, v118
v_mfma_f32_16x16x32_bf16 v[122:125], a[12:15], v[96:99], v[122:125]
s_waitcnt lgkmcnt(3)
ds_read_b128 v[190:193], v51 offset:30720
v_fma_f32 v239, v94, v227, v119
v_mfma_f32_16x16x32_bf16 v[134:137], v[174:177], a[248:251], v[134:137]
s_add_u32 m0, s49, 18432
ds_read_b128 v[194:197], v52 offset:30720
s_waitcnt lgkmcnt(4)
v_mfma_f32_16x16x32_bf16 v[134:137], v[178:181], a[252:255], v[134:137]
v_max3_f32 v109, v214, v215, v109
v_max3_f32 v254, v216, v217, v254
v_mfma_f32_16x16x32_bf16 v[126:129], a[16:19], v[96:99], v[126:129]
s_waitcnt lgkmcnt(3)
ds_read_b128 v[198:201], v51 offset:32768
v_mul_f32 v109, v109, v103
v_mfma_f32_16x16x32_bf16 v[134:137], v[182:185], v[54:57], v[134:137]
s_waitcnt lgkmcnt(3)
ds_read_b128 v[202:205], v52 offset:32768
global_load_lds_dwordx4 v11, s[42:43] offset:-2048 sc1
v_mfma_f32_16x16x32_bf16 v[134:137], v[186:189], v[58:61], v[134:137]
v_mul_f32 v254, v254, v108
v_max_f32 v255, v109, v254
v_mfma_f32_16x16x32_bf16 v[130:133], v[96:99], a[20:23], v[130:133]
s_waitcnt lgkmcnt(3)
ds_read_b128 v[206:209], v51 offset:34816
v_mov_b32 v8, v255
v_mfma_f32_16x16x32_bf16 v[134:137], v[190:193], v[62:65], v[134:137]
s_waitcnt lgkmcnt(3)
ds_read_b128 v[210:213], v52 offset:34816
v_fma_f32 v240, v94, v228, v120
v_mfma_f32_16x16x32_bf16 v[134:137], v[194:197], v[66:69], v[134:137]
v_permlane16_swap_b32 v255, v8
v_mfma_f32_16x16x32_bf16 v[122:125], a[24:27], v[104:107], v[122:125]
s_waitcnt lgkmcnt(3)
v_max_f32 v255, v255, v8
v_mov_b32 v8, v255
v_mfma_f32_16x16x32_bf16 v[134:137], v[198:201], v[70:73], v[134:137]
s_waitcnt lgkmcnt(2)
v_permlane32_swap_b32 v255, v8
v_mfma_f32_16x16x32_bf16 v[134:137], v[202:205], v[74:77], v[134:137]
v_max_f32 v255, v255, v8
v_sub_f32 v109, v109, v255
v_mfma_f32_16x16x32_bf16 v[126:129], a[28:31], v[104:107], v[126:129]
s_waitcnt lgkmcnt(1)
global_load_lds_dwordx4 v12, s[42:43] offset:-1024 sc1
v_sub_f32 v254, v254, v255
v_mfma_f32_16x16x32_bf16 v[134:137], v[206:209], v[78:81], v[134:137]
s_waitcnt lgkmcnt(0)
v_exp_f32 v9, v109
v_mfma_f32_16x16x32_bf16 v[134:137], v[210:213], v[82:85], v[134:137]
v_exp_f32 v10, v254
v_mfma_f32_16x16x32_bf16 v[130:133], v[104:107], a[32:35], v[130:133]
v_add_f32 v255, v9, v10
v_mov_b32 v8, v255
v_mfma_f32_16x16x32_bf16 v[122:125], a[36:39], v[0:3], v[122:125]
v_mul_f32 v9, v9, v53
v_fma_f32 v241, v94, v229, v121
v_mfma_f32_16x16x32_bf16 v[126:129], a[40:43], v[0:3], v[126:129]
v_permlane16_swap_b32 v255, v8
v_mfma_f32_16x16x32_bf16 v[130:133], v[0:3], a[44:47], v[130:133]
v_add_f32 v255, v255, v8
v_mov_b32 v8, v255
v_mfma_f32_16x16x32_bf16 v[122:125], a[48:51], v[4:7], v[122:125]
v_mul_f32 v10, v10, v95
v_cvt_pk_bf16_f32 v154, v238, v239
v_mfma_f32_16x16x32_bf16 v[126:129], a[52:55], v[4:7], v[126:129]
global_load_lds_dwordx4 v13, s[42:43] offset:0 sc1
v_cvt_pk_bf16_f32 v155, v240, v241
v_mfma_f32_16x16x32_bf16 v[130:133], v[4:7], a[56:59], v[130:133]
v_permlane32_swap_b32 v255, v8
v_mfma_f32_16x16x32_bf16 v[122:125], a[60:63], v[162:165], v[122:125]
v_add_f32 v255, v255, v8
v_fma_f32 v238, v86, v23, v110
v_mfma_f32_16x16x32_bf16 v[126:129], a[64:67], v[162:165], v[126:129]
v_rcp_f32 v255, v255
v_mfma_f32_16x16x32_bf16 v[130:133], v[162:165], a[68:71], v[130:133]
v_mfma_f32_16x16x32_bf16 v[122:125], a[72:75], v[166:169], v[122:125]
s_barrier
v_mfma_f32_16x16x32_bf16 v[126:129], a[76:79], v[166:169], v[126:129]
ds_read_b128 v[242:245], v25 offset:64
v_mul_f32 v9, v9, v255
v_mfma_f32_16x16x32_bf16 v[130:133], v[166:169], a[80:83], v[130:133]
v_mul_f32 v10, v10, v255
v_cvt_pk_bf16_f32 v9, v9, v9
v_mfma_f32_16x16x32_bf16 v[122:125], a[84:87], v[170:173], v[122:125]
global_load_lds_dwordx4 v14, s[42:43] offset:1024 sc1
v_cvt_pk_bf16_f32 v10, v10, v10
v_mfma_f32_16x16x32_bf16 v[126:129], a[88:91], v[170:173], v[126:129]
v_and_b32 v222, v9, v40
v_and_b32 v223, v10, v41
v_mfma_f32_16x16x32_bf16 v[130:133], v[170:173], a[92:95], v[130:133]
v_and_b32 v224, v9, v42
v_and_b32 v225, v10, v43
v_mfma_f32_16x16x32_bf16 v[122:125], a[96:99], v[174:177], v[122:125]
s_nop 0
v_mfma_f32_16x16x32_bf16 v[218:221], v[154:157], v[222:225], 0
v_mfma_f32_16x16x32_bf16 v[214:217], v[158:161], v[222:225], 0
v_fma_f32 v239, v87, v23, v111
v_fma_f32 v240, v88, v23, v112
v_mfma_f32_16x16x32_bf16 v[126:129], a[100:103], v[174:177], v[126:129]
s_waitcnt lgkmcnt(0)
v_fma_f32 v230, v102, v242, v134
v_fma_f32 v231, v102, v243, v135
v_mfma_f32_16x16x32_bf16 v[130:133], v[174:177], a[104:107], v[130:133]
v_fma_f32 v232, v102, v244, v136
v_fma_f32 v233, v102, v245, v137
v_mfma_f32_16x16x32_bf16 v[122:125], a[108:111], v[178:181], v[122:125]
v_cvt_pk_bf16_f32 v230, v230, v231
v_cvt_pk_bf16_f32 v231, v232, v233
v_mfma_f32_16x16x32_bf16 v[126:129], a[112:115], v[178:181], v[126:129]
v_cvt_pk_bf16_f32 v100, v218, v219
v_cvt_pk_bf16_f32 v101, v220, v221
v_mfma_f32_16x16x32_bf16 v[130:133], v[178:181], a[116:119], v[130:133]
global_load_lds_dwordx4 v15, s[42:43] offset:2048 sc1
s_sub_u32 s42, s42, 18432
s_subb_u32 s43, s43, 0
v_mfma_f32_16x16x32_bf16 v[122:125], a[120:123], v[182:185], v[122:125]
ds_write_b64 v18, v[100:101]
v_mfma_f32_16x16x32_bf16 v[126:129], a[124:127], v[182:185], v[126:129]
v_cvt_pk_bf16_f32 v9, v216, v217
v_fma_f32 v241, v89, v23, v113
v_mfma_f32_16x16x32_bf16 v[130:133], v[182:185], a[128:131], v[130:133]
ds_write_b32 v19, v9
v_mfma_f32_16x16x32_bf16 v[122:125], a[132:135], v[186:189], v[122:125]
ds_write_b64 v22, v[230:231] offset:41472
v_mfma_f32_16x16x32_bf16 v[126:129], a[136:139], v[186:189], v[126:129]
v_cvt_pk_bf16_f32 v138, v238, v239
v_cvt_pk_bf16_f32 v139, v240, v241
v_mfma_f32_16x16x32_bf16 v[130:133], v[186:189], a[140:143], v[130:133]
v_mfma_f32_16x16x32_bf16 v[122:125], a[144:147], v[190:193], v[122:125]
v_mfma_f32_16x16x32_bf16 v[126:129], a[148:151], v[190:193], v[126:129]
v_mfma_f32_16x16x32_bf16 v[130:133], v[190:193], a[152:155], v[130:133]
v_mfma_f32_16x16x32_bf16 v[122:125], a[156:159], v[194:197], v[122:125]
v_mfma_f32_16x16x32_bf16 v[126:129], a[160:163], v[194:197], v[126:129]
v_mfma_f32_16x16x32_bf16 v[130:133], v[194:197], a[164:167], v[130:133]
v_mfma_f32_16x16x32_bf16 v[122:125], a[168:171], v[198:201], v[122:125]
v_mfma_f32_16x16x32_bf16 v[126:129], a[172:175], v[198:201], v[126:129]
v_xor_b32 v51, 0x10000, v51
v_xor_b32 v52, 0x10000, v52
v_mfma_f32_16x16x32_bf16 v[130:133], v[198:201], a[176:179], v[130:133]
v_mfma_f32_16x16x32_bf16 v[122:125], a[180:183], v[202:205], v[122:125]
v_mfma_f32_16x16x32_bf16 v[126:129], a[184:187], v[202:205], v[126:129]
s_waitcnt vmcnt(5) lgkmcnt(0)
s_barrier
.Lqa_in18:
v_mfma_f32_16x16x32_bf16 v[130:133], v[202:205], a[188:191], v[130:133]
s_cmp_eq_u32 s70, s73
s_cbranch_scc1 .Lqa_hk19
.Lqa_hr20:
ds_read_b128 v[44:47], v51 offset:0
v_mfma_f32_16x16x32_bf16 v[122:125], a[192:195], v[206:209], v[122:125]
ds_read_b128 v[96:99], v52 offset:0
ds_read_b128 v[230:233], v17 offset:0
v_mfma_f32_16x16x32_bf16 v[126:129], a[196:199], v[206:209], v[126:129]
ds_read_b32 v23, v24 offset:64
ds_read_b32 v152, v21
v_mfma_f32_16x16x32_bf16 v[218:221], v[146:149], v[138:141], v[36:39]
v_mfma_f32_16x16x32_bf16 v[130:133], v[206:209], a[200:203], v[130:133]
ds_read_b128 v[104:107], v51 offset:2048
ds_read_b32 v26, v24 offset:128
v_mfma_f32_16x16x32_bf16 v[122:125], a[204:207], v[210:213], v[122:125]
ds_read_b128 v[0:3], v52 offset:2048
ds_read_b32 v53, v29
v_mfma_f32_16x16x32_bf16 v[126:129], a[208:211], v[210:213], v[126:129]
v_mfma_f32_16x16x32_bf16 v[130:133], v[210:213], a[212:215], v[130:133]
s_sub_u32 s70, s70, 1
ds_read_b128 v[4:7], v51 offset:4096
s_cbranch_scc0 .Lqa_top29
s_waitcnt lgkmcnt(0)
ds_read_b32 v95, v30
ds_read_b128 v[226:229], v25 offset:64
ds_read_b64 v[160:161], v22 offset:41472
ds_read_b32 v144, v20
v_fma_f32 v238, v90, v23, v126
global_store_dwordx4 v16, v[230:233], s[38:39] sc1
v_add_u32 v16, 18432, v16
v_fma_f32 v239, v91, v23, v127
v_fma_f32 v240, v92, v23, v128
v_fma_f32 v241, v93, v23, v129
v_cvt_pk_bf16_f32 v150, v238, v239
v_cvt_pk_bf16_f32 v151, v240, v241
s_nop 1
v_mfma_f32_16x16x32_bf16 v[214:217], v[150:153], v[138:141], v[32:35]
s_waitcnt lgkmcnt(2)
v_max_f32 v109, v218, v219
v_max_f32 v254, v220, v221
v_fma_f32 v238, v94, v226, v130
v_mul_f32 v26, 0x3eae1a93, v26
v_mul_f32 v103, v53, v26
v_mul_f32 v108, v95, v26
s_nop 0
v_max3_f32 v109, v214, v215, v109
v_max3_f32 v254, v216, v217, v254
v_mul_f32 v109, v109, v103
v_mul_f32 v254, v254, v108
v_max_f32 v255, v109, v254
v_mov_b32 v8, v255
s_nop 1
v_permlane16_swap_b32 v255, v8
v_max_f32 v255, v255, v8
v_mov_b32 v8, v255
s_nop 1
v_permlane32_swap_b32 v255, v8
v_max_f32 v255, v255, v8
v_sub_f32 v109, v109, v255
v_sub_f32 v254, v254, v255
v_exp_f32 v9, v109
v_exp_f32 v10, v254
s_nop 0
v_add_f32 v255, v9, v10
v_mov_b32 v8, v255
v_mul_f32 v9, v9, v53
v_fma_f32 v239, v94, v227, v131
v_permlane16_swap_b32 v255, v8
v_add_f32 v255, v255, v8
v_mov_b32 v8, v255
v_mul_f32 v10, v10, v95
v_fma_f32 v240, v94, v228, v132
v_fma_f32 v241, v94, v229, v133
v_permlane32_swap_b32 v255, v8
v_add_f32 v255, v255, v8
v_cvt_pk_bf16_f32 v156, v238, v239
s_waitcnt lgkmcnt(0)
s_barrier
v_cvt_pk_bf16_f32 v157, v240, v241
v_rcp_f32 v255, v255
s_nop 0
v_mul_f32 v9, v9, v255
v_mul_f32 v10, v10, v255
v_cvt_pk_bf16_f32 v9, v9, v9
v_cvt_pk_bf16_f32 v10, v10, v10
v_and_b32 v224, v9, v40
v_and_b32 v225, v10, v41
v_and_b32 v222, v9, v42
v_and_b32 v223, v10, v43
s_nop 1
v_mfma_f32_16x16x32_bf16 v[218:221], v[154:157], v[222:225], 0
v_mfma_f32_16x16x32_bf16 v[214:217], v[158:161], v[222:225], 0
v_fma_f32 v238, v86, v23, v122
s_nop 5
v_cvt_pk_bf16_f32 v100, v218, v219
v_cvt_pk_bf16_f32 v101, v220, v221
ds_write_b64 v18, v[100:101]
v_cvt_pk_bf16_f32 v9, v216, v217
v_fma_f32 v239, v87, v23, v123
ds_write_b32 v19, v9
v_fma_f32 v240, v88, v23, v124
v_fma_f32 v241, v89, v23, v125
v_cvt_pk_bf16_f32 v142, v238, v239
v_cvt_pk_bf16_f32 v143, v240, v241
s_waitcnt vmcnt(0)
s_waitcnt lgkmcnt(0)
s_barrier
ds_read_b128 v[230:233], v17 offset:0
s_waitcnt lgkmcnt(0)
global_store_dwordx4 v16, v[230:233], s[38:39] sc1
s_waitcnt vmcnt(0)
s_barrier
s_endpgm
.Lqa_w3:
s_mov_b32 s70, 500
s_branch .Lqa_hk32
.Lqa_bk45:
v_add_u32 v255, 0x10000, v9
v_add_u32 v162, 0x20000, v9
v_add_u32 v163, 0x10000, v10
v_add_u32 v164, 0x20000, v10
v_mov_b32 v106, 0x3F803F80
v_mov_b32 v107, 0x3F803F80
v_mov_b32 v108, 0x3F803F80
v_mov_b32 v109, 0x3F803F80
v_mov_b32 v141, 0
v_mov_b32 v145, 0
v_mov_b32 v149, 0
v_mov_b32 v153, 0
v_mov_b32 v165, 46080
s_waitcnt lgkmcnt(0)
s_mul_i32 s37, s37, 0x9E3779B1
s_add_u32 s37, s37, 0x7F4A7C15
v_add_u32 v46, s37, v46
v_add_u32 v48, s37, v48
v_add_u32 v50, s37, v50
ds_write_b32 v165, v141
s_mov_b64 s[76:77], 0
s_mov_b32 s73, 18
s_add_u32 m0, s48, 4096
s_nop 0
global_load_lds_dwordx4 v0, s[40:41] offset:-4096
global_load_lds_dwordx4 v1, s[40:41] offset:-3072
global_load_lds_dwordx4 v2, s[40:41] offset:-2048
global_load_lds_dwordx4 v3, s[40:41] offset:-1024
global_load_lds_dwordx4 v4, s[40:41] offset:0
global_load_lds_dwordx4 v5, s[40:41] offset:1024
global_load_lds_dwordx4 v6, s[40:41] offset:2048
global_load_lds_dwordx4 v7, s[40:41] offset:3072
s_add_u32 m0, s48, 12288
s_nop 0
global_load_lds_dwordx4 v8, s[40:41] offset:-4096
s_add_u32 s40, s40, 221184
s_addc_u32 s41, s41, 0
s_add_u32 m0, s48, 40960
s_nop 0
global_load_lds_dwordx4 v0, s[40:41] offset:-4096
global_load_lds_dwordx4 v1, s[40:41] offset:-3072
global_load_lds_dwordx4 v2, s[40:41] offset:-2048
global_load_lds_dwordx4 v3, s[40:41] offset:-1024
global_load_lds_dwordx4 v4, s[40:41] offset:0
global_load_lds_dwordx4 v5, s[40:41] offset:1024
global_load_lds_dwordx4 v6, s[40:41] offset:2048
global_load_lds_dwordx4 v7, s[40:41] offset:3072
s_add_u32 m0, s48, 49152
s_nop 0
)"
R"(global_load_lds_dwordx4 v8, s[40:41] offset:-4096
s_add_u32 s40, s40, 221184
s_addc_u32 s41, s41, 0
s_waitcnt vmcnt(9) lgkmcnt(0)
s_barrier
s_add_u32 m0, s48, 77824
ds_read_b64_tr_b16 a[0:1], v9 offset:47104
ds_read_b64_tr_b16 a[2:3], v9 offset:49408
global_load_lds_dwordx4 v0, s[40:41] offset:-4096
ds_read_b64_tr_b16 a[4:5], v9 offset:47296
global_load_lds_dwordx4 v1, s[40:41] offset:-3072
ds_read_b64_tr_b16 a[6:7], v9 offset:49600
global_load_lds_dwordx4 v2, s[40:41] offset:-2048
ds_read_b64_tr_b16 a[8:9], v9 offset:47488
global_load_lds_dwordx4 v3, s[40:41] offset:-1024
ds_read_b64_tr_b16 a[10:11], v9 offset:49792
ds_read_b128 v[166:169], v51 offset:0
global_load_lds_dwordx4 v4, s[40:41] offset:0
ds_read_b128 v[174:177], v51 offset:18432
global_load_lds_dwordx4 v5, s[40:41] offset:1024
ds_read_b64_tr_b16 a[12:13], v255 offset:0
global_load_lds_dwordx4 v6, s[40:41] offset:2048
ds_read_b64_tr_b16 a[14:15], v255 offset:2304
global_load_lds_dwordx4 v7, s[40:41] offset:3072
ds_read_b64_tr_b16 a[16:17], v255 offset:192
ds_read_b64_tr_b16 a[18:19], v255 offset:2496
s_add_u32 m0, s48, 86016
ds_read_b64_tr_b16 a[20:21], v255 offset:384
global_load_lds_dwordx4 v8, s[40:41] offset:-4096
ds_read_b64_tr_b16 a[22:23], v255 offset:2688
s_add_u32 s40, s40, 221184
ds_read_b128 v[170:173], v52 offset:0
s_addc_u32 s41, s41, 0
ds_read_b128 v[178:181], v52 offset:18432
s_waitcnt vmcnt(9) lgkmcnt(0)
s_barrier
s_add_u32 m0, s48, 4096
ds_read_b64_tr_b16 a[24:25], v255 offset:18432
v_mfma_f32_16x16x32_bf16 v[86:89], a[0:3], v[106:109], 0
v_mfma_f32_16x16x32_bf16 v[90:93], a[4:7], v[106:109], 0
global_load_lds_dwordx4 v0, s[40:41] offset:-4096
ds_read_b64_tr_b16 a[26:27], v255 offset:20736
v_mfma_f32_16x16x32_bf16 v[94:97], v[106:109], a[8:11], 0
ds_read_b64_tr_b16 a[28:29], v255 offset:18624
global_load_lds_dwordx4 v1, s[40:41] offset:-3072
v_mfma_f32_16x16x32_bf16 v[110:113], a[0:3], v[166:169], 0
ds_read_b64_tr_b16 a[30:31], v255 offset:20928
v_mfma_f32_16x16x32_bf16 v[114:117], a[4:7], v[166:169], 0
global_load_lds_dwordx4 v2, s[40:41] offset:-2048
ds_read_b64_tr_b16 a[32:33], v255 offset:18816
v_mfma_f32_16x16x32_bf16 v[118:121], v[166:169], a[8:11], 0
ds_read_b64_tr_b16 a[34:35], v255 offset:21120
global_load_lds_dwordx4 v3, s[40:41] offset:-1024
v_mfma_f32_16x16x32_bf16 v[134:137], v[166:169], v[166:169], 0
ds_read_b128 v[182:185], v51 offset:2048
v_mfma_f32_16x16x32_bf16 v[122:125], a[0:3], v[174:177], 0
global_load_lds_dwordx4 v4, s[40:41] offset:0
ds_read_b128 v[190:193], v51 offset:20480
v_mfma_f32_16x16x32_bf16 v[126:129], a[4:7], v[174:177], 0
ds_read_b64_tr_b16 a[36:37], v255 offset:36864
global_load_lds_dwordx4 v5, s[40:41] offset:1024
v_mfma_f32_16x16x32_bf16 v[130:133], v[174:177], a[8:11], 0
v_mfma_f32_16x16x32_bf16 v[158:161], v[174:177], v[174:177], 0
ds_read_b64_tr_b16 a[38:39], v255 offset:39168
global_load_lds_dwordx4 v6, s[40:41] offset:2048
v_mfma_f32_16x16x32_bf16 v[86:89], a[12:15], v[106:109], v[86:89]
ds_read_b64_tr_b16 a[40:41], v255 offset:37056
v_mfma_f32_16x16x32_bf16 v[90:93], a[16:19], v[106:109], v[90:93]
global_load_lds_dwordx4 v7, s[40:41] offset:3072
ds_read_b64_tr_b16 a[42:43], v255 offset:39360
v_mfma_f32_16x16x32_bf16 v[94:97], v[106:109], a[20:23], v[94:97]
s_add_u32 m0, s48, 12288
ds_read_b64_tr_b16 a[44:45], v255 offset:37248
v_mfma_f32_16x16x32_bf16 v[110:113], a[12:15], v[170:173], v[110:113]
ds_read_b64_tr_b16 a[46:47], v255 offset:39552
v_mfma_f32_16x16x32_bf16 v[114:117], a[16:19], v[170:173], v[114:117]
global_load_lds_dwordx4 v8, s[40:41] offset:-4096
ds_read_b128 v[186:189], v52 offset:2048
v_mfma_f32_16x16x32_bf16 v[118:121], v[170:173], a[20:23], v[118:121]
s_add_u32 s40, s40, 221184
ds_read_b128 v[194:197], v52 offset:20480
v_mfma_f32_16x16x32_bf16 v[134:137], v[170:173], v[170:173], v[134:137]
v_mfma_f32_16x16x32_bf16 v[122:125], a[12:15], v[178:181], v[122:125]
v_mfma_f32_16x16x32_bf16 v[126:129], a[16:19], v[178:181], v[126:129]
s_addc_u32 s41, s41, 0
v_mfma_f32_16x16x32_bf16 v[130:133], v[178:181], a[20:23], v[130:133]
v_mfma_f32_16x16x32_bf16 v[158:161], v[178:181], v[178:181], v[158:161]
s_waitcnt vmcnt(9) lgkmcnt(0)
s_barrier
s_add_u32 m0, s48, 40960
ds_read_b64_tr_b16 a[48:49], v255 offset:55296
v_mfma_f32_16x16x32_bf16 v[86:89], a[24:27], v[106:109], v[86:89]
v_mfma_f32_16x16x32_bf16 v[90:93], a[28:31], v[106:109], v[90:93]
global_load_lds_dwordx4 v0, s[40:41] offset:-4096
ds_read_b64_tr_b16 a[50:51], v255 offset:57600
v_mfma_f32_16x16x32_bf16 v[94:97], v[106:109], a[32:35], v[94:97]
ds_read_b64_tr_b16 a[52:53], v255 offset:55488
global_load_lds_dwordx4 v1, s[40:41] offset:-3072
v_mfma_f32_16x16x32_bf16 v[110:113], a[24:27], v[182:185], v[110:113]
ds_read_b64_tr_b16 a[54:55], v255 offset:57792
v_mfma_f32_16x16x32_bf16 v[114:117], a[28:31], v[182:185], v[114:117]
global_load_lds_dwordx4 v2, s[40:41] offset:-2048
ds_read_b64_tr_b16 a[56:57], v255 offset:55680
v_mfma_f32_16x16x32_bf16 v[118:121], v[182:185], a[32:35], v[118:121]
ds_read_b64_tr_b16 a[58:59], v255 offset:57984
global_load_lds_dwordx4 v3, s[40:41] offset:-1024
v_mfma_f32_16x16x32_bf16 v[134:137], v[182:185], v[182:185], v[134:137]
ds_read_b128 v[166:169], v51 offset:4096
v_mfma_f32_16x16x32_bf16 v[122:125], a[24:27], v[190:193], v[122:125]
global_load_lds_dwordx4 v4, s[40:41] offset:0
ds_read_b128 v[174:177], v51 offset:22528
v_mfma_f32_16x16x32_bf16 v[126:129], a[28:31], v[190:193], v[126:129]
ds_read_b64_tr_b16 a[60:61], v162 offset:8192
global_load_lds_dwordx4 v5, s[40:41] offset:1024
v_mfma_f32_16x16x32_bf16 v[130:133], v[190:193], a[32:35], v[130:133]
v_mfma_f32_16x16x32_bf16 v[158:161], v[190:193], v[190:193], v[158:161]
ds_read_b64_tr_b16 a[62:63], v162 offset:10496
global_load_lds_dwordx4 v6, s[40:41] offset:2048
v_mfma_f32_16x16x32_bf16 v[86:89], a[36:39], v[106:109], v[86:89]
ds_read_b64_tr_b16 a[64:65], v162 offset:8384
v_mfma_f32_16x16x32_bf16 v[90:93], a[40:43], v[106:109], v[90:93]
global_load_lds_dwordx4 v7, s[40:41] offset:3072
ds_read_b64_tr_b16 a[66:67], v162 offset:10688
v_mfma_f32_16x16x32_bf16 v[94:97], v[106:109], a[44:47], v[94:97]
s_add_u32 m0, s48, 49152
ds_read_b64_tr_b16 a[68:69], v162 offset:8576
v_mfma_f32_16x16x32_bf16 v[110:113], a[36:39], v[186:189], v[110:113]
ds_read_b64_tr_b16 a[70:71], v162 offset:10880
v_mfma_f32_16x16x32_bf16 v[114:117], a[40:43], v[186:189], v[114:117]
global_load_lds_dwordx4 v8, s[40:41] offset:-4096
ds_read_b128 v[170:173], v52 offset:4096
v_mfma_f32_16x16x32_bf16 v[118:121], v[186:189], a[44:47], v[118:121]
s_add_u32 s40, s40, 221184
ds_read_b128 v[178:181], v52 offset:22528
v_mfma_f32_16x16x32_bf16 v[134:137], v[186:189], v[186:189], v[134:137]
v_mfma_f32_16x16x32_bf16 v[122:125], a[36:39], v[194:197], v[122:125]
v_mfma_f32_16x16x32_bf16 v[126:129], a[40:43], v[194:197], v[126:129]
s_addc_u32 s41, s41, 0
v_mfma_f32_16x16x32_bf16 v[130:133], v[194:197], a[44:47], v[130:133]
v_mfma_f32_16x16x32_bf16 v[158:161], v[194:197], v[194:197], v[158:161]
s_waitcnt vmcnt(9) lgkmcnt(0)
s_barrier
s_add_u32 m0, s48, 77824
ds_read_b64_tr_b16 a[72:73], v9 offset:47104
v_mfma_f32_16x16x32_bf16 v[86:89], a[48:51], v[106:109], v[86:89]
v_mfma_f32_16x16x32_bf16 v[90:93], a[52:55], v[106:109], v[90:93]
global_load_lds_dwordx4 v0, s[40:41] offset:-4096
ds_read_b64_tr_b16 a[74:75], v9 offset:49408
v_mfma_f32_16x16x32_bf16 v[94:97], v[106:109], a[56:59], v[94:97]
ds_read_b64_tr_b16 a[76:77], v9 offset:47296
global_load_lds_dwordx4 v1, s[40:41] offset:-3072
v_mfma_f32_16x16x32_bf16 v[110:113], a[48:51], v[166:169], v[110:113]
ds_read_b64_tr_b16 a[78:79], v9 offset:49600
v_mfma_f32_16x16x32_bf16 v[114:117], a[52:55], v[166:169], v[114:117]
global_load_lds_dwordx4 v2, s[40:41] offset:-2048
ds_read_b64_tr_b16 a[80:81], v9 offset:47488
v_mfma_f32_16x16x32_bf16 v[118:121], v[166:169], a[56:59], v[118:121]
ds_read_b64_tr_b16 a[82:83], v9 offset:49792
global_load_lds_dwordx4 v3, s[40:41] offset:-1024
v_mfma_f32_16x16x32_bf16 v[134:137], v[166:169], v[166:169], v[134:137]
ds_read_b128 v[182:185], v51 offset:6144
v_mfma_f32_16x16x32_bf16 v[122:125], a[48:51], v[174:177], v[122:125]
global_load_lds_dwordx4 v4, s[40:41] offset:0
ds_read_b128 v[190:193], v51 offset:24576
v_mfma_f32_16x16x32_bf16 v[126:129], a[52:55], v[174:177], v[126:129]
ds_read_b64_tr_b16 a[84:85], v255 offset:0
global_load_lds_dwordx4 v5, s[40:41] offset:1024
v_mfma_f32_16x16x32_bf16 v[130:133], v[174:177], a[56:59], v[130:133]
v_mfma_f32_16x16x32_bf16 v[158:161], v[174:177], v[174:177], v[158:161]
ds_read_b64_tr_b16 a[86:87], v255 offset:2304
global_load_lds_dwordx4 v6, s[40:41] offset:2048
v_mfma_f32_16x16x32_bf16 v[86:89], a[60:63], v[106:109], v[86:89]
ds_read_b64_tr_b16 a[88:89], v255 offset:192
v_mfma_f32_16x16x32_bf16 v[90:93], a[64:67], v[106:109], v[90:93]
global_load_lds_dwordx4 v7, s[40:41] offset:3072
ds_read_b64_tr_b16 a[90:91], v255 offset:2496
v_mfma_f32_16x16x32_bf16 v[94:97], v[106:109], a[68:71], v[94:97]
s_add_u32 m0, s48, 86016
ds_read_b64_tr_b16 a[92:93], v255 offset:384
v_mfma_f32_16x16x32_bf16 v[110:113], a[60:63], v[170:173], v[110:113]
ds_read_b64_tr_b16 a[94:95], v255 offset:2688
v_mfma_f32_16x16x32_bf16 v[114:117], a[64:67], v[170:173], v[114:117]
global_load_lds_dwordx4 v8, s[40:41] offset:-4096
ds_read_b128 v[186:189], v52 offset:6144
v_mfma_f32_16x16x32_bf16 v[118:121], v[170:173], a[68:71], v[118:121]
s_add_u32 s40, s40, 221184
ds_read_b128 v[194:197], v52 offset:24576
v_mfma_f32_16x16x32_bf16 v[134:137], v[170:173], v[170:173], v[134:137]
v_mfma_f32_16x16x32_bf16 v[122:125], a[60:63], v[178:181], v[122:125]
v_mfma_f32_16x16x32_bf16 v[126:129], a[64:67], v[178:181], v[126:129]
s_addc_u32 s41, s41, 0
v_mfma_f32_16x16x32_bf16 v[130:133], v[178:181], a[68:71], v[130:133]
v_mfma_f32_16x16x32_bf16 v[158:161], v[178:181], v[178:181], v[158:161]
s_waitcnt vmcnt(9) lgkmcnt(0)
s_barrier
s_add_u32 m0, s48, 4096
ds_read_b64_tr_b16 a[96:97], v255 offset:18432
v_mfma_f32_16x16x32_bf16 v[86:89], a[72:75], v[106:109], v[86:89]
v_mfma_f32_16x16x32_bf16 v[90:93], a[76:79], v[106:109], v[90:93]
global_load_lds_dwordx4 v0, s[40:41] offset:-4096
ds_read_b64_tr_b16 a[98:99], v255 offset:20736
v_mfma_f32_16x16x32_bf16 v[94:97], v[106:109], a[80:83], v[94:97]
ds_read_b64_tr_b16 a[100:101], v255 offset:18624
global_load_lds_dwordx4 v1, s[40:41] offset:-3072
v_mfma_f32_16x16x32_bf16 v[110:113], a[72:75], v[182:185], v[110:113]
ds_read_b64_tr_b16 a[102:103], v255 offset:20928
v_mfma_f32_16x16x32_bf16 v[114:117], a[76:79], v[182:185], v[114:117]
global_load_lds_dwordx4 v2, s[40:41] offset:-2048
ds_read_b64_tr_b16 a[104:105], v255 offset:18816
v_mfma_f32_16x16x32_bf16 v[118:121], v[182:185], a[80:83], v[118:121]
ds_read_b64_tr_b16 a[106:107], v255 offset:21120
global_load_lds_dwordx4 v3, s[40:41] offset:-1024
v_mfma_f32_16x16x32_bf16 v[134:137], v[182:185], v[182:185], v[134:137]
ds_read_b128 v[166:169], v51 offset:8192
v_mfma_f32_16x16x32_bf16 v[122:125], a[72:75], v[190:193], v[122:125]
global_load_lds_dwordx4 v4, s[40:41] offset:0
ds_read_b128 v[174:177], v51 offset:26624
v_mfma_f32_16x16x32_bf16 v[126:129], a[76:79], v[190:193], v[126:129]
ds_read_b64_tr_b16 a[108:109], v255 offset:36864
global_load_lds_dwordx4 v5, s[40:41] offset:1024
v_mfma_f32_16x16x32_bf16 v[130:133], v[190:193], a[80:83], v[130:133]
v_mfma_f32_16x16x32_bf16 v[158:161], v[190:193], v[190:193], v[158:161]
ds_read_b64_tr_b16 a[110:111], v255 offset:39168
global_load_lds_dwordx4 v6, s[40:41] offset:2048
v_mfma_f32_16x16x32_bf16 v[86:89], a[84:87], v[106:109], v[86:89]
ds_read_b64_tr_b16 a[112:113], v255 offset:37056
v_mfma_f32_16x16x32_bf16 v[90:93], a[88:91], v[106:109], v[90:93]
global_load_lds_dwordx4 v7, s[40:41] offset:3072
ds_read_b64_tr_b16 a[114:115], v255 offset:39360
v_mfma_f32_16x16x32_bf16 v[94:97], v[106:109], a[92:95], v[94:97]
s_add_u32 m0, s48, 12288
ds_read_b64_tr_b16 a[116:117], v255 offset:37248
v_mfma_f32_16x16x32_bf16 v[110:113], a[84:87], v[186:189], v[110:113]
ds_read_b64_tr_b16 a[118:119], v255 offset:39552
v_mfma_f32_16x16x32_bf16 v[114:117], a[88:91], v[186:189], v[114:117]
global_load_lds_dwordx4 v8, s[40:41] offset:-4096
ds_read_b128 v[170:173], v52 offset:8192
v_mfma_f32_16x16x32_bf16 v[118:121], v[186:189], a[92:95], v[118:121]
s_add_u32 s40, s40, 221184
ds_read_b128 v[178:181], v52 offset:26624
v_mfma_f32_16x16x32_bf16 v[134:137], v[186:189], v[186:189], v[134:137]
v_mfma_f32_16x16x32_bf16 v[122:125], a[84:87], v[194:197], v[122:125]
v_mfma_f32_16x16x32_bf16 v[126:129], a[88:91], v[194:197], v[126:129]
s_addc_u32 s41, s41, 0
v_mfma_f32_16x16x32_bf16 v[130:133], v[194:197], a[92:95], v[130:133]
v_mfma_f32_16x16x32_bf16 v[158:161], v[194:197], v[194:197], v[158:161]
s_waitcnt vmcnt(9) lgkmcnt(0)
s_barrier
s_add_u32 m0, s48, 40960
ds_read_b64_tr_b16 a[120:121], v255 offset:55296
v_mfma_f32_16x16x32_bf16 v[86:89], a[96:99], v[106:109], v[86:89]
v_mfma_f32_16x16x32_bf16 v[90:93], a[100:103], v[106:109], v[90:93]
global_load_lds_dwordx4 v0, s[40:41] offset:-4096
ds_read_b64_tr_b16 a[122:123], v255 offset:57600
v_mfma_f32_16x16x32_bf16 v[94:97], v[106:109], a[104:107], v[94:97]
ds_read_b64_tr_b16 a[124:125], v255 offset:55488
global_load_lds_dwordx4 v1, s[40:41] offset:-3072
v_mfma_f32_16x16x32_bf16 v[110:113], a[96:99], v[166:169], v[110:113]
ds_read_b64_tr_b16 a[126:127], v255 offset:57792
v_mfma_f32_16x16x32_bf16 v[114:117], a[100:103], v[166:169], v[114:117]
global_load_lds_dwordx4 v2, s[40:41] offset:-2048
ds_read_b64_tr_b16 a[128:129], v255 offset:55680
v_mfma_f32_16x16x32_bf16 v[118:121], v[166:169], a[104:107], v[118:121]
ds_read_b64_tr_b16 a[130:131], v255 offset:57984
global_load_lds_dwordx4 v3, s[40:41] offset:-1024
v_mfma_f32_16x16x32_bf16 v[134:137], v[166:169], v[166:169], v[134:137]
ds_read_b128 v[182:185], v51 offset:10240
v_mfma_f32_16x16x32_bf16 v[122:125], a[96:99], v[174:177], v[122:125]
global_load_lds_dwordx4 v4, s[40:41] offset:0
ds_read_b128 v[190:193], v51 offset:28672
v_mfma_f32_16x16x32_bf16 v[126:129], a[100:103], v[174:177], v[126:129]
ds_read_b64_tr_b16 a[132:133], v162 offset:8192
global_load_lds_dwordx4 v5, s[40:41] offset:1024
v_mfma_f32_16x16x32_bf16 v[130:133], v[174:177], a[104:107], v[130:133]
v_mfma_f32_16x16x32_bf16 v[158:161], v[174:177], v[174:177], v[158:161]
ds_read_b64_tr_b16 a[134:135], v162 offset:10496
global_load_lds_dwordx4 v6, s[40:41] offset:2048
v_mfma_f32_16x16x32_bf16 v[86:89], a[108:111], v[106:109], v[86:89]
ds_read_b64_tr_b16 a[136:137], v162 offset:8384
v_mfma_f32_16x16x32_bf16 v[90:93], a[112:115], v[106:109], v[90:93]
global_load_lds_dwordx4 v7, s[40:41] offset:3072
ds_read_b64_tr_b16 a[138:139], v162 offset:10688
v_mfma_f32_16x16x32_bf16 v[94:97], v[106:109], a[116:119], v[94:97]
s_add_u32 m0, s48, 49152
ds_read_b64_tr_b16 a[140:141], v162 offset:8576
v_mfma_f32_16x16x32_bf16 v[110:113], a[108:111], v[170:173], v[110:113]
ds_read_b64_tr_b16 a[142:143], v162 offset:10880
v_mfma_f32_16x16x32_bf16 v[114:117], a[112:115], v[170:173], v[114:117]
global_load_lds_dwordx4 v8, s[40:41] offset:-4096
ds_read_b128 v[186:189], v52 offset:10240
v_mfma_f32_16x16x32_bf16 v[118:121], v[170:173], a[116:119], v[118:121]
s_add_u32 s40, s40, 221184
ds_read_b128 v[194:197], v52 offset:28672
v_mfma_f32_16x16x32_bf16 v[134:137], v[170:173], v[170:173], v[134:137]
v_mfma_f32_16x16x32_bf16 v[122:125], a[108:111], v[178:181], v[122:125]
v_mfma_f32_16x16x32_bf16 v[126:129], a[112:115], v[178:181], v[126:129]
s_addc_u32 s41, s41, 0
v_mfma_f32_16x16x32_bf16 v[130:133], v[178:181], a[116:119], v[130:133]
v_mfma_f32_16x16x32_bf16 v[158:161], v[178:181], v[178:181], v[158:161]
s_waitcnt vmcnt(9) lgkmcnt(0)
s_barrier
s_add_u32 m0, s48, 77824
ds_read_b64_tr_b16 a[144:145], v9 offset:47104
v_mfma_f32_16x16x32_bf16 v[86:89], a[120:123], v[106:109], v[86:89]
v_mfma_f32_16x16x32_bf16 v[90:93], a[124:127], v[106:109], v[90:93]
global_load_lds_dwordx4 v0, s[40:41] offset:-4096
ds_read_b64_tr_b16 a[146:147], v9 offset:49408
v_mfma_f32_16x16x32_bf16 v[94:97], v[106:109], a[128:131], v[94:97]
ds_read_b64_tr_b16 a[148:149], v9 offset:47296
global_load_lds_dwordx4 v1, s[40:41] offset:-3072
v_mfma_f32_16x16x32_bf16 v[110:113], a[120:123], v[182:185], v[110:113]
ds_read_b64_tr_b16 a[150:151], v9 offset:49600
v_mfma_f32_16x16x32_bf16 v[114:117], a[124:127], v[182:185], v[114:117]
global_load_lds_dwordx4 v2, s[40:41] offset:-2048
ds_read_b64_tr_b16 a[152:153], v9 offset:47488
v_mfma_f32_16x16x32_bf16 v[118:121], v[182:185], a[128:131], v[118:121]
ds_read_b64_tr_b16 a[154:155], v9 offset:49792
global_load_lds_dwordx4 v3, s[40:41] offset:-1024
v_mfma_f32_16x16x32_bf16 v[134:137], v[182:185], v[182:185], v[134:137]
ds_read_b128 v[166:169], v51 offset:12288
v_mfma_f32_16x16x32_bf16 v[122:125], a[120:123], v[190:193], v[122:125]
global_load_lds_dwordx4 v4, s[40:41] offset:0
ds_read_b128 v[174:177], v51 offset:30720
v_mfma_f32_16x16x32_bf16 v[126:129], a[124:127], v[190:193], v[126:129]
ds_read_b64_tr_b16 a[156:157], v255 offset:0
global_load_lds_dwordx4 v5, s[40:41] offset:1024
v_mfma_f32_16x16x32_bf16 v[130:133], v[190:193], a[128:131], v[130:133]
v_mfma_f32_16x16x32_bf16 v[158:161], v[190:193], v[190:193], v[158:161]
ds_read_b64_tr_b16 a[158:159], v255 offset:2304
global_load_lds_dwordx4 v6, s[40:41] offset:2048
v_mfma_f32_16x16x32_bf16 v[86:89], a[132:135], v[106:109], v[86:89]
ds_read_b64_tr_b16 a[160:161], v255 offset:192
v_mfma_f32_16x16x32_bf16 v[90:93], a[136:139], v[106:109], v[90:93]
global_load_lds_dwordx4 v7, s[40:41] offset:3072
ds_read_b64_tr_b16 a[162:163], v255 offset:2496
v_mfma_f32_16x16x32_bf16 v[94:97], v[106:109], a[140:143], v[94:97]
s_add_u32 m0, s48, 86016
ds_read_b64_tr_b16 a[164:165], v255 offset:384
v_mfma_f32_16x16x32_bf16 v[110:113], a[132:135], v[186:189], v[110:113]
ds_read_b64_tr_b16 a[166:167], v255 offset:2688
v_mfma_f32_16x16x32_bf16 v[114:117], a[136:139], v[186:189], v[114:117]
global_load_lds_dwordx4 v8, s[40:41] offset:-4096
ds_read_b128 v[170:173], v52 offset:12288
v_mfma_f32_16x16x32_bf16 v[118:121], v[186:189], a[140:143], v[118:121]
s_sub_u32 s40, s40, 1769472
ds_read_b128 v[178:181], v52 offset:30720
v_mfma_f32_16x16x32_bf16 v[134:137], v[186:189], v[186:189], v[134:137]
v_mfma_f32_16x16x32_bf16 v[122:125], a[132:135], v[194:197], v[122:125]
v_mfma_f32_16x16x32_bf16 v[126:129], a[136:139], v[194:197], v[126:129]
s_subb_u32 s41, s41, 0
v_mfma_f32_16x16x32_bf16 v[130:133], v[194:197], a[140:143], v[130:133]
v_mfma_f32_16x16x32_bf16 v[158:161], v[194:197], v[194:197], v[158:161]
s_waitcnt vmcnt(9) lgkmcnt(0)
s_barrier
s_add_u32 m0, s49, 0
ds_read_b64_tr_b16 a[168:169], v255 offset:18432
v_mfma_f32_16x16x32_bf16 v[86:89], a[144:147], v[106:109], v[86:89]
v_mfma_f32_16x16x32_bf16 v[90:93], a[148:151], v[106:109], v[90:93]
ds_read_b64_tr_b16 a[170:171], v255 offset:20736
v_mfma_f32_16x16x32_bf16 v[94:97], v[106:109], a[152:155], v[94:97]
ds_read_b64_tr_b16 a[172:173], v255 offset:18624
v_mfma_f32_16x16x32_bf16 v[110:113], a[144:147], v[166:169], v[110:113]
global_load_lds_dwordx4 v11, s[42:43] offset:-2048 sc1
ds_read_b64_tr_b16 a[174:175], v255 offset:20928
v_mfma_f32_16x16x32_bf16 v[114:117], a[148:151], v[166:169], v[114:117]
ds_read_b64_tr_b16 a[176:177], v255 offset:18816
v_mfma_f32_16x16x32_bf16 v[118:121], v[166:169], a[152:155], v[118:121]
ds_read_b64_tr_b16 a[178:179], v255 offset:21120
v_mfma_f32_16x16x32_bf16 v[134:137], v[166:169], v[166:169], v[134:137]
global_load_lds_dwordx4 v12, s[42:43] offset:-1024 sc1
ds_read_b128 v[182:185], v51 offset:14336
v_mfma_f32_16x16x32_bf16 v[122:125], a[144:147], v[174:177], v[122:125]
ds_read_b128 v[190:193], v51 offset:32768
v_mfma_f32_16x16x32_bf16 v[126:129], a[148:151], v[174:177], v[126:129]
ds_read_b64_tr_b16 a[180:181], v255 offset:36864
v_mfma_f32_16x16x32_bf16 v[130:133], v[174:177], a[152:155], v[130:133]
global_load_lds_dwordx4 v13, s[42:43] offset:0 sc1
v_mfma_f32_16x16x32_bf16 v[158:161], v[174:177], v[174:177], v[158:161]
ds_read_b64_tr_b16 a[182:183], v255 offset:39168
v_mfma_f32_16x16x32_bf16 v[86:89], a[156:159], v[106:109], v[86:89]
ds_read_b64_tr_b16 a[184:185], v255 offset:37056
v_mfma_f32_16x16x32_bf16 v[90:93], a[160:163], v[106:109], v[90:93]
ds_read_b64_tr_b16 a[186:187], v255 offset:39360
v_mfma_f32_16x16x32_bf16 v[94:97], v[106:109], a[164:167], v[94:97]
global_load_lds_dwordx4 v14, s[42:43] offset:1024 sc1
ds_read_b64_tr_b16 a[188:189], v255 offset:37248
v_mfma_f32_16x16x32_bf16 v[110:113], a[156:159], v[170:173], v[110:113]
ds_read_b64_tr_b16 a[190:191], v255 offset:39552
v_mfma_f32_16x16x32_bf16 v[114:117], a[160:163], v[170:173], v[114:117]
ds_read_b128 v[186:189], v52 offset:14336
v_mfma_f32_16x16x32_bf16 v[118:121], v[170:173], a[164:167], v[118:121]
s_sub_u32 s42, s42, 18432
s_subb_u32 s43, s43, 0
ds_read_b128 v[194:197], v52 offset:32768
v_mfma_f32_16x16x32_bf16 v[134:137], v[170:173], v[170:173], v[134:137]
v_mfma_f32_16x16x32_bf16 v[122:125], a[156:159], v[178:181], v[122:125]
v_mfma_f32_16x16x32_bf16 v[126:129], a[160:163], v[178:181], v[126:129]
v_mfma_f32_16x16x32_bf16 v[130:133], v[178:181], a[164:167], v[130:133]
v_mfma_f32_16x16x32_bf16 v[158:161], v[178:181], v[178:181], v[158:161]
s_waitcnt vmcnt(4) lgkmcnt(0)
)"
R"(s_barrier
s_add_u32 m0, s49, 18432
ds_read_b64_tr_b16 a[192:193], v255 offset:55296
v_mfma_f32_16x16x32_bf16 v[86:89], a[168:171], v[106:109], v[86:89]
v_mfma_f32_16x16x32_bf16 v[90:93], a[172:175], v[106:109], v[90:93]
ds_read_b64_tr_b16 a[194:195], v255 offset:57600
v_mfma_f32_16x16x32_bf16 v[94:97], v[106:109], a[176:179], v[94:97]
ds_read_b64_tr_b16 a[196:197], v255 offset:55488
v_mfma_f32_16x16x32_bf16 v[110:113], a[168:171], v[182:185], v[110:113]
global_load_lds_dwordx4 v11, s[42:43] offset:-2048 sc1
ds_read_b64_tr_b16 a[198:199], v255 offset:57792
v_mfma_f32_16x16x32_bf16 v[114:117], a[172:175], v[182:185], v[114:117]
ds_read_b64_tr_b16 a[200:201], v255 offset:55680
v_mfma_f32_16x16x32_bf16 v[118:121], v[182:185], a[176:179], v[118:121]
ds_read_b64_tr_b16 a[202:203], v255 offset:57984
v_mfma_f32_16x16x32_bf16 v[134:137], v[182:185], v[182:185], v[134:137]
global_load_lds_dwordx4 v12, s[42:43] offset:-1024 sc1
ds_read_b128 v[166:169], v51 offset:16384
v_mfma_f32_16x16x32_bf16 v[122:125], a[168:171], v[190:193], v[122:125]
ds_read_b128 v[174:177], v51 offset:34816
v_mfma_f32_16x16x32_bf16 v[126:129], a[172:175], v[190:193], v[126:129]
ds_read_b64_tr_b16 a[204:205], v162 offset:8192
v_mfma_f32_16x16x32_bf16 v[130:133], v[190:193], a[176:179], v[130:133]
global_load_lds_dwordx4 v13, s[42:43] offset:0 sc1
ds_read_b64_tr_b16 a[206:207], v162 offset:10496
v_mfma_f32_16x16x32_bf16 v[158:161], v[190:193], v[190:193], v[158:161]
ds_read_b64_tr_b16 a[208:209], v162 offset:8384
v_mfma_f32_16x16x32_bf16 v[86:89], a[180:183], v[106:109], v[86:89]
ds_read_b64_tr_b16 a[210:211], v162 offset:10688
v_mfma_f32_16x16x32_bf16 v[90:93], a[184:187], v[106:109], v[90:93]
global_load_lds_dwordx4 v14, s[42:43] offset:1024 sc1
ds_read_b64_tr_b16 a[212:213], v162 offset:8576
v_mfma_f32_16x16x32_bf16 v[94:97], v[106:109], a[188:191], v[94:97]
ds_read_b64_tr_b16 a[214:215], v162 offset:10880
v_mfma_f32_16x16x32_bf16 v[110:113], a[180:183], v[186:189], v[110:113]
ds_read_b128 v[170:173], v52 offset:16384
v_mfma_f32_16x16x32_bf16 v[114:117], a[184:187], v[186:189], v[114:117]
s_sub_u32 s42, s42, 18432
s_subb_u32 s43, s43, 0
ds_read_b128 v[178:181], v52 offset:34816
v_mfma_f32_16x16x32_bf16 v[118:121], v[186:189], a[188:191], v[118:121]
v_mfma_f32_16x16x32_bf16 v[134:137], v[186:189], v[186:189], v[134:137]
v_mfma_f32_16x16x32_bf16 v[122:125], a[180:183], v[194:197], v[122:125]
v_mfma_f32_16x16x32_bf16 v[126:129], a[184:187], v[194:197], v[126:129]
v_mfma_f32_16x16x32_bf16 v[158:161], v[194:197], v[194:197], v[158:161]
s_waitcnt lgkmcnt(14)
v_mfma_f32_16x16x32_bf16 v[86:89], a[192:195], v[106:109], v[86:89]
s_waitcnt lgkmcnt(12)
v_mfma_f32_16x16x32_bf16 v[90:93], a[196:199], v[106:109], v[90:93]
s_waitcnt lgkmcnt(10)
v_mfma_f32_16x16x32_bf16 v[94:97], v[106:109], a[200:203], v[94:97]
s_waitcnt lgkmcnt(9)
v_mfma_f32_16x16x32_bf16 v[110:113], a[192:195], v[166:169], v[110:113]
v_mfma_f32_16x16x32_bf16 v[114:117], a[196:199], v[166:169], v[114:117]
v_mfma_f32_16x16x32_bf16 v[118:121], v[166:169], a[200:203], v[118:121]
v_mfma_f32_16x16x32_bf16 v[134:137], v[166:169], v[166:169], v[134:137]
s_waitcnt lgkmcnt(8)
v_mfma_f32_16x16x32_bf16 v[158:161], v[174:177], v[174:177], v[158:161]
s_waitcnt lgkmcnt(6)
v_mfma_f32_16x16x32_bf16 v[86:89], a[204:207], v[106:109], v[86:89]
s_waitcnt lgkmcnt(4)
v_mfma_f32_16x16x32_bf16 v[90:93], a[208:211], v[106:109], v[90:93]
s_waitcnt lgkmcnt(2)
v_mfma_f32_16x16x32_bf16 v[94:97], v[106:109], a[212:215], v[94:97]
s_waitcnt lgkmcnt(1)
v_mfma_f32_16x16x32_bf16 v[110:113], a[204:207], v[170:173], v[110:113]
v_mfma_f32_16x16x32_bf16 v[114:117], a[208:211], v[170:173], v[114:117]
v_mfma_f32_16x16x32_bf16 v[118:121], v[170:173], a[212:215], v[118:121]
v_mfma_f32_16x16x32_bf16 v[134:137], v[170:173], v[170:173], v[134:137]
s_waitcnt lgkmcnt(0)
v_mfma_f32_16x16x32_bf16 v[158:161], v[178:181], v[178:181], v[158:161]
s_nop 5
ds_write_b128 v23, v[134:137] offset:41984
s_waitcnt lgkmcnt(0)
s_barrier
ds_read_b128 v[166:169], v51 offset:32768
ds_read_b128 v[170:173], v52 offset:32768
ds_read_b128 v[174:177], v51 offset:34816
ds_read_b128 v[178:181], v52 offset:34816
v_mov_b32 v202, v32
v_mov_b32 v203, v33
v_mov_b32 v204, v34
v_mov_b32 v205, v35
v_mov_b32 v134, v158
v_mov_b32 v135, v159
v_mov_b32 v136, v160
v_mov_b32 v137, v161
s_barrier
ds_read_b32 v26, v24 offset:0
ds_read_b32 v148, v21
ds_read_b32 v95, v24 offset:192
ds_read_b32 v255, v27
ds_read_b32 v8, v28
ds_read_b128 v[194:197], v25 offset:0
ds_read_b64 v[158:159], v22 offset:41472
ds_read_b32 v140, v20
v_xor_b32 v51, 0x10000, v51
v_xor_b32 v52, 0x10000, v52
ds_write_b128 v23, v[134:137] offset:41984
s_waitcnt lgkmcnt(8)
v_fma_f32 v206, v90, v26, v114
v_fma_f32 v207, v91, v26, v115
v_fma_f32 v208, v92, v26, v116
v_fma_f32 v209, v93, v26, v117
v_cvt_pk_bf16_f32 v146, v206, v207
v_cvt_pk_bf16_f32 v147, v208, v209
s_waitcnt lgkmcnt(0)
s_barrier
v_fma_f32 v206, v94, v194, v118
v_fma_f32 v207, v94, v195, v119
v_fma_f32 v208, v94, v196, v120
v_fma_f32 v209, v94, v197, v121
v_cvt_pk_bf16_f32 v154, v206, v207
v_cvt_pk_bf16_f32 v155, v208, v209
v_fma_f32 v206, v86, v26, v110
v_fma_f32 v207, v87, v26, v111
v_fma_f32 v208, v88, v26, v112
v_fma_f32 v209, v89, v26, v113
v_cvt_pk_bf16_f32 v138, v206, v207
v_cvt_pk_bf16_f32 v139, v208, v209
s_waitcnt vmcnt(4) lgkmcnt(0)
s_barrier
s_mov_b32 s70, 32
s_waitcnt lgkmcnt(0)
s_nop 1
s_branch .Lqa_in31
.p2align 6
.Lqa_top46:
s_waitcnt lgkmcnt(9)
v_mfma_f32_16x16x32_bf16 v[134:137], v[0:3], v[0:3], 0
s_waitcnt lgkmcnt(8)
ds_read_b128 v[54:57], v52 offset:4096
ds_read_b32 v8, v30
v_mfma_f32_16x16x32_bf16 v[134:137], v[4:7], v[4:7], v[134:137]
ds_read_b128 v[194:197], v25 offset:64
ds_read_b64 v[160:161], v22 offset:41472
v_mfma_f32_16x16x32_bf16 v[110:113], a[0:3], v[0:3], 0
s_waitcnt lgkmcnt(8)
ds_read_b128 v[58:61], v51 offset:6144
ds_read_b32 v144, v20
v_mfma_f32_16x16x32_bf16 v[134:137], v[96:99], v[96:99], v[134:137]
s_waitcnt lgkmcnt(8)
ds_read_b128 v[62:65], v52 offset:6144
v_add_u32 v46, 1, v46
v_mfma_f32_16x16x32_bf16 v[134:137], v[100:103], v[100:103], v[134:137]
s_mov_b64 exec, 65535
global_store_dwordx4 v16, v[198:201], s[38:39] sc1
s_mov_b64 exec, -1
v_mfma_f32_16x16x32_bf16 v[114:117], a[4:7], v[0:3], 0
s_waitcnt lgkmcnt(7)
ds_read_b128 v[66:69], v51 offset:8192
v_add_u32 v16, 18432, v16
v_mfma_f32_16x16x32_bf16 v[134:137], v[104:107], v[104:107], v[134:137]
s_waitcnt lgkmcnt(7)
ds_read_b128 v[70:73], v52 offset:8192
v_fma_f32 v206, v90, v26, v126
v_mfma_f32_16x16x32_bf16 v[134:137], v[54:57], v[54:57], v[134:137]
v_fma_f32 v207, v91, v26, v127
v_fma_f32 v208, v92, v26, v128
v_mfma_f32_16x16x32_bf16 v[118:121], v[0:3], a[8:11], 0
s_waitcnt lgkmcnt(4)
ds_read_b128 v[74:77], v51 offset:10240
v_fma_f32 v209, v93, v26, v129
v_mfma_f32_16x16x32_bf16 v[134:137], v[58:61], v[58:61], v[134:137]
s_waitcnt lgkmcnt(3)
ds_read_b128 v[78:81], v52 offset:10240
v_cvt_pk_bf16_f32 v150, v206, v207
v_mfma_f32_16x16x32_bf16 v[134:137], v[62:65], v[62:65], v[134:137]
v_cvt_pk_bf16_f32 v151, v208, v209
v_max_f32 v47, v186, v187
v_mfma_f32_16x16x32_bf16 v[110:113], a[12:15], v[4:7], v[110:113]
ds_read_b128 v[82:85], v51 offset:12288
v_mfma_f32_16x16x32_bf16 v[182:185], v[150:153], v[138:141], v[32:35]
s_waitcnt lgkmcnt(4)
v_max_f32 v48, v188, v189
v_fma_f32 v206, v94, v194, v130
v_mfma_f32_16x16x32_bf16 v[134:137], v[66:69], v[66:69], v[134:137]
s_xor_b32 s49, s49, 0x10000
ds_read_b128 v[162:165], v52 offset:12288
s_waitcnt lgkmcnt(4)
v_mfma_f32_16x16x32_bf16 v[134:137], v[70:73], v[70:73], v[134:137]
s_add_u32 m0, s49, 0
v_mul_f32 v95, 0x3eae1a93, v95
v_mul_f32 v9, v255, v95
v_mfma_f32_16x16x32_bf16 v[114:117], a[16:19], v[4:7], v[114:117]
s_waitcnt lgkmcnt(3)
ds_read_b128 v[166:169], v51 offset:14336
v_mul_f32 v10, v8, v95
v_mfma_f32_16x16x32_bf16 v[134:137], v[74:77], v[74:77], v[134:137]
s_waitcnt lgkmcnt(3)
ds_read_b128 v[170:173], v52 offset:14336
v_max3_f32 v47, v182, v183, v47
v_mfma_f32_16x16x32_bf16 v[134:137], v[78:81], v[78:81], v[134:137]
v_max3_f32 v48, v184, v185, v48
v_mul_f32 v47, v47, v9
v_mfma_f32_16x16x32_bf16 v[118:121], v[4:7], a[20:23], v[118:121]
s_waitcnt lgkmcnt(3)
ds_read_b128 v[174:177], v51 offset:16384
v_mul_f32 v48, v48, v10
v_mfma_f32_16x16x32_bf16 v[134:137], v[82:85], v[82:85], v[134:137]
s_waitcnt lgkmcnt(3)
ds_read_b128 v[178:181], v52 offset:16384
global_load_lds_dwordx4 v11, s[42:43] offset:-2048 sc1
v_mfma_f32_16x16x32_bf16 v[134:137], v[162:165], v[162:165], v[134:137]
v_max_f32 v49, v47, v48
v_mov_b32 v50, v49
v_mfma_f32_16x16x32_bf16 v[110:113], a[24:27], v[96:99], v[110:113]
s_waitcnt lgkmcnt(3)
v_permlane16_swap_b32 v49, v50
v_mfma_f32_16x16x32_bf16 v[134:137], v[166:169], v[166:169], v[134:137]
s_waitcnt lgkmcnt(2)
v_max_f32 v49, v49, v50
v_mov_b32 v50, v49
v_mfma_f32_16x16x32_bf16 v[134:137], v[170:173], v[170:173], v[134:137]
s_nop 0
v_permlane32_swap_b32 v49, v50
v_mfma_f32_16x16x32_bf16 v[114:117], a[28:31], v[96:99], v[114:117]
s_waitcnt lgkmcnt(1)
v_max_f32 v49, v49, v50
v_sub_f32 v47, v47, v49
v_mfma_f32_16x16x32_bf16 v[134:137], v[174:177], v[174:177], v[134:137]
s_waitcnt lgkmcnt(0)
v_sub_f32 v48, v48, v49
v_fma_f32 v207, v94, v195, v131
v_mfma_f32_16x16x32_bf16 v[134:137], v[178:181], v[178:181], v[134:137]
v_exp_f32 v210, v47
v_mfma_f32_16x16x32_bf16 v[118:121], v[96:99], a[32:35], v[118:121]
global_load_lds_dwordx4 v12, s[42:43] offset:-1024 sc1
v_fma_f32 v208, v94, v196, v132
v_mfma_f32_16x16x32_bf16 v[110:113], a[36:39], v[100:103], v[110:113]
v_exp_f32 v211, v48
v_mfma_f32_16x16x32_bf16 v[114:117], a[40:43], v[100:103], v[114:117]
v_add_f32 v49, v210, v211
v_mov_b32 v50, v49
v_mfma_f32_16x16x32_bf16 v[118:121], v[100:103], a[44:47], v[118:121]
ds_write_b128 v23, v[134:137] offset:41984
v_mfma_f32_16x16x32_bf16 v[110:113], a[48:51], v[104:107], v[110:113]
v_mul_f32 v210, v210, v255
v_fma_f32 v209, v94, v197, v133
v_mfma_f32_16x16x32_bf16 v[114:117], a[52:55], v[104:107], v[114:117]
v_permlane16_swap_b32 v49, v50
v_mfma_f32_16x16x32_bf16 v[118:121], v[104:107], a[56:59], v[118:121]
v_add_f32 v49, v49, v50
v_mov_b32 v50, v49
v_mfma_f32_16x16x32_bf16 v[110:113], a[60:63], v[54:57], v[110:113]
v_mul_f32 v211, v211, v8
v_cvt_pk_bf16_f32 v156, v206, v207
v_mfma_f32_16x16x32_bf16 v[114:117], a[64:67], v[54:57], v[114:117]
global_load_lds_dwordx4 v13, s[42:43] offset:0 sc1
v_cvt_pk_bf16_f32 v157, v208, v209
v_mfma_f32_16x16x32_bf16 v[118:121], v[54:57], a[68:71], v[118:121]
v_mfma_f32_16x16x32_bf16 v[110:113], a[72:75], v[58:61], v[110:113]
s_waitcnt lgkmcnt(0)
s_barrier
v_mfma_f32_16x16x32_bf16 v[114:117], a[76:79], v[58:61], v[114:117]
v_permlane32_swap_b32 v49, v50
v_mfma_f32_16x16x32_bf16 v[118:121], v[58:61], a[80:83], v[118:121]
v_add_f32 v49, v49, v50
v_fma_f32 v206, v86, v26, v122
v_mfma_f32_16x16x32_bf16 v[110:113], a[84:87], v[62:65], v[110:113]
v_rcp_f32 v49, v49
v_mfma_f32_16x16x32_bf16 v[114:117], a[88:91], v[62:65], v[114:117]
v_mul_f32 v210, v210, v49
v_mul_f32 v211, v211, v49
v_mfma_f32_16x16x32_bf16 v[118:121], v[62:65], a[92:95], v[118:121]
v_cvt_pk_bf16_f32 v210, v210, v210
v_cvt_pk_bf16_f32 v211, v211, v211
v_mfma_f32_16x16x32_bf16 v[110:113], a[96:99], v[66:69], v[110:113]
global_load_lds_dwordx4 v14, s[42:43] offset:1024 sc1
s_add_u32 s42, s42, s46
s_addc_u32 s43, s43, s47
v_mfma_f32_16x16x32_bf16 v[114:117], a[100:103], v[66:69], v[114:117]
v_and_b32 v192, v210, v40
v_and_b32 v193, v211, v41
v_mfma_f32_16x16x32_bf16 v[118:121], v[66:69], a[104:107], v[118:121]
v_and_b32 v190, v210, v42
v_and_b32 v191, v211, v43
v_mfma_f32_16x16x32_bf16 v[110:113], a[108:111], v[70:73], v[110:113]
s_nop 0
v_mfma_f32_16x16x32_bf16 v[186:189], v[154:157], v[190:193], 0
v_mfma_f32_16x16x32_bf16 v[182:185], v[158:161], v[190:193], 0
v_fma_f32 v207, v87, v26, v123
v_fma_f32 v208, v88, v26, v124
v_mfma_f32_16x16x32_bf16 v[114:117], a[112:115], v[70:73], v[114:117]
v_fma_f32 v209, v89, v26, v125
v_cvt_pk_bf16_f32 v142, v206, v207
v_mfma_f32_16x16x32_bf16 v[118:121], v[70:73], a[116:119], v[118:121]
v_cvt_pk_bf16_f32 v143, v208, v209
v_mfma_f32_16x16x32_bf16 v[110:113], a[120:123], v[74:77], v[110:113]
v_mfma_f32_16x16x32_bf16 v[114:117], a[124:127], v[74:77], v[114:117]
v_cvt_pk_bf16_f32 v108, v186, v187
v_cvt_pk_bf16_f32 v109, v188, v189
v_mfma_f32_16x16x32_bf16 v[118:121], v[74:77], a[128:131], v[118:121]
ds_write_b64 v18, v[108:109]
v_mfma_f32_16x16x32_bf16 v[110:113], a[132:135], v[78:81], v[110:113]
v_cvt_pk_bf16_f32 v210, v182, v183
v_mfma_f32_16x16x32_bf16 v[114:117], a[136:139], v[78:81], v[114:117]
ds_write_b32 v19, v210
v_mfma_f32_16x16x32_bf16 v[118:121], v[78:81], a[140:143], v[118:121]
v_mfma_f32_16x16x32_bf16 v[110:113], a[144:147], v[82:85], v[110:113]
v_mfma_f32_16x16x32_bf16 v[114:117], a[148:151], v[82:85], v[114:117]
v_mfma_f32_16x16x32_bf16 v[118:121], v[82:85], a[152:155], v[118:121]
v_mfma_f32_16x16x32_bf16 v[110:113], a[156:159], v[162:165], v[110:113]
v_add_lshl_u32 v253, v46, 1, 7
v_bfi_b32 v253, s69, v253, v44
v_mfma_f32_16x16x32_bf16 v[114:117], a[160:163], v[162:165], v[114:117]
global_load_dword v254, v253, s[38:39] sc1
v_mfma_f32_16x16x32_bf16 v[118:121], v[162:165], a[164:167], v[118:121]
s_waitcnt vmcnt(6)
v_sub_u32 v53, v53, v46
v_cmp_lt_u32 vcc, 0x7f, v53
v_mfma_f32_16x16x32_bf16 v[110:113], a[168:171], v[166:169], v[110:113]
v_mfma_f32_16x16x32_bf16 v[114:117], a[172:175], v[166:169], v[114:117]
v_mfma_f32_16x16x32_bf16 v[118:121], v[166:169], a[176:179], v[118:121]
s_and_b64 vcc, vcc, s[76:77]
s_cbranch_vccnz .Lqa_sp34
.Lqa_rt35:
v_mfma_f32_16x16x32_bf16 v[110:113], a[180:183], v[170:173], v[110:113]
v_mfma_f32_16x16x32_bf16 v[114:117], a[184:187], v[170:173], v[114:117]
s_waitcnt vmcnt(5) lgkmcnt(0)
s_barrier
v_mfma_f32_16x16x32_bf16 v[118:121], v[170:173], a[188:191], v[118:121]
ds_read_b128 v[0:3], v51 offset:18432
v_mfma_f32_16x16x32_bf16 v[110:113], a[192:195], v[174:177], v[110:113]
ds_read_b128 v[4:7], v52 offset:18432
ds_read_b128 v[198:201], v17 offset:0
v_mfma_f32_16x16x32_bf16 v[114:117], a[196:199], v[174:177], v[114:117]
ds_read_b32 v26, v24 offset:0
ds_read_b32 v148, v21
v_mfma_f32_16x16x32_bf16 v[186:189], v[150:153], v[142:145], v[36:39]
v_mfma_f32_16x16x32_bf16 v[118:121], v[174:177], a[200:203], v[118:121]
ds_read_b128 v[96:99], v51 offset:20480
ds_read_b32 v95, v24 offset:192
v_mfma_f32_16x16x32_bf16 v[110:113], a[204:207], v[178:181], v[110:113]
ds_read_b128 v[100:103], v52 offset:20480
ds_read_b32 v255, v27
v_mfma_f32_16x16x32_bf16 v[114:117], a[208:211], v[178:181], v[114:117]
ds_read_b32 v8, v28
ds_read_b128 v[194:197], v25 offset:0
v_mfma_f32_16x16x32_bf16 v[118:121], v[178:181], a[212:215], v[118:121]
s_waitcnt lgkmcnt(10)
ds_read_b128 v[104:107], v51 offset:22528
ds_read_b64 v[158:159], v22 offset:41472
v_mfma_f32_16x16x32_bf16 v[134:137], v[0:3], v[0:3], 0
s_waitcnt lgkmcnt(11)
ds_read_b128 v[54:57], v52 offset:22528
ds_read_b32 v140, v20
v_mfma_f32_16x16x32_bf16 v[134:137], v[4:7], v[4:7], v[134:137]
v_add_u32 v46, 1, v46
v_max_f32 v47, v186, v187
v_mfma_f32_16x16x32_bf16 v[122:125], a[0:3], v[0:3], 0
s_waitcnt lgkmcnt(11)
ds_read_b128 v[58:61], v51 offset:24576
v_fma_f32 v206, v90, v26, v114
s_waitcnt lgkmcnt(10)
v_mfma_f32_16x16x32_bf16 v[134:137], v[96:99], v[96:99], v[134:137]
s_waitcnt lgkmcnt(8)
ds_read_b128 v[62:65], v52 offset:24576
v_fma_f32 v207, v91, v26, v115
v_mfma_f32_16x16x32_bf16 v[134:137], v[100:103], v[100:103], v[134:137]
s_mov_b64 exec, 65535
global_store_dwordx4 v16, v[198:201], s[38:39] sc1
s_mov_b64 exec, -1
v_mfma_f32_16x16x32_bf16 v[126:129], a[4:7], v[0:3], 0
s_waitcnt lgkmcnt(5)
ds_read_b128 v[66:69], v51 offset:26624
v_add_u32 v16, 18432, v16
v_mfma_f32_16x16x32_bf16 v[134:137], v[104:107], v[104:107], v[134:137]
s_waitcnt lgkmcnt(4)
ds_read_b128 v[70:73], v52 offset:26624
v_fma_f32 v208, v92, v26, v116
v_mfma_f32_16x16x32_bf16 v[134:137], v[54:57], v[54:57], v[134:137]
v_fma_f32 v209, v93, v26, v117
v_cvt_pk_bf16_f32 v146, v206, v207
v_mfma_f32_16x16x32_bf16 v[130:133], v[0:3], a[8:11], 0
s_waitcnt lgkmcnt(3)
ds_read_b128 v[74:77], v51 offset:28672
v_cvt_pk_bf16_f32 v147, v208, v209
v_mfma_f32_16x16x32_bf16 v[134:137], v[58:61], v[58:61], v[134:137]
ds_read_b128 v[78:81], v52 offset:28672
v_mfma_f32_16x16x32_bf16 v[182:185], v[146:149], v[142:145], v[202:205]
s_waitcnt lgkmcnt(4)
v_max_f32 v48, v188, v189
v_fma_f32 v206, v94, v194, v118
v_mfma_f32_16x16x32_bf16 v[134:137], v[62:65], v[62:65], v[134:137]
v_fma_f32 v207, v94, v195, v119
v_fma_f32 v208, v94, v196, v120
v_mfma_f32_16x16x32_bf16 v[122:125], a[12:15], v[4:7], v[122:125]
s_waitcnt lgkmcnt(3)
ds_read_b128 v[82:85], v51 offset:30720
v_mul_f32 v95, 0x3eae1a93, v95
v_mfma_f32_16x16x32_bf16 v[134:137], v[66:69], v[66:69], v[134:137]
s_add_u32 m0, s49, 18432
ds_read_b128 v[162:165], v52 offset:30720
s_waitcnt lgkmcnt(4)
v_mfma_f32_16x16x32_bf16 v[134:137], v[70:73], v[70:73], v[134:137]
v_mul_f32 v9, v255, v95
v_mul_f32 v10, v8, v95
v_mfma_f32_16x16x32_bf16 v[126:129], a[16:19], v[4:7], v[126:129]
s_waitcnt lgkmcnt(3)
)"
R"(ds_read_b128 v[166:169], v51 offset:32768
v_max3_f32 v47, v182, v183, v47
v_mfma_f32_16x16x32_bf16 v[134:137], v[74:77], v[74:77], v[134:137]
s_waitcnt lgkmcnt(3)
ds_read_b128 v[170:173], v52 offset:32768
v_max3_f32 v48, v184, v185, v48
v_mfma_f32_16x16x32_bf16 v[134:137], v[78:81], v[78:81], v[134:137]
v_mul_f32 v47, v47, v9
v_mul_f32 v48, v48, v10
v_mfma_f32_16x16x32_bf16 v[130:133], v[4:7], a[20:23], v[130:133]
s_waitcnt lgkmcnt(3)
ds_read_b128 v[174:177], v51 offset:34816
global_load_lds_dwordx4 v11, s[42:43] offset:-2048 sc1
v_mfma_f32_16x16x32_bf16 v[134:137], v[82:85], v[82:85], v[134:137]
s_waitcnt lgkmcnt(3)
ds_read_b128 v[178:181], v52 offset:34816
v_max_f32 v49, v47, v48
v_mfma_f32_16x16x32_bf16 v[134:137], v[162:165], v[162:165], v[134:137]
v_mov_b32 v50, v49
v_fma_f32 v209, v94, v197, v121
v_mfma_f32_16x16x32_bf16 v[122:125], a[24:27], v[96:99], v[122:125]
s_waitcnt lgkmcnt(3)
v_permlane16_swap_b32 v49, v50
v_mfma_f32_16x16x32_bf16 v[134:137], v[166:169], v[166:169], v[134:137]
s_waitcnt lgkmcnt(2)
v_max_f32 v49, v49, v50
v_mov_b32 v50, v49
v_mfma_f32_16x16x32_bf16 v[134:137], v[170:173], v[170:173], v[134:137]
s_nop 0
v_permlane32_swap_b32 v49, v50
v_mfma_f32_16x16x32_bf16 v[126:129], a[28:31], v[96:99], v[126:129]
s_waitcnt lgkmcnt(1)
v_max_f32 v49, v49, v50
v_sub_f32 v47, v47, v49
v_mfma_f32_16x16x32_bf16 v[134:137], v[174:177], v[174:177], v[134:137]
s_waitcnt lgkmcnt(0)
v_sub_f32 v48, v48, v49
v_cvt_pk_bf16_f32 v154, v206, v207
v_mfma_f32_16x16x32_bf16 v[134:137], v[178:181], v[178:181], v[134:137]
global_load_lds_dwordx4 v12, s[42:43] offset:-1024 sc1
v_cvt_pk_bf16_f32 v155, v208, v209
v_mfma_f32_16x16x32_bf16 v[130:133], v[96:99], a[32:35], v[130:133]
v_exp_f32 v210, v47
v_mfma_f32_16x16x32_bf16 v[122:125], a[36:39], v[100:103], v[122:125]
v_exp_f32 v211, v48
v_mfma_f32_16x16x32_bf16 v[126:129], a[40:43], v[100:103], v[126:129]
v_add_f32 v49, v210, v211
v_mov_b32 v50, v49
v_mfma_f32_16x16x32_bf16 v[130:133], v[100:103], a[44:47], v[130:133]
ds_write_b128 v23, v[134:137] offset:41984
v_mfma_f32_16x16x32_bf16 v[122:125], a[48:51], v[104:107], v[122:125]
v_mul_f32 v210, v210, v255
v_fma_f32 v206, v86, v26, v110
v_mfma_f32_16x16x32_bf16 v[126:129], a[52:55], v[104:107], v[126:129]
v_permlane16_swap_b32 v49, v50
v_mfma_f32_16x16x32_bf16 v[130:133], v[104:107], a[56:59], v[130:133]
v_add_f32 v49, v49, v50
v_mov_b32 v50, v49
v_mfma_f32_16x16x32_bf16 v[122:125], a[60:63], v[54:57], v[122:125]
global_load_lds_dwordx4 v13, s[42:43] offset:0 sc1
v_mul_f32 v211, v211, v8
v_mfma_f32_16x16x32_bf16 v[126:129], a[64:67], v[54:57], v[126:129]
v_permlane32_swap_b32 v49, v50
v_mfma_f32_16x16x32_bf16 v[130:133], v[54:57], a[68:71], v[130:133]
v_mfma_f32_16x16x32_bf16 v[122:125], a[72:75], v[58:61], v[122:125]
s_waitcnt lgkmcnt(0)
s_barrier
v_mfma_f32_16x16x32_bf16 v[126:129], a[76:79], v[58:61], v[126:129]
v_add_f32 v49, v49, v50
v_fma_f32 v207, v87, v26, v111
v_mfma_f32_16x16x32_bf16 v[130:133], v[58:61], a[80:83], v[130:133]
v_rcp_f32 v49, v49
v_mfma_f32_16x16x32_bf16 v[122:125], a[84:87], v[62:65], v[122:125]
v_mul_f32 v210, v210, v49
v_mul_f32 v211, v211, v49
v_mfma_f32_16x16x32_bf16 v[126:129], a[88:91], v[62:65], v[126:129]
v_cvt_pk_bf16_f32 v210, v210, v210
v_cvt_pk_bf16_f32 v211, v211, v211
v_mfma_f32_16x16x32_bf16 v[130:133], v[62:65], a[92:95], v[130:133]
global_load_lds_dwordx4 v14, s[42:43] offset:1024 sc1
s_sub_u32 s42, s42, 18432
s_subb_u32 s43, s43, 0
v_mfma_f32_16x16x32_bf16 v[122:125], a[96:99], v[66:69], v[122:125]
v_and_b32 v190, v210, v40
v_and_b32 v191, v211, v41
v_mfma_f32_16x16x32_bf16 v[126:129], a[100:103], v[66:69], v[126:129]
v_and_b32 v192, v210, v42
v_and_b32 v193, v211, v43
v_mfma_f32_16x16x32_bf16 v[130:133], v[66:69], a[104:107], v[130:133]
s_nop 0
v_mfma_f32_16x16x32_bf16 v[186:189], v[154:157], v[190:193], 0
v_mfma_f32_16x16x32_bf16 v[182:185], v[158:161], v[190:193], 0
v_fma_f32 v208, v88, v26, v112
v_fma_f32 v209, v89, v26, v113
v_mfma_f32_16x16x32_bf16 v[122:125], a[108:111], v[70:73], v[122:125]
v_cvt_pk_bf16_f32 v138, v206, v207
v_cvt_pk_bf16_f32 v139, v208, v209
v_mfma_f32_16x16x32_bf16 v[126:129], a[112:115], v[70:73], v[126:129]
v_mfma_f32_16x16x32_bf16 v[130:133], v[70:73], a[116:119], v[130:133]
v_mfma_f32_16x16x32_bf16 v[122:125], a[120:123], v[74:77], v[122:125]
v_cvt_pk_bf16_f32 v108, v186, v187
v_cvt_pk_bf16_f32 v109, v188, v189
v_mfma_f32_16x16x32_bf16 v[126:129], a[124:127], v[74:77], v[126:129]
ds_write_b64 v18, v[108:109]
v_mfma_f32_16x16x32_bf16 v[130:133], v[74:77], a[128:131], v[130:133]
v_cvt_pk_bf16_f32 v210, v182, v183
v_mfma_f32_16x16x32_bf16 v[122:125], a[132:135], v[78:81], v[122:125]
ds_write_b32 v19, v210
v_mfma_f32_16x16x32_bf16 v[126:129], a[136:139], v[78:81], v[126:129]
v_mfma_f32_16x16x32_bf16 v[130:133], v[78:81], a[140:143], v[130:133]
v_mfma_f32_16x16x32_bf16 v[122:125], a[144:147], v[82:85], v[122:125]
v_mfma_f32_16x16x32_bf16 v[126:129], a[148:151], v[82:85], v[126:129]
v_mfma_f32_16x16x32_bf16 v[130:133], v[82:85], a[152:155], v[130:133]
v_mfma_f32_16x16x32_bf16 v[122:125], a[156:159], v[162:165], v[122:125]
v_add_lshl_u32 v253, v46, 1, 7
v_bfi_b32 v253, s69, v253, v45
v_mfma_f32_16x16x32_bf16 v[126:129], a[160:163], v[162:165], v[126:129]
global_load_dword v53, v253, s[38:39] sc1
v_mfma_f32_16x16x32_bf16 v[130:133], v[162:165], a[164:167], v[130:133]
s_waitcnt vmcnt(6)
v_sub_u32 v254, v254, v46
v_cmp_lt_u32 vcc, 0x7f, v254
v_mfma_f32_16x16x32_bf16 v[122:125], a[168:171], v[166:169], v[122:125]
v_mfma_f32_16x16x32_bf16 v[126:129], a[172:175], v[166:169], v[126:129]
v_xor_b32 v51, 0x10000, v51
v_xor_b32 v52, 0x10000, v52
v_mfma_f32_16x16x32_bf16 v[130:133], v[166:169], a[176:179], v[130:133]
s_and_b64 vcc, vcc, s[76:77]
s_cbranch_vccnz .Lqa_sp36
.Lqa_rt37:
v_mfma_f32_16x16x32_bf16 v[122:125], a[180:183], v[170:173], v[122:125]
v_mfma_f32_16x16x32_bf16 v[126:129], a[184:187], v[170:173], v[126:129]
s_waitcnt vmcnt(5) lgkmcnt(0)
s_barrier
.Lqa_in31:
v_mfma_f32_16x16x32_bf16 v[130:133], v[170:173], a[188:191], v[130:133]
s_cmp_eq_u32 s70, s73
s_cbranch_scc1 .Lqa_hk32
.Lqa_hr33:
ds_read_b128 v[0:3], v51 offset:0
v_mfma_f32_16x16x32_bf16 v[122:125], a[192:195], v[174:177], v[122:125]
ds_read_b128 v[4:7], v52 offset:0
ds_read_b128 v[198:201], v17 offset:0
v_mfma_f32_16x16x32_bf16 v[126:129], a[196:199], v[174:177], v[126:129]
ds_read_b32 v26, v24 offset:64
ds_read_b32 v152, v21
v_mfma_f32_16x16x32_bf16 v[186:189], v[146:149], v[138:141], v[36:39]
v_mfma_f32_16x16x32_bf16 v[130:133], v[174:177], a[200:203], v[130:133]
ds_read_b128 v[96:99], v51 offset:2048
ds_read_b32 v95, v24 offset:128
v_mfma_f32_16x16x32_bf16 v[122:125], a[204:207], v[178:181], v[122:125]
ds_read_b128 v[100:103], v52 offset:2048
ds_read_b32 v255, v29
v_mfma_f32_16x16x32_bf16 v[126:129], a[208:211], v[178:181], v[126:129]
v_mfma_f32_16x16x32_bf16 v[130:133], v[178:181], a[212:215], v[130:133]
s_sub_u32 s70, s70, 1
ds_read_b128 v[104:107], v51 offset:4096
s_cbranch_scc0 .Lqa_top46
s_waitcnt lgkmcnt(0)
ds_read_b32 v8, v30
ds_read_b128 v[194:197], v25 offset:64
ds_read_b64 v[160:161], v22 offset:41472
ds_read_b32 v144, v20
s_mov_b64 exec, 65535
global_store_dwordx4 v16, v[198:201], s[38:39] sc1
s_mov_b64 exec, -1
v_add_u32 v16, 18432, v16
v_fma_f32 v206, v90, v26, v126
v_fma_f32 v207, v91, v26, v127
v_fma_f32 v208, v92, v26, v128
v_fma_f32 v209, v93, v26, v129
v_cvt_pk_bf16_f32 v150, v206, v207
v_cvt_pk_bf16_f32 v151, v208, v209
v_max_f32 v47, v186, v187
s_nop 0
v_mfma_f32_16x16x32_bf16 v[182:185], v[150:153], v[138:141], v[32:35]
s_waitcnt lgkmcnt(2)
v_max_f32 v48, v188, v189
v_fma_f32 v206, v94, v194, v130
v_mul_f32 v95, 0x3eae1a93, v95
v_mul_f32 v9, v255, v95
v_mul_f32 v10, v8, v95
s_nop 1
v_max3_f32 v47, v182, v183, v47
v_max3_f32 v48, v184, v185, v48
v_mul_f32 v47, v47, v9
v_mul_f32 v48, v48, v10
v_max_f32 v49, v47, v48
v_mov_b32 v50, v49
s_nop 1
v_permlane16_swap_b32 v49, v50
v_max_f32 v49, v49, v50
v_mov_b32 v50, v49
s_nop 1
v_permlane32_swap_b32 v49, v50
v_max_f32 v49, v49, v50
v_sub_f32 v47, v47, v49
v_sub_f32 v48, v48, v49
v_fma_f32 v207, v94, v195, v131
v_exp_f32 v210, v47
v_fma_f32 v208, v94, v196, v132
v_exp_f32 v211, v48
s_nop 0
v_add_f32 v49, v210, v211
v_mov_b32 v50, v49
v_mul_f32 v210, v210, v255
v_fma_f32 v209, v94, v197, v133
v_permlane16_swap_b32 v49, v50
v_add_f32 v49, v49, v50
v_mov_b32 v50, v49
v_mul_f32 v211, v211, v8
v_cvt_pk_bf16_f32 v156, v206, v207
v_cvt_pk_bf16_f32 v157, v208, v209
s_waitcnt lgkmcnt(0)
s_barrier
v_permlane32_swap_b32 v49, v50
v_add_f32 v49, v49, v50
v_fma_f32 v206, v86, v26, v122
v_rcp_f32 v49, v49
s_nop 0
v_mul_f32 v210, v210, v49
v_mul_f32 v211, v211, v49
v_cvt_pk_bf16_f32 v210, v210, v210
v_cvt_pk_bf16_f32 v211, v211, v211
v_and_b32 v192, v210, v40
v_and_b32 v193, v211, v41
v_and_b32 v190, v210, v42
v_and_b32 v191, v211, v43
s_nop 1
v_mfma_f32_16x16x32_bf16 v[186:189], v[154:157], v[190:193], 0
v_mfma_f32_16x16x32_bf16 v[182:185], v[158:161], v[190:193], 0
v_fma_f32 v207, v87, v26, v123
v_fma_f32 v208, v88, v26, v124
v_fma_f32 v209, v89, v26, v125
v_cvt_pk_bf16_f32 v142, v206, v207
v_cvt_pk_bf16_f32 v143, v208, v209
s_nop 1
v_cvt_pk_bf16_f32 v108, v186, v187
v_cvt_pk_bf16_f32 v109, v188, v189
ds_write_b64 v18, v[108:109]
v_cvt_pk_bf16_f32 v210, v182, v183
ds_write_b32 v19, v210
s_waitcnt vmcnt(0)
s_waitcnt lgkmcnt(0)
s_barrier
ds_read_b128 v[198:201], v17 offset:0
s_waitcnt lgkmcnt(0)
global_store_dwordx4 v16, v[198:201], s[38:39] sc1
s_waitcnt vmcnt(0)
s_barrier
s_endpgm
.Lqa_w4:
s_mov_b32 s70, 500
s_branch .Lqa_hk49
.Lqa_bk58:
v_add_u32 v255, 0x10000, v9
v_add_u32 v162, 0x20000, v9
v_add_u32 v163, 0x10000, v10
v_add_u32 v164, 0x20000, v10
v_mov_b32 v106, 0x3F803F80
v_mov_b32 v107, 0x3F803F80
v_mov_b32 v108, 0x3F803F80
v_mov_b32 v109, 0x3F803F80
v_mov_b32 v141, 0
v_mov_b32 v145, 0
v_mov_b32 v149, 0
v_mov_b32 v153, 0
v_mov_b32 v165, 46080
s_waitcnt lgkmcnt(0)
s_mul_i32 s37, s37, 0x9E3779B1
s_add_u32 s37, s37, 0x7F4A7C15
v_add_u32 v46, s37, v46
v_add_u32 v48, s37, v48
v_add_u32 v50, s37, v50
ds_write_b32 v165, v141
s_mov_b64 s[76:77], 0
s_mov_b32 s73, 18
s_add_u32 m0, s48, 4096
s_nop 0
global_load_lds_dwordx4 v0, s[40:41] offset:-4096
global_load_lds_dwordx4 v1, s[40:41] offset:-3072
global_load_lds_dwordx4 v2, s[40:41] offset:-2048
global_load_lds_dwordx4 v3, s[40:41] offset:-1024
global_load_lds_dwordx4 v4, s[40:41] offset:0
global_load_lds_dwordx4 v5, s[40:41] offset:1024
global_load_lds_dwordx4 v6, s[40:41] offset:2048
global_load_lds_dwordx4 v7, s[40:41] offset:3072
s_add_u32 m0, s48, 12288
s_nop 0
global_load_lds_dwordx4 v8, s[40:41] offset:-4096
s_add_u32 s40, s40, 221184
s_addc_u32 s41, s41, 0
s_add_u32 m0, s48, 40960
s_nop 0
global_load_lds_dwordx4 v0, s[40:41] offset:-4096
global_load_lds_dwordx4 v1, s[40:41] offset:-3072
global_load_lds_dwordx4 v2, s[40:41] offset:-2048
global_load_lds_dwordx4 v3, s[40:41] offset:-1024
global_load_lds_dwordx4 v4, s[40:41] offset:0
global_load_lds_dwordx4 v5, s[40:41] offset:1024
global_load_lds_dwordx4 v6, s[40:41] offset:2048
global_load_lds_dwordx4 v7, s[40:41] offset:3072
s_add_u32 m0, s48, 49152
s_nop 0
global_load_lds_dwordx4 v8, s[40:41] offset:-4096
s_add_u32 s40, s40, 221184
s_addc_u32 s41, s41, 0
s_waitcnt vmcnt(9) lgkmcnt(0)
s_barrier
s_add_u32 m0, s48, 77824
ds_read_b64_tr_b16 a[0:1], v9 offset:47104
ds_read_b64_tr_b16 a[2:3], v9 offset:49408
global_load_lds_dwordx4 v0, s[40:41] offset:-4096
ds_read_b64_tr_b16 a[4:5], v9 offset:47296
global_load_lds_dwordx4 v1, s[40:41] offset:-3072
ds_read_b64_tr_b16 a[6:7], v9 offset:49600
global_load_lds_dwordx4 v2, s[40:41] offset:-2048
ds_read_b64_tr_b16 a[8:9], v9 offset:47488
global_load_lds_dwordx4 v3, s[40:41] offset:-1024
ds_read_b64_tr_b16 a[10:11], v9 offset:49792
ds_read_b128 v[166:169], v51 offset:0
global_load_lds_dwordx4 v4, s[40:41] offset:0
ds_read_b128 v[174:177], v51 offset:18432
global_load_lds_dwordx4 v5, s[40:41] offset:1024
ds_read_b64_tr_b16 a[12:13], v255 offset:0
global_load_lds_dwordx4 v6, s[40:41] offset:2048
ds_read_b64_tr_b16 a[14:15], v255 offset:2304
global_load_lds_dwordx4 v7, s[40:41] offset:3072
ds_read_b64_tr_b16 a[16:17], v255 offset:192
ds_read_b64_tr_b16 a[18:19], v255 offset:2496
s_add_u32 m0, s48, 86016
ds_read_b64_tr_b16 a[20:21], v255 offset:384
global_load_lds_dwordx4 v8, s[40:41] offset:-4096
ds_read_b64_tr_b16 a[22:23], v255 offset:2688
s_add_u32 s40, s40, 221184
ds_read_b128 v[170:173], v52 offset:0
s_addc_u32 s41, s41, 0
ds_read_b128 v[178:181], v52 offset:18432
s_waitcnt vmcnt(9) lgkmcnt(0)
s_barrier
s_add_u32 m0, s48, 4096
ds_read_b64_tr_b16 a[24:25], v255 offset:18432
v_mfma_f32_16x16x32_bf16 v[86:89], a[0:3], v[106:109], 0
v_mfma_f32_16x16x32_bf16 v[90:93], a[4:7], v[106:109], 0
global_load_lds_dwordx4 v0, s[40:41] offset:-4096
ds_read_b64_tr_b16 a[26:27], v255 offset:20736
v_mfma_f32_16x16x32_bf16 v[94:97], v[106:109], a[8:11], 0
ds_read_b64_tr_b16 a[28:29], v255 offset:18624
global_load_lds_dwordx4 v1, s[40:41] offset:-3072
v_mfma_f32_16x16x32_bf16 v[110:113], a[0:3], v[166:169], 0
ds_read_b64_tr_b16 a[30:31], v255 offset:20928
v_mfma_f32_16x16x32_bf16 v[114:117], a[4:7], v[166:169], 0
global_load_lds_dwordx4 v2, s[40:41] offset:-2048
ds_read_b64_tr_b16 a[32:33], v255 offset:18816
v_mfma_f32_16x16x32_bf16 v[118:121], v[166:169], a[8:11], 0
ds_read_b64_tr_b16 a[34:35], v255 offset:21120
global_load_lds_dwordx4 v3, s[40:41] offset:-1024
v_mfma_f32_16x16x32_bf16 v[134:137], v[106:109], v[166:169], 0
ds_read_b128 v[182:185], v51 offset:2048
v_mfma_f32_16x16x32_bf16 v[122:125], a[0:3], v[174:177], 0
global_load_lds_dwordx4 v4, s[40:41] offset:0
ds_read_b128 v[190:193], v51 offset:20480
v_mfma_f32_16x16x32_bf16 v[126:129], a[4:7], v[174:177], 0
ds_read_b64_tr_b16 a[36:37], v255 offset:36864
global_load_lds_dwordx4 v5, s[40:41] offset:1024
v_mfma_f32_16x16x32_bf16 v[130:133], v[174:177], a[8:11], 0
v_mfma_f32_16x16x32_bf16 v[158:161], v[106:109], v[174:177], 0
ds_read_b64_tr_b16 a[38:39], v255 offset:39168
global_load_lds_dwordx4 v6, s[40:41] offset:2048
v_mfma_f32_16x16x32_bf16 v[86:89], a[12:15], v[106:109], v[86:89]
ds_read_b64_tr_b16 a[40:41], v255 offset:37056
v_mfma_f32_16x16x32_bf16 v[90:93], a[16:19], v[106:109], v[90:93]
global_load_lds_dwordx4 v7, s[40:41] offset:3072
ds_read_b64_tr_b16 a[42:43], v255 offset:39360
v_mfma_f32_16x16x32_bf16 v[94:97], v[106:109], a[20:23], v[94:97]
s_add_u32 m0, s48, 12288
ds_read_b64_tr_b16 a[44:45], v255 offset:37248
v_mfma_f32_16x16x32_bf16 v[110:113], a[12:15], v[170:173], v[110:113]
ds_read_b64_tr_b16 a[46:47], v255 offset:39552
v_mfma_f32_16x16x32_bf16 v[114:117], a[16:19], v[170:173], v[114:117]
global_load_lds_dwordx4 v8, s[40:41] offset:-4096
ds_read_b128 v[186:189], v52 offset:2048
v_mfma_f32_16x16x32_bf16 v[118:121], v[170:173], a[20:23], v[118:121]
s_add_u32 s40, s40, 221184
ds_read_b128 v[194:197], v52 offset:20480
v_mfma_f32_16x16x32_bf16 v[134:137], v[106:109], v[170:173], v[134:137]
v_mfma_f32_16x16x32_bf16 v[122:125], a[12:15], v[178:181], v[122:125]
v_mfma_f32_16x16x32_bf16 v[126:129], a[16:19], v[178:181], v[126:129]
s_addc_u32 s41, s41, 0
v_mfma_f32_16x16x32_bf16 v[130:133], v[178:181], a[20:23], v[130:133]
v_mfma_f32_16x16x32_bf16 v[158:161], v[106:109], v[178:181], v[158:161]
s_waitcnt vmcnt(9) lgkmcnt(0)
s_barrier
s_add_u32 m0, s48, 40960
ds_read_b64_tr_b16 a[48:49], v255 offset:55296
v_mfma_f32_16x16x32_bf16 v[86:89], a[24:27], v[106:109], v[86:89]
v_mfma_f32_16x16x32_bf16 v[90:93], a[28:31], v[106:109], v[90:93]
global_load_lds_dwordx4 v0, s[40:41] offset:-4096
ds_read_b64_tr_b16 a[50:51], v255 offset:57600
v_mfma_f32_16x16x32_bf16 v[94:97], v[106:109], a[32:35], v[94:97]
)"
R"(ds_read_b64_tr_b16 a[52:53], v255 offset:55488
global_load_lds_dwordx4 v1, s[40:41] offset:-3072
v_mfma_f32_16x16x32_bf16 v[110:113], a[24:27], v[182:185], v[110:113]
ds_read_b64_tr_b16 a[54:55], v255 offset:57792
v_mfma_f32_16x16x32_bf16 v[114:117], a[28:31], v[182:185], v[114:117]
global_load_lds_dwordx4 v2, s[40:41] offset:-2048
ds_read_b64_tr_b16 a[56:57], v255 offset:55680
v_mfma_f32_16x16x32_bf16 v[118:121], v[182:185], a[32:35], v[118:121]
ds_read_b64_tr_b16 a[58:59], v255 offset:57984
global_load_lds_dwordx4 v3, s[40:41] offset:-1024
v_mfma_f32_16x16x32_bf16 v[134:137], v[106:109], v[182:185], v[134:137]
ds_read_b128 v[166:169], v51 offset:4096
v_mfma_f32_16x16x32_bf16 v[122:125], a[24:27], v[190:193], v[122:125]
global_load_lds_dwordx4 v4, s[40:41] offset:0
ds_read_b128 v[174:177], v51 offset:22528
v_mfma_f32_16x16x32_bf16 v[126:129], a[28:31], v[190:193], v[126:129]
ds_read_b64_tr_b16 a[60:61], v162 offset:8192
global_load_lds_dwordx4 v5, s[40:41] offset:1024
v_mfma_f32_16x16x32_bf16 v[130:133], v[190:193], a[32:35], v[130:133]
v_mfma_f32_16x16x32_bf16 v[158:161], v[106:109], v[190:193], v[158:161]
ds_read_b64_tr_b16 a[62:63], v162 offset:10496
global_load_lds_dwordx4 v6, s[40:41] offset:2048
v_mfma_f32_16x16x32_bf16 v[86:89], a[36:39], v[106:109], v[86:89]
ds_read_b64_tr_b16 a[64:65], v162 offset:8384
v_mfma_f32_16x16x32_bf16 v[90:93], a[40:43], v[106:109], v[90:93]
global_load_lds_dwordx4 v7, s[40:41] offset:3072
ds_read_b64_tr_b16 a[66:67], v162 offset:10688
v_mfma_f32_16x16x32_bf16 v[94:97], v[106:109], a[44:47], v[94:97]
s_add_u32 m0, s48, 49152
ds_read_b64_tr_b16 a[68:69], v162 offset:8576
v_mfma_f32_16x16x32_bf16 v[110:113], a[36:39], v[186:189], v[110:113]
ds_read_b64_tr_b16 a[70:71], v162 offset:10880
v_mfma_f32_16x16x32_bf16 v[114:117], a[40:43], v[186:189], v[114:117]
global_load_lds_dwordx4 v8, s[40:41] offset:-4096
ds_read_b128 v[170:173], v52 offset:4096
v_mfma_f32_16x16x32_bf16 v[118:121], v[186:189], a[44:47], v[118:121]
s_add_u32 s40, s40, 221184
ds_read_b128 v[178:181], v52 offset:22528
v_mfma_f32_16x16x32_bf16 v[134:137], v[106:109], v[186:189], v[134:137]
v_mfma_f32_16x16x32_bf16 v[122:125], a[36:39], v[194:197], v[122:125]
v_mfma_f32_16x16x32_bf16 v[126:129], a[40:43], v[194:197], v[126:129]
s_addc_u32 s41, s41, 0
v_mfma_f32_16x16x32_bf16 v[130:133], v[194:197], a[44:47], v[130:133]
v_mfma_f32_16x16x32_bf16 v[158:161], v[106:109], v[194:197], v[158:161]
s_waitcnt vmcnt(9) lgkmcnt(0)
s_barrier
s_add_u32 m0, s48, 77824
ds_read_b64_tr_b16 a[72:73], v9 offset:47104
v_mfma_f32_16x16x32_bf16 v[86:89], a[48:51], v[106:109], v[86:89]
v_mfma_f32_16x16x32_bf16 v[90:93], a[52:55], v[106:109], v[90:93]
global_load_lds_dwordx4 v0, s[40:41] offset:-4096
ds_read_b64_tr_b16 a[74:75], v9 offset:49408
v_mfma_f32_16x16x32_bf16 v[94:97], v[106:109], a[56:59], v[94:97]
ds_read_b64_tr_b16 a[76:77], v9 offset:47296
global_load_lds_dwordx4 v1, s[40:41] offset:-3072
v_mfma_f32_16x16x32_bf16 v[110:113], a[48:51], v[166:169], v[110:113]
ds_read_b64_tr_b16 a[78:79], v9 offset:49600
v_mfma_f32_16x16x32_bf16 v[114:117], a[52:55], v[166:169], v[114:117]
global_load_lds_dwordx4 v2, s[40:41] offset:-2048
ds_read_b64_tr_b16 a[80:81], v9 offset:47488
v_mfma_f32_16x16x32_bf16 v[118:121], v[166:169], a[56:59], v[118:121]
ds_read_b64_tr_b16 a[82:83], v9 offset:49792
global_load_lds_dwordx4 v3, s[40:41] offset:-1024
v_mfma_f32_16x16x32_bf16 v[134:137], v[106:109], v[166:169], v[134:137]
ds_read_b128 v[182:185], v51 offset:6144
v_mfma_f32_16x16x32_bf16 v[122:125], a[48:51], v[174:177], v[122:125]
global_load_lds_dwordx4 v4, s[40:41] offset:0
ds_read_b128 v[190:193], v51 offset:24576
v_mfma_f32_16x16x32_bf16 v[126:129], a[52:55], v[174:177], v[126:129]
ds_read_b64_tr_b16 a[84:85], v255 offset:0
global_load_lds_dwordx4 v5, s[40:41] offset:1024
v_mfma_f32_16x16x32_bf16 v[130:133], v[174:177], a[56:59], v[130:133]
v_mfma_f32_16x16x32_bf16 v[158:161], v[106:109], v[174:177], v[158:161]
ds_read_b64_tr_b16 a[86:87], v255 offset:2304
global_load_lds_dwordx4 v6, s[40:41] offset:2048
v_mfma_f32_16x16x32_bf16 v[86:89], a[60:63], v[106:109], v[86:89]
ds_read_b64_tr_b16 a[88:89], v255 offset:192
v_mfma_f32_16x16x32_bf16 v[90:93], a[64:67], v[106:109], v[90:93]
global_load_lds_dwordx4 v7, s[40:41] offset:3072
ds_read_b64_tr_b16 a[90:91], v255 offset:2496
v_mfma_f32_16x16x32_bf16 v[94:97], v[106:109], a[68:71], v[94:97]
s_add_u32 m0, s48, 86016
ds_read_b64_tr_b16 a[92:93], v255 offset:384
v_mfma_f32_16x16x32_bf16 v[110:113], a[60:63], v[170:173], v[110:113]
ds_read_b64_tr_b16 a[94:95], v255 offset:2688
v_mfma_f32_16x16x32_bf16 v[114:117], a[64:67], v[170:173], v[114:117]
global_load_lds_dwordx4 v8, s[40:41] offset:-4096
ds_read_b128 v[186:189], v52 offset:6144
v_mfma_f32_16x16x32_bf16 v[118:121], v[170:173], a[68:71], v[118:121]
s_add_u32 s40, s40, 221184
ds_read_b128 v[194:197], v52 offset:24576
v_mfma_f32_16x16x32_bf16 v[134:137], v[106:109], v[170:173], v[134:137]
v_mfma_f32_16x16x32_bf16 v[122:125], a[60:63], v[178:181], v[122:125]
v_mfma_f32_16x16x32_bf16 v[126:129], a[64:67], v[178:181], v[126:129]
s_addc_u32 s41, s41, 0
v_mfma_f32_16x16x32_bf16 v[130:133], v[178:181], a[68:71], v[130:133]
v_mfma_f32_16x16x32_bf16 v[158:161], v[106:109], v[178:181], v[158:161]
s_waitcnt vmcnt(9) lgkmcnt(0)
s_barrier
s_add_u32 m0, s48, 4096
ds_read_b64_tr_b16 a[96:97], v255 offset:18432
v_mfma_f32_16x16x32_bf16 v[86:89], a[72:75], v[106:109], v[86:89]
v_mfma_f32_16x16x32_bf16 v[90:93], a[76:79], v[106:109], v[90:93]
global_load_lds_dwordx4 v0, s[40:41] offset:-4096
ds_read_b64_tr_b16 a[98:99], v255 offset:20736
v_mfma_f32_16x16x32_bf16 v[94:97], v[106:109], a[80:83], v[94:97]
ds_read_b64_tr_b16 a[100:101], v255 offset:18624
global_load_lds_dwordx4 v1, s[40:41] offset:-3072
v_mfma_f32_16x16x32_bf16 v[110:113], a[72:75], v[182:185], v[110:113]
ds_read_b64_tr_b16 a[102:103], v255 offset:20928
v_mfma_f32_16x16x32_bf16 v[114:117], a[76:79], v[182:185], v[114:117]
global_load_lds_dwordx4 v2, s[40:41] offset:-2048
ds_read_b64_tr_b16 a[104:105], v255 offset:18816
v_mfma_f32_16x16x32_bf16 v[118:121], v[182:185], a[80:83], v[118:121]
ds_read_b64_tr_b16 a[106:107], v255 offset:21120
global_load_lds_dwordx4 v3, s[40:41] offset:-1024
v_mfma_f32_16x16x32_bf16 v[134:137], v[106:109], v[182:185], v[134:137]
ds_read_b128 v[166:169], v51 offset:8192
v_mfma_f32_16x16x32_bf16 v[122:125], a[72:75], v[190:193], v[122:125]
global_load_lds_dwordx4 v4, s[40:41] offset:0
ds_read_b128 v[174:177], v51 offset:26624
v_mfma_f32_16x16x32_bf16 v[126:129], a[76:79], v[190:193], v[126:129]
ds_read_b64_tr_b16 a[108:109], v255 offset:36864
global_load_lds_dwordx4 v5, s[40:41] offset:1024
v_mfma_f32_16x16x32_bf16 v[130:133], v[190:193], a[80:83], v[130:133]
v_mfma_f32_16x16x32_bf16 v[158:161], v[106:109], v[190:193], v[158:161]
ds_read_b64_tr_b16 a[110:111], v255 offset:39168
global_load_lds_dwordx4 v6, s[40:41] offset:2048
v_mfma_f32_16x16x32_bf16 v[86:89], a[84:87], v[106:109], v[86:89]
ds_read_b64_tr_b16 a[112:113], v255 offset:37056
v_mfma_f32_16x16x32_bf16 v[90:93], a[88:91], v[106:109], v[90:93]
global_load_lds_dwordx4 v7, s[40:41] offset:3072
ds_read_b64_tr_b16 a[114:115], v255 offset:39360
v_mfma_f32_16x16x32_bf16 v[94:97], v[106:109], a[92:95], v[94:97]
s_add_u32 m0, s48, 12288
ds_read_b64_tr_b16 a[116:117], v255 offset:37248
v_mfma_f32_16x16x32_bf16 v[110:113], a[84:87], v[186:189], v[110:113]
ds_read_b64_tr_b16 a[118:119], v255 offset:39552
v_mfma_f32_16x16x32_bf16 v[114:117], a[88:91], v[186:189], v[114:117]
global_load_lds_dwordx4 v8, s[40:41] offset:-4096
ds_read_b128 v[170:173], v52 offset:8192
v_mfma_f32_16x16x32_bf16 v[118:121], v[186:189], a[92:95], v[118:121]
s_add_u32 s40, s40, 221184
ds_read_b128 v[178:181], v52 offset:26624
v_mfma_f32_16x16x32_bf16 v[134:137], v[106:109], v[186:189], v[134:137]
v_mfma_f32_16x16x32_bf16 v[122:125], a[84:87], v[194:197], v[122:125]
v_mfma_f32_16x16x32_bf16 v[126:129], a[88:91], v[194:197], v[126:129]
s_addc_u32 s41, s41, 0
v_mfma_f32_16x16x32_bf16 v[130:133], v[194:197], a[92:95], v[130:133]
v_mfma_f32_16x16x32_bf16 v[158:161], v[106:109], v[194:197], v[158:161]
s_waitcnt vmcnt(9) lgkmcnt(0)
s_barrier
s_add_u32 m0, s48, 40960
ds_read_b64_tr_b16 a[120:121], v255 offset:55296
v_mfma_f32_16x16x32_bf16 v[86:89], a[96:99], v[106:109], v[86:89]
v_mfma_f32_16x16x32_bf16 v[90:93], a[100:103], v[106:109], v[90:93]
global_load_lds_dwordx4 v0, s[40:41] offset:-4096
ds_read_b64_tr_b16 a[122:123], v255 offset:57600
v_mfma_f32_16x16x32_bf16 v[94:97], v[106:109], a[104:107], v[94:97]
ds_read_b64_tr_b16 a[124:125], v255 offset:55488
global_load_lds_dwordx4 v1, s[40:41] offset:-3072
v_mfma_f32_16x16x32_bf16 v[110:113], a[96:99], v[166:169], v[110:113]
ds_read_b64_tr_b16 a[126:127], v255 offset:57792
v_mfma_f32_16x16x32_bf16 v[114:117], a[100:103], v[166:169], v[114:117]
global_load_lds_dwordx4 v2, s[40:41] offset:-2048
ds_read_b64_tr_b16 a[128:129], v255 offset:55680
v_mfma_f32_16x16x32_bf16 v[118:121], v[166:169], a[104:107], v[118:121]
ds_read_b64_tr_b16 a[130:131], v255 offset:57984
global_load_lds_dwordx4 v3, s[40:41] offset:-1024
v_mfma_f32_16x16x32_bf16 v[134:137], v[106:109], v[166:169], v[134:137]
ds_read_b128 v[182:185], v51 offset:10240
v_mfma_f32_16x16x32_bf16 v[122:125], a[96:99], v[174:177], v[122:125]
global_load_lds_dwordx4 v4, s[40:41] offset:0
ds_read_b128 v[190:193], v51 offset:28672
v_mfma_f32_16x16x32_bf16 v[126:129], a[100:103], v[174:177], v[126:129]
ds_read_b64_tr_b16 a[132:133], v162 offset:8192
global_load_lds_dwordx4 v5, s[40:41] offset:1024
v_mfma_f32_16x16x32_bf16 v[130:133], v[174:177], a[104:107], v[130:133]
v_mfma_f32_16x16x32_bf16 v[158:161], v[106:109], v[174:177], v[158:161]
ds_read_b64_tr_b16 a[134:135], v162 offset:10496
global_load_lds_dwordx4 v6, s[40:41] offset:2048
v_mfma_f32_16x16x32_bf16 v[86:89], a[108:111], v[106:109], v[86:89]
ds_read_b64_tr_b16 a[136:137], v162 offset:8384
v_mfma_f32_16x16x32_bf16 v[90:93], a[112:115], v[106:109], v[90:93]
global_load_lds_dwordx4 v7, s[40:41] offset:3072
ds_read_b64_tr_b16 a[138:139], v162 offset:10688
v_mfma_f32_16x16x32_bf16 v[94:97], v[106:109], a[116:119], v[94:97]
s_add_u32 m0, s48, 49152
ds_read_b64_tr_b16 a[140:141], v162 offset:8576
v_mfma_f32_16x16x32_bf16 v[110:113], a[108:111], v[170:173], v[110:113]
ds_read_b64_tr_b16 a[142:143], v162 offset:10880
v_mfma_f32_16x16x32_bf16 v[114:117], a[112:115], v[170:173], v[114:117]
global_load_lds_dwordx4 v8, s[40:41] offset:-4096
ds_read_b128 v[186:189], v52 offset:10240
v_mfma_f32_16x16x32_bf16 v[118:121], v[170:173], a[116:119], v[118:121]
s_add_u32 s40, s40, 221184
ds_read_b128 v[194:197], v52 offset:28672
v_mfma_f32_16x16x32_bf16 v[134:137], v[106:109], v[170:173], v[134:137]
v_mfma_f32_16x16x32_bf16 v[122:125], a[108:111], v[178:181], v[122:125]
v_mfma_f32_16x16x32_bf16 v[126:129], a[112:115], v[178:181], v[126:129]
s_addc_u32 s41, s41, 0
v_mfma_f32_16x16x32_bf16 v[130:133], v[178:181], a[116:119], v[130:133]
v_mfma_f32_16x16x32_bf16 v[158:161], v[106:109], v[178:181], v[158:161]
s_waitcnt vmcnt(9) lgkmcnt(0)
s_barrier
s_add_u32 m0, s48, 77824
ds_read_b64_tr_b16 a[144:145], v9 offset:47104
v_mfma_f32_16x16x32_bf16 v[86:89], a[120:123], v[106:109], v[86:89]
v_mfma_f32_16x16x32_bf16 v[90:93], a[124:127], v[106:109], v[90:93]
global_load_lds_dwordx4 v0, s[40:41] offset:-4096
ds_read_b64_tr_b16 a[146:147], v9 offset:49408
v_mfma_f32_16x16x32_bf16 v[94:97], v[106:109], a[128:131], v[94:97]
ds_read_b64_tr_b16 a[148:149], v9 offset:47296
global_load_lds_dwordx4 v1, s[40:41] offset:-3072
v_mfma_f32_16x16x32_bf16 v[110:113], a[120:123], v[182:185], v[110:113]
ds_read_b64_tr_b16 a[150:151], v9 offset:49600
v_mfma_f32_16x16x32_bf16 v[114:117], a[124:127], v[182:185], v[114:117]
global_load_lds_dwordx4 v2, s[40:41] offset:-2048
ds_read_b64_tr_b16 a[152:153], v9 offset:47488
v_mfma_f32_16x16x32_bf16 v[118:121], v[182:185], a[128:131], v[118:121]
ds_read_b64_tr_b16 a[154:155], v9 offset:49792
global_load_lds_dwordx4 v3, s[40:41] offset:-1024
v_mfma_f32_16x16x32_bf16 v[134:137], v[106:109], v[182:185], v[134:137]
ds_read_b128 v[166:169], v51 offset:12288
v_mfma_f32_16x16x32_bf16 v[122:125], a[120:123], v[190:193], v[122:125]
global_load_lds_dwordx4 v4, s[40:41] offset:0
ds_read_b128 v[174:177], v51 offset:30720
v_mfma_f32_16x16x32_bf16 v[126:129], a[124:127], v[190:193], v[126:129]
ds_read_b64_tr_b16 a[156:157], v255 offset:0
global_load_lds_dwordx4 v5, s[40:41] offset:1024
v_mfma_f32_16x16x32_bf16 v[130:133], v[190:193], a[128:131], v[130:133]
v_mfma_f32_16x16x32_bf16 v[158:161], v[106:109], v[190:193], v[158:161]
ds_read_b64_tr_b16 a[158:159], v255 offset:2304
global_load_lds_dwordx4 v6, s[40:41] offset:2048
v_mfma_f32_16x16x32_bf16 v[86:89], a[132:135], v[106:109], v[86:89]
ds_read_b64_tr_b16 a[160:161], v255 offset:192
v_mfma_f32_16x16x32_bf16 v[90:93], a[136:139], v[106:109], v[90:93]
global_load_lds_dwordx4 v7, s[40:41] offset:3072
ds_read_b64_tr_b16 a[162:163], v255 offset:2496
v_mfma_f32_16x16x32_bf16 v[94:97], v[106:109], a[140:143], v[94:97]
s_add_u32 m0, s48, 86016
ds_read_b64_tr_b16 a[164:165], v255 offset:384
v_mfma_f32_16x16x32_bf16 v[110:113], a[132:135], v[186:189], v[110:113]
ds_read_b64_tr_b16 a[166:167], v255 offset:2688
v_mfma_f32_16x16x32_bf16 v[114:117], a[136:139], v[186:189], v[114:117]
global_load_lds_dwordx4 v8, s[40:41] offset:-4096
ds_read_b128 v[170:173], v52 offset:12288
v_mfma_f32_16x16x32_bf16 v[118:121], v[186:189], a[140:143], v[118:121]
s_sub_u32 s40, s40, 1769472
ds_read_b128 v[178:181], v52 offset:30720
v_mfma_f32_16x16x32_bf16 v[134:137], v[106:109], v[186:189], v[134:137]
v_mfma_f32_16x16x32_bf16 v[122:125], a[132:135], v[194:197], v[122:125]
v_mfma_f32_16x16x32_bf16 v[126:129], a[136:139], v[194:197], v[126:129]
s_subb_u32 s41, s41, 0
v_mfma_f32_16x16x32_bf16 v[130:133], v[194:197], a[140:143], v[130:133]
v_mfma_f32_16x16x32_bf16 v[158:161], v[106:109], v[194:197], v[158:161]
s_waitcnt vmcnt(9) lgkmcnt(0)
s_barrier
s_add_u32 m0, s49, 0
ds_read_b64_tr_b16 a[168:169], v255 offset:18432
v_mfma_f32_16x16x32_bf16 v[86:89], a[144:147], v[106:109], v[86:89]
v_mfma_f32_16x16x32_bf16 v[90:93], a[148:151], v[106:109], v[90:93]
ds_read_b64_tr_b16 a[170:171], v255 offset:20736
v_mfma_f32_16x16x32_bf16 v[94:97], v[106:109], a[152:155], v[94:97]
ds_read_b64_tr_b16 a[172:173], v255 offset:18624
v_mfma_f32_16x16x32_bf16 v[110:113], a[144:147], v[166:169], v[110:113]
global_load_lds_dwordx4 v11, s[42:43] offset:-2048 sc1
ds_read_b64_tr_b16 a[174:175], v255 offset:20928
v_mfma_f32_16x16x32_bf16 v[114:117], a[148:151], v[166:169], v[114:117]
ds_read_b64_tr_b16 a[176:177], v255 offset:18816
v_mfma_f32_16x16x32_bf16 v[118:121], v[166:169], a[152:155], v[118:121]
ds_read_b64_tr_b16 a[178:179], v255 offset:21120
v_mfma_f32_16x16x32_bf16 v[134:137], v[106:109], v[166:169], v[134:137]
global_load_lds_dwordx4 v12, s[42:43] offset:-1024 sc1
ds_read_b128 v[182:185], v51 offset:14336
v_mfma_f32_16x16x32_bf16 v[122:125], a[144:147], v[174:177], v[122:125]
ds_read_b128 v[190:193], v51 offset:32768
v_mfma_f32_16x16x32_bf16 v[126:129], a[148:151], v[174:177], v[126:129]
ds_read_b64_tr_b16 a[180:181], v255 offset:36864
v_mfma_f32_16x16x32_bf16 v[130:133], v[174:177], a[152:155], v[130:133]
global_load_lds_dwordx4 v13, s[42:43] offset:0 sc1
v_mfma_f32_16x16x32_bf16 v[158:161], v[106:109], v[174:177], v[158:161]
ds_read_b64_tr_b16 a[182:183], v255 offset:39168
v_mfma_f32_16x16x32_bf16 v[86:89], a[156:159], v[106:109], v[86:89]
ds_read_b64_tr_b16 a[184:185], v255 offset:37056
v_mfma_f32_16x16x32_bf16 v[90:93], a[160:163], v[106:109], v[90:93]
ds_read_b64_tr_b16 a[186:187], v255 offset:39360
v_mfma_f32_16x16x32_bf16 v[94:97], v[106:109], a[164:167], v[94:97]
global_load_lds_dwordx4 v14, s[42:43] offset:1024 sc1
ds_read_b64_tr_b16 a[188:189], v255 offset:37248
v_mfma_f32_16x16x32_bf16 v[110:113], a[156:159], v[170:173], v[110:113]
ds_read_b64_tr_b16 a[190:191], v255 offset:39552
v_mfma_f32_16x16x32_bf16 v[114:117], a[160:163], v[170:173], v[114:117]
ds_read_b128 v[186:189], v52 offset:14336
v_mfma_f32_16x16x32_bf16 v[118:121], v[170:173], a[164:167], v[118:121]
s_sub_u32 s42, s42, 18432
s_subb_u32 s43, s43, 0
ds_read_b128 v[194:197], v52 offset:32768
v_mfma_f32_16x16x32_bf16 v[134:137], v[106:109], v[170:173], v[134:137]
v_mfma_f32_16x16x32_bf16 v[122:125], a[156:159], v[178:181], v[122:125]
v_mfma_f32_16x16x32_bf16 v[126:129], a[160:163], v[178:181], v[126:129]
v_mfma_f32_16x16x32_bf16 v[130:133], v[178:181], a[164:167], v[130:133]
v_mfma_f32_16x16x32_bf16 v[158:161], v[106:109], v[178:181], v[158:161]
s_waitcnt vmcnt(4) lgkmcnt(0)
s_barrier
s_add_u32 m0, s49, 18432
ds_read_b64_tr_b16 a[192:193], v255 offset:55296
v_mfma_f32_16x16x32_bf16 v[86:89], a[168:171], v[106:109], v[86:89]
v_mfma_f32_16x16x32_bf16 v[90:93], a[172:175], v[106:109], v[90:93]
ds_read_b64_tr_b16 a[194:195], v255 offset:57600
v_mfma_f32_16x16x32_bf16 v[94:97], v[106:109], a[176:179], v[94:97]
ds_read_b64_tr_b16 a[196:197], v255 offset:55488
v_mfma_f32_16x16x32_bf16 v[110:113], a[168:171], v[182:185], v[110:113]
global_load_lds_dwordx4 v11, s[42:43] offset:-2048 sc1
ds_read_b64_tr_b16 a[198:199], v255 offset:57792
v_mfma_f32_16x16x32_bf16 v[114:117], a[172:175], v[182:185], v[114:117]
ds_read_b64_tr_b16 a[200:201], v255 offset:55680
v_mfma_f32_16x16x32_bf16 v[118:121], v[182:185], a[176:179], v[118:121]
ds_read_b64_tr_b16 a[202:203], v255 offset:57984
v_mfma_f32_16x16x32_bf16 v[134:137], v[106:109], v[182:185], v[134:137]
global_load_lds_dwordx4 v12, s[42:43] offset:-1024 sc1
ds_read_b128 v[166:169], v51 offset:16384
v_mfma_f32_16x16x32_bf16 v[122:125], a[168:171], v[190:193], v[122:125]
ds_read_b128 v[174:177], v51 offset:34816
v_mfma_f32_16x16x32_bf16 v[126:129], a[172:175], v[190:193], v[126:129]
ds_read_b64_tr_b16 a[204:205], v162 offset:8192
v_mfma_f32_16x16x32_bf16 v[130:133], v[190:193], a[176:179], v[130:133]
global_load_lds_dwordx4 v13, s[42:43] offset:0 sc1
ds_read_b64_tr_b16 a[206:207], v162 offset:10496
v_mfma_f32_16x16x32_bf16 v[158:161], v[106:109], v[190:193], v[158:161]
ds_read_b64_tr_b16 a[208:209], v162 offset:8384
v_mfma_f32_16x16x32_bf16 v[86:89], a[180:183], v[106:109], v[86:89]
ds_read_b64_tr_b16 a[210:211], v162 offset:10688
v_mfma_f32_16x16x32_bf16 v[90:93], a[184:187], v[106:109], v[90:93]
global_load_lds_dwordx4 v14, s[42:43] offset:1024 sc1
ds_read_b64_tr_b16 a[212:213], v162 offset:8576
v_mfma_f32_16x16x32_bf16 v[94:97], v[106:109], a[188:191], v[94:97]
ds_read_b64_tr_b16 a[214:215], v162 offset:10880
v_mfma_f32_16x16x32_bf16 v[110:113], a[180:183], v[186:189], v[110:113]
ds_read_b128 v[170:173], v52 offset:16384
v_mfma_f32_16x16x32_bf16 v[114:117], a[184:187], v[186:189], v[114:117]
s_sub_u32 s42, s42, 18432
s_subb_u32 s43, s43, 0
ds_read_b128 v[178:181], v52 offset:34816
v_mfma_f32_16x16x32_bf16 v[118:121], v[186:189], a[188:191], v[118:121]
v_mfma_f32_16x16x32_bf16 v[134:137], v[106:109], v[186:189], v[134:137]
v_mfma_f32_16x16x32_bf16 v[122:125], a[180:183], v[194:197], v[122:125]
v_mfma_f32_16x16x32_bf16 v[126:129], a[184:187], v[194:197], v[126:129]
v_mfma_f32_16x16x32_bf16 v[158:161], v[106:109], v[194:197], v[158:161]
s_waitcnt lgkmcnt(14)
v_mfma_f32_16x16x32_bf16 v[86:89], a[192:195], v[106:109], v[86:89]
s_waitcnt lgkmcnt(12)
v_mfma_f32_16x16x32_bf16 v[90:93], a[196:199], v[106:109], v[90:93]
s_waitcnt lgkmcnt(10)
v_mfma_f32_16x16x32_bf16 v[94:97], v[106:109], a[200:203], v[94:97]
s_waitcnt lgkmcnt(9)
v_mfma_f32_16x16x32_bf16 v[110:113], a[192:195], v[166:169], v[110:113]
v_mfma_f32_16x16x32_bf16 v[114:117], a[196:199], v[166:169], v[114:117]
v_mfma_f32_16x16x32_bf16 v[118:121], v[166:169], a[200:203], v[118:121]
v_mfma_f32_16x16x32_bf16 v[134:137], v[106:109], v[166:169], v[134:137]
s_waitcnt lgkmcnt(8)
v_mfma_f32_16x16x32_bf16 v[158:161], v[106:109], v[174:177], v[158:161]
s_waitcnt lgkmcnt(6)
v_mfma_f32_16x16x32_bf16 v[86:89], a[204:207], v[106:109], v[86:89]
s_waitcnt lgkmcnt(4)
v_mfma_f32_16x16x32_bf16 v[90:93], a[208:211], v[106:109], v[90:93]
s_waitcnt lgkmcnt(2)
v_mfma_f32_16x16x32_bf16 v[94:97], v[106:109], a[212:215], v[94:97]
s_waitcnt lgkmcnt(1)
v_mfma_f32_16x16x32_bf16 v[110:113], a[204:207], v[170:173], v[110:113]
v_mfma_f32_16x16x32_bf16 v[114:117], a[208:211], v[170:173], v[114:117]
v_mfma_f32_16x16x32_bf16 v[118:121], v[170:173], a[212:215], v[118:121]
v_mfma_f32_16x16x32_bf16 v[134:137], v[106:109], v[170:173], v[134:137]
s_waitcnt lgkmcnt(0)
v_mfma_f32_16x16x32_bf16 v[158:161], v[106:109], v[178:181], v[158:161]
s_nop 5
v_mul_f32 v202, 0x3ae38e39, v134
v_cvt_pk_bf16_f32 v216, -v202, -v202
v_lshlrev_b32 v216, 16, v216
ds_write_b32 v24, v216 offset:0
s_waitcnt lgkmcnt(0)
s_barrier
ds_read_b32 v216, v26
s_waitcnt lgkmcnt(0)
v_mul_f32 v216, 0x3ae38e39, v216
v_fma_f32 v203, -v202, v202, v216
v_max_f32 v203, 0, v203
v_cvt_pk_bf16_f32 v203, v203, v203
v_lshlrev_b32 v203, 16, v203
v_add_f32 v203, 0x3727c5ac, v203
v_cvt_pk_bf16_f32 v203, v203, v203
v_lshlrev_b32 v203, 16, v203
v_sqrt_f32 v203, v203
s_nop 0
v_cvt_pk_bf16_f32 v203, v203, v203
v_lshlrev_b32 v203, 16, v203
v_rcp_f32 v216, v203
s_nop 0
ds_write_b32 v24, v216 offset:128
ds_read_b128 v[170:173], v51 offset:32768
)"
R"(ds_read_b128 v[174:177], v52 offset:32768
ds_read_b128 v[178:181], v51 offset:34816
ds_read_b128 v[182:185], v52 offset:34816
v_mov_b32 v206, v32
v_mov_b32 v207, v33
v_mov_b32 v208, v34
v_mov_b32 v209, v35
v_mov_b32 v134, v158
v_mov_b32 v135, v159
v_mov_b32 v136, v160
v_mov_b32 v137, v161
s_waitcnt lgkmcnt(4)
s_barrier
ds_read_b32 v23, v24 offset:0
ds_read_b32 v148, v21
ds_read_b32 v95, v24 offset:192
ds_read_b32 v254, v27
ds_read_b32 v255, v28
ds_read_b128 v[198:201], v25 offset:0
ds_read_b64 v[158:159], v22 offset:41472
ds_read_b32 v140, v20
v_xor_b32 v51, 0x10000, v51
v_xor_b32 v52, 0x10000, v52
v_mul_f32 v202, 0x3ae38e39, v134
v_cvt_pk_bf16_f32 v216, -v202, -v202
v_lshlrev_b32 v216, 16, v216
ds_write_b32 v24, v216 offset:64
s_waitcnt lgkmcnt(8)
v_fma_f32 v210, v90, v23, v114
v_fma_f32 v211, v91, v23, v115
v_fma_f32 v212, v92, v23, v116
v_fma_f32 v213, v93, v23, v117
v_cvt_pk_bf16_f32 v146, v210, v211
v_cvt_pk_bf16_f32 v147, v212, v213
s_waitcnt lgkmcnt(0)
s_barrier
ds_read_b32 v216, v26
v_fma_f32 v210, v94, v198, v118
v_fma_f32 v211, v94, v199, v119
v_fma_f32 v212, v94, v200, v120
v_fma_f32 v213, v94, v201, v121
v_cvt_pk_bf16_f32 v154, v210, v211
v_cvt_pk_bf16_f32 v155, v212, v213
v_fma_f32 v210, v86, v23, v110
v_fma_f32 v211, v87, v23, v111
v_fma_f32 v212, v88, v23, v112
v_fma_f32 v213, v89, v23, v113
v_cvt_pk_bf16_f32 v138, v210, v211
v_cvt_pk_bf16_f32 v139, v212, v213
s_waitcnt lgkmcnt(0)
v_mul_f32 v216, 0x3ae38e39, v216
v_fma_f32 v203, -v202, v202, v216
v_max_f32 v203, 0, v203
v_cvt_pk_bf16_f32 v203, v203, v203
v_lshlrev_b32 v203, 16, v203
v_add_f32 v203, 0x3727c5ac, v203
v_cvt_pk_bf16_f32 v203, v203, v203
v_lshlrev_b32 v203, 16, v203
v_sqrt_f32 v203, v203
s_nop 0
v_cvt_pk_bf16_f32 v203, v203, v203
v_lshlrev_b32 v203, 16, v203
v_rcp_f32 v216, v203
s_nop 0
ds_write_b32 v24, v216 offset:192
v_add_lshl_u32 v252, v48, 0, 7
v_bfi_b32 v252, s69, v252, v47
s_waitcnt vmcnt(4) lgkmcnt(0)
s_barrier
s_mov_b32 s70, 32
s_waitcnt lgkmcnt(0)
s_nop 1
s_branch .Lqa_in48
.p2align 6
.Lqa_top59:
s_waitcnt lgkmcnt(5)
v_mfma_f32_16x16x32_bf16 v[134:137], v[106:109], v[0:3], 0
s_waitcnt lgkmcnt(4)
ds_read_b128 v[58:61], v52 offset:4096
v_bfi_b32 v252, s69, v252, v47
v_mfma_f32_16x16x32_bf16 v[134:137], v[106:109], v[4:7], v[134:137]
ds_read_b32 v152, v21
ds_read_b32 v95, v24 offset:128
v_mfma_f32_16x16x32_bf16 v[110:113], a[0:3], v[0:3], 0
s_waitcnt lgkmcnt(5)
ds_read_b128 v[62:65], v51 offset:6144
ds_read_b32 v254, v29
v_mfma_f32_16x16x32_bf16 v[134:137], v[106:109], v[96:99], v[134:137]
s_waitcnt lgkmcnt(6)
ds_read_b128 v[66:69], v52 offset:6144
ds_read_b32 v255, v30
v_mfma_f32_16x16x32_bf16 v[134:137], v[106:109], v[100:103], v[134:137]
ds_read_b128 v[198:201], v25 offset:64
ds_read_b64 v[160:161], v22 offset:41472
v_mfma_f32_16x16x32_bf16 v[114:117], a[4:7], v[0:3], 0
s_waitcnt lgkmcnt(9)
ds_read_b128 v[70:73], v51 offset:8192
ds_read_b32 v144, v20
v_mfma_f32_16x16x32_bf16 v[134:137], v[106:109], v[54:57], v[134:137]
s_waitcnt lgkmcnt(10)
ds_read_b128 v[74:77], v52 offset:8192
v_fma_f32 v210, v90, v23, v126
v_mfma_f32_16x16x32_bf16 v[134:137], v[106:109], v[58:61], v[134:137]
v_fma_f32 v211, v91, v23, v127
v_fma_f32 v212, v92, v23, v128
v_mfma_f32_16x16x32_bf16 v[118:121], v[0:3], a[8:11], 0
s_waitcnt lgkmcnt(8)
ds_read_b128 v[78:81], v51 offset:10240
v_fma_f32 v213, v93, v23, v129
v_mfma_f32_16x16x32_bf16 v[134:137], v[106:109], v[62:65], v[134:137]
s_waitcnt lgkmcnt(7)
ds_read_b128 v[82:85], v52 offset:10240
v_cvt_pk_bf16_f32 v150, v210, v211
v_mfma_f32_16x16x32_bf16 v[134:137], v[106:109], v[66:69], v[134:137]
v_cvt_pk_bf16_f32 v151, v212, v213
v_max_f32 v10, v190, v191
v_mfma_f32_16x16x32_bf16 v[110:113], a[12:15], v[4:7], v[110:113]
ds_read_b128 v[162:165], v51 offset:12288
v_mfma_f32_16x16x32_bf16 v[186:189], v[150:153], v[138:141], v[32:35]
s_waitcnt lgkmcnt(7)
v_max_f32 v44, v192, v193
v_fma_f32 v210, v94, v198, v130
s_waitcnt lgkmcnt(5)
v_mfma_f32_16x16x32_bf16 v[134:137], v[106:109], v[70:73], v[134:137]
s_xor_b32 s49, s49, 0x10000
ds_read_b128 v[166:169], v52 offset:12288
s_waitcnt lgkmcnt(4)
v_mfma_f32_16x16x32_bf16 v[134:137], v[106:109], v[74:77], v[134:137]
s_add_u32 m0, s49, 0
v_mul_f32 v95, 0x3eae1a93, v95
v_mul_f32 v8, v254, v95
v_mfma_f32_16x16x32_bf16 v[114:117], a[16:19], v[4:7], v[114:117]
s_waitcnt lgkmcnt(3)
ds_read_b128 v[170:173], v51 offset:14336
v_mul_f32 v9, v255, v95
v_mfma_f32_16x16x32_bf16 v[134:137], v[106:109], v[78:81], v[134:137]
s_waitcnt lgkmcnt(3)
ds_read_b128 v[174:177], v52 offset:14336
v_max3_f32 v10, v186, v187, v10
v_mfma_f32_16x16x32_bf16 v[134:137], v[106:109], v[82:85], v[134:137]
v_max3_f32 v44, v188, v189, v44
v_mul_f32 v10, v10, v8
v_mfma_f32_16x16x32_bf16 v[118:121], v[4:7], a[20:23], v[118:121]
s_waitcnt lgkmcnt(3)
ds_read_b128 v[178:181], v51 offset:16384
v_mul_f32 v44, v44, v9
v_mfma_f32_16x16x32_bf16 v[134:137], v[106:109], v[162:165], v[134:137]
s_waitcnt lgkmcnt(3)
ds_read_b128 v[182:185], v52 offset:16384
v_max_f32 v45, v10, v44
v_mfma_f32_16x16x32_bf16 v[134:137], v[106:109], v[166:169], v[134:137]
v_mov_b32 v46, v45
v_fma_f32 v211, v94, v199, v131
v_mfma_f32_16x16x32_bf16 v[110:113], a[24:27], v[96:99], v[110:113]
s_waitcnt lgkmcnt(3)
global_load_lds_dwordx4 v11, s[42:43] offset:-2048 sc1
v_fma_f32 v212, v94, v200, v132
v_mfma_f32_16x16x32_bf16 v[134:137], v[106:109], v[170:173], v[134:137]
s_waitcnt lgkmcnt(2)
v_permlane16_swap_b32 v45, v46
v_mfma_f32_16x16x32_bf16 v[134:137], v[106:109], v[174:177], v[134:137]
v_max_f32 v45, v45, v46
v_mov_b32 v46, v45
v_mfma_f32_16x16x32_bf16 v[114:117], a[28:31], v[96:99], v[114:117]
s_waitcnt lgkmcnt(1)
v_permlane32_swap_b32 v45, v46
v_mfma_f32_16x16x32_bf16 v[134:137], v[106:109], v[178:181], v[134:137]
s_waitcnt lgkmcnt(0)
v_max_f32 v45, v45, v46
v_sub_f32 v10, v10, v45
v_mfma_f32_16x16x32_bf16 v[134:137], v[106:109], v[182:185], v[134:137]
v_sub_f32 v44, v44, v45
v_fma_f32 v213, v94, v201, v133
v_mfma_f32_16x16x32_bf16 v[118:121], v[96:99], a[32:35], v[118:121]
v_exp_f32 v214, v10
v_mfma_f32_16x16x32_bf16 v[110:113], a[36:39], v[100:103], v[110:113]
v_exp_f32 v215, v44
v_mfma_f32_16x16x32_bf16 v[114:117], a[40:43], v[100:103], v[114:117]
s_nop 0
v_mul_f32 v202, 0x3ae38e39, v134
v_cvt_pk_bf16_f32 v216, -v202, -v202
v_mfma_f32_16x16x32_bf16 v[118:121], v[100:103], a[44:47], v[118:121]
v_lshlrev_b32 v216, 16, v216
global_load_lds_dwordx4 v12, s[42:43] offset:-1024 sc1
v_mfma_f32_16x16x32_bf16 v[110:113], a[48:51], v[54:57], v[110:113]
ds_write_b32 v24, v216 offset:0
v_mfma_f32_16x16x32_bf16 v[114:117], a[52:55], v[54:57], v[114:117]
v_add_f32 v45, v214, v215
v_mov_b32 v46, v45
v_mfma_f32_16x16x32_bf16 v[118:121], v[54:57], a[56:59], v[118:121]
v_mul_f32 v214, v214, v254
v_cvt_pk_bf16_f32 v156, v210, v211
v_mfma_f32_16x16x32_bf16 v[110:113], a[60:63], v[58:61], v[110:113]
v_permlane16_swap_b32 v45, v46
v_mfma_f32_16x16x32_bf16 v[114:117], a[64:67], v[58:61], v[114:117]
v_add_f32 v45, v45, v46
v_mov_b32 v46, v45
v_mfma_f32_16x16x32_bf16 v[118:121], v[58:61], a[68:71], v[118:121]
v_mfma_f32_16x16x32_bf16 v[110:113], a[72:75], v[62:65], v[110:113]
s_waitcnt lgkmcnt(0)
s_barrier
v_mfma_f32_16x16x32_bf16 v[114:117], a[76:79], v[62:65], v[114:117]
global_load_lds_dwordx4 v13, s[42:43] offset:0 sc1
ds_read_b32 v216, v26
v_mfma_f32_16x16x32_bf16 v[118:121], v[62:65], a[80:83], v[118:121]
v_mul_f32 v215, v215, v255
v_cvt_pk_bf16_f32 v157, v212, v213
v_mfma_f32_16x16x32_bf16 v[110:113], a[84:87], v[66:69], v[110:113]
v_permlane32_swap_b32 v45, v46
v_mfma_f32_16x16x32_bf16 v[114:117], a[88:91], v[66:69], v[114:117]
v_add_f32 v45, v45, v46
v_fma_f32 v210, v86, v23, v122
v_mfma_f32_16x16x32_bf16 v[118:121], v[66:69], a[92:95], v[118:121]
v_rcp_f32 v45, v45
v_mfma_f32_16x16x32_bf16 v[110:113], a[96:99], v[70:73], v[110:113]
v_mul_f32 v214, v214, v45
v_mul_f32 v215, v215, v45
v_mfma_f32_16x16x32_bf16 v[114:117], a[100:103], v[70:73], v[114:117]
v_cvt_pk_bf16_f32 v214, v214, v214
v_cvt_pk_bf16_f32 v215, v215, v215
v_mfma_f32_16x16x32_bf16 v[118:121], v[70:73], a[104:107], v[118:121]
v_and_b32 v196, v214, v40
v_and_b32 v197, v215, v41
v_mfma_f32_16x16x32_bf16 v[110:113], a[108:111], v[74:77], v[110:113]
global_load_lds_dwordx4 v14, s[42:43] offset:1024 sc1
s_add_u32 s42, s42, s46
s_addc_u32 s43, s43, s47
v_mfma_f32_16x16x32_bf16 v[114:117], a[112:115], v[74:77], v[114:117]
v_and_b32 v194, v214, v42
v_and_b32 v195, v215, v43
v_mfma_f32_16x16x32_bf16 v[118:121], v[74:77], a[116:119], v[118:121]
s_nop 0
v_mfma_f32_16x16x32_bf16 v[190:193], v[154:157], v[194:197], 0
v_mfma_f32_16x16x32_bf16 v[186:189], v[158:161], v[194:197], 0
s_waitcnt lgkmcnt(0)
v_mul_f32 v216, 0x3ae38e39, v216
v_fma_f32 v203, -v202, v202, v216
v_mfma_f32_16x16x32_bf16 v[110:113], a[120:123], v[78:81], v[110:113]
v_max_f32 v203, 0, v203
v_cvt_pk_bf16_f32 v203, v203, v203
v_mfma_f32_16x16x32_bf16 v[114:117], a[124:127], v[78:81], v[114:117]
v_lshlrev_b32 v203, 16, v203
v_add_f32 v203, 0x3727c5ac, v203
v_mfma_f32_16x16x32_bf16 v[118:121], v[78:81], a[128:131], v[118:121]
v_cvt_pk_bf16_f32 v203, v203, v203
v_lshlrev_b32 v203, 16, v203
v_mfma_f32_16x16x32_bf16 v[110:113], a[132:135], v[82:85], v[110:113]
v_cvt_pk_bf16_f32 v104, v190, v191
v_cvt_pk_bf16_f32 v105, v192, v193
v_mfma_f32_16x16x32_bf16 v[114:117], a[136:139], v[82:85], v[114:117]
ds_write_b64 v18, v[104:105]
v_mfma_f32_16x16x32_bf16 v[118:121], v[82:85], a[140:143], v[118:121]
v_cvt_pk_bf16_f32 v214, v188, v189
v_fma_f32 v211, v87, v23, v123
v_mfma_f32_16x16x32_bf16 v[110:113], a[144:147], v[162:165], v[110:113]
ds_write_b32 v19, v214
v_mfma_f32_16x16x32_bf16 v[114:117], a[148:151], v[162:165], v[114:117]
v_sqrt_f32 v203, v203
v_mfma_f32_16x16x32_bf16 v[118:121], v[162:165], a[152:155], v[118:121]
v_cvt_pk_bf16_f32 v203, v203, v203
v_lshlrev_b32 v203, 16, v203
v_mfma_f32_16x16x32_bf16 v[110:113], a[156:159], v[166:169], v[110:113]
v_rcp_f32 v216, v203
v_mfma_f32_16x16x32_bf16 v[114:117], a[160:163], v[166:169], v[114:117]
ds_write_b32 v24, v216 offset:128
v_mfma_f32_16x16x32_bf16 v[118:121], v[166:169], a[164:167], v[118:121]
v_fma_f32 v212, v88, v23, v124
v_fma_f32 v213, v89, v23, v125
v_mfma_f32_16x16x32_bf16 v[110:113], a[168:171], v[170:173], v[110:113]
v_cvt_pk_bf16_f32 v142, v210, v211
v_cvt_pk_bf16_f32 v143, v212, v213
v_mfma_f32_16x16x32_bf16 v[114:117], a[172:175], v[170:173], v[114:117]
v_mfma_f32_16x16x32_bf16 v[118:121], v[170:173], a[176:179], v[118:121]
v_mfma_f32_16x16x32_bf16 v[110:113], a[180:183], v[174:177], v[110:113]
v_mfma_f32_16x16x32_bf16 v[114:117], a[184:187], v[174:177], v[114:117]
s_waitcnt vmcnt(5) lgkmcnt(0)
s_barrier
v_mfma_f32_16x16x32_bf16 v[118:121], v[174:177], a[188:191], v[118:121]
ds_read_b128 v[0:3], v51 offset:18432
v_mfma_f32_16x16x32_bf16 v[110:113], a[192:195], v[178:181], v[110:113]
ds_read_b128 v[4:7], v52 offset:18432
ds_read_b32 v23, v24 offset:0
v_mfma_f32_16x16x32_bf16 v[114:117], a[196:199], v[178:181], v[114:117]
s_mov_b64 exec, 1
global_store_dword v252, v48, s[38:39] sc1
s_mov_b64 exec, -1
v_mfma_f32_16x16x32_bf16 v[190:193], v[150:153], v[142:145], v[36:39]
v_mfma_f32_16x16x32_bf16 v[118:121], v[178:181], a[200:203], v[118:121]
ds_read_b128 v[96:99], v51 offset:20480
v_add_u32 v48, 1, v48
v_mfma_f32_16x16x32_bf16 v[110:113], a[204:207], v[182:185], v[110:113]
ds_read_b128 v[100:103], v52 offset:20480
v_add_lshl_u32 v252, v48, 0, 7
v_mfma_f32_16x16x32_bf16 v[114:117], a[208:211], v[182:185], v[114:117]
v_bfi_b32 v252, s69, v252, v47
ds_read_b32 v148, v21
v_mfma_f32_16x16x32_bf16 v[118:121], v[182:185], a[212:215], v[118:121]
s_waitcnt lgkmcnt(5)
ds_read_b128 v[54:57], v51 offset:22528
ds_read_b32 v95, v24 offset:192
v_mfma_f32_16x16x32_bf16 v[134:137], v[106:109], v[0:3], 0
s_waitcnt lgkmcnt(6)
ds_read_b128 v[58:61], v52 offset:22528
ds_read_b32 v254, v27
v_mfma_f32_16x16x32_bf16 v[134:137], v[106:109], v[4:7], v[134:137]
ds_read_b32 v255, v28
ds_read_b128 v[198:201], v25 offset:0
v_mfma_f32_16x16x32_bf16 v[122:125], a[0:3], v[0:3], 0
s_waitcnt lgkmcnt(8)
ds_read_b128 v[62:65], v51 offset:24576
ds_read_b64 v[158:159], v22 offset:41472
v_mfma_f32_16x16x32_bf16 v[134:137], v[106:109], v[96:99], v[134:137]
s_waitcnt lgkmcnt(9)
ds_read_b128 v[66:69], v52 offset:24576
ds_read_b32 v140, v20
v_mfma_f32_16x16x32_bf16 v[134:137], v[106:109], v[100:103], v[134:137]
v_fma_f32 v210, v90, v23, v114
v_fma_f32 v211, v91, v23, v115
v_mfma_f32_16x16x32_bf16 v[126:129], a[4:7], v[0:3], 0
s_waitcnt lgkmcnt(9)
ds_read_b128 v[70:73], v51 offset:26624
v_fma_f32 v212, v92, v23, v116
v_mfma_f32_16x16x32_bf16 v[134:137], v[106:109], v[54:57], v[134:137]
s_waitcnt lgkmcnt(8)
ds_read_b128 v[74:77], v52 offset:26624
v_fma_f32 v213, v93, v23, v117
v_mfma_f32_16x16x32_bf16 v[134:137], v[106:109], v[58:61], v[134:137]
v_cvt_pk_bf16_f32 v146, v210, v211
v_cvt_pk_bf16_f32 v147, v212, v213
v_mfma_f32_16x16x32_bf16 v[130:133], v[0:3], a[8:11], 0
ds_read_b128 v[78:81], v51 offset:28672
v_mfma_f32_16x16x32_bf16 v[186:189], v[146:149], v[142:145], v[206:209]
s_waitcnt lgkmcnt(6)
v_max_f32 v10, v190, v191
v_max_f32 v44, v192, v193
v_mfma_f32_16x16x32_bf16 v[134:137], v[106:109], v[62:65], v[134:137]
s_waitcnt lgkmcnt(4)
ds_read_b128 v[82:85], v52 offset:28672
v_fma_f32 v210, v94, v198, v118
v_mfma_f32_16x16x32_bf16 v[134:137], v[106:109], v[66:69], v[134:137]
v_mul_f32 v95, 0x3eae1a93, v95
v_mul_f32 v8, v254, v95
v_mfma_f32_16x16x32_bf16 v[122:125], a[12:15], v[4:7], v[122:125]
s_waitcnt lgkmcnt(3)
ds_read_b128 v[162:165], v51 offset:30720
v_mul_f32 v9, v255, v95
v_mfma_f32_16x16x32_bf16 v[134:137], v[106:109], v[70:73], v[134:137]
s_add_u32 m0, s49, 18432
ds_read_b128 v[166:169], v52 offset:30720
s_waitcnt lgkmcnt(4)
v_mfma_f32_16x16x32_bf16 v[134:137], v[106:109], v[74:77], v[134:137]
v_max3_f32 v10, v186, v187, v10
v_max3_f32 v44, v188, v189, v44
v_mfma_f32_16x16x32_bf16 v[126:129], a[16:19], v[4:7], v[126:129]
s_waitcnt lgkmcnt(3)
ds_read_b128 v[170:173], v51 offset:32768
v_mul_f32 v10, v10, v8
v_mfma_f32_16x16x32_bf16 v[134:137], v[106:109], v[78:81], v[134:137]
s_waitcnt lgkmcnt(3)
ds_read_b128 v[174:177], v52 offset:32768
v_mul_f32 v44, v44, v9
v_mfma_f32_16x16x32_bf16 v[134:137], v[106:109], v[82:85], v[134:137]
v_max_f32 v45, v10, v44
v_mov_b32 v46, v45
v_mfma_f32_16x16x32_bf16 v[130:133], v[4:7], a[20:23], v[130:133]
s_waitcnt lgkmcnt(3)
ds_read_b128 v[178:181], v51 offset:34816
v_fma_f32 v211, v94, v199, v119
v_mfma_f32_16x16x32_bf16 v[134:137], v[106:109], v[162:165], v[134:137]
s_waitcnt lgkmcnt(3)
ds_read_b128 v[182:185], v52 offset:34816
v_fma_f32 v212, v94, v200, v120
v_mfma_f32_16x16x32_bf16 v[134:137], v[106:109], v[166:169], v[134:137]
global_load_lds_dwordx4 v11, s[42:43] offset:-2048 sc1
v_fma_f32 v213, v94, v201, v121
v_mfma_f32_16x16x32_bf16 v[122:125], a[24:27], v[96:99], v[122:125]
s_waitcnt lgkmcnt(3)
v_permlane16_swap_b32 v45, v46
v_mfma_f32_16x16x32_bf16 v[134:137], v[106:109], v[170:173], v[134:137]
s_waitcnt lgkmcnt(2)
v_max_f32 v45, v45, v46
v_mov_b32 v46, v45
v_mfma_f32_16x16x32_bf16 v[134:137], v[106:109], v[174:177], v[134:137]
s_nop 0
v_permlane32_swap_b32 v45, v46
v_mfma_f32_16x16x32_bf16 v[126:129], a[28:31], v[96:99], v[126:129]
s_waitcnt lgkmcnt(1)
v_max_f32 v45, v45, v46
v_sub_f32 v10, v10, v45
v_mfma_f32_16x16x32_bf16 v[134:137], v[106:109], v[178:181], v[134:137]
s_waitcnt lgkmcnt(0)
v_sub_f32 v44, v44, v45
v_cvt_pk_bf16_f32 v154, v210, v211
v_mfma_f32_16x16x32_bf16 v[134:137], v[106:109], v[182:185], v[134:137]
v_exp_f32 v214, v10
v_mfma_f32_16x16x32_bf16 v[130:133], v[96:99], a[32:35], v[130:133]
v_exp_f32 v215, v44
v_mfma_f32_16x16x32_bf16 v[122:125], a[36:39], v[100:103], v[122:125]
global_load_lds_dwordx4 v12, s[42:43] offset:-1024 sc1
v_add_f32 v45, v214, v215
)"
R"(v_mfma_f32_16x16x32_bf16 v[126:129], a[40:43], v[100:103], v[126:129]
s_nop 0
v_mul_f32 v202, 0x3ae38e39, v134
v_cvt_pk_bf16_f32 v216, -v202, -v202
v_mfma_f32_16x16x32_bf16 v[130:133], v[100:103], a[44:47], v[130:133]
v_lshlrev_b32 v216, 16, v216
v_mov_b32 v46, v45
v_mfma_f32_16x16x32_bf16 v[122:125], a[48:51], v[54:57], v[122:125]
ds_write_b32 v24, v216 offset:64
v_mfma_f32_16x16x32_bf16 v[126:129], a[52:55], v[54:57], v[126:129]
v_mul_f32 v214, v214, v254
v_cvt_pk_bf16_f32 v155, v212, v213
v_mfma_f32_16x16x32_bf16 v[130:133], v[54:57], a[56:59], v[130:133]
v_permlane16_swap_b32 v45, v46
v_mfma_f32_16x16x32_bf16 v[122:125], a[60:63], v[58:61], v[122:125]
v_add_f32 v45, v45, v46
v_mov_b32 v46, v45
v_mfma_f32_16x16x32_bf16 v[126:129], a[64:67], v[58:61], v[126:129]
v_mul_f32 v215, v215, v255
v_fma_f32 v210, v86, v23, v110
v_mfma_f32_16x16x32_bf16 v[130:133], v[58:61], a[68:71], v[130:133]
v_mfma_f32_16x16x32_bf16 v[122:125], a[72:75], v[62:65], v[122:125]
s_waitcnt lgkmcnt(0)
s_barrier
v_mfma_f32_16x16x32_bf16 v[126:129], a[76:79], v[62:65], v[126:129]
global_load_lds_dwordx4 v13, s[42:43] offset:0 sc1
ds_read_b32 v216, v26
v_mfma_f32_16x16x32_bf16 v[130:133], v[62:65], a[80:83], v[130:133]
v_permlane32_swap_b32 v45, v46
v_mfma_f32_16x16x32_bf16 v[122:125], a[84:87], v[66:69], v[122:125]
v_add_f32 v45, v45, v46
v_fma_f32 v211, v87, v23, v111
v_mfma_f32_16x16x32_bf16 v[126:129], a[88:91], v[66:69], v[126:129]
v_rcp_f32 v45, v45
v_mfma_f32_16x16x32_bf16 v[130:133], v[66:69], a[92:95], v[130:133]
v_mul_f32 v214, v214, v45
v_mul_f32 v215, v215, v45
v_mfma_f32_16x16x32_bf16 v[122:125], a[96:99], v[70:73], v[122:125]
v_cvt_pk_bf16_f32 v214, v214, v214
v_cvt_pk_bf16_f32 v215, v215, v215
v_mfma_f32_16x16x32_bf16 v[126:129], a[100:103], v[70:73], v[126:129]
v_and_b32 v194, v214, v40
v_and_b32 v195, v215, v41
v_mfma_f32_16x16x32_bf16 v[130:133], v[70:73], a[104:107], v[130:133]
v_and_b32 v196, v214, v42
v_and_b32 v197, v215, v43
v_mfma_f32_16x16x32_bf16 v[122:125], a[108:111], v[74:77], v[122:125]
global_load_lds_dwordx4 v14, s[42:43] offset:1024 sc1
s_sub_u32 s42, s42, 18432
s_subb_u32 s43, s43, 0
v_mfma_f32_16x16x32_bf16 v[190:193], v[154:157], v[194:197], 0
v_mfma_f32_16x16x32_bf16 v[186:189], v[158:161], v[194:197], 0
s_waitcnt lgkmcnt(0)
v_mul_f32 v216, 0x3ae38e39, v216
v_fma_f32 v203, -v202, v202, v216
v_mfma_f32_16x16x32_bf16 v[126:129], a[112:115], v[74:77], v[126:129]
v_max_f32 v203, 0, v203
v_cvt_pk_bf16_f32 v203, v203, v203
v_mfma_f32_16x16x32_bf16 v[130:133], v[74:77], a[116:119], v[130:133]
v_lshlrev_b32 v203, 16, v203
v_add_f32 v203, 0x3727c5ac, v203
v_mfma_f32_16x16x32_bf16 v[122:125], a[120:123], v[78:81], v[122:125]
v_cvt_pk_bf16_f32 v203, v203, v203
v_lshlrev_b32 v203, 16, v203
v_mfma_f32_16x16x32_bf16 v[126:129], a[124:127], v[78:81], v[126:129]
v_cvt_pk_bf16_f32 v104, v190, v191
v_cvt_pk_bf16_f32 v105, v192, v193
v_mfma_f32_16x16x32_bf16 v[130:133], v[78:81], a[128:131], v[130:133]
ds_write_b64 v18, v[104:105]
v_mfma_f32_16x16x32_bf16 v[122:125], a[132:135], v[82:85], v[122:125]
v_cvt_pk_bf16_f32 v214, v188, v189
v_fma_f32 v212, v88, v23, v112
v_mfma_f32_16x16x32_bf16 v[126:129], a[136:139], v[82:85], v[126:129]
ds_write_b32 v19, v214
v_mfma_f32_16x16x32_bf16 v[130:133], v[82:85], a[140:143], v[130:133]
v_sqrt_f32 v203, v203
v_mfma_f32_16x16x32_bf16 v[122:125], a[144:147], v[162:165], v[122:125]
v_cvt_pk_bf16_f32 v203, v203, v203
v_lshlrev_b32 v203, 16, v203
v_mfma_f32_16x16x32_bf16 v[126:129], a[148:151], v[162:165], v[126:129]
v_rcp_f32 v216, v203
v_mfma_f32_16x16x32_bf16 v[130:133], v[162:165], a[152:155], v[130:133]
ds_write_b32 v24, v216 offset:192
v_mfma_f32_16x16x32_bf16 v[122:125], a[156:159], v[166:169], v[122:125]
v_fma_f32 v213, v89, v23, v113
v_cvt_pk_bf16_f32 v138, v210, v211
v_mfma_f32_16x16x32_bf16 v[126:129], a[160:163], v[166:169], v[126:129]
v_cvt_pk_bf16_f32 v139, v212, v213
v_mfma_f32_16x16x32_bf16 v[130:133], v[166:169], a[164:167], v[130:133]
v_mfma_f32_16x16x32_bf16 v[122:125], a[168:171], v[170:173], v[122:125]
v_mfma_f32_16x16x32_bf16 v[126:129], a[172:175], v[170:173], v[126:129]
v_xor_b32 v51, 0x10000, v51
v_xor_b32 v52, 0x10000, v52
v_mfma_f32_16x16x32_bf16 v[130:133], v[170:173], a[176:179], v[130:133]
v_mfma_f32_16x16x32_bf16 v[122:125], a[180:183], v[174:177], v[122:125]
v_mfma_f32_16x16x32_bf16 v[126:129], a[184:187], v[174:177], v[126:129]
s_waitcnt vmcnt(5) lgkmcnt(0)
s_barrier
.Lqa_in48:
v_mfma_f32_16x16x32_bf16 v[130:133], v[174:177], a[188:191], v[130:133]
s_cmp_eq_u32 s70, s73
s_cbranch_scc1 .Lqa_hk49
.Lqa_hr50:
ds_read_b128 v[0:3], v51 offset:0
v_mfma_f32_16x16x32_bf16 v[122:125], a[192:195], v[178:181], v[122:125]
ds_read_b128 v[4:7], v52 offset:0
ds_read_b32 v23, v24 offset:64
v_mfma_f32_16x16x32_bf16 v[126:129], a[196:199], v[178:181], v[126:129]
s_mov_b64 exec, 1
global_store_dword v252, v48, s[38:39] sc1
s_mov_b64 exec, -1
v_mfma_f32_16x16x32_bf16 v[190:193], v[146:149], v[138:141], v[36:39]
v_mfma_f32_16x16x32_bf16 v[130:133], v[178:181], a[200:203], v[130:133]
ds_read_b128 v[96:99], v51 offset:2048
v_add_u32 v48, 1, v48
v_mfma_f32_16x16x32_bf16 v[122:125], a[204:207], v[182:185], v[122:125]
ds_read_b128 v[100:103], v52 offset:2048
v_add_lshl_u32 v252, v48, 0, 7
v_mfma_f32_16x16x32_bf16 v[126:129], a[208:211], v[182:185], v[126:129]
v_mfma_f32_16x16x32_bf16 v[130:133], v[182:185], a[212:215], v[130:133]
s_sub_u32 s70, s70, 1
ds_read_b128 v[54:57], v51 offset:4096
s_cbranch_scc0 .Lqa_top59
s_waitcnt lgkmcnt(0)
v_bfi_b32 v252, s69, v252, v47
ds_read_b32 v152, v21
ds_read_b32 v95, v24 offset:128
ds_read_b32 v254, v29
ds_read_b32 v255, v30
ds_read_b128 v[198:201], v25 offset:64
ds_read_b64 v[160:161], v22 offset:41472
ds_read_b32 v144, v20
v_fma_f32 v210, v90, v23, v126
v_fma_f32 v211, v91, v23, v127
v_fma_f32 v212, v92, v23, v128
v_fma_f32 v213, v93, v23, v129
v_cvt_pk_bf16_f32 v150, v210, v211
v_cvt_pk_bf16_f32 v151, v212, v213
v_max_f32 v10, v190, v191
s_waitcnt lgkmcnt(6)
v_mfma_f32_16x16x32_bf16 v[186:189], v[150:153], v[138:141], v[32:35]
s_waitcnt lgkmcnt(2)
v_max_f32 v44, v192, v193
v_fma_f32 v210, v94, v198, v130
v_mul_f32 v95, 0x3eae1a93, v95
v_mul_f32 v8, v254, v95
v_mul_f32 v9, v255, v95
s_nop 1
v_max3_f32 v10, v186, v187, v10
v_max3_f32 v44, v188, v189, v44
v_mul_f32 v10, v10, v8
v_mul_f32 v44, v44, v9
v_max_f32 v45, v10, v44
v_mov_b32 v46, v45
v_fma_f32 v211, v94, v199, v131
v_fma_f32 v212, v94, v200, v132
v_permlane16_swap_b32 v45, v46
v_max_f32 v45, v45, v46
v_mov_b32 v46, v45
s_nop 1
v_permlane32_swap_b32 v45, v46
v_max_f32 v45, v45, v46
v_sub_f32 v10, v10, v45
v_sub_f32 v44, v44, v45
v_fma_f32 v213, v94, v201, v133
v_exp_f32 v214, v10
v_exp_f32 v215, v44
s_nop 0
v_add_f32 v45, v214, v215
v_mov_b32 v46, v45
v_mul_f32 v214, v214, v254
v_cvt_pk_bf16_f32 v156, v210, v211
v_permlane16_swap_b32 v45, v46
v_add_f32 v45, v45, v46
v_mov_b32 v46, v45
s_waitcnt lgkmcnt(0)
s_barrier
v_mul_f32 v215, v215, v255
v_cvt_pk_bf16_f32 v157, v212, v213
v_permlane32_swap_b32 v45, v46
v_add_f32 v45, v45, v46
v_fma_f32 v210, v86, v23, v122
v_rcp_f32 v45, v45
s_nop 0
v_mul_f32 v214, v214, v45
v_mul_f32 v215, v215, v45
v_cvt_pk_bf16_f32 v214, v214, v214
v_cvt_pk_bf16_f32 v215, v215, v215
v_and_b32 v196, v214, v40
v_and_b32 v197, v215, v41
v_and_b32 v194, v214, v42
v_and_b32 v195, v215, v43
s_nop 1
v_mfma_f32_16x16x32_bf16 v[190:193], v[154:157], v[194:197], 0
v_mfma_f32_16x16x32_bf16 v[186:189], v[158:161], v[194:197], 0
s_nop 6
v_cvt_pk_bf16_f32 v104, v190, v191
v_cvt_pk_bf16_f32 v105, v192, v193
ds_write_b64 v18, v[104:105]
v_cvt_pk_bf16_f32 v214, v188, v189
v_fma_f32 v211, v87, v23, v123
ds_write_b32 v19, v214
v_fma_f32 v212, v88, v23, v124
v_fma_f32 v213, v89, v23, v125
v_cvt_pk_bf16_f32 v142, v210, v211
v_cvt_pk_bf16_f32 v143, v212, v213
s_waitcnt vmcnt(0)
s_waitcnt lgkmcnt(0)
s_barrier
s_mov_b64 exec, 1
global_store_dword v252, v48, s[38:39] sc1
s_mov_b64 exec, -1
s_waitcnt vmcnt(1)
s_barrier
v_add_u32 v48, 8, v48
v_mbcnt_lo_u32_b32 v252, -1, 0
v_mbcnt_hi_u32_b32 v252, -1, v252
v_add_lshl_u32 v252, v252, 0, 7
v_bfi_b32 v252, s69, v252, v47
s_mov_b64 exec, 0xff
global_store_dword v252, v48, s[38:39] sc1
s_mov_b64 exec, -1
s_cmp_lg_u32 s53, 0
s_cbranch_scc1 .Lqa_n61
v_add_lshl_u32 v253, v50, 0, 7
v_bfi_b32 v253, s69, v253, v49
global_load_dword v53, v253, s[38:39] sc1
s_waitcnt vmcnt(0)
v_sub_u32 v53, v53, v50
v_cmp_lt_u32 vcc, 0x7f, v53
s_cbranch_vccnz .Lqa_sp62
.Lqa_rt63:
v_add_u32 v49, 102400, v49
v_add_lshl_u32 v253, v50, 0, 7
v_bfi_b32 v253, s69, v253, v49
global_load_dword v53, v253, s[38:39] sc1
s_waitcnt vmcnt(0)
v_sub_u32 v53, v53, v50
v_cmp_lt_u32 vcc, 0x7f, v53
s_cbranch_vccnz .Lqa_sp64
.Lqa_rt65:
v_add_u32 v49, 102400, v49
v_add_lshl_u32 v253, v50, 0, 7
v_bfi_b32 v253, s69, v253, v49
global_load_dword v53, v253, s[38:39] sc1
s_waitcnt vmcnt(0)
v_sub_u32 v53, v53, v50
v_cmp_lt_u32 vcc, 0x7f, v53
s_cbranch_vccnz .Lqa_sp66
.Lqa_rt67:
v_add_u32 v49, 102400, v49
v_add_lshl_u32 v253, v50, 0, 7
v_bfi_b32 v253, s69, v253, v49
global_load_dword v53, v253, s[38:39] sc1
s_waitcnt vmcnt(0)
v_sub_u32 v53, v53, v50
v_cmp_lt_u32 vcc, 0x7f, v53
s_cbranch_vccnz .Lqa_sp68
.Lqa_rt69:
v_add_u32 v49, 102400, v49
s_load_dword s75, s[38:39], 0x0
s_waitcnt vmcnt(0) lgkmcnt(0)
s_add_u32 s75, s75, 1
v_mov_b32 v53, s75
v_mov_b32 v49, 0
global_store_dword v49, v53, s[38:39] sc1
.Lqa_n61:
s_endpgm
.Lqa_hk6:
s_cmp_lg_u32 s70, 18
s_cbranch_scc1 .Lqa_e8
s_add_u32 s42, s42, s50
s_addc_u32 s43, s43, 0
s_mov_b64 s[76:77], -1
s_mov_b64 s[46:47], s[44:45]
s_mov_b32 s73, 17
s_nop 1
s_branch .Lqa_hr7
.Lqa_e8:
s_cmp_lg_u32 s70, 17
s_cbranch_scc1 .Lqa_e9
s_mov_b32 s46, -18432
s_mov_b32 s47, -1
v_or_b32 v234, s52, v234
v_or_b32 v235, s52, v235
v_or_b32 v236, s52, v236
v_or_b32 v237, s52, v237
s_mov_b32 s73, 16
s_nop 1
s_branch .Lqa_hr7
.Lqa_e9:
s_cmp_lg_u32 s70, 16
s_cbranch_scc1 .Lqa_e10
v_mov_b32 v234, v32
v_mov_b32 v235, v33
v_mov_b32 v236, v34
v_mov_b32 v237, v35
s_mov_b32 s73, 15
s_nop 1
s_branch .Lqa_hr7
.Lqa_e10:
s_cmp_lg_u32 s70, 15
s_cbranch_scc1 .Lqa_e11
v_subrev_u32 v16, 18432, v16
s_mov_b32 s73, 3
s_nop 1
s_branch .Lqa_hr7
.Lqa_e11:
s_cmp_lg_u32 s70, 3
s_cbranch_scc1 .Lqa_e12
s_mov_b32 s73, 2
s_nop 1
s_branch .Lqa_hr7
.Lqa_e12:
s_cmp_lg_u32 s70, 2
s_cbranch_scc1 .Lqa_e13
s_add_u32 s46, s46, s50
s_addc_u32 s47, s47, 0
s_mov_b32 s73, 0
s_nop 1
s_branch .Lqa_hr7
.Lqa_e13:
s_cmp_lg_u32 s70, 0
s_cbranch_scc1 .Lqa_e14
v_or_b32 v32, s52, v32
v_or_b32 v33, s52, v33
v_or_b32 v34, s52, v34
v_or_b32 v35, s52, v35
s_mov_b32 s73, 999
s_nop 1
s_branch .Lqa_hr7
.Lqa_e14:
s_branch .Lqa_bk15
.Lqa_hk19:
s_cmp_lg_u32 s70, 18
s_cbranch_scc1 .Lqa_e21
s_add_u32 s42, s42, s50
s_addc_u32 s43, s43, 0
s_mov_b64 s[76:77], -1
s_mov_b64 s[46:47], s[44:45]
s_mov_b32 s73, 17
s_nop 1
s_branch .Lqa_hr20
.Lqa_e21:
s_cmp_lg_u32 s70, 17
s_cbranch_scc1 .Lqa_e22
s_mov_b32 s46, -18432
s_mov_b32 s47, -1
v_or_b32 v234, s52, v234
v_or_b32 v235, s52, v235
v_or_b32 v236, s52, v236
v_or_b32 v237, s52, v237
s_mov_b32 s73, 16
s_nop 1
s_branch .Lqa_hr20
.Lqa_e22:
s_cmp_lg_u32 s70, 16
s_cbranch_scc1 .Lqa_e23
v_mov_b32 v234, v32
v_mov_b32 v235, v33
v_mov_b32 v236, v34
v_mov_b32 v237, v35
s_mov_b32 s73, 15
s_nop 1
s_branch .Lqa_hr20
.Lqa_e23:
s_cmp_lg_u32 s70, 15
s_cbranch_scc1 .Lqa_e24
v_subrev_u32 v16, 18432, v16
s_mov_b32 s73, 3
s_nop 1
s_branch .Lqa_hr20
.Lqa_e24:
s_cmp_lg_u32 s70, 3
s_cbranch_scc1 .Lqa_e25
s_mov_b32 s73, 2
s_nop 1
s_branch .Lqa_hr20
.Lqa_e25:
s_cmp_lg_u32 s70, 2
s_cbranch_scc1 .Lqa_e26
s_add_u32 s46, s46, s50
s_addc_u32 s47, s47, 0
s_mov_b32 s73, 0
s_nop 1
s_branch .Lqa_hr20
.Lqa_e26:
s_cmp_lg_u32 s70, 0
s_cbranch_scc1 .Lqa_e27
v_or_b32 v32, s52, v32
v_or_b32 v33, s52, v33
v_or_b32 v34, s52, v34
v_or_b32 v35, s52, v35
s_mov_b32 s73, 999
s_nop 1
s_branch .Lqa_hr20
.Lqa_e27:
s_branch .Lqa_bk28
.Lqa_sp34:
s_sleep 1
)"
R"(v_add_lshl_u32 v253, v46, 0, 7
v_bfi_b32 v253, s69, v253, v44
global_load_dword v53, v253, s[38:39] sc1
s_add_u32 s71, s71, 1
s_waitcnt vmcnt(0)
v_sub_u32 v53, v53, v46
v_cmp_lt_u32 vcc, 0x7f, v53
s_and_b64 vcc, vcc, s[76:77]
s_cbranch_vccz .Lqa_rt35
s_cmp_lt_u32 s71, 3000000
s_cbranch_scc1 .Lqa_sp34
s_branch .Lqa_rt35
.Lqa_sp36:
s_sleep 1
v_add_lshl_u32 v253, v46, 0, 7
v_bfi_b32 v253, s69, v253, v44
global_load_dword v254, v253, s[38:39] sc1
s_add_u32 s71, s71, 1
s_waitcnt vmcnt(0)
v_sub_u32 v254, v254, v46
v_cmp_lt_u32 vcc, 0x7f, v254
s_and_b64 vcc, vcc, s[76:77]
s_cbranch_vccz .Lqa_rt37
s_cmp_lt_u32 s71, 3000000
s_cbranch_scc1 .Lqa_sp36
s_branch .Lqa_rt37
.Lqa_hk32:
s_cmp_lg_u32 s70, 18
s_cbranch_scc1 .Lqa_e38
s_add_u32 s42, s42, s50
s_addc_u32 s43, s43, 0
s_mov_b64 s[76:77], -1
s_mov_b64 s[46:47], s[44:45]
s_mov_b32 s73, 17
s_nop 1
s_branch .Lqa_hr33
.Lqa_e38:
s_cmp_lg_u32 s70, 17
s_cbranch_scc1 .Lqa_e39
s_mov_b32 s46, -18432
s_mov_b32 s47, -1
v_or_b32 v202, s52, v202
v_or_b32 v203, s52, v203
v_or_b32 v204, s52, v204
v_or_b32 v205, s52, v205
s_mov_b32 s73, 16
s_nop 1
s_branch .Lqa_hr33
.Lqa_e39:
s_cmp_lg_u32 s70, 16
s_cbranch_scc1 .Lqa_e40
v_mov_b32 v202, v32
v_mov_b32 v203, v33
v_mov_b32 v204, v34
v_mov_b32 v205, v35
s_mov_b32 s73, 15
s_nop 1
s_branch .Lqa_hr33
.Lqa_e40:
s_cmp_lg_u32 s70, 15
s_cbranch_scc1 .Lqa_e41
v_subrev_u32 v16, 18432, v16
s_mov_b32 s73, 3
s_nop 1
s_branch .Lqa_hr33
.Lqa_e41:
s_cmp_lg_u32 s70, 3
s_cbranch_scc1 .Lqa_e42
v_add_u32 v45, s51, v45
s_mov_b32 s73, 2
s_nop 1
s_branch .Lqa_hr33
.Lqa_e42:
s_cmp_lg_u32 s70, 2
s_cbranch_scc1 .Lqa_e43
v_add_u32 v44, s51, v44
v_add_u32 v46, -32, v46
s_add_u32 s46, s46, s50
s_addc_u32 s47, s47, 0
s_mov_b32 s73, 0
s_nop 1
s_branch .Lqa_hr33
.Lqa_e43:
s_cmp_lg_u32 s70, 0
s_cbranch_scc1 .Lqa_e44
v_or_b32 v32, s52, v32
v_or_b32 v33, s52, v33
v_or_b32 v34, s52, v34
v_or_b32 v35, s52, v35
s_mov_b32 s73, 999
s_nop 1
s_branch .Lqa_hr33
.Lqa_e44:
s_branch .Lqa_bk45
.Lqa_hk49:
s_cmp_lg_u32 s70, 18
s_cbranch_scc1 .Lqa_e51
s_add_u32 s42, s42, s50
s_addc_u32 s43, s43, 0
s_mov_b64 s[76:77], -1
s_mov_b64 s[46:47], s[44:45]
s_mov_b32 s73, 17
s_nop 1
s_branch .Lqa_hr50
.Lqa_e51:
s_cmp_lg_u32 s70, 17
s_cbranch_scc1 .Lqa_e52
s_mov_b32 s46, -18432
s_mov_b32 s47, -1
v_or_b32 v206, s52, v206
v_or_b32 v207, s52, v207
v_or_b32 v208, s52, v208
v_or_b32 v209, s52, v209
s_mov_b32 s73, 16
s_nop 1
s_branch .Lqa_hr50
.Lqa_e52:
s_cmp_lg_u32 s70, 16
s_cbranch_scc1 .Lqa_e53
v_mov_b32 v206, v32
v_mov_b32 v207, v33
v_mov_b32 v208, v34
v_mov_b32 v209, v35
s_mov_b32 s73, 15
s_nop 1
s_branch .Lqa_hr50
.Lqa_e53:
s_cmp_lg_u32 s70, 15
s_cbranch_scc1 .Lqa_e54
v_add_u32 v48, -1, v48
v_add_lshl_u32 v252, v48, 0, 7
v_bfi_b32 v252, s69, v252, v47
s_mov_b32 s73, 3
s_nop 1
s_branch .Lqa_hr50
.Lqa_e54:
s_cmp_lg_u32 s70, 3
s_cbranch_scc1 .Lqa_e55
s_mov_b32 s73, 2
s_nop 1
s_branch .Lqa_hr50
.Lqa_e55:
s_cmp_lg_u32 s70, 2
s_cbranch_scc1 .Lqa_e56
s_add_u32 s46, s46, s50
s_addc_u32 s47, s47, 0
s_mov_b32 s73, 0
s_nop 1
s_branch .Lqa_hr50
.Lqa_e56:
s_cmp_lg_u32 s70, 0
s_cbranch_scc1 .Lqa_e57
v_or_b32 v32, s52, v32
v_or_b32 v33, s52, v33
v_or_b32 v34, s52, v34
v_or_b32 v35, s52, v35
s_mov_b32 s73, 999
s_nop 1
s_branch .Lqa_hr50
.Lqa_e57:
s_branch .Lqa_bk58
.Lqa_sp62:
s_sleep 1
v_add_lshl_u32 v253, v50, 0, 7
v_bfi_b32 v253, s69, v253, v49
global_load_dword v53, v253, s[38:39] sc1
s_add_u32 s71, s71, 1
s_waitcnt vmcnt(0)
v_sub_u32 v53, v53, v50
v_cmp_lt_u32 vcc, 0x7f, v53
s_cbranch_vccz .Lqa_rt63
s_cmp_lt_u32 s71, 3000000
s_cbranch_scc1 .Lqa_sp62
s_branch .Lqa_rt63
.Lqa_sp64:
s_sleep 1
v_add_lshl_u32 v253, v50, 0, 7
v_bfi_b32 v253, s69, v253, v49
global_load_dword v53, v253, s[38:39] sc1
s_add_u32 s71, s71, 1
s_waitcnt vmcnt(0)
v_sub_u32 v53, v53, v50
v_cmp_lt_u32 vcc, 0x7f, v53
s_cbranch_vccz .Lqa_rt65
s_cmp_lt_u32 s71, 3000000
s_cbranch_scc1 .Lqa_sp64
s_branch .Lqa_rt65
.Lqa_sp66:
s_sleep 1
v_add_lshl_u32 v253, v50, 0, 7
v_bfi_b32 v253, s69, v253, v49
global_load_dword v53, v253, s[38:39] sc1
s_add_u32 s71, s71, 1
s_waitcnt vmcnt(0)
v_sub_u32 v53, v53, v50
v_cmp_lt_u32 vcc, 0x7f, v53
s_cbranch_vccz .Lqa_rt67
s_cmp_lt_u32 s71, 3000000
s_cbranch_scc1 .Lqa_sp66
s_branch .Lqa_rt67
.Lqa_sp68:
s_sleep 1
v_add_lshl_u32 v253, v50, 0, 7
v_bfi_b32 v253, s69, v253, v49
global_load_dword v53, v253, s[38:39] sc1
s_add_u32 s71, s71, 1
s_waitcnt vmcnt(0)
v_sub_u32 v53, v53, v50
v_cmp_lt_u32 vcc, 0x7f, v53
s_cbranch_vccz .Lqa_rt69
s_cmp_lt_u32 s71, 3000000
s_cbranch_scc1 .Lqa_sp68
s_branch .Lqa_rt69
.Le0:
)"
    :: "{v[0:31]}"(va), "{v[32:63]}"(vb), "{s[36:51]}"(sa), "{s[52:67]}"(sb) : "v255", "a255", "s99", "vcc", "memory");
    return;
  }
  if (wave == 2) {
    const unsigned o = min((lane & 7) * 1048576u, 5308412u);
    touch(wg + o); touch(wu + o); touch(wd + o); touch(out + min(o, 2359292u));
    touch(ws + min(lane * 2097152u, WS_END - 4));
    if (blockIdx.x - 40 < 400 * FL / 8192) touch(ws + WS_FL + ((blockIdx.x - 40) * 64 + lane) * 128);
  }
  if (wave < 3 && !(xc & 1)) touch(wo + min((jj - 4) * 186 + threadIdx.x, 5183u) * 128);
  if (jj >= 14 && wave < 2) touch(x0 + ((blockIdx.x - 112) * 128 + threadIdx.x) * 128);
  if ((jj == 4 || jj == 31) && !(xc & 1)) {
    const unsigned wo_rho = xc >> 1, wo_cw = jj & 1;
    const unsigned r = lane & 15, g = lane >> 4, q = r >> 2, p = lane & 3;
    const unsigned rho = wo_rho, cw = wo_cw, row0 = rho * RP + PAD, pc0 = wave * 5;
    auto wv = [&](unsigned j) { const unsigned b = j * 1024 + lane * 16, rw = b / 576; return rw * 576 + b + 4096 - (j & 7) * 1024; };
    u32x32 va; u32x16 sa;
    va[0] = wv(0);
    va[1] = wv(1);
    va[2] = wv(2);
    va[3] = wv(3);
    va[4] = wv(4);
    va[5] = wv(5);
    va[6] = wv(6);
    va[7] = wv(7);
    va[8] = wv(8);
    va[9] = (8 * g + q) * 576 + cw * 64 + wave * 128 + p * 16;
    va[10] = (8 * g + q) * 576 + (cw ? 0 : 512) + p * 16 + (wave >> 1) * 8;
    va[11] = WS_O + (row0 - 2) * XT + pc0 * 1024 + 2048 + lane * 16;
    va[12] = WS_O + (row0 + 63) * XT + pc0 * 1024 + 2048 + lane * 16;
    va[13] = WS_X1 + (row0 - 5) * X1P + (cw * 10 + wave * 2) * 1024 + lane * 16;
    va[14] = WS_X1 + (row0 - 5) * X1P + (8 + cw) * 1024 + lane * 16;
    va[15] = WS_X1 + (row0 - 7) * X1P + XT + cw * 128 + r * 8;
    va[16] = ((rho * 32 + 31) * 16 + 0 + (lane >> 3)) * 1152 + (cw * 5 + wave) * 128 + (((lane ^ (lane >> 3)) & 7) << 4) - 0;
    va[17] = ((rho * 32 + 31) * 16 + 8 + (lane >> 3)) * 1152 + (cw * 5 + wave) * 128 + (((lane ^ (lane >> 3)) & 7) << 4) - 1024;
    va[18] = ((rho * 32 + 31) * 16 + r) * 1152 + (8 + cw) * 64 + g * 16;
    va[19] = 47104 + wave * 2048 + r * 128 + (((0 + g) ^ (r & 7)) << 4);
    va[20] = 47104 + wave * 2048 + r * 128 + (((4 + g) ^ (r & 7)) << 4);
    va[21] = WS_FL + rho * FR + FO + (lane & 7) * FL;
    va[22] = 0u - 2;
    va[23] = WS_FL + rho * FR + FX1 + cw * FL;
    va[24] = 0u - 6;
    va[25] = lane * 16;
    va[26] = lane * 16;
    va[27] = lane * 8;
    sa[0] = 4 + wave; sa[2] = (unsigned)(unsigned long long)ws; sa[3] = (unsigned)((unsigned long long)ws >> 32);
    sa[4] = uni((unsigned)(unsigned long long)(wo + wave * 16 * 1152 + cw * 576));
    sa[5] = uni((unsigned)((unsigned long long)(wo + wave * 16 * 1152 + cw * 576) >> 32));
    sa[6] = uni((unsigned)(unsigned long long)(x0));
    sa[7] = uni((unsigned)((unsigned long long)(x0) >> 32));
    sa[8] = uni((unsigned)(unsigned long long)(x0));
    sa[9] = uni((unsigned)((unsigned long long)(x0) >> 32));
    sa[10] = uni((unsigned)(unsigned long long)(ws + WS_X2));
    sa[11] = uni((unsigned)((unsigned long long)(ws + WS_X2) >> 32));
    sa[12] = uni(wave * 9216);
    sa[13] = uni(pc0 * 1024 + 2048);
    asm volatile(
R"(.Lb1:
s_movk_i32 s69, 0x380
s_mov_b32 s71, 0
s_mov_b32 s74, 0
s_load_dword s37, s[38:39], 0x0
s_nop 1
s_and_b32 s75, s36, 3
s_cmp_eq_u32 s75, 1
s_cbranch_scc1 .Lwo_w2
s_cmp_eq_u32 s75, 2
s_cbranch_scc1 .Lwo_w3
s_cmp_eq_u32 s75, 3
s_cbranch_scc1 .Lwo_w4
.Lwo_w1:
s_add_u32 m0, s48, 4096
s_nop 0
global_load_lds_dwordx4 v0, s[40:41] offset:-4096
global_load_lds_dwordx4 v1, s[40:41] offset:-3072
global_load_lds_dwordx4 v2, s[40:41] offset:-2048
global_load_lds_dwordx4 v3, s[40:41] offset:-1024
global_load_lds_dwordx4 v4, s[40:41] offset:0
global_load_lds_dwordx4 v5, s[40:41] offset:1024
global_load_lds_dwordx4 v6, s[40:41] offset:2048
global_load_lds_dwordx4 v7, s[40:41] offset:3072
s_add_u32 m0, s48, 12288
s_nop 0
global_load_lds_dwordx4 v8, s[40:41] offset:-4096
s_add_u32 s40, s40, 73728
s_addc_u32 s41, s41, 0
s_add_u32 m0, s48, 40960
s_nop 0
global_load_lds_dwordx4 v0, s[40:41] offset:-4096
global_load_lds_dwordx4 v1, s[40:41] offset:-3072
global_load_lds_dwordx4 v2, s[40:41] offset:-2048
global_load_lds_dwordx4 v3, s[40:41] offset:-1024
global_load_lds_dwordx4 v4, s[40:41] offset:0
global_load_lds_dwordx4 v5, s[40:41] offset:1024
global_load_lds_dwordx4 v6, s[40:41] offset:2048
global_load_lds_dwordx4 v7, s[40:41] offset:3072
s_add_u32 m0, s48, 49152
s_nop 0
global_load_lds_dwordx4 v8, s[40:41] offset:-4096
s_add_u32 s40, s40, 73728
s_addc_u32 s41, s41, 0
s_add_u32 m0, s48, 77824
s_nop 0
global_load_lds_dwordx4 v0, s[40:41] offset:-4096
global_load_lds_dwordx4 v1, s[40:41] offset:-3072
global_load_lds_dwordx4 v2, s[40:41] offset:-2048
global_load_lds_dwordx4 v3, s[40:41] offset:-1024
global_load_lds_dwordx4 v4, s[40:41] offset:0
global_load_lds_dwordx4 v5, s[40:41] offset:1024
global_load_lds_dwordx4 v6, s[40:41] offset:2048
global_load_lds_dwordx4 v7, s[40:41] offset:3072
s_add_u32 m0, s48, 86016
s_nop 0
global_load_lds_dwordx4 v8, s[40:41] offset:-4096
s_add_u32 s40, s40, 73728
s_addc_u32 s41, s41, 0
v_add_u32 v204, 0x10000, v9
v_add_u32 v205, 0x20000, v9
v_add_u32 v206, 0x10000, v10
v_add_u32 v207, 0x20000, v10
s_waitcnt lgkmcnt(0)
s_mul_i32 s37, s37, 0x9E3779B1
s_add_u32 s37, s37, 0x7F4A7C15
v_add_u32 v22, s37, v22
v_add_u32 v24, s37, v24
s_waitcnt vmcnt(18)
s_barrier
s_add_u32 m0, s48, 114688
ds_read_b64_tr_b16 a[0:1], v9 offset:0
ds_read_b64_tr_b16 a[2:3], v9 offset:2304
global_load_lds_dwordx4 v0, s[40:41] offset:-4096
ds_read_b64_tr_b16 a[4:5], v9 offset:8
ds_read_b64_tr_b16 a[6:7], v9 offset:2312
global_load_lds_dwordx4 v1, s[40:41] offset:-3072
ds_read_b64_tr_b16 a[8:9], v9 offset:64
global_load_lds_dwordx4 v2, s[40:41] offset:-2048
ds_read_b64_tr_b16 a[10:11], v9 offset:2368
ds_read_b64_tr_b16 a[12:13], v9 offset:72
global_load_lds_dwordx4 v3, s[40:41] offset:-1024
ds_read_b64_tr_b16 a[14:15], v9 offset:2376
global_load_lds_dwordx4 v4, s[40:41] offset:0
ds_read_b64_tr_b16 v[60:61], v10 offset:0
ds_read_b64_tr_b16 v[62:63], v10 offset:2304
global_load_lds_dwordx4 v5, s[40:41] offset:1024
ds_read_b64_tr_b16 a[16:17], v9 offset:18432
global_load_lds_dwordx4 v6, s[40:41] offset:2048
ds_read_b64_tr_b16 a[18:19], v9 offset:20736
ds_read_b64_tr_b16 a[20:21], v9 offset:18440
global_load_lds_dwordx4 v7, s[40:41] offset:3072
ds_read_b64_tr_b16 a[22:23], v9 offset:20744
s_add_u32 m0, s48, 122880
ds_read_b64_tr_b16 a[24:25], v9 offset:18496
ds_read_b64_tr_b16 a[26:27], v9 offset:20800
global_load_lds_dwordx4 v8, s[40:41] offset:-4096
ds_read_b64_tr_b16 a[28:29], v9 offset:18504
s_add_u32 s40, s40, 73728
ds_read_b64_tr_b16 a[30:31], v9 offset:20808
ds_read_b64_tr_b16 v[64:65], v10 offset:18432
s_addc_u32 s41, s41, 0
ds_read_b64_tr_b16 v[66:67], v10 offset:20736
s_waitcnt vmcnt(18) lgkmcnt(0)
s_barrier
s_add_u32 m0, s48, 4096
ds_read_b64_tr_b16 a[32:33], v9 offset:36864
ds_read_b64_tr_b16 a[34:35], v9 offset:39168
global_load_lds_dwordx4 v0, s[40:41] offset:-4096
ds_read_b64_tr_b16 a[36:37], v9 offset:36872
ds_read_b64_tr_b16 a[38:39], v9 offset:39176
global_load_lds_dwordx4 v1, s[40:41] offset:-3072
ds_read_b64_tr_b16 a[40:41], v9 offset:36928
global_load_lds_dwordx4 v2, s[40:41] offset:-2048
ds_read_b64_tr_b16 a[42:43], v9 offset:39232
ds_read_b64_tr_b16 a[44:45], v9 offset:36936
global_load_lds_dwordx4 v3, s[40:41] offset:-1024
ds_read_b64_tr_b16 a[46:47], v9 offset:39240
global_load_lds_dwordx4 v4, s[40:41] offset:0
ds_read_b64_tr_b16 v[68:69], v10 offset:36864
ds_read_b64_tr_b16 v[70:71], v10 offset:39168
global_load_lds_dwordx4 v5, s[40:41] offset:1024
ds_read_b64_tr_b16 a[48:49], v9 offset:55296
global_load_lds_dwordx4 v6, s[40:41] offset:2048
ds_read_b64_tr_b16 a[50:51], v9 offset:57600
ds_read_b64_tr_b16 a[52:53], v9 offset:55304
global_load_lds_dwordx4 v7, s[40:41] offset:3072
ds_read_b64_tr_b16 a[54:55], v9 offset:57608
s_add_u32 m0, s48, 12288
ds_read_b64_tr_b16 a[56:57], v9 offset:55360
ds_read_b64_tr_b16 a[58:59], v9 offset:57664
global_load_lds_dwordx4 v8, s[40:41] offset:-4096
ds_read_b64_tr_b16 a[60:61], v9 offset:55368
s_add_u32 s40, s40, 73728
ds_read_b64_tr_b16 a[62:63], v9 offset:57672
ds_read_b64_tr_b16 v[72:73], v10 offset:55296
s_addc_u32 s41, s41, 0
ds_read_b64_tr_b16 v[74:75], v10 offset:57600
s_waitcnt vmcnt(18) lgkmcnt(0)
s_barrier
s_add_u32 m0, s48, 40960
ds_read_b64_tr_b16 a[64:65], v204 offset:8192
ds_read_b64_tr_b16 a[66:67], v204 offset:10496
global_load_lds_dwordx4 v0, s[40:41] offset:-4096
ds_read_b64_tr_b16 a[68:69], v204 offset:8200
ds_read_b64_tr_b16 a[70:71], v204 offset:10504
global_load_lds_dwordx4 v1, s[40:41] offset:-3072
ds_read_b64_tr_b16 a[72:73], v204 offset:8256
global_load_lds_dwordx4 v2, s[40:41] offset:-2048
ds_read_b64_tr_b16 a[74:75], v204 offset:10560
ds_read_b64_tr_b16 a[76:77], v204 offset:8264
global_load_lds_dwordx4 v3, s[40:41] offset:-1024
ds_read_b64_tr_b16 a[78:79], v204 offset:10568
global_load_lds_dwordx4 v4, s[40:41] offset:0
ds_read_b64_tr_b16 v[76:77], v206 offset:8192
ds_read_b64_tr_b16 v[78:79], v206 offset:10496
global_load_lds_dwordx4 v5, s[40:41] offset:1024
ds_read_b64_tr_b16 a[80:81], v204 offset:26624
global_load_lds_dwordx4 v6, s[40:41] offset:2048
ds_read_b64_tr_b16 a[82:83], v204 offset:28928
ds_read_b64_tr_b16 a[84:85], v204 offset:26632
global_load_lds_dwordx4 v7, s[40:41] offset:3072
ds_read_b64_tr_b16 a[86:87], v204 offset:28936
s_add_u32 m0, s48, 49152
ds_read_b64_tr_b16 a[88:89], v204 offset:26688
ds_read_b64_tr_b16 a[90:91], v204 offset:28992
global_load_lds_dwordx4 v8, s[40:41] offset:-4096
ds_read_b64_tr_b16 a[92:93], v204 offset:26696
s_add_u32 s40, s40, 73728
ds_read_b64_tr_b16 a[94:95], v204 offset:29000
ds_read_b64_tr_b16 v[80:81], v206 offset:26624
s_addc_u32 s41, s41, 0
ds_read_b64_tr_b16 v[82:83], v206 offset:28928
s_waitcnt vmcnt(18) lgkmcnt(0)
s_barrier
s_add_u32 m0, s48, 77824
ds_read_b64_tr_b16 a[96:97], v204 offset:45056
ds_read_b64_tr_b16 a[98:99], v204 offset:47360
global_load_lds_dwordx4 v0, s[40:41] offset:-4096
ds_read_b64_tr_b16 a[100:101], v204 offset:45064
ds_read_b64_tr_b16 a[102:103], v204 offset:47368
global_load_lds_dwordx4 v1, s[40:41] offset:-3072
ds_read_b64_tr_b16 a[104:105], v204 offset:45120
global_load_lds_dwordx4 v2, s[40:41] offset:-2048
ds_read_b64_tr_b16 a[106:107], v204 offset:47424
ds_read_b64_tr_b16 a[108:109], v204 offset:45128
global_load_lds_dwordx4 v3, s[40:41] offset:-1024
ds_read_b64_tr_b16 a[110:111], v204 offset:47432
global_load_lds_dwordx4 v4, s[40:41] offset:0
ds_read_b64_tr_b16 v[84:85], v206 offset:45056
ds_read_b64_tr_b16 v[86:87], v206 offset:47360
global_load_lds_dwordx4 v5, s[40:41] offset:1024
ds_read_b64_tr_b16 a[112:113], v204 offset:63488
global_load_lds_dwordx4 v6, s[40:41] offset:2048
ds_read_b64_tr_b16 a[114:115], v205 offset:256
ds_read_b64_tr_b16 a[116:117], v204 offset:63496
global_load_lds_dwordx4 v7, s[40:41] offset:3072
ds_read_b64_tr_b16 a[118:119], v205 offset:264
s_add_u32 m0, s48, 86016
ds_read_b64_tr_b16 a[120:121], v204 offset:63552
ds_read_b64_tr_b16 a[122:123], v205 offset:320
global_load_lds_dwordx4 v8, s[40:41] offset:-4096
ds_read_b64_tr_b16 a[124:125], v204 offset:63560
s_add_u32 s40, s40, 73728
ds_read_b64_tr_b16 a[126:127], v205 offset:328
ds_read_b64_tr_b16 v[88:89], v206 offset:63488
s_addc_u32 s41, s41, 0
ds_read_b64_tr_b16 v[90:91], v207 offset:256
s_waitcnt vmcnt(18) lgkmcnt(0)
s_barrier
s_add_u32 m0, s48, 114688
ds_read_b64_tr_b16 a[128:129], v9 offset:0
ds_read_b64_tr_b16 a[130:131], v9 offset:2304
global_load_lds_dwordx4 v0, s[40:41] offset:-4096
ds_read_b64_tr_b16 a[132:133], v9 offset:8
global_load_lds_dwordx4 v1, s[40:41] offset:-3072
ds_read_b64_tr_b16 a[134:135], v9 offset:2312
ds_read_b64_tr_b16 a[136:137], v9 offset:64
global_load_lds_dwordx4 v2, s[40:41] offset:-2048
ds_read_b64_tr_b16 a[138:139], v9 offset:2368
global_load_lds_dwordx4 v3, s[40:41] offset:-1024
ds_read_b64_tr_b16 a[140:141], v9 offset:72
global_load_lds_dwordx4 v4, s[40:41] offset:0
ds_read_b64_tr_b16 a[142:143], v9 offset:2376
ds_read_b64_tr_b16 v[92:93], v10 offset:0
global_load_lds_dwordx4 v5, s[40:41] offset:1024
ds_read_b64_tr_b16 v[94:95], v10 offset:2304
global_load_lds_dwordx4 v6, s[40:41] offset:2048
ds_read_b64_tr_b16 a[144:145], v9 offset:18432
ds_read_b64_tr_b16 a[146:147], v9 offset:20736
global_load_lds_dwordx4 v7, s[40:41] offset:3072
ds_read_b64_tr_b16 a[148:149], v9 offset:18440
s_add_u32 m0, s48, 122880
ds_read_b64_tr_b16 a[150:151], v9 offset:20744
global_load_lds_dwordx4 v8, s[40:41] offset:-4096
ds_read_b64_tr_b16 a[152:153], v9 offset:18496
ds_read_b64_tr_b16 a[154:155], v9 offset:20800
s_add_u32 s40, s40, 73728
ds_read_b64_tr_b16 a[156:157], v9 offset:18504
s_addc_u32 s41, s41, 0
ds_read_b64_tr_b16 a[158:159], v9 offset:20808
s_waitcnt vmcnt(18) lgkmcnt(0)
s_barrier
s_add_u32 m0, s48, 4096
ds_read_b64_tr_b16 a[160:161], v9 offset:36864
ds_read_b64_tr_b16 a[162:163], v9 offset:39168
global_load_lds_dwordx4 v0, s[40:41] offset:-4096
ds_read_b64_tr_b16 a[164:165], v9 offset:36872
global_load_lds_dwordx4 v1, s[40:41] offset:-3072
ds_read_b64_tr_b16 a[166:167], v9 offset:39176
global_load_lds_dwordx4 v2, s[40:41] offset:-2048
ds_read_b64_tr_b16 a[168:169], v9 offset:36928
global_load_lds_dwordx4 v3, s[40:41] offset:-1024
ds_read_b64_tr_b16 a[170:171], v9 offset:39232
ds_read_b64_tr_b16 a[172:173], v9 offset:36936
global_load_lds_dwordx4 v4, s[40:41] offset:0
ds_read_b64_tr_b16 a[174:175], v9 offset:39240
global_load_lds_dwordx4 v5, s[40:41] offset:1024
ds_read_b64_tr_b16 a[176:177], v9 offset:55296
global_load_lds_dwordx4 v6, s[40:41] offset:2048
ds_read_b64_tr_b16 a[178:179], v9 offset:57600
global_load_lds_dwordx4 v7, s[40:41] offset:3072
ds_read_b64_tr_b16 a[180:181], v9 offset:55304
ds_read_b64_tr_b16 a[182:183], v9 offset:57608
s_add_u32 m0, s48, 12288
ds_read_b64_tr_b16 a[184:185], v9 offset:55360
global_load_lds_dwordx4 v8, s[40:41] offset:-4096
ds_read_b64_tr_b16 a[186:187], v9 offset:57664
s_sub_u32 s40, s40, 589824
ds_read_b64_tr_b16 a[188:189], v9 offset:55368
s_subb_u32 s41, s41, 0
ds_read_b64_tr_b16 a[190:191], v9 offset:57672
s_waitcnt vmcnt(18) lgkmcnt(0)
s_barrier
ds_read_b64_tr_b16 a[192:193], v204 offset:8192
ds_read_b64_tr_b16 a[194:195], v204 offset:10496
ds_read_b64_tr_b16 a[196:197], v204 offset:8200
ds_read_b64_tr_b16 a[198:199], v204 offset:10504
ds_read_b64_tr_b16 a[200:201], v204 offset:8256
ds_read_b64_tr_b16 a[202:203], v204 offset:10560
ds_read_b64_tr_b16 a[204:205], v204 offset:8264
ds_read_b64_tr_b16 a[206:207], v204 offset:10568
ds_read_b64_tr_b16 a[208:209], v204 offset:26624
ds_read_b64_tr_b16 a[210:211], v204 offset:28928
ds_read_b64_tr_b16 a[212:213], v204 offset:26632
ds_read_b64_tr_b16 a[214:215], v204 offset:28936
ds_read_b64_tr_b16 a[216:217], v204 offset:26688
ds_read_b64_tr_b16 a[218:219], v204 offset:28992
ds_read_b64_tr_b16 a[220:221], v204 offset:26696
ds_read_b64_tr_b16 a[222:223], v204 offset:29000
s_waitcnt vmcnt(9) lgkmcnt(0)
s_barrier
ds_read_b64_tr_b16 a[224:225], v204 offset:45056
ds_read_b64_tr_b16 a[226:227], v204 offset:47360
ds_read_b64_tr_b16 a[228:229], v204 offset:45064
ds_read_b64_tr_b16 a[230:231], v204 offset:47368
ds_read_b64_tr_b16 a[232:233], v204 offset:45120
ds_read_b64_tr_b16 a[234:235], v204 offset:47424
ds_read_b64_tr_b16 a[236:237], v204 offset:45128
ds_read_b64_tr_b16 a[238:239], v204 offset:47432
ds_read_b64_tr_b16 a[240:241], v204 offset:63488
ds_read_b64_tr_b16 a[242:243], v205 offset:256
ds_read_b64_tr_b16 a[244:245], v204 offset:63496
ds_read_b64_tr_b16 a[246:247], v205 offset:264
ds_read_b64_tr_b16 a[248:249], v204 offset:63552
ds_read_b64_tr_b16 a[250:251], v205 offset:320
ds_read_b64_tr_b16 a[252:253], v204 offset:63560
ds_read_b64_tr_b16 a[254:255], v205 offset:328
s_waitcnt vmcnt(0) lgkmcnt(0)
s_barrier
ds_read_b64_tr_b16 v[28:29], v9 offset:0
ds_read_b64_tr_b16 v[30:31], v9 offset:2304
ds_read_b64_tr_b16 v[32:33], v9 offset:8
ds_read_b64_tr_b16 v[34:35], v9 offset:2312
ds_read_b64_tr_b16 v[36:37], v9 offset:64
ds_read_b64_tr_b16 v[38:39], v9 offset:2368
ds_read_b64_tr_b16 v[40:41], v9 offset:72
ds_read_b64_tr_b16 v[42:43], v9 offset:2376
ds_read_b64_tr_b16 v[44:45], v9 offset:18432
ds_read_b64_tr_b16 v[46:47], v9 offset:20736
ds_read_b64_tr_b16 v[48:49], v9 offset:18440
ds_read_b64_tr_b16 v[50:51], v9 offset:20744
ds_read_b64_tr_b16 v[52:53], v9 offset:18496
ds_read_b64_tr_b16 v[54:55], v9 offset:20800
ds_read_b64_tr_b16 v[56:57], v9 offset:18504
ds_read_b64_tr_b16 v[58:59], v9 offset:20808
s_waitcnt lgkmcnt(0)
s_barrier
v_add_lshl_u32 v252, v24, 0, 7
v_bfi_b32 v252, s69, v252, v23
v_add_lshl_u32 v253, v22, 0, 7
v_bfi_b32 v253, s69, v253, v21
s_mov_b64 s[76:77], 0
s_add_u32 m0, s49, 18432
s_mov_b32 s70, 33
s_waitcnt vmcnt(0) lgkmcnt(0)
s_nop 7
.p2align 6
.Lwo_top5:
s_waitcnt lgkmcnt(2)
v_mfma_f32_16x16x32_bf16 v[96:99], a[0:3], v[132:135], v[96:99]
ds_read_b128 v[144:147], v25 offset:3072
v_mfma_f32_16x16x32_bf16 v[100:103], a[4:7], v[132:135], v[100:103]
v_mfma_f32_16x16x32_bf16 v[104:107], a[8:11], v[132:135], v[104:107]
s_waitcnt vmcnt(7)
s_barrier
v_mfma_f32_16x16x32_bf16 v[108:111], a[12:15], v[132:135], v[108:111]
v_cvt_pk_bf16_f32 v164, v112, v113
v_cvt_pk_bf16_f32 v165, v114, v115
s_mov_b64 exec, 1
global_store_dword v252, v24, s[38:39] sc1
s_mov_b64 exec, -1
v_mfma_f32_16x16x32_bf16 v[128:131], v[60:63], v[132:135], 0
s_waitcnt lgkmcnt(2)
v_cvt_pk_bf16_f32 v166, v116, v117
v_cvt_pk_bf16_f32 v167, v118, v119
v_add_u32 v24, 1, v24
v_add_lshl_u32 v252, v24, 0, 7
v_mfma_f32_16x16x32_bf16 v[96:99], a[16:19], v[136:139], v[96:99]
ds_read_b128 v[132:135], v25 offset:4096
v_bfi_b32 v252, s69, v252, v23
v_mfma_f32_16x16x32_bf16 v[100:103], a[20:23], v[136:139], v[100:103]
global_store_dwordx4 v13, v[164:167], s[38:39] offset:0 sc1
v_mfma_f32_16x16x32_bf16 v[104:107], a[24:27], v[136:139], v[104:107]
v_cvt_pk_bf16_f32 v168, v120, v121
v_cvt_pk_bf16_f32 v169, v122, v123
v_mfma_f32_16x16x32_bf16 v[108:111], a[28:31], v[136:139], v[108:111]
v_cvt_pk_bf16_f32 v170, v124, v125
v_cvt_pk_bf16_f32 v171, v126, v127
v_mfma_f32_16x16x32_bf16 v[128:131], v[64:67], v[136:139], v[128:131]
s_waitcnt lgkmcnt(2)
global_store_dwordx4 v13, v[168:171], s[38:39] offset:1024 sc1
v_mfma_f32_16x16x32_bf16 v[96:99], a[32:35], v[140:143], v[96:99]
s_cmp_eq_u32 s70, 32
ds_read_b128 v[136:139], v25 offset:5120
v_add_u32 v13, 18688, v13
v_mfma_f32_16x16x32_bf16 v[100:103], a[36:39], v[140:143], v[100:103]
s_cselect_b64 s[42:43], s[44:45], s[42:43]
v_add_f32 v254, v112, v113
v_mul_f32 v255, v112, v112
v_mfma_f32_16x16x32_bf16 v[104:107], a[40:43], v[140:143], v[104:107]
s_cmp_eq_u32 s70, 16
v_fmac_f32 v255, v113, v113
v_add_f32 v254, v254, v114
v_mfma_f32_16x16x32_bf16 v[108:111], a[44:47], v[140:143], v[108:111]
s_cselect_b64 s[42:43], s[46:47], s[42:43]
v_fmac_f32 v255, v114, v114
v_add_f32 v254, v254, v115
v_mfma_f32_16x16x32_bf16 v[128:131], v[68:71], v[140:143], v[128:131]
s_waitcnt lgkmcnt(2)
v_fmac_f32 v255, v115, v115
v_add_f32 v254, v254, v116
v_mfma_f32_16x16x32_bf16 v[96:99], a[48:51], v[144:147], v[96:99]
s_xor_b32 s49, s49, 0x10000
ds_read_b128 v[140:143], v25 offset:6144
v_fmac_f32 v255, v116, v116
v_mfma_f32_16x16x32_bf16 v[100:103], a[52:55], v[144:147], v[100:103]
s_mov_b32 m0, 47104
v_add_f32 v254, v254, v117
v_fmac_f32 v255, v117, v117
)"
R"(v_mfma_f32_16x16x32_bf16 v[104:107], a[56:59], v[144:147], v[104:107]
v_add_f32 v254, v254, v118
v_fmac_f32 v255, v118, v118
v_mfma_f32_16x16x32_bf16 v[108:111], a[60:63], v[144:147], v[108:111]
global_load_lds_dwordx4 v16, s[42:43] offset:0 sc1
v_add_f32 v254, v254, v119
v_mfma_f32_16x16x32_bf16 v[128:131], v[72:75], v[144:147], v[128:131]
s_waitcnt lgkmcnt(2)
v_fmac_f32 v255, v119, v119
v_add_f32 v254, v254, v120
v_mfma_f32_16x16x32_bf16 v[96:99], a[64:67], v[132:135], v[96:99]
ds_read_b128 v[144:147], v25 offset:7168
v_fmac_f32 v255, v120, v120
v_mfma_f32_16x16x32_bf16 v[100:103], a[68:71], v[132:135], v[100:103]
v_add_f32 v254, v254, v121
v_fmac_f32 v255, v121, v121
v_mfma_f32_16x16x32_bf16 v[104:107], a[72:75], v[132:135], v[104:107]
v_add_f32 v254, v254, v122
v_fmac_f32 v255, v122, v122
v_mfma_f32_16x16x32_bf16 v[108:111], a[76:79], v[132:135], v[108:111]
v_add_f32 v254, v254, v123
v_fmac_f32 v255, v123, v123
v_mfma_f32_16x16x32_bf16 v[128:131], v[76:79], v[132:135], v[128:131]
s_waitcnt vmcnt(9)
ds_read_b128 v[148:151], v19 offset:8192
ds_read_b128 v[152:155], v20 offset:8192
s_waitcnt lgkmcnt(4)
v_mfma_f32_16x16x32_bf16 v[96:99], a[80:83], v[136:139], v[96:99]
ds_read_b128 v[132:135], v25 offset:8192
global_load_lds_dwordx4 v17, s[42:43] offset:1024 sc1
v_mfma_f32_16x16x32_bf16 v[100:103], a[84:87], v[136:139], v[100:103]
v_add_f32 v254, v254, v124
v_fmac_f32 v255, v124, v124
v_mfma_f32_16x16x32_bf16 v[104:107], a[88:91], v[136:139], v[104:107]
v_add_f32 v254, v254, v125
v_fmac_f32 v255, v125, v125
v_mfma_f32_16x16x32_bf16 v[108:111], a[92:95], v[136:139], v[108:111]
v_add_f32 v254, v254, v126
v_fmac_f32 v255, v126, v126
v_mfma_f32_16x16x32_bf16 v[128:131], v[80:83], v[136:139], v[128:131]
s_waitcnt lgkmcnt(4)
v_add_f32 v254, v254, v127
v_fmac_f32 v255, v127, v127
v_mfma_f32_16x16x32_bf16 v[96:99], a[96:99], v[140:143], v[96:99]
s_add_u32 m0, s49, 0
ds_read_b128 v[136:139], v25 offset:9216
v_mfma_f32_16x16x32_bf16 v[100:103], a[100:103], v[140:143], v[100:103]
ds_write_b64 v27, v[254:255] offset:45056
v_mfma_f32_16x16x32_bf16 v[104:107], a[104:107], v[140:143], v[104:107]
s_waitcnt lgkmcnt(4)
v_lshlrev_b32 v112, 16, v148
v_and_b32 v113, 0xffff0000, v148
v_mfma_f32_16x16x32_bf16 v[108:111], a[108:111], v[140:143], v[108:111]
s_sub_u32 s42, s42, 18432
v_lshlrev_b32 v114, 16, v149
v_and_b32 v115, 0xffff0000, v149
v_mfma_f32_16x16x32_bf16 v[128:131], v[84:87], v[140:143], v[128:131]
v_lshlrev_b32 v116, 16, v150
v_and_b32 v117, 0xffff0000, v150
v_mfma_f32_16x16x32_bf16 v[96:99], a[112:115], v[144:147], v[96:99]
s_subb_u32 s43, s43, 0
ds_read_b128 v[140:143], v25 offset:10240
v_lshlrev_b32 v118, 16, v151
v_mfma_f32_16x16x32_bf16 v[100:103], a[116:119], v[144:147], v[100:103]
s_waitcnt lgkmcnt(4)
v_and_b32 v119, 0xffff0000, v151
v_lshlrev_b32 v120, 16, v152
v_mfma_f32_16x16x32_bf16 v[104:107], a[120:123], v[144:147], v[104:107]
v_and_b32 v121, 0xffff0000, v152
v_lshlrev_b32 v122, 16, v153
v_mfma_f32_16x16x32_bf16 v[108:111], a[124:127], v[144:147], v[108:111]
v_and_b32 v123, 0xffff0000, v153
v_lshlrev_b32 v124, 16, v154
v_mfma_f32_16x16x32_bf16 v[128:131], v[88:91], v[144:147], v[128:131]
s_waitcnt lgkmcnt(3)
v_and_b32 v125, 0xffff0000, v154
v_lshlrev_b32 v126, 16, v155
v_mfma_f32_16x16x32_bf16 v[96:99], a[128:131], v[132:135], v[96:99]
ds_read_b128 v[144:147], v25 offset:11264
v_and_b32 v127, 0xffff0000, v155
v_mfma_f32_16x16x32_bf16 v[100:103], a[132:135], v[132:135], v[100:103]
v_mfma_f32_16x16x32_bf16 v[104:107], a[136:139], v[132:135], v[104:107]
v_mfma_f32_16x16x32_bf16 v[108:111], a[140:143], v[132:135], v[108:111]
v_mfma_f32_16x16x32_bf16 v[128:131], v[92:95], v[132:135], v[128:131]
s_waitcnt lgkmcnt(3)
v_mfma_f32_16x16x32_bf16 v[96:99], a[144:147], v[136:139], v[96:99]
ds_read_b128 v[132:135], v25 offset:12288
v_mfma_f32_16x16x32_bf16 v[100:103], a[148:151], v[136:139], v[100:103]
v_mfma_f32_16x16x32_bf16 v[104:107], a[152:155], v[136:139], v[104:107]
v_mfma_f32_16x16x32_bf16 v[108:111], a[156:159], v[136:139], v[108:111]
s_waitcnt lgkmcnt(2)
v_mfma_f32_16x16x32_bf16 v[96:99], a[160:163], v[140:143], v[96:99]
ds_read_b128 v[136:139], v25 offset:13312
v_mfma_f32_16x16x32_bf16 v[100:103], a[164:167], v[140:143], v[100:103]
v_mfma_f32_16x16x32_bf16 v[104:107], a[168:171], v[140:143], v[104:107]
ds_write_b128 v26, v[128:131] offset:36864
v_mfma_f32_16x16x32_bf16 v[108:111], a[172:175], v[140:143], v[108:111]
s_waitcnt lgkmcnt(3)
v_mfma_f32_16x16x32_bf16 v[96:99], a[176:179], v[144:147], v[96:99]
ds_read_b128 v[140:143], v25 offset:14336
v_mfma_f32_16x16x32_bf16 v[100:103], a[180:183], v[144:147], v[100:103]
v_mfma_f32_16x16x32_bf16 v[104:107], a[184:187], v[144:147], v[104:107]
s_barrier
v_mfma_f32_16x16x32_bf16 v[108:111], a[188:191], v[144:147], v[108:111]
s_waitcnt lgkmcnt(3)
global_load_lds_dwordx4 v11, s[38:39] offset:-2048 sc1
v_mfma_f32_16x16x32_bf16 v[96:99], a[192:195], v[132:135], v[96:99]
ds_read_b128 v[144:147], v25 offset:15360
v_mfma_f32_16x16x32_bf16 v[100:103], a[196:199], v[132:135], v[100:103]
v_mfma_f32_16x16x32_bf16 v[104:107], a[200:203], v[132:135], v[104:107]
global_load_lds_dwordx4 v11, s[38:39] offset:-1024 sc1
v_mfma_f32_16x16x32_bf16 v[108:111], a[204:207], v[132:135], v[108:111]
s_waitcnt lgkmcnt(3)
v_mfma_f32_16x16x32_bf16 v[96:99], a[208:211], v[136:139], v[96:99]
ds_read_b128 v[132:135], v25 offset:16384
v_mfma_f32_16x16x32_bf16 v[100:103], a[212:215], v[136:139], v[100:103]
global_load_lds_dwordx4 v11, s[38:39] offset:0 sc1
v_mfma_f32_16x16x32_bf16 v[104:107], a[216:219], v[136:139], v[104:107]
v_mfma_f32_16x16x32_bf16 v[108:111], a[220:223], v[136:139], v[108:111]
s_waitcnt lgkmcnt(2)
v_mfma_f32_16x16x32_bf16 v[96:99], a[224:227], v[140:143], v[96:99]
ds_read_b128 v[136:139], v25 offset:17408
global_load_lds_dwordx4 v11, s[38:39] offset:1024 sc1
v_mfma_f32_16x16x32_bf16 v[100:103], a[228:231], v[140:143], v[100:103]
v_mfma_f32_16x16x32_bf16 v[104:107], a[232:235], v[140:143], v[104:107]
v_mfma_f32_16x16x32_bf16 v[108:111], a[236:239], v[140:143], v[108:111]
s_waitcnt lgkmcnt(2)
global_load_lds_dwordx4 v11, s[38:39] offset:2048 sc1
v_add_u32 v11, 18432, v11
v_mfma_f32_16x16x32_bf16 v[96:99], a[240:243], v[144:147], v[96:99]
v_min_u32 v11, v11, v12
v_mfma_f32_16x16x32_bf16 v[100:103], a[244:247], v[144:147], v[100:103]
v_mfma_f32_16x16x32_bf16 v[104:107], a[248:251], v[144:147], v[104:107]
s_waitcnt vmcnt(10)
s_barrier
v_mfma_f32_16x16x32_bf16 v[108:111], a[252:255], v[144:147], v[108:111]
s_waitcnt lgkmcnt(1)
ds_read_b128 v[140:143], v25 offset:18432
v_mfma_f32_16x16x32_bf16 v[96:99], v[28:31], v[132:135], v[96:99]
ds_read_b128 v[144:147], v25 offset:19456
v_mfma_f32_16x16x32_bf16 v[100:103], v[32:35], v[132:135], v[100:103]
v_mfma_f32_16x16x32_bf16 v[104:107], v[36:39], v[132:135], v[104:107]
v_mfma_f32_16x16x32_bf16 v[108:111], v[40:43], v[132:135], v[108:111]
s_waitcnt lgkmcnt(2)
v_mfma_f32_16x16x32_bf16 v[96:99], v[44:47], v[136:139], v[96:99]
ds_read_b128 v[132:135], v25 offset:20480
v_mfma_f32_16x16x32_bf16 v[100:103], v[48:51], v[136:139], v[100:103]
v_mfma_f32_16x16x32_bf16 v[104:107], v[52:55], v[136:139], v[104:107]
v_mfma_f32_16x16x32_bf16 v[108:111], v[56:59], v[136:139], v[108:111]
s_waitcnt lgkmcnt(2)
v_mfma_f32_16x16x32_bf16 v[112:115], a[0:3], v[140:143], v[112:115]
ds_read_b128 v[136:139], v25 offset:21504
v_mfma_f32_16x16x32_bf16 v[116:119], a[4:7], v[140:143], v[116:119]
v_mfma_f32_16x16x32_bf16 v[120:123], a[8:11], v[140:143], v[120:123]
s_waitcnt vmcnt(7)
s_barrier
v_mfma_f32_16x16x32_bf16 v[124:127], a[12:15], v[140:143], v[124:127]
s_mov_b64 exec, 1
global_store_dword v252, v24, s[38:39] sc1
s_mov_b64 exec, -1
v_add_u32 v24, 1, v24
v_add_lshl_u32 v252, v24, 0, 7
v_mfma_f32_16x16x32_bf16 v[128:131], v[60:63], v[140:143], 0
s_waitcnt lgkmcnt(2)
v_bfi_b32 v252, s69, v252, v23
v_cvt_pk_bf16_f32 v164, v96, v97
v_mfma_f32_16x16x32_bf16 v[112:115], a[16:19], v[144:147], v[112:115]
ds_read_b128 v[140:143], v25 offset:22528
v_cvt_pk_bf16_f32 v165, v98, v99
v_mfma_f32_16x16x32_bf16 v[116:119], a[20:23], v[144:147], v[116:119]
v_cvt_pk_bf16_f32 v166, v100, v101
v_cvt_pk_bf16_f32 v167, v102, v103
v_mfma_f32_16x16x32_bf16 v[120:123], a[24:27], v[144:147], v[120:123]
global_store_dwordx4 v13, v[164:167], s[38:39] offset:0 sc1
v_mfma_f32_16x16x32_bf16 v[124:127], a[28:31], v[144:147], v[124:127]
v_cvt_pk_bf16_f32 v168, v104, v105
v_cvt_pk_bf16_f32 v169, v106, v107
v_mfma_f32_16x16x32_bf16 v[128:131], v[64:67], v[144:147], v[128:131]
s_waitcnt lgkmcnt(2)
v_cvt_pk_bf16_f32 v170, v108, v109
v_cvt_pk_bf16_f32 v171, v110, v111
v_mfma_f32_16x16x32_bf16 v[112:115], a[32:35], v[132:135], v[112:115]
s_mov_b32 m0, 55296
ds_read_b128 v[144:147], v25 offset:23552
v_add_f32 v254, v96, v97
v_mfma_f32_16x16x32_bf16 v[116:119], a[36:39], v[132:135], v[116:119]
global_store_dwordx4 v13, v[168:171], s[38:39] offset:1024 sc1
v_mfma_f32_16x16x32_bf16 v[120:123], a[40:43], v[132:135], v[120:123]
v_add_u32 v13, 18688, v13
v_mul_f32 v255, v96, v96
v_mfma_f32_16x16x32_bf16 v[124:127], a[44:47], v[132:135], v[124:127]
v_fmac_f32 v255, v97, v97
v_add_f32 v254, v254, v98
v_mfma_f32_16x16x32_bf16 v[128:131], v[68:71], v[132:135], v[128:131]
s_waitcnt lgkmcnt(2)
v_fmac_f32 v255, v98, v98
v_add_f32 v254, v254, v99
v_mfma_f32_16x16x32_bf16 v[112:115], a[48:51], v[136:139], v[112:115]
ds_read_b128 v[132:135], v25 offset:24576
v_fmac_f32 v255, v99, v99
v_mfma_f32_16x16x32_bf16 v[116:119], a[52:55], v[136:139], v[116:119]
v_add_f32 v254, v254, v100
v_fmac_f32 v255, v100, v100
v_mfma_f32_16x16x32_bf16 v[120:123], a[56:59], v[136:139], v[120:123]
v_add_f32 v254, v254, v101
v_fmac_f32 v255, v101, v101
v_mfma_f32_16x16x32_bf16 v[124:127], a[60:63], v[136:139], v[124:127]
global_load_lds_dwordx4 v16, s[42:43] offset:0 sc1
v_add_f32 v254, v254, v102
v_mfma_f32_16x16x32_bf16 v[128:131], v[72:75], v[136:139], v[128:131]
s_waitcnt lgkmcnt(2)
v_fmac_f32 v255, v102, v102
v_add_f32 v254, v254, v103
v_mfma_f32_16x16x32_bf16 v[112:115], a[64:67], v[140:143], v[112:115]
ds_read_b128 v[136:139], v25 offset:25600
v_fmac_f32 v255, v103, v103
v_mfma_f32_16x16x32_bf16 v[116:119], a[68:71], v[140:143], v[116:119]
v_add_f32 v254, v254, v104
v_fmac_f32 v255, v104, v104
v_mfma_f32_16x16x32_bf16 v[120:123], a[72:75], v[140:143], v[120:123]
v_add_f32 v254, v254, v105
v_fmac_f32 v255, v105, v105
v_mfma_f32_16x16x32_bf16 v[124:127], a[76:79], v[140:143], v[124:127]
v_add_f32 v254, v254, v106
v_fmac_f32 v255, v106, v106
v_mfma_f32_16x16x32_bf16 v[128:131], v[76:79], v[140:143], v[128:131]
s_waitcnt vmcnt(9)
ds_read_b128 v[148:151], v19 offset:0
ds_read_b128 v[152:155], v20 offset:0
s_waitcnt lgkmcnt(4)
v_mfma_f32_16x16x32_bf16 v[112:115], a[80:83], v[144:147], v[112:115]
ds_read_b128 v[140:143], v25 offset:26624
global_load_lds_dwordx4 v17, s[42:43] offset:1024 sc1
v_mfma_f32_16x16x32_bf16 v[116:119], a[84:87], v[144:147], v[116:119]
v_add_f32 v254, v254, v107
v_fmac_f32 v255, v107, v107
v_mfma_f32_16x16x32_bf16 v[120:123], a[88:91], v[144:147], v[120:123]
v_add_f32 v254, v254, v108
v_fmac_f32 v255, v108, v108
v_mfma_f32_16x16x32_bf16 v[124:127], a[92:95], v[144:147], v[124:127]
v_add_f32 v254, v254, v109
v_fmac_f32 v255, v109, v109
v_mfma_f32_16x16x32_bf16 v[128:131], v[80:83], v[144:147], v[128:131]
s_waitcnt lgkmcnt(4)
v_add_f32 v254, v254, v110
v_fmac_f32 v255, v110, v110
v_mfma_f32_16x16x32_bf16 v[112:115], a[96:99], v[132:135], v[112:115]
s_add_u32 m0, s49, 18432
ds_read_b128 v[144:147], v25 offset:27648
v_add_f32 v254, v254, v111
v_mfma_f32_16x16x32_bf16 v[116:119], a[100:103], v[132:135], v[116:119]
v_fmac_f32 v255, v111, v111
v_mfma_f32_16x16x32_bf16 v[120:123], a[104:107], v[132:135], v[120:123]
ds_write_b64 v27, v[254:255] offset:43008
v_mfma_f32_16x16x32_bf16 v[124:127], a[108:111], v[132:135], v[124:127]
s_sub_u32 s42, s42, 18432
s_waitcnt lgkmcnt(4)
v_lshlrev_b32 v96, 16, v148
v_and_b32 v97, 0xffff0000, v148
v_mfma_f32_16x16x32_bf16 v[128:131], v[84:87], v[132:135], v[128:131]
v_lshlrev_b32 v98, 16, v149
v_and_b32 v99, 0xffff0000, v149
v_mfma_f32_16x16x32_bf16 v[112:115], a[112:115], v[136:139], v[112:115]
s_subb_u32 s43, s43, 0
ds_read_b128 v[132:135], v25 offset:28672
v_lshlrev_b32 v100, 16, v150
v_mfma_f32_16x16x32_bf16 v[116:119], a[116:119], v[136:139], v[116:119]
v_and_b32 v101, 0xffff0000, v150
v_lshlrev_b32 v102, 16, v151
v_mfma_f32_16x16x32_bf16 v[120:123], a[120:123], v[136:139], v[120:123]
s_waitcnt lgkmcnt(4)
v_and_b32 v103, 0xffff0000, v151
v_lshlrev_b32 v104, 16, v152
v_mfma_f32_16x16x32_bf16 v[124:127], a[124:127], v[136:139], v[124:127]
v_and_b32 v105, 0xffff0000, v152
v_lshlrev_b32 v106, 16, v153
v_mfma_f32_16x16x32_bf16 v[128:131], v[88:91], v[136:139], v[128:131]
s_waitcnt lgkmcnt(3)
v_and_b32 v107, 0xffff0000, v153
v_lshlrev_b32 v108, 16, v154
v_mfma_f32_16x16x32_bf16 v[112:115], a[128:131], v[140:143], v[112:115]
ds_read_b128 v[136:139], v25 offset:29696
v_and_b32 v109, 0xffff0000, v154
v_mfma_f32_16x16x32_bf16 v[116:119], a[132:135], v[140:143], v[116:119]
v_lshlrev_b32 v110, 16, v155
v_and_b32 v111, 0xffff0000, v155
v_mfma_f32_16x16x32_bf16 v[120:123], a[136:139], v[140:143], v[120:123]
v_mfma_f32_16x16x32_bf16 v[124:127], a[140:143], v[140:143], v[124:127]
v_mfma_f32_16x16x32_bf16 v[128:131], v[92:95], v[140:143], v[128:131]
s_waitcnt lgkmcnt(3)
v_mfma_f32_16x16x32_bf16 v[112:115], a[144:147], v[144:147], v[112:115]
ds_read_b128 v[140:143], v25 offset:30720
v_mfma_f32_16x16x32_bf16 v[116:119], a[148:151], v[144:147], v[116:119]
v_mfma_f32_16x16x32_bf16 v[120:123], a[152:155], v[144:147], v[120:123]
v_mfma_f32_16x16x32_bf16 v[124:127], a[156:159], v[144:147], v[124:127]
s_waitcnt lgkmcnt(2)
v_mfma_f32_16x16x32_bf16 v[112:115], a[160:163], v[132:135], v[112:115]
ds_read_b128 v[144:147], v25 offset:31744
v_mfma_f32_16x16x32_bf16 v[116:119], a[164:167], v[132:135], v[116:119]
v_mfma_f32_16x16x32_bf16 v[120:123], a[168:171], v[132:135], v[120:123]
ds_write_b128 v26, v[128:131] offset:39936
v_mfma_f32_16x16x32_bf16 v[124:127], a[172:175], v[132:135], v[124:127]
s_waitcnt lgkmcnt(3)
v_mfma_f32_16x16x32_bf16 v[112:115], a[176:179], v[136:139], v[112:115]
ds_read_b128 v[132:135], v25 offset:32768
v_mfma_f32_16x16x32_bf16 v[116:119], a[180:183], v[136:139], v[116:119]
v_mfma_f32_16x16x32_bf16 v[120:123], a[184:187], v[136:139], v[120:123]
s_barrier
v_mfma_f32_16x16x32_bf16 v[124:127], a[188:191], v[136:139], v[124:127]
s_waitcnt lgkmcnt(3)
global_load_lds_dwordx4 v11, s[38:39] offset:-2048 sc1
v_mfma_f32_16x16x32_bf16 v[112:115], a[192:195], v[140:143], v[112:115]
s_mov_b64 s[76:77], -1
ds_read_b128 v[136:139], v25 offset:33792
v_mfma_f32_16x16x32_bf16 v[116:119], a[196:199], v[140:143], v[116:119]
v_mfma_f32_16x16x32_bf16 v[120:123], a[200:203], v[140:143], v[120:123]
global_load_lds_dwordx4 v11, s[38:39] offset:-1024 sc1
v_mfma_f32_16x16x32_bf16 v[124:127], a[204:207], v[140:143], v[124:127]
s_waitcnt lgkmcnt(3)
v_mfma_f32_16x16x32_bf16 v[112:115], a[208:211], v[144:147], v[112:115]
ds_read_b128 v[140:143], v25 offset:34816
v_mfma_f32_16x16x32_bf16 v[116:119], a[212:215], v[144:147], v[116:119]
global_load_lds_dwordx4 v11, s[38:39] offset:0 sc1
v_mfma_f32_16x16x32_bf16 v[120:123], a[216:219], v[144:147], v[120:123]
v_mfma_f32_16x16x32_bf16 v[124:127], a[220:223], v[144:147], v[124:127]
s_waitcnt lgkmcnt(2)
v_mfma_f32_16x16x32_bf16 v[112:115], a[224:227], v[132:135], v[112:115]
ds_read_b128 v[144:147], v25 offset:35840
global_load_lds_dwordx4 v11, s[38:39] offset:1024 sc1
v_mfma_f32_16x16x32_bf16 v[116:119], a[228:231], v[132:135], v[116:119]
v_mfma_f32_16x16x32_bf16 v[120:123], a[232:235], v[132:135], v[120:123]
v_mfma_f32_16x16x32_bf16 v[124:127], a[236:239], v[132:135], v[124:127]
s_waitcnt lgkmcnt(2)
v_xor_b32 v25, 0x10000, v25
global_load_lds_dwordx4 v11, s[38:39] offset:2048 sc1
v_mfma_f32_16x16x32_bf16 v[112:115], a[240:243], v[136:139], v[112:115]
v_add_u32 v11, 18432, v11
v_min_u32 v11, v11, v12
v_mfma_f32_16x16x32_bf16 v[116:119], a[244:247], v[136:139], v[116:119]
v_mfma_f32_16x16x32_bf16 v[120:123], a[248:251], v[136:139], v[120:123]
s_waitcnt vmcnt(10)
s_barrier
v_mfma_f32_16x16x32_bf16 v[124:127], a[252:255], v[136:139], v[124:127]
s_waitcnt lgkmcnt(1)
ds_read_b128 v[132:135], v25 offset:0
v_mfma_f32_16x16x32_bf16 v[112:115], v[28:31], v[140:143], v[112:115]
ds_read_b128 v[136:139], v25 offset:1024
v_mfma_f32_16x16x32_bf16 v[116:119], v[32:35], v[140:143], v[116:119]
v_mfma_f32_16x16x32_bf16 v[120:123], v[36:39], v[140:143], v[120:123]
v_mfma_f32_16x16x32_bf16 v[124:127], v[40:43], v[140:143], v[124:127]
s_waitcnt lgkmcnt(2)
v_mfma_f32_16x16x32_bf16 v[112:115], v[44:47], v[144:147], v[112:115]
ds_read_b128 v[140:143], v25 offset:2048
v_mfma_f32_16x16x32_bf16 v[116:119], v[48:51], v[144:147], v[116:119]
v_mfma_f32_16x16x32_bf16 v[120:123], v[52:55], v[144:147], v[120:123]
v_mfma_f32_16x16x32_bf16 v[124:127], v[56:59], v[144:147], v[124:127]
s_sub_u32 s70, s70, 1
s_cbranch_scc0 .Lwo_top5
s_waitcnt vmcnt(7)
s_barrier
s_mov_b64 exec, 1
global_store_dword v252, v24, s[38:39] sc1
s_mov_b64 exec, -1
v_add_u32 v24, 1, v24
v_add_lshl_u32 v252, v24, 0, 7
v_bfi_b32 v252, s69, v252, v23
v_cvt_pk_bf16_f32 v164, v112, v113
v_cvt_pk_bf16_f32 v165, v114, v115
v_cvt_pk_bf16_f32 v166, v116, v117
v_cvt_pk_bf16_f32 v167, v118, v119
global_store_dwordx4 v13, v[164:167], s[38:39] offset:0 sc1
v_cvt_pk_bf16_f32 v168, v120, v121
v_cvt_pk_bf16_f32 v169, v122, v123
v_cvt_pk_bf16_f32 v170, v124, v125
v_cvt_pk_bf16_f32 v171, v126, v127
global_store_dwordx4 v13, v[168:171], s[38:39] offset:1024 sc1
v_add_f32 v254, v112, v113
v_mul_f32 v255, v112, v112
v_fmac_f32 v255, v113, v113
v_add_f32 v254, v254, v114
v_fmac_f32 v255, v114, v114
v_add_f32 v254, v254, v115
v_fmac_f32 v255, v115, v115
v_add_f32 v254, v254, v116
v_fmac_f32 v255, v116, v116
v_add_f32 v254, v254, v117
v_fmac_f32 v255, v117, v117
v_add_f32 v254, v254, v118
v_fmac_f32 v255, v118, v118
v_add_f32 v254, v254, v119
v_fmac_f32 v255, v119, v119
v_add_f32 v254, v254, v120
v_fmac_f32 v255, v120, v120
v_add_f32 v254, v254, v121
v_fmac_f32 v255, v121, v121
v_add_f32 v254, v254, v122
v_fmac_f32 v255, v122, v122
v_add_f32 v254, v254, v123
v_fmac_f32 v255, v123, v123
v_add_f32 v254, v254, v124
)"
R"(v_fmac_f32 v255, v124, v124
v_add_f32 v254, v254, v125
v_fmac_f32 v255, v125, v125
v_add_f32 v254, v254, v126
v_fmac_f32 v255, v126, v126
v_add_f32 v254, v254, v127
v_fmac_f32 v255, v127, v127
ds_write_b64 v27, v[254:255] offset:45056
s_waitcnt vmcnt(0) lgkmcnt(0)
s_barrier
s_mov_b64 exec, 1
global_store_dword v252, v24, s[38:39] sc1
s_mov_b64 exec, -1
s_waitcnt vmcnt(0)
s_barrier
v_add_u32 v24, 8, v24
v_mbcnt_lo_u32_b32 v252, -1, 0
v_mbcnt_hi_u32_b32 v252, -1, v252
v_add_lshl_u32 v252, v252, 0, 7
v_bfi_b32 v252, s69, v252, v23
s_mov_b64 exec, 0xff
global_store_dword v252, v24, s[38:39] sc1
s_mov_b64 exec, -1
s_endpgm
.Lwo_w2:
s_add_u32 m0, s48, 4096
s_nop 0
global_load_lds_dwordx4 v0, s[40:41] offset:-4096
global_load_lds_dwordx4 v1, s[40:41] offset:-3072
global_load_lds_dwordx4 v2, s[40:41] offset:-2048
global_load_lds_dwordx4 v3, s[40:41] offset:-1024
global_load_lds_dwordx4 v4, s[40:41] offset:0
global_load_lds_dwordx4 v5, s[40:41] offset:1024
global_load_lds_dwordx4 v6, s[40:41] offset:2048
global_load_lds_dwordx4 v7, s[40:41] offset:3072
s_add_u32 m0, s48, 12288
s_nop 0
global_load_lds_dwordx4 v8, s[40:41] offset:-4096
s_add_u32 s40, s40, 73728
s_addc_u32 s41, s41, 0
s_add_u32 m0, s48, 40960
s_nop 0
global_load_lds_dwordx4 v0, s[40:41] offset:-4096
global_load_lds_dwordx4 v1, s[40:41] offset:-3072
global_load_lds_dwordx4 v2, s[40:41] offset:-2048
global_load_lds_dwordx4 v3, s[40:41] offset:-1024
global_load_lds_dwordx4 v4, s[40:41] offset:0
global_load_lds_dwordx4 v5, s[40:41] offset:1024
global_load_lds_dwordx4 v6, s[40:41] offset:2048
global_load_lds_dwordx4 v7, s[40:41] offset:3072
s_add_u32 m0, s48, 49152
s_nop 0
global_load_lds_dwordx4 v8, s[40:41] offset:-4096
s_add_u32 s40, s40, 73728
s_addc_u32 s41, s41, 0
s_add_u32 m0, s48, 77824
s_nop 0
global_load_lds_dwordx4 v0, s[40:41] offset:-4096
global_load_lds_dwordx4 v1, s[40:41] offset:-3072
global_load_lds_dwordx4 v2, s[40:41] offset:-2048
global_load_lds_dwordx4 v3, s[40:41] offset:-1024
global_load_lds_dwordx4 v4, s[40:41] offset:0
global_load_lds_dwordx4 v5, s[40:41] offset:1024
global_load_lds_dwordx4 v6, s[40:41] offset:2048
global_load_lds_dwordx4 v7, s[40:41] offset:3072
s_add_u32 m0, s48, 86016
s_nop 0
global_load_lds_dwordx4 v8, s[40:41] offset:-4096
s_add_u32 s40, s40, 73728
s_addc_u32 s41, s41, 0
v_add_u32 v204, 0x10000, v9
v_add_u32 v205, 0x20000, v9
v_add_u32 v206, 0x10000, v10
v_add_u32 v207, 0x20000, v10
s_waitcnt lgkmcnt(0)
s_mul_i32 s37, s37, 0x9E3779B1
s_add_u32 s37, s37, 0x7F4A7C15
v_add_u32 v22, s37, v22
v_add_u32 v24, s37, v24
s_waitcnt vmcnt(18)
s_barrier
s_add_u32 m0, s48, 114688
ds_read_b64_tr_b16 a[0:1], v9 offset:0
ds_read_b64_tr_b16 a[2:3], v9 offset:2304
global_load_lds_dwordx4 v0, s[40:41] offset:-4096
ds_read_b64_tr_b16 a[4:5], v9 offset:8
global_load_lds_dwordx4 v1, s[40:41] offset:-3072
ds_read_b64_tr_b16 a[6:7], v9 offset:2312
global_load_lds_dwordx4 v2, s[40:41] offset:-2048
ds_read_b64_tr_b16 a[8:9], v9 offset:64
global_load_lds_dwordx4 v3, s[40:41] offset:-1024
ds_read_b64_tr_b16 a[10:11], v9 offset:2368
ds_read_b64_tr_b16 a[12:13], v9 offset:72
global_load_lds_dwordx4 v4, s[40:41] offset:0
ds_read_b64_tr_b16 a[14:15], v9 offset:2376
global_load_lds_dwordx4 v5, s[40:41] offset:1024
ds_read_b64_tr_b16 a[16:17], v9 offset:18432
global_load_lds_dwordx4 v6, s[40:41] offset:2048
ds_read_b64_tr_b16 a[18:19], v9 offset:20736
global_load_lds_dwordx4 v7, s[40:41] offset:3072
ds_read_b64_tr_b16 a[20:21], v9 offset:18440
ds_read_b64_tr_b16 a[22:23], v9 offset:20744
s_add_u32 m0, s48, 122880
ds_read_b64_tr_b16 a[24:25], v9 offset:18496
global_load_lds_dwordx4 v8, s[40:41] offset:-4096
ds_read_b64_tr_b16 a[26:27], v9 offset:20800
s_add_u32 s40, s40, 73728
ds_read_b64_tr_b16 a[28:29], v9 offset:18504
s_addc_u32 s41, s41, 0
ds_read_b64_tr_b16 a[30:31], v9 offset:20808
s_waitcnt vmcnt(18) lgkmcnt(0)
s_barrier
s_add_u32 m0, s48, 4096
ds_read_b64_tr_b16 a[32:33], v9 offset:36864
ds_read_b64_tr_b16 a[34:35], v9 offset:39168
global_load_lds_dwordx4 v0, s[40:41] offset:-4096
ds_read_b64_tr_b16 a[36:37], v9 offset:36872
global_load_lds_dwordx4 v1, s[40:41] offset:-3072
ds_read_b64_tr_b16 a[38:39], v9 offset:39176
global_load_lds_dwordx4 v2, s[40:41] offset:-2048
ds_read_b64_tr_b16 a[40:41], v9 offset:36928
global_load_lds_dwordx4 v3, s[40:41] offset:-1024
ds_read_b64_tr_b16 a[42:43], v9 offset:39232
ds_read_b64_tr_b16 a[44:45], v9 offset:36936
global_load_lds_dwordx4 v4, s[40:41] offset:0
ds_read_b64_tr_b16 a[46:47], v9 offset:39240
global_load_lds_dwordx4 v5, s[40:41] offset:1024
ds_read_b64_tr_b16 a[48:49], v9 offset:55296
global_load_lds_dwordx4 v6, s[40:41] offset:2048
ds_read_b64_tr_b16 a[50:51], v9 offset:57600
global_load_lds_dwordx4 v7, s[40:41] offset:3072
ds_read_b64_tr_b16 a[52:53], v9 offset:55304
ds_read_b64_tr_b16 a[54:55], v9 offset:57608
s_add_u32 m0, s48, 12288
ds_read_b64_tr_b16 a[56:57], v9 offset:55360
global_load_lds_dwordx4 v8, s[40:41] offset:-4096
ds_read_b64_tr_b16 a[58:59], v9 offset:57664
s_add_u32 s40, s40, 73728
ds_read_b64_tr_b16 a[60:61], v9 offset:55368
s_addc_u32 s41, s41, 0
ds_read_b64_tr_b16 a[62:63], v9 offset:57672
s_waitcnt vmcnt(18) lgkmcnt(0)
s_barrier
s_add_u32 m0, s48, 40960
ds_read_b64_tr_b16 a[64:65], v204 offset:8192
ds_read_b64_tr_b16 a[66:67], v204 offset:10496
global_load_lds_dwordx4 v0, s[40:41] offset:-4096
ds_read_b64_tr_b16 a[68:69], v204 offset:8200
global_load_lds_dwordx4 v1, s[40:41] offset:-3072
ds_read_b64_tr_b16 a[70:71], v204 offset:10504
global_load_lds_dwordx4 v2, s[40:41] offset:-2048
ds_read_b64_tr_b16 a[72:73], v204 offset:8256
global_load_lds_dwordx4 v3, s[40:41] offset:-1024
ds_read_b64_tr_b16 a[74:75], v204 offset:10560
ds_read_b64_tr_b16 a[76:77], v204 offset:8264
global_load_lds_dwordx4 v4, s[40:41] offset:0
ds_read_b64_tr_b16 a[78:79], v204 offset:10568
global_load_lds_dwordx4 v5, s[40:41] offset:1024
ds_read_b64_tr_b16 a[80:81], v204 offset:26624
global_load_lds_dwordx4 v6, s[40:41] offset:2048
ds_read_b64_tr_b16 a[82:83], v204 offset:28928
global_load_lds_dwordx4 v7, s[40:41] offset:3072
ds_read_b64_tr_b16 a[84:85], v204 offset:26632
ds_read_b64_tr_b16 a[86:87], v204 offset:28936
s_add_u32 m0, s48, 49152
ds_read_b64_tr_b16 a[88:89], v204 offset:26688
global_load_lds_dwordx4 v8, s[40:41] offset:-4096
ds_read_b64_tr_b16 a[90:91], v204 offset:28992
s_add_u32 s40, s40, 73728
ds_read_b64_tr_b16 a[92:93], v204 offset:26696
s_addc_u32 s41, s41, 0
ds_read_b64_tr_b16 a[94:95], v204 offset:29000
s_waitcnt vmcnt(18) lgkmcnt(0)
s_barrier
s_add_u32 m0, s48, 77824
ds_read_b64_tr_b16 a[96:97], v204 offset:45056
ds_read_b64_tr_b16 a[98:99], v204 offset:47360
global_load_lds_dwordx4 v0, s[40:41] offset:-4096
ds_read_b64_tr_b16 a[100:101], v204 offset:45064
global_load_lds_dwordx4 v1, s[40:41] offset:-3072
ds_read_b64_tr_b16 a[102:103], v204 offset:47368
global_load_lds_dwordx4 v2, s[40:41] offset:-2048
ds_read_b64_tr_b16 a[104:105], v204 offset:45120
global_load_lds_dwordx4 v3, s[40:41] offset:-1024
ds_read_b64_tr_b16 a[106:107], v204 offset:47424
ds_read_b64_tr_b16 a[108:109], v204 offset:45128
global_load_lds_dwordx4 v4, s[40:41] offset:0
ds_read_b64_tr_b16 a[110:111], v204 offset:47432
global_load_lds_dwordx4 v5, s[40:41] offset:1024
ds_read_b64_tr_b16 a[112:113], v204 offset:63488
global_load_lds_dwordx4 v6, s[40:41] offset:2048
ds_read_b64_tr_b16 a[114:115], v205 offset:256
global_load_lds_dwordx4 v7, s[40:41] offset:3072
ds_read_b64_tr_b16 a[116:117], v204 offset:63496
ds_read_b64_tr_b16 a[118:119], v205 offset:264
s_add_u32 m0, s48, 86016
ds_read_b64_tr_b16 a[120:121], v204 offset:63552
global_load_lds_dwordx4 v8, s[40:41] offset:-4096
ds_read_b64_tr_b16 a[122:123], v205 offset:320
s_add_u32 s40, s40, 73728
ds_read_b64_tr_b16 a[124:125], v204 offset:63560
s_addc_u32 s41, s41, 0
ds_read_b64_tr_b16 a[126:127], v205 offset:328
s_waitcnt vmcnt(18) lgkmcnt(0)
s_barrier
s_add_u32 m0, s48, 114688
ds_read_b64_tr_b16 a[128:129], v9 offset:0
ds_read_b64_tr_b16 a[130:131], v9 offset:2304
global_load_lds_dwordx4 v0, s[40:41] offset:-4096
ds_read_b64_tr_b16 a[132:133], v9 offset:8
global_load_lds_dwordx4 v1, s[40:41] offset:-3072
ds_read_b64_tr_b16 a[134:135], v9 offset:2312
ds_read_b64_tr_b16 a[136:137], v9 offset:64
global_load_lds_dwordx4 v2, s[40:41] offset:-2048
ds_read_b64_tr_b16 a[138:139], v9 offset:2368
global_load_lds_dwordx4 v3, s[40:41] offset:-1024
ds_read_b64_tr_b16 a[140:141], v9 offset:72
global_load_lds_dwordx4 v4, s[40:41] offset:0
ds_read_b64_tr_b16 a[142:143], v9 offset:2376
ds_read_b64_tr_b16 a[144:145], v9 offset:18432
global_load_lds_dwordx4 v5, s[40:41] offset:1024
ds_read_b64_tr_b16 a[146:147], v9 offset:20736
global_load_lds_dwordx4 v6, s[40:41] offset:2048
ds_read_b64_tr_b16 a[148:149], v9 offset:18440
ds_read_b64_tr_b16 a[150:151], v9 offset:20744
global_load_lds_dwordx4 v7, s[40:41] offset:3072
ds_read_b64_tr_b16 a[152:153], v9 offset:18496
s_add_u32 m0, s48, 122880
ds_read_b64_tr_b16 a[154:155], v9 offset:20800
global_load_lds_dwordx4 v8, s[40:41] offset:-4096
ds_read_b64_tr_b16 a[156:157], v9 offset:18504
ds_read_b64_tr_b16 a[158:159], v9 offset:20808
s_add_u32 s40, s40, 73728
ds_read_b64_tr_b16 v[60:61], v10 offset:18432
s_addc_u32 s41, s41, 0
ds_read_b64_tr_b16 v[62:63], v10 offset:20736
s_waitcnt vmcnt(18) lgkmcnt(0)
s_barrier
s_add_u32 m0, s48, 4096
ds_read_b64_tr_b16 a[160:161], v9 offset:36864
ds_read_b64_tr_b16 a[162:163], v9 offset:39168
global_load_lds_dwordx4 v0, s[40:41] offset:-4096
ds_read_b64_tr_b16 a[164:165], v9 offset:36872
ds_read_b64_tr_b16 a[166:167], v9 offset:39176
global_load_lds_dwordx4 v1, s[40:41] offset:-3072
ds_read_b64_tr_b16 a[168:169], v9 offset:36928
global_load_lds_dwordx4 v2, s[40:41] offset:-2048
ds_read_b64_tr_b16 a[170:171], v9 offset:39232
ds_read_b64_tr_b16 a[172:173], v9 offset:36936
global_load_lds_dwordx4 v3, s[40:41] offset:-1024
ds_read_b64_tr_b16 a[174:175], v9 offset:39240
global_load_lds_dwordx4 v4, s[40:41] offset:0
ds_read_b64_tr_b16 v[64:65], v10 offset:36864
ds_read_b64_tr_b16 v[66:67], v10 offset:39168
global_load_lds_dwordx4 v5, s[40:41] offset:1024
ds_read_b64_tr_b16 a[176:177], v9 offset:55296
global_load_lds_dwordx4 v6, s[40:41] offset:2048
ds_read_b64_tr_b16 a[178:179], v9 offset:57600
ds_read_b64_tr_b16 a[180:181], v9 offset:55304
global_load_lds_dwordx4 v7, s[40:41] offset:3072
ds_read_b64_tr_b16 a[182:183], v9 offset:57608
s_add_u32 m0, s48, 12288
ds_read_b64_tr_b16 a[184:185], v9 offset:55360
ds_read_b64_tr_b16 a[186:187], v9 offset:57664
global_load_lds_dwordx4 v8, s[40:41] offset:-4096
ds_read_b64_tr_b16 a[188:189], v9 offset:55368
s_sub_u32 s40, s40, 589824
ds_read_b64_tr_b16 a[190:191], v9 offset:57672
ds_read_b64_tr_b16 v[68:69], v10 offset:55296
s_subb_u32 s41, s41, 0
ds_read_b64_tr_b16 v[70:71], v10 offset:57600
s_waitcnt vmcnt(18) lgkmcnt(0)
s_barrier
ds_read_b64_tr_b16 a[192:193], v204 offset:8192
ds_read_b64_tr_b16 a[194:195], v204 offset:10496
ds_read_b64_tr_b16 a[196:197], v204 offset:8200
ds_read_b64_tr_b16 a[198:199], v204 offset:10504
ds_read_b64_tr_b16 a[200:201], v204 offset:8256
ds_read_b64_tr_b16 a[202:203], v204 offset:10560
ds_read_b64_tr_b16 a[204:205], v204 offset:8264
ds_read_b64_tr_b16 a[206:207], v204 offset:10568
ds_read_b64_tr_b16 v[72:73], v206 offset:8192
ds_read_b64_tr_b16 v[74:75], v206 offset:10496
ds_read_b64_tr_b16 a[208:209], v204 offset:26624
ds_read_b64_tr_b16 a[210:211], v204 offset:28928
ds_read_b64_tr_b16 a[212:213], v204 offset:26632
ds_read_b64_tr_b16 a[214:215], v204 offset:28936
ds_read_b64_tr_b16 a[216:217], v204 offset:26688
ds_read_b64_tr_b16 a[218:219], v204 offset:28992
ds_read_b64_tr_b16 a[220:221], v204 offset:26696
ds_read_b64_tr_b16 a[222:223], v204 offset:29000
ds_read_b64_tr_b16 v[76:77], v206 offset:26624
ds_read_b64_tr_b16 v[78:79], v206 offset:28928
s_waitcnt vmcnt(9) lgkmcnt(0)
s_barrier
ds_read_b64_tr_b16 a[224:225], v204 offset:45056
ds_read_b64_tr_b16 a[226:227], v204 offset:47360
ds_read_b64_tr_b16 a[228:229], v204 offset:45064
ds_read_b64_tr_b16 a[230:231], v204 offset:47368
ds_read_b64_tr_b16 a[232:233], v204 offset:45120
ds_read_b64_tr_b16 a[234:235], v204 offset:47424
ds_read_b64_tr_b16 a[236:237], v204 offset:45128
ds_read_b64_tr_b16 a[238:239], v204 offset:47432
ds_read_b64_tr_b16 v[80:81], v206 offset:45056
ds_read_b64_tr_b16 v[82:83], v206 offset:47360
ds_read_b64_tr_b16 a[240:241], v204 offset:63488
ds_read_b64_tr_b16 a[242:243], v205 offset:256
ds_read_b64_tr_b16 a[244:245], v204 offset:63496
ds_read_b64_tr_b16 a[246:247], v205 offset:264
ds_read_b64_tr_b16 a[248:249], v204 offset:63552
ds_read_b64_tr_b16 a[250:251], v205 offset:320
ds_read_b64_tr_b16 a[252:253], v204 offset:63560
ds_read_b64_tr_b16 a[254:255], v205 offset:328
ds_read_b64_tr_b16 v[84:85], v206 offset:63488
ds_read_b64_tr_b16 v[86:87], v207 offset:256
s_waitcnt vmcnt(0) lgkmcnt(0)
s_barrier
ds_read_b64_tr_b16 v[28:29], v9 offset:0
ds_read_b64_tr_b16 v[30:31], v9 offset:2304
ds_read_b64_tr_b16 v[32:33], v9 offset:8
ds_read_b64_tr_b16 v[34:35], v9 offset:2312
ds_read_b64_tr_b16 v[36:37], v9 offset:64
ds_read_b64_tr_b16 v[38:39], v9 offset:2368
ds_read_b64_tr_b16 v[40:41], v9 offset:72
ds_read_b64_tr_b16 v[42:43], v9 offset:2376
ds_read_b64_tr_b16 v[88:89], v10 offset:0
ds_read_b64_tr_b16 v[90:91], v10 offset:2304
ds_read_b64_tr_b16 v[44:45], v9 offset:18432
ds_read_b64_tr_b16 v[46:47], v9 offset:20736
ds_read_b64_tr_b16 v[48:49], v9 offset:18440
ds_read_b64_tr_b16 v[50:51], v9 offset:20744
ds_read_b64_tr_b16 v[52:53], v9 offset:18496
ds_read_b64_tr_b16 v[54:55], v9 offset:20800
ds_read_b64_tr_b16 v[56:57], v9 offset:18504
ds_read_b64_tr_b16 v[58:59], v9 offset:20808
ds_read_b64_tr_b16 v[92:93], v10 offset:18432
ds_read_b64_tr_b16 v[94:95], v10 offset:20736
s_waitcnt lgkmcnt(0)
s_barrier
v_add_lshl_u32 v252, v24, 0, 7
v_bfi_b32 v252, s69, v252, v23
v_add_lshl_u32 v253, v22, 0, 7
v_bfi_b32 v253, s69, v253, v21
s_mov_b64 s[76:77], 0
s_add_u32 m0, s49, 18432
s_mov_b32 s70, 33
s_waitcnt vmcnt(0) lgkmcnt(0)
s_nop 7
.p2align 6
.Lwo_top11:
s_waitcnt lgkmcnt(2)
v_mfma_f32_16x16x32_bf16 v[96:99], a[144:147], v[132:135], v[96:99]
ds_read_b128 v[144:147], v25 offset:12288
v_mfma_f32_16x16x32_bf16 v[100:103], a[148:151], v[132:135], v[100:103]
v_mfma_f32_16x16x32_bf16 v[104:107], a[152:155], v[132:135], v[104:107]
s_waitcnt vmcnt(8)
s_barrier
v_mfma_f32_16x16x32_bf16 v[108:111], a[156:159], v[132:135], v[108:111]
v_cvt_pk_bf16_f32 v164, v112, v113
v_cvt_pk_bf16_f32 v165, v114, v115
v_mfma_f32_16x16x32_bf16 v[128:131], v[60:63], v[132:135], 0
s_waitcnt lgkmcnt(2)
v_cvt_pk_bf16_f32 v166, v116, v117
v_cvt_pk_bf16_f32 v167, v118, v119
v_mfma_f32_16x16x32_bf16 v[96:99], a[160:163], v[136:139], v[96:99]
ds_read_b128 v[132:135], v25 offset:13312
v_add_f32 v254, v112, v113
v_mfma_f32_16x16x32_bf16 v[100:103], a[164:167], v[136:139], v[100:103]
global_store_dwordx4 v13, v[164:167], s[38:39] offset:0 sc1
v_mfma_f32_16x16x32_bf16 v[104:107], a[168:171], v[136:139], v[104:107]
v_cvt_pk_bf16_f32 v168, v120, v121
v_cvt_pk_bf16_f32 v169, v122, v123
v_mfma_f32_16x16x32_bf16 v[108:111], a[172:175], v[136:139], v[108:111]
v_cvt_pk_bf16_f32 v170, v124, v125
v_cvt_pk_bf16_f32 v171, v126, v127
v_mfma_f32_16x16x32_bf16 v[128:131], v[64:67], v[136:139], v[128:131]
s_waitcnt lgkmcnt(2)
v_mul_f32 v255, v112, v112
v_fmac_f32 v255, v113, v113
v_mfma_f32_16x16x32_bf16 v[96:99], a[176:179], v[140:143], v[96:99]
s_cmp_eq_u32 s70, 32
ds_read_b128 v[136:139], v25 offset:14336
v_add_f32 v254, v254, v114
v_mfma_f32_16x16x32_bf16 v[100:103], a[180:183], v[140:143], v[100:103]
s_cselect_b64 s[42:43], s[44:45], s[42:43]
global_store_dwordx4 v13, v[168:171], s[38:39] offset:1024 sc1
v_mfma_f32_16x16x32_bf16 v[104:107], a[184:187], v[140:143], v[104:107]
s_cmp_eq_u32 s70, 16
v_add_u32 v13, 18688, v13
v_fmac_f32 v255, v114, v114
v_mfma_f32_16x16x32_bf16 v[108:111], a[188:191], v[140:143], v[108:111]
s_cselect_b64 s[42:43], s[46:47], s[42:43]
v_add_f32 v254, v254, v115
v_fmac_f32 v255, v115, v115
v_mfma_f32_16x16x32_bf16 v[128:131], v[68:71], v[140:143], v[128:131]
s_waitcnt lgkmcnt(2)
v_add_f32 v254, v254, v116
v_fmac_f32 v255, v116, v116
v_mfma_f32_16x16x32_bf16 v[96:99], a[192:195], v[144:147], v[96:99]
s_xor_b32 s49, s49, 0x10000
)"
R"(ds_read_b128 v[140:143], v25 offset:15360
v_add_f32 v254, v254, v117
v_mfma_f32_16x16x32_bf16 v[100:103], a[196:199], v[144:147], v[100:103]
s_mov_b32 m0, 49152
v_fmac_f32 v255, v117, v117
v_add_f32 v254, v254, v118
v_mfma_f32_16x16x32_bf16 v[104:107], a[200:203], v[144:147], v[104:107]
v_fmac_f32 v255, v118, v118
v_add_f32 v254, v254, v119
v_mfma_f32_16x16x32_bf16 v[108:111], a[204:207], v[144:147], v[108:111]
v_fmac_f32 v255, v119, v119
v_add_f32 v254, v254, v120
v_mfma_f32_16x16x32_bf16 v[128:131], v[72:75], v[144:147], v[128:131]
s_waitcnt lgkmcnt(2)
v_fmac_f32 v255, v120, v120
v_add_f32 v254, v254, v121
v_mfma_f32_16x16x32_bf16 v[96:99], a[208:211], v[132:135], v[96:99]
ds_read_b128 v[144:147], v25 offset:16384
global_load_lds_dwordx4 v16, s[42:43] offset:0 sc1
v_mfma_f32_16x16x32_bf16 v[100:103], a[212:215], v[132:135], v[100:103]
v_fmac_f32 v255, v121, v121
v_add_f32 v254, v254, v122
v_mfma_f32_16x16x32_bf16 v[104:107], a[216:219], v[132:135], v[104:107]
v_fmac_f32 v255, v122, v122
v_add_f32 v254, v254, v123
v_mfma_f32_16x16x32_bf16 v[108:111], a[220:223], v[132:135], v[108:111]
v_fmac_f32 v255, v123, v123
v_add_f32 v254, v254, v124
v_mfma_f32_16x16x32_bf16 v[128:131], v[76:79], v[132:135], v[128:131]
s_waitcnt vmcnt(9)
ds_read_b128 v[148:151], v19 offset:8192
ds_read_b128 v[152:155], v20 offset:8192
s_waitcnt lgkmcnt(4)
v_mfma_f32_16x16x32_bf16 v[96:99], a[224:227], v[136:139], v[96:99]
ds_read_b128 v[132:135], v25 offset:17408
v_fmac_f32 v255, v124, v124
v_mfma_f32_16x16x32_bf16 v[100:103], a[228:231], v[136:139], v[100:103]
v_add_f32 v254, v254, v125
v_fmac_f32 v255, v125, v125
v_mfma_f32_16x16x32_bf16 v[104:107], a[232:235], v[136:139], v[104:107]
global_load_lds_dwordx4 v17, s[42:43] offset:1024 sc1
v_add_f32 v254, v254, v126
v_mfma_f32_16x16x32_bf16 v[108:111], a[236:239], v[136:139], v[108:111]
v_fmac_f32 v255, v126, v126
v_add_f32 v254, v254, v127
v_mfma_f32_16x16x32_bf16 v[128:131], v[80:83], v[136:139], v[128:131]
s_waitcnt lgkmcnt(4)
v_fmac_f32 v255, v127, v127
v_mfma_f32_16x16x32_bf16 v[96:99], a[240:243], v[140:143], v[96:99]
ds_read_b128 v[136:139], v25 offset:0
v_mfma_f32_16x16x32_bf16 v[100:103], a[244:247], v[140:143], v[100:103]
ds_write_b64 v27, v[254:255] offset:45568
v_mfma_f32_16x16x32_bf16 v[104:107], a[248:251], v[140:143], v[104:107]
s_add_u32 m0, s49, 0
s_waitcnt lgkmcnt(4)
v_lshlrev_b32 v112, 16, v148
v_and_b32 v113, 0xffff0000, v148
v_mfma_f32_16x16x32_bf16 v[108:111], a[252:255], v[140:143], v[108:111]
v_lshlrev_b32 v114, 16, v149
v_and_b32 v115, 0xffff0000, v149
v_mfma_f32_16x16x32_bf16 v[128:131], v[84:87], v[140:143], v[128:131]
v_lshlrev_b32 v116, 16, v150
v_and_b32 v117, 0xffff0000, v150
v_mfma_f32_16x16x32_bf16 v[96:99], v[28:31], v[144:147], v[96:99]
s_sub_u32 s42, s42, 18432
ds_read_b128 v[140:143], v25 offset:1024
v_lshlrev_b32 v118, 16, v151
v_mfma_f32_16x16x32_bf16 v[100:103], v[32:35], v[144:147], v[100:103]
s_subb_u32 s43, s43, 0
v_and_b32 v119, 0xffff0000, v151
s_waitcnt lgkmcnt(4)
v_lshlrev_b32 v120, 16, v152
v_mfma_f32_16x16x32_bf16 v[104:107], v[36:39], v[144:147], v[104:107]
v_and_b32 v121, 0xffff0000, v152
v_lshlrev_b32 v122, 16, v153
v_mfma_f32_16x16x32_bf16 v[108:111], v[40:43], v[144:147], v[108:111]
v_and_b32 v123, 0xffff0000, v153
v_lshlrev_b32 v124, 16, v154
v_mfma_f32_16x16x32_bf16 v[128:131], v[88:91], v[144:147], v[128:131]
s_waitcnt lgkmcnt(3)
v_and_b32 v125, 0xffff0000, v154
v_lshlrev_b32 v126, 16, v155
v_mfma_f32_16x16x32_bf16 v[96:99], v[44:47], v[132:135], v[96:99]
ds_read_b128 v[144:147], v25 offset:2048
v_and_b32 v127, 0xffff0000, v155
v_mfma_f32_16x16x32_bf16 v[100:103], v[48:51], v[132:135], v[100:103]
v_mfma_f32_16x16x32_bf16 v[104:107], v[52:55], v[132:135], v[104:107]
v_mfma_f32_16x16x32_bf16 v[108:111], v[56:59], v[132:135], v[108:111]
v_mfma_f32_16x16x32_bf16 v[128:131], v[92:95], v[132:135], v[128:131]
s_waitcnt lgkmcnt(3)
v_mfma_f32_16x16x32_bf16 v[96:99], a[0:3], v[136:139], v[96:99]
ds_read_b128 v[132:135], v25 offset:3072
v_mfma_f32_16x16x32_bf16 v[100:103], a[4:7], v[136:139], v[100:103]
v_mfma_f32_16x16x32_bf16 v[104:107], a[8:11], v[136:139], v[104:107]
v_mfma_f32_16x16x32_bf16 v[108:111], a[12:15], v[136:139], v[108:111]
s_waitcnt lgkmcnt(2)
v_mfma_f32_16x16x32_bf16 v[96:99], a[16:19], v[140:143], v[96:99]
ds_read_b128 v[136:139], v25 offset:4096
v_mfma_f32_16x16x32_bf16 v[100:103], a[20:23], v[140:143], v[100:103]
s_waitcnt vmcnt(6)
v_sub_u32 v200, v200, v22
v_cmp_lt_u32 vcc, 0x7f, v200
v_mfma_f32_16x16x32_bf16 v[104:107], a[24:27], v[140:143], v[104:107]
v_mfma_f32_16x16x32_bf16 v[108:111], a[28:31], v[140:143], v[108:111]
s_waitcnt lgkmcnt(2)
v_mfma_f32_16x16x32_bf16 v[96:99], a[32:35], v[144:147], v[96:99]
s_and_b64 vcc, vcc, s[76:77]
s_cbranch_vccnz .Lwo_sp7
.Lwo_rt8:
ds_read_b128 v[140:143], v25 offset:5120
v_mfma_f32_16x16x32_bf16 v[100:103], a[36:39], v[144:147], v[100:103]
v_mfma_f32_16x16x32_bf16 v[104:107], a[40:43], v[144:147], v[104:107]
s_barrier
v_mfma_f32_16x16x32_bf16 v[108:111], a[44:47], v[144:147], v[108:111]
s_waitcnt lgkmcnt(2)
ds_write_b128 v26, v[128:131] offset:37888
v_mfma_f32_16x16x32_bf16 v[96:99], a[48:51], v[132:135], v[96:99]
ds_read_b128 v[144:147], v25 offset:6144
v_add_u32 v22, 1, v22
v_mfma_f32_16x16x32_bf16 v[100:103], a[52:55], v[132:135], v[100:103]
v_add_lshl_u32 v253, v22, 0, 7
v_bfi_b32 v253, s69, v253, v21
v_mfma_f32_16x16x32_bf16 v[104:107], a[56:59], v[132:135], v[104:107]
global_load_lds_dwordx4 v11, s[38:39] offset:-2048 sc1
v_mfma_f32_16x16x32_bf16 v[108:111], a[60:63], v[132:135], v[108:111]
s_waitcnt lgkmcnt(3)
v_mfma_f32_16x16x32_bf16 v[96:99], a[64:67], v[136:139], v[96:99]
ds_read_b128 v[132:135], v25 offset:7168
v_mfma_f32_16x16x32_bf16 v[100:103], a[68:71], v[136:139], v[100:103]
global_load_lds_dwordx4 v11, s[38:39] offset:-1024 sc1
v_mfma_f32_16x16x32_bf16 v[104:107], a[72:75], v[136:139], v[104:107]
v_mfma_f32_16x16x32_bf16 v[108:111], a[76:79], v[136:139], v[108:111]
s_waitcnt lgkmcnt(3)
v_mfma_f32_16x16x32_bf16 v[96:99], a[80:83], v[140:143], v[96:99]
ds_read_b128 v[136:139], v25 offset:8192
global_load_lds_dwordx4 v11, s[38:39] offset:0 sc1
v_mfma_f32_16x16x32_bf16 v[100:103], a[84:87], v[140:143], v[100:103]
v_mfma_f32_16x16x32_bf16 v[104:107], a[88:91], v[140:143], v[104:107]
v_mfma_f32_16x16x32_bf16 v[108:111], a[92:95], v[140:143], v[108:111]
s_waitcnt lgkmcnt(2)
global_load_dword v201, v253, s[38:39] sc1
global_load_lds_dwordx4 v11, s[38:39] offset:1024 sc1
v_mfma_f32_16x16x32_bf16 v[96:99], a[96:99], v[144:147], v[96:99]
v_mfma_f32_16x16x32_bf16 v[100:103], a[100:103], v[144:147], v[100:103]
v_mfma_f32_16x16x32_bf16 v[104:107], a[104:107], v[144:147], v[104:107]
s_waitcnt vmcnt(9)
s_barrier
v_mfma_f32_16x16x32_bf16 v[108:111], a[108:111], v[144:147], v[108:111]
s_waitcnt lgkmcnt(1)
ds_read_b128 v[140:143], v25 offset:27648
global_load_lds_dwordx4 v11, s[38:39] offset:2048 sc1
v_mfma_f32_16x16x32_bf16 v[96:99], a[112:115], v[132:135], v[96:99]
ds_read_b128 v[144:147], v25 offset:28672
v_add_u32 v11, 18432, v11
v_mfma_f32_16x16x32_bf16 v[100:103], a[116:119], v[132:135], v[100:103]
v_min_u32 v11, v11, v12
v_mfma_f32_16x16x32_bf16 v[104:107], a[120:123], v[132:135], v[104:107]
v_mfma_f32_16x16x32_bf16 v[108:111], a[124:127], v[132:135], v[108:111]
s_waitcnt lgkmcnt(2)
v_mfma_f32_16x16x32_bf16 v[96:99], a[128:131], v[136:139], v[96:99]
ds_read_b128 v[132:135], v25 offset:29696
v_mfma_f32_16x16x32_bf16 v[100:103], a[132:135], v[136:139], v[100:103]
v_mfma_f32_16x16x32_bf16 v[104:107], a[136:139], v[136:139], v[104:107]
v_mfma_f32_16x16x32_bf16 v[108:111], a[140:143], v[136:139], v[108:111]
s_waitcnt lgkmcnt(2)
v_mfma_f32_16x16x32_bf16 v[112:115], a[144:147], v[140:143], v[112:115]
ds_read_b128 v[136:139], v25 offset:30720
v_mfma_f32_16x16x32_bf16 v[116:119], a[148:151], v[140:143], v[116:119]
v_mfma_f32_16x16x32_bf16 v[120:123], a[152:155], v[140:143], v[120:123]
s_waitcnt vmcnt(8)
s_barrier
v_mfma_f32_16x16x32_bf16 v[124:127], a[156:159], v[140:143], v[124:127]
v_cvt_pk_bf16_f32 v164, v96, v97
v_cvt_pk_bf16_f32 v165, v98, v99
v_mfma_f32_16x16x32_bf16 v[128:131], v[60:63], v[140:143], 0
s_waitcnt lgkmcnt(2)
v_cvt_pk_bf16_f32 v166, v100, v101
v_cvt_pk_bf16_f32 v167, v102, v103
v_mfma_f32_16x16x32_bf16 v[112:115], a[160:163], v[144:147], v[112:115]
ds_read_b128 v[140:143], v25 offset:31744
v_add_f32 v254, v96, v97
v_mfma_f32_16x16x32_bf16 v[116:119], a[164:167], v[144:147], v[116:119]
global_store_dwordx4 v13, v[164:167], s[38:39] offset:0 sc1
v_mfma_f32_16x16x32_bf16 v[120:123], a[168:171], v[144:147], v[120:123]
v_cvt_pk_bf16_f32 v168, v104, v105
v_cvt_pk_bf16_f32 v169, v106, v107
v_mfma_f32_16x16x32_bf16 v[124:127], a[172:175], v[144:147], v[124:127]
v_cvt_pk_bf16_f32 v170, v108, v109
v_cvt_pk_bf16_f32 v171, v110, v111
v_mfma_f32_16x16x32_bf16 v[128:131], v[64:67], v[144:147], v[128:131]
s_waitcnt lgkmcnt(2)
v_mul_f32 v255, v96, v96
v_fmac_f32 v255, v97, v97
v_mfma_f32_16x16x32_bf16 v[112:115], a[176:179], v[132:135], v[112:115]
s_mov_b32 m0, 57344
ds_read_b128 v[144:147], v25 offset:32768
v_add_f32 v254, v254, v98
v_mfma_f32_16x16x32_bf16 v[116:119], a[180:183], v[132:135], v[116:119]
global_store_dwordx4 v13, v[168:171], s[38:39] offset:1024 sc1
v_mfma_f32_16x16x32_bf16 v[120:123], a[184:187], v[132:135], v[120:123]
v_add_u32 v13, 18688, v13
v_fmac_f32 v255, v98, v98
v_mfma_f32_16x16x32_bf16 v[124:127], a[188:191], v[132:135], v[124:127]
v_add_f32 v254, v254, v99
v_fmac_f32 v255, v99, v99
v_mfma_f32_16x16x32_bf16 v[128:131], v[68:71], v[132:135], v[128:131]
s_waitcnt lgkmcnt(2)
v_add_f32 v254, v254, v100
v_fmac_f32 v255, v100, v100
v_mfma_f32_16x16x32_bf16 v[112:115], a[192:195], v[136:139], v[112:115]
ds_read_b128 v[132:135], v25 offset:33792
v_add_f32 v254, v254, v101
v_mfma_f32_16x16x32_bf16 v[116:119], a[196:199], v[136:139], v[116:119]
v_fmac_f32 v255, v101, v101
v_add_f32 v254, v254, v102
v_mfma_f32_16x16x32_bf16 v[120:123], a[200:203], v[136:139], v[120:123]
v_fmac_f32 v255, v102, v102
v_add_f32 v254, v254, v103
v_mfma_f32_16x16x32_bf16 v[124:127], a[204:207], v[136:139], v[124:127]
v_fmac_f32 v255, v103, v103
v_add_f32 v254, v254, v104
v_mfma_f32_16x16x32_bf16 v[128:131], v[72:75], v[136:139], v[128:131]
s_waitcnt lgkmcnt(2)
v_fmac_f32 v255, v104, v104
v_add_f32 v254, v254, v105
v_mfma_f32_16x16x32_bf16 v[112:115], a[208:211], v[140:143], v[112:115]
ds_read_b128 v[136:139], v25 offset:34816
global_load_lds_dwordx4 v16, s[42:43] offset:0 sc1
v_mfma_f32_16x16x32_bf16 v[116:119], a[212:215], v[140:143], v[116:119]
v_fmac_f32 v255, v105, v105
v_add_f32 v254, v254, v106
v_mfma_f32_16x16x32_bf16 v[120:123], a[216:219], v[140:143], v[120:123]
v_fmac_f32 v255, v106, v106
v_add_f32 v254, v254, v107
v_mfma_f32_16x16x32_bf16 v[124:127], a[220:223], v[140:143], v[124:127]
v_fmac_f32 v255, v107, v107
v_add_f32 v254, v254, v108
v_mfma_f32_16x16x32_bf16 v[128:131], v[76:79], v[140:143], v[128:131]
s_waitcnt vmcnt(9)
ds_read_b128 v[148:151], v19 offset:0
ds_read_b128 v[152:155], v20 offset:0
s_waitcnt lgkmcnt(4)
v_mfma_f32_16x16x32_bf16 v[112:115], a[224:227], v[144:147], v[112:115]
ds_read_b128 v[140:143], v25 offset:35840
v_fmac_f32 v255, v108, v108
v_mfma_f32_16x16x32_bf16 v[116:119], a[228:231], v[144:147], v[116:119]
v_add_f32 v254, v254, v109
v_fmac_f32 v255, v109, v109
v_mfma_f32_16x16x32_bf16 v[120:123], a[232:235], v[144:147], v[120:123]
global_load_lds_dwordx4 v17, s[42:43] offset:1024 sc1
v_add_f32 v254, v254, v110
v_mfma_f32_16x16x32_bf16 v[124:127], a[236:239], v[144:147], v[124:127]
v_fmac_f32 v255, v110, v110
v_add_f32 v254, v254, v111
v_mfma_f32_16x16x32_bf16 v[128:131], v[80:83], v[144:147], v[128:131]
s_waitcnt lgkmcnt(4)
v_fmac_f32 v255, v111, v111
v_mfma_f32_16x16x32_bf16 v[112:115], a[240:243], v[132:135], v[112:115]
ds_read_b128 v[144:147], v25 offset:18432
v_mfma_f32_16x16x32_bf16 v[116:119], a[244:247], v[132:135], v[116:119]
ds_write_b64 v27, v[254:255] offset:43520
v_mfma_f32_16x16x32_bf16 v[120:123], a[248:251], v[132:135], v[120:123]
s_add_u32 m0, s49, 18432
s_waitcnt lgkmcnt(4)
v_lshlrev_b32 v96, 16, v148
v_and_b32 v97, 0xffff0000, v148
v_mfma_f32_16x16x32_bf16 v[124:127], a[252:255], v[132:135], v[124:127]
v_lshlrev_b32 v98, 16, v149
v_and_b32 v99, 0xffff0000, v149
v_mfma_f32_16x16x32_bf16 v[128:131], v[84:87], v[132:135], v[128:131]
v_lshlrev_b32 v100, 16, v150
v_and_b32 v101, 0xffff0000, v150
v_mfma_f32_16x16x32_bf16 v[112:115], v[28:31], v[136:139], v[112:115]
s_sub_u32 s42, s42, 18432
ds_read_b128 v[132:135], v25 offset:19456
v_lshlrev_b32 v102, 16, v151
v_mfma_f32_16x16x32_bf16 v[116:119], v[32:35], v[136:139], v[116:119]
s_subb_u32 s43, s43, 0
v_and_b32 v103, 0xffff0000, v151
s_waitcnt lgkmcnt(4)
v_lshlrev_b32 v104, 16, v152
v_mfma_f32_16x16x32_bf16 v[120:123], v[36:39], v[136:139], v[120:123]
v_and_b32 v105, 0xffff0000, v152
v_lshlrev_b32 v106, 16, v153
v_mfma_f32_16x16x32_bf16 v[124:127], v[40:43], v[136:139], v[124:127]
v_and_b32 v107, 0xffff0000, v153
v_lshlrev_b32 v108, 16, v154
v_mfma_f32_16x16x32_bf16 v[128:131], v[88:91], v[136:139], v[128:131]
s_waitcnt lgkmcnt(3)
v_and_b32 v109, 0xffff0000, v154
v_lshlrev_b32 v110, 16, v155
v_mfma_f32_16x16x32_bf16 v[112:115], v[44:47], v[140:143], v[112:115]
ds_read_b128 v[136:139], v25 offset:20480
v_and_b32 v111, 0xffff0000, v155
v_mfma_f32_16x16x32_bf16 v[116:119], v[48:51], v[140:143], v[116:119]
v_mfma_f32_16x16x32_bf16 v[120:123], v[52:55], v[140:143], v[120:123]
v_mfma_f32_16x16x32_bf16 v[124:127], v[56:59], v[140:143], v[124:127]
v_mfma_f32_16x16x32_bf16 v[128:131], v[92:95], v[140:143], v[128:131]
s_waitcnt lgkmcnt(3)
v_mfma_f32_16x16x32_bf16 v[112:115], a[0:3], v[144:147], v[112:115]
ds_read_b128 v[140:143], v25 offset:21504
v_mfma_f32_16x16x32_bf16 v[116:119], a[4:7], v[144:147], v[116:119]
v_mfma_f32_16x16x32_bf16 v[120:123], a[8:11], v[144:147], v[120:123]
v_mfma_f32_16x16x32_bf16 v[124:127], a[12:15], v[144:147], v[124:127]
s_waitcnt lgkmcnt(2)
v_mfma_f32_16x16x32_bf16 v[112:115], a[16:19], v[132:135], v[112:115]
ds_read_b128 v[144:147], v25 offset:22528
v_mfma_f32_16x16x32_bf16 v[116:119], a[20:23], v[132:135], v[116:119]
s_waitcnt vmcnt(6)
v_sub_u32 v201, v201, v22
v_cmp_lt_u32 vcc, 0x7f, v201
v_mfma_f32_16x16x32_bf16 v[120:123], a[24:27], v[132:135], v[120:123]
v_mfma_f32_16x16x32_bf16 v[124:127], a[28:31], v[132:135], v[124:127]
s_waitcnt lgkmcnt(2)
v_mfma_f32_16x16x32_bf16 v[112:115], a[32:35], v[136:139], v[112:115]
s_and_b64 vcc, vcc, s[76:77]
s_cbranch_vccnz .Lwo_sp9
.Lwo_rt10:
ds_read_b128 v[132:135], v25 offset:23552
v_mfma_f32_16x16x32_bf16 v[116:119], a[36:39], v[136:139], v[116:119]
v_mfma_f32_16x16x32_bf16 v[120:123], a[40:43], v[136:139], v[120:123]
s_barrier
v_mfma_f32_16x16x32_bf16 v[124:127], a[44:47], v[136:139], v[124:127]
s_waitcnt lgkmcnt(2)
ds_write_b128 v26, v[128:131] offset:40960
v_mfma_f32_16x16x32_bf16 v[112:115], a[48:51], v[140:143], v[112:115]
s_mov_b64 s[76:77], -1
ds_read_b128 v[136:139], v25 offset:24576
v_add_u32 v22, 1, v22
v_mfma_f32_16x16x32_bf16 v[116:119], a[52:55], v[140:143], v[116:119]
v_add_lshl_u32 v253, v22, 0, 7
v_bfi_b32 v253, s69, v253, v21
v_mfma_f32_16x16x32_bf16 v[120:123], a[56:59], v[140:143], v[120:123]
global_load_lds_dwordx4 v11, s[38:39] offset:-2048 sc1
v_mfma_f32_16x16x32_bf16 v[124:127], a[60:63], v[140:143], v[124:127]
s_waitcnt lgkmcnt(3)
v_mfma_f32_16x16x32_bf16 v[112:115], a[64:67], v[144:147], v[112:115]
ds_read_b128 v[140:143], v25 offset:25600
v_mfma_f32_16x16x32_bf16 v[116:119], a[68:71], v[144:147], v[116:119]
global_load_lds_dwordx4 v11, s[38:39] offset:-1024 sc1
v_mfma_f32_16x16x32_bf16 v[120:123], a[72:75], v[144:147], v[120:123]
v_mfma_f32_16x16x32_bf16 v[124:127], a[76:79], v[144:147], v[124:127]
s_waitcnt lgkmcnt(3)
v_mfma_f32_16x16x32_bf16 v[112:115], a[80:83], v[132:135], v[112:115]
ds_read_b128 v[144:147], v25 offset:26624
global_load_lds_dwordx4 v11, s[38:39] offset:0 sc1
v_mfma_f32_16x16x32_bf16 v[116:119], a[84:87], v[132:135], v[116:119]
v_mfma_f32_16x16x32_bf16 v[120:123], a[88:91], v[132:135], v[120:123]
v_mfma_f32_16x16x32_bf16 v[124:127], a[92:95], v[132:135], v[124:127]
s_waitcnt lgkmcnt(2)
v_xor_b32 v25, 0x10000, v25
global_load_dword v200, v253, s[38:39] sc1
v_mfma_f32_16x16x32_bf16 v[112:115], a[96:99], v[136:139], v[112:115]
global_load_lds_dwordx4 v11, s[38:39] offset:1024 sc1
v_mfma_f32_16x16x32_bf16 v[116:119], a[100:103], v[136:139], v[116:119]
v_mfma_f32_16x16x32_bf16 v[120:123], a[104:107], v[136:139], v[120:123]
s_waitcnt vmcnt(9)
s_barrier
v_mfma_f32_16x16x32_bf16 v[124:127], a[108:111], v[136:139], v[124:127]
s_waitcnt lgkmcnt(1)
ds_read_b128 v[132:135], v25 offset:9216
global_load_lds_dwordx4 v11, s[38:39] offset:2048 sc1
v_mfma_f32_16x16x32_bf16 v[112:115], a[112:115], v[140:143], v[112:115]
ds_read_b128 v[136:139], v25 offset:10240
v_add_u32 v11, 18432, v11
v_mfma_f32_16x16x32_bf16 v[116:119], a[116:119], v[140:143], v[116:119]
v_min_u32 v11, v11, v12
v_mfma_f32_16x16x32_bf16 v[120:123], a[120:123], v[140:143], v[120:123]
v_mfma_f32_16x16x32_bf16 v[124:127], a[124:127], v[140:143], v[124:127]
s_waitcnt lgkmcnt(2)
v_mfma_f32_16x16x32_bf16 v[112:115], a[128:131], v[144:147], v[112:115]
ds_read_b128 v[140:143], v25 offset:11264
v_mfma_f32_16x16x32_bf16 v[116:119], a[132:135], v[144:147], v[116:119]
v_mfma_f32_16x16x32_bf16 v[120:123], a[136:139], v[144:147], v[120:123]
v_mfma_f32_16x16x32_bf16 v[124:127], a[140:143], v[144:147], v[124:127]
s_sub_u32 s70, s70, 1
s_cbranch_scc0 .Lwo_top11
s_waitcnt vmcnt(8)
s_barrier
v_cvt_pk_bf16_f32 v164, v112, v113
v_cvt_pk_bf16_f32 v165, v114, v115
v_cvt_pk_bf16_f32 v166, v116, v117
v_cvt_pk_bf16_f32 v167, v118, v119
global_store_dwordx4 v13, v[164:167], s[38:39] offset:0 sc1
v_cvt_pk_bf16_f32 v168, v120, v121
v_cvt_pk_bf16_f32 v169, v122, v123
v_cvt_pk_bf16_f32 v170, v124, v125
v_cvt_pk_bf16_f32 v171, v126, v127
global_store_dwordx4 v13, v[168:171], s[38:39] offset:1024 sc1
v_add_f32 v254, v112, v113
v_mul_f32 v255, v112, v112
v_fmac_f32 v255, v113, v113
v_add_f32 v254, v254, v114
v_fmac_f32 v255, v114, v114
v_add_f32 v254, v254, v115
v_fmac_f32 v255, v115, v115
v_add_f32 v254, v254, v116
v_fmac_f32 v255, v116, v116
v_add_f32 v254, v254, v117
v_fmac_f32 v255, v117, v117
)"
R"(v_add_f32 v254, v254, v118
v_fmac_f32 v255, v118, v118
v_add_f32 v254, v254, v119
v_fmac_f32 v255, v119, v119
v_add_f32 v254, v254, v120
v_fmac_f32 v255, v120, v120
v_add_f32 v254, v254, v121
v_fmac_f32 v255, v121, v121
v_add_f32 v254, v254, v122
v_fmac_f32 v255, v122, v122
v_add_f32 v254, v254, v123
v_fmac_f32 v255, v123, v123
v_add_f32 v254, v254, v124
v_fmac_f32 v255, v124, v124
v_add_f32 v254, v254, v125
v_fmac_f32 v255, v125, v125
v_add_f32 v254, v254, v126
v_fmac_f32 v255, v126, v126
v_add_f32 v254, v254, v127
v_fmac_f32 v255, v127, v127
ds_write_b64 v27, v[254:255] offset:45568
s_waitcnt vmcnt(0) lgkmcnt(0)
s_barrier
s_barrier
s_endpgm
.Lwo_w3:
s_add_u32 m0, s48, 4096
s_nop 0
global_load_lds_dwordx4 v0, s[40:41] offset:-4096
global_load_lds_dwordx4 v1, s[40:41] offset:-3072
global_load_lds_dwordx4 v2, s[40:41] offset:-2048
global_load_lds_dwordx4 v3, s[40:41] offset:-1024
global_load_lds_dwordx4 v4, s[40:41] offset:0
global_load_lds_dwordx4 v5, s[40:41] offset:1024
global_load_lds_dwordx4 v6, s[40:41] offset:2048
global_load_lds_dwordx4 v7, s[40:41] offset:3072
s_add_u32 m0, s48, 12288
s_nop 0
global_load_lds_dwordx4 v8, s[40:41] offset:-4096
s_add_u32 s40, s40, 73728
s_addc_u32 s41, s41, 0
s_add_u32 m0, s48, 40960
s_nop 0
global_load_lds_dwordx4 v0, s[40:41] offset:-4096
global_load_lds_dwordx4 v1, s[40:41] offset:-3072
global_load_lds_dwordx4 v2, s[40:41] offset:-2048
global_load_lds_dwordx4 v3, s[40:41] offset:-1024
global_load_lds_dwordx4 v4, s[40:41] offset:0
global_load_lds_dwordx4 v5, s[40:41] offset:1024
global_load_lds_dwordx4 v6, s[40:41] offset:2048
global_load_lds_dwordx4 v7, s[40:41] offset:3072
s_add_u32 m0, s48, 49152
s_nop 0
global_load_lds_dwordx4 v8, s[40:41] offset:-4096
s_add_u32 s40, s40, 73728
s_addc_u32 s41, s41, 0
s_add_u32 m0, s48, 77824
s_nop 0
global_load_lds_dwordx4 v0, s[40:41] offset:-4096
global_load_lds_dwordx4 v1, s[40:41] offset:-3072
global_load_lds_dwordx4 v2, s[40:41] offset:-2048
global_load_lds_dwordx4 v3, s[40:41] offset:-1024
global_load_lds_dwordx4 v4, s[40:41] offset:0
global_load_lds_dwordx4 v5, s[40:41] offset:1024
global_load_lds_dwordx4 v6, s[40:41] offset:2048
global_load_lds_dwordx4 v7, s[40:41] offset:3072
s_add_u32 m0, s48, 86016
s_nop 0
global_load_lds_dwordx4 v8, s[40:41] offset:-4096
s_add_u32 s40, s40, 73728
s_addc_u32 s41, s41, 0
v_add_u32 v204, 0x10000, v9
v_add_u32 v205, 0x20000, v9
v_add_u32 v206, 0x10000, v10
v_add_u32 v207, 0x20000, v10
s_waitcnt lgkmcnt(0)
s_mul_i32 s37, s37, 0x9E3779B1
s_add_u32 s37, s37, 0x7F4A7C15
v_add_u32 v22, s37, v22
v_add_u32 v24, s37, v24
s_waitcnt vmcnt(18)
s_barrier
s_add_u32 m0, s48, 114688
ds_read_b64_tr_b16 a[0:1], v9 offset:0
ds_read_b64_tr_b16 a[2:3], v9 offset:2304
global_load_lds_dwordx4 v0, s[40:41] offset:-4096
ds_read_b64_tr_b16 a[4:5], v9 offset:8
ds_read_b64_tr_b16 a[6:7], v9 offset:2312
global_load_lds_dwordx4 v1, s[40:41] offset:-3072
ds_read_b64_tr_b16 a[8:9], v9 offset:64
global_load_lds_dwordx4 v2, s[40:41] offset:-2048
ds_read_b64_tr_b16 a[10:11], v9 offset:2368
ds_read_b64_tr_b16 a[12:13], v9 offset:72
global_load_lds_dwordx4 v3, s[40:41] offset:-1024
ds_read_b64_tr_b16 a[14:15], v9 offset:2376
global_load_lds_dwordx4 v4, s[40:41] offset:0
ds_read_b64_tr_b16 v[60:61], v10 offset:0
ds_read_b64_tr_b16 v[62:63], v10 offset:2304
global_load_lds_dwordx4 v5, s[40:41] offset:1024
ds_read_b64_tr_b16 a[16:17], v9 offset:18432
global_load_lds_dwordx4 v6, s[40:41] offset:2048
ds_read_b64_tr_b16 a[18:19], v9 offset:20736
ds_read_b64_tr_b16 a[20:21], v9 offset:18440
global_load_lds_dwordx4 v7, s[40:41] offset:3072
ds_read_b64_tr_b16 a[22:23], v9 offset:20744
s_add_u32 m0, s48, 122880
ds_read_b64_tr_b16 a[24:25], v9 offset:18496
ds_read_b64_tr_b16 a[26:27], v9 offset:20800
global_load_lds_dwordx4 v8, s[40:41] offset:-4096
ds_read_b64_tr_b16 a[28:29], v9 offset:18504
s_add_u32 s40, s40, 73728
ds_read_b64_tr_b16 a[30:31], v9 offset:20808
ds_read_b64_tr_b16 v[64:65], v10 offset:18432
s_addc_u32 s41, s41, 0
ds_read_b64_tr_b16 v[66:67], v10 offset:20736
s_waitcnt vmcnt(18) lgkmcnt(0)
s_barrier
s_add_u32 m0, s48, 4096
ds_read_b64_tr_b16 a[32:33], v9 offset:36864
ds_read_b64_tr_b16 a[34:35], v9 offset:39168
global_load_lds_dwordx4 v0, s[40:41] offset:-4096
ds_read_b64_tr_b16 a[36:37], v9 offset:36872
ds_read_b64_tr_b16 a[38:39], v9 offset:39176
global_load_lds_dwordx4 v1, s[40:41] offset:-3072
ds_read_b64_tr_b16 a[40:41], v9 offset:36928
global_load_lds_dwordx4 v2, s[40:41] offset:-2048
ds_read_b64_tr_b16 a[42:43], v9 offset:39232
ds_read_b64_tr_b16 a[44:45], v9 offset:36936
global_load_lds_dwordx4 v3, s[40:41] offset:-1024
ds_read_b64_tr_b16 a[46:47], v9 offset:39240
global_load_lds_dwordx4 v4, s[40:41] offset:0
ds_read_b64_tr_b16 v[68:69], v10 offset:36864
ds_read_b64_tr_b16 v[70:71], v10 offset:39168
global_load_lds_dwordx4 v5, s[40:41] offset:1024
ds_read_b64_tr_b16 a[48:49], v9 offset:55296
global_load_lds_dwordx4 v6, s[40:41] offset:2048
ds_read_b64_tr_b16 a[50:51], v9 offset:57600
ds_read_b64_tr_b16 a[52:53], v9 offset:55304
global_load_lds_dwordx4 v7, s[40:41] offset:3072
ds_read_b64_tr_b16 a[54:55], v9 offset:57608
s_add_u32 m0, s48, 12288
ds_read_b64_tr_b16 a[56:57], v9 offset:55360
ds_read_b64_tr_b16 a[58:59], v9 offset:57664
global_load_lds_dwordx4 v8, s[40:41] offset:-4096
ds_read_b64_tr_b16 a[60:61], v9 offset:55368
s_add_u32 s40, s40, 73728
ds_read_b64_tr_b16 a[62:63], v9 offset:57672
ds_read_b64_tr_b16 v[72:73], v10 offset:55296
s_addc_u32 s41, s41, 0
ds_read_b64_tr_b16 v[74:75], v10 offset:57600
s_waitcnt vmcnt(18) lgkmcnt(0)
s_barrier
s_add_u32 m0, s48, 40960
ds_read_b64_tr_b16 a[64:65], v204 offset:8192
ds_read_b64_tr_b16 a[66:67], v204 offset:10496
global_load_lds_dwordx4 v0, s[40:41] offset:-4096
ds_read_b64_tr_b16 a[68:69], v204 offset:8200
ds_read_b64_tr_b16 a[70:71], v204 offset:10504
global_load_lds_dwordx4 v1, s[40:41] offset:-3072
ds_read_b64_tr_b16 a[72:73], v204 offset:8256
global_load_lds_dwordx4 v2, s[40:41] offset:-2048
ds_read_b64_tr_b16 a[74:75], v204 offset:10560
ds_read_b64_tr_b16 a[76:77], v204 offset:8264
global_load_lds_dwordx4 v3, s[40:41] offset:-1024
ds_read_b64_tr_b16 a[78:79], v204 offset:10568
global_load_lds_dwordx4 v4, s[40:41] offset:0
ds_read_b64_tr_b16 v[76:77], v206 offset:8192
ds_read_b64_tr_b16 v[78:79], v206 offset:10496
global_load_lds_dwordx4 v5, s[40:41] offset:1024
ds_read_b64_tr_b16 a[80:81], v204 offset:26624
global_load_lds_dwordx4 v6, s[40:41] offset:2048
ds_read_b64_tr_b16 a[82:83], v204 offset:28928
ds_read_b64_tr_b16 a[84:85], v204 offset:26632
global_load_lds_dwordx4 v7, s[40:41] offset:3072
ds_read_b64_tr_b16 a[86:87], v204 offset:28936
s_add_u32 m0, s48, 49152
ds_read_b64_tr_b16 a[88:89], v204 offset:26688
ds_read_b64_tr_b16 a[90:91], v204 offset:28992
global_load_lds_dwordx4 v8, s[40:41] offset:-4096
ds_read_b64_tr_b16 a[92:93], v204 offset:26696
s_add_u32 s40, s40, 73728
ds_read_b64_tr_b16 a[94:95], v204 offset:29000
ds_read_b64_tr_b16 v[80:81], v206 offset:26624
s_addc_u32 s41, s41, 0
ds_read_b64_tr_b16 v[82:83], v206 offset:28928
s_waitcnt vmcnt(18) lgkmcnt(0)
s_barrier
s_add_u32 m0, s48, 77824
ds_read_b64_tr_b16 a[96:97], v204 offset:45056
ds_read_b64_tr_b16 a[98:99], v204 offset:47360
global_load_lds_dwordx4 v0, s[40:41] offset:-4096
ds_read_b64_tr_b16 a[100:101], v204 offset:45064
ds_read_b64_tr_b16 a[102:103], v204 offset:47368
global_load_lds_dwordx4 v1, s[40:41] offset:-3072
ds_read_b64_tr_b16 a[104:105], v204 offset:45120
global_load_lds_dwordx4 v2, s[40:41] offset:-2048
ds_read_b64_tr_b16 a[106:107], v204 offset:47424
ds_read_b64_tr_b16 a[108:109], v204 offset:45128
global_load_lds_dwordx4 v3, s[40:41] offset:-1024
ds_read_b64_tr_b16 a[110:111], v204 offset:47432
global_load_lds_dwordx4 v4, s[40:41] offset:0
ds_read_b64_tr_b16 v[84:85], v206 offset:45056
ds_read_b64_tr_b16 v[86:87], v206 offset:47360
global_load_lds_dwordx4 v5, s[40:41] offset:1024
ds_read_b64_tr_b16 a[112:113], v204 offset:63488
global_load_lds_dwordx4 v6, s[40:41] offset:2048
ds_read_b64_tr_b16 a[114:115], v205 offset:256
ds_read_b64_tr_b16 a[116:117], v204 offset:63496
global_load_lds_dwordx4 v7, s[40:41] offset:3072
ds_read_b64_tr_b16 a[118:119], v205 offset:264
s_add_u32 m0, s48, 86016
ds_read_b64_tr_b16 a[120:121], v204 offset:63552
ds_read_b64_tr_b16 a[122:123], v205 offset:320
global_load_lds_dwordx4 v8, s[40:41] offset:-4096
ds_read_b64_tr_b16 a[124:125], v204 offset:63560
s_add_u32 s40, s40, 73728
ds_read_b64_tr_b16 a[126:127], v205 offset:328
ds_read_b64_tr_b16 v[88:89], v206 offset:63488
s_addc_u32 s41, s41, 0
ds_read_b64_tr_b16 v[90:91], v207 offset:256
s_waitcnt vmcnt(18) lgkmcnt(0)
s_barrier
s_add_u32 m0, s48, 114688
ds_read_b64_tr_b16 a[128:129], v9 offset:0
ds_read_b64_tr_b16 a[130:131], v9 offset:2304
global_load_lds_dwordx4 v0, s[40:41] offset:-4096
ds_read_b64_tr_b16 a[132:133], v9 offset:8
global_load_lds_dwordx4 v1, s[40:41] offset:-3072
ds_read_b64_tr_b16 a[134:135], v9 offset:2312
ds_read_b64_tr_b16 a[136:137], v9 offset:64
global_load_lds_dwordx4 v2, s[40:41] offset:-2048
ds_read_b64_tr_b16 a[138:139], v9 offset:2368
global_load_lds_dwordx4 v3, s[40:41] offset:-1024
ds_read_b64_tr_b16 a[140:141], v9 offset:72
global_load_lds_dwordx4 v4, s[40:41] offset:0
ds_read_b64_tr_b16 a[142:143], v9 offset:2376
ds_read_b64_tr_b16 v[92:93], v10 offset:0
global_load_lds_dwordx4 v5, s[40:41] offset:1024
ds_read_b64_tr_b16 v[94:95], v10 offset:2304
global_load_lds_dwordx4 v6, s[40:41] offset:2048
ds_read_b64_tr_b16 a[144:145], v9 offset:18432
ds_read_b64_tr_b16 a[146:147], v9 offset:20736
global_load_lds_dwordx4 v7, s[40:41] offset:3072
ds_read_b64_tr_b16 a[148:149], v9 offset:18440
s_add_u32 m0, s48, 122880
ds_read_b64_tr_b16 a[150:151], v9 offset:20744
global_load_lds_dwordx4 v8, s[40:41] offset:-4096
ds_read_b64_tr_b16 a[152:153], v9 offset:18496
ds_read_b64_tr_b16 a[154:155], v9 offset:20800
s_add_u32 s40, s40, 73728
ds_read_b64_tr_b16 a[156:157], v9 offset:18504
s_addc_u32 s41, s41, 0
ds_read_b64_tr_b16 a[158:159], v9 offset:20808
s_waitcnt vmcnt(18) lgkmcnt(0)
s_barrier
s_add_u32 m0, s48, 4096
ds_read_b64_tr_b16 a[160:161], v9 offset:36864
ds_read_b64_tr_b16 a[162:163], v9 offset:39168
global_load_lds_dwordx4 v0, s[40:41] offset:-4096
ds_read_b64_tr_b16 a[164:165], v9 offset:36872
global_load_lds_dwordx4 v1, s[40:41] offset:-3072
ds_read_b64_tr_b16 a[166:167], v9 offset:39176
global_load_lds_dwordx4 v2, s[40:41] offset:-2048
ds_read_b64_tr_b16 a[168:169], v9 offset:36928
global_load_lds_dwordx4 v3, s[40:41] offset:-1024
ds_read_b64_tr_b16 a[170:171], v9 offset:39232
ds_read_b64_tr_b16 a[172:173], v9 offset:36936
global_load_lds_dwordx4 v4, s[40:41] offset:0
ds_read_b64_tr_b16 a[174:175], v9 offset:39240
global_load_lds_dwordx4 v5, s[40:41] offset:1024
ds_read_b64_tr_b16 a[176:177], v9 offset:55296
global_load_lds_dwordx4 v6, s[40:41] offset:2048
ds_read_b64_tr_b16 a[178:179], v9 offset:57600
global_load_lds_dwordx4 v7, s[40:41] offset:3072
ds_read_b64_tr_b16 a[180:181], v9 offset:55304
ds_read_b64_tr_b16 a[182:183], v9 offset:57608
s_add_u32 m0, s48, 12288
ds_read_b64_tr_b16 a[184:185], v9 offset:55360
global_load_lds_dwordx4 v8, s[40:41] offset:-4096
ds_read_b64_tr_b16 a[186:187], v9 offset:57664
s_sub_u32 s40, s40, 589824
ds_read_b64_tr_b16 a[188:189], v9 offset:55368
s_subb_u32 s41, s41, 0
ds_read_b64_tr_b16 a[190:191], v9 offset:57672
s_waitcnt vmcnt(18) lgkmcnt(0)
s_barrier
ds_read_b64_tr_b16 a[192:193], v204 offset:8192
ds_read_b64_tr_b16 a[194:195], v204 offset:10496
ds_read_b64_tr_b16 a[196:197], v204 offset:8200
ds_read_b64_tr_b16 a[198:199], v204 offset:10504
ds_read_b64_tr_b16 a[200:201], v204 offset:8256
ds_read_b64_tr_b16 a[202:203], v204 offset:10560
ds_read_b64_tr_b16 a[204:205], v204 offset:8264
ds_read_b64_tr_b16 a[206:207], v204 offset:10568
ds_read_b64_tr_b16 a[208:209], v204 offset:26624
ds_read_b64_tr_b16 a[210:211], v204 offset:28928
ds_read_b64_tr_b16 a[212:213], v204 offset:26632
ds_read_b64_tr_b16 a[214:215], v204 offset:28936
ds_read_b64_tr_b16 a[216:217], v204 offset:26688
ds_read_b64_tr_b16 a[218:219], v204 offset:28992
ds_read_b64_tr_b16 a[220:221], v204 offset:26696
ds_read_b64_tr_b16 a[222:223], v204 offset:29000
s_waitcnt vmcnt(9) lgkmcnt(0)
s_barrier
ds_read_b64_tr_b16 a[224:225], v204 offset:45056
ds_read_b64_tr_b16 a[226:227], v204 offset:47360
ds_read_b64_tr_b16 a[228:229], v204 offset:45064
ds_read_b64_tr_b16 a[230:231], v204 offset:47368
ds_read_b64_tr_b16 a[232:233], v204 offset:45120
ds_read_b64_tr_b16 a[234:235], v204 offset:47424
ds_read_b64_tr_b16 a[236:237], v204 offset:45128
ds_read_b64_tr_b16 a[238:239], v204 offset:47432
ds_read_b64_tr_b16 a[240:241], v204 offset:63488
ds_read_b64_tr_b16 a[242:243], v205 offset:256
ds_read_b64_tr_b16 a[244:245], v204 offset:63496
ds_read_b64_tr_b16 a[246:247], v205 offset:264
ds_read_b64_tr_b16 a[248:249], v204 offset:63552
ds_read_b64_tr_b16 a[250:251], v205 offset:320
ds_read_b64_tr_b16 a[252:253], v204 offset:63560
ds_read_b64_tr_b16 a[254:255], v205 offset:328
s_waitcnt vmcnt(0) lgkmcnt(0)
s_barrier
ds_read_b64_tr_b16 v[28:29], v9 offset:0
ds_read_b64_tr_b16 v[30:31], v9 offset:2304
ds_read_b64_tr_b16 v[32:33], v9 offset:8
ds_read_b64_tr_b16 v[34:35], v9 offset:2312
ds_read_b64_tr_b16 v[36:37], v9 offset:64
ds_read_b64_tr_b16 v[38:39], v9 offset:2368
ds_read_b64_tr_b16 v[40:41], v9 offset:72
ds_read_b64_tr_b16 v[42:43], v9 offset:2376
ds_read_b64_tr_b16 v[44:45], v9 offset:18432
ds_read_b64_tr_b16 v[46:47], v9 offset:20736
ds_read_b64_tr_b16 v[48:49], v9 offset:18440
ds_read_b64_tr_b16 v[50:51], v9 offset:20744
ds_read_b64_tr_b16 v[52:53], v9 offset:18496
ds_read_b64_tr_b16 v[54:55], v9 offset:20800
ds_read_b64_tr_b16 v[56:57], v9 offset:18504
ds_read_b64_tr_b16 v[58:59], v9 offset:20808
s_waitcnt lgkmcnt(0)
s_barrier
v_add_lshl_u32 v252, v24, 0, 7
v_bfi_b32 v252, s69, v252, v23
v_add_lshl_u32 v253, v22, 0, 7
v_bfi_b32 v253, s69, v253, v21
s_mov_b64 s[76:77], 0
s_add_u32 m0, s49, 18432
s_mov_b32 s70, 33
s_waitcnt vmcnt(0) lgkmcnt(0)
s_nop 7
.p2align 6
.Lwo_top13:
s_waitcnt lgkmcnt(2)
v_mfma_f32_16x16x32_bf16 v[96:99], a[0:3], v[132:135], v[96:99]
ds_read_b128 v[144:147], v25 offset:3072
v_mfma_f32_16x16x32_bf16 v[100:103], a[4:7], v[132:135], v[100:103]
v_mfma_f32_16x16x32_bf16 v[104:107], a[8:11], v[132:135], v[104:107]
s_waitcnt vmcnt(7)
s_barrier
v_mfma_f32_16x16x32_bf16 v[108:111], a[12:15], v[132:135], v[108:111]
v_cvt_pk_bf16_f32 v164, v112, v113
v_cvt_pk_bf16_f32 v165, v114, v115
v_mfma_f32_16x16x32_bf16 v[128:131], v[60:63], v[132:135], 0
s_waitcnt lgkmcnt(2)
v_cvt_pk_bf16_f32 v166, v116, v117
v_cvt_pk_bf16_f32 v167, v118, v119
s_mov_b64 exec, 65535
global_store_dwordx2 v15, v[176:177], s[38:39] sc1
s_mov_b64 exec, -1
v_mfma_f32_16x16x32_bf16 v[96:99], a[16:19], v[136:139], v[96:99]
ds_read_b128 v[132:135], v25 offset:4096
v_add_f32 v254, v112, v113
v_add_u32 v15, 18688, v15
v_mfma_f32_16x16x32_bf16 v[100:103], a[20:23], v[136:139], v[100:103]
v_mul_f32 v255, v112, v112
v_fmac_f32 v255, v113, v113
v_mfma_f32_16x16x32_bf16 v[104:107], a[24:27], v[136:139], v[104:107]
global_store_dwordx4 v13, v[164:167], s[38:39] offset:0 sc1
v_mfma_f32_16x16x32_bf16 v[108:111], a[28:31], v[136:139], v[108:111]
v_cvt_pk_bf16_f32 v168, v120, v121
v_cvt_pk_bf16_f32 v169, v122, v123
v_mfma_f32_16x16x32_bf16 v[128:131], v[64:67], v[136:139], v[128:131]
s_waitcnt lgkmcnt(2)
v_cvt_pk_bf16_f32 v170, v124, v125
v_cvt_pk_bf16_f32 v171, v126, v127
v_mfma_f32_16x16x32_bf16 v[96:99], a[32:35], v[140:143], v[96:99]
s_cmp_eq_u32 s70, 32
ds_read_b128 v[136:139], v25 offset:5120
v_add_f32 v254, v254, v114
v_mfma_f32_16x16x32_bf16 v[100:103], a[36:39], v[140:143], v[100:103]
s_cselect_b64 s[42:43], s[44:45], s[42:43]
global_store_dwordx4 v13, v[168:171], s[38:39] offset:1024 sc1
v_mfma_f32_16x16x32_bf16 v[104:107], a[40:43], v[140:143], v[104:107]
s_cmp_eq_u32 s70, 16
v_add_u32 v13, 18688, v13
v_fmac_f32 v255, v114, v114
v_mfma_f32_16x16x32_bf16 v[108:111], a[44:47], v[140:143], v[108:111]
s_cselect_b64 s[42:43], s[46:47], s[42:43]
v_add_f32 v254, v254, v115
v_fmac_f32 v255, v115, v115
v_mfma_f32_16x16x32_bf16 v[128:131], v[68:71], v[140:143], v[128:131]
)"
R"(s_waitcnt lgkmcnt(2)
v_add_f32 v254, v254, v116
v_fmac_f32 v255, v116, v116
v_mfma_f32_16x16x32_bf16 v[96:99], a[48:51], v[144:147], v[96:99]
s_xor_b32 s49, s49, 0x10000
ds_read_b128 v[140:143], v25 offset:6144
v_add_f32 v254, v254, v117
v_mfma_f32_16x16x32_bf16 v[100:103], a[52:55], v[144:147], v[100:103]
s_mov_b32 m0, 51200
v_fmac_f32 v255, v117, v117
v_add_f32 v254, v254, v118
v_mfma_f32_16x16x32_bf16 v[104:107], a[56:59], v[144:147], v[104:107]
v_fmac_f32 v255, v118, v118
v_add_f32 v254, v254, v119
v_mfma_f32_16x16x32_bf16 v[108:111], a[60:63], v[144:147], v[108:111]
v_fmac_f32 v255, v119, v119
v_add_f32 v254, v254, v120
v_mfma_f32_16x16x32_bf16 v[128:131], v[72:75], v[144:147], v[128:131]
s_waitcnt lgkmcnt(2)
v_fmac_f32 v255, v120, v120
v_add_f32 v254, v254, v121
v_mfma_f32_16x16x32_bf16 v[96:99], a[64:67], v[132:135], v[96:99]
ds_read_b128 v[144:147], v25 offset:7168
v_fmac_f32 v255, v121, v121
v_mfma_f32_16x16x32_bf16 v[100:103], a[68:71], v[132:135], v[100:103]
v_add_f32 v254, v254, v122
v_fmac_f32 v255, v122, v122
v_mfma_f32_16x16x32_bf16 v[104:107], a[72:75], v[132:135], v[104:107]
global_load_lds_dwordx4 v16, s[42:43] offset:0 sc1
v_add_f32 v254, v254, v123
v_mfma_f32_16x16x32_bf16 v[108:111], a[76:79], v[132:135], v[108:111]
v_fmac_f32 v255, v123, v123
v_add_f32 v254, v254, v124
v_mfma_f32_16x16x32_bf16 v[128:131], v[76:79], v[132:135], v[128:131]
s_waitcnt vmcnt(9)
ds_read_b128 v[148:151], v19 offset:8192
ds_read_b128 v[152:155], v20 offset:8192
s_waitcnt lgkmcnt(4)
v_mfma_f32_16x16x32_bf16 v[96:99], a[80:83], v[136:139], v[96:99]
ds_read_b128 v[132:135], v25 offset:8192
v_fmac_f32 v255, v124, v124
v_mfma_f32_16x16x32_bf16 v[100:103], a[84:87], v[136:139], v[100:103]
v_add_f32 v254, v254, v125
v_fmac_f32 v255, v125, v125
v_mfma_f32_16x16x32_bf16 v[104:107], a[88:91], v[136:139], v[104:107]
v_add_f32 v254, v254, v126
v_fmac_f32 v255, v126, v126
v_mfma_f32_16x16x32_bf16 v[108:111], a[92:95], v[136:139], v[108:111]
v_add_f32 v254, v254, v127
v_fmac_f32 v255, v127, v127
v_mfma_f32_16x16x32_bf16 v[128:131], v[80:83], v[136:139], v[128:131]
s_waitcnt lgkmcnt(4)
global_load_lds_dwordx4 v17, s[42:43] offset:1024 sc1
v_mfma_f32_16x16x32_bf16 v[96:99], a[96:99], v[140:143], v[96:99]
ds_read_b128 v[136:139], v25 offset:9216
v_mfma_f32_16x16x32_bf16 v[100:103], a[100:103], v[140:143], v[100:103]
ds_write_b64 v27, v[254:255] offset:46080
v_mfma_f32_16x16x32_bf16 v[104:107], a[104:107], v[140:143], v[104:107]
s_waitcnt lgkmcnt(4)
v_lshlrev_b32 v112, 16, v148
v_and_b32 v113, 0xffff0000, v148
v_mfma_f32_16x16x32_bf16 v[108:111], a[108:111], v[140:143], v[108:111]
v_lshlrev_b32 v114, 16, v149
v_and_b32 v115, 0xffff0000, v149
v_mfma_f32_16x16x32_bf16 v[128:131], v[84:87], v[140:143], v[128:131]
v_lshlrev_b32 v116, 16, v150
v_and_b32 v117, 0xffff0000, v150
v_mfma_f32_16x16x32_bf16 v[96:99], a[112:115], v[144:147], v[96:99]
s_add_u32 m0, s49, 0
ds_read_b128 v[140:143], v25 offset:10240
v_lshlrev_b32 v118, 16, v151
v_mfma_f32_16x16x32_bf16 v[100:103], a[116:119], v[144:147], v[100:103]
s_waitcnt lgkmcnt(4)
v_and_b32 v119, 0xffff0000, v151
v_lshlrev_b32 v120, 16, v152
v_mfma_f32_16x16x32_bf16 v[104:107], a[120:123], v[144:147], v[104:107]
s_sub_u32 s42, s42, 18432
v_and_b32 v121, 0xffff0000, v152
v_lshlrev_b32 v122, 16, v153
v_mfma_f32_16x16x32_bf16 v[108:111], a[124:127], v[144:147], v[108:111]
s_subb_u32 s43, s43, 0
v_and_b32 v123, 0xffff0000, v153
v_lshlrev_b32 v124, 16, v154
v_mfma_f32_16x16x32_bf16 v[128:131], v[88:91], v[144:147], v[128:131]
s_waitcnt lgkmcnt(3)
v_and_b32 v125, 0xffff0000, v154
v_lshlrev_b32 v126, 16, v155
v_mfma_f32_16x16x32_bf16 v[96:99], a[128:131], v[132:135], v[96:99]
ds_read_b128 v[144:147], v25 offset:11264
v_and_b32 v127, 0xffff0000, v155
v_mfma_f32_16x16x32_bf16 v[100:103], a[132:135], v[132:135], v[100:103]
v_mfma_f32_16x16x32_bf16 v[104:107], a[136:139], v[132:135], v[104:107]
v_mfma_f32_16x16x32_bf16 v[108:111], a[140:143], v[132:135], v[108:111]
v_mfma_f32_16x16x32_bf16 v[128:131], v[92:95], v[132:135], v[128:131]
s_waitcnt lgkmcnt(3)
ds_read_b64 v[176:177], v27 offset:43008
ds_read_b64 v[180:181], v27 offset:43520
v_mfma_f32_16x16x32_bf16 v[96:99], a[144:147], v[136:139], v[96:99]
ds_read_b128 v[132:135], v25 offset:12288
ds_read_b64 v[184:185], v27 offset:44032
v_mfma_f32_16x16x32_bf16 v[100:103], a[148:151], v[136:139], v[100:103]
ds_read_b64 v[188:189], v27 offset:44544
v_mfma_f32_16x16x32_bf16 v[104:107], a[152:155], v[136:139], v[104:107]
v_mfma_f32_16x16x32_bf16 v[108:111], a[156:159], v[136:139], v[108:111]
s_waitcnt lgkmcnt(6)
v_mfma_f32_16x16x32_bf16 v[96:99], a[160:163], v[140:143], v[96:99]
ds_read_b128 v[136:139], v25 offset:13312
v_mfma_f32_16x16x32_bf16 v[100:103], a[164:167], v[140:143], v[100:103]
v_mfma_f32_16x16x32_bf16 v[104:107], a[168:171], v[140:143], v[104:107]
v_mfma_f32_16x16x32_bf16 v[108:111], a[172:175], v[140:143], v[108:111]
s_waitcnt lgkmcnt(4)
v_add_f32 v176, v176, v180
v_add_f32 v177, v177, v181
v_mfma_f32_16x16x32_bf16 v[96:99], a[176:179], v[144:147], v[96:99]
s_waitcnt lgkmcnt(1)
ds_read_b128 v[140:143], v25 offset:14336
v_add_f32 v184, v184, v188
v_mfma_f32_16x16x32_bf16 v[100:103], a[180:183], v[144:147], v[100:103]
v_mfma_f32_16x16x32_bf16 v[104:107], a[184:187], v[144:147], v[104:107]
s_barrier
v_mfma_f32_16x16x32_bf16 v[108:111], a[188:191], v[144:147], v[108:111]
global_load_lds_dwordx4 v11, s[38:39] offset:-2048 sc1
v_add_f32 v185, v185, v189
v_mfma_f32_16x16x32_bf16 v[96:99], a[192:195], v[132:135], v[96:99]
ds_read_b128 v[144:147], v25 offset:15360
v_add_f32 v176, v176, v184
v_mfma_f32_16x16x32_bf16 v[100:103], a[196:199], v[132:135], v[100:103]
v_add_f32 v177, v177, v185
v_mov_b32 v202, v176
v_mfma_f32_16x16x32_bf16 v[104:107], a[200:203], v[132:135], v[104:107]
ds_write_b128 v26, v[128:131] offset:38912
v_mfma_f32_16x16x32_bf16 v[108:111], a[204:207], v[132:135], v[108:111]
s_waitcnt lgkmcnt(3)
global_load_lds_dwordx4 v11, s[38:39] offset:-1024 sc1
v_mov_b32 v203, v177
v_mfma_f32_16x16x32_bf16 v[96:99], a[208:211], v[136:139], v[96:99]
ds_read_b128 v[132:135], v25 offset:16384
v_mfma_f32_16x16x32_bf16 v[100:103], a[212:215], v[136:139], v[100:103]
v_permlane16_swap_b32 v176, v202
v_mfma_f32_16x16x32_bf16 v[104:107], a[216:219], v[136:139], v[104:107]
global_load_lds_dwordx4 v11, s[38:39] offset:0 sc1
v_mfma_f32_16x16x32_bf16 v[108:111], a[220:223], v[136:139], v[108:111]
s_waitcnt lgkmcnt(3)
v_permlane16_swap_b32 v177, v203
v_mfma_f32_16x16x32_bf16 v[96:99], a[224:227], v[140:143], v[96:99]
ds_read_b128 v[136:139], v25 offset:17408
v_add_f32 v176, v176, v202
v_mfma_f32_16x16x32_bf16 v[100:103], a[228:231], v[140:143], v[100:103]
global_load_lds_dwordx4 v11, s[38:39] offset:1024 sc1
v_add_f32 v177, v177, v203
v_mfma_f32_16x16x32_bf16 v[104:107], a[232:235], v[140:143], v[104:107]
v_mov_b32 v202, v176
v_mov_b32 v203, v177
v_mfma_f32_16x16x32_bf16 v[108:111], a[236:239], v[140:143], v[108:111]
s_waitcnt lgkmcnt(3)
v_permlane32_swap_b32 v176, v202
v_mfma_f32_16x16x32_bf16 v[96:99], a[240:243], v[144:147], v[96:99]
global_load_lds_dwordx4 v11, s[38:39] offset:2048 sc1
v_add_u32 v11, 18432, v11
v_mfma_f32_16x16x32_bf16 v[100:103], a[244:247], v[144:147], v[100:103]
v_mfma_f32_16x16x32_bf16 v[104:107], a[248:251], v[144:147], v[104:107]
s_waitcnt vmcnt(10) lgkmcnt(2)
s_barrier
v_mfma_f32_16x16x32_bf16 v[108:111], a[252:255], v[144:147], v[108:111]
s_waitcnt lgkmcnt(1)
ds_read_b128 v[140:143], v25 offset:18432
v_min_u32 v11, v11, v12
v_mfma_f32_16x16x32_bf16 v[96:99], v[28:31], v[132:135], v[96:99]
ds_read_b128 v[144:147], v25 offset:19456
v_mfma_f32_16x16x32_bf16 v[100:103], v[32:35], v[132:135], v[100:103]
v_permlane32_swap_b32 v177, v203
v_mfma_f32_16x16x32_bf16 v[104:107], v[36:39], v[132:135], v[104:107]
v_add_f32 v176, v176, v202
v_add_f32 v177, v177, v203
v_mfma_f32_16x16x32_bf16 v[108:111], v[40:43], v[132:135], v[108:111]
s_waitcnt lgkmcnt(2)
v_mfma_f32_16x16x32_bf16 v[96:99], v[44:47], v[136:139], v[96:99]
ds_read_b128 v[132:135], v25 offset:20480
v_mfma_f32_16x16x32_bf16 v[100:103], v[48:51], v[136:139], v[100:103]
v_mfma_f32_16x16x32_bf16 v[104:107], v[52:55], v[136:139], v[104:107]
v_mfma_f32_16x16x32_bf16 v[108:111], v[56:59], v[136:139], v[108:111]
s_waitcnt lgkmcnt(2)
v_mfma_f32_16x16x32_bf16 v[112:115], a[0:3], v[140:143], v[112:115]
ds_read_b128 v[136:139], v25 offset:21504
v_mfma_f32_16x16x32_bf16 v[116:119], a[4:7], v[140:143], v[116:119]
v_mfma_f32_16x16x32_bf16 v[120:123], a[8:11], v[140:143], v[120:123]
s_waitcnt vmcnt(7)
s_barrier
v_mfma_f32_16x16x32_bf16 v[124:127], a[12:15], v[140:143], v[124:127]
v_cvt_pk_bf16_f32 v164, v96, v97
v_cvt_pk_bf16_f32 v165, v98, v99
v_mfma_f32_16x16x32_bf16 v[128:131], v[60:63], v[140:143], 0
s_waitcnt lgkmcnt(2)
v_cvt_pk_bf16_f32 v166, v100, v101
v_cvt_pk_bf16_f32 v167, v102, v103
s_mov_b64 exec, 65535
global_store_dwordx2 v15, v[176:177], s[38:39] sc1
s_mov_b64 exec, -1
v_mfma_f32_16x16x32_bf16 v[112:115], a[16:19], v[144:147], v[112:115]
ds_read_b128 v[140:143], v25 offset:22528
v_add_u32 v15, 18688, v15
v_mfma_f32_16x16x32_bf16 v[116:119], a[20:23], v[144:147], v[116:119]
v_add_f32 v254, v96, v97
v_mul_f32 v255, v96, v96
v_mfma_f32_16x16x32_bf16 v[120:123], a[24:27], v[144:147], v[120:123]
global_store_dwordx4 v13, v[164:167], s[38:39] offset:0 sc1
v_mfma_f32_16x16x32_bf16 v[124:127], a[28:31], v[144:147], v[124:127]
v_cvt_pk_bf16_f32 v168, v104, v105
v_cvt_pk_bf16_f32 v169, v106, v107
v_mfma_f32_16x16x32_bf16 v[128:131], v[64:67], v[144:147], v[128:131]
s_waitcnt lgkmcnt(2)
v_cvt_pk_bf16_f32 v170, v108, v109
v_cvt_pk_bf16_f32 v171, v110, v111
v_mfma_f32_16x16x32_bf16 v[112:115], a[32:35], v[132:135], v[112:115]
s_mov_b32 m0, 59392
ds_read_b128 v[144:147], v25 offset:23552
v_fmac_f32 v255, v97, v97
v_mfma_f32_16x16x32_bf16 v[116:119], a[36:39], v[132:135], v[116:119]
global_store_dwordx4 v13, v[168:171], s[38:39] offset:1024 sc1
v_mfma_f32_16x16x32_bf16 v[120:123], a[40:43], v[132:135], v[120:123]
v_add_u32 v13, 18688, v13
v_add_f32 v254, v254, v98
v_mfma_f32_16x16x32_bf16 v[124:127], a[44:47], v[132:135], v[124:127]
v_fmac_f32 v255, v98, v98
v_add_f32 v254, v254, v99
v_mfma_f32_16x16x32_bf16 v[128:131], v[68:71], v[132:135], v[128:131]
s_waitcnt lgkmcnt(2)
v_fmac_f32 v255, v99, v99
v_add_f32 v254, v254, v100
v_mfma_f32_16x16x32_bf16 v[112:115], a[48:51], v[136:139], v[112:115]
ds_read_b128 v[132:135], v25 offset:24576
v_fmac_f32 v255, v100, v100
v_mfma_f32_16x16x32_bf16 v[116:119], a[52:55], v[136:139], v[116:119]
v_add_f32 v254, v254, v101
v_fmac_f32 v255, v101, v101
v_mfma_f32_16x16x32_bf16 v[120:123], a[56:59], v[136:139], v[120:123]
v_add_f32 v254, v254, v102
v_fmac_f32 v255, v102, v102
v_mfma_f32_16x16x32_bf16 v[124:127], a[60:63], v[136:139], v[124:127]
v_add_f32 v254, v254, v103
v_fmac_f32 v255, v103, v103
v_mfma_f32_16x16x32_bf16 v[128:131], v[72:75], v[136:139], v[128:131]
s_waitcnt lgkmcnt(2)
v_add_f32 v254, v254, v104
v_fmac_f32 v255, v104, v104
v_mfma_f32_16x16x32_bf16 v[112:115], a[64:67], v[140:143], v[112:115]
ds_read_b128 v[136:139], v25 offset:25600
v_add_f32 v254, v254, v105
v_mfma_f32_16x16x32_bf16 v[116:119], a[68:71], v[140:143], v[116:119]
v_fmac_f32 v255, v105, v105
v_add_f32 v254, v254, v106
v_mfma_f32_16x16x32_bf16 v[120:123], a[72:75], v[140:143], v[120:123]
global_load_lds_dwordx4 v16, s[42:43] offset:0 sc1
v_fmac_f32 v255, v106, v106
v_mfma_f32_16x16x32_bf16 v[124:127], a[76:79], v[140:143], v[124:127]
v_add_f32 v254, v254, v107
v_fmac_f32 v255, v107, v107
v_mfma_f32_16x16x32_bf16 v[128:131], v[76:79], v[140:143], v[128:131]
s_waitcnt vmcnt(9)
ds_read_b128 v[148:151], v19 offset:0
ds_read_b128 v[152:155], v20 offset:0
s_waitcnt lgkmcnt(4)
v_mfma_f32_16x16x32_bf16 v[112:115], a[80:83], v[144:147], v[112:115]
ds_read_b128 v[140:143], v25 offset:26624
v_add_f32 v254, v254, v108
v_mfma_f32_16x16x32_bf16 v[116:119], a[84:87], v[144:147], v[116:119]
v_fmac_f32 v255, v108, v108
v_add_f32 v254, v254, v109
v_mfma_f32_16x16x32_bf16 v[120:123], a[88:91], v[144:147], v[120:123]
v_fmac_f32 v255, v109, v109
v_add_f32 v254, v254, v110
v_mfma_f32_16x16x32_bf16 v[124:127], a[92:95], v[144:147], v[124:127]
v_fmac_f32 v255, v110, v110
v_add_f32 v254, v254, v111
v_mfma_f32_16x16x32_bf16 v[128:131], v[80:83], v[144:147], v[128:131]
s_waitcnt lgkmcnt(4)
global_load_lds_dwordx4 v17, s[42:43] offset:1024 sc1
v_fmac_f32 v255, v111, v111
v_mfma_f32_16x16x32_bf16 v[112:115], a[96:99], v[132:135], v[112:115]
ds_read_b128 v[144:147], v25 offset:27648
v_mfma_f32_16x16x32_bf16 v[116:119], a[100:103], v[132:135], v[116:119]
ds_write_b64 v27, v[254:255] offset:44032
v_mfma_f32_16x16x32_bf16 v[120:123], a[104:107], v[132:135], v[120:123]
s_waitcnt lgkmcnt(4)
v_lshlrev_b32 v96, 16, v148
v_and_b32 v97, 0xffff0000, v148
v_mfma_f32_16x16x32_bf16 v[124:127], a[108:111], v[132:135], v[124:127]
v_lshlrev_b32 v98, 16, v149
v_and_b32 v99, 0xffff0000, v149
v_mfma_f32_16x16x32_bf16 v[128:131], v[84:87], v[132:135], v[128:131]
v_lshlrev_b32 v100, 16, v150
v_and_b32 v101, 0xffff0000, v150
v_mfma_f32_16x16x32_bf16 v[112:115], a[112:115], v[136:139], v[112:115]
s_add_u32 m0, s49, 18432
ds_read_b128 v[132:135], v25 offset:28672
v_lshlrev_b32 v102, 16, v151
v_mfma_f32_16x16x32_bf16 v[116:119], a[116:119], v[136:139], v[116:119]
s_waitcnt lgkmcnt(4)
v_and_b32 v103, 0xffff0000, v151
v_lshlrev_b32 v104, 16, v152
v_mfma_f32_16x16x32_bf16 v[120:123], a[120:123], v[136:139], v[120:123]
s_sub_u32 s42, s42, 18432
v_and_b32 v105, 0xffff0000, v152
v_lshlrev_b32 v106, 16, v153
v_mfma_f32_16x16x32_bf16 v[124:127], a[124:127], v[136:139], v[124:127]
s_subb_u32 s43, s43, 0
v_and_b32 v107, 0xffff0000, v153
v_lshlrev_b32 v108, 16, v154
v_mfma_f32_16x16x32_bf16 v[128:131], v[88:91], v[136:139], v[128:131]
s_waitcnt lgkmcnt(3)
v_and_b32 v109, 0xffff0000, v154
v_lshlrev_b32 v110, 16, v155
v_mfma_f32_16x16x32_bf16 v[112:115], a[128:131], v[140:143], v[112:115]
ds_read_b128 v[136:139], v25 offset:29696
v_and_b32 v111, 0xffff0000, v155
v_mfma_f32_16x16x32_bf16 v[116:119], a[132:135], v[140:143], v[116:119]
v_mfma_f32_16x16x32_bf16 v[120:123], a[136:139], v[140:143], v[120:123]
v_mfma_f32_16x16x32_bf16 v[124:127], a[140:143], v[140:143], v[124:127]
v_mfma_f32_16x16x32_bf16 v[128:131], v[92:95], v[140:143], v[128:131]
s_waitcnt lgkmcnt(3)
ds_read_b64 v[176:177], v27 offset:45056
ds_read_b64 v[180:181], v27 offset:45568
v_mfma_f32_16x16x32_bf16 v[112:115], a[144:147], v[144:147], v[112:115]
ds_read_b128 v[140:143], v25 offset:30720
ds_read_b64 v[184:185], v27 offset:46080
v_mfma_f32_16x16x32_bf16 v[116:119], a[148:151], v[144:147], v[116:119]
ds_read_b64 v[188:189], v27 offset:46592
v_mfma_f32_16x16x32_bf16 v[120:123], a[152:155], v[144:147], v[120:123]
v_mfma_f32_16x16x32_bf16 v[124:127], a[156:159], v[144:147], v[124:127]
s_waitcnt lgkmcnt(6)
v_mfma_f32_16x16x32_bf16 v[112:115], a[160:163], v[132:135], v[112:115]
ds_read_b128 v[144:147], v25 offset:31744
v_mfma_f32_16x16x32_bf16 v[116:119], a[164:167], v[132:135], v[116:119]
v_mfma_f32_16x16x32_bf16 v[120:123], a[168:171], v[132:135], v[120:123]
v_mfma_f32_16x16x32_bf16 v[124:127], a[172:175], v[132:135], v[124:127]
s_waitcnt lgkmcnt(4)
v_add_f32 v176, v176, v180
v_add_f32 v177, v177, v181
v_mfma_f32_16x16x32_bf16 v[112:115], a[176:179], v[136:139], v[112:115]
s_waitcnt lgkmcnt(1)
ds_read_b128 v[132:135], v25 offset:32768
v_add_f32 v184, v184, v188
v_mfma_f32_16x16x32_bf16 v[116:119], a[180:183], v[136:139], v[116:119]
v_mfma_f32_16x16x32_bf16 v[120:123], a[184:187], v[136:139], v[120:123]
s_barrier
v_mfma_f32_16x16x32_bf16 v[124:127], a[188:191], v[136:139], v[124:127]
global_load_lds_dwordx4 v11, s[38:39] offset:-2048 sc1
v_add_f32 v185, v185, v189
v_mfma_f32_16x16x32_bf16 v[112:115], a[192:195], v[140:143], v[112:115]
s_mov_b64 s[76:77], -1
ds_read_b128 v[136:139], v25 offset:33792
v_add_f32 v176, v176, v184
v_mfma_f32_16x16x32_bf16 v[116:119], a[196:199], v[140:143], v[116:119]
v_add_f32 v177, v177, v185
v_mov_b32 v202, v176
v_mfma_f32_16x16x32_bf16 v[120:123], a[200:203], v[140:143], v[120:123]
ds_write_b128 v26, v[128:131] offset:41984
v_mfma_f32_16x16x32_bf16 v[124:127], a[204:207], v[140:143], v[124:127]
s_waitcnt lgkmcnt(3)
global_load_lds_dwordx4 v11, s[38:39] offset:-1024 sc1
v_mov_b32 v203, v177
v_mfma_f32_16x16x32_bf16 v[112:115], a[208:211], v[144:147], v[112:115]
ds_read_b128 v[140:143], v25 offset:34816
v_mfma_f32_16x16x32_bf16 v[116:119], a[212:215], v[144:147], v[116:119]
v_permlane16_swap_b32 v176, v202
v_mfma_f32_16x16x32_bf16 v[120:123], a[216:219], v[144:147], v[120:123]
global_load_lds_dwordx4 v11, s[38:39] offset:0 sc1
v_mfma_f32_16x16x32_bf16 v[124:127], a[220:223], v[144:147], v[124:127]
s_waitcnt lgkmcnt(3)
v_permlane16_swap_b32 v177, v203
v_mfma_f32_16x16x32_bf16 v[112:115], a[224:227], v[132:135], v[112:115]
ds_read_b128 v[144:147], v25 offset:35840
v_add_f32 v176, v176, v202
v_mfma_f32_16x16x32_bf16 v[116:119], a[228:231], v[132:135], v[116:119]
global_load_lds_dwordx4 v11, s[38:39] offset:1024 sc1
v_add_f32 v177, v177, v203
v_mfma_f32_16x16x32_bf16 v[120:123], a[232:235], v[132:135], v[120:123]
v_mov_b32 v202, v176
v_mov_b32 v203, v177
v_mfma_f32_16x16x32_bf16 v[124:127], a[236:239], v[132:135], v[124:127]
s_waitcnt lgkmcnt(3)
v_xor_b32 v25, 0x10000, v25
v_mfma_f32_16x16x32_bf16 v[112:115], a[240:243], v[136:139], v[112:115]
global_load_lds_dwordx4 v11, s[38:39] offset:2048 sc1
v_add_u32 v11, 18432, v11
v_mfma_f32_16x16x32_bf16 v[116:119], a[244:247], v[136:139], v[116:119]
v_mfma_f32_16x16x32_bf16 v[120:123], a[248:251], v[136:139], v[120:123]
s_waitcnt vmcnt(10) lgkmcnt(2)
s_barrier
v_mfma_f32_16x16x32_bf16 v[124:127], a[252:255], v[136:139], v[124:127]
s_waitcnt lgkmcnt(1)
ds_read_b128 v[132:135], v25 offset:0
v_min_u32 v11, v11, v12
v_mfma_f32_16x16x32_bf16 v[112:115], v[28:31], v[140:143], v[112:115]
ds_read_b128 v[136:139], v25 offset:1024
v_mfma_f32_16x16x32_bf16 v[116:119], v[32:35], v[140:143], v[116:119]
v_permlane32_swap_b32 v176, v202
v_mfma_f32_16x16x32_bf16 v[120:123], v[36:39], v[140:143], v[120:123]
v_permlane32_swap_b32 v177, v203
v_mfma_f32_16x16x32_bf16 v[124:127], v[40:43], v[140:143], v[124:127]
)"
R"(s_waitcnt lgkmcnt(2)
v_add_f32 v176, v176, v202
v_add_f32 v177, v177, v203
v_mfma_f32_16x16x32_bf16 v[112:115], v[44:47], v[144:147], v[112:115]
ds_read_b128 v[140:143], v25 offset:2048
v_mfma_f32_16x16x32_bf16 v[116:119], v[48:51], v[144:147], v[116:119]
v_mfma_f32_16x16x32_bf16 v[120:123], v[52:55], v[144:147], v[120:123]
v_mfma_f32_16x16x32_bf16 v[124:127], v[56:59], v[144:147], v[124:127]
s_sub_u32 s70, s70, 1
s_cbranch_scc0 .Lwo_top13
s_waitcnt vmcnt(7)
s_barrier
s_mov_b64 exec, 65535
global_store_dwordx2 v15, v[176:177], s[38:39] sc1
s_mov_b64 exec, -1
v_add_u32 v15, 18688, v15
v_cvt_pk_bf16_f32 v164, v112, v113
v_cvt_pk_bf16_f32 v165, v114, v115
v_cvt_pk_bf16_f32 v166, v116, v117
v_cvt_pk_bf16_f32 v167, v118, v119
global_store_dwordx4 v13, v[164:167], s[38:39] offset:0 sc1
v_cvt_pk_bf16_f32 v168, v120, v121
v_cvt_pk_bf16_f32 v169, v122, v123
v_cvt_pk_bf16_f32 v170, v124, v125
v_cvt_pk_bf16_f32 v171, v126, v127
global_store_dwordx4 v13, v[168:171], s[38:39] offset:1024 sc1
v_add_f32 v254, v112, v113
v_mul_f32 v255, v112, v112
v_fmac_f32 v255, v113, v113
v_add_f32 v254, v254, v114
v_fmac_f32 v255, v114, v114
v_add_f32 v254, v254, v115
v_fmac_f32 v255, v115, v115
v_add_f32 v254, v254, v116
v_fmac_f32 v255, v116, v116
v_add_f32 v254, v254, v117
v_fmac_f32 v255, v117, v117
v_add_f32 v254, v254, v118
v_fmac_f32 v255, v118, v118
v_add_f32 v254, v254, v119
v_fmac_f32 v255, v119, v119
v_add_f32 v254, v254, v120
v_fmac_f32 v255, v120, v120
v_add_f32 v254, v254, v121
v_fmac_f32 v255, v121, v121
v_add_f32 v254, v254, v122
v_fmac_f32 v255, v122, v122
v_add_f32 v254, v254, v123
v_fmac_f32 v255, v123, v123
v_add_f32 v254, v254, v124
v_fmac_f32 v255, v124, v124
v_add_f32 v254, v254, v125
v_fmac_f32 v255, v125, v125
v_add_f32 v254, v254, v126
v_fmac_f32 v255, v126, v126
v_add_f32 v254, v254, v127
v_fmac_f32 v255, v127, v127
ds_write_b64 v27, v[254:255] offset:46080
s_waitcnt vmcnt(0) lgkmcnt(0)
s_barrier
ds_read_b64 v[176:177], v27 offset:43008
ds_read_b64 v[180:181], v27 offset:43520
ds_read_b64 v[184:185], v27 offset:44032
ds_read_b64 v[188:189], v27 offset:44544
s_waitcnt lgkmcnt(2)
v_add_f32 v176, v176, v180
v_add_f32 v177, v177, v181
s_waitcnt lgkmcnt(0)
v_add_f32 v184, v184, v188
v_add_f32 v185, v185, v189
v_add_f32 v176, v176, v184
v_add_f32 v177, v177, v185
v_mov_b32 v202, v176
v_mov_b32 v203, v177
s_nop 0
v_permlane16_swap_b32 v176, v202
v_permlane16_swap_b32 v177, v203
v_add_f32 v176, v176, v202
v_add_f32 v177, v177, v203
v_mov_b32 v202, v176
v_mov_b32 v203, v177
s_nop 0
v_permlane32_swap_b32 v176, v202
v_permlane32_swap_b32 v177, v203
v_add_f32 v176, v176, v202
v_add_f32 v177, v177, v203
s_mov_b64 exec, 65535
global_store_dwordx2 v15, v[176:177], s[38:39] sc1
s_mov_b64 exec, -1
v_add_u32 v15, 18688, v15
ds_read_b64 v[176:177], v27 offset:45056
ds_read_b64 v[180:181], v27 offset:45568
ds_read_b64 v[184:185], v27 offset:46080
ds_read_b64 v[188:189], v27 offset:46592
s_waitcnt lgkmcnt(2)
v_add_f32 v176, v176, v180
v_add_f32 v177, v177, v181
s_waitcnt lgkmcnt(0)
v_add_f32 v184, v184, v188
v_add_f32 v185, v185, v189
v_add_f32 v176, v176, v184
v_add_f32 v177, v177, v185
v_mov_b32 v202, v176
v_mov_b32 v203, v177
s_nop 0
v_permlane16_swap_b32 v176, v202
v_permlane16_swap_b32 v177, v203
v_add_f32 v176, v176, v202
v_add_f32 v177, v177, v203
v_mov_b32 v202, v176
v_mov_b32 v203, v177
s_nop 0
v_permlane32_swap_b32 v176, v202
v_permlane32_swap_b32 v177, v203
v_add_f32 v176, v176, v202
v_add_f32 v177, v177, v203
s_mov_b64 exec, 65535
global_store_dwordx2 v15, v[176:177], s[38:39] sc1
s_mov_b64 exec, -1
v_add_u32 v15, 18688, v15
s_waitcnt vmcnt(0)
s_barrier
s_endpgm
.Lwo_w4:
s_add_u32 m0, s48, 4096
s_nop 0
global_load_lds_dwordx4 v0, s[40:41] offset:-4096
global_load_lds_dwordx4 v1, s[40:41] offset:-3072
global_load_lds_dwordx4 v2, s[40:41] offset:-2048
global_load_lds_dwordx4 v3, s[40:41] offset:-1024
global_load_lds_dwordx4 v4, s[40:41] offset:0
global_load_lds_dwordx4 v5, s[40:41] offset:1024
global_load_lds_dwordx4 v6, s[40:41] offset:2048
global_load_lds_dwordx4 v7, s[40:41] offset:3072
s_add_u32 m0, s48, 12288
s_nop 0
global_load_lds_dwordx4 v8, s[40:41] offset:-4096
s_add_u32 s40, s40, 73728
s_addc_u32 s41, s41, 0
s_add_u32 m0, s48, 40960
s_nop 0
global_load_lds_dwordx4 v0, s[40:41] offset:-4096
global_load_lds_dwordx4 v1, s[40:41] offset:-3072
global_load_lds_dwordx4 v2, s[40:41] offset:-2048
global_load_lds_dwordx4 v3, s[40:41] offset:-1024
global_load_lds_dwordx4 v4, s[40:41] offset:0
global_load_lds_dwordx4 v5, s[40:41] offset:1024
global_load_lds_dwordx4 v6, s[40:41] offset:2048
global_load_lds_dwordx4 v7, s[40:41] offset:3072
s_add_u32 m0, s48, 49152
s_nop 0
global_load_lds_dwordx4 v8, s[40:41] offset:-4096
s_add_u32 s40, s40, 73728
s_addc_u32 s41, s41, 0
s_add_u32 m0, s48, 77824
s_nop 0
global_load_lds_dwordx4 v0, s[40:41] offset:-4096
global_load_lds_dwordx4 v1, s[40:41] offset:-3072
global_load_lds_dwordx4 v2, s[40:41] offset:-2048
global_load_lds_dwordx4 v3, s[40:41] offset:-1024
global_load_lds_dwordx4 v4, s[40:41] offset:0
global_load_lds_dwordx4 v5, s[40:41] offset:1024
global_load_lds_dwordx4 v6, s[40:41] offset:2048
global_load_lds_dwordx4 v7, s[40:41] offset:3072
s_add_u32 m0, s48, 86016
s_nop 0
global_load_lds_dwordx4 v8, s[40:41] offset:-4096
s_add_u32 s40, s40, 73728
s_addc_u32 s41, s41, 0
v_add_u32 v204, 0x10000, v9
v_add_u32 v205, 0x20000, v9
v_add_u32 v206, 0x10000, v10
v_add_u32 v207, 0x20000, v10
s_waitcnt lgkmcnt(0)
s_mul_i32 s37, s37, 0x9E3779B1
s_add_u32 s37, s37, 0x7F4A7C15
v_add_u32 v22, s37, v22
v_add_u32 v24, s37, v24
s_waitcnt vmcnt(18)
s_barrier
s_add_u32 m0, s48, 114688
ds_read_b64_tr_b16 a[0:1], v9 offset:0
ds_read_b64_tr_b16 a[2:3], v9 offset:2304
global_load_lds_dwordx4 v0, s[40:41] offset:-4096
ds_read_b64_tr_b16 a[4:5], v9 offset:8
global_load_lds_dwordx4 v1, s[40:41] offset:-3072
ds_read_b64_tr_b16 a[6:7], v9 offset:2312
global_load_lds_dwordx4 v2, s[40:41] offset:-2048
ds_read_b64_tr_b16 a[8:9], v9 offset:64
global_load_lds_dwordx4 v3, s[40:41] offset:-1024
ds_read_b64_tr_b16 a[10:11], v9 offset:2368
ds_read_b64_tr_b16 a[12:13], v9 offset:72
global_load_lds_dwordx4 v4, s[40:41] offset:0
ds_read_b64_tr_b16 a[14:15], v9 offset:2376
global_load_lds_dwordx4 v5, s[40:41] offset:1024
ds_read_b64_tr_b16 a[16:17], v9 offset:18432
global_load_lds_dwordx4 v6, s[40:41] offset:2048
ds_read_b64_tr_b16 a[18:19], v9 offset:20736
global_load_lds_dwordx4 v7, s[40:41] offset:3072
ds_read_b64_tr_b16 a[20:21], v9 offset:18440
ds_read_b64_tr_b16 a[22:23], v9 offset:20744
s_add_u32 m0, s48, 122880
ds_read_b64_tr_b16 a[24:25], v9 offset:18496
global_load_lds_dwordx4 v8, s[40:41] offset:-4096
ds_read_b64_tr_b16 a[26:27], v9 offset:20800
s_add_u32 s40, s40, 73728
ds_read_b64_tr_b16 a[28:29], v9 offset:18504
s_addc_u32 s41, s41, 0
ds_read_b64_tr_b16 a[30:31], v9 offset:20808
s_waitcnt vmcnt(18) lgkmcnt(0)
s_barrier
s_add_u32 m0, s48, 4096
ds_read_b64_tr_b16 a[32:33], v9 offset:36864
ds_read_b64_tr_b16 a[34:35], v9 offset:39168
global_load_lds_dwordx4 v0, s[40:41] offset:-4096
ds_read_b64_tr_b16 a[36:37], v9 offset:36872
global_load_lds_dwordx4 v1, s[40:41] offset:-3072
ds_read_b64_tr_b16 a[38:39], v9 offset:39176
global_load_lds_dwordx4 v2, s[40:41] offset:-2048
ds_read_b64_tr_b16 a[40:41], v9 offset:36928
global_load_lds_dwordx4 v3, s[40:41] offset:-1024
ds_read_b64_tr_b16 a[42:43], v9 offset:39232
ds_read_b64_tr_b16 a[44:45], v9 offset:36936
global_load_lds_dwordx4 v4, s[40:41] offset:0
ds_read_b64_tr_b16 a[46:47], v9 offset:39240
global_load_lds_dwordx4 v5, s[40:41] offset:1024
ds_read_b64_tr_b16 a[48:49], v9 offset:55296
global_load_lds_dwordx4 v6, s[40:41] offset:2048
ds_read_b64_tr_b16 a[50:51], v9 offset:57600
global_load_lds_dwordx4 v7, s[40:41] offset:3072
ds_read_b64_tr_b16 a[52:53], v9 offset:55304
ds_read_b64_tr_b16 a[54:55], v9 offset:57608
s_add_u32 m0, s48, 12288
ds_read_b64_tr_b16 a[56:57], v9 offset:55360
global_load_lds_dwordx4 v8, s[40:41] offset:-4096
ds_read_b64_tr_b16 a[58:59], v9 offset:57664
s_add_u32 s40, s40, 73728
ds_read_b64_tr_b16 a[60:61], v9 offset:55368
s_addc_u32 s41, s41, 0
ds_read_b64_tr_b16 a[62:63], v9 offset:57672
s_waitcnt vmcnt(18) lgkmcnt(0)
s_barrier
s_add_u32 m0, s48, 40960
ds_read_b64_tr_b16 a[64:65], v204 offset:8192
ds_read_b64_tr_b16 a[66:67], v204 offset:10496
global_load_lds_dwordx4 v0, s[40:41] offset:-4096
ds_read_b64_tr_b16 a[68:69], v204 offset:8200
global_load_lds_dwordx4 v1, s[40:41] offset:-3072
ds_read_b64_tr_b16 a[70:71], v204 offset:10504
global_load_lds_dwordx4 v2, s[40:41] offset:-2048
ds_read_b64_tr_b16 a[72:73], v204 offset:8256
global_load_lds_dwordx4 v3, s[40:41] offset:-1024
ds_read_b64_tr_b16 a[74:75], v204 offset:10560
ds_read_b64_tr_b16 a[76:77], v204 offset:8264
global_load_lds_dwordx4 v4, s[40:41] offset:0
ds_read_b64_tr_b16 a[78:79], v204 offset:10568
global_load_lds_dwordx4 v5, s[40:41] offset:1024
ds_read_b64_tr_b16 a[80:81], v204 offset:26624
global_load_lds_dwordx4 v6, s[40:41] offset:2048
ds_read_b64_tr_b16 a[82:83], v204 offset:28928
global_load_lds_dwordx4 v7, s[40:41] offset:3072
ds_read_b64_tr_b16 a[84:85], v204 offset:26632
ds_read_b64_tr_b16 a[86:87], v204 offset:28936
s_add_u32 m0, s48, 49152
ds_read_b64_tr_b16 a[88:89], v204 offset:26688
global_load_lds_dwordx4 v8, s[40:41] offset:-4096
ds_read_b64_tr_b16 a[90:91], v204 offset:28992
s_add_u32 s40, s40, 73728
ds_read_b64_tr_b16 a[92:93], v204 offset:26696
s_addc_u32 s41, s41, 0
ds_read_b64_tr_b16 a[94:95], v204 offset:29000
s_waitcnt vmcnt(18) lgkmcnt(0)
s_barrier
s_add_u32 m0, s48, 77824
ds_read_b64_tr_b16 a[96:97], v204 offset:45056
ds_read_b64_tr_b16 a[98:99], v204 offset:47360
global_load_lds_dwordx4 v0, s[40:41] offset:-4096
ds_read_b64_tr_b16 a[100:101], v204 offset:45064
global_load_lds_dwordx4 v1, s[40:41] offset:-3072
ds_read_b64_tr_b16 a[102:103], v204 offset:47368
global_load_lds_dwordx4 v2, s[40:41] offset:-2048
ds_read_b64_tr_b16 a[104:105], v204 offset:45120
global_load_lds_dwordx4 v3, s[40:41] offset:-1024
ds_read_b64_tr_b16 a[106:107], v204 offset:47424
ds_read_b64_tr_b16 a[108:109], v204 offset:45128
global_load_lds_dwordx4 v4, s[40:41] offset:0
ds_read_b64_tr_b16 a[110:111], v204 offset:47432
global_load_lds_dwordx4 v5, s[40:41] offset:1024
ds_read_b64_tr_b16 a[112:113], v204 offset:63488
global_load_lds_dwordx4 v6, s[40:41] offset:2048
ds_read_b64_tr_b16 a[114:115], v205 offset:256
global_load_lds_dwordx4 v7, s[40:41] offset:3072
ds_read_b64_tr_b16 a[116:117], v204 offset:63496
ds_read_b64_tr_b16 a[118:119], v205 offset:264
s_add_u32 m0, s48, 86016
ds_read_b64_tr_b16 a[120:121], v204 offset:63552
global_load_lds_dwordx4 v8, s[40:41] offset:-4096
ds_read_b64_tr_b16 a[122:123], v205 offset:320
s_add_u32 s40, s40, 73728
ds_read_b64_tr_b16 a[124:125], v204 offset:63560
s_addc_u32 s41, s41, 0
ds_read_b64_tr_b16 a[126:127], v205 offset:328
s_waitcnt vmcnt(18) lgkmcnt(0)
s_barrier
s_add_u32 m0, s48, 114688
ds_read_b64_tr_b16 a[128:129], v9 offset:0
ds_read_b64_tr_b16 a[130:131], v9 offset:2304
global_load_lds_dwordx4 v0, s[40:41] offset:-4096
ds_read_b64_tr_b16 a[132:133], v9 offset:8
global_load_lds_dwordx4 v1, s[40:41] offset:-3072
ds_read_b64_tr_b16 a[134:135], v9 offset:2312
ds_read_b64_tr_b16 a[136:137], v9 offset:64
global_load_lds_dwordx4 v2, s[40:41] offset:-2048
ds_read_b64_tr_b16 a[138:139], v9 offset:2368
global_load_lds_dwordx4 v3, s[40:41] offset:-1024
ds_read_b64_tr_b16 a[140:141], v9 offset:72
global_load_lds_dwordx4 v4, s[40:41] offset:0
ds_read_b64_tr_b16 a[142:143], v9 offset:2376
ds_read_b64_tr_b16 a[144:145], v9 offset:18432
global_load_lds_dwordx4 v5, s[40:41] offset:1024
ds_read_b64_tr_b16 a[146:147], v9 offset:20736
global_load_lds_dwordx4 v6, s[40:41] offset:2048
ds_read_b64_tr_b16 a[148:149], v9 offset:18440
ds_read_b64_tr_b16 a[150:151], v9 offset:20744
global_load_lds_dwordx4 v7, s[40:41] offset:3072
ds_read_b64_tr_b16 a[152:153], v9 offset:18496
s_add_u32 m0, s48, 122880
ds_read_b64_tr_b16 a[154:155], v9 offset:20800
global_load_lds_dwordx4 v8, s[40:41] offset:-4096
ds_read_b64_tr_b16 a[156:157], v9 offset:18504
ds_read_b64_tr_b16 a[158:159], v9 offset:20808
s_add_u32 s40, s40, 73728
ds_read_b64_tr_b16 v[60:61], v10 offset:18432
s_addc_u32 s41, s41, 0
ds_read_b64_tr_b16 v[62:63], v10 offset:20736
s_waitcnt vmcnt(18) lgkmcnt(0)
s_barrier
s_add_u32 m0, s48, 4096
ds_read_b64_tr_b16 a[160:161], v9 offset:36864
ds_read_b64_tr_b16 a[162:163], v9 offset:39168
global_load_lds_dwordx4 v0, s[40:41] offset:-4096
ds_read_b64_tr_b16 a[164:165], v9 offset:36872
ds_read_b64_tr_b16 a[166:167], v9 offset:39176
global_load_lds_dwordx4 v1, s[40:41] offset:-3072
ds_read_b64_tr_b16 a[168:169], v9 offset:36928
global_load_lds_dwordx4 v2, s[40:41] offset:-2048
ds_read_b64_tr_b16 a[170:171], v9 offset:39232
ds_read_b64_tr_b16 a[172:173], v9 offset:36936
global_load_lds_dwordx4 v3, s[40:41] offset:-1024
ds_read_b64_tr_b16 a[174:175], v9 offset:39240
global_load_lds_dwordx4 v4, s[40:41] offset:0
ds_read_b64_tr_b16 v[64:65], v10 offset:36864
ds_read_b64_tr_b16 v[66:67], v10 offset:39168
global_load_lds_dwordx4 v5, s[40:41] offset:1024
ds_read_b64_tr_b16 a[176:177], v9 offset:55296
global_load_lds_dwordx4 v6, s[40:41] offset:2048
ds_read_b64_tr_b16 a[178:179], v9 offset:57600
ds_read_b64_tr_b16 a[180:181], v9 offset:55304
global_load_lds_dwordx4 v7, s[40:41] offset:3072
ds_read_b64_tr_b16 a[182:183], v9 offset:57608
s_add_u32 m0, s48, 12288
ds_read_b64_tr_b16 a[184:185], v9 offset:55360
ds_read_b64_tr_b16 a[186:187], v9 offset:57664
global_load_lds_dwordx4 v8, s[40:41] offset:-4096
ds_read_b64_tr_b16 a[188:189], v9 offset:55368
s_sub_u32 s40, s40, 589824
ds_read_b64_tr_b16 a[190:191], v9 offset:57672
ds_read_b64_tr_b16 v[68:69], v10 offset:55296
s_subb_u32 s41, s41, 0
ds_read_b64_tr_b16 v[70:71], v10 offset:57600
s_waitcnt vmcnt(18) lgkmcnt(0)
s_barrier
ds_read_b64_tr_b16 a[192:193], v204 offset:8192
ds_read_b64_tr_b16 a[194:195], v204 offset:10496
ds_read_b64_tr_b16 a[196:197], v204 offset:8200
ds_read_b64_tr_b16 a[198:199], v204 offset:10504
ds_read_b64_tr_b16 a[200:201], v204 offset:8256
ds_read_b64_tr_b16 a[202:203], v204 offset:10560
ds_read_b64_tr_b16 a[204:205], v204 offset:8264
ds_read_b64_tr_b16 a[206:207], v204 offset:10568
ds_read_b64_tr_b16 v[72:73], v206 offset:8192
ds_read_b64_tr_b16 v[74:75], v206 offset:10496
ds_read_b64_tr_b16 a[208:209], v204 offset:26624
ds_read_b64_tr_b16 a[210:211], v204 offset:28928
ds_read_b64_tr_b16 a[212:213], v204 offset:26632
ds_read_b64_tr_b16 a[214:215], v204 offset:28936
ds_read_b64_tr_b16 a[216:217], v204 offset:26688
ds_read_b64_tr_b16 a[218:219], v204 offset:28992
ds_read_b64_tr_b16 a[220:221], v204 offset:26696
ds_read_b64_tr_b16 a[222:223], v204 offset:29000
ds_read_b64_tr_b16 v[76:77], v206 offset:26624
ds_read_b64_tr_b16 v[78:79], v206 offset:28928
s_waitcnt vmcnt(9) lgkmcnt(0)
s_barrier
ds_read_b64_tr_b16 a[224:225], v204 offset:45056
ds_read_b64_tr_b16 a[226:227], v204 offset:47360
ds_read_b64_tr_b16 a[228:229], v204 offset:45064
ds_read_b64_tr_b16 a[230:231], v204 offset:47368
ds_read_b64_tr_b16 a[232:233], v204 offset:45120
ds_read_b64_tr_b16 a[234:235], v204 offset:47424
)"
R"(ds_read_b64_tr_b16 a[236:237], v204 offset:45128
ds_read_b64_tr_b16 a[238:239], v204 offset:47432
ds_read_b64_tr_b16 v[80:81], v206 offset:45056
ds_read_b64_tr_b16 v[82:83], v206 offset:47360
ds_read_b64_tr_b16 a[240:241], v204 offset:63488
ds_read_b64_tr_b16 a[242:243], v205 offset:256
ds_read_b64_tr_b16 a[244:245], v204 offset:63496
ds_read_b64_tr_b16 a[246:247], v205 offset:264
ds_read_b64_tr_b16 a[248:249], v204 offset:63552
ds_read_b64_tr_b16 a[250:251], v205 offset:320
ds_read_b64_tr_b16 a[252:253], v204 offset:63560
ds_read_b64_tr_b16 a[254:255], v205 offset:328
ds_read_b64_tr_b16 v[84:85], v206 offset:63488
ds_read_b64_tr_b16 v[86:87], v207 offset:256
s_waitcnt vmcnt(0) lgkmcnt(0)
s_barrier
ds_read_b64_tr_b16 v[28:29], v9 offset:0
ds_read_b64_tr_b16 v[30:31], v9 offset:2304
ds_read_b64_tr_b16 v[32:33], v9 offset:8
ds_read_b64_tr_b16 v[34:35], v9 offset:2312
ds_read_b64_tr_b16 v[36:37], v9 offset:64
ds_read_b64_tr_b16 v[38:39], v9 offset:2368
ds_read_b64_tr_b16 v[40:41], v9 offset:72
ds_read_b64_tr_b16 v[42:43], v9 offset:2376
ds_read_b64_tr_b16 v[88:89], v10 offset:0
ds_read_b64_tr_b16 v[90:91], v10 offset:2304
ds_read_b64_tr_b16 v[44:45], v9 offset:18432
ds_read_b64_tr_b16 v[46:47], v9 offset:20736
ds_read_b64_tr_b16 v[48:49], v9 offset:18440
ds_read_b64_tr_b16 v[50:51], v9 offset:20744
ds_read_b64_tr_b16 v[52:53], v9 offset:18496
ds_read_b64_tr_b16 v[54:55], v9 offset:20800
ds_read_b64_tr_b16 v[56:57], v9 offset:18504
ds_read_b64_tr_b16 v[58:59], v9 offset:20808
ds_read_b64_tr_b16 v[92:93], v10 offset:18432
ds_read_b64_tr_b16 v[94:95], v10 offset:20736
s_waitcnt lgkmcnt(0)
s_barrier
v_add_lshl_u32 v252, v24, 0, 7
v_bfi_b32 v252, s69, v252, v23
v_add_lshl_u32 v253, v22, 0, 7
v_bfi_b32 v253, s69, v253, v21
s_mov_b64 s[76:77], 0
s_add_u32 m0, s49, 18432
s_mov_b32 s70, 33
s_waitcnt vmcnt(0) lgkmcnt(0)
s_nop 7
.p2align 6
.Lwo_top15:
s_waitcnt lgkmcnt(5)
v_mfma_f32_16x16x32_bf16 v[96:99], a[144:147], v[132:135], v[96:99]
s_waitcnt lgkmcnt(4)
ds_read_b128 v[144:147], v25 offset:12288
v_add_f32 v176, v176, v184
v_mfma_f32_16x16x32_bf16 v[100:103], a[148:151], v[132:135], v[100:103]
v_mfma_f32_16x16x32_bf16 v[104:107], a[152:155], v[132:135], v[104:107]
s_waitcnt vmcnt(6)
s_barrier
v_mfma_f32_16x16x32_bf16 v[108:111], a[156:159], v[132:135], v[108:111]
v_add_f32 v177, v177, v185
v_add_f32 v178, v178, v186
v_mfma_f32_16x16x32_bf16 v[128:131], v[60:63], v[132:135], 0
s_waitcnt lgkmcnt(3)
v_add_f32 v179, v179, v187
v_add_f32 v176, v176, v188
v_mfma_f32_16x16x32_bf16 v[96:99], a[160:163], v[136:139], v[96:99]
ds_read_b128 v[132:135], v25 offset:13312
v_add_f32 v177, v177, v189
v_mfma_f32_16x16x32_bf16 v[100:103], a[164:167], v[136:139], v[100:103]
v_add_f32 v178, v178, v190
v_add_f32 v179, v179, v191
v_mfma_f32_16x16x32_bf16 v[104:107], a[168:171], v[136:139], v[104:107]
s_waitcnt lgkmcnt(3)
v_add_f32 v180, v180, v192
v_add_f32 v181, v181, v193
v_mfma_f32_16x16x32_bf16 v[108:111], a[172:175], v[136:139], v[108:111]
v_add_f32 v182, v182, v194
v_add_f32 v183, v183, v195
v_mfma_f32_16x16x32_bf16 v[128:131], v[64:67], v[136:139], v[128:131]
s_waitcnt lgkmcnt(2)
v_cvt_pk_bf16_f32 v172, v176, v177
v_cvt_pk_bf16_f32 v173, v178, v179
v_mfma_f32_16x16x32_bf16 v[96:99], a[176:179], v[140:143], v[96:99]
s_cmp_eq_u32 s70, 32
ds_read_b128 v[136:139], v25 offset:14336
v_cvt_pk_bf16_f32 v174, v180, v181
v_mfma_f32_16x16x32_bf16 v[100:103], a[180:183], v[140:143], v[100:103]
s_cselect_b64 s[42:43], s[44:45], s[42:43]
v_cvt_pk_bf16_f32 v175, v182, v183
v_cvt_pk_bf16_f32 v164, v112, v113
v_mfma_f32_16x16x32_bf16 v[104:107], a[184:187], v[140:143], v[104:107]
s_cmp_eq_u32 s70, 16
global_store_dwordx4 v14, v[172:175], s[38:39] sc1
v_mfma_f32_16x16x32_bf16 v[108:111], a[188:191], v[140:143], v[108:111]
s_cselect_b64 s[42:43], s[46:47], s[42:43]
v_add_u32 v14, 18688, v14
v_cvt_pk_bf16_f32 v165, v114, v115
v_mfma_f32_16x16x32_bf16 v[128:131], v[68:71], v[140:143], v[128:131]
s_waitcnt lgkmcnt(2)
v_cvt_pk_bf16_f32 v166, v116, v117
v_cvt_pk_bf16_f32 v167, v118, v119
v_mfma_f32_16x16x32_bf16 v[96:99], a[192:195], v[144:147], v[96:99]
s_xor_b32 s49, s49, 0x10000
ds_read_b128 v[140:143], v25 offset:15360
v_add_f32 v254, v112, v113
v_mfma_f32_16x16x32_bf16 v[100:103], a[196:199], v[144:147], v[100:103]
s_mov_b32 m0, 53248
global_store_dwordx4 v13, v[164:167], s[38:39] offset:0 sc1
v_mfma_f32_16x16x32_bf16 v[104:107], a[200:203], v[144:147], v[104:107]
v_cvt_pk_bf16_f32 v168, v120, v121
v_cvt_pk_bf16_f32 v169, v122, v123
v_mfma_f32_16x16x32_bf16 v[108:111], a[204:207], v[144:147], v[108:111]
v_cvt_pk_bf16_f32 v170, v124, v125
v_cvt_pk_bf16_f32 v171, v126, v127
v_mfma_f32_16x16x32_bf16 v[128:131], v[72:75], v[144:147], v[128:131]
s_waitcnt lgkmcnt(2)
global_store_dwordx4 v13, v[168:171], s[38:39] offset:1024 sc1
v_mfma_f32_16x16x32_bf16 v[96:99], a[208:211], v[132:135], v[96:99]
ds_read_b128 v[144:147], v25 offset:16384
v_add_u32 v13, 18688, v13
v_mfma_f32_16x16x32_bf16 v[100:103], a[212:215], v[132:135], v[100:103]
v_mul_f32 v255, v112, v112
v_fmac_f32 v255, v113, v113
v_mfma_f32_16x16x32_bf16 v[104:107], a[216:219], v[132:135], v[104:107]
v_add_f32 v254, v254, v114
v_fmac_f32 v255, v114, v114
v_mfma_f32_16x16x32_bf16 v[108:111], a[220:223], v[132:135], v[108:111]
v_add_f32 v254, v254, v115
v_fmac_f32 v255, v115, v115
v_mfma_f32_16x16x32_bf16 v[128:131], v[76:79], v[132:135], v[128:131]
s_waitcnt vmcnt(7)
global_load_lds_dwordx4 v16, s[42:43] offset:0 sc1
ds_read_b128 v[148:151], v19 offset:8192
s_waitcnt lgkmcnt(3)
v_mfma_f32_16x16x32_bf16 v[96:99], a[224:227], v[136:139], v[96:99]
ds_read_b128 v[132:135], v25 offset:17408
ds_read_b128 v[152:155], v20 offset:8192
v_mfma_f32_16x16x32_bf16 v[100:103], a[228:231], v[136:139], v[100:103]
v_add_f32 v254, v254, v116
v_fmac_f32 v255, v116, v116
v_mfma_f32_16x16x32_bf16 v[104:107], a[232:235], v[136:139], v[104:107]
v_add_f32 v254, v254, v117
v_fmac_f32 v255, v117, v117
v_mfma_f32_16x16x32_bf16 v[108:111], a[236:239], v[136:139], v[108:111]
v_add_f32 v254, v254, v118
v_fmac_f32 v255, v118, v118
v_mfma_f32_16x16x32_bf16 v[128:131], v[80:83], v[136:139], v[128:131]
s_waitcnt lgkmcnt(4)
v_add_f32 v254, v254, v119
v_fmac_f32 v255, v119, v119
v_mfma_f32_16x16x32_bf16 v[96:99], a[240:243], v[140:143], v[96:99]
ds_read_b128 v[136:139], v25 offset:0
v_add_f32 v254, v254, v120
v_mfma_f32_16x16x32_bf16 v[100:103], a[244:247], v[140:143], v[100:103]
global_load_lds_dwordx4 v17, s[42:43] offset:1024 sc1
v_fmac_f32 v255, v120, v120
v_mfma_f32_16x16x32_bf16 v[104:107], a[248:251], v[140:143], v[104:107]
v_add_f32 v254, v254, v121
v_fmac_f32 v255, v121, v121
v_mfma_f32_16x16x32_bf16 v[108:111], a[252:255], v[140:143], v[108:111]
v_add_f32 v254, v254, v122
v_fmac_f32 v255, v122, v122
v_mfma_f32_16x16x32_bf16 v[128:131], v[84:87], v[140:143], v[128:131]
s_waitcnt lgkmcnt(4)
v_add_f32 v254, v254, v123
v_fmac_f32 v255, v123, v123
v_mfma_f32_16x16x32_bf16 v[96:99], v[28:31], v[144:147], v[96:99]
ds_read_b128 v[140:143], v25 offset:1024
v_add_f32 v254, v254, v124
v_mfma_f32_16x16x32_bf16 v[100:103], v[32:35], v[144:147], v[100:103]
s_add_u32 m0, s49, 0
v_fmac_f32 v255, v124, v124
v_add_f32 v254, v254, v125
v_mfma_f32_16x16x32_bf16 v[104:107], v[36:39], v[144:147], v[104:107]
v_fmac_f32 v255, v125, v125
v_add_f32 v254, v254, v126
v_mfma_f32_16x16x32_bf16 v[108:111], v[40:43], v[144:147], v[108:111]
v_fmac_f32 v255, v126, v126
v_add_f32 v254, v254, v127
v_mfma_f32_16x16x32_bf16 v[128:131], v[88:91], v[144:147], v[128:131]
s_waitcnt lgkmcnt(3)
v_fmac_f32 v255, v127, v127
v_add_f32 v254, v254, v176
v_mfma_f32_16x16x32_bf16 v[96:99], v[44:47], v[132:135], v[96:99]
ds_read_b128 v[144:147], v25 offset:2048
v_fmac_f32 v255, v176, v176
v_mfma_f32_16x16x32_bf16 v[100:103], v[48:51], v[132:135], v[100:103]
v_add_f32 v254, v254, v177
v_fmac_f32 v255, v177, v177
v_mfma_f32_16x16x32_bf16 v[104:107], v[52:55], v[132:135], v[104:107]
v_add_f32 v254, v254, v178
v_fmac_f32 v255, v178, v178
v_mfma_f32_16x16x32_bf16 v[108:111], v[56:59], v[132:135], v[108:111]
v_add_f32 v254, v254, v179
v_fmac_f32 v255, v179, v179
v_mfma_f32_16x16x32_bf16 v[128:131], v[92:95], v[132:135], v[128:131]
s_waitcnt lgkmcnt(2)
v_add_f32 v254, v254, v180
v_fmac_f32 v255, v180, v180
v_mfma_f32_16x16x32_bf16 v[96:99], a[0:3], v[136:139], v[96:99]
ds_read_b128 v[132:135], v25 offset:3072
v_add_f32 v254, v254, v181
v_mfma_f32_16x16x32_bf16 v[100:103], a[4:7], v[136:139], v[100:103]
v_fmac_f32 v255, v181, v181
v_add_f32 v254, v254, v182
v_mfma_f32_16x16x32_bf16 v[104:107], a[8:11], v[136:139], v[104:107]
v_fmac_f32 v255, v182, v182
v_add_f32 v254, v254, v183
v_mfma_f32_16x16x32_bf16 v[108:111], a[12:15], v[136:139], v[108:111]
s_waitcnt lgkmcnt(2)
v_fmac_f32 v255, v183, v183
v_mfma_f32_16x16x32_bf16 v[96:99], a[16:19], v[140:143], v[96:99]
ds_read_b128 v[136:139], v25 offset:4096
v_mfma_f32_16x16x32_bf16 v[100:103], a[20:23], v[140:143], v[100:103]
ds_write_b64 v27, v[254:255] offset:46592
v_mfma_f32_16x16x32_bf16 v[104:107], a[24:27], v[140:143], v[104:107]
v_lshlrev_b32 v176, 16, v156
v_and_b32 v177, 0xffff0000, v156
v_mfma_f32_16x16x32_bf16 v[108:111], a[28:31], v[140:143], v[108:111]
s_waitcnt lgkmcnt(3)
v_lshlrev_b32 v178, 16, v157
v_and_b32 v179, 0xffff0000, v157
v_mfma_f32_16x16x32_bf16 v[96:99], a[32:35], v[144:147], v[96:99]
ds_read_b128 v[140:143], v25 offset:5120
v_lshlrev_b32 v180, 16, v158
v_mfma_f32_16x16x32_bf16 v[100:103], a[36:39], v[144:147], v[100:103]
v_mfma_f32_16x16x32_bf16 v[104:107], a[40:43], v[144:147], v[104:107]
s_barrier
v_mfma_f32_16x16x32_bf16 v[108:111], a[44:47], v[144:147], v[108:111]
s_waitcnt lgkmcnt(3)
v_and_b32 v181, 0xffff0000, v158
v_lshlrev_b32 v182, 16, v159
v_mfma_f32_16x16x32_bf16 v[96:99], a[48:51], v[132:135], v[96:99]
ds_read_b128 v[144:147], v25 offset:6144
global_load_lds_dwordx4 v11, s[38:39] offset:-2048 sc1
v_mfma_f32_16x16x32_bf16 v[100:103], a[52:55], v[132:135], v[100:103]
v_and_b32 v183, 0xffff0000, v159
v_add_f32 v180, v180, v128
v_mfma_f32_16x16x32_bf16 v[104:107], a[56:59], v[132:135], v[104:107]
v_add_f32 v181, v181, v129
v_add_f32 v182, v182, v130
v_mfma_f32_16x16x32_bf16 v[108:111], a[60:63], v[132:135], v[108:111]
s_waitcnt lgkmcnt(3)
global_load_lds_dwordx4 v11, s[38:39] offset:-1024 sc1
v_add_f32 v183, v183, v131
v_mfma_f32_16x16x32_bf16 v[96:99], a[64:67], v[136:139], v[96:99]
ds_read_b128 v[132:135], v25 offset:7168
global_load_dwordx4 v[156:159], v18, s[42:43] sc1
v_mfma_f32_16x16x32_bf16 v[100:103], a[68:71], v[136:139], v[100:103]
s_sub_u32 s42, s42, 18432
v_lshlrev_b32 v112, 16, v148
v_and_b32 v113, 0xffff0000, v148
v_mfma_f32_16x16x32_bf16 v[104:107], a[72:75], v[136:139], v[104:107]
s_subb_u32 s43, s43, 0
global_load_lds_dwordx4 v11, s[38:39] offset:0 sc1
v_add_u32 v11, 18432, v11
v_mfma_f32_16x16x32_bf16 v[108:111], a[76:79], v[136:139], v[108:111]
s_waitcnt lgkmcnt(2)
v_min_u32 v11, v11, v12
v_lshlrev_b32 v114, 16, v149
v_mfma_f32_16x16x32_bf16 v[96:99], a[80:83], v[140:143], v[96:99]
ds_read_b128 v[136:139], v25 offset:8192
v_and_b32 v115, 0xffff0000, v149
v_mfma_f32_16x16x32_bf16 v[100:103], a[84:87], v[140:143], v[100:103]
v_lshlrev_b32 v116, 16, v150
v_and_b32 v117, 0xffff0000, v150
v_mfma_f32_16x16x32_bf16 v[104:107], a[88:91], v[140:143], v[104:107]
v_lshlrev_b32 v118, 16, v151
v_and_b32 v119, 0xffff0000, v151
v_mfma_f32_16x16x32_bf16 v[108:111], a[92:95], v[140:143], v[108:111]
s_waitcnt lgkmcnt(2)
v_lshlrev_b32 v120, 16, v152
v_and_b32 v121, 0xffff0000, v152
v_mfma_f32_16x16x32_bf16 v[96:99], a[96:99], v[144:147], v[96:99]
v_lshlrev_b32 v122, 16, v153
v_and_b32 v123, 0xffff0000, v153
v_mfma_f32_16x16x32_bf16 v[100:103], a[100:103], v[144:147], v[100:103]
v_mfma_f32_16x16x32_bf16 v[104:107], a[104:107], v[144:147], v[104:107]
s_waitcnt vmcnt(9)
s_barrier
v_mfma_f32_16x16x32_bf16 v[108:111], a[108:111], v[144:147], v[108:111]
s_waitcnt lgkmcnt(1)
ds_read_b128 v[140:143], v25 offset:27648
ds_read_b128 v[184:187], v26 offset:36864
v_mfma_f32_16x16x32_bf16 v[96:99], a[112:115], v[132:135], v[96:99]
ds_read_b128 v[144:147], v25 offset:28672
ds_read_b128 v[188:191], v26 offset:37888
v_mfma_f32_16x16x32_bf16 v[100:103], a[116:119], v[132:135], v[100:103]
ds_read_b128 v[192:195], v26 offset:38912
v_lshlrev_b32 v124, 16, v154
v_mfma_f32_16x16x32_bf16 v[104:107], a[120:123], v[132:135], v[104:107]
v_and_b32 v125, 0xffff0000, v154
v_lshlrev_b32 v126, 16, v155
v_mfma_f32_16x16x32_bf16 v[108:111], a[124:127], v[132:135], v[108:111]
s_waitcnt lgkmcnt(5)
v_and_b32 v127, 0xffff0000, v155
v_mfma_f32_16x16x32_bf16 v[96:99], a[128:131], v[136:139], v[96:99]
ds_read_b128 v[132:135], v25 offset:29696
v_mfma_f32_16x16x32_bf16 v[100:103], a[132:135], v[136:139], v[100:103]
v_mfma_f32_16x16x32_bf16 v[104:107], a[136:139], v[136:139], v[104:107]
s_waitcnt lgkmcnt(4)
v_add_f32 v176, v176, v184
v_add_f32 v177, v177, v185
v_mfma_f32_16x16x32_bf16 v[108:111], a[140:143], v[136:139], v[108:111]
v_add_f32 v178, v178, v186
v_add_f32 v179, v179, v187
v_mfma_f32_16x16x32_bf16 v[112:115], a[144:147], v[140:143], v[112:115]
s_waitcnt lgkmcnt(2)
ds_read_b128 v[136:139], v25 offset:30720
v_add_f32 v176, v176, v188
v_mfma_f32_16x16x32_bf16 v[116:119], a[148:151], v[140:143], v[116:119]
v_mfma_f32_16x16x32_bf16 v[120:123], a[152:155], v[140:143], v[120:123]
s_waitcnt vmcnt(6)
s_barrier
v_mfma_f32_16x16x32_bf16 v[124:127], a[156:159], v[140:143], v[124:127]
v_add_f32 v177, v177, v189
v_add_f32 v178, v178, v190
v_mfma_f32_16x16x32_bf16 v[128:131], v[60:63], v[140:143], 0
s_waitcnt lgkmcnt(2)
v_add_f32 v179, v179, v191
v_add_f32 v180, v180, v192
v_mfma_f32_16x16x32_bf16 v[112:115], a[160:163], v[144:147], v[112:115]
ds_read_b128 v[140:143], v25 offset:31744
v_add_f32 v181, v181, v193
v_mfma_f32_16x16x32_bf16 v[116:119], a[164:167], v[144:147], v[116:119]
v_add_f32 v182, v182, v194
v_add_f32 v183, v183, v195
v_mfma_f32_16x16x32_bf16 v[120:123], a[168:171], v[144:147], v[120:123]
v_cvt_pk_bf16_f32 v172, v176, v177
v_cvt_pk_bf16_f32 v173, v178, v179
v_mfma_f32_16x16x32_bf16 v[124:127], a[172:175], v[144:147], v[124:127]
v_cvt_pk_bf16_f32 v174, v180, v181
v_cvt_pk_bf16_f32 v175, v182, v183
v_mfma_f32_16x16x32_bf16 v[128:131], v[64:67], v[144:147], v[128:131]
s_waitcnt lgkmcnt(2)
global_store_dwordx4 v14, v[172:175], s[38:39] sc1
v_mfma_f32_16x16x32_bf16 v[112:115], a[176:179], v[132:135], v[112:115]
s_mov_b32 m0, 61440
ds_read_b128 v[144:147], v25 offset:32768
v_add_u32 v14, 18688, v14
v_mfma_f32_16x16x32_bf16 v[116:119], a[180:183], v[132:135], v[116:119]
v_cvt_pk_bf16_f32 v164, v96, v97
v_cvt_pk_bf16_f32 v165, v98, v99
v_mfma_f32_16x16x32_bf16 v[120:123], a[184:187], v[132:135], v[120:123]
v_cvt_pk_bf16_f32 v166, v100, v101
v_cvt_pk_bf16_f32 v167, v102, v103
v_mfma_f32_16x16x32_bf16 v[124:127], a[188:191], v[132:135], v[124:127]
global_store_dwordx4 v13, v[164:167], s[38:39] offset:0 sc1
v_mfma_f32_16x16x32_bf16 v[128:131], v[68:71], v[132:135], v[128:131]
s_waitcnt lgkmcnt(2)
v_cvt_pk_bf16_f32 v168, v104, v105
v_cvt_pk_bf16_f32 v169, v106, v107
v_mfma_f32_16x16x32_bf16 v[112:115], a[192:195], v[136:139], v[112:115]
ds_read_b128 v[132:135], v25 offset:33792
v_cvt_pk_bf16_f32 v170, v108, v109
v_mfma_f32_16x16x32_bf16 v[116:119], a[196:199], v[136:139], v[116:119]
v_cvt_pk_bf16_f32 v171, v110, v111
v_add_f32 v254, v96, v97
v_mfma_f32_16x16x32_bf16 v[120:123], a[200:203], v[136:139], v[120:123]
global_store_dwordx4 v13, v[168:171], s[38:39] offset:1024 sc1
v_mfma_f32_16x16x32_bf16 v[124:127], a[204:207], v[136:139], v[124:127]
v_add_u32 v13, 18688, v13
v_mul_f32 v255, v96, v96
v_mfma_f32_16x16x32_bf16 v[128:131], v[72:75], v[136:139], v[128:131]
s_waitcnt lgkmcnt(2)
v_fmac_f32 v255, v97, v97
v_add_f32 v254, v254, v98
v_mfma_f32_16x16x32_bf16 v[112:115], a[208:211], v[140:143], v[112:115]
ds_read_b128 v[136:139], v25 offset:34816
v_fmac_f32 v255, v98, v98
v_mfma_f32_16x16x32_bf16 v[116:119], a[212:215], v[140:143], v[116:119]
v_add_f32 v254, v254, v99
v_fmac_f32 v255, v99, v99
v_mfma_f32_16x16x32_bf16 v[120:123], a[216:219], v[140:143], v[120:123]
v_add_f32 v254, v254, v100
v_fmac_f32 v255, v100, v100
v_mfma_f32_16x16x32_bf16 v[124:127], a[220:223], v[140:143], v[124:127]
v_add_f32 v254, v254, v101
v_fmac_f32 v255, v101, v101
v_mfma_f32_16x16x32_bf16 v[128:131], v[76:79], v[140:143], v[128:131]
s_waitcnt vmcnt(7)
global_load_lds_dwordx4 v16, s[42:43] offset:0 sc1
ds_read_b128 v[148:151], v19 offset:0
s_waitcnt lgkmcnt(3)
v_mfma_f32_16x16x32_bf16 v[112:115], a[224:227], v[144:147], v[112:115]
ds_read_b128 v[140:143], v25 offset:35840
ds_read_b128 v[152:155], v20 offset:0
v_mfma_f32_16x16x32_bf16 v[116:119], a[228:231], v[144:147], v[116:119]
v_add_f32 v254, v254, v102
v_fmac_f32 v255, v102, v102
v_mfma_f32_16x16x32_bf16 v[120:123], a[232:235], v[144:147], v[120:123]
v_add_f32 v254, v254, v103
v_fmac_f32 v255, v103, v103
v_mfma_f32_16x16x32_bf16 v[124:127], a[236:239], v[144:147], v[124:127]
v_add_f32 v254, v254, v104
v_fmac_f32 v255, v104, v104
v_mfma_f32_16x16x32_bf16 v[128:131], v[80:83], v[144:147], v[128:131]
s_waitcnt lgkmcnt(4)
v_add_f32 v254, v254, v105
v_fmac_f32 v255, v105, v105
)"
R"(v_mfma_f32_16x16x32_bf16 v[112:115], a[240:243], v[132:135], v[112:115]
ds_read_b128 v[144:147], v25 offset:18432
v_add_f32 v254, v254, v106
v_mfma_f32_16x16x32_bf16 v[116:119], a[244:247], v[132:135], v[116:119]
global_load_lds_dwordx4 v17, s[42:43] offset:1024 sc1
v_fmac_f32 v255, v106, v106
v_mfma_f32_16x16x32_bf16 v[120:123], a[248:251], v[132:135], v[120:123]
v_add_f32 v254, v254, v107
v_fmac_f32 v255, v107, v107
v_mfma_f32_16x16x32_bf16 v[124:127], a[252:255], v[132:135], v[124:127]
v_add_f32 v254, v254, v108
v_fmac_f32 v255, v108, v108
v_mfma_f32_16x16x32_bf16 v[128:131], v[84:87], v[132:135], v[128:131]
s_waitcnt lgkmcnt(4)
v_add_f32 v254, v254, v109
v_fmac_f32 v255, v109, v109
v_mfma_f32_16x16x32_bf16 v[112:115], v[28:31], v[136:139], v[112:115]
ds_read_b128 v[132:135], v25 offset:19456
v_add_f32 v254, v254, v110
v_mfma_f32_16x16x32_bf16 v[116:119], v[32:35], v[136:139], v[116:119]
s_add_u32 m0, s49, 18432
v_fmac_f32 v255, v110, v110
v_add_f32 v254, v254, v111
v_mfma_f32_16x16x32_bf16 v[120:123], v[36:39], v[136:139], v[120:123]
v_fmac_f32 v255, v111, v111
v_add_f32 v254, v254, v176
v_mfma_f32_16x16x32_bf16 v[124:127], v[40:43], v[136:139], v[124:127]
v_fmac_f32 v255, v176, v176
v_add_f32 v254, v254, v177
v_mfma_f32_16x16x32_bf16 v[128:131], v[88:91], v[136:139], v[128:131]
s_waitcnt lgkmcnt(3)
v_fmac_f32 v255, v177, v177
v_add_f32 v254, v254, v178
v_mfma_f32_16x16x32_bf16 v[112:115], v[44:47], v[140:143], v[112:115]
ds_read_b128 v[136:139], v25 offset:20480
v_fmac_f32 v255, v178, v178
v_mfma_f32_16x16x32_bf16 v[116:119], v[48:51], v[140:143], v[116:119]
v_add_f32 v254, v254, v179
v_fmac_f32 v255, v179, v179
v_mfma_f32_16x16x32_bf16 v[120:123], v[52:55], v[140:143], v[120:123]
v_add_f32 v254, v254, v180
v_fmac_f32 v255, v180, v180
v_mfma_f32_16x16x32_bf16 v[124:127], v[56:59], v[140:143], v[124:127]
v_add_f32 v254, v254, v181
v_fmac_f32 v255, v181, v181
v_mfma_f32_16x16x32_bf16 v[128:131], v[92:95], v[140:143], v[128:131]
s_waitcnt lgkmcnt(2)
v_add_f32 v254, v254, v182
v_fmac_f32 v255, v182, v182
v_mfma_f32_16x16x32_bf16 v[112:115], a[0:3], v[144:147], v[112:115]
ds_read_b128 v[140:143], v25 offset:21504
v_add_f32 v254, v254, v183
v_mfma_f32_16x16x32_bf16 v[116:119], a[4:7], v[144:147], v[116:119]
v_fmac_f32 v255, v183, v183
v_mfma_f32_16x16x32_bf16 v[120:123], a[8:11], v[144:147], v[120:123]
ds_write_b64 v27, v[254:255] offset:44544
v_mfma_f32_16x16x32_bf16 v[124:127], a[12:15], v[144:147], v[124:127]
s_waitcnt lgkmcnt(3)
v_lshlrev_b32 v176, 16, v160
v_and_b32 v177, 0xffff0000, v160
v_mfma_f32_16x16x32_bf16 v[112:115], a[16:19], v[132:135], v[112:115]
ds_read_b128 v[144:147], v25 offset:22528
v_lshlrev_b32 v178, 16, v161
v_mfma_f32_16x16x32_bf16 v[116:119], a[20:23], v[132:135], v[116:119]
v_and_b32 v179, 0xffff0000, v161
v_lshlrev_b32 v180, 16, v162
v_mfma_f32_16x16x32_bf16 v[120:123], a[24:27], v[132:135], v[120:123]
v_and_b32 v181, 0xffff0000, v162
v_lshlrev_b32 v182, 16, v163
v_mfma_f32_16x16x32_bf16 v[124:127], a[28:31], v[132:135], v[124:127]
s_waitcnt lgkmcnt(3)
v_and_b32 v183, 0xffff0000, v163
v_add_f32 v180, v180, v128
v_mfma_f32_16x16x32_bf16 v[112:115], a[32:35], v[136:139], v[112:115]
ds_read_b128 v[132:135], v25 offset:23552
v_add_f32 v181, v181, v129
v_mfma_f32_16x16x32_bf16 v[116:119], a[36:39], v[136:139], v[116:119]
v_mfma_f32_16x16x32_bf16 v[120:123], a[40:43], v[136:139], v[120:123]
s_barrier
v_mfma_f32_16x16x32_bf16 v[124:127], a[44:47], v[136:139], v[124:127]
s_waitcnt lgkmcnt(3)
v_add_f32 v182, v182, v130
v_add_f32 v183, v183, v131
v_mfma_f32_16x16x32_bf16 v[112:115], a[48:51], v[140:143], v[112:115]
s_mov_b64 s[76:77], -1
ds_read_b128 v[136:139], v25 offset:24576
global_load_lds_dwordx4 v11, s[38:39] offset:-2048 sc1
v_mfma_f32_16x16x32_bf16 v[116:119], a[52:55], v[140:143], v[116:119]
global_load_dwordx4 v[160:163], v18, s[42:43] sc1
v_lshlrev_b32 v96, 16, v148
v_mfma_f32_16x16x32_bf16 v[120:123], a[56:59], v[140:143], v[120:123]
s_sub_u32 s42, s42, 18432
v_and_b32 v97, 0xffff0000, v148
v_lshlrev_b32 v98, 16, v149
v_mfma_f32_16x16x32_bf16 v[124:127], a[60:63], v[140:143], v[124:127]
s_waitcnt lgkmcnt(2)
global_load_lds_dwordx4 v11, s[38:39] offset:-1024 sc1
v_and_b32 v99, 0xffff0000, v149
v_mfma_f32_16x16x32_bf16 v[112:115], a[64:67], v[144:147], v[112:115]
s_subb_u32 s43, s43, 0
ds_read_b128 v[140:143], v25 offset:25600
v_lshlrev_b32 v100, 16, v150
v_mfma_f32_16x16x32_bf16 v[116:119], a[68:71], v[144:147], v[116:119]
v_and_b32 v101, 0xffff0000, v150
v_lshlrev_b32 v102, 16, v151
v_mfma_f32_16x16x32_bf16 v[120:123], a[72:75], v[144:147], v[120:123]
global_load_lds_dwordx4 v11, s[38:39] offset:0 sc1
v_add_u32 v11, 18432, v11
v_mfma_f32_16x16x32_bf16 v[124:127], a[76:79], v[144:147], v[124:127]
s_waitcnt lgkmcnt(2)
v_min_u32 v11, v11, v12
v_and_b32 v103, 0xffff0000, v151
v_mfma_f32_16x16x32_bf16 v[112:115], a[80:83], v[132:135], v[112:115]
ds_read_b128 v[144:147], v25 offset:26624
v_lshlrev_b32 v104, 16, v152
v_mfma_f32_16x16x32_bf16 v[116:119], a[84:87], v[132:135], v[116:119]
v_and_b32 v105, 0xffff0000, v152
v_lshlrev_b32 v106, 16, v153
v_mfma_f32_16x16x32_bf16 v[120:123], a[88:91], v[132:135], v[120:123]
v_and_b32 v107, 0xffff0000, v153
v_lshlrev_b32 v108, 16, v154
v_mfma_f32_16x16x32_bf16 v[124:127], a[92:95], v[132:135], v[124:127]
s_waitcnt lgkmcnt(2)
v_xor_b32 v25, 0x10000, v25
v_and_b32 v109, 0xffff0000, v154
v_mfma_f32_16x16x32_bf16 v[112:115], a[96:99], v[136:139], v[112:115]
v_lshlrev_b32 v110, 16, v155
v_and_b32 v111, 0xffff0000, v155
v_mfma_f32_16x16x32_bf16 v[116:119], a[100:103], v[136:139], v[116:119]
v_mfma_f32_16x16x32_bf16 v[120:123], a[104:107], v[136:139], v[120:123]
s_waitcnt vmcnt(9)
s_barrier
v_mfma_f32_16x16x32_bf16 v[124:127], a[108:111], v[136:139], v[124:127]
s_waitcnt lgkmcnt(1)
ds_read_b128 v[132:135], v25 offset:9216
ds_read_b128 v[184:187], v26 offset:39936
v_mfma_f32_16x16x32_bf16 v[112:115], a[112:115], v[140:143], v[112:115]
ds_read_b128 v[136:139], v25 offset:10240
ds_read_b128 v[188:191], v26 offset:40960
v_mfma_f32_16x16x32_bf16 v[116:119], a[116:119], v[140:143], v[116:119]
ds_read_b128 v[192:195], v26 offset:41984
v_mfma_f32_16x16x32_bf16 v[120:123], a[120:123], v[140:143], v[120:123]
v_mfma_f32_16x16x32_bf16 v[124:127], a[124:127], v[140:143], v[124:127]
s_waitcnt lgkmcnt(5)
v_mfma_f32_16x16x32_bf16 v[112:115], a[128:131], v[144:147], v[112:115]
ds_read_b128 v[140:143], v25 offset:11264
v_mfma_f32_16x16x32_bf16 v[116:119], a[132:135], v[144:147], v[116:119]
v_mfma_f32_16x16x32_bf16 v[120:123], a[136:139], v[144:147], v[120:123]
v_mfma_f32_16x16x32_bf16 v[124:127], a[140:143], v[144:147], v[124:127]
s_sub_u32 s70, s70, 1
s_cbranch_scc0 .Lwo_top15
s_waitcnt vmcnt(6)
s_barrier
s_waitcnt lgkmcnt(4)
v_add_f32 v176, v176, v184
v_add_f32 v177, v177, v185
v_add_f32 v178, v178, v186
v_add_f32 v179, v179, v187
s_waitcnt lgkmcnt(2)
v_add_f32 v176, v176, v188
v_add_f32 v177, v177, v189
v_add_f32 v178, v178, v190
v_add_f32 v179, v179, v191
s_waitcnt lgkmcnt(1)
v_add_f32 v180, v180, v192
v_add_f32 v181, v181, v193
v_add_f32 v182, v182, v194
v_add_f32 v183, v183, v195
v_cvt_pk_bf16_f32 v172, v176, v177
v_cvt_pk_bf16_f32 v173, v178, v179
v_cvt_pk_bf16_f32 v174, v180, v181
v_cvt_pk_bf16_f32 v175, v182, v183
v_cvt_pk_bf16_f32 v164, v112, v113
global_store_dwordx4 v14, v[172:175], s[38:39] sc1
v_cvt_pk_bf16_f32 v165, v114, v115
v_cvt_pk_bf16_f32 v166, v116, v117
v_cvt_pk_bf16_f32 v167, v118, v119
global_store_dwordx4 v13, v[164:167], s[38:39] offset:0 sc1
v_cvt_pk_bf16_f32 v168, v120, v121
v_cvt_pk_bf16_f32 v169, v122, v123
v_cvt_pk_bf16_f32 v170, v124, v125
v_cvt_pk_bf16_f32 v171, v126, v127
global_store_dwordx4 v13, v[168:171], s[38:39] offset:1024 sc1
v_add_f32 v254, v112, v113
v_mul_f32 v255, v112, v112
v_fmac_f32 v255, v113, v113
v_add_f32 v254, v254, v114
v_fmac_f32 v255, v114, v114
v_add_f32 v254, v254, v115
v_fmac_f32 v255, v115, v115
v_add_f32 v254, v254, v116
v_fmac_f32 v255, v116, v116
v_add_f32 v254, v254, v117
v_fmac_f32 v255, v117, v117
v_add_f32 v254, v254, v118
v_fmac_f32 v255, v118, v118
v_add_f32 v254, v254, v119
v_fmac_f32 v255, v119, v119
v_add_f32 v254, v254, v120
v_fmac_f32 v255, v120, v120
v_add_f32 v254, v254, v121
v_fmac_f32 v255, v121, v121
v_add_f32 v254, v254, v122
v_fmac_f32 v255, v122, v122
v_add_f32 v254, v254, v123
v_fmac_f32 v255, v123, v123
v_add_f32 v254, v254, v124
v_fmac_f32 v255, v124, v124
v_add_f32 v254, v254, v125
v_fmac_f32 v255, v125, v125
v_add_f32 v254, v254, v126
v_fmac_f32 v255, v126, v126
v_add_f32 v254, v254, v127
v_fmac_f32 v255, v127, v127
v_add_f32 v254, v254, v176
v_fmac_f32 v255, v176, v176
v_add_f32 v254, v254, v177
v_fmac_f32 v255, v177, v177
v_add_f32 v254, v254, v178
v_fmac_f32 v255, v178, v178
v_add_f32 v254, v254, v179
v_fmac_f32 v255, v179, v179
v_add_f32 v254, v254, v180
v_fmac_f32 v255, v180, v180
v_add_f32 v254, v254, v181
v_fmac_f32 v255, v181, v181
v_add_f32 v254, v254, v182
v_fmac_f32 v255, v182, v182
v_add_f32 v254, v254, v183
v_fmac_f32 v255, v183, v183
ds_write_b64 v27, v[254:255] offset:46592
s_waitcnt vmcnt(0) lgkmcnt(0)
s_barrier
s_barrier
s_endpgm
.Lwo_sp7:
s_sleep 1
v_add_lshl_u32 v253, v22, 0, 7
v_bfi_b32 v253, s69, v253, v21
global_load_dword v200, v253, s[38:39] sc1
s_add_u32 s71, s71, 1
s_waitcnt vmcnt(0)
v_sub_u32 v200, v200, v22
v_cmp_lt_u32 vcc, 0x7f, v200
s_and_b64 vcc, vcc, s[76:77]
s_cbranch_vccz .Lwo_rt8
s_cmp_lt_u32 s71, 3000000
s_cbranch_scc1 .Lwo_sp7
s_branch .Lwo_rt8
.Lwo_sp9:
s_sleep 1
v_add_lshl_u32 v253, v22, 0, 7
v_bfi_b32 v253, s69, v253, v21
global_load_dword v201, v253, s[38:39] sc1
s_add_u32 s71, s71, 1
s_waitcnt vmcnt(0)
v_sub_u32 v201, v201, v22
v_cmp_lt_u32 vcc, 0x7f, v201
s_and_b64 vcc, vcc, s[76:77]
s_cbranch_vccz .Lwo_rt10
s_cmp_lt_u32 s71, 3000000
s_cbranch_scc1 .Lwo_sp9
s_branch .Lwo_rt10
.Le1:
)"
    :: "{v[0:31]}"(va), "{s[36:51]}"(sa) : "v255", "a255", "s99", "vcc", "memory");
  } else if (jj - 5 < 9) {
    const unsigned dn_i = xc * 9 + jj - 5;
    const unsigned r = lane & 15, g = lane >> 4, q = r >> 2, p = lane & 3;
    const unsigned tri = dn_i / 3, nu = dn_i % 3, kap = tri >> 2, rho = tri & 3, row0 = rho * RP + PAD, pg = dn_i % 36;
    auto wv = [&](unsigned j) { const unsigned b = j * 1024 + lane * 16, rw = b / 384; return rw * (1152 - 384) + b + 4096 - j * 1024; };
    u32x32 va; u32x16 sa;
    va[0] = wv(0);
    va[1] = wv(1);
    va[2] = wv(2);
    va[3] = wv(3);
    va[4] = wv(4);
    va[5] = wv(5);
    va[6] = (8 * g + q) * 384 + wave * 64 + p * 16 + (kap & 1) * 8;
    va[7] = (8 * g + q) * 384 + wave * 64 + p * 16 + 8 - (kap & 1) * 8;
    va[8] = (8 * g + q) * 384 + 256 + wave * 32 + (p ^ (kap & 1)) * 8;
    va[9] = WS_H + row0 * HT + (kap * 24 + wave * 6 + 2) * 1024 + lane * 16;
    va[10] = WS_H + (row0 + 63) * HT + (kap * 24 + wave * 6 + 2) * 1024 + lane * 16;
    va[11] = WS_P + (row0 - 1) * PT + (((nu * 3 + (wave >> 1)) * 4 + q) * 6 + kap) * 512 + (wave & 1) * 256 + g * 64 + p * 16;
    va[12] = WS_P + (row0 - 1) * PT + (((nu * 3 + 2) * 4 + q) * 6 + kap) * 512 + wave * 128 + (g >> 1) * 64 + p * 16;
    va[13] = WS_FL + rho * FR + FH + (kap * 6 + min(lane, 5u)) * FL;
    va[14] = 0u;
    va[15] = WS_FL + rho * FR + FP + (nu * 6 + kap) * FL;
    va[16] = 0u - 2;
    va[17] = lane * 16;
    va[18] = (64 + wave * 128 + (lane >> 1)) * 9216 + (lane & 1) * 768;
    sa[0] = 8 + wave; sa[2] = (unsigned)(unsigned long long)ws; sa[3] = (unsigned)((unsigned long long)ws >> 32);
    sa[4] = uni((unsigned)(unsigned long long)(wu + pg / 6 * 1536 + (pg % 6) * 128));
    sa[5] = uni((unsigned)((unsigned long long)(wu + pg / 6 * 1536 + (pg % 6) * 128) >> 32));
    sa[6] = uni((unsigned)(unsigned long long)(wd + (kap * 768 + wave * 16) * 1152 + nu * 384));
    sa[7] = uni((unsigned)((unsigned long long)(wd + (kap * 768 + wave * 16) * 1152 + nu * 384) >> 32));
    sa[8] = uni(wave * 6144);
    sa[9] = uni(wave * 6144 + 2048);
    asm volatile(
R"(.Lb2:
s_movk_i32 s69, 0x380
s_mov_b32 s71, 0
s_mov_b32 s74, 0
s_load_dword s37, s[38:39], 0x0
s_nop 1
s_sleep 70
s_mov_b32 m0, 0x27f00
s_nop 0
global_load_lds_dword v18, s[40:41]
v_add_u32 v18, 294912, v18
global_load_lds_dword v18, s[40:41]
v_add_u32 v18, 294912, v18
global_load_lds_dword v18, s[40:41]
v_add_u32 v18, 294912, v18
global_load_lds_dword v18, s[40:41]
v_add_u32 v18, 294912, v18
s_waitcnt vmcnt(0)
s_add_u32 m0, s44, 4096
s_nop 0
global_load_lds_dwordx4 v0, s[42:43] offset:-4096 nt
global_load_lds_dwordx4 v1, s[42:43] offset:-3072 nt
global_load_lds_dwordx4 v2, s[42:43] offset:-2048 nt
global_load_lds_dwordx4 v3, s[42:43] offset:-1024 nt
global_load_lds_dwordx4 v4, s[42:43] offset:0 nt
global_load_lds_dwordx4 v5, s[42:43] offset:1024 nt
s_add_u32 s42, s42, 73728
s_addc_u32 s43, s43, 0
s_add_u32 m0, s44, 28672
s_nop 0
global_load_lds_dwordx4 v0, s[42:43] offset:-4096 nt
global_load_lds_dwordx4 v1, s[42:43] offset:-3072 nt
global_load_lds_dwordx4 v2, s[42:43] offset:-2048 nt
global_load_lds_dwordx4 v3, s[42:43] offset:-1024 nt
global_load_lds_dwordx4 v4, s[42:43] offset:0 nt
global_load_lds_dwordx4 v5, s[42:43] offset:1024 nt
s_add_u32 s42, s42, 73728
s_addc_u32 s43, s43, 0
s_add_u32 m0, s44, 53248
s_nop 0
global_load_lds_dwordx4 v0, s[42:43] offset:-4096 nt
global_load_lds_dwordx4 v1, s[42:43] offset:-3072 nt
global_load_lds_dwordx4 v2, s[42:43] offset:-2048 nt
global_load_lds_dwordx4 v3, s[42:43] offset:-1024 nt
global_load_lds_dwordx4 v4, s[42:43] offset:0 nt
global_load_lds_dwordx4 v5, s[42:43] offset:1024 nt
s_add_u32 s42, s42, 73728
s_addc_u32 s43, s43, 0
s_add_u32 m0, s44, 77824
s_nop 0
global_load_lds_dwordx4 v0, s[42:43] offset:-4096 nt
global_load_lds_dwordx4 v1, s[42:43] offset:-3072 nt
global_load_lds_dwordx4 v2, s[42:43] offset:-2048 nt
global_load_lds_dwordx4 v3, s[42:43] offset:-1024 nt
global_load_lds_dwordx4 v4, s[42:43] offset:0 nt
global_load_lds_dwordx4 v5, s[42:43] offset:1024 nt
s_add_u32 s42, s42, 73728
s_addc_u32 s43, s43, 0
s_add_u32 m0, s44, 102400
s_nop 0
global_load_lds_dwordx4 v0, s[42:43] offset:-4096 nt
global_load_lds_dwordx4 v1, s[42:43] offset:-3072 nt
global_load_lds_dwordx4 v2, s[42:43] offset:-2048 nt
global_load_lds_dwordx4 v3, s[42:43] offset:-1024 nt
global_load_lds_dwordx4 v4, s[42:43] offset:0 nt
global_load_lds_dwordx4 v5, s[42:43] offset:1024 nt
s_add_u32 s42, s42, 73728
s_addc_u32 s43, s43, 0
v_add_u32 v255, 0x10000, v6
v_add_u32 v132, 0x20000, v6
v_add_u32 v133, 0x10000, v7
v_add_u32 v134, 0x20000, v7
v_add_u32 v135, 0x10000, v8
v_add_u32 v136, 0x20000, v8
s_waitcnt lgkmcnt(0)
s_mul_i32 s37, s37, 0x9E3779B1
s_add_u32 s37, s37, 0x7F4A7C15
v_add_u32 v14, s37, v14
v_add_u32 v16, s37, v16
s_waitcnt vmcnt(24)
s_barrier
s_add_u32 m0, s44, 126976
ds_read_b64_tr_b16 a[0:1], v6 offset:0
ds_read_b64_tr_b16 a[2:3], v6 offset:1536
global_load_lds_dwordx4 v0, s[42:43] offset:-4096 nt
ds_read_b64_tr_b16 a[4:5], v7 offset:0
global_load_lds_dwordx4 v1, s[42:43] offset:-3072 nt
ds_read_b64_tr_b16 a[6:7], v7 offset:1536
global_load_lds_dwordx4 v2, s[42:43] offset:-2048 nt
ds_read_b64_tr_b16 a[8:9], v8 offset:0
ds_read_b64_tr_b16 a[10:11], v8 offset:1536
global_load_lds_dwordx4 v3, s[42:43] offset:-1024 nt
ds_read_b64_tr_b16 a[12:13], v6 offset:12288
global_load_lds_dwordx4 v4, s[42:43] offset:0 nt
ds_read_b64_tr_b16 a[14:15], v6 offset:13824
global_load_lds_dwordx4 v5, s[42:43] offset:1024 nt
ds_read_b64_tr_b16 a[16:17], v7 offset:12288
ds_read_b64_tr_b16 a[18:19], v7 offset:13824
s_add_u32 s42, s42, 73728
ds_read_b64_tr_b16 a[20:21], v8 offset:12288
s_addc_u32 s43, s43, 0
ds_read_b64_tr_b16 a[22:23], v8 offset:13824
s_waitcnt vmcnt(24) lgkmcnt(0)
s_barrier
s_add_u32 m0, s44, 4096
ds_read_b64_tr_b16 a[24:25], v6 offset:24576
ds_read_b64_tr_b16 a[26:27], v6 offset:26112
global_load_lds_dwordx4 v0, s[42:43] offset:-4096 nt
ds_read_b64_tr_b16 a[28:29], v7 offset:24576
global_load_lds_dwordx4 v1, s[42:43] offset:-3072 nt
ds_read_b64_tr_b16 a[30:31], v7 offset:26112
global_load_lds_dwordx4 v2, s[42:43] offset:-2048 nt
ds_read_b64_tr_b16 a[32:33], v8 offset:24576
ds_read_b64_tr_b16 a[34:35], v8 offset:26112
global_load_lds_dwordx4 v3, s[42:43] offset:-1024 nt
ds_read_b64_tr_b16 a[36:37], v6 offset:36864
global_load_lds_dwordx4 v4, s[42:43] offset:0 nt
ds_read_b64_tr_b16 a[38:39], v6 offset:38400
global_load_lds_dwordx4 v5, s[42:43] offset:1024 nt
ds_read_b64_tr_b16 a[40:41], v7 offset:36864
ds_read_b64_tr_b16 a[42:43], v7 offset:38400
s_add_u32 s42, s42, 73728
ds_read_b64_tr_b16 a[44:45], v8 offset:36864
s_addc_u32 s43, s43, 0
ds_read_b64_tr_b16 a[46:47], v8 offset:38400
s_waitcnt vmcnt(24) lgkmcnt(0)
s_barrier
s_add_u32 m0, s44, 28672
ds_read_b64_tr_b16 a[48:49], v6 offset:49152
ds_read_b64_tr_b16 a[50:51], v6 offset:50688
global_load_lds_dwordx4 v0, s[42:43] offset:-4096 nt
ds_read_b64_tr_b16 a[52:53], v7 offset:49152
global_load_lds_dwordx4 v1, s[42:43] offset:-3072 nt
ds_read_b64_tr_b16 a[54:55], v7 offset:50688
global_load_lds_dwordx4 v2, s[42:43] offset:-2048 nt
ds_read_b64_tr_b16 a[56:57], v8 offset:49152
ds_read_b64_tr_b16 a[58:59], v8 offset:50688
global_load_lds_dwordx4 v3, s[42:43] offset:-1024 nt
ds_read_b64_tr_b16 a[60:61], v6 offset:61440
global_load_lds_dwordx4 v4, s[42:43] offset:0 nt
ds_read_b64_tr_b16 a[62:63], v6 offset:62976
global_load_lds_dwordx4 v5, s[42:43] offset:1024 nt
ds_read_b64_tr_b16 a[64:65], v7 offset:61440
ds_read_b64_tr_b16 a[66:67], v7 offset:62976
s_add_u32 s42, s42, 73728
ds_read_b64_tr_b16 a[68:69], v8 offset:61440
s_addc_u32 s43, s43, 0
ds_read_b64_tr_b16 a[70:71], v8 offset:62976
s_waitcnt vmcnt(24) lgkmcnt(0)
s_barrier
s_add_u32 m0, s44, 53248
ds_read_b64_tr_b16 a[72:73], v255 offset:8192
ds_read_b64_tr_b16 a[74:75], v255 offset:9728
global_load_lds_dwordx4 v0, s[42:43] offset:-4096 nt
ds_read_b64_tr_b16 a[76:77], v133 offset:8192
global_load_lds_dwordx4 v1, s[42:43] offset:-3072 nt
ds_read_b64_tr_b16 a[78:79], v133 offset:9728
global_load_lds_dwordx4 v2, s[42:43] offset:-2048 nt
ds_read_b64_tr_b16 a[80:81], v135 offset:8192
ds_read_b64_tr_b16 a[82:83], v135 offset:9728
global_load_lds_dwordx4 v3, s[42:43] offset:-1024 nt
ds_read_b64_tr_b16 a[84:85], v255 offset:20480
global_load_lds_dwordx4 v4, s[42:43] offset:0 nt
ds_read_b64_tr_b16 a[86:87], v255 offset:22016
global_load_lds_dwordx4 v5, s[42:43] offset:1024 nt
ds_read_b64_tr_b16 a[88:89], v133 offset:20480
ds_read_b64_tr_b16 a[90:91], v133 offset:22016
s_add_u32 s42, s42, 73728
ds_read_b64_tr_b16 a[92:93], v135 offset:20480
s_addc_u32 s43, s43, 0
ds_read_b64_tr_b16 a[94:95], v135 offset:22016
s_waitcnt vmcnt(24) lgkmcnt(0)
s_barrier
s_add_u32 m0, s44, 77824
ds_read_b64_tr_b16 a[96:97], v255 offset:32768
ds_read_b64_tr_b16 a[98:99], v255 offset:34304
global_load_lds_dwordx4 v0, s[42:43] offset:-4096 nt
ds_read_b64_tr_b16 a[100:101], v133 offset:32768
global_load_lds_dwordx4 v1, s[42:43] offset:-3072 nt
ds_read_b64_tr_b16 a[102:103], v133 offset:34304
global_load_lds_dwordx4 v2, s[42:43] offset:-2048 nt
ds_read_b64_tr_b16 a[104:105], v135 offset:32768
ds_read_b64_tr_b16 a[106:107], v135 offset:34304
global_load_lds_dwordx4 v3, s[42:43] offset:-1024 nt
ds_read_b64_tr_b16 a[108:109], v255 offset:45056
global_load_lds_dwordx4 v4, s[42:43] offset:0 nt
ds_read_b64_tr_b16 a[110:111], v255 offset:46592
global_load_lds_dwordx4 v5, s[42:43] offset:1024 nt
ds_read_b64_tr_b16 a[112:113], v133 offset:45056
ds_read_b64_tr_b16 a[114:115], v133 offset:46592
s_add_u32 s42, s42, 73728
ds_read_b64_tr_b16 a[116:117], v135 offset:45056
s_addc_u32 s43, s43, 0
ds_read_b64_tr_b16 a[118:119], v135 offset:46592
s_waitcnt vmcnt(24) lgkmcnt(0)
s_barrier
s_add_u32 m0, s44, 102400
ds_read_b64_tr_b16 a[120:121], v255 offset:57344
ds_read_b64_tr_b16 a[122:123], v255 offset:58880
global_load_lds_dwordx4 v0, s[42:43] offset:-4096 nt
ds_read_b64_tr_b16 a[124:125], v133 offset:57344
global_load_lds_dwordx4 v1, s[42:43] offset:-3072 nt
ds_read_b64_tr_b16 a[126:127], v133 offset:58880
global_load_lds_dwordx4 v2, s[42:43] offset:-2048 nt
ds_read_b64_tr_b16 a[128:129], v135 offset:57344
ds_read_b64_tr_b16 a[130:131], v135 offset:58880
global_load_lds_dwordx4 v3, s[42:43] offset:-1024 nt
ds_read_b64_tr_b16 a[132:133], v132 offset:4096
global_load_lds_dwordx4 v4, s[42:43] offset:0 nt
ds_read_b64_tr_b16 a[134:135], v132 offset:5632
global_load_lds_dwordx4 v5, s[42:43] offset:1024 nt
ds_read_b64_tr_b16 a[136:137], v134 offset:4096
ds_read_b64_tr_b16 a[138:139], v134 offset:5632
s_add_u32 s42, s42, 73728
ds_read_b64_tr_b16 a[140:141], v136 offset:4096
s_addc_u32 s43, s43, 0
ds_read_b64_tr_b16 a[142:143], v136 offset:5632
s_waitcnt vmcnt(24) lgkmcnt(0)
s_barrier
s_add_u32 m0, s44, 126976
ds_read_b64_tr_b16 a[144:145], v6 offset:0
ds_read_b64_tr_b16 a[146:147], v6 offset:1536
global_load_lds_dwordx4 v0, s[42:43] offset:-4096 nt
ds_read_b64_tr_b16 a[148:149], v7 offset:0
global_load_lds_dwordx4 v1, s[42:43] offset:-3072 nt
ds_read_b64_tr_b16 a[150:151], v7 offset:1536
global_load_lds_dwordx4 v2, s[42:43] offset:-2048 nt
ds_read_b64_tr_b16 a[152:153], v8 offset:0
ds_read_b64_tr_b16 a[154:155], v8 offset:1536
global_load_lds_dwordx4 v3, s[42:43] offset:-1024 nt
ds_read_b64_tr_b16 a[156:157], v6 offset:12288
global_load_lds_dwordx4 v4, s[42:43] offset:0 nt
ds_read_b64_tr_b16 a[158:159], v6 offset:13824
global_load_lds_dwordx4 v5, s[42:43] offset:1024 nt
ds_read_b64_tr_b16 a[160:161], v7 offset:12288
ds_read_b64_tr_b16 a[162:163], v7 offset:13824
s_sub_u32 s42, s42, 811008
ds_read_b64_tr_b16 a[164:165], v8 offset:12288
s_subb_u32 s43, s43, 0
ds_read_b64_tr_b16 a[166:167], v8 offset:13824
s_waitcnt vmcnt(24) lgkmcnt(0)
s_barrier
ds_read_b64_tr_b16 a[168:169], v6 offset:24576
ds_read_b64_tr_b16 a[170:171], v6 offset:26112
ds_read_b64_tr_b16 a[172:173], v7 offset:24576
ds_read_b64_tr_b16 a[174:175], v7 offset:26112
ds_read_b64_tr_b16 a[176:177], v8 offset:24576
ds_read_b64_tr_b16 a[178:179], v8 offset:26112
ds_read_b64_tr_b16 a[180:181], v6 offset:36864
ds_read_b64_tr_b16 a[182:183], v6 offset:38400
ds_read_b64_tr_b16 a[184:185], v7 offset:36864
ds_read_b64_tr_b16 a[186:187], v7 offset:38400
ds_read_b64_tr_b16 a[188:189], v8 offset:36864
ds_read_b64_tr_b16 a[190:191], v8 offset:38400
s_waitcnt vmcnt(18) lgkmcnt(0)
s_barrier
ds_read_b64_tr_b16 a[192:193], v6 offset:49152
ds_read_b64_tr_b16 a[194:195], v6 offset:50688
ds_read_b64_tr_b16 a[196:197], v7 offset:49152
ds_read_b64_tr_b16 a[198:199], v7 offset:50688
ds_read_b64_tr_b16 a[200:201], v8 offset:49152
ds_read_b64_tr_b16 a[202:203], v8 offset:50688
ds_read_b64_tr_b16 a[204:205], v6 offset:61440
ds_read_b64_tr_b16 a[206:207], v6 offset:62976
ds_read_b64_tr_b16 a[208:209], v7 offset:61440
ds_read_b64_tr_b16 a[210:211], v7 offset:62976
ds_read_b64_tr_b16 a[212:213], v8 offset:61440
ds_read_b64_tr_b16 a[214:215], v8 offset:62976
s_waitcnt vmcnt(12) lgkmcnt(0)
s_barrier
ds_read_b64_tr_b16 a[216:217], v255 offset:8192
ds_read_b64_tr_b16 a[218:219], v255 offset:9728
ds_read_b64_tr_b16 a[220:221], v133 offset:8192
ds_read_b64_tr_b16 a[222:223], v133 offset:9728
ds_read_b64_tr_b16 a[224:225], v135 offset:8192
ds_read_b64_tr_b16 a[226:227], v135 offset:9728
ds_read_b64_tr_b16 a[228:229], v255 offset:20480
ds_read_b64_tr_b16 a[230:231], v255 offset:22016
ds_read_b64_tr_b16 a[232:233], v133 offset:20480
ds_read_b64_tr_b16 a[234:235], v133 offset:22016
ds_read_b64_tr_b16 a[236:237], v135 offset:20480
ds_read_b64_tr_b16 a[238:239], v135 offset:22016
s_waitcnt vmcnt(6) lgkmcnt(0)
s_barrier
ds_read_b64_tr_b16 a[240:241], v255 offset:32768
ds_read_b64_tr_b16 a[242:243], v255 offset:34304
ds_read_b64_tr_b16 a[244:245], v133 offset:32768
ds_read_b64_tr_b16 a[246:247], v133 offset:34304
ds_read_b64_tr_b16 a[248:249], v135 offset:32768
ds_read_b64_tr_b16 a[250:251], v135 offset:34304
ds_read_b64_tr_b16 a[252:253], v255 offset:45056
ds_read_b64_tr_b16 a[254:255], v255 offset:46592
ds_read_b64_tr_b16 v[20:21], v133 offset:45056
ds_read_b64_tr_b16 v[22:23], v133 offset:46592
ds_read_b64_tr_b16 v[24:25], v135 offset:45056
ds_read_b64_tr_b16 v[26:27], v135 offset:46592
s_waitcnt vmcnt(0) lgkmcnt(0)
s_barrier
ds_read_b64_tr_b16 v[28:29], v255 offset:57344
ds_read_b64_tr_b16 v[30:31], v255 offset:58880
ds_read_b64_tr_b16 v[32:33], v133 offset:57344
ds_read_b64_tr_b16 v[34:35], v133 offset:58880
ds_read_b64_tr_b16 v[36:37], v135 offset:57344
ds_read_b64_tr_b16 v[38:39], v135 offset:58880
ds_read_b64_tr_b16 v[40:41], v132 offset:4096
ds_read_b64_tr_b16 v[42:43], v132 offset:5632
ds_read_b64_tr_b16 v[44:45], v134 offset:4096
ds_read_b64_tr_b16 v[46:47], v134 offset:5632
ds_read_b64_tr_b16 v[48:49], v136 offset:4096
ds_read_b64_tr_b16 v[50:51], v136 offset:5632
s_waitcnt lgkmcnt(0)
s_barrier
s_and_b32 s75, s36, 3
s_cmp_eq_u32 s75, 1
s_cbranch_scc1 .Ldn_w2
s_cmp_eq_u32 s75, 2
s_cbranch_scc1 .Ldn_w3
s_cmp_eq_u32 s75, 3
s_cbranch_scc1 .Ldn_w4
.Ldn_w1:
v_add_u32 v19, -1, v14
v_add_u32 v254, -1, v14
v_add_lshl_u32 v252, v16, 0, 7
v_bfi_b32 v252, s69, v252, v15
v_add_lshl_u32 v253, v14, 0, 7
v_bfi_b32 v253, s69, v253, v13
s_mov_b32 s76, 0xffff
s_mov_b32 s77, 0xffff
s_add_u32 m0, s45, 24576
s_xor_b32 s45, s45, 0x10000
s_mov_b32 m0, s45
s_barrier
global_load_lds_dwordx4 v9, s[38:39] offset:-2048 sc1
global_load_lds_dwordx4 v9, s[38:39] offset:-1024 sc1
global_load_lds_dwordx4 v9, s[38:39] offset:0 sc1
global_load_lds_dwordx4 v9, s[38:39] offset:1024 sc1
global_load_lds_dwordx4 v9, s[38:39] offset:2048 sc1
global_load_lds_dwordx4 v9, s[38:39] offset:3072 sc1
v_add_u32 v9, 147456, v9
s_mov_b32 s70, 32
s_waitcnt vmcnt(0) lgkmcnt(0)
s_nop 7
s_branch .Ldn_in5
.p2align 6
.Ldn_top6:
s_waitcnt lgkmcnt(5)
v_mfma_f32_16x16x32_bf16 v[52:55], a[0:3], v[76:79], 0
s_waitcnt lgkmcnt(4)
v_mfma_f32_16x16x32_bf16 v[52:55], a[12:15], v[80:83], v[52:55]
s_waitcnt lgkmcnt(3)
v_mfma_f32_16x16x32_bf16 v[52:55], a[24:27], v[84:87], v[52:55]
s_waitcnt lgkmcnt(2)
v_mfma_f32_16x16x32_bf16 v[52:55], a[36:39], v[88:91], v[52:55]
s_waitcnt lgkmcnt(1)
ds_read_b128 v[100:103], v17 offset:6144
v_cvt_pk_bf16_f32 v128, v72, v73
v_mfma_f32_16x16x32_bf16 v[52:55], a[48:51], v[92:95], v[52:55]
s_waitcnt lgkmcnt(1)
v_mfma_f32_16x16x32_bf16 v[52:55], a[60:63], v[96:99], v[52:55]
s_waitcnt vmcnt(6)
s_barrier
v_mfma_f32_16x16x32_bf16 v[56:59], a[4:7], v[76:79], 0
ds_read_b128 v[104:107], v17 offset:7168
v_cvt_pk_bf16_f32 v130, v72, v73
v_mfma_f32_16x16x32_bf16 v[56:59], a[16:19], v[80:83], v[56:59]
global_store_dwordx4 v11, v[124:127], s[38:39] sc1
v_mfma_f32_16x16x32_bf16 v[56:59], a[28:31], v[84:87], v[56:59]
ds_read_b128 v[108:111], v17 offset:8192
v_add_u32 v11, 110592, v11
v_mfma_f32_16x16x32_bf16 v[56:59], a[40:43], v[88:91], v[56:59]
v_cvt_pk_bf16_f32 v129, v74, v75
v_cvt_pk_bf16_f32 v131, v74, v75
v_mfma_f32_16x16x32_bf16 v[56:59], a[52:55], v[92:95], v[56:59]
ds_read_b128 v[112:115], v17 offset:9216
v_mfma_f32_16x16x32_bf16 v[56:59], a[64:67], v[96:99], v[56:59]
v_permlane16_swap_b32 v128, v130
v_mfma_f32_16x16x32_bf16 v[60:63], a[8:11], v[76:79], 0
ds_read_b128 v[116:119], v17 offset:10240
v_mfma_f32_16x16x32_bf16 v[60:63], a[20:23], v[80:83], v[60:63]
v_permlane16_swap_b32 v129, v131
v_mfma_f32_16x16x32_bf16 v[60:63], a[32:35], v[84:87], v[60:63]
ds_read_b128 v[120:123], v17 offset:11264
v_mfma_f32_16x16x32_bf16 v[60:63], a[44:47], v[88:91], v[60:63]
s_mov_b64 exec, s[76:77]
global_store_dwordx4 v12, v[128:131], s[38:39] sc1
s_mov_b64 exec, -1
v_mfma_f32_16x16x32_bf16 v[60:63], a[56:59], v[92:95], v[60:63]
v_add_u32 v12, 110592, v12
v_mfma_f32_16x16x32_bf16 v[60:63], a[68:71], v[96:99], v[60:63]
s_waitcnt lgkmcnt(5)
v_mfma_f32_16x16x32_bf16 v[52:55], a[72:75], v[100:103], v[52:55]
s_waitcnt lgkmcnt(4)
v_mfma_f32_16x16x32_bf16 v[52:55], a[84:87], v[104:107], v[52:55]
s_waitcnt lgkmcnt(3)
v_mfma_f32_16x16x32_bf16 v[52:55], a[96:99], v[108:111], v[52:55]
s_waitcnt lgkmcnt(2)
v_mfma_f32_16x16x32_bf16 v[52:55], a[108:111], v[112:115], v[52:55]
s_waitcnt lgkmcnt(1)
v_mfma_f32_16x16x32_bf16 v[52:55], a[120:123], v[116:119], v[52:55]
s_waitcnt lgkmcnt(0)
)"
R"(ds_read_b128 v[76:79], v17 offset:12288
v_mfma_f32_16x16x32_bf16 v[52:55], a[132:135], v[120:123], v[52:55]
v_mfma_f32_16x16x32_bf16 v[56:59], a[76:79], v[100:103], v[56:59]
ds_read_b128 v[80:83], v17 offset:13312
v_mfma_f32_16x16x32_bf16 v[56:59], a[88:91], v[104:107], v[56:59]
v_mfma_f32_16x16x32_bf16 v[56:59], a[100:103], v[108:111], v[56:59]
ds_read_b128 v[84:87], v17 offset:14336
v_mfma_f32_16x16x32_bf16 v[56:59], a[112:115], v[112:115], v[56:59]
v_mfma_f32_16x16x32_bf16 v[56:59], a[124:127], v[116:119], v[56:59]
ds_read_b128 v[88:91], v17 offset:15360
v_mfma_f32_16x16x32_bf16 v[56:59], a[136:139], v[120:123], v[56:59]
v_mfma_f32_16x16x32_bf16 v[60:63], a[80:83], v[100:103], v[60:63]
ds_read_b128 v[92:95], v17 offset:16384
v_mfma_f32_16x16x32_bf16 v[60:63], a[92:95], v[104:107], v[60:63]
v_mfma_f32_16x16x32_bf16 v[60:63], a[104:107], v[108:111], v[60:63]
ds_read_b128 v[96:99], v17 offset:17408
v_mfma_f32_16x16x32_bf16 v[60:63], a[116:119], v[112:115], v[60:63]
v_mfma_f32_16x16x32_bf16 v[60:63], a[128:131], v[116:119], v[60:63]
v_mfma_f32_16x16x32_bf16 v[60:63], a[140:143], v[120:123], v[60:63]
s_waitcnt lgkmcnt(5)
v_mfma_f32_16x16x32_bf16 v[52:55], a[144:147], v[76:79], v[52:55]
s_waitcnt lgkmcnt(4)
v_mfma_f32_16x16x32_bf16 v[52:55], a[156:159], v[80:83], v[52:55]
s_waitcnt lgkmcnt(3)
v_mfma_f32_16x16x32_bf16 v[52:55], a[168:171], v[84:87], v[52:55]
s_waitcnt lgkmcnt(2)
v_mfma_f32_16x16x32_bf16 v[52:55], a[180:183], v[88:91], v[52:55]
s_waitcnt lgkmcnt(1)
v_mfma_f32_16x16x32_bf16 v[52:55], a[192:195], v[92:95], v[52:55]
s_xor_b32 s45, s45, 0x10000
ds_read_b128 v[100:103], v17 offset:18432
s_waitcnt lgkmcnt(1)
v_mfma_f32_16x16x32_bf16 v[52:55], a[204:207], v[96:99], v[52:55]
s_add_u32 m0, s45, 0
v_mfma_f32_16x16x32_bf16 v[56:59], a[148:151], v[76:79], v[56:59]
ds_read_b128 v[104:107], v17 offset:19456
ds_read_b128 v[108:111], v17 offset:20480
v_mfma_f32_16x16x32_bf16 v[56:59], a[160:163], v[80:83], v[56:59]
v_mfma_f32_16x16x32_bf16 v[56:59], a[172:175], v[84:87], v[56:59]
s_barrier
v_mfma_f32_16x16x32_bf16 v[56:59], a[184:187], v[88:91], v[56:59]
global_load_lds_dwordx4 v9, s[38:39] offset:-2048 sc1
v_mfma_f32_16x16x32_bf16 v[56:59], a[196:199], v[92:95], v[56:59]
ds_read_b128 v[112:115], v17 offset:21504
v_mfma_f32_16x16x32_bf16 v[56:59], a[208:211], v[96:99], v[56:59]
v_mfma_f32_16x16x32_bf16 v[60:63], a[152:155], v[76:79], v[60:63]
ds_read_b128 v[116:119], v17 offset:22528
global_load_lds_dwordx4 v9, s[38:39] offset:-1024 sc1
v_mfma_f32_16x16x32_bf16 v[60:63], a[164:167], v[80:83], v[60:63]
v_mfma_f32_16x16x32_bf16 v[60:63], a[176:179], v[84:87], v[60:63]
ds_read_b128 v[120:123], v17 offset:23552
v_mfma_f32_16x16x32_bf16 v[60:63], a[188:191], v[88:91], v[60:63]
global_load_lds_dwordx4 v9, s[38:39] offset:0 sc1
v_mfma_f32_16x16x32_bf16 v[60:63], a[200:203], v[92:95], v[60:63]
v_mfma_f32_16x16x32_bf16 v[60:63], a[212:215], v[96:99], v[60:63]
s_waitcnt lgkmcnt(5)
v_mfma_f32_16x16x32_bf16 v[52:55], a[216:219], v[100:103], v[52:55]
s_waitcnt lgkmcnt(4)
global_load_lds_dwordx4 v9, s[38:39] offset:1024 sc1
v_mfma_f32_16x16x32_bf16 v[52:55], a[228:231], v[104:107], v[52:55]
s_waitcnt lgkmcnt(3)
v_mfma_f32_16x16x32_bf16 v[52:55], a[240:243], v[108:111], v[52:55]
s_waitcnt lgkmcnt(2)
v_mfma_f32_16x16x32_bf16 v[52:55], a[252:255], v[112:115], v[52:55]
s_waitcnt lgkmcnt(1)
global_load_lds_dwordx4 v9, s[38:39] offset:2048 sc1
v_mfma_f32_16x16x32_bf16 v[52:55], v[28:31], v[116:119], v[52:55]
s_waitcnt lgkmcnt(0)
v_mfma_f32_16x16x32_bf16 v[52:55], v[40:43], v[120:123], v[52:55]
v_mfma_f32_16x16x32_bf16 v[56:59], a[220:223], v[100:103], v[56:59]
global_load_lds_dwordx4 v9, s[38:39] offset:3072 sc1
v_add_u32 v9, 147456, v9
v_mfma_f32_16x16x32_bf16 v[56:59], a[232:235], v[104:107], v[56:59]
v_mfma_f32_16x16x32_bf16 v[56:59], a[244:247], v[108:111], v[56:59]
s_waitcnt vmcnt(8)
s_barrier
v_mfma_f32_16x16x32_bf16 v[56:59], v[20:23], v[112:115], v[56:59]
ds_read_b128 v[76:79], v17 offset:24576
v_min_u32 v9, v9, v10
v_mfma_f32_16x16x32_bf16 v[56:59], v[32:35], v[116:119], v[56:59]
ds_read_b128 v[80:83], v17 offset:25600
v_cvt_pk_bf16_f32 v124, v52, v53
v_mfma_f32_16x16x32_bf16 v[56:59], v[44:47], v[120:123], v[56:59]
ds_read_b128 v[84:87], v17 offset:26624
v_cvt_pk_bf16_f32 v125, v54, v55
v_mfma_f32_16x16x32_bf16 v[60:63], a[224:227], v[100:103], v[60:63]
ds_read_b128 v[88:91], v17 offset:27648
v_mfma_f32_16x16x32_bf16 v[60:63], a[236:239], v[104:107], v[60:63]
ds_read_b128 v[92:95], v17 offset:28672
v_mfma_f32_16x16x32_bf16 v[60:63], a[248:251], v[108:111], v[60:63]
ds_read_b128 v[96:99], v17 offset:29696
v_mfma_f32_16x16x32_bf16 v[60:63], v[24:27], v[112:115], v[60:63]
v_cvt_pk_bf16_f32 v126, v56, v57
v_cvt_pk_bf16_f32 v127, v58, v59
v_mfma_f32_16x16x32_bf16 v[60:63], v[36:39], v[116:119], v[60:63]
v_mfma_f32_16x16x32_bf16 v[60:63], v[48:51], v[120:123], v[60:63]
s_waitcnt lgkmcnt(5)
v_mfma_f32_16x16x32_bf16 v[64:67], a[0:3], v[76:79], 0
s_waitcnt lgkmcnt(4)
v_mfma_f32_16x16x32_bf16 v[64:67], a[12:15], v[80:83], v[64:67]
s_waitcnt lgkmcnt(3)
v_mfma_f32_16x16x32_bf16 v[64:67], a[24:27], v[84:87], v[64:67]
s_waitcnt lgkmcnt(2)
v_mfma_f32_16x16x32_bf16 v[64:67], a[36:39], v[88:91], v[64:67]
s_waitcnt lgkmcnt(1)
ds_read_b128 v[100:103], v17 offset:30720
v_cvt_pk_bf16_f32 v128, v60, v61
v_mfma_f32_16x16x32_bf16 v[64:67], a[48:51], v[92:95], v[64:67]
s_waitcnt lgkmcnt(1)
v_mfma_f32_16x16x32_bf16 v[64:67], a[60:63], v[96:99], v[64:67]
s_waitcnt vmcnt(6)
s_barrier
v_mfma_f32_16x16x32_bf16 v[68:71], a[4:7], v[76:79], 0
ds_read_b128 v[104:107], v17 offset:31744
v_cvt_pk_bf16_f32 v130, v60, v61
v_mfma_f32_16x16x32_bf16 v[68:71], a[16:19], v[80:83], v[68:71]
global_store_dwordx4 v11, v[124:127], s[38:39] sc1
v_mfma_f32_16x16x32_bf16 v[68:71], a[28:31], v[84:87], v[68:71]
ds_read_b128 v[108:111], v17 offset:32768
v_add_u32 v11, 110592, v11
v_mfma_f32_16x16x32_bf16 v[68:71], a[40:43], v[88:91], v[68:71]
v_cvt_pk_bf16_f32 v129, v62, v63
v_cvt_pk_bf16_f32 v131, v62, v63
v_mfma_f32_16x16x32_bf16 v[68:71], a[52:55], v[92:95], v[68:71]
ds_read_b128 v[112:115], v17 offset:33792
v_mfma_f32_16x16x32_bf16 v[68:71], a[64:67], v[96:99], v[68:71]
v_permlane16_swap_b32 v128, v130
v_mfma_f32_16x16x32_bf16 v[72:75], a[8:11], v[76:79], 0
ds_read_b128 v[116:119], v17 offset:34816
v_mfma_f32_16x16x32_bf16 v[72:75], a[20:23], v[80:83], v[72:75]
v_permlane16_swap_b32 v129, v131
v_mfma_f32_16x16x32_bf16 v[72:75], a[32:35], v[84:87], v[72:75]
ds_read_b128 v[120:123], v17 offset:35840
v_mfma_f32_16x16x32_bf16 v[72:75], a[44:47], v[88:91], v[72:75]
s_mov_b64 exec, s[76:77]
global_store_dwordx4 v12, v[128:131], s[38:39] sc1
s_mov_b64 exec, -1
v_mfma_f32_16x16x32_bf16 v[72:75], a[56:59], v[92:95], v[72:75]
v_add_u32 v12, 110592, v12
v_mfma_f32_16x16x32_bf16 v[72:75], a[68:71], v[96:99], v[72:75]
s_waitcnt lgkmcnt(5)
v_mfma_f32_16x16x32_bf16 v[64:67], a[72:75], v[100:103], v[64:67]
s_waitcnt lgkmcnt(4)
v_mfma_f32_16x16x32_bf16 v[64:67], a[84:87], v[104:107], v[64:67]
s_waitcnt lgkmcnt(3)
v_mfma_f32_16x16x32_bf16 v[64:67], a[96:99], v[108:111], v[64:67]
s_waitcnt lgkmcnt(2)
v_mfma_f32_16x16x32_bf16 v[64:67], a[108:111], v[112:115], v[64:67]
s_waitcnt lgkmcnt(1)
v_mfma_f32_16x16x32_bf16 v[64:67], a[120:123], v[116:119], v[64:67]
s_waitcnt lgkmcnt(0)
ds_read_b128 v[76:79], v17 offset:36864
v_mfma_f32_16x16x32_bf16 v[64:67], a[132:135], v[120:123], v[64:67]
v_mfma_f32_16x16x32_bf16 v[68:71], a[76:79], v[100:103], v[68:71]
ds_read_b128 v[80:83], v17 offset:37888
v_mfma_f32_16x16x32_bf16 v[68:71], a[88:91], v[104:107], v[68:71]
v_mfma_f32_16x16x32_bf16 v[68:71], a[100:103], v[108:111], v[68:71]
ds_read_b128 v[84:87], v17 offset:38912
v_mfma_f32_16x16x32_bf16 v[68:71], a[112:115], v[112:115], v[68:71]
v_mfma_f32_16x16x32_bf16 v[68:71], a[124:127], v[116:119], v[68:71]
ds_read_b128 v[88:91], v17 offset:39936
v_mfma_f32_16x16x32_bf16 v[68:71], a[136:139], v[120:123], v[68:71]
v_mfma_f32_16x16x32_bf16 v[72:75], a[80:83], v[100:103], v[72:75]
ds_read_b128 v[92:95], v17 offset:40960
v_mfma_f32_16x16x32_bf16 v[72:75], a[92:95], v[104:107], v[72:75]
v_mfma_f32_16x16x32_bf16 v[72:75], a[104:107], v[108:111], v[72:75]
ds_read_b128 v[96:99], v17 offset:41984
v_mfma_f32_16x16x32_bf16 v[72:75], a[116:119], v[112:115], v[72:75]
v_mfma_f32_16x16x32_bf16 v[72:75], a[128:131], v[116:119], v[72:75]
v_mfma_f32_16x16x32_bf16 v[72:75], a[140:143], v[120:123], v[72:75]
s_waitcnt lgkmcnt(5)
v_mfma_f32_16x16x32_bf16 v[64:67], a[144:147], v[76:79], v[64:67]
s_waitcnt lgkmcnt(4)
v_mfma_f32_16x16x32_bf16 v[64:67], a[156:159], v[80:83], v[64:67]
s_waitcnt lgkmcnt(3)
v_mfma_f32_16x16x32_bf16 v[64:67], a[168:171], v[84:87], v[64:67]
.Ldn_in5:
s_waitcnt lgkmcnt(2)
v_mfma_f32_16x16x32_bf16 v[64:67], a[180:183], v[88:91], v[64:67]
s_waitcnt lgkmcnt(1)
v_mfma_f32_16x16x32_bf16 v[64:67], a[192:195], v[92:95], v[64:67]
s_waitcnt lgkmcnt(0)
ds_read_b128 v[100:103], v17 offset:43008
v_mfma_f32_16x16x32_bf16 v[64:67], a[204:207], v[96:99], v[64:67]
s_add_u32 m0, s45, 24576
v_mfma_f32_16x16x32_bf16 v[68:71], a[148:151], v[76:79], v[68:71]
ds_read_b128 v[104:107], v17 offset:44032
ds_read_b128 v[108:111], v17 offset:45056
v_mfma_f32_16x16x32_bf16 v[68:71], a[160:163], v[80:83], v[68:71]
v_mfma_f32_16x16x32_bf16 v[68:71], a[172:175], v[84:87], v[68:71]
s_barrier
v_mfma_f32_16x16x32_bf16 v[68:71], a[184:187], v[88:91], v[68:71]
global_load_lds_dwordx4 v9, s[38:39] offset:-2048 sc1
v_mfma_f32_16x16x32_bf16 v[68:71], a[196:199], v[92:95], v[68:71]
ds_read_b128 v[112:115], v17 offset:46080
v_mfma_f32_16x16x32_bf16 v[68:71], a[208:211], v[96:99], v[68:71]
v_mfma_f32_16x16x32_bf16 v[72:75], a[152:155], v[76:79], v[72:75]
ds_read_b128 v[116:119], v17 offset:47104
global_load_lds_dwordx4 v9, s[38:39] offset:-1024 sc1
v_mfma_f32_16x16x32_bf16 v[72:75], a[164:167], v[80:83], v[72:75]
v_mfma_f32_16x16x32_bf16 v[72:75], a[176:179], v[84:87], v[72:75]
ds_read_b128 v[120:123], v17 offset:48128
v_mfma_f32_16x16x32_bf16 v[72:75], a[188:191], v[88:91], v[72:75]
global_load_lds_dwordx4 v9, s[38:39] offset:0 sc1
v_mfma_f32_16x16x32_bf16 v[72:75], a[200:203], v[92:95], v[72:75]
v_mfma_f32_16x16x32_bf16 v[72:75], a[212:215], v[96:99], v[72:75]
s_waitcnt lgkmcnt(5)
v_mfma_f32_16x16x32_bf16 v[64:67], a[216:219], v[100:103], v[64:67]
s_waitcnt lgkmcnt(4)
global_load_lds_dwordx4 v9, s[38:39] offset:1024 sc1
v_mfma_f32_16x16x32_bf16 v[64:67], a[228:231], v[104:107], v[64:67]
s_waitcnt lgkmcnt(3)
v_mfma_f32_16x16x32_bf16 v[64:67], a[240:243], v[108:111], v[64:67]
s_waitcnt lgkmcnt(2)
v_mfma_f32_16x16x32_bf16 v[64:67], a[252:255], v[112:115], v[64:67]
s_waitcnt lgkmcnt(1)
global_load_lds_dwordx4 v9, s[38:39] offset:2048 sc1
v_mfma_f32_16x16x32_bf16 v[64:67], v[28:31], v[116:119], v[64:67]
s_waitcnt lgkmcnt(0)
v_mfma_f32_16x16x32_bf16 v[64:67], v[40:43], v[120:123], v[64:67]
v_xor_b32 v17, 0x10000, v17
v_mfma_f32_16x16x32_bf16 v[68:71], a[220:223], v[100:103], v[68:71]
global_load_lds_dwordx4 v9, s[38:39] offset:3072 sc1
v_add_u32 v9, 147456, v9
v_mfma_f32_16x16x32_bf16 v[68:71], a[232:235], v[104:107], v[68:71]
v_mfma_f32_16x16x32_bf16 v[68:71], a[244:247], v[108:111], v[68:71]
s_waitcnt vmcnt(8)
s_barrier
v_mfma_f32_16x16x32_bf16 v[68:71], v[20:23], v[112:115], v[68:71]
ds_read_b128 v[76:79], v17 offset:0
v_min_u32 v9, v9, v10
v_mfma_f32_16x16x32_bf16 v[68:71], v[32:35], v[116:119], v[68:71]
ds_read_b128 v[80:83], v17 offset:1024
v_cvt_pk_bf16_f32 v124, v64, v65
v_mfma_f32_16x16x32_bf16 v[68:71], v[44:47], v[120:123], v[68:71]
ds_read_b128 v[84:87], v17 offset:2048
v_cvt_pk_bf16_f32 v125, v66, v67
v_mfma_f32_16x16x32_bf16 v[72:75], a[224:227], v[100:103], v[72:75]
ds_read_b128 v[88:91], v17 offset:3072
v_mfma_f32_16x16x32_bf16 v[72:75], a[236:239], v[104:107], v[72:75]
ds_read_b128 v[92:95], v17 offset:4096
v_mfma_f32_16x16x32_bf16 v[72:75], a[248:251], v[108:111], v[72:75]
ds_read_b128 v[96:99], v17 offset:5120
v_mfma_f32_16x16x32_bf16 v[72:75], v[24:27], v[112:115], v[72:75]
v_cvt_pk_bf16_f32 v126, v68, v69
v_cvt_pk_bf16_f32 v127, v70, v71
v_mfma_f32_16x16x32_bf16 v[72:75], v[36:39], v[116:119], v[72:75]
v_mfma_f32_16x16x32_bf16 v[72:75], v[48:51], v[120:123], v[72:75]
s_sub_u32 s70, s70, 1
s_cbranch_scc0 .Ldn_top6
v_cvt_pk_bf16_f32 v124, v64, v65
v_cvt_pk_bf16_f32 v125, v66, v67
v_cvt_pk_bf16_f32 v126, v68, v69
v_cvt_pk_bf16_f32 v127, v70, v71
global_store_dwordx4 v11, v[124:127], s[38:39] sc1
v_add_u32 v11, 110592, v11
v_cvt_pk_bf16_f32 v128, v72, v73
v_cvt_pk_bf16_f32 v130, v72, v73
v_cvt_pk_bf16_f32 v129, v74, v75
v_cvt_pk_bf16_f32 v131, v74, v75
v_permlane16_swap_b32 v128, v130
s_nop 0
v_permlane16_swap_b32 v129, v131
s_mov_b64 exec, s[76:77]
global_store_dwordx4 v12, v[128:131], s[38:39] sc1
s_mov_b64 exec, -1
v_add_u32 v12, 110592, v12
s_waitcnt vmcnt(2)
s_barrier
s_waitcnt vmcnt(0)
s_barrier
s_endpgm
.Ldn_w2:
v_add_u32 v19, -1, v14
v_add_u32 v254, -1, v14
v_add_lshl_u32 v252, v16, 0, 7
v_bfi_b32 v252, s69, v252, v15
v_add_lshl_u32 v253, v14, 0, 7
v_bfi_b32 v253, s69, v253, v13
s_mov_b32 s76, 0xffff
s_mov_b32 s77, 0xffff
s_add_u32 m0, s45, 24576
s_xor_b32 s45, s45, 0x10000
s_mov_b32 m0, s45
s_barrier
global_load_lds_dwordx4 v9, s[38:39] offset:-2048 sc1
global_load_lds_dwordx4 v9, s[38:39] offset:-1024 sc1
global_load_lds_dwordx4 v9, s[38:39] offset:0 sc1
global_load_lds_dwordx4 v9, s[38:39] offset:1024 sc1
global_load_lds_dwordx4 v9, s[38:39] offset:2048 sc1
global_load_lds_dwordx4 v9, s[38:39] offset:3072 sc1
v_add_u32 v9, 147456, v9
s_mov_b32 s70, 32
s_waitcnt vmcnt(0) lgkmcnt(0)
s_nop 7
s_branch .Ldn_in8
.p2align 6
.Ldn_top9:
s_waitcnt lgkmcnt(5)
v_mfma_f32_16x16x32_bf16 v[52:55], a[0:3], v[76:79], 0
s_waitcnt lgkmcnt(4)
v_mfma_f32_16x16x32_bf16 v[52:55], a[12:15], v[80:83], v[52:55]
s_waitcnt lgkmcnt(3)
v_mfma_f32_16x16x32_bf16 v[52:55], a[24:27], v[84:87], v[52:55]
s_waitcnt lgkmcnt(2)
v_mfma_f32_16x16x32_bf16 v[52:55], a[36:39], v[88:91], v[52:55]
s_waitcnt lgkmcnt(1)
ds_read_b128 v[100:103], v17 offset:6144
v_cvt_pk_bf16_f32 v128, v72, v73
v_mfma_f32_16x16x32_bf16 v[52:55], a[48:51], v[92:95], v[52:55]
s_waitcnt lgkmcnt(1)
v_mfma_f32_16x16x32_bf16 v[52:55], a[60:63], v[96:99], v[52:55]
s_waitcnt vmcnt(6)
s_barrier
v_mfma_f32_16x16x32_bf16 v[56:59], a[4:7], v[76:79], 0
ds_read_b128 v[104:107], v17 offset:7168
v_cvt_pk_bf16_f32 v130, v72, v73
v_mfma_f32_16x16x32_bf16 v[56:59], a[16:19], v[80:83], v[56:59]
v_cvt_pk_bf16_f32 v129, v74, v75
v_cvt_pk_bf16_f32 v131, v74, v75
v_mfma_f32_16x16x32_bf16 v[56:59], a[28:31], v[84:87], v[56:59]
ds_read_b128 v[108:111], v17 offset:8192
v_mfma_f32_16x16x32_bf16 v[56:59], a[40:43], v[88:91], v[56:59]
global_store_dwordx4 v11, v[124:127], s[38:39] sc1
v_mfma_f32_16x16x32_bf16 v[56:59], a[52:55], v[92:95], v[56:59]
ds_read_b128 v[112:115], v17 offset:9216
v_add_u32 v11, 110592, v11
v_mfma_f32_16x16x32_bf16 v[56:59], a[64:67], v[96:99], v[56:59]
v_permlane16_swap_b32 v128, v130
v_mfma_f32_16x16x32_bf16 v[60:63], a[8:11], v[76:79], 0
ds_read_b128 v[116:119], v17 offset:10240
v_mfma_f32_16x16x32_bf16 v[60:63], a[20:23], v[80:83], v[60:63]
v_permlane16_swap_b32 v129, v131
v_mfma_f32_16x16x32_bf16 v[60:63], a[32:35], v[84:87], v[60:63]
ds_read_b128 v[120:123], v17 offset:11264
v_mfma_f32_16x16x32_bf16 v[60:63], a[44:47], v[88:91], v[60:63]
s_mov_b64 exec, s[76:77]
global_store_dwordx4 v12, v[128:131], s[38:39] sc1
s_mov_b64 exec, -1
v_mfma_f32_16x16x32_bf16 v[60:63], a[56:59], v[92:95], v[60:63]
v_add_u32 v12, 110592, v12
v_mfma_f32_16x16x32_bf16 v[60:63], a[68:71], v[96:99], v[60:63]
s_waitcnt lgkmcnt(5)
v_mfma_f32_16x16x32_bf16 v[52:55], a[72:75], v[100:103], v[52:55]
s_waitcnt lgkmcnt(4)
v_mfma_f32_16x16x32_bf16 v[52:55], a[84:87], v[104:107], v[52:55]
s_waitcnt lgkmcnt(3)
v_mfma_f32_16x16x32_bf16 v[52:55], a[96:99], v[108:111], v[52:55]
s_waitcnt lgkmcnt(2)
v_mfma_f32_16x16x32_bf16 v[52:55], a[108:111], v[112:115], v[52:55]
s_waitcnt lgkmcnt(1)
v_mfma_f32_16x16x32_bf16 v[52:55], a[120:123], v[116:119], v[52:55]
s_waitcnt lgkmcnt(0)
ds_read_b128 v[76:79], v17 offset:12288
v_mfma_f32_16x16x32_bf16 v[52:55], a[132:135], v[120:123], v[52:55]
v_mfma_f32_16x16x32_bf16 v[56:59], a[76:79], v[100:103], v[56:59]
ds_read_b128 v[80:83], v17 offset:13312
v_mfma_f32_16x16x32_bf16 v[56:59], a[88:91], v[104:107], v[56:59]
v_mfma_f32_16x16x32_bf16 v[56:59], a[100:103], v[108:111], v[56:59]
ds_read_b128 v[84:87], v17 offset:14336
v_mfma_f32_16x16x32_bf16 v[56:59], a[112:115], v[112:115], v[56:59]
v_mfma_f32_16x16x32_bf16 v[56:59], a[124:127], v[116:119], v[56:59]
ds_read_b128 v[88:91], v17 offset:15360
v_mfma_f32_16x16x32_bf16 v[56:59], a[136:139], v[120:123], v[56:59]
v_mfma_f32_16x16x32_bf16 v[60:63], a[80:83], v[100:103], v[60:63]
ds_read_b128 v[92:95], v17 offset:16384
v_mfma_f32_16x16x32_bf16 v[60:63], a[92:95], v[104:107], v[60:63]
v_mfma_f32_16x16x32_bf16 v[60:63], a[104:107], v[108:111], v[60:63]
ds_read_b128 v[96:99], v17 offset:17408
v_mfma_f32_16x16x32_bf16 v[60:63], a[116:119], v[112:115], v[60:63]
v_mfma_f32_16x16x32_bf16 v[60:63], a[128:131], v[116:119], v[60:63]
v_mfma_f32_16x16x32_bf16 v[60:63], a[140:143], v[120:123], v[60:63]
s_waitcnt lgkmcnt(5)
v_mfma_f32_16x16x32_bf16 v[52:55], a[144:147], v[76:79], v[52:55]
s_waitcnt lgkmcnt(4)
v_mfma_f32_16x16x32_bf16 v[52:55], a[156:159], v[80:83], v[52:55]
s_waitcnt lgkmcnt(3)
v_mfma_f32_16x16x32_bf16 v[52:55], a[168:171], v[84:87], v[52:55]
s_waitcnt lgkmcnt(2)
v_mfma_f32_16x16x32_bf16 v[52:55], a[180:183], v[88:91], v[52:55]
s_waitcnt lgkmcnt(1)
v_mfma_f32_16x16x32_bf16 v[52:55], a[192:195], v[92:95], v[52:55]
s_xor_b32 s45, s45, 0x10000
ds_read_b128 v[100:103], v17 offset:18432
s_waitcnt lgkmcnt(1)
v_mfma_f32_16x16x32_bf16 v[52:55], a[204:207], v[96:99], v[52:55]
s_add_u32 m0, s45, 0
v_mfma_f32_16x16x32_bf16 v[56:59], a[148:151], v[76:79], v[56:59]
ds_read_b128 v[104:107], v17 offset:19456
ds_read_b128 v[108:111], v17 offset:20480
v_mfma_f32_16x16x32_bf16 v[56:59], a[160:163], v[80:83], v[56:59]
v_mfma_f32_16x16x32_bf16 v[56:59], a[172:175], v[84:87], v[56:59]
s_barrier
v_mfma_f32_16x16x32_bf16 v[56:59], a[184:187], v[88:91], v[56:59]
v_mfma_f32_16x16x32_bf16 v[56:59], a[196:199], v[92:95], v[56:59]
ds_read_b128 v[112:115], v17 offset:21504
global_load_lds_dwordx4 v9, s[38:39] offset:-2048 sc1
v_mfma_f32_16x16x32_bf16 v[56:59], a[208:211], v[96:99], v[56:59]
v_mfma_f32_16x16x32_bf16 v[60:63], a[152:155], v[76:79], v[60:63]
ds_read_b128 v[116:119], v17 offset:22528
)"
R"(v_mfma_f32_16x16x32_bf16 v[60:63], a[164:167], v[80:83], v[60:63]
global_load_lds_dwordx4 v9, s[38:39] offset:-1024 sc1
v_mfma_f32_16x16x32_bf16 v[60:63], a[176:179], v[84:87], v[60:63]
ds_read_b128 v[120:123], v17 offset:23552
v_mfma_f32_16x16x32_bf16 v[60:63], a[188:191], v[88:91], v[60:63]
v_mfma_f32_16x16x32_bf16 v[60:63], a[200:203], v[92:95], v[60:63]
global_load_lds_dwordx4 v9, s[38:39] offset:0 sc1
v_mfma_f32_16x16x32_bf16 v[60:63], a[212:215], v[96:99], v[60:63]
s_waitcnt lgkmcnt(5)
v_mfma_f32_16x16x32_bf16 v[52:55], a[216:219], v[100:103], v[52:55]
s_waitcnt lgkmcnt(4)
v_mfma_f32_16x16x32_bf16 v[52:55], a[228:231], v[104:107], v[52:55]
s_waitcnt lgkmcnt(3)
global_load_lds_dwordx4 v9, s[38:39] offset:1024 sc1
v_mfma_f32_16x16x32_bf16 v[52:55], a[240:243], v[108:111], v[52:55]
s_waitcnt lgkmcnt(2)
v_mfma_f32_16x16x32_bf16 v[52:55], a[252:255], v[112:115], v[52:55]
s_waitcnt lgkmcnt(1)
v_mfma_f32_16x16x32_bf16 v[52:55], v[28:31], v[116:119], v[52:55]
s_waitcnt lgkmcnt(0)
global_load_lds_dwordx4 v9, s[38:39] offset:2048 sc1
v_mfma_f32_16x16x32_bf16 v[52:55], v[40:43], v[120:123], v[52:55]
v_mfma_f32_16x16x32_bf16 v[56:59], a[220:223], v[100:103], v[56:59]
v_mfma_f32_16x16x32_bf16 v[56:59], a[232:235], v[104:107], v[56:59]
v_mfma_f32_16x16x32_bf16 v[56:59], a[244:247], v[108:111], v[56:59]
s_waitcnt vmcnt(7)
s_barrier
v_mfma_f32_16x16x32_bf16 v[56:59], v[20:23], v[112:115], v[56:59]
ds_read_b128 v[76:79], v17 offset:24576
global_load_lds_dwordx4 v9, s[38:39] offset:3072 sc1
v_mfma_f32_16x16x32_bf16 v[56:59], v[32:35], v[116:119], v[56:59]
ds_read_b128 v[80:83], v17 offset:25600
v_add_u32 v9, 147456, v9
v_mfma_f32_16x16x32_bf16 v[56:59], v[44:47], v[120:123], v[56:59]
ds_read_b128 v[84:87], v17 offset:26624
v_min_u32 v9, v9, v10
v_mfma_f32_16x16x32_bf16 v[60:63], a[224:227], v[100:103], v[60:63]
ds_read_b128 v[88:91], v17 offset:27648
v_cvt_pk_bf16_f32 v124, v52, v53
v_mfma_f32_16x16x32_bf16 v[60:63], a[236:239], v[104:107], v[60:63]
ds_read_b128 v[92:95], v17 offset:28672
v_cvt_pk_bf16_f32 v125, v54, v55
v_mfma_f32_16x16x32_bf16 v[60:63], a[248:251], v[108:111], v[60:63]
ds_read_b128 v[96:99], v17 offset:29696
v_mfma_f32_16x16x32_bf16 v[60:63], v[24:27], v[112:115], v[60:63]
v_cvt_pk_bf16_f32 v126, v56, v57
v_cvt_pk_bf16_f32 v127, v58, v59
v_mfma_f32_16x16x32_bf16 v[60:63], v[36:39], v[116:119], v[60:63]
v_mfma_f32_16x16x32_bf16 v[60:63], v[48:51], v[120:123], v[60:63]
s_waitcnt lgkmcnt(5)
v_mfma_f32_16x16x32_bf16 v[64:67], a[0:3], v[76:79], 0
s_waitcnt lgkmcnt(4)
v_mfma_f32_16x16x32_bf16 v[64:67], a[12:15], v[80:83], v[64:67]
s_waitcnt lgkmcnt(3)
v_mfma_f32_16x16x32_bf16 v[64:67], a[24:27], v[84:87], v[64:67]
s_waitcnt lgkmcnt(2)
v_mfma_f32_16x16x32_bf16 v[64:67], a[36:39], v[88:91], v[64:67]
s_waitcnt lgkmcnt(1)
ds_read_b128 v[100:103], v17 offset:30720
v_cvt_pk_bf16_f32 v128, v60, v61
v_mfma_f32_16x16x32_bf16 v[64:67], a[48:51], v[92:95], v[64:67]
s_waitcnt lgkmcnt(1)
v_mfma_f32_16x16x32_bf16 v[64:67], a[60:63], v[96:99], v[64:67]
s_waitcnt vmcnt(6)
s_barrier
v_mfma_f32_16x16x32_bf16 v[68:71], a[4:7], v[76:79], 0
ds_read_b128 v[104:107], v17 offset:31744
v_cvt_pk_bf16_f32 v130, v60, v61
v_mfma_f32_16x16x32_bf16 v[68:71], a[16:19], v[80:83], v[68:71]
v_cvt_pk_bf16_f32 v129, v62, v63
v_cvt_pk_bf16_f32 v131, v62, v63
v_mfma_f32_16x16x32_bf16 v[68:71], a[28:31], v[84:87], v[68:71]
ds_read_b128 v[108:111], v17 offset:32768
v_mfma_f32_16x16x32_bf16 v[68:71], a[40:43], v[88:91], v[68:71]
global_store_dwordx4 v11, v[124:127], s[38:39] sc1
v_mfma_f32_16x16x32_bf16 v[68:71], a[52:55], v[92:95], v[68:71]
ds_read_b128 v[112:115], v17 offset:33792
v_add_u32 v11, 110592, v11
v_mfma_f32_16x16x32_bf16 v[68:71], a[64:67], v[96:99], v[68:71]
v_permlane16_swap_b32 v128, v130
v_mfma_f32_16x16x32_bf16 v[72:75], a[8:11], v[76:79], 0
ds_read_b128 v[116:119], v17 offset:34816
v_mfma_f32_16x16x32_bf16 v[72:75], a[20:23], v[80:83], v[72:75]
v_permlane16_swap_b32 v129, v131
v_mfma_f32_16x16x32_bf16 v[72:75], a[32:35], v[84:87], v[72:75]
ds_read_b128 v[120:123], v17 offset:35840
v_mfma_f32_16x16x32_bf16 v[72:75], a[44:47], v[88:91], v[72:75]
s_mov_b64 exec, s[76:77]
global_store_dwordx4 v12, v[128:131], s[38:39] sc1
s_mov_b64 exec, -1
v_mfma_f32_16x16x32_bf16 v[72:75], a[56:59], v[92:95], v[72:75]
v_add_u32 v12, 110592, v12
v_mfma_f32_16x16x32_bf16 v[72:75], a[68:71], v[96:99], v[72:75]
s_waitcnt lgkmcnt(5)
v_mfma_f32_16x16x32_bf16 v[64:67], a[72:75], v[100:103], v[64:67]
s_waitcnt lgkmcnt(4)
v_mfma_f32_16x16x32_bf16 v[64:67], a[84:87], v[104:107], v[64:67]
s_waitcnt lgkmcnt(3)
v_mfma_f32_16x16x32_bf16 v[64:67], a[96:99], v[108:111], v[64:67]
s_waitcnt lgkmcnt(2)
v_mfma_f32_16x16x32_bf16 v[64:67], a[108:111], v[112:115], v[64:67]
s_waitcnt lgkmcnt(1)
v_mfma_f32_16x16x32_bf16 v[64:67], a[120:123], v[116:119], v[64:67]
s_waitcnt lgkmcnt(0)
ds_read_b128 v[76:79], v17 offset:36864
v_mfma_f32_16x16x32_bf16 v[64:67], a[132:135], v[120:123], v[64:67]
v_mfma_f32_16x16x32_bf16 v[68:71], a[76:79], v[100:103], v[68:71]
ds_read_b128 v[80:83], v17 offset:37888
v_mfma_f32_16x16x32_bf16 v[68:71], a[88:91], v[104:107], v[68:71]
v_mfma_f32_16x16x32_bf16 v[68:71], a[100:103], v[108:111], v[68:71]
ds_read_b128 v[84:87], v17 offset:38912
v_mfma_f32_16x16x32_bf16 v[68:71], a[112:115], v[112:115], v[68:71]
v_mfma_f32_16x16x32_bf16 v[68:71], a[124:127], v[116:119], v[68:71]
ds_read_b128 v[88:91], v17 offset:39936
v_mfma_f32_16x16x32_bf16 v[68:71], a[136:139], v[120:123], v[68:71]
v_mfma_f32_16x16x32_bf16 v[72:75], a[80:83], v[100:103], v[72:75]
ds_read_b128 v[92:95], v17 offset:40960
v_mfma_f32_16x16x32_bf16 v[72:75], a[92:95], v[104:107], v[72:75]
v_mfma_f32_16x16x32_bf16 v[72:75], a[104:107], v[108:111], v[72:75]
ds_read_b128 v[96:99], v17 offset:41984
v_mfma_f32_16x16x32_bf16 v[72:75], a[116:119], v[112:115], v[72:75]
v_mfma_f32_16x16x32_bf16 v[72:75], a[128:131], v[116:119], v[72:75]
v_mfma_f32_16x16x32_bf16 v[72:75], a[140:143], v[120:123], v[72:75]
s_waitcnt lgkmcnt(5)
v_mfma_f32_16x16x32_bf16 v[64:67], a[144:147], v[76:79], v[64:67]
s_waitcnt lgkmcnt(4)
v_mfma_f32_16x16x32_bf16 v[64:67], a[156:159], v[80:83], v[64:67]
s_waitcnt lgkmcnt(3)
v_mfma_f32_16x16x32_bf16 v[64:67], a[168:171], v[84:87], v[64:67]
.Ldn_in8:
s_waitcnt lgkmcnt(2)
v_mfma_f32_16x16x32_bf16 v[64:67], a[180:183], v[88:91], v[64:67]
s_waitcnt lgkmcnt(1)
v_mfma_f32_16x16x32_bf16 v[64:67], a[192:195], v[92:95], v[64:67]
s_waitcnt lgkmcnt(0)
ds_read_b128 v[100:103], v17 offset:43008
v_mfma_f32_16x16x32_bf16 v[64:67], a[204:207], v[96:99], v[64:67]
s_add_u32 m0, s45, 24576
v_mfma_f32_16x16x32_bf16 v[68:71], a[148:151], v[76:79], v[68:71]
ds_read_b128 v[104:107], v17 offset:44032
ds_read_b128 v[108:111], v17 offset:45056
v_mfma_f32_16x16x32_bf16 v[68:71], a[160:163], v[80:83], v[68:71]
v_mfma_f32_16x16x32_bf16 v[68:71], a[172:175], v[84:87], v[68:71]
s_barrier
v_mfma_f32_16x16x32_bf16 v[68:71], a[184:187], v[88:91], v[68:71]
v_mfma_f32_16x16x32_bf16 v[68:71], a[196:199], v[92:95], v[68:71]
ds_read_b128 v[112:115], v17 offset:46080
global_load_lds_dwordx4 v9, s[38:39] offset:-2048 sc1
v_mfma_f32_16x16x32_bf16 v[68:71], a[208:211], v[96:99], v[68:71]
v_mfma_f32_16x16x32_bf16 v[72:75], a[152:155], v[76:79], v[72:75]
ds_read_b128 v[116:119], v17 offset:47104
v_mfma_f32_16x16x32_bf16 v[72:75], a[164:167], v[80:83], v[72:75]
global_load_lds_dwordx4 v9, s[38:39] offset:-1024 sc1
v_mfma_f32_16x16x32_bf16 v[72:75], a[176:179], v[84:87], v[72:75]
ds_read_b128 v[120:123], v17 offset:48128
v_mfma_f32_16x16x32_bf16 v[72:75], a[188:191], v[88:91], v[72:75]
v_mfma_f32_16x16x32_bf16 v[72:75], a[200:203], v[92:95], v[72:75]
global_load_lds_dwordx4 v9, s[38:39] offset:0 sc1
v_mfma_f32_16x16x32_bf16 v[72:75], a[212:215], v[96:99], v[72:75]
s_waitcnt lgkmcnt(5)
v_mfma_f32_16x16x32_bf16 v[64:67], a[216:219], v[100:103], v[64:67]
s_waitcnt lgkmcnt(4)
v_mfma_f32_16x16x32_bf16 v[64:67], a[228:231], v[104:107], v[64:67]
s_waitcnt lgkmcnt(3)
global_load_lds_dwordx4 v9, s[38:39] offset:1024 sc1
v_mfma_f32_16x16x32_bf16 v[64:67], a[240:243], v[108:111], v[64:67]
s_waitcnt lgkmcnt(2)
v_mfma_f32_16x16x32_bf16 v[64:67], a[252:255], v[112:115], v[64:67]
s_waitcnt lgkmcnt(1)
v_mfma_f32_16x16x32_bf16 v[64:67], v[28:31], v[116:119], v[64:67]
s_waitcnt lgkmcnt(0)
global_load_lds_dwordx4 v9, s[38:39] offset:2048 sc1
v_mfma_f32_16x16x32_bf16 v[64:67], v[40:43], v[120:123], v[64:67]
v_xor_b32 v17, 0x10000, v17
v_mfma_f32_16x16x32_bf16 v[68:71], a[220:223], v[100:103], v[68:71]
v_mfma_f32_16x16x32_bf16 v[68:71], a[232:235], v[104:107], v[68:71]
v_mfma_f32_16x16x32_bf16 v[68:71], a[244:247], v[108:111], v[68:71]
s_waitcnt vmcnt(7)
s_barrier
v_mfma_f32_16x16x32_bf16 v[68:71], v[20:23], v[112:115], v[68:71]
ds_read_b128 v[76:79], v17 offset:0
global_load_lds_dwordx4 v9, s[38:39] offset:3072 sc1
v_mfma_f32_16x16x32_bf16 v[68:71], v[32:35], v[116:119], v[68:71]
ds_read_b128 v[80:83], v17 offset:1024
v_add_u32 v9, 147456, v9
v_mfma_f32_16x16x32_bf16 v[68:71], v[44:47], v[120:123], v[68:71]
ds_read_b128 v[84:87], v17 offset:2048
v_min_u32 v9, v9, v10
v_mfma_f32_16x16x32_bf16 v[72:75], a[224:227], v[100:103], v[72:75]
ds_read_b128 v[88:91], v17 offset:3072
v_cvt_pk_bf16_f32 v124, v64, v65
v_mfma_f32_16x16x32_bf16 v[72:75], a[236:239], v[104:107], v[72:75]
ds_read_b128 v[92:95], v17 offset:4096
v_cvt_pk_bf16_f32 v125, v66, v67
v_mfma_f32_16x16x32_bf16 v[72:75], a[248:251], v[108:111], v[72:75]
ds_read_b128 v[96:99], v17 offset:5120
v_mfma_f32_16x16x32_bf16 v[72:75], v[24:27], v[112:115], v[72:75]
v_cvt_pk_bf16_f32 v126, v68, v69
v_cvt_pk_bf16_f32 v127, v70, v71
v_mfma_f32_16x16x32_bf16 v[72:75], v[36:39], v[116:119], v[72:75]
v_mfma_f32_16x16x32_bf16 v[72:75], v[48:51], v[120:123], v[72:75]
s_sub_u32 s70, s70, 1
s_cbranch_scc0 .Ldn_top9
v_cvt_pk_bf16_f32 v124, v64, v65
v_cvt_pk_bf16_f32 v125, v66, v67
v_cvt_pk_bf16_f32 v126, v68, v69
v_cvt_pk_bf16_f32 v127, v70, v71
global_store_dwordx4 v11, v[124:127], s[38:39] sc1
v_add_u32 v11, 110592, v11
v_cvt_pk_bf16_f32 v128, v72, v73
v_cvt_pk_bf16_f32 v130, v72, v73
v_cvt_pk_bf16_f32 v129, v74, v75
v_cvt_pk_bf16_f32 v131, v74, v75
v_permlane16_swap_b32 v128, v130
s_nop 0
v_permlane16_swap_b32 v129, v131
s_mov_b64 exec, s[76:77]
global_store_dwordx4 v12, v[128:131], s[38:39] sc1
s_mov_b64 exec, -1
v_add_u32 v12, 110592, v12
s_waitcnt vmcnt(2)
s_barrier
s_waitcnt vmcnt(0)
s_barrier
s_endpgm
.Ldn_w3:
v_add_u32 v19, -1, v14
v_add_u32 v254, -1, v14
v_add_lshl_u32 v252, v16, 0, 7
v_bfi_b32 v252, s69, v252, v15
v_add_lshl_u32 v253, v14, 0, 7
v_bfi_b32 v253, s69, v253, v13
s_mov_b32 s76, 0xffff
s_mov_b32 s77, 0xffff
s_add_u32 m0, s45, 24576
s_xor_b32 s45, s45, 0x10000
s_mov_b32 m0, s45
s_barrier
global_load_lds_dwordx4 v9, s[38:39] offset:-2048 sc1
global_load_lds_dwordx4 v9, s[38:39] offset:-1024 sc1
global_load_lds_dwordx4 v9, s[38:39] offset:0 sc1
global_load_lds_dwordx4 v9, s[38:39] offset:1024 sc1
global_load_lds_dwordx4 v9, s[38:39] offset:2048 sc1
global_load_lds_dwordx4 v9, s[38:39] offset:3072 sc1
v_add_u32 v9, 147456, v9
s_mov_b32 s70, 32
s_waitcnt vmcnt(0) lgkmcnt(0)
s_nop 7
s_branch .Ldn_in11
.p2align 6
.Ldn_top12:
s_waitcnt lgkmcnt(5)
v_mfma_f32_16x16x32_bf16 v[52:55], a[0:3], v[76:79], 0
s_waitcnt lgkmcnt(4)
v_mfma_f32_16x16x32_bf16 v[52:55], a[12:15], v[80:83], v[52:55]
s_waitcnt lgkmcnt(3)
v_mfma_f32_16x16x32_bf16 v[52:55], a[24:27], v[84:87], v[52:55]
s_waitcnt lgkmcnt(2)
v_mfma_f32_16x16x32_bf16 v[52:55], a[36:39], v[88:91], v[52:55]
s_waitcnt lgkmcnt(1)
ds_read_b128 v[100:103], v17 offset:6144
v_cvt_pk_bf16_f32 v128, v72, v73
v_mfma_f32_16x16x32_bf16 v[52:55], a[48:51], v[92:95], v[52:55]
s_waitcnt lgkmcnt(1)
v_mfma_f32_16x16x32_bf16 v[52:55], a[60:63], v[96:99], v[52:55]
s_waitcnt vmcnt(7)
s_barrier
v_mfma_f32_16x16x32_bf16 v[56:59], a[4:7], v[76:79], 0
ds_read_b128 v[104:107], v17 offset:7168
v_cvt_pk_bf16_f32 v130, v72, v73
v_mfma_f32_16x16x32_bf16 v[56:59], a[16:19], v[80:83], v[56:59]
s_mov_b64 exec, 1
global_store_dword v252, v16, s[38:39] sc1
s_mov_b64 exec, -1
v_mfma_f32_16x16x32_bf16 v[56:59], a[28:31], v[84:87], v[56:59]
ds_read_b128 v[108:111], v17 offset:8192
v_add_u32 v16, 1, v16
v_mfma_f32_16x16x32_bf16 v[56:59], a[40:43], v[88:91], v[56:59]
v_add_lshl_u32 v252, v16, 0, 7
v_bfi_b32 v252, s69, v252, v15
v_mfma_f32_16x16x32_bf16 v[56:59], a[52:55], v[92:95], v[56:59]
ds_read_b128 v[112:115], v17 offset:9216
v_cvt_pk_bf16_f32 v129, v74, v75
v_mfma_f32_16x16x32_bf16 v[56:59], a[64:67], v[96:99], v[56:59]
global_store_dwordx4 v11, v[124:127], s[38:39] sc1
v_mfma_f32_16x16x32_bf16 v[60:63], a[8:11], v[76:79], 0
ds_read_b128 v[116:119], v17 offset:10240
v_add_u32 v11, 110592, v11
v_mfma_f32_16x16x32_bf16 v[60:63], a[20:23], v[80:83], v[60:63]
v_cvt_pk_bf16_f32 v131, v74, v75
v_mfma_f32_16x16x32_bf16 v[60:63], a[32:35], v[84:87], v[60:63]
ds_read_b128 v[120:123], v17 offset:11264
v_mfma_f32_16x16x32_bf16 v[60:63], a[44:47], v[88:91], v[60:63]
v_permlane16_swap_b32 v128, v130
v_mfma_f32_16x16x32_bf16 v[60:63], a[56:59], v[92:95], v[60:63]
v_permlane16_swap_b32 v129, v131
v_mfma_f32_16x16x32_bf16 v[60:63], a[68:71], v[96:99], v[60:63]
s_waitcnt lgkmcnt(5)
s_mov_b64 exec, s[76:77]
global_store_dwordx4 v12, v[128:131], s[38:39] sc1
s_mov_b64 exec, -1
v_mfma_f32_16x16x32_bf16 v[52:55], a[72:75], v[100:103], v[52:55]
s_waitcnt lgkmcnt(4)
v_add_u32 v12, 110592, v12
v_mfma_f32_16x16x32_bf16 v[52:55], a[84:87], v[104:107], v[52:55]
s_waitcnt lgkmcnt(3)
v_mfma_f32_16x16x32_bf16 v[52:55], a[96:99], v[108:111], v[52:55]
s_waitcnt lgkmcnt(2)
v_mfma_f32_16x16x32_bf16 v[52:55], a[108:111], v[112:115], v[52:55]
s_waitcnt lgkmcnt(1)
v_mfma_f32_16x16x32_bf16 v[52:55], a[120:123], v[116:119], v[52:55]
s_waitcnt lgkmcnt(0)
ds_read_b128 v[76:79], v17 offset:12288
v_mfma_f32_16x16x32_bf16 v[52:55], a[132:135], v[120:123], v[52:55]
v_mfma_f32_16x16x32_bf16 v[56:59], a[76:79], v[100:103], v[56:59]
ds_read_b128 v[80:83], v17 offset:13312
v_mfma_f32_16x16x32_bf16 v[56:59], a[88:91], v[104:107], v[56:59]
v_mfma_f32_16x16x32_bf16 v[56:59], a[100:103], v[108:111], v[56:59]
ds_read_b128 v[84:87], v17 offset:14336
v_mfma_f32_16x16x32_bf16 v[56:59], a[112:115], v[112:115], v[56:59]
v_mfma_f32_16x16x32_bf16 v[56:59], a[124:127], v[116:119], v[56:59]
ds_read_b128 v[88:91], v17 offset:15360
v_mfma_f32_16x16x32_bf16 v[56:59], a[136:139], v[120:123], v[56:59]
v_mfma_f32_16x16x32_bf16 v[60:63], a[80:83], v[100:103], v[60:63]
ds_read_b128 v[92:95], v17 offset:16384
v_mfma_f32_16x16x32_bf16 v[60:63], a[92:95], v[104:107], v[60:63]
v_mfma_f32_16x16x32_bf16 v[60:63], a[104:107], v[108:111], v[60:63]
ds_read_b128 v[96:99], v17 offset:17408
v_mfma_f32_16x16x32_bf16 v[60:63], a[116:119], v[112:115], v[60:63]
v_mfma_f32_16x16x32_bf16 v[60:63], a[128:131], v[116:119], v[60:63]
v_mfma_f32_16x16x32_bf16 v[60:63], a[140:143], v[120:123], v[60:63]
s_waitcnt lgkmcnt(5)
v_mfma_f32_16x16x32_bf16 v[52:55], a[144:147], v[76:79], v[52:55]
s_waitcnt lgkmcnt(4)
v_mfma_f32_16x16x32_bf16 v[52:55], a[156:159], v[80:83], v[52:55]
s_waitcnt lgkmcnt(3)
v_mfma_f32_16x16x32_bf16 v[52:55], a[168:171], v[84:87], v[52:55]
s_waitcnt lgkmcnt(2)
v_mfma_f32_16x16x32_bf16 v[52:55], a[180:183], v[88:91], v[52:55]
s_waitcnt lgkmcnt(1)
v_mfma_f32_16x16x32_bf16 v[52:55], a[192:195], v[92:95], v[52:55]
s_xor_b32 s45, s45, 0x10000
ds_read_b128 v[100:103], v17 offset:18432
s_waitcnt lgkmcnt(1)
v_mfma_f32_16x16x32_bf16 v[52:55], a[204:207], v[96:99], v[52:55]
s_add_u32 m0, s45, 0
v_mfma_f32_16x16x32_bf16 v[56:59], a[148:151], v[76:79], v[56:59]
ds_read_b128 v[104:107], v17 offset:19456
ds_read_b128 v[108:111], v17 offset:20480
v_mfma_f32_16x16x32_bf16 v[56:59], a[160:163], v[80:83], v[56:59]
v_mfma_f32_16x16x32_bf16 v[56:59], a[172:175], v[84:87], v[56:59]
s_barrier
v_mfma_f32_16x16x32_bf16 v[56:59], a[184:187], v[88:91], v[56:59]
v_mfma_f32_16x16x32_bf16 v[56:59], a[196:199], v[92:95], v[56:59]
ds_read_b128 v[112:115], v17 offset:21504
v_mfma_f32_16x16x32_bf16 v[56:59], a[208:211], v[96:99], v[56:59]
global_load_lds_dwordx4 v9, s[38:39] offset:-2048 sc1
v_mfma_f32_16x16x32_bf16 v[60:63], a[152:155], v[76:79], v[60:63]
ds_read_b128 v[116:119], v17 offset:22528
v_mfma_f32_16x16x32_bf16 v[60:63], a[164:167], v[80:83], v[60:63]
v_mfma_f32_16x16x32_bf16 v[60:63], a[176:179], v[84:87], v[60:63]
ds_read_b128 v[120:123], v17 offset:23552
global_load_lds_dwordx4 v9, s[38:39] offset:-1024 sc1
v_mfma_f32_16x16x32_bf16 v[60:63], a[188:191], v[88:91], v[60:63]
v_mfma_f32_16x16x32_bf16 v[60:63], a[200:203], v[92:95], v[60:63]
v_mfma_f32_16x16x32_bf16 v[60:63], a[212:215], v[96:99], v[60:63]
s_waitcnt lgkmcnt(5)
global_load_lds_dwordx4 v9, s[38:39] offset:0 sc1
v_mfma_f32_16x16x32_bf16 v[52:55], a[216:219], v[100:103], v[52:55]
s_waitcnt lgkmcnt(4)
v_mfma_f32_16x16x32_bf16 v[52:55], a[228:231], v[104:107], v[52:55]
s_waitcnt lgkmcnt(3)
v_mfma_f32_16x16x32_bf16 v[52:55], a[240:243], v[108:111], v[52:55]
s_waitcnt lgkmcnt(2)
global_load_lds_dwordx4 v9, s[38:39] offset:1024 sc1
v_mfma_f32_16x16x32_bf16 v[52:55], a[252:255], v[112:115], v[52:55]
s_waitcnt lgkmcnt(1)
v_mfma_f32_16x16x32_bf16 v[52:55], v[28:31], v[116:119], v[52:55]
s_waitcnt lgkmcnt(0)
v_mfma_f32_16x16x32_bf16 v[52:55], v[40:43], v[120:123], v[52:55]
global_load_lds_dwordx4 v9, s[38:39] offset:2048 sc1
v_mfma_f32_16x16x32_bf16 v[56:59], a[220:223], v[100:103], v[56:59]
v_mfma_f32_16x16x32_bf16 v[56:59], a[232:235], v[104:107], v[56:59]
v_mfma_f32_16x16x32_bf16 v[56:59], a[244:247], v[108:111], v[56:59]
s_waitcnt vmcnt(8)
s_barrier
v_mfma_f32_16x16x32_bf16 v[56:59], v[20:23], v[112:115], v[56:59]
ds_read_b128 v[76:79], v17 offset:24576
global_load_lds_dwordx4 v9, s[38:39] offset:3072 sc1
v_mfma_f32_16x16x32_bf16 v[56:59], v[32:35], v[116:119], v[56:59]
ds_read_b128 v[80:83], v17 offset:25600
v_add_u32 v9, 147456, v9
v_mfma_f32_16x16x32_bf16 v[56:59], v[44:47], v[120:123], v[56:59]
ds_read_b128 v[84:87], v17 offset:26624
v_min_u32 v9, v9, v10
v_mfma_f32_16x16x32_bf16 v[60:63], a[224:227], v[100:103], v[60:63]
ds_read_b128 v[88:91], v17 offset:27648
v_cvt_pk_bf16_f32 v124, v52, v53
v_mfma_f32_16x16x32_bf16 v[60:63], a[236:239], v[104:107], v[60:63]
ds_read_b128 v[92:95], v17 offset:28672
)"
R"(v_cvt_pk_bf16_f32 v125, v54, v55
v_mfma_f32_16x16x32_bf16 v[60:63], a[248:251], v[108:111], v[60:63]
ds_read_b128 v[96:99], v17 offset:29696
v_mfma_f32_16x16x32_bf16 v[60:63], v[24:27], v[112:115], v[60:63]
v_cvt_pk_bf16_f32 v126, v56, v57
v_cvt_pk_bf16_f32 v127, v58, v59
v_mfma_f32_16x16x32_bf16 v[60:63], v[36:39], v[116:119], v[60:63]
v_mfma_f32_16x16x32_bf16 v[60:63], v[48:51], v[120:123], v[60:63]
s_waitcnt lgkmcnt(5)
v_mfma_f32_16x16x32_bf16 v[64:67], a[0:3], v[76:79], 0
s_waitcnt lgkmcnt(4)
v_mfma_f32_16x16x32_bf16 v[64:67], a[12:15], v[80:83], v[64:67]
s_waitcnt lgkmcnt(3)
v_mfma_f32_16x16x32_bf16 v[64:67], a[24:27], v[84:87], v[64:67]
s_waitcnt lgkmcnt(2)
v_mfma_f32_16x16x32_bf16 v[64:67], a[36:39], v[88:91], v[64:67]
s_waitcnt lgkmcnt(1)
ds_read_b128 v[100:103], v17 offset:30720
v_cvt_pk_bf16_f32 v128, v60, v61
v_mfma_f32_16x16x32_bf16 v[64:67], a[48:51], v[92:95], v[64:67]
s_waitcnt lgkmcnt(1)
v_mfma_f32_16x16x32_bf16 v[64:67], a[60:63], v[96:99], v[64:67]
s_waitcnt vmcnt(6)
s_barrier
v_mfma_f32_16x16x32_bf16 v[68:71], a[4:7], v[76:79], 0
ds_read_b128 v[104:107], v17 offset:31744
v_cvt_pk_bf16_f32 v130, v60, v61
v_mfma_f32_16x16x32_bf16 v[68:71], a[16:19], v[80:83], v[68:71]
v_cvt_pk_bf16_f32 v129, v62, v63
v_cvt_pk_bf16_f32 v131, v62, v63
v_mfma_f32_16x16x32_bf16 v[68:71], a[28:31], v[84:87], v[68:71]
ds_read_b128 v[108:111], v17 offset:32768
v_mfma_f32_16x16x32_bf16 v[68:71], a[40:43], v[88:91], v[68:71]
global_store_dwordx4 v11, v[124:127], s[38:39] sc1
v_mfma_f32_16x16x32_bf16 v[68:71], a[52:55], v[92:95], v[68:71]
ds_read_b128 v[112:115], v17 offset:33792
v_add_u32 v11, 110592, v11
v_mfma_f32_16x16x32_bf16 v[68:71], a[64:67], v[96:99], v[68:71]
v_permlane16_swap_b32 v128, v130
v_mfma_f32_16x16x32_bf16 v[72:75], a[8:11], v[76:79], 0
ds_read_b128 v[116:119], v17 offset:34816
v_mfma_f32_16x16x32_bf16 v[72:75], a[20:23], v[80:83], v[72:75]
v_permlane16_swap_b32 v129, v131
v_mfma_f32_16x16x32_bf16 v[72:75], a[32:35], v[84:87], v[72:75]
ds_read_b128 v[120:123], v17 offset:35840
v_mfma_f32_16x16x32_bf16 v[72:75], a[44:47], v[88:91], v[72:75]
s_mov_b64 exec, s[76:77]
global_store_dwordx4 v12, v[128:131], s[38:39] sc1
s_mov_b64 exec, -1
v_mfma_f32_16x16x32_bf16 v[72:75], a[56:59], v[92:95], v[72:75]
v_add_u32 v12, 110592, v12
v_mfma_f32_16x16x32_bf16 v[72:75], a[68:71], v[96:99], v[72:75]
s_waitcnt lgkmcnt(5)
s_mov_b64 exec, 1
global_store_dword v252, v16, s[38:39] sc1
s_mov_b64 exec, -1
v_mfma_f32_16x16x32_bf16 v[64:67], a[72:75], v[100:103], v[64:67]
s_waitcnt lgkmcnt(4)
v_add_u32 v16, 1, v16
v_add_lshl_u32 v252, v16, 0, 7
v_mfma_f32_16x16x32_bf16 v[64:67], a[84:87], v[104:107], v[64:67]
s_waitcnt lgkmcnt(3)
v_bfi_b32 v252, s69, v252, v15
v_mfma_f32_16x16x32_bf16 v[64:67], a[96:99], v[108:111], v[64:67]
s_waitcnt lgkmcnt(2)
v_mfma_f32_16x16x32_bf16 v[64:67], a[108:111], v[112:115], v[64:67]
s_waitcnt lgkmcnt(1)
v_mfma_f32_16x16x32_bf16 v[64:67], a[120:123], v[116:119], v[64:67]
s_waitcnt lgkmcnt(0)
ds_read_b128 v[76:79], v17 offset:36864
v_mfma_f32_16x16x32_bf16 v[64:67], a[132:135], v[120:123], v[64:67]
v_mfma_f32_16x16x32_bf16 v[68:71], a[76:79], v[100:103], v[68:71]
ds_read_b128 v[80:83], v17 offset:37888
v_mfma_f32_16x16x32_bf16 v[68:71], a[88:91], v[104:107], v[68:71]
v_mfma_f32_16x16x32_bf16 v[68:71], a[100:103], v[108:111], v[68:71]
ds_read_b128 v[84:87], v17 offset:38912
v_mfma_f32_16x16x32_bf16 v[68:71], a[112:115], v[112:115], v[68:71]
v_mfma_f32_16x16x32_bf16 v[68:71], a[124:127], v[116:119], v[68:71]
ds_read_b128 v[88:91], v17 offset:39936
v_mfma_f32_16x16x32_bf16 v[68:71], a[136:139], v[120:123], v[68:71]
v_mfma_f32_16x16x32_bf16 v[72:75], a[80:83], v[100:103], v[72:75]
ds_read_b128 v[92:95], v17 offset:40960
v_mfma_f32_16x16x32_bf16 v[72:75], a[92:95], v[104:107], v[72:75]
v_mfma_f32_16x16x32_bf16 v[72:75], a[104:107], v[108:111], v[72:75]
ds_read_b128 v[96:99], v17 offset:41984
v_mfma_f32_16x16x32_bf16 v[72:75], a[116:119], v[112:115], v[72:75]
v_mfma_f32_16x16x32_bf16 v[72:75], a[128:131], v[116:119], v[72:75]
v_mfma_f32_16x16x32_bf16 v[72:75], a[140:143], v[120:123], v[72:75]
s_waitcnt lgkmcnt(5)
v_mfma_f32_16x16x32_bf16 v[64:67], a[144:147], v[76:79], v[64:67]
s_waitcnt lgkmcnt(4)
v_mfma_f32_16x16x32_bf16 v[64:67], a[156:159], v[80:83], v[64:67]
s_waitcnt lgkmcnt(3)
v_mfma_f32_16x16x32_bf16 v[64:67], a[168:171], v[84:87], v[64:67]
.Ldn_in11:
s_waitcnt lgkmcnt(2)
v_mfma_f32_16x16x32_bf16 v[64:67], a[180:183], v[88:91], v[64:67]
s_waitcnt lgkmcnt(1)
v_mfma_f32_16x16x32_bf16 v[64:67], a[192:195], v[92:95], v[64:67]
s_waitcnt lgkmcnt(0)
ds_read_b128 v[100:103], v17 offset:43008
v_mfma_f32_16x16x32_bf16 v[64:67], a[204:207], v[96:99], v[64:67]
s_add_u32 m0, s45, 24576
v_mfma_f32_16x16x32_bf16 v[68:71], a[148:151], v[76:79], v[68:71]
ds_read_b128 v[104:107], v17 offset:44032
ds_read_b128 v[108:111], v17 offset:45056
v_mfma_f32_16x16x32_bf16 v[68:71], a[160:163], v[80:83], v[68:71]
v_mfma_f32_16x16x32_bf16 v[68:71], a[172:175], v[84:87], v[68:71]
s_barrier
v_mfma_f32_16x16x32_bf16 v[68:71], a[184:187], v[88:91], v[68:71]
v_mfma_f32_16x16x32_bf16 v[68:71], a[196:199], v[92:95], v[68:71]
ds_read_b128 v[112:115], v17 offset:46080
v_mfma_f32_16x16x32_bf16 v[68:71], a[208:211], v[96:99], v[68:71]
global_load_lds_dwordx4 v9, s[38:39] offset:-2048 sc1
v_mfma_f32_16x16x32_bf16 v[72:75], a[152:155], v[76:79], v[72:75]
ds_read_b128 v[116:119], v17 offset:47104
v_mfma_f32_16x16x32_bf16 v[72:75], a[164:167], v[80:83], v[72:75]
v_mfma_f32_16x16x32_bf16 v[72:75], a[176:179], v[84:87], v[72:75]
ds_read_b128 v[120:123], v17 offset:48128
global_load_lds_dwordx4 v9, s[38:39] offset:-1024 sc1
v_mfma_f32_16x16x32_bf16 v[72:75], a[188:191], v[88:91], v[72:75]
v_mfma_f32_16x16x32_bf16 v[72:75], a[200:203], v[92:95], v[72:75]
v_mfma_f32_16x16x32_bf16 v[72:75], a[212:215], v[96:99], v[72:75]
s_waitcnt lgkmcnt(5)
global_load_lds_dwordx4 v9, s[38:39] offset:0 sc1
v_mfma_f32_16x16x32_bf16 v[64:67], a[216:219], v[100:103], v[64:67]
s_waitcnt lgkmcnt(4)
v_mfma_f32_16x16x32_bf16 v[64:67], a[228:231], v[104:107], v[64:67]
s_waitcnt lgkmcnt(3)
v_mfma_f32_16x16x32_bf16 v[64:67], a[240:243], v[108:111], v[64:67]
s_waitcnt lgkmcnt(2)
global_load_lds_dwordx4 v9, s[38:39] offset:1024 sc1
v_mfma_f32_16x16x32_bf16 v[64:67], a[252:255], v[112:115], v[64:67]
s_waitcnt lgkmcnt(1)
v_mfma_f32_16x16x32_bf16 v[64:67], v[28:31], v[116:119], v[64:67]
s_waitcnt lgkmcnt(0)
v_mfma_f32_16x16x32_bf16 v[64:67], v[40:43], v[120:123], v[64:67]
v_xor_b32 v17, 0x10000, v17
global_load_lds_dwordx4 v9, s[38:39] offset:2048 sc1
v_mfma_f32_16x16x32_bf16 v[68:71], a[220:223], v[100:103], v[68:71]
v_mfma_f32_16x16x32_bf16 v[68:71], a[232:235], v[104:107], v[68:71]
v_mfma_f32_16x16x32_bf16 v[68:71], a[244:247], v[108:111], v[68:71]
s_waitcnt vmcnt(8)
s_barrier
v_mfma_f32_16x16x32_bf16 v[68:71], v[20:23], v[112:115], v[68:71]
ds_read_b128 v[76:79], v17 offset:0
global_load_lds_dwordx4 v9, s[38:39] offset:3072 sc1
v_mfma_f32_16x16x32_bf16 v[68:71], v[32:35], v[116:119], v[68:71]
ds_read_b128 v[80:83], v17 offset:1024
v_add_u32 v9, 147456, v9
v_mfma_f32_16x16x32_bf16 v[68:71], v[44:47], v[120:123], v[68:71]
ds_read_b128 v[84:87], v17 offset:2048
v_min_u32 v9, v9, v10
v_mfma_f32_16x16x32_bf16 v[72:75], a[224:227], v[100:103], v[72:75]
ds_read_b128 v[88:91], v17 offset:3072
v_cvt_pk_bf16_f32 v124, v64, v65
v_mfma_f32_16x16x32_bf16 v[72:75], a[236:239], v[104:107], v[72:75]
ds_read_b128 v[92:95], v17 offset:4096
v_cvt_pk_bf16_f32 v125, v66, v67
v_mfma_f32_16x16x32_bf16 v[72:75], a[248:251], v[108:111], v[72:75]
ds_read_b128 v[96:99], v17 offset:5120
v_mfma_f32_16x16x32_bf16 v[72:75], v[24:27], v[112:115], v[72:75]
v_cvt_pk_bf16_f32 v126, v68, v69
v_cvt_pk_bf16_f32 v127, v70, v71
v_mfma_f32_16x16x32_bf16 v[72:75], v[36:39], v[116:119], v[72:75]
v_mfma_f32_16x16x32_bf16 v[72:75], v[48:51], v[120:123], v[72:75]
s_sub_u32 s70, s70, 1
s_cbranch_scc0 .Ldn_top12
v_cvt_pk_bf16_f32 v124, v64, v65
v_cvt_pk_bf16_f32 v125, v66, v67
v_cvt_pk_bf16_f32 v126, v68, v69
v_cvt_pk_bf16_f32 v127, v70, v71
global_store_dwordx4 v11, v[124:127], s[38:39] sc1
v_add_u32 v11, 110592, v11
v_cvt_pk_bf16_f32 v128, v72, v73
v_cvt_pk_bf16_f32 v130, v72, v73
v_cvt_pk_bf16_f32 v129, v74, v75
v_cvt_pk_bf16_f32 v131, v74, v75
v_permlane16_swap_b32 v128, v130
s_nop 0
v_permlane16_swap_b32 v129, v131
s_mov_b64 exec, s[76:77]
global_store_dwordx4 v12, v[128:131], s[38:39] sc1
s_mov_b64 exec, -1
v_add_u32 v12, 110592, v12
s_waitcnt vmcnt(2)
s_barrier
s_mov_b64 exec, 1
global_store_dword v252, v16, s[38:39] sc1
s_mov_b64 exec, -1
v_add_u32 v16, 1, v16
v_add_lshl_u32 v252, v16, 0, 7
v_bfi_b32 v252, s69, v252, v15
s_waitcnt vmcnt(1)
s_barrier
s_mov_b64 exec, 1
global_store_dword v252, v16, s[38:39] sc1
s_mov_b64 exec, -1
s_endpgm
.Ldn_w4:
v_add_u32 v19, -1, v14
v_add_u32 v254, -1, v14
v_add_lshl_u32 v252, v16, 0, 7
v_bfi_b32 v252, s69, v252, v15
v_add_lshl_u32 v253, v14, 0, 7
v_bfi_b32 v253, s69, v253, v13
s_mov_b32 s76, 0xffff
s_mov_b32 s77, 0xffff
s_add_u32 m0, s45, 24576
v_add_lshl_u32 v253, v14, 0, 7
v_bfi_b32 v253, s69, v253, v13
global_load_dword v19, v253, s[38:39] sc1
s_waitcnt vmcnt(0)
v_sub_u32 v19, v19, v14
v_cmp_lt_u32 vcc, 0x7f, v19
s_cbranch_vccnz .Ldn_sp19
.Ldn_rt20:
v_add_u32 v14, 1, v14
s_xor_b32 s45, s45, 0x10000
s_mov_b32 m0, s45
s_barrier
global_load_lds_dwordx4 v9, s[38:39] offset:-2048 sc1
global_load_lds_dwordx4 v9, s[38:39] offset:-1024 sc1
global_load_lds_dwordx4 v9, s[38:39] offset:0 sc1
global_load_lds_dwordx4 v9, s[38:39] offset:1024 sc1
global_load_lds_dwordx4 v9, s[38:39] offset:2048 sc1
global_load_lds_dwordx4 v9, s[38:39] offset:3072 sc1
v_add_u32 v9, 147456, v9
s_mov_b32 s70, 32
s_waitcnt vmcnt(0) lgkmcnt(0)
s_nop 7
s_branch .Ldn_in18
.p2align 6
.Ldn_top21:
s_waitcnt lgkmcnt(5)
v_mfma_f32_16x16x32_bf16 v[52:55], a[0:3], v[76:79], 0
s_waitcnt lgkmcnt(4)
v_mfma_f32_16x16x32_bf16 v[52:55], a[12:15], v[80:83], v[52:55]
s_waitcnt lgkmcnt(3)
global_load_dword v19, v253, s[38:39] sc1
v_mfma_f32_16x16x32_bf16 v[52:55], a[24:27], v[84:87], v[52:55]
s_waitcnt lgkmcnt(2)
v_mfma_f32_16x16x32_bf16 v[52:55], a[36:39], v[88:91], v[52:55]
s_waitcnt lgkmcnt(1)
ds_read_b128 v[100:103], v17 offset:6144
v_cvt_pk_bf16_f32 v128, v72, v73
v_mfma_f32_16x16x32_bf16 v[52:55], a[48:51], v[92:95], v[52:55]
s_waitcnt lgkmcnt(1)
v_mfma_f32_16x16x32_bf16 v[52:55], a[60:63], v[96:99], v[52:55]
s_waitcnt vmcnt(7)
s_barrier
v_mfma_f32_16x16x32_bf16 v[56:59], a[4:7], v[76:79], 0
ds_read_b128 v[104:107], v17 offset:7168
v_cvt_pk_bf16_f32 v130, v72, v73
v_mfma_f32_16x16x32_bf16 v[56:59], a[16:19], v[80:83], v[56:59]
v_cvt_pk_bf16_f32 v129, v74, v75
v_cvt_pk_bf16_f32 v131, v74, v75
v_mfma_f32_16x16x32_bf16 v[56:59], a[28:31], v[84:87], v[56:59]
ds_read_b128 v[108:111], v17 offset:8192
v_mfma_f32_16x16x32_bf16 v[56:59], a[40:43], v[88:91], v[56:59]
v_permlane16_swap_b32 v128, v130
v_mfma_f32_16x16x32_bf16 v[56:59], a[52:55], v[92:95], v[56:59]
ds_read_b128 v[112:115], v17 offset:9216
v_mfma_f32_16x16x32_bf16 v[56:59], a[64:67], v[96:99], v[56:59]
global_store_dwordx4 v11, v[124:127], s[38:39] sc1
v_mfma_f32_16x16x32_bf16 v[60:63], a[8:11], v[76:79], 0
ds_read_b128 v[116:119], v17 offset:10240
v_add_u32 v11, 110592, v11
v_mfma_f32_16x16x32_bf16 v[60:63], a[20:23], v[80:83], v[60:63]
v_permlane16_swap_b32 v129, v131
v_mfma_f32_16x16x32_bf16 v[60:63], a[32:35], v[84:87], v[60:63]
ds_read_b128 v[120:123], v17 offset:11264
v_mfma_f32_16x16x32_bf16 v[60:63], a[44:47], v[88:91], v[60:63]
s_mov_b64 exec, s[76:77]
global_store_dwordx4 v12, v[128:131], s[38:39] sc1
s_mov_b64 exec, -1
v_mfma_f32_16x16x32_bf16 v[60:63], a[56:59], v[92:95], v[60:63]
v_add_u32 v12, 110592, v12
v_mfma_f32_16x16x32_bf16 v[60:63], a[68:71], v[96:99], v[60:63]
s_waitcnt lgkmcnt(5)
v_mfma_f32_16x16x32_bf16 v[52:55], a[72:75], v[100:103], v[52:55]
s_waitcnt lgkmcnt(4)
v_mfma_f32_16x16x32_bf16 v[52:55], a[84:87], v[104:107], v[52:55]
s_waitcnt lgkmcnt(3)
v_mfma_f32_16x16x32_bf16 v[52:55], a[96:99], v[108:111], v[52:55]
s_waitcnt lgkmcnt(2)
v_mfma_f32_16x16x32_bf16 v[52:55], a[108:111], v[112:115], v[52:55]
s_waitcnt lgkmcnt(1)
v_mfma_f32_16x16x32_bf16 v[52:55], a[120:123], v[116:119], v[52:55]
s_waitcnt lgkmcnt(0)
ds_read_b128 v[76:79], v17 offset:12288
v_mfma_f32_16x16x32_bf16 v[52:55], a[132:135], v[120:123], v[52:55]
v_mfma_f32_16x16x32_bf16 v[56:59], a[76:79], v[100:103], v[56:59]
ds_read_b128 v[80:83], v17 offset:13312
v_mfma_f32_16x16x32_bf16 v[56:59], a[88:91], v[104:107], v[56:59]
v_mfma_f32_16x16x32_bf16 v[56:59], a[100:103], v[108:111], v[56:59]
ds_read_b128 v[84:87], v17 offset:14336
v_mfma_f32_16x16x32_bf16 v[56:59], a[112:115], v[112:115], v[56:59]
v_mfma_f32_16x16x32_bf16 v[56:59], a[124:127], v[116:119], v[56:59]
ds_read_b128 v[88:91], v17 offset:15360
v_mfma_f32_16x16x32_bf16 v[56:59], a[136:139], v[120:123], v[56:59]
v_mfma_f32_16x16x32_bf16 v[60:63], a[80:83], v[100:103], v[60:63]
ds_read_b128 v[92:95], v17 offset:16384
v_mfma_f32_16x16x32_bf16 v[60:63], a[92:95], v[104:107], v[60:63]
v_mfma_f32_16x16x32_bf16 v[60:63], a[104:107], v[108:111], v[60:63]
ds_read_b128 v[96:99], v17 offset:17408
v_mfma_f32_16x16x32_bf16 v[60:63], a[116:119], v[112:115], v[60:63]
v_mfma_f32_16x16x32_bf16 v[60:63], a[128:131], v[116:119], v[60:63]
v_mfma_f32_16x16x32_bf16 v[60:63], a[140:143], v[120:123], v[60:63]
s_waitcnt lgkmcnt(5)
v_mfma_f32_16x16x32_bf16 v[52:55], a[144:147], v[76:79], v[52:55]
s_waitcnt lgkmcnt(4)
v_mfma_f32_16x16x32_bf16 v[52:55], a[156:159], v[80:83], v[52:55]
s_waitcnt lgkmcnt(3)
v_mfma_f32_16x16x32_bf16 v[52:55], a[168:171], v[84:87], v[52:55]
s_waitcnt lgkmcnt(2)
v_mfma_f32_16x16x32_bf16 v[52:55], a[180:183], v[88:91], v[52:55]
s_waitcnt vmcnt(2)
v_sub_u32 v19, v19, v14
v_cmp_lt_u32 vcc, 0x7f, v19
s_waitcnt lgkmcnt(1)
v_mfma_f32_16x16x32_bf16 v[52:55], a[192:195], v[92:95], v[52:55]
s_xor_b32 s45, s45, 0x10000
ds_read_b128 v[100:103], v17 offset:18432
s_waitcnt lgkmcnt(1)
v_mfma_f32_16x16x32_bf16 v[52:55], a[204:207], v[96:99], v[52:55]
s_add_u32 m0, s45, 0
v_mfma_f32_16x16x32_bf16 v[56:59], a[148:151], v[76:79], v[56:59]
s_cbranch_vccnz .Ldn_sp14
.Ldn_rt15:
ds_read_b128 v[104:107], v17 offset:19456
ds_read_b128 v[108:111], v17 offset:20480
v_mfma_f32_16x16x32_bf16 v[56:59], a[160:163], v[80:83], v[56:59]
v_mfma_f32_16x16x32_bf16 v[56:59], a[172:175], v[84:87], v[56:59]
s_barrier
v_mfma_f32_16x16x32_bf16 v[56:59], a[184:187], v[88:91], v[56:59]
v_add_u32 v14, 1, v14
v_add_lshl_u32 v253, v14, 0, 7
v_mfma_f32_16x16x32_bf16 v[56:59], a[196:199], v[92:95], v[56:59]
ds_read_b128 v[112:115], v17 offset:21504
v_bfi_b32 v253, s69, v253, v13
v_mfma_f32_16x16x32_bf16 v[56:59], a[208:211], v[96:99], v[56:59]
v_mfma_f32_16x16x32_bf16 v[60:63], a[152:155], v[76:79], v[60:63]
ds_read_b128 v[116:119], v17 offset:22528
global_load_lds_dwordx4 v9, s[38:39] offset:-2048 sc1
v_mfma_f32_16x16x32_bf16 v[60:63], a[164:167], v[80:83], v[60:63]
v_mfma_f32_16x16x32_bf16 v[60:63], a[176:179], v[84:87], v[60:63]
ds_read_b128 v[120:123], v17 offset:23552
v_mfma_f32_16x16x32_bf16 v[60:63], a[188:191], v[88:91], v[60:63]
global_load_lds_dwordx4 v9, s[38:39] offset:-1024 sc1
v_mfma_f32_16x16x32_bf16 v[60:63], a[200:203], v[92:95], v[60:63]
v_mfma_f32_16x16x32_bf16 v[60:63], a[212:215], v[96:99], v[60:63]
s_waitcnt lgkmcnt(5)
v_mfma_f32_16x16x32_bf16 v[52:55], a[216:219], v[100:103], v[52:55]
s_waitcnt lgkmcnt(4)
global_load_lds_dwordx4 v9, s[38:39] offset:0 sc1
v_mfma_f32_16x16x32_bf16 v[52:55], a[228:231], v[104:107], v[52:55]
s_waitcnt lgkmcnt(3)
v_mfma_f32_16x16x32_bf16 v[52:55], a[240:243], v[108:111], v[52:55]
s_waitcnt lgkmcnt(2)
v_mfma_f32_16x16x32_bf16 v[52:55], a[252:255], v[112:115], v[52:55]
s_waitcnt lgkmcnt(1)
global_load_lds_dwordx4 v9, s[38:39] offset:1024 sc1
v_mfma_f32_16x16x32_bf16 v[52:55], v[28:31], v[116:119], v[52:55]
s_waitcnt lgkmcnt(0)
v_mfma_f32_16x16x32_bf16 v[52:55], v[40:43], v[120:123], v[52:55]
v_mfma_f32_16x16x32_bf16 v[56:59], a[220:223], v[100:103], v[56:59]
global_load_lds_dwordx4 v9, s[38:39] offset:2048 sc1
v_mfma_f32_16x16x32_bf16 v[56:59], a[232:235], v[104:107], v[56:59]
v_mfma_f32_16x16x32_bf16 v[56:59], a[244:247], v[108:111], v[56:59]
s_barrier
v_mfma_f32_16x16x32_bf16 v[56:59], v[20:23], v[112:115], v[56:59]
ds_read_b128 v[76:79], v17 offset:24576
global_load_lds_dwordx4 v9, s[38:39] offset:3072 sc1
v_mfma_f32_16x16x32_bf16 v[56:59], v[32:35], v[116:119], v[56:59]
ds_read_b128 v[80:83], v17 offset:25600
v_add_u32 v9, 147456, v9
v_mfma_f32_16x16x32_bf16 v[56:59], v[44:47], v[120:123], v[56:59]
ds_read_b128 v[84:87], v17 offset:26624
v_min_u32 v9, v9, v10
v_mfma_f32_16x16x32_bf16 v[60:63], a[224:227], v[100:103], v[60:63]
ds_read_b128 v[88:91], v17 offset:27648
v_cvt_pk_bf16_f32 v124, v52, v53
v_mfma_f32_16x16x32_bf16 v[60:63], a[236:239], v[104:107], v[60:63]
ds_read_b128 v[92:95], v17 offset:28672
v_cvt_pk_bf16_f32 v125, v54, v55
v_mfma_f32_16x16x32_bf16 v[60:63], a[248:251], v[108:111], v[60:63]
ds_read_b128 v[96:99], v17 offset:29696
v_mfma_f32_16x16x32_bf16 v[60:63], v[24:27], v[112:115], v[60:63]
v_cvt_pk_bf16_f32 v126, v56, v57
v_cvt_pk_bf16_f32 v127, v58, v59
v_mfma_f32_16x16x32_bf16 v[60:63], v[36:39], v[116:119], v[60:63]
v_mfma_f32_16x16x32_bf16 v[60:63], v[48:51], v[120:123], v[60:63]
s_waitcnt lgkmcnt(5)
v_mfma_f32_16x16x32_bf16 v[64:67], a[0:3], v[76:79], 0
s_waitcnt lgkmcnt(4)
v_mfma_f32_16x16x32_bf16 v[64:67], a[12:15], v[80:83], v[64:67]
s_waitcnt lgkmcnt(3)
global_load_dword v254, v253, s[38:39] sc1
v_mfma_f32_16x16x32_bf16 v[64:67], a[24:27], v[84:87], v[64:67]
)"
R"(s_waitcnt lgkmcnt(2)
v_mfma_f32_16x16x32_bf16 v[64:67], a[36:39], v[88:91], v[64:67]
s_waitcnt lgkmcnt(1)
ds_read_b128 v[100:103], v17 offset:30720
v_cvt_pk_bf16_f32 v128, v60, v61
v_mfma_f32_16x16x32_bf16 v[64:67], a[48:51], v[92:95], v[64:67]
s_waitcnt lgkmcnt(1)
v_mfma_f32_16x16x32_bf16 v[64:67], a[60:63], v[96:99], v[64:67]
s_waitcnt vmcnt(7)
s_barrier
v_mfma_f32_16x16x32_bf16 v[68:71], a[4:7], v[76:79], 0
ds_read_b128 v[104:107], v17 offset:31744
v_cvt_pk_bf16_f32 v130, v60, v61
v_mfma_f32_16x16x32_bf16 v[68:71], a[16:19], v[80:83], v[68:71]
v_cvt_pk_bf16_f32 v129, v62, v63
v_cvt_pk_bf16_f32 v131, v62, v63
v_mfma_f32_16x16x32_bf16 v[68:71], a[28:31], v[84:87], v[68:71]
ds_read_b128 v[108:111], v17 offset:32768
v_mfma_f32_16x16x32_bf16 v[68:71], a[40:43], v[88:91], v[68:71]
v_permlane16_swap_b32 v128, v130
v_mfma_f32_16x16x32_bf16 v[68:71], a[52:55], v[92:95], v[68:71]
ds_read_b128 v[112:115], v17 offset:33792
v_mfma_f32_16x16x32_bf16 v[68:71], a[64:67], v[96:99], v[68:71]
global_store_dwordx4 v11, v[124:127], s[38:39] sc1
v_mfma_f32_16x16x32_bf16 v[72:75], a[8:11], v[76:79], 0
ds_read_b128 v[116:119], v17 offset:34816
v_add_u32 v11, 110592, v11
v_mfma_f32_16x16x32_bf16 v[72:75], a[20:23], v[80:83], v[72:75]
v_permlane16_swap_b32 v129, v131
v_mfma_f32_16x16x32_bf16 v[72:75], a[32:35], v[84:87], v[72:75]
ds_read_b128 v[120:123], v17 offset:35840
v_mfma_f32_16x16x32_bf16 v[72:75], a[44:47], v[88:91], v[72:75]
s_mov_b64 exec, s[76:77]
global_store_dwordx4 v12, v[128:131], s[38:39] sc1
s_mov_b64 exec, -1
v_mfma_f32_16x16x32_bf16 v[72:75], a[56:59], v[92:95], v[72:75]
v_add_u32 v12, 110592, v12
v_mfma_f32_16x16x32_bf16 v[72:75], a[68:71], v[96:99], v[72:75]
s_waitcnt lgkmcnt(5)
v_mfma_f32_16x16x32_bf16 v[64:67], a[72:75], v[100:103], v[64:67]
s_waitcnt lgkmcnt(4)
v_mfma_f32_16x16x32_bf16 v[64:67], a[84:87], v[104:107], v[64:67]
s_waitcnt lgkmcnt(3)
v_mfma_f32_16x16x32_bf16 v[64:67], a[96:99], v[108:111], v[64:67]
s_waitcnt lgkmcnt(2)
v_mfma_f32_16x16x32_bf16 v[64:67], a[108:111], v[112:115], v[64:67]
s_waitcnt lgkmcnt(1)
v_mfma_f32_16x16x32_bf16 v[64:67], a[120:123], v[116:119], v[64:67]
s_waitcnt lgkmcnt(0)
ds_read_b128 v[76:79], v17 offset:36864
v_mfma_f32_16x16x32_bf16 v[64:67], a[132:135], v[120:123], v[64:67]
v_mfma_f32_16x16x32_bf16 v[68:71], a[76:79], v[100:103], v[68:71]
ds_read_b128 v[80:83], v17 offset:37888
v_mfma_f32_16x16x32_bf16 v[68:71], a[88:91], v[104:107], v[68:71]
v_mfma_f32_16x16x32_bf16 v[68:71], a[100:103], v[108:111], v[68:71]
ds_read_b128 v[84:87], v17 offset:38912
v_mfma_f32_16x16x32_bf16 v[68:71], a[112:115], v[112:115], v[68:71]
v_mfma_f32_16x16x32_bf16 v[68:71], a[124:127], v[116:119], v[68:71]
ds_read_b128 v[88:91], v17 offset:39936
v_mfma_f32_16x16x32_bf16 v[68:71], a[136:139], v[120:123], v[68:71]
v_mfma_f32_16x16x32_bf16 v[72:75], a[80:83], v[100:103], v[72:75]
ds_read_b128 v[92:95], v17 offset:40960
v_mfma_f32_16x16x32_bf16 v[72:75], a[92:95], v[104:107], v[72:75]
v_mfma_f32_16x16x32_bf16 v[72:75], a[104:107], v[108:111], v[72:75]
ds_read_b128 v[96:99], v17 offset:41984
v_mfma_f32_16x16x32_bf16 v[72:75], a[116:119], v[112:115], v[72:75]
v_mfma_f32_16x16x32_bf16 v[72:75], a[128:131], v[116:119], v[72:75]
v_mfma_f32_16x16x32_bf16 v[72:75], a[140:143], v[120:123], v[72:75]
s_waitcnt lgkmcnt(5)
v_mfma_f32_16x16x32_bf16 v[64:67], a[144:147], v[76:79], v[64:67]
s_waitcnt lgkmcnt(4)
v_mfma_f32_16x16x32_bf16 v[64:67], a[156:159], v[80:83], v[64:67]
s_waitcnt lgkmcnt(3)
v_mfma_f32_16x16x32_bf16 v[64:67], a[168:171], v[84:87], v[64:67]
.Ldn_in18:
s_waitcnt lgkmcnt(2)
v_mfma_f32_16x16x32_bf16 v[64:67], a[180:183], v[88:91], v[64:67]
s_waitcnt vmcnt(2)
v_sub_u32 v254, v254, v14
v_cmp_lt_u32 vcc, 0x7f, v254
s_waitcnt lgkmcnt(1)
v_mfma_f32_16x16x32_bf16 v[64:67], a[192:195], v[92:95], v[64:67]
s_waitcnt lgkmcnt(0)
ds_read_b128 v[100:103], v17 offset:43008
v_mfma_f32_16x16x32_bf16 v[64:67], a[204:207], v[96:99], v[64:67]
s_add_u32 m0, s45, 24576
v_mfma_f32_16x16x32_bf16 v[68:71], a[148:151], v[76:79], v[68:71]
s_cbranch_vccnz .Ldn_sp16
.Ldn_rt17:
ds_read_b128 v[104:107], v17 offset:44032
ds_read_b128 v[108:111], v17 offset:45056
v_mfma_f32_16x16x32_bf16 v[68:71], a[160:163], v[80:83], v[68:71]
v_mfma_f32_16x16x32_bf16 v[68:71], a[172:175], v[84:87], v[68:71]
s_barrier
v_mfma_f32_16x16x32_bf16 v[68:71], a[184:187], v[88:91], v[68:71]
v_add_u32 v14, 1, v14
v_add_lshl_u32 v253, v14, 0, 7
v_mfma_f32_16x16x32_bf16 v[68:71], a[196:199], v[92:95], v[68:71]
ds_read_b128 v[112:115], v17 offset:46080
v_bfi_b32 v253, s69, v253, v13
v_mfma_f32_16x16x32_bf16 v[68:71], a[208:211], v[96:99], v[68:71]
v_mfma_f32_16x16x32_bf16 v[72:75], a[152:155], v[76:79], v[72:75]
ds_read_b128 v[116:119], v17 offset:47104
global_load_lds_dwordx4 v9, s[38:39] offset:-2048 sc1
v_mfma_f32_16x16x32_bf16 v[72:75], a[164:167], v[80:83], v[72:75]
v_mfma_f32_16x16x32_bf16 v[72:75], a[176:179], v[84:87], v[72:75]
ds_read_b128 v[120:123], v17 offset:48128
v_mfma_f32_16x16x32_bf16 v[72:75], a[188:191], v[88:91], v[72:75]
global_load_lds_dwordx4 v9, s[38:39] offset:-1024 sc1
v_mfma_f32_16x16x32_bf16 v[72:75], a[200:203], v[92:95], v[72:75]
v_mfma_f32_16x16x32_bf16 v[72:75], a[212:215], v[96:99], v[72:75]
s_waitcnt lgkmcnt(5)
v_mfma_f32_16x16x32_bf16 v[64:67], a[216:219], v[100:103], v[64:67]
s_waitcnt lgkmcnt(4)
global_load_lds_dwordx4 v9, s[38:39] offset:0 sc1
v_mfma_f32_16x16x32_bf16 v[64:67], a[228:231], v[104:107], v[64:67]
s_waitcnt lgkmcnt(3)
v_mfma_f32_16x16x32_bf16 v[64:67], a[240:243], v[108:111], v[64:67]
s_waitcnt lgkmcnt(2)
v_mfma_f32_16x16x32_bf16 v[64:67], a[252:255], v[112:115], v[64:67]
s_waitcnt lgkmcnt(1)
global_load_lds_dwordx4 v9, s[38:39] offset:1024 sc1
v_mfma_f32_16x16x32_bf16 v[64:67], v[28:31], v[116:119], v[64:67]
s_waitcnt lgkmcnt(0)
v_mfma_f32_16x16x32_bf16 v[64:67], v[40:43], v[120:123], v[64:67]
v_xor_b32 v17, 0x10000, v17
v_mfma_f32_16x16x32_bf16 v[68:71], a[220:223], v[100:103], v[68:71]
global_load_lds_dwordx4 v9, s[38:39] offset:2048 sc1
v_mfma_f32_16x16x32_bf16 v[68:71], a[232:235], v[104:107], v[68:71]
v_mfma_f32_16x16x32_bf16 v[68:71], a[244:247], v[108:111], v[68:71]
s_barrier
v_mfma_f32_16x16x32_bf16 v[68:71], v[20:23], v[112:115], v[68:71]
ds_read_b128 v[76:79], v17 offset:0
global_load_lds_dwordx4 v9, s[38:39] offset:3072 sc1
v_mfma_f32_16x16x32_bf16 v[68:71], v[32:35], v[116:119], v[68:71]
ds_read_b128 v[80:83], v17 offset:1024
v_add_u32 v9, 147456, v9
v_mfma_f32_16x16x32_bf16 v[68:71], v[44:47], v[120:123], v[68:71]
ds_read_b128 v[84:87], v17 offset:2048
v_min_u32 v9, v9, v10
v_mfma_f32_16x16x32_bf16 v[72:75], a[224:227], v[100:103], v[72:75]
ds_read_b128 v[88:91], v17 offset:3072
v_cvt_pk_bf16_f32 v124, v64, v65
v_mfma_f32_16x16x32_bf16 v[72:75], a[236:239], v[104:107], v[72:75]
ds_read_b128 v[92:95], v17 offset:4096
v_cvt_pk_bf16_f32 v125, v66, v67
v_mfma_f32_16x16x32_bf16 v[72:75], a[248:251], v[108:111], v[72:75]
ds_read_b128 v[96:99], v17 offset:5120
v_mfma_f32_16x16x32_bf16 v[72:75], v[24:27], v[112:115], v[72:75]
v_cvt_pk_bf16_f32 v126, v68, v69
v_cvt_pk_bf16_f32 v127, v70, v71
v_mfma_f32_16x16x32_bf16 v[72:75], v[36:39], v[116:119], v[72:75]
v_mfma_f32_16x16x32_bf16 v[72:75], v[48:51], v[120:123], v[72:75]
s_sub_u32 s70, s70, 1
s_cbranch_scc0 .Ldn_top21
v_cvt_pk_bf16_f32 v124, v64, v65
v_cvt_pk_bf16_f32 v125, v66, v67
v_cvt_pk_bf16_f32 v126, v68, v69
v_cvt_pk_bf16_f32 v127, v70, v71
global_store_dwordx4 v11, v[124:127], s[38:39] sc1
v_add_u32 v11, 110592, v11
v_cvt_pk_bf16_f32 v128, v72, v73
v_cvt_pk_bf16_f32 v130, v72, v73
v_cvt_pk_bf16_f32 v129, v74, v75
v_cvt_pk_bf16_f32 v131, v74, v75
v_permlane16_swap_b32 v128, v130
s_nop 0
v_permlane16_swap_b32 v129, v131
s_mov_b64 exec, s[76:77]
global_store_dwordx4 v12, v[128:131], s[38:39] sc1
s_mov_b64 exec, -1
v_add_u32 v12, 110592, v12
s_waitcnt vmcnt(2)
s_barrier
s_waitcnt vmcnt(0)
s_barrier
s_endpgm
.Ldn_sp14:
s_sleep 1
v_add_lshl_u32 v253, v14, 0, 7
v_bfi_b32 v253, s69, v253, v13
global_load_dword v19, v253, s[38:39] sc1
s_add_u32 s71, s71, 1
s_waitcnt vmcnt(0)
v_sub_u32 v19, v19, v14
v_cmp_lt_u32 vcc, 0x7f, v19
s_cbranch_vccz .Ldn_rt15
s_cmp_lt_u32 s71, 3000000
s_cbranch_scc1 .Ldn_sp14
s_branch .Ldn_rt15
.Ldn_sp16:
s_sleep 1
v_add_lshl_u32 v253, v14, 0, 7
v_bfi_b32 v253, s69, v253, v13
global_load_dword v254, v253, s[38:39] sc1
s_add_u32 s71, s71, 1
s_waitcnt vmcnt(0)
v_sub_u32 v254, v254, v14
v_cmp_lt_u32 vcc, 0x7f, v254
s_cbranch_vccz .Ldn_rt17
s_cmp_lt_u32 s71, 3000000
s_cbranch_scc1 .Ldn_sp16
s_branch .Ldn_rt17
.Ldn_sp19:
s_sleep 1
v_add_lshl_u32 v253, v14, 0, 7
v_bfi_b32 v253, s69, v253, v13
global_load_dword v19, v253, s[38:39] sc1
s_add_u32 s71, s71, 1
s_waitcnt vmcnt(0)
v_sub_u32 v19, v19, v14
v_cmp_lt_u32 vcc, 0x7f, v19
s_cbranch_vccz .Ldn_rt20
s_cmp_lt_u32 s71, 3000000
s_cbranch_scc1 .Ldn_sp19
s_branch .Ldn_rt20
.Le2:
)"
    :: "{v[0:31]}"(va), "{s[36:51]}"(sa) : "v255", "a255", "s99", "vcc", "memory");
  } else {
    const unsigned gu_i = jj == 4 ? (xc ^ 1) * 18 + 17 : xc * 18 + jj - 14;
    const unsigned r = lane & 15, g = lane >> 4, q = r >> 2, p = lane & 3;
    const unsigned ii = gu_i, gam = (ii % 72) >> 1, rho = ii / 72 * 2 + (ii & 1), row0 = rho * RP + PAD, pc0 = wave * 6;
    const unsigned bk = gam >> 2, qd = gam & 3, od = (lane >> 3) & 1, un = g * 8 + (lane & 7), jr = qd * 4 + p, jc = (un >> 2) * 8 + od * 4;
    u32x32 va; u32x16 sa, sb;
    va[0] = g * 9216 + ((r ^ (g << 2)) << 4) + 4096 + (((r ^ (g << 2)) >> 3) & 1) * 640;
    va[1] = (8 * g + q) * 256 + (((4 * wave + p) ^ (q << 2)) << 4);
    va[2] = WS_X1 + row0 * X1P + pc0 * 1024 + 2048 + lane * 16;
    va[3] = WS_H + (row0 - 1) * HT + (gam / 6 * 24 + (gam % 6) * 2 + (wave >> 1) * 12 + (wave & 1)) * 1024 + lane * 16;
    va[4] = WS_FL + rho * FR + FX1 + (lane & 1) * FL;
    va[5] = 0u;
    va[6] = WS_FL + rho * FR + FH + gam * FL;
    va[7] = 0u - 2;
    va[8] = lane * 16;
    va[9] = WS_X1 + (row0 + 63) * X1P + pc0 * 1024 + 2048 + lane * 16;
    va[10] = WS_P + row0 * PT + gam * 3072 + od * 512 + un * 16;
    va[11] = WS_X1 + row0 * X1P + bk * 2048 + (un >> 2) * 256 + jr * 16 + od * 8;
    va[12] = WS_X1 + row0 * X1P + XT + jr * 8;
    va[13] = ((rho * 32 + 31) * 16 + qd * 4 + g) * 1152 + bk * 128 + r * 8;
    va[14] = 49152 + wave * 1024 + p * 144 + jc * 2;
    va[15] = 49152 + wave * 1024 + g * 144 + r * 8;
    va[16] = WS_FL + rho * FR + FP + (bk / 3 * 6 + min(lane, 5u)) * FL;
    va[17] = 0u;
    va[18] = WS_FL + rho * FR + FX2 + gam * FL;
    va[19] = 0u;
    va[20] = 0u;
    va[21] = 0u;
    va[22] = (g == q && p < 2) ? (0x3F80u << (16 * p)) : 0u;
    va[23] = (g == q && p >= 2) ? (0x3F80u << (16 * (p - 2))) : 0u;
    va[24] = 0u;
    va[25] = 0u;
    sa[0] = 12 + wave; sa[2] = (unsigned)(unsigned long long)ws; sa[3] = (unsigned)((unsigned long long)ws >> 32);
    sa[4] = uni((unsigned)(unsigned long long)(wg + gam / 6 * 1536 + (gam % 6) * 128 + wave * (16 * 9216)));
    sa[5] = uni((unsigned)((unsigned long long)(wg + gam / 6 * 1536 + (gam % 6) * 128 + wave * (16 * 9216)) >> 32));
    sa[6] = uni((unsigned)(unsigned long long)(wu + gam / 6 * 1536 + (gam % 6) * 128 + wave * (16 * 9216)));
    sa[7] = uni((unsigned)((unsigned long long)(wu + gam / 6 * 1536 + (gam % 6) * 128 + wave * (16 * 9216)) >> 32));
    sa[8] = uni(wave * 4096);
    sa[9] = uni(pc0 * 1024 + 2048);
    sa[10] = uni((unsigned)(unsigned long long)(ws + WS_X2));
    sa[11] = uni((unsigned)((unsigned long long)(ws + WS_X2) >> 32));
    sa[12] = uni((unsigned)(unsigned long long)(out + 32 * XT));
    sa[13] = uni((unsigned)((unsigned long long)(out + 32 * XT) >> 32));
    sa[14] = uni((unsigned)(unsigned long long)(ws + WS_XD));
    sa[15] = uni((unsigned)((unsigned long long)(ws + WS_XD) >> 32));
    sb[0] = uni((unsigned)(unsigned long long)(ws - PT));
    sb[1] = uni((unsigned)((unsigned long long)(ws - PT) >> 32));
    sb[2] = uni((unsigned)(unsigned long long)(ws - X1P));
    sb[3] = uni((unsigned)((unsigned long long)(ws - X1P) >> 32));
    sb[4] = uni((unsigned)(unsigned long long)(ws + WS_X2));
    sb[5] = uni((unsigned)((unsigned long long)(ws + WS_X2) >> 32));
    sb[6] = uni((unsigned)(unsigned long long)(ws + WS_XD));
    sb[7] = uni((unsigned)((unsigned long long)(ws + WS_XD) >> 32));
    sb[8] = uni((unsigned)(unsigned long long)(ws + WS_XD));
    sb[9] = uni((unsigned)((unsigned long long)(ws + WS_XD) >> 32));
    sb[10] = uni(0u);
    asm volatile(
R"(.Lb3:
s_movk_i32 s69, 0x380
s_mov_b32 s71, 0
s_mov_b32 s74, 0
s_load_dword s37, s[38:39], 0x0
s_nop 1
v_add_u32 v166, 35840, v0
v_add_u32 v167, 71680, v0
v_add_u32 v168, 107520, v0
s_sleep 20
s_add_u32 m0, s44, 4096
s_nop 0
global_load_lds_dwordx4 v0, s[40:41] offset:-4096 nt
global_load_lds_dwordx4 v166, s[40:41] offset:-3072 nt
global_load_lds_dwordx4 v167, s[40:41] offset:-2048 nt
global_load_lds_dwordx4 v168, s[40:41] offset:-1024 nt
s_add_u32 s40, s40, 589824
s_addc_u32 s41, s41, 0
s_add_u32 m0, s44, 20480
s_nop 0
global_load_lds_dwordx4 v0, s[40:41] offset:-4096 nt
global_load_lds_dwordx4 v166, s[40:41] offset:-3072 nt
global_load_lds_dwordx4 v167, s[40:41] offset:-2048 nt
global_load_lds_dwordx4 v168, s[40:41] offset:-1024 nt
s_add_u32 s40, s40, 589824
s_addc_u32 s41, s41, 0
s_add_u32 m0, s44, 36864
s_nop 0
global_load_lds_dwordx4 v0, s[40:41] offset:-4096 nt
global_load_lds_dwordx4 v166, s[40:41] offset:-3072 nt
global_load_lds_dwordx4 v167, s[40:41] offset:-2048 nt
global_load_lds_dwordx4 v168, s[40:41] offset:-1024 nt
s_add_u32 s40, s40, 589824
s_addc_u32 s41, s41, 0
v_add_u32 v169, 0x10000, v1
v_mov_b32 v186, 0x3F803F80
v_mov_b32 v187, 0x3F803F80
v_mov_b32 v188, 0x3F803F80
v_mov_b32 v189, 0x3F803F80
s_waitcnt lgkmcnt(0)
s_mul_i32 s37, s37, 0x9E3779B1
s_add_u32 s37, s37, 0x7F4A7C15
v_add_u32 v5, s37, v5
v_add_u32 v7, s37, v7
v_add_u32 v17, s37, v17
s_waitcnt vmcnt(8)
s_barrier
s_add_u32 m0, s44, 53248
ds_read_b64_tr_b16 a[0:1], v1 offset:0
ds_read_b64_tr_b16 a[2:3], v1 offset:1024
global_load_lds_dwordx4 v0, s[40:41] offset:-4096 nt
ds_read_b64_tr_b16 a[4:5], v1 offset:8
global_load_lds_dwordx4 v166, s[40:41] offset:-3072 nt
ds_read_b64_tr_b16 a[6:7], v1 offset:1032
global_load_lds_dwordx4 v167, s[40:41] offset:-2048 nt
ds_read_b64_tr_b16 a[8:9], v1 offset:8192
global_load_lds_dwordx4 v168, s[40:41] offset:-1024 nt
ds_read_b64_tr_b16 a[10:11], v1 offset:9216
s_add_u32 s40, s40, 589824
ds_read_b64_tr_b16 a[12:13], v1 offset:8200
s_addc_u32 s41, s41, 0
ds_read_b64_tr_b16 a[14:15], v1 offset:9224
s_waitcnt vmcnt(8) lgkmcnt(0)
s_barrier
s_add_u32 m0, s44, 4096
ds_read_b64_tr_b16 a[16:17], v1 offset:16384
ds_read_b64_tr_b16 a[18:19], v1 offset:17408
global_load_lds_dwordx4 v0, s[40:41] offset:-4096 nt
ds_read_b64_tr_b16 a[20:21], v1 offset:16392
ds_read_b64_tr_b16 a[22:23], v1 offset:17416
global_load_lds_dwordx4 v166, s[40:41] offset:-3072 nt
ds_read_b64_tr_b16 a[24:25], v1 offset:24576
ds_read_b64_tr_b16 a[26:27], v1 offset:25600
global_load_lds_dwordx4 v167, s[40:41] offset:-2048 nt
ds_read_b64_tr_b16 a[28:29], v1 offset:24584
global_load_lds_dwordx4 v168, s[40:41] offset:-1024 nt
ds_read_b64_tr_b16 a[30:31], v1 offset:25608
v_mfma_f32_16x16x32_bf16 v[170:173], v[186:189], a[0:3], 0
s_add_u32 s40, s40, 589824
v_mfma_f32_16x16x32_bf16 v[174:177], v[186:189], a[4:7], 0
v_mfma_f32_16x16x32_bf16 v[170:173], v[186:189], a[8:11], v[170:173]
s_addc_u32 s41, s41, 0
v_mfma_f32_16x16x32_bf16 v[174:177], v[186:189], a[12:15], v[174:177]
s_waitcnt vmcnt(8) lgkmcnt(0)
s_barrier
s_add_u32 m0, s44, 20480
ds_read_b64_tr_b16 a[32:33], v1 offset:32768
ds_read_b64_tr_b16 a[34:35], v1 offset:33792
global_load_lds_dwordx4 v0, s[40:41] offset:-4096 nt
ds_read_b64_tr_b16 a[36:37], v1 offset:32776
ds_read_b64_tr_b16 a[38:39], v1 offset:33800
global_load_lds_dwordx4 v166, s[40:41] offset:-3072 nt
ds_read_b64_tr_b16 a[40:41], v1 offset:40960
ds_read_b64_tr_b16 a[42:43], v1 offset:41984
global_load_lds_dwordx4 v167, s[40:41] offset:-2048 nt
ds_read_b64_tr_b16 a[44:45], v1 offset:40968
global_load_lds_dwordx4 v168, s[40:41] offset:-1024 nt
ds_read_b64_tr_b16 a[46:47], v1 offset:41992
v_mfma_f32_16x16x32_bf16 v[170:173], v[186:189], a[16:19], v[170:173]
s_add_u32 s40, s40, 589824
v_mfma_f32_16x16x32_bf16 v[174:177], v[186:189], a[20:23], v[174:177]
v_mfma_f32_16x16x32_bf16 v[170:173], v[186:189], a[24:27], v[170:173]
s_addc_u32 s41, s41, 0
v_mfma_f32_16x16x32_bf16 v[174:177], v[186:189], a[28:31], v[174:177]
s_waitcnt vmcnt(8) lgkmcnt(0)
s_barrier
s_add_u32 m0, s44, 36864
ds_read_b64_tr_b16 a[48:49], v1 offset:49152
ds_read_b64_tr_b16 a[50:51], v1 offset:50176
global_load_lds_dwordx4 v0, s[40:41] offset:-4096 nt
ds_read_b64_tr_b16 a[52:53], v1 offset:49160
ds_read_b64_tr_b16 a[54:55], v1 offset:50184
global_load_lds_dwordx4 v166, s[40:41] offset:-3072 nt
ds_read_b64_tr_b16 a[56:57], v1 offset:57344
ds_read_b64_tr_b16 a[58:59], v1 offset:58368
global_load_lds_dwordx4 v167, s[40:41] offset:-2048 nt
ds_read_b64_tr_b16 a[60:61], v1 offset:57352
global_load_lds_dwordx4 v168, s[40:41] offset:-1024 nt
ds_read_b64_tr_b16 a[62:63], v1 offset:58376
v_mfma_f32_16x16x32_bf16 v[170:173], v[186:189], a[32:35], v[170:173]
s_add_u32 s40, s40, 589824
v_mfma_f32_16x16x32_bf16 v[174:177], v[186:189], a[36:39], v[174:177]
v_mfma_f32_16x16x32_bf16 v[170:173], v[186:189], a[40:43], v[170:173]
s_addc_u32 s41, s41, 0
v_mfma_f32_16x16x32_bf16 v[174:177], v[186:189], a[44:47], v[174:177]
s_waitcnt vmcnt(8) lgkmcnt(0)
s_barrier
s_add_u32 m0, s44, 53248
ds_read_b64_tr_b16 a[64:65], v1 offset:0
ds_read_b64_tr_b16 a[66:67], v1 offset:1024
global_load_lds_dwordx4 v0, s[40:41] offset:-4096 nt
ds_read_b64_tr_b16 a[68:69], v1 offset:8
ds_read_b64_tr_b16 a[70:71], v1 offset:1032
global_load_lds_dwordx4 v166, s[40:41] offset:-3072 nt
ds_read_b64_tr_b16 a[72:73], v1 offset:8192
ds_read_b64_tr_b16 a[74:75], v1 offset:9216
global_load_lds_dwordx4 v167, s[40:41] offset:-2048 nt
ds_read_b64_tr_b16 a[76:77], v1 offset:8200
global_load_lds_dwordx4 v168, s[40:41] offset:-1024 nt
ds_read_b64_tr_b16 a[78:79], v1 offset:9224
v_mfma_f32_16x16x32_bf16 v[170:173], v[186:189], a[48:51], v[170:173]
s_add_u32 s40, s40, 589824
v_mfma_f32_16x16x32_bf16 v[174:177], v[186:189], a[52:55], v[174:177]
v_mfma_f32_16x16x32_bf16 v[170:173], v[186:189], a[56:59], v[170:173]
s_addc_u32 s41, s41, 0
v_mfma_f32_16x16x32_bf16 v[174:177], v[186:189], a[60:63], v[174:177]
s_waitcnt vmcnt(8) lgkmcnt(0)
s_barrier
s_add_u32 m0, s44, 4096
ds_read_b64_tr_b16 a[80:81], v1 offset:16384
ds_read_b64_tr_b16 a[82:83], v1 offset:17408
global_load_lds_dwordx4 v0, s[40:41] offset:-4096 nt
ds_read_b64_tr_b16 a[84:85], v1 offset:16392
ds_read_b64_tr_b16 a[86:87], v1 offset:17416
global_load_lds_dwordx4 v166, s[40:41] offset:-3072 nt
ds_read_b64_tr_b16 a[88:89], v1 offset:24576
ds_read_b64_tr_b16 a[90:91], v1 offset:25600
global_load_lds_dwordx4 v167, s[40:41] offset:-2048 nt
ds_read_b64_tr_b16 a[92:93], v1 offset:24584
ds_read_b64_tr_b16 a[94:95], v1 offset:25608
global_load_lds_dwordx4 v168, s[40:41] offset:-1024 nt
v_mfma_f32_16x16x32_bf16 v[170:173], v[186:189], a[64:67], v[170:173]
v_mfma_f32_16x16x32_bf16 v[174:177], v[186:189], a[68:71], v[174:177]
s_mov_b64 s[40:41], s[42:43]
v_mfma_f32_16x16x32_bf16 v[170:173], v[186:189], a[72:75], v[170:173]
v_mfma_f32_16x16x32_bf16 v[174:177], v[186:189], a[76:79], v[174:177]
s_waitcnt vmcnt(8) lgkmcnt(0)
s_barrier
s_add_u32 m0, s44, 20480
ds_read_b64_tr_b16 a[96:97], v1 offset:32768
ds_read_b64_tr_b16 a[98:99], v1 offset:33792
global_load_lds_dwordx4 v0, s[40:41] offset:-4096 nt
ds_read_b64_tr_b16 a[100:101], v1 offset:32776
ds_read_b64_tr_b16 a[102:103], v1 offset:33800
global_load_lds_dwordx4 v166, s[40:41] offset:-3072 nt
ds_read_b64_tr_b16 a[104:105], v1 offset:40960
ds_read_b64_tr_b16 a[106:107], v1 offset:41984
global_load_lds_dwordx4 v167, s[40:41] offset:-2048 nt
ds_read_b64_tr_b16 a[108:109], v1 offset:40968
global_load_lds_dwordx4 v168, s[40:41] offset:-1024 nt
ds_read_b64_tr_b16 a[110:111], v1 offset:41992
v_mfma_f32_16x16x32_bf16 v[170:173], v[186:189], a[80:83], v[170:173]
s_add_u32 s40, s40, 589824
v_mfma_f32_16x16x32_bf16 v[174:177], v[186:189], a[84:87], v[174:177]
v_mfma_f32_16x16x32_bf16 v[170:173], v[186:189], a[88:91], v[170:173]
s_addc_u32 s41, s41, 0
v_mfma_f32_16x16x32_bf16 v[174:177], v[186:189], a[92:95], v[174:177]
s_waitcnt vmcnt(8) lgkmcnt(0)
s_barrier
s_add_u32 m0, s44, 36864
ds_read_b64_tr_b16 a[112:113], v1 offset:49152
ds_read_b64_tr_b16 a[114:115], v1 offset:50176
global_load_lds_dwordx4 v0, s[40:41] offset:-4096 nt
ds_read_b64_tr_b16 a[116:117], v1 offset:49160
ds_read_b64_tr_b16 a[118:119], v1 offset:50184
global_load_lds_dwordx4 v166, s[40:41] offset:-3072 nt
ds_read_b64_tr_b16 a[120:121], v1 offset:57344
ds_read_b64_tr_b16 a[122:123], v1 offset:58368
global_load_lds_dwordx4 v167, s[40:41] offset:-2048 nt
ds_read_b64_tr_b16 a[124:125], v1 offset:57352
global_load_lds_dwordx4 v168, s[40:41] offset:-1024 nt
ds_read_b64_tr_b16 a[126:127], v1 offset:58376
v_mfma_f32_16x16x32_bf16 v[170:173], v[186:189], a[96:99], v[170:173]
s_add_u32 s40, s40, 589824
v_mfma_f32_16x16x32_bf16 v[174:177], v[186:189], a[100:103], v[174:177]
v_mfma_f32_16x16x32_bf16 v[170:173], v[186:189], a[104:107], v[170:173]
s_addc_u32 s41, s41, 0
v_mfma_f32_16x16x32_bf16 v[174:177], v[186:189], a[108:111], v[174:177]
s_waitcnt vmcnt(8) lgkmcnt(0)
s_barrier
s_add_u32 m0, s44, 53248
ds_read_b64_tr_b16 a[128:129], v1 offset:0
ds_read_b64_tr_b16 a[130:131], v1 offset:1024
global_load_lds_dwordx4 v0, s[40:41] offset:-4096 nt
ds_read_b64_tr_b16 a[132:133], v1 offset:8
ds_read_b64_tr_b16 a[134:135], v1 offset:1032
global_load_lds_dwordx4 v166, s[40:41] offset:-3072 nt
ds_read_b64_tr_b16 a[136:137], v1 offset:8192
ds_read_b64_tr_b16 a[138:139], v1 offset:9216
global_load_lds_dwordx4 v167, s[40:41] offset:-2048 nt
ds_read_b64_tr_b16 a[140:141], v1 offset:8200
global_load_lds_dwordx4 v168, s[40:41] offset:-1024 nt
ds_read_b64_tr_b16 a[142:143], v1 offset:9224
v_mfma_f32_16x16x32_bf16 v[170:173], v[186:189], a[112:115], v[170:173]
s_add_u32 s40, s40, 589824
v_mfma_f32_16x16x32_bf16 v[174:177], v[186:189], a[116:119], v[174:177]
v_mfma_f32_16x16x32_bf16 v[170:173], v[186:189], a[120:123], v[170:173]
s_addc_u32 s41, s41, 0
v_mfma_f32_16x16x32_bf16 v[174:177], v[186:189], a[124:127], v[174:177]
s_waitcnt vmcnt(8) lgkmcnt(0)
s_barrier
s_add_u32 m0, s44, 4096
ds_read_b64_tr_b16 a[144:145], v1 offset:16384
ds_read_b64_tr_b16 a[146:147], v1 offset:17408
global_load_lds_dwordx4 v0, s[40:41] offset:-4096 nt
ds_read_b64_tr_b16 a[148:149], v1 offset:16392
ds_read_b64_tr_b16 a[150:151], v1 offset:17416
global_load_lds_dwordx4 v166, s[40:41] offset:-3072 nt
ds_read_b64_tr_b16 a[152:153], v1 offset:24576
ds_read_b64_tr_b16 a[154:155], v1 offset:25600
global_load_lds_dwordx4 v167, s[40:41] offset:-2048 nt
ds_read_b64_tr_b16 a[156:157], v1 offset:24584
global_load_lds_dwordx4 v168, s[40:41] offset:-1024 nt
ds_read_b64_tr_b16 a[158:159], v1 offset:25608
v_mfma_f32_16x16x32_bf16 v[170:173], v[186:189], a[128:131], v[170:173]
s_add_u32 s40, s40, 589824
v_mfma_f32_16x16x32_bf16 v[174:177], v[186:189], a[132:135], v[174:177]
v_mfma_f32_16x16x32_bf16 v[170:173], v[186:189], a[136:139], v[170:173]
s_addc_u32 s41, s41, 0
v_mfma_f32_16x16x32_bf16 v[174:177], v[186:189], a[140:143], v[174:177]
s_waitcnt vmcnt(8) lgkmcnt(0)
s_barrier
s_add_u32 m0, s44, 20480
ds_read_b64_tr_b16 a[160:161], v1 offset:32768
v_mfma_f32_16x16x32_bf16 v[178:181], v[186:189], a[144:147], 0
v_mfma_f32_16x16x32_bf16 v[182:185], v[186:189], a[148:151], 0
v_mfma_f32_16x16x32_bf16 v[178:181], v[186:189], a[152:155], v[178:181]
v_mfma_f32_16x16x32_bf16 v[182:185], v[186:189], a[156:159], v[182:185]
v_mul_f32 v170, 0xbae38e39, v170
v_mul_f32 v171, 0xbae38e39, v171
ds_read_b64_tr_b16 a[162:163], v1 offset:33792
v_mul_f32 v172, 0xbae38e39, v172
global_load_lds_dwordx4 v0, s[40:41] offset:-4096 nt
v_mul_f32 v173, 0xbae38e39, v173
v_mul_f32 v174, 0xbae38e39, v174
v_mul_f32 v175, 0xbae38e39, v175
v_mul_f32 v176, 0xbae38e39, v176
v_mul_f32 v177, 0xbae38e39, v177
ds_read_b64_tr_b16 a[164:165], v1 offset:32776
v_mfma_f32_16x16x32_bf16 v[190:193], v[22:25], a[0:3], v[170:173]
v_mfma_f32_16x16x32_bf16 v[194:197], v[20:23], a[0:3], v[170:173]
global_load_lds_dwordx4 v166, s[40:41] offset:-3072 nt
v_mfma_f32_16x16x32_bf16 v[198:201], v[22:25], a[4:7], v[174:177]
v_mfma_f32_16x16x32_bf16 v[202:205], v[20:23], a[4:7], v[174:177]
v_mfma_f32_16x16x32_bf16 v[206:209], v[22:25], a[8:11], v[170:173]
s_nop 2
v_cvt_pk_bf16_f32 v222, v190, v191
ds_read_b64_tr_b16 a[166:167], v1 offset:33800
v_cvt_pk_bf16_f32 v223, v192, v193
v_cvt_pk_bf16_f32 v224, v194, v195
v_cvt_pk_bf16_f32 v225, v196, v197
global_load_lds_dwordx4 v167, s[40:41] offset:-2048 nt
v_mfma_f32_16x16x32_bf16 v[210:213], v[20:23], a[8:11], v[170:173]
v_accvgpr_write_b32 a0, v222
ds_read_b64_tr_b16 a[168:169], v1 offset:40960
v_accvgpr_write_b32 a1, v223
v_accvgpr_write_b32 a2, v224
v_accvgpr_write_b32 a3, v225
v_mfma_f32_16x16x32_bf16 v[214:217], v[22:25], a[12:15], v[174:177]
v_cvt_pk_bf16_f32 v226, v198, v199
v_cvt_pk_bf16_f32 v227, v200, v201
global_load_lds_dwordx4 v168, s[40:41] offset:-1024 nt
ds_read_b64_tr_b16 a[170:171], v1 offset:41984
v_cvt_pk_bf16_f32 v228, v202, v203
v_cvt_pk_bf16_f32 v229, v204, v205
v_mfma_f32_16x16x32_bf16 v[218:221], v[20:23], a[12:15], v[174:177]
s_add_u32 s40, s40, 589824
v_accvgpr_write_b32 a4, v226
v_accvgpr_write_b32 a5, v227
v_accvgpr_write_b32 a6, v228
ds_read_b64_tr_b16 a[172:173], v1 offset:40968
v_accvgpr_write_b32 a7, v229
v_mfma_f32_16x16x32_bf16 v[190:193], v[22:25], a[16:19], v[170:173]
v_cvt_pk_bf16_f32 v222, v206, v207
v_cvt_pk_bf16_f32 v223, v208, v209
v_cvt_pk_bf16_f32 v224, v210, v211
v_cvt_pk_bf16_f32 v225, v212, v213
ds_read_b64_tr_b16 a[174:175], v1 offset:41992
v_mfma_f32_16x16x32_bf16 v[194:197], v[20:23], a[16:19], v[170:173]
s_addc_u32 s41, s41, 0
v_accvgpr_write_b32 a8, v222
v_accvgpr_write_b32 a9, v223
v_accvgpr_write_b32 a10, v224
v_accvgpr_write_b32 a11, v225
v_mfma_f32_16x16x32_bf16 v[198:201], v[22:25], a[20:23], v[174:177]
s_waitcnt vmcnt(8) lgkmcnt(0)
v_cvt_pk_bf16_f32 v226, v214, v215
v_cvt_pk_bf16_f32 v227, v216, v217
v_cvt_pk_bf16_f32 v228, v218, v219
s_barrier
s_add_u32 m0, s44, 36864
ds_read_b64_tr_b16 a[176:177], v1 offset:49152
v_mfma_f32_16x16x32_bf16 v[178:181], v[186:189], a[160:163], v[178:181]
v_mfma_f32_16x16x32_bf16 v[182:185], v[186:189], a[164:167], v[182:185]
v_mfma_f32_16x16x32_bf16 v[178:181], v[186:189], a[168:171], v[178:181]
v_mfma_f32_16x16x32_bf16 v[182:185], v[186:189], a[172:175], v[182:185]
v_cvt_pk_bf16_f32 v229, v220, v221
v_mfma_f32_16x16x32_bf16 v[202:205], v[20:23], a[20:23], v[174:177]
ds_read_b64_tr_b16 a[178:179], v1 offset:50176
v_accvgpr_write_b32 a12, v226
global_load_lds_dwordx4 v0, s[40:41] offset:-4096 nt
v_accvgpr_write_b32 a13, v227
v_accvgpr_write_b32 a14, v228
v_accvgpr_write_b32 a15, v229
v_mfma_f32_16x16x32_bf16 v[206:209], v[22:25], a[24:27], v[170:173]
v_cvt_pk_bf16_f32 v222, v190, v191
ds_read_b64_tr_b16 a[180:181], v1 offset:49160
v_cvt_pk_bf16_f32 v223, v192, v193
v_cvt_pk_bf16_f32 v224, v194, v195
global_load_lds_dwordx4 v166, s[40:41] offset:-3072 nt
v_cvt_pk_bf16_f32 v225, v196, v197
v_mfma_f32_16x16x32_bf16 v[210:213], v[20:23], a[24:27], v[170:173]
v_accvgpr_write_b32 a16, v222
v_accvgpr_write_b32 a17, v223
ds_read_b64_tr_b16 a[182:183], v1 offset:50184
v_accvgpr_write_b32 a18, v224
v_accvgpr_write_b32 a19, v225
v_mfma_f32_16x16x32_bf16 v[214:217], v[22:25], a[28:31], v[174:177]
global_load_lds_dwordx4 v167, s[40:41] offset:-2048 nt
v_cvt_pk_bf16_f32 v226, v198, v199
v_cvt_pk_bf16_f32 v227, v200, v201
ds_read_b64_tr_b16 a[184:185], v1 offset:57344
v_cvt_pk_bf16_f32 v228, v202, v203
v_cvt_pk_bf16_f32 v229, v204, v205
v_mfma_f32_16x16x32_bf16 v[218:221], v[20:23], a[28:31], v[174:177]
v_accvgpr_write_b32 a20, v226
v_accvgpr_write_b32 a21, v227
v_accvgpr_write_b32 a22, v228
global_load_lds_dwordx4 v168, s[40:41] offset:-1024 nt
ds_read_b64_tr_b16 a[186:187], v1 offset:58368
v_accvgpr_write_b32 a23, v229
v_mfma_f32_16x16x32_bf16 v[190:193], v[22:25], a[32:35], v[170:173]
s_add_u32 s40, s40, 589824
v_cvt_pk_bf16_f32 v222, v206, v207
v_cvt_pk_bf16_f32 v223, v208, v209
v_cvt_pk_bf16_f32 v224, v210, v211
v_cvt_pk_bf16_f32 v225, v212, v213
ds_read_b64_tr_b16 a[188:189], v1 offset:57352
v_mfma_f32_16x16x32_bf16 v[194:197], v[20:23], a[32:35], v[170:173]
v_accvgpr_write_b32 a24, v222
v_accvgpr_write_b32 a25, v223
v_accvgpr_write_b32 a26, v224
v_accvgpr_write_b32 a27, v225
v_mfma_f32_16x16x32_bf16 v[198:201], v[22:25], a[36:39], v[174:177]
s_addc_u32 s41, s41, 0
ds_read_b64_tr_b16 a[190:191], v1 offset:58376
v_cvt_pk_bf16_f32 v226, v214, v215
v_cvt_pk_bf16_f32 v227, v216, v217
v_cvt_pk_bf16_f32 v228, v218, v219
v_cvt_pk_bf16_f32 v229, v220, v221
v_mfma_f32_16x16x32_bf16 v[202:205], v[20:23], a[36:39], v[174:177]
s_waitcnt vmcnt(8) lgkmcnt(0)
v_accvgpr_write_b32 a28, v226
v_accvgpr_write_b32 a29, v227
v_accvgpr_write_b32 a30, v228
v_accvgpr_write_b32 a31, v229
s_barrier
s_add_u32 m0, s44, 53248
ds_read_b64_tr_b16 a[192:193], v1 offset:0
v_mfma_f32_16x16x32_bf16 v[178:181], v[186:189], a[176:179], v[178:181]
v_mfma_f32_16x16x32_bf16 v[182:185], v[186:189], a[180:183], v[182:185]
v_mfma_f32_16x16x32_bf16 v[178:181], v[186:189], a[184:187], v[178:181]
v_mfma_f32_16x16x32_bf16 v[182:185], v[186:189], a[188:191], v[182:185]
v_mfma_f32_16x16x32_bf16 v[206:209], v[22:25], a[40:43], v[170:173]
v_cvt_pk_bf16_f32 v222, v190, v191
ds_read_b64_tr_b16 a[194:195], v1 offset:1024
v_cvt_pk_bf16_f32 v223, v192, v193
global_load_lds_dwordx4 v0, s[40:41] offset:-4096 nt
v_cvt_pk_bf16_f32 v224, v194, v195
v_cvt_pk_bf16_f32 v225, v196, v197
)"
R"(v_mfma_f32_16x16x32_bf16 v[210:213], v[20:23], a[40:43], v[170:173]
v_accvgpr_write_b32 a32, v222
v_accvgpr_write_b32 a33, v223
ds_read_b64_tr_b16 a[196:197], v1 offset:8
v_accvgpr_write_b32 a34, v224
v_accvgpr_write_b32 a35, v225
global_load_lds_dwordx4 v166, s[40:41] offset:-3072 nt
v_mfma_f32_16x16x32_bf16 v[214:217], v[22:25], a[44:47], v[174:177]
v_cvt_pk_bf16_f32 v226, v198, v199
v_cvt_pk_bf16_f32 v227, v200, v201
v_cvt_pk_bf16_f32 v228, v202, v203
ds_read_b64_tr_b16 a[198:199], v1 offset:1032
v_cvt_pk_bf16_f32 v229, v204, v205
v_mfma_f32_16x16x32_bf16 v[218:221], v[20:23], a[44:47], v[174:177]
v_accvgpr_write_b32 a36, v226
global_load_lds_dwordx4 v167, s[40:41] offset:-2048 nt
v_accvgpr_write_b32 a37, v227
v_accvgpr_write_b32 a38, v228
ds_read_b64_tr_b16 a[200:201], v1 offset:8192
v_accvgpr_write_b32 a39, v229
v_mfma_f32_16x16x32_bf16 v[190:193], v[22:25], a[48:51], v[170:173]
v_cvt_pk_bf16_f32 v222, v206, v207
v_cvt_pk_bf16_f32 v223, v208, v209
v_cvt_pk_bf16_f32 v224, v210, v211
v_cvt_pk_bf16_f32 v225, v212, v213
global_load_lds_dwordx4 v168, s[40:41] offset:-1024 nt
ds_read_b64_tr_b16 a[202:203], v1 offset:9216
v_mfma_f32_16x16x32_bf16 v[194:197], v[20:23], a[48:51], v[170:173]
v_accvgpr_write_b32 a40, v222
v_accvgpr_write_b32 a41, v223
v_accvgpr_write_b32 a42, v224
v_accvgpr_write_b32 a43, v225
v_mfma_f32_16x16x32_bf16 v[198:201], v[22:25], a[52:55], v[174:177]
s_add_u32 s40, s40, 589824
ds_read_b64_tr_b16 a[204:205], v1 offset:8200
v_cvt_pk_bf16_f32 v226, v214, v215
v_cvt_pk_bf16_f32 v227, v216, v217
v_cvt_pk_bf16_f32 v228, v218, v219
v_cvt_pk_bf16_f32 v229, v220, v221
v_mfma_f32_16x16x32_bf16 v[202:205], v[20:23], a[52:55], v[174:177]
s_addc_u32 s41, s41, 0
v_accvgpr_write_b32 a44, v226
ds_read_b64_tr_b16 a[206:207], v1 offset:9224
v_accvgpr_write_b32 a45, v227
v_accvgpr_write_b32 a46, v228
v_accvgpr_write_b32 a47, v229
v_mfma_f32_16x16x32_bf16 v[206:209], v[22:25], a[56:59], v[170:173]
v_cvt_pk_bf16_f32 v222, v190, v191
v_cvt_pk_bf16_f32 v223, v192, v193
v_cvt_pk_bf16_f32 v224, v194, v195
v_cvt_pk_bf16_f32 v225, v196, v197
v_mfma_f32_16x16x32_bf16 v[210:213], v[20:23], a[56:59], v[170:173]
s_waitcnt vmcnt(8) lgkmcnt(0)
s_barrier
s_add_u32 m0, s44, 4096
ds_read_b64_tr_b16 a[208:209], v1 offset:16384
v_mfma_f32_16x16x32_bf16 v[178:181], v[186:189], a[192:195], v[178:181]
v_mfma_f32_16x16x32_bf16 v[182:185], v[186:189], a[196:199], v[182:185]
v_mfma_f32_16x16x32_bf16 v[178:181], v[186:189], a[200:203], v[178:181]
v_mfma_f32_16x16x32_bf16 v[182:185], v[186:189], a[204:207], v[182:185]
v_accvgpr_write_b32 a48, v222
v_accvgpr_write_b32 a49, v223
ds_read_b64_tr_b16 a[210:211], v1 offset:17408
v_accvgpr_write_b32 a50, v224
global_load_lds_dwordx4 v0, s[40:41] offset:-4096 nt
v_accvgpr_write_b32 a51, v225
v_mfma_f32_16x16x32_bf16 v[214:217], v[22:25], a[60:63], v[174:177]
v_cvt_pk_bf16_f32 v226, v198, v199
v_cvt_pk_bf16_f32 v227, v200, v201
v_cvt_pk_bf16_f32 v228, v202, v203
ds_read_b64_tr_b16 a[212:213], v1 offset:16392
v_cvt_pk_bf16_f32 v229, v204, v205
v_mfma_f32_16x16x32_bf16 v[218:221], v[20:23], a[60:63], v[174:177]
global_load_lds_dwordx4 v166, s[40:41] offset:-3072 nt
v_accvgpr_write_b32 a52, v226
v_accvgpr_write_b32 a53, v227
v_accvgpr_write_b32 a54, v228
v_accvgpr_write_b32 a55, v229
ds_read_b64_tr_b16 a[214:215], v1 offset:17416
v_mfma_f32_16x16x32_bf16 v[190:193], v[22:25], a[64:67], v[170:173]
v_cvt_pk_bf16_f32 v222, v206, v207
v_cvt_pk_bf16_f32 v223, v208, v209
global_load_lds_dwordx4 v167, s[40:41] offset:-2048 nt
v_cvt_pk_bf16_f32 v224, v210, v211
v_cvt_pk_bf16_f32 v225, v212, v213
ds_read_b64_tr_b16 a[216:217], v1 offset:24576
v_mfma_f32_16x16x32_bf16 v[194:197], v[20:23], a[64:67], v[170:173]
v_accvgpr_write_b32 a56, v222
v_accvgpr_write_b32 a57, v223
v_accvgpr_write_b32 a58, v224
v_accvgpr_write_b32 a59, v225
v_mfma_f32_16x16x32_bf16 v[198:201], v[22:25], a[68:71], v[174:177]
global_load_lds_dwordx4 v168, s[40:41] offset:-1024 nt
ds_read_b64_tr_b16 a[218:219], v1 offset:25600
v_cvt_pk_bf16_f32 v226, v214, v215
v_cvt_pk_bf16_f32 v227, v216, v217
v_cvt_pk_bf16_f32 v228, v218, v219
v_cvt_pk_bf16_f32 v229, v220, v221
v_mfma_f32_16x16x32_bf16 v[202:205], v[20:23], a[68:71], v[174:177]
s_add_u32 s40, s40, 589824
v_accvgpr_write_b32 a60, v226
ds_read_b64_tr_b16 a[220:221], v1 offset:24584
v_accvgpr_write_b32 a61, v227
v_accvgpr_write_b32 a62, v228
v_accvgpr_write_b32 a63, v229
v_mfma_f32_16x16x32_bf16 v[206:209], v[22:25], a[72:75], v[170:173]
s_addc_u32 s41, s41, 0
v_cvt_pk_bf16_f32 v222, v190, v191
v_cvt_pk_bf16_f32 v223, v192, v193
ds_read_b64_tr_b16 a[222:223], v1 offset:25608
v_cvt_pk_bf16_f32 v224, v194, v195
v_cvt_pk_bf16_f32 v225, v196, v197
v_mfma_f32_16x16x32_bf16 v[210:213], v[20:23], a[72:75], v[170:173]
v_accvgpr_write_b32 a64, v222
v_accvgpr_write_b32 a65, v223
v_accvgpr_write_b32 a66, v224
v_accvgpr_write_b32 a67, v225
v_mfma_f32_16x16x32_bf16 v[214:217], v[22:25], a[76:79], v[174:177]
s_waitcnt vmcnt(8) lgkmcnt(0)
v_cvt_pk_bf16_f32 v226, v198, v199
s_barrier
s_add_u32 m0, s44, 20480
ds_read_b64_tr_b16 a[224:225], v1 offset:32768
v_mfma_f32_16x16x32_bf16 v[178:181], v[186:189], a[208:211], v[178:181]
v_mfma_f32_16x16x32_bf16 v[182:185], v[186:189], a[212:215], v[182:185]
v_mfma_f32_16x16x32_bf16 v[178:181], v[186:189], a[216:219], v[178:181]
v_mfma_f32_16x16x32_bf16 v[182:185], v[186:189], a[220:223], v[182:185]
v_cvt_pk_bf16_f32 v227, v200, v201
v_cvt_pk_bf16_f32 v228, v202, v203
ds_read_b64_tr_b16 a[226:227], v1 offset:33792
v_cvt_pk_bf16_f32 v229, v204, v205
global_load_lds_dwordx4 v0, s[40:41] offset:-4096 nt
v_mfma_f32_16x16x32_bf16 v[218:221], v[20:23], a[76:79], v[174:177]
v_accvgpr_write_b32 a68, v226
v_accvgpr_write_b32 a69, v227
v_accvgpr_write_b32 a70, v228
v_accvgpr_write_b32 a71, v229
ds_read_b64_tr_b16 a[228:229], v1 offset:32776
v_mfma_f32_16x16x32_bf16 v[190:193], v[22:25], a[80:83], v[170:173]
v_cvt_pk_bf16_f32 v222, v206, v207
global_load_lds_dwordx4 v166, s[40:41] offset:-3072 nt
v_cvt_pk_bf16_f32 v223, v208, v209
v_cvt_pk_bf16_f32 v224, v210, v211
v_cvt_pk_bf16_f32 v225, v212, v213
v_mfma_f32_16x16x32_bf16 v[194:197], v[20:23], a[80:83], v[170:173]
ds_read_b64_tr_b16 a[230:231], v1 offset:33800
v_accvgpr_write_b32 a72, v222
v_accvgpr_write_b32 a73, v223
v_accvgpr_write_b32 a74, v224
global_load_lds_dwordx4 v167, s[40:41] offset:-2048 nt
v_accvgpr_write_b32 a75, v225
v_mfma_f32_16x16x32_bf16 v[198:201], v[22:25], a[84:87], v[174:177]
ds_read_b64_tr_b16 a[232:233], v1 offset:40960
v_cvt_pk_bf16_f32 v226, v214, v215
v_cvt_pk_bf16_f32 v227, v216, v217
v_cvt_pk_bf16_f32 v228, v218, v219
v_cvt_pk_bf16_f32 v229, v220, v221
v_mfma_f32_16x16x32_bf16 v[202:205], v[20:23], a[84:87], v[174:177]
v_accvgpr_write_b32 a76, v226
global_load_lds_dwordx4 v168, s[40:41] offset:-1024 nt
ds_read_b64_tr_b16 a[234:235], v1 offset:41984
v_accvgpr_write_b32 a77, v227
v_accvgpr_write_b32 a78, v228
v_accvgpr_write_b32 a79, v229
v_mfma_f32_16x16x32_bf16 v[206:209], v[22:25], a[88:91], v[170:173]
s_sub_u32 s40, s40, 10027008
v_cvt_pk_bf16_f32 v222, v190, v191
v_cvt_pk_bf16_f32 v223, v192, v193
ds_read_b64_tr_b16 a[236:237], v1 offset:40968
v_cvt_pk_bf16_f32 v224, v194, v195
v_cvt_pk_bf16_f32 v225, v196, v197
v_mfma_f32_16x16x32_bf16 v[210:213], v[20:23], a[88:91], v[170:173]
s_subb_u32 s41, s41, 0
v_accvgpr_write_b32 a80, v222
v_accvgpr_write_b32 a81, v223
v_accvgpr_write_b32 a82, v224
ds_read_b64_tr_b16 a[238:239], v1 offset:41992
v_accvgpr_write_b32 a83, v225
v_mfma_f32_16x16x32_bf16 v[214:217], v[22:25], a[92:95], v[174:177]
v_cvt_pk_bf16_f32 v226, v198, v199
v_cvt_pk_bf16_f32 v227, v200, v201
v_cvt_pk_bf16_f32 v228, v202, v203
v_cvt_pk_bf16_f32 v229, v204, v205
v_mfma_f32_16x16x32_bf16 v[218:221], v[20:23], a[92:95], v[174:177]
s_waitcnt vmcnt(8) lgkmcnt(0)
v_accvgpr_write_b32 a84, v226
v_accvgpr_write_b32 a85, v227
s_barrier
ds_read_b64_tr_b16 v[26:27], v1 offset:49152
v_mfma_f32_16x16x32_bf16 v[178:181], v[186:189], a[224:227], v[178:181]
v_mfma_f32_16x16x32_bf16 v[182:185], v[186:189], a[228:231], v[182:185]
v_mfma_f32_16x16x32_bf16 v[178:181], v[186:189], a[232:235], v[178:181]
v_mfma_f32_16x16x32_bf16 v[182:185], v[186:189], a[236:239], v[182:185]
v_accvgpr_write_b32 a86, v228
v_accvgpr_write_b32 a87, v229
ds_read_b64_tr_b16 v[28:29], v1 offset:50176
v_mfma_f32_16x16x32_bf16 v[190:193], v[22:25], a[96:99], v[170:173]
v_cvt_pk_bf16_f32 v222, v206, v207
v_cvt_pk_bf16_f32 v223, v208, v209
v_cvt_pk_bf16_f32 v224, v210, v211
v_cvt_pk_bf16_f32 v225, v212, v213
v_mfma_f32_16x16x32_bf16 v[194:197], v[20:23], a[96:99], v[170:173]
ds_read_b64_tr_b16 v[30:31], v1 offset:49160
v_accvgpr_write_b32 a88, v222
v_accvgpr_write_b32 a89, v223
v_accvgpr_write_b32 a90, v224
v_accvgpr_write_b32 a91, v225
v_mfma_f32_16x16x32_bf16 v[198:201], v[22:25], a[100:103], v[174:177]
v_cvt_pk_bf16_f32 v226, v214, v215
ds_read_b64_tr_b16 v[32:33], v1 offset:50184
v_cvt_pk_bf16_f32 v227, v216, v217
v_cvt_pk_bf16_f32 v228, v218, v219
v_cvt_pk_bf16_f32 v229, v220, v221
v_mfma_f32_16x16x32_bf16 v[202:205], v[20:23], a[100:103], v[174:177]
v_accvgpr_write_b32 a92, v226
ds_read_b64_tr_b16 v[34:35], v1 offset:57344
v_accvgpr_write_b32 a93, v227
v_accvgpr_write_b32 a94, v228
v_accvgpr_write_b32 a95, v229
v_mfma_f32_16x16x32_bf16 v[206:209], v[22:25], a[104:107], v[170:173]
v_cvt_pk_bf16_f32 v222, v190, v191
v_cvt_pk_bf16_f32 v223, v192, v193
ds_read_b64_tr_b16 v[36:37], v1 offset:58368
v_cvt_pk_bf16_f32 v224, v194, v195
v_cvt_pk_bf16_f32 v225, v196, v197
v_mfma_f32_16x16x32_bf16 v[210:213], v[20:23], a[104:107], v[170:173]
v_accvgpr_write_b32 a96, v222
v_accvgpr_write_b32 a97, v223
v_accvgpr_write_b32 a98, v224
ds_read_b64_tr_b16 v[38:39], v1 offset:57352
v_accvgpr_write_b32 a99, v225
v_mfma_f32_16x16x32_bf16 v[214:217], v[22:25], a[108:111], v[174:177]
v_cvt_pk_bf16_f32 v226, v198, v199
v_cvt_pk_bf16_f32 v227, v200, v201
v_cvt_pk_bf16_f32 v228, v202, v203
v_cvt_pk_bf16_f32 v229, v204, v205
ds_read_b64_tr_b16 v[40:41], v1 offset:58376
v_mfma_f32_16x16x32_bf16 v[218:221], v[20:23], a[108:111], v[174:177]
v_accvgpr_write_b32 a100, v226
v_accvgpr_write_b32 a101, v227
v_accvgpr_write_b32 a102, v228
v_accvgpr_write_b32 a103, v229
v_mfma_f32_16x16x32_bf16 v[190:193], v[22:25], a[112:115], v[170:173]
s_waitcnt vmcnt(4) lgkmcnt(0)
v_cvt_pk_bf16_f32 v222, v206, v207
v_cvt_pk_bf16_f32 v223, v208, v209
v_cvt_pk_bf16_f32 v224, v210, v211
s_barrier
ds_read_b64_tr_b16 v[42:43], v1 offset:0
v_mfma_f32_16x16x32_bf16 v[178:181], v[186:189], v[26:29], v[178:181]
v_mfma_f32_16x16x32_bf16 v[182:185], v[186:189], v[30:33], v[182:185]
v_mfma_f32_16x16x32_bf16 v[178:181], v[186:189], v[34:37], v[178:181]
v_mfma_f32_16x16x32_bf16 v[182:185], v[186:189], v[38:41], v[182:185]
v_cvt_pk_bf16_f32 v225, v212, v213
v_mfma_f32_16x16x32_bf16 v[194:197], v[20:23], a[112:115], v[170:173]
ds_read_b64_tr_b16 v[44:45], v1 offset:1024
v_accvgpr_write_b32 a104, v222
v_accvgpr_write_b32 a105, v223
v_accvgpr_write_b32 a106, v224
v_accvgpr_write_b32 a107, v225
v_mfma_f32_16x16x32_bf16 v[198:201], v[22:25], a[116:119], v[174:177]
v_cvt_pk_bf16_f32 v226, v214, v215
ds_read_b64_tr_b16 v[46:47], v1 offset:8
v_cvt_pk_bf16_f32 v227, v216, v217
v_cvt_pk_bf16_f32 v228, v218, v219
v_cvt_pk_bf16_f32 v229, v220, v221
v_mfma_f32_16x16x32_bf16 v[202:205], v[20:23], a[116:119], v[174:177]
v_accvgpr_write_b32 a108, v226
v_accvgpr_write_b32 a109, v227
ds_read_b64_tr_b16 v[48:49], v1 offset:1032
v_accvgpr_write_b32 a110, v228
v_accvgpr_write_b32 a111, v229
v_mfma_f32_16x16x32_bf16 v[206:209], v[22:25], a[120:123], v[170:173]
v_cvt_pk_bf16_f32 v222, v190, v191
v_cvt_pk_bf16_f32 v223, v192, v193
ds_read_b64_tr_b16 v[50:51], v1 offset:8192
v_cvt_pk_bf16_f32 v224, v194, v195
v_cvt_pk_bf16_f32 v225, v196, v197
v_mfma_f32_16x16x32_bf16 v[210:213], v[20:23], a[120:123], v[170:173]
v_accvgpr_write_b32 a112, v222
v_accvgpr_write_b32 a113, v223
v_accvgpr_write_b32 a114, v224
ds_read_b64_tr_b16 v[52:53], v1 offset:9216
v_accvgpr_write_b32 a115, v225
v_mfma_f32_16x16x32_bf16 v[214:217], v[22:25], a[124:127], v[174:177]
v_cvt_pk_bf16_f32 v226, v198, v199
v_cvt_pk_bf16_f32 v227, v200, v201
v_cvt_pk_bf16_f32 v228, v202, v203
v_cvt_pk_bf16_f32 v229, v204, v205
ds_read_b64_tr_b16 v[54:55], v1 offset:8200
v_mfma_f32_16x16x32_bf16 v[218:221], v[20:23], a[124:127], v[174:177]
v_accvgpr_write_b32 a116, v226
v_accvgpr_write_b32 a117, v227
v_accvgpr_write_b32 a118, v228
v_accvgpr_write_b32 a119, v229
v_mfma_f32_16x16x32_bf16 v[190:193], v[22:25], a[128:131], v[170:173]
ds_read_b64_tr_b16 v[56:57], v1 offset:9224
v_cvt_pk_bf16_f32 v222, v206, v207
v_cvt_pk_bf16_f32 v223, v208, v209
v_cvt_pk_bf16_f32 v224, v210, v211
v_cvt_pk_bf16_f32 v225, v212, v213
v_mfma_f32_16x16x32_bf16 v[194:197], v[20:23], a[128:131], v[170:173]
s_waitcnt vmcnt(0) lgkmcnt(0)
v_accvgpr_write_b32 a120, v222
v_accvgpr_write_b32 a121, v223
v_accvgpr_write_b32 a122, v224
v_accvgpr_write_b32 a123, v225
s_barrier
ds_read_b64_tr_b16 v[58:59], v1 offset:16384
v_mfma_f32_16x16x32_bf16 v[178:181], v[186:189], v[42:45], v[178:181]
v_mfma_f32_16x16x32_bf16 v[182:185], v[186:189], v[46:49], v[182:185]
v_mfma_f32_16x16x32_bf16 v[178:181], v[186:189], v[50:53], v[178:181]
v_mfma_f32_16x16x32_bf16 v[182:185], v[186:189], v[54:57], v[182:185]
v_mfma_f32_16x16x32_bf16 v[198:201], v[22:25], a[132:135], v[174:177]
v_cvt_pk_bf16_f32 v226, v214, v215
ds_read_b64_tr_b16 v[60:61], v1 offset:17408
v_cvt_pk_bf16_f32 v227, v216, v217
v_cvt_pk_bf16_f32 v228, v218, v219
v_cvt_pk_bf16_f32 v229, v220, v221
v_mfma_f32_16x16x32_bf16 v[202:205], v[20:23], a[132:135], v[174:177]
v_accvgpr_write_b32 a124, v226
v_accvgpr_write_b32 a125, v227
ds_read_b64_tr_b16 v[62:63], v1 offset:16392
v_accvgpr_write_b32 a126, v228
v_accvgpr_write_b32 a127, v229
v_mfma_f32_16x16x32_bf16 v[206:209], v[22:25], a[136:139], v[170:173]
v_cvt_pk_bf16_f32 v222, v190, v191
v_cvt_pk_bf16_f32 v223, v192, v193
v_cvt_pk_bf16_f32 v224, v194, v195
ds_read_b64_tr_b16 v[64:65], v1 offset:17416
v_cvt_pk_bf16_f32 v225, v196, v197
v_mfma_f32_16x16x32_bf16 v[210:213], v[20:23], a[136:139], v[170:173]
v_accvgpr_write_b32 a128, v222
v_accvgpr_write_b32 a129, v223
v_accvgpr_write_b32 a130, v224
ds_read_b64_tr_b16 v[66:67], v1 offset:24576
v_accvgpr_write_b32 a131, v225
v_mfma_f32_16x16x32_bf16 v[214:217], v[22:25], a[140:143], v[174:177]
v_cvt_pk_bf16_f32 v226, v198, v199
v_cvt_pk_bf16_f32 v227, v200, v201
v_cvt_pk_bf16_f32 v228, v202, v203
v_cvt_pk_bf16_f32 v229, v204, v205
ds_read_b64_tr_b16 v[68:69], v1 offset:25600
v_mfma_f32_16x16x32_bf16 v[218:221], v[20:23], a[140:143], v[174:177]
s_waitcnt lgkmcnt(4)
v_accvgpr_write_b32 a132, v226
v_accvgpr_write_b32 a133, v227
v_accvgpr_write_b32 a134, v228
v_accvgpr_write_b32 a135, v229
v_cvt_pk_bf16_f32 v222, v206, v207
ds_read_b64_tr_b16 v[70:71], v1 offset:24584
v_cvt_pk_bf16_f32 v223, v208, v209
v_cvt_pk_bf16_f32 v224, v210, v211
v_cvt_pk_bf16_f32 v225, v212, v213
v_accvgpr_write_b32 a136, v222
v_accvgpr_write_b32 a137, v223
v_accvgpr_write_b32 a138, v224
ds_read_b64_tr_b16 v[72:73], v1 offset:25608
v_accvgpr_write_b32 a139, v225
v_cvt_pk_bf16_f32 v226, v214, v215
v_cvt_pk_bf16_f32 v227, v216, v217
v_cvt_pk_bf16_f32 v228, v218, v219
v_cvt_pk_bf16_f32 v229, v220, v221
v_accvgpr_write_b32 a140, v226
v_accvgpr_write_b32 a141, v227
v_accvgpr_write_b32 a142, v228
v_accvgpr_write_b32 a143, v229
v_mfma_f32_16x16x32_bf16 v[178:181], v[186:189], v[58:61], v[178:181]
s_waitcnt lgkmcnt(4)
v_mfma_f32_16x16x32_bf16 v[182:185], v[186:189], v[62:65], v[182:185]
s_waitcnt lgkmcnt(2)
v_mfma_f32_16x16x32_bf16 v[178:181], v[186:189], v[66:69], v[178:181]
s_waitcnt lgkmcnt(0)
v_mfma_f32_16x16x32_bf16 v[182:185], v[186:189], v[70:73], v[182:185]
s_nop 5
v_mul_f32 v178, 0xbae38e39, v178
v_mul_f32 v179, 0xbae38e39, v179
v_mul_f32 v180, 0xbae38e39, v180
v_mul_f32 v181, 0xbae38e39, v181
v_mul_f32 v182, 0xbae38e39, v182
v_mul_f32 v183, 0xbae38e39, v183
v_mul_f32 v184, 0xbae38e39, v184
v_mul_f32 v185, 0xbae38e39, v185
v_mfma_f32_16x16x32_bf16 v[190:193], v[22:25], a[144:147], v[178:181]
v_mfma_f32_16x16x32_bf16 v[194:197], v[20:23], a[144:147], v[178:181]
v_mfma_f32_16x16x32_bf16 v[198:201], v[22:25], a[148:151], v[182:185]
v_mfma_f32_16x16x32_bf16 v[202:205], v[20:23], a[148:151], v[182:185]
v_mfma_f32_16x16x32_bf16 v[206:209], v[22:25], a[152:155], v[178:181]
s_nop 3
v_cvt_pk_bf16_f32 v222, v190, v191
v_cvt_pk_bf16_f32 v223, v192, v193
v_cvt_pk_bf16_f32 v224, v194, v195
v_cvt_pk_bf16_f32 v225, v196, v197
v_mfma_f32_16x16x32_bf16 v[210:213], v[20:23], a[152:155], v[178:181]
v_accvgpr_write_b32 a144, v222
v_accvgpr_write_b32 a145, v223
v_accvgpr_write_b32 a146, v224
v_accvgpr_write_b32 a147, v225
v_mfma_f32_16x16x32_bf16 v[214:217], v[22:25], a[156:159], v[182:185]
v_cvt_pk_bf16_f32 v226, v198, v199
)"
R"(v_cvt_pk_bf16_f32 v227, v200, v201
v_cvt_pk_bf16_f32 v228, v202, v203
v_cvt_pk_bf16_f32 v229, v204, v205
v_mfma_f32_16x16x32_bf16 v[218:221], v[20:23], a[156:159], v[182:185]
v_accvgpr_write_b32 a148, v226
v_accvgpr_write_b32 a149, v227
v_accvgpr_write_b32 a150, v228
v_accvgpr_write_b32 a151, v229
v_mfma_f32_16x16x32_bf16 v[190:193], v[22:25], a[160:163], v[178:181]
v_cvt_pk_bf16_f32 v222, v206, v207
v_cvt_pk_bf16_f32 v223, v208, v209
v_cvt_pk_bf16_f32 v224, v210, v211
v_cvt_pk_bf16_f32 v225, v212, v213
v_mfma_f32_16x16x32_bf16 v[194:197], v[20:23], a[160:163], v[178:181]
v_accvgpr_write_b32 a152, v222
v_accvgpr_write_b32 a153, v223
v_accvgpr_write_b32 a154, v224
v_accvgpr_write_b32 a155, v225
v_mfma_f32_16x16x32_bf16 v[198:201], v[22:25], a[164:167], v[182:185]
v_cvt_pk_bf16_f32 v226, v214, v215
v_cvt_pk_bf16_f32 v227, v216, v217
v_cvt_pk_bf16_f32 v228, v218, v219
v_cvt_pk_bf16_f32 v229, v220, v221
v_mfma_f32_16x16x32_bf16 v[202:205], v[20:23], a[164:167], v[182:185]
v_accvgpr_write_b32 a156, v226
v_accvgpr_write_b32 a157, v227
v_accvgpr_write_b32 a158, v228
v_accvgpr_write_b32 a159, v229
v_mfma_f32_16x16x32_bf16 v[206:209], v[22:25], a[168:171], v[178:181]
v_cvt_pk_bf16_f32 v222, v190, v191
v_cvt_pk_bf16_f32 v223, v192, v193
v_cvt_pk_bf16_f32 v224, v194, v195
v_cvt_pk_bf16_f32 v225, v196, v197
v_mfma_f32_16x16x32_bf16 v[210:213], v[20:23], a[168:171], v[178:181]
v_accvgpr_write_b32 a160, v222
v_accvgpr_write_b32 a161, v223
v_accvgpr_write_b32 a162, v224
v_accvgpr_write_b32 a163, v225
v_mfma_f32_16x16x32_bf16 v[214:217], v[22:25], a[172:175], v[182:185]
v_cvt_pk_bf16_f32 v226, v198, v199
v_cvt_pk_bf16_f32 v227, v200, v201
v_cvt_pk_bf16_f32 v228, v202, v203
v_cvt_pk_bf16_f32 v229, v204, v205
v_mfma_f32_16x16x32_bf16 v[218:221], v[20:23], a[172:175], v[182:185]
v_accvgpr_write_b32 a164, v226
v_accvgpr_write_b32 a165, v227
v_accvgpr_write_b32 a166, v228
v_accvgpr_write_b32 a167, v229
v_mfma_f32_16x16x32_bf16 v[190:193], v[22:25], a[176:179], v[178:181]
v_cvt_pk_bf16_f32 v222, v206, v207
v_cvt_pk_bf16_f32 v223, v208, v209
v_cvt_pk_bf16_f32 v224, v210, v211
v_cvt_pk_bf16_f32 v225, v212, v213
v_mfma_f32_16x16x32_bf16 v[194:197], v[20:23], a[176:179], v[178:181]
v_accvgpr_write_b32 a168, v222
v_accvgpr_write_b32 a169, v223
v_accvgpr_write_b32 a170, v224
v_accvgpr_write_b32 a171, v225
v_mfma_f32_16x16x32_bf16 v[198:201], v[22:25], a[180:183], v[182:185]
v_cvt_pk_bf16_f32 v226, v214, v215
v_cvt_pk_bf16_f32 v227, v216, v217
v_cvt_pk_bf16_f32 v228, v218, v219
v_cvt_pk_bf16_f32 v229, v220, v221
v_mfma_f32_16x16x32_bf16 v[202:205], v[20:23], a[180:183], v[182:185]
v_accvgpr_write_b32 a172, v226
v_accvgpr_write_b32 a173, v227
v_accvgpr_write_b32 a174, v228
v_accvgpr_write_b32 a175, v229
v_mfma_f32_16x16x32_bf16 v[206:209], v[22:25], a[184:187], v[178:181]
v_cvt_pk_bf16_f32 v222, v190, v191
v_cvt_pk_bf16_f32 v223, v192, v193
v_cvt_pk_bf16_f32 v224, v194, v195
v_cvt_pk_bf16_f32 v225, v196, v197
v_mfma_f32_16x16x32_bf16 v[210:213], v[20:23], a[184:187], v[178:181]
v_accvgpr_write_b32 a176, v222
v_accvgpr_write_b32 a177, v223
v_accvgpr_write_b32 a178, v224
v_accvgpr_write_b32 a179, v225
v_mfma_f32_16x16x32_bf16 v[214:217], v[22:25], a[188:191], v[182:185]
v_cvt_pk_bf16_f32 v226, v198, v199
v_cvt_pk_bf16_f32 v227, v200, v201
v_cvt_pk_bf16_f32 v228, v202, v203
v_cvt_pk_bf16_f32 v229, v204, v205
v_mfma_f32_16x16x32_bf16 v[218:221], v[20:23], a[188:191], v[182:185]
v_accvgpr_write_b32 a180, v226
v_accvgpr_write_b32 a181, v227
v_accvgpr_write_b32 a182, v228
v_accvgpr_write_b32 a183, v229
v_mfma_f32_16x16x32_bf16 v[190:193], v[22:25], a[192:195], v[178:181]
v_cvt_pk_bf16_f32 v222, v206, v207
v_cvt_pk_bf16_f32 v223, v208, v209
v_cvt_pk_bf16_f32 v224, v210, v211
v_cvt_pk_bf16_f32 v225, v212, v213
v_mfma_f32_16x16x32_bf16 v[194:197], v[20:23], a[192:195], v[178:181]
v_accvgpr_write_b32 a184, v222
v_accvgpr_write_b32 a185, v223
v_accvgpr_write_b32 a186, v224
v_accvgpr_write_b32 a187, v225
v_mfma_f32_16x16x32_bf16 v[198:201], v[22:25], a[196:199], v[182:185]
v_cvt_pk_bf16_f32 v226, v214, v215
v_cvt_pk_bf16_f32 v227, v216, v217
v_cvt_pk_bf16_f32 v228, v218, v219
v_cvt_pk_bf16_f32 v229, v220, v221
v_mfma_f32_16x16x32_bf16 v[202:205], v[20:23], a[196:199], v[182:185]
v_accvgpr_write_b32 a188, v226
v_accvgpr_write_b32 a189, v227
v_accvgpr_write_b32 a190, v228
v_accvgpr_write_b32 a191, v229
v_mfma_f32_16x16x32_bf16 v[206:209], v[22:25], a[200:203], v[178:181]
v_cvt_pk_bf16_f32 v222, v190, v191
v_cvt_pk_bf16_f32 v223, v192, v193
v_cvt_pk_bf16_f32 v224, v194, v195
v_cvt_pk_bf16_f32 v225, v196, v197
v_mfma_f32_16x16x32_bf16 v[210:213], v[20:23], a[200:203], v[178:181]
v_accvgpr_write_b32 a192, v222
v_accvgpr_write_b32 a193, v223
v_accvgpr_write_b32 a194, v224
v_accvgpr_write_b32 a195, v225
v_mfma_f32_16x16x32_bf16 v[214:217], v[22:25], a[204:207], v[182:185]
v_cvt_pk_bf16_f32 v226, v198, v199
v_cvt_pk_bf16_f32 v227, v200, v201
v_cvt_pk_bf16_f32 v228, v202, v203
v_cvt_pk_bf16_f32 v229, v204, v205
v_mfma_f32_16x16x32_bf16 v[218:221], v[20:23], a[204:207], v[182:185]
v_accvgpr_write_b32 a196, v226
v_accvgpr_write_b32 a197, v227
v_accvgpr_write_b32 a198, v228
v_accvgpr_write_b32 a199, v229
v_mfma_f32_16x16x32_bf16 v[190:193], v[22:25], a[208:211], v[178:181]
v_cvt_pk_bf16_f32 v222, v206, v207
v_cvt_pk_bf16_f32 v223, v208, v209
v_cvt_pk_bf16_f32 v224, v210, v211
v_cvt_pk_bf16_f32 v225, v212, v213
v_mfma_f32_16x16x32_bf16 v[194:197], v[20:23], a[208:211], v[178:181]
v_accvgpr_write_b32 a200, v222
v_accvgpr_write_b32 a201, v223
v_accvgpr_write_b32 a202, v224
v_accvgpr_write_b32 a203, v225
v_mfma_f32_16x16x32_bf16 v[198:201], v[22:25], a[212:215], v[182:185]
v_cvt_pk_bf16_f32 v226, v214, v215
v_cvt_pk_bf16_f32 v227, v216, v217
v_cvt_pk_bf16_f32 v228, v218, v219
v_cvt_pk_bf16_f32 v229, v220, v221
v_mfma_f32_16x16x32_bf16 v[202:205], v[20:23], a[212:215], v[182:185]
v_accvgpr_write_b32 a204, v226
v_accvgpr_write_b32 a205, v227
v_accvgpr_write_b32 a206, v228
v_accvgpr_write_b32 a207, v229
v_mfma_f32_16x16x32_bf16 v[206:209], v[22:25], a[216:219], v[178:181]
v_cvt_pk_bf16_f32 v222, v190, v191
v_cvt_pk_bf16_f32 v223, v192, v193
v_cvt_pk_bf16_f32 v224, v194, v195
v_cvt_pk_bf16_f32 v225, v196, v197
v_mfma_f32_16x16x32_bf16 v[210:213], v[20:23], a[216:219], v[178:181]
v_accvgpr_write_b32 a208, v222
v_accvgpr_write_b32 a209, v223
v_accvgpr_write_b32 a210, v224
v_accvgpr_write_b32 a211, v225
v_mfma_f32_16x16x32_bf16 v[214:217], v[22:25], a[220:223], v[182:185]
v_cvt_pk_bf16_f32 v226, v198, v199
v_cvt_pk_bf16_f32 v227, v200, v201
v_cvt_pk_bf16_f32 v228, v202, v203
v_cvt_pk_bf16_f32 v229, v204, v205
v_mfma_f32_16x16x32_bf16 v[218:221], v[20:23], a[220:223], v[182:185]
v_accvgpr_write_b32 a212, v226
v_accvgpr_write_b32 a213, v227
v_accvgpr_write_b32 a214, v228
v_accvgpr_write_b32 a215, v229
v_mfma_f32_16x16x32_bf16 v[190:193], v[22:25], a[224:227], v[178:181]
v_cvt_pk_bf16_f32 v222, v206, v207
v_cvt_pk_bf16_f32 v223, v208, v209
v_cvt_pk_bf16_f32 v224, v210, v211
v_cvt_pk_bf16_f32 v225, v212, v213
v_mfma_f32_16x16x32_bf16 v[194:197], v[20:23], a[224:227], v[178:181]
v_accvgpr_write_b32 a216, v222
v_accvgpr_write_b32 a217, v223
v_accvgpr_write_b32 a218, v224
v_accvgpr_write_b32 a219, v225
v_mfma_f32_16x16x32_bf16 v[198:201], v[22:25], a[228:231], v[182:185]
v_cvt_pk_bf16_f32 v226, v214, v215
v_cvt_pk_bf16_f32 v227, v216, v217
v_cvt_pk_bf16_f32 v228, v218, v219
v_cvt_pk_bf16_f32 v229, v220, v221
v_mfma_f32_16x16x32_bf16 v[202:205], v[20:23], a[228:231], v[182:185]
v_accvgpr_write_b32 a220, v226
v_accvgpr_write_b32 a221, v227
v_accvgpr_write_b32 a222, v228
v_accvgpr_write_b32 a223, v229
v_mfma_f32_16x16x32_bf16 v[206:209], v[22:25], a[232:235], v[178:181]
v_cvt_pk_bf16_f32 v222, v190, v191
v_cvt_pk_bf16_f32 v223, v192, v193
v_cvt_pk_bf16_f32 v224, v194, v195
v_cvt_pk_bf16_f32 v225, v196, v197
v_mfma_f32_16x16x32_bf16 v[210:213], v[20:23], a[232:235], v[178:181]
v_accvgpr_write_b32 a224, v222
v_accvgpr_write_b32 a225, v223
v_accvgpr_write_b32 a226, v224
v_accvgpr_write_b32 a227, v225
v_mfma_f32_16x16x32_bf16 v[214:217], v[22:25], a[236:239], v[182:185]
v_cvt_pk_bf16_f32 v226, v198, v199
v_cvt_pk_bf16_f32 v227, v200, v201
v_cvt_pk_bf16_f32 v228, v202, v203
v_cvt_pk_bf16_f32 v229, v204, v205
v_mfma_f32_16x16x32_bf16 v[218:221], v[20:23], a[236:239], v[182:185]
v_accvgpr_write_b32 a228, v226
v_accvgpr_write_b32 a229, v227
v_accvgpr_write_b32 a230, v228
v_accvgpr_write_b32 a231, v229
v_mfma_f32_16x16x32_bf16 v[190:193], v[22:25], v[26:29], v[178:181]
v_cvt_pk_bf16_f32 v222, v206, v207
v_cvt_pk_bf16_f32 v223, v208, v209
v_cvt_pk_bf16_f32 v224, v210, v211
v_cvt_pk_bf16_f32 v225, v212, v213
v_mfma_f32_16x16x32_bf16 v[194:197], v[20:23], v[26:29], v[178:181]
v_accvgpr_write_b32 a232, v222
v_accvgpr_write_b32 a233, v223
v_accvgpr_write_b32 a234, v224
v_accvgpr_write_b32 a235, v225
v_mfma_f32_16x16x32_bf16 v[198:201], v[22:25], v[30:33], v[182:185]
v_cvt_pk_bf16_f32 v226, v214, v215
v_cvt_pk_bf16_f32 v227, v216, v217
v_cvt_pk_bf16_f32 v228, v218, v219
v_cvt_pk_bf16_f32 v229, v220, v221
v_mfma_f32_16x16x32_bf16 v[202:205], v[20:23], v[30:33], v[182:185]
v_accvgpr_write_b32 a236, v226
v_accvgpr_write_b32 a237, v227
v_accvgpr_write_b32 a238, v228
v_accvgpr_write_b32 a239, v229
v_mfma_f32_16x16x32_bf16 v[206:209], v[22:25], v[34:37], v[178:181]
v_cvt_pk_bf16_f32 v26, v190, v191
v_cvt_pk_bf16_f32 v27, v192, v193
v_cvt_pk_bf16_f32 v28, v194, v195
v_cvt_pk_bf16_f32 v29, v196, v197
v_mfma_f32_16x16x32_bf16 v[210:213], v[20:23], v[34:37], v[178:181]
v_mfma_f32_16x16x32_bf16 v[214:217], v[22:25], v[38:41], v[182:185]
v_cvt_pk_bf16_f32 v30, v198, v199
v_cvt_pk_bf16_f32 v31, v200, v201
v_cvt_pk_bf16_f32 v32, v202, v203
v_cvt_pk_bf16_f32 v33, v204, v205
v_mfma_f32_16x16x32_bf16 v[218:221], v[20:23], v[38:41], v[182:185]
v_mfma_f32_16x16x32_bf16 v[190:193], v[22:25], v[42:45], v[178:181]
v_cvt_pk_bf16_f32 v34, v206, v207
v_cvt_pk_bf16_f32 v35, v208, v209
v_cvt_pk_bf16_f32 v36, v210, v211
v_cvt_pk_bf16_f32 v37, v212, v213
v_mfma_f32_16x16x32_bf16 v[194:197], v[20:23], v[42:45], v[178:181]
v_mfma_f32_16x16x32_bf16 v[198:201], v[22:25], v[46:49], v[182:185]
v_cvt_pk_bf16_f32 v38, v214, v215
v_cvt_pk_bf16_f32 v39, v216, v217
v_cvt_pk_bf16_f32 v40, v218, v219
v_cvt_pk_bf16_f32 v41, v220, v221
v_mfma_f32_16x16x32_bf16 v[202:205], v[20:23], v[46:49], v[182:185]
v_mfma_f32_16x16x32_bf16 v[206:209], v[22:25], v[50:53], v[178:181]
v_cvt_pk_bf16_f32 v42, v190, v191
v_cvt_pk_bf16_f32 v43, v192, v193
v_cvt_pk_bf16_f32 v44, v194, v195
v_cvt_pk_bf16_f32 v45, v196, v197
v_mfma_f32_16x16x32_bf16 v[210:213], v[20:23], v[50:53], v[178:181]
v_mfma_f32_16x16x32_bf16 v[214:217], v[22:25], v[54:57], v[182:185]
v_cvt_pk_bf16_f32 v46, v198, v199
v_cvt_pk_bf16_f32 v47, v200, v201
v_cvt_pk_bf16_f32 v48, v202, v203
v_cvt_pk_bf16_f32 v49, v204, v205
v_mfma_f32_16x16x32_bf16 v[218:221], v[20:23], v[54:57], v[182:185]
v_mfma_f32_16x16x32_bf16 v[190:193], v[22:25], v[58:61], v[178:181]
v_cvt_pk_bf16_f32 v50, v206, v207
v_cvt_pk_bf16_f32 v51, v208, v209
v_cvt_pk_bf16_f32 v52, v210, v211
v_cvt_pk_bf16_f32 v53, v212, v213
v_mfma_f32_16x16x32_bf16 v[194:197], v[20:23], v[58:61], v[178:181]
v_mfma_f32_16x16x32_bf16 v[198:201], v[22:25], v[62:65], v[182:185]
v_cvt_pk_bf16_f32 v54, v214, v215
v_cvt_pk_bf16_f32 v55, v216, v217
v_cvt_pk_bf16_f32 v56, v218, v219
v_cvt_pk_bf16_f32 v57, v220, v221
v_mfma_f32_16x16x32_bf16 v[202:205], v[20:23], v[62:65], v[182:185]
v_mfma_f32_16x16x32_bf16 v[206:209], v[22:25], v[66:69], v[178:181]
v_cvt_pk_bf16_f32 v58, v190, v191
v_cvt_pk_bf16_f32 v59, v192, v193
v_cvt_pk_bf16_f32 v60, v194, v195
v_cvt_pk_bf16_f32 v61, v196, v197
v_mfma_f32_16x16x32_bf16 v[210:213], v[20:23], v[66:69], v[178:181]
v_mfma_f32_16x16x32_bf16 v[214:217], v[22:25], v[70:73], v[182:185]
v_cvt_pk_bf16_f32 v62, v198, v199
v_cvt_pk_bf16_f32 v63, v200, v201
v_cvt_pk_bf16_f32 v64, v202, v203
v_cvt_pk_bf16_f32 v65, v204, v205
v_mfma_f32_16x16x32_bf16 v[218:221], v[20:23], v[70:73], v[182:185]
v_cvt_pk_bf16_f32 v66, v206, v207
v_cvt_pk_bf16_f32 v67, v208, v209
v_cvt_pk_bf16_f32 v68, v210, v211
v_cvt_pk_bf16_f32 v69, v212, v213
v_cvt_pk_bf16_f32 v70, v214, v215
v_cvt_pk_bf16_f32 v71, v216, v217
s_nop 1
v_cvt_pk_bf16_f32 v72, v218, v219
v_cvt_pk_bf16_f32 v73, v220, v221
s_barrier
s_and_b32 s75, s36, 3
s_cmp_eq_u32 s75, 1
s_cbranch_scc1 .Lgu_w2
s_cmp_eq_u32 s75, 2
s_cbranch_scc1 .Lgu_w3
s_cmp_eq_u32 s75, 3
s_cbranch_scc1 .Lgu_w4
.Lgu_w1:
v_add_u32 v254, -1, v5
v_add_u32 v255, -1, v5
v_add_lshl_u32 v252, v7, 0, 7
v_bfi_b32 v252, s69, v252, v6
v_add_lshl_u32 v253, v5, 0, 7
v_bfi_b32 v253, s69, v253, v4
s_add_u32 m0, s45, 18432
s_xor_b32 s45, s45, 0x10000
s_mov_b32 m0, s45
s_barrier
global_load_lds_dwordx4 v2, s[38:39] offset:-2048 sc1
global_load_lds_dwordx4 v2, s[38:39] offset:-1024 sc1
global_load_lds_dwordx4 v2, s[38:39] offset:0 sc1
global_load_lds_dwordx4 v2, s[38:39] offset:1024 sc1
global_load_lds_dwordx4 v2, s[38:39] offset:2048 sc1
global_load_lds_dwordx4 v2, s[38:39] offset:3072 sc1
v_add_u32 v2, 18688, v2
s_mov_b32 s70, 32
s_waitcnt vmcnt(0) lgkmcnt(0)
s_nop 7
s_branch .Lgu_in5
.p2align 6
.Lgu_top6:
s_waitcnt lgkmcnt(5)
v_mfma_f32_16x16x32_bf16 v[74:77], a[0:3], v[106:109], 0
s_waitcnt lgkmcnt(4)
v_cvt_pk_bf16_f32 v162, v154, v155
v_cvt_pk_bf16_f32 v163, v156, v157
v_mfma_f32_16x16x32_bf16 v[74:77], a[8:11], v[110:113], v[74:77]
s_waitcnt lgkmcnt(3)
v_mfma_f32_16x16x32_bf16 v[74:77], a[16:19], v[114:117], v[74:77]
s_waitcnt lgkmcnt(2)
v_mfma_f32_16x16x32_bf16 v[74:77], a[24:27], v[118:121], v[74:77]
s_waitcnt lgkmcnt(1)
v_mul_f32 v158, v94, v102
v_mul_f32 v159, v95, v103
v_mfma_f32_16x16x32_bf16 v[74:77], a[32:35], v[122:125], v[74:77]
s_waitcnt lgkmcnt(0)
v_mul_f32 v160, v96, v104
v_mul_f32 v161, v97, v105
v_mfma_f32_16x16x32_bf16 v[74:77], a[40:43], v[126:129], v[74:77]
ds_read_b128 v[130:133], v8 offset:6144
v_cvt_pk_bf16_f32 v164, v158, v159
v_mfma_f32_16x16x32_bf16 v[82:85], a[144:147], v[106:109], 0
ds_read_b128 v[134:137], v8 offset:7168
v_cvt_pk_bf16_f32 v165, v160, v161
v_mfma_f32_16x16x32_bf16 v[82:85], a[152:155], v[110:113], v[82:85]
v_mfma_f32_16x16x32_bf16 v[82:85], a[160:163], v[114:117], v[82:85]
s_waitcnt vmcnt(6)
s_barrier
v_mfma_f32_16x16x32_bf16 v[82:85], a[168:171], v[118:121], v[82:85]
v_mfma_f32_16x16x32_bf16 v[82:85], a[176:179], v[122:125], v[82:85]
global_store_dwordx4 v3, v[162:165], s[38:39] sc1
v_mfma_f32_16x16x32_bf16 v[82:85], a[184:187], v[126:129], v[82:85]
ds_read_b128 v[138:141], v8 offset:8192
v_add_u32 v3, 147456, v3
v_mfma_f32_16x16x32_bf16 v[78:81], a[4:7], v[106:109], 0
v_mfma_f32_16x16x32_bf16 v[78:81], a[12:15], v[110:113], v[78:81]
v_mfma_f32_16x16x32_bf16 v[78:81], a[20:23], v[114:117], v[78:81]
ds_read_b128 v[142:145], v8 offset:9216
v_mfma_f32_16x16x32_bf16 v[78:81], a[28:31], v[118:121], v[78:81]
v_mfma_f32_16x16x32_bf16 v[78:81], a[36:39], v[122:125], v[78:81]
v_mfma_f32_16x16x32_bf16 v[78:81], a[44:47], v[126:129], v[78:81]
ds_read_b128 v[146:149], v8 offset:10240
v_mfma_f32_16x16x32_bf16 v[86:89], a[148:151], v[106:109], 0
v_mfma_f32_16x16x32_bf16 v[86:89], a[156:159], v[110:113], v[86:89]
v_mfma_f32_16x16x32_bf16 v[86:89], a[164:167], v[114:117], v[86:89]
ds_read_b128 v[150:153], v8 offset:11264
v_mfma_f32_16x16x32_bf16 v[86:89], a[172:175], v[118:121], v[86:89]
v_mfma_f32_16x16x32_bf16 v[86:89], a[180:183], v[122:125], v[86:89]
v_mfma_f32_16x16x32_bf16 v[86:89], a[188:191], v[126:129], v[86:89]
s_waitcnt lgkmcnt(5)
v_mfma_f32_16x16x32_bf16 v[74:77], a[48:51], v[130:133], v[74:77]
s_waitcnt lgkmcnt(4)
v_mfma_f32_16x16x32_bf16 v[74:77], a[56:59], v[134:137], v[74:77]
s_waitcnt lgkmcnt(3)
v_mfma_f32_16x16x32_bf16 v[74:77], a[64:67], v[138:141], v[74:77]
s_waitcnt lgkmcnt(2)
v_mfma_f32_16x16x32_bf16 v[74:77], a[72:75], v[142:145], v[74:77]
s_waitcnt lgkmcnt(1)
v_mfma_f32_16x16x32_bf16 v[74:77], a[80:83], v[146:149], v[74:77]
s_waitcnt lgkmcnt(0)
v_mfma_f32_16x16x32_bf16 v[74:77], a[88:91], v[150:153], v[74:77]
ds_read_b128 v[106:109], v8 offset:12288
v_mfma_f32_16x16x32_bf16 v[82:85], a[192:195], v[130:133], v[82:85]
v_mfma_f32_16x16x32_bf16 v[82:85], a[200:203], v[134:137], v[82:85]
v_mfma_f32_16x16x32_bf16 v[82:85], a[208:211], v[138:141], v[82:85]
ds_read_b128 v[110:113], v8 offset:13312
v_mfma_f32_16x16x32_bf16 v[82:85], a[216:219], v[142:145], v[82:85]
v_mfma_f32_16x16x32_bf16 v[82:85], a[224:227], v[146:149], v[82:85]
v_mfma_f32_16x16x32_bf16 v[82:85], a[232:235], v[150:153], v[82:85]
ds_read_b128 v[114:117], v8 offset:14336
v_mfma_f32_16x16x32_bf16 v[78:81], a[52:55], v[130:133], v[78:81]
v_mfma_f32_16x16x32_bf16 v[78:81], a[60:63], v[134:137], v[78:81]
)"
R"(v_mfma_f32_16x16x32_bf16 v[78:81], a[68:71], v[138:141], v[78:81]
ds_read_b128 v[118:121], v8 offset:15360
v_mfma_f32_16x16x32_bf16 v[78:81], a[76:79], v[142:145], v[78:81]
v_mfma_f32_16x16x32_bf16 v[78:81], a[84:87], v[146:149], v[78:81]
v_mfma_f32_16x16x32_bf16 v[78:81], a[92:95], v[150:153], v[78:81]
ds_read_b128 v[122:125], v8 offset:16384
v_mfma_f32_16x16x32_bf16 v[86:89], a[196:199], v[130:133], v[86:89]
v_mfma_f32_16x16x32_bf16 v[86:89], a[204:207], v[134:137], v[86:89]
v_mfma_f32_16x16x32_bf16 v[86:89], a[212:215], v[138:141], v[86:89]
ds_read_b128 v[126:129], v8 offset:17408
v_mfma_f32_16x16x32_bf16 v[86:89], a[220:223], v[142:145], v[86:89]
s_xor_b32 s45, s45, 0x10000
v_mfma_f32_16x16x32_bf16 v[86:89], a[228:231], v[146:149], v[86:89]
s_add_u32 m0, s45, 0
v_mfma_f32_16x16x32_bf16 v[86:89], a[236:239], v[150:153], v[86:89]
s_waitcnt lgkmcnt(5)
v_mfma_f32_16x16x32_bf16 v[74:77], a[96:99], v[106:109], v[74:77]
s_barrier
s_waitcnt lgkmcnt(4)
v_mfma_f32_16x16x32_bf16 v[74:77], a[104:107], v[110:113], v[74:77]
s_waitcnt lgkmcnt(3)
global_load_lds_dwordx4 v2, s[38:39] offset:-2048 sc1
v_mfma_f32_16x16x32_bf16 v[74:77], a[112:115], v[114:117], v[74:77]
s_waitcnt lgkmcnt(2)
v_mfma_f32_16x16x32_bf16 v[74:77], a[120:123], v[118:121], v[74:77]
s_waitcnt lgkmcnt(1)
v_mfma_f32_16x16x32_bf16 v[74:77], a[128:131], v[122:125], v[74:77]
s_waitcnt lgkmcnt(0)
global_load_lds_dwordx4 v2, s[38:39] offset:-1024 sc1
v_mfma_f32_16x16x32_bf16 v[74:77], a[136:139], v[126:129], v[74:77]
v_mfma_f32_16x16x32_bf16 v[82:85], v[26:29], v[106:109], v[82:85]
v_mfma_f32_16x16x32_bf16 v[82:85], v[34:37], v[110:113], v[82:85]
global_load_lds_dwordx4 v2, s[38:39] offset:0 sc1
v_mfma_f32_16x16x32_bf16 v[82:85], v[42:45], v[114:117], v[82:85]
v_mfma_f32_16x16x32_bf16 v[82:85], v[50:53], v[118:121], v[82:85]
v_mfma_f32_16x16x32_bf16 v[82:85], v[58:61], v[122:125], v[82:85]
global_load_lds_dwordx4 v2, s[38:39] offset:1024 sc1
v_mfma_f32_16x16x32_bf16 v[82:85], v[66:69], v[126:129], v[82:85]
v_mfma_f32_16x16x32_bf16 v[78:81], a[100:103], v[106:109], v[78:81]
s_waitcnt vmcnt(5)
s_barrier
v_mfma_f32_16x16x32_bf16 v[78:81], a[108:111], v[110:113], v[78:81]
global_load_lds_dwordx4 v2, s[38:39] offset:2048 sc1
v_mfma_f32_16x16x32_bf16 v[78:81], a[116:119], v[114:117], v[78:81]
v_mfma_f32_16x16x32_bf16 v[78:81], a[124:127], v[118:121], v[78:81]
ds_read_b128 v[130:133], v8 offset:18432
v_mfma_f32_16x16x32_bf16 v[78:81], a[132:135], v[122:125], v[78:81]
ds_read_b128 v[134:137], v8 offset:19456
global_load_lds_dwordx4 v2, s[38:39] offset:3072 sc1
v_mfma_f32_16x16x32_bf16 v[78:81], a[140:143], v[126:129], v[78:81]
ds_read_b128 v[138:141], v8 offset:20480
v_add_u32 v2, 18688, v2
v_mfma_f32_16x16x32_bf16 v[86:89], v[30:33], v[106:109], v[86:89]
ds_read_b128 v[142:145], v8 offset:21504
v_min_u32 v2, v2, v9
v_mfma_f32_16x16x32_bf16 v[86:89], v[38:41], v[110:113], v[86:89]
ds_read_b128 v[146:149], v8 offset:22528
v_mul_f32 v154, v74, v82
v_mfma_f32_16x16x32_bf16 v[86:89], v[46:49], v[114:117], v[86:89]
ds_read_b128 v[150:153], v8 offset:23552
v_mul_f32 v155, v75, v83
v_mfma_f32_16x16x32_bf16 v[86:89], v[54:57], v[118:121], v[86:89]
v_mul_f32 v156, v76, v84
v_mul_f32 v157, v77, v85
v_mfma_f32_16x16x32_bf16 v[86:89], v[62:65], v[122:125], v[86:89]
v_cvt_pk_bf16_f32 v162, v154, v155
v_cvt_pk_bf16_f32 v163, v156, v157
v_mfma_f32_16x16x32_bf16 v[86:89], v[70:73], v[126:129], v[86:89]
s_waitcnt lgkmcnt(5)
v_mfma_f32_16x16x32_bf16 v[90:93], a[0:3], v[130:133], 0
s_waitcnt lgkmcnt(4)
v_mfma_f32_16x16x32_bf16 v[90:93], a[8:11], v[134:137], v[90:93]
s_waitcnt lgkmcnt(3)
v_mfma_f32_16x16x32_bf16 v[90:93], a[16:19], v[138:141], v[90:93]
s_waitcnt lgkmcnt(2)
v_mfma_f32_16x16x32_bf16 v[90:93], a[24:27], v[142:145], v[90:93]
s_waitcnt lgkmcnt(1)
v_mul_f32 v158, v78, v86
v_mul_f32 v159, v79, v87
v_mfma_f32_16x16x32_bf16 v[90:93], a[32:35], v[146:149], v[90:93]
s_waitcnt lgkmcnt(0)
v_mul_f32 v160, v80, v88
v_mul_f32 v161, v81, v89
v_mfma_f32_16x16x32_bf16 v[90:93], a[40:43], v[150:153], v[90:93]
ds_read_b128 v[106:109], v8 offset:24576
v_cvt_pk_bf16_f32 v164, v158, v159
v_mfma_f32_16x16x32_bf16 v[98:101], a[144:147], v[130:133], 0
ds_read_b128 v[110:113], v8 offset:25600
v_cvt_pk_bf16_f32 v165, v160, v161
v_mfma_f32_16x16x32_bf16 v[98:101], a[152:155], v[134:137], v[98:101]
v_mfma_f32_16x16x32_bf16 v[98:101], a[160:163], v[138:141], v[98:101]
s_waitcnt vmcnt(6)
s_barrier
v_mfma_f32_16x16x32_bf16 v[98:101], a[168:171], v[142:145], v[98:101]
v_mfma_f32_16x16x32_bf16 v[98:101], a[176:179], v[146:149], v[98:101]
global_store_dwordx4 v3, v[162:165], s[38:39] sc1
v_mfma_f32_16x16x32_bf16 v[98:101], a[184:187], v[150:153], v[98:101]
ds_read_b128 v[114:117], v8 offset:26624
v_add_u32 v3, 147456, v3
v_mfma_f32_16x16x32_bf16 v[94:97], a[4:7], v[130:133], 0
v_mfma_f32_16x16x32_bf16 v[94:97], a[12:15], v[134:137], v[94:97]
v_mfma_f32_16x16x32_bf16 v[94:97], a[20:23], v[138:141], v[94:97]
ds_read_b128 v[118:121], v8 offset:27648
v_mfma_f32_16x16x32_bf16 v[94:97], a[28:31], v[142:145], v[94:97]
v_mfma_f32_16x16x32_bf16 v[94:97], a[36:39], v[146:149], v[94:97]
v_mfma_f32_16x16x32_bf16 v[94:97], a[44:47], v[150:153], v[94:97]
ds_read_b128 v[122:125], v8 offset:28672
v_mfma_f32_16x16x32_bf16 v[102:105], a[148:151], v[130:133], 0
v_mfma_f32_16x16x32_bf16 v[102:105], a[156:159], v[134:137], v[102:105]
v_mfma_f32_16x16x32_bf16 v[102:105], a[164:167], v[138:141], v[102:105]
ds_read_b128 v[126:129], v8 offset:29696
v_mfma_f32_16x16x32_bf16 v[102:105], a[172:175], v[142:145], v[102:105]
v_mfma_f32_16x16x32_bf16 v[102:105], a[180:183], v[146:149], v[102:105]
v_mfma_f32_16x16x32_bf16 v[102:105], a[188:191], v[150:153], v[102:105]
s_waitcnt lgkmcnt(5)
v_mfma_f32_16x16x32_bf16 v[90:93], a[48:51], v[106:109], v[90:93]
s_waitcnt lgkmcnt(4)
v_mfma_f32_16x16x32_bf16 v[90:93], a[56:59], v[110:113], v[90:93]
s_waitcnt lgkmcnt(3)
v_mfma_f32_16x16x32_bf16 v[90:93], a[64:67], v[114:117], v[90:93]
s_waitcnt lgkmcnt(2)
v_mfma_f32_16x16x32_bf16 v[90:93], a[72:75], v[118:121], v[90:93]
s_waitcnt lgkmcnt(1)
v_mfma_f32_16x16x32_bf16 v[90:93], a[80:83], v[122:125], v[90:93]
s_waitcnt lgkmcnt(0)
v_mfma_f32_16x16x32_bf16 v[90:93], a[88:91], v[126:129], v[90:93]
ds_read_b128 v[130:133], v8 offset:30720
v_mfma_f32_16x16x32_bf16 v[98:101], a[192:195], v[106:109], v[98:101]
v_mfma_f32_16x16x32_bf16 v[98:101], a[200:203], v[110:113], v[98:101]
v_mfma_f32_16x16x32_bf16 v[98:101], a[208:211], v[114:117], v[98:101]
ds_read_b128 v[134:137], v8 offset:31744
v_mfma_f32_16x16x32_bf16 v[98:101], a[216:219], v[118:121], v[98:101]
v_mfma_f32_16x16x32_bf16 v[98:101], a[224:227], v[122:125], v[98:101]
v_mfma_f32_16x16x32_bf16 v[98:101], a[232:235], v[126:129], v[98:101]
ds_read_b128 v[138:141], v8 offset:32768
v_mfma_f32_16x16x32_bf16 v[94:97], a[52:55], v[106:109], v[94:97]
v_mfma_f32_16x16x32_bf16 v[94:97], a[60:63], v[110:113], v[94:97]
v_mfma_f32_16x16x32_bf16 v[94:97], a[68:71], v[114:117], v[94:97]
ds_read_b128 v[142:145], v8 offset:33792
v_mfma_f32_16x16x32_bf16 v[94:97], a[76:79], v[118:121], v[94:97]
v_mfma_f32_16x16x32_bf16 v[94:97], a[84:87], v[122:125], v[94:97]
v_mfma_f32_16x16x32_bf16 v[94:97], a[92:95], v[126:129], v[94:97]
ds_read_b128 v[146:149], v8 offset:34816
v_mfma_f32_16x16x32_bf16 v[102:105], a[196:199], v[106:109], v[102:105]
.Lgu_in5:
v_mfma_f32_16x16x32_bf16 v[102:105], a[204:207], v[110:113], v[102:105]
v_mfma_f32_16x16x32_bf16 v[102:105], a[212:215], v[114:117], v[102:105]
ds_read_b128 v[150:153], v8 offset:35840
v_mfma_f32_16x16x32_bf16 v[102:105], a[220:223], v[118:121], v[102:105]
v_mfma_f32_16x16x32_bf16 v[102:105], a[228:231], v[122:125], v[102:105]
s_add_u32 m0, s45, 18432
v_mfma_f32_16x16x32_bf16 v[102:105], a[236:239], v[126:129], v[102:105]
s_waitcnt lgkmcnt(5)
v_mfma_f32_16x16x32_bf16 v[90:93], a[96:99], v[130:133], v[90:93]
s_barrier
s_waitcnt lgkmcnt(4)
v_mfma_f32_16x16x32_bf16 v[90:93], a[104:107], v[134:137], v[90:93]
s_waitcnt lgkmcnt(3)
global_load_lds_dwordx4 v2, s[38:39] offset:-2048 sc1
v_mfma_f32_16x16x32_bf16 v[90:93], a[112:115], v[138:141], v[90:93]
s_waitcnt lgkmcnt(2)
v_mfma_f32_16x16x32_bf16 v[90:93], a[120:123], v[142:145], v[90:93]
s_waitcnt lgkmcnt(1)
v_mfma_f32_16x16x32_bf16 v[90:93], a[128:131], v[146:149], v[90:93]
s_waitcnt lgkmcnt(0)
global_load_lds_dwordx4 v2, s[38:39] offset:-1024 sc1
v_mfma_f32_16x16x32_bf16 v[90:93], a[136:139], v[150:153], v[90:93]
v_mfma_f32_16x16x32_bf16 v[98:101], v[26:29], v[130:133], v[98:101]
v_mfma_f32_16x16x32_bf16 v[98:101], v[34:37], v[134:137], v[98:101]
global_load_lds_dwordx4 v2, s[38:39] offset:0 sc1
v_mfma_f32_16x16x32_bf16 v[98:101], v[42:45], v[138:141], v[98:101]
v_mfma_f32_16x16x32_bf16 v[98:101], v[50:53], v[142:145], v[98:101]
v_xor_b32 v8, 0x10000, v8
v_mfma_f32_16x16x32_bf16 v[98:101], v[58:61], v[146:149], v[98:101]
global_load_lds_dwordx4 v2, s[38:39] offset:1024 sc1
v_mfma_f32_16x16x32_bf16 v[98:101], v[66:69], v[150:153], v[98:101]
v_mfma_f32_16x16x32_bf16 v[94:97], a[100:103], v[130:133], v[94:97]
s_waitcnt vmcnt(5)
s_barrier
v_mfma_f32_16x16x32_bf16 v[94:97], a[108:111], v[134:137], v[94:97]
global_load_lds_dwordx4 v2, s[38:39] offset:2048 sc1
v_mfma_f32_16x16x32_bf16 v[94:97], a[116:119], v[138:141], v[94:97]
v_mfma_f32_16x16x32_bf16 v[94:97], a[124:127], v[142:145], v[94:97]
ds_read_b128 v[106:109], v8 offset:0
v_mfma_f32_16x16x32_bf16 v[94:97], a[132:135], v[146:149], v[94:97]
ds_read_b128 v[110:113], v8 offset:1024
global_load_lds_dwordx4 v2, s[38:39] offset:3072 sc1
v_mfma_f32_16x16x32_bf16 v[94:97], a[140:143], v[150:153], v[94:97]
ds_read_b128 v[114:117], v8 offset:2048
v_add_u32 v2, 18688, v2
v_mfma_f32_16x16x32_bf16 v[102:105], v[30:33], v[130:133], v[102:105]
ds_read_b128 v[118:121], v8 offset:3072
v_min_u32 v2, v2, v9
v_mfma_f32_16x16x32_bf16 v[102:105], v[38:41], v[134:137], v[102:105]
ds_read_b128 v[122:125], v8 offset:4096
v_mul_f32 v154, v90, v98
v_mfma_f32_16x16x32_bf16 v[102:105], v[46:49], v[138:141], v[102:105]
ds_read_b128 v[126:129], v8 offset:5120
v_mul_f32 v155, v91, v99
v_mfma_f32_16x16x32_bf16 v[102:105], v[54:57], v[142:145], v[102:105]
v_mul_f32 v156, v92, v100
v_mul_f32 v157, v93, v101
v_mfma_f32_16x16x32_bf16 v[102:105], v[62:65], v[146:149], v[102:105]
v_mfma_f32_16x16x32_bf16 v[102:105], v[70:73], v[150:153], v[102:105]
s_sub_u32 s70, s70, 1
s_cbranch_scc0 .Lgu_top6
s_waitcnt vmcnt(6) lgkmcnt(0)
s_barrier
v_mul_f32 v154, v90, v98
v_mul_f32 v155, v91, v99
v_mul_f32 v156, v92, v100
v_mul_f32 v157, v93, v101
v_cvt_pk_bf16_f32 v162, v154, v155
v_cvt_pk_bf16_f32 v163, v156, v157
v_mul_f32 v158, v94, v102
v_mul_f32 v159, v95, v103
v_mul_f32 v160, v96, v104
v_mul_f32 v161, v97, v105
v_cvt_pk_bf16_f32 v164, v158, v159
v_cvt_pk_bf16_f32 v165, v160, v161
global_store_dwordx4 v3, v[162:165], s[38:39] sc1
v_mov_b32 v231, 192
ds_read_b32 v231, v231 offset:36864
s_waitcnt vmcnt(0)
s_barrier
s_waitcnt lgkmcnt(0)
v_readfirstlane_b32 s62, v231
s_nop 3
s_max_i32 s62, s62, 2
s_min_i32 s62, s62, 66
s_add_u32 s62, s62, -2
s_branch .Lgu_fin
.Lgu_w2:
v_add_u32 v254, -1, v5
v_add_u32 v255, -1, v5
v_add_lshl_u32 v252, v7, 0, 7
v_bfi_b32 v252, s69, v252, v6
v_add_lshl_u32 v253, v5, 0, 7
v_bfi_b32 v253, s69, v253, v4
s_add_u32 m0, s45, 18432
s_xor_b32 s45, s45, 0x10000
s_mov_b32 m0, s45
s_barrier
global_load_lds_dwordx4 v2, s[38:39] offset:-2048 sc1
global_load_lds_dwordx4 v2, s[38:39] offset:-1024 sc1
global_load_lds_dwordx4 v2, s[38:39] offset:0 sc1
global_load_lds_dwordx4 v2, s[38:39] offset:1024 sc1
global_load_lds_dwordx4 v2, s[38:39] offset:2048 sc1
global_load_lds_dwordx4 v2, s[38:39] offset:3072 sc1
v_add_u32 v2, 18688, v2
s_mov_b32 s70, 32
s_waitcnt vmcnt(0) lgkmcnt(0)
s_nop 7
s_branch .Lgu_in8
.p2align 6
.Lgu_top9:
s_waitcnt lgkmcnt(5)
v_mfma_f32_16x16x32_bf16 v[74:77], a[0:3], v[106:109], 0
s_waitcnt lgkmcnt(4)
v_mul_f32 v157, v93, v101
v_cvt_pk_bf16_f32 v162, v154, v155
v_mfma_f32_16x16x32_bf16 v[74:77], a[8:11], v[110:113], v[74:77]
s_waitcnt lgkmcnt(3)
v_cvt_pk_bf16_f32 v163, v156, v157
v_mfma_f32_16x16x32_bf16 v[74:77], a[16:19], v[114:117], v[74:77]
s_waitcnt lgkmcnt(2)
v_mfma_f32_16x16x32_bf16 v[74:77], a[24:27], v[118:121], v[74:77]
s_waitcnt lgkmcnt(1)
v_mul_f32 v158, v94, v102
v_mul_f32 v159, v95, v103
v_mfma_f32_16x16x32_bf16 v[74:77], a[32:35], v[122:125], v[74:77]
s_waitcnt lgkmcnt(0)
v_mul_f32 v160, v96, v104
v_mul_f32 v161, v97, v105
v_mfma_f32_16x16x32_bf16 v[74:77], a[40:43], v[126:129], v[74:77]
ds_read_b128 v[130:133], v8 offset:6144
v_cvt_pk_bf16_f32 v164, v158, v159
v_mfma_f32_16x16x32_bf16 v[82:85], a[144:147], v[106:109], 0
ds_read_b128 v[134:137], v8 offset:7168
v_cvt_pk_bf16_f32 v165, v160, v161
v_mfma_f32_16x16x32_bf16 v[82:85], a[152:155], v[110:113], v[82:85]
v_mfma_f32_16x16x32_bf16 v[82:85], a[160:163], v[114:117], v[82:85]
s_waitcnt vmcnt(6)
s_barrier
v_mfma_f32_16x16x32_bf16 v[82:85], a[168:171], v[118:121], v[82:85]
s_mov_b64 exec, 1
global_store_dword v252, v7, s[38:39] sc1
s_mov_b64 exec, -1
v_mfma_f32_16x16x32_bf16 v[82:85], a[176:179], v[122:125], v[82:85]
v_add_u32 v7, 1, v7
v_add_lshl_u32 v252, v7, 0, 7
v_mfma_f32_16x16x32_bf16 v[82:85], a[184:187], v[126:129], v[82:85]
ds_read_b128 v[138:141], v8 offset:8192
v_bfi_b32 v252, s69, v252, v6
v_mfma_f32_16x16x32_bf16 v[78:81], a[4:7], v[106:109], 0
global_store_dwordx4 v3, v[162:165], s[38:39] sc1
v_mfma_f32_16x16x32_bf16 v[78:81], a[12:15], v[110:113], v[78:81]
v_add_u32 v3, 147456, v3
v_mfma_f32_16x16x32_bf16 v[78:81], a[20:23], v[114:117], v[78:81]
ds_read_b128 v[142:145], v8 offset:9216
v_mfma_f32_16x16x32_bf16 v[78:81], a[28:31], v[118:121], v[78:81]
v_mfma_f32_16x16x32_bf16 v[78:81], a[36:39], v[122:125], v[78:81]
v_mfma_f32_16x16x32_bf16 v[78:81], a[44:47], v[126:129], v[78:81]
ds_read_b128 v[146:149], v8 offset:10240
v_mfma_f32_16x16x32_bf16 v[86:89], a[148:151], v[106:109], 0
v_mfma_f32_16x16x32_bf16 v[86:89], a[156:159], v[110:113], v[86:89]
v_mfma_f32_16x16x32_bf16 v[86:89], a[164:167], v[114:117], v[86:89]
ds_read_b128 v[150:153], v8 offset:11264
v_mfma_f32_16x16x32_bf16 v[86:89], a[172:175], v[118:121], v[86:89]
v_mfma_f32_16x16x32_bf16 v[86:89], a[180:183], v[122:125], v[86:89]
v_mfma_f32_16x16x32_bf16 v[86:89], a[188:191], v[126:129], v[86:89]
s_waitcnt lgkmcnt(5)
v_mfma_f32_16x16x32_bf16 v[74:77], a[48:51], v[130:133], v[74:77]
s_waitcnt lgkmcnt(4)
v_mfma_f32_16x16x32_bf16 v[74:77], a[56:59], v[134:137], v[74:77]
s_waitcnt lgkmcnt(3)
v_mfma_f32_16x16x32_bf16 v[74:77], a[64:67], v[138:141], v[74:77]
s_waitcnt lgkmcnt(2)
v_mfma_f32_16x16x32_bf16 v[74:77], a[72:75], v[142:145], v[74:77]
s_waitcnt lgkmcnt(1)
v_mfma_f32_16x16x32_bf16 v[74:77], a[80:83], v[146:149], v[74:77]
s_waitcnt lgkmcnt(0)
v_mfma_f32_16x16x32_bf16 v[74:77], a[88:91], v[150:153], v[74:77]
ds_read_b128 v[106:109], v8 offset:12288
v_mfma_f32_16x16x32_bf16 v[82:85], a[192:195], v[130:133], v[82:85]
v_mfma_f32_16x16x32_bf16 v[82:85], a[200:203], v[134:137], v[82:85]
v_mfma_f32_16x16x32_bf16 v[82:85], a[208:211], v[138:141], v[82:85]
ds_read_b128 v[110:113], v8 offset:13312
v_mfma_f32_16x16x32_bf16 v[82:85], a[216:219], v[142:145], v[82:85]
v_mfma_f32_16x16x32_bf16 v[82:85], a[224:227], v[146:149], v[82:85]
v_mfma_f32_16x16x32_bf16 v[82:85], a[232:235], v[150:153], v[82:85]
ds_read_b128 v[114:117], v8 offset:14336
v_mfma_f32_16x16x32_bf16 v[78:81], a[52:55], v[130:133], v[78:81]
v_mfma_f32_16x16x32_bf16 v[78:81], a[60:63], v[134:137], v[78:81]
v_mfma_f32_16x16x32_bf16 v[78:81], a[68:71], v[138:141], v[78:81]
ds_read_b128 v[118:121], v8 offset:15360
v_mfma_f32_16x16x32_bf16 v[78:81], a[76:79], v[142:145], v[78:81]
v_mfma_f32_16x16x32_bf16 v[78:81], a[84:87], v[146:149], v[78:81]
v_mfma_f32_16x16x32_bf16 v[78:81], a[92:95], v[150:153], v[78:81]
ds_read_b128 v[122:125], v8 offset:16384
v_mfma_f32_16x16x32_bf16 v[86:89], a[196:199], v[130:133], v[86:89]
v_mfma_f32_16x16x32_bf16 v[86:89], a[204:207], v[134:137], v[86:89]
v_mfma_f32_16x16x32_bf16 v[86:89], a[212:215], v[138:141], v[86:89]
ds_read_b128 v[126:129], v8 offset:17408
v_mfma_f32_16x16x32_bf16 v[86:89], a[220:223], v[142:145], v[86:89]
s_xor_b32 s45, s45, 0x10000
v_mfma_f32_16x16x32_bf16 v[86:89], a[228:231], v[146:149], v[86:89]
s_add_u32 m0, s45, 0
v_mfma_f32_16x16x32_bf16 v[86:89], a[236:239], v[150:153], v[86:89]
s_waitcnt lgkmcnt(5)
v_mfma_f32_16x16x32_bf16 v[74:77], a[96:99], v[106:109], v[74:77]
s_barrier
s_waitcnt lgkmcnt(4)
v_mfma_f32_16x16x32_bf16 v[74:77], a[104:107], v[110:113], v[74:77]
s_waitcnt lgkmcnt(3)
v_mfma_f32_16x16x32_bf16 v[74:77], a[112:115], v[114:117], v[74:77]
s_waitcnt lgkmcnt(2)
global_load_lds_dwordx4 v2, s[38:39] offset:-2048 sc1
v_mfma_f32_16x16x32_bf16 v[74:77], a[120:123], v[118:121], v[74:77]
s_waitcnt lgkmcnt(1)
v_mfma_f32_16x16x32_bf16 v[74:77], a[128:131], v[122:125], v[74:77]
s_waitcnt lgkmcnt(0)
v_mfma_f32_16x16x32_bf16 v[74:77], a[136:139], v[126:129], v[74:77]
global_load_lds_dwordx4 v2, s[38:39] offset:-1024 sc1
v_mfma_f32_16x16x32_bf16 v[82:85], v[26:29], v[106:109], v[82:85]
v_mfma_f32_16x16x32_bf16 v[82:85], v[34:37], v[110:113], v[82:85]
v_mfma_f32_16x16x32_bf16 v[82:85], v[42:45], v[114:117], v[82:85]
global_load_lds_dwordx4 v2, s[38:39] offset:0 sc1
v_mfma_f32_16x16x32_bf16 v[82:85], v[50:53], v[118:121], v[82:85]
v_mfma_f32_16x16x32_bf16 v[82:85], v[58:61], v[122:125], v[82:85]
v_mfma_f32_16x16x32_bf16 v[82:85], v[66:69], v[126:129], v[82:85]
v_mfma_f32_16x16x32_bf16 v[78:81], a[100:103], v[106:109], v[78:81]
s_waitcnt vmcnt(5)
s_barrier
v_mfma_f32_16x16x32_bf16 v[78:81], a[108:111], v[110:113], v[78:81]
global_load_lds_dwordx4 v2, s[38:39] offset:1024 sc1
v_mfma_f32_16x16x32_bf16 v[78:81], a[116:119], v[114:117], v[78:81]
v_mfma_f32_16x16x32_bf16 v[78:81], a[124:127], v[118:121], v[78:81]
ds_read_b128 v[130:133], v8 offset:18432
v_mfma_f32_16x16x32_bf16 v[78:81], a[132:135], v[122:125], v[78:81]
ds_read_b128 v[134:137], v8 offset:19456
global_load_lds_dwordx4 v2, s[38:39] offset:2048 sc1
v_mfma_f32_16x16x32_bf16 v[78:81], a[140:143], v[126:129], v[78:81]
ds_read_b128 v[138:141], v8 offset:20480
v_mul_f32 v154, v74, v82
v_mfma_f32_16x16x32_bf16 v[86:89], v[30:33], v[106:109], v[86:89]
ds_read_b128 v[142:145], v8 offset:21504
v_mul_f32 v155, v75, v83
v_mfma_f32_16x16x32_bf16 v[86:89], v[38:41], v[110:113], v[86:89]
ds_read_b128 v[146:149], v8 offset:22528
global_load_lds_dwordx4 v2, s[38:39] offset:3072 sc1
v_mfma_f32_16x16x32_bf16 v[86:89], v[46:49], v[114:117], v[86:89]
ds_read_b128 v[150:153], v8 offset:23552
v_add_u32 v2, 18688, v2
v_mfma_f32_16x16x32_bf16 v[86:89], v[54:57], v[118:121], v[86:89]
v_min_u32 v2, v2, v9
v_mul_f32 v156, v76, v84
v_mfma_f32_16x16x32_bf16 v[86:89], v[62:65], v[122:125], v[86:89]
v_mul_f32 v157, v77, v85
v_cvt_pk_bf16_f32 v162, v154, v155
)"
R"(v_mfma_f32_16x16x32_bf16 v[86:89], v[70:73], v[126:129], v[86:89]
s_waitcnt lgkmcnt(5)
v_cvt_pk_bf16_f32 v163, v156, v157
v_mfma_f32_16x16x32_bf16 v[90:93], a[0:3], v[130:133], 0
s_waitcnt lgkmcnt(4)
v_mfma_f32_16x16x32_bf16 v[90:93], a[8:11], v[134:137], v[90:93]
s_waitcnt lgkmcnt(3)
v_mfma_f32_16x16x32_bf16 v[90:93], a[16:19], v[138:141], v[90:93]
s_waitcnt lgkmcnt(2)
v_mfma_f32_16x16x32_bf16 v[90:93], a[24:27], v[142:145], v[90:93]
s_waitcnt lgkmcnt(1)
v_mul_f32 v158, v78, v86
v_mul_f32 v159, v79, v87
v_mfma_f32_16x16x32_bf16 v[90:93], a[32:35], v[146:149], v[90:93]
s_waitcnt lgkmcnt(0)
v_mul_f32 v160, v80, v88
v_mul_f32 v161, v81, v89
v_mfma_f32_16x16x32_bf16 v[90:93], a[40:43], v[150:153], v[90:93]
ds_read_b128 v[106:109], v8 offset:24576
v_cvt_pk_bf16_f32 v164, v158, v159
v_mfma_f32_16x16x32_bf16 v[98:101], a[144:147], v[130:133], 0
ds_read_b128 v[110:113], v8 offset:25600
v_cvt_pk_bf16_f32 v165, v160, v161
v_mfma_f32_16x16x32_bf16 v[98:101], a[152:155], v[134:137], v[98:101]
v_mfma_f32_16x16x32_bf16 v[98:101], a[160:163], v[138:141], v[98:101]
s_waitcnt vmcnt(6)
s_barrier
v_mfma_f32_16x16x32_bf16 v[98:101], a[168:171], v[142:145], v[98:101]
s_mov_b64 exec, 1
global_store_dword v252, v7, s[38:39] sc1
s_mov_b64 exec, -1
v_mfma_f32_16x16x32_bf16 v[98:101], a[176:179], v[146:149], v[98:101]
v_add_u32 v7, 1, v7
v_add_lshl_u32 v252, v7, 0, 7
v_mfma_f32_16x16x32_bf16 v[98:101], a[184:187], v[150:153], v[98:101]
ds_read_b128 v[114:117], v8 offset:26624
v_bfi_b32 v252, s69, v252, v6
v_mfma_f32_16x16x32_bf16 v[94:97], a[4:7], v[130:133], 0
global_store_dwordx4 v3, v[162:165], s[38:39] sc1
v_mfma_f32_16x16x32_bf16 v[94:97], a[12:15], v[134:137], v[94:97]
v_add_u32 v3, 147456, v3
v_mfma_f32_16x16x32_bf16 v[94:97], a[20:23], v[138:141], v[94:97]
ds_read_b128 v[118:121], v8 offset:27648
v_mfma_f32_16x16x32_bf16 v[94:97], a[28:31], v[142:145], v[94:97]
v_mfma_f32_16x16x32_bf16 v[94:97], a[36:39], v[146:149], v[94:97]
v_mfma_f32_16x16x32_bf16 v[94:97], a[44:47], v[150:153], v[94:97]
ds_read_b128 v[122:125], v8 offset:28672
v_mfma_f32_16x16x32_bf16 v[102:105], a[148:151], v[130:133], 0
v_mfma_f32_16x16x32_bf16 v[102:105], a[156:159], v[134:137], v[102:105]
v_mfma_f32_16x16x32_bf16 v[102:105], a[164:167], v[138:141], v[102:105]
ds_read_b128 v[126:129], v8 offset:29696
v_mfma_f32_16x16x32_bf16 v[102:105], a[172:175], v[142:145], v[102:105]
v_mfma_f32_16x16x32_bf16 v[102:105], a[180:183], v[146:149], v[102:105]
v_mfma_f32_16x16x32_bf16 v[102:105], a[188:191], v[150:153], v[102:105]
s_waitcnt lgkmcnt(5)
v_mfma_f32_16x16x32_bf16 v[90:93], a[48:51], v[106:109], v[90:93]
s_waitcnt lgkmcnt(4)
v_mfma_f32_16x16x32_bf16 v[90:93], a[56:59], v[110:113], v[90:93]
s_waitcnt lgkmcnt(3)
v_mfma_f32_16x16x32_bf16 v[90:93], a[64:67], v[114:117], v[90:93]
s_waitcnt lgkmcnt(2)
v_mfma_f32_16x16x32_bf16 v[90:93], a[72:75], v[118:121], v[90:93]
s_waitcnt lgkmcnt(1)
v_mfma_f32_16x16x32_bf16 v[90:93], a[80:83], v[122:125], v[90:93]
s_waitcnt lgkmcnt(0)
v_mfma_f32_16x16x32_bf16 v[90:93], a[88:91], v[126:129], v[90:93]
ds_read_b128 v[130:133], v8 offset:30720
v_mfma_f32_16x16x32_bf16 v[98:101], a[192:195], v[106:109], v[98:101]
v_mfma_f32_16x16x32_bf16 v[98:101], a[200:203], v[110:113], v[98:101]
v_mfma_f32_16x16x32_bf16 v[98:101], a[208:211], v[114:117], v[98:101]
ds_read_b128 v[134:137], v8 offset:31744
v_mfma_f32_16x16x32_bf16 v[98:101], a[216:219], v[118:121], v[98:101]
v_mfma_f32_16x16x32_bf16 v[98:101], a[224:227], v[122:125], v[98:101]
v_mfma_f32_16x16x32_bf16 v[98:101], a[232:235], v[126:129], v[98:101]
ds_read_b128 v[138:141], v8 offset:32768
v_mfma_f32_16x16x32_bf16 v[94:97], a[52:55], v[106:109], v[94:97]
v_mfma_f32_16x16x32_bf16 v[94:97], a[60:63], v[110:113], v[94:97]
v_mfma_f32_16x16x32_bf16 v[94:97], a[68:71], v[114:117], v[94:97]
ds_read_b128 v[142:145], v8 offset:33792
v_mfma_f32_16x16x32_bf16 v[94:97], a[76:79], v[118:121], v[94:97]
v_mfma_f32_16x16x32_bf16 v[94:97], a[84:87], v[122:125], v[94:97]
v_mfma_f32_16x16x32_bf16 v[94:97], a[92:95], v[126:129], v[94:97]
ds_read_b128 v[146:149], v8 offset:34816
v_mfma_f32_16x16x32_bf16 v[102:105], a[196:199], v[106:109], v[102:105]
.Lgu_in8:
v_mfma_f32_16x16x32_bf16 v[102:105], a[204:207], v[110:113], v[102:105]
v_mfma_f32_16x16x32_bf16 v[102:105], a[212:215], v[114:117], v[102:105]
ds_read_b128 v[150:153], v8 offset:35840
v_mfma_f32_16x16x32_bf16 v[102:105], a[220:223], v[118:121], v[102:105]
v_mfma_f32_16x16x32_bf16 v[102:105], a[228:231], v[122:125], v[102:105]
s_add_u32 m0, s45, 18432
v_mfma_f32_16x16x32_bf16 v[102:105], a[236:239], v[126:129], v[102:105]
s_waitcnt lgkmcnt(5)
v_mfma_f32_16x16x32_bf16 v[90:93], a[96:99], v[130:133], v[90:93]
s_barrier
s_waitcnt lgkmcnt(4)
v_mfma_f32_16x16x32_bf16 v[90:93], a[104:107], v[134:137], v[90:93]
s_waitcnt lgkmcnt(3)
v_mfma_f32_16x16x32_bf16 v[90:93], a[112:115], v[138:141], v[90:93]
s_waitcnt lgkmcnt(2)
global_load_lds_dwordx4 v2, s[38:39] offset:-2048 sc1
v_mfma_f32_16x16x32_bf16 v[90:93], a[120:123], v[142:145], v[90:93]
s_waitcnt lgkmcnt(1)
v_mfma_f32_16x16x32_bf16 v[90:93], a[128:131], v[146:149], v[90:93]
s_waitcnt lgkmcnt(0)
v_mfma_f32_16x16x32_bf16 v[90:93], a[136:139], v[150:153], v[90:93]
global_load_lds_dwordx4 v2, s[38:39] offset:-1024 sc1
v_mfma_f32_16x16x32_bf16 v[98:101], v[26:29], v[130:133], v[98:101]
v_mfma_f32_16x16x32_bf16 v[98:101], v[34:37], v[134:137], v[98:101]
v_mfma_f32_16x16x32_bf16 v[98:101], v[42:45], v[138:141], v[98:101]
global_load_lds_dwordx4 v2, s[38:39] offset:0 sc1
v_mfma_f32_16x16x32_bf16 v[98:101], v[50:53], v[142:145], v[98:101]
v_xor_b32 v8, 0x10000, v8
v_mfma_f32_16x16x32_bf16 v[98:101], v[58:61], v[146:149], v[98:101]
v_mfma_f32_16x16x32_bf16 v[98:101], v[66:69], v[150:153], v[98:101]
v_mfma_f32_16x16x32_bf16 v[94:97], a[100:103], v[130:133], v[94:97]
s_waitcnt vmcnt(5)
s_barrier
v_mfma_f32_16x16x32_bf16 v[94:97], a[108:111], v[134:137], v[94:97]
global_load_lds_dwordx4 v2, s[38:39] offset:1024 sc1
v_mfma_f32_16x16x32_bf16 v[94:97], a[116:119], v[138:141], v[94:97]
v_mfma_f32_16x16x32_bf16 v[94:97], a[124:127], v[142:145], v[94:97]
ds_read_b128 v[106:109], v8 offset:0
v_mfma_f32_16x16x32_bf16 v[94:97], a[132:135], v[146:149], v[94:97]
ds_read_b128 v[110:113], v8 offset:1024
global_load_lds_dwordx4 v2, s[38:39] offset:2048 sc1
v_mfma_f32_16x16x32_bf16 v[94:97], a[140:143], v[150:153], v[94:97]
ds_read_b128 v[114:117], v8 offset:2048
v_mul_f32 v154, v90, v98
v_mfma_f32_16x16x32_bf16 v[102:105], v[30:33], v[130:133], v[102:105]
ds_read_b128 v[118:121], v8 offset:3072
v_mul_f32 v155, v91, v99
v_mfma_f32_16x16x32_bf16 v[102:105], v[38:41], v[134:137], v[102:105]
ds_read_b128 v[122:125], v8 offset:4096
global_load_lds_dwordx4 v2, s[38:39] offset:3072 sc1
v_mfma_f32_16x16x32_bf16 v[102:105], v[46:49], v[138:141], v[102:105]
ds_read_b128 v[126:129], v8 offset:5120
v_add_u32 v2, 18688, v2
v_mfma_f32_16x16x32_bf16 v[102:105], v[54:57], v[142:145], v[102:105]
v_min_u32 v2, v2, v9
v_mul_f32 v156, v92, v100
v_mfma_f32_16x16x32_bf16 v[102:105], v[62:65], v[146:149], v[102:105]
v_mfma_f32_16x16x32_bf16 v[102:105], v[70:73], v[150:153], v[102:105]
s_sub_u32 s70, s70, 1
s_cbranch_scc0 .Lgu_top9
s_waitcnt vmcnt(6) lgkmcnt(0)
s_barrier
s_mov_b64 exec, 1
global_store_dword v252, v7, s[38:39] sc1
s_mov_b64 exec, -1
v_mul_f32 v154, v90, v98
v_mul_f32 v155, v91, v99
v_mul_f32 v156, v92, v100
v_mul_f32 v157, v93, v101
v_cvt_pk_bf16_f32 v162, v154, v155
v_cvt_pk_bf16_f32 v163, v156, v157
v_mul_f32 v158, v94, v102
v_mul_f32 v159, v95, v103
v_mul_f32 v160, v96, v104
v_mul_f32 v161, v97, v105
v_cvt_pk_bf16_f32 v164, v158, v159
v_cvt_pk_bf16_f32 v165, v160, v161
global_store_dwordx4 v3, v[162:165], s[38:39] sc1
v_mov_b32 v231, 192
ds_read_b32 v231, v231 offset:36864
s_waitcnt vmcnt(0)
s_barrier
v_add_u32 v7, 8, v7
v_mbcnt_lo_u32_b32 v252, -1, 0
v_mbcnt_hi_u32_b32 v252, -1, v252
v_add_lshl_u32 v252, v252, 0, 7
v_bfi_b32 v252, s69, v252, v6
s_mov_b64 exec, 0xff
global_store_dword v252, v7, s[38:39] sc1
s_mov_b64 exec, -1
s_waitcnt lgkmcnt(0)
v_readfirstlane_b32 s62, v231
s_nop 3
s_max_i32 s62, s62, 2
s_min_i32 s62, s62, 66
s_add_u32 s62, s62, -1
s_branch .Lgu_fin
.Lgu_w3:
v_add_u32 v254, -1, v5
v_add_u32 v255, -1, v5
v_add_lshl_u32 v252, v7, 0, 7
v_bfi_b32 v252, s69, v252, v6
v_add_lshl_u32 v253, v5, 0, 7
v_bfi_b32 v253, s69, v253, v4
s_add_u32 m0, s45, 18432
v_add_lshl_u32 v253, v5, 0, 7
v_bfi_b32 v253, s69, v253, v4
global_load_dword v254, v253, s[38:39] sc1
s_waitcnt vmcnt(0)
v_sub_u32 v254, v254, v5
v_cmp_lt_u32 vcc, 0x7f, v254
s_cbranch_vccnz .Lgu_sp16
.Lgu_rt17:
v_add_u32 v5, 1, v5
s_xor_b32 s45, s45, 0x10000
s_mov_b32 m0, s45
s_barrier
global_load_lds_dwordx4 v2, s[38:39] offset:-2048 sc1
global_load_lds_dwordx4 v2, s[38:39] offset:-1024 sc1
global_load_lds_dwordx4 v2, s[38:39] offset:0 sc1
global_load_lds_dwordx4 v2, s[38:39] offset:1024 sc1
global_load_lds_dwordx4 v2, s[38:39] offset:2048 sc1
global_load_lds_dwordx4 v2, s[38:39] offset:3072 sc1
v_add_u32 v2, 18688, v2
s_mov_b32 s70, 32
s_waitcnt vmcnt(0) lgkmcnt(0)
s_nop 7
s_branch .Lgu_in15
.p2align 6
.Lgu_top18:
s_waitcnt lgkmcnt(5)
v_mfma_f32_16x16x32_bf16 v[74:77], a[0:3], v[106:109], 0
s_waitcnt lgkmcnt(4)
v_mul_f32 v157, v93, v101
v_cvt_pk_bf16_f32 v162, v154, v155
v_mfma_f32_16x16x32_bf16 v[74:77], a[8:11], v[110:113], v[74:77]
s_waitcnt lgkmcnt(3)
v_cvt_pk_bf16_f32 v163, v156, v157
v_mfma_f32_16x16x32_bf16 v[74:77], a[16:19], v[114:117], v[74:77]
s_waitcnt lgkmcnt(2)
global_load_dword v254, v253, s[38:39] sc1
v_mfma_f32_16x16x32_bf16 v[74:77], a[24:27], v[118:121], v[74:77]
s_waitcnt lgkmcnt(1)
v_mul_f32 v158, v94, v102
v_mul_f32 v159, v95, v103
v_mfma_f32_16x16x32_bf16 v[74:77], a[32:35], v[122:125], v[74:77]
s_waitcnt lgkmcnt(0)
v_mul_f32 v160, v96, v104
v_mul_f32 v161, v97, v105
v_mfma_f32_16x16x32_bf16 v[74:77], a[40:43], v[126:129], v[74:77]
ds_read_b128 v[130:133], v8 offset:6144
v_cvt_pk_bf16_f32 v164, v158, v159
v_mfma_f32_16x16x32_bf16 v[82:85], a[144:147], v[106:109], 0
ds_read_b128 v[134:137], v8 offset:7168
v_cvt_pk_bf16_f32 v165, v160, v161
v_mfma_f32_16x16x32_bf16 v[82:85], a[152:155], v[110:113], v[82:85]
v_mfma_f32_16x16x32_bf16 v[82:85], a[160:163], v[114:117], v[82:85]
s_waitcnt vmcnt(7)
s_barrier
v_mfma_f32_16x16x32_bf16 v[82:85], a[168:171], v[118:121], v[82:85]
v_mfma_f32_16x16x32_bf16 v[82:85], a[176:179], v[122:125], v[82:85]
v_mfma_f32_16x16x32_bf16 v[82:85], a[184:187], v[126:129], v[82:85]
ds_read_b128 v[138:141], v8 offset:8192
v_mfma_f32_16x16x32_bf16 v[78:81], a[4:7], v[106:109], 0
global_store_dwordx4 v3, v[162:165], s[38:39] sc1
v_mfma_f32_16x16x32_bf16 v[78:81], a[12:15], v[110:113], v[78:81]
v_add_u32 v3, 147456, v3
v_mfma_f32_16x16x32_bf16 v[78:81], a[20:23], v[114:117], v[78:81]
ds_read_b128 v[142:145], v8 offset:9216
v_mfma_f32_16x16x32_bf16 v[78:81], a[28:31], v[118:121], v[78:81]
v_mfma_f32_16x16x32_bf16 v[78:81], a[36:39], v[122:125], v[78:81]
v_mfma_f32_16x16x32_bf16 v[78:81], a[44:47], v[126:129], v[78:81]
ds_read_b128 v[146:149], v8 offset:10240
v_mfma_f32_16x16x32_bf16 v[86:89], a[148:151], v[106:109], 0
v_mfma_f32_16x16x32_bf16 v[86:89], a[156:159], v[110:113], v[86:89]
v_mfma_f32_16x16x32_bf16 v[86:89], a[164:167], v[114:117], v[86:89]
ds_read_b128 v[150:153], v8 offset:11264
v_mfma_f32_16x16x32_bf16 v[86:89], a[172:175], v[118:121], v[86:89]
v_mfma_f32_16x16x32_bf16 v[86:89], a[180:183], v[122:125], v[86:89]
v_mfma_f32_16x16x32_bf16 v[86:89], a[188:191], v[126:129], v[86:89]
s_waitcnt lgkmcnt(5)
v_mfma_f32_16x16x32_bf16 v[74:77], a[48:51], v[130:133], v[74:77]
s_waitcnt lgkmcnt(4)
v_mfma_f32_16x16x32_bf16 v[74:77], a[56:59], v[134:137], v[74:77]
s_waitcnt lgkmcnt(3)
v_mfma_f32_16x16x32_bf16 v[74:77], a[64:67], v[138:141], v[74:77]
s_waitcnt lgkmcnt(2)
v_mfma_f32_16x16x32_bf16 v[74:77], a[72:75], v[142:145], v[74:77]
s_waitcnt lgkmcnt(1)
v_mfma_f32_16x16x32_bf16 v[74:77], a[80:83], v[146:149], v[74:77]
s_waitcnt lgkmcnt(0)
v_mfma_f32_16x16x32_bf16 v[74:77], a[88:91], v[150:153], v[74:77]
ds_read_b128 v[106:109], v8 offset:12288
v_mfma_f32_16x16x32_bf16 v[82:85], a[192:195], v[130:133], v[82:85]
v_mfma_f32_16x16x32_bf16 v[82:85], a[200:203], v[134:137], v[82:85]
v_mfma_f32_16x16x32_bf16 v[82:85], a[208:211], v[138:141], v[82:85]
ds_read_b128 v[110:113], v8 offset:13312
v_mfma_f32_16x16x32_bf16 v[82:85], a[216:219], v[142:145], v[82:85]
v_mfma_f32_16x16x32_bf16 v[82:85], a[224:227], v[146:149], v[82:85]
v_mfma_f32_16x16x32_bf16 v[82:85], a[232:235], v[150:153], v[82:85]
ds_read_b128 v[114:117], v8 offset:14336
v_mfma_f32_16x16x32_bf16 v[78:81], a[52:55], v[130:133], v[78:81]
v_mfma_f32_16x16x32_bf16 v[78:81], a[60:63], v[134:137], v[78:81]
v_mfma_f32_16x16x32_bf16 v[78:81], a[68:71], v[138:141], v[78:81]
ds_read_b128 v[118:121], v8 offset:15360
v_mfma_f32_16x16x32_bf16 v[78:81], a[76:79], v[142:145], v[78:81]
v_mfma_f32_16x16x32_bf16 v[78:81], a[84:87], v[146:149], v[78:81]
v_mfma_f32_16x16x32_bf16 v[78:81], a[92:95], v[150:153], v[78:81]
ds_read_b128 v[122:125], v8 offset:16384
v_mfma_f32_16x16x32_bf16 v[86:89], a[196:199], v[130:133], v[86:89]
v_mfma_f32_16x16x32_bf16 v[86:89], a[204:207], v[134:137], v[86:89]
s_waitcnt vmcnt(1)
v_sub_u32 v254, v254, v5
v_cmp_lt_u32 vcc, 0x7f, v254
v_mfma_f32_16x16x32_bf16 v[86:89], a[212:215], v[138:141], v[86:89]
ds_read_b128 v[126:129], v8 offset:17408
v_mfma_f32_16x16x32_bf16 v[86:89], a[220:223], v[142:145], v[86:89]
s_xor_b32 s45, s45, 0x10000
v_mfma_f32_16x16x32_bf16 v[86:89], a[228:231], v[146:149], v[86:89]
s_cbranch_vccnz .Lgu_sp11
.Lgu_rt12:
s_add_u32 m0, s45, 0
v_mfma_f32_16x16x32_bf16 v[86:89], a[236:239], v[150:153], v[86:89]
s_waitcnt lgkmcnt(5)
v_mfma_f32_16x16x32_bf16 v[74:77], a[96:99], v[106:109], v[74:77]
s_barrier
s_waitcnt lgkmcnt(4)
v_mfma_f32_16x16x32_bf16 v[74:77], a[104:107], v[110:113], v[74:77]
s_waitcnt lgkmcnt(3)
v_add_u32 v5, 1, v5
v_add_lshl_u32 v253, v5, 0, 7
v_mfma_f32_16x16x32_bf16 v[74:77], a[112:115], v[114:117], v[74:77]
s_waitcnt lgkmcnt(2)
v_bfi_b32 v253, s69, v253, v4
v_mfma_f32_16x16x32_bf16 v[74:77], a[120:123], v[118:121], v[74:77]
s_waitcnt lgkmcnt(1)
global_load_lds_dwordx4 v2, s[38:39] offset:-2048 sc1
v_mfma_f32_16x16x32_bf16 v[74:77], a[128:131], v[122:125], v[74:77]
s_waitcnt lgkmcnt(0)
v_mfma_f32_16x16x32_bf16 v[74:77], a[136:139], v[126:129], v[74:77]
v_mfma_f32_16x16x32_bf16 v[82:85], v[26:29], v[106:109], v[82:85]
global_load_lds_dwordx4 v2, s[38:39] offset:-1024 sc1
v_mfma_f32_16x16x32_bf16 v[82:85], v[34:37], v[110:113], v[82:85]
v_mfma_f32_16x16x32_bf16 v[82:85], v[42:45], v[114:117], v[82:85]
v_mfma_f32_16x16x32_bf16 v[82:85], v[50:53], v[118:121], v[82:85]
global_load_lds_dwordx4 v2, s[38:39] offset:0 sc1
v_mfma_f32_16x16x32_bf16 v[82:85], v[58:61], v[122:125], v[82:85]
v_mfma_f32_16x16x32_bf16 v[82:85], v[66:69], v[126:129], v[82:85]
v_mfma_f32_16x16x32_bf16 v[78:81], a[100:103], v[106:109], v[78:81]
s_barrier
v_mfma_f32_16x16x32_bf16 v[78:81], a[108:111], v[110:113], v[78:81]
global_load_lds_dwordx4 v2, s[38:39] offset:1024 sc1
v_mfma_f32_16x16x32_bf16 v[78:81], a[116:119], v[114:117], v[78:81]
v_mfma_f32_16x16x32_bf16 v[78:81], a[124:127], v[118:121], v[78:81]
ds_read_b128 v[130:133], v8 offset:18432
v_mfma_f32_16x16x32_bf16 v[78:81], a[132:135], v[122:125], v[78:81]
ds_read_b128 v[134:137], v8 offset:19456
global_load_lds_dwordx4 v2, s[38:39] offset:2048 sc1
v_mfma_f32_16x16x32_bf16 v[78:81], a[140:143], v[126:129], v[78:81]
ds_read_b128 v[138:141], v8 offset:20480
v_mul_f32 v154, v74, v82
v_mfma_f32_16x16x32_bf16 v[86:89], v[30:33], v[106:109], v[86:89]
ds_read_b128 v[142:145], v8 offset:21504
v_mul_f32 v155, v75, v83
v_mfma_f32_16x16x32_bf16 v[86:89], v[38:41], v[110:113], v[86:89]
ds_read_b128 v[146:149], v8 offset:22528
global_load_lds_dwordx4 v2, s[38:39] offset:3072 sc1
v_mfma_f32_16x16x32_bf16 v[86:89], v[46:49], v[114:117], v[86:89]
ds_read_b128 v[150:153], v8 offset:23552
v_add_u32 v2, 18688, v2
v_mfma_f32_16x16x32_bf16 v[86:89], v[54:57], v[118:121], v[86:89]
v_min_u32 v2, v2, v9
v_mul_f32 v156, v76, v84
v_mfma_f32_16x16x32_bf16 v[86:89], v[62:65], v[122:125], v[86:89]
v_mul_f32 v157, v77, v85
v_cvt_pk_bf16_f32 v162, v154, v155
v_mfma_f32_16x16x32_bf16 v[86:89], v[70:73], v[126:129], v[86:89]
s_waitcnt lgkmcnt(5)
v_cvt_pk_bf16_f32 v163, v156, v157
v_mfma_f32_16x16x32_bf16 v[90:93], a[0:3], v[130:133], 0
s_waitcnt lgkmcnt(4)
v_mfma_f32_16x16x32_bf16 v[90:93], a[8:11], v[134:137], v[90:93]
s_waitcnt lgkmcnt(3)
v_mfma_f32_16x16x32_bf16 v[90:93], a[16:19], v[138:141], v[90:93]
s_waitcnt lgkmcnt(2)
global_load_dword v255, v253, s[38:39] sc1
v_mfma_f32_16x16x32_bf16 v[90:93], a[24:27], v[142:145], v[90:93]
s_waitcnt lgkmcnt(1)
v_mul_f32 v158, v78, v86
v_mul_f32 v159, v79, v87
v_mfma_f32_16x16x32_bf16 v[90:93], a[32:35], v[146:149], v[90:93]
s_waitcnt lgkmcnt(0)
v_mul_f32 v160, v80, v88
v_mul_f32 v161, v81, v89
v_mfma_f32_16x16x32_bf16 v[90:93], a[40:43], v[150:153], v[90:93]
ds_read_b128 v[106:109], v8 offset:24576
v_cvt_pk_bf16_f32 v164, v158, v159
v_mfma_f32_16x16x32_bf16 v[98:101], a[144:147], v[130:133], 0
ds_read_b128 v[110:113], v8 offset:25600
v_cvt_pk_bf16_f32 v165, v160, v161
v_mfma_f32_16x16x32_bf16 v[98:101], a[152:155], v[134:137], v[98:101]
v_mfma_f32_16x16x32_bf16 v[98:101], a[160:163], v[138:141], v[98:101]
s_waitcnt vmcnt(7)
s_barrier
v_mfma_f32_16x16x32_bf16 v[98:101], a[168:171], v[142:145], v[98:101]
v_mfma_f32_16x16x32_bf16 v[98:101], a[176:179], v[146:149], v[98:101]
v_mfma_f32_16x16x32_bf16 v[98:101], a[184:187], v[150:153], v[98:101]
ds_read_b128 v[114:117], v8 offset:26624
v_mfma_f32_16x16x32_bf16 v[94:97], a[4:7], v[130:133], 0
global_store_dwordx4 v3, v[162:165], s[38:39] sc1
v_mfma_f32_16x16x32_bf16 v[94:97], a[12:15], v[134:137], v[94:97]
v_add_u32 v3, 147456, v3
v_mfma_f32_16x16x32_bf16 v[94:97], a[20:23], v[138:141], v[94:97]
ds_read_b128 v[118:121], v8 offset:27648
v_mfma_f32_16x16x32_bf16 v[94:97], a[28:31], v[142:145], v[94:97]
)"
R"(v_mfma_f32_16x16x32_bf16 v[94:97], a[36:39], v[146:149], v[94:97]
v_mfma_f32_16x16x32_bf16 v[94:97], a[44:47], v[150:153], v[94:97]
ds_read_b128 v[122:125], v8 offset:28672
v_mfma_f32_16x16x32_bf16 v[102:105], a[148:151], v[130:133], 0
v_mfma_f32_16x16x32_bf16 v[102:105], a[156:159], v[134:137], v[102:105]
v_mfma_f32_16x16x32_bf16 v[102:105], a[164:167], v[138:141], v[102:105]
ds_read_b128 v[126:129], v8 offset:29696
v_mfma_f32_16x16x32_bf16 v[102:105], a[172:175], v[142:145], v[102:105]
v_mfma_f32_16x16x32_bf16 v[102:105], a[180:183], v[146:149], v[102:105]
v_mfma_f32_16x16x32_bf16 v[102:105], a[188:191], v[150:153], v[102:105]
s_waitcnt lgkmcnt(5)
v_mfma_f32_16x16x32_bf16 v[90:93], a[48:51], v[106:109], v[90:93]
s_waitcnt lgkmcnt(4)
v_mfma_f32_16x16x32_bf16 v[90:93], a[56:59], v[110:113], v[90:93]
s_waitcnt lgkmcnt(3)
v_mfma_f32_16x16x32_bf16 v[90:93], a[64:67], v[114:117], v[90:93]
s_waitcnt lgkmcnt(2)
v_mfma_f32_16x16x32_bf16 v[90:93], a[72:75], v[118:121], v[90:93]
s_waitcnt lgkmcnt(1)
v_mfma_f32_16x16x32_bf16 v[90:93], a[80:83], v[122:125], v[90:93]
s_waitcnt lgkmcnt(0)
v_mfma_f32_16x16x32_bf16 v[90:93], a[88:91], v[126:129], v[90:93]
ds_read_b128 v[130:133], v8 offset:30720
v_mfma_f32_16x16x32_bf16 v[98:101], a[192:195], v[106:109], v[98:101]
v_mfma_f32_16x16x32_bf16 v[98:101], a[200:203], v[110:113], v[98:101]
v_mfma_f32_16x16x32_bf16 v[98:101], a[208:211], v[114:117], v[98:101]
ds_read_b128 v[134:137], v8 offset:31744
v_mfma_f32_16x16x32_bf16 v[98:101], a[216:219], v[118:121], v[98:101]
v_mfma_f32_16x16x32_bf16 v[98:101], a[224:227], v[122:125], v[98:101]
v_mfma_f32_16x16x32_bf16 v[98:101], a[232:235], v[126:129], v[98:101]
ds_read_b128 v[138:141], v8 offset:32768
v_mfma_f32_16x16x32_bf16 v[94:97], a[52:55], v[106:109], v[94:97]
v_mfma_f32_16x16x32_bf16 v[94:97], a[60:63], v[110:113], v[94:97]
v_mfma_f32_16x16x32_bf16 v[94:97], a[68:71], v[114:117], v[94:97]
ds_read_b128 v[142:145], v8 offset:33792
v_mfma_f32_16x16x32_bf16 v[94:97], a[76:79], v[118:121], v[94:97]
v_mfma_f32_16x16x32_bf16 v[94:97], a[84:87], v[122:125], v[94:97]
v_mfma_f32_16x16x32_bf16 v[94:97], a[92:95], v[126:129], v[94:97]
ds_read_b128 v[146:149], v8 offset:34816
v_mfma_f32_16x16x32_bf16 v[102:105], a[196:199], v[106:109], v[102:105]
.Lgu_in15:
v_mfma_f32_16x16x32_bf16 v[102:105], a[204:207], v[110:113], v[102:105]
s_waitcnt vmcnt(1)
v_sub_u32 v255, v255, v5
v_cmp_lt_u32 vcc, 0x7f, v255
v_mfma_f32_16x16x32_bf16 v[102:105], a[212:215], v[114:117], v[102:105]
ds_read_b128 v[150:153], v8 offset:35840
v_mfma_f32_16x16x32_bf16 v[102:105], a[220:223], v[118:121], v[102:105]
v_mfma_f32_16x16x32_bf16 v[102:105], a[228:231], v[122:125], v[102:105]
s_cbranch_vccnz .Lgu_sp13
.Lgu_rt14:
s_add_u32 m0, s45, 18432
v_mfma_f32_16x16x32_bf16 v[102:105], a[236:239], v[126:129], v[102:105]
s_waitcnt lgkmcnt(5)
v_mfma_f32_16x16x32_bf16 v[90:93], a[96:99], v[130:133], v[90:93]
s_barrier
s_waitcnt lgkmcnt(4)
v_mfma_f32_16x16x32_bf16 v[90:93], a[104:107], v[134:137], v[90:93]
s_waitcnt lgkmcnt(3)
v_add_u32 v5, 1, v5
v_add_lshl_u32 v253, v5, 0, 7
v_mfma_f32_16x16x32_bf16 v[90:93], a[112:115], v[138:141], v[90:93]
s_waitcnt lgkmcnt(2)
v_bfi_b32 v253, s69, v253, v4
v_mfma_f32_16x16x32_bf16 v[90:93], a[120:123], v[142:145], v[90:93]
s_waitcnt lgkmcnt(1)
global_load_lds_dwordx4 v2, s[38:39] offset:-2048 sc1
v_mfma_f32_16x16x32_bf16 v[90:93], a[128:131], v[146:149], v[90:93]
s_waitcnt lgkmcnt(0)
v_mfma_f32_16x16x32_bf16 v[90:93], a[136:139], v[150:153], v[90:93]
v_mfma_f32_16x16x32_bf16 v[98:101], v[26:29], v[130:133], v[98:101]
global_load_lds_dwordx4 v2, s[38:39] offset:-1024 sc1
v_mfma_f32_16x16x32_bf16 v[98:101], v[34:37], v[134:137], v[98:101]
v_mfma_f32_16x16x32_bf16 v[98:101], v[42:45], v[138:141], v[98:101]
v_mfma_f32_16x16x32_bf16 v[98:101], v[50:53], v[142:145], v[98:101]
v_xor_b32 v8, 0x10000, v8
global_load_lds_dwordx4 v2, s[38:39] offset:0 sc1
v_mfma_f32_16x16x32_bf16 v[98:101], v[58:61], v[146:149], v[98:101]
v_mfma_f32_16x16x32_bf16 v[98:101], v[66:69], v[150:153], v[98:101]
v_mfma_f32_16x16x32_bf16 v[94:97], a[100:103], v[130:133], v[94:97]
s_barrier
v_mfma_f32_16x16x32_bf16 v[94:97], a[108:111], v[134:137], v[94:97]
global_load_lds_dwordx4 v2, s[38:39] offset:1024 sc1
v_mfma_f32_16x16x32_bf16 v[94:97], a[116:119], v[138:141], v[94:97]
v_mfma_f32_16x16x32_bf16 v[94:97], a[124:127], v[142:145], v[94:97]
ds_read_b128 v[106:109], v8 offset:0
v_mfma_f32_16x16x32_bf16 v[94:97], a[132:135], v[146:149], v[94:97]
ds_read_b128 v[110:113], v8 offset:1024
global_load_lds_dwordx4 v2, s[38:39] offset:2048 sc1
v_mfma_f32_16x16x32_bf16 v[94:97], a[140:143], v[150:153], v[94:97]
ds_read_b128 v[114:117], v8 offset:2048
v_mul_f32 v154, v90, v98
v_mfma_f32_16x16x32_bf16 v[102:105], v[30:33], v[130:133], v[102:105]
ds_read_b128 v[118:121], v8 offset:3072
v_mul_f32 v155, v91, v99
v_mfma_f32_16x16x32_bf16 v[102:105], v[38:41], v[134:137], v[102:105]
ds_read_b128 v[122:125], v8 offset:4096
global_load_lds_dwordx4 v2, s[38:39] offset:3072 sc1
v_mfma_f32_16x16x32_bf16 v[102:105], v[46:49], v[138:141], v[102:105]
ds_read_b128 v[126:129], v8 offset:5120
v_add_u32 v2, 18688, v2
v_mfma_f32_16x16x32_bf16 v[102:105], v[54:57], v[142:145], v[102:105]
v_min_u32 v2, v2, v9
v_mul_f32 v156, v92, v100
v_mfma_f32_16x16x32_bf16 v[102:105], v[62:65], v[146:149], v[102:105]
v_mfma_f32_16x16x32_bf16 v[102:105], v[70:73], v[150:153], v[102:105]
s_sub_u32 s70, s70, 1
s_cbranch_scc0 .Lgu_top18
s_waitcnt vmcnt(6) lgkmcnt(0)
s_barrier
v_mul_f32 v154, v90, v98
v_mul_f32 v155, v91, v99
v_mul_f32 v156, v92, v100
v_mul_f32 v157, v93, v101
v_cvt_pk_bf16_f32 v162, v154, v155
v_cvt_pk_bf16_f32 v163, v156, v157
v_mul_f32 v158, v94, v102
v_mul_f32 v159, v95, v103
v_mul_f32 v160, v96, v104
v_mul_f32 v161, v97, v105
v_cvt_pk_bf16_f32 v164, v158, v159
v_cvt_pk_bf16_f32 v165, v160, v161
global_store_dwordx4 v3, v[162:165], s[38:39] sc1
v_mov_b32 v231, 192
ds_read_b32 v231, v231 offset:36864
s_waitcnt vmcnt(0)
s_barrier
s_waitcnt lgkmcnt(0)
v_readfirstlane_b32 s62, v231
s_nop 3
s_max_i32 s62, s62, 2
s_min_i32 s62, s62, 66
s_add_u32 s62, s62, 0
s_branch .Lgu_fin
.Lgu_w4:
s_add_u32 s84, s54, 18688
s_addc_u32 s85, s55, 0
s_add_u32 s86, s52, 110592
s_addc_u32 s87, s53, 0
s_mov_b32 s80, 0
s_mov_b64 s[76:77], 0
s_mov_b32 s72, -14
v_add_u32 v230, -1, v17
v_add_u32 v226, -1, s37
v_add_u32 v227, -1, s37
v_add_u32 v228, -1, s37
v_add_u32 v229, -1, s37
v_add_lshl_u32 v252, v228, 0, 7
v_bfi_b32 v252, s69, v252, v18
v_add_lshl_u32 v253, v17, 0, 7
v_bfi_b32 v253, s69, v253, v16
v_add_u32 v254, -1, v5
v_add_u32 v255, -1, v5
s_add_u32 m0, s45, 18432
s_xor_b32 s45, s45, 0x10000
s_mov_b32 m0, s45
s_barrier
v_add_u32 v2, 18688, v2
s_mov_b32 s70, 32
s_waitcnt vmcnt(0) lgkmcnt(0)
s_nop 7
s_branch .Lgu_in24
.p2align 6
.Lgu_top25:
s_waitcnt lgkmcnt(5)
v_mfma_f32_16x16x32_bf16 v[74:77], a[0:3], v[106:109], 0
s_waitcnt lgkmcnt(4)
global_load_dword v230, v253, s[38:39] sc1
v_lshlrev_b32 v216, 16, v187
v_mfma_f32_16x16x32_bf16 v[74:77], a[8:11], v[110:113], v[74:77]
s_waitcnt lgkmcnt(3)
v_and_b32 v217, 0xffff0000, v187
v_add_f32_dpp v206, v210, v206 row_ror:8 row_mask:0xf bank_mask:0xf
v_mfma_f32_16x16x32_bf16 v[74:77], a[16:19], v[114:117], v[74:77]
s_waitcnt lgkmcnt(2)
v_add_f32_dpp v207, v211, v207 row_ror:8 row_mask:0xf bank_mask:0xf
v_add_f32_dpp v208, v212, v208 row_ror:8 row_mask:0xf bank_mask:0xf
v_mfma_f32_16x16x32_bf16 v[74:77], a[24:27], v[118:121], v[74:77]
s_waitcnt lgkmcnt(1)
v_mul_f32 v158, v94, v102
v_mul_f32 v159, v95, v103
v_mfma_f32_16x16x32_bf16 v[74:77], a[32:35], v[122:125], v[74:77]
s_waitcnt lgkmcnt(0)
v_mul_f32 v160, v96, v104
v_mul_f32 v161, v97, v105
v_mfma_f32_16x16x32_bf16 v[74:77], a[40:43], v[126:129], v[74:77]
ds_read_b128 v[130:133], v8 offset:6144
v_cvt_pk_bf16_f32 v164, v158, v159
v_mfma_f32_16x16x32_bf16 v[82:85], a[144:147], v[106:109], 0
ds_read_b128 v[134:137], v8 offset:7168
v_cvt_pk_bf16_f32 v165, v160, v161
v_mfma_f32_16x16x32_bf16 v[82:85], a[152:155], v[110:113], v[82:85]
v_mfma_f32_16x16x32_bf16 v[82:85], a[160:163], v[114:117], v[82:85]
s_waitcnt vmcnt(8)
s_barrier
v_mfma_f32_16x16x32_bf16 v[82:85], a[168:171], v[118:121], v[82:85]
v_add_f32_dpp v209, v213, v209 row_ror:8 row_mask:0xf bank_mask:0xf
v_fma_f32 v206, v206, v231, v214
v_mfma_f32_16x16x32_bf16 v[82:85], a[176:179], v[122:125], v[82:85]
v_fma_f32 v207, v207, v231, v215
v_fma_f32 v208, v208, v231, v216
v_mfma_f32_16x16x32_bf16 v[82:85], a[184:187], v[126:129], v[82:85]
ds_read_b128 v[138:141], v8 offset:8192
v_fma_f32 v209, v209, v231, v217
v_mfma_f32_16x16x32_bf16 v[78:81], a[4:7], v[106:109], 0
v_cvt_pk_bf16_f32 v222, v206, v207
v_cvt_pk_bf16_f32 v223, v208, v209
v_mfma_f32_16x16x32_bf16 v[78:81], a[12:15], v[110:113], v[78:81]
global_store_dwordx4 v3, v[162:165], s[38:39] sc1
v_mfma_f32_16x16x32_bf16 v[78:81], a[20:23], v[114:117], v[78:81]
ds_read_b128 v[142:145], v8 offset:9216
v_add_u32 v3, 147456, v3
v_mfma_f32_16x16x32_bf16 v[78:81], a[28:31], v[118:121], v[78:81]
ds_write_b64 v14, v[222:223]
v_mfma_f32_16x16x32_bf16 v[78:81], a[36:39], v[122:125], v[78:81]
s_waitcnt vmcnt(8)
ds_read_b64 v[224:225], v15
s_mov_b64 exec, 1
global_store_dword v252, v229, s[38:39] sc1
s_mov_b64 exec, -1
v_mfma_f32_16x16x32_bf16 v[78:81], a[44:47], v[126:129], v[78:81]
ds_read_b128 v[146:149], v8 offset:10240
v_mfma_f32_16x16x32_bf16 v[86:89], a[148:151], v[106:109], 0
v_mfma_f32_16x16x32_bf16 v[86:89], a[156:159], v[110:113], v[86:89]
v_mfma_f32_16x16x32_bf16 v[86:89], a[164:167], v[114:117], v[86:89]
ds_read_b128 v[150:153], v8 offset:11264
v_mfma_f32_16x16x32_bf16 v[86:89], a[172:175], v[118:121], v[86:89]
v_mfma_f32_16x16x32_bf16 v[86:89], a[180:183], v[122:125], v[86:89]
v_mfma_f32_16x16x32_bf16 v[86:89], a[188:191], v[126:129], v[86:89]
s_waitcnt lgkmcnt(2)
global_store_dwordx2 v13, v[224:225], s[58:59] sc1
v_mov_b32 v228, v226
v_mfma_f32_16x16x32_bf16 v[74:77], a[48:51], v[130:133], v[74:77]
v_mfma_f32_16x16x32_bf16 v[74:77], a[56:59], v[134:137], v[74:77]
v_mfma_f32_16x16x32_bf16 v[74:77], a[64:67], v[138:141], v[74:77]
v_mfma_f32_16x16x32_bf16 v[74:77], a[72:75], v[142:145], v[74:77]
s_waitcnt vmcnt(8)
v_add_f32 v200, v200, v202
s_waitcnt vmcnt(7)
v_lshlrev_b32 v206, 16, v188
v_add_f32 v201, v201, v203
s_waitcnt lgkmcnt(1)
v_mfma_f32_16x16x32_bf16 v[74:77], a[80:83], v[146:149], v[74:77]
s_waitcnt lgkmcnt(0)
v_and_b32 v207, 0xffff0000, v188
v_mul_f32 v200, 0x3ae38e39, v200
v_lshlrev_b32 v208, 16, v189
v_mfma_f32_16x16x32_bf16 v[74:77], a[88:91], v[150:153], v[74:77]
ds_read_b128 v[106:109], v8 offset:12288
v_mul_f32 v201, 0x3ae38e39, v201
v_and_b32 v209, 0xffff0000, v189
v_mfma_f32_16x16x32_bf16 v[82:85], a[192:195], v[130:133], v[82:85]
v_fma_f32 v232, -v200, v200, v201
v_lshlrev_b32 v210, 16, v190
v_max_f32 v232, 0, v232
v_mfma_f32_16x16x32_bf16 v[82:85], a[200:203], v[134:137], v[82:85]
v_and_b32 v211, 0xffff0000, v190
v_add_f32 v232, 0x3727c5ac, v232
v_mfma_f32_16x16x32_bf16 v[82:85], a[208:211], v[138:141], v[82:85]
ds_read_b128 v[110:113], v8 offset:13312
v_lshlrev_b32 v212, 16, v191
v_mfma_f32_16x16x32_bf16 v[82:85], a[216:219], v[142:145], v[82:85]
s_waitcnt vmcnt(3)
v_sub_u32 v230, v230, v17
v_cmp_lt_u32 vcc, 0x7f, v230
v_mfma_f32_16x16x32_bf16 v[82:85], a[224:227], v[146:149], v[82:85]
v_rcp_f32 v231, v232
v_mfma_f32_16x16x32_bf16 v[82:85], a[232:235], v[150:153], v[82:85]
ds_read_b128 v[114:117], v8 offset:14336
v_and_b32 v213, 0xffff0000, v191
v_mfma_f32_16x16x32_bf16 v[78:81], a[52:55], v[130:133], v[78:81]
s_and_b64 s[88:89], vcc, s[76:77]
s_cbranch_scc1 .Lgu_sp20
.Lgu_rt21:
v_mfma_f32_16x16x32_bf16 v[78:81], a[60:63], v[134:137], v[78:81]
s_cmp_eq_u64 vcc, 0
v_lshlrev_b32 v214, 16, v192
v_and_b32 v215, 0xffff0000, v192
v_mfma_f32_16x16x32_bf16 v[78:81], a[68:71], v[138:141], v[78:81]
s_cselect_b64 s[54:55], s[84:85], s[54:55]
ds_read_b128 v[118:121], v8 offset:15360
v_lshlrev_b32 v216, 16, v193
v_mfma_f32_16x16x32_bf16 v[78:81], a[76:79], v[142:145], v[78:81]
s_cselect_b64 s[52:53], s[86:87], s[52:53]
global_load_dwordx2 v[182:183], v12, s[54:55] offset:0 sc1
v_and_b32 v217, 0xffff0000, v193
v_mfma_f32_16x16x32_bf16 v[78:81], a[84:87], v[146:149], v[78:81]
s_cselect_b64 s[58:59], s[56:57], s[50:51]
global_load_dwordx2 v[184:185], v12, s[54:55] offset:128 sc1
v_lshlrev_b32 v218, 16, v194
v_mfma_f32_16x16x32_bf16 v[78:81], a[92:95], v[150:153], v[78:81]
s_cselect_b32 s82, 1, 0
ds_read_b128 v[122:125], v8 offset:16384
global_load_dwordx4 v[170:173], v10, s[52:53] offset:0 sc1
v_mfma_f32_16x16x32_bf16 v[86:89], a[196:199], v[130:133], v[86:89]
s_add_u32 s62, s62, s82
global_load_dwordx4 v[174:177], v10, s[52:53] offset:1024 sc1
v_and_b32 v219, 0xffff0000, v194
v_mfma_f32_16x16x32_bf16 v[86:89], a[204:207], v[134:137], v[86:89]
s_add_u32 s83, s62, s37
global_load_dwordx4 v[178:181], v10, s[52:53] offset:2048 sc1
v_add_u32 v17, s82, v17
v_add_u32 v226, -1, s83
v_mfma_f32_16x16x32_bf16 v[86:89], a[212:215], v[138:141], v[86:89]
s_cmp_lt_i32 s62, 32
ds_read_b128 v[126:129], v8 offset:17408
global_load_dwordx2 v[186:187], v11, s[54:55] sc1
v_add_lshl_u32 v253, v17, 0, 7
v_mfma_f32_16x16x32_bf16 v[86:89], a[220:223], v[142:145], v[86:89]
s_cselect_b64 s[56:57], s[46:47], s[48:49]
v_bfi_b32 v253, s69, v253, v16
v_lshlrev_b32 v220, 16, v195
v_mfma_f32_16x16x32_bf16 v[86:89], a[228:231], v[146:149], v[86:89]
s_mul_i32 s80, s62, 18432
v_and_b32 v221, 0xffff0000, v195
v_add_f32 v206, v206, v214
v_mfma_f32_16x16x32_bf16 v[86:89], a[236:239], v[150:153], v[86:89]
s_waitcnt lgkmcnt(5)
v_mfma_f32_16x16x32_bf16 v[74:77], a[96:99], v[106:109], v[74:77]
s_barrier
s_waitcnt lgkmcnt(4)
v_mfma_f32_16x16x32_bf16 v[74:77], a[104:107], v[110:113], v[74:77]
s_waitcnt lgkmcnt(3)
v_add_f32 v207, v207, v215
v_add_f32 v208, v208, v216
v_mfma_f32_16x16x32_bf16 v[74:77], a[112:115], v[114:117], v[74:77]
s_waitcnt lgkmcnt(2)
v_add_f32 v209, v209, v217
v_add_f32 v210, v210, v218
v_mfma_f32_16x16x32_bf16 v[74:77], a[120:123], v[118:121], v[74:77]
s_waitcnt lgkmcnt(1)
v_add_f32 v211, v211, v219
v_add_f32 v212, v212, v220
v_mfma_f32_16x16x32_bf16 v[74:77], a[128:131], v[122:125], v[74:77]
s_waitcnt lgkmcnt(0)
v_add_f32 v213, v213, v221
v_lshlrev_b32 v214, 16, v196
v_mfma_f32_16x16x32_bf16 v[74:77], a[136:139], v[126:129], v[74:77]
s_sub_u32 s56, s56, s80
v_and_b32 v215, 0xffff0000, v196
v_lshlrev_b32 v216, 16, v197
v_mfma_f32_16x16x32_bf16 v[82:85], v[26:29], v[106:109], v[82:85]
s_subb_u32 s57, s57, 0
v_and_b32 v217, 0xffff0000, v197
v_lshlrev_b32 v218, 16, v198
v_mfma_f32_16x16x32_bf16 v[82:85], v[34:37], v[110:113], v[82:85]
s_add_u32 s84, s54, 18688
v_and_b32 v219, 0xffff0000, v198
v_lshlrev_b32 v220, 16, v199
v_mfma_f32_16x16x32_bf16 v[82:85], v[42:45], v[114:117], v[82:85]
s_addc_u32 s85, s55, 0
v_and_b32 v221, 0xffff0000, v199
v_add_f32 v206, v206, v214
v_mfma_f32_16x16x32_bf16 v[82:85], v[50:53], v[118:121], v[82:85]
s_add_u32 s86, s52, 110592
v_add_f32 v207, v207, v215
v_add_f32 v208, v208, v216
v_mfma_f32_16x16x32_bf16 v[82:85], v[58:61], v[122:125], v[82:85]
s_addc_u32 s87, s53, 0
v_add_f32 v209, v209, v217
v_add_f32 v210, v210, v218
v_mfma_f32_16x16x32_bf16 v[82:85], v[66:69], v[126:129], v[82:85]
v_mfma_f32_16x16x32_bf16 v[78:81], a[100:103], v[106:109], v[78:81]
s_barrier
v_add_lshl_u32 v252, v228, 0, 7
v_mfma_f32_16x16x32_bf16 v[78:81], a[108:111], v[110:113], v[78:81]
s_add_u32 s72, s72, 1
v_bfi_b32 v252, s69, v252, v18
v_add_f32 v211, v211, v219
v_mfma_f32_16x16x32_bf16 v[78:81], a[116:119], v[114:117], v[78:81]
s_cmp_le_i32 s62, s72
v_add_f32 v212, v212, v220
v_add_f32 v213, v213, v221
v_mfma_f32_16x16x32_bf16 v[78:81], a[124:127], v[118:121], v[78:81]
s_cselect_b64 s[76:77], -1, 0
ds_read_b128 v[130:133], v8 offset:18432
v_lshlrev_b32 v214, 16, v204
v_mfma_f32_16x16x32_bf16 v[78:81], a[132:135], v[122:125], v[78:81]
ds_read_b128 v[134:137], v8 offset:19456
v_mul_f32 v154, v74, v82
v_mfma_f32_16x16x32_bf16 v[78:81], a[140:143], v[126:129], v[78:81]
ds_read_b128 v[138:141], v8 offset:20480
v_mul_f32 v155, v75, v83
v_mfma_f32_16x16x32_bf16 v[86:89], v[30:33], v[106:109], v[86:89]
ds_read_b128 v[142:145], v8 offset:21504
v_mul_f32 v156, v76, v84
v_mfma_f32_16x16x32_bf16 v[86:89], v[38:41], v[110:113], v[86:89]
ds_read_b128 v[146:149], v8 offset:22528
v_mul_f32 v157, v77, v85
v_mfma_f32_16x16x32_bf16 v[86:89], v[46:49], v[114:117], v[86:89]
ds_read_b128 v[150:153], v8 offset:23552
v_cvt_pk_bf16_f32 v162, v154, v155
v_mfma_f32_16x16x32_bf16 v[86:89], v[54:57], v[118:121], v[86:89]
v_cvt_pk_bf16_f32 v163, v156, v157
v_and_b32 v215, 0xffff0000, v204
v_mfma_f32_16x16x32_bf16 v[86:89], v[62:65], v[122:125], v[86:89]
v_lshlrev_b32 v216, 16, v205
v_and_b32 v217, 0xffff0000, v205
v_mfma_f32_16x16x32_bf16 v[86:89], v[70:73], v[126:129], v[86:89]
s_waitcnt lgkmcnt(5)
)"
R"(global_load_dword v230, v253, s[38:39] sc1
v_add_f32_dpp v206, v210, v206 row_ror:8 row_mask:0xf bank_mask:0xf
v_mfma_f32_16x16x32_bf16 v[90:93], a[0:3], v[130:133], 0
s_waitcnt lgkmcnt(4)
v_add_f32_dpp v207, v211, v207 row_ror:8 row_mask:0xf bank_mask:0xf
v_add_f32_dpp v208, v212, v208 row_ror:8 row_mask:0xf bank_mask:0xf
v_mfma_f32_16x16x32_bf16 v[90:93], a[8:11], v[134:137], v[90:93]
s_waitcnt lgkmcnt(3)
v_add_f32_dpp v209, v213, v209 row_ror:8 row_mask:0xf bank_mask:0xf
v_fma_f32 v206, v206, v231, v214
v_mfma_f32_16x16x32_bf16 v[90:93], a[16:19], v[138:141], v[90:93]
s_waitcnt lgkmcnt(2)
v_fma_f32 v207, v207, v231, v215
v_fma_f32 v208, v208, v231, v216
v_mfma_f32_16x16x32_bf16 v[90:93], a[24:27], v[142:145], v[90:93]
s_waitcnt lgkmcnt(1)
v_mul_f32 v158, v78, v86
v_mul_f32 v159, v79, v87
v_mfma_f32_16x16x32_bf16 v[90:93], a[32:35], v[146:149], v[90:93]
s_waitcnt lgkmcnt(0)
v_mul_f32 v160, v80, v88
v_mul_f32 v161, v81, v89
v_mfma_f32_16x16x32_bf16 v[90:93], a[40:43], v[150:153], v[90:93]
ds_read_b128 v[106:109], v8 offset:24576
v_cvt_pk_bf16_f32 v164, v158, v159
v_mfma_f32_16x16x32_bf16 v[98:101], a[144:147], v[130:133], 0
ds_read_b128 v[110:113], v8 offset:25600
v_cvt_pk_bf16_f32 v165, v160, v161
v_mfma_f32_16x16x32_bf16 v[98:101], a[152:155], v[134:137], v[98:101]
v_mfma_f32_16x16x32_bf16 v[98:101], a[160:163], v[138:141], v[98:101]
s_waitcnt vmcnt(9)
s_barrier
v_mfma_f32_16x16x32_bf16 v[98:101], a[168:171], v[142:145], v[98:101]
s_waitcnt vmcnt(7)
s_mov_b64 exec, 1
global_store_dword v252, v228, s[38:39] sc1
s_mov_b64 exec, -1
v_mfma_f32_16x16x32_bf16 v[98:101], a[176:179], v[146:149], v[98:101]
v_fma_f32 v209, v209, v231, v217
v_cvt_pk_bf16_f32 v222, v206, v207
v_mfma_f32_16x16x32_bf16 v[98:101], a[184:187], v[150:153], v[98:101]
ds_read_b128 v[114:117], v8 offset:26624
v_cvt_pk_bf16_f32 v223, v208, v209
v_mfma_f32_16x16x32_bf16 v[94:97], a[4:7], v[130:133], 0
ds_write_b64 v14, v[222:223]
v_mfma_f32_16x16x32_bf16 v[94:97], a[12:15], v[134:137], v[94:97]
global_store_dwordx4 v3, v[162:165], s[38:39] sc1
v_mfma_f32_16x16x32_bf16 v[94:97], a[20:23], v[138:141], v[94:97]
ds_read_b128 v[118:121], v8 offset:27648
v_add_u32 v3, 147456, v3
v_mfma_f32_16x16x32_bf16 v[94:97], a[28:31], v[142:145], v[94:97]
ds_read_b64 v[224:225], v15
v_mfma_f32_16x16x32_bf16 v[94:97], a[36:39], v[146:149], v[94:97]
v_mfma_f32_16x16x32_bf16 v[94:97], a[44:47], v[150:153], v[94:97]
ds_read_b128 v[122:125], v8 offset:28672
v_mfma_f32_16x16x32_bf16 v[102:105], a[148:151], v[130:133], 0
v_mfma_f32_16x16x32_bf16 v[102:105], a[156:159], v[134:137], v[102:105]
v_mfma_f32_16x16x32_bf16 v[102:105], a[164:167], v[138:141], v[102:105]
ds_read_b128 v[126:129], v8 offset:29696
v_mfma_f32_16x16x32_bf16 v[102:105], a[172:175], v[142:145], v[102:105]
v_mfma_f32_16x16x32_bf16 v[102:105], a[180:183], v[146:149], v[102:105]
s_waitcnt lgkmcnt(2)
global_store_dwordx2 v13, v[224:225], s[60:61] sc1
v_mov_b32 v229, v227
v_mfma_f32_16x16x32_bf16 v[102:105], a[188:191], v[150:153], v[102:105]
v_mfma_f32_16x16x32_bf16 v[90:93], a[48:51], v[106:109], v[90:93]
v_mfma_f32_16x16x32_bf16 v[90:93], a[56:59], v[110:113], v[90:93]
v_mfma_f32_16x16x32_bf16 v[90:93], a[64:67], v[114:117], v[90:93]
v_mfma_f32_16x16x32_bf16 v[90:93], a[72:75], v[118:121], v[90:93]
s_waitcnt vmcnt(8)
v_add_f32 v182, v182, v184
s_waitcnt vmcnt(7)
v_lshlrev_b32 v206, 16, v170
v_add_f32 v183, v183, v185
s_waitcnt lgkmcnt(1)
v_mfma_f32_16x16x32_bf16 v[90:93], a[80:83], v[122:125], v[90:93]
s_waitcnt lgkmcnt(0)
v_and_b32 v207, 0xffff0000, v170
v_mul_f32 v182, 0x3ae38e39, v182
v_lshlrev_b32 v208, 16, v171
v_mfma_f32_16x16x32_bf16 v[90:93], a[88:91], v[126:129], v[90:93]
ds_read_b128 v[130:133], v8 offset:30720
v_mul_f32 v183, 0x3ae38e39, v183
v_and_b32 v209, 0xffff0000, v171
v_mfma_f32_16x16x32_bf16 v[98:101], a[192:195], v[106:109], v[98:101]
v_fma_f32 v232, -v182, v182, v183
v_lshlrev_b32 v210, 16, v172
v_max_f32 v232, 0, v232
v_mfma_f32_16x16x32_bf16 v[98:101], a[200:203], v[110:113], v[98:101]
v_and_b32 v211, 0xffff0000, v172
v_add_f32 v232, 0x3727c5ac, v232
v_mfma_f32_16x16x32_bf16 v[98:101], a[208:211], v[114:117], v[98:101]
ds_read_b128 v[134:137], v8 offset:31744
v_lshlrev_b32 v212, 16, v173
.Lgu_in24:
v_mfma_f32_16x16x32_bf16 v[98:101], a[216:219], v[118:121], v[98:101]
s_waitcnt vmcnt(3)
v_sub_u32 v230, v230, v17
v_cmp_lt_u32 vcc, 0x7f, v230
v_mfma_f32_16x16x32_bf16 v[98:101], a[224:227], v[122:125], v[98:101]
v_rcp_f32 v231, v232
v_mfma_f32_16x16x32_bf16 v[98:101], a[232:235], v[126:129], v[98:101]
ds_read_b128 v[138:141], v8 offset:32768
v_and_b32 v213, 0xffff0000, v173
v_mfma_f32_16x16x32_bf16 v[94:97], a[52:55], v[106:109], v[94:97]
s_and_b64 s[88:89], vcc, s[76:77]
s_cbranch_scc1 .Lgu_sp22
.Lgu_rt23:
v_mfma_f32_16x16x32_bf16 v[94:97], a[60:63], v[110:113], v[94:97]
s_cmp_eq_u64 vcc, 0
v_lshlrev_b32 v214, 16, v174
v_and_b32 v215, 0xffff0000, v174
v_mfma_f32_16x16x32_bf16 v[94:97], a[68:71], v[114:117], v[94:97]
s_cselect_b64 s[54:55], s[84:85], s[54:55]
ds_read_b128 v[142:145], v8 offset:33792
v_lshlrev_b32 v216, 16, v175
v_mfma_f32_16x16x32_bf16 v[94:97], a[76:79], v[118:121], v[94:97]
s_cselect_b64 s[52:53], s[86:87], s[52:53]
global_load_dwordx2 v[200:201], v12, s[54:55] offset:0 sc1
v_and_b32 v217, 0xffff0000, v175
v_mfma_f32_16x16x32_bf16 v[94:97], a[84:87], v[122:125], v[94:97]
s_cselect_b64 s[60:61], s[56:57], s[50:51]
global_load_dwordx2 v[202:203], v12, s[54:55] offset:128 sc1
v_lshlrev_b32 v218, 16, v176
v_mfma_f32_16x16x32_bf16 v[94:97], a[92:95], v[126:129], v[94:97]
s_cselect_b32 s82, 1, 0
ds_read_b128 v[146:149], v8 offset:34816
global_load_dwordx4 v[188:191], v10, s[52:53] offset:0 sc1
v_mfma_f32_16x16x32_bf16 v[102:105], a[196:199], v[106:109], v[102:105]
s_add_u32 s62, s62, s82
global_load_dwordx4 v[192:195], v10, s[52:53] offset:1024 sc1
v_and_b32 v219, 0xffff0000, v176
v_mfma_f32_16x16x32_bf16 v[102:105], a[204:207], v[110:113], v[102:105]
s_add_u32 s83, s62, s37
global_load_dwordx4 v[196:199], v10, s[52:53] offset:2048 sc1
v_add_u32 v17, s82, v17
v_add_u32 v227, -1, s83
v_mfma_f32_16x16x32_bf16 v[102:105], a[212:215], v[114:117], v[102:105]
s_cmp_lt_i32 s62, 32
ds_read_b128 v[150:153], v8 offset:35840
global_load_dwordx2 v[204:205], v11, s[54:55] sc1
v_add_lshl_u32 v253, v17, 0, 7
v_mfma_f32_16x16x32_bf16 v[102:105], a[220:223], v[118:121], v[102:105]
s_cselect_b64 s[56:57], s[46:47], s[48:49]
v_bfi_b32 v253, s69, v253, v16
v_lshlrev_b32 v220, 16, v177
v_mfma_f32_16x16x32_bf16 v[102:105], a[228:231], v[122:125], v[102:105]
s_mul_i32 s80, s62, 18432
v_and_b32 v221, 0xffff0000, v177
v_add_f32 v206, v206, v214
v_mfma_f32_16x16x32_bf16 v[102:105], a[236:239], v[126:129], v[102:105]
s_waitcnt lgkmcnt(5)
v_mfma_f32_16x16x32_bf16 v[90:93], a[96:99], v[130:133], v[90:93]
s_barrier
s_waitcnt lgkmcnt(4)
v_mfma_f32_16x16x32_bf16 v[90:93], a[104:107], v[134:137], v[90:93]
s_waitcnt lgkmcnt(3)
v_add_f32 v207, v207, v215
v_add_f32 v208, v208, v216
v_mfma_f32_16x16x32_bf16 v[90:93], a[112:115], v[138:141], v[90:93]
s_waitcnt lgkmcnt(2)
v_add_f32 v209, v209, v217
v_add_f32 v210, v210, v218
v_mfma_f32_16x16x32_bf16 v[90:93], a[120:123], v[142:145], v[90:93]
s_waitcnt lgkmcnt(1)
v_add_f32 v211, v211, v219
v_add_f32 v212, v212, v220
v_mfma_f32_16x16x32_bf16 v[90:93], a[128:131], v[146:149], v[90:93]
s_waitcnt lgkmcnt(0)
v_add_f32 v213, v213, v221
v_lshlrev_b32 v214, 16, v178
v_mfma_f32_16x16x32_bf16 v[90:93], a[136:139], v[150:153], v[90:93]
s_sub_u32 s56, s56, s80
v_and_b32 v215, 0xffff0000, v178
v_lshlrev_b32 v216, 16, v179
v_mfma_f32_16x16x32_bf16 v[98:101], v[26:29], v[130:133], v[98:101]
s_subb_u32 s57, s57, 0
v_and_b32 v217, 0xffff0000, v179
v_lshlrev_b32 v218, 16, v180
v_mfma_f32_16x16x32_bf16 v[98:101], v[34:37], v[134:137], v[98:101]
s_add_u32 s84, s54, 18688
v_and_b32 v219, 0xffff0000, v180
v_lshlrev_b32 v220, 16, v181
v_mfma_f32_16x16x32_bf16 v[98:101], v[42:45], v[138:141], v[98:101]
s_addc_u32 s85, s55, 0
v_and_b32 v221, 0xffff0000, v181
v_add_f32 v206, v206, v214
v_mfma_f32_16x16x32_bf16 v[98:101], v[50:53], v[142:145], v[98:101]
s_add_u32 s86, s52, 110592
v_xor_b32 v8, 0x10000, v8
v_add_f32 v207, v207, v215
v_mfma_f32_16x16x32_bf16 v[98:101], v[58:61], v[146:149], v[98:101]
s_addc_u32 s87, s53, 0
v_add_f32 v208, v208, v216
v_add_f32 v209, v209, v217
v_mfma_f32_16x16x32_bf16 v[98:101], v[66:69], v[150:153], v[98:101]
v_mfma_f32_16x16x32_bf16 v[94:97], a[100:103], v[130:133], v[94:97]
s_barrier
v_add_lshl_u32 v252, v229, 0, 7
v_mfma_f32_16x16x32_bf16 v[94:97], a[108:111], v[134:137], v[94:97]
s_add_u32 s72, s72, 1
v_add_f32 v210, v210, v218
v_add_f32 v211, v211, v219
v_bfi_b32 v252, s69, v252, v18
v_mfma_f32_16x16x32_bf16 v[94:97], a[116:119], v[138:141], v[94:97]
s_cmp_le_i32 s62, s72
v_add_f32 v212, v212, v220
v_add_f32 v213, v213, v221
v_mfma_f32_16x16x32_bf16 v[94:97], a[124:127], v[142:145], v[94:97]
s_cselect_b64 s[76:77], -1, 0
ds_read_b128 v[106:109], v8 offset:0
v_lshlrev_b32 v214, 16, v186
v_mfma_f32_16x16x32_bf16 v[94:97], a[132:135], v[146:149], v[94:97]
ds_read_b128 v[110:113], v8 offset:1024
v_mul_f32 v154, v90, v98
v_mfma_f32_16x16x32_bf16 v[94:97], a[140:143], v[150:153], v[94:97]
ds_read_b128 v[114:117], v8 offset:2048
v_mul_f32 v155, v91, v99
v_mfma_f32_16x16x32_bf16 v[102:105], v[30:33], v[130:133], v[102:105]
ds_read_b128 v[118:121], v8 offset:3072
v_mul_f32 v156, v92, v100
v_mfma_f32_16x16x32_bf16 v[102:105], v[38:41], v[134:137], v[102:105]
ds_read_b128 v[122:125], v8 offset:4096
v_mul_f32 v157, v93, v101
v_mfma_f32_16x16x32_bf16 v[102:105], v[46:49], v[138:141], v[102:105]
ds_read_b128 v[126:129], v8 offset:5120
v_cvt_pk_bf16_f32 v162, v154, v155
v_mfma_f32_16x16x32_bf16 v[102:105], v[54:57], v[142:145], v[102:105]
v_cvt_pk_bf16_f32 v163, v156, v157
v_and_b32 v215, 0xffff0000, v186
v_mfma_f32_16x16x32_bf16 v[102:105], v[62:65], v[146:149], v[102:105]
v_mfma_f32_16x16x32_bf16 v[102:105], v[70:73], v[150:153], v[102:105]
s_sub_u32 s70, s70, 1
s_cbranch_scc0 .Lgu_top25
v_mov_b32 v232, s62
ds_write_b32 v1, v232 offset:36864
v_subrev_u32 v17, s62, v17
s_waitcnt vmcnt(7) lgkmcnt(0)
s_barrier
v_mul_f32 v154, v90, v98
v_mul_f32 v155, v91, v99
v_mul_f32 v156, v92, v100
v_mul_f32 v157, v93, v101
v_cvt_pk_bf16_f32 v162, v154, v155
v_cvt_pk_bf16_f32 v163, v156, v157
v_mul_f32 v158, v94, v102
v_mul_f32 v159, v95, v103
v_mul_f32 v160, v96, v104
v_mul_f32 v161, v97, v105
v_cvt_pk_bf16_f32 v164, v158, v159
v_cvt_pk_bf16_f32 v165, v160, v161
global_store_dwordx4 v3, v[162:165], s[38:39] sc1
v_mov_b32 v231, 192
ds_read_b32 v231, v231 offset:36864
s_waitcnt vmcnt(0)
s_barrier
s_waitcnt lgkmcnt(0)
v_readfirstlane_b32 s62, v231
s_nop 3
s_max_i32 s62, s62, 2
s_min_i32 s62, s62, 66
s_add_u32 s62, s62, 1
s_branch .Lgu_fin
.Lgu_fin:
s_cmp_gt_i32 s62, 63
s_cbranch_scc1 .Lgu_end
v_add_u32 v233, s62, v17
v_add_lshl_u32 v253, v233, 0, 7
v_bfi_b32 v253, s69, v253, v16
global_load_dword v230, v253, s[38:39] sc1
s_cmp_lt_i32 s62, 32
s_cselect_b64 s[56:57], s[46:47], s[48:49]
s_mul_i32 s80, s62, 18432
s_sub_u32 s56, s56, s80
s_subb_u32 s57, s57, 0
s_mov_b64 s[58:59], s[56:57]
s_mul_i32 s80, s62, 110592
s_mul_i32 s81, s62, 18688
s_mov_b64 s[52:53], s[38:39]
s_mov_b64 s[54:55], s[38:39]
s_add_u32 s52, s52, s80
s_addc_u32 s53, s53, 0
s_add_u32 s54, s54, s81
s_addc_u32 s55, s55, 0
s_waitcnt vmcnt(0)
v_sub_u32 v230, v230, v233
v_cmp_lt_u32 vcc, 0x7f, v230
s_cbranch_vccnz .Lgu_sp27
.Lgu_rt28:
global_load_dwordx2 v[182:183], v12, s[54:55] offset:0 sc1
global_load_dwordx2 v[184:185], v12, s[54:55] offset:128 sc1
global_load_dwordx4 v[170:173], v10, s[52:53] offset:0 sc1
global_load_dwordx4 v[174:177], v10, s[52:53] offset:1024 sc1
global_load_dwordx4 v[178:181], v10, s[52:53] offset:2048 sc1
global_load_dwordx2 v[186:187], v11, s[54:55] sc1
s_waitcnt vmcnt(4)
v_add_f32 v182, v182, v184
s_waitcnt vmcnt(3)
v_lshlrev_b32 v206, 16, v170
v_add_f32 v183, v183, v185
v_and_b32 v207, 0xffff0000, v170
v_mul_f32 v182, 0x3ae38e39, v182
v_lshlrev_b32 v208, 16, v171
v_mul_f32 v183, 0x3ae38e39, v183
v_and_b32 v209, 0xffff0000, v171
v_fma_f32 v232, -v182, v182, v183
v_lshlrev_b32 v210, 16, v172
v_max_f32 v232, 0, v232
v_and_b32 v211, 0xffff0000, v172
v_add_f32 v232, 0x3727c5ac, v232
v_lshlrev_b32 v212, 16, v173
v_rcp_f32 v231, v232
v_and_b32 v213, 0xffff0000, v173
s_waitcnt vmcnt(2)
v_lshlrev_b32 v214, 16, v174
v_and_b32 v215, 0xffff0000, v174
v_lshlrev_b32 v216, 16, v175
v_and_b32 v217, 0xffff0000, v175
v_lshlrev_b32 v218, 16, v176
v_and_b32 v219, 0xffff0000, v176
v_lshlrev_b32 v220, 16, v177
v_and_b32 v221, 0xffff0000, v177
v_add_f32 v206, v206, v214
v_add_f32 v207, v207, v215
v_add_f32 v208, v208, v216
v_add_f32 v209, v209, v217
v_add_f32 v210, v210, v218
v_add_f32 v211, v211, v219
v_add_f32 v212, v212, v220
v_add_f32 v213, v213, v221
s_waitcnt vmcnt(1)
v_lshlrev_b32 v214, 16, v178
v_and_b32 v215, 0xffff0000, v178
v_lshlrev_b32 v216, 16, v179
v_and_b32 v217, 0xffff0000, v179
v_lshlrev_b32 v218, 16, v180
v_and_b32 v219, 0xffff0000, v180
v_lshlrev_b32 v220, 16, v181
v_and_b32 v221, 0xffff0000, v181
v_add_f32 v206, v206, v214
v_add_f32 v207, v207, v215
v_add_f32 v208, v208, v216
v_add_f32 v209, v209, v217
v_add_f32 v210, v210, v218
v_add_f32 v211, v211, v219
v_add_f32 v212, v212, v220
v_add_f32 v213, v213, v221
s_waitcnt vmcnt(0)
v_lshlrev_b32 v214, 16, v186
v_and_b32 v215, 0xffff0000, v186
v_lshlrev_b32 v216, 16, v187
v_and_b32 v217, 0xffff0000, v187
v_add_f32_dpp v206, v210, v206 row_ror:8 row_mask:0xf bank_mask:0xf
v_add_f32_dpp v207, v211, v207 row_ror:8 row_mask:0xf bank_mask:0xf
v_add_f32_dpp v208, v212, v208 row_ror:8 row_mask:0xf bank_mask:0xf
v_add_f32_dpp v209, v213, v209 row_ror:8 row_mask:0xf bank_mask:0xf
v_fma_f32 v206, v206, v231, v214
v_fma_f32 v207, v207, v231, v215
v_fma_f32 v208, v208, v231, v216
v_fma_f32 v209, v209, v231, v217
v_cvt_pk_bf16_f32 v222, v206, v207
v_cvt_pk_bf16_f32 v223, v208, v209
ds_write_b64 v14, v[222:223]
ds_read_b64 v[224:225], v15
s_waitcnt lgkmcnt(0)
global_store_dwordx2 v13, v[224:225], s[58:59] sc1
s_add_u32 s62, s62, 4
s_branch .Lgu_fin
.Lgu_end:
s_endpgm
.Lgu_sp11:
s_sleep 1
v_add_lshl_u32 v253, v5, 0, 7
v_bfi_b32 v253, s69, v253, v4
global_load_dword v254, v253, s[38:39] sc1
s_add_u32 s71, s71, 1
s_waitcnt vmcnt(0)
v_sub_u32 v254, v254, v5
v_cmp_lt_u32 vcc, 0x7f, v254
s_cbranch_vccz .Lgu_rt12
s_cmp_lt_u32 s71, 3000000
s_cbranch_scc1 .Lgu_sp11
s_branch .Lgu_rt12
.Lgu_sp13:
s_sleep 1
v_add_lshl_u32 v253, v5, 0, 7
v_bfi_b32 v253, s69, v253, v4
global_load_dword v255, v253, s[38:39] sc1
s_add_u32 s71, s71, 1
s_waitcnt vmcnt(0)
v_sub_u32 v255, v255, v5
v_cmp_lt_u32 vcc, 0x7f, v255
s_cbranch_vccz .Lgu_rt14
s_cmp_lt_u32 s71, 3000000
s_cbranch_scc1 .Lgu_sp13
s_branch .Lgu_rt14
.Lgu_sp16:
s_sleep 1
v_add_lshl_u32 v253, v5, 0, 7
v_bfi_b32 v253, s69, v253, v4
)"
R"(global_load_dword v254, v253, s[38:39] sc1
s_add_u32 s71, s71, 1
s_waitcnt vmcnt(0)
v_sub_u32 v254, v254, v5
v_cmp_lt_u32 vcc, 0x7f, v254
s_cbranch_vccz .Lgu_rt17
s_cmp_lt_u32 s71, 3000000
s_cbranch_scc1 .Lgu_sp16
s_branch .Lgu_rt17
.Lgu_sp20:
s_sleep 1
v_add_lshl_u32 v253, v17, 0, 7
v_bfi_b32 v253, s69, v253, v16
global_load_dword v230, v253, s[38:39] sc1
s_add_u32 s71, s71, 1
s_waitcnt vmcnt(0)
v_sub_u32 v230, v230, v17
v_cmp_lt_u32 vcc, 0x7f, v230
s_and_b64 vcc, vcc, s[76:77]
s_cbranch_vccz .Lgu_rt21
s_cmp_lt_u32 s71, 3000000
s_cbranch_scc1 .Lgu_sp20
s_branch .Lgu_rt21
.Lgu_sp22:
s_sleep 1
v_add_lshl_u32 v253, v17, 0, 7
v_bfi_b32 v253, s69, v253, v16
global_load_dword v230, v253, s[38:39] sc1
s_add_u32 s71, s71, 1
s_waitcnt vmcnt(0)
v_sub_u32 v230, v230, v17
v_cmp_lt_u32 vcc, 0x7f, v230
s_and_b64 vcc, vcc, s[76:77]
s_cbranch_vccz .Lgu_rt23
s_cmp_lt_u32 s71, 3000000
s_cbranch_scc1 .Lgu_sp22
s_branch .Lgu_rt23
.Lgu_sp27:
s_sleep 1
v_add_lshl_u32 v253, v233, 0, 7
v_bfi_b32 v253, s69, v253, v16
global_load_dword v230, v253, s[38:39] sc1
s_add_u32 s71, s71, 1
s_waitcnt vmcnt(0)
v_sub_u32 v230, v230, v233
v_cmp_lt_u32 vcc, 0x7f, v230
s_cbranch_vccz .Lgu_rt28
s_cmp_lt_u32 s71, 3000000
s_cbranch_scc1 .Lgu_sp27
s_branch .Lgu_rt28
.Le3:
)"
    :: "{v[0:31]}"(va), "{s[36:51]}"(sa), "{s[52:67]}"(sb) : "v255", "a255", "s99", "vcc", "memory");
  }
}

extern "C" void kernel_launch(void* const* d_in, const int* in_sizes, int n_in,
                              void* d_out, int out_size, void* d_ws, size_t ws_size,
                              hipStream_t stream) {
  k_main<<<256, 256, 0, stream>>>((const char*)d_in[0], (const char*)d_in[1], (const char*)d_in[2], (const char*)d_in[4],
                                  (const char*)d_in[5], (const char*)d_in[6], (char*)d_ws, (char*)d_out);
}
```

```cpp
#include <hip/hip_runtime.h>
#include <hip/hip_bf16.h>
#include <stdint.h>
typedef __attribute__((ext_vector_type(32))) unsigned u32x32;
typedef __attribute__((ext_vector_type(16))) unsigned u32x16;
constexpr unsigned RP = 80u;
constexpr unsigned PAD = 12u;
constexpr unsigned XT = 18432u;
constexpr unsigned X1P = 18688u;
constexpr unsigned HT = 147456u;
constexpr unsigned PT = 110592u;
constexpr unsigned FL = 1024u;
constexpr unsigned FR = 102400u;
constexpr unsigned WS_FL = 16384u;
constexpr unsigned WS_ST = 425984u;
constexpr unsigned WS_O = 1048576u;
constexpr unsigned WS_X1 = 6946816u;
constexpr unsigned WS_X2 = 13000704u;
constexpr unsigned WS_XD = 15396864u;
constexpr unsigned WS_P = 17977344u;
constexpr unsigned WS_H = 53366784u;
constexpr unsigned WS_END = 100552704u;
constexpr unsigned FO = 0u;
constexpr unsigned FX1 = 8192u;
constexpr unsigned FX2 = 65536u;
constexpr unsigned FP = 10240u;
constexpr unsigned FH = 28672u;
__device__ __forceinline__ void touch(const void* p) {
  asm volatile("s_mov_b32 m0, 0x27f00\n\ts_nop 0\n\tglobal_load_lds_dword %0, off" :: "v"(p) : "memory");
}
__device__ __forceinline__ unsigned uni(unsigned v) { return __builtin_amdgcn_readfirstlane(v); }
#define TOUCH_CODE(K, B, N) do { unsigned long long pc; unsigned len; int rel;                   \
    asm volatile("s_getpc_b64 %0\n.Lpc" #K ":\n\ts_mov_b32 %1, .Le" #K "-(" B ")-4\n\ts_mov_b32 %2, (" B ")-.Lpc" #K : "=s"(pc), "=s"(len), "=s"(rel)); \
    touch((const char*)pc + rel + min(((N) * 64 + lane) * 128, len)); } while (0)
__global__ __launch_bounds__(256, 1) void k_main(const char* x0, const char* wqkv, const char* wo, const char* wg, const char* wu, const char* wd,
                                                 char* ws, char* out) {
  __shared__ __attribute__((aligned(1024))) char smem[163840];
  asm volatile("" :: "s"((unsigned)(size_t)(__attribute__((address_space(3))) const char*)smem) : "memory");
  unsigned lane = threadIdx.x & 63;
  const unsigned wave = uni(threadIdx.x >> 6), xc = blockIdx.x & 7, jj = blockIdx.x >> 3;
  if (wave == 3) { if (jj < 8) TOUCH_CODE(0, ".Lpc0", jj); else if (jj < 16) TOUCH_CODE(1, ".Lb1-2048", jj - 8); else if (jj < 24) TOUCH_CODE(2, ".Lb2-2048", jj - 16); else TOUCH_CODE(3, ".Lb3-2048", jj - 24); };
  const bool ev = !(xc & 1); const unsigned xh = xc >> 1;
  if (ev && jj >= 8) { const unsigned u = min((jj - 8) * 216 + threadIdx.x, 5183u), sg = u / 3; touch(wqkv + sg * 1152 + xh * 288 + min((u - 3 * sg) * 128, 284u)); }
  if (__builtin_expect(ev && jj < 8, 1)) {
    const unsigned qa_hg = xc + (jj >> 2), qa_rho = jj & 3, pq0 = wave < 2 ? wave * 5 : 2 + wave * 4;
    unsigned qx[5];
#pragma unroll
    for (unsigned j = 0; j < 5; ++j)
      qx[j] = ((qa_rho * 32 + 31) * 16 + ((pq0 + j) & 1) * 8 + (lane >> 3)) * 1152 + ((pq0 + j) >> 1) * 128 + (((lane ^ (lane >> 3)) & 7) << 4) + 4096 - j * 1024;
    const unsigned long long xb = (unsigned long long)(x0 - 2048);
    asm volatile("s_add_u32 m0, %6, 0\n\ts_sub_u32 vcc_lo, %7, 0\n\ts_subb_u32 vcc_hi, %8, 0\n\tglobal_load_lds_dwordx4 %1, vcc offset:-2048 sc1\n\tglobal_load_lds_dwordx4 %2, vcc offset:-1024 sc1\n\tglobal_load_lds_dwordx4 %3, vcc offset:0 sc1\n\tglobal_load_lds_dwordx4 %4, vcc offset:1024 sc1\n\ts_cmp_gt_u32 %9, 1\n\ts_cbranch_scc1 .Lqx0\n\tglobal_load_lds_dwordx4 %5, vcc offset:2048 sc1\n.Lqx0:\n\ts_add_u32 m0, %6, 18432\n\ts_sub_u32 vcc_lo, %7, 18432\n\ts_subb_u32 vcc_hi, %8, 0\n\tglobal_load_lds_dwordx4 %1, vcc offset:-2048 sc1\n\tglobal_load_lds_dwordx4 %2, vcc offset:-1024 sc1\n\tglobal_load_lds_dwordx4 %3, vcc offset:0 sc1\n\tglobal_load_lds_dwordx4 %4, vcc offset:1024 sc1\n\ts_cmp_gt_u32 %9, 1\n\ts_cbranch_scc1 .Lqx1\n\tglobal_load_lds_dwordx4 %5, vcc offset:2048 sc1\n.Lqx1:\n\t" : "+v"(lane) : "v"(qx[0]), "v"(qx[1]), "v"(qx[2]), "v"(qx[3]), "v"(qx[4]), "s"(pq0 * 1024 + 2048),
                 "s"((unsigned)xb), "s"((unsigned)(xb >> 32)), "s"(wave) : "vcc", "scc", "memory");
    const unsigned r = lane & 15, g = lane >> 4, q = r >> 2, p = lane & 3;
    const unsigned hg = qa_hg, rho = qa_rho, row0 = rho * RP + PAD, pc0 = wave < 2 ? wave * 5 : 2 + wave * 4, hb = wave * 36 + (wave & 1) * 4;
    auto oa = [&](unsigned b) { return 43520 + (b >> 4) * 256 + r * 16 + (b & 15); };
    const unsigned m0 = 4 * g + (r & 1), m1 = m0 + 2, hm = (r & 1) ? 0xFFFF0000u : 0xFFFFu, ninf = 0xFF800000u;
    u32x32 va, vb; u32x16 sa, sb;
    va[0] = (((0 + lane) * 5462u) >> 16) * (1152 - 192) + lane * 16 + 4096;
    va[1] = (((64 + lane) * 5462u) >> 16) * (1152 - 192) + lane * 16 + 4096;
    va[2] = (((128 + lane) * 5462u) >> 16) * (1152 - 192) + lane * 16 + 4096;
    va[3] = (((192 + lane) * 5462u) >> 16) * (1152 - 192) + lane * 16 + 4096;
    va[4] = (((256 + lane) * 5462u) >> 16) * (1152 - 192) + lane * 16 + 4096;
    va[5] = (((320 + lane) * 5462u) >> 16) * (1152 - 192) + lane * 16 + 4096;
    va[6] = (((384 + lane) * 5462u) >> 16) * (1152 - 192) + lane * 16 + 4096;
    va[7] = (((448 + lane) * 5462u) >> 16) * (1152 - 192) + lane * 16 + 4096;
    va[8] = (((512 + lane) * 5462u) >> 16) * (1152 - 192) + lane * 16 + 12288;
    va[9] = (8 * g + q) * 576 + (hg & 3) * 16 + hb + p * 8;
    va[10] = (8 * g + q) * 576 + (hg & 3) * 16 + 32 + (p & 1) * 72 + (wave == 0 ? (p >> 1) * 192 : 384);
    va[11] = qx[0];
    va[12] = qx[1];
    va[13] = qx[2];
    va[14] = qx[3];
    va[15] = qx[4];
    va[16] = WS_O + (row0 - 1) * XT + hg * 2304 + wave * 1024 + (wave < 2 ? lane : r) * 16;
    va[17] = 43520 + wave * 1024 + (wave < 2 ? lane : r) * 16;
    va[18] = oa(hb + g * 8);
    va[19] = ((g & 1) == (wave >> 1)) ? oa(32 + (wave & 1) * 4 + (wave >> 1) * 72) : 46336 + lane * 4;
    va[20] = g == 0 ? 40960 + ((wave >> 1) * 16 + r) * 8 + (wave & 1) * 4 : 46080;
    va[21] = 41216 + ((wave >> 1) * 16 + r) * 8 + (wave & 1) * 4;
    va[22] = lane * 8;
    va[23] = lane * 16;
    va[24] = 43008 + r * 4;
    va[25] = 43008 + g * 16;
    va[26] = 41984 + (q * 16 + r) * 16 + p * 4;
    va[27] = 43136 + (((m0 > r) ? 0 : 1) * 16 + m0) * 4;
    va[28] = 43136 + (((m1 > r) ? 0 : 1) * 16 + m1) * 4;
    va[29] = 43136 + (((m0 > r) ? 1 : 0) * 16 + m0) * 4;
    va[30] = 43136 + (((m1 > r) ? 1 : 0) * 16 + m1) * 4;
    va[31] = 0u;
    vb[0] = (4 * g + 0 > r && !((r ^ 0) & 1)) ? 0u : ninf;
    vb[1] = (4 * g + 1 > r && !((r ^ 1) & 1)) ? 0u : ninf;
    vb[2] = (4 * g + 2 > r && !((r ^ 2) & 1)) ? 0u : ninf;
    vb[3] = (4 * g + 3 > r && !((r ^ 3) & 1)) ? 0u : ninf;
    vb[4] = (4 * g + 0 <= r && !((r ^ 0) & 1)) ? 0u : ninf;
    vb[5] = (4 * g + 1 <= r && !((r ^ 1) & 1)) ? 0u : ninf;
    vb[6] = (4 * g + 2 <= r && !((r ^ 2) & 1)) ? 0u : ninf;
    vb[7] = (4 * g + 3 <= r && !((r ^ 3) & 1)) ? 0u : ninf;
    vb[8] = m0 > r ? hm : 0u;
    vb[9] = m1 > r ? hm : 0u;
    vb[10] = m0 > r ? 0u : hm;
    vb[11] = m1 > r ? 0u : hm;
    vb[12] = WS_FL + rho * FR + FX2 + min(lane, 35u) * FL;
    vb[13] = WS_FL + rho * FR + FX2 + min(lane, 35u) * FL;
    vb[14] = 0u - 29;
    vb[15] = WS_FL + rho * FR + FO + hg * FL;
    vb[16] = 0u - 2;
    vb[17] = WS_FL + lane * FL;
    vb[18] = 0u;
    vb[19] = r * 128 + (((0 + g) ^ (r & 7)) << 4);
    vb[20] = r * 128 + (((4 + g) ^ (r & 7)) << 4);
    sa[0] = 0 + wave; sa[2] = (unsigned)(unsigned long long)ws; sa[3] = (unsigned)((unsigned long long)ws >> 32);
    sa[4] = uni((unsigned)(unsigned long long)(wqkv + ((hg * 144) & ~63u) + wave * (16 * 3456) + 0 * 64 * 3456));
    sa[5] = uni((unsigned)((unsigned long long)(wqkv + ((hg * 144) & ~63u) + wave * (16 * 3456) + 0 * 64 * 3456) >> 32));
    sa[6] = uni((unsigned)(unsigned long long)(x0 - 2048 - 2 * XT));
    sa[7] = uni((unsigned)((unsigned long long)(x0 - 2048 - 2 * XT) >> 32));
    sa[8] = uni((unsigned)(unsigned long long)((ws + WS_X2) - x0 + (rho ? 32 : 31) * (long long)XT));
    sa[9] = uni((unsigned)((unsigned long long)((ws + WS_X2) - x0 + (rho ? 32 : 31) * (long long)XT) >> 32));
    sa[10] = uni((unsigned)(unsigned long long)(-(long long)XT));
    sa[11] = uni((unsigned)((unsigned long long)(-(long long)XT) >> 32));
    sa[12] = uni(47104 + wave * 9216);
    sa[13] = uni(pc0 * 1024 + 2048 + 0x10000);
    sa[14] = uni(rho ? 0u : XT);
    sa[15] = uni(rho ? 0u - FR : 0u);
    sb[0] = uni(rho ? 0u : ninf);
    sb[1] = uni(hg * 4 + rho);
    asm volatile(
R"(.Lb0:
s_movk_i32 s69, 0x380
s_mov_b32 s71, 0
s_mov_b32 s74, 0
s_load_dword s37, s[38:39], 0x0
s_nop 1
s_and_b32 s75, s36, 3
s_cmp_eq_u32 s75, 1
s_cbranch_scc1 .Lqa_w2
s_cmp_eq_u32 s75, 2
s_cbranch_scc1 .Lqa_w3
s_cmp_eq_u32 s75, 3
s_cbranch_scc1 .Lqa_w4
.Lqa_w1:
s_getpc_b64 s[90:91]
.Lqa_pc23:
s_add_u32 s90, s90, .Lqa_e8-.Lqa_pc23
s_addc_u32 s91, s91, 0
s_branch .Lqa_k15
.Lqa_bk22:
v_add_u32 v255, 0x10000, v9
v_add_u32 v162, 0x20000, v9
v_add_u32 v163, 0x10000, v10
v_add_u32 v164, 0x20000, v10
v_mov_b32 v106, 0x3F803F80
v_mov_b32 v107, 0x3F803F80
v_mov_b32 v108, 0x3F803F80
v_mov_b32 v109, 0x3F803F80
v_mov_b32 v141, 0
v_mov_b32 v145, 0
v_mov_b32 v149, 0
v_mov_b32 v153, 0
v_mov_b32 v165, 46080
s_waitcnt lgkmcnt(0)
s_mul_i32 s37, s37, 0x9E3779B1
s_add_u32 s37, s37, 0x7F4A7C15
v_add_u32 v46, s37, v46
v_add_u32 v48, s37, v48
v_add_u32 v50, s37, v50
ds_write_b32 v165, v141
s_mov_b64 s[76:77], 0
s_mov_b32 s73, 18
s_add_u32 m0, s48, 4096
s_nop 0
global_load_lds_dwordx4 v0, s[40:41] offset:-4096
global_load_lds_dwordx4 v1, s[40:41] offset:-3072
global_load_lds_dwordx4 v2, s[40:41] offset:-2048
global_load_lds_dwordx4 v3, s[40:41] offset:-1024
global_load_lds_dwordx4 v4, s[40:41] offset:0
global_load_lds_dwordx4 v5, s[40:41] offset:1024
global_load_lds_dwordx4 v6, s[40:41] offset:2048
global_load_lds_dwordx4 v7, s[40:41] offset:3072
s_add_u32 m0, s48, 12288
s_nop 0
global_load_lds_dwordx4 v8, s[40:41] offset:-4096
s_add_u32 s40, s40, 221184
s_addc_u32 s41, s41, 0
s_add_u32 m0, s48, 40960
s_nop 0
global_load_lds_dwordx4 v0, s[40:41] offset:-4096
global_load_lds_dwordx4 v1, s[40:41] offset:-3072
global_load_lds_dwordx4 v2, s[40:41] offset:-2048
global_load_lds_dwordx4 v3, s[40:41] offset:-1024
global_load_lds_dwordx4 v4, s[40:41] offset:0
global_load_lds_dwordx4 v5, s[40:41] offset:1024
global_load_lds_dwordx4 v6, s[40:41] offset:2048
global_load_lds_dwordx4 v7, s[40:41] offset:3072
s_add_u32 m0, s48, 49152
s_nop 0
global_load_lds_dwordx4 v8, s[40:41] offset:-4096
s_add_u32 s40, s40, 221184
s_addc_u32 s41, s41, 0
s_waitcnt vmcnt(9) lgkmcnt(0)
s_barrier
s_add_u32 m0, s48, 77824
ds_read_b64_tr_b16 a[0:1], v9 offset:47104
ds_read_b64_tr_b16 a[2:3], v9 offset:49408
global_load_lds_dwordx4 v0, s[40:41] offset:-4096
ds_read_b64_tr_b16 a[4:5], v9 offset:47296
ds_read_b64_tr_b16 a[6:7], v9 offset:49600
global_load_lds_dwordx4 v1, s[40:41] offset:-3072
ds_read_b64_tr_b16 a[8:9], v9 offset:47488
global_load_lds_dwordx4 v2, s[40:41] offset:-2048
ds_read_b64_tr_b16 a[10:11], v9 offset:49792
ds_read_b64_tr_b16 a[216:217], v10 offset:47104
global_load_lds_dwordx4 v3, s[40:41] offset:-1024
ds_read_b64_tr_b16 a[218:219], v10 offset:49408
global_load_lds_dwordx4 v4, s[40:41] offset:0
ds_read_b128 v[166:169], v51 offset:0
ds_read_b128 v[174:177], v51 offset:18432
global_load_lds_dwordx4 v5, s[40:41] offset:1024
ds_read_b64_tr_b16 a[12:13], v255 offset:0
global_load_lds_dwordx4 v6, s[40:41] offset:2048
ds_read_b64_tr_b16 a[14:15], v255 offset:2304
ds_read_b64_tr_b16 a[16:17], v255 offset:192
global_load_lds_dwordx4 v7, s[40:41] offset:3072
ds_read_b64_tr_b16 a[18:19], v255 offset:2496
s_add_u32 m0, s48, 86016
ds_read_b64_tr_b16 a[20:21], v255 offset:384
ds_read_b64_tr_b16 a[22:23], v255 offset:2688
global_load_lds_dwordx4 v8, s[40:41] offset:-4096
ds_read_b64_tr_b16 a[220:221], v163 offset:0
s_add_u32 s40, s40, 221184
ds_read_b64_tr_b16 a[222:223], v163 offset:2304
ds_read_b128 v[170:173], v52 offset:0
s_addc_u32 s41, s41, 0
ds_read_b128 v[178:181], v52 offset:18432
s_waitcnt vmcnt(9) lgkmcnt(0)
s_barrier
s_add_u32 m0, s48, 4096
ds_read_b64_tr_b16 a[24:25], v255 offset:18432
v_mfma_f32_16x16x32_bf16 v[86:89], a[0:3], v[106:109], 0
ds_read_b64_tr_b16 a[26:27], v255 offset:20736
v_mfma_f32_16x16x32_bf16 v[90:93], a[4:7], v[106:109], 0
global_load_lds_dwordx4 v0, s[40:41] offset:-4096
ds_read_b64_tr_b16 a[28:29], v255 offset:18624
v_mfma_f32_16x16x32_bf16 v[94:97], v[106:109], a[8:11], 0
ds_read_b64_tr_b16 a[30:31], v255 offset:20928
global_load_lds_dwordx4 v1, s[40:41] offset:-3072
v_mfma_f32_16x16x32_bf16 v[98:101], a[216:219], v[106:109], 0
ds_read_b64_tr_b16 a[32:33], v255 offset:18816
v_mfma_f32_16x16x32_bf16 v[110:113], a[0:3], v[166:169], 0
ds_read_b64_tr_b16 a[34:35], v255 offset:21120
global_load_lds_dwordx4 v2, s[40:41] offset:-2048
v_mfma_f32_16x16x32_bf16 v[114:117], a[4:7], v[166:169], 0
ds_read_b64_tr_b16 a[224:225], v163 offset:18432
v_mfma_f32_16x16x32_bf16 v[118:121], v[166:169], a[8:11], 0
global_load_lds_dwordx4 v3, s[40:41] offset:-1024
ds_read_b64_tr_b16 a[226:227], v163 offset:20736
v_mfma_f32_16x16x32_bf16 v[134:137], a[216:219], v[166:169], 0
ds_read_b128 v[182:185], v51 offset:2048
global_load_lds_dwordx4 v4, s[40:41] offset:0
v_mfma_f32_16x16x32_bf16 v[122:125], a[0:3], v[174:177], 0
ds_read_b128 v[190:193], v51 offset:20480
v_mfma_f32_16x16x32_bf16 v[126:129], a[4:7], v[174:177], 0
ds_read_b64_tr_b16 a[36:37], v255 offset:36864
global_load_lds_dwordx4 v5, s[40:41] offset:1024
v_mfma_f32_16x16x32_bf16 v[130:133], v[174:177], a[8:11], 0
ds_read_b64_tr_b16 a[38:39], v255 offset:39168
v_mfma_f32_16x16x32_bf16 v[158:161], a[216:219], v[174:177], 0
global_load_lds_dwordx4 v6, s[40:41] offset:2048
ds_read_b64_tr_b16 a[40:41], v255 offset:37056
v_mfma_f32_16x16x32_bf16 v[86:89], a[12:15], v[106:109], v[86:89]
ds_read_b64_tr_b16 a[42:43], v255 offset:39360
v_mfma_f32_16x16x32_bf16 v[90:93], a[16:19], v[106:109], v[90:93]
global_load_lds_dwordx4 v7, s[40:41] offset:3072
ds_read_b64_tr_b16 a[44:45], v255 offset:37248
v_mfma_f32_16x16x32_bf16 v[94:97], v[106:109], a[20:23], v[94:97]
s_add_u32 m0, s48, 12288
ds_read_b64_tr_b16 a[46:47], v255 offset:39552
v_mfma_f32_16x16x32_bf16 v[98:101], a[220:223], v[106:109], v[98:101]
ds_read_b64_tr_b16 a[228:229], v163 offset:36864
v_mfma_f32_16x16x32_bf16 v[110:113], a[12:15], v[170:173], v[110:113]
global_load_lds_dwordx4 v8, s[40:41] offset:-4096
ds_read_b64_tr_b16 a[230:231], v163 offset:39168
v_mfma_f32_16x16x32_bf16 v[114:117], a[16:19], v[170:173], v[114:117]
ds_read_b128 v[186:189], v52 offset:2048
v_mfma_f32_16x16x32_bf16 v[118:121], v[170:173], a[20:23], v[118:121]
s_add_u32 s40, s40, 221184
ds_read_b128 v[194:197], v52 offset:20480
v_mfma_f32_16x16x32_bf16 v[134:137], a[220:223], v[170:173], v[134:137]
v_mfma_f32_16x16x32_bf16 v[122:125], a[12:15], v[178:181], v[122:125]
s_addc_u32 s41, s41, 0
v_mfma_f32_16x16x32_bf16 v[126:129], a[16:19], v[178:181], v[126:129]
v_mfma_f32_16x16x32_bf16 v[130:133], v[178:181], a[20:23], v[130:133]
v_mfma_f32_16x16x32_bf16 v[158:161], a[220:223], v[178:181], v[158:161]
s_waitcnt vmcnt(9) lgkmcnt(0)
s_barrier
s_add_u32 m0, s48, 40960
ds_read_b64_tr_b16 a[48:49], v255 offset:55296
v_mfma_f32_16x16x32_bf16 v[86:89], a[24:27], v[106:109], v[86:89]
ds_read_b64_tr_b16 a[50:51], v255 offset:57600
v_mfma_f32_16x16x32_bf16 v[90:93], a[28:31], v[106:109], v[90:93]
global_load_lds_dwordx4 v0, s[40:41] offset:-4096
ds_read_b64_tr_b16 a[52:53], v255 offset:55488
v_mfma_f32_16x16x32_bf16 v[94:97], v[106:109], a[32:35], v[94:97]
ds_read_b64_tr_b16 a[54:55], v255 offset:57792
global_load_lds_dwordx4 v1, s[40:41] offset:-3072
v_mfma_f32_16x16x32_bf16 v[98:101], a[224:227], v[106:109], v[98:101]
ds_read_b64_tr_b16 a[56:57], v255 offset:55680
v_mfma_f32_16x16x32_bf16 v[110:113], a[24:27], v[182:185], v[110:113]
ds_read_b64_tr_b16 a[58:59], v255 offset:57984
global_load_lds_dwordx4 v2, s[40:41] offset:-2048
v_mfma_f32_16x16x32_bf16 v[114:117], a[28:31], v[182:185], v[114:117]
ds_read_b64_tr_b16 a[232:233], v163 offset:55296
v_mfma_f32_16x16x32_bf16 v[118:121], v[182:185], a[32:35], v[118:121]
global_load_lds_dwordx4 v3, s[40:41] offset:-1024
ds_read_b64_tr_b16 a[234:235], v163 offset:57600
v_mfma_f32_16x16x32_bf16 v[134:137], a[224:227], v[182:185], v[134:137]
ds_read_b128 v[166:169], v51 offset:4096
global_load_lds_dwordx4 v4, s[40:41] offset:0
v_mfma_f32_16x16x32_bf16 v[122:125], a[24:27], v[190:193], v[122:125]
ds_read_b128 v[174:177], v51 offset:22528
v_mfma_f32_16x16x32_bf16 v[126:129], a[28:31], v[190:193], v[126:129]
ds_read_b64_tr_b16 a[60:61], v162 offset:8192
global_load_lds_dwordx4 v5, s[40:41] offset:1024
v_mfma_f32_16x16x32_bf16 v[130:133], v[190:193], a[32:35], v[130:133]
ds_read_b64_tr_b16 a[62:63], v162 offset:10496
v_mfma_f32_16x16x32_bf16 v[158:161], a[224:227], v[190:193], v[158:161]
global_load_lds_dwordx4 v6, s[40:41] offset:2048
ds_read_b64_tr_b16 a[64:65], v162 offset:8384
v_mfma_f32_16x16x32_bf16 v[86:89], a[36:39], v[106:109], v[86:89]
ds_read_b64_tr_b16 a[66:67], v162 offset:10688
v_mfma_f32_16x16x32_bf16 v[90:93], a[40:43], v[106:109], v[90:93]
global_load_lds_dwordx4 v7, s[40:41] offset:3072
ds_read_b64_tr_b16 a[68:69], v162 offset:8576
v_mfma_f32_16x16x32_bf16 v[94:97], v[106:109], a[44:47], v[94:97]
s_add_u32 m0, s48, 49152
ds_read_b64_tr_b16 a[70:71], v162 offset:10880
v_mfma_f32_16x16x32_bf16 v[98:101], a[228:231], v[106:109], v[98:101]
ds_read_b64_tr_b16 a[236:237], v164 offset:8192
v_mfma_f32_16x16x32_bf16 v[110:113], a[36:39], v[186:189], v[110:113]
global_load_lds_dwordx4 v8, s[40:41] offset:-4096
ds_read_b64_tr_b16 a[238:239], v164 offset:10496
v_mfma_f32_16x16x32_bf16 v[114:117], a[40:43], v[186:189], v[114:117]
ds_read_b128 v[170:173], v52 offset:4096
v_mfma_f32_16x16x32_bf16 v[118:121], v[186:189], a[44:47], v[118:121]
s_add_u32 s40, s40, 221184
ds_read_b128 v[178:181], v52 offset:22528
v_mfma_f32_16x16x32_bf16 v[134:137], a[228:231], v[186:189], v[134:137]
v_mfma_f32_16x16x32_bf16 v[122:125], a[36:39], v[194:197], v[122:125]
s_addc_u32 s41, s41, 0
v_mfma_f32_16x16x32_bf16 v[126:129], a[40:43], v[194:197], v[126:129]
v_mfma_f32_16x16x32_bf16 v[130:133], v[194:197], a[44:47], v[130:133]
v_mfma_f32_16x16x32_bf16 v[158:161], a[228:231], v[194:197], v[158:161]
s_waitcnt vmcnt(9) lgkmcnt(0)
s_barrier
s_add_u32 m0, s48, 77824
ds_read_b64_tr_b16 a[72:73], v9 offset:47104
v_mfma_f32_16x16x32_bf16 v[86:89], a[48:51], v[106:109], v[86:89]
ds_read_b64_tr_b16 a[74:75], v9 offset:49408
v_mfma_f32_16x16x32_bf16 v[90:93], a[52:55], v[106:109], v[90:93]
global_load_lds_dwordx4 v0, s[40:41] offset:-4096
ds_read_b64_tr_b16 a[76:77], v9 offset:47296
v_mfma_f32_16x16x32_bf16 v[94:97], v[106:109], a[56:59], v[94:97]
ds_read_b64_tr_b16 a[78:79], v9 offset:49600
global_load_lds_dwordx4 v1, s[40:41] offset:-3072
v_mfma_f32_16x16x32_bf16 v[98:101], a[232:235], v[106:109], v[98:101]
ds_read_b64_tr_b16 a[80:81], v9 offset:47488
v_mfma_f32_16x16x32_bf16 v[110:113], a[48:51], v[166:169], v[110:113]
ds_read_b64_tr_b16 a[82:83], v9 offset:49792
global_load_lds_dwordx4 v2, s[40:41] offset:-2048
v_mfma_f32_16x16x32_bf16 v[114:117], a[52:55], v[166:169], v[114:117]
ds_read_b64_tr_b16 a[240:241], v10 offset:47104
v_mfma_f32_16x16x32_bf16 v[118:121], v[166:169], a[56:59], v[118:121]
global_load_lds_dwordx4 v3, s[40:41] offset:-1024
ds_read_b64_tr_b16 a[242:243], v10 offset:49408
v_mfma_f32_16x16x32_bf16 v[134:137], a[232:235], v[166:169], v[134:137]
ds_read_b128 v[182:185], v51 offset:6144
global_load_lds_dwordx4 v4, s[40:41] offset:0
v_mfma_f32_16x16x32_bf16 v[122:125], a[48:51], v[174:177], v[122:125]
ds_read_b128 v[190:193], v51 offset:24576
v_mfma_f32_16x16x32_bf16 v[126:129], a[52:55], v[174:177], v[126:129]
ds_read_b64_tr_b16 a[84:85], v255 offset:0
global_load_lds_dwordx4 v5, s[40:41] offset:1024
v_mfma_f32_16x16x32_bf16 v[130:133], v[174:177], a[56:59], v[130:133]
ds_read_b64_tr_b16 a[86:87], v255 offset:2304
v_mfma_f32_16x16x32_bf16 v[158:161], a[232:235], v[174:177], v[158:161]
global_load_lds_dwordx4 v6, s[40:41] offset:2048
ds_read_b64_tr_b16 a[88:89], v255 offset:192
v_mfma_f32_16x16x32_bf16 v[86:89], a[60:63], v[106:109], v[86:89]
ds_read_b64_tr_b16 a[90:91], v255 offset:2496
v_mfma_f32_16x16x32_bf16 v[90:93], a[64:67], v[106:109], v[90:93]
global_load_lds_dwordx4 v7, s[40:41] offset:3072
ds_read_b64_tr_b16 a[92:93], v255 offset:384
v_mfma_f32_16x16x32_bf16 v[94:97], v[106:109], a[68:71], v[94:97]
s_add_u32 m0, s48, 86016
ds_read_b64_tr_b16 a[94:95], v255 offset:2688
v_mfma_f32_16x16x32_bf16 v[98:101], a[236:239], v[106:109], v[98:101]
ds_read_b64_tr_b16 a[244:245], v163 offset:0
v_mfma_f32_16x16x32_bf16 v[110:113], a[60:63], v[170:173], v[110:113]
global_load_lds_dwordx4 v8, s[40:41] offset:-4096
ds_read_b64_tr_b16 a[246:247], v163 offset:2304
v_mfma_f32_16x16x32_bf16 v[114:117], a[64:67], v[170:173], v[114:117]
ds_read_b128 v[186:189], v52 offset:6144
v_mfma_f32_16x16x32_bf16 v[118:121], v[170:173], a[68:71], v[118:121]
s_add_u32 s40, s40, 221184
ds_read_b128 v[194:197], v52 offset:24576
v_mfma_f32_16x16x32_bf16 v[134:137], a[236:239], v[170:173], v[134:137]
v_mfma_f32_16x16x32_bf16 v[122:125], a[60:63], v[178:181], v[122:125]
s_addc_u32 s41, s41, 0
v_mfma_f32_16x16x32_bf16 v[126:129], a[64:67], v[178:181], v[126:129]
v_mfma_f32_16x16x32_bf16 v[130:133], v[178:181], a[68:71], v[130:133]
v_mfma_f32_16x16x32_bf16 v[158:161], a[236:239], v[178:181], v[158:161]
s_waitcnt vmcnt(9) lgkmcnt(0)
s_barrier
s_add_u32 m0, s48, 4096
ds_read_b64_tr_b16 a[96:97], v255 offset:18432
v_mfma_f32_16x16x32_bf16 v[86:89], a[72:75], v[106:109], v[86:89]
ds_read_b64_tr_b16 a[98:99], v255 offset:20736
v_mfma_f32_16x16x32_bf16 v[90:93], a[76:79], v[106:109], v[90:93]
global_load_lds_dwordx4 v0, s[40:41] offset:-4096
ds_read_b64_tr_b16 a[100:101], v255 offset:18624
v_mfma_f32_16x16x32_bf16 v[94:97], v[106:109], a[80:83], v[94:97]
ds_read_b64_tr_b16 a[102:103], v255 offset:20928
global_load_lds_dwordx4 v1, s[40:41] offset:-3072
v_mfma_f32_16x16x32_bf16 v[98:101], a[240:243], v[106:109], v[98:101]
ds_read_b64_tr_b16 a[104:105], v255 offset:18816
v_mfma_f32_16x16x32_bf16 v[110:113], a[72:75], v[182:185], v[110:113]
ds_read_b64_tr_b16 a[106:107], v255 offset:21120
global_load_lds_dwordx4 v2, s[40:41] offset:-2048
v_mfma_f32_16x16x32_bf16 v[114:117], a[76:79], v[182:185], v[114:117]
ds_read_b64_tr_b16 a[248:249], v163 offset:18432
v_mfma_f32_16x16x32_bf16 v[118:121], v[182:185], a[80:83], v[118:121]
global_load_lds_dwordx4 v3, s[40:41] offset:-1024
ds_read_b64_tr_b16 a[250:251], v163 offset:20736
v_mfma_f32_16x16x32_bf16 v[134:137], a[240:243], v[182:185], v[134:137]
ds_read_b128 v[166:169], v51 offset:8192
global_load_lds_dwordx4 v4, s[40:41] offset:0
v_mfma_f32_16x16x32_bf16 v[122:125], a[72:75], v[190:193], v[122:125]
ds_read_b128 v[174:177], v51 offset:26624
v_mfma_f32_16x16x32_bf16 v[126:129], a[76:79], v[190:193], v[126:129]
ds_read_b64_tr_b16 a[108:109], v255 offset:36864
global_load_lds_dwordx4 v5, s[40:41] offset:1024
v_mfma_f32_16x16x32_bf16 v[130:133], v[190:193], a[80:83], v[130:133]
ds_read_b64_tr_b16 a[110:111], v255 offset:39168
v_mfma_f32_16x16x32_bf16 v[158:161], a[240:243], v[190:193], v[158:161]
global_load_lds_dwordx4 v6, s[40:41] offset:2048
ds_read_b64_tr_b16 a[112:113], v255 offset:37056
v_mfma_f32_16x16x32_bf16 v[86:89], a[84:87], v[106:109], v[86:89]
ds_read_b64_tr_b16 a[114:115], v255 offset:39360
v_mfma_f32_16x16x32_bf16 v[90:93], a[88:91], v[106:109], v[90:93]
global_load_lds_dwordx4 v7, s[40:41] offset:3072
ds_read_b64_tr_b16 a[116:117], v255 offset:37248
v_mfma_f32_16x16x32_bf16 v[94:97], v[106:109], a[92:95], v[94:97]
s_add_u32 m0, s48, 12288
ds_read_b64_tr_b16 a[118:119], v255 offset:39552
v_mfma_f32_16x16x32_bf16 v[98:101], a[244:247], v[106:109], v[98:101]
ds_read_b64_tr_b16 a[252:253], v163 offset:36864
v_mfma_f32_16x16x32_bf16 v[110:113], a[84:87], v[186:189], v[110:113]
global_load_lds_dwordx4 v8, s[40:41] offset:-4096
ds_read_b64_tr_b16 a[254:255], v163 offset:39168
v_mfma_f32_16x16x32_bf16 v[114:117], a[88:91], v[186:189], v[114:117]
ds_read_b128 v[170:173], v52 offset:8192
v_mfma_f32_16x16x32_bf16 v[118:121], v[186:189], a[92:95], v[118:121]
s_add_u32 s40, s40, 221184
ds_read_b128 v[178:181], v52 offset:26624
v_mfma_f32_16x16x32_bf16 v[134:137], a[244:247], v[186:189], v[134:137]
v_mfma_f32_16x16x32_bf16 v[122:125], a[84:87], v[194:197], v[122:125]
s_addc_u32 s41, s41, 0
v_mfma_f32_16x16x32_bf16 v[126:129], a[88:91], v[194:197], v[126:129]
v_mfma_f32_16x16x32_bf16 v[130:133], v[194:197], a[92:95], v[130:133]
v_mfma_f32_16x16x32_bf16 v[158:161], a[244:247], v[194:197], v[158:161]
s_waitcnt vmcnt(9) lgkmcnt(0)
s_barrier
s_add_u32 m0, s48, 40960
ds_read_b64_tr_b16 a[120:121], v255 offset:55296
v_mfma_f32_16x16x32_bf16 v[86:89], a[96:99], v[106:109], v[86:89]
ds_read_b64_tr_b16 a[122:123], v255 offset:57600
v_mfma_f32_16x16x32_bf16 v[90:93], a[100:103], v[106:109], v[90:93]
global_load_lds_dwordx4 v0, s[40:41] offset:-4096
ds_read_b64_tr_b16 a[124:125], v255 offset:55488
v_mfma_f32_16x16x32_bf16 v[94:97], v[106:109], a[104:107], v[94:97]
ds_read_b64_tr_b16 a[126:127], v255 offset:57792
global_load_lds_dwordx4 v1, s[40:41] offset:-3072
v_mfma_f32_16x16x32_bf16 v[98:101], a[248:251], v[106:109], v[98:101]
ds_read_b64_tr_b16 a[128:129], v255 offset:55680
v_mfma_f32_16x16x32_bf16 v[110:113], a[96:99], v[166:169], v[110:113]
ds_read_b64_tr_b16 a[130:131], v255 offset:57984
global_load_lds_dwordx4 v2, s[40:41] offset:-2048
v_mfma_f32_16x16x32_bf16 v[114:117], a[100:103], v[166:169], v[114:117]
ds_read_b64_tr_b16 v[54:55], v163 offset:55296
v_mfma_f32_16x16x32_bf16 v[118:121], v[166:169], a[104:107], v[118:121]
global_load_lds_dwordx4 v3, s[40:41] offset:-1024
ds_read_b64_tr_b16 v[56:57], v163 offset:57600
v_mfma_f32_16x16x32_bf16 v[134:137], a[248:251], v[166:169], v[134:137]
ds_read_b128 v[182:185], v51 offset:10240
global_load_lds_dwordx4 v4, s[40:41] offset:0
v_mfma_f32_16x16x32_bf16 v[122:125], a[96:99], v[174:177], v[122:125]
ds_read_b128 v[190:193], v51 offset:28672
v_mfma_f32_16x16x32_bf16 v[126:129], a[100:103], v[174:177], v[126:129]
ds_read_b64_tr_b16 a[132:133], v162 offset:8192
global_load_lds_dwordx4 v5, s[40:41] offset:1024
v_mfma_f32_16x16x32_bf16 v[130:133], v[174:177], a[104:107], v[130:133]
ds_read_b64_tr_b16 a[134:135], v162 offset:10496
v_mfma_f32_16x16x32_bf16 v[158:161], a[248:251], v[174:177], v[158:161]
global_load_lds_dwordx4 v6, s[40:41] offset:2048
ds_read_b64_tr_b16 a[136:137], v162 offset:8384
v_mfma_f32_16x16x32_bf16 v[86:89], a[108:111], v[106:109], v[86:89]
ds_read_b64_tr_b16 a[138:139], v162 offset:10688
v_mfma_f32_16x16x32_bf16 v[90:93], a[112:115], v[106:109], v[90:93]
global_load_lds_dwordx4 v7, s[40:41] offset:3072
ds_read_b64_tr_b16 a[140:141], v162 offset:8576
v_mfma_f32_16x16x32_bf16 v[94:97], v[106:109], a[116:119], v[94:97]
s_add_u32 m0, s48, 49152
ds_read_b64_tr_b16 a[142:143], v162 offset:10880
v_mfma_f32_16x16x32_bf16 v[98:101], a[252:255], v[106:109], v[98:101]
ds_read_b64_tr_b16 v[58:59], v164 offset:8192
v_mfma_f32_16x16x32_bf16 v[110:113], a[108:111], v[170:173], v[110:113]
global_load_lds_dwordx4 v8, s[40:41] offset:-4096
ds_read_b64_tr_b16 v[60:61], v164 offset:10496
v_mfma_f32_16x16x32_bf16 v[114:117], a[112:115], v[170:173], v[114:117]
ds_read_b128 v[186:189], v52 offset:10240
v_mfma_f32_16x16x32_bf16 v[118:121], v[170:173], a[116:119], v[118:121]
s_add_u32 s40, s40, 221184
ds_read_b128 v[194:197], v52 offset:28672
v_mfma_f32_16x16x32_bf16 v[134:137], a[252:255], v[170:173], v[134:137]
v_mfma_f32_16x16x32_bf16 v[122:125], a[108:111], v[178:181], v[122:125]
s_addc_u32 s41, s41, 0
v_mfma_f32_16x16x32_bf16 v[126:129], a[112:115], v[178:181], v[126:129]
)"
R"(v_mfma_f32_16x16x32_bf16 v[130:133], v[178:181], a[116:119], v[130:133]
v_mfma_f32_16x16x32_bf16 v[158:161], a[252:255], v[178:181], v[158:161]
s_waitcnt vmcnt(9) lgkmcnt(0)
s_barrier
s_add_u32 m0, s48, 77824
ds_read_b64_tr_b16 a[144:145], v9 offset:47104
v_mfma_f32_16x16x32_bf16 v[86:89], a[120:123], v[106:109], v[86:89]
ds_read_b64_tr_b16 a[146:147], v9 offset:49408
v_mfma_f32_16x16x32_bf16 v[90:93], a[124:127], v[106:109], v[90:93]
global_load_lds_dwordx4 v0, s[40:41] offset:-4096
ds_read_b64_tr_b16 a[148:149], v9 offset:47296
v_mfma_f32_16x16x32_bf16 v[94:97], v[106:109], a[128:131], v[94:97]
ds_read_b64_tr_b16 a[150:151], v9 offset:49600
global_load_lds_dwordx4 v1, s[40:41] offset:-3072
v_mfma_f32_16x16x32_bf16 v[98:101], v[54:57], v[106:109], v[98:101]
ds_read_b64_tr_b16 a[152:153], v9 offset:47488
v_mfma_f32_16x16x32_bf16 v[110:113], a[120:123], v[182:185], v[110:113]
ds_read_b64_tr_b16 a[154:155], v9 offset:49792
global_load_lds_dwordx4 v2, s[40:41] offset:-2048
v_mfma_f32_16x16x32_bf16 v[114:117], a[124:127], v[182:185], v[114:117]
ds_read_b64_tr_b16 v[62:63], v10 offset:47104
v_mfma_f32_16x16x32_bf16 v[118:121], v[182:185], a[128:131], v[118:121]
global_load_lds_dwordx4 v3, s[40:41] offset:-1024
ds_read_b64_tr_b16 v[64:65], v10 offset:49408
v_mfma_f32_16x16x32_bf16 v[134:137], v[54:57], v[182:185], v[134:137]
ds_read_b128 v[166:169], v51 offset:12288
global_load_lds_dwordx4 v4, s[40:41] offset:0
v_mfma_f32_16x16x32_bf16 v[122:125], a[120:123], v[190:193], v[122:125]
ds_read_b128 v[174:177], v51 offset:30720
v_mfma_f32_16x16x32_bf16 v[126:129], a[124:127], v[190:193], v[126:129]
ds_read_b64_tr_b16 a[156:157], v255 offset:0
global_load_lds_dwordx4 v5, s[40:41] offset:1024
v_mfma_f32_16x16x32_bf16 v[130:133], v[190:193], a[128:131], v[130:133]
ds_read_b64_tr_b16 a[158:159], v255 offset:2304
v_mfma_f32_16x16x32_bf16 v[158:161], v[54:57], v[190:193], v[158:161]
global_load_lds_dwordx4 v6, s[40:41] offset:2048
ds_read_b64_tr_b16 a[160:161], v255 offset:192
v_mfma_f32_16x16x32_bf16 v[86:89], a[132:135], v[106:109], v[86:89]
ds_read_b64_tr_b16 a[162:163], v255 offset:2496
v_mfma_f32_16x16x32_bf16 v[90:93], a[136:139], v[106:109], v[90:93]
global_load_lds_dwordx4 v7, s[40:41] offset:3072
ds_read_b64_tr_b16 a[164:165], v255 offset:384
v_mfma_f32_16x16x32_bf16 v[94:97], v[106:109], a[140:143], v[94:97]
s_add_u32 m0, s48, 86016
ds_read_b64_tr_b16 a[166:167], v255 offset:2688
v_mfma_f32_16x16x32_bf16 v[98:101], v[58:61], v[106:109], v[98:101]
ds_read_b64_tr_b16 v[66:67], v163 offset:0
v_mfma_f32_16x16x32_bf16 v[110:113], a[132:135], v[186:189], v[110:113]
global_load_lds_dwordx4 v8, s[40:41] offset:-4096
ds_read_b64_tr_b16 v[68:69], v163 offset:2304
v_mfma_f32_16x16x32_bf16 v[114:117], a[136:139], v[186:189], v[114:117]
ds_read_b128 v[170:173], v52 offset:12288
v_mfma_f32_16x16x32_bf16 v[118:121], v[186:189], a[140:143], v[118:121]
s_sub_u32 s40, s40, 1769472
ds_read_b128 v[178:181], v52 offset:30720
v_mfma_f32_16x16x32_bf16 v[134:137], v[58:61], v[186:189], v[134:137]
v_mfma_f32_16x16x32_bf16 v[122:125], a[132:135], v[194:197], v[122:125]
s_subb_u32 s41, s41, 0
v_mfma_f32_16x16x32_bf16 v[126:129], a[136:139], v[194:197], v[126:129]
v_mfma_f32_16x16x32_bf16 v[130:133], v[194:197], a[140:143], v[130:133]
v_mfma_f32_16x16x32_bf16 v[158:161], v[58:61], v[194:197], v[158:161]
s_waitcnt vmcnt(9) lgkmcnt(0)
s_barrier
s_add_u32 m0, s49, 0
ds_read_b64_tr_b16 a[168:169], v255 offset:18432
v_mfma_f32_16x16x32_bf16 v[86:89], a[144:147], v[106:109], v[86:89]
ds_read_b64_tr_b16 a[170:171], v255 offset:20736
v_mfma_f32_16x16x32_bf16 v[90:93], a[148:151], v[106:109], v[90:93]
ds_read_b64_tr_b16 a[172:173], v255 offset:18624
v_mfma_f32_16x16x32_bf16 v[94:97], v[106:109], a[152:155], v[94:97]
ds_read_b64_tr_b16 a[174:175], v255 offset:20928
global_load_lds_dwordx4 v11, s[42:43] offset:-2048 sc1
v_mfma_f32_16x16x32_bf16 v[98:101], v[62:65], v[106:109], v[98:101]
ds_read_b64_tr_b16 a[176:177], v255 offset:18816
v_mfma_f32_16x16x32_bf16 v[110:113], a[144:147], v[166:169], v[110:113]
ds_read_b64_tr_b16 a[178:179], v255 offset:21120
v_mfma_f32_16x16x32_bf16 v[114:117], a[148:151], v[166:169], v[114:117]
ds_read_b64_tr_b16 v[70:71], v163 offset:18432
global_load_lds_dwordx4 v12, s[42:43] offset:-1024 sc1
v_mfma_f32_16x16x32_bf16 v[118:121], v[166:169], a[152:155], v[118:121]
ds_read_b64_tr_b16 v[72:73], v163 offset:20736
v_mfma_f32_16x16x32_bf16 v[134:137], v[62:65], v[166:169], v[134:137]
ds_read_b128 v[182:185], v51 offset:14336
v_mfma_f32_16x16x32_bf16 v[122:125], a[144:147], v[174:177], v[122:125]
ds_read_b128 v[190:193], v51 offset:32768
global_load_lds_dwordx4 v13, s[42:43] offset:0 sc1
v_mfma_f32_16x16x32_bf16 v[126:129], a[148:151], v[174:177], v[126:129]
ds_read_b64_tr_b16 a[180:181], v255 offset:36864
v_mfma_f32_16x16x32_bf16 v[130:133], v[174:177], a[152:155], v[130:133]
ds_read_b64_tr_b16 a[182:183], v255 offset:39168
v_mfma_f32_16x16x32_bf16 v[158:161], v[62:65], v[174:177], v[158:161]
ds_read_b64_tr_b16 a[184:185], v255 offset:37056
v_mfma_f32_16x16x32_bf16 v[86:89], a[156:159], v[106:109], v[86:89]
global_load_lds_dwordx4 v14, s[42:43] offset:1024 sc1
ds_read_b64_tr_b16 a[186:187], v255 offset:39360
v_mfma_f32_16x16x32_bf16 v[90:93], a[160:163], v[106:109], v[90:93]
ds_read_b64_tr_b16 a[188:189], v255 offset:37248
v_mfma_f32_16x16x32_bf16 v[94:97], v[106:109], a[164:167], v[94:97]
ds_read_b64_tr_b16 a[190:191], v255 offset:39552
v_mfma_f32_16x16x32_bf16 v[98:101], v[66:69], v[106:109], v[98:101]
global_load_lds_dwordx4 v15, s[42:43] offset:2048 sc1
ds_read_b64_tr_b16 v[74:75], v163 offset:36864
v_mfma_f32_16x16x32_bf16 v[110:113], a[156:159], v[170:173], v[110:113]
ds_read_b64_tr_b16 v[76:77], v163 offset:39168
v_mfma_f32_16x16x32_bf16 v[114:117], a[160:163], v[170:173], v[114:117]
ds_read_b128 v[186:189], v52 offset:14336
v_mfma_f32_16x16x32_bf16 v[118:121], v[170:173], a[164:167], v[118:121]
s_sub_u32 s42, s42, 18432
s_subb_u32 s43, s43, 0
ds_read_b128 v[194:197], v52 offset:32768
v_mfma_f32_16x16x32_bf16 v[134:137], v[66:69], v[170:173], v[134:137]
v_mfma_f32_16x16x32_bf16 v[122:125], a[156:159], v[178:181], v[122:125]
v_mfma_f32_16x16x32_bf16 v[126:129], a[160:163], v[178:181], v[126:129]
v_mfma_f32_16x16x32_bf16 v[130:133], v[178:181], a[164:167], v[130:133]
v_mfma_f32_16x16x32_bf16 v[158:161], v[66:69], v[178:181], v[158:161]
s_waitcnt vmcnt(5) lgkmcnt(0)
s_barrier
s_add_u32 m0, s49, 18432
ds_read_b64_tr_b16 a[192:193], v255 offset:55296
v_mfma_f32_16x16x32_bf16 v[86:89], a[168:171], v[106:109], v[86:89]
ds_read_b64_tr_b16 a[194:195], v255 offset:57600
v_mfma_f32_16x16x32_bf16 v[90:93], a[172:175], v[106:109], v[90:93]
ds_read_b64_tr_b16 a[196:197], v255 offset:55488
v_mfma_f32_16x16x32_bf16 v[94:97], v[106:109], a[176:179], v[94:97]
ds_read_b64_tr_b16 a[198:199], v255 offset:57792
global_load_lds_dwordx4 v11, s[42:43] offset:-2048 sc1
v_mfma_f32_16x16x32_bf16 v[98:101], v[70:73], v[106:109], v[98:101]
ds_read_b64_tr_b16 a[200:201], v255 offset:55680
v_mfma_f32_16x16x32_bf16 v[110:113], a[168:171], v[182:185], v[110:113]
ds_read_b64_tr_b16 a[202:203], v255 offset:57984
v_mfma_f32_16x16x32_bf16 v[114:117], a[172:175], v[182:185], v[114:117]
ds_read_b64_tr_b16 v[78:79], v163 offset:55296
global_load_lds_dwordx4 v12, s[42:43] offset:-1024 sc1
v_mfma_f32_16x16x32_bf16 v[118:121], v[182:185], a[176:179], v[118:121]
ds_read_b64_tr_b16 v[80:81], v163 offset:57600
v_mfma_f32_16x16x32_bf16 v[134:137], v[70:73], v[182:185], v[134:137]
ds_read_b128 v[166:169], v51 offset:16384
v_mfma_f32_16x16x32_bf16 v[122:125], a[168:171], v[190:193], v[122:125]
ds_read_b128 v[174:177], v51 offset:34816
global_load_lds_dwordx4 v13, s[42:43] offset:0 sc1
v_mfma_f32_16x16x32_bf16 v[126:129], a[172:175], v[190:193], v[126:129]
ds_read_b64_tr_b16 a[204:205], v162 offset:8192
v_mfma_f32_16x16x32_bf16 v[130:133], v[190:193], a[176:179], v[130:133]
ds_read_b64_tr_b16 a[206:207], v162 offset:10496
v_mfma_f32_16x16x32_bf16 v[158:161], v[70:73], v[190:193], v[158:161]
ds_read_b64_tr_b16 a[208:209], v162 offset:8384
global_load_lds_dwordx4 v14, s[42:43] offset:1024 sc1
v_mfma_f32_16x16x32_bf16 v[86:89], a[180:183], v[106:109], v[86:89]
ds_read_b64_tr_b16 a[210:211], v162 offset:10688
v_mfma_f32_16x16x32_bf16 v[90:93], a[184:187], v[106:109], v[90:93]
ds_read_b64_tr_b16 a[212:213], v162 offset:8576
v_mfma_f32_16x16x32_bf16 v[94:97], v[106:109], a[188:191], v[94:97]
ds_read_b64_tr_b16 a[214:215], v162 offset:10880
global_load_lds_dwordx4 v15, s[42:43] offset:2048 sc1
v_mfma_f32_16x16x32_bf16 v[98:101], v[74:77], v[106:109], v[98:101]
ds_read_b64_tr_b16 v[82:83], v164 offset:8192
v_mfma_f32_16x16x32_bf16 v[110:113], a[180:183], v[186:189], v[110:113]
ds_read_b64_tr_b16 v[84:85], v164 offset:10496
v_mfma_f32_16x16x32_bf16 v[114:117], a[184:187], v[186:189], v[114:117]
ds_read_b128 v[170:173], v52 offset:16384
s_sub_u32 s42, s42, 18432
s_subb_u32 s43, s43, 0
v_mfma_f32_16x16x32_bf16 v[118:121], v[186:189], a[188:191], v[118:121]
ds_read_b128 v[178:181], v52 offset:34816
v_mfma_f32_16x16x32_bf16 v[134:137], v[74:77], v[186:189], v[134:137]
v_mfma_f32_16x16x32_bf16 v[122:125], a[180:183], v[194:197], v[122:125]
v_mfma_f32_16x16x32_bf16 v[126:129], a[184:187], v[194:197], v[126:129]
v_mfma_f32_16x16x32_bf16 v[158:161], v[74:77], v[194:197], v[158:161]
s_waitcnt lgkmcnt(15)
v_mfma_f32_16x16x32_bf16 v[86:89], a[192:195], v[106:109], v[86:89]
v_mfma_f32_16x16x32_bf16 v[90:93], a[196:199], v[106:109], v[90:93]
s_waitcnt lgkmcnt(14)
v_mfma_f32_16x16x32_bf16 v[94:97], v[106:109], a[200:203], v[94:97]
s_waitcnt lgkmcnt(12)
v_mfma_f32_16x16x32_bf16 v[98:101], v[78:81], v[106:109], v[98:101]
s_waitcnt lgkmcnt(11)
v_mfma_f32_16x16x32_bf16 v[110:113], a[192:195], v[166:169], v[110:113]
v_mfma_f32_16x16x32_bf16 v[114:117], a[196:199], v[166:169], v[114:117]
v_mfma_f32_16x16x32_bf16 v[118:121], v[166:169], a[200:203], v[118:121]
v_mfma_f32_16x16x32_bf16 v[134:137], v[78:81], v[166:169], v[134:137]
s_waitcnt lgkmcnt(10)
v_mfma_f32_16x16x32_bf16 v[158:161], v[78:81], v[174:177], v[158:161]
s_waitcnt lgkmcnt(8)
v_mfma_f32_16x16x32_bf16 v[86:89], a[204:207], v[106:109], v[86:89]
s_waitcnt lgkmcnt(6)
v_mfma_f32_16x16x32_bf16 v[90:93], a[208:211], v[106:109], v[90:93]
s_waitcnt lgkmcnt(4)
v_mfma_f32_16x16x32_bf16 v[94:97], v[106:109], a[212:215], v[94:97]
s_waitcnt lgkmcnt(2)
v_mfma_f32_16x16x32_bf16 v[98:101], v[82:85], v[106:109], v[98:101]
s_waitcnt lgkmcnt(1)
v_mfma_f32_16x16x32_bf16 v[110:113], a[204:207], v[170:173], v[110:113]
v_mfma_f32_16x16x32_bf16 v[114:117], a[208:211], v[170:173], v[114:117]
v_mfma_f32_16x16x32_bf16 v[118:121], v[170:173], a[212:215], v[118:121]
v_mfma_f32_16x16x32_bf16 v[134:137], v[82:85], v[170:173], v[134:137]
s_waitcnt lgkmcnt(0)
v_mfma_f32_16x16x32_bf16 v[158:161], v[82:85], v[178:181], v[158:161]
s_barrier
ds_read_b32 v242, v24 offset:0
s_waitcnt lgkmcnt(0)
s_nop 2
v_fma_f32 v230, v98, v242, v134
v_fma_f32 v231, v99, v242, v135
v_fma_f32 v232, v100, v242, v136
v_fma_f32 v233, v101, v242, v137
v_cvt_pk_bf16_f32 v230, v230, v231
v_cvt_pk_bf16_f32 v231, v232, v233
ds_write_b64 v22, v[230:231] offset:40960
ds_read_b128 v[198:201], v51 offset:32768
ds_read_b128 v[202:205], v52 offset:32768
ds_read_b128 v[206:209], v51 offset:34816
ds_read_b128 v[210:213], v52 offset:34816
v_mov_b32 v234, v32
v_mov_b32 v235, v33
v_mov_b32 v236, v34
v_mov_b32 v237, v35
v_mov_b32 v134, v158
v_mov_b32 v135, v159
v_mov_b32 v136, v160
v_mov_b32 v137, v161
s_waitcnt lgkmcnt(4)
s_barrier
ds_read_b32 v23, v24 offset:0
ds_read_b32 v148, v21
ds_read_b32 v26, v24 offset:192
ds_read_b32 v53, v27
ds_read_b32 v8, v28
ds_read_b128 v[226:229], v25 offset:0
ds_read_b64 v[158:159], v22 offset:41472
ds_read_b32 v140, v20
v_xor_b32 v51, 0x10000, v51
v_xor_b32 v52, 0x10000, v52
s_waitcnt lgkmcnt(7)
v_fma_f32 v238, v90, v23, v114
v_fma_f32 v239, v91, v23, v115
v_fma_f32 v240, v92, v23, v116
v_fma_f32 v241, v93, v23, v117
v_cvt_pk_bf16_f32 v146, v238, v239
v_cvt_pk_bf16_f32 v147, v240, v241
s_waitcnt lgkmcnt(0)
s_barrier
ds_read_b32 v242, v24 offset:64
v_fma_f32 v238, v94, v226, v118
v_fma_f32 v239, v94, v227, v119
v_fma_f32 v240, v94, v228, v120
v_fma_f32 v241, v94, v229, v121
v_cvt_pk_bf16_f32 v154, v238, v239
v_cvt_pk_bf16_f32 v155, v240, v241
v_fma_f32 v238, v86, v23, v110
v_fma_f32 v239, v87, v23, v111
v_fma_f32 v240, v88, v23, v112
v_fma_f32 v241, v89, v23, v113
v_cvt_pk_bf16_f32 v138, v238, v239
v_cvt_pk_bf16_f32 v139, v240, v241
s_waitcnt lgkmcnt(0)
v_fma_f32 v230, v98, v242, v134
v_fma_f32 v231, v99, v242, v135
v_fma_f32 v232, v100, v242, v136
v_fma_f32 v233, v101, v242, v137
v_cvt_pk_bf16_f32 v230, v230, v231
v_cvt_pk_bf16_f32 v231, v232, v233
ds_write_b64 v22, v[230:231] offset:40960
s_waitcnt vmcnt(5) lgkmcnt(0)
s_barrier
s_mov_b32 s70, 32
s_waitcnt lgkmcnt(0)
s_nop 1
s_branch .Lqa_in5
.p2align 6
.Lqa_top24:
s_waitcnt lgkmcnt(9)
v_mfma_f32_16x16x32_bf16 v[134:137], a[216:219], v[44:47], 0
s_waitcnt lgkmcnt(8)
ds_read_b128 v[162:165], v52 offset:4096
ds_read_b32 v8, v30
v_mfma_f32_16x16x32_bf16 v[134:137], a[220:223], v[102:105], v[134:137]
s_waitcnt lgkmcnt(9)
global_store_dwordx4 v16, v[230:233], s[38:39] sc1
v_mfma_f32_16x16x32_bf16 v[110:113], a[0:3], v[44:47], 0
s_waitcnt lgkmcnt(6)
ds_read_b128 v[166:169], v51 offset:6144
v_add_u32 v16, 18432, v16
v_mfma_f32_16x16x32_bf16 v[134:137], a[224:227], v[106:109], v[134:137]
s_waitcnt lgkmcnt(5)
ds_read_b128 v[170:173], v52 offset:6144
ds_read_b128 v[226:229], v25 offset:64
v_mfma_f32_16x16x32_bf16 v[134:137], a[228:231], v[0:3], v[134:137]
ds_read_b64 v[160:161], v22 offset:41472
ds_read_b32 v144, v20
v_mfma_f32_16x16x32_bf16 v[114:117], a[4:7], v[44:47], 0
s_waitcnt lgkmcnt(7)
ds_read_b128 v[174:177], v51 offset:8192
v_fma_f32 v238, v90, v23, v126
v_mfma_f32_16x16x32_bf16 v[134:137], a[232:235], v[4:7], v[134:137]
s_waitcnt lgkmcnt(7)
ds_read_b128 v[178:181], v52 offset:8192
v_fma_f32 v239, v91, v23, v127
v_mfma_f32_16x16x32_bf16 v[134:137], a[236:239], v[162:165], v[134:137]
v_fma_f32 v240, v92, v23, v128
v_fma_f32 v241, v93, v23, v129
v_mfma_f32_16x16x32_bf16 v[118:121], v[44:47], a[8:11], 0
s_waitcnt lgkmcnt(6)
ds_read_b128 v[182:185], v51 offset:10240
v_cvt_pk_bf16_f32 v150, v238, v239
v_mfma_f32_16x16x32_bf16 v[134:137], a[240:243], v[166:169], v[134:137]
s_waitcnt lgkmcnt(6)
ds_read_b128 v[186:189], v52 offset:10240
v_cvt_pk_bf16_f32 v151, v240, v241
v_mfma_f32_16x16x32_bf16 v[134:137], a[244:247], v[170:173], v[134:137]
s_nop 0
v_mfma_f32_16x16x32_bf16 v[214:217], v[150:153], v[138:141], v[32:35]
v_max_f32 v48, v218, v219
v_max_f32 v49, v220, v221
v_mfma_f32_16x16x32_bf16 v[110:113], a[12:15], v[102:105], v[110:113]
s_waitcnt lgkmcnt(6)
ds_read_b128 v[190:193], v51 offset:12288
v_fma_f32 v238, v94, v226, v130
s_waitcnt lgkmcnt(4)
v_mfma_f32_16x16x32_bf16 v[134:137], a[248:251], v[174:177], v[134:137]
s_xor_b32 s49, s49, 0x10000
ds_read_b128 v[194:197], v52 offset:12288
s_waitcnt lgkmcnt(4)
v_mfma_f32_16x16x32_bf16 v[134:137], a[252:255], v[178:181], v[134:137]
s_add_u32 m0, s49, 0
v_mul_f32 v26, 0x3eae1a93, v26
v_mul_f32 v9, v53, v26
v_mfma_f32_16x16x32_bf16 v[114:117], a[16:19], v[102:105], v[114:117]
s_waitcnt lgkmcnt(3)
ds_read_b128 v[198:201], v51 offset:14336
global_load_lds_dwordx4 v11, s[42:43] offset:-2048 sc1
v_mfma_f32_16x16x32_bf16 v[134:137], v[54:57], v[182:185], v[134:137]
s_waitcnt lgkmcnt(3)
ds_read_b128 v[202:205], v52 offset:14336
v_mul_f32 v10, v8, v26
v_mfma_f32_16x16x32_bf16 v[134:137], v[58:61], v[186:189], v[134:137]
v_max3_f32 v48, v214, v215, v48
v_max3_f32 v49, v216, v217, v49
v_mfma_f32_16x16x32_bf16 v[118:121], v[102:105], a[20:23], v[118:121]
s_waitcnt lgkmcnt(3)
ds_read_b128 v[206:209], v51 offset:16384
v_mul_f32 v48, v48, v9
v_mfma_f32_16x16x32_bf16 v[134:137], v[62:65], v[190:193], v[134:137]
s_waitcnt lgkmcnt(3)
ds_read_b128 v[210:213], v52 offset:16384
v_mul_f32 v49, v49, v10
v_mfma_f32_16x16x32_bf16 v[134:137], v[66:69], v[194:197], v[134:137]
v_max_f32 v50, v48, v49
v_mov_b32 v95, v50
v_mfma_f32_16x16x32_bf16 v[110:113], a[24:27], v[106:109], v[110:113]
s_waitcnt lgkmcnt(3)
v_permlane16_swap_b32 v50, v95
v_mfma_f32_16x16x32_bf16 v[134:137], v[70:73], v[198:201], v[134:137]
s_waitcnt lgkmcnt(2)
v_max_f32 v50, v50, v95
v_mov_b32 v95, v50
v_mfma_f32_16x16x32_bf16 v[134:137], v[74:77], v[202:205], v[134:137]
global_load_lds_dwordx4 v12, s[42:43] offset:-1024 sc1
v_fma_f32 v239, v94, v227, v131
v_mfma_f32_16x16x32_bf16 v[114:117], a[28:31], v[106:109], v[114:117]
s_waitcnt lgkmcnt(1)
v_permlane32_swap_b32 v50, v95
v_mfma_f32_16x16x32_bf16 v[134:137], v[78:81], v[206:209], v[134:137]
s_waitcnt lgkmcnt(0)
v_max_f32 v50, v50, v95
v_sub_f32 v48, v48, v50
v_mfma_f32_16x16x32_bf16 v[134:137], v[82:85], v[210:213], v[134:137]
v_sub_f32 v49, v49, v50
v_fma_f32 v240, v94, v228, v132
v_mfma_f32_16x16x32_bf16 v[118:121], v[106:109], a[32:35], v[118:121]
v_exp_f32 v96, v48
v_mfma_f32_16x16x32_bf16 v[110:113], a[36:39], v[0:3], v[110:113]
v_exp_f32 v97, v49
v_mfma_f32_16x16x32_bf16 v[114:117], a[40:43], v[0:3], v[114:117]
v_add_f32 v50, v96, v97
v_mov_b32 v95, v50
v_mfma_f32_16x16x32_bf16 v[118:121], v[0:3], a[44:47], v[118:121]
v_mul_f32 v96, v96, v53
v_fma_f32 v241, v94, v229, v133
v_mfma_f32_16x16x32_bf16 v[110:113], a[48:51], v[4:7], v[110:113]
global_load_lds_dwordx4 v13, s[42:43] offset:0 sc1
v_cvt_pk_bf16_f32 v156, v238, v239
v_mfma_f32_16x16x32_bf16 v[114:117], a[52:55], v[4:7], v[114:117]
v_permlane16_swap_b32 v50, v95
v_mfma_f32_16x16x32_bf16 v[118:121], v[4:7], a[56:59], v[118:121]
v_add_f32 v50, v50, v95
v_mov_b32 v95, v50
v_mfma_f32_16x16x32_bf16 v[110:113], a[60:63], v[162:165], v[110:113]
v_mul_f32 v97, v97, v8
v_cvt_pk_bf16_f32 v157, v240, v241
v_mfma_f32_16x16x32_bf16 v[114:117], a[64:67], v[162:165], v[114:117]
v_permlane32_swap_b32 v50, v95
v_mfma_f32_16x16x32_bf16 v[118:121], v[162:165], a[68:71], v[118:121]
v_mfma_f32_16x16x32_bf16 v[110:113], a[72:75], v[166:169], v[110:113]
s_barrier
v_mfma_f32_16x16x32_bf16 v[114:117], a[76:79], v[166:169], v[114:117]
ds_read_b32 v242, v24 offset:0
v_add_f32 v50, v50, v95
v_mfma_f32_16x16x32_bf16 v[118:121], v[166:169], a[80:83], v[118:121]
global_load_lds_dwordx4 v14, s[42:43] offset:1024 sc1
v_fma_f32 v238, v86, v23, v122
)"
R"(v_mfma_f32_16x16x32_bf16 v[110:113], a[84:87], v[170:173], v[110:113]
v_rcp_f32 v50, v50
v_mfma_f32_16x16x32_bf16 v[114:117], a[88:91], v[170:173], v[114:117]
v_mul_f32 v96, v96, v50
v_mul_f32 v97, v97, v50
v_mfma_f32_16x16x32_bf16 v[118:121], v[170:173], a[92:95], v[118:121]
v_cvt_pk_bf16_f32 v96, v96, v96
v_cvt_pk_bf16_f32 v97, v97, v97
v_mfma_f32_16x16x32_bf16 v[110:113], a[96:99], v[174:177], v[110:113]
v_and_b32 v224, v96, v40
v_and_b32 v225, v97, v41
v_mfma_f32_16x16x32_bf16 v[114:117], a[100:103], v[174:177], v[114:117]
v_and_b32 v222, v96, v42
v_and_b32 v223, v97, v43
v_mfma_f32_16x16x32_bf16 v[118:121], v[174:177], a[104:107], v[118:121]
s_nop 0
v_mfma_f32_16x16x32_bf16 v[218:221], v[154:157], v[222:225], 0
v_mfma_f32_16x16x32_bf16 v[214:217], v[158:161], v[222:225], 0
s_waitcnt lgkmcnt(0)
v_fma_f32 v230, v98, v242, v134
v_fma_f32 v231, v99, v242, v135
v_mfma_f32_16x16x32_bf16 v[110:113], a[108:111], v[178:181], v[110:113]
v_fma_f32 v232, v100, v242, v136
v_fma_f32 v233, v101, v242, v137
v_mfma_f32_16x16x32_bf16 v[114:117], a[112:115], v[178:181], v[114:117]
global_load_lds_dwordx4 v15, s[42:43] offset:2048 sc1
s_add_u32 s42, s42, s46
s_addc_u32 s43, s43, s47
v_mfma_f32_16x16x32_bf16 v[118:121], v[178:181], a[116:119], v[118:121]
v_cvt_pk_bf16_f32 v230, v230, v231
v_cvt_pk_bf16_f32 v231, v232, v233
v_mfma_f32_16x16x32_bf16 v[110:113], a[120:123], v[182:185], v[110:113]
v_cvt_pk_bf16_f32 v254, v218, v219
v_cvt_pk_bf16_f32 v255, v220, v221
v_mfma_f32_16x16x32_bf16 v[114:117], a[124:127], v[182:185], v[114:117]
ds_write_b64 v18, v[254:255]
v_mfma_f32_16x16x32_bf16 v[118:121], v[182:185], a[128:131], v[118:121]
v_cvt_pk_bf16_f32 v96, v214, v215
v_fma_f32 v239, v87, v23, v123
v_mfma_f32_16x16x32_bf16 v[110:113], a[132:135], v[186:189], v[110:113]
ds_write_b32 v19, v96
v_mfma_f32_16x16x32_bf16 v[114:117], a[136:139], v[186:189], v[114:117]
ds_write_b64 v22, v[230:231] offset:40960
v_mfma_f32_16x16x32_bf16 v[118:121], v[186:189], a[140:143], v[118:121]
v_fma_f32 v240, v88, v23, v124
v_fma_f32 v241, v89, v23, v125
v_mfma_f32_16x16x32_bf16 v[110:113], a[144:147], v[190:193], v[110:113]
v_cvt_pk_bf16_f32 v142, v238, v239
v_cvt_pk_bf16_f32 v143, v240, v241
v_mfma_f32_16x16x32_bf16 v[114:117], a[148:151], v[190:193], v[114:117]
v_mfma_f32_16x16x32_bf16 v[118:121], v[190:193], a[152:155], v[118:121]
v_mfma_f32_16x16x32_bf16 v[110:113], a[156:159], v[194:197], v[110:113]
v_mfma_f32_16x16x32_bf16 v[114:117], a[160:163], v[194:197], v[114:117]
v_mfma_f32_16x16x32_bf16 v[118:121], v[194:197], a[164:167], v[118:121]
v_mfma_f32_16x16x32_bf16 v[110:113], a[168:171], v[198:201], v[110:113]
v_mfma_f32_16x16x32_bf16 v[114:117], a[172:175], v[198:201], v[114:117]
v_mfma_f32_16x16x32_bf16 v[118:121], v[198:201], a[176:179], v[118:121]
v_mfma_f32_16x16x32_bf16 v[110:113], a[180:183], v[202:205], v[110:113]
v_mfma_f32_16x16x32_bf16 v[114:117], a[184:187], v[202:205], v[114:117]
s_waitcnt vmcnt(5) lgkmcnt(0)
s_barrier
v_mfma_f32_16x16x32_bf16 v[118:121], v[202:205], a[188:191], v[118:121]
ds_read_b128 v[44:47], v51 offset:18432
v_mfma_f32_16x16x32_bf16 v[110:113], a[192:195], v[206:209], v[110:113]
ds_read_b128 v[102:105], v52 offset:18432
ds_read_b128 v[230:233], v17 offset:0
v_mfma_f32_16x16x32_bf16 v[114:117], a[196:199], v[206:209], v[114:117]
ds_read_b32 v23, v24 offset:0
ds_read_b32 v148, v21
v_mfma_f32_16x16x32_bf16 v[218:221], v[150:153], v[142:145], v[36:39]
v_mfma_f32_16x16x32_bf16 v[118:121], v[206:209], a[200:203], v[118:121]
ds_read_b128 v[106:109], v51 offset:20480
ds_read_b32 v26, v24 offset:192
v_mfma_f32_16x16x32_bf16 v[110:113], a[204:207], v[210:213], v[110:113]
ds_read_b128 v[0:3], v52 offset:20480
ds_read_b32 v53, v27
v_mfma_f32_16x16x32_bf16 v[114:117], a[208:211], v[210:213], v[114:117]
ds_read_b32 v8, v28
ds_read_b128 v[226:229], v25 offset:0
v_mfma_f32_16x16x32_bf16 v[118:121], v[210:213], a[212:215], v[118:121]
s_waitcnt lgkmcnt(10)
ds_read_b128 v[4:7], v51 offset:22528
ds_read_b64 v[158:159], v22 offset:41472
v_mfma_f32_16x16x32_bf16 v[134:137], a[216:219], v[44:47], 0
s_waitcnt lgkmcnt(11)
ds_read_b128 v[162:165], v52 offset:22528
ds_read_b32 v140, v20
v_mfma_f32_16x16x32_bf16 v[134:137], a[220:223], v[102:105], v[134:137]
s_waitcnt lgkmcnt(12)
global_store_dwordx4 v16, v[230:233], s[38:39] sc1
v_mfma_f32_16x16x32_bf16 v[122:125], a[0:3], v[44:47], 0
s_waitcnt lgkmcnt(9)
ds_read_b128 v[166:169], v51 offset:24576
v_add_u32 v16, 18432, v16
v_mfma_f32_16x16x32_bf16 v[134:137], a[224:227], v[106:109], v[134:137]
s_waitcnt lgkmcnt(8)
ds_read_b128 v[170:173], v52 offset:24576
v_fma_f32 v238, v90, v23, v114
v_mfma_f32_16x16x32_bf16 v[134:137], a[228:231], v[0:3], v[134:137]
v_fma_f32 v239, v91, v23, v115
v_fma_f32 v240, v92, v23, v116
v_mfma_f32_16x16x32_bf16 v[126:129], a[4:7], v[44:47], 0
s_waitcnt lgkmcnt(5)
ds_read_b128 v[174:177], v51 offset:26624
v_fma_f32 v241, v93, v23, v117
v_mfma_f32_16x16x32_bf16 v[134:137], a[232:235], v[4:7], v[134:137]
s_waitcnt lgkmcnt(4)
ds_read_b128 v[178:181], v52 offset:26624
v_cvt_pk_bf16_f32 v146, v238, v239
v_mfma_f32_16x16x32_bf16 v[134:137], a[236:239], v[162:165], v[134:137]
v_cvt_pk_bf16_f32 v147, v240, v241
v_max_f32 v48, v218, v219
v_mfma_f32_16x16x32_bf16 v[130:133], v[44:47], a[8:11], 0
ds_read_b128 v[182:185], v51 offset:28672
v_mfma_f32_16x16x32_bf16 v[214:217], v[146:149], v[142:145], v[234:237]
s_waitcnt lgkmcnt(4)
v_max_f32 v49, v220, v221
v_fma_f32 v238, v94, v226, v118
v_mfma_f32_16x16x32_bf16 v[134:137], a[240:243], v[166:169], v[134:137]
s_waitcnt lgkmcnt(3)
ds_read_b128 v[186:189], v52 offset:28672
v_fma_f32 v239, v94, v227, v119
v_mfma_f32_16x16x32_bf16 v[134:137], a[244:247], v[170:173], v[134:137]
v_mul_f32 v26, 0x3eae1a93, v26
v_mul_f32 v9, v53, v26
v_mfma_f32_16x16x32_bf16 v[122:125], a[12:15], v[102:105], v[122:125]
s_waitcnt lgkmcnt(3)
ds_read_b128 v[190:193], v51 offset:30720
v_mul_f32 v10, v8, v26
v_mfma_f32_16x16x32_bf16 v[134:137], a[248:251], v[174:177], v[134:137]
s_add_u32 m0, s49, 18432
ds_read_b128 v[194:197], v52 offset:30720
s_waitcnt lgkmcnt(4)
v_mfma_f32_16x16x32_bf16 v[134:137], a[252:255], v[178:181], v[134:137]
global_load_lds_dwordx4 v11, s[42:43] offset:-2048 sc1
v_max3_f32 v48, v214, v215, v48
v_mfma_f32_16x16x32_bf16 v[126:129], a[16:19], v[102:105], v[126:129]
s_waitcnt lgkmcnt(3)
ds_read_b128 v[198:201], v51 offset:32768
v_max3_f32 v49, v216, v217, v49
v_mfma_f32_16x16x32_bf16 v[134:137], v[54:57], v[182:185], v[134:137]
s_waitcnt lgkmcnt(3)
ds_read_b128 v[202:205], v52 offset:32768
v_mul_f32 v48, v48, v9
v_mfma_f32_16x16x32_bf16 v[134:137], v[58:61], v[186:189], v[134:137]
v_mul_f32 v49, v49, v10
v_max_f32 v50, v48, v49
v_mfma_f32_16x16x32_bf16 v[130:133], v[102:105], a[20:23], v[130:133]
s_waitcnt lgkmcnt(3)
ds_read_b128 v[206:209], v51 offset:34816
v_mov_b32 v95, v50
v_mfma_f32_16x16x32_bf16 v[134:137], v[62:65], v[190:193], v[134:137]
s_waitcnt lgkmcnt(3)
ds_read_b128 v[210:213], v52 offset:34816
v_fma_f32 v240, v94, v228, v120
v_mfma_f32_16x16x32_bf16 v[134:137], v[66:69], v[194:197], v[134:137]
v_permlane16_swap_b32 v50, v95
v_mfma_f32_16x16x32_bf16 v[122:125], a[24:27], v[106:109], v[122:125]
s_waitcnt lgkmcnt(3)
v_max_f32 v50, v50, v95
v_mov_b32 v95, v50
v_mfma_f32_16x16x32_bf16 v[134:137], v[70:73], v[198:201], v[134:137]
s_waitcnt lgkmcnt(2)
global_load_lds_dwordx4 v12, s[42:43] offset:-1024 sc1
v_fma_f32 v241, v94, v229, v121
v_mfma_f32_16x16x32_bf16 v[134:137], v[74:77], v[202:205], v[134:137]
v_permlane32_swap_b32 v50, v95
v_mfma_f32_16x16x32_bf16 v[126:129], a[28:31], v[106:109], v[126:129]
s_waitcnt lgkmcnt(1)
v_max_f32 v50, v50, v95
v_sub_f32 v48, v48, v50
v_mfma_f32_16x16x32_bf16 v[134:137], v[78:81], v[206:209], v[134:137]
s_waitcnt lgkmcnt(0)
v_sub_f32 v49, v49, v50
v_cvt_pk_bf16_f32 v154, v238, v239
v_mfma_f32_16x16x32_bf16 v[134:137], v[82:85], v[210:213], v[134:137]
v_exp_f32 v96, v48
v_mfma_f32_16x16x32_bf16 v[130:133], v[106:109], a[32:35], v[130:133]
v_exp_f32 v97, v49
v_mfma_f32_16x16x32_bf16 v[122:125], a[36:39], v[0:3], v[122:125]
v_add_f32 v50, v96, v97
v_mov_b32 v95, v50
v_mfma_f32_16x16x32_bf16 v[126:129], a[40:43], v[0:3], v[126:129]
v_mul_f32 v96, v96, v53
v_cvt_pk_bf16_f32 v155, v240, v241
v_mfma_f32_16x16x32_bf16 v[130:133], v[0:3], a[44:47], v[130:133]
global_load_lds_dwordx4 v13, s[42:43] offset:0 sc1
v_fma_f32 v238, v86, v23, v110
v_mfma_f32_16x16x32_bf16 v[122:125], a[48:51], v[4:7], v[122:125]
v_permlane16_swap_b32 v50, v95
v_mfma_f32_16x16x32_bf16 v[126:129], a[52:55], v[4:7], v[126:129]
v_add_f32 v50, v50, v95
v_mov_b32 v95, v50
v_mfma_f32_16x16x32_bf16 v[130:133], v[4:7], a[56:59], v[130:133]
v_mul_f32 v97, v97, v8
v_fma_f32 v239, v87, v23, v111
v_mfma_f32_16x16x32_bf16 v[122:125], a[60:63], v[162:165], v[122:125]
v_permlane32_swap_b32 v50, v95
v_mfma_f32_16x16x32_bf16 v[126:129], a[64:67], v[162:165], v[126:129]
v_add_f32 v50, v50, v95
v_fma_f32 v240, v88, v23, v112
v_mfma_f32_16x16x32_bf16 v[130:133], v[162:165], a[68:71], v[130:133]
v_mfma_f32_16x16x32_bf16 v[122:125], a[72:75], v[166:169], v[122:125]
s_barrier
v_mfma_f32_16x16x32_bf16 v[126:129], a[76:79], v[166:169], v[126:129]
global_load_lds_dwordx4 v14, s[42:43] offset:1024 sc1
ds_read_b32 v242, v24 offset:64
v_mfma_f32_16x16x32_bf16 v[130:133], v[166:169], a[80:83], v[130:133]
v_rcp_f32 v50, v50
v_mfma_f32_16x16x32_bf16 v[122:125], a[84:87], v[170:173], v[122:125]
v_mul_f32 v96, v96, v50
v_mul_f32 v97, v97, v50
v_mfma_f32_16x16x32_bf16 v[126:129], a[88:91], v[170:173], v[126:129]
v_cvt_pk_bf16_f32 v96, v96, v96
v_cvt_pk_bf16_f32 v97, v97, v97
v_mfma_f32_16x16x32_bf16 v[130:133], v[170:173], a[92:95], v[130:133]
v_and_b32 v222, v96, v40
v_and_b32 v223, v97, v41
v_mfma_f32_16x16x32_bf16 v[122:125], a[96:99], v[174:177], v[122:125]
v_and_b32 v224, v96, v42
v_and_b32 v225, v97, v43
v_mfma_f32_16x16x32_bf16 v[126:129], a[100:103], v[174:177], v[126:129]
s_nop 0
v_mfma_f32_16x16x32_bf16 v[218:221], v[154:157], v[222:225], 0
v_mfma_f32_16x16x32_bf16 v[214:217], v[158:161], v[222:225], 0
s_waitcnt lgkmcnt(0)
v_fma_f32 v230, v98, v242, v134
v_fma_f32 v231, v99, v242, v135
v_mfma_f32_16x16x32_bf16 v[130:133], v[174:177], a[104:107], v[130:133]
v_fma_f32 v232, v100, v242, v136
v_fma_f32 v233, v101, v242, v137
v_mfma_f32_16x16x32_bf16 v[122:125], a[108:111], v[178:181], v[122:125]
global_load_lds_dwordx4 v15, s[42:43] offset:2048 sc1
s_sub_u32 s42, s42, 18432
s_subb_u32 s43, s43, 0
v_mfma_f32_16x16x32_bf16 v[126:129], a[112:115], v[178:181], v[126:129]
v_cvt_pk_bf16_f32 v230, v230, v231
v_cvt_pk_bf16_f32 v231, v232, v233
v_mfma_f32_16x16x32_bf16 v[130:133], v[178:181], a[116:119], v[130:133]
v_cvt_pk_bf16_f32 v254, v218, v219
v_cvt_pk_bf16_f32 v255, v220, v221
v_mfma_f32_16x16x32_bf16 v[122:125], a[120:123], v[182:185], v[122:125]
ds_write_b64 v18, v[254:255]
v_mfma_f32_16x16x32_bf16 v[126:129], a[124:127], v[182:185], v[126:129]
v_cvt_pk_bf16_f32 v96, v214, v215
v_fma_f32 v241, v89, v23, v113
v_mfma_f32_16x16x32_bf16 v[130:133], v[182:185], a[128:131], v[130:133]
ds_write_b32 v19, v96
v_mfma_f32_16x16x32_bf16 v[122:125], a[132:135], v[186:189], v[122:125]
ds_write_b64 v22, v[230:231] offset:40960
v_mfma_f32_16x16x32_bf16 v[126:129], a[136:139], v[186:189], v[126:129]
v_cvt_pk_bf16_f32 v138, v238, v239
v_cvt_pk_bf16_f32 v139, v240, v241
v_mfma_f32_16x16x32_bf16 v[130:133], v[186:189], a[140:143], v[130:133]
v_mfma_f32_16x16x32_bf16 v[122:125], a[144:147], v[190:193], v[122:125]
v_mfma_f32_16x16x32_bf16 v[126:129], a[148:151], v[190:193], v[126:129]
v_mfma_f32_16x16x32_bf16 v[130:133], v[190:193], a[152:155], v[130:133]
v_mfma_f32_16x16x32_bf16 v[122:125], a[156:159], v[194:197], v[122:125]
v_mfma_f32_16x16x32_bf16 v[126:129], a[160:163], v[194:197], v[126:129]
v_mfma_f32_16x16x32_bf16 v[130:133], v[194:197], a[164:167], v[130:133]
v_mfma_f32_16x16x32_bf16 v[122:125], a[168:171], v[198:201], v[122:125]
v_mfma_f32_16x16x32_bf16 v[126:129], a[172:175], v[198:201], v[126:129]
v_xor_b32 v51, 0x10000, v51
v_xor_b32 v52, 0x10000, v52
v_mfma_f32_16x16x32_bf16 v[130:133], v[198:201], a[176:179], v[130:133]
v_mfma_f32_16x16x32_bf16 v[122:125], a[180:183], v[202:205], v[122:125]
v_mfma_f32_16x16x32_bf16 v[126:129], a[184:187], v[202:205], v[126:129]
s_waitcnt vmcnt(5) lgkmcnt(0)
s_barrier
.Lqa_in5:
v_mfma_f32_16x16x32_bf16 v[130:133], v[202:205], a[188:191], v[130:133]
s_cmp_eq_u32 s70, s73
s_cbranch_scc1 .Lqa_hk6
.Lqa_hr7:
ds_read_b128 v[44:47], v51 offset:0
v_mfma_f32_16x16x32_bf16 v[122:125], a[192:195], v[206:209], v[122:125]
ds_read_b128 v[102:105], v52 offset:0
ds_read_b128 v[230:233], v17 offset:0
v_mfma_f32_16x16x32_bf16 v[126:129], a[196:199], v[206:209], v[126:129]
ds_read_b32 v23, v24 offset:64
ds_read_b32 v152, v21
v_mfma_f32_16x16x32_bf16 v[218:221], v[146:149], v[138:141], v[36:39]
v_mfma_f32_16x16x32_bf16 v[130:133], v[206:209], a[200:203], v[130:133]
ds_read_b128 v[106:109], v51 offset:2048
ds_read_b32 v26, v24 offset:128
v_mfma_f32_16x16x32_bf16 v[122:125], a[204:207], v[210:213], v[122:125]
ds_read_b128 v[0:3], v52 offset:2048
ds_read_b32 v53, v29
v_mfma_f32_16x16x32_bf16 v[126:129], a[208:211], v[210:213], v[126:129]
v_mfma_f32_16x16x32_bf16 v[130:133], v[210:213], a[212:215], v[130:133]
s_sub_u32 s70, s70, 1
ds_read_b128 v[4:7], v51 offset:4096
s_cbranch_scc0 .Lqa_top24
s_waitcnt lgkmcnt(0)
ds_read_b32 v8, v30
global_store_dwordx4 v16, v[230:233], s[38:39] sc1
v_add_u32 v16, 18432, v16
ds_read_b128 v[226:229], v25 offset:64
ds_read_b64 v[160:161], v22 offset:41472
ds_read_b32 v144, v20
v_fma_f32 v238, v90, v23, v126
v_fma_f32 v239, v91, v23, v127
v_fma_f32 v240, v92, v23, v128
v_fma_f32 v241, v93, v23, v129
v_cvt_pk_bf16_f32 v150, v238, v239
v_cvt_pk_bf16_f32 v151, v240, v241
s_nop 1
v_mfma_f32_16x16x32_bf16 v[214:217], v[150:153], v[138:141], v[32:35]
s_waitcnt lgkmcnt(2)
v_max_f32 v48, v218, v219
v_max_f32 v49, v220, v221
v_fma_f32 v238, v94, v226, v130
v_mul_f32 v26, 0x3eae1a93, v26
v_mul_f32 v9, v53, v26
v_mul_f32 v10, v8, v26
s_nop 0
v_max3_f32 v48, v214, v215, v48
v_max3_f32 v49, v216, v217, v49
v_mul_f32 v48, v48, v9
v_mul_f32 v49, v49, v10
v_max_f32 v50, v48, v49
v_mov_b32 v95, v50
s_nop 1
v_permlane16_swap_b32 v50, v95
v_max_f32 v50, v50, v95
v_mov_b32 v95, v50
v_fma_f32 v239, v94, v227, v131
s_nop 0
v_permlane32_swap_b32 v50, v95
v_max_f32 v50, v50, v95
v_sub_f32 v48, v48, v50
v_sub_f32 v49, v49, v50
v_fma_f32 v240, v94, v228, v132
v_exp_f32 v96, v48
v_exp_f32 v97, v49
s_nop 0
v_add_f32 v50, v96, v97
v_mov_b32 v95, v50
v_mul_f32 v96, v96, v53
v_fma_f32 v241, v94, v229, v133
v_cvt_pk_bf16_f32 v156, v238, v239
v_permlane16_swap_b32 v50, v95
v_add_f32 v50, v50, v95
v_mov_b32 v95, v50
v_mul_f32 v97, v97, v8
v_cvt_pk_bf16_f32 v157, v240, v241
v_permlane32_swap_b32 v50, v95
s_waitcnt lgkmcnt(0)
s_barrier
v_add_f32 v50, v50, v95
v_fma_f32 v238, v86, v23, v122
v_rcp_f32 v50, v50
s_nop 0
v_mul_f32 v96, v96, v50
v_mul_f32 v97, v97, v50
v_cvt_pk_bf16_f32 v96, v96, v96
v_cvt_pk_bf16_f32 v97, v97, v97
v_and_b32 v224, v96, v40
v_and_b32 v225, v97, v41
v_and_b32 v222, v96, v42
v_and_b32 v223, v97, v43
s_nop 1
v_mfma_f32_16x16x32_bf16 v[218:221], v[154:157], v[222:225], 0
v_mfma_f32_16x16x32_bf16 v[214:217], v[158:161], v[222:225], 0
s_nop 6
v_cvt_pk_bf16_f32 v254, v218, v219
v_cvt_pk_bf16_f32 v255, v220, v221
ds_write_b64 v18, v[254:255]
v_cvt_pk_bf16_f32 v96, v214, v215
v_fma_f32 v239, v87, v23, v123
ds_write_b32 v19, v96
v_fma_f32 v240, v88, v23, v124
v_fma_f32 v241, v89, v23, v125
v_cvt_pk_bf16_f32 v142, v238, v239
v_cvt_pk_bf16_f32 v143, v240, v241
s_waitcnt vmcnt(0)
s_waitcnt lgkmcnt(0)
s_barrier
ds_read_b128 v[230:233], v17 offset:0
s_waitcnt lgkmcnt(0)
global_store_dwordx4 v16, v[230:233], s[38:39] sc1
s_waitcnt vmcnt(0)
s_barrier
s_endpgm
.Lqa_w2:
s_getpc_b64 s[90:91]
.Lqa_pc44:
s_add_u32 s90, s90, .Lqa_e29-.Lqa_pc44
s_addc_u32 s91, s91, 0
s_branch .Lqa_k36
.Lqa_bk43:
v_add_u32 v255, 0x10000, v9
v_add_u32 v162, 0x20000, v9
v_add_u32 v163, 0x10000, v10
v_add_u32 v164, 0x20000, v10
v_mov_b32 v106, 0x3F803F80
v_mov_b32 v107, 0x3F803F80
v_mov_b32 v108, 0x3F803F80
v_mov_b32 v109, 0x3F803F80
v_mov_b32 v141, 0
)"
R"(v_mov_b32 v145, 0
v_mov_b32 v149, 0
v_mov_b32 v153, 0
v_mov_b32 v165, 46080
s_waitcnt lgkmcnt(0)
s_mul_i32 s37, s37, 0x9E3779B1
s_add_u32 s37, s37, 0x7F4A7C15
v_add_u32 v46, s37, v46
v_add_u32 v48, s37, v48
v_add_u32 v50, s37, v50
ds_write_b32 v165, v141
s_mov_b64 s[76:77], 0
s_mov_b32 s73, 18
s_add_u32 m0, s48, 4096
s_nop 0
global_load_lds_dwordx4 v0, s[40:41] offset:-4096
global_load_lds_dwordx4 v1, s[40:41] offset:-3072
global_load_lds_dwordx4 v2, s[40:41] offset:-2048
global_load_lds_dwordx4 v3, s[40:41] offset:-1024
global_load_lds_dwordx4 v4, s[40:41] offset:0
global_load_lds_dwordx4 v5, s[40:41] offset:1024
global_load_lds_dwordx4 v6, s[40:41] offset:2048
global_load_lds_dwordx4 v7, s[40:41] offset:3072
s_add_u32 m0, s48, 12288
s_nop 0
global_load_lds_dwordx4 v8, s[40:41] offset:-4096
s_add_u32 s40, s40, 221184
s_addc_u32 s41, s41, 0
s_add_u32 m0, s48, 40960
s_nop 0
global_load_lds_dwordx4 v0, s[40:41] offset:-4096
global_load_lds_dwordx4 v1, s[40:41] offset:-3072
global_load_lds_dwordx4 v2, s[40:41] offset:-2048
global_load_lds_dwordx4 v3, s[40:41] offset:-1024
global_load_lds_dwordx4 v4, s[40:41] offset:0
global_load_lds_dwordx4 v5, s[40:41] offset:1024
global_load_lds_dwordx4 v6, s[40:41] offset:2048
global_load_lds_dwordx4 v7, s[40:41] offset:3072
s_add_u32 m0, s48, 49152
s_nop 0
global_load_lds_dwordx4 v8, s[40:41] offset:-4096
s_add_u32 s40, s40, 221184
s_addc_u32 s41, s41, 0
s_waitcnt vmcnt(9) lgkmcnt(0)
s_barrier
s_add_u32 m0, s48, 77824
ds_read_b64_tr_b16 a[0:1], v9 offset:47104
ds_read_b64_tr_b16 a[2:3], v9 offset:49408
global_load_lds_dwordx4 v0, s[40:41] offset:-4096
ds_read_b64_tr_b16 a[4:5], v9 offset:47296
ds_read_b64_tr_b16 a[6:7], v9 offset:49600
global_load_lds_dwordx4 v1, s[40:41] offset:-3072
ds_read_b64_tr_b16 a[8:9], v9 offset:47488
global_load_lds_dwordx4 v2, s[40:41] offset:-2048
ds_read_b64_tr_b16 a[10:11], v9 offset:49792
ds_read_b64_tr_b16 a[216:217], v10 offset:47104
global_load_lds_dwordx4 v3, s[40:41] offset:-1024
ds_read_b64_tr_b16 a[218:219], v10 offset:49408
global_load_lds_dwordx4 v4, s[40:41] offset:0
ds_read_b128 v[166:169], v51 offset:0
ds_read_b128 v[174:177], v51 offset:18432
global_load_lds_dwordx4 v5, s[40:41] offset:1024
ds_read_b64_tr_b16 a[12:13], v255 offset:0
global_load_lds_dwordx4 v6, s[40:41] offset:2048
ds_read_b64_tr_b16 a[14:15], v255 offset:2304
ds_read_b64_tr_b16 a[16:17], v255 offset:192
global_load_lds_dwordx4 v7, s[40:41] offset:3072
ds_read_b64_tr_b16 a[18:19], v255 offset:2496
s_add_u32 m0, s48, 86016
ds_read_b64_tr_b16 a[20:21], v255 offset:384
ds_read_b64_tr_b16 a[22:23], v255 offset:2688
global_load_lds_dwordx4 v8, s[40:41] offset:-4096
ds_read_b64_tr_b16 a[220:221], v163 offset:0
s_add_u32 s40, s40, 221184
ds_read_b64_tr_b16 a[222:223], v163 offset:2304
ds_read_b128 v[170:173], v52 offset:0
s_addc_u32 s41, s41, 0
ds_read_b128 v[178:181], v52 offset:18432
s_waitcnt vmcnt(9) lgkmcnt(0)
s_barrier
s_add_u32 m0, s48, 4096
ds_read_b64_tr_b16 a[24:25], v255 offset:18432
v_mfma_f32_16x16x32_bf16 v[86:89], a[0:3], v[106:109], 0
ds_read_b64_tr_b16 a[26:27], v255 offset:20736
v_mfma_f32_16x16x32_bf16 v[90:93], a[4:7], v[106:109], 0
global_load_lds_dwordx4 v0, s[40:41] offset:-4096
ds_read_b64_tr_b16 a[28:29], v255 offset:18624
v_mfma_f32_16x16x32_bf16 v[94:97], v[106:109], a[8:11], 0
ds_read_b64_tr_b16 a[30:31], v255 offset:20928
global_load_lds_dwordx4 v1, s[40:41] offset:-3072
v_mfma_f32_16x16x32_bf16 v[102:105], v[106:109], a[216:219], 0
ds_read_b64_tr_b16 a[32:33], v255 offset:18816
v_mfma_f32_16x16x32_bf16 v[110:113], a[0:3], v[166:169], 0
ds_read_b64_tr_b16 a[34:35], v255 offset:21120
global_load_lds_dwordx4 v2, s[40:41] offset:-2048
v_mfma_f32_16x16x32_bf16 v[114:117], a[4:7], v[166:169], 0
ds_read_b64_tr_b16 a[224:225], v163 offset:18432
v_mfma_f32_16x16x32_bf16 v[118:121], v[166:169], a[8:11], 0
global_load_lds_dwordx4 v3, s[40:41] offset:-1024
ds_read_b64_tr_b16 a[226:227], v163 offset:20736
v_mfma_f32_16x16x32_bf16 v[134:137], v[166:169], a[216:219], 0
ds_read_b128 v[182:185], v51 offset:2048
global_load_lds_dwordx4 v4, s[40:41] offset:0
v_mfma_f32_16x16x32_bf16 v[122:125], a[0:3], v[174:177], 0
ds_read_b128 v[190:193], v51 offset:20480
v_mfma_f32_16x16x32_bf16 v[126:129], a[4:7], v[174:177], 0
ds_read_b64_tr_b16 a[36:37], v255 offset:36864
global_load_lds_dwordx4 v5, s[40:41] offset:1024
v_mfma_f32_16x16x32_bf16 v[130:133], v[174:177], a[8:11], 0
ds_read_b64_tr_b16 a[38:39], v255 offset:39168
v_mfma_f32_16x16x32_bf16 v[158:161], v[174:177], a[216:219], 0
global_load_lds_dwordx4 v6, s[40:41] offset:2048
ds_read_b64_tr_b16 a[40:41], v255 offset:37056
v_mfma_f32_16x16x32_bf16 v[86:89], a[12:15], v[106:109], v[86:89]
ds_read_b64_tr_b16 a[42:43], v255 offset:39360
v_mfma_f32_16x16x32_bf16 v[90:93], a[16:19], v[106:109], v[90:93]
global_load_lds_dwordx4 v7, s[40:41] offset:3072
ds_read_b64_tr_b16 a[44:45], v255 offset:37248
v_mfma_f32_16x16x32_bf16 v[94:97], v[106:109], a[20:23], v[94:97]
s_add_u32 m0, s48, 12288
ds_read_b64_tr_b16 a[46:47], v255 offset:39552
v_mfma_f32_16x16x32_bf16 v[102:105], v[106:109], a[220:223], v[102:105]
ds_read_b64_tr_b16 a[228:229], v163 offset:36864
v_mfma_f32_16x16x32_bf16 v[110:113], a[12:15], v[170:173], v[110:113]
global_load_lds_dwordx4 v8, s[40:41] offset:-4096
ds_read_b64_tr_b16 a[230:231], v163 offset:39168
v_mfma_f32_16x16x32_bf16 v[114:117], a[16:19], v[170:173], v[114:117]
ds_read_b128 v[186:189], v52 offset:2048
v_mfma_f32_16x16x32_bf16 v[118:121], v[170:173], a[20:23], v[118:121]
s_add_u32 s40, s40, 221184
ds_read_b128 v[194:197], v52 offset:20480
v_mfma_f32_16x16x32_bf16 v[134:137], v[170:173], a[220:223], v[134:137]
v_mfma_f32_16x16x32_bf16 v[122:125], a[12:15], v[178:181], v[122:125]
s_addc_u32 s41, s41, 0
v_mfma_f32_16x16x32_bf16 v[126:129], a[16:19], v[178:181], v[126:129]
v_mfma_f32_16x16x32_bf16 v[130:133], v[178:181], a[20:23], v[130:133]
v_mfma_f32_16x16x32_bf16 v[158:161], v[178:181], a[220:223], v[158:161]
s_waitcnt vmcnt(9) lgkmcnt(0)
s_barrier
s_add_u32 m0, s48, 40960
ds_read_b64_tr_b16 a[48:49], v255 offset:55296
v_mfma_f32_16x16x32_bf16 v[86:89], a[24:27], v[106:109], v[86:89]
ds_read_b64_tr_b16 a[50:51], v255 offset:57600
v_mfma_f32_16x16x32_bf16 v[90:93], a[28:31], v[106:109], v[90:93]
global_load_lds_dwordx4 v0, s[40:41] offset:-4096
ds_read_b64_tr_b16 a[52:53], v255 offset:55488
v_mfma_f32_16x16x32_bf16 v[94:97], v[106:109], a[32:35], v[94:97]
ds_read_b64_tr_b16 a[54:55], v255 offset:57792
global_load_lds_dwordx4 v1, s[40:41] offset:-3072
v_mfma_f32_16x16x32_bf16 v[102:105], v[106:109], a[224:227], v[102:105]
ds_read_b64_tr_b16 a[56:57], v255 offset:55680
v_mfma_f32_16x16x32_bf16 v[110:113], a[24:27], v[182:185], v[110:113]
ds_read_b64_tr_b16 a[58:59], v255 offset:57984
global_load_lds_dwordx4 v2, s[40:41] offset:-2048
v_mfma_f32_16x16x32_bf16 v[114:117], a[28:31], v[182:185], v[114:117]
ds_read_b64_tr_b16 a[232:233], v163 offset:55296
v_mfma_f32_16x16x32_bf16 v[118:121], v[182:185], a[32:35], v[118:121]
global_load_lds_dwordx4 v3, s[40:41] offset:-1024
ds_read_b64_tr_b16 a[234:235], v163 offset:57600
v_mfma_f32_16x16x32_bf16 v[134:137], v[182:185], a[224:227], v[134:137]
ds_read_b128 v[166:169], v51 offset:4096
global_load_lds_dwordx4 v4, s[40:41] offset:0
v_mfma_f32_16x16x32_bf16 v[122:125], a[24:27], v[190:193], v[122:125]
ds_read_b128 v[174:177], v51 offset:22528
v_mfma_f32_16x16x32_bf16 v[126:129], a[28:31], v[190:193], v[126:129]
ds_read_b64_tr_b16 a[60:61], v162 offset:8192
global_load_lds_dwordx4 v5, s[40:41] offset:1024
v_mfma_f32_16x16x32_bf16 v[130:133], v[190:193], a[32:35], v[130:133]
ds_read_b64_tr_b16 a[62:63], v162 offset:10496
v_mfma_f32_16x16x32_bf16 v[158:161], v[190:193], a[224:227], v[158:161]
global_load_lds_dwordx4 v6, s[40:41] offset:2048
ds_read_b64_tr_b16 a[64:65], v162 offset:8384
v_mfma_f32_16x16x32_bf16 v[86:89], a[36:39], v[106:109], v[86:89]
ds_read_b64_tr_b16 a[66:67], v162 offset:10688
v_mfma_f32_16x16x32_bf16 v[90:93], a[40:43], v[106:109], v[90:93]
global_load_lds_dwordx4 v7, s[40:41] offset:3072
ds_read_b64_tr_b16 a[68:69], v162 offset:8576
v_mfma_f32_16x16x32_bf16 v[94:97], v[106:109], a[44:47], v[94:97]
s_add_u32 m0, s48, 49152
ds_read_b64_tr_b16 a[70:71], v162 offset:10880
v_mfma_f32_16x16x32_bf16 v[102:105], v[106:109], a[228:231], v[102:105]
ds_read_b64_tr_b16 a[236:237], v164 offset:8192
v_mfma_f32_16x16x32_bf16 v[110:113], a[36:39], v[186:189], v[110:113]
global_load_lds_dwordx4 v8, s[40:41] offset:-4096
ds_read_b64_tr_b16 a[238:239], v164 offset:10496
v_mfma_f32_16x16x32_bf16 v[114:117], a[40:43], v[186:189], v[114:117]
ds_read_b128 v[170:173], v52 offset:4096
v_mfma_f32_16x16x32_bf16 v[118:121], v[186:189], a[44:47], v[118:121]
s_add_u32 s40, s40, 221184
ds_read_b128 v[178:181], v52 offset:22528
v_mfma_f32_16x16x32_bf16 v[134:137], v[186:189], a[228:231], v[134:137]
v_mfma_f32_16x16x32_bf16 v[122:125], a[36:39], v[194:197], v[122:125]
s_addc_u32 s41, s41, 0
v_mfma_f32_16x16x32_bf16 v[126:129], a[40:43], v[194:197], v[126:129]
v_mfma_f32_16x16x32_bf16 v[130:133], v[194:197], a[44:47], v[130:133]
v_mfma_f32_16x16x32_bf16 v[158:161], v[194:197], a[228:231], v[158:161]
s_waitcnt vmcnt(9) lgkmcnt(0)
s_barrier
s_add_u32 m0, s48, 77824
ds_read_b64_tr_b16 a[72:73], v9 offset:47104
v_mfma_f32_16x16x32_bf16 v[86:89], a[48:51], v[106:109], v[86:89]
ds_read_b64_tr_b16 a[74:75], v9 offset:49408
v_mfma_f32_16x16x32_bf16 v[90:93], a[52:55], v[106:109], v[90:93]
global_load_lds_dwordx4 v0, s[40:41] offset:-4096
ds_read_b64_tr_b16 a[76:77], v9 offset:47296
v_mfma_f32_16x16x32_bf16 v[94:97], v[106:109], a[56:59], v[94:97]
ds_read_b64_tr_b16 a[78:79], v9 offset:49600
global_load_lds_dwordx4 v1, s[40:41] offset:-3072
v_mfma_f32_16x16x32_bf16 v[102:105], v[106:109], a[232:235], v[102:105]
ds_read_b64_tr_b16 a[80:81], v9 offset:47488
v_mfma_f32_16x16x32_bf16 v[110:113], a[48:51], v[166:169], v[110:113]
ds_read_b64_tr_b16 a[82:83], v9 offset:49792
global_load_lds_dwordx4 v2, s[40:41] offset:-2048
v_mfma_f32_16x16x32_bf16 v[114:117], a[52:55], v[166:169], v[114:117]
ds_read_b64_tr_b16 a[240:241], v10 offset:47104
v_mfma_f32_16x16x32_bf16 v[118:121], v[166:169], a[56:59], v[118:121]
global_load_lds_dwordx4 v3, s[40:41] offset:-1024
ds_read_b64_tr_b16 a[242:243], v10 offset:49408
v_mfma_f32_16x16x32_bf16 v[134:137], v[166:169], a[232:235], v[134:137]
ds_read_b128 v[182:185], v51 offset:6144
global_load_lds_dwordx4 v4, s[40:41] offset:0
v_mfma_f32_16x16x32_bf16 v[122:125], a[48:51], v[174:177], v[122:125]
ds_read_b128 v[190:193], v51 offset:24576
v_mfma_f32_16x16x32_bf16 v[126:129], a[52:55], v[174:177], v[126:129]
ds_read_b64_tr_b16 a[84:85], v255 offset:0
global_load_lds_dwordx4 v5, s[40:41] offset:1024
v_mfma_f32_16x16x32_bf16 v[130:133], v[174:177], a[56:59], v[130:133]
ds_read_b64_tr_b16 a[86:87], v255 offset:2304
v_mfma_f32_16x16x32_bf16 v[158:161], v[174:177], a[232:235], v[158:161]
global_load_lds_dwordx4 v6, s[40:41] offset:2048
ds_read_b64_tr_b16 a[88:89], v255 offset:192
v_mfma_f32_16x16x32_bf16 v[86:89], a[60:63], v[106:109], v[86:89]
ds_read_b64_tr_b16 a[90:91], v255 offset:2496
v_mfma_f32_16x16x32_bf16 v[90:93], a[64:67], v[106:109], v[90:93]
global_load_lds_dwordx4 v7, s[40:41] offset:3072
ds_read_b64_tr_b16 a[92:93], v255 offset:384
v_mfma_f32_16x16x32_bf16 v[94:97], v[106:109], a[68:71], v[94:97]
s_add_u32 m0, s48, 86016
ds_read_b64_tr_b16 a[94:95], v255 offset:2688
v_mfma_f32_16x16x32_bf16 v[102:105], v[106:109], a[236:239], v[102:105]
ds_read_b64_tr_b16 a[244:245], v163 offset:0
v_mfma_f32_16x16x32_bf16 v[110:113], a[60:63], v[170:173], v[110:113]
global_load_lds_dwordx4 v8, s[40:41] offset:-4096
ds_read_b64_tr_b16 a[246:247], v163 offset:2304
v_mfma_f32_16x16x32_bf16 v[114:117], a[64:67], v[170:173], v[114:117]
ds_read_b128 v[186:189], v52 offset:6144
v_mfma_f32_16x16x32_bf16 v[118:121], v[170:173], a[68:71], v[118:121]
s_add_u32 s40, s40, 221184
ds_read_b128 v[194:197], v52 offset:24576
v_mfma_f32_16x16x32_bf16 v[134:137], v[170:173], a[236:239], v[134:137]
v_mfma_f32_16x16x32_bf16 v[122:125], a[60:63], v[178:181], v[122:125]
s_addc_u32 s41, s41, 0
v_mfma_f32_16x16x32_bf16 v[126:129], a[64:67], v[178:181], v[126:129]
v_mfma_f32_16x16x32_bf16 v[130:133], v[178:181], a[68:71], v[130:133]
v_mfma_f32_16x16x32_bf16 v[158:161], v[178:181], a[236:239], v[158:161]
s_waitcnt vmcnt(9) lgkmcnt(0)
s_barrier
s_add_u32 m0, s48, 4096
ds_read_b64_tr_b16 a[96:97], v255 offset:18432
v_mfma_f32_16x16x32_bf16 v[86:89], a[72:75], v[106:109], v[86:89]
ds_read_b64_tr_b16 a[98:99], v255 offset:20736
v_mfma_f32_16x16x32_bf16 v[90:93], a[76:79], v[106:109], v[90:93]
global_load_lds_dwordx4 v0, s[40:41] offset:-4096
ds_read_b64_tr_b16 a[100:101], v255 offset:18624
v_mfma_f32_16x16x32_bf16 v[94:97], v[106:109], a[80:83], v[94:97]
ds_read_b64_tr_b16 a[102:103], v255 offset:20928
global_load_lds_dwordx4 v1, s[40:41] offset:-3072
v_mfma_f32_16x16x32_bf16 v[102:105], v[106:109], a[240:243], v[102:105]
ds_read_b64_tr_b16 a[104:105], v255 offset:18816
v_mfma_f32_16x16x32_bf16 v[110:113], a[72:75], v[182:185], v[110:113]
ds_read_b64_tr_b16 a[106:107], v255 offset:21120
global_load_lds_dwordx4 v2, s[40:41] offset:-2048
v_mfma_f32_16x16x32_bf16 v[114:117], a[76:79], v[182:185], v[114:117]
ds_read_b64_tr_b16 a[248:249], v163 offset:18432
v_mfma_f32_16x16x32_bf16 v[118:121], v[182:185], a[80:83], v[118:121]
global_load_lds_dwordx4 v3, s[40:41] offset:-1024
ds_read_b64_tr_b16 a[250:251], v163 offset:20736
v_mfma_f32_16x16x32_bf16 v[134:137], v[182:185], a[240:243], v[134:137]
ds_read_b128 v[166:169], v51 offset:8192
global_load_lds_dwordx4 v4, s[40:41] offset:0
v_mfma_f32_16x16x32_bf16 v[122:125], a[72:75], v[190:193], v[122:125]
ds_read_b128 v[174:177], v51 offset:26624
v_mfma_f32_16x16x32_bf16 v[126:129], a[76:79], v[190:193], v[126:129]
ds_read_b64_tr_b16 a[108:109], v255 offset:36864
global_load_lds_dwordx4 v5, s[40:41] offset:1024
v_mfma_f32_16x16x32_bf16 v[130:133], v[190:193], a[80:83], v[130:133]
ds_read_b64_tr_b16 a[110:111], v255 offset:39168
v_mfma_f32_16x16x32_bf16 v[158:161], v[190:193], a[240:243], v[158:161]
global_load_lds_dwordx4 v6, s[40:41] offset:2048
ds_read_b64_tr_b16 a[112:113], v255 offset:37056
v_mfma_f32_16x16x32_bf16 v[86:89], a[84:87], v[106:109], v[86:89]
ds_read_b64_tr_b16 a[114:115], v255 offset:39360
v_mfma_f32_16x16x32_bf16 v[90:93], a[88:91], v[106:109], v[90:93]
global_load_lds_dwordx4 v7, s[40:41] offset:3072
ds_read_b64_tr_b16 a[116:117], v255 offset:37248
v_mfma_f32_16x16x32_bf16 v[94:97], v[106:109], a[92:95], v[94:97]
s_add_u32 m0, s48, 12288
ds_read_b64_tr_b16 a[118:119], v255 offset:39552
v_mfma_f32_16x16x32_bf16 v[102:105], v[106:109], a[244:247], v[102:105]
ds_read_b64_tr_b16 a[252:253], v163 offset:36864
v_mfma_f32_16x16x32_bf16 v[110:113], a[84:87], v[186:189], v[110:113]
global_load_lds_dwordx4 v8, s[40:41] offset:-4096
ds_read_b64_tr_b16 a[254:255], v163 offset:39168
v_mfma_f32_16x16x32_bf16 v[114:117], a[88:91], v[186:189], v[114:117]
ds_read_b128 v[170:173], v52 offset:8192
v_mfma_f32_16x16x32_bf16 v[118:121], v[186:189], a[92:95], v[118:121]
s_add_u32 s40, s40, 221184
ds_read_b128 v[178:181], v52 offset:26624
v_mfma_f32_16x16x32_bf16 v[134:137], v[186:189], a[244:247], v[134:137]
v_mfma_f32_16x16x32_bf16 v[122:125], a[84:87], v[194:197], v[122:125]
s_addc_u32 s41, s41, 0
v_mfma_f32_16x16x32_bf16 v[126:129], a[88:91], v[194:197], v[126:129]
v_mfma_f32_16x16x32_bf16 v[130:133], v[194:197], a[92:95], v[130:133]
v_mfma_f32_16x16x32_bf16 v[158:161], v[194:197], a[244:247], v[158:161]
s_waitcnt vmcnt(9) lgkmcnt(0)
s_barrier
s_add_u32 m0, s48, 40960
ds_read_b64_tr_b16 a[120:121], v255 offset:55296
v_mfma_f32_16x16x32_bf16 v[86:89], a[96:99], v[106:109], v[86:89]
ds_read_b64_tr_b16 a[122:123], v255 offset:57600
v_mfma_f32_16x16x32_bf16 v[90:93], a[100:103], v[106:109], v[90:93]
global_load_lds_dwordx4 v0, s[40:41] offset:-4096
ds_read_b64_tr_b16 a[124:125], v255 offset:55488
v_mfma_f32_16x16x32_bf16 v[94:97], v[106:109], a[104:107], v[94:97]
ds_read_b64_tr_b16 a[126:127], v255 offset:57792
global_load_lds_dwordx4 v1, s[40:41] offset:-3072
v_mfma_f32_16x16x32_bf16 v[102:105], v[106:109], a[248:251], v[102:105]
ds_read_b64_tr_b16 a[128:129], v255 offset:55680
v_mfma_f32_16x16x32_bf16 v[110:113], a[96:99], v[166:169], v[110:113]
ds_read_b64_tr_b16 a[130:131], v255 offset:57984
global_load_lds_dwordx4 v2, s[40:41] offset:-2048
v_mfma_f32_16x16x32_bf16 v[114:117], a[100:103], v[166:169], v[114:117]
ds_read_b64_tr_b16 v[54:55], v163 offset:55296
v_mfma_f32_16x16x32_bf16 v[118:121], v[166:169], a[104:107], v[118:121]
global_load_lds_dwordx4 v3, s[40:41] offset:-1024
ds_read_b64_tr_b16 v[56:57], v163 offset:57600
v_mfma_f32_16x16x32_bf16 v[134:137], v[166:169], a[248:251], v[134:137]
ds_read_b128 v[182:185], v51 offset:10240
global_load_lds_dwordx4 v4, s[40:41] offset:0
v_mfma_f32_16x16x32_bf16 v[122:125], a[96:99], v[174:177], v[122:125]
ds_read_b128 v[190:193], v51 offset:28672
v_mfma_f32_16x16x32_bf16 v[126:129], a[100:103], v[174:177], v[126:129]
ds_read_b64_tr_b16 a[132:133], v162 offset:8192
global_load_lds_dwordx4 v5, s[40:41] offset:1024
v_mfma_f32_16x16x32_bf16 v[130:133], v[174:177], a[104:107], v[130:133]
ds_read_b64_tr_b16 a[134:135], v162 offset:10496
v_mfma_f32_16x16x32_bf16 v[158:161], v[174:177], a[248:251], v[158:161]
global_load_lds_dwordx4 v6, s[40:41] offset:2048
ds_read_b64_tr_b16 a[136:137], v162 offset:8384
v_mfma_f32_16x16x32_bf16 v[86:89], a[108:111], v[106:109], v[86:89]
ds_read_b64_tr_b16 a[138:139], v162 offset:10688
v_mfma_f32_16x16x32_bf16 v[90:93], a[112:115], v[106:109], v[90:93]
global_load_lds_dwordx4 v7, s[40:41] offset:3072
ds_read_b64_tr_b16 a[140:141], v162 offset:8576
v_mfma_f32_16x16x32_bf16 v[94:97], v[106:109], a[116:119], v[94:97]
s_add_u32 m0, s48, 49152
ds_read_b64_tr_b16 a[142:143], v162 offset:10880
v_mfma_f32_16x16x32_bf16 v[102:105], v[106:109], a[252:255], v[102:105]
ds_read_b64_tr_b16 v[58:59], v164 offset:8192
v_mfma_f32_16x16x32_bf16 v[110:113], a[108:111], v[170:173], v[110:113]
global_load_lds_dwordx4 v8, s[40:41] offset:-4096
ds_read_b64_tr_b16 v[60:61], v164 offset:10496
v_mfma_f32_16x16x32_bf16 v[114:117], a[112:115], v[170:173], v[114:117]
ds_read_b128 v[186:189], v52 offset:10240
v_mfma_f32_16x16x32_bf16 v[118:121], v[170:173], a[116:119], v[118:121]
s_add_u32 s40, s40, 221184
ds_read_b128 v[194:197], v52 offset:28672
v_mfma_f32_16x16x32_bf16 v[134:137], v[170:173], a[252:255], v[134:137]
v_mfma_f32_16x16x32_bf16 v[122:125], a[108:111], v[178:181], v[122:125]
s_addc_u32 s41, s41, 0
v_mfma_f32_16x16x32_bf16 v[126:129], a[112:115], v[178:181], v[126:129]
v_mfma_f32_16x16x32_bf16 v[130:133], v[178:181], a[116:119], v[130:133]
v_mfma_f32_16x16x32_bf16 v[158:161], v[178:181], a[252:255], v[158:161]
s_waitcnt vmcnt(9) lgkmcnt(0)
s_barrier
s_add_u32 m0, s48, 77824
ds_read_b64_tr_b16 a[144:145], v9 offset:47104
v_mfma_f32_16x16x32_bf16 v[86:89], a[120:123], v[106:109], v[86:89]
ds_read_b64_tr_b16 a[146:147], v9 offset:49408
v_mfma_f32_16x16x32_bf16 v[90:93], a[124:127], v[106:109], v[90:93]
global_load_lds_dwordx4 v0, s[40:41] offset:-4096
ds_read_b64_tr_b16 a[148:149], v9 offset:47296
v_mfma_f32_16x16x32_bf16 v[94:97], v[106:109], a[128:131], v[94:97]
ds_read_b64_tr_b16 a[150:151], v9 offset:49600
global_load_lds_dwordx4 v1, s[40:41] offset:-3072
v_mfma_f32_16x16x32_bf16 v[102:105], v[106:109], v[54:57], v[102:105]
ds_read_b64_tr_b16 a[152:153], v9 offset:47488
v_mfma_f32_16x16x32_bf16 v[110:113], a[120:123], v[182:185], v[110:113]
ds_read_b64_tr_b16 a[154:155], v9 offset:49792
global_load_lds_dwordx4 v2, s[40:41] offset:-2048
v_mfma_f32_16x16x32_bf16 v[114:117], a[124:127], v[182:185], v[114:117]
ds_read_b64_tr_b16 v[62:63], v10 offset:47104
v_mfma_f32_16x16x32_bf16 v[118:121], v[182:185], a[128:131], v[118:121]
global_load_lds_dwordx4 v3, s[40:41] offset:-1024
ds_read_b64_tr_b16 v[64:65], v10 offset:49408
v_mfma_f32_16x16x32_bf16 v[134:137], v[182:185], v[54:57], v[134:137]
ds_read_b128 v[166:169], v51 offset:12288
global_load_lds_dwordx4 v4, s[40:41] offset:0
v_mfma_f32_16x16x32_bf16 v[122:125], a[120:123], v[190:193], v[122:125]
ds_read_b128 v[174:177], v51 offset:30720
)"
R"(v_mfma_f32_16x16x32_bf16 v[126:129], a[124:127], v[190:193], v[126:129]
ds_read_b64_tr_b16 a[156:157], v255 offset:0
global_load_lds_dwordx4 v5, s[40:41] offset:1024
v_mfma_f32_16x16x32_bf16 v[130:133], v[190:193], a[128:131], v[130:133]
ds_read_b64_tr_b16 a[158:159], v255 offset:2304
v_mfma_f32_16x16x32_bf16 v[158:161], v[190:193], v[54:57], v[158:161]
global_load_lds_dwordx4 v6, s[40:41] offset:2048
ds_read_b64_tr_b16 a[160:161], v255 offset:192
v_mfma_f32_16x16x32_bf16 v[86:89], a[132:135], v[106:109], v[86:89]
ds_read_b64_tr_b16 a[162:163], v255 offset:2496
v_mfma_f32_16x16x32_bf16 v[90:93], a[136:139], v[106:109], v[90:93]
global_load_lds_dwordx4 v7, s[40:41] offset:3072
ds_read_b64_tr_b16 a[164:165], v255 offset:384
v_mfma_f32_16x16x32_bf16 v[94:97], v[106:109], a[140:143], v[94:97]
s_add_u32 m0, s48, 86016
ds_read_b64_tr_b16 a[166:167], v255 offset:2688
v_mfma_f32_16x16x32_bf16 v[102:105], v[106:109], v[58:61], v[102:105]
ds_read_b64_tr_b16 v[66:67], v163 offset:0
v_mfma_f32_16x16x32_bf16 v[110:113], a[132:135], v[186:189], v[110:113]
global_load_lds_dwordx4 v8, s[40:41] offset:-4096
ds_read_b64_tr_b16 v[68:69], v163 offset:2304
v_mfma_f32_16x16x32_bf16 v[114:117], a[136:139], v[186:189], v[114:117]
ds_read_b128 v[170:173], v52 offset:12288
v_mfma_f32_16x16x32_bf16 v[118:121], v[186:189], a[140:143], v[118:121]
s_sub_u32 s40, s40, 1769472
ds_read_b128 v[178:181], v52 offset:30720
v_mfma_f32_16x16x32_bf16 v[134:137], v[186:189], v[58:61], v[134:137]
v_mfma_f32_16x16x32_bf16 v[122:125], a[132:135], v[194:197], v[122:125]
s_subb_u32 s41, s41, 0
v_mfma_f32_16x16x32_bf16 v[126:129], a[136:139], v[194:197], v[126:129]
v_mfma_f32_16x16x32_bf16 v[130:133], v[194:197], a[140:143], v[130:133]
v_mfma_f32_16x16x32_bf16 v[158:161], v[194:197], v[58:61], v[158:161]
s_waitcnt vmcnt(9) lgkmcnt(0)
s_barrier
s_add_u32 m0, s49, 0
ds_read_b64_tr_b16 a[168:169], v255 offset:18432
v_mfma_f32_16x16x32_bf16 v[86:89], a[144:147], v[106:109], v[86:89]
ds_read_b64_tr_b16 a[170:171], v255 offset:20736
v_mfma_f32_16x16x32_bf16 v[90:93], a[148:151], v[106:109], v[90:93]
ds_read_b64_tr_b16 a[172:173], v255 offset:18624
v_mfma_f32_16x16x32_bf16 v[94:97], v[106:109], a[152:155], v[94:97]
ds_read_b64_tr_b16 a[174:175], v255 offset:20928
global_load_lds_dwordx4 v11, s[42:43] offset:-2048 sc1
v_mfma_f32_16x16x32_bf16 v[102:105], v[106:109], v[62:65], v[102:105]
ds_read_b64_tr_b16 a[176:177], v255 offset:18816
v_mfma_f32_16x16x32_bf16 v[110:113], a[144:147], v[166:169], v[110:113]
ds_read_b64_tr_b16 a[178:179], v255 offset:21120
v_mfma_f32_16x16x32_bf16 v[114:117], a[148:151], v[166:169], v[114:117]
ds_read_b64_tr_b16 v[70:71], v163 offset:18432
global_load_lds_dwordx4 v12, s[42:43] offset:-1024 sc1
v_mfma_f32_16x16x32_bf16 v[118:121], v[166:169], a[152:155], v[118:121]
ds_read_b64_tr_b16 v[72:73], v163 offset:20736
v_mfma_f32_16x16x32_bf16 v[134:137], v[166:169], v[62:65], v[134:137]
ds_read_b128 v[182:185], v51 offset:14336
v_mfma_f32_16x16x32_bf16 v[122:125], a[144:147], v[174:177], v[122:125]
ds_read_b128 v[190:193], v51 offset:32768
global_load_lds_dwordx4 v13, s[42:43] offset:0 sc1
v_mfma_f32_16x16x32_bf16 v[126:129], a[148:151], v[174:177], v[126:129]
ds_read_b64_tr_b16 a[180:181], v255 offset:36864
v_mfma_f32_16x16x32_bf16 v[130:133], v[174:177], a[152:155], v[130:133]
ds_read_b64_tr_b16 a[182:183], v255 offset:39168
v_mfma_f32_16x16x32_bf16 v[158:161], v[174:177], v[62:65], v[158:161]
ds_read_b64_tr_b16 a[184:185], v255 offset:37056
v_mfma_f32_16x16x32_bf16 v[86:89], a[156:159], v[106:109], v[86:89]
global_load_lds_dwordx4 v14, s[42:43] offset:1024 sc1
ds_read_b64_tr_b16 a[186:187], v255 offset:39360
v_mfma_f32_16x16x32_bf16 v[90:93], a[160:163], v[106:109], v[90:93]
ds_read_b64_tr_b16 a[188:189], v255 offset:37248
v_mfma_f32_16x16x32_bf16 v[94:97], v[106:109], a[164:167], v[94:97]
ds_read_b64_tr_b16 a[190:191], v255 offset:39552
v_mfma_f32_16x16x32_bf16 v[102:105], v[106:109], v[66:69], v[102:105]
global_load_lds_dwordx4 v15, s[42:43] offset:2048 sc1
ds_read_b64_tr_b16 v[74:75], v163 offset:36864
v_mfma_f32_16x16x32_bf16 v[110:113], a[156:159], v[170:173], v[110:113]
ds_read_b64_tr_b16 v[76:77], v163 offset:39168
v_mfma_f32_16x16x32_bf16 v[114:117], a[160:163], v[170:173], v[114:117]
ds_read_b128 v[186:189], v52 offset:14336
v_mfma_f32_16x16x32_bf16 v[118:121], v[170:173], a[164:167], v[118:121]
s_sub_u32 s42, s42, 18432
s_subb_u32 s43, s43, 0
ds_read_b128 v[194:197], v52 offset:32768
v_mfma_f32_16x16x32_bf16 v[134:137], v[170:173], v[66:69], v[134:137]
v_mfma_f32_16x16x32_bf16 v[122:125], a[156:159], v[178:181], v[122:125]
v_mfma_f32_16x16x32_bf16 v[126:129], a[160:163], v[178:181], v[126:129]
v_mfma_f32_16x16x32_bf16 v[130:133], v[178:181], a[164:167], v[130:133]
v_mfma_f32_16x16x32_bf16 v[158:161], v[178:181], v[66:69], v[158:161]
s_waitcnt vmcnt(5) lgkmcnt(0)
s_barrier
s_add_u32 m0, s49, 18432
ds_read_b64_tr_b16 a[192:193], v255 offset:55296
v_mfma_f32_16x16x32_bf16 v[86:89], a[168:171], v[106:109], v[86:89]
ds_read_b64_tr_b16 a[194:195], v255 offset:57600
v_mfma_f32_16x16x32_bf16 v[90:93], a[172:175], v[106:109], v[90:93]
ds_read_b64_tr_b16 a[196:197], v255 offset:55488
v_mfma_f32_16x16x32_bf16 v[94:97], v[106:109], a[176:179], v[94:97]
ds_read_b64_tr_b16 a[198:199], v255 offset:57792
global_load_lds_dwordx4 v11, s[42:43] offset:-2048 sc1
v_mfma_f32_16x16x32_bf16 v[102:105], v[106:109], v[70:73], v[102:105]
ds_read_b64_tr_b16 a[200:201], v255 offset:55680
v_mfma_f32_16x16x32_bf16 v[110:113], a[168:171], v[182:185], v[110:113]
ds_read_b64_tr_b16 a[202:203], v255 offset:57984
v_mfma_f32_16x16x32_bf16 v[114:117], a[172:175], v[182:185], v[114:117]
ds_read_b64_tr_b16 v[78:79], v163 offset:55296
global_load_lds_dwordx4 v12, s[42:43] offset:-1024 sc1
v_mfma_f32_16x16x32_bf16 v[118:121], v[182:185], a[176:179], v[118:121]
ds_read_b64_tr_b16 v[80:81], v163 offset:57600
v_mfma_f32_16x16x32_bf16 v[134:137], v[182:185], v[70:73], v[134:137]
ds_read_b128 v[166:169], v51 offset:16384
v_mfma_f32_16x16x32_bf16 v[122:125], a[168:171], v[190:193], v[122:125]
ds_read_b128 v[174:177], v51 offset:34816
global_load_lds_dwordx4 v13, s[42:43] offset:0 sc1
v_mfma_f32_16x16x32_bf16 v[126:129], a[172:175], v[190:193], v[126:129]
ds_read_b64_tr_b16 a[204:205], v162 offset:8192
v_mfma_f32_16x16x32_bf16 v[130:133], v[190:193], a[176:179], v[130:133]
ds_read_b64_tr_b16 a[206:207], v162 offset:10496
v_mfma_f32_16x16x32_bf16 v[158:161], v[190:193], v[70:73], v[158:161]
ds_read_b64_tr_b16 a[208:209], v162 offset:8384
global_load_lds_dwordx4 v14, s[42:43] offset:1024 sc1
v_mfma_f32_16x16x32_bf16 v[86:89], a[180:183], v[106:109], v[86:89]
ds_read_b64_tr_b16 a[210:211], v162 offset:10688
v_mfma_f32_16x16x32_bf16 v[90:93], a[184:187], v[106:109], v[90:93]
ds_read_b64_tr_b16 a[212:213], v162 offset:8576
v_mfma_f32_16x16x32_bf16 v[94:97], v[106:109], a[188:191], v[94:97]
ds_read_b64_tr_b16 a[214:215], v162 offset:10880
global_load_lds_dwordx4 v15, s[42:43] offset:2048 sc1
v_mfma_f32_16x16x32_bf16 v[102:105], v[106:109], v[74:77], v[102:105]
ds_read_b64_tr_b16 v[82:83], v164 offset:8192
v_mfma_f32_16x16x32_bf16 v[110:113], a[180:183], v[186:189], v[110:113]
ds_read_b64_tr_b16 v[84:85], v164 offset:10496
v_mfma_f32_16x16x32_bf16 v[114:117], a[184:187], v[186:189], v[114:117]
ds_read_b128 v[170:173], v52 offset:16384
s_sub_u32 s42, s42, 18432
s_subb_u32 s43, s43, 0
v_mfma_f32_16x16x32_bf16 v[118:121], v[186:189], a[188:191], v[118:121]
ds_read_b128 v[178:181], v52 offset:34816
v_mfma_f32_16x16x32_bf16 v[134:137], v[186:189], v[74:77], v[134:137]
v_mfma_f32_16x16x32_bf16 v[122:125], a[180:183], v[194:197], v[122:125]
v_mfma_f32_16x16x32_bf16 v[126:129], a[184:187], v[194:197], v[126:129]
v_mfma_f32_16x16x32_bf16 v[158:161], v[194:197], v[74:77], v[158:161]
s_waitcnt lgkmcnt(15)
v_mfma_f32_16x16x32_bf16 v[86:89], a[192:195], v[106:109], v[86:89]
v_mfma_f32_16x16x32_bf16 v[90:93], a[196:199], v[106:109], v[90:93]
s_waitcnt lgkmcnt(14)
v_mfma_f32_16x16x32_bf16 v[94:97], v[106:109], a[200:203], v[94:97]
s_waitcnt lgkmcnt(12)
v_mfma_f32_16x16x32_bf16 v[102:105], v[106:109], v[78:81], v[102:105]
s_waitcnt lgkmcnt(11)
v_mfma_f32_16x16x32_bf16 v[110:113], a[192:195], v[166:169], v[110:113]
v_mfma_f32_16x16x32_bf16 v[114:117], a[196:199], v[166:169], v[114:117]
v_mfma_f32_16x16x32_bf16 v[118:121], v[166:169], a[200:203], v[118:121]
v_mfma_f32_16x16x32_bf16 v[134:137], v[166:169], v[78:81], v[134:137]
s_waitcnt lgkmcnt(10)
v_mfma_f32_16x16x32_bf16 v[158:161], v[174:177], v[78:81], v[158:161]
s_waitcnt lgkmcnt(8)
v_mfma_f32_16x16x32_bf16 v[86:89], a[204:207], v[106:109], v[86:89]
s_waitcnt lgkmcnt(6)
v_mfma_f32_16x16x32_bf16 v[90:93], a[208:211], v[106:109], v[90:93]
s_waitcnt lgkmcnt(4)
v_mfma_f32_16x16x32_bf16 v[94:97], v[106:109], a[212:215], v[94:97]
s_waitcnt lgkmcnt(2)
v_mfma_f32_16x16x32_bf16 v[102:105], v[106:109], v[82:85], v[102:105]
s_waitcnt lgkmcnt(1)
v_mfma_f32_16x16x32_bf16 v[110:113], a[204:207], v[170:173], v[110:113]
v_mfma_f32_16x16x32_bf16 v[114:117], a[208:211], v[170:173], v[114:117]
v_mfma_f32_16x16x32_bf16 v[118:121], v[170:173], a[212:215], v[118:121]
v_mfma_f32_16x16x32_bf16 v[134:137], v[170:173], v[82:85], v[134:137]
s_waitcnt lgkmcnt(0)
v_mfma_f32_16x16x32_bf16 v[158:161], v[178:181], v[82:85], v[158:161]
s_barrier
ds_read_b128 v[242:245], v25 offset:0
s_waitcnt lgkmcnt(0)
s_nop 2
v_fma_f32 v230, v102, v242, v134
v_fma_f32 v231, v102, v243, v135
v_fma_f32 v232, v102, v244, v136
v_fma_f32 v233, v102, v245, v137
v_cvt_pk_bf16_f32 v230, v230, v231
v_cvt_pk_bf16_f32 v231, v232, v233
ds_write_b64 v22, v[230:231] offset:41472
ds_read_b128 v[198:201], v51 offset:32768
ds_read_b128 v[202:205], v52 offset:32768
ds_read_b128 v[206:209], v51 offset:34816
ds_read_b128 v[210:213], v52 offset:34816
v_mov_b32 v234, v32
v_mov_b32 v235, v33
v_mov_b32 v236, v34
v_mov_b32 v237, v35
v_mov_b32 v134, v158
v_mov_b32 v135, v159
v_mov_b32 v136, v160
v_mov_b32 v137, v161
s_waitcnt lgkmcnt(4)
s_barrier
ds_read_b32 v23, v24 offset:0
ds_read_b32 v148, v21
ds_read_b32 v26, v24 offset:192
ds_read_b32 v53, v27
ds_read_b32 v95, v28
ds_read_b128 v[226:229], v25 offset:0
ds_read_b64 v[158:159], v22 offset:41472
ds_read_b32 v140, v20
v_xor_b32 v51, 0x10000, v51
v_xor_b32 v52, 0x10000, v52
s_waitcnt lgkmcnt(7)
v_fma_f32 v238, v90, v23, v114
v_fma_f32 v239, v91, v23, v115
v_fma_f32 v240, v92, v23, v116
v_fma_f32 v241, v93, v23, v117
v_cvt_pk_bf16_f32 v146, v238, v239
v_cvt_pk_bf16_f32 v147, v240, v241
s_waitcnt lgkmcnt(0)
s_barrier
ds_read_b128 v[242:245], v25 offset:64
v_fma_f32 v238, v94, v226, v118
v_fma_f32 v239, v94, v227, v119
v_fma_f32 v240, v94, v228, v120
v_fma_f32 v241, v94, v229, v121
v_cvt_pk_bf16_f32 v154, v238, v239
v_cvt_pk_bf16_f32 v155, v240, v241
v_fma_f32 v238, v86, v23, v110
v_fma_f32 v239, v87, v23, v111
v_fma_f32 v240, v88, v23, v112
v_fma_f32 v241, v89, v23, v113
v_cvt_pk_bf16_f32 v138, v238, v239
v_cvt_pk_bf16_f32 v139, v240, v241
s_waitcnt lgkmcnt(0)
v_fma_f32 v230, v102, v242, v134
v_fma_f32 v231, v102, v243, v135
v_fma_f32 v232, v102, v244, v136
v_fma_f32 v233, v102, v245, v137
v_cvt_pk_bf16_f32 v230, v230, v231
v_cvt_pk_bf16_f32 v231, v232, v233
ds_write_b64 v22, v[230:231] offset:41472
s_waitcnt vmcnt(5) lgkmcnt(0)
s_barrier
s_mov_b32 s70, 32
s_waitcnt lgkmcnt(0)
s_nop 1
s_branch .Lqa_in26
.p2align 6
.Lqa_top45:
s_waitcnt lgkmcnt(9)
v_mfma_f32_16x16x32_bf16 v[134:137], v[44:47], a[216:219], 0
s_waitcnt lgkmcnt(8)
ds_read_b128 v[162:165], v52 offset:4096
ds_read_b32 v95, v30
v_mfma_f32_16x16x32_bf16 v[134:137], v[96:99], a[220:223], v[134:137]
ds_read_b128 v[226:229], v25 offset:64
ds_read_b64 v[160:161], v22 offset:41472
v_mfma_f32_16x16x32_bf16 v[110:113], a[0:3], v[44:47], 0
s_waitcnt lgkmcnt(8)
ds_read_b128 v[166:169], v51 offset:6144
ds_read_b32 v144, v20
v_mfma_f32_16x16x32_bf16 v[134:137], v[104:107], a[224:227], v[134:137]
s_waitcnt lgkmcnt(8)
ds_read_b128 v[170:173], v52 offset:6144
v_fma_f32 v238, v90, v23, v126
v_mfma_f32_16x16x32_bf16 v[134:137], v[0:3], a[228:231], v[134:137]
global_store_dwordx4 v16, v[230:233], s[38:39] sc1
v_mfma_f32_16x16x32_bf16 v[114:117], a[4:7], v[44:47], 0
s_waitcnt lgkmcnt(7)
ds_read_b128 v[174:177], v51 offset:8192
v_add_u32 v16, 18432, v16
v_mfma_f32_16x16x32_bf16 v[134:137], v[4:7], a[232:235], v[134:137]
s_waitcnt lgkmcnt(7)
ds_read_b128 v[178:181], v52 offset:8192
v_fma_f32 v239, v91, v23, v127
v_mfma_f32_16x16x32_bf16 v[134:137], v[162:165], a[236:239], v[134:137]
v_fma_f32 v240, v92, v23, v128
v_fma_f32 v241, v93, v23, v129
v_mfma_f32_16x16x32_bf16 v[118:121], v[44:47], a[8:11], 0
s_waitcnt lgkmcnt(4)
ds_read_b128 v[182:185], v51 offset:10240
v_cvt_pk_bf16_f32 v150, v238, v239
v_mfma_f32_16x16x32_bf16 v[134:137], v[166:169], a[240:243], v[134:137]
s_waitcnt lgkmcnt(3)
ds_read_b128 v[186:189], v52 offset:10240
v_cvt_pk_bf16_f32 v151, v240, v241
v_mfma_f32_16x16x32_bf16 v[134:137], v[170:173], a[244:247], v[134:137]
s_nop 0
v_mfma_f32_16x16x32_bf16 v[214:217], v[150:153], v[138:141], v[32:35]
v_max_f32 v109, v218, v219
v_max_f32 v254, v220, v221
v_mfma_f32_16x16x32_bf16 v[110:113], a[12:15], v[96:99], v[110:113]
s_waitcnt lgkmcnt(3)
ds_read_b128 v[190:193], v51 offset:12288
v_fma_f32 v238, v94, v226, v130
v_mfma_f32_16x16x32_bf16 v[134:137], v[174:177], a[248:251], v[134:137]
s_xor_b32 s49, s49, 0x10000
ds_read_b128 v[194:197], v52 offset:12288
s_waitcnt lgkmcnt(4)
v_mfma_f32_16x16x32_bf16 v[134:137], v[178:181], a[252:255], v[134:137]
s_add_u32 m0, s49, 0
v_mul_f32 v26, 0x3eae1a93, v26
v_mul_f32 v103, v53, v26
v_mfma_f32_16x16x32_bf16 v[114:117], a[16:19], v[96:99], v[114:117]
s_waitcnt lgkmcnt(3)
ds_read_b128 v[198:201], v51 offset:14336
v_mul_f32 v108, v95, v26
v_mfma_f32_16x16x32_bf16 v[134:137], v[182:185], v[54:57], v[134:137]
s_waitcnt lgkmcnt(3)
ds_read_b128 v[202:205], v52 offset:14336
v_max3_f32 v109, v214, v215, v109
v_mfma_f32_16x16x32_bf16 v[134:137], v[186:189], v[58:61], v[134:137]
global_load_lds_dwordx4 v11, s[42:43] offset:-2048 sc1
v_max3_f32 v254, v216, v217, v254
v_mfma_f32_16x16x32_bf16 v[118:121], v[96:99], a[20:23], v[118:121]
s_waitcnt lgkmcnt(3)
ds_read_b128 v[206:209], v51 offset:16384
v_mul_f32 v109, v109, v103
v_mfma_f32_16x16x32_bf16 v[134:137], v[190:193], v[62:65], v[134:137]
s_waitcnt lgkmcnt(3)
ds_read_b128 v[210:213], v52 offset:16384
v_mul_f32 v254, v254, v108
v_mfma_f32_16x16x32_bf16 v[134:137], v[194:197], v[66:69], v[134:137]
v_max_f32 v255, v109, v254
v_mov_b32 v8, v255
v_mfma_f32_16x16x32_bf16 v[110:113], a[24:27], v[104:107], v[110:113]
s_waitcnt lgkmcnt(3)
v_permlane16_swap_b32 v255, v8
v_mfma_f32_16x16x32_bf16 v[134:137], v[198:201], v[70:73], v[134:137]
s_waitcnt lgkmcnt(2)
v_max_f32 v255, v255, v8
v_mov_b32 v8, v255
v_mfma_f32_16x16x32_bf16 v[134:137], v[202:205], v[74:77], v[134:137]
s_nop 0
v_permlane32_swap_b32 v255, v8
v_mfma_f32_16x16x32_bf16 v[114:117], a[28:31], v[104:107], v[114:117]
s_waitcnt lgkmcnt(1)
v_max_f32 v255, v255, v8
v_sub_f32 v109, v109, v255
v_mfma_f32_16x16x32_bf16 v[134:137], v[206:209], v[78:81], v[134:137]
s_waitcnt lgkmcnt(0)
global_load_lds_dwordx4 v12, s[42:43] offset:-1024 sc1
v_sub_f32 v254, v254, v255
v_mfma_f32_16x16x32_bf16 v[134:137], v[210:213], v[82:85], v[134:137]
v_exp_f32 v9, v109
v_mfma_f32_16x16x32_bf16 v[118:121], v[104:107], a[32:35], v[118:121]
v_exp_f32 v10, v254
v_mfma_f32_16x16x32_bf16 v[110:113], a[36:39], v[0:3], v[110:113]
v_add_f32 v255, v9, v10
v_mov_b32 v8, v255
v_mfma_f32_16x16x32_bf16 v[114:117], a[40:43], v[0:3], v[114:117]
v_mul_f32 v9, v9, v53
v_fma_f32 v239, v94, v227, v131
v_mfma_f32_16x16x32_bf16 v[118:121], v[0:3], a[44:47], v[118:121]
v_permlane16_swap_b32 v255, v8
v_mfma_f32_16x16x32_bf16 v[110:113], a[48:51], v[4:7], v[110:113]
v_add_f32 v255, v255, v8
v_mov_b32 v8, v255
v_mfma_f32_16x16x32_bf16 v[114:117], a[52:55], v[4:7], v[114:117]
v_mul_f32 v10, v10, v95
v_fma_f32 v240, v94, v228, v132
v_mfma_f32_16x16x32_bf16 v[118:121], v[4:7], a[56:59], v[118:121]
global_load_lds_dwordx4 v13, s[42:43] offset:0 sc1
v_fma_f32 v241, v94, v229, v133
v_mfma_f32_16x16x32_bf16 v[110:113], a[60:63], v[162:165], v[110:113]
v_permlane32_swap_b32 v255, v8
v_mfma_f32_16x16x32_bf16 v[114:117], a[64:67], v[162:165], v[114:117]
v_add_f32 v255, v255, v8
v_cvt_pk_bf16_f32 v156, v238, v239
v_mfma_f32_16x16x32_bf16 v[118:121], v[162:165], a[68:71], v[118:121]
v_mfma_f32_16x16x32_bf16 v[110:113], a[72:75], v[166:169], v[110:113]
s_barrier
v_mfma_f32_16x16x32_bf16 v[114:117], a[76:79], v[166:169], v[114:117]
ds_read_b128 v[242:245], v25 offset:0
v_cvt_pk_bf16_f32 v157, v240, v241
v_mfma_f32_16x16x32_bf16 v[118:121], v[166:169], a[80:83], v[118:121]
v_rcp_f32 v255, v255
v_mfma_f32_16x16x32_bf16 v[110:113], a[84:87], v[170:173], v[110:113]
v_mul_f32 v9, v9, v255
v_mul_f32 v10, v10, v255
v_mfma_f32_16x16x32_bf16 v[114:117], a[88:91], v[170:173], v[114:117]
global_load_lds_dwordx4 v14, s[42:43] offset:1024 sc1
v_cvt_pk_bf16_f32 v9, v9, v9
v_mfma_f32_16x16x32_bf16 v[118:121], v[170:173], a[92:95], v[118:121]
v_cvt_pk_bf16_f32 v10, v10, v10
v_and_b32 v224, v9, v40
v_mfma_f32_16x16x32_bf16 v[110:113], a[96:99], v[174:177], v[110:113]
v_and_b32 v225, v10, v41
v_and_b32 v222, v9, v42
v_mfma_f32_16x16x32_bf16 v[114:117], a[100:103], v[174:177], v[114:117]
s_waitcnt lgkmcnt(0)
v_and_b32 v223, v10, v43
v_fma_f32 v230, v102, v242, v134
v_mfma_f32_16x16x32_bf16 v[118:121], v[174:177], a[104:107], v[118:121]
v_mfma_f32_16x16x32_bf16 v[218:221], v[154:157], v[222:225], 0
v_mfma_f32_16x16x32_bf16 v[214:217], v[158:161], v[222:225], 0
v_fma_f32 v231, v102, v243, v135
v_fma_f32 v232, v102, v244, v136
v_mfma_f32_16x16x32_bf16 v[110:113], a[108:111], v[178:181], v[110:113]
v_fma_f32 v233, v102, v245, v137
v_cvt_pk_bf16_f32 v230, v230, v231
v_mfma_f32_16x16x32_bf16 v[114:117], a[112:115], v[178:181], v[114:117]
v_cvt_pk_bf16_f32 v231, v232, v233
v_fma_f32 v238, v86, v23, v122
v_mfma_f32_16x16x32_bf16 v[118:121], v[178:181], a[116:119], v[118:121]
ds_write_b64 v22, v[230:231] offset:41472
v_mfma_f32_16x16x32_bf16 v[110:113], a[120:123], v[182:185], v[110:113]
global_load_lds_dwordx4 v15, s[42:43] offset:2048 sc1
)"
R"(s_add_u32 s42, s42, s46
s_addc_u32 s43, s43, s47
v_mfma_f32_16x16x32_bf16 v[114:117], a[124:127], v[182:185], v[114:117]
v_cvt_pk_bf16_f32 v100, v218, v219
v_cvt_pk_bf16_f32 v101, v220, v221
v_mfma_f32_16x16x32_bf16 v[118:121], v[182:185], a[128:131], v[118:121]
ds_write_b64 v18, v[100:101]
v_mfma_f32_16x16x32_bf16 v[110:113], a[132:135], v[186:189], v[110:113]
v_cvt_pk_bf16_f32 v9, v216, v217
v_fma_f32 v239, v87, v23, v123
v_mfma_f32_16x16x32_bf16 v[114:117], a[136:139], v[186:189], v[114:117]
ds_write_b32 v19, v9
v_mfma_f32_16x16x32_bf16 v[118:121], v[186:189], a[140:143], v[118:121]
v_fma_f32 v240, v88, v23, v124
v_fma_f32 v241, v89, v23, v125
v_mfma_f32_16x16x32_bf16 v[110:113], a[144:147], v[190:193], v[110:113]
v_cvt_pk_bf16_f32 v142, v238, v239
v_cvt_pk_bf16_f32 v143, v240, v241
v_mfma_f32_16x16x32_bf16 v[114:117], a[148:151], v[190:193], v[114:117]
v_mfma_f32_16x16x32_bf16 v[118:121], v[190:193], a[152:155], v[118:121]
v_mfma_f32_16x16x32_bf16 v[110:113], a[156:159], v[194:197], v[110:113]
v_mfma_f32_16x16x32_bf16 v[114:117], a[160:163], v[194:197], v[114:117]
v_mfma_f32_16x16x32_bf16 v[118:121], v[194:197], a[164:167], v[118:121]
v_mfma_f32_16x16x32_bf16 v[110:113], a[168:171], v[198:201], v[110:113]
v_mfma_f32_16x16x32_bf16 v[114:117], a[172:175], v[198:201], v[114:117]
v_mfma_f32_16x16x32_bf16 v[118:121], v[198:201], a[176:179], v[118:121]
v_mfma_f32_16x16x32_bf16 v[110:113], a[180:183], v[202:205], v[110:113]
v_mfma_f32_16x16x32_bf16 v[114:117], a[184:187], v[202:205], v[114:117]
s_waitcnt vmcnt(5) lgkmcnt(0)
s_barrier
v_mfma_f32_16x16x32_bf16 v[118:121], v[202:205], a[188:191], v[118:121]
ds_read_b128 v[44:47], v51 offset:18432
v_mfma_f32_16x16x32_bf16 v[110:113], a[192:195], v[206:209], v[110:113]
ds_read_b128 v[96:99], v52 offset:18432
ds_read_b128 v[230:233], v17 offset:0
v_mfma_f32_16x16x32_bf16 v[114:117], a[196:199], v[206:209], v[114:117]
ds_read_b32 v23, v24 offset:0
ds_read_b32 v148, v21
v_mfma_f32_16x16x32_bf16 v[218:221], v[150:153], v[142:145], v[36:39]
v_mfma_f32_16x16x32_bf16 v[118:121], v[206:209], a[200:203], v[118:121]
ds_read_b128 v[104:107], v51 offset:20480
ds_read_b32 v26, v24 offset:192
v_mfma_f32_16x16x32_bf16 v[110:113], a[204:207], v[210:213], v[110:113]
ds_read_b128 v[0:3], v52 offset:20480
ds_read_b32 v53, v27
v_mfma_f32_16x16x32_bf16 v[114:117], a[208:211], v[210:213], v[114:117]
ds_read_b32 v95, v28
ds_read_b128 v[226:229], v25 offset:0
v_mfma_f32_16x16x32_bf16 v[118:121], v[210:213], a[212:215], v[118:121]
s_waitcnt lgkmcnt(10)
ds_read_b128 v[4:7], v51 offset:22528
ds_read_b64 v[158:159], v22 offset:41472
v_mfma_f32_16x16x32_bf16 v[134:137], v[44:47], a[216:219], 0
s_waitcnt lgkmcnt(11)
ds_read_b128 v[162:165], v52 offset:22528
ds_read_b32 v140, v20
v_mfma_f32_16x16x32_bf16 v[134:137], v[96:99], a[220:223], v[134:137]
v_max_f32 v109, v218, v219
v_max_f32 v254, v220, v221
v_mfma_f32_16x16x32_bf16 v[122:125], a[0:3], v[44:47], 0
s_waitcnt lgkmcnt(11)
ds_read_b128 v[166:169], v51 offset:24576
v_fma_f32 v238, v90, v23, v114
s_waitcnt lgkmcnt(10)
v_mfma_f32_16x16x32_bf16 v[134:137], v[104:107], a[224:227], v[134:137]
s_waitcnt lgkmcnt(8)
ds_read_b128 v[170:173], v52 offset:24576
v_fma_f32 v239, v91, v23, v115
v_mfma_f32_16x16x32_bf16 v[134:137], v[0:3], a[228:231], v[134:137]
global_store_dwordx4 v16, v[230:233], s[38:39] sc1
v_mfma_f32_16x16x32_bf16 v[126:129], a[4:7], v[44:47], 0
s_waitcnt lgkmcnt(5)
ds_read_b128 v[174:177], v51 offset:26624
v_add_u32 v16, 18432, v16
v_mfma_f32_16x16x32_bf16 v[134:137], v[4:7], a[232:235], v[134:137]
s_waitcnt lgkmcnt(4)
ds_read_b128 v[178:181], v52 offset:26624
v_fma_f32 v240, v92, v23, v116
v_mfma_f32_16x16x32_bf16 v[134:137], v[162:165], a[236:239], v[134:137]
v_fma_f32 v241, v93, v23, v117
v_cvt_pk_bf16_f32 v146, v238, v239
v_mfma_f32_16x16x32_bf16 v[130:133], v[44:47], a[8:11], 0
s_waitcnt lgkmcnt(3)
ds_read_b128 v[182:185], v51 offset:28672
v_cvt_pk_bf16_f32 v147, v240, v241
v_mfma_f32_16x16x32_bf16 v[134:137], v[166:169], a[240:243], v[134:137]
ds_read_b128 v[186:189], v52 offset:28672
v_mfma_f32_16x16x32_bf16 v[214:217], v[146:149], v[142:145], v[234:237]
s_waitcnt lgkmcnt(4)
v_mul_f32 v26, 0x3eae1a93, v26
v_mul_f32 v103, v53, v26
v_mfma_f32_16x16x32_bf16 v[134:137], v[170:173], a[244:247], v[134:137]
v_mul_f32 v108, v95, v26
v_fma_f32 v238, v94, v226, v118
v_mfma_f32_16x16x32_bf16 v[122:125], a[12:15], v[96:99], v[122:125]
s_waitcnt lgkmcnt(3)
ds_read_b128 v[190:193], v51 offset:30720
v_fma_f32 v239, v94, v227, v119
v_mfma_f32_16x16x32_bf16 v[134:137], v[174:177], a[248:251], v[134:137]
s_add_u32 m0, s49, 18432
ds_read_b128 v[194:197], v52 offset:30720
s_waitcnt lgkmcnt(4)
v_mfma_f32_16x16x32_bf16 v[134:137], v[178:181], a[252:255], v[134:137]
v_max3_f32 v109, v214, v215, v109
v_max3_f32 v254, v216, v217, v254
v_mfma_f32_16x16x32_bf16 v[126:129], a[16:19], v[96:99], v[126:129]
s_waitcnt lgkmcnt(3)
ds_read_b128 v[198:201], v51 offset:32768
v_mul_f32 v109, v109, v103
v_mfma_f32_16x16x32_bf16 v[134:137], v[182:185], v[54:57], v[134:137]
s_waitcnt lgkmcnt(3)
ds_read_b128 v[202:205], v52 offset:32768
global_load_lds_dwordx4 v11, s[42:43] offset:-2048 sc1
v_mfma_f32_16x16x32_bf16 v[134:137], v[186:189], v[58:61], v[134:137]
v_mul_f32 v254, v254, v108
v_max_f32 v255, v109, v254
v_mfma_f32_16x16x32_bf16 v[130:133], v[96:99], a[20:23], v[130:133]
s_waitcnt lgkmcnt(3)
ds_read_b128 v[206:209], v51 offset:34816
v_mov_b32 v8, v255
v_mfma_f32_16x16x32_bf16 v[134:137], v[190:193], v[62:65], v[134:137]
s_waitcnt lgkmcnt(3)
ds_read_b128 v[210:213], v52 offset:34816
v_fma_f32 v240, v94, v228, v120
v_mfma_f32_16x16x32_bf16 v[134:137], v[194:197], v[66:69], v[134:137]
v_permlane16_swap_b32 v255, v8
v_mfma_f32_16x16x32_bf16 v[122:125], a[24:27], v[104:107], v[122:125]
s_waitcnt lgkmcnt(3)
v_max_f32 v255, v255, v8
v_mov_b32 v8, v255
v_mfma_f32_16x16x32_bf16 v[134:137], v[198:201], v[70:73], v[134:137]
s_waitcnt lgkmcnt(2)
v_permlane32_swap_b32 v255, v8
v_mfma_f32_16x16x32_bf16 v[134:137], v[202:205], v[74:77], v[134:137]
v_max_f32 v255, v255, v8
v_sub_f32 v109, v109, v255
v_mfma_f32_16x16x32_bf16 v[126:129], a[28:31], v[104:107], v[126:129]
s_waitcnt lgkmcnt(1)
global_load_lds_dwordx4 v12, s[42:43] offset:-1024 sc1
v_sub_f32 v254, v254, v255
v_mfma_f32_16x16x32_bf16 v[134:137], v[206:209], v[78:81], v[134:137]
s_waitcnt lgkmcnt(0)
v_exp_f32 v9, v109
v_mfma_f32_16x16x32_bf16 v[134:137], v[210:213], v[82:85], v[134:137]
v_exp_f32 v10, v254
v_mfma_f32_16x16x32_bf16 v[130:133], v[104:107], a[32:35], v[130:133]
v_add_f32 v255, v9, v10
v_mov_b32 v8, v255
v_mfma_f32_16x16x32_bf16 v[122:125], a[36:39], v[0:3], v[122:125]
v_mul_f32 v9, v9, v53
v_fma_f32 v241, v94, v229, v121
v_mfma_f32_16x16x32_bf16 v[126:129], a[40:43], v[0:3], v[126:129]
v_permlane16_swap_b32 v255, v8
v_mfma_f32_16x16x32_bf16 v[130:133], v[0:3], a[44:47], v[130:133]
v_add_f32 v255, v255, v8
v_mov_b32 v8, v255
v_mfma_f32_16x16x32_bf16 v[122:125], a[48:51], v[4:7], v[122:125]
v_mul_f32 v10, v10, v95
v_cvt_pk_bf16_f32 v154, v238, v239
v_mfma_f32_16x16x32_bf16 v[126:129], a[52:55], v[4:7], v[126:129]
global_load_lds_dwordx4 v13, s[42:43] offset:0 sc1
v_cvt_pk_bf16_f32 v155, v240, v241
v_mfma_f32_16x16x32_bf16 v[130:133], v[4:7], a[56:59], v[130:133]
v_permlane32_swap_b32 v255, v8
v_mfma_f32_16x16x32_bf16 v[122:125], a[60:63], v[162:165], v[122:125]
v_add_f32 v255, v255, v8
v_fma_f32 v238, v86, v23, v110
v_mfma_f32_16x16x32_bf16 v[126:129], a[64:67], v[162:165], v[126:129]
v_rcp_f32 v255, v255
v_mfma_f32_16x16x32_bf16 v[130:133], v[162:165], a[68:71], v[130:133]
v_mfma_f32_16x16x32_bf16 v[122:125], a[72:75], v[166:169], v[122:125]
s_barrier
v_mfma_f32_16x16x32_bf16 v[126:129], a[76:79], v[166:169], v[126:129]
ds_read_b128 v[242:245], v25 offset:64
v_mul_f32 v9, v9, v255
v_mfma_f32_16x16x32_bf16 v[130:133], v[166:169], a[80:83], v[130:133]
v_mul_f32 v10, v10, v255
v_cvt_pk_bf16_f32 v9, v9, v9
v_mfma_f32_16x16x32_bf16 v[122:125], a[84:87], v[170:173], v[122:125]
global_load_lds_dwordx4 v14, s[42:43] offset:1024 sc1
v_cvt_pk_bf16_f32 v10, v10, v10
v_mfma_f32_16x16x32_bf16 v[126:129], a[88:91], v[170:173], v[126:129]
v_and_b32 v222, v9, v40
v_and_b32 v223, v10, v41
v_mfma_f32_16x16x32_bf16 v[130:133], v[170:173], a[92:95], v[130:133]
v_and_b32 v224, v9, v42
v_and_b32 v225, v10, v43
v_mfma_f32_16x16x32_bf16 v[122:125], a[96:99], v[174:177], v[122:125]
s_nop 0
v_mfma_f32_16x16x32_bf16 v[218:221], v[154:157], v[222:225], 0
v_mfma_f32_16x16x32_bf16 v[214:217], v[158:161], v[222:225], 0
v_fma_f32 v239, v87, v23, v111
v_fma_f32 v240, v88, v23, v112
v_mfma_f32_16x16x32_bf16 v[126:129], a[100:103], v[174:177], v[126:129]
s_waitcnt lgkmcnt(0)
v_fma_f32 v230, v102, v242, v134
v_fma_f32 v231, v102, v243, v135
v_mfma_f32_16x16x32_bf16 v[130:133], v[174:177], a[104:107], v[130:133]
v_fma_f32 v232, v102, v244, v136
v_fma_f32 v233, v102, v245, v137
v_mfma_f32_16x16x32_bf16 v[122:125], a[108:111], v[178:181], v[122:125]
v_cvt_pk_bf16_f32 v230, v230, v231
v_cvt_pk_bf16_f32 v231, v232, v233
v_mfma_f32_16x16x32_bf16 v[126:129], a[112:115], v[178:181], v[126:129]
v_cvt_pk_bf16_f32 v100, v218, v219
v_cvt_pk_bf16_f32 v101, v220, v221
v_mfma_f32_16x16x32_bf16 v[130:133], v[178:181], a[116:119], v[130:133]
global_load_lds_dwordx4 v15, s[42:43] offset:2048 sc1
s_sub_u32 s42, s42, 18432
s_subb_u32 s43, s43, 0
v_mfma_f32_16x16x32_bf16 v[122:125], a[120:123], v[182:185], v[122:125]
ds_write_b64 v18, v[100:101]
v_mfma_f32_16x16x32_bf16 v[126:129], a[124:127], v[182:185], v[126:129]
v_cvt_pk_bf16_f32 v9, v216, v217
v_fma_f32 v241, v89, v23, v113
v_mfma_f32_16x16x32_bf16 v[130:133], v[182:185], a[128:131], v[130:133]
ds_write_b32 v19, v9
v_mfma_f32_16x16x32_bf16 v[122:125], a[132:135], v[186:189], v[122:125]
ds_write_b64 v22, v[230:231] offset:41472
v_mfma_f32_16x16x32_bf16 v[126:129], a[136:139], v[186:189], v[126:129]
v_cvt_pk_bf16_f32 v138, v238, v239
v_cvt_pk_bf16_f32 v139, v240, v241
v_mfma_f32_16x16x32_bf16 v[130:133], v[186:189], a[140:143], v[130:133]
v_mfma_f32_16x16x32_bf16 v[122:125], a[144:147], v[190:193], v[122:125]
v_mfma_f32_16x16x32_bf16 v[126:129], a[148:151], v[190:193], v[126:129]
v_mfma_f32_16x16x32_bf16 v[130:133], v[190:193], a[152:155], v[130:133]
v_mfma_f32_16x16x32_bf16 v[122:125], a[156:159], v[194:197], v[122:125]
v_mfma_f32_16x16x32_bf16 v[126:129], a[160:163], v[194:197], v[126:129]
v_mfma_f32_16x16x32_bf16 v[130:133], v[194:197], a[164:167], v[130:133]
v_mfma_f32_16x16x32_bf16 v[122:125], a[168:171], v[198:201], v[122:125]
v_mfma_f32_16x16x32_bf16 v[126:129], a[172:175], v[198:201], v[126:129]
v_xor_b32 v51, 0x10000, v51
v_xor_b32 v52, 0x10000, v52
v_mfma_f32_16x16x32_bf16 v[130:133], v[198:201], a[176:179], v[130:133]
v_mfma_f32_16x16x32_bf16 v[122:125], a[180:183], v[202:205], v[122:125]
v_mfma_f32_16x16x32_bf16 v[126:129], a[184:187], v[202:205], v[126:129]
s_waitcnt vmcnt(5) lgkmcnt(0)
s_barrier
.Lqa_in26:
v_mfma_f32_16x16x32_bf16 v[130:133], v[202:205], a[188:191], v[130:133]
s_cmp_eq_u32 s70, s73
s_cbranch_scc1 .Lqa_hk27
.Lqa_hr28:
ds_read_b128 v[44:47], v51 offset:0
v_mfma_f32_16x16x32_bf16 v[122:125], a[192:195], v[206:209], v[122:125]
ds_read_b128 v[96:99], v52 offset:0
ds_read_b128 v[230:233], v17 offset:0
v_mfma_f32_16x16x32_bf16 v[126:129], a[196:199], v[206:209], v[126:129]
ds_read_b32 v23, v24 offset:64
ds_read_b32 v152, v21
v_mfma_f32_16x16x32_bf16 v[218:221], v[146:149], v[138:141], v[36:39]
v_mfma_f32_16x16x32_bf16 v[130:133], v[206:209], a[200:203], v[130:133]
ds_read_b128 v[104:107], v51 offset:2048
ds_read_b32 v26, v24 offset:128
v_mfma_f32_16x16x32_bf16 v[122:125], a[204:207], v[210:213], v[122:125]
ds_read_b128 v[0:3], v52 offset:2048
ds_read_b32 v53, v29
v_mfma_f32_16x16x32_bf16 v[126:129], a[208:211], v[210:213], v[126:129]
v_mfma_f32_16x16x32_bf16 v[130:133], v[210:213], a[212:215], v[130:133]
s_sub_u32 s70, s70, 1
ds_read_b128 v[4:7], v51 offset:4096
s_cbranch_scc0 .Lqa_top45
s_waitcnt lgkmcnt(0)
ds_read_b32 v95, v30
ds_read_b128 v[226:229], v25 offset:64
ds_read_b64 v[160:161], v22 offset:41472
ds_read_b32 v144, v20
v_fma_f32 v238, v90, v23, v126
global_store_dwordx4 v16, v[230:233], s[38:39] sc1
v_add_u32 v16, 18432, v16
v_fma_f32 v239, v91, v23, v127
v_fma_f32 v240, v92, v23, v128
v_fma_f32 v241, v93, v23, v129
v_cvt_pk_bf16_f32 v150, v238, v239
v_cvt_pk_bf16_f32 v151, v240, v241
s_nop 1
v_mfma_f32_16x16x32_bf16 v[214:217], v[150:153], v[138:141], v[32:35]
s_waitcnt lgkmcnt(2)
v_max_f32 v109, v218, v219
v_max_f32 v254, v220, v221
v_fma_f32 v238, v94, v226, v130
v_mul_f32 v26, 0x3eae1a93, v26
v_mul_f32 v103, v53, v26
v_mul_f32 v108, v95, v26
s_nop 0
v_max3_f32 v109, v214, v215, v109
v_max3_f32 v254, v216, v217, v254
v_mul_f32 v109, v109, v103
v_mul_f32 v254, v254, v108
v_max_f32 v255, v109, v254
v_mov_b32 v8, v255
s_nop 1
v_permlane16_swap_b32 v255, v8
v_max_f32 v255, v255, v8
v_mov_b32 v8, v255
s_nop 1
v_permlane32_swap_b32 v255, v8
v_max_f32 v255, v255, v8
v_sub_f32 v109, v109, v255
v_sub_f32 v254, v254, v255
v_exp_f32 v9, v109
v_exp_f32 v10, v254
s_nop 0
v_add_f32 v255, v9, v10
v_mov_b32 v8, v255
v_mul_f32 v9, v9, v53
v_fma_f32 v239, v94, v227, v131
v_permlane16_swap_b32 v255, v8
v_add_f32 v255, v255, v8
v_mov_b32 v8, v255
v_mul_f32 v10, v10, v95
v_fma_f32 v240, v94, v228, v132
v_fma_f32 v241, v94, v229, v133
v_permlane32_swap_b32 v255, v8
v_add_f32 v255, v255, v8
v_cvt_pk_bf16_f32 v156, v238, v239
s_waitcnt lgkmcnt(0)
s_barrier
v_cvt_pk_bf16_f32 v157, v240, v241
v_rcp_f32 v255, v255
s_nop 0
v_mul_f32 v9, v9, v255
v_mul_f32 v10, v10, v255
v_cvt_pk_bf16_f32 v9, v9, v9
v_cvt_pk_bf16_f32 v10, v10, v10
v_and_b32 v224, v9, v40
v_and_b32 v225, v10, v41
v_and_b32 v222, v9, v42
v_and_b32 v223, v10, v43
s_nop 1
v_mfma_f32_16x16x32_bf16 v[218:221], v[154:157], v[222:225], 0
v_mfma_f32_16x16x32_bf16 v[214:217], v[158:161], v[222:225], 0
v_fma_f32 v238, v86, v23, v122
s_nop 5
v_cvt_pk_bf16_f32 v100, v218, v219
v_cvt_pk_bf16_f32 v101, v220, v221
ds_write_b64 v18, v[100:101]
v_cvt_pk_bf16_f32 v9, v216, v217
v_fma_f32 v239, v87, v23, v123
ds_write_b32 v19, v9
v_fma_f32 v240, v88, v23, v124
v_fma_f32 v241, v89, v23, v125
v_cvt_pk_bf16_f32 v142, v238, v239
v_cvt_pk_bf16_f32 v143, v240, v241
s_waitcnt vmcnt(0)
s_waitcnt lgkmcnt(0)
s_barrier
ds_read_b128 v[230:233], v17 offset:0
s_waitcnt lgkmcnt(0)
global_store_dwordx4 v16, v[230:233], s[38:39] sc1
s_waitcnt vmcnt(0)
s_barrier
s_endpgm
.Lqa_w3:
s_getpc_b64 s[90:91]
.Lqa_pc69:
s_add_u32 s90, s90, .Lqa_e54-.Lqa_pc69
s_addc_u32 s91, s91, 0
s_branch .Lqa_k61
.Lqa_bk68:
v_add_u32 v255, 0x10000, v9
v_add_u32 v162, 0x20000, v9
v_add_u32 v163, 0x10000, v10
v_add_u32 v164, 0x20000, v10
v_mov_b32 v106, 0x3F803F80
v_mov_b32 v107, 0x3F803F80
v_mov_b32 v108, 0x3F803F80
v_mov_b32 v109, 0x3F803F80
v_mov_b32 v141, 0
v_mov_b32 v145, 0
v_mov_b32 v149, 0
v_mov_b32 v153, 0
v_mov_b32 v165, 46080
s_waitcnt lgkmcnt(0)
s_mul_i32 s37, s37, 0x9E3779B1
s_add_u32 s37, s37, 0x7F4A7C15
v_add_u32 v46, s37, v46
v_add_u32 v48, s37, v48
v_add_u32 v50, s37, v50
ds_write_b32 v165, v141
s_mov_b64 s[76:77], 0
s_mov_b32 s73, 18
s_add_u32 m0, s48, 4096
s_nop 0
global_load_lds_dwordx4 v0, s[40:41] offset:-4096
global_load_lds_dwordx4 v1, s[40:41] offset:-3072
global_load_lds_dwordx4 v2, s[40:41] offset:-2048
global_load_lds_dwordx4 v3, s[40:41] offset:-1024
global_load_lds_dwordx4 v4, s[40:41] offset:0
global_load_lds_dwordx4 v5, s[40:41] offset:1024
global_load_lds_dwordx4 v6, s[40:41] offset:2048
global_load_lds_dwordx4 v7, s[40:41] offset:3072
s_add_u32 m0, s48, 12288
s_nop 0
global_load_lds_dwordx4 v8, s[40:41] offset:-4096
s_add_u32 s40, s40, 221184
s_addc_u32 s41, s41, 0
s_add_u32 m0, s48, 40960
s_nop 0
global_load_lds_dwordx4 v0, s[40:41] offset:-4096
)"
R"(global_load_lds_dwordx4 v1, s[40:41] offset:-3072
global_load_lds_dwordx4 v2, s[40:41] offset:-2048
global_load_lds_dwordx4 v3, s[40:41] offset:-1024
global_load_lds_dwordx4 v4, s[40:41] offset:0
global_load_lds_dwordx4 v5, s[40:41] offset:1024
global_load_lds_dwordx4 v6, s[40:41] offset:2048
global_load_lds_dwordx4 v7, s[40:41] offset:3072
s_add_u32 m0, s48, 49152
s_nop 0
global_load_lds_dwordx4 v8, s[40:41] offset:-4096
s_add_u32 s40, s40, 221184
s_addc_u32 s41, s41, 0
s_waitcnt vmcnt(9) lgkmcnt(0)
s_barrier
s_add_u32 m0, s48, 77824
ds_read_b64_tr_b16 a[0:1], v9 offset:47104
ds_read_b64_tr_b16 a[2:3], v9 offset:49408
global_load_lds_dwordx4 v0, s[40:41] offset:-4096
ds_read_b64_tr_b16 a[4:5], v9 offset:47296
global_load_lds_dwordx4 v1, s[40:41] offset:-3072
ds_read_b64_tr_b16 a[6:7], v9 offset:49600
global_load_lds_dwordx4 v2, s[40:41] offset:-2048
ds_read_b64_tr_b16 a[8:9], v9 offset:47488
global_load_lds_dwordx4 v3, s[40:41] offset:-1024
ds_read_b64_tr_b16 a[10:11], v9 offset:49792
ds_read_b128 v[166:169], v51 offset:0
global_load_lds_dwordx4 v4, s[40:41] offset:0
ds_read_b128 v[174:177], v51 offset:18432
global_load_lds_dwordx4 v5, s[40:41] offset:1024
ds_read_b64_tr_b16 a[12:13], v255 offset:0
global_load_lds_dwordx4 v6, s[40:41] offset:2048
ds_read_b64_tr_b16 a[14:15], v255 offset:2304
global_load_lds_dwordx4 v7, s[40:41] offset:3072
ds_read_b64_tr_b16 a[16:17], v255 offset:192
ds_read_b64_tr_b16 a[18:19], v255 offset:2496
s_add_u32 m0, s48, 86016
ds_read_b64_tr_b16 a[20:21], v255 offset:384
global_load_lds_dwordx4 v8, s[40:41] offset:-4096
ds_read_b64_tr_b16 a[22:23], v255 offset:2688
s_add_u32 s40, s40, 221184
ds_read_b128 v[170:173], v52 offset:0
s_addc_u32 s41, s41, 0
ds_read_b128 v[178:181], v52 offset:18432
s_waitcnt vmcnt(9) lgkmcnt(0)
s_barrier
s_add_u32 m0, s48, 4096
ds_read_b64_tr_b16 a[24:25], v255 offset:18432
v_mfma_f32_16x16x32_bf16 v[86:89], a[0:3], v[106:109], 0
v_mfma_f32_16x16x32_bf16 v[90:93], a[4:7], v[106:109], 0
global_load_lds_dwordx4 v0, s[40:41] offset:-4096
ds_read_b64_tr_b16 a[26:27], v255 offset:20736
v_mfma_f32_16x16x32_bf16 v[94:97], v[106:109], a[8:11], 0
ds_read_b64_tr_b16 a[28:29], v255 offset:18624
global_load_lds_dwordx4 v1, s[40:41] offset:-3072
v_mfma_f32_16x16x32_bf16 v[110:113], a[0:3], v[166:169], 0
ds_read_b64_tr_b16 a[30:31], v255 offset:20928
v_mfma_f32_16x16x32_bf16 v[114:117], a[4:7], v[166:169], 0
global_load_lds_dwordx4 v2, s[40:41] offset:-2048
ds_read_b64_tr_b16 a[32:33], v255 offset:18816
v_mfma_f32_16x16x32_bf16 v[118:121], v[166:169], a[8:11], 0
ds_read_b64_tr_b16 a[34:35], v255 offset:21120
global_load_lds_dwordx4 v3, s[40:41] offset:-1024
v_mfma_f32_16x16x32_bf16 v[134:137], v[166:169], v[166:169], 0
ds_read_b128 v[182:185], v51 offset:2048
v_mfma_f32_16x16x32_bf16 v[122:125], a[0:3], v[174:177], 0
global_load_lds_dwordx4 v4, s[40:41] offset:0
ds_read_b128 v[190:193], v51 offset:20480
v_mfma_f32_16x16x32_bf16 v[126:129], a[4:7], v[174:177], 0
ds_read_b64_tr_b16 a[36:37], v255 offset:36864
global_load_lds_dwordx4 v5, s[40:41] offset:1024
v_mfma_f32_16x16x32_bf16 v[130:133], v[174:177], a[8:11], 0
v_mfma_f32_16x16x32_bf16 v[158:161], v[174:177], v[174:177], 0
ds_read_b64_tr_b16 a[38:39], v255 offset:39168
global_load_lds_dwordx4 v6, s[40:41] offset:2048
v_mfma_f32_16x16x32_bf16 v[86:89], a[12:15], v[106:109], v[86:89]
ds_read_b64_tr_b16 a[40:41], v255 offset:37056
v_mfma_f32_16x16x32_bf16 v[90:93], a[16:19], v[106:109], v[90:93]
global_load_lds_dwordx4 v7, s[40:41] offset:3072
ds_read_b64_tr_b16 a[42:43], v255 offset:39360
v_mfma_f32_16x16x32_bf16 v[94:97], v[106:109], a[20:23], v[94:97]
s_add_u32 m0, s48, 12288
ds_read_b64_tr_b16 a[44:45], v255 offset:37248
v_mfma_f32_16x16x32_bf16 v[110:113], a[12:15], v[170:173], v[110:113]
ds_read_b64_tr_b16 a[46:47], v255 offset:39552
v_mfma_f32_16x16x32_bf16 v[114:117], a[16:19], v[170:173], v[114:117]
global_load_lds_dwordx4 v8, s[40:41] offset:-4096
ds_read_b128 v[186:189], v52 offset:2048
v_mfma_f32_16x16x32_bf16 v[118:121], v[170:173], a[20:23], v[118:121]
s_add_u32 s40, s40, 221184
ds_read_b128 v[194:197], v52 offset:20480
v_mfma_f32_16x16x32_bf16 v[134:137], v[170:173], v[170:173], v[134:137]
v_mfma_f32_16x16x32_bf16 v[122:125], a[12:15], v[178:181], v[122:125]
v_mfma_f32_16x16x32_bf16 v[126:129], a[16:19], v[178:181], v[126:129]
s_addc_u32 s41, s41, 0
v_mfma_f32_16x16x32_bf16 v[130:133], v[178:181], a[20:23], v[130:133]
v_mfma_f32_16x16x32_bf16 v[158:161], v[178:181], v[178:181], v[158:161]
s_waitcnt vmcnt(9) lgkmcnt(0)
s_barrier
s_add_u32 m0, s48, 40960
ds_read_b64_tr_b16 a[48:49], v255 offset:55296
v_mfma_f32_16x16x32_bf16 v[86:89], a[24:27], v[106:109], v[86:89]
v_mfma_f32_16x16x32_bf16 v[90:93], a[28:31], v[106:109], v[90:93]
global_load_lds_dwordx4 v0, s[40:41] offset:-4096
ds_read_b64_tr_b16 a[50:51], v255 offset:57600
v_mfma_f32_16x16x32_bf16 v[94:97], v[106:109], a[32:35], v[94:97]
ds_read_b64_tr_b16 a[52:53], v255 offset:55488
global_load_lds_dwordx4 v1, s[40:41] offset:-3072
v_mfma_f32_16x16x32_bf16 v[110:113], a[24:27], v[182:185], v[110:113]
ds_read_b64_tr_b16 a[54:55], v255 offset:57792
v_mfma_f32_16x16x32_bf16 v[114:117], a[28:31], v[182:185], v[114:117]
global_load_lds_dwordx4 v2, s[40:41] offset:-2048
ds_read_b64_tr_b16 a[56:57], v255 offset:55680
v_mfma_f32_16x16x32_bf16 v[118:121], v[182:185], a[32:35], v[118:121]
ds_read_b64_tr_b16 a[58:59], v255 offset:57984
global_load_lds_dwordx4 v3, s[40:41] offset:-1024
v_mfma_f32_16x16x32_bf16 v[134:137], v[182:185], v[182:185], v[134:137]
ds_read_b128 v[166:169], v51 offset:4096
v_mfma_f32_16x16x32_bf16 v[122:125], a[24:27], v[190:193], v[122:125]
global_load_lds_dwordx4 v4, s[40:41] offset:0
ds_read_b128 v[174:177], v51 offset:22528
v_mfma_f32_16x16x32_bf16 v[126:129], a[28:31], v[190:193], v[126:129]
ds_read_b64_tr_b16 a[60:61], v162 offset:8192
global_load_lds_dwordx4 v5, s[40:41] offset:1024
v_mfma_f32_16x16x32_bf16 v[130:133], v[190:193], a[32:35], v[130:133]
v_mfma_f32_16x16x32_bf16 v[158:161], v[190:193], v[190:193], v[158:161]
ds_read_b64_tr_b16 a[62:63], v162 offset:10496
global_load_lds_dwordx4 v6, s[40:41] offset:2048
v_mfma_f32_16x16x32_bf16 v[86:89], a[36:39], v[106:109], v[86:89]
ds_read_b64_tr_b16 a[64:65], v162 offset:8384
v_mfma_f32_16x16x32_bf16 v[90:93], a[40:43], v[106:109], v[90:93]
global_load_lds_dwordx4 v7, s[40:41] offset:3072
ds_read_b64_tr_b16 a[66:67], v162 offset:10688
v_mfma_f32_16x16x32_bf16 v[94:97], v[106:109], a[44:47], v[94:97]
s_add_u32 m0, s48, 49152
ds_read_b64_tr_b16 a[68:69], v162 offset:8576
v_mfma_f32_16x16x32_bf16 v[110:113], a[36:39], v[186:189], v[110:113]
ds_read_b64_tr_b16 a[70:71], v162 offset:10880
v_mfma_f32_16x16x32_bf16 v[114:117], a[40:43], v[186:189], v[114:117]
global_load_lds_dwordx4 v8, s[40:41] offset:-4096
ds_read_b128 v[170:173], v52 offset:4096
v_mfma_f32_16x16x32_bf16 v[118:121], v[186:189], a[44:47], v[118:121]
s_add_u32 s40, s40, 221184
ds_read_b128 v[178:181], v52 offset:22528
v_mfma_f32_16x16x32_bf16 v[134:137], v[186:189], v[186:189], v[134:137]
v_mfma_f32_16x16x32_bf16 v[122:125], a[36:39], v[194:197], v[122:125]
v_mfma_f32_16x16x32_bf16 v[126:129], a[40:43], v[194:197], v[126:129]
s_addc_u32 s41, s41, 0
v_mfma_f32_16x16x32_bf16 v[130:133], v[194:197], a[44:47], v[130:133]
v_mfma_f32_16x16x32_bf16 v[158:161], v[194:197], v[194:197], v[158:161]
s_waitcnt vmcnt(9) lgkmcnt(0)
s_barrier
s_add_u32 m0, s48, 77824
ds_read_b64_tr_b16 a[72:73], v9 offset:47104
v_mfma_f32_16x16x32_bf16 v[86:89], a[48:51], v[106:109], v[86:89]
v_mfma_f32_16x16x32_bf16 v[90:93], a[52:55], v[106:109], v[90:93]
global_load_lds_dwordx4 v0, s[40:41] offset:-4096
ds_read_b64_tr_b16 a[74:75], v9 offset:49408
v_mfma_f32_16x16x32_bf16 v[94:97], v[106:109], a[56:59], v[94:97]
ds_read_b64_tr_b16 a[76:77], v9 offset:47296
global_load_lds_dwordx4 v1, s[40:41] offset:-3072
v_mfma_f32_16x16x32_bf16 v[110:113], a[48:51], v[166:169], v[110:113]
ds_read_b64_tr_b16 a[78:79], v9 offset:49600
v_mfma_f32_16x16x32_bf16 v[114:117], a[52:55], v[166:169], v[114:117]
global_load_lds_dwordx4 v2, s[40:41] offset:-2048
ds_read_b64_tr_b16 a[80:81], v9 offset:47488
v_mfma_f32_16x16x32_bf16 v[118:121], v[166:169], a[56:59], v[118:121]
ds_read_b64_tr_b16 a[82:83], v9 offset:49792
global_load_lds_dwordx4 v3, s[40:41] offset:-1024
v_mfma_f32_16x16x32_bf16 v[134:137], v[166:169], v[166:169], v[134:137]
ds_read_b128 v[182:185], v51 offset:6144
v_mfma_f32_16x16x32_bf16 v[122:125], a[48:51], v[174:177], v[122:125]
global_load_lds_dwordx4 v4, s[40:41] offset:0
ds_read_b128 v[190:193], v51 offset:24576
v_mfma_f32_16x16x32_bf16 v[126:129], a[52:55], v[174:177], v[126:129]
ds_read_b64_tr_b16 a[84:85], v255 offset:0
global_load_lds_dwordx4 v5, s[40:41] offset:1024
v_mfma_f32_16x16x32_bf16 v[130:133], v[174:177], a[56:59], v[130:133]
v_mfma_f32_16x16x32_bf16 v[158:161], v[174:177], v[174:177], v[158:161]
ds_read_b64_tr_b16 a[86:87], v255 offset:2304
global_load_lds_dwordx4 v6, s[40:41] offset:2048
v_mfma_f32_16x16x32_bf16 v[86:89], a[60:63], v[106:109], v[86:89]
ds_read_b64_tr_b16 a[88:89], v255 offset:192
v_mfma_f32_16x16x32_bf16 v[90:93], a[64:67], v[106:109], v[90:93]
global_load_lds_dwordx4 v7, s[40:41] offset:3072
ds_read_b64_tr_b16 a[90:91], v255 offset:2496
v_mfma_f32_16x16x32_bf16 v[94:97], v[106:109], a[68:71], v[94:97]
s_add_u32 m0, s48, 86016
ds_read_b64_tr_b16 a[92:93], v255 offset:384
v_mfma_f32_16x16x32_bf16 v[110:113], a[60:63], v[170:173], v[110:113]
ds_read_b64_tr_b16 a[94:95], v255 offset:2688
v_mfma_f32_16x16x32_bf16 v[114:117], a[64:67], v[170:173], v[114:117]
global_load_lds_dwordx4 v8, s[40:41] offset:-4096
ds_read_b128 v[186:189], v52 offset:6144
v_mfma_f32_16x16x32_bf16 v[118:121], v[170:173], a[68:71], v[118:121]
s_add_u32 s40, s40, 221184
ds_read_b128 v[194:197], v52 offset:24576
v_mfma_f32_16x16x32_bf16 v[134:137], v[170:173], v[170:173], v[134:137]
v_mfma_f32_16x16x32_bf16 v[122:125], a[60:63], v[178:181], v[122:125]
v_mfma_f32_16x16x32_bf16 v[126:129], a[64:67], v[178:181], v[126:129]
s_addc_u32 s41, s41, 0
v_mfma_f32_16x16x32_bf16 v[130:133], v[178:181], a[68:71], v[130:133]
v_mfma_f32_16x16x32_bf16 v[158:161], v[178:181], v[178:181], v[158:161]
s_waitcnt vmcnt(9) lgkmcnt(0)
s_barrier
s_add_u32 m0, s48, 4096
ds_read_b64_tr_b16 a[96:97], v255 offset:18432
v_mfma_f32_16x16x32_bf16 v[86:89], a[72:75], v[106:109], v[86:89]
v_mfma_f32_16x16x32_bf16 v[90:93], a[76:79], v[106:109], v[90:93]
global_load_lds_dwordx4 v0, s[40:41] offset:-4096
ds_read_b64_tr_b16 a[98:99], v255 offset:20736
v_mfma_f32_16x16x32_bf16 v[94:97], v[106:109], a[80:83], v[94:97]
ds_read_b64_tr_b16 a[100:101], v255 offset:18624
global_load_lds_dwordx4 v1, s[40:41] offset:-3072
v_mfma_f32_16x16x32_bf16 v[110:113], a[72:75], v[182:185], v[110:113]
ds_read_b64_tr_b16 a[102:103], v255 offset:20928
v_mfma_f32_16x16x32_bf16 v[114:117], a[76:79], v[182:185], v[114:117]
global_load_lds_dwordx4 v2, s[40:41] offset:-2048
ds_read_b64_tr_b16 a[104:105], v255 offset:18816
v_mfma_f32_16x16x32_bf16 v[118:121], v[182:185], a[80:83], v[118:121]
ds_read_b64_tr_b16 a[106:107], v255 offset:21120
global_load_lds_dwordx4 v3, s[40:41] offset:-1024
v_mfma_f32_16x16x32_bf16 v[134:137], v[182:185], v[182:185], v[134:137]
ds_read_b128 v[166:169], v51 offset:8192
v_mfma_f32_16x16x32_bf16 v[122:125], a[72:75], v[190:193], v[122:125]
global_load_lds_dwordx4 v4, s[40:41] offset:0
ds_read_b128 v[174:177], v51 offset:26624
v_mfma_f32_16x16x32_bf16 v[126:129], a[76:79], v[190:193], v[126:129]
ds_read_b64_tr_b16 a[108:109], v255 offset:36864
global_load_lds_dwordx4 v5, s[40:41] offset:1024
v_mfma_f32_16x16x32_bf16 v[130:133], v[190:193], a[80:83], v[130:133]
v_mfma_f32_16x16x32_bf16 v[158:161], v[190:193], v[190:193], v[158:161]
ds_read_b64_tr_b16 a[110:111], v255 offset:39168
global_load_lds_dwordx4 v6, s[40:41] offset:2048
v_mfma_f32_16x16x32_bf16 v[86:89], a[84:87], v[106:109], v[86:89]
ds_read_b64_tr_b16 a[112:113], v255 offset:37056
v_mfma_f32_16x16x32_bf16 v[90:93], a[88:91], v[106:109], v[90:93]
global_load_lds_dwordx4 v7, s[40:41] offset:3072
ds_read_b64_tr_b16 a[114:115], v255 offset:39360
v_mfma_f32_16x16x32_bf16 v[94:97], v[106:109], a[92:95], v[94:97]
s_add_u32 m0, s48, 12288
ds_read_b64_tr_b16 a[116:117], v255 offset:37248
v_mfma_f32_16x16x32_bf16 v[110:113], a[84:87], v[186:189], v[110:113]
ds_read_b64_tr_b16 a[118:119], v255 offset:39552
v_mfma_f32_16x16x32_bf16 v[114:117], a[88:91], v[186:189], v[114:117]
global_load_lds_dwordx4 v8, s[40:41] offset:-4096
ds_read_b128 v[170:173], v52 offset:8192
v_mfma_f32_16x16x32_bf16 v[118:121], v[186:189], a[92:95], v[118:121]
s_add_u32 s40, s40, 221184
ds_read_b128 v[178:181], v52 offset:26624
v_mfma_f32_16x16x32_bf16 v[134:137], v[186:189], v[186:189], v[134:137]
v_mfma_f32_16x16x32_bf16 v[122:125], a[84:87], v[194:197], v[122:125]
v_mfma_f32_16x16x32_bf16 v[126:129], a[88:91], v[194:197], v[126:129]
s_addc_u32 s41, s41, 0
v_mfma_f32_16x16x32_bf16 v[130:133], v[194:197], a[92:95], v[130:133]
v_mfma_f32_16x16x32_bf16 v[158:161], v[194:197], v[194:197], v[158:161]
s_waitcnt vmcnt(9) lgkmcnt(0)
s_barrier
s_add_u32 m0, s48, 40960
ds_read_b64_tr_b16 a[120:121], v255 offset:55296
v_mfma_f32_16x16x32_bf16 v[86:89], a[96:99], v[106:109], v[86:89]
v_mfma_f32_16x16x32_bf16 v[90:93], a[100:103], v[106:109], v[90:93]
global_load_lds_dwordx4 v0, s[40:41] offset:-4096
ds_read_b64_tr_b16 a[122:123], v255 offset:57600
v_mfma_f32_16x16x32_bf16 v[94:97], v[106:109], a[104:107], v[94:97]
ds_read_b64_tr_b16 a[124:125], v255 offset:55488
global_load_lds_dwordx4 v1, s[40:41] offset:-3072
v_mfma_f32_16x16x32_bf16 v[110:113], a[96:99], v[166:169], v[110:113]
ds_read_b64_tr_b16 a[126:127], v255 offset:57792
v_mfma_f32_16x16x32_bf16 v[114:117], a[100:103], v[166:169], v[114:117]
global_load_lds_dwordx4 v2, s[40:41] offset:-2048
ds_read_b64_tr_b16 a[128:129], v255 offset:55680
v_mfma_f32_16x16x32_bf16 v[118:121], v[166:169], a[104:107], v[118:121]
ds_read_b64_tr_b16 a[130:131], v255 offset:57984
global_load_lds_dwordx4 v3, s[40:41] offset:-1024
v_mfma_f32_16x16x32_bf16 v[134:137], v[166:169], v[166:169], v[134:137]
ds_read_b128 v[182:185], v51 offset:10240
v_mfma_f32_16x16x32_bf16 v[122:125], a[96:99], v[174:177], v[122:125]
global_load_lds_dwordx4 v4, s[40:41] offset:0
ds_read_b128 v[190:193], v51 offset:28672
v_mfma_f32_16x16x32_bf16 v[126:129], a[100:103], v[174:177], v[126:129]
ds_read_b64_tr_b16 a[132:133], v162 offset:8192
global_load_lds_dwordx4 v5, s[40:41] offset:1024
v_mfma_f32_16x16x32_bf16 v[130:133], v[174:177], a[104:107], v[130:133]
v_mfma_f32_16x16x32_bf16 v[158:161], v[174:177], v[174:177], v[158:161]
ds_read_b64_tr_b16 a[134:135], v162 offset:10496
global_load_lds_dwordx4 v6, s[40:41] offset:2048
v_mfma_f32_16x16x32_bf16 v[86:89], a[108:111], v[106:109], v[86:89]
ds_read_b64_tr_b16 a[136:137], v162 offset:8384
v_mfma_f32_16x16x32_bf16 v[90:93], a[112:115], v[106:109], v[90:93]
global_load_lds_dwordx4 v7, s[40:41] offset:3072
ds_read_b64_tr_b16 a[138:139], v162 offset:10688
v_mfma_f32_16x16x32_bf16 v[94:97], v[106:109], a[116:119], v[94:97]
s_add_u32 m0, s48, 49152
ds_read_b64_tr_b16 a[140:141], v162 offset:8576
v_mfma_f32_16x16x32_bf16 v[110:113], a[108:111], v[170:173], v[110:113]
ds_read_b64_tr_b16 a[142:143], v162 offset:10880
v_mfma_f32_16x16x32_bf16 v[114:117], a[112:115], v[170:173], v[114:117]
global_load_lds_dwordx4 v8, s[40:41] offset:-4096
ds_read_b128 v[186:189], v52 offset:10240
v_mfma_f32_16x16x32_bf16 v[118:121], v[170:173], a[116:119], v[118:121]
s_add_u32 s40, s40, 221184
ds_read_b128 v[194:197], v52 offset:28672
v_mfma_f32_16x16x32_bf16 v[134:137], v[170:173], v[170:173], v[134:137]
v_mfma_f32_16x16x32_bf16 v[122:125], a[108:111], v[178:181], v[122:125]
v_mfma_f32_16x16x32_bf16 v[126:129], a[112:115], v[178:181], v[126:129]
s_addc_u32 s41, s41, 0
v_mfma_f32_16x16x32_bf16 v[130:133], v[178:181], a[116:119], v[130:133]
v_mfma_f32_16x16x32_bf16 v[158:161], v[178:181], v[178:181], v[158:161]
s_waitcnt vmcnt(9) lgkmcnt(0)
s_barrier
s_add_u32 m0, s48, 77824
ds_read_b64_tr_b16 a[144:145], v9 offset:47104
v_mfma_f32_16x16x32_bf16 v[86:89], a[120:123], v[106:109], v[86:89]
v_mfma_f32_16x16x32_bf16 v[90:93], a[124:127], v[106:109], v[90:93]
global_load_lds_dwordx4 v0, s[40:41] offset:-4096
ds_read_b64_tr_b16 a[146:147], v9 offset:49408
v_mfma_f32_16x16x32_bf16 v[94:97], v[106:109], a[128:131], v[94:97]
ds_read_b64_tr_b16 a[148:149], v9 offset:47296
global_load_lds_dwordx4 v1, s[40:41] offset:-3072
v_mfma_f32_16x16x32_bf16 v[110:113], a[120:123], v[182:185], v[110:113]
ds_read_b64_tr_b16 a[150:151], v9 offset:49600
v_mfma_f32_16x16x32_bf16 v[114:117], a[124:127], v[182:185], v[114:117]
global_load_lds_dwordx4 v2, s[40:41] offset:-2048
ds_read_b64_tr_b16 a[152:153], v9 offset:47488
v_mfma_f32_16x16x32_bf16 v[118:121], v[182:185], a[128:131], v[118:121]
ds_read_b64_tr_b16 a[154:155], v9 offset:49792
global_load_lds_dwordx4 v3, s[40:41] offset:-1024
v_mfma_f32_16x16x32_bf16 v[134:137], v[182:185], v[182:185], v[134:137]
ds_read_b128 v[166:169], v51 offset:12288
v_mfma_f32_16x16x32_bf16 v[122:125], a[120:123], v[190:193], v[122:125]
global_load_lds_dwordx4 v4, s[40:41] offset:0
ds_read_b128 v[174:177], v51 offset:30720
v_mfma_f32_16x16x32_bf16 v[126:129], a[124:127], v[190:193], v[126:129]
ds_read_b64_tr_b16 a[156:157], v255 offset:0
global_load_lds_dwordx4 v5, s[40:41] offset:1024
v_mfma_f32_16x16x32_bf16 v[130:133], v[190:193], a[128:131], v[130:133]
v_mfma_f32_16x16x32_bf16 v[158:161], v[190:193], v[190:193], v[158:161]
ds_read_b64_tr_b16 a[158:159], v255 offset:2304
global_load_lds_dwordx4 v6, s[40:41] offset:2048
v_mfma_f32_16x16x32_bf16 v[86:89], a[132:135], v[106:109], v[86:89]
ds_read_b64_tr_b16 a[160:161], v255 offset:192
v_mfma_f32_16x16x32_bf16 v[90:93], a[136:139], v[106:109], v[90:93]
global_load_lds_dwordx4 v7, s[40:41] offset:3072
ds_read_b64_tr_b16 a[162:163], v255 offset:2496
v_mfma_f32_16x16x32_bf16 v[94:97], v[106:109], a[140:143], v[94:97]
s_add_u32 m0, s48, 86016
ds_read_b64_tr_b16 a[164:165], v255 offset:384
v_mfma_f32_16x16x32_bf16 v[110:113], a[132:135], v[186:189], v[110:113]
ds_read_b64_tr_b16 a[166:167], v255 offset:2688
v_mfma_f32_16x16x32_bf16 v[114:117], a[136:139], v[186:189], v[114:117]
global_load_lds_dwordx4 v8, s[40:41] offset:-4096
ds_read_b128 v[170:173], v52 offset:12288
v_mfma_f32_16x16x32_bf16 v[118:121], v[186:189], a[140:143], v[118:121]
s_sub_u32 s40, s40, 1769472
ds_read_b128 v[178:181], v52 offset:30720
v_mfma_f32_16x16x32_bf16 v[134:137], v[186:189], v[186:189], v[134:137]
v_mfma_f32_16x16x32_bf16 v[122:125], a[132:135], v[194:197], v[122:125]
v_mfma_f32_16x16x32_bf16 v[126:129], a[136:139], v[194:197], v[126:129]
s_subb_u32 s41, s41, 0
v_mfma_f32_16x16x32_bf16 v[130:133], v[194:197], a[140:143], v[130:133]
v_mfma_f32_16x16x32_bf16 v[158:161], v[194:197], v[194:197], v[158:161]
s_waitcnt vmcnt(9) lgkmcnt(0)
s_barrier
s_add_u32 m0, s49, 0
ds_read_b64_tr_b16 a[168:169], v255 offset:18432
v_mfma_f32_16x16x32_bf16 v[86:89], a[144:147], v[106:109], v[86:89]
v_mfma_f32_16x16x32_bf16 v[90:93], a[148:151], v[106:109], v[90:93]
ds_read_b64_tr_b16 a[170:171], v255 offset:20736
v_mfma_f32_16x16x32_bf16 v[94:97], v[106:109], a[152:155], v[94:97]
ds_read_b64_tr_b16 a[172:173], v255 offset:18624
v_mfma_f32_16x16x32_bf16 v[110:113], a[144:147], v[166:169], v[110:113]
global_load_lds_dwordx4 v11, s[42:43] offset:-2048 sc1
ds_read_b64_tr_b16 a[174:175], v255 offset:20928
v_mfma_f32_16x16x32_bf16 v[114:117], a[148:151], v[166:169], v[114:117]
ds_read_b64_tr_b16 a[176:177], v255 offset:18816
v_mfma_f32_16x16x32_bf16 v[118:121], v[166:169], a[152:155], v[118:121]
ds_read_b64_tr_b16 a[178:179], v255 offset:21120
v_mfma_f32_16x16x32_bf16 v[134:137], v[166:169], v[166:169], v[134:137]
global_load_lds_dwordx4 v12, s[42:43] offset:-1024 sc1
ds_read_b128 v[182:185], v51 offset:14336
v_mfma_f32_16x16x32_bf16 v[122:125], a[144:147], v[174:177], v[122:125]
ds_read_b128 v[190:193], v51 offset:32768
v_mfma_f32_16x16x32_bf16 v[126:129], a[148:151], v[174:177], v[126:129]
ds_read_b64_tr_b16 a[180:181], v255 offset:36864
v_mfma_f32_16x16x32_bf16 v[130:133], v[174:177], a[152:155], v[130:133]
global_load_lds_dwordx4 v13, s[42:43] offset:0 sc1
v_mfma_f32_16x16x32_bf16 v[158:161], v[174:177], v[174:177], v[158:161]
ds_read_b64_tr_b16 a[182:183], v255 offset:39168
v_mfma_f32_16x16x32_bf16 v[86:89], a[156:159], v[106:109], v[86:89]
ds_read_b64_tr_b16 a[184:185], v255 offset:37056
v_mfma_f32_16x16x32_bf16 v[90:93], a[160:163], v[106:109], v[90:93]
ds_read_b64_tr_b16 a[186:187], v255 offset:39360
v_mfma_f32_16x16x32_bf16 v[94:97], v[106:109], a[164:167], v[94:97]
global_load_lds_dwordx4 v14, s[42:43] offset:1024 sc1
ds_read_b64_tr_b16 a[188:189], v255 offset:37248
v_mfma_f32_16x16x32_bf16 v[110:113], a[156:159], v[170:173], v[110:113]
ds_read_b64_tr_b16 a[190:191], v255 offset:39552
v_mfma_f32_16x16x32_bf16 v[114:117], a[160:163], v[170:173], v[114:117]
ds_read_b128 v[186:189], v52 offset:14336
v_mfma_f32_16x16x32_bf16 v[118:121], v[170:173], a[164:167], v[118:121]
)"
R"(s_sub_u32 s42, s42, 18432
s_subb_u32 s43, s43, 0
ds_read_b128 v[194:197], v52 offset:32768
v_mfma_f32_16x16x32_bf16 v[134:137], v[170:173], v[170:173], v[134:137]
v_mfma_f32_16x16x32_bf16 v[122:125], a[156:159], v[178:181], v[122:125]
v_mfma_f32_16x16x32_bf16 v[126:129], a[160:163], v[178:181], v[126:129]
v_mfma_f32_16x16x32_bf16 v[130:133], v[178:181], a[164:167], v[130:133]
v_mfma_f32_16x16x32_bf16 v[158:161], v[178:181], v[178:181], v[158:161]
s_waitcnt vmcnt(4) lgkmcnt(0)
s_barrier
s_add_u32 m0, s49, 18432
ds_read_b64_tr_b16 a[192:193], v255 offset:55296
v_mfma_f32_16x16x32_bf16 v[86:89], a[168:171], v[106:109], v[86:89]
v_mfma_f32_16x16x32_bf16 v[90:93], a[172:175], v[106:109], v[90:93]
ds_read_b64_tr_b16 a[194:195], v255 offset:57600
v_mfma_f32_16x16x32_bf16 v[94:97], v[106:109], a[176:179], v[94:97]
ds_read_b64_tr_b16 a[196:197], v255 offset:55488
v_mfma_f32_16x16x32_bf16 v[110:113], a[168:171], v[182:185], v[110:113]
global_load_lds_dwordx4 v11, s[42:43] offset:-2048 sc1
ds_read_b64_tr_b16 a[198:199], v255 offset:57792
v_mfma_f32_16x16x32_bf16 v[114:117], a[172:175], v[182:185], v[114:117]
ds_read_b64_tr_b16 a[200:201], v255 offset:55680
v_mfma_f32_16x16x32_bf16 v[118:121], v[182:185], a[176:179], v[118:121]
ds_read_b64_tr_b16 a[202:203], v255 offset:57984
v_mfma_f32_16x16x32_bf16 v[134:137], v[182:185], v[182:185], v[134:137]
global_load_lds_dwordx4 v12, s[42:43] offset:-1024 sc1
ds_read_b128 v[166:169], v51 offset:16384
v_mfma_f32_16x16x32_bf16 v[122:125], a[168:171], v[190:193], v[122:125]
ds_read_b128 v[174:177], v51 offset:34816
v_mfma_f32_16x16x32_bf16 v[126:129], a[172:175], v[190:193], v[126:129]
ds_read_b64_tr_b16 a[204:205], v162 offset:8192
v_mfma_f32_16x16x32_bf16 v[130:133], v[190:193], a[176:179], v[130:133]
global_load_lds_dwordx4 v13, s[42:43] offset:0 sc1
ds_read_b64_tr_b16 a[206:207], v162 offset:10496
v_mfma_f32_16x16x32_bf16 v[158:161], v[190:193], v[190:193], v[158:161]
ds_read_b64_tr_b16 a[208:209], v162 offset:8384
v_mfma_f32_16x16x32_bf16 v[86:89], a[180:183], v[106:109], v[86:89]
ds_read_b64_tr_b16 a[210:211], v162 offset:10688
v_mfma_f32_16x16x32_bf16 v[90:93], a[184:187], v[106:109], v[90:93]
global_load_lds_dwordx4 v14, s[42:43] offset:1024 sc1
ds_read_b64_tr_b16 a[212:213], v162 offset:8576
v_mfma_f32_16x16x32_bf16 v[94:97], v[106:109], a[188:191], v[94:97]
ds_read_b64_tr_b16 a[214:215], v162 offset:10880
v_mfma_f32_16x16x32_bf16 v[110:113], a[180:183], v[186:189], v[110:113]
ds_read_b128 v[170:173], v52 offset:16384
v_mfma_f32_16x16x32_bf16 v[114:117], a[184:187], v[186:189], v[114:117]
s_sub_u32 s42, s42, 18432
s_subb_u32 s43, s43, 0
ds_read_b128 v[178:181], v52 offset:34816
v_mfma_f32_16x16x32_bf16 v[118:121], v[186:189], a[188:191], v[118:121]
v_mfma_f32_16x16x32_bf16 v[134:137], v[186:189], v[186:189], v[134:137]
v_mfma_f32_16x16x32_bf16 v[122:125], a[180:183], v[194:197], v[122:125]
v_mfma_f32_16x16x32_bf16 v[126:129], a[184:187], v[194:197], v[126:129]
v_mfma_f32_16x16x32_bf16 v[158:161], v[194:197], v[194:197], v[158:161]
s_waitcnt lgkmcnt(14)
v_mfma_f32_16x16x32_bf16 v[86:89], a[192:195], v[106:109], v[86:89]
s_waitcnt lgkmcnt(12)
v_mfma_f32_16x16x32_bf16 v[90:93], a[196:199], v[106:109], v[90:93]
s_waitcnt lgkmcnt(10)
v_mfma_f32_16x16x32_bf16 v[94:97], v[106:109], a[200:203], v[94:97]
s_waitcnt lgkmcnt(9)
v_mfma_f32_16x16x32_bf16 v[110:113], a[192:195], v[166:169], v[110:113]
v_mfma_f32_16x16x32_bf16 v[114:117], a[196:199], v[166:169], v[114:117]
v_mfma_f32_16x16x32_bf16 v[118:121], v[166:169], a[200:203], v[118:121]
v_mfma_f32_16x16x32_bf16 v[134:137], v[166:169], v[166:169], v[134:137]
s_waitcnt lgkmcnt(8)
v_mfma_f32_16x16x32_bf16 v[158:161], v[174:177], v[174:177], v[158:161]
s_waitcnt lgkmcnt(6)
v_mfma_f32_16x16x32_bf16 v[86:89], a[204:207], v[106:109], v[86:89]
s_waitcnt lgkmcnt(4)
v_mfma_f32_16x16x32_bf16 v[90:93], a[208:211], v[106:109], v[90:93]
s_waitcnt lgkmcnt(2)
v_mfma_f32_16x16x32_bf16 v[94:97], v[106:109], a[212:215], v[94:97]
s_waitcnt lgkmcnt(1)
v_mfma_f32_16x16x32_bf16 v[110:113], a[204:207], v[170:173], v[110:113]
v_mfma_f32_16x16x32_bf16 v[114:117], a[208:211], v[170:173], v[114:117]
v_mfma_f32_16x16x32_bf16 v[118:121], v[170:173], a[212:215], v[118:121]
v_mfma_f32_16x16x32_bf16 v[134:137], v[170:173], v[170:173], v[134:137]
s_waitcnt lgkmcnt(0)
v_mfma_f32_16x16x32_bf16 v[158:161], v[178:181], v[178:181], v[158:161]
s_nop 5
ds_write_b128 v23, v[134:137] offset:41984
s_waitcnt lgkmcnt(0)
s_barrier
ds_read_b128 v[166:169], v51 offset:32768
ds_read_b128 v[170:173], v52 offset:32768
ds_read_b128 v[174:177], v51 offset:34816
ds_read_b128 v[178:181], v52 offset:34816
v_mov_b32 v202, v32
v_mov_b32 v203, v33
v_mov_b32 v204, v34
v_mov_b32 v205, v35
v_mov_b32 v134, v158
v_mov_b32 v135, v159
v_mov_b32 v136, v160
v_mov_b32 v137, v161
s_barrier
ds_read_b32 v26, v24 offset:0
ds_read_b32 v148, v21
ds_read_b32 v95, v24 offset:192
ds_read_b32 v255, v27
ds_read_b32 v8, v28
ds_read_b128 v[194:197], v25 offset:0
ds_read_b64 v[158:159], v22 offset:41472
ds_read_b32 v140, v20
v_xor_b32 v51, 0x10000, v51
v_xor_b32 v52, 0x10000, v52
ds_write_b128 v23, v[134:137] offset:41984
s_waitcnt lgkmcnt(8)
v_fma_f32 v206, v90, v26, v114
v_fma_f32 v207, v91, v26, v115
v_fma_f32 v208, v92, v26, v116
v_fma_f32 v209, v93, v26, v117
v_cvt_pk_bf16_f32 v146, v206, v207
v_cvt_pk_bf16_f32 v147, v208, v209
s_waitcnt lgkmcnt(0)
s_barrier
v_fma_f32 v206, v94, v194, v118
v_fma_f32 v207, v94, v195, v119
v_fma_f32 v208, v94, v196, v120
v_fma_f32 v209, v94, v197, v121
v_cvt_pk_bf16_f32 v154, v206, v207
v_cvt_pk_bf16_f32 v155, v208, v209
v_fma_f32 v206, v86, v26, v110
v_fma_f32 v207, v87, v26, v111
v_fma_f32 v208, v88, v26, v112
v_fma_f32 v209, v89, v26, v113
v_cvt_pk_bf16_f32 v138, v206, v207
v_cvt_pk_bf16_f32 v139, v208, v209
s_waitcnt vmcnt(4) lgkmcnt(0)
s_barrier
s_mov_b32 s70, 32
s_waitcnt lgkmcnt(0)
s_nop 1
s_branch .Lqa_in47
.p2align 6
.Lqa_top70:
s_waitcnt lgkmcnt(9)
v_mfma_f32_16x16x32_bf16 v[134:137], v[0:3], v[0:3], 0
s_waitcnt lgkmcnt(8)
ds_read_b128 v[54:57], v52 offset:4096
ds_read_b32 v8, v30
v_mfma_f32_16x16x32_bf16 v[134:137], v[4:7], v[4:7], v[134:137]
ds_read_b128 v[194:197], v25 offset:64
ds_read_b64 v[160:161], v22 offset:41472
v_mfma_f32_16x16x32_bf16 v[110:113], a[0:3], v[0:3], 0
s_waitcnt lgkmcnt(8)
ds_read_b128 v[58:61], v51 offset:6144
ds_read_b32 v144, v20
v_mfma_f32_16x16x32_bf16 v[134:137], v[96:99], v[96:99], v[134:137]
s_waitcnt lgkmcnt(8)
ds_read_b128 v[62:65], v52 offset:6144
v_add_u32 v46, 1, v46
v_mfma_f32_16x16x32_bf16 v[134:137], v[100:103], v[100:103], v[134:137]
s_mov_b64 exec, 65535
global_store_dwordx4 v16, v[198:201], s[38:39] sc1
s_mov_b64 exec, -1
v_mfma_f32_16x16x32_bf16 v[114:117], a[4:7], v[0:3], 0
s_waitcnt lgkmcnt(7)
ds_read_b128 v[66:69], v51 offset:8192
v_add_u32 v16, 18432, v16
v_mfma_f32_16x16x32_bf16 v[134:137], v[104:107], v[104:107], v[134:137]
s_waitcnt lgkmcnt(7)
ds_read_b128 v[70:73], v52 offset:8192
v_fma_f32 v206, v90, v26, v126
v_mfma_f32_16x16x32_bf16 v[134:137], v[54:57], v[54:57], v[134:137]
v_fma_f32 v207, v91, v26, v127
v_fma_f32 v208, v92, v26, v128
v_mfma_f32_16x16x32_bf16 v[118:121], v[0:3], a[8:11], 0
s_waitcnt lgkmcnt(4)
ds_read_b128 v[74:77], v51 offset:10240
v_fma_f32 v209, v93, v26, v129
v_mfma_f32_16x16x32_bf16 v[134:137], v[58:61], v[58:61], v[134:137]
s_waitcnt lgkmcnt(3)
ds_read_b128 v[78:81], v52 offset:10240
v_cvt_pk_bf16_f32 v150, v206, v207
v_mfma_f32_16x16x32_bf16 v[134:137], v[62:65], v[62:65], v[134:137]
v_cvt_pk_bf16_f32 v151, v208, v209
v_max_f32 v47, v186, v187
v_mfma_f32_16x16x32_bf16 v[110:113], a[12:15], v[4:7], v[110:113]
ds_read_b128 v[82:85], v51 offset:12288
v_mfma_f32_16x16x32_bf16 v[182:185], v[150:153], v[138:141], v[32:35]
s_waitcnt lgkmcnt(4)
v_max_f32 v48, v188, v189
v_fma_f32 v206, v94, v194, v130
v_mfma_f32_16x16x32_bf16 v[134:137], v[66:69], v[66:69], v[134:137]
s_xor_b32 s49, s49, 0x10000
ds_read_b128 v[162:165], v52 offset:12288
s_waitcnt lgkmcnt(4)
v_mfma_f32_16x16x32_bf16 v[134:137], v[70:73], v[70:73], v[134:137]
s_add_u32 m0, s49, 0
v_mul_f32 v95, 0x3eae1a93, v95
v_mul_f32 v9, v255, v95
v_mfma_f32_16x16x32_bf16 v[114:117], a[16:19], v[4:7], v[114:117]
s_waitcnt lgkmcnt(3)
ds_read_b128 v[166:169], v51 offset:14336
v_mul_f32 v10, v8, v95
v_mfma_f32_16x16x32_bf16 v[134:137], v[74:77], v[74:77], v[134:137]
s_waitcnt lgkmcnt(3)
ds_read_b128 v[170:173], v52 offset:14336
v_max3_f32 v47, v182, v183, v47
v_mfma_f32_16x16x32_bf16 v[134:137], v[78:81], v[78:81], v[134:137]
v_max3_f32 v48, v184, v185, v48
v_mul_f32 v47, v47, v9
v_mfma_f32_16x16x32_bf16 v[118:121], v[4:7], a[20:23], v[118:121]
s_waitcnt lgkmcnt(3)
ds_read_b128 v[174:177], v51 offset:16384
v_mul_f32 v48, v48, v10
v_mfma_f32_16x16x32_bf16 v[134:137], v[82:85], v[82:85], v[134:137]
s_waitcnt lgkmcnt(3)
ds_read_b128 v[178:181], v52 offset:16384
global_load_lds_dwordx4 v11, s[42:43] offset:-2048 sc1
v_mfma_f32_16x16x32_bf16 v[134:137], v[162:165], v[162:165], v[134:137]
v_max_f32 v49, v47, v48
v_mov_b32 v50, v49
v_mfma_f32_16x16x32_bf16 v[110:113], a[24:27], v[96:99], v[110:113]
s_waitcnt lgkmcnt(3)
v_permlane16_swap_b32 v49, v50
v_mfma_f32_16x16x32_bf16 v[134:137], v[166:169], v[166:169], v[134:137]
s_waitcnt lgkmcnt(2)
v_max_f32 v49, v49, v50
v_mov_b32 v50, v49
v_mfma_f32_16x16x32_bf16 v[134:137], v[170:173], v[170:173], v[134:137]
s_nop 0
v_permlane32_swap_b32 v49, v50
v_mfma_f32_16x16x32_bf16 v[114:117], a[28:31], v[96:99], v[114:117]
s_waitcnt lgkmcnt(1)
v_max_f32 v49, v49, v50
v_sub_f32 v47, v47, v49
v_mfma_f32_16x16x32_bf16 v[134:137], v[174:177], v[174:177], v[134:137]
s_waitcnt lgkmcnt(0)
v_sub_f32 v48, v48, v49
v_fma_f32 v207, v94, v195, v131
v_mfma_f32_16x16x32_bf16 v[134:137], v[178:181], v[178:181], v[134:137]
v_exp_f32 v210, v47
v_mfma_f32_16x16x32_bf16 v[118:121], v[96:99], a[32:35], v[118:121]
global_load_lds_dwordx4 v12, s[42:43] offset:-1024 sc1
v_fma_f32 v208, v94, v196, v132
v_mfma_f32_16x16x32_bf16 v[110:113], a[36:39], v[100:103], v[110:113]
v_exp_f32 v211, v48
v_mfma_f32_16x16x32_bf16 v[114:117], a[40:43], v[100:103], v[114:117]
v_add_f32 v49, v210, v211
v_mov_b32 v50, v49
v_mfma_f32_16x16x32_bf16 v[118:121], v[100:103], a[44:47], v[118:121]
ds_write_b128 v23, v[134:137] offset:41984
v_mfma_f32_16x16x32_bf16 v[110:113], a[48:51], v[104:107], v[110:113]
v_mul_f32 v210, v210, v255
v_fma_f32 v209, v94, v197, v133
v_mfma_f32_16x16x32_bf16 v[114:117], a[52:55], v[104:107], v[114:117]
v_permlane16_swap_b32 v49, v50
v_mfma_f32_16x16x32_bf16 v[118:121], v[104:107], a[56:59], v[118:121]
v_add_f32 v49, v49, v50
v_mov_b32 v50, v49
v_mfma_f32_16x16x32_bf16 v[110:113], a[60:63], v[54:57], v[110:113]
v_mul_f32 v211, v211, v8
v_cvt_pk_bf16_f32 v156, v206, v207
v_mfma_f32_16x16x32_bf16 v[114:117], a[64:67], v[54:57], v[114:117]
global_load_lds_dwordx4 v13, s[42:43] offset:0 sc1
v_cvt_pk_bf16_f32 v157, v208, v209
v_mfma_f32_16x16x32_bf16 v[118:121], v[54:57], a[68:71], v[118:121]
v_mfma_f32_16x16x32_bf16 v[110:113], a[72:75], v[58:61], v[110:113]
s_waitcnt lgkmcnt(0)
s_barrier
v_mfma_f32_16x16x32_bf16 v[114:117], a[76:79], v[58:61], v[114:117]
v_permlane32_swap_b32 v49, v50
v_mfma_f32_16x16x32_bf16 v[118:121], v[58:61], a[80:83], v[118:121]
v_add_f32 v49, v49, v50
v_fma_f32 v206, v86, v26, v122
v_mfma_f32_16x16x32_bf16 v[110:113], a[84:87], v[62:65], v[110:113]
v_rcp_f32 v49, v49
v_mfma_f32_16x16x32_bf16 v[114:117], a[88:91], v[62:65], v[114:117]
v_mul_f32 v210, v210, v49
v_mul_f32 v211, v211, v49
v_mfma_f32_16x16x32_bf16 v[118:121], v[62:65], a[92:95], v[118:121]
v_cvt_pk_bf16_f32 v210, v210, v210
v_cvt_pk_bf16_f32 v211, v211, v211
v_mfma_f32_16x16x32_bf16 v[110:113], a[96:99], v[66:69], v[110:113]
global_load_lds_dwordx4 v14, s[42:43] offset:1024 sc1
s_add_u32 s42, s42, s46
s_addc_u32 s43, s43, s47
v_mfma_f32_16x16x32_bf16 v[114:117], a[100:103], v[66:69], v[114:117]
v_and_b32 v192, v210, v40
v_and_b32 v193, v211, v41
v_mfma_f32_16x16x32_bf16 v[118:121], v[66:69], a[104:107], v[118:121]
v_and_b32 v190, v210, v42
v_and_b32 v191, v211, v43
v_mfma_f32_16x16x32_bf16 v[110:113], a[108:111], v[70:73], v[110:113]
s_nop 0
v_mfma_f32_16x16x32_bf16 v[186:189], v[154:157], v[190:193], 0
v_mfma_f32_16x16x32_bf16 v[182:185], v[158:161], v[190:193], 0
v_fma_f32 v207, v87, v26, v123
v_fma_f32 v208, v88, v26, v124
v_mfma_f32_16x16x32_bf16 v[114:117], a[112:115], v[70:73], v[114:117]
v_fma_f32 v209, v89, v26, v125
v_cvt_pk_bf16_f32 v142, v206, v207
v_mfma_f32_16x16x32_bf16 v[118:121], v[70:73], a[116:119], v[118:121]
v_cvt_pk_bf16_f32 v143, v208, v209
v_mfma_f32_16x16x32_bf16 v[110:113], a[120:123], v[74:77], v[110:113]
v_mfma_f32_16x16x32_bf16 v[114:117], a[124:127], v[74:77], v[114:117]
v_cvt_pk_bf16_f32 v108, v186, v187
v_cvt_pk_bf16_f32 v109, v188, v189
v_mfma_f32_16x16x32_bf16 v[118:121], v[74:77], a[128:131], v[118:121]
ds_write_b64 v18, v[108:109]
v_mfma_f32_16x16x32_bf16 v[110:113], a[132:135], v[78:81], v[110:113]
v_cvt_pk_bf16_f32 v210, v182, v183
v_mfma_f32_16x16x32_bf16 v[114:117], a[136:139], v[78:81], v[114:117]
ds_write_b32 v19, v210
v_mfma_f32_16x16x32_bf16 v[118:121], v[78:81], a[140:143], v[118:121]
v_mfma_f32_16x16x32_bf16 v[110:113], a[144:147], v[82:85], v[110:113]
v_mfma_f32_16x16x32_bf16 v[114:117], a[148:151], v[82:85], v[114:117]
v_mfma_f32_16x16x32_bf16 v[118:121], v[82:85], a[152:155], v[118:121]
v_mfma_f32_16x16x32_bf16 v[110:113], a[156:159], v[162:165], v[110:113]
v_add_lshl_u32 v253, v46, 1, 7
v_bfi_b32 v253, s69, v253, v44
v_mfma_f32_16x16x32_bf16 v[114:117], a[160:163], v[162:165], v[114:117]
global_load_dword v254, v253, s[38:39] sc1
v_mfma_f32_16x16x32_bf16 v[118:121], v[162:165], a[164:167], v[118:121]
s_waitcnt vmcnt(6)
v_sub_u32 v53, v53, v46
v_cmp_lt_u32 vcc, 0x7f, v53
v_mfma_f32_16x16x32_bf16 v[110:113], a[168:171], v[166:169], v[110:113]
v_mfma_f32_16x16x32_bf16 v[114:117], a[172:175], v[166:169], v[114:117]
v_mfma_f32_16x16x32_bf16 v[118:121], v[166:169], a[176:179], v[118:121]
s_and_b64 vcc, vcc, s[76:77]
s_cbranch_vccnz .Lqa_sp50
.Lqa_rt51:
v_mfma_f32_16x16x32_bf16 v[110:113], a[180:183], v[170:173], v[110:113]
v_mfma_f32_16x16x32_bf16 v[114:117], a[184:187], v[170:173], v[114:117]
s_waitcnt vmcnt(5) lgkmcnt(0)
s_barrier
v_mfma_f32_16x16x32_bf16 v[118:121], v[170:173], a[188:191], v[118:121]
ds_read_b128 v[0:3], v51 offset:18432
v_mfma_f32_16x16x32_bf16 v[110:113], a[192:195], v[174:177], v[110:113]
ds_read_b128 v[4:7], v52 offset:18432
ds_read_b128 v[198:201], v17 offset:0
v_mfma_f32_16x16x32_bf16 v[114:117], a[196:199], v[174:177], v[114:117]
ds_read_b32 v26, v24 offset:0
ds_read_b32 v148, v21
v_mfma_f32_16x16x32_bf16 v[186:189], v[150:153], v[142:145], v[36:39]
v_mfma_f32_16x16x32_bf16 v[118:121], v[174:177], a[200:203], v[118:121]
ds_read_b128 v[96:99], v51 offset:20480
ds_read_b32 v95, v24 offset:192
v_mfma_f32_16x16x32_bf16 v[110:113], a[204:207], v[178:181], v[110:113]
ds_read_b128 v[100:103], v52 offset:20480
ds_read_b32 v255, v27
v_mfma_f32_16x16x32_bf16 v[114:117], a[208:211], v[178:181], v[114:117]
ds_read_b32 v8, v28
ds_read_b128 v[194:197], v25 offset:0
v_mfma_f32_16x16x32_bf16 v[118:121], v[178:181], a[212:215], v[118:121]
s_waitcnt lgkmcnt(10)
ds_read_b128 v[104:107], v51 offset:22528
ds_read_b64 v[158:159], v22 offset:41472
v_mfma_f32_16x16x32_bf16 v[134:137], v[0:3], v[0:3], 0
s_waitcnt lgkmcnt(11)
ds_read_b128 v[54:57], v52 offset:22528
ds_read_b32 v140, v20
v_mfma_f32_16x16x32_bf16 v[134:137], v[4:7], v[4:7], v[134:137]
v_add_u32 v46, 1, v46
v_max_f32 v47, v186, v187
v_mfma_f32_16x16x32_bf16 v[122:125], a[0:3], v[0:3], 0
s_waitcnt lgkmcnt(11)
ds_read_b128 v[58:61], v51 offset:24576
v_fma_f32 v206, v90, v26, v114
s_waitcnt lgkmcnt(10)
v_mfma_f32_16x16x32_bf16 v[134:137], v[96:99], v[96:99], v[134:137]
s_waitcnt lgkmcnt(8)
ds_read_b128 v[62:65], v52 offset:24576
v_fma_f32 v207, v91, v26, v115
v_mfma_f32_16x16x32_bf16 v[134:137], v[100:103], v[100:103], v[134:137]
s_mov_b64 exec, 65535
global_store_dwordx4 v16, v[198:201], s[38:39] sc1
s_mov_b64 exec, -1
v_mfma_f32_16x16x32_bf16 v[126:129], a[4:7], v[0:3], 0
s_waitcnt lgkmcnt(5)
ds_read_b128 v[66:69], v51 offset:26624
v_add_u32 v16, 18432, v16
v_mfma_f32_16x16x32_bf16 v[134:137], v[104:107], v[104:107], v[134:137]
s_waitcnt lgkmcnt(4)
ds_read_b128 v[70:73], v52 offset:26624
v_fma_f32 v208, v92, v26, v116
v_mfma_f32_16x16x32_bf16 v[134:137], v[54:57], v[54:57], v[134:137]
v_fma_f32 v209, v93, v26, v117
v_cvt_pk_bf16_f32 v146, v206, v207
v_mfma_f32_16x16x32_bf16 v[130:133], v[0:3], a[8:11], 0
s_waitcnt lgkmcnt(3)
ds_read_b128 v[74:77], v51 offset:28672
v_cvt_pk_bf16_f32 v147, v208, v209
v_mfma_f32_16x16x32_bf16 v[134:137], v[58:61], v[58:61], v[134:137]
ds_read_b128 v[78:81], v52 offset:28672
v_mfma_f32_16x16x32_bf16 v[182:185], v[146:149], v[142:145], v[202:205]
s_waitcnt lgkmcnt(4)
v_max_f32 v48, v188, v189
v_fma_f32 v206, v94, v194, v118
v_mfma_f32_16x16x32_bf16 v[134:137], v[62:65], v[62:65], v[134:137]
v_fma_f32 v207, v94, v195, v119
v_fma_f32 v208, v94, v196, v120
v_mfma_f32_16x16x32_bf16 v[122:125], a[12:15], v[4:7], v[122:125]
s_waitcnt lgkmcnt(3)
ds_read_b128 v[82:85], v51 offset:30720
v_mul_f32 v95, 0x3eae1a93, v95
)"
R"(v_mfma_f32_16x16x32_bf16 v[134:137], v[66:69], v[66:69], v[134:137]
s_add_u32 m0, s49, 18432
ds_read_b128 v[162:165], v52 offset:30720
s_waitcnt lgkmcnt(4)
v_mfma_f32_16x16x32_bf16 v[134:137], v[70:73], v[70:73], v[134:137]
v_mul_f32 v9, v255, v95
v_mul_f32 v10, v8, v95
v_mfma_f32_16x16x32_bf16 v[126:129], a[16:19], v[4:7], v[126:129]
s_waitcnt lgkmcnt(3)
ds_read_b128 v[166:169], v51 offset:32768
v_max3_f32 v47, v182, v183, v47
v_mfma_f32_16x16x32_bf16 v[134:137], v[74:77], v[74:77], v[134:137]
s_waitcnt lgkmcnt(3)
ds_read_b128 v[170:173], v52 offset:32768
v_max3_f32 v48, v184, v185, v48
v_mfma_f32_16x16x32_bf16 v[134:137], v[78:81], v[78:81], v[134:137]
v_mul_f32 v47, v47, v9
v_mul_f32 v48, v48, v10
v_mfma_f32_16x16x32_bf16 v[130:133], v[4:7], a[20:23], v[130:133]
s_waitcnt lgkmcnt(3)
ds_read_b128 v[174:177], v51 offset:34816
global_load_lds_dwordx4 v11, s[42:43] offset:-2048 sc1
v_mfma_f32_16x16x32_bf16 v[134:137], v[82:85], v[82:85], v[134:137]
s_waitcnt lgkmcnt(3)
ds_read_b128 v[178:181], v52 offset:34816
v_max_f32 v49, v47, v48
v_mfma_f32_16x16x32_bf16 v[134:137], v[162:165], v[162:165], v[134:137]
v_mov_b32 v50, v49
v_fma_f32 v209, v94, v197, v121
v_mfma_f32_16x16x32_bf16 v[122:125], a[24:27], v[96:99], v[122:125]
s_waitcnt lgkmcnt(3)
v_permlane16_swap_b32 v49, v50
v_mfma_f32_16x16x32_bf16 v[134:137], v[166:169], v[166:169], v[134:137]
s_waitcnt lgkmcnt(2)
v_max_f32 v49, v49, v50
v_mov_b32 v50, v49
v_mfma_f32_16x16x32_bf16 v[134:137], v[170:173], v[170:173], v[134:137]
s_nop 0
v_permlane32_swap_b32 v49, v50
v_mfma_f32_16x16x32_bf16 v[126:129], a[28:31], v[96:99], v[126:129]
s_waitcnt lgkmcnt(1)
v_max_f32 v49, v49, v50
v_sub_f32 v47, v47, v49
v_mfma_f32_16x16x32_bf16 v[134:137], v[174:177], v[174:177], v[134:137]
s_waitcnt lgkmcnt(0)
v_sub_f32 v48, v48, v49
v_cvt_pk_bf16_f32 v154, v206, v207
v_mfma_f32_16x16x32_bf16 v[134:137], v[178:181], v[178:181], v[134:137]
global_load_lds_dwordx4 v12, s[42:43] offset:-1024 sc1
v_cvt_pk_bf16_f32 v155, v208, v209
v_mfma_f32_16x16x32_bf16 v[130:133], v[96:99], a[32:35], v[130:133]
v_exp_f32 v210, v47
v_mfma_f32_16x16x32_bf16 v[122:125], a[36:39], v[100:103], v[122:125]
v_exp_f32 v211, v48
v_mfma_f32_16x16x32_bf16 v[126:129], a[40:43], v[100:103], v[126:129]
v_add_f32 v49, v210, v211
v_mov_b32 v50, v49
v_mfma_f32_16x16x32_bf16 v[130:133], v[100:103], a[44:47], v[130:133]
ds_write_b128 v23, v[134:137] offset:41984
v_mfma_f32_16x16x32_bf16 v[122:125], a[48:51], v[104:107], v[122:125]
v_mul_f32 v210, v210, v255
v_fma_f32 v206, v86, v26, v110
v_mfma_f32_16x16x32_bf16 v[126:129], a[52:55], v[104:107], v[126:129]
v_permlane16_swap_b32 v49, v50
v_mfma_f32_16x16x32_bf16 v[130:133], v[104:107], a[56:59], v[130:133]
v_add_f32 v49, v49, v50
v_mov_b32 v50, v49
v_mfma_f32_16x16x32_bf16 v[122:125], a[60:63], v[54:57], v[122:125]
global_load_lds_dwordx4 v13, s[42:43] offset:0 sc1
v_mul_f32 v211, v211, v8
v_mfma_f32_16x16x32_bf16 v[126:129], a[64:67], v[54:57], v[126:129]
v_permlane32_swap_b32 v49, v50
v_mfma_f32_16x16x32_bf16 v[130:133], v[54:57], a[68:71], v[130:133]
v_mfma_f32_16x16x32_bf16 v[122:125], a[72:75], v[58:61], v[122:125]
s_waitcnt lgkmcnt(0)
s_barrier
v_mfma_f32_16x16x32_bf16 v[126:129], a[76:79], v[58:61], v[126:129]
v_add_f32 v49, v49, v50
v_fma_f32 v207, v87, v26, v111
v_mfma_f32_16x16x32_bf16 v[130:133], v[58:61], a[80:83], v[130:133]
v_rcp_f32 v49, v49
v_mfma_f32_16x16x32_bf16 v[122:125], a[84:87], v[62:65], v[122:125]
v_mul_f32 v210, v210, v49
v_mul_f32 v211, v211, v49
v_mfma_f32_16x16x32_bf16 v[126:129], a[88:91], v[62:65], v[126:129]
v_cvt_pk_bf16_f32 v210, v210, v210
v_cvt_pk_bf16_f32 v211, v211, v211
v_mfma_f32_16x16x32_bf16 v[130:133], v[62:65], a[92:95], v[130:133]
global_load_lds_dwordx4 v14, s[42:43] offset:1024 sc1
s_sub_u32 s42, s42, 18432
s_subb_u32 s43, s43, 0
v_mfma_f32_16x16x32_bf16 v[122:125], a[96:99], v[66:69], v[122:125]
v_and_b32 v190, v210, v40
v_and_b32 v191, v211, v41
v_mfma_f32_16x16x32_bf16 v[126:129], a[100:103], v[66:69], v[126:129]
v_and_b32 v192, v210, v42
v_and_b32 v193, v211, v43
v_mfma_f32_16x16x32_bf16 v[130:133], v[66:69], a[104:107], v[130:133]
s_nop 0
v_mfma_f32_16x16x32_bf16 v[186:189], v[154:157], v[190:193], 0
v_mfma_f32_16x16x32_bf16 v[182:185], v[158:161], v[190:193], 0
v_fma_f32 v208, v88, v26, v112
v_fma_f32 v209, v89, v26, v113
v_mfma_f32_16x16x32_bf16 v[122:125], a[108:111], v[70:73], v[122:125]
v_cvt_pk_bf16_f32 v138, v206, v207
v_cvt_pk_bf16_f32 v139, v208, v209
v_mfma_f32_16x16x32_bf16 v[126:129], a[112:115], v[70:73], v[126:129]
v_mfma_f32_16x16x32_bf16 v[130:133], v[70:73], a[116:119], v[130:133]
v_mfma_f32_16x16x32_bf16 v[122:125], a[120:123], v[74:77], v[122:125]
v_cvt_pk_bf16_f32 v108, v186, v187
v_cvt_pk_bf16_f32 v109, v188, v189
v_mfma_f32_16x16x32_bf16 v[126:129], a[124:127], v[74:77], v[126:129]
ds_write_b64 v18, v[108:109]
v_mfma_f32_16x16x32_bf16 v[130:133], v[74:77], a[128:131], v[130:133]
v_cvt_pk_bf16_f32 v210, v182, v183
v_mfma_f32_16x16x32_bf16 v[122:125], a[132:135], v[78:81], v[122:125]
ds_write_b32 v19, v210
v_mfma_f32_16x16x32_bf16 v[126:129], a[136:139], v[78:81], v[126:129]
v_mfma_f32_16x16x32_bf16 v[130:133], v[78:81], a[140:143], v[130:133]
v_mfma_f32_16x16x32_bf16 v[122:125], a[144:147], v[82:85], v[122:125]
v_mfma_f32_16x16x32_bf16 v[126:129], a[148:151], v[82:85], v[126:129]
v_mfma_f32_16x16x32_bf16 v[130:133], v[82:85], a[152:155], v[130:133]
v_mfma_f32_16x16x32_bf16 v[122:125], a[156:159], v[162:165], v[122:125]
v_add_lshl_u32 v253, v46, 1, 7
v_bfi_b32 v253, s69, v253, v45
v_mfma_f32_16x16x32_bf16 v[126:129], a[160:163], v[162:165], v[126:129]
global_load_dword v53, v253, s[38:39] sc1
v_mfma_f32_16x16x32_bf16 v[130:133], v[162:165], a[164:167], v[130:133]
s_waitcnt vmcnt(6)
v_sub_u32 v254, v254, v46
v_cmp_lt_u32 vcc, 0x7f, v254
v_mfma_f32_16x16x32_bf16 v[122:125], a[168:171], v[166:169], v[122:125]
v_mfma_f32_16x16x32_bf16 v[126:129], a[172:175], v[166:169], v[126:129]
v_xor_b32 v51, 0x10000, v51
v_xor_b32 v52, 0x10000, v52
v_mfma_f32_16x16x32_bf16 v[130:133], v[166:169], a[176:179], v[130:133]
s_and_b64 vcc, vcc, s[76:77]
s_cbranch_vccnz .Lqa_sp52
.Lqa_rt53:
v_mfma_f32_16x16x32_bf16 v[122:125], a[180:183], v[170:173], v[122:125]
v_mfma_f32_16x16x32_bf16 v[126:129], a[184:187], v[170:173], v[126:129]
s_waitcnt vmcnt(5) lgkmcnt(0)
s_barrier
.Lqa_in47:
v_mfma_f32_16x16x32_bf16 v[130:133], v[170:173], a[188:191], v[130:133]
s_cmp_eq_u32 s70, s73
s_cbranch_scc1 .Lqa_hk48
.Lqa_hr49:
ds_read_b128 v[0:3], v51 offset:0
v_mfma_f32_16x16x32_bf16 v[122:125], a[192:195], v[174:177], v[122:125]
ds_read_b128 v[4:7], v52 offset:0
ds_read_b128 v[198:201], v17 offset:0
v_mfma_f32_16x16x32_bf16 v[126:129], a[196:199], v[174:177], v[126:129]
ds_read_b32 v26, v24 offset:64
ds_read_b32 v152, v21
v_mfma_f32_16x16x32_bf16 v[186:189], v[146:149], v[138:141], v[36:39]
v_mfma_f32_16x16x32_bf16 v[130:133], v[174:177], a[200:203], v[130:133]
ds_read_b128 v[96:99], v51 offset:2048
ds_read_b32 v95, v24 offset:128
v_mfma_f32_16x16x32_bf16 v[122:125], a[204:207], v[178:181], v[122:125]
ds_read_b128 v[100:103], v52 offset:2048
ds_read_b32 v255, v29
v_mfma_f32_16x16x32_bf16 v[126:129], a[208:211], v[178:181], v[126:129]
v_mfma_f32_16x16x32_bf16 v[130:133], v[178:181], a[212:215], v[130:133]
s_sub_u32 s70, s70, 1
ds_read_b128 v[104:107], v51 offset:4096
s_cbranch_scc0 .Lqa_top70
s_waitcnt lgkmcnt(0)
ds_read_b32 v8, v30
ds_read_b128 v[194:197], v25 offset:64
ds_read_b64 v[160:161], v22 offset:41472
ds_read_b32 v144, v20
s_mov_b64 exec, 65535
global_store_dwordx4 v16, v[198:201], s[38:39] sc1
s_mov_b64 exec, -1
v_add_u32 v16, 18432, v16
v_fma_f32 v206, v90, v26, v126
v_fma_f32 v207, v91, v26, v127
v_fma_f32 v208, v92, v26, v128
v_fma_f32 v209, v93, v26, v129
v_cvt_pk_bf16_f32 v150, v206, v207
v_cvt_pk_bf16_f32 v151, v208, v209
v_max_f32 v47, v186, v187
s_nop 0
v_mfma_f32_16x16x32_bf16 v[182:185], v[150:153], v[138:141], v[32:35]
s_waitcnt lgkmcnt(2)
v_max_f32 v48, v188, v189
v_fma_f32 v206, v94, v194, v130
v_mul_f32 v95, 0x3eae1a93, v95
v_mul_f32 v9, v255, v95
v_mul_f32 v10, v8, v95
s_nop 1
v_max3_f32 v47, v182, v183, v47
v_max3_f32 v48, v184, v185, v48
v_mul_f32 v47, v47, v9
v_mul_f32 v48, v48, v10
v_max_f32 v49, v47, v48
v_mov_b32 v50, v49
s_nop 1
v_permlane16_swap_b32 v49, v50
v_max_f32 v49, v49, v50
v_mov_b32 v50, v49
s_nop 1
v_permlane32_swap_b32 v49, v50
v_max_f32 v49, v49, v50
v_sub_f32 v47, v47, v49
v_sub_f32 v48, v48, v49
v_fma_f32 v207, v94, v195, v131
v_exp_f32 v210, v47
v_fma_f32 v208, v94, v196, v132
v_exp_f32 v211, v48
s_nop 0
v_add_f32 v49, v210, v211
v_mov_b32 v50, v49
v_mul_f32 v210, v210, v255
v_fma_f32 v209, v94, v197, v133
v_permlane16_swap_b32 v49, v50
v_add_f32 v49, v49, v50
v_mov_b32 v50, v49
v_mul_f32 v211, v211, v8
v_cvt_pk_bf16_f32 v156, v206, v207
v_cvt_pk_bf16_f32 v157, v208, v209
s_waitcnt lgkmcnt(0)
s_barrier
v_permlane32_swap_b32 v49, v50
v_add_f32 v49, v49, v50
v_fma_f32 v206, v86, v26, v122
v_rcp_f32 v49, v49
s_nop 0
v_mul_f32 v210, v210, v49
v_mul_f32 v211, v211, v49
v_cvt_pk_bf16_f32 v210, v210, v210
v_cvt_pk_bf16_f32 v211, v211, v211
v_and_b32 v192, v210, v40
v_and_b32 v193, v211, v41
v_and_b32 v190, v210, v42
v_and_b32 v191, v211, v43
s_nop 1
v_mfma_f32_16x16x32_bf16 v[186:189], v[154:157], v[190:193], 0
v_mfma_f32_16x16x32_bf16 v[182:185], v[158:161], v[190:193], 0
v_fma_f32 v207, v87, v26, v123
v_fma_f32 v208, v88, v26, v124
v_fma_f32 v209, v89, v26, v125
v_cvt_pk_bf16_f32 v142, v206, v207
v_cvt_pk_bf16_f32 v143, v208, v209
s_nop 1
v_cvt_pk_bf16_f32 v108, v186, v187
v_cvt_pk_bf16_f32 v109, v188, v189
ds_write_b64 v18, v[108:109]
v_cvt_pk_bf16_f32 v210, v182, v183
ds_write_b32 v19, v210
s_waitcnt vmcnt(0)
s_waitcnt lgkmcnt(0)
s_barrier
ds_read_b128 v[198:201], v17 offset:0
s_waitcnt lgkmcnt(0)
global_store_dwordx4 v16, v[198:201], s[38:39] sc1
s_waitcnt vmcnt(0)
s_barrier
s_endpgm
.Lqa_w4:
s_getpc_b64 s[90:91]
.Lqa_pc90:
s_add_u32 s90, s90, .Lqa_e75-.Lqa_pc90
s_addc_u32 s91, s91, 0
s_branch .Lqa_k82
.Lqa_bk89:
v_add_u32 v255, 0x10000, v9
v_add_u32 v162, 0x20000, v9
v_add_u32 v163, 0x10000, v10
v_add_u32 v164, 0x20000, v10
v_mov_b32 v106, 0x3F803F80
v_mov_b32 v107, 0x3F803F80
v_mov_b32 v108, 0x3F803F80
v_mov_b32 v109, 0x3F803F80
v_mov_b32 v141, 0
v_mov_b32 v145, 0
v_mov_b32 v149, 0
v_mov_b32 v153, 0
v_mov_b32 v165, 46080
s_waitcnt lgkmcnt(0)
s_mul_i32 s37, s37, 0x9E3779B1
s_add_u32 s37, s37, 0x7F4A7C15
v_add_u32 v46, s37, v46
v_add_u32 v48, s37, v48
v_add_u32 v50, s37, v50
ds_write_b32 v165, v141
s_mov_b64 s[76:77], 0
s_mov_b32 s73, 18
s_add_u32 m0, s48, 4096
s_nop 0
global_load_lds_dwordx4 v0, s[40:41] offset:-4096
global_load_lds_dwordx4 v1, s[40:41] offset:-3072
global_load_lds_dwordx4 v2, s[40:41] offset:-2048
global_load_lds_dwordx4 v3, s[40:41] offset:-1024
global_load_lds_dwordx4 v4, s[40:41] offset:0
global_load_lds_dwordx4 v5, s[40:41] offset:1024
global_load_lds_dwordx4 v6, s[40:41] offset:2048
global_load_lds_dwordx4 v7, s[40:41] offset:3072
s_add_u32 m0, s48, 12288
s_nop 0
global_load_lds_dwordx4 v8, s[40:41] offset:-4096
s_add_u32 s40, s40, 221184
s_addc_u32 s41, s41, 0
s_add_u32 m0, s48, 40960
s_nop 0
global_load_lds_dwordx4 v0, s[40:41] offset:-4096
global_load_lds_dwordx4 v1, s[40:41] offset:-3072
global_load_lds_dwordx4 v2, s[40:41] offset:-2048
global_load_lds_dwordx4 v3, s[40:41] offset:-1024
global_load_lds_dwordx4 v4, s[40:41] offset:0
global_load_lds_dwordx4 v5, s[40:41] offset:1024
global_load_lds_dwordx4 v6, s[40:41] offset:2048
global_load_lds_dwordx4 v7, s[40:41] offset:3072
s_add_u32 m0, s48, 49152
s_nop 0
global_load_lds_dwordx4 v8, s[40:41] offset:-4096
s_add_u32 s40, s40, 221184
s_addc_u32 s41, s41, 0
s_waitcnt vmcnt(9) lgkmcnt(0)
s_barrier
s_add_u32 m0, s48, 77824
ds_read_b64_tr_b16 a[0:1], v9 offset:47104
ds_read_b64_tr_b16 a[2:3], v9 offset:49408
global_load_lds_dwordx4 v0, s[40:41] offset:-4096
ds_read_b64_tr_b16 a[4:5], v9 offset:47296
global_load_lds_dwordx4 v1, s[40:41] offset:-3072
ds_read_b64_tr_b16 a[6:7], v9 offset:49600
global_load_lds_dwordx4 v2, s[40:41] offset:-2048
ds_read_b64_tr_b16 a[8:9], v9 offset:47488
global_load_lds_dwordx4 v3, s[40:41] offset:-1024
ds_read_b64_tr_b16 a[10:11], v9 offset:49792
ds_read_b128 v[166:169], v51 offset:0
global_load_lds_dwordx4 v4, s[40:41] offset:0
ds_read_b128 v[174:177], v51 offset:18432
global_load_lds_dwordx4 v5, s[40:41] offset:1024
ds_read_b64_tr_b16 a[12:13], v255 offset:0
global_load_lds_dwordx4 v6, s[40:41] offset:2048
ds_read_b64_tr_b16 a[14:15], v255 offset:2304
global_load_lds_dwordx4 v7, s[40:41] offset:3072
ds_read_b64_tr_b16 a[16:17], v255 offset:192
ds_read_b64_tr_b16 a[18:19], v255 offset:2496
s_add_u32 m0, s48, 86016
ds_read_b64_tr_b16 a[20:21], v255 offset:384
global_load_lds_dwordx4 v8, s[40:41] offset:-4096
ds_read_b64_tr_b16 a[22:23], v255 offset:2688
s_add_u32 s40, s40, 221184
ds_read_b128 v[170:173], v52 offset:0
s_addc_u32 s41, s41, 0
ds_read_b128 v[178:181], v52 offset:18432
s_waitcnt vmcnt(9) lgkmcnt(0)
s_barrier
s_add_u32 m0, s48, 4096
ds_read_b64_tr_b16 a[24:25], v255 offset:18432
v_mfma_f32_16x16x32_bf16 v[86:89], a[0:3], v[106:109], 0
v_mfma_f32_16x16x32_bf16 v[90:93], a[4:7], v[106:109], 0
global_load_lds_dwordx4 v0, s[40:41] offset:-4096
ds_read_b64_tr_b16 a[26:27], v255 offset:20736
v_mfma_f32_16x16x32_bf16 v[94:97], v[106:109], a[8:11], 0
ds_read_b64_tr_b16 a[28:29], v255 offset:18624
global_load_lds_dwordx4 v1, s[40:41] offset:-3072
v_mfma_f32_16x16x32_bf16 v[110:113], a[0:3], v[166:169], 0
ds_read_b64_tr_b16 a[30:31], v255 offset:20928
v_mfma_f32_16x16x32_bf16 v[114:117], a[4:7], v[166:169], 0
global_load_lds_dwordx4 v2, s[40:41] offset:-2048
ds_read_b64_tr_b16 a[32:33], v255 offset:18816
v_mfma_f32_16x16x32_bf16 v[118:121], v[166:169], a[8:11], 0
ds_read_b64_tr_b16 a[34:35], v255 offset:21120
global_load_lds_dwordx4 v3, s[40:41] offset:-1024
v_mfma_f32_16x16x32_bf16 v[134:137], v[106:109], v[166:169], 0
ds_read_b128 v[182:185], v51 offset:2048
v_mfma_f32_16x16x32_bf16 v[122:125], a[0:3], v[174:177], 0
global_load_lds_dwordx4 v4, s[40:41] offset:0
ds_read_b128 v[190:193], v51 offset:20480
v_mfma_f32_16x16x32_bf16 v[126:129], a[4:7], v[174:177], 0
ds_read_b64_tr_b16 a[36:37], v255 offset:36864
global_load_lds_dwordx4 v5, s[40:41] offset:1024
v_mfma_f32_16x16x32_bf16 v[130:133], v[174:177], a[8:11], 0
v_mfma_f32_16x16x32_bf16 v[158:161], v[106:109], v[174:177], 0
ds_read_b64_tr_b16 a[38:39], v255 offset:39168
global_load_lds_dwordx4 v6, s[40:41] offset:2048
v_mfma_f32_16x16x32_bf16 v[86:89], a[12:15], v[106:109], v[86:89]
ds_read_b64_tr_b16 a[40:41], v255 offset:37056
v_mfma_f32_16x16x32_bf16 v[90:93], a[16:19], v[106:109], v[90:93]
global_load_lds_dwordx4 v7, s[40:41] offset:3072
ds_read_b64_tr_b16 a[42:43], v255 offset:39360
v_mfma_f32_16x16x32_bf16 v[94:97], v[106:109], a[20:23], v[94:97]
s_add_u32 m0, s48, 12288
ds_read_b64_tr_b16 a[44:45], v255 offset:37248
v_mfma_f32_16x16x32_bf16 v[110:113], a[12:15], v[170:173], v[110:113]
ds_read_b64_tr_b16 a[46:47], v255 offset:39552
v_mfma_f32_16x16x32_bf16 v[114:117], a[16:19], v[170:173], v[114:117]
global_load_lds_dwordx4 v8, s[40:41] offset:-4096
ds_read_b128 v[186:189], v52 offset:2048
v_mfma_f32_16x16x32_bf16 v[118:121], v[170:173], a[20:23], v[118:121]
s_add_u32 s40, s40, 221184
ds_read_b128 v[194:197], v52 offset:20480
v_mfma_f32_16x16x32_bf16 v[134:137], v[106:109], v[170:173], v[134:137]
v_mfma_f32_16x16x32_bf16 v[122:125], a[12:15], v[178:181], v[122:125]
v_mfma_f32_16x16x32_bf16 v[126:129], a[16:19], v[178:181], v[126:129]
)"
R"(s_addc_u32 s41, s41, 0
v_mfma_f32_16x16x32_bf16 v[130:133], v[178:181], a[20:23], v[130:133]
v_mfma_f32_16x16x32_bf16 v[158:161], v[106:109], v[178:181], v[158:161]
s_waitcnt vmcnt(9) lgkmcnt(0)
s_barrier
s_add_u32 m0, s48, 40960
ds_read_b64_tr_b16 a[48:49], v255 offset:55296
v_mfma_f32_16x16x32_bf16 v[86:89], a[24:27], v[106:109], v[86:89]
v_mfma_f32_16x16x32_bf16 v[90:93], a[28:31], v[106:109], v[90:93]
global_load_lds_dwordx4 v0, s[40:41] offset:-4096
ds_read_b64_tr_b16 a[50:51], v255 offset:57600
v_mfma_f32_16x16x32_bf16 v[94:97], v[106:109], a[32:35], v[94:97]
ds_read_b64_tr_b16 a[52:53], v255 offset:55488
global_load_lds_dwordx4 v1, s[40:41] offset:-3072
v_mfma_f32_16x16x32_bf16 v[110:113], a[24:27], v[182:185], v[110:113]
ds_read_b64_tr_b16 a[54:55], v255 offset:57792
v_mfma_f32_16x16x32_bf16 v[114:117], a[28:31], v[182:185], v[114:117]
global_load_lds_dwordx4 v2, s[40:41] offset:-2048
ds_read_b64_tr_b16 a[56:57], v255 offset:55680
v_mfma_f32_16x16x32_bf16 v[118:121], v[182:185], a[32:35], v[118:121]
ds_read_b64_tr_b16 a[58:59], v255 offset:57984
global_load_lds_dwordx4 v3, s[40:41] offset:-1024
v_mfma_f32_16x16x32_bf16 v[134:137], v[106:109], v[182:185], v[134:137]
ds_read_b128 v[166:169], v51 offset:4096
v_mfma_f32_16x16x32_bf16 v[122:125], a[24:27], v[190:193], v[122:125]
global_load_lds_dwordx4 v4, s[40:41] offset:0
ds_read_b128 v[174:177], v51 offset:22528
v_mfma_f32_16x16x32_bf16 v[126:129], a[28:31], v[190:193], v[126:129]
ds_read_b64_tr_b16 a[60:61], v162 offset:8192
global_load_lds_dwordx4 v5, s[40:41] offset:1024
v_mfma_f32_16x16x32_bf16 v[130:133], v[190:193], a[32:35], v[130:133]
v_mfma_f32_16x16x32_bf16 v[158:161], v[106:109], v[190:193], v[158:161]
ds_read_b64_tr_b16 a[62:63], v162 offset:10496
global_load_lds_dwordx4 v6, s[40:41] offset:2048
v_mfma_f32_16x16x32_bf16 v[86:89], a[36:39], v[106:109], v[86:89]
ds_read_b64_tr_b16 a[64:65], v162 offset:8384
v_mfma_f32_16x16x32_bf16 v[90:93], a[40:43], v[106:109], v[90:93]
global_load_lds_dwordx4 v7, s[40:41] offset:3072
ds_read_b64_tr_b16 a[66:67], v162 offset:10688
v_mfma_f32_16x16x32_bf16 v[94:97], v[106:109], a[44:47], v[94:97]
s_add_u32 m0, s48, 49152
ds_read_b64_tr_b16 a[68:69], v162 offset:8576
v_mfma_f32_16x16x32_bf16 v[110:113], a[36:39], v[186:189], v[110:113]
ds_read_b64_tr_b16 a[70:71], v162 offset:10880
v_mfma_f32_16x16x32_bf16 v[114:117], a[40:43], v[186:189], v[114:117]
global_load_lds_dwordx4 v8, s[40:41] offset:-4096
ds_read_b128 v[170:173], v52 offset:4096
v_mfma_f32_16x16x32_bf16 v[118:121], v[186:189], a[44:47], v[118:121]
s_add_u32 s40, s40, 221184
ds_read_b128 v[178:181], v52 offset:22528
v_mfma_f32_16x16x32_bf16 v[134:137], v[106:109], v[186:189], v[134:137]
v_mfma_f32_16x16x32_bf16 v[122:125], a[36:39], v[194:197], v[122:125]
v_mfma_f32_16x16x32_bf16 v[126:129], a[40:43], v[194:197], v[126:129]
s_addc_u32 s41, s41, 0
v_mfma_f32_16x16x32_bf16 v[130:133], v[194:197], a[44:47], v[130:133]
v_mfma_f32_16x16x32_bf16 v[158:161], v[106:109], v[194:197], v[158:161]
s_waitcnt vmcnt(9) lgkmcnt(0)
s_barrier
s_add_u32 m0, s48, 77824
ds_read_b64_tr_b16 a[72:73], v9 offset:47104
v_mfma_f32_16x16x32_bf16 v[86:89], a[48:51], v[106:109], v[86:89]
v_mfma_f32_16x16x32_bf16 v[90:93], a[52:55], v[106:109], v[90:93]
global_load_lds_dwordx4 v0, s[40:41] offset:-4096
ds_read_b64_tr_b16 a[74:75], v9 offset:49408
v_mfma_f32_16x16x32_bf16 v[94:97], v[106:109], a[56:59], v[94:97]
ds_read_b64_tr_b16 a[76:77], v9 offset:47296
global_load_lds_dwordx4 v1, s[40:41] offset:-3072
v_mfma_f32_16x16x32_bf16 v[110:113], a[48:51], v[166:169], v[110:113]
ds_read_b64_tr_b16 a[78:79], v9 offset:49600
v_mfma_f32_16x16x32_bf16 v[114:117], a[52:55], v[166:169], v[114:117]
global_load_lds_dwordx4 v2, s[40:41] offset:-2048
ds_read_b64_tr_b16 a[80:81], v9 offset:47488
v_mfma_f32_16x16x32_bf16 v[118:121], v[166:169], a[56:59], v[118:121]
ds_read_b64_tr_b16 a[82:83], v9 offset:49792
global_load_lds_dwordx4 v3, s[40:41] offset:-1024
v_mfma_f32_16x16x32_bf16 v[134:137], v[106:109], v[166:169], v[134:137]
ds_read_b128 v[182:185], v51 offset:6144
v_mfma_f32_16x16x32_bf16 v[122:125], a[48:51], v[174:177], v[122:125]
global_load_lds_dwordx4 v4, s[40:41] offset:0
ds_read_b128 v[190:193], v51 offset:24576
v_mfma_f32_16x16x32_bf16 v[126:129], a[52:55], v[174:177], v[126:129]
ds_read_b64_tr_b16 a[84:85], v255 offset:0
global_load_lds_dwordx4 v5, s[40:41] offset:1024
v_mfma_f32_16x16x32_bf16 v[130:133], v[174:177], a[56:59], v[130:133]
v_mfma_f32_16x16x32_bf16 v[158:161], v[106:109], v[174:177], v[158:161]
ds_read_b64_tr_b16 a[86:87], v255 offset:2304
global_load_lds_dwordx4 v6, s[40:41] offset:2048
v_mfma_f32_16x16x32_bf16 v[86:89], a[60:63], v[106:109], v[86:89]
ds_read_b64_tr_b16 a[88:89], v255 offset:192
v_mfma_f32_16x16x32_bf16 v[90:93], a[64:67], v[106:109], v[90:93]
global_load_lds_dwordx4 v7, s[40:41] offset:3072
ds_read_b64_tr_b16 a[90:91], v255 offset:2496
v_mfma_f32_16x16x32_bf16 v[94:97], v[106:109], a[68:71], v[94:97]
s_add_u32 m0, s48, 86016
ds_read_b64_tr_b16 a[92:93], v255 offset:384
v_mfma_f32_16x16x32_bf16 v[110:113], a[60:63], v[170:173], v[110:113]
ds_read_b64_tr_b16 a[94:95], v255 offset:2688
v_mfma_f32_16x16x32_bf16 v[114:117], a[64:67], v[170:173], v[114:117]
global_load_lds_dwordx4 v8, s[40:41] offset:-4096
ds_read_b128 v[186:189], v52 offset:6144
v_mfma_f32_16x16x32_bf16 v[118:121], v[170:173], a[68:71], v[118:121]
s_add_u32 s40, s40, 221184
ds_read_b128 v[194:197], v52 offset:24576
v_mfma_f32_16x16x32_bf16 v[134:137], v[106:109], v[170:173], v[134:137]
v_mfma_f32_16x16x32_bf16 v[122:125], a[60:63], v[178:181], v[122:125]
v_mfma_f32_16x16x32_bf16 v[126:129], a[64:67], v[178:181], v[126:129]
s_addc_u32 s41, s41, 0
v_mfma_f32_16x16x32_bf16 v[130:133], v[178:181], a[68:71], v[130:133]
v_mfma_f32_16x16x32_bf16 v[158:161], v[106:109], v[178:181], v[158:161]
s_waitcnt vmcnt(9) lgkmcnt(0)
s_barrier
s_add_u32 m0, s48, 4096
ds_read_b64_tr_b16 a[96:97], v255 offset:18432
v_mfma_f32_16x16x32_bf16 v[86:89], a[72:75], v[106:109], v[86:89]
v_mfma_f32_16x16x32_bf16 v[90:93], a[76:79], v[106:109], v[90:93]
global_load_lds_dwordx4 v0, s[40:41] offset:-4096
ds_read_b64_tr_b16 a[98:99], v255 offset:20736
v_mfma_f32_16x16x32_bf16 v[94:97], v[106:109], a[80:83], v[94:97]
ds_read_b64_tr_b16 a[100:101], v255 offset:18624
global_load_lds_dwordx4 v1, s[40:41] offset:-3072
v_mfma_f32_16x16x32_bf16 v[110:113], a[72:75], v[182:185], v[110:113]
ds_read_b64_tr_b16 a[102:103], v255 offset:20928
v_mfma_f32_16x16x32_bf16 v[114:117], a[76:79], v[182:185], v[114:117]
global_load_lds_dwordx4 v2, s[40:41] offset:-2048
ds_read_b64_tr_b16 a[104:105], v255 offset:18816
v_mfma_f32_16x16x32_bf16 v[118:121], v[182:185], a[80:83], v[118:121]
ds_read_b64_tr_b16 a[106:107], v255 offset:21120
global_load_lds_dwordx4 v3, s[40:41] offset:-1024
v_mfma_f32_16x16x32_bf16 v[134:137], v[106:109], v[182:185], v[134:137]
ds_read_b128 v[166:169], v51 offset:8192
v_mfma_f32_16x16x32_bf16 v[122:125], a[72:75], v[190:193], v[122:125]
global_load_lds_dwordx4 v4, s[40:41] offset:0
ds_read_b128 v[174:177], v51 offset:26624
v_mfma_f32_16x16x32_bf16 v[126:129], a[76:79], v[190:193], v[126:129]
ds_read_b64_tr_b16 a[108:109], v255 offset:36864
global_load_lds_dwordx4 v5, s[40:41] offset:1024
v_mfma_f32_16x16x32_bf16 v[130:133], v[190:193], a[80:83], v[130:133]
v_mfma_f32_16x16x32_bf16 v[158:161], v[106:109], v[190:193], v[158:161]
ds_read_b64_tr_b16 a[110:111], v255 offset:39168
global_load_lds_dwordx4 v6, s[40:41] offset:2048
v_mfma_f32_16x16x32_bf16 v[86:89], a[84:87], v[106:109], v[86:89]
ds_read_b64_tr_b16 a[112:113], v255 offset:37056
v_mfma_f32_16x16x32_bf16 v[90:93], a[88:91], v[106:109], v[90:93]
global_load_lds_dwordx4 v7, s[40:41] offset:3072
ds_read_b64_tr_b16 a[114:115], v255 offset:39360
v_mfma_f32_16x16x32_bf16 v[94:97], v[106:109], a[92:95], v[94:97]
s_add_u32 m0, s48, 12288
ds_read_b64_tr_b16 a[116:117], v255 offset:37248
v_mfma_f32_16x16x32_bf16 v[110:113], a[84:87], v[186:189], v[110:113]
ds_read_b64_tr_b16 a[118:119], v255 offset:39552
v_mfma_f32_16x16x32_bf16 v[114:117], a[88:91], v[186:189], v[114:117]
global_load_lds_dwordx4 v8, s[40:41] offset:-4096
ds_read_b128 v[170:173], v52 offset:8192
v_mfma_f32_16x16x32_bf16 v[118:121], v[186:189], a[92:95], v[118:121]
s_add_u32 s40, s40, 221184
ds_read_b128 v[178:181], v52 offset:26624
v_mfma_f32_16x16x32_bf16 v[134:137], v[106:109], v[186:189], v[134:137]
v_mfma_f32_16x16x32_bf16 v[122:125], a[84:87], v[194:197], v[122:125]
v_mfma_f32_16x16x32_bf16 v[126:129], a[88:91], v[194:197], v[126:129]
s_addc_u32 s41, s41, 0
v_mfma_f32_16x16x32_bf16 v[130:133], v[194:197], a[92:95], v[130:133]
v_mfma_f32_16x16x32_bf16 v[158:161], v[106:109], v[194:197], v[158:161]
s_waitcnt vmcnt(9) lgkmcnt(0)
s_barrier
s_add_u32 m0, s48, 40960
ds_read_b64_tr_b16 a[120:121], v255 offset:55296
v_mfma_f32_16x16x32_bf16 v[86:89], a[96:99], v[106:109], v[86:89]
v_mfma_f32_16x16x32_bf16 v[90:93], a[100:103], v[106:109], v[90:93]
global_load_lds_dwordx4 v0, s[40:41] offset:-4096
ds_read_b64_tr_b16 a[122:123], v255 offset:57600
v_mfma_f32_16x16x32_bf16 v[94:97], v[106:109], a[104:107], v[94:97]
ds_read_b64_tr_b16 a[124:125], v255 offset:55488
global_load_lds_dwordx4 v1, s[40:41] offset:-3072
v_mfma_f32_16x16x32_bf16 v[110:113], a[96:99], v[166:169], v[110:113]
ds_read_b64_tr_b16 a[126:127], v255 offset:57792
v_mfma_f32_16x16x32_bf16 v[114:117], a[100:103], v[166:169], v[114:117]
global_load_lds_dwordx4 v2, s[40:41] offset:-2048
ds_read_b64_tr_b16 a[128:129], v255 offset:55680
v_mfma_f32_16x16x32_bf16 v[118:121], v[166:169], a[104:107], v[118:121]
ds_read_b64_tr_b16 a[130:131], v255 offset:57984
global_load_lds_dwordx4 v3, s[40:41] offset:-1024
v_mfma_f32_16x16x32_bf16 v[134:137], v[106:109], v[166:169], v[134:137]
ds_read_b128 v[182:185], v51 offset:10240
v_mfma_f32_16x16x32_bf16 v[122:125], a[96:99], v[174:177], v[122:125]
global_load_lds_dwordx4 v4, s[40:41] offset:0
ds_read_b128 v[190:193], v51 offset:28672
v_mfma_f32_16x16x32_bf16 v[126:129], a[100:103], v[174:177], v[126:129]
ds_read_b64_tr_b16 a[132:133], v162 offset:8192
global_load_lds_dwordx4 v5, s[40:41] offset:1024
v_mfma_f32_16x16x32_bf16 v[130:133], v[174:177], a[104:107], v[130:133]
v_mfma_f32_16x16x32_bf16 v[158:161], v[106:109], v[174:177], v[158:161]
ds_read_b64_tr_b16 a[134:135], v162 offset:10496
global_load_lds_dwordx4 v6, s[40:41] offset:2048
v_mfma_f32_16x16x32_bf16 v[86:89], a[108:111], v[106:109], v[86:89]
ds_read_b64_tr_b16 a[136:137], v162 offset:8384
v_mfma_f32_16x16x32_bf16 v[90:93], a[112:115], v[106:109], v[90:93]
global_load_lds_dwordx4 v7, s[40:41] offset:3072
ds_read_b64_tr_b16 a[138:139], v162 offset:10688
v_mfma_f32_16x16x32_bf16 v[94:97], v[106:109], a[116:119], v[94:97]
s_add_u32 m0, s48, 49152
ds_read_b64_tr_b16 a[140:141], v162 offset:8576
v_mfma_f32_16x16x32_bf16 v[110:113], a[108:111], v[170:173], v[110:113]
ds_read_b64_tr_b16 a[142:143], v162 offset:10880
v_mfma_f32_16x16x32_bf16 v[114:117], a[112:115], v[170:173], v[114:117]
global_load_lds_dwordx4 v8, s[40:41] offset:-4096
ds_read_b128 v[186:189], v52 offset:10240
v_mfma_f32_16x16x32_bf16 v[118:121], v[170:173], a[116:119], v[118:121]
s_add_u32 s40, s40, 221184
ds_read_b128 v[194:197], v52 offset:28672
v_mfma_f32_16x16x32_bf16 v[134:137], v[106:109], v[170:173], v[134:137]
v_mfma_f32_16x16x32_bf16 v[122:125], a[108:111], v[178:181], v[122:125]
v_mfma_f32_16x16x32_bf16 v[126:129], a[112:115], v[178:181], v[126:129]
s_addc_u32 s41, s41, 0
v_mfma_f32_16x16x32_bf16 v[130:133], v[178:181], a[116:119], v[130:133]
v_mfma_f32_16x16x32_bf16 v[158:161], v[106:109], v[178:181], v[158:161]
s_waitcnt vmcnt(9) lgkmcnt(0)
s_barrier
s_add_u32 m0, s48, 77824
ds_read_b64_tr_b16 a[144:145], v9 offset:47104
v_mfma_f32_16x16x32_bf16 v[86:89], a[120:123], v[106:109], v[86:89]
v_mfma_f32_16x16x32_bf16 v[90:93], a[124:127], v[106:109], v[90:93]
global_load_lds_dwordx4 v0, s[40:41] offset:-4096
ds_read_b64_tr_b16 a[146:147], v9 offset:49408
v_mfma_f32_16x16x32_bf16 v[94:97], v[106:109], a[128:131], v[94:97]
ds_read_b64_tr_b16 a[148:149], v9 offset:47296
global_load_lds_dwordx4 v1, s[40:41] offset:-3072
v_mfma_f32_16x16x32_bf16 v[110:113], a[120:123], v[182:185], v[110:113]
ds_read_b64_tr_b16 a[150:151], v9 offset:49600
v_mfma_f32_16x16x32_bf16 v[114:117], a[124:127], v[182:185], v[114:117]
global_load_lds_dwordx4 v2, s[40:41] offset:-2048
ds_read_b64_tr_b16 a[152:153], v9 offset:47488
v_mfma_f32_16x16x32_bf16 v[118:121], v[182:185], a[128:131], v[118:121]
ds_read_b64_tr_b16 a[154:155], v9 offset:49792
global_load_lds_dwordx4 v3, s[40:41] offset:-1024
v_mfma_f32_16x16x32_bf16 v[134:137], v[106:109], v[182:185], v[134:137]
ds_read_b128 v[166:169], v51 offset:12288
v_mfma_f32_16x16x32_bf16 v[122:125], a[120:123], v[190:193], v[122:125]
global_load_lds_dwordx4 v4, s[40:41] offset:0
ds_read_b128 v[174:177], v51 offset:30720
v_mfma_f32_16x16x32_bf16 v[126:129], a[124:127], v[190:193], v[126:129]
ds_read_b64_tr_b16 a[156:157], v255 offset:0
global_load_lds_dwordx4 v5, s[40:41] offset:1024
v_mfma_f32_16x16x32_bf16 v[130:133], v[190:193], a[128:131], v[130:133]
v_mfma_f32_16x16x32_bf16 v[158:161], v[106:109], v[190:193], v[158:161]
ds_read_b64_tr_b16 a[158:159], v255 offset:2304
global_load_lds_dwordx4 v6, s[40:41] offset:2048
v_mfma_f32_16x16x32_bf16 v[86:89], a[132:135], v[106:109], v[86:89]
ds_read_b64_tr_b16 a[160:161], v255 offset:192
v_mfma_f32_16x16x32_bf16 v[90:93], a[136:139], v[106:109], v[90:93]
global_load_lds_dwordx4 v7, s[40:41] offset:3072
ds_read_b64_tr_b16 a[162:163], v255 offset:2496
v_mfma_f32_16x16x32_bf16 v[94:97], v[106:109], a[140:143], v[94:97]
s_add_u32 m0, s48, 86016
ds_read_b64_tr_b16 a[164:165], v255 offset:384
v_mfma_f32_16x16x32_bf16 v[110:113], a[132:135], v[186:189], v[110:113]
ds_read_b64_tr_b16 a[166:167], v255 offset:2688
v_mfma_f32_16x16x32_bf16 v[114:117], a[136:139], v[186:189], v[114:117]
global_load_lds_dwordx4 v8, s[40:41] offset:-4096
ds_read_b128 v[170:173], v52 offset:12288
v_mfma_f32_16x16x32_bf16 v[118:121], v[186:189], a[140:143], v[118:121]
s_sub_u32 s40, s40, 1769472
ds_read_b128 v[178:181], v52 offset:30720
v_mfma_f32_16x16x32_bf16 v[134:137], v[106:109], v[186:189], v[134:137]
v_mfma_f32_16x16x32_bf16 v[122:125], a[132:135], v[194:197], v[122:125]
v_mfma_f32_16x16x32_bf16 v[126:129], a[136:139], v[194:197], v[126:129]
s_subb_u32 s41, s41, 0
v_mfma_f32_16x16x32_bf16 v[130:133], v[194:197], a[140:143], v[130:133]
v_mfma_f32_16x16x32_bf16 v[158:161], v[106:109], v[194:197], v[158:161]
s_waitcnt vmcnt(9) lgkmcnt(0)
s_barrier
s_add_u32 m0, s49, 0
ds_read_b64_tr_b16 a[168:169], v255 offset:18432
v_mfma_f32_16x16x32_bf16 v[86:89], a[144:147], v[106:109], v[86:89]
v_mfma_f32_16x16x32_bf16 v[90:93], a[148:151], v[106:109], v[90:93]
ds_read_b64_tr_b16 a[170:171], v255 offset:20736
v_mfma_f32_16x16x32_bf16 v[94:97], v[106:109], a[152:155], v[94:97]
ds_read_b64_tr_b16 a[172:173], v255 offset:18624
v_mfma_f32_16x16x32_bf16 v[110:113], a[144:147], v[166:169], v[110:113]
global_load_lds_dwordx4 v11, s[42:43] offset:-2048 sc1
ds_read_b64_tr_b16 a[174:175], v255 offset:20928
v_mfma_f32_16x16x32_bf16 v[114:117], a[148:151], v[166:169], v[114:117]
ds_read_b64_tr_b16 a[176:177], v255 offset:18816
v_mfma_f32_16x16x32_bf16 v[118:121], v[166:169], a[152:155], v[118:121]
ds_read_b64_tr_b16 a[178:179], v255 offset:21120
v_mfma_f32_16x16x32_bf16 v[134:137], v[106:109], v[166:169], v[134:137]
global_load_lds_dwordx4 v12, s[42:43] offset:-1024 sc1
ds_read_b128 v[182:185], v51 offset:14336
v_mfma_f32_16x16x32_bf16 v[122:125], a[144:147], v[174:177], v[122:125]
ds_read_b128 v[190:193], v51 offset:32768
v_mfma_f32_16x16x32_bf16 v[126:129], a[148:151], v[174:177], v[126:129]
ds_read_b64_tr_b16 a[180:181], v255 offset:36864
v_mfma_f32_16x16x32_bf16 v[130:133], v[174:177], a[152:155], v[130:133]
global_load_lds_dwordx4 v13, s[42:43] offset:0 sc1
v_mfma_f32_16x16x32_bf16 v[158:161], v[106:109], v[174:177], v[158:161]
ds_read_b64_tr_b16 a[182:183], v255 offset:39168
v_mfma_f32_16x16x32_bf16 v[86:89], a[156:159], v[106:109], v[86:89]
ds_read_b64_tr_b16 a[184:185], v255 offset:37056
v_mfma_f32_16x16x32_bf16 v[90:93], a[160:163], v[106:109], v[90:93]
ds_read_b64_tr_b16 a[186:187], v255 offset:39360
v_mfma_f32_16x16x32_bf16 v[94:97], v[106:109], a[164:167], v[94:97]
global_load_lds_dwordx4 v14, s[42:43] offset:1024 sc1
ds_read_b64_tr_b16 a[188:189], v255 offset:37248
v_mfma_f32_16x16x32_bf16 v[110:113], a[156:159], v[170:173], v[110:113]
ds_read_b64_tr_b16 a[190:191], v255 offset:39552
v_mfma_f32_16x16x32_bf16 v[114:117], a[160:163], v[170:173], v[114:117]
ds_read_b128 v[186:189], v52 offset:14336
v_mfma_f32_16x16x32_bf16 v[118:121], v[170:173], a[164:167], v[118:121]
s_sub_u32 s42, s42, 18432
s_subb_u32 s43, s43, 0
ds_read_b128 v[194:197], v52 offset:32768
v_mfma_f32_16x16x32_bf16 v[134:137], v[106:109], v[170:173], v[134:137]
v_mfma_f32_16x16x32_bf16 v[122:125], a[156:159], v[178:181], v[122:125]
v_mfma_f32_16x16x32_bf16 v[126:129], a[160:163], v[178:181], v[126:129]
v_mfma_f32_16x16x32_bf16 v[130:133], v[178:181], a[164:167], v[130:133]
v_mfma_f32_16x16x32_bf16 v[158:161], v[106:109], v[178:181], v[158:161]
s_waitcnt vmcnt(4) lgkmcnt(0)
s_barrier
s_add_u32 m0, s49, 18432
ds_read_b64_tr_b16 a[192:193], v255 offset:55296
v_mfma_f32_16x16x32_bf16 v[86:89], a[168:171], v[106:109], v[86:89]
v_mfma_f32_16x16x32_bf16 v[90:93], a[172:175], v[106:109], v[90:93]
ds_read_b64_tr_b16 a[194:195], v255 offset:57600
v_mfma_f32_16x16x32_bf16 v[94:97], v[106:109], a[176:179], v[94:97]
ds_read_b64_tr_b16 a[196:197], v255 offset:55488
v_mfma_f32_16x16x32_bf16 v[110:113], a[168:171], v[182:185], v[110:113]
global_load_lds_dwordx4 v11, s[42:43] offset:-2048 sc1
ds_read_b64_tr_b16 a[198:199], v255 offset:57792
v_mfma_f32_16x16x32_bf16 v[114:117], a[172:175], v[182:185], v[114:117]
ds_read_b64_tr_b16 a[200:201], v255 offset:55680
v_mfma_f32_16x16x32_bf16 v[118:121], v[182:185], a[176:179], v[118:121]
ds_read_b64_tr_b16 a[202:203], v255 offset:57984
v_mfma_f32_16x16x32_bf16 v[134:137], v[106:109], v[182:185], v[134:137]
global_load_lds_dwordx4 v12, s[42:43] offset:-1024 sc1
ds_read_b128 v[166:169], v51 offset:16384
v_mfma_f32_16x16x32_bf16 v[122:125], a[168:171], v[190:193], v[122:125]
ds_read_b128 v[174:177], v51 offset:34816
v_mfma_f32_16x16x32_bf16 v[126:129], a[172:175], v[190:193], v[126:129]
ds_read_b64_tr_b16 a[204:205], v162 offset:8192
v_mfma_f32_16x16x32_bf16 v[130:133], v[190:193], a[176:179], v[130:133]
global_load_lds_dwordx4 v13, s[42:43] offset:0 sc1
ds_read_b64_tr_b16 a[206:207], v162 offset:10496
v_mfma_f32_16x16x32_bf16 v[158:161], v[106:109], v[190:193], v[158:161]
ds_read_b64_tr_b16 a[208:209], v162 offset:8384
v_mfma_f32_16x16x32_bf16 v[86:89], a[180:183], v[106:109], v[86:89]
ds_read_b64_tr_b16 a[210:211], v162 offset:10688
v_mfma_f32_16x16x32_bf16 v[90:93], a[184:187], v[106:109], v[90:93]
global_load_lds_dwordx4 v14, s[42:43] offset:1024 sc1
ds_read_b64_tr_b16 a[212:213], v162 offset:8576
v_mfma_f32_16x16x32_bf16 v[94:97], v[106:109], a[188:191], v[94:97]
ds_read_b64_tr_b16 a[214:215], v162 offset:10880
v_mfma_f32_16x16x32_bf16 v[110:113], a[180:183], v[186:189], v[110:113]
ds_read_b128 v[170:173], v52 offset:16384
v_mfma_f32_16x16x32_bf16 v[114:117], a[184:187], v[186:189], v[114:117]
s_sub_u32 s42, s42, 18432
s_subb_u32 s43, s43, 0
ds_read_b128 v[178:181], v52 offset:34816
v_mfma_f32_16x16x32_bf16 v[118:121], v[186:189], a[188:191], v[118:121]
v_mfma_f32_16x16x32_bf16 v[134:137], v[106:109], v[186:189], v[134:137]
v_mfma_f32_16x16x32_bf16 v[122:125], a[180:183], v[194:197], v[122:125]
v_mfma_f32_16x16x32_bf16 v[126:129], a[184:187], v[194:197], v[126:129]
v_mfma_f32_16x16x32_bf16 v[158:161], v[106:109], v[194:197], v[158:161]
s_waitcnt lgkmcnt(14)
v_mfma_f32_16x16x32_bf16 v[86:89], a[192:195], v[106:109], v[86:89]
s_waitcnt lgkmcnt(12)
v_mfma_f32_16x16x32_bf16 v[90:93], a[196:199], v[106:109], v[90:93]
s_waitcnt lgkmcnt(10)
v_mfma_f32_16x16x32_bf16 v[94:97], v[106:109], a[200:203], v[94:97]
s_waitcnt lgkmcnt(9)
v_mfma_f32_16x16x32_bf16 v[110:113], a[192:195], v[166:169], v[110:113]
v_mfma_f32_16x16x32_bf16 v[114:117], a[196:199], v[166:169], v[114:117]
v_mfma_f32_16x16x32_bf16 v[118:121], v[166:169], a[200:203], v[118:121]
v_mfma_f32_16x16x32_bf16 v[134:137], v[106:109], v[166:169], v[134:137]
s_waitcnt lgkmcnt(8)
v_mfma_f32_16x16x32_bf16 v[158:161], v[106:109], v[174:177], v[158:161]
s_waitcnt lgkmcnt(6)
v_mfma_f32_16x16x32_bf16 v[86:89], a[204:207], v[106:109], v[86:89]
s_waitcnt lgkmcnt(4)
v_mfma_f32_16x16x32_bf16 v[90:93], a[208:211], v[106:109], v[90:93]
s_waitcnt lgkmcnt(2)
v_mfma_f32_16x16x32_bf16 v[94:97], v[106:109], a[212:215], v[94:97]
s_waitcnt lgkmcnt(1)
v_mfma_f32_16x16x32_bf16 v[110:113], a[204:207], v[170:173], v[110:113]
v_mfma_f32_16x16x32_bf16 v[114:117], a[208:211], v[170:173], v[114:117]
v_mfma_f32_16x16x32_bf16 v[118:121], v[170:173], a[212:215], v[118:121]
v_mfma_f32_16x16x32_bf16 v[134:137], v[106:109], v[170:173], v[134:137]
s_waitcnt lgkmcnt(0)
v_mfma_f32_16x16x32_bf16 v[158:161], v[106:109], v[178:181], v[158:161]
s_nop 5
v_mul_f32 v202, 0x3ae38e39, v134
v_cvt_pk_bf16_f32 v216, -v202, -v202
v_lshlrev_b32 v216, 16, v216
ds_write_b32 v24, v216 offset:0
s_waitcnt lgkmcnt(0)
s_barrier
ds_read_b32 v216, v26
s_waitcnt lgkmcnt(0)
v_mul_f32 v216, 0x3ae38e39, v216
v_fma_f32 v203, -v202, v202, v216
v_max_f32 v203, 0, v203
v_cvt_pk_bf16_f32 v203, v203, v203
)"
R"(v_lshlrev_b32 v203, 16, v203
v_add_f32 v203, 0x3727c5ac, v203
v_cvt_pk_bf16_f32 v203, v203, v203
v_lshlrev_b32 v203, 16, v203
v_sqrt_f32 v203, v203
s_nop 0
v_cvt_pk_bf16_f32 v203, v203, v203
v_lshlrev_b32 v203, 16, v203
v_rcp_f32 v216, v203
s_nop 0
ds_write_b32 v24, v216 offset:128
ds_read_b128 v[170:173], v51 offset:32768
ds_read_b128 v[174:177], v52 offset:32768
ds_read_b128 v[178:181], v51 offset:34816
ds_read_b128 v[182:185], v52 offset:34816
v_mov_b32 v206, v32
v_mov_b32 v207, v33
v_mov_b32 v208, v34
v_mov_b32 v209, v35
v_mov_b32 v134, v158
v_mov_b32 v135, v159
v_mov_b32 v136, v160
v_mov_b32 v137, v161
s_waitcnt lgkmcnt(4)
s_barrier
ds_read_b32 v23, v24 offset:0
ds_read_b32 v148, v21
ds_read_b32 v95, v24 offset:192
ds_read_b32 v254, v27
ds_read_b32 v255, v28
ds_read_b128 v[198:201], v25 offset:0
ds_read_b64 v[158:159], v22 offset:41472
ds_read_b32 v140, v20
v_xor_b32 v51, 0x10000, v51
v_xor_b32 v52, 0x10000, v52
v_mul_f32 v202, 0x3ae38e39, v134
v_cvt_pk_bf16_f32 v216, -v202, -v202
v_lshlrev_b32 v216, 16, v216
ds_write_b32 v24, v216 offset:64
s_waitcnt lgkmcnt(8)
v_fma_f32 v210, v90, v23, v114
v_fma_f32 v211, v91, v23, v115
v_fma_f32 v212, v92, v23, v116
v_fma_f32 v213, v93, v23, v117
v_cvt_pk_bf16_f32 v146, v210, v211
v_cvt_pk_bf16_f32 v147, v212, v213
s_waitcnt lgkmcnt(0)
s_barrier
ds_read_b32 v216, v26
v_fma_f32 v210, v94, v198, v118
v_fma_f32 v211, v94, v199, v119
v_fma_f32 v212, v94, v200, v120
v_fma_f32 v213, v94, v201, v121
v_cvt_pk_bf16_f32 v154, v210, v211
v_cvt_pk_bf16_f32 v155, v212, v213
v_fma_f32 v210, v86, v23, v110
v_fma_f32 v211, v87, v23, v111
v_fma_f32 v212, v88, v23, v112
v_fma_f32 v213, v89, v23, v113
v_cvt_pk_bf16_f32 v138, v210, v211
v_cvt_pk_bf16_f32 v139, v212, v213
s_waitcnt lgkmcnt(0)
v_mul_f32 v216, 0x3ae38e39, v216
v_fma_f32 v203, -v202, v202, v216
v_max_f32 v203, 0, v203
v_cvt_pk_bf16_f32 v203, v203, v203
v_lshlrev_b32 v203, 16, v203
v_add_f32 v203, 0x3727c5ac, v203
v_cvt_pk_bf16_f32 v203, v203, v203
v_lshlrev_b32 v203, 16, v203
v_sqrt_f32 v203, v203
s_nop 0
v_cvt_pk_bf16_f32 v203, v203, v203
v_lshlrev_b32 v203, 16, v203
v_rcp_f32 v216, v203
s_nop 0
ds_write_b32 v24, v216 offset:192
v_add_lshl_u32 v252, v48, 0, 7
v_bfi_b32 v252, s69, v252, v47
s_waitcnt vmcnt(4) lgkmcnt(0)
s_barrier
s_mov_b32 s70, 32
s_waitcnt lgkmcnt(0)
s_nop 1
s_branch .Lqa_in72
.p2align 6
.Lqa_top91:
s_waitcnt lgkmcnt(5)
v_mfma_f32_16x16x32_bf16 v[134:137], v[106:109], v[0:3], 0
s_waitcnt lgkmcnt(4)
ds_read_b128 v[58:61], v52 offset:4096
v_bfi_b32 v252, s69, v252, v47
v_mfma_f32_16x16x32_bf16 v[134:137], v[106:109], v[4:7], v[134:137]
ds_read_b32 v152, v21
ds_read_b32 v95, v24 offset:128
v_mfma_f32_16x16x32_bf16 v[110:113], a[0:3], v[0:3], 0
s_waitcnt lgkmcnt(5)
ds_read_b128 v[62:65], v51 offset:6144
ds_read_b32 v254, v29
v_mfma_f32_16x16x32_bf16 v[134:137], v[106:109], v[96:99], v[134:137]
s_waitcnt lgkmcnt(6)
ds_read_b128 v[66:69], v52 offset:6144
ds_read_b32 v255, v30
v_mfma_f32_16x16x32_bf16 v[134:137], v[106:109], v[100:103], v[134:137]
ds_read_b128 v[198:201], v25 offset:64
ds_read_b64 v[160:161], v22 offset:41472
v_mfma_f32_16x16x32_bf16 v[114:117], a[4:7], v[0:3], 0
s_waitcnt lgkmcnt(9)
ds_read_b128 v[70:73], v51 offset:8192
ds_read_b32 v144, v20
v_mfma_f32_16x16x32_bf16 v[134:137], v[106:109], v[54:57], v[134:137]
s_waitcnt lgkmcnt(10)
ds_read_b128 v[74:77], v52 offset:8192
v_fma_f32 v210, v90, v23, v126
v_mfma_f32_16x16x32_bf16 v[134:137], v[106:109], v[58:61], v[134:137]
v_fma_f32 v211, v91, v23, v127
v_fma_f32 v212, v92, v23, v128
v_mfma_f32_16x16x32_bf16 v[118:121], v[0:3], a[8:11], 0
s_waitcnt lgkmcnt(8)
ds_read_b128 v[78:81], v51 offset:10240
v_fma_f32 v213, v93, v23, v129
v_mfma_f32_16x16x32_bf16 v[134:137], v[106:109], v[62:65], v[134:137]
s_waitcnt lgkmcnt(7)
ds_read_b128 v[82:85], v52 offset:10240
v_cvt_pk_bf16_f32 v150, v210, v211
v_mfma_f32_16x16x32_bf16 v[134:137], v[106:109], v[66:69], v[134:137]
v_cvt_pk_bf16_f32 v151, v212, v213
v_max_f32 v10, v190, v191
v_mfma_f32_16x16x32_bf16 v[110:113], a[12:15], v[4:7], v[110:113]
ds_read_b128 v[162:165], v51 offset:12288
v_mfma_f32_16x16x32_bf16 v[186:189], v[150:153], v[138:141], v[32:35]
s_waitcnt lgkmcnt(7)
v_max_f32 v44, v192, v193
v_fma_f32 v210, v94, v198, v130
s_waitcnt lgkmcnt(5)
v_mfma_f32_16x16x32_bf16 v[134:137], v[106:109], v[70:73], v[134:137]
s_xor_b32 s49, s49, 0x10000
ds_read_b128 v[166:169], v52 offset:12288
s_waitcnt lgkmcnt(4)
v_mfma_f32_16x16x32_bf16 v[134:137], v[106:109], v[74:77], v[134:137]
s_add_u32 m0, s49, 0
v_mul_f32 v95, 0x3eae1a93, v95
v_mul_f32 v8, v254, v95
v_mfma_f32_16x16x32_bf16 v[114:117], a[16:19], v[4:7], v[114:117]
s_waitcnt lgkmcnt(3)
ds_read_b128 v[170:173], v51 offset:14336
v_mul_f32 v9, v255, v95
v_mfma_f32_16x16x32_bf16 v[134:137], v[106:109], v[78:81], v[134:137]
s_waitcnt lgkmcnt(3)
ds_read_b128 v[174:177], v52 offset:14336
v_max3_f32 v10, v186, v187, v10
v_mfma_f32_16x16x32_bf16 v[134:137], v[106:109], v[82:85], v[134:137]
v_max3_f32 v44, v188, v189, v44
v_mul_f32 v10, v10, v8
v_mfma_f32_16x16x32_bf16 v[118:121], v[4:7], a[20:23], v[118:121]
s_waitcnt lgkmcnt(3)
ds_read_b128 v[178:181], v51 offset:16384
v_mul_f32 v44, v44, v9
v_mfma_f32_16x16x32_bf16 v[134:137], v[106:109], v[162:165], v[134:137]
s_waitcnt lgkmcnt(3)
ds_read_b128 v[182:185], v52 offset:16384
v_max_f32 v45, v10, v44
v_mfma_f32_16x16x32_bf16 v[134:137], v[106:109], v[166:169], v[134:137]
v_mov_b32 v46, v45
v_fma_f32 v211, v94, v199, v131
v_mfma_f32_16x16x32_bf16 v[110:113], a[24:27], v[96:99], v[110:113]
s_waitcnt lgkmcnt(3)
global_load_lds_dwordx4 v11, s[42:43] offset:-2048 sc1
v_fma_f32 v212, v94, v200, v132
v_mfma_f32_16x16x32_bf16 v[134:137], v[106:109], v[170:173], v[134:137]
s_waitcnt lgkmcnt(2)
v_permlane16_swap_b32 v45, v46
v_mfma_f32_16x16x32_bf16 v[134:137], v[106:109], v[174:177], v[134:137]
v_max_f32 v45, v45, v46
v_mov_b32 v46, v45
v_mfma_f32_16x16x32_bf16 v[114:117], a[28:31], v[96:99], v[114:117]
s_waitcnt lgkmcnt(1)
v_permlane32_swap_b32 v45, v46
v_mfma_f32_16x16x32_bf16 v[134:137], v[106:109], v[178:181], v[134:137]
s_waitcnt lgkmcnt(0)
v_max_f32 v45, v45, v46
v_sub_f32 v10, v10, v45
v_mfma_f32_16x16x32_bf16 v[134:137], v[106:109], v[182:185], v[134:137]
v_sub_f32 v44, v44, v45
v_fma_f32 v213, v94, v201, v133
v_mfma_f32_16x16x32_bf16 v[118:121], v[96:99], a[32:35], v[118:121]
v_exp_f32 v214, v10
v_mfma_f32_16x16x32_bf16 v[110:113], a[36:39], v[100:103], v[110:113]
v_exp_f32 v215, v44
v_mfma_f32_16x16x32_bf16 v[114:117], a[40:43], v[100:103], v[114:117]
s_nop 0
v_mul_f32 v202, 0x3ae38e39, v134
v_cvt_pk_bf16_f32 v216, -v202, -v202
v_mfma_f32_16x16x32_bf16 v[118:121], v[100:103], a[44:47], v[118:121]
v_lshlrev_b32 v216, 16, v216
global_load_lds_dwordx4 v12, s[42:43] offset:-1024 sc1
v_mfma_f32_16x16x32_bf16 v[110:113], a[48:51], v[54:57], v[110:113]
ds_write_b32 v24, v216 offset:0
v_mfma_f32_16x16x32_bf16 v[114:117], a[52:55], v[54:57], v[114:117]
v_add_f32 v45, v214, v215
v_mov_b32 v46, v45
v_mfma_f32_16x16x32_bf16 v[118:121], v[54:57], a[56:59], v[118:121]
v_mul_f32 v214, v214, v254
v_cvt_pk_bf16_f32 v156, v210, v211
v_mfma_f32_16x16x32_bf16 v[110:113], a[60:63], v[58:61], v[110:113]
v_permlane16_swap_b32 v45, v46
v_mfma_f32_16x16x32_bf16 v[114:117], a[64:67], v[58:61], v[114:117]
v_add_f32 v45, v45, v46
v_mov_b32 v46, v45
v_mfma_f32_16x16x32_bf16 v[118:121], v[58:61], a[68:71], v[118:121]
v_mfma_f32_16x16x32_bf16 v[110:113], a[72:75], v[62:65], v[110:113]
s_waitcnt lgkmcnt(0)
s_barrier
v_mfma_f32_16x16x32_bf16 v[114:117], a[76:79], v[62:65], v[114:117]
global_load_lds_dwordx4 v13, s[42:43] offset:0 sc1
ds_read_b32 v216, v26
v_mfma_f32_16x16x32_bf16 v[118:121], v[62:65], a[80:83], v[118:121]
v_mul_f32 v215, v215, v255
v_cvt_pk_bf16_f32 v157, v212, v213
v_mfma_f32_16x16x32_bf16 v[110:113], a[84:87], v[66:69], v[110:113]
v_permlane32_swap_b32 v45, v46
v_mfma_f32_16x16x32_bf16 v[114:117], a[88:91], v[66:69], v[114:117]
v_add_f32 v45, v45, v46
v_fma_f32 v210, v86, v23, v122
v_mfma_f32_16x16x32_bf16 v[118:121], v[66:69], a[92:95], v[118:121]
v_rcp_f32 v45, v45
v_mfma_f32_16x16x32_bf16 v[110:113], a[96:99], v[70:73], v[110:113]
v_mul_f32 v214, v214, v45
v_mul_f32 v215, v215, v45
v_mfma_f32_16x16x32_bf16 v[114:117], a[100:103], v[70:73], v[114:117]
v_cvt_pk_bf16_f32 v214, v214, v214
v_cvt_pk_bf16_f32 v215, v215, v215
v_mfma_f32_16x16x32_bf16 v[118:121], v[70:73], a[104:107], v[118:121]
v_and_b32 v196, v214, v40
v_and_b32 v197, v215, v41
v_mfma_f32_16x16x32_bf16 v[110:113], a[108:111], v[74:77], v[110:113]
global_load_lds_dwordx4 v14, s[42:43] offset:1024 sc1
s_add_u32 s42, s42, s46
s_addc_u32 s43, s43, s47
v_mfma_f32_16x16x32_bf16 v[114:117], a[112:115], v[74:77], v[114:117]
v_and_b32 v194, v214, v42
v_and_b32 v195, v215, v43
v_mfma_f32_16x16x32_bf16 v[118:121], v[74:77], a[116:119], v[118:121]
s_nop 0
v_mfma_f32_16x16x32_bf16 v[190:193], v[154:157], v[194:197], 0
v_mfma_f32_16x16x32_bf16 v[186:189], v[158:161], v[194:197], 0
s_waitcnt lgkmcnt(0)
v_mul_f32 v216, 0x3ae38e39, v216
v_fma_f32 v203, -v202, v202, v216
v_mfma_f32_16x16x32_bf16 v[110:113], a[120:123], v[78:81], v[110:113]
v_max_f32 v203, 0, v203
v_cvt_pk_bf16_f32 v203, v203, v203
v_mfma_f32_16x16x32_bf16 v[114:117], a[124:127], v[78:81], v[114:117]
v_lshlrev_b32 v203, 16, v203
v_add_f32 v203, 0x3727c5ac, v203
v_mfma_f32_16x16x32_bf16 v[118:121], v[78:81], a[128:131], v[118:121]
v_cvt_pk_bf16_f32 v203, v203, v203
v_lshlrev_b32 v203, 16, v203
v_mfma_f32_16x16x32_bf16 v[110:113], a[132:135], v[82:85], v[110:113]
v_cvt_pk_bf16_f32 v104, v190, v191
v_cvt_pk_bf16_f32 v105, v192, v193
v_mfma_f32_16x16x32_bf16 v[114:117], a[136:139], v[82:85], v[114:117]
ds_write_b64 v18, v[104:105]
v_mfma_f32_16x16x32_bf16 v[118:121], v[82:85], a[140:143], v[118:121]
v_cvt_pk_bf16_f32 v214, v188, v189
v_fma_f32 v211, v87, v23, v123
v_mfma_f32_16x16x32_bf16 v[110:113], a[144:147], v[162:165], v[110:113]
ds_write_b32 v19, v214
v_mfma_f32_16x16x32_bf16 v[114:117], a[148:151], v[162:165], v[114:117]
v_sqrt_f32 v203, v203
v_mfma_f32_16x16x32_bf16 v[118:121], v[162:165], a[152:155], v[118:121]
v_cvt_pk_bf16_f32 v203, v203, v203
v_lshlrev_b32 v203, 16, v203
v_mfma_f32_16x16x32_bf16 v[110:113], a[156:159], v[166:169], v[110:113]
v_rcp_f32 v216, v203
v_mfma_f32_16x16x32_bf16 v[114:117], a[160:163], v[166:169], v[114:117]
ds_write_b32 v24, v216 offset:128
v_mfma_f32_16x16x32_bf16 v[118:121], v[166:169], a[164:167], v[118:121]
v_fma_f32 v212, v88, v23, v124
v_fma_f32 v213, v89, v23, v125
v_mfma_f32_16x16x32_bf16 v[110:113], a[168:171], v[170:173], v[110:113]
v_cvt_pk_bf16_f32 v142, v210, v211
v_cvt_pk_bf16_f32 v143, v212, v213
v_mfma_f32_16x16x32_bf16 v[114:117], a[172:175], v[170:173], v[114:117]
v_mfma_f32_16x16x32_bf16 v[118:121], v[170:173], a[176:179], v[118:121]
v_mfma_f32_16x16x32_bf16 v[110:113], a[180:183], v[174:177], v[110:113]
v_mfma_f32_16x16x32_bf16 v[114:117], a[184:187], v[174:177], v[114:117]
s_waitcnt vmcnt(5) lgkmcnt(0)
s_barrier
v_mfma_f32_16x16x32_bf16 v[118:121], v[174:177], a[188:191], v[118:121]
ds_read_b128 v[0:3], v51 offset:18432
v_mfma_f32_16x16x32_bf16 v[110:113], a[192:195], v[178:181], v[110:113]
ds_read_b128 v[4:7], v52 offset:18432
ds_read_b32 v23, v24 offset:0
v_mfma_f32_16x16x32_bf16 v[114:117], a[196:199], v[178:181], v[114:117]
s_mov_b64 exec, 1
global_store_dword v252, v48, s[38:39] sc1
s_mov_b64 exec, -1
v_mfma_f32_16x16x32_bf16 v[190:193], v[150:153], v[142:145], v[36:39]
v_mfma_f32_16x16x32_bf16 v[118:121], v[178:181], a[200:203], v[118:121]
ds_read_b128 v[96:99], v51 offset:20480
v_add_u32 v48, 1, v48
v_mfma_f32_16x16x32_bf16 v[110:113], a[204:207], v[182:185], v[110:113]
ds_read_b128 v[100:103], v52 offset:20480
v_add_lshl_u32 v252, v48, 0, 7
v_mfma_f32_16x16x32_bf16 v[114:117], a[208:211], v[182:185], v[114:117]
v_bfi_b32 v252, s69, v252, v47
ds_read_b32 v148, v21
v_mfma_f32_16x16x32_bf16 v[118:121], v[182:185], a[212:215], v[118:121]
s_waitcnt lgkmcnt(5)
ds_read_b128 v[54:57], v51 offset:22528
ds_read_b32 v95, v24 offset:192
v_mfma_f32_16x16x32_bf16 v[134:137], v[106:109], v[0:3], 0
s_waitcnt lgkmcnt(6)
ds_read_b128 v[58:61], v52 offset:22528
ds_read_b32 v254, v27
v_mfma_f32_16x16x32_bf16 v[134:137], v[106:109], v[4:7], v[134:137]
ds_read_b32 v255, v28
ds_read_b128 v[198:201], v25 offset:0
v_mfma_f32_16x16x32_bf16 v[122:125], a[0:3], v[0:3], 0
s_waitcnt lgkmcnt(8)
ds_read_b128 v[62:65], v51 offset:24576
ds_read_b64 v[158:159], v22 offset:41472
v_mfma_f32_16x16x32_bf16 v[134:137], v[106:109], v[96:99], v[134:137]
s_waitcnt lgkmcnt(9)
ds_read_b128 v[66:69], v52 offset:24576
ds_read_b32 v140, v20
v_mfma_f32_16x16x32_bf16 v[134:137], v[106:109], v[100:103], v[134:137]
v_fma_f32 v210, v90, v23, v114
v_fma_f32 v211, v91, v23, v115
v_mfma_f32_16x16x32_bf16 v[126:129], a[4:7], v[0:3], 0
s_waitcnt lgkmcnt(9)
ds_read_b128 v[70:73], v51 offset:26624
v_fma_f32 v212, v92, v23, v116
v_mfma_f32_16x16x32_bf16 v[134:137], v[106:109], v[54:57], v[134:137]
s_waitcnt lgkmcnt(8)
ds_read_b128 v[74:77], v52 offset:26624
v_fma_f32 v213, v93, v23, v117
v_mfma_f32_16x16x32_bf16 v[134:137], v[106:109], v[58:61], v[134:137]
v_cvt_pk_bf16_f32 v146, v210, v211
v_cvt_pk_bf16_f32 v147, v212, v213
v_mfma_f32_16x16x32_bf16 v[130:133], v[0:3], a[8:11], 0
ds_read_b128 v[78:81], v51 offset:28672
v_mfma_f32_16x16x32_bf16 v[186:189], v[146:149], v[142:145], v[206:209]
s_waitcnt lgkmcnt(6)
v_max_f32 v10, v190, v191
v_max_f32 v44, v192, v193
v_mfma_f32_16x16x32_bf16 v[134:137], v[106:109], v[62:65], v[134:137]
s_waitcnt lgkmcnt(4)
ds_read_b128 v[82:85], v52 offset:28672
v_fma_f32 v210, v94, v198, v118
v_mfma_f32_16x16x32_bf16 v[134:137], v[106:109], v[66:69], v[134:137]
v_mul_f32 v95, 0x3eae1a93, v95
v_mul_f32 v8, v254, v95
v_mfma_f32_16x16x32_bf16 v[122:125], a[12:15], v[4:7], v[122:125]
s_waitcnt lgkmcnt(3)
ds_read_b128 v[162:165], v51 offset:30720
v_mul_f32 v9, v255, v95
v_mfma_f32_16x16x32_bf16 v[134:137], v[106:109], v[70:73], v[134:137]
s_add_u32 m0, s49, 18432
ds_read_b128 v[166:169], v52 offset:30720
s_waitcnt lgkmcnt(4)
v_mfma_f32_16x16x32_bf16 v[134:137], v[106:109], v[74:77], v[134:137]
v_max3_f32 v10, v186, v187, v10
v_max3_f32 v44, v188, v189, v44
v_mfma_f32_16x16x32_bf16 v[126:129], a[16:19], v[4:7], v[126:129]
s_waitcnt lgkmcnt(3)
ds_read_b128 v[170:173], v51 offset:32768
v_mul_f32 v10, v10, v8
v_mfma_f32_16x16x32_bf16 v[134:137], v[106:109], v[78:81], v[134:137]
s_waitcnt lgkmcnt(3)
ds_read_b128 v[174:177], v52 offset:32768
v_mul_f32 v44, v44, v9
v_mfma_f32_16x16x32_bf16 v[134:137], v[106:109], v[82:85], v[134:137]
v_max_f32 v45, v10, v44
v_mov_b32 v46, v45
v_mfma_f32_16x16x32_bf16 v[130:133], v[4:7], a[20:23], v[130:133]
s_waitcnt lgkmcnt(3)
ds_read_b128 v[178:181], v51 offset:34816
v_fma_f32 v211, v94, v199, v119
v_mfma_f32_16x16x32_bf16 v[134:137], v[106:109], v[162:165], v[134:137]
s_waitcnt lgkmcnt(3)
ds_read_b128 v[182:185], v52 offset:34816
v_fma_f32 v212, v94, v200, v120
v_mfma_f32_16x16x32_bf16 v[134:137], v[106:109], v[166:169], v[134:137]
global_load_lds_dwordx4 v11, s[42:43] offset:-2048 sc1
v_fma_f32 v213, v94, v201, v121
v_mfma_f32_16x16x32_bf16 v[122:125], a[24:27], v[96:99], v[122:125]
s_waitcnt lgkmcnt(3)
v_permlane16_swap_b32 v45, v46
v_mfma_f32_16x16x32_bf16 v[134:137], v[106:109], v[170:173], v[134:137]
s_waitcnt lgkmcnt(2)
v_max_f32 v45, v45, v46
v_mov_b32 v46, v45
v_mfma_f32_16x16x32_bf16 v[134:137], v[106:109], v[174:177], v[134:137]
s_nop 0
v_permlane32_swap_b32 v45, v46
v_mfma_f32_16x16x32_bf16 v[126:129], a[28:31], v[96:99], v[126:129]
s_waitcnt lgkmcnt(1)
v_max_f32 v45, v45, v46
)"
R"(v_sub_f32 v10, v10, v45
v_mfma_f32_16x16x32_bf16 v[134:137], v[106:109], v[178:181], v[134:137]
s_waitcnt lgkmcnt(0)
v_sub_f32 v44, v44, v45
v_cvt_pk_bf16_f32 v154, v210, v211
v_mfma_f32_16x16x32_bf16 v[134:137], v[106:109], v[182:185], v[134:137]
v_exp_f32 v214, v10
v_mfma_f32_16x16x32_bf16 v[130:133], v[96:99], a[32:35], v[130:133]
v_exp_f32 v215, v44
v_mfma_f32_16x16x32_bf16 v[122:125], a[36:39], v[100:103], v[122:125]
global_load_lds_dwordx4 v12, s[42:43] offset:-1024 sc1
v_add_f32 v45, v214, v215
v_mfma_f32_16x16x32_bf16 v[126:129], a[40:43], v[100:103], v[126:129]
s_nop 0
v_mul_f32 v202, 0x3ae38e39, v134
v_cvt_pk_bf16_f32 v216, -v202, -v202
v_mfma_f32_16x16x32_bf16 v[130:133], v[100:103], a[44:47], v[130:133]
v_lshlrev_b32 v216, 16, v216
v_mov_b32 v46, v45
v_mfma_f32_16x16x32_bf16 v[122:125], a[48:51], v[54:57], v[122:125]
ds_write_b32 v24, v216 offset:64
v_mfma_f32_16x16x32_bf16 v[126:129], a[52:55], v[54:57], v[126:129]
v_mul_f32 v214, v214, v254
v_cvt_pk_bf16_f32 v155, v212, v213
v_mfma_f32_16x16x32_bf16 v[130:133], v[54:57], a[56:59], v[130:133]
v_permlane16_swap_b32 v45, v46
v_mfma_f32_16x16x32_bf16 v[122:125], a[60:63], v[58:61], v[122:125]
v_add_f32 v45, v45, v46
v_mov_b32 v46, v45
v_mfma_f32_16x16x32_bf16 v[126:129], a[64:67], v[58:61], v[126:129]
v_mul_f32 v215, v215, v255
v_fma_f32 v210, v86, v23, v110
v_mfma_f32_16x16x32_bf16 v[130:133], v[58:61], a[68:71], v[130:133]
v_mfma_f32_16x16x32_bf16 v[122:125], a[72:75], v[62:65], v[122:125]
s_waitcnt lgkmcnt(0)
s_barrier
v_mfma_f32_16x16x32_bf16 v[126:129], a[76:79], v[62:65], v[126:129]
global_load_lds_dwordx4 v13, s[42:43] offset:0 sc1
ds_read_b32 v216, v26
v_mfma_f32_16x16x32_bf16 v[130:133], v[62:65], a[80:83], v[130:133]
v_permlane32_swap_b32 v45, v46
v_mfma_f32_16x16x32_bf16 v[122:125], a[84:87], v[66:69], v[122:125]
v_add_f32 v45, v45, v46
v_fma_f32 v211, v87, v23, v111
v_mfma_f32_16x16x32_bf16 v[126:129], a[88:91], v[66:69], v[126:129]
v_rcp_f32 v45, v45
v_mfma_f32_16x16x32_bf16 v[130:133], v[66:69], a[92:95], v[130:133]
v_mul_f32 v214, v214, v45
v_mul_f32 v215, v215, v45
v_mfma_f32_16x16x32_bf16 v[122:125], a[96:99], v[70:73], v[122:125]
v_cvt_pk_bf16_f32 v214, v214, v214
v_cvt_pk_bf16_f32 v215, v215, v215
v_mfma_f32_16x16x32_bf16 v[126:129], a[100:103], v[70:73], v[126:129]
v_and_b32 v194, v214, v40
v_and_b32 v195, v215, v41
v_mfma_f32_16x16x32_bf16 v[130:133], v[70:73], a[104:107], v[130:133]
v_and_b32 v196, v214, v42
v_and_b32 v197, v215, v43
v_mfma_f32_16x16x32_bf16 v[122:125], a[108:111], v[74:77], v[122:125]
global_load_lds_dwordx4 v14, s[42:43] offset:1024 sc1
s_sub_u32 s42, s42, 18432
s_subb_u32 s43, s43, 0
v_mfma_f32_16x16x32_bf16 v[190:193], v[154:157], v[194:197], 0
v_mfma_f32_16x16x32_bf16 v[186:189], v[158:161], v[194:197], 0
s_waitcnt lgkmcnt(0)
v_mul_f32 v216, 0x3ae38e39, v216
v_fma_f32 v203, -v202, v202, v216
v_mfma_f32_16x16x32_bf16 v[126:129], a[112:115], v[74:77], v[126:129]
v_max_f32 v203, 0, v203
v_cvt_pk_bf16_f32 v203, v203, v203
v_mfma_f32_16x16x32_bf16 v[130:133], v[74:77], a[116:119], v[130:133]
v_lshlrev_b32 v203, 16, v203
v_add_f32 v203, 0x3727c5ac, v203
v_mfma_f32_16x16x32_bf16 v[122:125], a[120:123], v[78:81], v[122:125]
v_cvt_pk_bf16_f32 v203, v203, v203
v_lshlrev_b32 v203, 16, v203
v_mfma_f32_16x16x32_bf16 v[126:129], a[124:127], v[78:81], v[126:129]
v_cvt_pk_bf16_f32 v104, v190, v191
v_cvt_pk_bf16_f32 v105, v192, v193
v_mfma_f32_16x16x32_bf16 v[130:133], v[78:81], a[128:131], v[130:133]
ds_write_b64 v18, v[104:105]
v_mfma_f32_16x16x32_bf16 v[122:125], a[132:135], v[82:85], v[122:125]
v_cvt_pk_bf16_f32 v214, v188, v189
v_fma_f32 v212, v88, v23, v112
v_mfma_f32_16x16x32_bf16 v[126:129], a[136:139], v[82:85], v[126:129]
ds_write_b32 v19, v214
v_mfma_f32_16x16x32_bf16 v[130:133], v[82:85], a[140:143], v[130:133]
v_sqrt_f32 v203, v203
v_mfma_f32_16x16x32_bf16 v[122:125], a[144:147], v[162:165], v[122:125]
v_cvt_pk_bf16_f32 v203, v203, v203
v_lshlrev_b32 v203, 16, v203
v_mfma_f32_16x16x32_bf16 v[126:129], a[148:151], v[162:165], v[126:129]
v_rcp_f32 v216, v203
v_mfma_f32_16x16x32_bf16 v[130:133], v[162:165], a[152:155], v[130:133]
ds_write_b32 v24, v216 offset:192
v_mfma_f32_16x16x32_bf16 v[122:125], a[156:159], v[166:169], v[122:125]
v_fma_f32 v213, v89, v23, v113
v_cvt_pk_bf16_f32 v138, v210, v211
v_mfma_f32_16x16x32_bf16 v[126:129], a[160:163], v[166:169], v[126:129]
v_cvt_pk_bf16_f32 v139, v212, v213
v_mfma_f32_16x16x32_bf16 v[130:133], v[166:169], a[164:167], v[130:133]
v_mfma_f32_16x16x32_bf16 v[122:125], a[168:171], v[170:173], v[122:125]
v_mfma_f32_16x16x32_bf16 v[126:129], a[172:175], v[170:173], v[126:129]
v_xor_b32 v51, 0x10000, v51
v_xor_b32 v52, 0x10000, v52
v_mfma_f32_16x16x32_bf16 v[130:133], v[170:173], a[176:179], v[130:133]
v_mfma_f32_16x16x32_bf16 v[122:125], a[180:183], v[174:177], v[122:125]
v_mfma_f32_16x16x32_bf16 v[126:129], a[184:187], v[174:177], v[126:129]
s_waitcnt vmcnt(5) lgkmcnt(0)
s_barrier
.Lqa_in72:
v_mfma_f32_16x16x32_bf16 v[130:133], v[174:177], a[188:191], v[130:133]
s_cmp_eq_u32 s70, s73
s_cbranch_scc1 .Lqa_hk73
.Lqa_hr74:
ds_read_b128 v[0:3], v51 offset:0
v_mfma_f32_16x16x32_bf16 v[122:125], a[192:195], v[178:181], v[122:125]
ds_read_b128 v[4:7], v52 offset:0
ds_read_b32 v23, v24 offset:64
v_mfma_f32_16x16x32_bf16 v[126:129], a[196:199], v[178:181], v[126:129]
s_mov_b64 exec, 1
global_store_dword v252, v48, s[38:39] sc1
s_mov_b64 exec, -1
v_mfma_f32_16x16x32_bf16 v[190:193], v[146:149], v[138:141], v[36:39]
v_mfma_f32_16x16x32_bf16 v[130:133], v[178:181], a[200:203], v[130:133]
ds_read_b128 v[96:99], v51 offset:2048
v_add_u32 v48, 1, v48
v_mfma_f32_16x16x32_bf16 v[122:125], a[204:207], v[182:185], v[122:125]
ds_read_b128 v[100:103], v52 offset:2048
v_add_lshl_u32 v252, v48, 0, 7
v_mfma_f32_16x16x32_bf16 v[126:129], a[208:211], v[182:185], v[126:129]
v_mfma_f32_16x16x32_bf16 v[130:133], v[182:185], a[212:215], v[130:133]
s_sub_u32 s70, s70, 1
ds_read_b128 v[54:57], v51 offset:4096
s_cbranch_scc0 .Lqa_top91
s_waitcnt lgkmcnt(0)
v_bfi_b32 v252, s69, v252, v47
ds_read_b32 v152, v21
ds_read_b32 v95, v24 offset:128
ds_read_b32 v254, v29
ds_read_b32 v255, v30
ds_read_b128 v[198:201], v25 offset:64
ds_read_b64 v[160:161], v22 offset:41472
ds_read_b32 v144, v20
v_fma_f32 v210, v90, v23, v126
v_fma_f32 v211, v91, v23, v127
v_fma_f32 v212, v92, v23, v128
v_fma_f32 v213, v93, v23, v129
v_cvt_pk_bf16_f32 v150, v210, v211
v_cvt_pk_bf16_f32 v151, v212, v213
v_max_f32 v10, v190, v191
s_waitcnt lgkmcnt(6)
v_mfma_f32_16x16x32_bf16 v[186:189], v[150:153], v[138:141], v[32:35]
s_waitcnt lgkmcnt(2)
v_max_f32 v44, v192, v193
v_fma_f32 v210, v94, v198, v130
v_mul_f32 v95, 0x3eae1a93, v95
v_mul_f32 v8, v254, v95
v_mul_f32 v9, v255, v95
s_nop 1
v_max3_f32 v10, v186, v187, v10
v_max3_f32 v44, v188, v189, v44
v_mul_f32 v10, v10, v8
v_mul_f32 v44, v44, v9
v_max_f32 v45, v10, v44
v_mov_b32 v46, v45
v_fma_f32 v211, v94, v199, v131
v_fma_f32 v212, v94, v200, v132
v_permlane16_swap_b32 v45, v46
v_max_f32 v45, v45, v46
v_mov_b32 v46, v45
s_nop 1
v_permlane32_swap_b32 v45, v46
v_max_f32 v45, v45, v46
v_sub_f32 v10, v10, v45
v_sub_f32 v44, v44, v45
v_fma_f32 v213, v94, v201, v133
v_exp_f32 v214, v10
v_exp_f32 v215, v44
s_nop 0
v_add_f32 v45, v214, v215
v_mov_b32 v46, v45
v_mul_f32 v214, v214, v254
v_cvt_pk_bf16_f32 v156, v210, v211
v_permlane16_swap_b32 v45, v46
v_add_f32 v45, v45, v46
v_mov_b32 v46, v45
s_waitcnt lgkmcnt(0)
s_barrier
v_mul_f32 v215, v215, v255
v_cvt_pk_bf16_f32 v157, v212, v213
v_permlane32_swap_b32 v45, v46
v_add_f32 v45, v45, v46
v_fma_f32 v210, v86, v23, v122
v_rcp_f32 v45, v45
s_nop 0
v_mul_f32 v214, v214, v45
v_mul_f32 v215, v215, v45
v_cvt_pk_bf16_f32 v214, v214, v214
v_cvt_pk_bf16_f32 v215, v215, v215
v_and_b32 v196, v214, v40
v_and_b32 v197, v215, v41
v_and_b32 v194, v214, v42
v_and_b32 v195, v215, v43
s_nop 1
v_mfma_f32_16x16x32_bf16 v[190:193], v[154:157], v[194:197], 0
v_mfma_f32_16x16x32_bf16 v[186:189], v[158:161], v[194:197], 0
s_nop 6
v_cvt_pk_bf16_f32 v104, v190, v191
v_cvt_pk_bf16_f32 v105, v192, v193
ds_write_b64 v18, v[104:105]
v_cvt_pk_bf16_f32 v214, v188, v189
v_fma_f32 v211, v87, v23, v123
ds_write_b32 v19, v214
v_fma_f32 v212, v88, v23, v124
v_fma_f32 v213, v89, v23, v125
v_cvt_pk_bf16_f32 v142, v210, v211
v_cvt_pk_bf16_f32 v143, v212, v213
s_waitcnt vmcnt(0)
s_waitcnt lgkmcnt(0)
s_barrier
s_mov_b64 exec, 1
global_store_dword v252, v48, s[38:39] sc1
s_mov_b64 exec, -1
s_waitcnt vmcnt(1)
s_barrier
v_add_u32 v48, 8, v48
v_mbcnt_lo_u32_b32 v252, -1, 0
v_mbcnt_hi_u32_b32 v252, -1, v252
v_add_lshl_u32 v252, v252, 0, 7
v_bfi_b32 v252, s69, v252, v47
s_mov_b64 exec, 0xff
global_store_dword v252, v48, s[38:39] sc1
s_mov_b64 exec, -1
s_cmp_lg_u32 s53, 0
s_cbranch_scc1 .Lqa_n93
v_add_lshl_u32 v253, v50, 0, 7
v_bfi_b32 v253, s69, v253, v49
global_load_dword v53, v253, s[38:39] sc1
s_waitcnt vmcnt(0)
v_sub_u32 v53, v53, v50
v_cmp_lt_u32 vcc, 0x7f, v53
s_cbranch_vccnz .Lqa_sp94
.Lqa_rt95:
v_add_u32 v49, 102400, v49
v_add_lshl_u32 v253, v50, 0, 7
v_bfi_b32 v253, s69, v253, v49
global_load_dword v53, v253, s[38:39] sc1
s_waitcnt vmcnt(0)
v_sub_u32 v53, v53, v50
v_cmp_lt_u32 vcc, 0x7f, v53
s_cbranch_vccnz .Lqa_sp96
.Lqa_rt97:
v_add_u32 v49, 102400, v49
v_add_lshl_u32 v253, v50, 0, 7
v_bfi_b32 v253, s69, v253, v49
global_load_dword v53, v253, s[38:39] sc1
s_waitcnt vmcnt(0)
v_sub_u32 v53, v53, v50
v_cmp_lt_u32 vcc, 0x7f, v53
s_cbranch_vccnz .Lqa_sp98
.Lqa_rt99:
v_add_u32 v49, 102400, v49
v_add_lshl_u32 v253, v50, 0, 7
v_bfi_b32 v253, s69, v253, v49
global_load_dword v53, v253, s[38:39] sc1
s_waitcnt vmcnt(0)
v_sub_u32 v53, v53, v50
v_cmp_lt_u32 vcc, 0x7f, v53
s_cbranch_vccnz .Lqa_sp100
.Lqa_rt101:
v_add_u32 v49, 102400, v49
s_load_dword s75, s[38:39], 0x0
s_waitcnt vmcnt(0) lgkmcnt(0)
s_add_u32 s75, s75, 1
v_mov_b32 v53, s75
v_mov_b32 v49, 0
global_store_dword v49, v53, s[38:39] sc1
.Lqa_n93:
s_endpgm
.Lqa_hk6:
s_setpc_b64 s[90:91]
.Lqa_k15:
s_branch .Lqa_k16
.Lqa_e8:
s_add_u32 s42, s42, s50
s_addc_u32 s43, s43, 0
s_mov_b64 s[76:77], -1
s_mov_b64 s[46:47], s[44:45]
s_mov_b32 s73, 17
s_add_u32 s90, s90, .Lqa_e9-.Lqa_e8
s_addc_u32 s91, s91, 0
s_branch .Lqa_hr7
.Lqa_k16:
s_branch .Lqa_k17
.Lqa_e9:
s_mov_b32 s46, -18432
s_mov_b32 s47, -1
v_or_b32 v234, s52, v234
v_or_b32 v235, s52, v235
v_or_b32 v236, s52, v236
v_or_b32 v237, s52, v237
s_mov_b32 s73, 16
s_add_u32 s90, s90, .Lqa_e10-.Lqa_e9
s_addc_u32 s91, s91, 0
s_branch .Lqa_hr7
.Lqa_k17:
s_branch .Lqa_k18
.Lqa_e10:
v_mov_b32 v234, v32
v_mov_b32 v235, v33
v_mov_b32 v236, v34
v_mov_b32 v237, v35
s_mov_b32 s73, 15
s_add_u32 s90, s90, .Lqa_e11-.Lqa_e10
s_addc_u32 s91, s91, 0
s_branch .Lqa_hr7
.Lqa_k18:
s_branch .Lqa_k19
.Lqa_e11:
v_subrev_u32 v16, 18432, v16
s_mov_b32 s73, 3
s_add_u32 s90, s90, .Lqa_e12-.Lqa_e11
s_addc_u32 s91, s91, 0
s_branch .Lqa_hr7
.Lqa_k19:
s_branch .Lqa_k20
.Lqa_e12:
s_mov_b32 s73, 2
s_add_u32 s90, s90, .Lqa_e13-.Lqa_e12
s_addc_u32 s91, s91, 0
s_branch .Lqa_hr7
.Lqa_k20:
s_branch .Lqa_k21
.Lqa_e13:
s_add_u32 s46, s46, s50
s_addc_u32 s47, s47, 0
s_mov_b32 s73, 0
s_add_u32 s90, s90, .Lqa_e14-.Lqa_e13
s_addc_u32 s91, s91, 0
s_branch .Lqa_hr7
.Lqa_k21:
s_branch .Lqa_bk22
.Lqa_e14:
v_or_b32 v32, s52, v32
v_or_b32 v33, s52, v33
v_or_b32 v34, s52, v34
v_or_b32 v35, s52, v35
s_mov_b32 s73, 999
s_nop 0
s_addc_u32 s91, s91, 0
s_branch .Lqa_hr7
.Lqa_hk27:
s_setpc_b64 s[90:91]
.Lqa_k36:
s_branch .Lqa_k37
.Lqa_e29:
s_add_u32 s42, s42, s50
s_addc_u32 s43, s43, 0
s_mov_b64 s[76:77], -1
s_mov_b64 s[46:47], s[44:45]
s_mov_b32 s73, 17
s_add_u32 s90, s90, .Lqa_e30-.Lqa_e29
s_addc_u32 s91, s91, 0
s_branch .Lqa_hr28
.Lqa_k37:
s_branch .Lqa_k38
.Lqa_e30:
s_mov_b32 s46, -18432
s_mov_b32 s47, -1
v_or_b32 v234, s52, v234
v_or_b32 v235, s52, v235
v_or_b32 v236, s52, v236
v_or_b32 v237, s52, v237
s_mov_b32 s73, 16
s_add_u32 s90, s90, .Lqa_e31-.Lqa_e30
s_addc_u32 s91, s91, 0
s_branch .Lqa_hr28
.Lqa_k38:
s_branch .Lqa_k39
.Lqa_e31:
v_mov_b32 v234, v32
v_mov_b32 v235, v33
v_mov_b32 v236, v34
v_mov_b32 v237, v35
s_mov_b32 s73, 15
s_add_u32 s90, s90, .Lqa_e32-.Lqa_e31
s_addc_u32 s91, s91, 0
s_branch .Lqa_hr28
.Lqa_k39:
s_branch .Lqa_k40
.Lqa_e32:
v_subrev_u32 v16, 18432, v16
s_mov_b32 s73, 3
s_add_u32 s90, s90, .Lqa_e33-.Lqa_e32
s_addc_u32 s91, s91, 0
s_branch .Lqa_hr28
.Lqa_k40:
s_branch .Lqa_k41
.Lqa_e33:
)"
R"(s_mov_b32 s73, 2
s_add_u32 s90, s90, .Lqa_e34-.Lqa_e33
s_addc_u32 s91, s91, 0
s_branch .Lqa_hr28
.Lqa_k41:
s_branch .Lqa_k42
.Lqa_e34:
s_add_u32 s46, s46, s50
s_addc_u32 s47, s47, 0
s_mov_b32 s73, 0
s_add_u32 s90, s90, .Lqa_e35-.Lqa_e34
s_addc_u32 s91, s91, 0
s_branch .Lqa_hr28
.Lqa_k42:
s_branch .Lqa_bk43
.Lqa_e35:
v_or_b32 v32, s52, v32
v_or_b32 v33, s52, v33
v_or_b32 v34, s52, v34
v_or_b32 v35, s52, v35
s_mov_b32 s73, 999
s_nop 0
s_addc_u32 s91, s91, 0
s_branch .Lqa_hr28
.Lqa_sp50:
s_sleep 1
v_add_lshl_u32 v253, v46, 0, 7
v_bfi_b32 v253, s69, v253, v44
global_load_dword v53, v253, s[38:39] sc1
s_add_u32 s71, s71, 1
s_waitcnt vmcnt(0)
v_sub_u32 v53, v53, v46
v_cmp_lt_u32 vcc, 0x7f, v53
s_and_b64 vcc, vcc, s[76:77]
s_cbranch_vccz .Lqa_rt51
s_cmp_lt_u32 s71, 3000000
s_cbranch_scc1 .Lqa_sp50
s_branch .Lqa_rt51
.Lqa_sp52:
s_sleep 1
v_add_lshl_u32 v253, v46, 0, 7
v_bfi_b32 v253, s69, v253, v44
global_load_dword v254, v253, s[38:39] sc1
s_add_u32 s71, s71, 1
s_waitcnt vmcnt(0)
v_sub_u32 v254, v254, v46
v_cmp_lt_u32 vcc, 0x7f, v254
s_and_b64 vcc, vcc, s[76:77]
s_cbranch_vccz .Lqa_rt53
s_cmp_lt_u32 s71, 3000000
s_cbranch_scc1 .Lqa_sp52
s_branch .Lqa_rt53
.Lqa_hk48:
s_setpc_b64 s[90:91]
.Lqa_k61:
s_branch .Lqa_k62
.Lqa_e54:
s_add_u32 s42, s42, s50
s_addc_u32 s43, s43, 0
s_mov_b64 s[76:77], -1
s_mov_b64 s[46:47], s[44:45]
s_mov_b32 s73, 17
s_add_u32 s90, s90, .Lqa_e55-.Lqa_e54
s_addc_u32 s91, s91, 0
s_branch .Lqa_hr49
.Lqa_k62:
s_branch .Lqa_k63
.Lqa_e55:
s_mov_b32 s46, -18432
s_mov_b32 s47, -1
v_or_b32 v202, s52, v202
v_or_b32 v203, s52, v203
v_or_b32 v204, s52, v204
v_or_b32 v205, s52, v205
s_mov_b32 s73, 16
s_add_u32 s90, s90, .Lqa_e56-.Lqa_e55
s_addc_u32 s91, s91, 0
s_branch .Lqa_hr49
.Lqa_k63:
s_branch .Lqa_k64
.Lqa_e56:
v_mov_b32 v202, v32
v_mov_b32 v203, v33
v_mov_b32 v204, v34
v_mov_b32 v205, v35
s_mov_b32 s73, 15
s_add_u32 s90, s90, .Lqa_e57-.Lqa_e56
s_addc_u32 s91, s91, 0
s_branch .Lqa_hr49
.Lqa_k64:
s_branch .Lqa_k65
.Lqa_e57:
v_subrev_u32 v16, 18432, v16
s_mov_b32 s73, 3
s_add_u32 s90, s90, .Lqa_e58-.Lqa_e57
s_addc_u32 s91, s91, 0
s_branch .Lqa_hr49
.Lqa_k65:
s_branch .Lqa_k66
.Lqa_e58:
v_add_u32 v45, s51, v45
s_mov_b32 s73, 2
s_add_u32 s90, s90, .Lqa_e59-.Lqa_e58
s_addc_u32 s91, s91, 0
s_branch .Lqa_hr49
.Lqa_k66:
s_branch .Lqa_k67
.Lqa_e59:
v_add_u32 v44, s51, v44
v_add_u32 v46, -32, v46
s_add_u32 s46, s46, s50
s_addc_u32 s47, s47, 0
s_mov_b32 s73, 0
s_add_u32 s90, s90, .Lqa_e60-.Lqa_e59
s_addc_u32 s91, s91, 0
s_branch .Lqa_hr49
.Lqa_k67:
s_branch .Lqa_bk68
.Lqa_e60:
v_or_b32 v32, s52, v32
v_or_b32 v33, s52, v33
v_or_b32 v34, s52, v34
v_or_b32 v35, s52, v35
s_mov_b32 s73, 999
s_nop 0
s_addc_u32 s91, s91, 0
s_branch .Lqa_hr49
.Lqa_hk73:
s_setpc_b64 s[90:91]
.Lqa_k82:
s_branch .Lqa_k83
.Lqa_e75:
s_add_u32 s42, s42, s50
s_addc_u32 s43, s43, 0
s_mov_b64 s[76:77], -1
s_mov_b64 s[46:47], s[44:45]
s_mov_b32 s73, 17
s_add_u32 s90, s90, .Lqa_e76-.Lqa_e75
s_addc_u32 s91, s91, 0
s_branch .Lqa_hr74
.Lqa_k83:
s_branch .Lqa_k84
.Lqa_e76:
s_mov_b32 s46, -18432
s_mov_b32 s47, -1
v_or_b32 v206, s52, v206
v_or_b32 v207, s52, v207
v_or_b32 v208, s52, v208
v_or_b32 v209, s52, v209
s_mov_b32 s73, 16
s_add_u32 s90, s90, .Lqa_e77-.Lqa_e76
s_addc_u32 s91, s91, 0
s_branch .Lqa_hr74
.Lqa_k84:
s_branch .Lqa_k85
.Lqa_e77:
v_mov_b32 v206, v32
v_mov_b32 v207, v33
v_mov_b32 v208, v34
v_mov_b32 v209, v35
s_mov_b32 s73, 15
s_add_u32 s90, s90, .Lqa_e78-.Lqa_e77
s_addc_u32 s91, s91, 0
s_branch .Lqa_hr74
.Lqa_k85:
s_branch .Lqa_k86
.Lqa_e78:
v_add_u32 v48, -1, v48
v_add_lshl_u32 v252, v48, 0, 7
v_bfi_b32 v252, s69, v252, v47
s_mov_b32 s73, 3
s_add_u32 s90, s90, .Lqa_e79-.Lqa_e78
s_addc_u32 s91, s91, 0
s_branch .Lqa_hr74
.Lqa_k86:
s_branch .Lqa_k87
.Lqa_e79:
s_mov_b32 s73, 2
s_add_u32 s90, s90, .Lqa_e80-.Lqa_e79
s_addc_u32 s91, s91, 0
s_branch .Lqa_hr74
.Lqa_k87:
s_branch .Lqa_k88
.Lqa_e80:
s_add_u32 s46, s46, s50
s_addc_u32 s47, s47, 0
s_mov_b32 s73, 0
s_add_u32 s90, s90, .Lqa_e81-.Lqa_e80
s_addc_u32 s91, s91, 0
s_branch .Lqa_hr74
.Lqa_k88:
s_branch .Lqa_bk89
.Lqa_e81:
v_or_b32 v32, s52, v32
v_or_b32 v33, s52, v33
v_or_b32 v34, s52, v34
v_or_b32 v35, s52, v35
s_mov_b32 s73, 999
s_nop 0
s_addc_u32 s91, s91, 0
s_branch .Lqa_hr74
.Lqa_sp94:
s_sleep 1
v_add_lshl_u32 v253, v50, 0, 7
v_bfi_b32 v253, s69, v253, v49
global_load_dword v53, v253, s[38:39] sc1
s_add_u32 s71, s71, 1
s_waitcnt vmcnt(0)
v_sub_u32 v53, v53, v50
v_cmp_lt_u32 vcc, 0x7f, v53
s_cbranch_vccz .Lqa_rt95
s_cmp_lt_u32 s71, 3000000
s_cbranch_scc1 .Lqa_sp94
s_branch .Lqa_rt95
.Lqa_sp96:
s_sleep 1
v_add_lshl_u32 v253, v50, 0, 7
v_bfi_b32 v253, s69, v253, v49
global_load_dword v53, v253, s[38:39] sc1
s_add_u32 s71, s71, 1
s_waitcnt vmcnt(0)
v_sub_u32 v53, v53, v50
v_cmp_lt_u32 vcc, 0x7f, v53
s_cbranch_vccz .Lqa_rt97
s_cmp_lt_u32 s71, 3000000
s_cbranch_scc1 .Lqa_sp96
s_branch .Lqa_rt97
.Lqa_sp98:
s_sleep 1
v_add_lshl_u32 v253, v50, 0, 7
v_bfi_b32 v253, s69, v253, v49
global_load_dword v53, v253, s[38:39] sc1
s_add_u32 s71, s71, 1
s_waitcnt vmcnt(0)
v_sub_u32 v53, v53, v50
v_cmp_lt_u32 vcc, 0x7f, v53
s_cbranch_vccz .Lqa_rt99
s_cmp_lt_u32 s71, 3000000
s_cbranch_scc1 .Lqa_sp98
s_branch .Lqa_rt99
.Lqa_sp100:
s_sleep 1
v_add_lshl_u32 v253, v50, 0, 7
v_bfi_b32 v253, s69, v253, v49
global_load_dword v53, v253, s[38:39] sc1
s_add_u32 s71, s71, 1
s_waitcnt vmcnt(0)
v_sub_u32 v53, v53, v50
v_cmp_lt_u32 vcc, 0x7f, v53
s_cbranch_vccz .Lqa_rt101
s_cmp_lt_u32 s71, 3000000
s_cbranch_scc1 .Lqa_sp100
s_branch .Lqa_rt101
.Le0:
)"
    :: "{v[0:31]}"(va), "{v[32:63]}"(vb), "{s[36:51]}"(sa), "{s[52:67]}"(sb) : "v255", "a255", "s99", "vcc", "memory");
    return;
  }
  if (wave == 2) {
    const unsigned o = min((lane & 7) * 1048576u, 5308412u);
    touch(wg + o); touch(wu + o); touch(wd + o); touch(out + min(o, 2359292u));
    touch(ws + min(lane * 2097152u, WS_END - 4));
    if (blockIdx.x - 64 < 400 * FL / 8192) touch(ws + WS_FL + ((blockIdx.x - 64) * 64 + lane) * 128);
  }
  if (ev) touch(wo + min((jj - 8) * 216 + threadIdx.x, 5183u) * 128);
  if (!ev && wave != 2) touch(x0 + min((xh * 32 + jj) * 144 + min(wave, 2u) * 64 + lane, 18431u) * 128);
  if (ev && jj < 10) {
    const unsigned wo_rho = xc >> 1, wo_cw = jj & 1;
    const unsigned r = lane & 15, g = lane >> 4, q = r >> 2, p = lane & 3;
    const unsigned rho = wo_rho, cw = wo_cw, row0 = rho * RP + PAD, pc0 = wave * 5;
    auto wv = [&](unsigned j) { const unsigned b = j * 1024 + lane * 16, rw = b / 576; return rw * 576 + b + 4096 - (j & 7) * 1024; };
    u32x32 va; u32x16 sa;
    va[0] = wv(0);
    va[1] = wv(1);
    va[2] = wv(2);
    va[3] = wv(3);
    va[4] = wv(4);
    va[5] = wv(5);
    va[6] = wv(6);
    va[7] = wv(7);
    va[8] = wv(8);
    va[9] = (8 * g + q) * 576 + cw * 64 + wave * 128 + p * 16;
    va[10] = (8 * g + q) * 576 + (cw ? 0 : 512) + p * 16 + (wave >> 1) * 8;
    va[11] = WS_O + (row0 - 2) * XT + pc0 * 1024 + 2048 + lane * 16;
    va[12] = WS_O + (row0 + 63) * XT + pc0 * 1024 + 2048 + lane * 16;
    va[13] = WS_X1 + (row0 - 5) * X1P + (cw * 10 + wave * 2) * 1024 + lane * 16;
    va[14] = WS_X1 + (row0 - 5) * X1P + (8 + cw) * 1024 + lane * 16;
    va[15] = WS_X1 + (row0 - 7) * X1P + XT + cw * 128 + r * 8;
    va[16] = ((rho * 32 + 31) * 16 + 0 + (lane >> 3)) * 1152 + (cw * 5 + wave) * 128 + (((lane ^ (lane >> 3)) & 7) << 4) - 0;
    va[17] = ((rho * 32 + 31) * 16 + 8 + (lane >> 3)) * 1152 + (cw * 5 + wave) * 128 + (((lane ^ (lane >> 3)) & 7) << 4) - 1024;
    va[18] = ((rho * 32 + 31) * 16 + r) * 1152 + (8 + cw) * 64 + g * 16;
    va[19] = 47104 + wave * 2048 + r * 128 + (((0 + g) ^ (r & 7)) << 4);
    va[20] = 47104 + wave * 2048 + r * 128 + (((4 + g) ^ (r & 7)) << 4);
    va[21] = WS_FL + rho * FR + FO + (lane & 7) * FL;
    va[22] = 0u - 2;
    va[23] = WS_FL + rho * FR + FX1 + cw * FL;
    va[24] = 0u - 6;
    va[25] = lane * 16;
    va[26] = lane * 16;
    va[27] = lane * 8;
    sa[0] = 4 + wave; sa[2] = (unsigned)(unsigned long long)ws; sa[3] = (unsigned)((unsigned long long)ws >> 32);
    sa[4] = uni((unsigned)(unsigned long long)(wo + wave * 16 * 1152 + cw * 576));
    sa[5] = uni((unsigned)((unsigned long long)(wo + wave * 16 * 1152 + cw * 576) >> 32));
    sa[6] = uni((unsigned)(unsigned long long)(x0));
    sa[7] = uni((unsigned)((unsigned long long)(x0) >> 32));
    sa[8] = uni((unsigned)(unsigned long long)(x0));
    sa[9] = uni((unsigned)((unsigned long long)(x0) >> 32));
    sa[10] = uni((unsigned)(unsigned long long)(ws + WS_X2));
    sa[11] = uni((unsigned)((unsigned long long)(ws + WS_X2) >> 32));
    sa[12] = uni(wave * 9216);
    sa[13] = uni(pc0 * 1024 + 2048);
    asm volatile(
R"(.Lb1:
s_movk_i32 s69, 0x380
s_mov_b32 s71, 0
s_mov_b32 s74, 0
s_load_dword s37, s[38:39], 0x0
s_nop 1
s_and_b32 s75, s36, 3
s_cmp_eq_u32 s75, 1
s_cbranch_scc1 .Lwo_w2
s_cmp_eq_u32 s75, 2
s_cbranch_scc1 .Lwo_w3
s_cmp_eq_u32 s75, 3
s_cbranch_scc1 .Lwo_w4
.Lwo_w1:
s_add_u32 m0, s48, 4096
s_nop 0
global_load_lds_dwordx4 v0, s[40:41] offset:-4096
global_load_lds_dwordx4 v1, s[40:41] offset:-3072
global_load_lds_dwordx4 v2, s[40:41] offset:-2048
global_load_lds_dwordx4 v3, s[40:41] offset:-1024
global_load_lds_dwordx4 v4, s[40:41] offset:0
global_load_lds_dwordx4 v5, s[40:41] offset:1024
global_load_lds_dwordx4 v6, s[40:41] offset:2048
global_load_lds_dwordx4 v7, s[40:41] offset:3072
s_add_u32 m0, s48, 12288
s_nop 0
global_load_lds_dwordx4 v8, s[40:41] offset:-4096
s_add_u32 s40, s40, 73728
s_addc_u32 s41, s41, 0
s_add_u32 m0, s48, 40960
s_nop 0
global_load_lds_dwordx4 v0, s[40:41] offset:-4096
global_load_lds_dwordx4 v1, s[40:41] offset:-3072
global_load_lds_dwordx4 v2, s[40:41] offset:-2048
global_load_lds_dwordx4 v3, s[40:41] offset:-1024
global_load_lds_dwordx4 v4, s[40:41] offset:0
global_load_lds_dwordx4 v5, s[40:41] offset:1024
global_load_lds_dwordx4 v6, s[40:41] offset:2048
global_load_lds_dwordx4 v7, s[40:41] offset:3072
s_add_u32 m0, s48, 49152
s_nop 0
global_load_lds_dwordx4 v8, s[40:41] offset:-4096
s_add_u32 s40, s40, 73728
s_addc_u32 s41, s41, 0
s_add_u32 m0, s48, 77824
s_nop 0
global_load_lds_dwordx4 v0, s[40:41] offset:-4096
global_load_lds_dwordx4 v1, s[40:41] offset:-3072
global_load_lds_dwordx4 v2, s[40:41] offset:-2048
global_load_lds_dwordx4 v3, s[40:41] offset:-1024
global_load_lds_dwordx4 v4, s[40:41] offset:0
global_load_lds_dwordx4 v5, s[40:41] offset:1024
global_load_lds_dwordx4 v6, s[40:41] offset:2048
global_load_lds_dwordx4 v7, s[40:41] offset:3072
s_add_u32 m0, s48, 86016
s_nop 0
global_load_lds_dwordx4 v8, s[40:41] offset:-4096
s_add_u32 s40, s40, 73728
s_addc_u32 s41, s41, 0
v_add_u32 v204, 0x10000, v9
v_add_u32 v205, 0x20000, v9
v_add_u32 v206, 0x10000, v10
v_add_u32 v207, 0x20000, v10
s_waitcnt lgkmcnt(0)
s_mul_i32 s37, s37, 0x9E3779B1
s_add_u32 s37, s37, 0x7F4A7C15
v_add_u32 v22, s37, v22
v_add_u32 v24, s37, v24
s_waitcnt vmcnt(18)
s_barrier
s_add_u32 m0, s48, 114688
ds_read_b64_tr_b16 a[0:1], v9 offset:0
ds_read_b64_tr_b16 a[2:3], v9 offset:2304
global_load_lds_dwordx4 v0, s[40:41] offset:-4096
ds_read_b64_tr_b16 a[4:5], v9 offset:8
ds_read_b64_tr_b16 a[6:7], v9 offset:2312
global_load_lds_dwordx4 v1, s[40:41] offset:-3072
ds_read_b64_tr_b16 a[8:9], v9 offset:64
global_load_lds_dwordx4 v2, s[40:41] offset:-2048
ds_read_b64_tr_b16 a[10:11], v9 offset:2368
ds_read_b64_tr_b16 a[12:13], v9 offset:72
global_load_lds_dwordx4 v3, s[40:41] offset:-1024
ds_read_b64_tr_b16 a[14:15], v9 offset:2376
global_load_lds_dwordx4 v4, s[40:41] offset:0
ds_read_b64_tr_b16 v[60:61], v10 offset:0
ds_read_b64_tr_b16 v[62:63], v10 offset:2304
global_load_lds_dwordx4 v5, s[40:41] offset:1024
ds_read_b64_tr_b16 a[16:17], v9 offset:18432
global_load_lds_dwordx4 v6, s[40:41] offset:2048
ds_read_b64_tr_b16 a[18:19], v9 offset:20736
ds_read_b64_tr_b16 a[20:21], v9 offset:18440
global_load_lds_dwordx4 v7, s[40:41] offset:3072
ds_read_b64_tr_b16 a[22:23], v9 offset:20744
s_add_u32 m0, s48, 122880
ds_read_b64_tr_b16 a[24:25], v9 offset:18496
ds_read_b64_tr_b16 a[26:27], v9 offset:20800
global_load_lds_dwordx4 v8, s[40:41] offset:-4096
ds_read_b64_tr_b16 a[28:29], v9 offset:18504
s_add_u32 s40, s40, 73728
ds_read_b64_tr_b16 a[30:31], v9 offset:20808
ds_read_b64_tr_b16 v[64:65], v10 offset:18432
s_addc_u32 s41, s41, 0
ds_read_b64_tr_b16 v[66:67], v10 offset:20736
s_waitcnt vmcnt(18) lgkmcnt(0)
s_barrier
s_add_u32 m0, s48, 4096
ds_read_b64_tr_b16 a[32:33], v9 offset:36864
ds_read_b64_tr_b16 a[34:35], v9 offset:39168
global_load_lds_dwordx4 v0, s[40:41] offset:-4096
ds_read_b64_tr_b16 a[36:37], v9 offset:36872
ds_read_b64_tr_b16 a[38:39], v9 offset:39176
global_load_lds_dwordx4 v1, s[40:41] offset:-3072
ds_read_b64_tr_b16 a[40:41], v9 offset:36928
global_load_lds_dwordx4 v2, s[40:41] offset:-2048
ds_read_b64_tr_b16 a[42:43], v9 offset:39232
ds_read_b64_tr_b16 a[44:45], v9 offset:36936
global_load_lds_dwordx4 v3, s[40:41] offset:-1024
ds_read_b64_tr_b16 a[46:47], v9 offset:39240
global_load_lds_dwordx4 v4, s[40:41] offset:0
ds_read_b64_tr_b16 v[68:69], v10 offset:36864
ds_read_b64_tr_b16 v[70:71], v10 offset:39168
global_load_lds_dwordx4 v5, s[40:41] offset:1024
ds_read_b64_tr_b16 a[48:49], v9 offset:55296
global_load_lds_dwordx4 v6, s[40:41] offset:2048
ds_read_b64_tr_b16 a[50:51], v9 offset:57600
ds_read_b64_tr_b16 a[52:53], v9 offset:55304
global_load_lds_dwordx4 v7, s[40:41] offset:3072
ds_read_b64_tr_b16 a[54:55], v9 offset:57608
s_add_u32 m0, s48, 12288
ds_read_b64_tr_b16 a[56:57], v9 offset:55360
ds_read_b64_tr_b16 a[58:59], v9 offset:57664
global_load_lds_dwordx4 v8, s[40:41] offset:-4096
ds_read_b64_tr_b16 a[60:61], v9 offset:55368
s_add_u32 s40, s40, 73728
ds_read_b64_tr_b16 a[62:63], v9 offset:57672
ds_read_b64_tr_b16 v[72:73], v10 offset:55296
s_addc_u32 s41, s41, 0
ds_read_b64_tr_b16 v[74:75], v10 offset:57600
s_waitcnt vmcnt(18) lgkmcnt(0)
s_barrier
s_add_u32 m0, s48, 40960
ds_read_b64_tr_b16 a[64:65], v204 offset:8192
ds_read_b64_tr_b16 a[66:67], v204 offset:10496
global_load_lds_dwordx4 v0, s[40:41] offset:-4096
ds_read_b64_tr_b16 a[68:69], v204 offset:8200
ds_read_b64_tr_b16 a[70:71], v204 offset:10504
global_load_lds_dwordx4 v1, s[40:41] offset:-3072
ds_read_b64_tr_b16 a[72:73], v204 offset:8256
global_load_lds_dwordx4 v2, s[40:41] offset:-2048
ds_read_b64_tr_b16 a[74:75], v204 offset:10560
ds_read_b64_tr_b16 a[76:77], v204 offset:8264
global_load_lds_dwordx4 v3, s[40:41] offset:-1024
ds_read_b64_tr_b16 a[78:79], v204 offset:10568
global_load_lds_dwordx4 v4, s[40:41] offset:0
ds_read_b64_tr_b16 v[76:77], v206 offset:8192
ds_read_b64_tr_b16 v[78:79], v206 offset:10496
global_load_lds_dwordx4 v5, s[40:41] offset:1024
ds_read_b64_tr_b16 a[80:81], v204 offset:26624
global_load_lds_dwordx4 v6, s[40:41] offset:2048
ds_read_b64_tr_b16 a[82:83], v204 offset:28928
ds_read_b64_tr_b16 a[84:85], v204 offset:26632
global_load_lds_dwordx4 v7, s[40:41] offset:3072
ds_read_b64_tr_b16 a[86:87], v204 offset:28936
s_add_u32 m0, s48, 49152
ds_read_b64_tr_b16 a[88:89], v204 offset:26688
ds_read_b64_tr_b16 a[90:91], v204 offset:28992
global_load_lds_dwordx4 v8, s[40:41] offset:-4096
ds_read_b64_tr_b16 a[92:93], v204 offset:26696
s_add_u32 s40, s40, 73728
ds_read_b64_tr_b16 a[94:95], v204 offset:29000
ds_read_b64_tr_b16 v[80:81], v206 offset:26624
s_addc_u32 s41, s41, 0
ds_read_b64_tr_b16 v[82:83], v206 offset:28928
s_waitcnt vmcnt(18) lgkmcnt(0)
s_barrier
s_add_u32 m0, s48, 77824
ds_read_b64_tr_b16 a[96:97], v204 offset:45056
ds_read_b64_tr_b16 a[98:99], v204 offset:47360
global_load_lds_dwordx4 v0, s[40:41] offset:-4096
ds_read_b64_tr_b16 a[100:101], v204 offset:45064
ds_read_b64_tr_b16 a[102:103], v204 offset:47368
global_load_lds_dwordx4 v1, s[40:41] offset:-3072
ds_read_b64_tr_b16 a[104:105], v204 offset:45120
global_load_lds_dwordx4 v2, s[40:41] offset:-2048
ds_read_b64_tr_b16 a[106:107], v204 offset:47424
ds_read_b64_tr_b16 a[108:109], v204 offset:45128
global_load_lds_dwordx4 v3, s[40:41] offset:-1024
ds_read_b64_tr_b16 a[110:111], v204 offset:47432
global_load_lds_dwordx4 v4, s[40:41] offset:0
ds_read_b64_tr_b16 v[84:85], v206 offset:45056
ds_read_b64_tr_b16 v[86:87], v206 offset:47360
global_load_lds_dwordx4 v5, s[40:41] offset:1024
ds_read_b64_tr_b16 a[112:113], v204 offset:63488
global_load_lds_dwordx4 v6, s[40:41] offset:2048
ds_read_b64_tr_b16 a[114:115], v205 offset:256
ds_read_b64_tr_b16 a[116:117], v204 offset:63496
global_load_lds_dwordx4 v7, s[40:41] offset:3072
ds_read_b64_tr_b16 a[118:119], v205 offset:264
s_add_u32 m0, s48, 86016
ds_read_b64_tr_b16 a[120:121], v204 offset:63552
ds_read_b64_tr_b16 a[122:123], v205 offset:320
global_load_lds_dwordx4 v8, s[40:41] offset:-4096
ds_read_b64_tr_b16 a[124:125], v204 offset:63560
s_add_u32 s40, s40, 73728
ds_read_b64_tr_b16 a[126:127], v205 offset:328
ds_read_b64_tr_b16 v[88:89], v206 offset:63488
s_addc_u32 s41, s41, 0
ds_read_b64_tr_b16 v[90:91], v207 offset:256
s_waitcnt vmcnt(18) lgkmcnt(0)
s_barrier
s_add_u32 m0, s48, 114688
ds_read_b64_tr_b16 a[128:129], v9 offset:0
ds_read_b64_tr_b16 a[130:131], v9 offset:2304
global_load_lds_dwordx4 v0, s[40:41] offset:-4096
ds_read_b64_tr_b16 a[132:133], v9 offset:8
global_load_lds_dwordx4 v1, s[40:41] offset:-3072
ds_read_b64_tr_b16 a[134:135], v9 offset:2312
ds_read_b64_tr_b16 a[136:137], v9 offset:64
global_load_lds_dwordx4 v2, s[40:41] offset:-2048
ds_read_b64_tr_b16 a[138:139], v9 offset:2368
global_load_lds_dwordx4 v3, s[40:41] offset:-1024
ds_read_b64_tr_b16 a[140:141], v9 offset:72
global_load_lds_dwordx4 v4, s[40:41] offset:0
ds_read_b64_tr_b16 a[142:143], v9 offset:2376
ds_read_b64_tr_b16 v[92:93], v10 offset:0
global_load_lds_dwordx4 v5, s[40:41] offset:1024
ds_read_b64_tr_b16 v[94:95], v10 offset:2304
global_load_lds_dwordx4 v6, s[40:41] offset:2048
ds_read_b64_tr_b16 a[144:145], v9 offset:18432
ds_read_b64_tr_b16 a[146:147], v9 offset:20736
global_load_lds_dwordx4 v7, s[40:41] offset:3072
ds_read_b64_tr_b16 a[148:149], v9 offset:18440
s_add_u32 m0, s48, 122880
ds_read_b64_tr_b16 a[150:151], v9 offset:20744
global_load_lds_dwordx4 v8, s[40:41] offset:-4096
ds_read_b64_tr_b16 a[152:153], v9 offset:18496
ds_read_b64_tr_b16 a[154:155], v9 offset:20800
s_add_u32 s40, s40, 73728
ds_read_b64_tr_b16 a[156:157], v9 offset:18504
s_addc_u32 s41, s41, 0
ds_read_b64_tr_b16 a[158:159], v9 offset:20808
s_waitcnt vmcnt(18) lgkmcnt(0)
s_barrier
s_add_u32 m0, s48, 4096
ds_read_b64_tr_b16 a[160:161], v9 offset:36864
ds_read_b64_tr_b16 a[162:163], v9 offset:39168
global_load_lds_dwordx4 v0, s[40:41] offset:-4096
ds_read_b64_tr_b16 a[164:165], v9 offset:36872
global_load_lds_dwordx4 v1, s[40:41] offset:-3072
ds_read_b64_tr_b16 a[166:167], v9 offset:39176
global_load_lds_dwordx4 v2, s[40:41] offset:-2048
ds_read_b64_tr_b16 a[168:169], v9 offset:36928
global_load_lds_dwordx4 v3, s[40:41] offset:-1024
ds_read_b64_tr_b16 a[170:171], v9 offset:39232
ds_read_b64_tr_b16 a[172:173], v9 offset:36936
global_load_lds_dwordx4 v4, s[40:41] offset:0
ds_read_b64_tr_b16 a[174:175], v9 offset:39240
global_load_lds_dwordx4 v5, s[40:41] offset:1024
ds_read_b64_tr_b16 a[176:177], v9 offset:55296
global_load_lds_dwordx4 v6, s[40:41] offset:2048
ds_read_b64_tr_b16 a[178:179], v9 offset:57600
global_load_lds_dwordx4 v7, s[40:41] offset:3072
ds_read_b64_tr_b16 a[180:181], v9 offset:55304
ds_read_b64_tr_b16 a[182:183], v9 offset:57608
s_add_u32 m0, s48, 12288
ds_read_b64_tr_b16 a[184:185], v9 offset:55360
global_load_lds_dwordx4 v8, s[40:41] offset:-4096
ds_read_b64_tr_b16 a[186:187], v9 offset:57664
s_sub_u32 s40, s40, 589824
ds_read_b64_tr_b16 a[188:189], v9 offset:55368
s_subb_u32 s41, s41, 0
ds_read_b64_tr_b16 a[190:191], v9 offset:57672
s_waitcnt vmcnt(18) lgkmcnt(0)
s_barrier
ds_read_b64_tr_b16 a[192:193], v204 offset:8192
ds_read_b64_tr_b16 a[194:195], v204 offset:10496
ds_read_b64_tr_b16 a[196:197], v204 offset:8200
ds_read_b64_tr_b16 a[198:199], v204 offset:10504
ds_read_b64_tr_b16 a[200:201], v204 offset:8256
ds_read_b64_tr_b16 a[202:203], v204 offset:10560
ds_read_b64_tr_b16 a[204:205], v204 offset:8264
ds_read_b64_tr_b16 a[206:207], v204 offset:10568
ds_read_b64_tr_b16 a[208:209], v204 offset:26624
ds_read_b64_tr_b16 a[210:211], v204 offset:28928
ds_read_b64_tr_b16 a[212:213], v204 offset:26632
ds_read_b64_tr_b16 a[214:215], v204 offset:28936
ds_read_b64_tr_b16 a[216:217], v204 offset:26688
ds_read_b64_tr_b16 a[218:219], v204 offset:28992
ds_read_b64_tr_b16 a[220:221], v204 offset:26696
ds_read_b64_tr_b16 a[222:223], v204 offset:29000
s_waitcnt vmcnt(9) lgkmcnt(0)
s_barrier
ds_read_b64_tr_b16 a[224:225], v204 offset:45056
ds_read_b64_tr_b16 a[226:227], v204 offset:47360
ds_read_b64_tr_b16 a[228:229], v204 offset:45064
ds_read_b64_tr_b16 a[230:231], v204 offset:47368
ds_read_b64_tr_b16 a[232:233], v204 offset:45120
ds_read_b64_tr_b16 a[234:235], v204 offset:47424
ds_read_b64_tr_b16 a[236:237], v204 offset:45128
ds_read_b64_tr_b16 a[238:239], v204 offset:47432
ds_read_b64_tr_b16 a[240:241], v204 offset:63488
ds_read_b64_tr_b16 a[242:243], v205 offset:256
ds_read_b64_tr_b16 a[244:245], v204 offset:63496
ds_read_b64_tr_b16 a[246:247], v205 offset:264
ds_read_b64_tr_b16 a[248:249], v204 offset:63552
ds_read_b64_tr_b16 a[250:251], v205 offset:320
ds_read_b64_tr_b16 a[252:253], v204 offset:63560
ds_read_b64_tr_b16 a[254:255], v205 offset:328
s_waitcnt vmcnt(0) lgkmcnt(0)
s_barrier
ds_read_b64_tr_b16 v[28:29], v9 offset:0
ds_read_b64_tr_b16 v[30:31], v9 offset:2304
ds_read_b64_tr_b16 v[32:33], v9 offset:8
ds_read_b64_tr_b16 v[34:35], v9 offset:2312
ds_read_b64_tr_b16 v[36:37], v9 offset:64
ds_read_b64_tr_b16 v[38:39], v9 offset:2368
ds_read_b64_tr_b16 v[40:41], v9 offset:72
ds_read_b64_tr_b16 v[42:43], v9 offset:2376
ds_read_b64_tr_b16 v[44:45], v9 offset:18432
ds_read_b64_tr_b16 v[46:47], v9 offset:20736
ds_read_b64_tr_b16 v[48:49], v9 offset:18440
ds_read_b64_tr_b16 v[50:51], v9 offset:20744
ds_read_b64_tr_b16 v[52:53], v9 offset:18496
ds_read_b64_tr_b16 v[54:55], v9 offset:20800
ds_read_b64_tr_b16 v[56:57], v9 offset:18504
ds_read_b64_tr_b16 v[58:59], v9 offset:20808
s_waitcnt lgkmcnt(0)
s_barrier
v_add_lshl_u32 v252, v24, 0, 7
v_bfi_b32 v252, s69, v252, v23
v_add_lshl_u32 v253, v22, 0, 7
v_bfi_b32 v253, s69, v253, v21
s_mov_b64 s[76:77], 0
s_add_u32 m0, s49, 18432
s_mov_b32 s70, 33
s_waitcnt vmcnt(0) lgkmcnt(0)
s_nop 7
.p2align 6
.Lwo_top5:
s_waitcnt lgkmcnt(2)
v_mfma_f32_16x16x32_bf16 v[96:99], a[0:3], v[132:135], v[96:99]
ds_read_b128 v[144:147], v25 offset:3072
v_mfma_f32_16x16x32_bf16 v[100:103], a[4:7], v[132:135], v[100:103]
v_mfma_f32_16x16x32_bf16 v[104:107], a[8:11], v[132:135], v[104:107]
s_waitcnt vmcnt(7)
s_barrier
v_mfma_f32_16x16x32_bf16 v[108:111], a[12:15], v[132:135], v[108:111]
v_cvt_pk_bf16_f32 v164, v112, v113
v_cvt_pk_bf16_f32 v165, v114, v115
s_mov_b64 exec, 1
global_store_dword v252, v24, s[38:39] sc1
s_mov_b64 exec, -1
v_mfma_f32_16x16x32_bf16 v[128:131], v[60:63], v[132:135], 0
s_waitcnt lgkmcnt(2)
v_cvt_pk_bf16_f32 v166, v116, v117
v_cvt_pk_bf16_f32 v167, v118, v119
v_add_u32 v24, 1, v24
v_add_lshl_u32 v252, v24, 0, 7
v_mfma_f32_16x16x32_bf16 v[96:99], a[16:19], v[136:139], v[96:99]
ds_read_b128 v[132:135], v25 offset:4096
v_bfi_b32 v252, s69, v252, v23
v_mfma_f32_16x16x32_bf16 v[100:103], a[20:23], v[136:139], v[100:103]
global_store_dwordx4 v13, v[164:167], s[38:39] offset:0 sc1
v_mfma_f32_16x16x32_bf16 v[104:107], a[24:27], v[136:139], v[104:107]
v_cvt_pk_bf16_f32 v168, v120, v121
v_cvt_pk_bf16_f32 v169, v122, v123
v_mfma_f32_16x16x32_bf16 v[108:111], a[28:31], v[136:139], v[108:111]
v_cvt_pk_bf16_f32 v170, v124, v125
v_cvt_pk_bf16_f32 v171, v126, v127
v_mfma_f32_16x16x32_bf16 v[128:131], v[64:67], v[136:139], v[128:131]
s_waitcnt lgkmcnt(2)
global_store_dwordx4 v13, v[168:171], s[38:39] offset:1024 sc1
v_mfma_f32_16x16x32_bf16 v[96:99], a[32:35], v[140:143], v[96:99]
s_cmp_eq_u32 s70, 32
ds_read_b128 v[136:139], v25 offset:5120
v_add_u32 v13, 18688, v13
v_mfma_f32_16x16x32_bf16 v[100:103], a[36:39], v[140:143], v[100:103]
s_cselect_b64 s[42:43], s[44:45], s[42:43]
v_add_f32 v254, v112, v113
v_mul_f32 v255, v112, v112
v_mfma_f32_16x16x32_bf16 v[104:107], a[40:43], v[140:143], v[104:107]
s_cmp_eq_u32 s70, 16
v_fmac_f32 v255, v113, v113
v_add_f32 v254, v254, v114
v_mfma_f32_16x16x32_bf16 v[108:111], a[44:47], v[140:143], v[108:111]
s_cselect_b64 s[42:43], s[46:47], s[42:43]
v_fmac_f32 v255, v114, v114
v_add_f32 v254, v254, v115
v_mfma_f32_16x16x32_bf16 v[128:131], v[68:71], v[140:143], v[128:131]
s_waitcnt lgkmcnt(2)
v_fmac_f32 v255, v115, v115
v_add_f32 v254, v254, v116
v_mfma_f32_16x16x32_bf16 v[96:99], a[48:51], v[144:147], v[96:99]
s_xor_b32 s49, s49, 0x10000
ds_read_b128 v[140:143], v25 offset:6144
v_fmac_f32 v255, v116, v116
v_mfma_f32_16x16x32_bf16 v[100:103], a[52:55], v[144:147], v[100:103]
s_mov_b32 m0, 47104
v_add_f32 v254, v254, v117
v_fmac_f32 v255, v117, v117
)"
R"(v_mfma_f32_16x16x32_bf16 v[104:107], a[56:59], v[144:147], v[104:107]
v_add_f32 v254, v254, v118
v_fmac_f32 v255, v118, v118
v_mfma_f32_16x16x32_bf16 v[108:111], a[60:63], v[144:147], v[108:111]
global_load_lds_dwordx4 v16, s[42:43] offset:0 sc1
v_add_f32 v254, v254, v119
v_mfma_f32_16x16x32_bf16 v[128:131], v[72:75], v[144:147], v[128:131]
s_waitcnt lgkmcnt(2)
v_fmac_f32 v255, v119, v119
v_add_f32 v254, v254, v120
v_mfma_f32_16x16x32_bf16 v[96:99], a[64:67], v[132:135], v[96:99]
ds_read_b128 v[144:147], v25 offset:7168
v_fmac_f32 v255, v120, v120
v_mfma_f32_16x16x32_bf16 v[100:103], a[68:71], v[132:135], v[100:103]
v_add_f32 v254, v254, v121
v_fmac_f32 v255, v121, v121
v_mfma_f32_16x16x32_bf16 v[104:107], a[72:75], v[132:135], v[104:107]
v_add_f32 v254, v254, v122
v_fmac_f32 v255, v122, v122
v_mfma_f32_16x16x32_bf16 v[108:111], a[76:79], v[132:135], v[108:111]
v_add_f32 v254, v254, v123
v_fmac_f32 v255, v123, v123
v_mfma_f32_16x16x32_bf16 v[128:131], v[76:79], v[132:135], v[128:131]
s_waitcnt vmcnt(9)
ds_read_b128 v[148:151], v19 offset:8192
ds_read_b128 v[152:155], v20 offset:8192
s_waitcnt lgkmcnt(4)
v_mfma_f32_16x16x32_bf16 v[96:99], a[80:83], v[136:139], v[96:99]
ds_read_b128 v[132:135], v25 offset:8192
global_load_lds_dwordx4 v17, s[42:43] offset:1024 sc1
v_mfma_f32_16x16x32_bf16 v[100:103], a[84:87], v[136:139], v[100:103]
v_add_f32 v254, v254, v124
v_fmac_f32 v255, v124, v124
v_mfma_f32_16x16x32_bf16 v[104:107], a[88:91], v[136:139], v[104:107]
v_add_f32 v254, v254, v125
v_fmac_f32 v255, v125, v125
v_mfma_f32_16x16x32_bf16 v[108:111], a[92:95], v[136:139], v[108:111]
v_add_f32 v254, v254, v126
v_fmac_f32 v255, v126, v126
v_mfma_f32_16x16x32_bf16 v[128:131], v[80:83], v[136:139], v[128:131]
s_waitcnt lgkmcnt(4)
v_add_f32 v254, v254, v127
v_fmac_f32 v255, v127, v127
v_mfma_f32_16x16x32_bf16 v[96:99], a[96:99], v[140:143], v[96:99]
s_add_u32 m0, s49, 0
ds_read_b128 v[136:139], v25 offset:9216
v_mfma_f32_16x16x32_bf16 v[100:103], a[100:103], v[140:143], v[100:103]
ds_write_b64 v27, v[254:255] offset:45056
v_mfma_f32_16x16x32_bf16 v[104:107], a[104:107], v[140:143], v[104:107]
s_waitcnt lgkmcnt(4)
v_lshlrev_b32 v112, 16, v148
v_and_b32 v113, 0xffff0000, v148
v_mfma_f32_16x16x32_bf16 v[108:111], a[108:111], v[140:143], v[108:111]
s_sub_u32 s42, s42, 18432
v_lshlrev_b32 v114, 16, v149
v_and_b32 v115, 0xffff0000, v149
v_mfma_f32_16x16x32_bf16 v[128:131], v[84:87], v[140:143], v[128:131]
v_lshlrev_b32 v116, 16, v150
v_and_b32 v117, 0xffff0000, v150
v_mfma_f32_16x16x32_bf16 v[96:99], a[112:115], v[144:147], v[96:99]
s_subb_u32 s43, s43, 0
ds_read_b128 v[140:143], v25 offset:10240
v_lshlrev_b32 v118, 16, v151
v_mfma_f32_16x16x32_bf16 v[100:103], a[116:119], v[144:147], v[100:103]
s_waitcnt lgkmcnt(4)
v_and_b32 v119, 0xffff0000, v151
v_lshlrev_b32 v120, 16, v152
v_mfma_f32_16x16x32_bf16 v[104:107], a[120:123], v[144:147], v[104:107]
v_and_b32 v121, 0xffff0000, v152
v_lshlrev_b32 v122, 16, v153
v_mfma_f32_16x16x32_bf16 v[108:111], a[124:127], v[144:147], v[108:111]
v_and_b32 v123, 0xffff0000, v153
v_lshlrev_b32 v124, 16, v154
v_mfma_f32_16x16x32_bf16 v[128:131], v[88:91], v[144:147], v[128:131]
s_waitcnt lgkmcnt(3)
v_and_b32 v125, 0xffff0000, v154
v_lshlrev_b32 v126, 16, v155
v_mfma_f32_16x16x32_bf16 v[96:99], a[128:131], v[132:135], v[96:99]
ds_read_b128 v[144:147], v25 offset:11264
v_and_b32 v127, 0xffff0000, v155
v_mfma_f32_16x16x32_bf16 v[100:103], a[132:135], v[132:135], v[100:103]
v_mfma_f32_16x16x32_bf16 v[104:107], a[136:139], v[132:135], v[104:107]
v_mfma_f32_16x16x32_bf16 v[108:111], a[140:143], v[132:135], v[108:111]
v_mfma_f32_16x16x32_bf16 v[128:131], v[92:95], v[132:135], v[128:131]
s_waitcnt lgkmcnt(3)
v_mfma_f32_16x16x32_bf16 v[96:99], a[144:147], v[136:139], v[96:99]
ds_read_b128 v[132:135], v25 offset:12288
v_mfma_f32_16x16x32_bf16 v[100:103], a[148:151], v[136:139], v[100:103]
v_mfma_f32_16x16x32_bf16 v[104:107], a[152:155], v[136:139], v[104:107]
v_mfma_f32_16x16x32_bf16 v[108:111], a[156:159], v[136:139], v[108:111]
s_waitcnt lgkmcnt(2)
v_mfma_f32_16x16x32_bf16 v[96:99], a[160:163], v[140:143], v[96:99]
ds_read_b128 v[136:139], v25 offset:13312
v_mfma_f32_16x16x32_bf16 v[100:103], a[164:167], v[140:143], v[100:103]
v_mfma_f32_16x16x32_bf16 v[104:107], a[168:171], v[140:143], v[104:107]
ds_write_b128 v26, v[128:131] offset:36864
v_mfma_f32_16x16x32_bf16 v[108:111], a[172:175], v[140:143], v[108:111]
s_waitcnt lgkmcnt(3)
v_mfma_f32_16x16x32_bf16 v[96:99], a[176:179], v[144:147], v[96:99]
ds_read_b128 v[140:143], v25 offset:14336
v_mfma_f32_16x16x32_bf16 v[100:103], a[180:183], v[144:147], v[100:103]
v_mfma_f32_16x16x32_bf16 v[104:107], a[184:187], v[144:147], v[104:107]
s_barrier
v_mfma_f32_16x16x32_bf16 v[108:111], a[188:191], v[144:147], v[108:111]
s_waitcnt lgkmcnt(3)
global_load_lds_dwordx4 v11, s[38:39] offset:-2048 sc1
v_mfma_f32_16x16x32_bf16 v[96:99], a[192:195], v[132:135], v[96:99]
ds_read_b128 v[144:147], v25 offset:15360
v_mfma_f32_16x16x32_bf16 v[100:103], a[196:199], v[132:135], v[100:103]
v_mfma_f32_16x16x32_bf16 v[104:107], a[200:203], v[132:135], v[104:107]
global_load_lds_dwordx4 v11, s[38:39] offset:-1024 sc1
v_mfma_f32_16x16x32_bf16 v[108:111], a[204:207], v[132:135], v[108:111]
s_waitcnt lgkmcnt(3)
v_mfma_f32_16x16x32_bf16 v[96:99], a[208:211], v[136:139], v[96:99]
ds_read_b128 v[132:135], v25 offset:16384
v_mfma_f32_16x16x32_bf16 v[100:103], a[212:215], v[136:139], v[100:103]
global_load_lds_dwordx4 v11, s[38:39] offset:0 sc1
v_mfma_f32_16x16x32_bf16 v[104:107], a[216:219], v[136:139], v[104:107]
v_mfma_f32_16x16x32_bf16 v[108:111], a[220:223], v[136:139], v[108:111]
s_waitcnt lgkmcnt(2)
v_mfma_f32_16x16x32_bf16 v[96:99], a[224:227], v[140:143], v[96:99]
ds_read_b128 v[136:139], v25 offset:17408
global_load_lds_dwordx4 v11, s[38:39] offset:1024 sc1
v_mfma_f32_16x16x32_bf16 v[100:103], a[228:231], v[140:143], v[100:103]
v_mfma_f32_16x16x32_bf16 v[104:107], a[232:235], v[140:143], v[104:107]
v_mfma_f32_16x16x32_bf16 v[108:111], a[236:239], v[140:143], v[108:111]
s_waitcnt lgkmcnt(2)
global_load_lds_dwordx4 v11, s[38:39] offset:2048 sc1
v_add_u32 v11, 18432, v11
v_mfma_f32_16x16x32_bf16 v[96:99], a[240:243], v[144:147], v[96:99]
v_min_u32 v11, v11, v12
v_mfma_f32_16x16x32_bf16 v[100:103], a[244:247], v[144:147], v[100:103]
v_mfma_f32_16x16x32_bf16 v[104:107], a[248:251], v[144:147], v[104:107]
s_waitcnt vmcnt(10)
s_barrier
v_mfma_f32_16x16x32_bf16 v[108:111], a[252:255], v[144:147], v[108:111]
s_waitcnt lgkmcnt(1)
ds_read_b128 v[140:143], v25 offset:18432
v_mfma_f32_16x16x32_bf16 v[96:99], v[28:31], v[132:135], v[96:99]
ds_read_b128 v[144:147], v25 offset:19456
v_mfma_f32_16x16x32_bf16 v[100:103], v[32:35], v[132:135], v[100:103]
v_mfma_f32_16x16x32_bf16 v[104:107], v[36:39], v[132:135], v[104:107]
v_mfma_f32_16x16x32_bf16 v[108:111], v[40:43], v[132:135], v[108:111]
s_waitcnt lgkmcnt(2)
v_mfma_f32_16x16x32_bf16 v[96:99], v[44:47], v[136:139], v[96:99]
ds_read_b128 v[132:135], v25 offset:20480
v_mfma_f32_16x16x32_bf16 v[100:103], v[48:51], v[136:139], v[100:103]
v_mfma_f32_16x16x32_bf16 v[104:107], v[52:55], v[136:139], v[104:107]
v_mfma_f32_16x16x32_bf16 v[108:111], v[56:59], v[136:139], v[108:111]
s_waitcnt lgkmcnt(2)
v_mfma_f32_16x16x32_bf16 v[112:115], a[0:3], v[140:143], v[112:115]
ds_read_b128 v[136:139], v25 offset:21504
v_mfma_f32_16x16x32_bf16 v[116:119], a[4:7], v[140:143], v[116:119]
v_mfma_f32_16x16x32_bf16 v[120:123], a[8:11], v[140:143], v[120:123]
s_waitcnt vmcnt(7)
s_barrier
v_mfma_f32_16x16x32_bf16 v[124:127], a[12:15], v[140:143], v[124:127]
s_mov_b64 exec, 1
global_store_dword v252, v24, s[38:39] sc1
s_mov_b64 exec, -1
v_add_u32 v24, 1, v24
v_add_lshl_u32 v252, v24, 0, 7
v_mfma_f32_16x16x32_bf16 v[128:131], v[60:63], v[140:143], 0
s_waitcnt lgkmcnt(2)
v_bfi_b32 v252, s69, v252, v23
v_cvt_pk_bf16_f32 v164, v96, v97
v_mfma_f32_16x16x32_bf16 v[112:115], a[16:19], v[144:147], v[112:115]
ds_read_b128 v[140:143], v25 offset:22528
v_cvt_pk_bf16_f32 v165, v98, v99
v_mfma_f32_16x16x32_bf16 v[116:119], a[20:23], v[144:147], v[116:119]
v_cvt_pk_bf16_f32 v166, v100, v101
v_cvt_pk_bf16_f32 v167, v102, v103
v_mfma_f32_16x16x32_bf16 v[120:123], a[24:27], v[144:147], v[120:123]
global_store_dwordx4 v13, v[164:167], s[38:39] offset:0 sc1
v_mfma_f32_16x16x32_bf16 v[124:127], a[28:31], v[144:147], v[124:127]
v_cvt_pk_bf16_f32 v168, v104, v105
v_cvt_pk_bf16_f32 v169, v106, v107
v_mfma_f32_16x16x32_bf16 v[128:131], v[64:67], v[144:147], v[128:131]
s_waitcnt lgkmcnt(2)
v_cvt_pk_bf16_f32 v170, v108, v109
v_cvt_pk_bf16_f32 v171, v110, v111
v_mfma_f32_16x16x32_bf16 v[112:115], a[32:35], v[132:135], v[112:115]
s_mov_b32 m0, 55296
ds_read_b128 v[144:147], v25 offset:23552
v_add_f32 v254, v96, v97
v_mfma_f32_16x16x32_bf16 v[116:119], a[36:39], v[132:135], v[116:119]
global_store_dwordx4 v13, v[168:171], s[38:39] offset:1024 sc1
v_mfma_f32_16x16x32_bf16 v[120:123], a[40:43], v[132:135], v[120:123]
v_add_u32 v13, 18688, v13
v_mul_f32 v255, v96, v96
v_mfma_f32_16x16x32_bf16 v[124:127], a[44:47], v[132:135], v[124:127]
v_fmac_f32 v255, v97, v97
v_add_f32 v254, v254, v98
v_mfma_f32_16x16x32_bf16 v[128:131], v[68:71], v[132:135], v[128:131]
s_waitcnt lgkmcnt(2)
v_fmac_f32 v255, v98, v98
v_add_f32 v254, v254, v99
v_mfma_f32_16x16x32_bf16 v[112:115], a[48:51], v[136:139], v[112:115]
ds_read_b128 v[132:135], v25 offset:24576
v_fmac_f32 v255, v99, v99
v_mfma_f32_16x16x32_bf16 v[116:119], a[52:55], v[136:139], v[116:119]
v_add_f32 v254, v254, v100
v_fmac_f32 v255, v100, v100
v_mfma_f32_16x16x32_bf16 v[120:123], a[56:59], v[136:139], v[120:123]
v_add_f32 v254, v254, v101
v_fmac_f32 v255, v101, v101
v_mfma_f32_16x16x32_bf16 v[124:127], a[60:63], v[136:139], v[124:127]
global_load_lds_dwordx4 v16, s[42:43] offset:0 sc1
v_add_f32 v254, v254, v102
v_mfma_f32_16x16x32_bf16 v[128:131], v[72:75], v[136:139], v[128:131]
s_waitcnt lgkmcnt(2)
v_fmac_f32 v255, v102, v102
v_add_f32 v254, v254, v103
v_mfma_f32_16x16x32_bf16 v[112:115], a[64:67], v[140:143], v[112:115]
ds_read_b128 v[136:139], v25 offset:25600
v_fmac_f32 v255, v103, v103
v_mfma_f32_16x16x32_bf16 v[116:119], a[68:71], v[140:143], v[116:119]
v_add_f32 v254, v254, v104
v_fmac_f32 v255, v104, v104
v_mfma_f32_16x16x32_bf16 v[120:123], a[72:75], v[140:143], v[120:123]
v_add_f32 v254, v254, v105
v_fmac_f32 v255, v105, v105
v_mfma_f32_16x16x32_bf16 v[124:127], a[76:79], v[140:143], v[124:127]
v_add_f32 v254, v254, v106
v_fmac_f32 v255, v106, v106
v_mfma_f32_16x16x32_bf16 v[128:131], v[76:79], v[140:143], v[128:131]
s_waitcnt vmcnt(9)
ds_read_b128 v[148:151], v19 offset:0
ds_read_b128 v[152:155], v20 offset:0
s_waitcnt lgkmcnt(4)
v_mfma_f32_16x16x32_bf16 v[112:115], a[80:83], v[144:147], v[112:115]
ds_read_b128 v[140:143], v25 offset:26624
global_load_lds_dwordx4 v17, s[42:43] offset:1024 sc1
v_mfma_f32_16x16x32_bf16 v[116:119], a[84:87], v[144:147], v[116:119]
v_add_f32 v254, v254, v107
v_fmac_f32 v255, v107, v107
v_mfma_f32_16x16x32_bf16 v[120:123], a[88:91], v[144:147], v[120:123]
v_add_f32 v254, v254, v108
v_fmac_f32 v255, v108, v108
v_mfma_f32_16x16x32_bf16 v[124:127], a[92:95], v[144:147], v[124:127]
v_add_f32 v254, v254, v109
v_fmac_f32 v255, v109, v109
v_mfma_f32_16x16x32_bf16 v[128:131], v[80:83], v[144:147], v[128:131]
s_waitcnt lgkmcnt(4)
v_add_f32 v254, v254, v110
v_fmac_f32 v255, v110, v110
v_mfma_f32_16x16x32_bf16 v[112:115], a[96:99], v[132:135], v[112:115]
s_add_u32 m0, s49, 18432
ds_read_b128 v[144:147], v25 offset:27648
v_add_f32 v254, v254, v111
v_mfma_f32_16x16x32_bf16 v[116:119], a[100:103], v[132:135], v[116:119]
v_fmac_f32 v255, v111, v111
v_mfma_f32_16x16x32_bf16 v[120:123], a[104:107], v[132:135], v[120:123]
ds_write_b64 v27, v[254:255] offset:43008
v_mfma_f32_16x16x32_bf16 v[124:127], a[108:111], v[132:135], v[124:127]
s_sub_u32 s42, s42, 18432
s_waitcnt lgkmcnt(4)
v_lshlrev_b32 v96, 16, v148
v_and_b32 v97, 0xffff0000, v148
v_mfma_f32_16x16x32_bf16 v[128:131], v[84:87], v[132:135], v[128:131]
v_lshlrev_b32 v98, 16, v149
v_and_b32 v99, 0xffff0000, v149
v_mfma_f32_16x16x32_bf16 v[112:115], a[112:115], v[136:139], v[112:115]
s_subb_u32 s43, s43, 0
ds_read_b128 v[132:135], v25 offset:28672
v_lshlrev_b32 v100, 16, v150
v_mfma_f32_16x16x32_bf16 v[116:119], a[116:119], v[136:139], v[116:119]
v_and_b32 v101, 0xffff0000, v150
v_lshlrev_b32 v102, 16, v151
v_mfma_f32_16x16x32_bf16 v[120:123], a[120:123], v[136:139], v[120:123]
s_waitcnt lgkmcnt(4)
v_and_b32 v103, 0xffff0000, v151
v_lshlrev_b32 v104, 16, v152
v_mfma_f32_16x16x32_bf16 v[124:127], a[124:127], v[136:139], v[124:127]
v_and_b32 v105, 0xffff0000, v152
v_lshlrev_b32 v106, 16, v153
v_mfma_f32_16x16x32_bf16 v[128:131], v[88:91], v[136:139], v[128:131]
s_waitcnt lgkmcnt(3)
v_and_b32 v107, 0xffff0000, v153
v_lshlrev_b32 v108, 16, v154
v_mfma_f32_16x16x32_bf16 v[112:115], a[128:131], v[140:143], v[112:115]
ds_read_b128 v[136:139], v25 offset:29696
v_and_b32 v109, 0xffff0000, v154
v_mfma_f32_16x16x32_bf16 v[116:119], a[132:135], v[140:143], v[116:119]
v_lshlrev_b32 v110, 16, v155
v_and_b32 v111, 0xffff0000, v155
v_mfma_f32_16x16x32_bf16 v[120:123], a[136:139], v[140:143], v[120:123]
v_mfma_f32_16x16x32_bf16 v[124:127], a[140:143], v[140:143], v[124:127]
v_mfma_f32_16x16x32_bf16 v[128:131], v[92:95], v[140:143], v[128:131]
s_waitcnt lgkmcnt(3)
v_mfma_f32_16x16x32_bf16 v[112:115], a[144:147], v[144:147], v[112:115]
ds_read_b128 v[140:143], v25 offset:30720
v_mfma_f32_16x16x32_bf16 v[116:119], a[148:151], v[144:147], v[116:119]
v_mfma_f32_16x16x32_bf16 v[120:123], a[152:155], v[144:147], v[120:123]
v_mfma_f32_16x16x32_bf16 v[124:127], a[156:159], v[144:147], v[124:127]
s_waitcnt lgkmcnt(2)
v_mfma_f32_16x16x32_bf16 v[112:115], a[160:163], v[132:135], v[112:115]
ds_read_b128 v[144:147], v25 offset:31744
v_mfma_f32_16x16x32_bf16 v[116:119], a[164:167], v[132:135], v[116:119]
v_mfma_f32_16x16x32_bf16 v[120:123], a[168:171], v[132:135], v[120:123]
ds_write_b128 v26, v[128:131] offset:39936
v_mfma_f32_16x16x32_bf16 v[124:127], a[172:175], v[132:135], v[124:127]
s_waitcnt lgkmcnt(3)
v_mfma_f32_16x16x32_bf16 v[112:115], a[176:179], v[136:139], v[112:115]
ds_read_b128 v[132:135], v25 offset:32768
v_mfma_f32_16x16x32_bf16 v[116:119], a[180:183], v[136:139], v[116:119]
v_mfma_f32_16x16x32_bf16 v[120:123], a[184:187], v[136:139], v[120:123]
s_barrier
v_mfma_f32_16x16x32_bf16 v[124:127], a[188:191], v[136:139], v[124:127]
s_waitcnt lgkmcnt(3)
global_load_lds_dwordx4 v11, s[38:39] offset:-2048 sc1
v_mfma_f32_16x16x32_bf16 v[112:115], a[192:195], v[140:143], v[112:115]
s_mov_b64 s[76:77], -1
ds_read_b128 v[136:139], v25 offset:33792
v_mfma_f32_16x16x32_bf16 v[116:119], a[196:199], v[140:143], v[116:119]
v_mfma_f32_16x16x32_bf16 v[120:123], a[200:203], v[140:143], v[120:123]
global_load_lds_dwordx4 v11, s[38:39] offset:-1024 sc1
v_mfma_f32_16x16x32_bf16 v[124:127], a[204:207], v[140:143], v[124:127]
s_waitcnt lgkmcnt(3)
v_mfma_f32_16x16x32_bf16 v[112:115], a[208:211], v[144:147], v[112:115]
ds_read_b128 v[140:143], v25 offset:34816
v_mfma_f32_16x16x32_bf16 v[116:119], a[212:215], v[144:147], v[116:119]
global_load_lds_dwordx4 v11, s[38:39] offset:0 sc1
v_mfma_f32_16x16x32_bf16 v[120:123], a[216:219], v[144:147], v[120:123]
v_mfma_f32_16x16x32_bf16 v[124:127], a[220:223], v[144:147], v[124:127]
s_waitcnt lgkmcnt(2)
v_mfma_f32_16x16x32_bf16 v[112:115], a[224:227], v[132:135], v[112:115]
ds_read_b128 v[144:147], v25 offset:35840
global_load_lds_dwordx4 v11, s[38:39] offset:1024 sc1
v_mfma_f32_16x16x32_bf16 v[116:119], a[228:231], v[132:135], v[116:119]
v_mfma_f32_16x16x32_bf16 v[120:123], a[232:235], v[132:135], v[120:123]
v_mfma_f32_16x16x32_bf16 v[124:127], a[236:239], v[132:135], v[124:127]
s_waitcnt lgkmcnt(2)
v_xor_b32 v25, 0x10000, v25
global_load_lds_dwordx4 v11, s[38:39] offset:2048 sc1
v_mfma_f32_16x16x32_bf16 v[112:115], a[240:243], v[136:139], v[112:115]
v_add_u32 v11, 18432, v11
v_min_u32 v11, v11, v12
v_mfma_f32_16x16x32_bf16 v[116:119], a[244:247], v[136:139], v[116:119]
v_mfma_f32_16x16x32_bf16 v[120:123], a[248:251], v[136:139], v[120:123]
s_waitcnt vmcnt(10)
s_barrier
v_mfma_f32_16x16x32_bf16 v[124:127], a[252:255], v[136:139], v[124:127]
s_waitcnt lgkmcnt(1)
ds_read_b128 v[132:135], v25 offset:0
v_mfma_f32_16x16x32_bf16 v[112:115], v[28:31], v[140:143], v[112:115]
ds_read_b128 v[136:139], v25 offset:1024
v_mfma_f32_16x16x32_bf16 v[116:119], v[32:35], v[140:143], v[116:119]
v_mfma_f32_16x16x32_bf16 v[120:123], v[36:39], v[140:143], v[120:123]
v_mfma_f32_16x16x32_bf16 v[124:127], v[40:43], v[140:143], v[124:127]
s_waitcnt lgkmcnt(2)
v_mfma_f32_16x16x32_bf16 v[112:115], v[44:47], v[144:147], v[112:115]
ds_read_b128 v[140:143], v25 offset:2048
v_mfma_f32_16x16x32_bf16 v[116:119], v[48:51], v[144:147], v[116:119]
v_mfma_f32_16x16x32_bf16 v[120:123], v[52:55], v[144:147], v[120:123]
v_mfma_f32_16x16x32_bf16 v[124:127], v[56:59], v[144:147], v[124:127]
s_sub_u32 s70, s70, 1
s_cbranch_scc0 .Lwo_top5
s_waitcnt vmcnt(7)
s_barrier
s_mov_b64 exec, 1
global_store_dword v252, v24, s[38:39] sc1
s_mov_b64 exec, -1
v_add_u32 v24, 1, v24
v_add_lshl_u32 v252, v24, 0, 7
v_bfi_b32 v252, s69, v252, v23
v_cvt_pk_bf16_f32 v164, v112, v113
v_cvt_pk_bf16_f32 v165, v114, v115
v_cvt_pk_bf16_f32 v166, v116, v117
v_cvt_pk_bf16_f32 v167, v118, v119
global_store_dwordx4 v13, v[164:167], s[38:39] offset:0 sc1
v_cvt_pk_bf16_f32 v168, v120, v121
v_cvt_pk_bf16_f32 v169, v122, v123
v_cvt_pk_bf16_f32 v170, v124, v125
v_cvt_pk_bf16_f32 v171, v126, v127
global_store_dwordx4 v13, v[168:171], s[38:39] offset:1024 sc1
v_add_f32 v254, v112, v113
v_mul_f32 v255, v112, v112
v_fmac_f32 v255, v113, v113
v_add_f32 v254, v254, v114
v_fmac_f32 v255, v114, v114
v_add_f32 v254, v254, v115
v_fmac_f32 v255, v115, v115
v_add_f32 v254, v254, v116
v_fmac_f32 v255, v116, v116
v_add_f32 v254, v254, v117
v_fmac_f32 v255, v117, v117
v_add_f32 v254, v254, v118
v_fmac_f32 v255, v118, v118
v_add_f32 v254, v254, v119
v_fmac_f32 v255, v119, v119
v_add_f32 v254, v254, v120
v_fmac_f32 v255, v120, v120
v_add_f32 v254, v254, v121
v_fmac_f32 v255, v121, v121
v_add_f32 v254, v254, v122
v_fmac_f32 v255, v122, v122
v_add_f32 v254, v254, v123
v_fmac_f32 v255, v123, v123
v_add_f32 v254, v254, v124
)"
R"(v_fmac_f32 v255, v124, v124
v_add_f32 v254, v254, v125
v_fmac_f32 v255, v125, v125
v_add_f32 v254, v254, v126
v_fmac_f32 v255, v126, v126
v_add_f32 v254, v254, v127
v_fmac_f32 v255, v127, v127
ds_write_b64 v27, v[254:255] offset:45056
s_waitcnt vmcnt(0) lgkmcnt(0)
s_barrier
s_mov_b64 exec, 1
global_store_dword v252, v24, s[38:39] sc1
s_mov_b64 exec, -1
s_waitcnt vmcnt(0)
s_barrier
v_add_u32 v24, 8, v24
v_mbcnt_lo_u32_b32 v252, -1, 0
v_mbcnt_hi_u32_b32 v252, -1, v252
v_add_lshl_u32 v252, v252, 0, 7
v_bfi_b32 v252, s69, v252, v23
s_mov_b64 exec, 0xff
global_store_dword v252, v24, s[38:39] sc1
s_mov_b64 exec, -1
s_endpgm
.Lwo_w2:
s_add_u32 m0, s48, 4096
s_nop 0
global_load_lds_dwordx4 v0, s[40:41] offset:-4096
global_load_lds_dwordx4 v1, s[40:41] offset:-3072
global_load_lds_dwordx4 v2, s[40:41] offset:-2048
global_load_lds_dwordx4 v3, s[40:41] offset:-1024
global_load_lds_dwordx4 v4, s[40:41] offset:0
global_load_lds_dwordx4 v5, s[40:41] offset:1024
global_load_lds_dwordx4 v6, s[40:41] offset:2048
global_load_lds_dwordx4 v7, s[40:41] offset:3072
s_add_u32 m0, s48, 12288
s_nop 0
global_load_lds_dwordx4 v8, s[40:41] offset:-4096
s_add_u32 s40, s40, 73728
s_addc_u32 s41, s41, 0
s_add_u32 m0, s48, 40960
s_nop 0
global_load_lds_dwordx4 v0, s[40:41] offset:-4096
global_load_lds_dwordx4 v1, s[40:41] offset:-3072
global_load_lds_dwordx4 v2, s[40:41] offset:-2048
global_load_lds_dwordx4 v3, s[40:41] offset:-1024
global_load_lds_dwordx4 v4, s[40:41] offset:0
global_load_lds_dwordx4 v5, s[40:41] offset:1024
global_load_lds_dwordx4 v6, s[40:41] offset:2048
global_load_lds_dwordx4 v7, s[40:41] offset:3072
s_add_u32 m0, s48, 49152
s_nop 0
global_load_lds_dwordx4 v8, s[40:41] offset:-4096
s_add_u32 s40, s40, 73728
s_addc_u32 s41, s41, 0
s_add_u32 m0, s48, 77824
s_nop 0
global_load_lds_dwordx4 v0, s[40:41] offset:-4096
global_load_lds_dwordx4 v1, s[40:41] offset:-3072
global_load_lds_dwordx4 v2, s[40:41] offset:-2048
global_load_lds_dwordx4 v3, s[40:41] offset:-1024
global_load_lds_dwordx4 v4, s[40:41] offset:0
global_load_lds_dwordx4 v5, s[40:41] offset:1024
global_load_lds_dwordx4 v6, s[40:41] offset:2048
global_load_lds_dwordx4 v7, s[40:41] offset:3072
s_add_u32 m0, s48, 86016
s_nop 0
global_load_lds_dwordx4 v8, s[40:41] offset:-4096
s_add_u32 s40, s40, 73728
s_addc_u32 s41, s41, 0
v_add_u32 v204, 0x10000, v9
v_add_u32 v205, 0x20000, v9
v_add_u32 v206, 0x10000, v10
v_add_u32 v207, 0x20000, v10
s_waitcnt lgkmcnt(0)
s_mul_i32 s37, s37, 0x9E3779B1
s_add_u32 s37, s37, 0x7F4A7C15
v_add_u32 v22, s37, v22
v_add_u32 v24, s37, v24
s_waitcnt vmcnt(18)
s_barrier
s_add_u32 m0, s48, 114688
ds_read_b64_tr_b16 a[0:1], v9 offset:0
ds_read_b64_tr_b16 a[2:3], v9 offset:2304
global_load_lds_dwordx4 v0, s[40:41] offset:-4096
ds_read_b64_tr_b16 a[4:5], v9 offset:8
global_load_lds_dwordx4 v1, s[40:41] offset:-3072
ds_read_b64_tr_b16 a[6:7], v9 offset:2312
global_load_lds_dwordx4 v2, s[40:41] offset:-2048
ds_read_b64_tr_b16 a[8:9], v9 offset:64
global_load_lds_dwordx4 v3, s[40:41] offset:-1024
ds_read_b64_tr_b16 a[10:11], v9 offset:2368
ds_read_b64_tr_b16 a[12:13], v9 offset:72
global_load_lds_dwordx4 v4, s[40:41] offset:0
ds_read_b64_tr_b16 a[14:15], v9 offset:2376
global_load_lds_dwordx4 v5, s[40:41] offset:1024
ds_read_b64_tr_b16 a[16:17], v9 offset:18432
global_load_lds_dwordx4 v6, s[40:41] offset:2048
ds_read_b64_tr_b16 a[18:19], v9 offset:20736
global_load_lds_dwordx4 v7, s[40:41] offset:3072
ds_read_b64_tr_b16 a[20:21], v9 offset:18440
ds_read_b64_tr_b16 a[22:23], v9 offset:20744
s_add_u32 m0, s48, 122880
ds_read_b64_tr_b16 a[24:25], v9 offset:18496
global_load_lds_dwordx4 v8, s[40:41] offset:-4096
ds_read_b64_tr_b16 a[26:27], v9 offset:20800
s_add_u32 s40, s40, 73728
ds_read_b64_tr_b16 a[28:29], v9 offset:18504
s_addc_u32 s41, s41, 0
ds_read_b64_tr_b16 a[30:31], v9 offset:20808
s_waitcnt vmcnt(18) lgkmcnt(0)
s_barrier
s_add_u32 m0, s48, 4096
ds_read_b64_tr_b16 a[32:33], v9 offset:36864
ds_read_b64_tr_b16 a[34:35], v9 offset:39168
global_load_lds_dwordx4 v0, s[40:41] offset:-4096
ds_read_b64_tr_b16 a[36:37], v9 offset:36872
global_load_lds_dwordx4 v1, s[40:41] offset:-3072
ds_read_b64_tr_b16 a[38:39], v9 offset:39176
global_load_lds_dwordx4 v2, s[40:41] offset:-2048
ds_read_b64_tr_b16 a[40:41], v9 offset:36928
global_load_lds_dwordx4 v3, s[40:41] offset:-1024
ds_read_b64_tr_b16 a[42:43], v9 offset:39232
ds_read_b64_tr_b16 a[44:45], v9 offset:36936
global_load_lds_dwordx4 v4, s[40:41] offset:0
ds_read_b64_tr_b16 a[46:47], v9 offset:39240
global_load_lds_dwordx4 v5, s[40:41] offset:1024
ds_read_b64_tr_b16 a[48:49], v9 offset:55296
global_load_lds_dwordx4 v6, s[40:41] offset:2048
ds_read_b64_tr_b16 a[50:51], v9 offset:57600
global_load_lds_dwordx4 v7, s[40:41] offset:3072
ds_read_b64_tr_b16 a[52:53], v9 offset:55304
ds_read_b64_tr_b16 a[54:55], v9 offset:57608
s_add_u32 m0, s48, 12288
ds_read_b64_tr_b16 a[56:57], v9 offset:55360
global_load_lds_dwordx4 v8, s[40:41] offset:-4096
ds_read_b64_tr_b16 a[58:59], v9 offset:57664
s_add_u32 s40, s40, 73728
ds_read_b64_tr_b16 a[60:61], v9 offset:55368
s_addc_u32 s41, s41, 0
ds_read_b64_tr_b16 a[62:63], v9 offset:57672
s_waitcnt vmcnt(18) lgkmcnt(0)
s_barrier
s_add_u32 m0, s48, 40960
ds_read_b64_tr_b16 a[64:65], v204 offset:8192
ds_read_b64_tr_b16 a[66:67], v204 offset:10496
global_load_lds_dwordx4 v0, s[40:41] offset:-4096
ds_read_b64_tr_b16 a[68:69], v204 offset:8200
global_load_lds_dwordx4 v1, s[40:41] offset:-3072
ds_read_b64_tr_b16 a[70:71], v204 offset:10504
global_load_lds_dwordx4 v2, s[40:41] offset:-2048
ds_read_b64_tr_b16 a[72:73], v204 offset:8256
global_load_lds_dwordx4 v3, s[40:41] offset:-1024
ds_read_b64_tr_b16 a[74:75], v204 offset:10560
ds_read_b64_tr_b16 a[76:77], v204 offset:8264
global_load_lds_dwordx4 v4, s[40:41] offset:0
ds_read_b64_tr_b16 a[78:79], v204 offset:10568
global_load_lds_dwordx4 v5, s[40:41] offset:1024
ds_read_b64_tr_b16 a[80:81], v204 offset:26624
global_load_lds_dwordx4 v6, s[40:41] offset:2048
ds_read_b64_tr_b16 a[82:83], v204 offset:28928
global_load_lds_dwordx4 v7, s[40:41] offset:3072
ds_read_b64_tr_b16 a[84:85], v204 offset:26632
ds_read_b64_tr_b16 a[86:87], v204 offset:28936
s_add_u32 m0, s48, 49152
ds_read_b64_tr_b16 a[88:89], v204 offset:26688
global_load_lds_dwordx4 v8, s[40:41] offset:-4096
ds_read_b64_tr_b16 a[90:91], v204 offset:28992
s_add_u32 s40, s40, 73728
ds_read_b64_tr_b16 a[92:93], v204 offset:26696
s_addc_u32 s41, s41, 0
ds_read_b64_tr_b16 a[94:95], v204 offset:29000
s_waitcnt vmcnt(18) lgkmcnt(0)
s_barrier
s_add_u32 m0, s48, 77824
ds_read_b64_tr_b16 a[96:97], v204 offset:45056
ds_read_b64_tr_b16 a[98:99], v204 offset:47360
global_load_lds_dwordx4 v0, s[40:41] offset:-4096
ds_read_b64_tr_b16 a[100:101], v204 offset:45064
global_load_lds_dwordx4 v1, s[40:41] offset:-3072
ds_read_b64_tr_b16 a[102:103], v204 offset:47368
global_load_lds_dwordx4 v2, s[40:41] offset:-2048
ds_read_b64_tr_b16 a[104:105], v204 offset:45120
global_load_lds_dwordx4 v3, s[40:41] offset:-1024
ds_read_b64_tr_b16 a[106:107], v204 offset:47424
ds_read_b64_tr_b16 a[108:109], v204 offset:45128
global_load_lds_dwordx4 v4, s[40:41] offset:0
ds_read_b64_tr_b16 a[110:111], v204 offset:47432
global_load_lds_dwordx4 v5, s[40:41] offset:1024
ds_read_b64_tr_b16 a[112:113], v204 offset:63488
global_load_lds_dwordx4 v6, s[40:41] offset:2048
ds_read_b64_tr_b16 a[114:115], v205 offset:256
global_load_lds_dwordx4 v7, s[40:41] offset:3072
ds_read_b64_tr_b16 a[116:117], v204 offset:63496
ds_read_b64_tr_b16 a[118:119], v205 offset:264
s_add_u32 m0, s48, 86016
ds_read_b64_tr_b16 a[120:121], v204 offset:63552
global_load_lds_dwordx4 v8, s[40:41] offset:-4096
ds_read_b64_tr_b16 a[122:123], v205 offset:320
s_add_u32 s40, s40, 73728
ds_read_b64_tr_b16 a[124:125], v204 offset:63560
s_addc_u32 s41, s41, 0
ds_read_b64_tr_b16 a[126:127], v205 offset:328
s_waitcnt vmcnt(18) lgkmcnt(0)
s_barrier
s_add_u32 m0, s48, 114688
ds_read_b64_tr_b16 a[128:129], v9 offset:0
ds_read_b64_tr_b16 a[130:131], v9 offset:2304
global_load_lds_dwordx4 v0, s[40:41] offset:-4096
ds_read_b64_tr_b16 a[132:133], v9 offset:8
global_load_lds_dwordx4 v1, s[40:41] offset:-3072
ds_read_b64_tr_b16 a[134:135], v9 offset:2312
ds_read_b64_tr_b16 a[136:137], v9 offset:64
global_load_lds_dwordx4 v2, s[40:41] offset:-2048
ds_read_b64_tr_b16 a[138:139], v9 offset:2368
global_load_lds_dwordx4 v3, s[40:41] offset:-1024
ds_read_b64_tr_b16 a[140:141], v9 offset:72
global_load_lds_dwordx4 v4, s[40:41] offset:0
ds_read_b64_tr_b16 a[142:143], v9 offset:2376
ds_read_b64_tr_b16 a[144:145], v9 offset:18432
global_load_lds_dwordx4 v5, s[40:41] offset:1024
ds_read_b64_tr_b16 a[146:147], v9 offset:20736
global_load_lds_dwordx4 v6, s[40:41] offset:2048
ds_read_b64_tr_b16 a[148:149], v9 offset:18440
ds_read_b64_tr_b16 a[150:151], v9 offset:20744
global_load_lds_dwordx4 v7, s[40:41] offset:3072
ds_read_b64_tr_b16 a[152:153], v9 offset:18496
s_add_u32 m0, s48, 122880
ds_read_b64_tr_b16 a[154:155], v9 offset:20800
global_load_lds_dwordx4 v8, s[40:41] offset:-4096
ds_read_b64_tr_b16 a[156:157], v9 offset:18504
ds_read_b64_tr_b16 a[158:159], v9 offset:20808
s_add_u32 s40, s40, 73728
ds_read_b64_tr_b16 v[60:61], v10 offset:18432
s_addc_u32 s41, s41, 0
ds_read_b64_tr_b16 v[62:63], v10 offset:20736
s_waitcnt vmcnt(18) lgkmcnt(0)
s_barrier
s_add_u32 m0, s48, 4096
ds_read_b64_tr_b16 a[160:161], v9 offset:36864
ds_read_b64_tr_b16 a[162:163], v9 offset:39168
global_load_lds_dwordx4 v0, s[40:41] offset:-4096
ds_read_b64_tr_b16 a[164:165], v9 offset:36872
ds_read_b64_tr_b16 a[166:167], v9 offset:39176
global_load_lds_dwordx4 v1, s[40:41] offset:-3072
ds_read_b64_tr_b16 a[168:169], v9 offset:36928
global_load_lds_dwordx4 v2, s[40:41] offset:-2048
ds_read_b64_tr_b16 a[170:171], v9 offset:39232
ds_read_b64_tr_b16 a[172:173], v9 offset:36936
global_load_lds_dwordx4 v3, s[40:41] offset:-1024
ds_read_b64_tr_b16 a[174:175], v9 offset:39240
global_load_lds_dwordx4 v4, s[40:41] offset:0
ds_read_b64_tr_b16 v[64:65], v10 offset:36864
ds_read_b64_tr_b16 v[66:67], v10 offset:39168
global_load_lds_dwordx4 v5, s[40:41] offset:1024
ds_read_b64_tr_b16 a[176:177], v9 offset:55296
global_load_lds_dwordx4 v6, s[40:41] offset:2048
ds_read_b64_tr_b16 a[178:179], v9 offset:57600
ds_read_b64_tr_b16 a[180:181], v9 offset:55304
global_load_lds_dwordx4 v7, s[40:41] offset:3072
ds_read_b64_tr_b16 a[182:183], v9 offset:57608
s_add_u32 m0, s48, 12288
ds_read_b64_tr_b16 a[184:185], v9 offset:55360
ds_read_b64_tr_b16 a[186:187], v9 offset:57664
global_load_lds_dwordx4 v8, s[40:41] offset:-4096
ds_read_b64_tr_b16 a[188:189], v9 offset:55368
s_sub_u32 s40, s40, 589824
ds_read_b64_tr_b16 a[190:191], v9 offset:57672
ds_read_b64_tr_b16 v[68:69], v10 offset:55296
s_subb_u32 s41, s41, 0
ds_read_b64_tr_b16 v[70:71], v10 offset:57600
s_waitcnt vmcnt(18) lgkmcnt(0)
s_barrier
ds_read_b64_tr_b16 a[192:193], v204 offset:8192
ds_read_b64_tr_b16 a[194:195], v204 offset:10496
ds_read_b64_tr_b16 a[196:197], v204 offset:8200
ds_read_b64_tr_b16 a[198:199], v204 offset:10504
ds_read_b64_tr_b16 a[200:201], v204 offset:8256
ds_read_b64_tr_b16 a[202:203], v204 offset:10560
ds_read_b64_tr_b16 a[204:205], v204 offset:8264
ds_read_b64_tr_b16 a[206:207], v204 offset:10568
ds_read_b64_tr_b16 v[72:73], v206 offset:8192
ds_read_b64_tr_b16 v[74:75], v206 offset:10496
ds_read_b64_tr_b16 a[208:209], v204 offset:26624
ds_read_b64_tr_b16 a[210:211], v204 offset:28928
ds_read_b64_tr_b16 a[212:213], v204 offset:26632
ds_read_b64_tr_b16 a[214:215], v204 offset:28936
ds_read_b64_tr_b16 a[216:217], v204 offset:26688
ds_read_b64_tr_b16 a[218:219], v204 offset:28992
ds_read_b64_tr_b16 a[220:221], v204 offset:26696
ds_read_b64_tr_b16 a[222:223], v204 offset:29000
ds_read_b64_tr_b16 v[76:77], v206 offset:26624
ds_read_b64_tr_b16 v[78:79], v206 offset:28928
s_waitcnt vmcnt(9) lgkmcnt(0)
s_barrier
ds_read_b64_tr_b16 a[224:225], v204 offset:45056
ds_read_b64_tr_b16 a[226:227], v204 offset:47360
ds_read_b64_tr_b16 a[228:229], v204 offset:45064
ds_read_b64_tr_b16 a[230:231], v204 offset:47368
ds_read_b64_tr_b16 a[232:233], v204 offset:45120
ds_read_b64_tr_b16 a[234:235], v204 offset:47424
ds_read_b64_tr_b16 a[236:237], v204 offset:45128
ds_read_b64_tr_b16 a[238:239], v204 offset:47432
ds_read_b64_tr_b16 v[80:81], v206 offset:45056
ds_read_b64_tr_b16 v[82:83], v206 offset:47360
ds_read_b64_tr_b16 a[240:241], v204 offset:63488
ds_read_b64_tr_b16 a[242:243], v205 offset:256
ds_read_b64_tr_b16 a[244:245], v204 offset:63496
ds_read_b64_tr_b16 a[246:247], v205 offset:264
ds_read_b64_tr_b16 a[248:249], v204 offset:63552
ds_read_b64_tr_b16 a[250:251], v205 offset:320
ds_read_b64_tr_b16 a[252:253], v204 offset:63560
ds_read_b64_tr_b16 a[254:255], v205 offset:328
ds_read_b64_tr_b16 v[84:85], v206 offset:63488
ds_read_b64_tr_b16 v[86:87], v207 offset:256
s_waitcnt vmcnt(0) lgkmcnt(0)
s_barrier
ds_read_b64_tr_b16 v[28:29], v9 offset:0
ds_read_b64_tr_b16 v[30:31], v9 offset:2304
ds_read_b64_tr_b16 v[32:33], v9 offset:8
ds_read_b64_tr_b16 v[34:35], v9 offset:2312
ds_read_b64_tr_b16 v[36:37], v9 offset:64
ds_read_b64_tr_b16 v[38:39], v9 offset:2368
ds_read_b64_tr_b16 v[40:41], v9 offset:72
ds_read_b64_tr_b16 v[42:43], v9 offset:2376
ds_read_b64_tr_b16 v[88:89], v10 offset:0
ds_read_b64_tr_b16 v[90:91], v10 offset:2304
ds_read_b64_tr_b16 v[44:45], v9 offset:18432
ds_read_b64_tr_b16 v[46:47], v9 offset:20736
ds_read_b64_tr_b16 v[48:49], v9 offset:18440
ds_read_b64_tr_b16 v[50:51], v9 offset:20744
ds_read_b64_tr_b16 v[52:53], v9 offset:18496
ds_read_b64_tr_b16 v[54:55], v9 offset:20800
ds_read_b64_tr_b16 v[56:57], v9 offset:18504
ds_read_b64_tr_b16 v[58:59], v9 offset:20808
ds_read_b64_tr_b16 v[92:93], v10 offset:18432
ds_read_b64_tr_b16 v[94:95], v10 offset:20736
s_waitcnt lgkmcnt(0)
s_barrier
v_add_lshl_u32 v252, v24, 0, 7
v_bfi_b32 v252, s69, v252, v23
v_add_lshl_u32 v253, v22, 0, 7
v_bfi_b32 v253, s69, v253, v21
s_mov_b64 s[76:77], 0
s_add_u32 m0, s49, 18432
s_mov_b32 s70, 33
s_waitcnt vmcnt(0) lgkmcnt(0)
s_nop 7
.p2align 6
.Lwo_top11:
s_waitcnt lgkmcnt(2)
v_mfma_f32_16x16x32_bf16 v[96:99], a[144:147], v[132:135], v[96:99]
ds_read_b128 v[144:147], v25 offset:12288
v_mfma_f32_16x16x32_bf16 v[100:103], a[148:151], v[132:135], v[100:103]
v_mfma_f32_16x16x32_bf16 v[104:107], a[152:155], v[132:135], v[104:107]
s_waitcnt vmcnt(8)
s_barrier
v_mfma_f32_16x16x32_bf16 v[108:111], a[156:159], v[132:135], v[108:111]
v_cvt_pk_bf16_f32 v164, v112, v113
v_cvt_pk_bf16_f32 v165, v114, v115
v_mfma_f32_16x16x32_bf16 v[128:131], v[60:63], v[132:135], 0
s_waitcnt lgkmcnt(2)
v_cvt_pk_bf16_f32 v166, v116, v117
v_cvt_pk_bf16_f32 v167, v118, v119
v_mfma_f32_16x16x32_bf16 v[96:99], a[160:163], v[136:139], v[96:99]
ds_read_b128 v[132:135], v25 offset:13312
v_add_f32 v254, v112, v113
v_mfma_f32_16x16x32_bf16 v[100:103], a[164:167], v[136:139], v[100:103]
global_store_dwordx4 v13, v[164:167], s[38:39] offset:0 sc1
v_mfma_f32_16x16x32_bf16 v[104:107], a[168:171], v[136:139], v[104:107]
v_cvt_pk_bf16_f32 v168, v120, v121
v_cvt_pk_bf16_f32 v169, v122, v123
v_mfma_f32_16x16x32_bf16 v[108:111], a[172:175], v[136:139], v[108:111]
v_cvt_pk_bf16_f32 v170, v124, v125
v_cvt_pk_bf16_f32 v171, v126, v127
v_mfma_f32_16x16x32_bf16 v[128:131], v[64:67], v[136:139], v[128:131]
s_waitcnt lgkmcnt(2)
v_mul_f32 v255, v112, v112
v_fmac_f32 v255, v113, v113
v_mfma_f32_16x16x32_bf16 v[96:99], a[176:179], v[140:143], v[96:99]
s_cmp_eq_u32 s70, 32
ds_read_b128 v[136:139], v25 offset:14336
v_add_f32 v254, v254, v114
v_mfma_f32_16x16x32_bf16 v[100:103], a[180:183], v[140:143], v[100:103]
s_cselect_b64 s[42:43], s[44:45], s[42:43]
global_store_dwordx4 v13, v[168:171], s[38:39] offset:1024 sc1
v_mfma_f32_16x16x32_bf16 v[104:107], a[184:187], v[140:143], v[104:107]
s_cmp_eq_u32 s70, 16
v_add_u32 v13, 18688, v13
v_fmac_f32 v255, v114, v114
v_mfma_f32_16x16x32_bf16 v[108:111], a[188:191], v[140:143], v[108:111]
s_cselect_b64 s[42:43], s[46:47], s[42:43]
v_add_f32 v254, v254, v115
v_fmac_f32 v255, v115, v115
v_mfma_f32_16x16x32_bf16 v[128:131], v[68:71], v[140:143], v[128:131]
s_waitcnt lgkmcnt(2)
v_add_f32 v254, v254, v116
v_fmac_f32 v255, v116, v116
v_mfma_f32_16x16x32_bf16 v[96:99], a[192:195], v[144:147], v[96:99]
s_xor_b32 s49, s49, 0x10000
)"
R"(ds_read_b128 v[140:143], v25 offset:15360
v_add_f32 v254, v254, v117
v_mfma_f32_16x16x32_bf16 v[100:103], a[196:199], v[144:147], v[100:103]
s_mov_b32 m0, 49152
v_fmac_f32 v255, v117, v117
v_add_f32 v254, v254, v118
v_mfma_f32_16x16x32_bf16 v[104:107], a[200:203], v[144:147], v[104:107]
v_fmac_f32 v255, v118, v118
v_add_f32 v254, v254, v119
v_mfma_f32_16x16x32_bf16 v[108:111], a[204:207], v[144:147], v[108:111]
v_fmac_f32 v255, v119, v119
v_add_f32 v254, v254, v120
v_mfma_f32_16x16x32_bf16 v[128:131], v[72:75], v[144:147], v[128:131]
s_waitcnt lgkmcnt(2)
v_fmac_f32 v255, v120, v120
v_add_f32 v254, v254, v121
v_mfma_f32_16x16x32_bf16 v[96:99], a[208:211], v[132:135], v[96:99]
ds_read_b128 v[144:147], v25 offset:16384
global_load_lds_dwordx4 v16, s[42:43] offset:0 sc1
v_mfma_f32_16x16x32_bf16 v[100:103], a[212:215], v[132:135], v[100:103]
v_fmac_f32 v255, v121, v121
v_add_f32 v254, v254, v122
v_mfma_f32_16x16x32_bf16 v[104:107], a[216:219], v[132:135], v[104:107]
v_fmac_f32 v255, v122, v122
v_add_f32 v254, v254, v123
v_mfma_f32_16x16x32_bf16 v[108:111], a[220:223], v[132:135], v[108:111]
v_fmac_f32 v255, v123, v123
v_add_f32 v254, v254, v124
v_mfma_f32_16x16x32_bf16 v[128:131], v[76:79], v[132:135], v[128:131]
s_waitcnt vmcnt(9)
ds_read_b128 v[148:151], v19 offset:8192
ds_read_b128 v[152:155], v20 offset:8192
s_waitcnt lgkmcnt(4)
v_mfma_f32_16x16x32_bf16 v[96:99], a[224:227], v[136:139], v[96:99]
ds_read_b128 v[132:135], v25 offset:17408
v_fmac_f32 v255, v124, v124
v_mfma_f32_16x16x32_bf16 v[100:103], a[228:231], v[136:139], v[100:103]
v_add_f32 v254, v254, v125
v_fmac_f32 v255, v125, v125
v_mfma_f32_16x16x32_bf16 v[104:107], a[232:235], v[136:139], v[104:107]
global_load_lds_dwordx4 v17, s[42:43] offset:1024 sc1
v_add_f32 v254, v254, v126
v_mfma_f32_16x16x32_bf16 v[108:111], a[236:239], v[136:139], v[108:111]
v_fmac_f32 v255, v126, v126
v_add_f32 v254, v254, v127
v_mfma_f32_16x16x32_bf16 v[128:131], v[80:83], v[136:139], v[128:131]
s_waitcnt lgkmcnt(4)
v_fmac_f32 v255, v127, v127
v_mfma_f32_16x16x32_bf16 v[96:99], a[240:243], v[140:143], v[96:99]
ds_read_b128 v[136:139], v25 offset:0
v_mfma_f32_16x16x32_bf16 v[100:103], a[244:247], v[140:143], v[100:103]
ds_write_b64 v27, v[254:255] offset:45568
v_mfma_f32_16x16x32_bf16 v[104:107], a[248:251], v[140:143], v[104:107]
s_add_u32 m0, s49, 0
s_waitcnt lgkmcnt(4)
v_lshlrev_b32 v112, 16, v148
v_and_b32 v113, 0xffff0000, v148
v_mfma_f32_16x16x32_bf16 v[108:111], a[252:255], v[140:143], v[108:111]
v_lshlrev_b32 v114, 16, v149
v_and_b32 v115, 0xffff0000, v149
v_mfma_f32_16x16x32_bf16 v[128:131], v[84:87], v[140:143], v[128:131]
v_lshlrev_b32 v116, 16, v150
v_and_b32 v117, 0xffff0000, v150
v_mfma_f32_16x16x32_bf16 v[96:99], v[28:31], v[144:147], v[96:99]
s_sub_u32 s42, s42, 18432
ds_read_b128 v[140:143], v25 offset:1024
v_lshlrev_b32 v118, 16, v151
v_mfma_f32_16x16x32_bf16 v[100:103], v[32:35], v[144:147], v[100:103]
s_subb_u32 s43, s43, 0
v_and_b32 v119, 0xffff0000, v151
s_waitcnt lgkmcnt(4)
v_lshlrev_b32 v120, 16, v152
v_mfma_f32_16x16x32_bf16 v[104:107], v[36:39], v[144:147], v[104:107]
v_and_b32 v121, 0xffff0000, v152
v_lshlrev_b32 v122, 16, v153
v_mfma_f32_16x16x32_bf16 v[108:111], v[40:43], v[144:147], v[108:111]
v_and_b32 v123, 0xffff0000, v153
v_lshlrev_b32 v124, 16, v154
v_mfma_f32_16x16x32_bf16 v[128:131], v[88:91], v[144:147], v[128:131]
s_waitcnt lgkmcnt(3)
v_and_b32 v125, 0xffff0000, v154
v_lshlrev_b32 v126, 16, v155
v_mfma_f32_16x16x32_bf16 v[96:99], v[44:47], v[132:135], v[96:99]
ds_read_b128 v[144:147], v25 offset:2048
v_and_b32 v127, 0xffff0000, v155
v_mfma_f32_16x16x32_bf16 v[100:103], v[48:51], v[132:135], v[100:103]
v_mfma_f32_16x16x32_bf16 v[104:107], v[52:55], v[132:135], v[104:107]
v_mfma_f32_16x16x32_bf16 v[108:111], v[56:59], v[132:135], v[108:111]
v_mfma_f32_16x16x32_bf16 v[128:131], v[92:95], v[132:135], v[128:131]
s_waitcnt lgkmcnt(3)
v_mfma_f32_16x16x32_bf16 v[96:99], a[0:3], v[136:139], v[96:99]
ds_read_b128 v[132:135], v25 offset:3072
v_mfma_f32_16x16x32_bf16 v[100:103], a[4:7], v[136:139], v[100:103]
v_mfma_f32_16x16x32_bf16 v[104:107], a[8:11], v[136:139], v[104:107]
v_mfma_f32_16x16x32_bf16 v[108:111], a[12:15], v[136:139], v[108:111]
s_waitcnt lgkmcnt(2)
v_mfma_f32_16x16x32_bf16 v[96:99], a[16:19], v[140:143], v[96:99]
ds_read_b128 v[136:139], v25 offset:4096
v_mfma_f32_16x16x32_bf16 v[100:103], a[20:23], v[140:143], v[100:103]
s_waitcnt vmcnt(6)
v_sub_u32 v200, v200, v22
v_cmp_lt_u32 vcc, 0x7f, v200
v_mfma_f32_16x16x32_bf16 v[104:107], a[24:27], v[140:143], v[104:107]
v_mfma_f32_16x16x32_bf16 v[108:111], a[28:31], v[140:143], v[108:111]
s_waitcnt lgkmcnt(2)
v_mfma_f32_16x16x32_bf16 v[96:99], a[32:35], v[144:147], v[96:99]
s_and_b64 vcc, vcc, s[76:77]
s_cbranch_vccnz .Lwo_sp7
.Lwo_rt8:
ds_read_b128 v[140:143], v25 offset:5120
v_mfma_f32_16x16x32_bf16 v[100:103], a[36:39], v[144:147], v[100:103]
v_mfma_f32_16x16x32_bf16 v[104:107], a[40:43], v[144:147], v[104:107]
s_barrier
v_mfma_f32_16x16x32_bf16 v[108:111], a[44:47], v[144:147], v[108:111]
s_waitcnt lgkmcnt(2)
ds_write_b128 v26, v[128:131] offset:37888
v_mfma_f32_16x16x32_bf16 v[96:99], a[48:51], v[132:135], v[96:99]
ds_read_b128 v[144:147], v25 offset:6144
v_add_u32 v22, 1, v22
v_mfma_f32_16x16x32_bf16 v[100:103], a[52:55], v[132:135], v[100:103]
v_add_lshl_u32 v253, v22, 0, 7
v_bfi_b32 v253, s69, v253, v21
v_mfma_f32_16x16x32_bf16 v[104:107], a[56:59], v[132:135], v[104:107]
global_load_lds_dwordx4 v11, s[38:39] offset:-2048 sc1
v_mfma_f32_16x16x32_bf16 v[108:111], a[60:63], v[132:135], v[108:111]
s_waitcnt lgkmcnt(3)
v_mfma_f32_16x16x32_bf16 v[96:99], a[64:67], v[136:139], v[96:99]
ds_read_b128 v[132:135], v25 offset:7168
v_mfma_f32_16x16x32_bf16 v[100:103], a[68:71], v[136:139], v[100:103]
global_load_lds_dwordx4 v11, s[38:39] offset:-1024 sc1
v_mfma_f32_16x16x32_bf16 v[104:107], a[72:75], v[136:139], v[104:107]
v_mfma_f32_16x16x32_bf16 v[108:111], a[76:79], v[136:139], v[108:111]
s_waitcnt lgkmcnt(3)
v_mfma_f32_16x16x32_bf16 v[96:99], a[80:83], v[140:143], v[96:99]
ds_read_b128 v[136:139], v25 offset:8192
global_load_lds_dwordx4 v11, s[38:39] offset:0 sc1
v_mfma_f32_16x16x32_bf16 v[100:103], a[84:87], v[140:143], v[100:103]
v_mfma_f32_16x16x32_bf16 v[104:107], a[88:91], v[140:143], v[104:107]
v_mfma_f32_16x16x32_bf16 v[108:111], a[92:95], v[140:143], v[108:111]
s_waitcnt lgkmcnt(2)
global_load_dword v201, v253, s[38:39] sc1
global_load_lds_dwordx4 v11, s[38:39] offset:1024 sc1
v_mfma_f32_16x16x32_bf16 v[96:99], a[96:99], v[144:147], v[96:99]
v_mfma_f32_16x16x32_bf16 v[100:103], a[100:103], v[144:147], v[100:103]
v_mfma_f32_16x16x32_bf16 v[104:107], a[104:107], v[144:147], v[104:107]
s_waitcnt vmcnt(9)
s_barrier
v_mfma_f32_16x16x32_bf16 v[108:111], a[108:111], v[144:147], v[108:111]
s_waitcnt lgkmcnt(1)
ds_read_b128 v[140:143], v25 offset:27648
global_load_lds_dwordx4 v11, s[38:39] offset:2048 sc1
v_mfma_f32_16x16x32_bf16 v[96:99], a[112:115], v[132:135], v[96:99]
ds_read_b128 v[144:147], v25 offset:28672
v_add_u32 v11, 18432, v11
v_mfma_f32_16x16x32_bf16 v[100:103], a[116:119], v[132:135], v[100:103]
v_min_u32 v11, v11, v12
v_mfma_f32_16x16x32_bf16 v[104:107], a[120:123], v[132:135], v[104:107]
v_mfma_f32_16x16x32_bf16 v[108:111], a[124:127], v[132:135], v[108:111]
s_waitcnt lgkmcnt(2)
v_mfma_f32_16x16x32_bf16 v[96:99], a[128:131], v[136:139], v[96:99]
ds_read_b128 v[132:135], v25 offset:29696
v_mfma_f32_16x16x32_bf16 v[100:103], a[132:135], v[136:139], v[100:103]
v_mfma_f32_16x16x32_bf16 v[104:107], a[136:139], v[136:139], v[104:107]
v_mfma_f32_16x16x32_bf16 v[108:111], a[140:143], v[136:139], v[108:111]
s_waitcnt lgkmcnt(2)
v_mfma_f32_16x16x32_bf16 v[112:115], a[144:147], v[140:143], v[112:115]
ds_read_b128 v[136:139], v25 offset:30720
v_mfma_f32_16x16x32_bf16 v[116:119], a[148:151], v[140:143], v[116:119]
v_mfma_f32_16x16x32_bf16 v[120:123], a[152:155], v[140:143], v[120:123]
s_waitcnt vmcnt(8)
s_barrier
v_mfma_f32_16x16x32_bf16 v[124:127], a[156:159], v[140:143], v[124:127]
v_cvt_pk_bf16_f32 v164, v96, v97
v_cvt_pk_bf16_f32 v165, v98, v99
v_mfma_f32_16x16x32_bf16 v[128:131], v[60:63], v[140:143], 0
s_waitcnt lgkmcnt(2)
v_cvt_pk_bf16_f32 v166, v100, v101
v_cvt_pk_bf16_f32 v167, v102, v103
v_mfma_f32_16x16x32_bf16 v[112:115], a[160:163], v[144:147], v[112:115]
ds_read_b128 v[140:143], v25 offset:31744
v_add_f32 v254, v96, v97
v_mfma_f32_16x16x32_bf16 v[116:119], a[164:167], v[144:147], v[116:119]
global_store_dwordx4 v13, v[164:167], s[38:39] offset:0 sc1
v_mfma_f32_16x16x32_bf16 v[120:123], a[168:171], v[144:147], v[120:123]
v_cvt_pk_bf16_f32 v168, v104, v105
v_cvt_pk_bf16_f32 v169, v106, v107
v_mfma_f32_16x16x32_bf16 v[124:127], a[172:175], v[144:147], v[124:127]
v_cvt_pk_bf16_f32 v170, v108, v109
v_cvt_pk_bf16_f32 v171, v110, v111
v_mfma_f32_16x16x32_bf16 v[128:131], v[64:67], v[144:147], v[128:131]
s_waitcnt lgkmcnt(2)
v_mul_f32 v255, v96, v96
v_fmac_f32 v255, v97, v97
v_mfma_f32_16x16x32_bf16 v[112:115], a[176:179], v[132:135], v[112:115]
s_mov_b32 m0, 57344
ds_read_b128 v[144:147], v25 offset:32768
v_add_f32 v254, v254, v98
v_mfma_f32_16x16x32_bf16 v[116:119], a[180:183], v[132:135], v[116:119]
global_store_dwordx4 v13, v[168:171], s[38:39] offset:1024 sc1
v_mfma_f32_16x16x32_bf16 v[120:123], a[184:187], v[132:135], v[120:123]
v_add_u32 v13, 18688, v13
v_fmac_f32 v255, v98, v98
v_mfma_f32_16x16x32_bf16 v[124:127], a[188:191], v[132:135], v[124:127]
v_add_f32 v254, v254, v99
v_fmac_f32 v255, v99, v99
v_mfma_f32_16x16x32_bf16 v[128:131], v[68:71], v[132:135], v[128:131]
s_waitcnt lgkmcnt(2)
v_add_f32 v254, v254, v100
v_fmac_f32 v255, v100, v100
v_mfma_f32_16x16x32_bf16 v[112:115], a[192:195], v[136:139], v[112:115]
ds_read_b128 v[132:135], v25 offset:33792
v_add_f32 v254, v254, v101
v_mfma_f32_16x16x32_bf16 v[116:119], a[196:199], v[136:139], v[116:119]
v_fmac_f32 v255, v101, v101
v_add_f32 v254, v254, v102
v_mfma_f32_16x16x32_bf16 v[120:123], a[200:203], v[136:139], v[120:123]
v_fmac_f32 v255, v102, v102
v_add_f32 v254, v254, v103
v_mfma_f32_16x16x32_bf16 v[124:127], a[204:207], v[136:139], v[124:127]
v_fmac_f32 v255, v103, v103
v_add_f32 v254, v254, v104
v_mfma_f32_16x16x32_bf16 v[128:131], v[72:75], v[136:139], v[128:131]
s_waitcnt lgkmcnt(2)
v_fmac_f32 v255, v104, v104
v_add_f32 v254, v254, v105
v_mfma_f32_16x16x32_bf16 v[112:115], a[208:211], v[140:143], v[112:115]
ds_read_b128 v[136:139], v25 offset:34816
global_load_lds_dwordx4 v16, s[42:43] offset:0 sc1
v_mfma_f32_16x16x32_bf16 v[116:119], a[212:215], v[140:143], v[116:119]
v_fmac_f32 v255, v105, v105
v_add_f32 v254, v254, v106
v_mfma_f32_16x16x32_bf16 v[120:123], a[216:219], v[140:143], v[120:123]
v_fmac_f32 v255, v106, v106
v_add_f32 v254, v254, v107
v_mfma_f32_16x16x32_bf16 v[124:127], a[220:223], v[140:143], v[124:127]
v_fmac_f32 v255, v107, v107
v_add_f32 v254, v254, v108
v_mfma_f32_16x16x32_bf16 v[128:131], v[76:79], v[140:143], v[128:131]
s_waitcnt vmcnt(9)
ds_read_b128 v[148:151], v19 offset:0
ds_read_b128 v[152:155], v20 offset:0
s_waitcnt lgkmcnt(4)
v_mfma_f32_16x16x32_bf16 v[112:115], a[224:227], v[144:147], v[112:115]
ds_read_b128 v[140:143], v25 offset:35840
v_fmac_f32 v255, v108, v108
v_mfma_f32_16x16x32_bf16 v[116:119], a[228:231], v[144:147], v[116:119]
v_add_f32 v254, v254, v109
v_fmac_f32 v255, v109, v109
v_mfma_f32_16x16x32_bf16 v[120:123], a[232:235], v[144:147], v[120:123]
global_load_lds_dwordx4 v17, s[42:43] offset:1024 sc1
v_add_f32 v254, v254, v110
v_mfma_f32_16x16x32_bf16 v[124:127], a[236:239], v[144:147], v[124:127]
v_fmac_f32 v255, v110, v110
v_add_f32 v254, v254, v111
v_mfma_f32_16x16x32_bf16 v[128:131], v[80:83], v[144:147], v[128:131]
s_waitcnt lgkmcnt(4)
v_fmac_f32 v255, v111, v111
v_mfma_f32_16x16x32_bf16 v[112:115], a[240:243], v[132:135], v[112:115]
ds_read_b128 v[144:147], v25 offset:18432
v_mfma_f32_16x16x32_bf16 v[116:119], a[244:247], v[132:135], v[116:119]
ds_write_b64 v27, v[254:255] offset:43520
v_mfma_f32_16x16x32_bf16 v[120:123], a[248:251], v[132:135], v[120:123]
s_add_u32 m0, s49, 18432
s_waitcnt lgkmcnt(4)
v_lshlrev_b32 v96, 16, v148
v_and_b32 v97, 0xffff0000, v148
v_mfma_f32_16x16x32_bf16 v[124:127], a[252:255], v[132:135], v[124:127]
v_lshlrev_b32 v98, 16, v149
v_and_b32 v99, 0xffff0000, v149
v_mfma_f32_16x16x32_bf16 v[128:131], v[84:87], v[132:135], v[128:131]
v_lshlrev_b32 v100, 16, v150
v_and_b32 v101, 0xffff0000, v150
v_mfma_f32_16x16x32_bf16 v[112:115], v[28:31], v[136:139], v[112:115]
s_sub_u32 s42, s42, 18432
ds_read_b128 v[132:135], v25 offset:19456
v_lshlrev_b32 v102, 16, v151
v_mfma_f32_16x16x32_bf16 v[116:119], v[32:35], v[136:139], v[116:119]
s_subb_u32 s43, s43, 0
v_and_b32 v103, 0xffff0000, v151
s_waitcnt lgkmcnt(4)
v_lshlrev_b32 v104, 16, v152
v_mfma_f32_16x16x32_bf16 v[120:123], v[36:39], v[136:139], v[120:123]
v_and_b32 v105, 0xffff0000, v152
v_lshlrev_b32 v106, 16, v153
v_mfma_f32_16x16x32_bf16 v[124:127], v[40:43], v[136:139], v[124:127]
v_and_b32 v107, 0xffff0000, v153
v_lshlrev_b32 v108, 16, v154
v_mfma_f32_16x16x32_bf16 v[128:131], v[88:91], v[136:139], v[128:131]
s_waitcnt lgkmcnt(3)
v_and_b32 v109, 0xffff0000, v154
v_lshlrev_b32 v110, 16, v155
v_mfma_f32_16x16x32_bf16 v[112:115], v[44:47], v[140:143], v[112:115]
ds_read_b128 v[136:139], v25 offset:20480
v_and_b32 v111, 0xffff0000, v155
v_mfma_f32_16x16x32_bf16 v[116:119], v[48:51], v[140:143], v[116:119]
v_mfma_f32_16x16x32_bf16 v[120:123], v[52:55], v[140:143], v[120:123]
v_mfma_f32_16x16x32_bf16 v[124:127], v[56:59], v[140:143], v[124:127]
v_mfma_f32_16x16x32_bf16 v[128:131], v[92:95], v[140:143], v[128:131]
s_waitcnt lgkmcnt(3)
v_mfma_f32_16x16x32_bf16 v[112:115], a[0:3], v[144:147], v[112:115]
ds_read_b128 v[140:143], v25 offset:21504
v_mfma_f32_16x16x32_bf16 v[116:119], a[4:7], v[144:147], v[116:119]
v_mfma_f32_16x16x32_bf16 v[120:123], a[8:11], v[144:147], v[120:123]
v_mfma_f32_16x16x32_bf16 v[124:127], a[12:15], v[144:147], v[124:127]
s_waitcnt lgkmcnt(2)
v_mfma_f32_16x16x32_bf16 v[112:115], a[16:19], v[132:135], v[112:115]
ds_read_b128 v[144:147], v25 offset:22528
v_mfma_f32_16x16x32_bf16 v[116:119], a[20:23], v[132:135], v[116:119]
s_waitcnt vmcnt(6)
v_sub_u32 v201, v201, v22
v_cmp_lt_u32 vcc, 0x7f, v201
v_mfma_f32_16x16x32_bf16 v[120:123], a[24:27], v[132:135], v[120:123]
v_mfma_f32_16x16x32_bf16 v[124:127], a[28:31], v[132:135], v[124:127]
s_waitcnt lgkmcnt(2)
v_mfma_f32_16x16x32_bf16 v[112:115], a[32:35], v[136:139], v[112:115]
s_and_b64 vcc, vcc, s[76:77]
s_cbranch_vccnz .Lwo_sp9
.Lwo_rt10:
ds_read_b128 v[132:135], v25 offset:23552
v_mfma_f32_16x16x32_bf16 v[116:119], a[36:39], v[136:139], v[116:119]
v_mfma_f32_16x16x32_bf16 v[120:123], a[40:43], v[136:139], v[120:123]
s_barrier
v_mfma_f32_16x16x32_bf16 v[124:127], a[44:47], v[136:139], v[124:127]
s_waitcnt lgkmcnt(2)
ds_write_b128 v26, v[128:131] offset:40960
v_mfma_f32_16x16x32_bf16 v[112:115], a[48:51], v[140:143], v[112:115]
s_mov_b64 s[76:77], -1
ds_read_b128 v[136:139], v25 offset:24576
v_add_u32 v22, 1, v22
v_mfma_f32_16x16x32_bf16 v[116:119], a[52:55], v[140:143], v[116:119]
v_add_lshl_u32 v253, v22, 0, 7
v_bfi_b32 v253, s69, v253, v21
v_mfma_f32_16x16x32_bf16 v[120:123], a[56:59], v[140:143], v[120:123]
global_load_lds_dwordx4 v11, s[38:39] offset:-2048 sc1
v_mfma_f32_16x16x32_bf16 v[124:127], a[60:63], v[140:143], v[124:127]
s_waitcnt lgkmcnt(3)
v_mfma_f32_16x16x32_bf16 v[112:115], a[64:67], v[144:147], v[112:115]
ds_read_b128 v[140:143], v25 offset:25600
v_mfma_f32_16x16x32_bf16 v[116:119], a[68:71], v[144:147], v[116:119]
global_load_lds_dwordx4 v11, s[38:39] offset:-1024 sc1
v_mfma_f32_16x16x32_bf16 v[120:123], a[72:75], v[144:147], v[120:123]
v_mfma_f32_16x16x32_bf16 v[124:127], a[76:79], v[144:147], v[124:127]
s_waitcnt lgkmcnt(3)
v_mfma_f32_16x16x32_bf16 v[112:115], a[80:83], v[132:135], v[112:115]
ds_read_b128 v[144:147], v25 offset:26624
global_load_lds_dwordx4 v11, s[38:39] offset:0 sc1
v_mfma_f32_16x16x32_bf16 v[116:119], a[84:87], v[132:135], v[116:119]
v_mfma_f32_16x16x32_bf16 v[120:123], a[88:91], v[132:135], v[120:123]
v_mfma_f32_16x16x32_bf16 v[124:127], a[92:95], v[132:135], v[124:127]
s_waitcnt lgkmcnt(2)
v_xor_b32 v25, 0x10000, v25
global_load_dword v200, v253, s[38:39] sc1
v_mfma_f32_16x16x32_bf16 v[112:115], a[96:99], v[136:139], v[112:115]
global_load_lds_dwordx4 v11, s[38:39] offset:1024 sc1
v_mfma_f32_16x16x32_bf16 v[116:119], a[100:103], v[136:139], v[116:119]
v_mfma_f32_16x16x32_bf16 v[120:123], a[104:107], v[136:139], v[120:123]
s_waitcnt vmcnt(9)
s_barrier
v_mfma_f32_16x16x32_bf16 v[124:127], a[108:111], v[136:139], v[124:127]
s_waitcnt lgkmcnt(1)
ds_read_b128 v[132:135], v25 offset:9216
global_load_lds_dwordx4 v11, s[38:39] offset:2048 sc1
v_mfma_f32_16x16x32_bf16 v[112:115], a[112:115], v[140:143], v[112:115]
ds_read_b128 v[136:139], v25 offset:10240
v_add_u32 v11, 18432, v11
v_mfma_f32_16x16x32_bf16 v[116:119], a[116:119], v[140:143], v[116:119]
v_min_u32 v11, v11, v12
v_mfma_f32_16x16x32_bf16 v[120:123], a[120:123], v[140:143], v[120:123]
v_mfma_f32_16x16x32_bf16 v[124:127], a[124:127], v[140:143], v[124:127]
s_waitcnt lgkmcnt(2)
v_mfma_f32_16x16x32_bf16 v[112:115], a[128:131], v[144:147], v[112:115]
ds_read_b128 v[140:143], v25 offset:11264
v_mfma_f32_16x16x32_bf16 v[116:119], a[132:135], v[144:147], v[116:119]
v_mfma_f32_16x16x32_bf16 v[120:123], a[136:139], v[144:147], v[120:123]
v_mfma_f32_16x16x32_bf16 v[124:127], a[140:143], v[144:147], v[124:127]
s_sub_u32 s70, s70, 1
s_cbranch_scc0 .Lwo_top11
s_waitcnt vmcnt(8)
s_barrier
v_cvt_pk_bf16_f32 v164, v112, v113
v_cvt_pk_bf16_f32 v165, v114, v115
v_cvt_pk_bf16_f32 v166, v116, v117
v_cvt_pk_bf16_f32 v167, v118, v119
global_store_dwordx4 v13, v[164:167], s[38:39] offset:0 sc1
v_cvt_pk_bf16_f32 v168, v120, v121
v_cvt_pk_bf16_f32 v169, v122, v123
v_cvt_pk_bf16_f32 v170, v124, v125
v_cvt_pk_bf16_f32 v171, v126, v127
global_store_dwordx4 v13, v[168:171], s[38:39] offset:1024 sc1
v_add_f32 v254, v112, v113
v_mul_f32 v255, v112, v112
v_fmac_f32 v255, v113, v113
v_add_f32 v254, v254, v114
v_fmac_f32 v255, v114, v114
v_add_f32 v254, v254, v115
v_fmac_f32 v255, v115, v115
v_add_f32 v254, v254, v116
v_fmac_f32 v255, v116, v116
v_add_f32 v254, v254, v117
v_fmac_f32 v255, v117, v117
)"
R"(v_add_f32 v254, v254, v118
v_fmac_f32 v255, v118, v118
v_add_f32 v254, v254, v119
v_fmac_f32 v255, v119, v119
v_add_f32 v254, v254, v120
v_fmac_f32 v255, v120, v120
v_add_f32 v254, v254, v121
v_fmac_f32 v255, v121, v121
v_add_f32 v254, v254, v122
v_fmac_f32 v255, v122, v122
v_add_f32 v254, v254, v123
v_fmac_f32 v255, v123, v123
v_add_f32 v254, v254, v124
v_fmac_f32 v255, v124, v124
v_add_f32 v254, v254, v125
v_fmac_f32 v255, v125, v125
v_add_f32 v254, v254, v126
v_fmac_f32 v255, v126, v126
v_add_f32 v254, v254, v127
v_fmac_f32 v255, v127, v127
ds_write_b64 v27, v[254:255] offset:45568
s_waitcnt vmcnt(0) lgkmcnt(0)
s_barrier
s_barrier
s_endpgm
.Lwo_w3:
s_add_u32 m0, s48, 4096
s_nop 0
global_load_lds_dwordx4 v0, s[40:41] offset:-4096
global_load_lds_dwordx4 v1, s[40:41] offset:-3072
global_load_lds_dwordx4 v2, s[40:41] offset:-2048
global_load_lds_dwordx4 v3, s[40:41] offset:-1024
global_load_lds_dwordx4 v4, s[40:41] offset:0
global_load_lds_dwordx4 v5, s[40:41] offset:1024
global_load_lds_dwordx4 v6, s[40:41] offset:2048
global_load_lds_dwordx4 v7, s[40:41] offset:3072
s_add_u32 m0, s48, 12288
s_nop 0
global_load_lds_dwordx4 v8, s[40:41] offset:-4096
s_add_u32 s40, s40, 73728
s_addc_u32 s41, s41, 0
s_add_u32 m0, s48, 40960
s_nop 0
global_load_lds_dwordx4 v0, s[40:41] offset:-4096
global_load_lds_dwordx4 v1, s[40:41] offset:-3072
global_load_lds_dwordx4 v2, s[40:41] offset:-2048
global_load_lds_dwordx4 v3, s[40:41] offset:-1024
global_load_lds_dwordx4 v4, s[40:41] offset:0
global_load_lds_dwordx4 v5, s[40:41] offset:1024
global_load_lds_dwordx4 v6, s[40:41] offset:2048
global_load_lds_dwordx4 v7, s[40:41] offset:3072
s_add_u32 m0, s48, 49152
s_nop 0
global_load_lds_dwordx4 v8, s[40:41] offset:-4096
s_add_u32 s40, s40, 73728
s_addc_u32 s41, s41, 0
s_add_u32 m0, s48, 77824
s_nop 0
global_load_lds_dwordx4 v0, s[40:41] offset:-4096
global_load_lds_dwordx4 v1, s[40:41] offset:-3072
global_load_lds_dwordx4 v2, s[40:41] offset:-2048
global_load_lds_dwordx4 v3, s[40:41] offset:-1024
global_load_lds_dwordx4 v4, s[40:41] offset:0
global_load_lds_dwordx4 v5, s[40:41] offset:1024
global_load_lds_dwordx4 v6, s[40:41] offset:2048
global_load_lds_dwordx4 v7, s[40:41] offset:3072
s_add_u32 m0, s48, 86016
s_nop 0
global_load_lds_dwordx4 v8, s[40:41] offset:-4096
s_add_u32 s40, s40, 73728
s_addc_u32 s41, s41, 0
v_add_u32 v204, 0x10000, v9
v_add_u32 v205, 0x20000, v9
v_add_u32 v206, 0x10000, v10
v_add_u32 v207, 0x20000, v10
s_waitcnt lgkmcnt(0)
s_mul_i32 s37, s37, 0x9E3779B1
s_add_u32 s37, s37, 0x7F4A7C15
v_add_u32 v22, s37, v22
v_add_u32 v24, s37, v24
s_waitcnt vmcnt(18)
s_barrier
s_add_u32 m0, s48, 114688
ds_read_b64_tr_b16 a[0:1], v9 offset:0
ds_read_b64_tr_b16 a[2:3], v9 offset:2304
global_load_lds_dwordx4 v0, s[40:41] offset:-4096
ds_read_b64_tr_b16 a[4:5], v9 offset:8
ds_read_b64_tr_b16 a[6:7], v9 offset:2312
global_load_lds_dwordx4 v1, s[40:41] offset:-3072
ds_read_b64_tr_b16 a[8:9], v9 offset:64
global_load_lds_dwordx4 v2, s[40:41] offset:-2048
ds_read_b64_tr_b16 a[10:11], v9 offset:2368
ds_read_b64_tr_b16 a[12:13], v9 offset:72
global_load_lds_dwordx4 v3, s[40:41] offset:-1024
ds_read_b64_tr_b16 a[14:15], v9 offset:2376
global_load_lds_dwordx4 v4, s[40:41] offset:0
ds_read_b64_tr_b16 v[60:61], v10 offset:0
ds_read_b64_tr_b16 v[62:63], v10 offset:2304
global_load_lds_dwordx4 v5, s[40:41] offset:1024
ds_read_b64_tr_b16 a[16:17], v9 offset:18432
global_load_lds_dwordx4 v6, s[40:41] offset:2048
ds_read_b64_tr_b16 a[18:19], v9 offset:20736
ds_read_b64_tr_b16 a[20:21], v9 offset:18440
global_load_lds_dwordx4 v7, s[40:41] offset:3072
ds_read_b64_tr_b16 a[22:23], v9 offset:20744
s_add_u32 m0, s48, 122880
ds_read_b64_tr_b16 a[24:25], v9 offset:18496
ds_read_b64_tr_b16 a[26:27], v9 offset:20800
global_load_lds_dwordx4 v8, s[40:41] offset:-4096
ds_read_b64_tr_b16 a[28:29], v9 offset:18504
s_add_u32 s40, s40, 73728
ds_read_b64_tr_b16 a[30:31], v9 offset:20808
ds_read_b64_tr_b16 v[64:65], v10 offset:18432
s_addc_u32 s41, s41, 0
ds_read_b64_tr_b16 v[66:67], v10 offset:20736
s_waitcnt vmcnt(18) lgkmcnt(0)
s_barrier
s_add_u32 m0, s48, 4096
ds_read_b64_tr_b16 a[32:33], v9 offset:36864
ds_read_b64_tr_b16 a[34:35], v9 offset:39168
global_load_lds_dwordx4 v0, s[40:41] offset:-4096
ds_read_b64_tr_b16 a[36:37], v9 offset:36872
ds_read_b64_tr_b16 a[38:39], v9 offset:39176
global_load_lds_dwordx4 v1, s[40:41] offset:-3072
ds_read_b64_tr_b16 a[40:41], v9 offset:36928
global_load_lds_dwordx4 v2, s[40:41] offset:-2048
ds_read_b64_tr_b16 a[42:43], v9 offset:39232
ds_read_b64_tr_b16 a[44:45], v9 offset:36936
global_load_lds_dwordx4 v3, s[40:41] offset:-1024
ds_read_b64_tr_b16 a[46:47], v9 offset:39240
global_load_lds_dwordx4 v4, s[40:41] offset:0
ds_read_b64_tr_b16 v[68:69], v10 offset:36864
ds_read_b64_tr_b16 v[70:71], v10 offset:39168
global_load_lds_dwordx4 v5, s[40:41] offset:1024
ds_read_b64_tr_b16 a[48:49], v9 offset:55296
global_load_lds_dwordx4 v6, s[40:41] offset:2048
ds_read_b64_tr_b16 a[50:51], v9 offset:57600
ds_read_b64_tr_b16 a[52:53], v9 offset:55304
global_load_lds_dwordx4 v7, s[40:41] offset:3072
ds_read_b64_tr_b16 a[54:55], v9 offset:57608
s_add_u32 m0, s48, 12288
ds_read_b64_tr_b16 a[56:57], v9 offset:55360
ds_read_b64_tr_b16 a[58:59], v9 offset:57664
global_load_lds_dwordx4 v8, s[40:41] offset:-4096
ds_read_b64_tr_b16 a[60:61], v9 offset:55368
s_add_u32 s40, s40, 73728
ds_read_b64_tr_b16 a[62:63], v9 offset:57672
ds_read_b64_tr_b16 v[72:73], v10 offset:55296
s_addc_u32 s41, s41, 0
ds_read_b64_tr_b16 v[74:75], v10 offset:57600
s_waitcnt vmcnt(18) lgkmcnt(0)
s_barrier
s_add_u32 m0, s48, 40960
ds_read_b64_tr_b16 a[64:65], v204 offset:8192
ds_read_b64_tr_b16 a[66:67], v204 offset:10496
global_load_lds_dwordx4 v0, s[40:41] offset:-4096
ds_read_b64_tr_b16 a[68:69], v204 offset:8200
ds_read_b64_tr_b16 a[70:71], v204 offset:10504
global_load_lds_dwordx4 v1, s[40:41] offset:-3072
ds_read_b64_tr_b16 a[72:73], v204 offset:8256
global_load_lds_dwordx4 v2, s[40:41] offset:-2048
ds_read_b64_tr_b16 a[74:75], v204 offset:10560
ds_read_b64_tr_b16 a[76:77], v204 offset:8264
global_load_lds_dwordx4 v3, s[40:41] offset:-1024
ds_read_b64_tr_b16 a[78:79], v204 offset:10568
global_load_lds_dwordx4 v4, s[40:41] offset:0
ds_read_b64_tr_b16 v[76:77], v206 offset:8192
ds_read_b64_tr_b16 v[78:79], v206 offset:10496
global_load_lds_dwordx4 v5, s[40:41] offset:1024
ds_read_b64_tr_b16 a[80:81], v204 offset:26624
global_load_lds_dwordx4 v6, s[40:41] offset:2048
ds_read_b64_tr_b16 a[82:83], v204 offset:28928
ds_read_b64_tr_b16 a[84:85], v204 offset:26632
global_load_lds_dwordx4 v7, s[40:41] offset:3072
ds_read_b64_tr_b16 a[86:87], v204 offset:28936
s_add_u32 m0, s48, 49152
ds_read_b64_tr_b16 a[88:89], v204 offset:26688
ds_read_b64_tr_b16 a[90:91], v204 offset:28992
global_load_lds_dwordx4 v8, s[40:41] offset:-4096
ds_read_b64_tr_b16 a[92:93], v204 offset:26696
s_add_u32 s40, s40, 73728
ds_read_b64_tr_b16 a[94:95], v204 offset:29000
ds_read_b64_tr_b16 v[80:81], v206 offset:26624
s_addc_u32 s41, s41, 0
ds_read_b64_tr_b16 v[82:83], v206 offset:28928
s_waitcnt vmcnt(18) lgkmcnt(0)
s_barrier
s_add_u32 m0, s48, 77824
ds_read_b64_tr_b16 a[96:97], v204 offset:45056
ds_read_b64_tr_b16 a[98:99], v204 offset:47360
global_load_lds_dwordx4 v0, s[40:41] offset:-4096
ds_read_b64_tr_b16 a[100:101], v204 offset:45064
ds_read_b64_tr_b16 a[102:103], v204 offset:47368
global_load_lds_dwordx4 v1, s[40:41] offset:-3072
ds_read_b64_tr_b16 a[104:105], v204 offset:45120
global_load_lds_dwordx4 v2, s[40:41] offset:-2048
ds_read_b64_tr_b16 a[106:107], v204 offset:47424
ds_read_b64_tr_b16 a[108:109], v204 offset:45128
global_load_lds_dwordx4 v3, s[40:41] offset:-1024
ds_read_b64_tr_b16 a[110:111], v204 offset:47432
global_load_lds_dwordx4 v4, s[40:41] offset:0
ds_read_b64_tr_b16 v[84:85], v206 offset:45056
ds_read_b64_tr_b16 v[86:87], v206 offset:47360
global_load_lds_dwordx4 v5, s[40:41] offset:1024
ds_read_b64_tr_b16 a[112:113], v204 offset:63488
global_load_lds_dwordx4 v6, s[40:41] offset:2048
ds_read_b64_tr_b16 a[114:115], v205 offset:256
ds_read_b64_tr_b16 a[116:117], v204 offset:63496
global_load_lds_dwordx4 v7, s[40:41] offset:3072
ds_read_b64_tr_b16 a[118:119], v205 offset:264
s_add_u32 m0, s48, 86016
ds_read_b64_tr_b16 a[120:121], v204 offset:63552
ds_read_b64_tr_b16 a[122:123], v205 offset:320
global_load_lds_dwordx4 v8, s[40:41] offset:-4096
ds_read_b64_tr_b16 a[124:125], v204 offset:63560
s_add_u32 s40, s40, 73728
ds_read_b64_tr_b16 a[126:127], v205 offset:328
ds_read_b64_tr_b16 v[88:89], v206 offset:63488
s_addc_u32 s41, s41, 0
ds_read_b64_tr_b16 v[90:91], v207 offset:256
s_waitcnt vmcnt(18) lgkmcnt(0)
s_barrier
s_add_u32 m0, s48, 114688
ds_read_b64_tr_b16 a[128:129], v9 offset:0
ds_read_b64_tr_b16 a[130:131], v9 offset:2304
global_load_lds_dwordx4 v0, s[40:41] offset:-4096
ds_read_b64_tr_b16 a[132:133], v9 offset:8
global_load_lds_dwordx4 v1, s[40:41] offset:-3072
ds_read_b64_tr_b16 a[134:135], v9 offset:2312
ds_read_b64_tr_b16 a[136:137], v9 offset:64
global_load_lds_dwordx4 v2, s[40:41] offset:-2048
ds_read_b64_tr_b16 a[138:139], v9 offset:2368
global_load_lds_dwordx4 v3, s[40:41] offset:-1024
ds_read_b64_tr_b16 a[140:141], v9 offset:72
global_load_lds_dwordx4 v4, s[40:41] offset:0
ds_read_b64_tr_b16 a[142:143], v9 offset:2376
ds_read_b64_tr_b16 v[92:93], v10 offset:0
global_load_lds_dwordx4 v5, s[40:41] offset:1024
ds_read_b64_tr_b16 v[94:95], v10 offset:2304
global_load_lds_dwordx4 v6, s[40:41] offset:2048
ds_read_b64_tr_b16 a[144:145], v9 offset:18432
ds_read_b64_tr_b16 a[146:147], v9 offset:20736
global_load_lds_dwordx4 v7, s[40:41] offset:3072
ds_read_b64_tr_b16 a[148:149], v9 offset:18440
s_add_u32 m0, s48, 122880
ds_read_b64_tr_b16 a[150:151], v9 offset:20744
global_load_lds_dwordx4 v8, s[40:41] offset:-4096
ds_read_b64_tr_b16 a[152:153], v9 offset:18496
ds_read_b64_tr_b16 a[154:155], v9 offset:20800
s_add_u32 s40, s40, 73728
ds_read_b64_tr_b16 a[156:157], v9 offset:18504
s_addc_u32 s41, s41, 0
ds_read_b64_tr_b16 a[158:159], v9 offset:20808
s_waitcnt vmcnt(18) lgkmcnt(0)
s_barrier
s_add_u32 m0, s48, 4096
ds_read_b64_tr_b16 a[160:161], v9 offset:36864
ds_read_b64_tr_b16 a[162:163], v9 offset:39168
global_load_lds_dwordx4 v0, s[40:41] offset:-4096
ds_read_b64_tr_b16 a[164:165], v9 offset:36872
global_load_lds_dwordx4 v1, s[40:41] offset:-3072
ds_read_b64_tr_b16 a[166:167], v9 offset:39176
global_load_lds_dwordx4 v2, s[40:41] offset:-2048
ds_read_b64_tr_b16 a[168:169], v9 offset:36928
global_load_lds_dwordx4 v3, s[40:41] offset:-1024
ds_read_b64_tr_b16 a[170:171], v9 offset:39232
ds_read_b64_tr_b16 a[172:173], v9 offset:36936
global_load_lds_dwordx4 v4, s[40:41] offset:0
ds_read_b64_tr_b16 a[174:175], v9 offset:39240
global_load_lds_dwordx4 v5, s[40:41] offset:1024
ds_read_b64_tr_b16 a[176:177], v9 offset:55296
global_load_lds_dwordx4 v6, s[40:41] offset:2048
ds_read_b64_tr_b16 a[178:179], v9 offset:57600
global_load_lds_dwordx4 v7, s[40:41] offset:3072
ds_read_b64_tr_b16 a[180:181], v9 offset:55304
ds_read_b64_tr_b16 a[182:183], v9 offset:57608
s_add_u32 m0, s48, 12288
ds_read_b64_tr_b16 a[184:185], v9 offset:55360
global_load_lds_dwordx4 v8, s[40:41] offset:-4096
ds_read_b64_tr_b16 a[186:187], v9 offset:57664
s_sub_u32 s40, s40, 589824
ds_read_b64_tr_b16 a[188:189], v9 offset:55368
s_subb_u32 s41, s41, 0
ds_read_b64_tr_b16 a[190:191], v9 offset:57672
s_waitcnt vmcnt(18) lgkmcnt(0)
s_barrier
ds_read_b64_tr_b16 a[192:193], v204 offset:8192
ds_read_b64_tr_b16 a[194:195], v204 offset:10496
ds_read_b64_tr_b16 a[196:197], v204 offset:8200
ds_read_b64_tr_b16 a[198:199], v204 offset:10504
ds_read_b64_tr_b16 a[200:201], v204 offset:8256
ds_read_b64_tr_b16 a[202:203], v204 offset:10560
ds_read_b64_tr_b16 a[204:205], v204 offset:8264
ds_read_b64_tr_b16 a[206:207], v204 offset:10568
ds_read_b64_tr_b16 a[208:209], v204 offset:26624
ds_read_b64_tr_b16 a[210:211], v204 offset:28928
ds_read_b64_tr_b16 a[212:213], v204 offset:26632
ds_read_b64_tr_b16 a[214:215], v204 offset:28936
ds_read_b64_tr_b16 a[216:217], v204 offset:26688
ds_read_b64_tr_b16 a[218:219], v204 offset:28992
ds_read_b64_tr_b16 a[220:221], v204 offset:26696
ds_read_b64_tr_b16 a[222:223], v204 offset:29000
s_waitcnt vmcnt(9) lgkmcnt(0)
s_barrier
ds_read_b64_tr_b16 a[224:225], v204 offset:45056
ds_read_b64_tr_b16 a[226:227], v204 offset:47360
ds_read_b64_tr_b16 a[228:229], v204 offset:45064
ds_read_b64_tr_b16 a[230:231], v204 offset:47368
ds_read_b64_tr_b16 a[232:233], v204 offset:45120
ds_read_b64_tr_b16 a[234:235], v204 offset:47424
ds_read_b64_tr_b16 a[236:237], v204 offset:45128
ds_read_b64_tr_b16 a[238:239], v204 offset:47432
ds_read_b64_tr_b16 a[240:241], v204 offset:63488
ds_read_b64_tr_b16 a[242:243], v205 offset:256
ds_read_b64_tr_b16 a[244:245], v204 offset:63496
ds_read_b64_tr_b16 a[246:247], v205 offset:264
ds_read_b64_tr_b16 a[248:249], v204 offset:63552
ds_read_b64_tr_b16 a[250:251], v205 offset:320
ds_read_b64_tr_b16 a[252:253], v204 offset:63560
ds_read_b64_tr_b16 a[254:255], v205 offset:328
s_waitcnt vmcnt(0) lgkmcnt(0)
s_barrier
ds_read_b64_tr_b16 v[28:29], v9 offset:0
ds_read_b64_tr_b16 v[30:31], v9 offset:2304
ds_read_b64_tr_b16 v[32:33], v9 offset:8
ds_read_b64_tr_b16 v[34:35], v9 offset:2312
ds_read_b64_tr_b16 v[36:37], v9 offset:64
ds_read_b64_tr_b16 v[38:39], v9 offset:2368
ds_read_b64_tr_b16 v[40:41], v9 offset:72
ds_read_b64_tr_b16 v[42:43], v9 offset:2376
ds_read_b64_tr_b16 v[44:45], v9 offset:18432
ds_read_b64_tr_b16 v[46:47], v9 offset:20736
ds_read_b64_tr_b16 v[48:49], v9 offset:18440
ds_read_b64_tr_b16 v[50:51], v9 offset:20744
ds_read_b64_tr_b16 v[52:53], v9 offset:18496
ds_read_b64_tr_b16 v[54:55], v9 offset:20800
ds_read_b64_tr_b16 v[56:57], v9 offset:18504
ds_read_b64_tr_b16 v[58:59], v9 offset:20808
s_waitcnt lgkmcnt(0)
s_barrier
v_add_lshl_u32 v252, v24, 0, 7
v_bfi_b32 v252, s69, v252, v23
v_add_lshl_u32 v253, v22, 0, 7
v_bfi_b32 v253, s69, v253, v21
s_mov_b64 s[76:77], 0
s_add_u32 m0, s49, 18432
s_mov_b32 s70, 33
s_waitcnt vmcnt(0) lgkmcnt(0)
s_nop 7
.p2align 6
.Lwo_top13:
s_waitcnt lgkmcnt(2)
v_mfma_f32_16x16x32_bf16 v[96:99], a[0:3], v[132:135], v[96:99]
ds_read_b128 v[144:147], v25 offset:3072
v_mfma_f32_16x16x32_bf16 v[100:103], a[4:7], v[132:135], v[100:103]
v_mfma_f32_16x16x32_bf16 v[104:107], a[8:11], v[132:135], v[104:107]
s_waitcnt vmcnt(7)
s_barrier
v_mfma_f32_16x16x32_bf16 v[108:111], a[12:15], v[132:135], v[108:111]
v_cvt_pk_bf16_f32 v164, v112, v113
v_cvt_pk_bf16_f32 v165, v114, v115
v_mfma_f32_16x16x32_bf16 v[128:131], v[60:63], v[132:135], 0
s_waitcnt lgkmcnt(2)
v_cvt_pk_bf16_f32 v166, v116, v117
v_cvt_pk_bf16_f32 v167, v118, v119
s_mov_b64 exec, 65535
global_store_dwordx2 v15, v[176:177], s[38:39] sc1
s_mov_b64 exec, -1
v_mfma_f32_16x16x32_bf16 v[96:99], a[16:19], v[136:139], v[96:99]
ds_read_b128 v[132:135], v25 offset:4096
v_add_f32 v254, v112, v113
v_add_u32 v15, 18688, v15
v_mfma_f32_16x16x32_bf16 v[100:103], a[20:23], v[136:139], v[100:103]
v_mul_f32 v255, v112, v112
v_fmac_f32 v255, v113, v113
v_mfma_f32_16x16x32_bf16 v[104:107], a[24:27], v[136:139], v[104:107]
global_store_dwordx4 v13, v[164:167], s[38:39] offset:0 sc1
v_mfma_f32_16x16x32_bf16 v[108:111], a[28:31], v[136:139], v[108:111]
v_cvt_pk_bf16_f32 v168, v120, v121
v_cvt_pk_bf16_f32 v169, v122, v123
v_mfma_f32_16x16x32_bf16 v[128:131], v[64:67], v[136:139], v[128:131]
s_waitcnt lgkmcnt(2)
v_cvt_pk_bf16_f32 v170, v124, v125
v_cvt_pk_bf16_f32 v171, v126, v127
v_mfma_f32_16x16x32_bf16 v[96:99], a[32:35], v[140:143], v[96:99]
s_cmp_eq_u32 s70, 32
ds_read_b128 v[136:139], v25 offset:5120
v_add_f32 v254, v254, v114
v_mfma_f32_16x16x32_bf16 v[100:103], a[36:39], v[140:143], v[100:103]
s_cselect_b64 s[42:43], s[44:45], s[42:43]
global_store_dwordx4 v13, v[168:171], s[38:39] offset:1024 sc1
v_mfma_f32_16x16x32_bf16 v[104:107], a[40:43], v[140:143], v[104:107]
s_cmp_eq_u32 s70, 16
v_add_u32 v13, 18688, v13
v_fmac_f32 v255, v114, v114
v_mfma_f32_16x16x32_bf16 v[108:111], a[44:47], v[140:143], v[108:111]
s_cselect_b64 s[42:43], s[46:47], s[42:43]
v_add_f32 v254, v254, v115
v_fmac_f32 v255, v115, v115
v_mfma_f32_16x16x32_bf16 v[128:131], v[68:71], v[140:143], v[128:131]
)"
R"(s_waitcnt lgkmcnt(2)
v_add_f32 v254, v254, v116
v_fmac_f32 v255, v116, v116
v_mfma_f32_16x16x32_bf16 v[96:99], a[48:51], v[144:147], v[96:99]
s_xor_b32 s49, s49, 0x10000
ds_read_b128 v[140:143], v25 offset:6144
v_add_f32 v254, v254, v117
v_mfma_f32_16x16x32_bf16 v[100:103], a[52:55], v[144:147], v[100:103]
s_mov_b32 m0, 51200
v_fmac_f32 v255, v117, v117
v_add_f32 v254, v254, v118
v_mfma_f32_16x16x32_bf16 v[104:107], a[56:59], v[144:147], v[104:107]
v_fmac_f32 v255, v118, v118
v_add_f32 v254, v254, v119
v_mfma_f32_16x16x32_bf16 v[108:111], a[60:63], v[144:147], v[108:111]
v_fmac_f32 v255, v119, v119
v_add_f32 v254, v254, v120
v_mfma_f32_16x16x32_bf16 v[128:131], v[72:75], v[144:147], v[128:131]
s_waitcnt lgkmcnt(2)
v_fmac_f32 v255, v120, v120
v_add_f32 v254, v254, v121
v_mfma_f32_16x16x32_bf16 v[96:99], a[64:67], v[132:135], v[96:99]
ds_read_b128 v[144:147], v25 offset:7168
v_fmac_f32 v255, v121, v121
v_mfma_f32_16x16x32_bf16 v[100:103], a[68:71], v[132:135], v[100:103]
v_add_f32 v254, v254, v122
v_fmac_f32 v255, v122, v122
v_mfma_f32_16x16x32_bf16 v[104:107], a[72:75], v[132:135], v[104:107]
global_load_lds_dwordx4 v16, s[42:43] offset:0 sc1
v_add_f32 v254, v254, v123
v_mfma_f32_16x16x32_bf16 v[108:111], a[76:79], v[132:135], v[108:111]
v_fmac_f32 v255, v123, v123
v_add_f32 v254, v254, v124
v_mfma_f32_16x16x32_bf16 v[128:131], v[76:79], v[132:135], v[128:131]
s_waitcnt vmcnt(9)
ds_read_b128 v[148:151], v19 offset:8192
ds_read_b128 v[152:155], v20 offset:8192
s_waitcnt lgkmcnt(4)
v_mfma_f32_16x16x32_bf16 v[96:99], a[80:83], v[136:139], v[96:99]
ds_read_b128 v[132:135], v25 offset:8192
v_fmac_f32 v255, v124, v124
v_mfma_f32_16x16x32_bf16 v[100:103], a[84:87], v[136:139], v[100:103]
v_add_f32 v254, v254, v125
v_fmac_f32 v255, v125, v125
v_mfma_f32_16x16x32_bf16 v[104:107], a[88:91], v[136:139], v[104:107]
v_add_f32 v254, v254, v126
v_fmac_f32 v255, v126, v126
v_mfma_f32_16x16x32_bf16 v[108:111], a[92:95], v[136:139], v[108:111]
v_add_f32 v254, v254, v127
v_fmac_f32 v255, v127, v127
v_mfma_f32_16x16x32_bf16 v[128:131], v[80:83], v[136:139], v[128:131]
s_waitcnt lgkmcnt(4)
global_load_lds_dwordx4 v17, s[42:43] offset:1024 sc1
v_mfma_f32_16x16x32_bf16 v[96:99], a[96:99], v[140:143], v[96:99]
ds_read_b128 v[136:139], v25 offset:9216
v_mfma_f32_16x16x32_bf16 v[100:103], a[100:103], v[140:143], v[100:103]
ds_write_b64 v27, v[254:255] offset:46080
v_mfma_f32_16x16x32_bf16 v[104:107], a[104:107], v[140:143], v[104:107]
s_waitcnt lgkmcnt(4)
v_lshlrev_b32 v112, 16, v148
v_and_b32 v113, 0xffff0000, v148
v_mfma_f32_16x16x32_bf16 v[108:111], a[108:111], v[140:143], v[108:111]
v_lshlrev_b32 v114, 16, v149
v_and_b32 v115, 0xffff0000, v149
v_mfma_f32_16x16x32_bf16 v[128:131], v[84:87], v[140:143], v[128:131]
v_lshlrev_b32 v116, 16, v150
v_and_b32 v117, 0xffff0000, v150
v_mfma_f32_16x16x32_bf16 v[96:99], a[112:115], v[144:147], v[96:99]
s_add_u32 m0, s49, 0
ds_read_b128 v[140:143], v25 offset:10240
v_lshlrev_b32 v118, 16, v151
v_mfma_f32_16x16x32_bf16 v[100:103], a[116:119], v[144:147], v[100:103]
s_waitcnt lgkmcnt(4)
v_and_b32 v119, 0xffff0000, v151
v_lshlrev_b32 v120, 16, v152
v_mfma_f32_16x16x32_bf16 v[104:107], a[120:123], v[144:147], v[104:107]
s_sub_u32 s42, s42, 18432
v_and_b32 v121, 0xffff0000, v152
v_lshlrev_b32 v122, 16, v153
v_mfma_f32_16x16x32_bf16 v[108:111], a[124:127], v[144:147], v[108:111]
s_subb_u32 s43, s43, 0
v_and_b32 v123, 0xffff0000, v153
v_lshlrev_b32 v124, 16, v154
v_mfma_f32_16x16x32_bf16 v[128:131], v[88:91], v[144:147], v[128:131]
s_waitcnt lgkmcnt(3)
v_and_b32 v125, 0xffff0000, v154
v_lshlrev_b32 v126, 16, v155
v_mfma_f32_16x16x32_bf16 v[96:99], a[128:131], v[132:135], v[96:99]
ds_read_b128 v[144:147], v25 offset:11264
v_and_b32 v127, 0xffff0000, v155
v_mfma_f32_16x16x32_bf16 v[100:103], a[132:135], v[132:135], v[100:103]
v_mfma_f32_16x16x32_bf16 v[104:107], a[136:139], v[132:135], v[104:107]
v_mfma_f32_16x16x32_bf16 v[108:111], a[140:143], v[132:135], v[108:111]
v_mfma_f32_16x16x32_bf16 v[128:131], v[92:95], v[132:135], v[128:131]
s_waitcnt lgkmcnt(3)
ds_read_b64 v[176:177], v27 offset:43008
ds_read_b64 v[180:181], v27 offset:43520
v_mfma_f32_16x16x32_bf16 v[96:99], a[144:147], v[136:139], v[96:99]
ds_read_b128 v[132:135], v25 offset:12288
ds_read_b64 v[184:185], v27 offset:44032
v_mfma_f32_16x16x32_bf16 v[100:103], a[148:151], v[136:139], v[100:103]
ds_read_b64 v[188:189], v27 offset:44544
v_mfma_f32_16x16x32_bf16 v[104:107], a[152:155], v[136:139], v[104:107]
v_mfma_f32_16x16x32_bf16 v[108:111], a[156:159], v[136:139], v[108:111]
s_waitcnt lgkmcnt(6)
v_mfma_f32_16x16x32_bf16 v[96:99], a[160:163], v[140:143], v[96:99]
ds_read_b128 v[136:139], v25 offset:13312
v_mfma_f32_16x16x32_bf16 v[100:103], a[164:167], v[140:143], v[100:103]
v_mfma_f32_16x16x32_bf16 v[104:107], a[168:171], v[140:143], v[104:107]
v_mfma_f32_16x16x32_bf16 v[108:111], a[172:175], v[140:143], v[108:111]
s_waitcnt lgkmcnt(4)
v_add_f32 v176, v176, v180
v_add_f32 v177, v177, v181
v_mfma_f32_16x16x32_bf16 v[96:99], a[176:179], v[144:147], v[96:99]
s_waitcnt lgkmcnt(1)
ds_read_b128 v[140:143], v25 offset:14336
v_add_f32 v184, v184, v188
v_mfma_f32_16x16x32_bf16 v[100:103], a[180:183], v[144:147], v[100:103]
v_mfma_f32_16x16x32_bf16 v[104:107], a[184:187], v[144:147], v[104:107]
s_barrier
v_mfma_f32_16x16x32_bf16 v[108:111], a[188:191], v[144:147], v[108:111]
global_load_lds_dwordx4 v11, s[38:39] offset:-2048 sc1
v_add_f32 v185, v185, v189
v_mfma_f32_16x16x32_bf16 v[96:99], a[192:195], v[132:135], v[96:99]
ds_read_b128 v[144:147], v25 offset:15360
v_add_f32 v176, v176, v184
v_mfma_f32_16x16x32_bf16 v[100:103], a[196:199], v[132:135], v[100:103]
v_add_f32 v177, v177, v185
v_mov_b32 v202, v176
v_mfma_f32_16x16x32_bf16 v[104:107], a[200:203], v[132:135], v[104:107]
ds_write_b128 v26, v[128:131] offset:38912
v_mfma_f32_16x16x32_bf16 v[108:111], a[204:207], v[132:135], v[108:111]
s_waitcnt lgkmcnt(3)
global_load_lds_dwordx4 v11, s[38:39] offset:-1024 sc1
v_mov_b32 v203, v177
v_mfma_f32_16x16x32_bf16 v[96:99], a[208:211], v[136:139], v[96:99]
ds_read_b128 v[132:135], v25 offset:16384
v_mfma_f32_16x16x32_bf16 v[100:103], a[212:215], v[136:139], v[100:103]
v_permlane16_swap_b32 v176, v202
v_mfma_f32_16x16x32_bf16 v[104:107], a[216:219], v[136:139], v[104:107]
global_load_lds_dwordx4 v11, s[38:39] offset:0 sc1
v_mfma_f32_16x16x32_bf16 v[108:111], a[220:223], v[136:139], v[108:111]
s_waitcnt lgkmcnt(3)
v_permlane16_swap_b32 v177, v203
v_mfma_f32_16x16x32_bf16 v[96:99], a[224:227], v[140:143], v[96:99]
ds_read_b128 v[136:139], v25 offset:17408
v_add_f32 v176, v176, v202
v_mfma_f32_16x16x32_bf16 v[100:103], a[228:231], v[140:143], v[100:103]
global_load_lds_dwordx4 v11, s[38:39] offset:1024 sc1
v_add_f32 v177, v177, v203
v_mfma_f32_16x16x32_bf16 v[104:107], a[232:235], v[140:143], v[104:107]
v_mov_b32 v202, v176
v_mov_b32 v203, v177
v_mfma_f32_16x16x32_bf16 v[108:111], a[236:239], v[140:143], v[108:111]
s_waitcnt lgkmcnt(3)
v_permlane32_swap_b32 v176, v202
v_mfma_f32_16x16x32_bf16 v[96:99], a[240:243], v[144:147], v[96:99]
global_load_lds_dwordx4 v11, s[38:39] offset:2048 sc1
v_add_u32 v11, 18432, v11
v_mfma_f32_16x16x32_bf16 v[100:103], a[244:247], v[144:147], v[100:103]
v_mfma_f32_16x16x32_bf16 v[104:107], a[248:251], v[144:147], v[104:107]
s_waitcnt vmcnt(10) lgkmcnt(2)
s_barrier
v_mfma_f32_16x16x32_bf16 v[108:111], a[252:255], v[144:147], v[108:111]
s_waitcnt lgkmcnt(1)
ds_read_b128 v[140:143], v25 offset:18432
v_min_u32 v11, v11, v12
v_mfma_f32_16x16x32_bf16 v[96:99], v[28:31], v[132:135], v[96:99]
ds_read_b128 v[144:147], v25 offset:19456
v_mfma_f32_16x16x32_bf16 v[100:103], v[32:35], v[132:135], v[100:103]
v_permlane32_swap_b32 v177, v203
v_mfma_f32_16x16x32_bf16 v[104:107], v[36:39], v[132:135], v[104:107]
v_add_f32 v176, v176, v202
v_add_f32 v177, v177, v203
v_mfma_f32_16x16x32_bf16 v[108:111], v[40:43], v[132:135], v[108:111]
s_waitcnt lgkmcnt(2)
v_mfma_f32_16x16x32_bf16 v[96:99], v[44:47], v[136:139], v[96:99]
ds_read_b128 v[132:135], v25 offset:20480
v_mfma_f32_16x16x32_bf16 v[100:103], v[48:51], v[136:139], v[100:103]
v_mfma_f32_16x16x32_bf16 v[104:107], v[52:55], v[136:139], v[104:107]
v_mfma_f32_16x16x32_bf16 v[108:111], v[56:59], v[136:139], v[108:111]
s_waitcnt lgkmcnt(2)
v_mfma_f32_16x16x32_bf16 v[112:115], a[0:3], v[140:143], v[112:115]
ds_read_b128 v[136:139], v25 offset:21504
v_mfma_f32_16x16x32_bf16 v[116:119], a[4:7], v[140:143], v[116:119]
v_mfma_f32_16x16x32_bf16 v[120:123], a[8:11], v[140:143], v[120:123]
s_waitcnt vmcnt(7)
s_barrier
v_mfma_f32_16x16x32_bf16 v[124:127], a[12:15], v[140:143], v[124:127]
v_cvt_pk_bf16_f32 v164, v96, v97
v_cvt_pk_bf16_f32 v165, v98, v99
v_mfma_f32_16x16x32_bf16 v[128:131], v[60:63], v[140:143], 0
s_waitcnt lgkmcnt(2)
v_cvt_pk_bf16_f32 v166, v100, v101
v_cvt_pk_bf16_f32 v167, v102, v103
s_mov_b64 exec, 65535
global_store_dwordx2 v15, v[176:177], s[38:39] sc1
s_mov_b64 exec, -1
v_mfma_f32_16x16x32_bf16 v[112:115], a[16:19], v[144:147], v[112:115]
ds_read_b128 v[140:143], v25 offset:22528
v_add_u32 v15, 18688, v15
v_mfma_f32_16x16x32_bf16 v[116:119], a[20:23], v[144:147], v[116:119]
v_add_f32 v254, v96, v97
v_mul_f32 v255, v96, v96
v_mfma_f32_16x16x32_bf16 v[120:123], a[24:27], v[144:147], v[120:123]
global_store_dwordx4 v13, v[164:167], s[38:39] offset:0 sc1
v_mfma_f32_16x16x32_bf16 v[124:127], a[28:31], v[144:147], v[124:127]
v_cvt_pk_bf16_f32 v168, v104, v105
v_cvt_pk_bf16_f32 v169, v106, v107
v_mfma_f32_16x16x32_bf16 v[128:131], v[64:67], v[144:147], v[128:131]
s_waitcnt lgkmcnt(2)
v_cvt_pk_bf16_f32 v170, v108, v109
v_cvt_pk_bf16_f32 v171, v110, v111
v_mfma_f32_16x16x32_bf16 v[112:115], a[32:35], v[132:135], v[112:115]
s_mov_b32 m0, 59392
ds_read_b128 v[144:147], v25 offset:23552
v_fmac_f32 v255, v97, v97
v_mfma_f32_16x16x32_bf16 v[116:119], a[36:39], v[132:135], v[116:119]
global_store_dwordx4 v13, v[168:171], s[38:39] offset:1024 sc1
v_mfma_f32_16x16x32_bf16 v[120:123], a[40:43], v[132:135], v[120:123]
v_add_u32 v13, 18688, v13
v_add_f32 v254, v254, v98
v_mfma_f32_16x16x32_bf16 v[124:127], a[44:47], v[132:135], v[124:127]
v_fmac_f32 v255, v98, v98
v_add_f32 v254, v254, v99
v_mfma_f32_16x16x32_bf16 v[128:131], v[68:71], v[132:135], v[128:131]
s_waitcnt lgkmcnt(2)
v_fmac_f32 v255, v99, v99
v_add_f32 v254, v254, v100
v_mfma_f32_16x16x32_bf16 v[112:115], a[48:51], v[136:139], v[112:115]
ds_read_b128 v[132:135], v25 offset:24576
v_fmac_f32 v255, v100, v100
v_mfma_f32_16x16x32_bf16 v[116:119], a[52:55], v[136:139], v[116:119]
v_add_f32 v254, v254, v101
v_fmac_f32 v255, v101, v101
v_mfma_f32_16x16x32_bf16 v[120:123], a[56:59], v[136:139], v[120:123]
v_add_f32 v254, v254, v102
v_fmac_f32 v255, v102, v102
v_mfma_f32_16x16x32_bf16 v[124:127], a[60:63], v[136:139], v[124:127]
v_add_f32 v254, v254, v103
v_fmac_f32 v255, v103, v103
v_mfma_f32_16x16x32_bf16 v[128:131], v[72:75], v[136:139], v[128:131]
s_waitcnt lgkmcnt(2)
v_add_f32 v254, v254, v104
v_fmac_f32 v255, v104, v104
v_mfma_f32_16x16x32_bf16 v[112:115], a[64:67], v[140:143], v[112:115]
ds_read_b128 v[136:139], v25 offset:25600
v_add_f32 v254, v254, v105
v_mfma_f32_16x16x32_bf16 v[116:119], a[68:71], v[140:143], v[116:119]
v_fmac_f32 v255, v105, v105
v_add_f32 v254, v254, v106
v_mfma_f32_16x16x32_bf16 v[120:123], a[72:75], v[140:143], v[120:123]
global_load_lds_dwordx4 v16, s[42:43] offset:0 sc1
v_fmac_f32 v255, v106, v106
v_mfma_f32_16x16x32_bf16 v[124:127], a[76:79], v[140:143], v[124:127]
v_add_f32 v254, v254, v107
v_fmac_f32 v255, v107, v107
v_mfma_f32_16x16x32_bf16 v[128:131], v[76:79], v[140:143], v[128:131]
s_waitcnt vmcnt(9)
ds_read_b128 v[148:151], v19 offset:0
ds_read_b128 v[152:155], v20 offset:0
s_waitcnt lgkmcnt(4)
v_mfma_f32_16x16x32_bf16 v[112:115], a[80:83], v[144:147], v[112:115]
ds_read_b128 v[140:143], v25 offset:26624
v_add_f32 v254, v254, v108
v_mfma_f32_16x16x32_bf16 v[116:119], a[84:87], v[144:147], v[116:119]
v_fmac_f32 v255, v108, v108
v_add_f32 v254, v254, v109
v_mfma_f32_16x16x32_bf16 v[120:123], a[88:91], v[144:147], v[120:123]
v_fmac_f32 v255, v109, v109
v_add_f32 v254, v254, v110
v_mfma_f32_16x16x32_bf16 v[124:127], a[92:95], v[144:147], v[124:127]
v_fmac_f32 v255, v110, v110
v_add_f32 v254, v254, v111
v_mfma_f32_16x16x32_bf16 v[128:131], v[80:83], v[144:147], v[128:131]
s_waitcnt lgkmcnt(4)
global_load_lds_dwordx4 v17, s[42:43] offset:1024 sc1
v_fmac_f32 v255, v111, v111
v_mfma_f32_16x16x32_bf16 v[112:115], a[96:99], v[132:135], v[112:115]
ds_read_b128 v[144:147], v25 offset:27648
v_mfma_f32_16x16x32_bf16 v[116:119], a[100:103], v[132:135], v[116:119]
ds_write_b64 v27, v[254:255] offset:44032
v_mfma_f32_16x16x32_bf16 v[120:123], a[104:107], v[132:135], v[120:123]
s_waitcnt lgkmcnt(4)
v_lshlrev_b32 v96, 16, v148
v_and_b32 v97, 0xffff0000, v148
v_mfma_f32_16x16x32_bf16 v[124:127], a[108:111], v[132:135], v[124:127]
v_lshlrev_b32 v98, 16, v149
v_and_b32 v99, 0xffff0000, v149
v_mfma_f32_16x16x32_bf16 v[128:131], v[84:87], v[132:135], v[128:131]
v_lshlrev_b32 v100, 16, v150
v_and_b32 v101, 0xffff0000, v150
v_mfma_f32_16x16x32_bf16 v[112:115], a[112:115], v[136:139], v[112:115]
s_add_u32 m0, s49, 18432
ds_read_b128 v[132:135], v25 offset:28672
v_lshlrev_b32 v102, 16, v151
v_mfma_f32_16x16x32_bf16 v[116:119], a[116:119], v[136:139], v[116:119]
s_waitcnt lgkmcnt(4)
v_and_b32 v103, 0xffff0000, v151
v_lshlrev_b32 v104, 16, v152
v_mfma_f32_16x16x32_bf16 v[120:123], a[120:123], v[136:139], v[120:123]
s_sub_u32 s42, s42, 18432
v_and_b32 v105, 0xffff0000, v152
v_lshlrev_b32 v106, 16, v153
v_mfma_f32_16x16x32_bf16 v[124:127], a[124:127], v[136:139], v[124:127]
s_subb_u32 s43, s43, 0
v_and_b32 v107, 0xffff0000, v153
v_lshlrev_b32 v108, 16, v154
v_mfma_f32_16x16x32_bf16 v[128:131], v[88:91], v[136:139], v[128:131]
s_waitcnt lgkmcnt(3)
v_and_b32 v109, 0xffff0000, v154
v_lshlrev_b32 v110, 16, v155
v_mfma_f32_16x16x32_bf16 v[112:115], a[128:131], v[140:143], v[112:115]
ds_read_b128 v[136:139], v25 offset:29696
v_and_b32 v111, 0xffff0000, v155
v_mfma_f32_16x16x32_bf16 v[116:119], a[132:135], v[140:143], v[116:119]
v_mfma_f32_16x16x32_bf16 v[120:123], a[136:139], v[140:143], v[120:123]
v_mfma_f32_16x16x32_bf16 v[124:127], a[140:143], v[140:143], v[124:127]
v_mfma_f32_16x16x32_bf16 v[128:131], v[92:95], v[140:143], v[128:131]
s_waitcnt lgkmcnt(3)
ds_read_b64 v[176:177], v27 offset:45056
ds_read_b64 v[180:181], v27 offset:45568
v_mfma_f32_16x16x32_bf16 v[112:115], a[144:147], v[144:147], v[112:115]
ds_read_b128 v[140:143], v25 offset:30720
ds_read_b64 v[184:185], v27 offset:46080
v_mfma_f32_16x16x32_bf16 v[116:119], a[148:151], v[144:147], v[116:119]
ds_read_b64 v[188:189], v27 offset:46592
v_mfma_f32_16x16x32_bf16 v[120:123], a[152:155], v[144:147], v[120:123]
v_mfma_f32_16x16x32_bf16 v[124:127], a[156:159], v[144:147], v[124:127]
s_waitcnt lgkmcnt(6)
v_mfma_f32_16x16x32_bf16 v[112:115], a[160:163], v[132:135], v[112:115]
ds_read_b128 v[144:147], v25 offset:31744
v_mfma_f32_16x16x32_bf16 v[116:119], a[164:167], v[132:135], v[116:119]
v_mfma_f32_16x16x32_bf16 v[120:123], a[168:171], v[132:135], v[120:123]
v_mfma_f32_16x16x32_bf16 v[124:127], a[172:175], v[132:135], v[124:127]
s_waitcnt lgkmcnt(4)
v_add_f32 v176, v176, v180
v_add_f32 v177, v177, v181
v_mfma_f32_16x16x32_bf16 v[112:115], a[176:179], v[136:139], v[112:115]
s_waitcnt lgkmcnt(1)
ds_read_b128 v[132:135], v25 offset:32768
v_add_f32 v184, v184, v188
v_mfma_f32_16x16x32_bf16 v[116:119], a[180:183], v[136:139], v[116:119]
v_mfma_f32_16x16x32_bf16 v[120:123], a[184:187], v[136:139], v[120:123]
s_barrier
v_mfma_f32_16x16x32_bf16 v[124:127], a[188:191], v[136:139], v[124:127]
global_load_lds_dwordx4 v11, s[38:39] offset:-2048 sc1
v_add_f32 v185, v185, v189
v_mfma_f32_16x16x32_bf16 v[112:115], a[192:195], v[140:143], v[112:115]
s_mov_b64 s[76:77], -1
ds_read_b128 v[136:139], v25 offset:33792
v_add_f32 v176, v176, v184
v_mfma_f32_16x16x32_bf16 v[116:119], a[196:199], v[140:143], v[116:119]
v_add_f32 v177, v177, v185
v_mov_b32 v202, v176
v_mfma_f32_16x16x32_bf16 v[120:123], a[200:203], v[140:143], v[120:123]
ds_write_b128 v26, v[128:131] offset:41984
v_mfma_f32_16x16x32_bf16 v[124:127], a[204:207], v[140:143], v[124:127]
s_waitcnt lgkmcnt(3)
global_load_lds_dwordx4 v11, s[38:39] offset:-1024 sc1
v_mov_b32 v203, v177
v_mfma_f32_16x16x32_bf16 v[112:115], a[208:211], v[144:147], v[112:115]
ds_read_b128 v[140:143], v25 offset:34816
v_mfma_f32_16x16x32_bf16 v[116:119], a[212:215], v[144:147], v[116:119]
v_permlane16_swap_b32 v176, v202
v_mfma_f32_16x16x32_bf16 v[120:123], a[216:219], v[144:147], v[120:123]
global_load_lds_dwordx4 v11, s[38:39] offset:0 sc1
v_mfma_f32_16x16x32_bf16 v[124:127], a[220:223], v[144:147], v[124:127]
s_waitcnt lgkmcnt(3)
v_permlane16_swap_b32 v177, v203
v_mfma_f32_16x16x32_bf16 v[112:115], a[224:227], v[132:135], v[112:115]
ds_read_b128 v[144:147], v25 offset:35840
v_add_f32 v176, v176, v202
v_mfma_f32_16x16x32_bf16 v[116:119], a[228:231], v[132:135], v[116:119]
global_load_lds_dwordx4 v11, s[38:39] offset:1024 sc1
v_add_f32 v177, v177, v203
v_mfma_f32_16x16x32_bf16 v[120:123], a[232:235], v[132:135], v[120:123]
v_mov_b32 v202, v176
v_mov_b32 v203, v177
v_mfma_f32_16x16x32_bf16 v[124:127], a[236:239], v[132:135], v[124:127]
s_waitcnt lgkmcnt(3)
v_xor_b32 v25, 0x10000, v25
v_mfma_f32_16x16x32_bf16 v[112:115], a[240:243], v[136:139], v[112:115]
global_load_lds_dwordx4 v11, s[38:39] offset:2048 sc1
v_add_u32 v11, 18432, v11
v_mfma_f32_16x16x32_bf16 v[116:119], a[244:247], v[136:139], v[116:119]
v_mfma_f32_16x16x32_bf16 v[120:123], a[248:251], v[136:139], v[120:123]
s_waitcnt vmcnt(10) lgkmcnt(2)
s_barrier
v_mfma_f32_16x16x32_bf16 v[124:127], a[252:255], v[136:139], v[124:127]
s_waitcnt lgkmcnt(1)
ds_read_b128 v[132:135], v25 offset:0
v_min_u32 v11, v11, v12
v_mfma_f32_16x16x32_bf16 v[112:115], v[28:31], v[140:143], v[112:115]
ds_read_b128 v[136:139], v25 offset:1024
v_mfma_f32_16x16x32_bf16 v[116:119], v[32:35], v[140:143], v[116:119]
v_permlane32_swap_b32 v176, v202
v_mfma_f32_16x16x32_bf16 v[120:123], v[36:39], v[140:143], v[120:123]
v_permlane32_swap_b32 v177, v203
v_mfma_f32_16x16x32_bf16 v[124:127], v[40:43], v[140:143], v[124:127]
)"
R"(s_waitcnt lgkmcnt(2)
v_add_f32 v176, v176, v202
v_add_f32 v177, v177, v203
v_mfma_f32_16x16x32_bf16 v[112:115], v[44:47], v[144:147], v[112:115]
ds_read_b128 v[140:143], v25 offset:2048
v_mfma_f32_16x16x32_bf16 v[116:119], v[48:51], v[144:147], v[116:119]
v_mfma_f32_16x16x32_bf16 v[120:123], v[52:55], v[144:147], v[120:123]
v_mfma_f32_16x16x32_bf16 v[124:127], v[56:59], v[144:147], v[124:127]
s_sub_u32 s70, s70, 1
s_cbranch_scc0 .Lwo_top13
s_waitcnt vmcnt(7)
s_barrier
s_mov_b64 exec, 65535
global_store_dwordx2 v15, v[176:177], s[38:39] sc1
s_mov_b64 exec, -1
v_add_u32 v15, 18688, v15
v_cvt_pk_bf16_f32 v164, v112, v113
v_cvt_pk_bf16_f32 v165, v114, v115
v_cvt_pk_bf16_f32 v166, v116, v117
v_cvt_pk_bf16_f32 v167, v118, v119
global_store_dwordx4 v13, v[164:167], s[38:39] offset:0 sc1
v_cvt_pk_bf16_f32 v168, v120, v121
v_cvt_pk_bf16_f32 v169, v122, v123
v_cvt_pk_bf16_f32 v170, v124, v125
v_cvt_pk_bf16_f32 v171, v126, v127
global_store_dwordx4 v13, v[168:171], s[38:39] offset:1024 sc1
v_add_f32 v254, v112, v113
v_mul_f32 v255, v112, v112
v_fmac_f32 v255, v113, v113
v_add_f32 v254, v254, v114
v_fmac_f32 v255, v114, v114
v_add_f32 v254, v254, v115
v_fmac_f32 v255, v115, v115
v_add_f32 v254, v254, v116
v_fmac_f32 v255, v116, v116
v_add_f32 v254, v254, v117
v_fmac_f32 v255, v117, v117
v_add_f32 v254, v254, v118
v_fmac_f32 v255, v118, v118
v_add_f32 v254, v254, v119
v_fmac_f32 v255, v119, v119
v_add_f32 v254, v254, v120
v_fmac_f32 v255, v120, v120
v_add_f32 v254, v254, v121
v_fmac_f32 v255, v121, v121
v_add_f32 v254, v254, v122
v_fmac_f32 v255, v122, v122
v_add_f32 v254, v254, v123
v_fmac_f32 v255, v123, v123
v_add_f32 v254, v254, v124
v_fmac_f32 v255, v124, v124
v_add_f32 v254, v254, v125
v_fmac_f32 v255, v125, v125
v_add_f32 v254, v254, v126
v_fmac_f32 v255, v126, v126
v_add_f32 v254, v254, v127
v_fmac_f32 v255, v127, v127
ds_write_b64 v27, v[254:255] offset:46080
s_waitcnt vmcnt(0) lgkmcnt(0)
s_barrier
ds_read_b64 v[176:177], v27 offset:43008
ds_read_b64 v[180:181], v27 offset:43520
ds_read_b64 v[184:185], v27 offset:44032
ds_read_b64 v[188:189], v27 offset:44544
s_waitcnt lgkmcnt(2)
v_add_f32 v176, v176, v180
v_add_f32 v177, v177, v181
s_waitcnt lgkmcnt(0)
v_add_f32 v184, v184, v188
v_add_f32 v185, v185, v189
v_add_f32 v176, v176, v184
v_add_f32 v177, v177, v185
v_mov_b32 v202, v176
v_mov_b32 v203, v177
s_nop 0
v_permlane16_swap_b32 v176, v202
v_permlane16_swap_b32 v177, v203
v_add_f32 v176, v176, v202
v_add_f32 v177, v177, v203
v_mov_b32 v202, v176
v_mov_b32 v203, v177
s_nop 0
v_permlane32_swap_b32 v176, v202
v_permlane32_swap_b32 v177, v203
v_add_f32 v176, v176, v202
v_add_f32 v177, v177, v203
s_mov_b64 exec, 65535
global_store_dwordx2 v15, v[176:177], s[38:39] sc1
s_mov_b64 exec, -1
v_add_u32 v15, 18688, v15
ds_read_b64 v[176:177], v27 offset:45056
ds_read_b64 v[180:181], v27 offset:45568
ds_read_b64 v[184:185], v27 offset:46080
ds_read_b64 v[188:189], v27 offset:46592
s_waitcnt lgkmcnt(2)
v_add_f32 v176, v176, v180
v_add_f32 v177, v177, v181
s_waitcnt lgkmcnt(0)
v_add_f32 v184, v184, v188
v_add_f32 v185, v185, v189
v_add_f32 v176, v176, v184
v_add_f32 v177, v177, v185
v_mov_b32 v202, v176
v_mov_b32 v203, v177
s_nop 0
v_permlane16_swap_b32 v176, v202
v_permlane16_swap_b32 v177, v203
v_add_f32 v176, v176, v202
v_add_f32 v177, v177, v203
v_mov_b32 v202, v176
v_mov_b32 v203, v177
s_nop 0
v_permlane32_swap_b32 v176, v202
v_permlane32_swap_b32 v177, v203
v_add_f32 v176, v176, v202
v_add_f32 v177, v177, v203
s_mov_b64 exec, 65535
global_store_dwordx2 v15, v[176:177], s[38:39] sc1
s_mov_b64 exec, -1
v_add_u32 v15, 18688, v15
s_waitcnt vmcnt(0)
s_barrier
s_endpgm
.Lwo_w4:
s_add_u32 m0, s48, 4096
s_nop 0
global_load_lds_dwordx4 v0, s[40:41] offset:-4096
global_load_lds_dwordx4 v1, s[40:41] offset:-3072
global_load_lds_dwordx4 v2, s[40:41] offset:-2048
global_load_lds_dwordx4 v3, s[40:41] offset:-1024
global_load_lds_dwordx4 v4, s[40:41] offset:0
global_load_lds_dwordx4 v5, s[40:41] offset:1024
global_load_lds_dwordx4 v6, s[40:41] offset:2048
global_load_lds_dwordx4 v7, s[40:41] offset:3072
s_add_u32 m0, s48, 12288
s_nop 0
global_load_lds_dwordx4 v8, s[40:41] offset:-4096
s_add_u32 s40, s40, 73728
s_addc_u32 s41, s41, 0
s_add_u32 m0, s48, 40960
s_nop 0
global_load_lds_dwordx4 v0, s[40:41] offset:-4096
global_load_lds_dwordx4 v1, s[40:41] offset:-3072
global_load_lds_dwordx4 v2, s[40:41] offset:-2048
global_load_lds_dwordx4 v3, s[40:41] offset:-1024
global_load_lds_dwordx4 v4, s[40:41] offset:0
global_load_lds_dwordx4 v5, s[40:41] offset:1024
global_load_lds_dwordx4 v6, s[40:41] offset:2048
global_load_lds_dwordx4 v7, s[40:41] offset:3072
s_add_u32 m0, s48, 49152
s_nop 0
global_load_lds_dwordx4 v8, s[40:41] offset:-4096
s_add_u32 s40, s40, 73728
s_addc_u32 s41, s41, 0
s_add_u32 m0, s48, 77824
s_nop 0
global_load_lds_dwordx4 v0, s[40:41] offset:-4096
global_load_lds_dwordx4 v1, s[40:41] offset:-3072
global_load_lds_dwordx4 v2, s[40:41] offset:-2048
global_load_lds_dwordx4 v3, s[40:41] offset:-1024
global_load_lds_dwordx4 v4, s[40:41] offset:0
global_load_lds_dwordx4 v5, s[40:41] offset:1024
global_load_lds_dwordx4 v6, s[40:41] offset:2048
global_load_lds_dwordx4 v7, s[40:41] offset:3072
s_add_u32 m0, s48, 86016
s_nop 0
global_load_lds_dwordx4 v8, s[40:41] offset:-4096
s_add_u32 s40, s40, 73728
s_addc_u32 s41, s41, 0
v_add_u32 v204, 0x10000, v9
v_add_u32 v205, 0x20000, v9
v_add_u32 v206, 0x10000, v10
v_add_u32 v207, 0x20000, v10
s_waitcnt lgkmcnt(0)
s_mul_i32 s37, s37, 0x9E3779B1
s_add_u32 s37, s37, 0x7F4A7C15
v_add_u32 v22, s37, v22
v_add_u32 v24, s37, v24
s_waitcnt vmcnt(18)
s_barrier
s_add_u32 m0, s48, 114688
ds_read_b64_tr_b16 a[0:1], v9 offset:0
ds_read_b64_tr_b16 a[2:3], v9 offset:2304
global_load_lds_dwordx4 v0, s[40:41] offset:-4096
ds_read_b64_tr_b16 a[4:5], v9 offset:8
global_load_lds_dwordx4 v1, s[40:41] offset:-3072
ds_read_b64_tr_b16 a[6:7], v9 offset:2312
global_load_lds_dwordx4 v2, s[40:41] offset:-2048
ds_read_b64_tr_b16 a[8:9], v9 offset:64
global_load_lds_dwordx4 v3, s[40:41] offset:-1024
ds_read_b64_tr_b16 a[10:11], v9 offset:2368
ds_read_b64_tr_b16 a[12:13], v9 offset:72
global_load_lds_dwordx4 v4, s[40:41] offset:0
ds_read_b64_tr_b16 a[14:15], v9 offset:2376
global_load_lds_dwordx4 v5, s[40:41] offset:1024
ds_read_b64_tr_b16 a[16:17], v9 offset:18432
global_load_lds_dwordx4 v6, s[40:41] offset:2048
ds_read_b64_tr_b16 a[18:19], v9 offset:20736
global_load_lds_dwordx4 v7, s[40:41] offset:3072
ds_read_b64_tr_b16 a[20:21], v9 offset:18440
ds_read_b64_tr_b16 a[22:23], v9 offset:20744
s_add_u32 m0, s48, 122880
ds_read_b64_tr_b16 a[24:25], v9 offset:18496
global_load_lds_dwordx4 v8, s[40:41] offset:-4096
ds_read_b64_tr_b16 a[26:27], v9 offset:20800
s_add_u32 s40, s40, 73728
ds_read_b64_tr_b16 a[28:29], v9 offset:18504
s_addc_u32 s41, s41, 0
ds_read_b64_tr_b16 a[30:31], v9 offset:20808
s_waitcnt vmcnt(18) lgkmcnt(0)
s_barrier
s_add_u32 m0, s48, 4096
ds_read_b64_tr_b16 a[32:33], v9 offset:36864
ds_read_b64_tr_b16 a[34:35], v9 offset:39168
global_load_lds_dwordx4 v0, s[40:41] offset:-4096
ds_read_b64_tr_b16 a[36:37], v9 offset:36872
global_load_lds_dwordx4 v1, s[40:41] offset:-3072
ds_read_b64_tr_b16 a[38:39], v9 offset:39176
global_load_lds_dwordx4 v2, s[40:41] offset:-2048
ds_read_b64_tr_b16 a[40:41], v9 offset:36928
global_load_lds_dwordx4 v3, s[40:41] offset:-1024
ds_read_b64_tr_b16 a[42:43], v9 offset:39232
ds_read_b64_tr_b16 a[44:45], v9 offset:36936
global_load_lds_dwordx4 v4, s[40:41] offset:0
ds_read_b64_tr_b16 a[46:47], v9 offset:39240
global_load_lds_dwordx4 v5, s[40:41] offset:1024
ds_read_b64_tr_b16 a[48:49], v9 offset:55296
global_load_lds_dwordx4 v6, s[40:41] offset:2048
ds_read_b64_tr_b16 a[50:51], v9 offset:57600
global_load_lds_dwordx4 v7, s[40:41] offset:3072
ds_read_b64_tr_b16 a[52:53], v9 offset:55304
ds_read_b64_tr_b16 a[54:55], v9 offset:57608
s_add_u32 m0, s48, 12288
ds_read_b64_tr_b16 a[56:57], v9 offset:55360
global_load_lds_dwordx4 v8, s[40:41] offset:-4096
ds_read_b64_tr_b16 a[58:59], v9 offset:57664
s_add_u32 s40, s40, 73728
ds_read_b64_tr_b16 a[60:61], v9 offset:55368
s_addc_u32 s41, s41, 0
ds_read_b64_tr_b16 a[62:63], v9 offset:57672
s_waitcnt vmcnt(18) lgkmcnt(0)
s_barrier
s_add_u32 m0, s48, 40960
ds_read_b64_tr_b16 a[64:65], v204 offset:8192
ds_read_b64_tr_b16 a[66:67], v204 offset:10496
global_load_lds_dwordx4 v0, s[40:41] offset:-4096
ds_read_b64_tr_b16 a[68:69], v204 offset:8200
global_load_lds_dwordx4 v1, s[40:41] offset:-3072
ds_read_b64_tr_b16 a[70:71], v204 offset:10504
global_load_lds_dwordx4 v2, s[40:41] offset:-2048
ds_read_b64_tr_b16 a[72:73], v204 offset:8256
global_load_lds_dwordx4 v3, s[40:41] offset:-1024
ds_read_b64_tr_b16 a[74:75], v204 offset:10560
ds_read_b64_tr_b16 a[76:77], v204 offset:8264
global_load_lds_dwordx4 v4, s[40:41] offset:0
ds_read_b64_tr_b16 a[78:79], v204 offset:10568
global_load_lds_dwordx4 v5, s[40:41] offset:1024
ds_read_b64_tr_b16 a[80:81], v204 offset:26624
global_load_lds_dwordx4 v6, s[40:41] offset:2048
ds_read_b64_tr_b16 a[82:83], v204 offset:28928
global_load_lds_dwordx4 v7, s[40:41] offset:3072
ds_read_b64_tr_b16 a[84:85], v204 offset:26632
ds_read_b64_tr_b16 a[86:87], v204 offset:28936
s_add_u32 m0, s48, 49152
ds_read_b64_tr_b16 a[88:89], v204 offset:26688
global_load_lds_dwordx4 v8, s[40:41] offset:-4096
ds_read_b64_tr_b16 a[90:91], v204 offset:28992
s_add_u32 s40, s40, 73728
ds_read_b64_tr_b16 a[92:93], v204 offset:26696
s_addc_u32 s41, s41, 0
ds_read_b64_tr_b16 a[94:95], v204 offset:29000
s_waitcnt vmcnt(18) lgkmcnt(0)
s_barrier
s_add_u32 m0, s48, 77824
ds_read_b64_tr_b16 a[96:97], v204 offset:45056
ds_read_b64_tr_b16 a[98:99], v204 offset:47360
global_load_lds_dwordx4 v0, s[40:41] offset:-4096
ds_read_b64_tr_b16 a[100:101], v204 offset:45064
global_load_lds_dwordx4 v1, s[40:41] offset:-3072
ds_read_b64_tr_b16 a[102:103], v204 offset:47368
global_load_lds_dwordx4 v2, s[40:41] offset:-2048
ds_read_b64_tr_b16 a[104:105], v204 offset:45120
global_load_lds_dwordx4 v3, s[40:41] offset:-1024
ds_read_b64_tr_b16 a[106:107], v204 offset:47424
ds_read_b64_tr_b16 a[108:109], v204 offset:45128
global_load_lds_dwordx4 v4, s[40:41] offset:0
ds_read_b64_tr_b16 a[110:111], v204 offset:47432
global_load_lds_dwordx4 v5, s[40:41] offset:1024
ds_read_b64_tr_b16 a[112:113], v204 offset:63488
global_load_lds_dwordx4 v6, s[40:41] offset:2048
ds_read_b64_tr_b16 a[114:115], v205 offset:256
global_load_lds_dwordx4 v7, s[40:41] offset:3072
ds_read_b64_tr_b16 a[116:117], v204 offset:63496
ds_read_b64_tr_b16 a[118:119], v205 offset:264
s_add_u32 m0, s48, 86016
ds_read_b64_tr_b16 a[120:121], v204 offset:63552
global_load_lds_dwordx4 v8, s[40:41] offset:-4096
ds_read_b64_tr_b16 a[122:123], v205 offset:320
s_add_u32 s40, s40, 73728
ds_read_b64_tr_b16 a[124:125], v204 offset:63560
s_addc_u32 s41, s41, 0
ds_read_b64_tr_b16 a[126:127], v205 offset:328
s_waitcnt vmcnt(18) lgkmcnt(0)
s_barrier
s_add_u32 m0, s48, 114688
ds_read_b64_tr_b16 a[128:129], v9 offset:0
ds_read_b64_tr_b16 a[130:131], v9 offset:2304
global_load_lds_dwordx4 v0, s[40:41] offset:-4096
ds_read_b64_tr_b16 a[132:133], v9 offset:8
global_load_lds_dwordx4 v1, s[40:41] offset:-3072
ds_read_b64_tr_b16 a[134:135], v9 offset:2312
ds_read_b64_tr_b16 a[136:137], v9 offset:64
global_load_lds_dwordx4 v2, s[40:41] offset:-2048
ds_read_b64_tr_b16 a[138:139], v9 offset:2368
global_load_lds_dwordx4 v3, s[40:41] offset:-1024
ds_read_b64_tr_b16 a[140:141], v9 offset:72
global_load_lds_dwordx4 v4, s[40:41] offset:0
ds_read_b64_tr_b16 a[142:143], v9 offset:2376
ds_read_b64_tr_b16 a[144:145], v9 offset:18432
global_load_lds_dwordx4 v5, s[40:41] offset:1024
ds_read_b64_tr_b16 a[146:147], v9 offset:20736
global_load_lds_dwordx4 v6, s[40:41] offset:2048
ds_read_b64_tr_b16 a[148:149], v9 offset:18440
ds_read_b64_tr_b16 a[150:151], v9 offset:20744
global_load_lds_dwordx4 v7, s[40:41] offset:3072
ds_read_b64_tr_b16 a[152:153], v9 offset:18496
s_add_u32 m0, s48, 122880
ds_read_b64_tr_b16 a[154:155], v9 offset:20800
global_load_lds_dwordx4 v8, s[40:41] offset:-4096
ds_read_b64_tr_b16 a[156:157], v9 offset:18504
ds_read_b64_tr_b16 a[158:159], v9 offset:20808
s_add_u32 s40, s40, 73728
ds_read_b64_tr_b16 v[60:61], v10 offset:18432
s_addc_u32 s41, s41, 0
ds_read_b64_tr_b16 v[62:63], v10 offset:20736
s_waitcnt vmcnt(18) lgkmcnt(0)
s_barrier
s_add_u32 m0, s48, 4096
ds_read_b64_tr_b16 a[160:161], v9 offset:36864
ds_read_b64_tr_b16 a[162:163], v9 offset:39168
global_load_lds_dwordx4 v0, s[40:41] offset:-4096
ds_read_b64_tr_b16 a[164:165], v9 offset:36872
ds_read_b64_tr_b16 a[166:167], v9 offset:39176
global_load_lds_dwordx4 v1, s[40:41] offset:-3072
ds_read_b64_tr_b16 a[168:169], v9 offset:36928
global_load_lds_dwordx4 v2, s[40:41] offset:-2048
ds_read_b64_tr_b16 a[170:171], v9 offset:39232
ds_read_b64_tr_b16 a[172:173], v9 offset:36936
global_load_lds_dwordx4 v3, s[40:41] offset:-1024
ds_read_b64_tr_b16 a[174:175], v9 offset:39240
global_load_lds_dwordx4 v4, s[40:41] offset:0
ds_read_b64_tr_b16 v[64:65], v10 offset:36864
ds_read_b64_tr_b16 v[66:67], v10 offset:39168
global_load_lds_dwordx4 v5, s[40:41] offset:1024
ds_read_b64_tr_b16 a[176:177], v9 offset:55296
global_load_lds_dwordx4 v6, s[40:41] offset:2048
ds_read_b64_tr_b16 a[178:179], v9 offset:57600
ds_read_b64_tr_b16 a[180:181], v9 offset:55304
global_load_lds_dwordx4 v7, s[40:41] offset:3072
ds_read_b64_tr_b16 a[182:183], v9 offset:57608
s_add_u32 m0, s48, 12288
ds_read_b64_tr_b16 a[184:185], v9 offset:55360
ds_read_b64_tr_b16 a[186:187], v9 offset:57664
global_load_lds_dwordx4 v8, s[40:41] offset:-4096
ds_read_b64_tr_b16 a[188:189], v9 offset:55368
s_sub_u32 s40, s40, 589824
ds_read_b64_tr_b16 a[190:191], v9 offset:57672
ds_read_b64_tr_b16 v[68:69], v10 offset:55296
s_subb_u32 s41, s41, 0
ds_read_b64_tr_b16 v[70:71], v10 offset:57600
s_waitcnt vmcnt(18) lgkmcnt(0)
s_barrier
ds_read_b64_tr_b16 a[192:193], v204 offset:8192
ds_read_b64_tr_b16 a[194:195], v204 offset:10496
ds_read_b64_tr_b16 a[196:197], v204 offset:8200
ds_read_b64_tr_b16 a[198:199], v204 offset:10504
ds_read_b64_tr_b16 a[200:201], v204 offset:8256
ds_read_b64_tr_b16 a[202:203], v204 offset:10560
ds_read_b64_tr_b16 a[204:205], v204 offset:8264
ds_read_b64_tr_b16 a[206:207], v204 offset:10568
ds_read_b64_tr_b16 v[72:73], v206 offset:8192
ds_read_b64_tr_b16 v[74:75], v206 offset:10496
ds_read_b64_tr_b16 a[208:209], v204 offset:26624
ds_read_b64_tr_b16 a[210:211], v204 offset:28928
ds_read_b64_tr_b16 a[212:213], v204 offset:26632
ds_read_b64_tr_b16 a[214:215], v204 offset:28936
ds_read_b64_tr_b16 a[216:217], v204 offset:26688
ds_read_b64_tr_b16 a[218:219], v204 offset:28992
ds_read_b64_tr_b16 a[220:221], v204 offset:26696
ds_read_b64_tr_b16 a[222:223], v204 offset:29000
ds_read_b64_tr_b16 v[76:77], v206 offset:26624
ds_read_b64_tr_b16 v[78:79], v206 offset:28928
s_waitcnt vmcnt(9) lgkmcnt(0)
s_barrier
ds_read_b64_tr_b16 a[224:225], v204 offset:45056
ds_read_b64_tr_b16 a[226:227], v204 offset:47360
ds_read_b64_tr_b16 a[228:229], v204 offset:45064
ds_read_b64_tr_b16 a[230:231], v204 offset:47368
ds_read_b64_tr_b16 a[232:233], v204 offset:45120
ds_read_b64_tr_b16 a[234:235], v204 offset:47424
)"
R"(ds_read_b64_tr_b16 a[236:237], v204 offset:45128
ds_read_b64_tr_b16 a[238:239], v204 offset:47432
ds_read_b64_tr_b16 v[80:81], v206 offset:45056
ds_read_b64_tr_b16 v[82:83], v206 offset:47360
ds_read_b64_tr_b16 a[240:241], v204 offset:63488
ds_read_b64_tr_b16 a[242:243], v205 offset:256
ds_read_b64_tr_b16 a[244:245], v204 offset:63496
ds_read_b64_tr_b16 a[246:247], v205 offset:264
ds_read_b64_tr_b16 a[248:249], v204 offset:63552
ds_read_b64_tr_b16 a[250:251], v205 offset:320
ds_read_b64_tr_b16 a[252:253], v204 offset:63560
ds_read_b64_tr_b16 a[254:255], v205 offset:328
ds_read_b64_tr_b16 v[84:85], v206 offset:63488
ds_read_b64_tr_b16 v[86:87], v207 offset:256
s_waitcnt vmcnt(0) lgkmcnt(0)
s_barrier
ds_read_b64_tr_b16 v[28:29], v9 offset:0
ds_read_b64_tr_b16 v[30:31], v9 offset:2304
ds_read_b64_tr_b16 v[32:33], v9 offset:8
ds_read_b64_tr_b16 v[34:35], v9 offset:2312
ds_read_b64_tr_b16 v[36:37], v9 offset:64
ds_read_b64_tr_b16 v[38:39], v9 offset:2368
ds_read_b64_tr_b16 v[40:41], v9 offset:72
ds_read_b64_tr_b16 v[42:43], v9 offset:2376
ds_read_b64_tr_b16 v[88:89], v10 offset:0
ds_read_b64_tr_b16 v[90:91], v10 offset:2304
ds_read_b64_tr_b16 v[44:45], v9 offset:18432
ds_read_b64_tr_b16 v[46:47], v9 offset:20736
ds_read_b64_tr_b16 v[48:49], v9 offset:18440
ds_read_b64_tr_b16 v[50:51], v9 offset:20744
ds_read_b64_tr_b16 v[52:53], v9 offset:18496
ds_read_b64_tr_b16 v[54:55], v9 offset:20800
ds_read_b64_tr_b16 v[56:57], v9 offset:18504
ds_read_b64_tr_b16 v[58:59], v9 offset:20808
ds_read_b64_tr_b16 v[92:93], v10 offset:18432
ds_read_b64_tr_b16 v[94:95], v10 offset:20736
s_waitcnt lgkmcnt(0)
s_barrier
v_add_lshl_u32 v252, v24, 0, 7
v_bfi_b32 v252, s69, v252, v23
v_add_lshl_u32 v253, v22, 0, 7
v_bfi_b32 v253, s69, v253, v21
s_mov_b64 s[76:77], 0
s_add_u32 m0, s49, 18432
s_mov_b32 s70, 33
s_waitcnt vmcnt(0) lgkmcnt(0)
s_nop 7
.p2align 6
.Lwo_top15:
s_waitcnt lgkmcnt(5)
v_mfma_f32_16x16x32_bf16 v[96:99], a[144:147], v[132:135], v[96:99]
s_waitcnt lgkmcnt(4)
ds_read_b128 v[144:147], v25 offset:12288
v_add_f32 v176, v176, v184
v_mfma_f32_16x16x32_bf16 v[100:103], a[148:151], v[132:135], v[100:103]
v_mfma_f32_16x16x32_bf16 v[104:107], a[152:155], v[132:135], v[104:107]
s_waitcnt vmcnt(6)
s_barrier
v_mfma_f32_16x16x32_bf16 v[108:111], a[156:159], v[132:135], v[108:111]
v_add_f32 v177, v177, v185
v_add_f32 v178, v178, v186
v_mfma_f32_16x16x32_bf16 v[128:131], v[60:63], v[132:135], 0
s_waitcnt lgkmcnt(3)
v_add_f32 v179, v179, v187
v_add_f32 v176, v176, v188
v_mfma_f32_16x16x32_bf16 v[96:99], a[160:163], v[136:139], v[96:99]
ds_read_b128 v[132:135], v25 offset:13312
v_add_f32 v177, v177, v189
v_mfma_f32_16x16x32_bf16 v[100:103], a[164:167], v[136:139], v[100:103]
v_add_f32 v178, v178, v190
v_add_f32 v179, v179, v191
v_mfma_f32_16x16x32_bf16 v[104:107], a[168:171], v[136:139], v[104:107]
s_waitcnt lgkmcnt(3)
v_add_f32 v180, v180, v192
v_add_f32 v181, v181, v193
v_mfma_f32_16x16x32_bf16 v[108:111], a[172:175], v[136:139], v[108:111]
v_add_f32 v182, v182, v194
v_add_f32 v183, v183, v195
v_mfma_f32_16x16x32_bf16 v[128:131], v[64:67], v[136:139], v[128:131]
s_waitcnt lgkmcnt(2)
v_cvt_pk_bf16_f32 v172, v176, v177
v_cvt_pk_bf16_f32 v173, v178, v179
v_mfma_f32_16x16x32_bf16 v[96:99], a[176:179], v[140:143], v[96:99]
s_cmp_eq_u32 s70, 32
ds_read_b128 v[136:139], v25 offset:14336
v_cvt_pk_bf16_f32 v174, v180, v181
v_mfma_f32_16x16x32_bf16 v[100:103], a[180:183], v[140:143], v[100:103]
s_cselect_b64 s[42:43], s[44:45], s[42:43]
v_cvt_pk_bf16_f32 v175, v182, v183
v_cvt_pk_bf16_f32 v164, v112, v113
v_mfma_f32_16x16x32_bf16 v[104:107], a[184:187], v[140:143], v[104:107]
s_cmp_eq_u32 s70, 16
global_store_dwordx4 v14, v[172:175], s[38:39] sc1
v_mfma_f32_16x16x32_bf16 v[108:111], a[188:191], v[140:143], v[108:111]
s_cselect_b64 s[42:43], s[46:47], s[42:43]
v_add_u32 v14, 18688, v14
v_cvt_pk_bf16_f32 v165, v114, v115
v_mfma_f32_16x16x32_bf16 v[128:131], v[68:71], v[140:143], v[128:131]
s_waitcnt lgkmcnt(2)
v_cvt_pk_bf16_f32 v166, v116, v117
v_cvt_pk_bf16_f32 v167, v118, v119
v_mfma_f32_16x16x32_bf16 v[96:99], a[192:195], v[144:147], v[96:99]
s_xor_b32 s49, s49, 0x10000
ds_read_b128 v[140:143], v25 offset:15360
v_add_f32 v254, v112, v113
v_mfma_f32_16x16x32_bf16 v[100:103], a[196:199], v[144:147], v[100:103]
s_mov_b32 m0, 53248
global_store_dwordx4 v13, v[164:167], s[38:39] offset:0 sc1
v_mfma_f32_16x16x32_bf16 v[104:107], a[200:203], v[144:147], v[104:107]
v_cvt_pk_bf16_f32 v168, v120, v121
v_cvt_pk_bf16_f32 v169, v122, v123
v_mfma_f32_16x16x32_bf16 v[108:111], a[204:207], v[144:147], v[108:111]
v_cvt_pk_bf16_f32 v170, v124, v125
v_cvt_pk_bf16_f32 v171, v126, v127
v_mfma_f32_16x16x32_bf16 v[128:131], v[72:75], v[144:147], v[128:131]
s_waitcnt lgkmcnt(2)
global_store_dwordx4 v13, v[168:171], s[38:39] offset:1024 sc1
v_mfma_f32_16x16x32_bf16 v[96:99], a[208:211], v[132:135], v[96:99]
ds_read_b128 v[144:147], v25 offset:16384
v_add_u32 v13, 18688, v13
v_mfma_f32_16x16x32_bf16 v[100:103], a[212:215], v[132:135], v[100:103]
v_mul_f32 v255, v112, v112
v_fmac_f32 v255, v113, v113
v_mfma_f32_16x16x32_bf16 v[104:107], a[216:219], v[132:135], v[104:107]
v_add_f32 v254, v254, v114
v_fmac_f32 v255, v114, v114
v_mfma_f32_16x16x32_bf16 v[108:111], a[220:223], v[132:135], v[108:111]
v_add_f32 v254, v254, v115
v_fmac_f32 v255, v115, v115
v_mfma_f32_16x16x32_bf16 v[128:131], v[76:79], v[132:135], v[128:131]
s_waitcnt vmcnt(7)
global_load_lds_dwordx4 v16, s[42:43] offset:0 sc1
ds_read_b128 v[148:151], v19 offset:8192
s_waitcnt lgkmcnt(3)
v_mfma_f32_16x16x32_bf16 v[96:99], a[224:227], v[136:139], v[96:99]
ds_read_b128 v[132:135], v25 offset:17408
ds_read_b128 v[152:155], v20 offset:8192
v_mfma_f32_16x16x32_bf16 v[100:103], a[228:231], v[136:139], v[100:103]
v_add_f32 v254, v254, v116
v_fmac_f32 v255, v116, v116
v_mfma_f32_16x16x32_bf16 v[104:107], a[232:235], v[136:139], v[104:107]
v_add_f32 v254, v254, v117
v_fmac_f32 v255, v117, v117
v_mfma_f32_16x16x32_bf16 v[108:111], a[236:239], v[136:139], v[108:111]
v_add_f32 v254, v254, v118
v_fmac_f32 v255, v118, v118
v_mfma_f32_16x16x32_bf16 v[128:131], v[80:83], v[136:139], v[128:131]
s_waitcnt lgkmcnt(4)
v_add_f32 v254, v254, v119
v_fmac_f32 v255, v119, v119
v_mfma_f32_16x16x32_bf16 v[96:99], a[240:243], v[140:143], v[96:99]
ds_read_b128 v[136:139], v25 offset:0
v_add_f32 v254, v254, v120
v_mfma_f32_16x16x32_bf16 v[100:103], a[244:247], v[140:143], v[100:103]
global_load_lds_dwordx4 v17, s[42:43] offset:1024 sc1
v_fmac_f32 v255, v120, v120
v_mfma_f32_16x16x32_bf16 v[104:107], a[248:251], v[140:143], v[104:107]
v_add_f32 v254, v254, v121
v_fmac_f32 v255, v121, v121
v_mfma_f32_16x16x32_bf16 v[108:111], a[252:255], v[140:143], v[108:111]
v_add_f32 v254, v254, v122
v_fmac_f32 v255, v122, v122
v_mfma_f32_16x16x32_bf16 v[128:131], v[84:87], v[140:143], v[128:131]
s_waitcnt lgkmcnt(4)
v_add_f32 v254, v254, v123
v_fmac_f32 v255, v123, v123
v_mfma_f32_16x16x32_bf16 v[96:99], v[28:31], v[144:147], v[96:99]
ds_read_b128 v[140:143], v25 offset:1024
v_add_f32 v254, v254, v124
v_mfma_f32_16x16x32_bf16 v[100:103], v[32:35], v[144:147], v[100:103]
s_add_u32 m0, s49, 0
v_fmac_f32 v255, v124, v124
v_add_f32 v254, v254, v125
v_mfma_f32_16x16x32_bf16 v[104:107], v[36:39], v[144:147], v[104:107]
v_fmac_f32 v255, v125, v125
v_add_f32 v254, v254, v126
v_mfma_f32_16x16x32_bf16 v[108:111], v[40:43], v[144:147], v[108:111]
v_fmac_f32 v255, v126, v126
v_add_f32 v254, v254, v127
v_mfma_f32_16x16x32_bf16 v[128:131], v[88:91], v[144:147], v[128:131]
s_waitcnt lgkmcnt(3)
v_fmac_f32 v255, v127, v127
v_add_f32 v254, v254, v176
v_mfma_f32_16x16x32_bf16 v[96:99], v[44:47], v[132:135], v[96:99]
ds_read_b128 v[144:147], v25 offset:2048
v_fmac_f32 v255, v176, v176
v_mfma_f32_16x16x32_bf16 v[100:103], v[48:51], v[132:135], v[100:103]
v_add_f32 v254, v254, v177
v_fmac_f32 v255, v177, v177
v_mfma_f32_16x16x32_bf16 v[104:107], v[52:55], v[132:135], v[104:107]
v_add_f32 v254, v254, v178
v_fmac_f32 v255, v178, v178
v_mfma_f32_16x16x32_bf16 v[108:111], v[56:59], v[132:135], v[108:111]
v_add_f32 v254, v254, v179
v_fmac_f32 v255, v179, v179
v_mfma_f32_16x16x32_bf16 v[128:131], v[92:95], v[132:135], v[128:131]
s_waitcnt lgkmcnt(2)
v_add_f32 v254, v254, v180
v_fmac_f32 v255, v180, v180
v_mfma_f32_16x16x32_bf16 v[96:99], a[0:3], v[136:139], v[96:99]
ds_read_b128 v[132:135], v25 offset:3072
v_add_f32 v254, v254, v181
v_mfma_f32_16x16x32_bf16 v[100:103], a[4:7], v[136:139], v[100:103]
v_fmac_f32 v255, v181, v181
v_add_f32 v254, v254, v182
v_mfma_f32_16x16x32_bf16 v[104:107], a[8:11], v[136:139], v[104:107]
v_fmac_f32 v255, v182, v182
v_add_f32 v254, v254, v183
v_mfma_f32_16x16x32_bf16 v[108:111], a[12:15], v[136:139], v[108:111]
s_waitcnt lgkmcnt(2)
v_fmac_f32 v255, v183, v183
v_mfma_f32_16x16x32_bf16 v[96:99], a[16:19], v[140:143], v[96:99]
ds_read_b128 v[136:139], v25 offset:4096
v_mfma_f32_16x16x32_bf16 v[100:103], a[20:23], v[140:143], v[100:103]
ds_write_b64 v27, v[254:255] offset:46592
v_mfma_f32_16x16x32_bf16 v[104:107], a[24:27], v[140:143], v[104:107]
v_lshlrev_b32 v176, 16, v156
v_and_b32 v177, 0xffff0000, v156
v_mfma_f32_16x16x32_bf16 v[108:111], a[28:31], v[140:143], v[108:111]
s_waitcnt lgkmcnt(3)
v_lshlrev_b32 v178, 16, v157
v_and_b32 v179, 0xffff0000, v157
v_mfma_f32_16x16x32_bf16 v[96:99], a[32:35], v[144:147], v[96:99]
ds_read_b128 v[140:143], v25 offset:5120
v_lshlrev_b32 v180, 16, v158
v_mfma_f32_16x16x32_bf16 v[100:103], a[36:39], v[144:147], v[100:103]
v_mfma_f32_16x16x32_bf16 v[104:107], a[40:43], v[144:147], v[104:107]
s_barrier
v_mfma_f32_16x16x32_bf16 v[108:111], a[44:47], v[144:147], v[108:111]
s_waitcnt lgkmcnt(3)
v_and_b32 v181, 0xffff0000, v158
v_lshlrev_b32 v182, 16, v159
v_mfma_f32_16x16x32_bf16 v[96:99], a[48:51], v[132:135], v[96:99]
ds_read_b128 v[144:147], v25 offset:6144
global_load_lds_dwordx4 v11, s[38:39] offset:-2048 sc1
v_mfma_f32_16x16x32_bf16 v[100:103], a[52:55], v[132:135], v[100:103]
v_and_b32 v183, 0xffff0000, v159
v_add_f32 v180, v180, v128
v_mfma_f32_16x16x32_bf16 v[104:107], a[56:59], v[132:135], v[104:107]
v_add_f32 v181, v181, v129
v_add_f32 v182, v182, v130
v_mfma_f32_16x16x32_bf16 v[108:111], a[60:63], v[132:135], v[108:111]
s_waitcnt lgkmcnt(3)
global_load_lds_dwordx4 v11, s[38:39] offset:-1024 sc1
v_add_f32 v183, v183, v131
v_mfma_f32_16x16x32_bf16 v[96:99], a[64:67], v[136:139], v[96:99]
ds_read_b128 v[132:135], v25 offset:7168
global_load_dwordx4 v[156:159], v18, s[42:43] sc1
v_mfma_f32_16x16x32_bf16 v[100:103], a[68:71], v[136:139], v[100:103]
s_sub_u32 s42, s42, 18432
v_lshlrev_b32 v112, 16, v148
v_and_b32 v113, 0xffff0000, v148
v_mfma_f32_16x16x32_bf16 v[104:107], a[72:75], v[136:139], v[104:107]
s_subb_u32 s43, s43, 0
global_load_lds_dwordx4 v11, s[38:39] offset:0 sc1
v_add_u32 v11, 18432, v11
v_mfma_f32_16x16x32_bf16 v[108:111], a[76:79], v[136:139], v[108:111]
s_waitcnt lgkmcnt(2)
v_min_u32 v11, v11, v12
v_lshlrev_b32 v114, 16, v149
v_mfma_f32_16x16x32_bf16 v[96:99], a[80:83], v[140:143], v[96:99]
ds_read_b128 v[136:139], v25 offset:8192
v_and_b32 v115, 0xffff0000, v149
v_mfma_f32_16x16x32_bf16 v[100:103], a[84:87], v[140:143], v[100:103]
v_lshlrev_b32 v116, 16, v150
v_and_b32 v117, 0xffff0000, v150
v_mfma_f32_16x16x32_bf16 v[104:107], a[88:91], v[140:143], v[104:107]
v_lshlrev_b32 v118, 16, v151
v_and_b32 v119, 0xffff0000, v151
v_mfma_f32_16x16x32_bf16 v[108:111], a[92:95], v[140:143], v[108:111]
s_waitcnt lgkmcnt(2)
v_lshlrev_b32 v120, 16, v152
v_and_b32 v121, 0xffff0000, v152
v_mfma_f32_16x16x32_bf16 v[96:99], a[96:99], v[144:147], v[96:99]
v_lshlrev_b32 v122, 16, v153
v_and_b32 v123, 0xffff0000, v153
v_mfma_f32_16x16x32_bf16 v[100:103], a[100:103], v[144:147], v[100:103]
v_mfma_f32_16x16x32_bf16 v[104:107], a[104:107], v[144:147], v[104:107]
s_waitcnt vmcnt(9)
s_barrier
v_mfma_f32_16x16x32_bf16 v[108:111], a[108:111], v[144:147], v[108:111]
s_waitcnt lgkmcnt(1)
ds_read_b128 v[140:143], v25 offset:27648
ds_read_b128 v[184:187], v26 offset:36864
v_mfma_f32_16x16x32_bf16 v[96:99], a[112:115], v[132:135], v[96:99]
ds_read_b128 v[144:147], v25 offset:28672
ds_read_b128 v[188:191], v26 offset:37888
v_mfma_f32_16x16x32_bf16 v[100:103], a[116:119], v[132:135], v[100:103]
ds_read_b128 v[192:195], v26 offset:38912
v_lshlrev_b32 v124, 16, v154
v_mfma_f32_16x16x32_bf16 v[104:107], a[120:123], v[132:135], v[104:107]
v_and_b32 v125, 0xffff0000, v154
v_lshlrev_b32 v126, 16, v155
v_mfma_f32_16x16x32_bf16 v[108:111], a[124:127], v[132:135], v[108:111]
s_waitcnt lgkmcnt(5)
v_and_b32 v127, 0xffff0000, v155
v_mfma_f32_16x16x32_bf16 v[96:99], a[128:131], v[136:139], v[96:99]
ds_read_b128 v[132:135], v25 offset:29696
v_mfma_f32_16x16x32_bf16 v[100:103], a[132:135], v[136:139], v[100:103]
v_mfma_f32_16x16x32_bf16 v[104:107], a[136:139], v[136:139], v[104:107]
s_waitcnt lgkmcnt(4)
v_add_f32 v176, v176, v184
v_add_f32 v177, v177, v185
v_mfma_f32_16x16x32_bf16 v[108:111], a[140:143], v[136:139], v[108:111]
v_add_f32 v178, v178, v186
v_add_f32 v179, v179, v187
v_mfma_f32_16x16x32_bf16 v[112:115], a[144:147], v[140:143], v[112:115]
s_waitcnt lgkmcnt(2)
ds_read_b128 v[136:139], v25 offset:30720
v_add_f32 v176, v176, v188
v_mfma_f32_16x16x32_bf16 v[116:119], a[148:151], v[140:143], v[116:119]
v_mfma_f32_16x16x32_bf16 v[120:123], a[152:155], v[140:143], v[120:123]
s_waitcnt vmcnt(6)
s_barrier
v_mfma_f32_16x16x32_bf16 v[124:127], a[156:159], v[140:143], v[124:127]
v_add_f32 v177, v177, v189
v_add_f32 v178, v178, v190
v_mfma_f32_16x16x32_bf16 v[128:131], v[60:63], v[140:143], 0
s_waitcnt lgkmcnt(2)
v_add_f32 v179, v179, v191
v_add_f32 v180, v180, v192
v_mfma_f32_16x16x32_bf16 v[112:115], a[160:163], v[144:147], v[112:115]
ds_read_b128 v[140:143], v25 offset:31744
v_add_f32 v181, v181, v193
v_mfma_f32_16x16x32_bf16 v[116:119], a[164:167], v[144:147], v[116:119]
v_add_f32 v182, v182, v194
v_add_f32 v183, v183, v195
v_mfma_f32_16x16x32_bf16 v[120:123], a[168:171], v[144:147], v[120:123]
v_cvt_pk_bf16_f32 v172, v176, v177
v_cvt_pk_bf16_f32 v173, v178, v179
v_mfma_f32_16x16x32_bf16 v[124:127], a[172:175], v[144:147], v[124:127]
v_cvt_pk_bf16_f32 v174, v180, v181
v_cvt_pk_bf16_f32 v175, v182, v183
v_mfma_f32_16x16x32_bf16 v[128:131], v[64:67], v[144:147], v[128:131]
s_waitcnt lgkmcnt(2)
global_store_dwordx4 v14, v[172:175], s[38:39] sc1
v_mfma_f32_16x16x32_bf16 v[112:115], a[176:179], v[132:135], v[112:115]
s_mov_b32 m0, 61440
ds_read_b128 v[144:147], v25 offset:32768
v_add_u32 v14, 18688, v14
v_mfma_f32_16x16x32_bf16 v[116:119], a[180:183], v[132:135], v[116:119]
v_cvt_pk_bf16_f32 v164, v96, v97
v_cvt_pk_bf16_f32 v165, v98, v99
v_mfma_f32_16x16x32_bf16 v[120:123], a[184:187], v[132:135], v[120:123]
v_cvt_pk_bf16_f32 v166, v100, v101
v_cvt_pk_bf16_f32 v167, v102, v103
v_mfma_f32_16x16x32_bf16 v[124:127], a[188:191], v[132:135], v[124:127]
global_store_dwordx4 v13, v[164:167], s[38:39] offset:0 sc1
v_mfma_f32_16x16x32_bf16 v[128:131], v[68:71], v[132:135], v[128:131]
s_waitcnt lgkmcnt(2)
v_cvt_pk_bf16_f32 v168, v104, v105
v_cvt_pk_bf16_f32 v169, v106, v107
v_mfma_f32_16x16x32_bf16 v[112:115], a[192:195], v[136:139], v[112:115]
ds_read_b128 v[132:135], v25 offset:33792
v_cvt_pk_bf16_f32 v170, v108, v109
v_mfma_f32_16x16x32_bf16 v[116:119], a[196:199], v[136:139], v[116:119]
v_cvt_pk_bf16_f32 v171, v110, v111
v_add_f32 v254, v96, v97
v_mfma_f32_16x16x32_bf16 v[120:123], a[200:203], v[136:139], v[120:123]
global_store_dwordx4 v13, v[168:171], s[38:39] offset:1024 sc1
v_mfma_f32_16x16x32_bf16 v[124:127], a[204:207], v[136:139], v[124:127]
v_add_u32 v13, 18688, v13
v_mul_f32 v255, v96, v96
v_mfma_f32_16x16x32_bf16 v[128:131], v[72:75], v[136:139], v[128:131]
s_waitcnt lgkmcnt(2)
v_fmac_f32 v255, v97, v97
v_add_f32 v254, v254, v98
v_mfma_f32_16x16x32_bf16 v[112:115], a[208:211], v[140:143], v[112:115]
ds_read_b128 v[136:139], v25 offset:34816
v_fmac_f32 v255, v98, v98
v_mfma_f32_16x16x32_bf16 v[116:119], a[212:215], v[140:143], v[116:119]
v_add_f32 v254, v254, v99
v_fmac_f32 v255, v99, v99
v_mfma_f32_16x16x32_bf16 v[120:123], a[216:219], v[140:143], v[120:123]
v_add_f32 v254, v254, v100
v_fmac_f32 v255, v100, v100
v_mfma_f32_16x16x32_bf16 v[124:127], a[220:223], v[140:143], v[124:127]
v_add_f32 v254, v254, v101
v_fmac_f32 v255, v101, v101
v_mfma_f32_16x16x32_bf16 v[128:131], v[76:79], v[140:143], v[128:131]
s_waitcnt vmcnt(7)
global_load_lds_dwordx4 v16, s[42:43] offset:0 sc1
ds_read_b128 v[148:151], v19 offset:0
s_waitcnt lgkmcnt(3)
v_mfma_f32_16x16x32_bf16 v[112:115], a[224:227], v[144:147], v[112:115]
ds_read_b128 v[140:143], v25 offset:35840
ds_read_b128 v[152:155], v20 offset:0
v_mfma_f32_16x16x32_bf16 v[116:119], a[228:231], v[144:147], v[116:119]
v_add_f32 v254, v254, v102
v_fmac_f32 v255, v102, v102
v_mfma_f32_16x16x32_bf16 v[120:123], a[232:235], v[144:147], v[120:123]
v_add_f32 v254, v254, v103
v_fmac_f32 v255, v103, v103
v_mfma_f32_16x16x32_bf16 v[124:127], a[236:239], v[144:147], v[124:127]
v_add_f32 v254, v254, v104
v_fmac_f32 v255, v104, v104
v_mfma_f32_16x16x32_bf16 v[128:131], v[80:83], v[144:147], v[128:131]
s_waitcnt lgkmcnt(4)
v_add_f32 v254, v254, v105
v_fmac_f32 v255, v105, v105
)"
R"(v_mfma_f32_16x16x32_bf16 v[112:115], a[240:243], v[132:135], v[112:115]
ds_read_b128 v[144:147], v25 offset:18432
v_add_f32 v254, v254, v106
v_mfma_f32_16x16x32_bf16 v[116:119], a[244:247], v[132:135], v[116:119]
global_load_lds_dwordx4 v17, s[42:43] offset:1024 sc1
v_fmac_f32 v255, v106, v106
v_mfma_f32_16x16x32_bf16 v[120:123], a[248:251], v[132:135], v[120:123]
v_add_f32 v254, v254, v107
v_fmac_f32 v255, v107, v107
v_mfma_f32_16x16x32_bf16 v[124:127], a[252:255], v[132:135], v[124:127]
v_add_f32 v254, v254, v108
v_fmac_f32 v255, v108, v108
v_mfma_f32_16x16x32_bf16 v[128:131], v[84:87], v[132:135], v[128:131]
s_waitcnt lgkmcnt(4)
v_add_f32 v254, v254, v109
v_fmac_f32 v255, v109, v109
v_mfma_f32_16x16x32_bf16 v[112:115], v[28:31], v[136:139], v[112:115]
ds_read_b128 v[132:135], v25 offset:19456
v_add_f32 v254, v254, v110
v_mfma_f32_16x16x32_bf16 v[116:119], v[32:35], v[136:139], v[116:119]
s_add_u32 m0, s49, 18432
v_fmac_f32 v255, v110, v110
v_add_f32 v254, v254, v111
v_mfma_f32_16x16x32_bf16 v[120:123], v[36:39], v[136:139], v[120:123]
v_fmac_f32 v255, v111, v111
v_add_f32 v254, v254, v176
v_mfma_f32_16x16x32_bf16 v[124:127], v[40:43], v[136:139], v[124:127]
v_fmac_f32 v255, v176, v176
v_add_f32 v254, v254, v177
v_mfma_f32_16x16x32_bf16 v[128:131], v[88:91], v[136:139], v[128:131]
s_waitcnt lgkmcnt(3)
v_fmac_f32 v255, v177, v177
v_add_f32 v254, v254, v178
v_mfma_f32_16x16x32_bf16 v[112:115], v[44:47], v[140:143], v[112:115]
ds_read_b128 v[136:139], v25 offset:20480
v_fmac_f32 v255, v178, v178
v_mfma_f32_16x16x32_bf16 v[116:119], v[48:51], v[140:143], v[116:119]
v_add_f32 v254, v254, v179
v_fmac_f32 v255, v179, v179
v_mfma_f32_16x16x32_bf16 v[120:123], v[52:55], v[140:143], v[120:123]
v_add_f32 v254, v254, v180
v_fmac_f32 v255, v180, v180
v_mfma_f32_16x16x32_bf16 v[124:127], v[56:59], v[140:143], v[124:127]
v_add_f32 v254, v254, v181
v_fmac_f32 v255, v181, v181
v_mfma_f32_16x16x32_bf16 v[128:131], v[92:95], v[140:143], v[128:131]
s_waitcnt lgkmcnt(2)
v_add_f32 v254, v254, v182
v_fmac_f32 v255, v182, v182
v_mfma_f32_16x16x32_bf16 v[112:115], a[0:3], v[144:147], v[112:115]
ds_read_b128 v[140:143], v25 offset:21504
v_add_f32 v254, v254, v183
v_mfma_f32_16x16x32_bf16 v[116:119], a[4:7], v[144:147], v[116:119]
v_fmac_f32 v255, v183, v183
v_mfma_f32_16x16x32_bf16 v[120:123], a[8:11], v[144:147], v[120:123]
ds_write_b64 v27, v[254:255] offset:44544
v_mfma_f32_16x16x32_bf16 v[124:127], a[12:15], v[144:147], v[124:127]
s_waitcnt lgkmcnt(3)
v_lshlrev_b32 v176, 16, v160
v_and_b32 v177, 0xffff0000, v160
v_mfma_f32_16x16x32_bf16 v[112:115], a[16:19], v[132:135], v[112:115]
ds_read_b128 v[144:147], v25 offset:22528
v_lshlrev_b32 v178, 16, v161
v_mfma_f32_16x16x32_bf16 v[116:119], a[20:23], v[132:135], v[116:119]
v_and_b32 v179, 0xffff0000, v161
v_lshlrev_b32 v180, 16, v162
v_mfma_f32_16x16x32_bf16 v[120:123], a[24:27], v[132:135], v[120:123]
v_and_b32 v181, 0xffff0000, v162
v_lshlrev_b32 v182, 16, v163
v_mfma_f32_16x16x32_bf16 v[124:127], a[28:31], v[132:135], v[124:127]
s_waitcnt lgkmcnt(3)
v_and_b32 v183, 0xffff0000, v163
v_add_f32 v180, v180, v128
v_mfma_f32_16x16x32_bf16 v[112:115], a[32:35], v[136:139], v[112:115]
ds_read_b128 v[132:135], v25 offset:23552
v_add_f32 v181, v181, v129
v_mfma_f32_16x16x32_bf16 v[116:119], a[36:39], v[136:139], v[116:119]
v_mfma_f32_16x16x32_bf16 v[120:123], a[40:43], v[136:139], v[120:123]
s_barrier
v_mfma_f32_16x16x32_bf16 v[124:127], a[44:47], v[136:139], v[124:127]
s_waitcnt lgkmcnt(3)
v_add_f32 v182, v182, v130
v_add_f32 v183, v183, v131
v_mfma_f32_16x16x32_bf16 v[112:115], a[48:51], v[140:143], v[112:115]
s_mov_b64 s[76:77], -1
ds_read_b128 v[136:139], v25 offset:24576
global_load_lds_dwordx4 v11, s[38:39] offset:-2048 sc1
v_mfma_f32_16x16x32_bf16 v[116:119], a[52:55], v[140:143], v[116:119]
global_load_dwordx4 v[160:163], v18, s[42:43] sc1
v_lshlrev_b32 v96, 16, v148
v_mfma_f32_16x16x32_bf16 v[120:123], a[56:59], v[140:143], v[120:123]
s_sub_u32 s42, s42, 18432
v_and_b32 v97, 0xffff0000, v148
v_lshlrev_b32 v98, 16, v149
v_mfma_f32_16x16x32_bf16 v[124:127], a[60:63], v[140:143], v[124:127]
s_waitcnt lgkmcnt(2)
global_load_lds_dwordx4 v11, s[38:39] offset:-1024 sc1
v_and_b32 v99, 0xffff0000, v149
v_mfma_f32_16x16x32_bf16 v[112:115], a[64:67], v[144:147], v[112:115]
s_subb_u32 s43, s43, 0
ds_read_b128 v[140:143], v25 offset:25600
v_lshlrev_b32 v100, 16, v150
v_mfma_f32_16x16x32_bf16 v[116:119], a[68:71], v[144:147], v[116:119]
v_and_b32 v101, 0xffff0000, v150
v_lshlrev_b32 v102, 16, v151
v_mfma_f32_16x16x32_bf16 v[120:123], a[72:75], v[144:147], v[120:123]
global_load_lds_dwordx4 v11, s[38:39] offset:0 sc1
v_add_u32 v11, 18432, v11
v_mfma_f32_16x16x32_bf16 v[124:127], a[76:79], v[144:147], v[124:127]
s_waitcnt lgkmcnt(2)
v_min_u32 v11, v11, v12
v_and_b32 v103, 0xffff0000, v151
v_mfma_f32_16x16x32_bf16 v[112:115], a[80:83], v[132:135], v[112:115]
ds_read_b128 v[144:147], v25 offset:26624
v_lshlrev_b32 v104, 16, v152
v_mfma_f32_16x16x32_bf16 v[116:119], a[84:87], v[132:135], v[116:119]
v_and_b32 v105, 0xffff0000, v152
v_lshlrev_b32 v106, 16, v153
v_mfma_f32_16x16x32_bf16 v[120:123], a[88:91], v[132:135], v[120:123]
v_and_b32 v107, 0xffff0000, v153
v_lshlrev_b32 v108, 16, v154
v_mfma_f32_16x16x32_bf16 v[124:127], a[92:95], v[132:135], v[124:127]
s_waitcnt lgkmcnt(2)
v_xor_b32 v25, 0x10000, v25
v_and_b32 v109, 0xffff0000, v154
v_mfma_f32_16x16x32_bf16 v[112:115], a[96:99], v[136:139], v[112:115]
v_lshlrev_b32 v110, 16, v155
v_and_b32 v111, 0xffff0000, v155
v_mfma_f32_16x16x32_bf16 v[116:119], a[100:103], v[136:139], v[116:119]
v_mfma_f32_16x16x32_bf16 v[120:123], a[104:107], v[136:139], v[120:123]
s_waitcnt vmcnt(9)
s_barrier
v_mfma_f32_16x16x32_bf16 v[124:127], a[108:111], v[136:139], v[124:127]
s_waitcnt lgkmcnt(1)
ds_read_b128 v[132:135], v25 offset:9216
ds_read_b128 v[184:187], v26 offset:39936
v_mfma_f32_16x16x32_bf16 v[112:115], a[112:115], v[140:143], v[112:115]
ds_read_b128 v[136:139], v25 offset:10240
ds_read_b128 v[188:191], v26 offset:40960
v_mfma_f32_16x16x32_bf16 v[116:119], a[116:119], v[140:143], v[116:119]
ds_read_b128 v[192:195], v26 offset:41984
v_mfma_f32_16x16x32_bf16 v[120:123], a[120:123], v[140:143], v[120:123]
v_mfma_f32_16x16x32_bf16 v[124:127], a[124:127], v[140:143], v[124:127]
s_waitcnt lgkmcnt(5)
v_mfma_f32_16x16x32_bf16 v[112:115], a[128:131], v[144:147], v[112:115]
ds_read_b128 v[140:143], v25 offset:11264
v_mfma_f32_16x16x32_bf16 v[116:119], a[132:135], v[144:147], v[116:119]
v_mfma_f32_16x16x32_bf16 v[120:123], a[136:139], v[144:147], v[120:123]
v_mfma_f32_16x16x32_bf16 v[124:127], a[140:143], v[144:147], v[124:127]
s_sub_u32 s70, s70, 1
s_cbranch_scc0 .Lwo_top15
s_waitcnt vmcnt(6)
s_barrier
s_waitcnt lgkmcnt(4)
v_add_f32 v176, v176, v184
v_add_f32 v177, v177, v185
v_add_f32 v178, v178, v186
v_add_f32 v179, v179, v187
s_waitcnt lgkmcnt(2)
v_add_f32 v176, v176, v188
v_add_f32 v177, v177, v189
v_add_f32 v178, v178, v190
v_add_f32 v179, v179, v191
s_waitcnt lgkmcnt(1)
v_add_f32 v180, v180, v192
v_add_f32 v181, v181, v193
v_add_f32 v182, v182, v194
v_add_f32 v183, v183, v195
v_cvt_pk_bf16_f32 v172, v176, v177
v_cvt_pk_bf16_f32 v173, v178, v179
v_cvt_pk_bf16_f32 v174, v180, v181
v_cvt_pk_bf16_f32 v175, v182, v183
v_cvt_pk_bf16_f32 v164, v112, v113
global_store_dwordx4 v14, v[172:175], s[38:39] sc1
v_cvt_pk_bf16_f32 v165, v114, v115
v_cvt_pk_bf16_f32 v166, v116, v117
v_cvt_pk_bf16_f32 v167, v118, v119
global_store_dwordx4 v13, v[164:167], s[38:39] offset:0 sc1
v_cvt_pk_bf16_f32 v168, v120, v121
v_cvt_pk_bf16_f32 v169, v122, v123
v_cvt_pk_bf16_f32 v170, v124, v125
v_cvt_pk_bf16_f32 v171, v126, v127
global_store_dwordx4 v13, v[168:171], s[38:39] offset:1024 sc1
v_add_f32 v254, v112, v113
v_mul_f32 v255, v112, v112
v_fmac_f32 v255, v113, v113
v_add_f32 v254, v254, v114
v_fmac_f32 v255, v114, v114
v_add_f32 v254, v254, v115
v_fmac_f32 v255, v115, v115
v_add_f32 v254, v254, v116
v_fmac_f32 v255, v116, v116
v_add_f32 v254, v254, v117
v_fmac_f32 v255, v117, v117
v_add_f32 v254, v254, v118
v_fmac_f32 v255, v118, v118
v_add_f32 v254, v254, v119
v_fmac_f32 v255, v119, v119
v_add_f32 v254, v254, v120
v_fmac_f32 v255, v120, v120
v_add_f32 v254, v254, v121
v_fmac_f32 v255, v121, v121
v_add_f32 v254, v254, v122
v_fmac_f32 v255, v122, v122
v_add_f32 v254, v254, v123
v_fmac_f32 v255, v123, v123
v_add_f32 v254, v254, v124
v_fmac_f32 v255, v124, v124
v_add_f32 v254, v254, v125
v_fmac_f32 v255, v125, v125
v_add_f32 v254, v254, v126
v_fmac_f32 v255, v126, v126
v_add_f32 v254, v254, v127
v_fmac_f32 v255, v127, v127
v_add_f32 v254, v254, v176
v_fmac_f32 v255, v176, v176
v_add_f32 v254, v254, v177
v_fmac_f32 v255, v177, v177
v_add_f32 v254, v254, v178
v_fmac_f32 v255, v178, v178
v_add_f32 v254, v254, v179
v_fmac_f32 v255, v179, v179
v_add_f32 v254, v254, v180
v_fmac_f32 v255, v180, v180
v_add_f32 v254, v254, v181
v_fmac_f32 v255, v181, v181
v_add_f32 v254, v254, v182
v_fmac_f32 v255, v182, v182
v_add_f32 v254, v254, v183
v_fmac_f32 v255, v183, v183
ds_write_b64 v27, v[254:255] offset:46592
s_waitcnt vmcnt(0) lgkmcnt(0)
s_barrier
s_barrier
s_endpgm
.Lwo_sp7:
s_sleep 1
v_add_lshl_u32 v253, v22, 0, 7
v_bfi_b32 v253, s69, v253, v21
global_load_dword v200, v253, s[38:39] sc1
s_add_u32 s71, s71, 1
s_waitcnt vmcnt(0)
v_sub_u32 v200, v200, v22
v_cmp_lt_u32 vcc, 0x7f, v200
s_and_b64 vcc, vcc, s[76:77]
s_cbranch_vccz .Lwo_rt8
s_cmp_lt_u32 s71, 3000000
s_cbranch_scc1 .Lwo_sp7
s_branch .Lwo_rt8
.Lwo_sp9:
s_sleep 1
v_add_lshl_u32 v253, v22, 0, 7
v_bfi_b32 v253, s69, v253, v21
global_load_dword v201, v253, s[38:39] sc1
s_add_u32 s71, s71, 1
s_waitcnt vmcnt(0)
v_sub_u32 v201, v201, v22
v_cmp_lt_u32 vcc, 0x7f, v201
s_and_b64 vcc, vcc, s[76:77]
s_cbranch_vccz .Lwo_rt10
s_cmp_lt_u32 s71, 3000000
s_cbranch_scc1 .Lwo_sp9
s_branch .Lwo_rt10
.Le1:
)"
    :: "{v[0:31]}"(va), "{s[36:51]}"(sa) : "v255", "a255", "s99", "vcc", "memory");
  } else if (jj < (ev ? 16 : 12)) {
    const unsigned dn_i = ev ? 48 + xh * 6 + jj - 10 : xh * 12 + jj, pf_g = (xh & 1) * 18 + (ev ? 10 : 0), pf_n = ev ? 24 : 48, pf_w = (ev ? jj - 10 : jj) * 4 + wave, pf_m = ev ? 8 : 10;
    const unsigned r = lane & 15, g = lane >> 4, q = r >> 2, p = lane & 3;
    const unsigned tri = dn_i / 3, nu = dn_i % 3, kap = tri >> 2, rho = tri & 3, row0 = rho * RP + PAD;
    auto pf = [&](unsigned k) { const unsigned u = k * pf_n + pf_w, b = u / pf_m, pg = pf_g + u - b * pf_m; return b < 16 ? (64 + b * 32 + (lane >> 1)) * 9216 + pg / 6 * 1536 + (pg % 6) * 128 + (lane & 1) * 768 : 0u; };
    auto wv = [&](unsigned j) { const unsigned b = j * 1024 + lane * 16, rw = b / 384; return rw * (1152 - 384) + b + 4096 - j * 1024; };
    u32x32 va; u32x16 sa;
    va[0] = wv(0);
    va[1] = wv(1);
    va[2] = wv(2);
    va[3] = wv(3);
    va[4] = wv(4);
    va[5] = wv(5);
    va[6] = (8 * g + q) * 384 + wave * 64 + p * 16 + (kap & 1) * 8;
    va[7] = (8 * g + q) * 384 + wave * 64 + p * 16 + 8 - (kap & 1) * 8;
    va[8] = (8 * g + q) * 384 + 256 + wave * 32 + (p ^ (kap & 1)) * 8;
    va[9] = WS_H + row0 * HT + (kap * 24 + wave * 6 + 2) * 1024 + lane * 16;
    va[10] = WS_H + (row0 + 63) * HT + (kap * 24 + wave * 6 + 2) * 1024 + lane * 16;
    va[11] = WS_P + (row0 - 1) * PT + (((nu * 3 + (wave >> 1)) * 4 + q) * 6 + kap) * 512 + (wave & 1) * 256 + g * 64 + p * 16;
    va[12] = WS_P + (row0 - 1) * PT + (((nu * 3 + 2) * 4 + q) * 6 + kap) * 512 + wave * 128 + (g >> 1) * 64 + p * 16;
    va[13] = WS_FL + rho * FR + FH + (kap * 6 + min(lane, 5u)) * FL;
    va[14] = 0u;
    va[15] = WS_FL + rho * FR + FP + (nu * 6 + kap) * FL;
    va[16] = 0u - 2;
    va[17] = lane * 16;
    va[18] = pf(0);
    va[19] = pf(1);
    va[20] = pf(2);
    va[21] = pf(3);
    va[22] = pf(4);
    va[23] = pf(5);
    sa[0] = 8 + wave; sa[2] = (unsigned)(unsigned long long)ws; sa[3] = (unsigned)((unsigned long long)ws >> 32);
    sa[4] = uni((unsigned)(unsigned long long)(wu));
    sa[5] = uni((unsigned)((unsigned long long)(wu) >> 32));
    sa[6] = uni((unsigned)(unsigned long long)(wd + (kap * 768 + wave * 16) * 1152 + nu * 384));
    sa[7] = uni((unsigned)((unsigned long long)(wd + (kap * 768 + wave * 16) * 1152 + nu * 384) >> 32));
    sa[8] = uni(wave * 6144);
    sa[9] = uni(wave * 6144 + 2048);
    asm volatile(
R"(.Lb2:
s_movk_i32 s69, 0x380
s_mov_b32 s71, 0
s_mov_b32 s74, 0
s_load_dword s37, s[38:39], 0x0
s_nop 1
s_sleep 70
s_mov_b32 m0, 0x27f00
s_nop 0
global_load_lds_dword v18, s[40:41]
global_load_lds_dword v19, s[40:41]
global_load_lds_dword v20, s[40:41]
global_load_lds_dword v21, s[40:41]
global_load_lds_dword v22, s[40:41]
global_load_lds_dword v23, s[40:41]
s_waitcnt vmcnt(0)
s_add_u32 m0, s44, 4096
s_nop 0
global_load_lds_dwordx4 v0, s[42:43] offset:-4096 nt
global_load_lds_dwordx4 v1, s[42:43] offset:-3072 nt
global_load_lds_dwordx4 v2, s[42:43] offset:-2048 nt
global_load_lds_dwordx4 v3, s[42:43] offset:-1024 nt
global_load_lds_dwordx4 v4, s[42:43] offset:0 nt
global_load_lds_dwordx4 v5, s[42:43] offset:1024 nt
s_add_u32 s42, s42, 73728
s_addc_u32 s43, s43, 0
s_add_u32 m0, s44, 28672
s_nop 0
global_load_lds_dwordx4 v0, s[42:43] offset:-4096 nt
global_load_lds_dwordx4 v1, s[42:43] offset:-3072 nt
global_load_lds_dwordx4 v2, s[42:43] offset:-2048 nt
global_load_lds_dwordx4 v3, s[42:43] offset:-1024 nt
global_load_lds_dwordx4 v4, s[42:43] offset:0 nt
global_load_lds_dwordx4 v5, s[42:43] offset:1024 nt
s_add_u32 s42, s42, 73728
s_addc_u32 s43, s43, 0
s_add_u32 m0, s44, 53248
s_nop 0
global_load_lds_dwordx4 v0, s[42:43] offset:-4096 nt
global_load_lds_dwordx4 v1, s[42:43] offset:-3072 nt
global_load_lds_dwordx4 v2, s[42:43] offset:-2048 nt
global_load_lds_dwordx4 v3, s[42:43] offset:-1024 nt
global_load_lds_dwordx4 v4, s[42:43] offset:0 nt
global_load_lds_dwordx4 v5, s[42:43] offset:1024 nt
s_add_u32 s42, s42, 73728
s_addc_u32 s43, s43, 0
s_add_u32 m0, s44, 77824
s_nop 0
global_load_lds_dwordx4 v0, s[42:43] offset:-4096 nt
global_load_lds_dwordx4 v1, s[42:43] offset:-3072 nt
global_load_lds_dwordx4 v2, s[42:43] offset:-2048 nt
global_load_lds_dwordx4 v3, s[42:43] offset:-1024 nt
global_load_lds_dwordx4 v4, s[42:43] offset:0 nt
global_load_lds_dwordx4 v5, s[42:43] offset:1024 nt
s_add_u32 s42, s42, 73728
s_addc_u32 s43, s43, 0
s_add_u32 m0, s44, 102400
s_nop 0
global_load_lds_dwordx4 v0, s[42:43] offset:-4096 nt
global_load_lds_dwordx4 v1, s[42:43] offset:-3072 nt
global_load_lds_dwordx4 v2, s[42:43] offset:-2048 nt
global_load_lds_dwordx4 v3, s[42:43] offset:-1024 nt
global_load_lds_dwordx4 v4, s[42:43] offset:0 nt
global_load_lds_dwordx4 v5, s[42:43] offset:1024 nt
s_add_u32 s42, s42, 73728
s_addc_u32 s43, s43, 0
v_add_u32 v136, 0x10000, v6
v_add_u32 v137, 0x20000, v6
v_add_u32 v138, 0x10000, v7
v_add_u32 v139, 0x20000, v7
v_add_u32 v140, 0x10000, v8
v_add_u32 v141, 0x20000, v8
s_waitcnt lgkmcnt(0)
s_mul_i32 s37, s37, 0x9E3779B1
s_add_u32 s37, s37, 0x7F4A7C15
v_add_u32 v14, s37, v14
v_add_u32 v16, s37, v16
s_waitcnt vmcnt(24)
s_barrier
s_add_u32 m0, s44, 126976
ds_read_b64_tr_b16 a[0:1], v6 offset:0
ds_read_b64_tr_b16 a[2:3], v6 offset:1536
global_load_lds_dwordx4 v0, s[42:43] offset:-4096 nt
ds_read_b64_tr_b16 a[4:5], v7 offset:0
global_load_lds_dwordx4 v1, s[42:43] offset:-3072 nt
ds_read_b64_tr_b16 a[6:7], v7 offset:1536
global_load_lds_dwordx4 v2, s[42:43] offset:-2048 nt
ds_read_b64_tr_b16 a[8:9], v8 offset:0
ds_read_b64_tr_b16 a[10:11], v8 offset:1536
global_load_lds_dwordx4 v3, s[42:43] offset:-1024 nt
ds_read_b64_tr_b16 a[12:13], v6 offset:12288
global_load_lds_dwordx4 v4, s[42:43] offset:0 nt
ds_read_b64_tr_b16 a[14:15], v6 offset:13824
global_load_lds_dwordx4 v5, s[42:43] offset:1024 nt
ds_read_b64_tr_b16 a[16:17], v7 offset:12288
ds_read_b64_tr_b16 a[18:19], v7 offset:13824
s_add_u32 s42, s42, 73728
ds_read_b64_tr_b16 a[20:21], v8 offset:12288
s_addc_u32 s43, s43, 0
ds_read_b64_tr_b16 a[22:23], v8 offset:13824
s_waitcnt vmcnt(24) lgkmcnt(0)
s_barrier
s_add_u32 m0, s44, 4096
ds_read_b64_tr_b16 a[24:25], v6 offset:24576
ds_read_b64_tr_b16 a[26:27], v6 offset:26112
global_load_lds_dwordx4 v0, s[42:43] offset:-4096 nt
ds_read_b64_tr_b16 a[28:29], v7 offset:24576
global_load_lds_dwordx4 v1, s[42:43] offset:-3072 nt
ds_read_b64_tr_b16 a[30:31], v7 offset:26112
global_load_lds_dwordx4 v2, s[42:43] offset:-2048 nt
ds_read_b64_tr_b16 a[32:33], v8 offset:24576
ds_read_b64_tr_b16 a[34:35], v8 offset:26112
global_load_lds_dwordx4 v3, s[42:43] offset:-1024 nt
ds_read_b64_tr_b16 a[36:37], v6 offset:36864
global_load_lds_dwordx4 v4, s[42:43] offset:0 nt
ds_read_b64_tr_b16 a[38:39], v6 offset:38400
global_load_lds_dwordx4 v5, s[42:43] offset:1024 nt
ds_read_b64_tr_b16 a[40:41], v7 offset:36864
ds_read_b64_tr_b16 a[42:43], v7 offset:38400
s_add_u32 s42, s42, 73728
ds_read_b64_tr_b16 a[44:45], v8 offset:36864
s_addc_u32 s43, s43, 0
ds_read_b64_tr_b16 a[46:47], v8 offset:38400
s_waitcnt vmcnt(24) lgkmcnt(0)
s_barrier
s_add_u32 m0, s44, 28672
ds_read_b64_tr_b16 a[48:49], v6 offset:49152
ds_read_b64_tr_b16 a[50:51], v6 offset:50688
global_load_lds_dwordx4 v0, s[42:43] offset:-4096 nt
ds_read_b64_tr_b16 a[52:53], v7 offset:49152
global_load_lds_dwordx4 v1, s[42:43] offset:-3072 nt
ds_read_b64_tr_b16 a[54:55], v7 offset:50688
global_load_lds_dwordx4 v2, s[42:43] offset:-2048 nt
ds_read_b64_tr_b16 a[56:57], v8 offset:49152
ds_read_b64_tr_b16 a[58:59], v8 offset:50688
global_load_lds_dwordx4 v3, s[42:43] offset:-1024 nt
ds_read_b64_tr_b16 a[60:61], v6 offset:61440
global_load_lds_dwordx4 v4, s[42:43] offset:0 nt
ds_read_b64_tr_b16 a[62:63], v6 offset:62976
global_load_lds_dwordx4 v5, s[42:43] offset:1024 nt
ds_read_b64_tr_b16 a[64:65], v7 offset:61440
ds_read_b64_tr_b16 a[66:67], v7 offset:62976
s_add_u32 s42, s42, 73728
ds_read_b64_tr_b16 a[68:69], v8 offset:61440
s_addc_u32 s43, s43, 0
ds_read_b64_tr_b16 a[70:71], v8 offset:62976
s_waitcnt vmcnt(24) lgkmcnt(0)
s_barrier
s_add_u32 m0, s44, 53248
ds_read_b64_tr_b16 a[72:73], v136 offset:8192
ds_read_b64_tr_b16 a[74:75], v136 offset:9728
global_load_lds_dwordx4 v0, s[42:43] offset:-4096 nt
ds_read_b64_tr_b16 a[76:77], v138 offset:8192
global_load_lds_dwordx4 v1, s[42:43] offset:-3072 nt
ds_read_b64_tr_b16 a[78:79], v138 offset:9728
global_load_lds_dwordx4 v2, s[42:43] offset:-2048 nt
ds_read_b64_tr_b16 a[80:81], v140 offset:8192
ds_read_b64_tr_b16 a[82:83], v140 offset:9728
global_load_lds_dwordx4 v3, s[42:43] offset:-1024 nt
ds_read_b64_tr_b16 a[84:85], v136 offset:20480
global_load_lds_dwordx4 v4, s[42:43] offset:0 nt
ds_read_b64_tr_b16 a[86:87], v136 offset:22016
global_load_lds_dwordx4 v5, s[42:43] offset:1024 nt
ds_read_b64_tr_b16 a[88:89], v138 offset:20480
ds_read_b64_tr_b16 a[90:91], v138 offset:22016
s_add_u32 s42, s42, 73728
ds_read_b64_tr_b16 a[92:93], v140 offset:20480
s_addc_u32 s43, s43, 0
ds_read_b64_tr_b16 a[94:95], v140 offset:22016
s_waitcnt vmcnt(24) lgkmcnt(0)
s_barrier
s_add_u32 m0, s44, 77824
ds_read_b64_tr_b16 a[96:97], v136 offset:32768
ds_read_b64_tr_b16 a[98:99], v136 offset:34304
global_load_lds_dwordx4 v0, s[42:43] offset:-4096 nt
ds_read_b64_tr_b16 a[100:101], v138 offset:32768
global_load_lds_dwordx4 v1, s[42:43] offset:-3072 nt
ds_read_b64_tr_b16 a[102:103], v138 offset:34304
global_load_lds_dwordx4 v2, s[42:43] offset:-2048 nt
ds_read_b64_tr_b16 a[104:105], v140 offset:32768
ds_read_b64_tr_b16 a[106:107], v140 offset:34304
global_load_lds_dwordx4 v3, s[42:43] offset:-1024 nt
ds_read_b64_tr_b16 a[108:109], v136 offset:45056
global_load_lds_dwordx4 v4, s[42:43] offset:0 nt
ds_read_b64_tr_b16 a[110:111], v136 offset:46592
global_load_lds_dwordx4 v5, s[42:43] offset:1024 nt
ds_read_b64_tr_b16 a[112:113], v138 offset:45056
ds_read_b64_tr_b16 a[114:115], v138 offset:46592
s_add_u32 s42, s42, 73728
ds_read_b64_tr_b16 a[116:117], v140 offset:45056
s_addc_u32 s43, s43, 0
ds_read_b64_tr_b16 a[118:119], v140 offset:46592
s_waitcnt vmcnt(24) lgkmcnt(0)
s_barrier
s_add_u32 m0, s44, 102400
ds_read_b64_tr_b16 a[120:121], v136 offset:57344
ds_read_b64_tr_b16 a[122:123], v136 offset:58880
global_load_lds_dwordx4 v0, s[42:43] offset:-4096 nt
ds_read_b64_tr_b16 a[124:125], v138 offset:57344
global_load_lds_dwordx4 v1, s[42:43] offset:-3072 nt
ds_read_b64_tr_b16 a[126:127], v138 offset:58880
global_load_lds_dwordx4 v2, s[42:43] offset:-2048 nt
ds_read_b64_tr_b16 a[128:129], v140 offset:57344
ds_read_b64_tr_b16 a[130:131], v140 offset:58880
global_load_lds_dwordx4 v3, s[42:43] offset:-1024 nt
ds_read_b64_tr_b16 a[132:133], v137 offset:4096
global_load_lds_dwordx4 v4, s[42:43] offset:0 nt
ds_read_b64_tr_b16 a[134:135], v137 offset:5632
global_load_lds_dwordx4 v5, s[42:43] offset:1024 nt
ds_read_b64_tr_b16 a[136:137], v139 offset:4096
ds_read_b64_tr_b16 a[138:139], v139 offset:5632
s_add_u32 s42, s42, 73728
ds_read_b64_tr_b16 a[140:141], v141 offset:4096
s_addc_u32 s43, s43, 0
ds_read_b64_tr_b16 a[142:143], v141 offset:5632
s_waitcnt vmcnt(24) lgkmcnt(0)
s_barrier
s_add_u32 m0, s44, 126976
ds_read_b64_tr_b16 a[144:145], v6 offset:0
ds_read_b64_tr_b16 a[146:147], v6 offset:1536
global_load_lds_dwordx4 v0, s[42:43] offset:-4096 nt
ds_read_b64_tr_b16 a[148:149], v7 offset:0
global_load_lds_dwordx4 v1, s[42:43] offset:-3072 nt
ds_read_b64_tr_b16 a[150:151], v7 offset:1536
global_load_lds_dwordx4 v2, s[42:43] offset:-2048 nt
ds_read_b64_tr_b16 a[152:153], v8 offset:0
ds_read_b64_tr_b16 a[154:155], v8 offset:1536
global_load_lds_dwordx4 v3, s[42:43] offset:-1024 nt
ds_read_b64_tr_b16 a[156:157], v6 offset:12288
global_load_lds_dwordx4 v4, s[42:43] offset:0 nt
ds_read_b64_tr_b16 a[158:159], v6 offset:13824
global_load_lds_dwordx4 v5, s[42:43] offset:1024 nt
ds_read_b64_tr_b16 a[160:161], v7 offset:12288
ds_read_b64_tr_b16 a[162:163], v7 offset:13824
s_sub_u32 s42, s42, 811008
ds_read_b64_tr_b16 a[164:165], v8 offset:12288
s_subb_u32 s43, s43, 0
ds_read_b64_tr_b16 a[166:167], v8 offset:13824
s_waitcnt vmcnt(24) lgkmcnt(0)
s_barrier
ds_read_b64_tr_b16 a[168:169], v6 offset:24576
ds_read_b64_tr_b16 a[170:171], v6 offset:26112
ds_read_b64_tr_b16 a[172:173], v7 offset:24576
ds_read_b64_tr_b16 a[174:175], v7 offset:26112
ds_read_b64_tr_b16 a[176:177], v8 offset:24576
ds_read_b64_tr_b16 a[178:179], v8 offset:26112
ds_read_b64_tr_b16 a[180:181], v6 offset:36864
ds_read_b64_tr_b16 a[182:183], v6 offset:38400
ds_read_b64_tr_b16 a[184:185], v7 offset:36864
ds_read_b64_tr_b16 a[186:187], v7 offset:38400
ds_read_b64_tr_b16 a[188:189], v8 offset:36864
ds_read_b64_tr_b16 a[190:191], v8 offset:38400
s_waitcnt vmcnt(18) lgkmcnt(0)
s_barrier
ds_read_b64_tr_b16 a[192:193], v6 offset:49152
ds_read_b64_tr_b16 a[194:195], v6 offset:50688
ds_read_b64_tr_b16 a[196:197], v7 offset:49152
ds_read_b64_tr_b16 a[198:199], v7 offset:50688
ds_read_b64_tr_b16 a[200:201], v8 offset:49152
ds_read_b64_tr_b16 a[202:203], v8 offset:50688
ds_read_b64_tr_b16 a[204:205], v6 offset:61440
ds_read_b64_tr_b16 a[206:207], v6 offset:62976
ds_read_b64_tr_b16 a[208:209], v7 offset:61440
ds_read_b64_tr_b16 a[210:211], v7 offset:62976
ds_read_b64_tr_b16 a[212:213], v8 offset:61440
ds_read_b64_tr_b16 a[214:215], v8 offset:62976
s_waitcnt vmcnt(12) lgkmcnt(0)
s_barrier
ds_read_b64_tr_b16 a[216:217], v136 offset:8192
ds_read_b64_tr_b16 a[218:219], v136 offset:9728
ds_read_b64_tr_b16 a[220:221], v138 offset:8192
ds_read_b64_tr_b16 a[222:223], v138 offset:9728
ds_read_b64_tr_b16 a[224:225], v140 offset:8192
ds_read_b64_tr_b16 a[226:227], v140 offset:9728
ds_read_b64_tr_b16 a[228:229], v136 offset:20480
ds_read_b64_tr_b16 a[230:231], v136 offset:22016
ds_read_b64_tr_b16 a[232:233], v138 offset:20480
ds_read_b64_tr_b16 a[234:235], v138 offset:22016
ds_read_b64_tr_b16 a[236:237], v140 offset:20480
ds_read_b64_tr_b16 a[238:239], v140 offset:22016
s_waitcnt vmcnt(6) lgkmcnt(0)
s_barrier
ds_read_b64_tr_b16 a[240:241], v136 offset:32768
ds_read_b64_tr_b16 a[242:243], v136 offset:34304
ds_read_b64_tr_b16 a[244:245], v138 offset:32768
ds_read_b64_tr_b16 a[246:247], v138 offset:34304
ds_read_b64_tr_b16 a[248:249], v140 offset:32768
ds_read_b64_tr_b16 a[250:251], v140 offset:34304
ds_read_b64_tr_b16 a[252:253], v136 offset:45056
ds_read_b64_tr_b16 a[254:255], v136 offset:46592
ds_read_b64_tr_b16 v[24:25], v138 offset:45056
ds_read_b64_tr_b16 v[26:27], v138 offset:46592
ds_read_b64_tr_b16 v[28:29], v140 offset:45056
ds_read_b64_tr_b16 v[30:31], v140 offset:46592
s_waitcnt vmcnt(0) lgkmcnt(0)
s_barrier
ds_read_b64_tr_b16 v[32:33], v136 offset:57344
ds_read_b64_tr_b16 v[34:35], v136 offset:58880
ds_read_b64_tr_b16 v[36:37], v138 offset:57344
ds_read_b64_tr_b16 v[38:39], v138 offset:58880
ds_read_b64_tr_b16 v[40:41], v140 offset:57344
ds_read_b64_tr_b16 v[42:43], v140 offset:58880
ds_read_b64_tr_b16 v[44:45], v137 offset:4096
ds_read_b64_tr_b16 v[46:47], v137 offset:5632
ds_read_b64_tr_b16 v[48:49], v139 offset:4096
ds_read_b64_tr_b16 v[50:51], v139 offset:5632
ds_read_b64_tr_b16 v[52:53], v141 offset:4096
ds_read_b64_tr_b16 v[54:55], v141 offset:5632
s_waitcnt lgkmcnt(0)
s_barrier
s_and_b32 s75, s36, 3
s_cmp_eq_u32 s75, 1
s_cbranch_scc1 .Ldn_w2
s_cmp_eq_u32 s75, 2
s_cbranch_scc1 .Ldn_w3
s_cmp_eq_u32 s75, 3
s_cbranch_scc1 .Ldn_w4
.Ldn_w1:
v_add_u32 v254, -1, v14
v_add_u32 v255, -1, v14
v_add_lshl_u32 v252, v16, 0, 7
v_bfi_b32 v252, s69, v252, v15
v_add_lshl_u32 v253, v14, 0, 7
v_bfi_b32 v253, s69, v253, v13
s_mov_b32 s76, 0xffff
s_mov_b32 s77, 0xffff
s_add_u32 m0, s45, 24576
s_xor_b32 s45, s45, 0x10000
s_mov_b32 m0, s45
s_barrier
global_load_lds_dwordx4 v9, s[38:39] offset:-2048 sc1
global_load_lds_dwordx4 v9, s[38:39] offset:-1024 sc1
global_load_lds_dwordx4 v9, s[38:39] offset:0 sc1
global_load_lds_dwordx4 v9, s[38:39] offset:1024 sc1
global_load_lds_dwordx4 v9, s[38:39] offset:2048 sc1
global_load_lds_dwordx4 v9, s[38:39] offset:3072 sc1
v_add_u32 v9, 147456, v9
s_mov_b32 s70, 32
s_waitcnt vmcnt(0) lgkmcnt(0)
s_nop 7
s_branch .Ldn_in5
.p2align 6
.Ldn_top6:
s_waitcnt lgkmcnt(5)
v_mfma_f32_16x16x32_bf16 v[56:59], a[0:3], v[80:83], 0
s_waitcnt lgkmcnt(4)
v_mfma_f32_16x16x32_bf16 v[56:59], a[12:15], v[84:87], v[56:59]
s_waitcnt lgkmcnt(3)
v_mfma_f32_16x16x32_bf16 v[56:59], a[24:27], v[88:91], v[56:59]
s_waitcnt lgkmcnt(2)
v_mfma_f32_16x16x32_bf16 v[56:59], a[36:39], v[92:95], v[56:59]
s_waitcnt lgkmcnt(1)
ds_read_b128 v[104:107], v17 offset:6144
v_cvt_pk_bf16_f32 v132, v76, v77
v_mfma_f32_16x16x32_bf16 v[56:59], a[48:51], v[96:99], v[56:59]
s_waitcnt lgkmcnt(1)
v_mfma_f32_16x16x32_bf16 v[56:59], a[60:63], v[100:103], v[56:59]
s_waitcnt vmcnt(6)
s_barrier
v_mfma_f32_16x16x32_bf16 v[60:63], a[4:7], v[80:83], 0
ds_read_b128 v[108:111], v17 offset:7168
v_cvt_pk_bf16_f32 v134, v76, v77
v_mfma_f32_16x16x32_bf16 v[60:63], a[16:19], v[84:87], v[60:63]
global_store_dwordx4 v11, v[128:131], s[38:39] sc1
v_mfma_f32_16x16x32_bf16 v[60:63], a[28:31], v[88:91], v[60:63]
ds_read_b128 v[112:115], v17 offset:8192
v_add_u32 v11, 110592, v11
v_mfma_f32_16x16x32_bf16 v[60:63], a[40:43], v[92:95], v[60:63]
v_cvt_pk_bf16_f32 v133, v78, v79
v_cvt_pk_bf16_f32 v135, v78, v79
v_mfma_f32_16x16x32_bf16 v[60:63], a[52:55], v[96:99], v[60:63]
ds_read_b128 v[116:119], v17 offset:9216
v_mfma_f32_16x16x32_bf16 v[60:63], a[64:67], v[100:103], v[60:63]
v_permlane16_swap_b32 v132, v134
v_mfma_f32_16x16x32_bf16 v[64:67], a[8:11], v[80:83], 0
ds_read_b128 v[120:123], v17 offset:10240
v_mfma_f32_16x16x32_bf16 v[64:67], a[20:23], v[84:87], v[64:67]
v_permlane16_swap_b32 v133, v135
v_mfma_f32_16x16x32_bf16 v[64:67], a[32:35], v[88:91], v[64:67]
ds_read_b128 v[124:127], v17 offset:11264
v_mfma_f32_16x16x32_bf16 v[64:67], a[44:47], v[92:95], v[64:67]
s_mov_b64 exec, s[76:77]
global_store_dwordx4 v12, v[132:135], s[38:39] sc1
s_mov_b64 exec, -1
v_mfma_f32_16x16x32_bf16 v[64:67], a[56:59], v[96:99], v[64:67]
v_add_u32 v12, 110592, v12
v_mfma_f32_16x16x32_bf16 v[64:67], a[68:71], v[100:103], v[64:67]
s_waitcnt lgkmcnt(5)
v_mfma_f32_16x16x32_bf16 v[56:59], a[72:75], v[104:107], v[56:59]
s_waitcnt lgkmcnt(4)
v_mfma_f32_16x16x32_bf16 v[56:59], a[84:87], v[108:111], v[56:59]
s_waitcnt lgkmcnt(3)
v_mfma_f32_16x16x32_bf16 v[56:59], a[96:99], v[112:115], v[56:59]
s_waitcnt lgkmcnt(2)
v_mfma_f32_16x16x32_bf16 v[56:59], a[108:111], v[116:119], v[56:59]
s_waitcnt lgkmcnt(1)
v_mfma_f32_16x16x32_bf16 v[56:59], a[120:123], v[120:123], v[56:59]
s_waitcnt lgkmcnt(0)
ds_read_b128 v[80:83], v17 offset:12288
v_mfma_f32_16x16x32_bf16 v[56:59], a[132:135], v[124:127], v[56:59]
)"
R"(v_mfma_f32_16x16x32_bf16 v[60:63], a[76:79], v[104:107], v[60:63]
ds_read_b128 v[84:87], v17 offset:13312
v_mfma_f32_16x16x32_bf16 v[60:63], a[88:91], v[108:111], v[60:63]
v_mfma_f32_16x16x32_bf16 v[60:63], a[100:103], v[112:115], v[60:63]
ds_read_b128 v[88:91], v17 offset:14336
v_mfma_f32_16x16x32_bf16 v[60:63], a[112:115], v[116:119], v[60:63]
v_mfma_f32_16x16x32_bf16 v[60:63], a[124:127], v[120:123], v[60:63]
ds_read_b128 v[92:95], v17 offset:15360
v_mfma_f32_16x16x32_bf16 v[60:63], a[136:139], v[124:127], v[60:63]
v_mfma_f32_16x16x32_bf16 v[64:67], a[80:83], v[104:107], v[64:67]
ds_read_b128 v[96:99], v17 offset:16384
v_mfma_f32_16x16x32_bf16 v[64:67], a[92:95], v[108:111], v[64:67]
v_mfma_f32_16x16x32_bf16 v[64:67], a[104:107], v[112:115], v[64:67]
ds_read_b128 v[100:103], v17 offset:17408
v_mfma_f32_16x16x32_bf16 v[64:67], a[116:119], v[116:119], v[64:67]
v_mfma_f32_16x16x32_bf16 v[64:67], a[128:131], v[120:123], v[64:67]
v_mfma_f32_16x16x32_bf16 v[64:67], a[140:143], v[124:127], v[64:67]
s_waitcnt lgkmcnt(5)
v_mfma_f32_16x16x32_bf16 v[56:59], a[144:147], v[80:83], v[56:59]
s_waitcnt lgkmcnt(4)
v_mfma_f32_16x16x32_bf16 v[56:59], a[156:159], v[84:87], v[56:59]
s_waitcnt lgkmcnt(3)
v_mfma_f32_16x16x32_bf16 v[56:59], a[168:171], v[88:91], v[56:59]
s_waitcnt lgkmcnt(2)
v_mfma_f32_16x16x32_bf16 v[56:59], a[180:183], v[92:95], v[56:59]
s_waitcnt lgkmcnt(1)
v_mfma_f32_16x16x32_bf16 v[56:59], a[192:195], v[96:99], v[56:59]
s_xor_b32 s45, s45, 0x10000
ds_read_b128 v[104:107], v17 offset:18432
s_waitcnt lgkmcnt(1)
v_mfma_f32_16x16x32_bf16 v[56:59], a[204:207], v[100:103], v[56:59]
s_add_u32 m0, s45, 0
v_mfma_f32_16x16x32_bf16 v[60:63], a[148:151], v[80:83], v[60:63]
ds_read_b128 v[108:111], v17 offset:19456
ds_read_b128 v[112:115], v17 offset:20480
v_mfma_f32_16x16x32_bf16 v[60:63], a[160:163], v[84:87], v[60:63]
v_mfma_f32_16x16x32_bf16 v[60:63], a[172:175], v[88:91], v[60:63]
s_barrier
v_mfma_f32_16x16x32_bf16 v[60:63], a[184:187], v[92:95], v[60:63]
global_load_lds_dwordx4 v9, s[38:39] offset:-2048 sc1
v_mfma_f32_16x16x32_bf16 v[60:63], a[196:199], v[96:99], v[60:63]
ds_read_b128 v[116:119], v17 offset:21504
v_mfma_f32_16x16x32_bf16 v[60:63], a[208:211], v[100:103], v[60:63]
v_mfma_f32_16x16x32_bf16 v[64:67], a[152:155], v[80:83], v[64:67]
ds_read_b128 v[120:123], v17 offset:22528
global_load_lds_dwordx4 v9, s[38:39] offset:-1024 sc1
v_mfma_f32_16x16x32_bf16 v[64:67], a[164:167], v[84:87], v[64:67]
v_mfma_f32_16x16x32_bf16 v[64:67], a[176:179], v[88:91], v[64:67]
ds_read_b128 v[124:127], v17 offset:23552
v_mfma_f32_16x16x32_bf16 v[64:67], a[188:191], v[92:95], v[64:67]
global_load_lds_dwordx4 v9, s[38:39] offset:0 sc1
v_mfma_f32_16x16x32_bf16 v[64:67], a[200:203], v[96:99], v[64:67]
v_mfma_f32_16x16x32_bf16 v[64:67], a[212:215], v[100:103], v[64:67]
s_waitcnt lgkmcnt(5)
v_mfma_f32_16x16x32_bf16 v[56:59], a[216:219], v[104:107], v[56:59]
s_waitcnt lgkmcnt(4)
global_load_lds_dwordx4 v9, s[38:39] offset:1024 sc1
v_mfma_f32_16x16x32_bf16 v[56:59], a[228:231], v[108:111], v[56:59]
s_waitcnt lgkmcnt(3)
v_mfma_f32_16x16x32_bf16 v[56:59], a[240:243], v[112:115], v[56:59]
s_waitcnt lgkmcnt(2)
v_mfma_f32_16x16x32_bf16 v[56:59], a[252:255], v[116:119], v[56:59]
s_waitcnt lgkmcnt(1)
global_load_lds_dwordx4 v9, s[38:39] offset:2048 sc1
v_mfma_f32_16x16x32_bf16 v[56:59], v[32:35], v[120:123], v[56:59]
s_waitcnt lgkmcnt(0)
v_mfma_f32_16x16x32_bf16 v[56:59], v[44:47], v[124:127], v[56:59]
v_mfma_f32_16x16x32_bf16 v[60:63], a[220:223], v[104:107], v[60:63]
global_load_lds_dwordx4 v9, s[38:39] offset:3072 sc1
v_add_u32 v9, 147456, v9
v_mfma_f32_16x16x32_bf16 v[60:63], a[232:235], v[108:111], v[60:63]
v_mfma_f32_16x16x32_bf16 v[60:63], a[244:247], v[112:115], v[60:63]
s_waitcnt vmcnt(8)
s_barrier
v_mfma_f32_16x16x32_bf16 v[60:63], v[24:27], v[116:119], v[60:63]
ds_read_b128 v[80:83], v17 offset:24576
v_min_u32 v9, v9, v10
v_mfma_f32_16x16x32_bf16 v[60:63], v[36:39], v[120:123], v[60:63]
ds_read_b128 v[84:87], v17 offset:25600
v_cvt_pk_bf16_f32 v128, v56, v57
v_mfma_f32_16x16x32_bf16 v[60:63], v[48:51], v[124:127], v[60:63]
ds_read_b128 v[88:91], v17 offset:26624
v_cvt_pk_bf16_f32 v129, v58, v59
v_mfma_f32_16x16x32_bf16 v[64:67], a[224:227], v[104:107], v[64:67]
ds_read_b128 v[92:95], v17 offset:27648
v_mfma_f32_16x16x32_bf16 v[64:67], a[236:239], v[108:111], v[64:67]
ds_read_b128 v[96:99], v17 offset:28672
v_mfma_f32_16x16x32_bf16 v[64:67], a[248:251], v[112:115], v[64:67]
ds_read_b128 v[100:103], v17 offset:29696
v_mfma_f32_16x16x32_bf16 v[64:67], v[28:31], v[116:119], v[64:67]
v_cvt_pk_bf16_f32 v130, v60, v61
v_cvt_pk_bf16_f32 v131, v62, v63
v_mfma_f32_16x16x32_bf16 v[64:67], v[40:43], v[120:123], v[64:67]
v_mfma_f32_16x16x32_bf16 v[64:67], v[52:55], v[124:127], v[64:67]
s_waitcnt lgkmcnt(5)
v_mfma_f32_16x16x32_bf16 v[68:71], a[0:3], v[80:83], 0
s_waitcnt lgkmcnt(4)
v_mfma_f32_16x16x32_bf16 v[68:71], a[12:15], v[84:87], v[68:71]
s_waitcnt lgkmcnt(3)
v_mfma_f32_16x16x32_bf16 v[68:71], a[24:27], v[88:91], v[68:71]
s_waitcnt lgkmcnt(2)
v_mfma_f32_16x16x32_bf16 v[68:71], a[36:39], v[92:95], v[68:71]
s_waitcnt lgkmcnt(1)
ds_read_b128 v[104:107], v17 offset:30720
v_cvt_pk_bf16_f32 v132, v64, v65
v_mfma_f32_16x16x32_bf16 v[68:71], a[48:51], v[96:99], v[68:71]
s_waitcnt lgkmcnt(1)
v_mfma_f32_16x16x32_bf16 v[68:71], a[60:63], v[100:103], v[68:71]
s_waitcnt vmcnt(6)
s_barrier
v_mfma_f32_16x16x32_bf16 v[72:75], a[4:7], v[80:83], 0
ds_read_b128 v[108:111], v17 offset:31744
v_cvt_pk_bf16_f32 v134, v64, v65
v_mfma_f32_16x16x32_bf16 v[72:75], a[16:19], v[84:87], v[72:75]
global_store_dwordx4 v11, v[128:131], s[38:39] sc1
v_mfma_f32_16x16x32_bf16 v[72:75], a[28:31], v[88:91], v[72:75]
ds_read_b128 v[112:115], v17 offset:32768
v_add_u32 v11, 110592, v11
v_mfma_f32_16x16x32_bf16 v[72:75], a[40:43], v[92:95], v[72:75]
v_cvt_pk_bf16_f32 v133, v66, v67
v_cvt_pk_bf16_f32 v135, v66, v67
v_mfma_f32_16x16x32_bf16 v[72:75], a[52:55], v[96:99], v[72:75]
ds_read_b128 v[116:119], v17 offset:33792
v_mfma_f32_16x16x32_bf16 v[72:75], a[64:67], v[100:103], v[72:75]
v_permlane16_swap_b32 v132, v134
v_mfma_f32_16x16x32_bf16 v[76:79], a[8:11], v[80:83], 0
ds_read_b128 v[120:123], v17 offset:34816
v_mfma_f32_16x16x32_bf16 v[76:79], a[20:23], v[84:87], v[76:79]
v_permlane16_swap_b32 v133, v135
v_mfma_f32_16x16x32_bf16 v[76:79], a[32:35], v[88:91], v[76:79]
ds_read_b128 v[124:127], v17 offset:35840
v_mfma_f32_16x16x32_bf16 v[76:79], a[44:47], v[92:95], v[76:79]
s_mov_b64 exec, s[76:77]
global_store_dwordx4 v12, v[132:135], s[38:39] sc1
s_mov_b64 exec, -1
v_mfma_f32_16x16x32_bf16 v[76:79], a[56:59], v[96:99], v[76:79]
v_add_u32 v12, 110592, v12
v_mfma_f32_16x16x32_bf16 v[76:79], a[68:71], v[100:103], v[76:79]
s_waitcnt lgkmcnt(5)
v_mfma_f32_16x16x32_bf16 v[68:71], a[72:75], v[104:107], v[68:71]
s_waitcnt lgkmcnt(4)
v_mfma_f32_16x16x32_bf16 v[68:71], a[84:87], v[108:111], v[68:71]
s_waitcnt lgkmcnt(3)
v_mfma_f32_16x16x32_bf16 v[68:71], a[96:99], v[112:115], v[68:71]
s_waitcnt lgkmcnt(2)
v_mfma_f32_16x16x32_bf16 v[68:71], a[108:111], v[116:119], v[68:71]
s_waitcnt lgkmcnt(1)
v_mfma_f32_16x16x32_bf16 v[68:71], a[120:123], v[120:123], v[68:71]
s_waitcnt lgkmcnt(0)
ds_read_b128 v[80:83], v17 offset:36864
v_mfma_f32_16x16x32_bf16 v[68:71], a[132:135], v[124:127], v[68:71]
v_mfma_f32_16x16x32_bf16 v[72:75], a[76:79], v[104:107], v[72:75]
ds_read_b128 v[84:87], v17 offset:37888
v_mfma_f32_16x16x32_bf16 v[72:75], a[88:91], v[108:111], v[72:75]
v_mfma_f32_16x16x32_bf16 v[72:75], a[100:103], v[112:115], v[72:75]
ds_read_b128 v[88:91], v17 offset:38912
v_mfma_f32_16x16x32_bf16 v[72:75], a[112:115], v[116:119], v[72:75]
v_mfma_f32_16x16x32_bf16 v[72:75], a[124:127], v[120:123], v[72:75]
ds_read_b128 v[92:95], v17 offset:39936
v_mfma_f32_16x16x32_bf16 v[72:75], a[136:139], v[124:127], v[72:75]
v_mfma_f32_16x16x32_bf16 v[76:79], a[80:83], v[104:107], v[76:79]
ds_read_b128 v[96:99], v17 offset:40960
v_mfma_f32_16x16x32_bf16 v[76:79], a[92:95], v[108:111], v[76:79]
v_mfma_f32_16x16x32_bf16 v[76:79], a[104:107], v[112:115], v[76:79]
ds_read_b128 v[100:103], v17 offset:41984
v_mfma_f32_16x16x32_bf16 v[76:79], a[116:119], v[116:119], v[76:79]
v_mfma_f32_16x16x32_bf16 v[76:79], a[128:131], v[120:123], v[76:79]
v_mfma_f32_16x16x32_bf16 v[76:79], a[140:143], v[124:127], v[76:79]
s_waitcnt lgkmcnt(5)
v_mfma_f32_16x16x32_bf16 v[68:71], a[144:147], v[80:83], v[68:71]
s_waitcnt lgkmcnt(4)
v_mfma_f32_16x16x32_bf16 v[68:71], a[156:159], v[84:87], v[68:71]
s_waitcnt lgkmcnt(3)
v_mfma_f32_16x16x32_bf16 v[68:71], a[168:171], v[88:91], v[68:71]
.Ldn_in5:
s_waitcnt lgkmcnt(2)
v_mfma_f32_16x16x32_bf16 v[68:71], a[180:183], v[92:95], v[68:71]
s_waitcnt lgkmcnt(1)
v_mfma_f32_16x16x32_bf16 v[68:71], a[192:195], v[96:99], v[68:71]
s_waitcnt lgkmcnt(0)
ds_read_b128 v[104:107], v17 offset:43008
v_mfma_f32_16x16x32_bf16 v[68:71], a[204:207], v[100:103], v[68:71]
s_add_u32 m0, s45, 24576
v_mfma_f32_16x16x32_bf16 v[72:75], a[148:151], v[80:83], v[72:75]
ds_read_b128 v[108:111], v17 offset:44032
ds_read_b128 v[112:115], v17 offset:45056
v_mfma_f32_16x16x32_bf16 v[72:75], a[160:163], v[84:87], v[72:75]
v_mfma_f32_16x16x32_bf16 v[72:75], a[172:175], v[88:91], v[72:75]
s_barrier
v_mfma_f32_16x16x32_bf16 v[72:75], a[184:187], v[92:95], v[72:75]
global_load_lds_dwordx4 v9, s[38:39] offset:-2048 sc1
v_mfma_f32_16x16x32_bf16 v[72:75], a[196:199], v[96:99], v[72:75]
ds_read_b128 v[116:119], v17 offset:46080
v_mfma_f32_16x16x32_bf16 v[72:75], a[208:211], v[100:103], v[72:75]
v_mfma_f32_16x16x32_bf16 v[76:79], a[152:155], v[80:83], v[76:79]
ds_read_b128 v[120:123], v17 offset:47104
global_load_lds_dwordx4 v9, s[38:39] offset:-1024 sc1
v_mfma_f32_16x16x32_bf16 v[76:79], a[164:167], v[84:87], v[76:79]
v_mfma_f32_16x16x32_bf16 v[76:79], a[176:179], v[88:91], v[76:79]
ds_read_b128 v[124:127], v17 offset:48128
v_mfma_f32_16x16x32_bf16 v[76:79], a[188:191], v[92:95], v[76:79]
global_load_lds_dwordx4 v9, s[38:39] offset:0 sc1
v_mfma_f32_16x16x32_bf16 v[76:79], a[200:203], v[96:99], v[76:79]
v_mfma_f32_16x16x32_bf16 v[76:79], a[212:215], v[100:103], v[76:79]
s_waitcnt lgkmcnt(5)
v_mfma_f32_16x16x32_bf16 v[68:71], a[216:219], v[104:107], v[68:71]
s_waitcnt lgkmcnt(4)
global_load_lds_dwordx4 v9, s[38:39] offset:1024 sc1
v_mfma_f32_16x16x32_bf16 v[68:71], a[228:231], v[108:111], v[68:71]
s_waitcnt lgkmcnt(3)
v_mfma_f32_16x16x32_bf16 v[68:71], a[240:243], v[112:115], v[68:71]
s_waitcnt lgkmcnt(2)
v_mfma_f32_16x16x32_bf16 v[68:71], a[252:255], v[116:119], v[68:71]
s_waitcnt lgkmcnt(1)
global_load_lds_dwordx4 v9, s[38:39] offset:2048 sc1
v_mfma_f32_16x16x32_bf16 v[68:71], v[32:35], v[120:123], v[68:71]
s_waitcnt lgkmcnt(0)
v_mfma_f32_16x16x32_bf16 v[68:71], v[44:47], v[124:127], v[68:71]
v_xor_b32 v17, 0x10000, v17
v_mfma_f32_16x16x32_bf16 v[72:75], a[220:223], v[104:107], v[72:75]
global_load_lds_dwordx4 v9, s[38:39] offset:3072 sc1
v_add_u32 v9, 147456, v9
v_mfma_f32_16x16x32_bf16 v[72:75], a[232:235], v[108:111], v[72:75]
v_mfma_f32_16x16x32_bf16 v[72:75], a[244:247], v[112:115], v[72:75]
s_waitcnt vmcnt(8)
s_barrier
v_mfma_f32_16x16x32_bf16 v[72:75], v[24:27], v[116:119], v[72:75]
ds_read_b128 v[80:83], v17 offset:0
v_min_u32 v9, v9, v10
v_mfma_f32_16x16x32_bf16 v[72:75], v[36:39], v[120:123], v[72:75]
ds_read_b128 v[84:87], v17 offset:1024
v_cvt_pk_bf16_f32 v128, v68, v69
v_mfma_f32_16x16x32_bf16 v[72:75], v[48:51], v[124:127], v[72:75]
ds_read_b128 v[88:91], v17 offset:2048
v_cvt_pk_bf16_f32 v129, v70, v71
v_mfma_f32_16x16x32_bf16 v[76:79], a[224:227], v[104:107], v[76:79]
ds_read_b128 v[92:95], v17 offset:3072
v_mfma_f32_16x16x32_bf16 v[76:79], a[236:239], v[108:111], v[76:79]
ds_read_b128 v[96:99], v17 offset:4096
v_mfma_f32_16x16x32_bf16 v[76:79], a[248:251], v[112:115], v[76:79]
ds_read_b128 v[100:103], v17 offset:5120
v_mfma_f32_16x16x32_bf16 v[76:79], v[28:31], v[116:119], v[76:79]
v_cvt_pk_bf16_f32 v130, v72, v73
v_cvt_pk_bf16_f32 v131, v74, v75
v_mfma_f32_16x16x32_bf16 v[76:79], v[40:43], v[120:123], v[76:79]
v_mfma_f32_16x16x32_bf16 v[76:79], v[52:55], v[124:127], v[76:79]
s_sub_u32 s70, s70, 1
s_cbranch_scc0 .Ldn_top6
v_cvt_pk_bf16_f32 v128, v68, v69
v_cvt_pk_bf16_f32 v129, v70, v71
v_cvt_pk_bf16_f32 v130, v72, v73
v_cvt_pk_bf16_f32 v131, v74, v75
global_store_dwordx4 v11, v[128:131], s[38:39] sc1
v_add_u32 v11, 110592, v11
v_cvt_pk_bf16_f32 v132, v76, v77
v_cvt_pk_bf16_f32 v134, v76, v77
v_cvt_pk_bf16_f32 v133, v78, v79
v_cvt_pk_bf16_f32 v135, v78, v79
v_permlane16_swap_b32 v132, v134
s_nop 0
v_permlane16_swap_b32 v133, v135
s_mov_b64 exec, s[76:77]
global_store_dwordx4 v12, v[132:135], s[38:39] sc1
s_mov_b64 exec, -1
v_add_u32 v12, 110592, v12
s_waitcnt vmcnt(2)
s_barrier
s_waitcnt vmcnt(0)
s_barrier
s_endpgm
.Ldn_w2:
v_add_u32 v254, -1, v14
v_add_u32 v255, -1, v14
v_add_lshl_u32 v252, v16, 0, 7
v_bfi_b32 v252, s69, v252, v15
v_add_lshl_u32 v253, v14, 0, 7
v_bfi_b32 v253, s69, v253, v13
s_mov_b32 s76, 0xffff
s_mov_b32 s77, 0xffff
s_add_u32 m0, s45, 24576
s_xor_b32 s45, s45, 0x10000
s_mov_b32 m0, s45
s_barrier
global_load_lds_dwordx4 v9, s[38:39] offset:-2048 sc1
global_load_lds_dwordx4 v9, s[38:39] offset:-1024 sc1
global_load_lds_dwordx4 v9, s[38:39] offset:0 sc1
global_load_lds_dwordx4 v9, s[38:39] offset:1024 sc1
global_load_lds_dwordx4 v9, s[38:39] offset:2048 sc1
global_load_lds_dwordx4 v9, s[38:39] offset:3072 sc1
v_add_u32 v9, 147456, v9
s_mov_b32 s70, 32
s_waitcnt vmcnt(0) lgkmcnt(0)
s_nop 7
s_branch .Ldn_in8
.p2align 6
.Ldn_top9:
s_waitcnt lgkmcnt(5)
v_mfma_f32_16x16x32_bf16 v[56:59], a[0:3], v[80:83], 0
s_waitcnt lgkmcnt(4)
v_mfma_f32_16x16x32_bf16 v[56:59], a[12:15], v[84:87], v[56:59]
s_waitcnt lgkmcnt(3)
v_mfma_f32_16x16x32_bf16 v[56:59], a[24:27], v[88:91], v[56:59]
s_waitcnt lgkmcnt(2)
v_mfma_f32_16x16x32_bf16 v[56:59], a[36:39], v[92:95], v[56:59]
s_waitcnt lgkmcnt(1)
ds_read_b128 v[104:107], v17 offset:6144
v_cvt_pk_bf16_f32 v132, v76, v77
v_mfma_f32_16x16x32_bf16 v[56:59], a[48:51], v[96:99], v[56:59]
s_waitcnt lgkmcnt(1)
v_mfma_f32_16x16x32_bf16 v[56:59], a[60:63], v[100:103], v[56:59]
s_waitcnt vmcnt(6)
s_barrier
v_mfma_f32_16x16x32_bf16 v[60:63], a[4:7], v[80:83], 0
ds_read_b128 v[108:111], v17 offset:7168
v_cvt_pk_bf16_f32 v134, v76, v77
v_mfma_f32_16x16x32_bf16 v[60:63], a[16:19], v[84:87], v[60:63]
v_cvt_pk_bf16_f32 v133, v78, v79
v_cvt_pk_bf16_f32 v135, v78, v79
v_mfma_f32_16x16x32_bf16 v[60:63], a[28:31], v[88:91], v[60:63]
ds_read_b128 v[112:115], v17 offset:8192
v_mfma_f32_16x16x32_bf16 v[60:63], a[40:43], v[92:95], v[60:63]
global_store_dwordx4 v11, v[128:131], s[38:39] sc1
v_mfma_f32_16x16x32_bf16 v[60:63], a[52:55], v[96:99], v[60:63]
ds_read_b128 v[116:119], v17 offset:9216
v_add_u32 v11, 110592, v11
v_mfma_f32_16x16x32_bf16 v[60:63], a[64:67], v[100:103], v[60:63]
v_permlane16_swap_b32 v132, v134
v_mfma_f32_16x16x32_bf16 v[64:67], a[8:11], v[80:83], 0
ds_read_b128 v[120:123], v17 offset:10240
v_mfma_f32_16x16x32_bf16 v[64:67], a[20:23], v[84:87], v[64:67]
v_permlane16_swap_b32 v133, v135
v_mfma_f32_16x16x32_bf16 v[64:67], a[32:35], v[88:91], v[64:67]
ds_read_b128 v[124:127], v17 offset:11264
v_mfma_f32_16x16x32_bf16 v[64:67], a[44:47], v[92:95], v[64:67]
s_mov_b64 exec, s[76:77]
global_store_dwordx4 v12, v[132:135], s[38:39] sc1
s_mov_b64 exec, -1
v_mfma_f32_16x16x32_bf16 v[64:67], a[56:59], v[96:99], v[64:67]
v_add_u32 v12, 110592, v12
v_mfma_f32_16x16x32_bf16 v[64:67], a[68:71], v[100:103], v[64:67]
s_waitcnt lgkmcnt(5)
v_mfma_f32_16x16x32_bf16 v[56:59], a[72:75], v[104:107], v[56:59]
s_waitcnt lgkmcnt(4)
v_mfma_f32_16x16x32_bf16 v[56:59], a[84:87], v[108:111], v[56:59]
s_waitcnt lgkmcnt(3)
v_mfma_f32_16x16x32_bf16 v[56:59], a[96:99], v[112:115], v[56:59]
s_waitcnt lgkmcnt(2)
v_mfma_f32_16x16x32_bf16 v[56:59], a[108:111], v[116:119], v[56:59]
s_waitcnt lgkmcnt(1)
v_mfma_f32_16x16x32_bf16 v[56:59], a[120:123], v[120:123], v[56:59]
s_waitcnt lgkmcnt(0)
ds_read_b128 v[80:83], v17 offset:12288
v_mfma_f32_16x16x32_bf16 v[56:59], a[132:135], v[124:127], v[56:59]
v_mfma_f32_16x16x32_bf16 v[60:63], a[76:79], v[104:107], v[60:63]
ds_read_b128 v[84:87], v17 offset:13312
v_mfma_f32_16x16x32_bf16 v[60:63], a[88:91], v[108:111], v[60:63]
v_mfma_f32_16x16x32_bf16 v[60:63], a[100:103], v[112:115], v[60:63]
ds_read_b128 v[88:91], v17 offset:14336
v_mfma_f32_16x16x32_bf16 v[60:63], a[112:115], v[116:119], v[60:63]
v_mfma_f32_16x16x32_bf16 v[60:63], a[124:127], v[120:123], v[60:63]
ds_read_b128 v[92:95], v17 offset:15360
v_mfma_f32_16x16x32_bf16 v[60:63], a[136:139], v[124:127], v[60:63]
v_mfma_f32_16x16x32_bf16 v[64:67], a[80:83], v[104:107], v[64:67]
ds_read_b128 v[96:99], v17 offset:16384
v_mfma_f32_16x16x32_bf16 v[64:67], a[92:95], v[108:111], v[64:67]
v_mfma_f32_16x16x32_bf16 v[64:67], a[104:107], v[112:115], v[64:67]
ds_read_b128 v[100:103], v17 offset:17408
v_mfma_f32_16x16x32_bf16 v[64:67], a[116:119], v[116:119], v[64:67]
v_mfma_f32_16x16x32_bf16 v[64:67], a[128:131], v[120:123], v[64:67]
v_mfma_f32_16x16x32_bf16 v[64:67], a[140:143], v[124:127], v[64:67]
s_waitcnt lgkmcnt(5)
v_mfma_f32_16x16x32_bf16 v[56:59], a[144:147], v[80:83], v[56:59]
s_waitcnt lgkmcnt(4)
v_mfma_f32_16x16x32_bf16 v[56:59], a[156:159], v[84:87], v[56:59]
s_waitcnt lgkmcnt(3)
v_mfma_f32_16x16x32_bf16 v[56:59], a[168:171], v[88:91], v[56:59]
s_waitcnt lgkmcnt(2)
v_mfma_f32_16x16x32_bf16 v[56:59], a[180:183], v[92:95], v[56:59]
s_waitcnt lgkmcnt(1)
v_mfma_f32_16x16x32_bf16 v[56:59], a[192:195], v[96:99], v[56:59]
s_xor_b32 s45, s45, 0x10000
ds_read_b128 v[104:107], v17 offset:18432
s_waitcnt lgkmcnt(1)
v_mfma_f32_16x16x32_bf16 v[56:59], a[204:207], v[100:103], v[56:59]
s_add_u32 m0, s45, 0
v_mfma_f32_16x16x32_bf16 v[60:63], a[148:151], v[80:83], v[60:63]
ds_read_b128 v[108:111], v17 offset:19456
ds_read_b128 v[112:115], v17 offset:20480
v_mfma_f32_16x16x32_bf16 v[60:63], a[160:163], v[84:87], v[60:63]
v_mfma_f32_16x16x32_bf16 v[60:63], a[172:175], v[88:91], v[60:63]
s_barrier
v_mfma_f32_16x16x32_bf16 v[60:63], a[184:187], v[92:95], v[60:63]
v_mfma_f32_16x16x32_bf16 v[60:63], a[196:199], v[96:99], v[60:63]
ds_read_b128 v[116:119], v17 offset:21504
global_load_lds_dwordx4 v9, s[38:39] offset:-2048 sc1
v_mfma_f32_16x16x32_bf16 v[60:63], a[208:211], v[100:103], v[60:63]
v_mfma_f32_16x16x32_bf16 v[64:67], a[152:155], v[80:83], v[64:67]
ds_read_b128 v[120:123], v17 offset:22528
v_mfma_f32_16x16x32_bf16 v[64:67], a[164:167], v[84:87], v[64:67]
global_load_lds_dwordx4 v9, s[38:39] offset:-1024 sc1
)"
R"(v_mfma_f32_16x16x32_bf16 v[64:67], a[176:179], v[88:91], v[64:67]
ds_read_b128 v[124:127], v17 offset:23552
v_mfma_f32_16x16x32_bf16 v[64:67], a[188:191], v[92:95], v[64:67]
v_mfma_f32_16x16x32_bf16 v[64:67], a[200:203], v[96:99], v[64:67]
global_load_lds_dwordx4 v9, s[38:39] offset:0 sc1
v_mfma_f32_16x16x32_bf16 v[64:67], a[212:215], v[100:103], v[64:67]
s_waitcnt lgkmcnt(5)
v_mfma_f32_16x16x32_bf16 v[56:59], a[216:219], v[104:107], v[56:59]
s_waitcnt lgkmcnt(4)
v_mfma_f32_16x16x32_bf16 v[56:59], a[228:231], v[108:111], v[56:59]
s_waitcnt lgkmcnt(3)
global_load_lds_dwordx4 v9, s[38:39] offset:1024 sc1
v_mfma_f32_16x16x32_bf16 v[56:59], a[240:243], v[112:115], v[56:59]
s_waitcnt lgkmcnt(2)
v_mfma_f32_16x16x32_bf16 v[56:59], a[252:255], v[116:119], v[56:59]
s_waitcnt lgkmcnt(1)
v_mfma_f32_16x16x32_bf16 v[56:59], v[32:35], v[120:123], v[56:59]
s_waitcnt lgkmcnt(0)
global_load_lds_dwordx4 v9, s[38:39] offset:2048 sc1
v_mfma_f32_16x16x32_bf16 v[56:59], v[44:47], v[124:127], v[56:59]
v_mfma_f32_16x16x32_bf16 v[60:63], a[220:223], v[104:107], v[60:63]
v_mfma_f32_16x16x32_bf16 v[60:63], a[232:235], v[108:111], v[60:63]
v_mfma_f32_16x16x32_bf16 v[60:63], a[244:247], v[112:115], v[60:63]
s_waitcnt vmcnt(7)
s_barrier
v_mfma_f32_16x16x32_bf16 v[60:63], v[24:27], v[116:119], v[60:63]
ds_read_b128 v[80:83], v17 offset:24576
global_load_lds_dwordx4 v9, s[38:39] offset:3072 sc1
v_mfma_f32_16x16x32_bf16 v[60:63], v[36:39], v[120:123], v[60:63]
ds_read_b128 v[84:87], v17 offset:25600
v_add_u32 v9, 147456, v9
v_mfma_f32_16x16x32_bf16 v[60:63], v[48:51], v[124:127], v[60:63]
ds_read_b128 v[88:91], v17 offset:26624
v_min_u32 v9, v9, v10
v_mfma_f32_16x16x32_bf16 v[64:67], a[224:227], v[104:107], v[64:67]
ds_read_b128 v[92:95], v17 offset:27648
v_cvt_pk_bf16_f32 v128, v56, v57
v_mfma_f32_16x16x32_bf16 v[64:67], a[236:239], v[108:111], v[64:67]
ds_read_b128 v[96:99], v17 offset:28672
v_cvt_pk_bf16_f32 v129, v58, v59
v_mfma_f32_16x16x32_bf16 v[64:67], a[248:251], v[112:115], v[64:67]
ds_read_b128 v[100:103], v17 offset:29696
v_mfma_f32_16x16x32_bf16 v[64:67], v[28:31], v[116:119], v[64:67]
v_cvt_pk_bf16_f32 v130, v60, v61
v_cvt_pk_bf16_f32 v131, v62, v63
v_mfma_f32_16x16x32_bf16 v[64:67], v[40:43], v[120:123], v[64:67]
v_mfma_f32_16x16x32_bf16 v[64:67], v[52:55], v[124:127], v[64:67]
s_waitcnt lgkmcnt(5)
v_mfma_f32_16x16x32_bf16 v[68:71], a[0:3], v[80:83], 0
s_waitcnt lgkmcnt(4)
v_mfma_f32_16x16x32_bf16 v[68:71], a[12:15], v[84:87], v[68:71]
s_waitcnt lgkmcnt(3)
v_mfma_f32_16x16x32_bf16 v[68:71], a[24:27], v[88:91], v[68:71]
s_waitcnt lgkmcnt(2)
v_mfma_f32_16x16x32_bf16 v[68:71], a[36:39], v[92:95], v[68:71]
s_waitcnt lgkmcnt(1)
ds_read_b128 v[104:107], v17 offset:30720
v_cvt_pk_bf16_f32 v132, v64, v65
v_mfma_f32_16x16x32_bf16 v[68:71], a[48:51], v[96:99], v[68:71]
s_waitcnt lgkmcnt(1)
v_mfma_f32_16x16x32_bf16 v[68:71], a[60:63], v[100:103], v[68:71]
s_waitcnt vmcnt(6)
s_barrier
v_mfma_f32_16x16x32_bf16 v[72:75], a[4:7], v[80:83], 0
ds_read_b128 v[108:111], v17 offset:31744
v_cvt_pk_bf16_f32 v134, v64, v65
v_mfma_f32_16x16x32_bf16 v[72:75], a[16:19], v[84:87], v[72:75]
v_cvt_pk_bf16_f32 v133, v66, v67
v_cvt_pk_bf16_f32 v135, v66, v67
v_mfma_f32_16x16x32_bf16 v[72:75], a[28:31], v[88:91], v[72:75]
ds_read_b128 v[112:115], v17 offset:32768
v_mfma_f32_16x16x32_bf16 v[72:75], a[40:43], v[92:95], v[72:75]
global_store_dwordx4 v11, v[128:131], s[38:39] sc1
v_mfma_f32_16x16x32_bf16 v[72:75], a[52:55], v[96:99], v[72:75]
ds_read_b128 v[116:119], v17 offset:33792
v_add_u32 v11, 110592, v11
v_mfma_f32_16x16x32_bf16 v[72:75], a[64:67], v[100:103], v[72:75]
v_permlane16_swap_b32 v132, v134
v_mfma_f32_16x16x32_bf16 v[76:79], a[8:11], v[80:83], 0
ds_read_b128 v[120:123], v17 offset:34816
v_mfma_f32_16x16x32_bf16 v[76:79], a[20:23], v[84:87], v[76:79]
v_permlane16_swap_b32 v133, v135
v_mfma_f32_16x16x32_bf16 v[76:79], a[32:35], v[88:91], v[76:79]
ds_read_b128 v[124:127], v17 offset:35840
v_mfma_f32_16x16x32_bf16 v[76:79], a[44:47], v[92:95], v[76:79]
s_mov_b64 exec, s[76:77]
global_store_dwordx4 v12, v[132:135], s[38:39] sc1
s_mov_b64 exec, -1
v_mfma_f32_16x16x32_bf16 v[76:79], a[56:59], v[96:99], v[76:79]
v_add_u32 v12, 110592, v12
v_mfma_f32_16x16x32_bf16 v[76:79], a[68:71], v[100:103], v[76:79]
s_waitcnt lgkmcnt(5)
v_mfma_f32_16x16x32_bf16 v[68:71], a[72:75], v[104:107], v[68:71]
s_waitcnt lgkmcnt(4)
v_mfma_f32_16x16x32_bf16 v[68:71], a[84:87], v[108:111], v[68:71]
s_waitcnt lgkmcnt(3)
v_mfma_f32_16x16x32_bf16 v[68:71], a[96:99], v[112:115], v[68:71]
s_waitcnt lgkmcnt(2)
v_mfma_f32_16x16x32_bf16 v[68:71], a[108:111], v[116:119], v[68:71]
s_waitcnt lgkmcnt(1)
v_mfma_f32_16x16x32_bf16 v[68:71], a[120:123], v[120:123], v[68:71]
s_waitcnt lgkmcnt(0)
ds_read_b128 v[80:83], v17 offset:36864
v_mfma_f32_16x16x32_bf16 v[68:71], a[132:135], v[124:127], v[68:71]
v_mfma_f32_16x16x32_bf16 v[72:75], a[76:79], v[104:107], v[72:75]
ds_read_b128 v[84:87], v17 offset:37888
v_mfma_f32_16x16x32_bf16 v[72:75], a[88:91], v[108:111], v[72:75]
v_mfma_f32_16x16x32_bf16 v[72:75], a[100:103], v[112:115], v[72:75]
ds_read_b128 v[88:91], v17 offset:38912
v_mfma_f32_16x16x32_bf16 v[72:75], a[112:115], v[116:119], v[72:75]
v_mfma_f32_16x16x32_bf16 v[72:75], a[124:127], v[120:123], v[72:75]
ds_read_b128 v[92:95], v17 offset:39936
v_mfma_f32_16x16x32_bf16 v[72:75], a[136:139], v[124:127], v[72:75]
v_mfma_f32_16x16x32_bf16 v[76:79], a[80:83], v[104:107], v[76:79]
ds_read_b128 v[96:99], v17 offset:40960
v_mfma_f32_16x16x32_bf16 v[76:79], a[92:95], v[108:111], v[76:79]
v_mfma_f32_16x16x32_bf16 v[76:79], a[104:107], v[112:115], v[76:79]
ds_read_b128 v[100:103], v17 offset:41984
v_mfma_f32_16x16x32_bf16 v[76:79], a[116:119], v[116:119], v[76:79]
v_mfma_f32_16x16x32_bf16 v[76:79], a[128:131], v[120:123], v[76:79]
v_mfma_f32_16x16x32_bf16 v[76:79], a[140:143], v[124:127], v[76:79]
s_waitcnt lgkmcnt(5)
v_mfma_f32_16x16x32_bf16 v[68:71], a[144:147], v[80:83], v[68:71]
s_waitcnt lgkmcnt(4)
v_mfma_f32_16x16x32_bf16 v[68:71], a[156:159], v[84:87], v[68:71]
s_waitcnt lgkmcnt(3)
v_mfma_f32_16x16x32_bf16 v[68:71], a[168:171], v[88:91], v[68:71]
.Ldn_in8:
s_waitcnt lgkmcnt(2)
v_mfma_f32_16x16x32_bf16 v[68:71], a[180:183], v[92:95], v[68:71]
s_waitcnt lgkmcnt(1)
v_mfma_f32_16x16x32_bf16 v[68:71], a[192:195], v[96:99], v[68:71]
s_waitcnt lgkmcnt(0)
ds_read_b128 v[104:107], v17 offset:43008
v_mfma_f32_16x16x32_bf16 v[68:71], a[204:207], v[100:103], v[68:71]
s_add_u32 m0, s45, 24576
v_mfma_f32_16x16x32_bf16 v[72:75], a[148:151], v[80:83], v[72:75]
ds_read_b128 v[108:111], v17 offset:44032
ds_read_b128 v[112:115], v17 offset:45056
v_mfma_f32_16x16x32_bf16 v[72:75], a[160:163], v[84:87], v[72:75]
v_mfma_f32_16x16x32_bf16 v[72:75], a[172:175], v[88:91], v[72:75]
s_barrier
v_mfma_f32_16x16x32_bf16 v[72:75], a[184:187], v[92:95], v[72:75]
v_mfma_f32_16x16x32_bf16 v[72:75], a[196:199], v[96:99], v[72:75]
ds_read_b128 v[116:119], v17 offset:46080
global_load_lds_dwordx4 v9, s[38:39] offset:-2048 sc1
v_mfma_f32_16x16x32_bf16 v[72:75], a[208:211], v[100:103], v[72:75]
v_mfma_f32_16x16x32_bf16 v[76:79], a[152:155], v[80:83], v[76:79]
ds_read_b128 v[120:123], v17 offset:47104
v_mfma_f32_16x16x32_bf16 v[76:79], a[164:167], v[84:87], v[76:79]
global_load_lds_dwordx4 v9, s[38:39] offset:-1024 sc1
v_mfma_f32_16x16x32_bf16 v[76:79], a[176:179], v[88:91], v[76:79]
ds_read_b128 v[124:127], v17 offset:48128
v_mfma_f32_16x16x32_bf16 v[76:79], a[188:191], v[92:95], v[76:79]
v_mfma_f32_16x16x32_bf16 v[76:79], a[200:203], v[96:99], v[76:79]
global_load_lds_dwordx4 v9, s[38:39] offset:0 sc1
v_mfma_f32_16x16x32_bf16 v[76:79], a[212:215], v[100:103], v[76:79]
s_waitcnt lgkmcnt(5)
v_mfma_f32_16x16x32_bf16 v[68:71], a[216:219], v[104:107], v[68:71]
s_waitcnt lgkmcnt(4)
v_mfma_f32_16x16x32_bf16 v[68:71], a[228:231], v[108:111], v[68:71]
s_waitcnt lgkmcnt(3)
global_load_lds_dwordx4 v9, s[38:39] offset:1024 sc1
v_mfma_f32_16x16x32_bf16 v[68:71], a[240:243], v[112:115], v[68:71]
s_waitcnt lgkmcnt(2)
v_mfma_f32_16x16x32_bf16 v[68:71], a[252:255], v[116:119], v[68:71]
s_waitcnt lgkmcnt(1)
v_mfma_f32_16x16x32_bf16 v[68:71], v[32:35], v[120:123], v[68:71]
s_waitcnt lgkmcnt(0)
global_load_lds_dwordx4 v9, s[38:39] offset:2048 sc1
v_mfma_f32_16x16x32_bf16 v[68:71], v[44:47], v[124:127], v[68:71]
v_xor_b32 v17, 0x10000, v17
v_mfma_f32_16x16x32_bf16 v[72:75], a[220:223], v[104:107], v[72:75]
v_mfma_f32_16x16x32_bf16 v[72:75], a[232:235], v[108:111], v[72:75]
v_mfma_f32_16x16x32_bf16 v[72:75], a[244:247], v[112:115], v[72:75]
s_waitcnt vmcnt(7)
s_barrier
v_mfma_f32_16x16x32_bf16 v[72:75], v[24:27], v[116:119], v[72:75]
ds_read_b128 v[80:83], v17 offset:0
global_load_lds_dwordx4 v9, s[38:39] offset:3072 sc1
v_mfma_f32_16x16x32_bf16 v[72:75], v[36:39], v[120:123], v[72:75]
ds_read_b128 v[84:87], v17 offset:1024
v_add_u32 v9, 147456, v9
v_mfma_f32_16x16x32_bf16 v[72:75], v[48:51], v[124:127], v[72:75]
ds_read_b128 v[88:91], v17 offset:2048
v_min_u32 v9, v9, v10
v_mfma_f32_16x16x32_bf16 v[76:79], a[224:227], v[104:107], v[76:79]
ds_read_b128 v[92:95], v17 offset:3072
v_cvt_pk_bf16_f32 v128, v68, v69
v_mfma_f32_16x16x32_bf16 v[76:79], a[236:239], v[108:111], v[76:79]
ds_read_b128 v[96:99], v17 offset:4096
v_cvt_pk_bf16_f32 v129, v70, v71
v_mfma_f32_16x16x32_bf16 v[76:79], a[248:251], v[112:115], v[76:79]
ds_read_b128 v[100:103], v17 offset:5120
v_mfma_f32_16x16x32_bf16 v[76:79], v[28:31], v[116:119], v[76:79]
v_cvt_pk_bf16_f32 v130, v72, v73
v_cvt_pk_bf16_f32 v131, v74, v75
v_mfma_f32_16x16x32_bf16 v[76:79], v[40:43], v[120:123], v[76:79]
v_mfma_f32_16x16x32_bf16 v[76:79], v[52:55], v[124:127], v[76:79]
s_sub_u32 s70, s70, 1
s_cbranch_scc0 .Ldn_top9
v_cvt_pk_bf16_f32 v128, v68, v69
v_cvt_pk_bf16_f32 v129, v70, v71
v_cvt_pk_bf16_f32 v130, v72, v73
v_cvt_pk_bf16_f32 v131, v74, v75
global_store_dwordx4 v11, v[128:131], s[38:39] sc1
v_add_u32 v11, 110592, v11
v_cvt_pk_bf16_f32 v132, v76, v77
v_cvt_pk_bf16_f32 v134, v76, v77
v_cvt_pk_bf16_f32 v133, v78, v79
v_cvt_pk_bf16_f32 v135, v78, v79
v_permlane16_swap_b32 v132, v134
s_nop 0
v_permlane16_swap_b32 v133, v135
s_mov_b64 exec, s[76:77]
global_store_dwordx4 v12, v[132:135], s[38:39] sc1
s_mov_b64 exec, -1
v_add_u32 v12, 110592, v12
s_waitcnt vmcnt(2)
s_barrier
s_waitcnt vmcnt(0)
s_barrier
s_endpgm
.Ldn_w3:
v_add_u32 v254, -1, v14
v_add_u32 v255, -1, v14
v_add_lshl_u32 v252, v16, 0, 7
v_bfi_b32 v252, s69, v252, v15
v_add_lshl_u32 v253, v14, 0, 7
v_bfi_b32 v253, s69, v253, v13
s_mov_b32 s76, 0xffff
s_mov_b32 s77, 0xffff
s_add_u32 m0, s45, 24576
s_xor_b32 s45, s45, 0x10000
s_mov_b32 m0, s45
s_barrier
global_load_lds_dwordx4 v9, s[38:39] offset:-2048 sc1
global_load_lds_dwordx4 v9, s[38:39] offset:-1024 sc1
global_load_lds_dwordx4 v9, s[38:39] offset:0 sc1
global_load_lds_dwordx4 v9, s[38:39] offset:1024 sc1
global_load_lds_dwordx4 v9, s[38:39] offset:2048 sc1
global_load_lds_dwordx4 v9, s[38:39] offset:3072 sc1
v_add_u32 v9, 147456, v9
s_mov_b32 s70, 32
s_waitcnt vmcnt(0) lgkmcnt(0)
s_nop 7
s_branch .Ldn_in11
.p2align 6
.Ldn_top12:
s_waitcnt lgkmcnt(5)
v_mfma_f32_16x16x32_bf16 v[56:59], a[0:3], v[80:83], 0
s_waitcnt lgkmcnt(4)
v_mfma_f32_16x16x32_bf16 v[56:59], a[12:15], v[84:87], v[56:59]
s_waitcnt lgkmcnt(3)
v_mfma_f32_16x16x32_bf16 v[56:59], a[24:27], v[88:91], v[56:59]
s_waitcnt lgkmcnt(2)
v_mfma_f32_16x16x32_bf16 v[56:59], a[36:39], v[92:95], v[56:59]
s_waitcnt lgkmcnt(1)
ds_read_b128 v[104:107], v17 offset:6144
v_cvt_pk_bf16_f32 v132, v76, v77
v_mfma_f32_16x16x32_bf16 v[56:59], a[48:51], v[96:99], v[56:59]
s_waitcnt lgkmcnt(1)
v_mfma_f32_16x16x32_bf16 v[56:59], a[60:63], v[100:103], v[56:59]
s_waitcnt vmcnt(7)
s_barrier
v_mfma_f32_16x16x32_bf16 v[60:63], a[4:7], v[80:83], 0
ds_read_b128 v[108:111], v17 offset:7168
v_cvt_pk_bf16_f32 v134, v76, v77
v_mfma_f32_16x16x32_bf16 v[60:63], a[16:19], v[84:87], v[60:63]
s_mov_b64 exec, 1
global_store_dword v252, v16, s[38:39] sc1
s_mov_b64 exec, -1
v_mfma_f32_16x16x32_bf16 v[60:63], a[28:31], v[88:91], v[60:63]
ds_read_b128 v[112:115], v17 offset:8192
v_add_u32 v16, 1, v16
v_mfma_f32_16x16x32_bf16 v[60:63], a[40:43], v[92:95], v[60:63]
v_add_lshl_u32 v252, v16, 0, 7
v_bfi_b32 v252, s69, v252, v15
v_mfma_f32_16x16x32_bf16 v[60:63], a[52:55], v[96:99], v[60:63]
ds_read_b128 v[116:119], v17 offset:9216
v_cvt_pk_bf16_f32 v133, v78, v79
v_mfma_f32_16x16x32_bf16 v[60:63], a[64:67], v[100:103], v[60:63]
global_store_dwordx4 v11, v[128:131], s[38:39] sc1
v_mfma_f32_16x16x32_bf16 v[64:67], a[8:11], v[80:83], 0
ds_read_b128 v[120:123], v17 offset:10240
v_add_u32 v11, 110592, v11
v_mfma_f32_16x16x32_bf16 v[64:67], a[20:23], v[84:87], v[64:67]
v_cvt_pk_bf16_f32 v135, v78, v79
v_mfma_f32_16x16x32_bf16 v[64:67], a[32:35], v[88:91], v[64:67]
ds_read_b128 v[124:127], v17 offset:11264
v_mfma_f32_16x16x32_bf16 v[64:67], a[44:47], v[92:95], v[64:67]
v_permlane16_swap_b32 v132, v134
v_mfma_f32_16x16x32_bf16 v[64:67], a[56:59], v[96:99], v[64:67]
v_permlane16_swap_b32 v133, v135
v_mfma_f32_16x16x32_bf16 v[64:67], a[68:71], v[100:103], v[64:67]
s_waitcnt lgkmcnt(5)
s_mov_b64 exec, s[76:77]
global_store_dwordx4 v12, v[132:135], s[38:39] sc1
s_mov_b64 exec, -1
v_mfma_f32_16x16x32_bf16 v[56:59], a[72:75], v[104:107], v[56:59]
s_waitcnt lgkmcnt(4)
v_add_u32 v12, 110592, v12
v_mfma_f32_16x16x32_bf16 v[56:59], a[84:87], v[108:111], v[56:59]
s_waitcnt lgkmcnt(3)
v_mfma_f32_16x16x32_bf16 v[56:59], a[96:99], v[112:115], v[56:59]
s_waitcnt lgkmcnt(2)
v_mfma_f32_16x16x32_bf16 v[56:59], a[108:111], v[116:119], v[56:59]
s_waitcnt lgkmcnt(1)
v_mfma_f32_16x16x32_bf16 v[56:59], a[120:123], v[120:123], v[56:59]
s_waitcnt lgkmcnt(0)
ds_read_b128 v[80:83], v17 offset:12288
v_mfma_f32_16x16x32_bf16 v[56:59], a[132:135], v[124:127], v[56:59]
v_mfma_f32_16x16x32_bf16 v[60:63], a[76:79], v[104:107], v[60:63]
ds_read_b128 v[84:87], v17 offset:13312
v_mfma_f32_16x16x32_bf16 v[60:63], a[88:91], v[108:111], v[60:63]
v_mfma_f32_16x16x32_bf16 v[60:63], a[100:103], v[112:115], v[60:63]
ds_read_b128 v[88:91], v17 offset:14336
v_mfma_f32_16x16x32_bf16 v[60:63], a[112:115], v[116:119], v[60:63]
v_mfma_f32_16x16x32_bf16 v[60:63], a[124:127], v[120:123], v[60:63]
ds_read_b128 v[92:95], v17 offset:15360
v_mfma_f32_16x16x32_bf16 v[60:63], a[136:139], v[124:127], v[60:63]
v_mfma_f32_16x16x32_bf16 v[64:67], a[80:83], v[104:107], v[64:67]
ds_read_b128 v[96:99], v17 offset:16384
v_mfma_f32_16x16x32_bf16 v[64:67], a[92:95], v[108:111], v[64:67]
v_mfma_f32_16x16x32_bf16 v[64:67], a[104:107], v[112:115], v[64:67]
ds_read_b128 v[100:103], v17 offset:17408
v_mfma_f32_16x16x32_bf16 v[64:67], a[116:119], v[116:119], v[64:67]
v_mfma_f32_16x16x32_bf16 v[64:67], a[128:131], v[120:123], v[64:67]
v_mfma_f32_16x16x32_bf16 v[64:67], a[140:143], v[124:127], v[64:67]
s_waitcnt lgkmcnt(5)
v_mfma_f32_16x16x32_bf16 v[56:59], a[144:147], v[80:83], v[56:59]
s_waitcnt lgkmcnt(4)
v_mfma_f32_16x16x32_bf16 v[56:59], a[156:159], v[84:87], v[56:59]
s_waitcnt lgkmcnt(3)
v_mfma_f32_16x16x32_bf16 v[56:59], a[168:171], v[88:91], v[56:59]
s_waitcnt lgkmcnt(2)
v_mfma_f32_16x16x32_bf16 v[56:59], a[180:183], v[92:95], v[56:59]
s_waitcnt lgkmcnt(1)
v_mfma_f32_16x16x32_bf16 v[56:59], a[192:195], v[96:99], v[56:59]
s_xor_b32 s45, s45, 0x10000
ds_read_b128 v[104:107], v17 offset:18432
s_waitcnt lgkmcnt(1)
v_mfma_f32_16x16x32_bf16 v[56:59], a[204:207], v[100:103], v[56:59]
s_add_u32 m0, s45, 0
v_mfma_f32_16x16x32_bf16 v[60:63], a[148:151], v[80:83], v[60:63]
ds_read_b128 v[108:111], v17 offset:19456
ds_read_b128 v[112:115], v17 offset:20480
v_mfma_f32_16x16x32_bf16 v[60:63], a[160:163], v[84:87], v[60:63]
v_mfma_f32_16x16x32_bf16 v[60:63], a[172:175], v[88:91], v[60:63]
s_barrier
v_mfma_f32_16x16x32_bf16 v[60:63], a[184:187], v[92:95], v[60:63]
v_mfma_f32_16x16x32_bf16 v[60:63], a[196:199], v[96:99], v[60:63]
ds_read_b128 v[116:119], v17 offset:21504
v_mfma_f32_16x16x32_bf16 v[60:63], a[208:211], v[100:103], v[60:63]
global_load_lds_dwordx4 v9, s[38:39] offset:-2048 sc1
v_mfma_f32_16x16x32_bf16 v[64:67], a[152:155], v[80:83], v[64:67]
ds_read_b128 v[120:123], v17 offset:22528
v_mfma_f32_16x16x32_bf16 v[64:67], a[164:167], v[84:87], v[64:67]
v_mfma_f32_16x16x32_bf16 v[64:67], a[176:179], v[88:91], v[64:67]
ds_read_b128 v[124:127], v17 offset:23552
global_load_lds_dwordx4 v9, s[38:39] offset:-1024 sc1
v_mfma_f32_16x16x32_bf16 v[64:67], a[188:191], v[92:95], v[64:67]
v_mfma_f32_16x16x32_bf16 v[64:67], a[200:203], v[96:99], v[64:67]
v_mfma_f32_16x16x32_bf16 v[64:67], a[212:215], v[100:103], v[64:67]
s_waitcnt lgkmcnt(5)
global_load_lds_dwordx4 v9, s[38:39] offset:0 sc1
v_mfma_f32_16x16x32_bf16 v[56:59], a[216:219], v[104:107], v[56:59]
s_waitcnt lgkmcnt(4)
v_mfma_f32_16x16x32_bf16 v[56:59], a[228:231], v[108:111], v[56:59]
s_waitcnt lgkmcnt(3)
v_mfma_f32_16x16x32_bf16 v[56:59], a[240:243], v[112:115], v[56:59]
s_waitcnt lgkmcnt(2)
global_load_lds_dwordx4 v9, s[38:39] offset:1024 sc1
v_mfma_f32_16x16x32_bf16 v[56:59], a[252:255], v[116:119], v[56:59]
s_waitcnt lgkmcnt(1)
v_mfma_f32_16x16x32_bf16 v[56:59], v[32:35], v[120:123], v[56:59]
s_waitcnt lgkmcnt(0)
v_mfma_f32_16x16x32_bf16 v[56:59], v[44:47], v[124:127], v[56:59]
global_load_lds_dwordx4 v9, s[38:39] offset:2048 sc1
v_mfma_f32_16x16x32_bf16 v[60:63], a[220:223], v[104:107], v[60:63]
v_mfma_f32_16x16x32_bf16 v[60:63], a[232:235], v[108:111], v[60:63]
v_mfma_f32_16x16x32_bf16 v[60:63], a[244:247], v[112:115], v[60:63]
s_waitcnt vmcnt(8)
s_barrier
v_mfma_f32_16x16x32_bf16 v[60:63], v[24:27], v[116:119], v[60:63]
ds_read_b128 v[80:83], v17 offset:24576
global_load_lds_dwordx4 v9, s[38:39] offset:3072 sc1
v_mfma_f32_16x16x32_bf16 v[60:63], v[36:39], v[120:123], v[60:63]
ds_read_b128 v[84:87], v17 offset:25600
v_add_u32 v9, 147456, v9
v_mfma_f32_16x16x32_bf16 v[60:63], v[48:51], v[124:127], v[60:63]
ds_read_b128 v[88:91], v17 offset:26624
v_min_u32 v9, v9, v10
v_mfma_f32_16x16x32_bf16 v[64:67], a[224:227], v[104:107], v[64:67]
ds_read_b128 v[92:95], v17 offset:27648
v_cvt_pk_bf16_f32 v128, v56, v57
v_mfma_f32_16x16x32_bf16 v[64:67], a[236:239], v[108:111], v[64:67]
ds_read_b128 v[96:99], v17 offset:28672
v_cvt_pk_bf16_f32 v129, v58, v59
v_mfma_f32_16x16x32_bf16 v[64:67], a[248:251], v[112:115], v[64:67]
)"
R"(ds_read_b128 v[100:103], v17 offset:29696
v_mfma_f32_16x16x32_bf16 v[64:67], v[28:31], v[116:119], v[64:67]
v_cvt_pk_bf16_f32 v130, v60, v61
v_cvt_pk_bf16_f32 v131, v62, v63
v_mfma_f32_16x16x32_bf16 v[64:67], v[40:43], v[120:123], v[64:67]
v_mfma_f32_16x16x32_bf16 v[64:67], v[52:55], v[124:127], v[64:67]
s_waitcnt lgkmcnt(5)
v_mfma_f32_16x16x32_bf16 v[68:71], a[0:3], v[80:83], 0
s_waitcnt lgkmcnt(4)
v_mfma_f32_16x16x32_bf16 v[68:71], a[12:15], v[84:87], v[68:71]
s_waitcnt lgkmcnt(3)
v_mfma_f32_16x16x32_bf16 v[68:71], a[24:27], v[88:91], v[68:71]
s_waitcnt lgkmcnt(2)
v_mfma_f32_16x16x32_bf16 v[68:71], a[36:39], v[92:95], v[68:71]
s_waitcnt lgkmcnt(1)
ds_read_b128 v[104:107], v17 offset:30720
v_cvt_pk_bf16_f32 v132, v64, v65
v_mfma_f32_16x16x32_bf16 v[68:71], a[48:51], v[96:99], v[68:71]
s_waitcnt lgkmcnt(1)
v_mfma_f32_16x16x32_bf16 v[68:71], a[60:63], v[100:103], v[68:71]
s_waitcnt vmcnt(6)
s_barrier
v_mfma_f32_16x16x32_bf16 v[72:75], a[4:7], v[80:83], 0
ds_read_b128 v[108:111], v17 offset:31744
v_cvt_pk_bf16_f32 v134, v64, v65
v_mfma_f32_16x16x32_bf16 v[72:75], a[16:19], v[84:87], v[72:75]
v_cvt_pk_bf16_f32 v133, v66, v67
v_cvt_pk_bf16_f32 v135, v66, v67
v_mfma_f32_16x16x32_bf16 v[72:75], a[28:31], v[88:91], v[72:75]
ds_read_b128 v[112:115], v17 offset:32768
v_mfma_f32_16x16x32_bf16 v[72:75], a[40:43], v[92:95], v[72:75]
global_store_dwordx4 v11, v[128:131], s[38:39] sc1
v_mfma_f32_16x16x32_bf16 v[72:75], a[52:55], v[96:99], v[72:75]
ds_read_b128 v[116:119], v17 offset:33792
v_add_u32 v11, 110592, v11
v_mfma_f32_16x16x32_bf16 v[72:75], a[64:67], v[100:103], v[72:75]
v_permlane16_swap_b32 v132, v134
v_mfma_f32_16x16x32_bf16 v[76:79], a[8:11], v[80:83], 0
ds_read_b128 v[120:123], v17 offset:34816
v_mfma_f32_16x16x32_bf16 v[76:79], a[20:23], v[84:87], v[76:79]
v_permlane16_swap_b32 v133, v135
v_mfma_f32_16x16x32_bf16 v[76:79], a[32:35], v[88:91], v[76:79]
ds_read_b128 v[124:127], v17 offset:35840
v_mfma_f32_16x16x32_bf16 v[76:79], a[44:47], v[92:95], v[76:79]
s_mov_b64 exec, s[76:77]
global_store_dwordx4 v12, v[132:135], s[38:39] sc1
s_mov_b64 exec, -1
v_mfma_f32_16x16x32_bf16 v[76:79], a[56:59], v[96:99], v[76:79]
v_add_u32 v12, 110592, v12
v_mfma_f32_16x16x32_bf16 v[76:79], a[68:71], v[100:103], v[76:79]
s_waitcnt lgkmcnt(5)
s_mov_b64 exec, 1
global_store_dword v252, v16, s[38:39] sc1
s_mov_b64 exec, -1
v_mfma_f32_16x16x32_bf16 v[68:71], a[72:75], v[104:107], v[68:71]
s_waitcnt lgkmcnt(4)
v_add_u32 v16, 1, v16
v_add_lshl_u32 v252, v16, 0, 7
v_mfma_f32_16x16x32_bf16 v[68:71], a[84:87], v[108:111], v[68:71]
s_waitcnt lgkmcnt(3)
v_bfi_b32 v252, s69, v252, v15
v_mfma_f32_16x16x32_bf16 v[68:71], a[96:99], v[112:115], v[68:71]
s_waitcnt lgkmcnt(2)
v_mfma_f32_16x16x32_bf16 v[68:71], a[108:111], v[116:119], v[68:71]
s_waitcnt lgkmcnt(1)
v_mfma_f32_16x16x32_bf16 v[68:71], a[120:123], v[120:123], v[68:71]
s_waitcnt lgkmcnt(0)
ds_read_b128 v[80:83], v17 offset:36864
v_mfma_f32_16x16x32_bf16 v[68:71], a[132:135], v[124:127], v[68:71]
v_mfma_f32_16x16x32_bf16 v[72:75], a[76:79], v[104:107], v[72:75]
ds_read_b128 v[84:87], v17 offset:37888
v_mfma_f32_16x16x32_bf16 v[72:75], a[88:91], v[108:111], v[72:75]
v_mfma_f32_16x16x32_bf16 v[72:75], a[100:103], v[112:115], v[72:75]
ds_read_b128 v[88:91], v17 offset:38912
v_mfma_f32_16x16x32_bf16 v[72:75], a[112:115], v[116:119], v[72:75]
v_mfma_f32_16x16x32_bf16 v[72:75], a[124:127], v[120:123], v[72:75]
ds_read_b128 v[92:95], v17 offset:39936
v_mfma_f32_16x16x32_bf16 v[72:75], a[136:139], v[124:127], v[72:75]
v_mfma_f32_16x16x32_bf16 v[76:79], a[80:83], v[104:107], v[76:79]
ds_read_b128 v[96:99], v17 offset:40960
v_mfma_f32_16x16x32_bf16 v[76:79], a[92:95], v[108:111], v[76:79]
v_mfma_f32_16x16x32_bf16 v[76:79], a[104:107], v[112:115], v[76:79]
ds_read_b128 v[100:103], v17 offset:41984
v_mfma_f32_16x16x32_bf16 v[76:79], a[116:119], v[116:119], v[76:79]
v_mfma_f32_16x16x32_bf16 v[76:79], a[128:131], v[120:123], v[76:79]
v_mfma_f32_16x16x32_bf16 v[76:79], a[140:143], v[124:127], v[76:79]
s_waitcnt lgkmcnt(5)
v_mfma_f32_16x16x32_bf16 v[68:71], a[144:147], v[80:83], v[68:71]
s_waitcnt lgkmcnt(4)
v_mfma_f32_16x16x32_bf16 v[68:71], a[156:159], v[84:87], v[68:71]
s_waitcnt lgkmcnt(3)
v_mfma_f32_16x16x32_bf16 v[68:71], a[168:171], v[88:91], v[68:71]
.Ldn_in11:
s_waitcnt lgkmcnt(2)
v_mfma_f32_16x16x32_bf16 v[68:71], a[180:183], v[92:95], v[68:71]
s_waitcnt lgkmcnt(1)
v_mfma_f32_16x16x32_bf16 v[68:71], a[192:195], v[96:99], v[68:71]
s_waitcnt lgkmcnt(0)
ds_read_b128 v[104:107], v17 offset:43008
v_mfma_f32_16x16x32_bf16 v[68:71], a[204:207], v[100:103], v[68:71]
s_add_u32 m0, s45, 24576
v_mfma_f32_16x16x32_bf16 v[72:75], a[148:151], v[80:83], v[72:75]
ds_read_b128 v[108:111], v17 offset:44032
ds_read_b128 v[112:115], v17 offset:45056
v_mfma_f32_16x16x32_bf16 v[72:75], a[160:163], v[84:87], v[72:75]
v_mfma_f32_16x16x32_bf16 v[72:75], a[172:175], v[88:91], v[72:75]
s_barrier
v_mfma_f32_16x16x32_bf16 v[72:75], a[184:187], v[92:95], v[72:75]
v_mfma_f32_16x16x32_bf16 v[72:75], a[196:199], v[96:99], v[72:75]
ds_read_b128 v[116:119], v17 offset:46080
v_mfma_f32_16x16x32_bf16 v[72:75], a[208:211], v[100:103], v[72:75]
global_load_lds_dwordx4 v9, s[38:39] offset:-2048 sc1
v_mfma_f32_16x16x32_bf16 v[76:79], a[152:155], v[80:83], v[76:79]
ds_read_b128 v[120:123], v17 offset:47104
v_mfma_f32_16x16x32_bf16 v[76:79], a[164:167], v[84:87], v[76:79]
v_mfma_f32_16x16x32_bf16 v[76:79], a[176:179], v[88:91], v[76:79]
ds_read_b128 v[124:127], v17 offset:48128
global_load_lds_dwordx4 v9, s[38:39] offset:-1024 sc1
v_mfma_f32_16x16x32_bf16 v[76:79], a[188:191], v[92:95], v[76:79]
v_mfma_f32_16x16x32_bf16 v[76:79], a[200:203], v[96:99], v[76:79]
v_mfma_f32_16x16x32_bf16 v[76:79], a[212:215], v[100:103], v[76:79]
s_waitcnt lgkmcnt(5)
global_load_lds_dwordx4 v9, s[38:39] offset:0 sc1
v_mfma_f32_16x16x32_bf16 v[68:71], a[216:219], v[104:107], v[68:71]
s_waitcnt lgkmcnt(4)
v_mfma_f32_16x16x32_bf16 v[68:71], a[228:231], v[108:111], v[68:71]
s_waitcnt lgkmcnt(3)
v_mfma_f32_16x16x32_bf16 v[68:71], a[240:243], v[112:115], v[68:71]
s_waitcnt lgkmcnt(2)
global_load_lds_dwordx4 v9, s[38:39] offset:1024 sc1
v_mfma_f32_16x16x32_bf16 v[68:71], a[252:255], v[116:119], v[68:71]
s_waitcnt lgkmcnt(1)
v_mfma_f32_16x16x32_bf16 v[68:71], v[32:35], v[120:123], v[68:71]
s_waitcnt lgkmcnt(0)
v_mfma_f32_16x16x32_bf16 v[68:71], v[44:47], v[124:127], v[68:71]
v_xor_b32 v17, 0x10000, v17
global_load_lds_dwordx4 v9, s[38:39] offset:2048 sc1
v_mfma_f32_16x16x32_bf16 v[72:75], a[220:223], v[104:107], v[72:75]
v_mfma_f32_16x16x32_bf16 v[72:75], a[232:235], v[108:111], v[72:75]
v_mfma_f32_16x16x32_bf16 v[72:75], a[244:247], v[112:115], v[72:75]
s_waitcnt vmcnt(8)
s_barrier
v_mfma_f32_16x16x32_bf16 v[72:75], v[24:27], v[116:119], v[72:75]
ds_read_b128 v[80:83], v17 offset:0
global_load_lds_dwordx4 v9, s[38:39] offset:3072 sc1
v_mfma_f32_16x16x32_bf16 v[72:75], v[36:39], v[120:123], v[72:75]
ds_read_b128 v[84:87], v17 offset:1024
v_add_u32 v9, 147456, v9
v_mfma_f32_16x16x32_bf16 v[72:75], v[48:51], v[124:127], v[72:75]
ds_read_b128 v[88:91], v17 offset:2048
v_min_u32 v9, v9, v10
v_mfma_f32_16x16x32_bf16 v[76:79], a[224:227], v[104:107], v[76:79]
ds_read_b128 v[92:95], v17 offset:3072
v_cvt_pk_bf16_f32 v128, v68, v69
v_mfma_f32_16x16x32_bf16 v[76:79], a[236:239], v[108:111], v[76:79]
ds_read_b128 v[96:99], v17 offset:4096
v_cvt_pk_bf16_f32 v129, v70, v71
v_mfma_f32_16x16x32_bf16 v[76:79], a[248:251], v[112:115], v[76:79]
ds_read_b128 v[100:103], v17 offset:5120
v_mfma_f32_16x16x32_bf16 v[76:79], v[28:31], v[116:119], v[76:79]
v_cvt_pk_bf16_f32 v130, v72, v73
v_cvt_pk_bf16_f32 v131, v74, v75
v_mfma_f32_16x16x32_bf16 v[76:79], v[40:43], v[120:123], v[76:79]
v_mfma_f32_16x16x32_bf16 v[76:79], v[52:55], v[124:127], v[76:79]
s_sub_u32 s70, s70, 1
s_cbranch_scc0 .Ldn_top12
v_cvt_pk_bf16_f32 v128, v68, v69
v_cvt_pk_bf16_f32 v129, v70, v71
v_cvt_pk_bf16_f32 v130, v72, v73
v_cvt_pk_bf16_f32 v131, v74, v75
global_store_dwordx4 v11, v[128:131], s[38:39] sc1
v_add_u32 v11, 110592, v11
v_cvt_pk_bf16_f32 v132, v76, v77
v_cvt_pk_bf16_f32 v134, v76, v77
v_cvt_pk_bf16_f32 v133, v78, v79
v_cvt_pk_bf16_f32 v135, v78, v79
v_permlane16_swap_b32 v132, v134
s_nop 0
v_permlane16_swap_b32 v133, v135
s_mov_b64 exec, s[76:77]
global_store_dwordx4 v12, v[132:135], s[38:39] sc1
s_mov_b64 exec, -1
v_add_u32 v12, 110592, v12
s_waitcnt vmcnt(2)
s_barrier
s_mov_b64 exec, 1
global_store_dword v252, v16, s[38:39] sc1
s_mov_b64 exec, -1
v_add_u32 v16, 1, v16
v_add_lshl_u32 v252, v16, 0, 7
v_bfi_b32 v252, s69, v252, v15
s_waitcnt vmcnt(1)
s_barrier
s_mov_b64 exec, 1
global_store_dword v252, v16, s[38:39] sc1
s_mov_b64 exec, -1
s_endpgm
.Ldn_w4:
v_add_u32 v254, -1, v14
v_add_u32 v255, -1, v14
v_add_lshl_u32 v252, v16, 0, 7
v_bfi_b32 v252, s69, v252, v15
v_add_lshl_u32 v253, v14, 0, 7
v_bfi_b32 v253, s69, v253, v13
s_mov_b32 s76, 0xffff
s_mov_b32 s77, 0xffff
s_add_u32 m0, s45, 24576
v_add_lshl_u32 v253, v14, 0, 7
v_bfi_b32 v253, s69, v253, v13
global_load_dword v254, v253, s[38:39] sc1
s_waitcnt vmcnt(0)
v_sub_u32 v254, v254, v14
v_cmp_lt_u32 vcc, 0x7f, v254
s_cbranch_vccnz .Ldn_sp19
.Ldn_rt20:
v_add_u32 v14, 1, v14
s_xor_b32 s45, s45, 0x10000
s_mov_b32 m0, s45
s_barrier
global_load_lds_dwordx4 v9, s[38:39] offset:-2048 sc1
global_load_lds_dwordx4 v9, s[38:39] offset:-1024 sc1
global_load_lds_dwordx4 v9, s[38:39] offset:0 sc1
global_load_lds_dwordx4 v9, s[38:39] offset:1024 sc1
global_load_lds_dwordx4 v9, s[38:39] offset:2048 sc1
global_load_lds_dwordx4 v9, s[38:39] offset:3072 sc1
v_add_u32 v9, 147456, v9
s_mov_b32 s70, 32
s_waitcnt vmcnt(0) lgkmcnt(0)
s_nop 7
s_branch .Ldn_in18
.p2align 6
.Ldn_top21:
s_waitcnt lgkmcnt(5)
v_mfma_f32_16x16x32_bf16 v[56:59], a[0:3], v[80:83], 0
s_waitcnt lgkmcnt(4)
v_mfma_f32_16x16x32_bf16 v[56:59], a[12:15], v[84:87], v[56:59]
s_waitcnt lgkmcnt(3)
global_load_dword v254, v253, s[38:39] sc1
v_mfma_f32_16x16x32_bf16 v[56:59], a[24:27], v[88:91], v[56:59]
s_waitcnt lgkmcnt(2)
v_mfma_f32_16x16x32_bf16 v[56:59], a[36:39], v[92:95], v[56:59]
s_waitcnt lgkmcnt(1)
ds_read_b128 v[104:107], v17 offset:6144
v_cvt_pk_bf16_f32 v132, v76, v77
v_mfma_f32_16x16x32_bf16 v[56:59], a[48:51], v[96:99], v[56:59]
s_waitcnt lgkmcnt(1)
v_mfma_f32_16x16x32_bf16 v[56:59], a[60:63], v[100:103], v[56:59]
s_waitcnt vmcnt(7)
s_barrier
v_mfma_f32_16x16x32_bf16 v[60:63], a[4:7], v[80:83], 0
ds_read_b128 v[108:111], v17 offset:7168
v_cvt_pk_bf16_f32 v134, v76, v77
v_mfma_f32_16x16x32_bf16 v[60:63], a[16:19], v[84:87], v[60:63]
v_cvt_pk_bf16_f32 v133, v78, v79
v_cvt_pk_bf16_f32 v135, v78, v79
v_mfma_f32_16x16x32_bf16 v[60:63], a[28:31], v[88:91], v[60:63]
ds_read_b128 v[112:115], v17 offset:8192
v_mfma_f32_16x16x32_bf16 v[60:63], a[40:43], v[92:95], v[60:63]
v_permlane16_swap_b32 v132, v134
v_mfma_f32_16x16x32_bf16 v[60:63], a[52:55], v[96:99], v[60:63]
ds_read_b128 v[116:119], v17 offset:9216
v_mfma_f32_16x16x32_bf16 v[60:63], a[64:67], v[100:103], v[60:63]
global_store_dwordx4 v11, v[128:131], s[38:39] sc1
v_mfma_f32_16x16x32_bf16 v[64:67], a[8:11], v[80:83], 0
ds_read_b128 v[120:123], v17 offset:10240
v_add_u32 v11, 110592, v11
v_mfma_f32_16x16x32_bf16 v[64:67], a[20:23], v[84:87], v[64:67]
v_permlane16_swap_b32 v133, v135
v_mfma_f32_16x16x32_bf16 v[64:67], a[32:35], v[88:91], v[64:67]
ds_read_b128 v[124:127], v17 offset:11264
v_mfma_f32_16x16x32_bf16 v[64:67], a[44:47], v[92:95], v[64:67]
s_mov_b64 exec, s[76:77]
global_store_dwordx4 v12, v[132:135], s[38:39] sc1
s_mov_b64 exec, -1
v_mfma_f32_16x16x32_bf16 v[64:67], a[56:59], v[96:99], v[64:67]
v_add_u32 v12, 110592, v12
v_mfma_f32_16x16x32_bf16 v[64:67], a[68:71], v[100:103], v[64:67]
s_waitcnt lgkmcnt(5)
v_mfma_f32_16x16x32_bf16 v[56:59], a[72:75], v[104:107], v[56:59]
s_waitcnt lgkmcnt(4)
v_mfma_f32_16x16x32_bf16 v[56:59], a[84:87], v[108:111], v[56:59]
s_waitcnt lgkmcnt(3)
v_mfma_f32_16x16x32_bf16 v[56:59], a[96:99], v[112:115], v[56:59]
s_waitcnt lgkmcnt(2)
v_mfma_f32_16x16x32_bf16 v[56:59], a[108:111], v[116:119], v[56:59]
s_waitcnt lgkmcnt(1)
v_mfma_f32_16x16x32_bf16 v[56:59], a[120:123], v[120:123], v[56:59]
s_waitcnt lgkmcnt(0)
ds_read_b128 v[80:83], v17 offset:12288
v_mfma_f32_16x16x32_bf16 v[56:59], a[132:135], v[124:127], v[56:59]
v_mfma_f32_16x16x32_bf16 v[60:63], a[76:79], v[104:107], v[60:63]
ds_read_b128 v[84:87], v17 offset:13312
v_mfma_f32_16x16x32_bf16 v[60:63], a[88:91], v[108:111], v[60:63]
v_mfma_f32_16x16x32_bf16 v[60:63], a[100:103], v[112:115], v[60:63]
ds_read_b128 v[88:91], v17 offset:14336
v_mfma_f32_16x16x32_bf16 v[60:63], a[112:115], v[116:119], v[60:63]
v_mfma_f32_16x16x32_bf16 v[60:63], a[124:127], v[120:123], v[60:63]
ds_read_b128 v[92:95], v17 offset:15360
v_mfma_f32_16x16x32_bf16 v[60:63], a[136:139], v[124:127], v[60:63]
v_mfma_f32_16x16x32_bf16 v[64:67], a[80:83], v[104:107], v[64:67]
ds_read_b128 v[96:99], v17 offset:16384
v_mfma_f32_16x16x32_bf16 v[64:67], a[92:95], v[108:111], v[64:67]
v_mfma_f32_16x16x32_bf16 v[64:67], a[104:107], v[112:115], v[64:67]
ds_read_b128 v[100:103], v17 offset:17408
v_mfma_f32_16x16x32_bf16 v[64:67], a[116:119], v[116:119], v[64:67]
v_mfma_f32_16x16x32_bf16 v[64:67], a[128:131], v[120:123], v[64:67]
v_mfma_f32_16x16x32_bf16 v[64:67], a[140:143], v[124:127], v[64:67]
s_waitcnt lgkmcnt(5)
v_mfma_f32_16x16x32_bf16 v[56:59], a[144:147], v[80:83], v[56:59]
s_waitcnt lgkmcnt(4)
v_mfma_f32_16x16x32_bf16 v[56:59], a[156:159], v[84:87], v[56:59]
s_waitcnt lgkmcnt(3)
v_mfma_f32_16x16x32_bf16 v[56:59], a[168:171], v[88:91], v[56:59]
s_waitcnt lgkmcnt(2)
v_mfma_f32_16x16x32_bf16 v[56:59], a[180:183], v[92:95], v[56:59]
s_waitcnt vmcnt(2)
v_sub_u32 v254, v254, v14
v_cmp_lt_u32 vcc, 0x7f, v254
s_waitcnt lgkmcnt(1)
v_mfma_f32_16x16x32_bf16 v[56:59], a[192:195], v[96:99], v[56:59]
s_xor_b32 s45, s45, 0x10000
ds_read_b128 v[104:107], v17 offset:18432
s_waitcnt lgkmcnt(1)
v_mfma_f32_16x16x32_bf16 v[56:59], a[204:207], v[100:103], v[56:59]
s_add_u32 m0, s45, 0
v_mfma_f32_16x16x32_bf16 v[60:63], a[148:151], v[80:83], v[60:63]
s_cbranch_vccnz .Ldn_sp14
.Ldn_rt15:
ds_read_b128 v[108:111], v17 offset:19456
ds_read_b128 v[112:115], v17 offset:20480
v_mfma_f32_16x16x32_bf16 v[60:63], a[160:163], v[84:87], v[60:63]
v_mfma_f32_16x16x32_bf16 v[60:63], a[172:175], v[88:91], v[60:63]
s_barrier
v_mfma_f32_16x16x32_bf16 v[60:63], a[184:187], v[92:95], v[60:63]
v_add_u32 v14, 1, v14
v_add_lshl_u32 v253, v14, 0, 7
v_mfma_f32_16x16x32_bf16 v[60:63], a[196:199], v[96:99], v[60:63]
ds_read_b128 v[116:119], v17 offset:21504
v_bfi_b32 v253, s69, v253, v13
v_mfma_f32_16x16x32_bf16 v[60:63], a[208:211], v[100:103], v[60:63]
v_mfma_f32_16x16x32_bf16 v[64:67], a[152:155], v[80:83], v[64:67]
ds_read_b128 v[120:123], v17 offset:22528
global_load_lds_dwordx4 v9, s[38:39] offset:-2048 sc1
v_mfma_f32_16x16x32_bf16 v[64:67], a[164:167], v[84:87], v[64:67]
v_mfma_f32_16x16x32_bf16 v[64:67], a[176:179], v[88:91], v[64:67]
ds_read_b128 v[124:127], v17 offset:23552
v_mfma_f32_16x16x32_bf16 v[64:67], a[188:191], v[92:95], v[64:67]
global_load_lds_dwordx4 v9, s[38:39] offset:-1024 sc1
v_mfma_f32_16x16x32_bf16 v[64:67], a[200:203], v[96:99], v[64:67]
v_mfma_f32_16x16x32_bf16 v[64:67], a[212:215], v[100:103], v[64:67]
s_waitcnt lgkmcnt(5)
v_mfma_f32_16x16x32_bf16 v[56:59], a[216:219], v[104:107], v[56:59]
s_waitcnt lgkmcnt(4)
global_load_lds_dwordx4 v9, s[38:39] offset:0 sc1
v_mfma_f32_16x16x32_bf16 v[56:59], a[228:231], v[108:111], v[56:59]
s_waitcnt lgkmcnt(3)
v_mfma_f32_16x16x32_bf16 v[56:59], a[240:243], v[112:115], v[56:59]
s_waitcnt lgkmcnt(2)
v_mfma_f32_16x16x32_bf16 v[56:59], a[252:255], v[116:119], v[56:59]
s_waitcnt lgkmcnt(1)
global_load_lds_dwordx4 v9, s[38:39] offset:1024 sc1
v_mfma_f32_16x16x32_bf16 v[56:59], v[32:35], v[120:123], v[56:59]
s_waitcnt lgkmcnt(0)
v_mfma_f32_16x16x32_bf16 v[56:59], v[44:47], v[124:127], v[56:59]
v_mfma_f32_16x16x32_bf16 v[60:63], a[220:223], v[104:107], v[60:63]
global_load_lds_dwordx4 v9, s[38:39] offset:2048 sc1
v_mfma_f32_16x16x32_bf16 v[60:63], a[232:235], v[108:111], v[60:63]
v_mfma_f32_16x16x32_bf16 v[60:63], a[244:247], v[112:115], v[60:63]
s_barrier
v_mfma_f32_16x16x32_bf16 v[60:63], v[24:27], v[116:119], v[60:63]
ds_read_b128 v[80:83], v17 offset:24576
global_load_lds_dwordx4 v9, s[38:39] offset:3072 sc1
v_mfma_f32_16x16x32_bf16 v[60:63], v[36:39], v[120:123], v[60:63]
ds_read_b128 v[84:87], v17 offset:25600
v_add_u32 v9, 147456, v9
v_mfma_f32_16x16x32_bf16 v[60:63], v[48:51], v[124:127], v[60:63]
ds_read_b128 v[88:91], v17 offset:26624
v_min_u32 v9, v9, v10
v_mfma_f32_16x16x32_bf16 v[64:67], a[224:227], v[104:107], v[64:67]
ds_read_b128 v[92:95], v17 offset:27648
v_cvt_pk_bf16_f32 v128, v56, v57
v_mfma_f32_16x16x32_bf16 v[64:67], a[236:239], v[108:111], v[64:67]
ds_read_b128 v[96:99], v17 offset:28672
v_cvt_pk_bf16_f32 v129, v58, v59
v_mfma_f32_16x16x32_bf16 v[64:67], a[248:251], v[112:115], v[64:67]
ds_read_b128 v[100:103], v17 offset:29696
v_mfma_f32_16x16x32_bf16 v[64:67], v[28:31], v[116:119], v[64:67]
v_cvt_pk_bf16_f32 v130, v60, v61
v_cvt_pk_bf16_f32 v131, v62, v63
v_mfma_f32_16x16x32_bf16 v[64:67], v[40:43], v[120:123], v[64:67]
v_mfma_f32_16x16x32_bf16 v[64:67], v[52:55], v[124:127], v[64:67]
s_waitcnt lgkmcnt(5)
v_mfma_f32_16x16x32_bf16 v[68:71], a[0:3], v[80:83], 0
s_waitcnt lgkmcnt(4)
v_mfma_f32_16x16x32_bf16 v[68:71], a[12:15], v[84:87], v[68:71]
s_waitcnt lgkmcnt(3)
global_load_dword v255, v253, s[38:39] sc1
v_mfma_f32_16x16x32_bf16 v[68:71], a[24:27], v[88:91], v[68:71]
s_waitcnt lgkmcnt(2)
v_mfma_f32_16x16x32_bf16 v[68:71], a[36:39], v[92:95], v[68:71]
)"
R"(s_waitcnt lgkmcnt(1)
ds_read_b128 v[104:107], v17 offset:30720
v_cvt_pk_bf16_f32 v132, v64, v65
v_mfma_f32_16x16x32_bf16 v[68:71], a[48:51], v[96:99], v[68:71]
s_waitcnt lgkmcnt(1)
v_mfma_f32_16x16x32_bf16 v[68:71], a[60:63], v[100:103], v[68:71]
s_waitcnt vmcnt(7)
s_barrier
v_mfma_f32_16x16x32_bf16 v[72:75], a[4:7], v[80:83], 0
ds_read_b128 v[108:111], v17 offset:31744
v_cvt_pk_bf16_f32 v134, v64, v65
v_mfma_f32_16x16x32_bf16 v[72:75], a[16:19], v[84:87], v[72:75]
v_cvt_pk_bf16_f32 v133, v66, v67
v_cvt_pk_bf16_f32 v135, v66, v67
v_mfma_f32_16x16x32_bf16 v[72:75], a[28:31], v[88:91], v[72:75]
ds_read_b128 v[112:115], v17 offset:32768
v_mfma_f32_16x16x32_bf16 v[72:75], a[40:43], v[92:95], v[72:75]
v_permlane16_swap_b32 v132, v134
v_mfma_f32_16x16x32_bf16 v[72:75], a[52:55], v[96:99], v[72:75]
ds_read_b128 v[116:119], v17 offset:33792
v_mfma_f32_16x16x32_bf16 v[72:75], a[64:67], v[100:103], v[72:75]
global_store_dwordx4 v11, v[128:131], s[38:39] sc1
v_mfma_f32_16x16x32_bf16 v[76:79], a[8:11], v[80:83], 0
ds_read_b128 v[120:123], v17 offset:34816
v_add_u32 v11, 110592, v11
v_mfma_f32_16x16x32_bf16 v[76:79], a[20:23], v[84:87], v[76:79]
v_permlane16_swap_b32 v133, v135
v_mfma_f32_16x16x32_bf16 v[76:79], a[32:35], v[88:91], v[76:79]
ds_read_b128 v[124:127], v17 offset:35840
v_mfma_f32_16x16x32_bf16 v[76:79], a[44:47], v[92:95], v[76:79]
s_mov_b64 exec, s[76:77]
global_store_dwordx4 v12, v[132:135], s[38:39] sc1
s_mov_b64 exec, -1
v_mfma_f32_16x16x32_bf16 v[76:79], a[56:59], v[96:99], v[76:79]
v_add_u32 v12, 110592, v12
v_mfma_f32_16x16x32_bf16 v[76:79], a[68:71], v[100:103], v[76:79]
s_waitcnt lgkmcnt(5)
v_mfma_f32_16x16x32_bf16 v[68:71], a[72:75], v[104:107], v[68:71]
s_waitcnt lgkmcnt(4)
v_mfma_f32_16x16x32_bf16 v[68:71], a[84:87], v[108:111], v[68:71]
s_waitcnt lgkmcnt(3)
v_mfma_f32_16x16x32_bf16 v[68:71], a[96:99], v[112:115], v[68:71]
s_waitcnt lgkmcnt(2)
v_mfma_f32_16x16x32_bf16 v[68:71], a[108:111], v[116:119], v[68:71]
s_waitcnt lgkmcnt(1)
v_mfma_f32_16x16x32_bf16 v[68:71], a[120:123], v[120:123], v[68:71]
s_waitcnt lgkmcnt(0)
ds_read_b128 v[80:83], v17 offset:36864
v_mfma_f32_16x16x32_bf16 v[68:71], a[132:135], v[124:127], v[68:71]
v_mfma_f32_16x16x32_bf16 v[72:75], a[76:79], v[104:107], v[72:75]
ds_read_b128 v[84:87], v17 offset:37888
v_mfma_f32_16x16x32_bf16 v[72:75], a[88:91], v[108:111], v[72:75]
v_mfma_f32_16x16x32_bf16 v[72:75], a[100:103], v[112:115], v[72:75]
ds_read_b128 v[88:91], v17 offset:38912
v_mfma_f32_16x16x32_bf16 v[72:75], a[112:115], v[116:119], v[72:75]
v_mfma_f32_16x16x32_bf16 v[72:75], a[124:127], v[120:123], v[72:75]
ds_read_b128 v[92:95], v17 offset:39936
v_mfma_f32_16x16x32_bf16 v[72:75], a[136:139], v[124:127], v[72:75]
v_mfma_f32_16x16x32_bf16 v[76:79], a[80:83], v[104:107], v[76:79]
ds_read_b128 v[96:99], v17 offset:40960
v_mfma_f32_16x16x32_bf16 v[76:79], a[92:95], v[108:111], v[76:79]
v_mfma_f32_16x16x32_bf16 v[76:79], a[104:107], v[112:115], v[76:79]
ds_read_b128 v[100:103], v17 offset:41984
v_mfma_f32_16x16x32_bf16 v[76:79], a[116:119], v[116:119], v[76:79]
v_mfma_f32_16x16x32_bf16 v[76:79], a[128:131], v[120:123], v[76:79]
v_mfma_f32_16x16x32_bf16 v[76:79], a[140:143], v[124:127], v[76:79]
s_waitcnt lgkmcnt(5)
v_mfma_f32_16x16x32_bf16 v[68:71], a[144:147], v[80:83], v[68:71]
s_waitcnt lgkmcnt(4)
v_mfma_f32_16x16x32_bf16 v[68:71], a[156:159], v[84:87], v[68:71]
s_waitcnt lgkmcnt(3)
v_mfma_f32_16x16x32_bf16 v[68:71], a[168:171], v[88:91], v[68:71]
.Ldn_in18:
s_waitcnt lgkmcnt(2)
v_mfma_f32_16x16x32_bf16 v[68:71], a[180:183], v[92:95], v[68:71]
s_waitcnt vmcnt(2)
v_sub_u32 v255, v255, v14
v_cmp_lt_u32 vcc, 0x7f, v255
s_waitcnt lgkmcnt(1)
v_mfma_f32_16x16x32_bf16 v[68:71], a[192:195], v[96:99], v[68:71]
s_waitcnt lgkmcnt(0)
ds_read_b128 v[104:107], v17 offset:43008
v_mfma_f32_16x16x32_bf16 v[68:71], a[204:207], v[100:103], v[68:71]
s_add_u32 m0, s45, 24576
v_mfma_f32_16x16x32_bf16 v[72:75], a[148:151], v[80:83], v[72:75]
s_cbranch_vccnz .Ldn_sp16
.Ldn_rt17:
ds_read_b128 v[108:111], v17 offset:44032
ds_read_b128 v[112:115], v17 offset:45056
v_mfma_f32_16x16x32_bf16 v[72:75], a[160:163], v[84:87], v[72:75]
v_mfma_f32_16x16x32_bf16 v[72:75], a[172:175], v[88:91], v[72:75]
s_barrier
v_mfma_f32_16x16x32_bf16 v[72:75], a[184:187], v[92:95], v[72:75]
v_add_u32 v14, 1, v14
v_add_lshl_u32 v253, v14, 0, 7
v_mfma_f32_16x16x32_bf16 v[72:75], a[196:199], v[96:99], v[72:75]
ds_read_b128 v[116:119], v17 offset:46080
v_bfi_b32 v253, s69, v253, v13
v_mfma_f32_16x16x32_bf16 v[72:75], a[208:211], v[100:103], v[72:75]
v_mfma_f32_16x16x32_bf16 v[76:79], a[152:155], v[80:83], v[76:79]
ds_read_b128 v[120:123], v17 offset:47104
global_load_lds_dwordx4 v9, s[38:39] offset:-2048 sc1
v_mfma_f32_16x16x32_bf16 v[76:79], a[164:167], v[84:87], v[76:79]
v_mfma_f32_16x16x32_bf16 v[76:79], a[176:179], v[88:91], v[76:79]
ds_read_b128 v[124:127], v17 offset:48128
v_mfma_f32_16x16x32_bf16 v[76:79], a[188:191], v[92:95], v[76:79]
global_load_lds_dwordx4 v9, s[38:39] offset:-1024 sc1
v_mfma_f32_16x16x32_bf16 v[76:79], a[200:203], v[96:99], v[76:79]
v_mfma_f32_16x16x32_bf16 v[76:79], a[212:215], v[100:103], v[76:79]
s_waitcnt lgkmcnt(5)
v_mfma_f32_16x16x32_bf16 v[68:71], a[216:219], v[104:107], v[68:71]
s_waitcnt lgkmcnt(4)
global_load_lds_dwordx4 v9, s[38:39] offset:0 sc1
v_mfma_f32_16x16x32_bf16 v[68:71], a[228:231], v[108:111], v[68:71]
s_waitcnt lgkmcnt(3)
v_mfma_f32_16x16x32_bf16 v[68:71], a[240:243], v[112:115], v[68:71]
s_waitcnt lgkmcnt(2)
v_mfma_f32_16x16x32_bf16 v[68:71], a[252:255], v[116:119], v[68:71]
s_waitcnt lgkmcnt(1)
global_load_lds_dwordx4 v9, s[38:39] offset:1024 sc1
v_mfma_f32_16x16x32_bf16 v[68:71], v[32:35], v[120:123], v[68:71]
s_waitcnt lgkmcnt(0)
v_mfma_f32_16x16x32_bf16 v[68:71], v[44:47], v[124:127], v[68:71]
v_xor_b32 v17, 0x10000, v17
v_mfma_f32_16x16x32_bf16 v[72:75], a[220:223], v[104:107], v[72:75]
global_load_lds_dwordx4 v9, s[38:39] offset:2048 sc1
v_mfma_f32_16x16x32_bf16 v[72:75], a[232:235], v[108:111], v[72:75]
v_mfma_f32_16x16x32_bf16 v[72:75], a[244:247], v[112:115], v[72:75]
s_barrier
v_mfma_f32_16x16x32_bf16 v[72:75], v[24:27], v[116:119], v[72:75]
ds_read_b128 v[80:83], v17 offset:0
global_load_lds_dwordx4 v9, s[38:39] offset:3072 sc1
v_mfma_f32_16x16x32_bf16 v[72:75], v[36:39], v[120:123], v[72:75]
ds_read_b128 v[84:87], v17 offset:1024
v_add_u32 v9, 147456, v9
v_mfma_f32_16x16x32_bf16 v[72:75], v[48:51], v[124:127], v[72:75]
ds_read_b128 v[88:91], v17 offset:2048
v_min_u32 v9, v9, v10
v_mfma_f32_16x16x32_bf16 v[76:79], a[224:227], v[104:107], v[76:79]
ds_read_b128 v[92:95], v17 offset:3072
v_cvt_pk_bf16_f32 v128, v68, v69
v_mfma_f32_16x16x32_bf16 v[76:79], a[236:239], v[108:111], v[76:79]
ds_read_b128 v[96:99], v17 offset:4096
v_cvt_pk_bf16_f32 v129, v70, v71
v_mfma_f32_16x16x32_bf16 v[76:79], a[248:251], v[112:115], v[76:79]
ds_read_b128 v[100:103], v17 offset:5120
v_mfma_f32_16x16x32_bf16 v[76:79], v[28:31], v[116:119], v[76:79]
v_cvt_pk_bf16_f32 v130, v72, v73
v_cvt_pk_bf16_f32 v131, v74, v75
v_mfma_f32_16x16x32_bf16 v[76:79], v[40:43], v[120:123], v[76:79]
v_mfma_f32_16x16x32_bf16 v[76:79], v[52:55], v[124:127], v[76:79]
s_sub_u32 s70, s70, 1
s_cbranch_scc0 .Ldn_top21
v_cvt_pk_bf16_f32 v128, v68, v69
v_cvt_pk_bf16_f32 v129, v70, v71
v_cvt_pk_bf16_f32 v130, v72, v73
v_cvt_pk_bf16_f32 v131, v74, v75
global_store_dwordx4 v11, v[128:131], s[38:39] sc1
v_add_u32 v11, 110592, v11
v_cvt_pk_bf16_f32 v132, v76, v77
v_cvt_pk_bf16_f32 v134, v76, v77
v_cvt_pk_bf16_f32 v133, v78, v79
v_cvt_pk_bf16_f32 v135, v78, v79
v_permlane16_swap_b32 v132, v134
s_nop 0
v_permlane16_swap_b32 v133, v135
s_mov_b64 exec, s[76:77]
global_store_dwordx4 v12, v[132:135], s[38:39] sc1
s_mov_b64 exec, -1
v_add_u32 v12, 110592, v12
s_waitcnt vmcnt(2)
s_barrier
s_waitcnt vmcnt(0)
s_barrier
s_endpgm
.Ldn_sp14:
s_sleep 1
v_add_lshl_u32 v253, v14, 0, 7
v_bfi_b32 v253, s69, v253, v13
global_load_dword v254, v253, s[38:39] sc1
s_add_u32 s71, s71, 1
s_waitcnt vmcnt(0)
v_sub_u32 v254, v254, v14
v_cmp_lt_u32 vcc, 0x7f, v254
s_cbranch_vccz .Ldn_rt15
s_cmp_lt_u32 s71, 3000000
s_cbranch_scc1 .Ldn_sp14
s_branch .Ldn_rt15
.Ldn_sp16:
s_sleep 1
v_add_lshl_u32 v253, v14, 0, 7
v_bfi_b32 v253, s69, v253, v13
global_load_dword v255, v253, s[38:39] sc1
s_add_u32 s71, s71, 1
s_waitcnt vmcnt(0)
v_sub_u32 v255, v255, v14
v_cmp_lt_u32 vcc, 0x7f, v255
s_cbranch_vccz .Ldn_rt17
s_cmp_lt_u32 s71, 3000000
s_cbranch_scc1 .Ldn_sp16
s_branch .Ldn_rt17
.Ldn_sp19:
s_sleep 1
v_add_lshl_u32 v253, v14, 0, 7
v_bfi_b32 v253, s69, v253, v13
global_load_dword v254, v253, s[38:39] sc1
s_add_u32 s71, s71, 1
s_waitcnt vmcnt(0)
v_sub_u32 v254, v254, v14
v_cmp_lt_u32 vcc, 0x7f, v254
s_cbranch_vccz .Ldn_rt20
s_cmp_lt_u32 s71, 3000000
s_cbranch_scc1 .Ldn_sp19
s_branch .Ldn_rt20
.Le2:
)"
    :: "{v[0:31]}"(va), "{s[36:51]}"(sa) : "v255", "a255", "s99", "vcc", "memory");
  } else {
    const unsigned gu_i = xh * 36 + (ev ? jj + 4 : jj - 12);
    const unsigned r = lane & 15, g = lane >> 4, q = r >> 2, p = lane & 3;
    const unsigned ii = gu_i, gam = (ii % 72) >> 1, rho = ii / 72 * 2 + (ii & 1), row0 = rho * RP + PAD, pc0 = wave * 6;
    const unsigned bk = gam >> 2, qd = gam & 3, od = (lane >> 3) & 1, un = g * 8 + (lane & 7), jr = qd * 4 + p, jc = (un >> 2) * 8 + od * 4;
    u32x32 va; u32x16 sa, sb;
    va[0] = g * 9216 + ((r ^ (g << 2)) << 4) + 4096 + (((r ^ (g << 2)) >> 3) & 1) * 640;
    va[1] = (8 * g + q) * 256 + (((4 * wave + p) ^ (q << 2)) << 4);
    va[2] = WS_X1 + row0 * X1P + pc0 * 1024 + 2048 + lane * 16;
    va[3] = WS_H + (row0 - 1) * HT + (gam / 6 * 24 + (gam % 6) * 2 + (wave >> 1) * 12 + (wave & 1)) * 1024 + lane * 16;
    va[4] = WS_FL + rho * FR + FX1 + (lane & 1) * FL;
    va[5] = 0u;
    va[6] = WS_FL + rho * FR + FH + gam * FL;
    va[7] = 0u - 2;
    va[8] = lane * 16;
    va[9] = WS_X1 + (row0 + 63) * X1P + pc0 * 1024 + 2048 + lane * 16;
    va[10] = WS_P + row0 * PT + gam * 3072 + od * 512 + un * 16;
    va[11] = WS_X1 + row0 * X1P + bk * 2048 + (un >> 2) * 256 + jr * 16 + od * 8;
    va[12] = WS_X1 + row0 * X1P + XT + jr * 8;
    va[13] = ((rho * 32 + 31) * 16 + qd * 4 + g) * 1152 + bk * 128 + r * 8;
    va[14] = 49152 + wave * 1024 + p * 144 + jc * 2;
    va[15] = 49152 + wave * 1024 + g * 144 + r * 8;
    va[16] = WS_FL + rho * FR + FP + (bk / 3 * 6 + min(lane, 5u)) * FL;
    va[17] = 0u;
    va[18] = WS_FL + rho * FR + FX2 + gam * FL;
    va[19] = 0u;
    va[20] = 0u;
    va[21] = 0u;
    va[22] = (g == q && p < 2) ? (0x3F80u << (16 * p)) : 0u;
    va[23] = (g == q && p >= 2) ? (0x3F80u << (16 * (p - 2))) : 0u;
    va[24] = 0u;
    va[25] = 0u;
    sa[0] = 12 + wave; sa[2] = (unsigned)(unsigned long long)ws; sa[3] = (unsigned)((unsigned long long)ws >> 32);
    sa[4] = uni((unsigned)(unsigned long long)(wg + gam / 6 * 1536 + (gam % 6) * 128 + wave * (16 * 9216)));
    sa[5] = uni((unsigned)((unsigned long long)(wg + gam / 6 * 1536 + (gam % 6) * 128 + wave * (16 * 9216)) >> 32));
    sa[6] = uni((unsigned)(unsigned long long)(wu + gam / 6 * 1536 + (gam % 6) * 128 + wave * (16 * 9216)));
    sa[7] = uni((unsigned)((unsigned long long)(wu + gam / 6 * 1536 + (gam % 6) * 128 + wave * (16 * 9216)) >> 32));
    sa[8] = uni(wave * 4096);
    sa[9] = uni(pc0 * 1024 + 2048);
    sa[10] = uni((unsigned)(unsigned long long)(ws + WS_X2));
    sa[11] = uni((unsigned)((unsigned long long)(ws + WS_X2) >> 32));
    sa[12] = uni((unsigned)(unsigned long long)(out + 32 * XT));
    sa[13] = uni((unsigned)((unsigned long long)(out + 32 * XT) >> 32));
    sa[14] = uni((unsigned)(unsigned long long)(ws + WS_XD));
    sa[15] = uni((unsigned)((unsigned long long)(ws + WS_XD) >> 32));
    sb[0] = uni((unsigned)(unsigned long long)(ws - PT));
    sb[1] = uni((unsigned)((unsigned long long)(ws - PT) >> 32));
    sb[2] = uni((unsigned)(unsigned long long)(ws - X1P));
    sb[3] = uni((unsigned)((unsigned long long)(ws - X1P) >> 32));
    sb[4] = uni((unsigned)(unsigned long long)(ws + WS_X2));
    sb[5] = uni((unsigned)((unsigned long long)(ws + WS_X2) >> 32));
    sb[6] = uni((unsigned)(unsigned long long)(ws + WS_XD));
    sb[7] = uni((unsigned)((unsigned long long)(ws + WS_XD) >> 32));
    sb[8] = uni((unsigned)(unsigned long long)(ws + WS_XD));
    sb[9] = uni((unsigned)((unsigned long long)(ws + WS_XD) >> 32));
    sb[10] = uni(0u);
    asm volatile(
R"(.Lb3:
s_movk_i32 s69, 0x380
s_mov_b32 s71, 0
s_mov_b32 s74, 0
s_load_dword s37, s[38:39], 0x0
s_nop 1
v_add_u32 v166, 35840, v0
v_add_u32 v167, 71680, v0
v_add_u32 v168, 107520, v0
s_sleep 20
s_add_u32 m0, s44, 4096
s_nop 0
global_load_lds_dwordx4 v0, s[40:41] offset:-4096 nt
global_load_lds_dwordx4 v166, s[40:41] offset:-3072 nt
global_load_lds_dwordx4 v167, s[40:41] offset:-2048 nt
global_load_lds_dwordx4 v168, s[40:41] offset:-1024 nt
s_add_u32 s40, s40, 589824
s_addc_u32 s41, s41, 0
s_add_u32 m0, s44, 20480
s_nop 0
global_load_lds_dwordx4 v0, s[40:41] offset:-4096 nt
global_load_lds_dwordx4 v166, s[40:41] offset:-3072 nt
global_load_lds_dwordx4 v167, s[40:41] offset:-2048 nt
global_load_lds_dwordx4 v168, s[40:41] offset:-1024 nt
s_add_u32 s40, s40, 589824
s_addc_u32 s41, s41, 0
s_add_u32 m0, s44, 36864
s_nop 0
global_load_lds_dwordx4 v0, s[40:41] offset:-4096 nt
global_load_lds_dwordx4 v166, s[40:41] offset:-3072 nt
global_load_lds_dwordx4 v167, s[40:41] offset:-2048 nt
global_load_lds_dwordx4 v168, s[40:41] offset:-1024 nt
s_add_u32 s40, s40, 589824
s_addc_u32 s41, s41, 0
v_add_u32 v169, 0x10000, v1
v_mov_b32 v186, 0x3F803F80
v_mov_b32 v187, 0x3F803F80
v_mov_b32 v188, 0x3F803F80
v_mov_b32 v189, 0x3F803F80
s_waitcnt lgkmcnt(0)
s_mul_i32 s37, s37, 0x9E3779B1
s_add_u32 s37, s37, 0x7F4A7C15
v_add_u32 v5, s37, v5
v_add_u32 v7, s37, v7
v_add_u32 v17, s37, v17
s_waitcnt vmcnt(8)
s_barrier
s_add_u32 m0, s44, 53248
ds_read_b64_tr_b16 a[0:1], v1 offset:0
ds_read_b64_tr_b16 a[2:3], v1 offset:1024
global_load_lds_dwordx4 v0, s[40:41] offset:-4096 nt
ds_read_b64_tr_b16 a[4:5], v1 offset:8
global_load_lds_dwordx4 v166, s[40:41] offset:-3072 nt
ds_read_b64_tr_b16 a[6:7], v1 offset:1032
global_load_lds_dwordx4 v167, s[40:41] offset:-2048 nt
ds_read_b64_tr_b16 a[8:9], v1 offset:8192
global_load_lds_dwordx4 v168, s[40:41] offset:-1024 nt
ds_read_b64_tr_b16 a[10:11], v1 offset:9216
s_add_u32 s40, s40, 589824
ds_read_b64_tr_b16 a[12:13], v1 offset:8200
s_addc_u32 s41, s41, 0
ds_read_b64_tr_b16 a[14:15], v1 offset:9224
s_waitcnt vmcnt(8) lgkmcnt(0)
s_barrier
s_add_u32 m0, s44, 4096
ds_read_b64_tr_b16 a[16:17], v1 offset:16384
ds_read_b64_tr_b16 a[18:19], v1 offset:17408
global_load_lds_dwordx4 v0, s[40:41] offset:-4096 nt
ds_read_b64_tr_b16 a[20:21], v1 offset:16392
ds_read_b64_tr_b16 a[22:23], v1 offset:17416
global_load_lds_dwordx4 v166, s[40:41] offset:-3072 nt
ds_read_b64_tr_b16 a[24:25], v1 offset:24576
ds_read_b64_tr_b16 a[26:27], v1 offset:25600
global_load_lds_dwordx4 v167, s[40:41] offset:-2048 nt
ds_read_b64_tr_b16 a[28:29], v1 offset:24584
global_load_lds_dwordx4 v168, s[40:41] offset:-1024 nt
ds_read_b64_tr_b16 a[30:31], v1 offset:25608
v_mfma_f32_16x16x32_bf16 v[170:173], v[186:189], a[0:3], 0
s_add_u32 s40, s40, 589824
v_mfma_f32_16x16x32_bf16 v[174:177], v[186:189], a[4:7], 0
v_mfma_f32_16x16x32_bf16 v[170:173], v[186:189], a[8:11], v[170:173]
s_addc_u32 s41, s41, 0
v_mfma_f32_16x16x32_bf16 v[174:177], v[186:189], a[12:15], v[174:177]
s_waitcnt vmcnt(8) lgkmcnt(0)
s_barrier
s_add_u32 m0, s44, 20480
ds_read_b64_tr_b16 a[32:33], v1 offset:32768
ds_read_b64_tr_b16 a[34:35], v1 offset:33792
global_load_lds_dwordx4 v0, s[40:41] offset:-4096 nt
ds_read_b64_tr_b16 a[36:37], v1 offset:32776
ds_read_b64_tr_b16 a[38:39], v1 offset:33800
global_load_lds_dwordx4 v166, s[40:41] offset:-3072 nt
ds_read_b64_tr_b16 a[40:41], v1 offset:40960
ds_read_b64_tr_b16 a[42:43], v1 offset:41984
global_load_lds_dwordx4 v167, s[40:41] offset:-2048 nt
ds_read_b64_tr_b16 a[44:45], v1 offset:40968
global_load_lds_dwordx4 v168, s[40:41] offset:-1024 nt
ds_read_b64_tr_b16 a[46:47], v1 offset:41992
v_mfma_f32_16x16x32_bf16 v[170:173], v[186:189], a[16:19], v[170:173]
s_add_u32 s40, s40, 589824
v_mfma_f32_16x16x32_bf16 v[174:177], v[186:189], a[20:23], v[174:177]
v_mfma_f32_16x16x32_bf16 v[170:173], v[186:189], a[24:27], v[170:173]
s_addc_u32 s41, s41, 0
v_mfma_f32_16x16x32_bf16 v[174:177], v[186:189], a[28:31], v[174:177]
s_waitcnt vmcnt(8) lgkmcnt(0)
s_barrier
s_add_u32 m0, s44, 36864
ds_read_b64_tr_b16 a[48:49], v1 offset:49152
ds_read_b64_tr_b16 a[50:51], v1 offset:50176
global_load_lds_dwordx4 v0, s[40:41] offset:-4096 nt
ds_read_b64_tr_b16 a[52:53], v1 offset:49160
ds_read_b64_tr_b16 a[54:55], v1 offset:50184
global_load_lds_dwordx4 v166, s[40:41] offset:-3072 nt
ds_read_b64_tr_b16 a[56:57], v1 offset:57344
ds_read_b64_tr_b16 a[58:59], v1 offset:58368
global_load_lds_dwordx4 v167, s[40:41] offset:-2048 nt
ds_read_b64_tr_b16 a[60:61], v1 offset:57352
global_load_lds_dwordx4 v168, s[40:41] offset:-1024 nt
ds_read_b64_tr_b16 a[62:63], v1 offset:58376
v_mfma_f32_16x16x32_bf16 v[170:173], v[186:189], a[32:35], v[170:173]
s_add_u32 s40, s40, 589824
v_mfma_f32_16x16x32_bf16 v[174:177], v[186:189], a[36:39], v[174:177]
v_mfma_f32_16x16x32_bf16 v[170:173], v[186:189], a[40:43], v[170:173]
s_addc_u32 s41, s41, 0
v_mfma_f32_16x16x32_bf16 v[174:177], v[186:189], a[44:47], v[174:177]
s_waitcnt vmcnt(8) lgkmcnt(0)
s_barrier
s_add_u32 m0, s44, 53248
ds_read_b64_tr_b16 a[64:65], v1 offset:0
ds_read_b64_tr_b16 a[66:67], v1 offset:1024
global_load_lds_dwordx4 v0, s[40:41] offset:-4096 nt
ds_read_b64_tr_b16 a[68:69], v1 offset:8
ds_read_b64_tr_b16 a[70:71], v1 offset:1032
global_load_lds_dwordx4 v166, s[40:41] offset:-3072 nt
ds_read_b64_tr_b16 a[72:73], v1 offset:8192
ds_read_b64_tr_b16 a[74:75], v1 offset:9216
global_load_lds_dwordx4 v167, s[40:41] offset:-2048 nt
ds_read_b64_tr_b16 a[76:77], v1 offset:8200
global_load_lds_dwordx4 v168, s[40:41] offset:-1024 nt
ds_read_b64_tr_b16 a[78:79], v1 offset:9224
v_mfma_f32_16x16x32_bf16 v[170:173], v[186:189], a[48:51], v[170:173]
s_add_u32 s40, s40, 589824
v_mfma_f32_16x16x32_bf16 v[174:177], v[186:189], a[52:55], v[174:177]
v_mfma_f32_16x16x32_bf16 v[170:173], v[186:189], a[56:59], v[170:173]
s_addc_u32 s41, s41, 0
v_mfma_f32_16x16x32_bf16 v[174:177], v[186:189], a[60:63], v[174:177]
s_waitcnt vmcnt(8) lgkmcnt(0)
s_barrier
s_add_u32 m0, s44, 4096
ds_read_b64_tr_b16 a[80:81], v1 offset:16384
ds_read_b64_tr_b16 a[82:83], v1 offset:17408
global_load_lds_dwordx4 v0, s[40:41] offset:-4096 nt
ds_read_b64_tr_b16 a[84:85], v1 offset:16392
ds_read_b64_tr_b16 a[86:87], v1 offset:17416
global_load_lds_dwordx4 v166, s[40:41] offset:-3072 nt
ds_read_b64_tr_b16 a[88:89], v1 offset:24576
ds_read_b64_tr_b16 a[90:91], v1 offset:25600
global_load_lds_dwordx4 v167, s[40:41] offset:-2048 nt
ds_read_b64_tr_b16 a[92:93], v1 offset:24584
ds_read_b64_tr_b16 a[94:95], v1 offset:25608
global_load_lds_dwordx4 v168, s[40:41] offset:-1024 nt
v_mfma_f32_16x16x32_bf16 v[170:173], v[186:189], a[64:67], v[170:173]
v_mfma_f32_16x16x32_bf16 v[174:177], v[186:189], a[68:71], v[174:177]
s_mov_b64 s[40:41], s[42:43]
v_mfma_f32_16x16x32_bf16 v[170:173], v[186:189], a[72:75], v[170:173]
v_mfma_f32_16x16x32_bf16 v[174:177], v[186:189], a[76:79], v[174:177]
s_waitcnt vmcnt(8) lgkmcnt(0)
s_barrier
s_add_u32 m0, s44, 20480
ds_read_b64_tr_b16 a[96:97], v1 offset:32768
ds_read_b64_tr_b16 a[98:99], v1 offset:33792
global_load_lds_dwordx4 v0, s[40:41] offset:-4096 nt
ds_read_b64_tr_b16 a[100:101], v1 offset:32776
ds_read_b64_tr_b16 a[102:103], v1 offset:33800
global_load_lds_dwordx4 v166, s[40:41] offset:-3072 nt
ds_read_b64_tr_b16 a[104:105], v1 offset:40960
ds_read_b64_tr_b16 a[106:107], v1 offset:41984
global_load_lds_dwordx4 v167, s[40:41] offset:-2048 nt
ds_read_b64_tr_b16 a[108:109], v1 offset:40968
global_load_lds_dwordx4 v168, s[40:41] offset:-1024 nt
ds_read_b64_tr_b16 a[110:111], v1 offset:41992
v_mfma_f32_16x16x32_bf16 v[170:173], v[186:189], a[80:83], v[170:173]
s_add_u32 s40, s40, 589824
v_mfma_f32_16x16x32_bf16 v[174:177], v[186:189], a[84:87], v[174:177]
v_mfma_f32_16x16x32_bf16 v[170:173], v[186:189], a[88:91], v[170:173]
s_addc_u32 s41, s41, 0
v_mfma_f32_16x16x32_bf16 v[174:177], v[186:189], a[92:95], v[174:177]
s_waitcnt vmcnt(8) lgkmcnt(0)
s_barrier
s_add_u32 m0, s44, 36864
ds_read_b64_tr_b16 a[112:113], v1 offset:49152
ds_read_b64_tr_b16 a[114:115], v1 offset:50176
global_load_lds_dwordx4 v0, s[40:41] offset:-4096 nt
ds_read_b64_tr_b16 a[116:117], v1 offset:49160
ds_read_b64_tr_b16 a[118:119], v1 offset:50184
global_load_lds_dwordx4 v166, s[40:41] offset:-3072 nt
ds_read_b64_tr_b16 a[120:121], v1 offset:57344
ds_read_b64_tr_b16 a[122:123], v1 offset:58368
global_load_lds_dwordx4 v167, s[40:41] offset:-2048 nt
ds_read_b64_tr_b16 a[124:125], v1 offset:57352
global_load_lds_dwordx4 v168, s[40:41] offset:-1024 nt
ds_read_b64_tr_b16 a[126:127], v1 offset:58376
v_mfma_f32_16x16x32_bf16 v[170:173], v[186:189], a[96:99], v[170:173]
s_add_u32 s40, s40, 589824
v_mfma_f32_16x16x32_bf16 v[174:177], v[186:189], a[100:103], v[174:177]
v_mfma_f32_16x16x32_bf16 v[170:173], v[186:189], a[104:107], v[170:173]
s_addc_u32 s41, s41, 0
v_mfma_f32_16x16x32_bf16 v[174:177], v[186:189], a[108:111], v[174:177]
s_waitcnt vmcnt(8) lgkmcnt(0)
s_barrier
s_add_u32 m0, s44, 53248
ds_read_b64_tr_b16 a[128:129], v1 offset:0
ds_read_b64_tr_b16 a[130:131], v1 offset:1024
global_load_lds_dwordx4 v0, s[40:41] offset:-4096 nt
ds_read_b64_tr_b16 a[132:133], v1 offset:8
ds_read_b64_tr_b16 a[134:135], v1 offset:1032
global_load_lds_dwordx4 v166, s[40:41] offset:-3072 nt
ds_read_b64_tr_b16 a[136:137], v1 offset:8192
ds_read_b64_tr_b16 a[138:139], v1 offset:9216
global_load_lds_dwordx4 v167, s[40:41] offset:-2048 nt
ds_read_b64_tr_b16 a[140:141], v1 offset:8200
global_load_lds_dwordx4 v168, s[40:41] offset:-1024 nt
ds_read_b64_tr_b16 a[142:143], v1 offset:9224
v_mfma_f32_16x16x32_bf16 v[170:173], v[186:189], a[112:115], v[170:173]
s_add_u32 s40, s40, 589824
v_mfma_f32_16x16x32_bf16 v[174:177], v[186:189], a[116:119], v[174:177]
v_mfma_f32_16x16x32_bf16 v[170:173], v[186:189], a[120:123], v[170:173]
s_addc_u32 s41, s41, 0
v_mfma_f32_16x16x32_bf16 v[174:177], v[186:189], a[124:127], v[174:177]
s_waitcnt vmcnt(8) lgkmcnt(0)
s_barrier
s_add_u32 m0, s44, 4096
ds_read_b64_tr_b16 a[144:145], v1 offset:16384
ds_read_b64_tr_b16 a[146:147], v1 offset:17408
global_load_lds_dwordx4 v0, s[40:41] offset:-4096 nt
ds_read_b64_tr_b16 a[148:149], v1 offset:16392
ds_read_b64_tr_b16 a[150:151], v1 offset:17416
global_load_lds_dwordx4 v166, s[40:41] offset:-3072 nt
ds_read_b64_tr_b16 a[152:153], v1 offset:24576
ds_read_b64_tr_b16 a[154:155], v1 offset:25600
global_load_lds_dwordx4 v167, s[40:41] offset:-2048 nt
ds_read_b64_tr_b16 a[156:157], v1 offset:24584
global_load_lds_dwordx4 v168, s[40:41] offset:-1024 nt
ds_read_b64_tr_b16 a[158:159], v1 offset:25608
v_mfma_f32_16x16x32_bf16 v[170:173], v[186:189], a[128:131], v[170:173]
s_add_u32 s40, s40, 589824
v_mfma_f32_16x16x32_bf16 v[174:177], v[186:189], a[132:135], v[174:177]
v_mfma_f32_16x16x32_bf16 v[170:173], v[186:189], a[136:139], v[170:173]
s_addc_u32 s41, s41, 0
v_mfma_f32_16x16x32_bf16 v[174:177], v[186:189], a[140:143], v[174:177]
s_waitcnt vmcnt(8) lgkmcnt(0)
s_barrier
s_add_u32 m0, s44, 20480
ds_read_b64_tr_b16 a[160:161], v1 offset:32768
v_mfma_f32_16x16x32_bf16 v[178:181], v[186:189], a[144:147], 0
v_mfma_f32_16x16x32_bf16 v[182:185], v[186:189], a[148:151], 0
v_mfma_f32_16x16x32_bf16 v[178:181], v[186:189], a[152:155], v[178:181]
v_mfma_f32_16x16x32_bf16 v[182:185], v[186:189], a[156:159], v[182:185]
v_mul_f32 v170, 0xbae38e39, v170
v_mul_f32 v171, 0xbae38e39, v171
ds_read_b64_tr_b16 a[162:163], v1 offset:33792
v_mul_f32 v172, 0xbae38e39, v172
global_load_lds_dwordx4 v0, s[40:41] offset:-4096 nt
v_mul_f32 v173, 0xbae38e39, v173
v_mul_f32 v174, 0xbae38e39, v174
v_mul_f32 v175, 0xbae38e39, v175
v_mul_f32 v176, 0xbae38e39, v176
v_mul_f32 v177, 0xbae38e39, v177
ds_read_b64_tr_b16 a[164:165], v1 offset:32776
v_mfma_f32_16x16x32_bf16 v[190:193], v[22:25], a[0:3], v[170:173]
v_mfma_f32_16x16x32_bf16 v[194:197], v[20:23], a[0:3], v[170:173]
global_load_lds_dwordx4 v166, s[40:41] offset:-3072 nt
v_mfma_f32_16x16x32_bf16 v[198:201], v[22:25], a[4:7], v[174:177]
v_mfma_f32_16x16x32_bf16 v[202:205], v[20:23], a[4:7], v[174:177]
v_mfma_f32_16x16x32_bf16 v[206:209], v[22:25], a[8:11], v[170:173]
s_nop 2
v_cvt_pk_bf16_f32 v222, v190, v191
ds_read_b64_tr_b16 a[166:167], v1 offset:33800
v_cvt_pk_bf16_f32 v223, v192, v193
v_cvt_pk_bf16_f32 v224, v194, v195
v_cvt_pk_bf16_f32 v225, v196, v197
global_load_lds_dwordx4 v167, s[40:41] offset:-2048 nt
v_mfma_f32_16x16x32_bf16 v[210:213], v[20:23], a[8:11], v[170:173]
v_accvgpr_write_b32 a0, v222
ds_read_b64_tr_b16 a[168:169], v1 offset:40960
v_accvgpr_write_b32 a1, v223
v_accvgpr_write_b32 a2, v224
v_accvgpr_write_b32 a3, v225
v_mfma_f32_16x16x32_bf16 v[214:217], v[22:25], a[12:15], v[174:177]
v_cvt_pk_bf16_f32 v226, v198, v199
v_cvt_pk_bf16_f32 v227, v200, v201
global_load_lds_dwordx4 v168, s[40:41] offset:-1024 nt
ds_read_b64_tr_b16 a[170:171], v1 offset:41984
v_cvt_pk_bf16_f32 v228, v202, v203
v_cvt_pk_bf16_f32 v229, v204, v205
v_mfma_f32_16x16x32_bf16 v[218:221], v[20:23], a[12:15], v[174:177]
s_add_u32 s40, s40, 589824
v_accvgpr_write_b32 a4, v226
v_accvgpr_write_b32 a5, v227
v_accvgpr_write_b32 a6, v228
ds_read_b64_tr_b16 a[172:173], v1 offset:40968
v_accvgpr_write_b32 a7, v229
v_mfma_f32_16x16x32_bf16 v[190:193], v[22:25], a[16:19], v[170:173]
v_cvt_pk_bf16_f32 v222, v206, v207
v_cvt_pk_bf16_f32 v223, v208, v209
v_cvt_pk_bf16_f32 v224, v210, v211
v_cvt_pk_bf16_f32 v225, v212, v213
ds_read_b64_tr_b16 a[174:175], v1 offset:41992
v_mfma_f32_16x16x32_bf16 v[194:197], v[20:23], a[16:19], v[170:173]
s_addc_u32 s41, s41, 0
v_accvgpr_write_b32 a8, v222
v_accvgpr_write_b32 a9, v223
v_accvgpr_write_b32 a10, v224
v_accvgpr_write_b32 a11, v225
v_mfma_f32_16x16x32_bf16 v[198:201], v[22:25], a[20:23], v[174:177]
s_waitcnt vmcnt(8) lgkmcnt(0)
v_cvt_pk_bf16_f32 v226, v214, v215
v_cvt_pk_bf16_f32 v227, v216, v217
v_cvt_pk_bf16_f32 v228, v218, v219
s_barrier
s_add_u32 m0, s44, 36864
ds_read_b64_tr_b16 a[176:177], v1 offset:49152
v_mfma_f32_16x16x32_bf16 v[178:181], v[186:189], a[160:163], v[178:181]
v_mfma_f32_16x16x32_bf16 v[182:185], v[186:189], a[164:167], v[182:185]
v_mfma_f32_16x16x32_bf16 v[178:181], v[186:189], a[168:171], v[178:181]
v_mfma_f32_16x16x32_bf16 v[182:185], v[186:189], a[172:175], v[182:185]
v_cvt_pk_bf16_f32 v229, v220, v221
v_mfma_f32_16x16x32_bf16 v[202:205], v[20:23], a[20:23], v[174:177]
ds_read_b64_tr_b16 a[178:179], v1 offset:50176
v_accvgpr_write_b32 a12, v226
global_load_lds_dwordx4 v0, s[40:41] offset:-4096 nt
v_accvgpr_write_b32 a13, v227
v_accvgpr_write_b32 a14, v228
v_accvgpr_write_b32 a15, v229
v_mfma_f32_16x16x32_bf16 v[206:209], v[22:25], a[24:27], v[170:173]
v_cvt_pk_bf16_f32 v222, v190, v191
ds_read_b64_tr_b16 a[180:181], v1 offset:49160
v_cvt_pk_bf16_f32 v223, v192, v193
v_cvt_pk_bf16_f32 v224, v194, v195
global_load_lds_dwordx4 v166, s[40:41] offset:-3072 nt
v_cvt_pk_bf16_f32 v225, v196, v197
v_mfma_f32_16x16x32_bf16 v[210:213], v[20:23], a[24:27], v[170:173]
v_accvgpr_write_b32 a16, v222
v_accvgpr_write_b32 a17, v223
ds_read_b64_tr_b16 a[182:183], v1 offset:50184
v_accvgpr_write_b32 a18, v224
v_accvgpr_write_b32 a19, v225
v_mfma_f32_16x16x32_bf16 v[214:217], v[22:25], a[28:31], v[174:177]
global_load_lds_dwordx4 v167, s[40:41] offset:-2048 nt
v_cvt_pk_bf16_f32 v226, v198, v199
v_cvt_pk_bf16_f32 v227, v200, v201
ds_read_b64_tr_b16 a[184:185], v1 offset:57344
v_cvt_pk_bf16_f32 v228, v202, v203
v_cvt_pk_bf16_f32 v229, v204, v205
v_mfma_f32_16x16x32_bf16 v[218:221], v[20:23], a[28:31], v[174:177]
v_accvgpr_write_b32 a20, v226
v_accvgpr_write_b32 a21, v227
v_accvgpr_write_b32 a22, v228
global_load_lds_dwordx4 v168, s[40:41] offset:-1024 nt
ds_read_b64_tr_b16 a[186:187], v1 offset:58368
v_accvgpr_write_b32 a23, v229
v_mfma_f32_16x16x32_bf16 v[190:193], v[22:25], a[32:35], v[170:173]
s_add_u32 s40, s40, 589824
v_cvt_pk_bf16_f32 v222, v206, v207
v_cvt_pk_bf16_f32 v223, v208, v209
v_cvt_pk_bf16_f32 v224, v210, v211
v_cvt_pk_bf16_f32 v225, v212, v213
ds_read_b64_tr_b16 a[188:189], v1 offset:57352
v_mfma_f32_16x16x32_bf16 v[194:197], v[20:23], a[32:35], v[170:173]
v_accvgpr_write_b32 a24, v222
v_accvgpr_write_b32 a25, v223
v_accvgpr_write_b32 a26, v224
v_accvgpr_write_b32 a27, v225
v_mfma_f32_16x16x32_bf16 v[198:201], v[22:25], a[36:39], v[174:177]
s_addc_u32 s41, s41, 0
ds_read_b64_tr_b16 a[190:191], v1 offset:58376
v_cvt_pk_bf16_f32 v226, v214, v215
v_cvt_pk_bf16_f32 v227, v216, v217
v_cvt_pk_bf16_f32 v228, v218, v219
v_cvt_pk_bf16_f32 v229, v220, v221
v_mfma_f32_16x16x32_bf16 v[202:205], v[20:23], a[36:39], v[174:177]
s_waitcnt vmcnt(8) lgkmcnt(0)
v_accvgpr_write_b32 a28, v226
v_accvgpr_write_b32 a29, v227
v_accvgpr_write_b32 a30, v228
v_accvgpr_write_b32 a31, v229
s_barrier
s_add_u32 m0, s44, 53248
ds_read_b64_tr_b16 a[192:193], v1 offset:0
v_mfma_f32_16x16x32_bf16 v[178:181], v[186:189], a[176:179], v[178:181]
v_mfma_f32_16x16x32_bf16 v[182:185], v[186:189], a[180:183], v[182:185]
v_mfma_f32_16x16x32_bf16 v[178:181], v[186:189], a[184:187], v[178:181]
v_mfma_f32_16x16x32_bf16 v[182:185], v[186:189], a[188:191], v[182:185]
v_mfma_f32_16x16x32_bf16 v[206:209], v[22:25], a[40:43], v[170:173]
v_cvt_pk_bf16_f32 v222, v190, v191
ds_read_b64_tr_b16 a[194:195], v1 offset:1024
v_cvt_pk_bf16_f32 v223, v192, v193
global_load_lds_dwordx4 v0, s[40:41] offset:-4096 nt
v_cvt_pk_bf16_f32 v224, v194, v195
v_cvt_pk_bf16_f32 v225, v196, v197
)"
R"(v_mfma_f32_16x16x32_bf16 v[210:213], v[20:23], a[40:43], v[170:173]
v_accvgpr_write_b32 a32, v222
v_accvgpr_write_b32 a33, v223
ds_read_b64_tr_b16 a[196:197], v1 offset:8
v_accvgpr_write_b32 a34, v224
v_accvgpr_write_b32 a35, v225
global_load_lds_dwordx4 v166, s[40:41] offset:-3072 nt
v_mfma_f32_16x16x32_bf16 v[214:217], v[22:25], a[44:47], v[174:177]
v_cvt_pk_bf16_f32 v226, v198, v199
v_cvt_pk_bf16_f32 v227, v200, v201
v_cvt_pk_bf16_f32 v228, v202, v203
ds_read_b64_tr_b16 a[198:199], v1 offset:1032
v_cvt_pk_bf16_f32 v229, v204, v205
v_mfma_f32_16x16x32_bf16 v[218:221], v[20:23], a[44:47], v[174:177]
v_accvgpr_write_b32 a36, v226
global_load_lds_dwordx4 v167, s[40:41] offset:-2048 nt
v_accvgpr_write_b32 a37, v227
v_accvgpr_write_b32 a38, v228
ds_read_b64_tr_b16 a[200:201], v1 offset:8192
v_accvgpr_write_b32 a39, v229
v_mfma_f32_16x16x32_bf16 v[190:193], v[22:25], a[48:51], v[170:173]
v_cvt_pk_bf16_f32 v222, v206, v207
v_cvt_pk_bf16_f32 v223, v208, v209
v_cvt_pk_bf16_f32 v224, v210, v211
v_cvt_pk_bf16_f32 v225, v212, v213
global_load_lds_dwordx4 v168, s[40:41] offset:-1024 nt
ds_read_b64_tr_b16 a[202:203], v1 offset:9216
v_mfma_f32_16x16x32_bf16 v[194:197], v[20:23], a[48:51], v[170:173]
v_accvgpr_write_b32 a40, v222
v_accvgpr_write_b32 a41, v223
v_accvgpr_write_b32 a42, v224
v_accvgpr_write_b32 a43, v225
v_mfma_f32_16x16x32_bf16 v[198:201], v[22:25], a[52:55], v[174:177]
s_add_u32 s40, s40, 589824
ds_read_b64_tr_b16 a[204:205], v1 offset:8200
v_cvt_pk_bf16_f32 v226, v214, v215
v_cvt_pk_bf16_f32 v227, v216, v217
v_cvt_pk_bf16_f32 v228, v218, v219
v_cvt_pk_bf16_f32 v229, v220, v221
v_mfma_f32_16x16x32_bf16 v[202:205], v[20:23], a[52:55], v[174:177]
s_addc_u32 s41, s41, 0
v_accvgpr_write_b32 a44, v226
ds_read_b64_tr_b16 a[206:207], v1 offset:9224
v_accvgpr_write_b32 a45, v227
v_accvgpr_write_b32 a46, v228
v_accvgpr_write_b32 a47, v229
v_mfma_f32_16x16x32_bf16 v[206:209], v[22:25], a[56:59], v[170:173]
v_cvt_pk_bf16_f32 v222, v190, v191
v_cvt_pk_bf16_f32 v223, v192, v193
v_cvt_pk_bf16_f32 v224, v194, v195
v_cvt_pk_bf16_f32 v225, v196, v197
v_mfma_f32_16x16x32_bf16 v[210:213], v[20:23], a[56:59], v[170:173]
s_waitcnt vmcnt(8) lgkmcnt(0)
s_barrier
s_add_u32 m0, s44, 4096
ds_read_b64_tr_b16 a[208:209], v1 offset:16384
v_mfma_f32_16x16x32_bf16 v[178:181], v[186:189], a[192:195], v[178:181]
v_mfma_f32_16x16x32_bf16 v[182:185], v[186:189], a[196:199], v[182:185]
v_mfma_f32_16x16x32_bf16 v[178:181], v[186:189], a[200:203], v[178:181]
v_mfma_f32_16x16x32_bf16 v[182:185], v[186:189], a[204:207], v[182:185]
v_accvgpr_write_b32 a48, v222
v_accvgpr_write_b32 a49, v223
ds_read_b64_tr_b16 a[210:211], v1 offset:17408
v_accvgpr_write_b32 a50, v224
global_load_lds_dwordx4 v0, s[40:41] offset:-4096 nt
v_accvgpr_write_b32 a51, v225
v_mfma_f32_16x16x32_bf16 v[214:217], v[22:25], a[60:63], v[174:177]
v_cvt_pk_bf16_f32 v226, v198, v199
v_cvt_pk_bf16_f32 v227, v200, v201
v_cvt_pk_bf16_f32 v228, v202, v203
ds_read_b64_tr_b16 a[212:213], v1 offset:16392
v_cvt_pk_bf16_f32 v229, v204, v205
v_mfma_f32_16x16x32_bf16 v[218:221], v[20:23], a[60:63], v[174:177]
global_load_lds_dwordx4 v166, s[40:41] offset:-3072 nt
v_accvgpr_write_b32 a52, v226
v_accvgpr_write_b32 a53, v227
v_accvgpr_write_b32 a54, v228
v_accvgpr_write_b32 a55, v229
ds_read_b64_tr_b16 a[214:215], v1 offset:17416
v_mfma_f32_16x16x32_bf16 v[190:193], v[22:25], a[64:67], v[170:173]
v_cvt_pk_bf16_f32 v222, v206, v207
v_cvt_pk_bf16_f32 v223, v208, v209
global_load_lds_dwordx4 v167, s[40:41] offset:-2048 nt
v_cvt_pk_bf16_f32 v224, v210, v211
v_cvt_pk_bf16_f32 v225, v212, v213
ds_read_b64_tr_b16 a[216:217], v1 offset:24576
v_mfma_f32_16x16x32_bf16 v[194:197], v[20:23], a[64:67], v[170:173]
v_accvgpr_write_b32 a56, v222
v_accvgpr_write_b32 a57, v223
v_accvgpr_write_b32 a58, v224
v_accvgpr_write_b32 a59, v225
v_mfma_f32_16x16x32_bf16 v[198:201], v[22:25], a[68:71], v[174:177]
global_load_lds_dwordx4 v168, s[40:41] offset:-1024 nt
ds_read_b64_tr_b16 a[218:219], v1 offset:25600
v_cvt_pk_bf16_f32 v226, v214, v215
v_cvt_pk_bf16_f32 v227, v216, v217
v_cvt_pk_bf16_f32 v228, v218, v219
v_cvt_pk_bf16_f32 v229, v220, v221
v_mfma_f32_16x16x32_bf16 v[202:205], v[20:23], a[68:71], v[174:177]
s_add_u32 s40, s40, 589824
v_accvgpr_write_b32 a60, v226
ds_read_b64_tr_b16 a[220:221], v1 offset:24584
v_accvgpr_write_b32 a61, v227
v_accvgpr_write_b32 a62, v228
v_accvgpr_write_b32 a63, v229
v_mfma_f32_16x16x32_bf16 v[206:209], v[22:25], a[72:75], v[170:173]
s_addc_u32 s41, s41, 0
v_cvt_pk_bf16_f32 v222, v190, v191
v_cvt_pk_bf16_f32 v223, v192, v193
ds_read_b64_tr_b16 a[222:223], v1 offset:25608
v_cvt_pk_bf16_f32 v224, v194, v195
v_cvt_pk_bf16_f32 v225, v196, v197
v_mfma_f32_16x16x32_bf16 v[210:213], v[20:23], a[72:75], v[170:173]
v_accvgpr_write_b32 a64, v222
v_accvgpr_write_b32 a65, v223
v_accvgpr_write_b32 a66, v224
v_accvgpr_write_b32 a67, v225
v_mfma_f32_16x16x32_bf16 v[214:217], v[22:25], a[76:79], v[174:177]
s_waitcnt vmcnt(8) lgkmcnt(0)
v_cvt_pk_bf16_f32 v226, v198, v199
s_barrier
s_add_u32 m0, s44, 20480
ds_read_b64_tr_b16 a[224:225], v1 offset:32768
v_mfma_f32_16x16x32_bf16 v[178:181], v[186:189], a[208:211], v[178:181]
v_mfma_f32_16x16x32_bf16 v[182:185], v[186:189], a[212:215], v[182:185]
v_mfma_f32_16x16x32_bf16 v[178:181], v[186:189], a[216:219], v[178:181]
v_mfma_f32_16x16x32_bf16 v[182:185], v[186:189], a[220:223], v[182:185]
v_cvt_pk_bf16_f32 v227, v200, v201
v_cvt_pk_bf16_f32 v228, v202, v203
ds_read_b64_tr_b16 a[226:227], v1 offset:33792
v_cvt_pk_bf16_f32 v229, v204, v205
global_load_lds_dwordx4 v0, s[40:41] offset:-4096 nt
v_mfma_f32_16x16x32_bf16 v[218:221], v[20:23], a[76:79], v[174:177]
v_accvgpr_write_b32 a68, v226
v_accvgpr_write_b32 a69, v227
v_accvgpr_write_b32 a70, v228
v_accvgpr_write_b32 a71, v229
ds_read_b64_tr_b16 a[228:229], v1 offset:32776
v_mfma_f32_16x16x32_bf16 v[190:193], v[22:25], a[80:83], v[170:173]
v_cvt_pk_bf16_f32 v222, v206, v207
global_load_lds_dwordx4 v166, s[40:41] offset:-3072 nt
v_cvt_pk_bf16_f32 v223, v208, v209
v_cvt_pk_bf16_f32 v224, v210, v211
v_cvt_pk_bf16_f32 v225, v212, v213
v_mfma_f32_16x16x32_bf16 v[194:197], v[20:23], a[80:83], v[170:173]
ds_read_b64_tr_b16 a[230:231], v1 offset:33800
v_accvgpr_write_b32 a72, v222
v_accvgpr_write_b32 a73, v223
v_accvgpr_write_b32 a74, v224
global_load_lds_dwordx4 v167, s[40:41] offset:-2048 nt
v_accvgpr_write_b32 a75, v225
v_mfma_f32_16x16x32_bf16 v[198:201], v[22:25], a[84:87], v[174:177]
ds_read_b64_tr_b16 a[232:233], v1 offset:40960
v_cvt_pk_bf16_f32 v226, v214, v215
v_cvt_pk_bf16_f32 v227, v216, v217
v_cvt_pk_bf16_f32 v228, v218, v219
v_cvt_pk_bf16_f32 v229, v220, v221
v_mfma_f32_16x16x32_bf16 v[202:205], v[20:23], a[84:87], v[174:177]
v_accvgpr_write_b32 a76, v226
global_load_lds_dwordx4 v168, s[40:41] offset:-1024 nt
ds_read_b64_tr_b16 a[234:235], v1 offset:41984
v_accvgpr_write_b32 a77, v227
v_accvgpr_write_b32 a78, v228
v_accvgpr_write_b32 a79, v229
v_mfma_f32_16x16x32_bf16 v[206:209], v[22:25], a[88:91], v[170:173]
s_sub_u32 s40, s40, 10027008
v_cvt_pk_bf16_f32 v222, v190, v191
v_cvt_pk_bf16_f32 v223, v192, v193
ds_read_b64_tr_b16 a[236:237], v1 offset:40968
v_cvt_pk_bf16_f32 v224, v194, v195
v_cvt_pk_bf16_f32 v225, v196, v197
v_mfma_f32_16x16x32_bf16 v[210:213], v[20:23], a[88:91], v[170:173]
s_subb_u32 s41, s41, 0
v_accvgpr_write_b32 a80, v222
v_accvgpr_write_b32 a81, v223
v_accvgpr_write_b32 a82, v224
ds_read_b64_tr_b16 a[238:239], v1 offset:41992
v_accvgpr_write_b32 a83, v225
v_mfma_f32_16x16x32_bf16 v[214:217], v[22:25], a[92:95], v[174:177]
v_cvt_pk_bf16_f32 v226, v198, v199
v_cvt_pk_bf16_f32 v227, v200, v201
v_cvt_pk_bf16_f32 v228, v202, v203
v_cvt_pk_bf16_f32 v229, v204, v205
v_mfma_f32_16x16x32_bf16 v[218:221], v[20:23], a[92:95], v[174:177]
s_waitcnt vmcnt(8) lgkmcnt(0)
v_accvgpr_write_b32 a84, v226
v_accvgpr_write_b32 a85, v227
s_barrier
ds_read_b64_tr_b16 v[26:27], v1 offset:49152
v_mfma_f32_16x16x32_bf16 v[178:181], v[186:189], a[224:227], v[178:181]
v_mfma_f32_16x16x32_bf16 v[182:185], v[186:189], a[228:231], v[182:185]
v_mfma_f32_16x16x32_bf16 v[178:181], v[186:189], a[232:235], v[178:181]
v_mfma_f32_16x16x32_bf16 v[182:185], v[186:189], a[236:239], v[182:185]
v_accvgpr_write_b32 a86, v228
v_accvgpr_write_b32 a87, v229
ds_read_b64_tr_b16 v[28:29], v1 offset:50176
v_mfma_f32_16x16x32_bf16 v[190:193], v[22:25], a[96:99], v[170:173]
v_cvt_pk_bf16_f32 v222, v206, v207
v_cvt_pk_bf16_f32 v223, v208, v209
v_cvt_pk_bf16_f32 v224, v210, v211
v_cvt_pk_bf16_f32 v225, v212, v213
v_mfma_f32_16x16x32_bf16 v[194:197], v[20:23], a[96:99], v[170:173]
ds_read_b64_tr_b16 v[30:31], v1 offset:49160
v_accvgpr_write_b32 a88, v222
v_accvgpr_write_b32 a89, v223
v_accvgpr_write_b32 a90, v224
v_accvgpr_write_b32 a91, v225
v_mfma_f32_16x16x32_bf16 v[198:201], v[22:25], a[100:103], v[174:177]
v_cvt_pk_bf16_f32 v226, v214, v215
ds_read_b64_tr_b16 v[32:33], v1 offset:50184
v_cvt_pk_bf16_f32 v227, v216, v217
v_cvt_pk_bf16_f32 v228, v218, v219
v_cvt_pk_bf16_f32 v229, v220, v221
v_mfma_f32_16x16x32_bf16 v[202:205], v[20:23], a[100:103], v[174:177]
v_accvgpr_write_b32 a92, v226
ds_read_b64_tr_b16 v[34:35], v1 offset:57344
v_accvgpr_write_b32 a93, v227
v_accvgpr_write_b32 a94, v228
v_accvgpr_write_b32 a95, v229
v_mfma_f32_16x16x32_bf16 v[206:209], v[22:25], a[104:107], v[170:173]
v_cvt_pk_bf16_f32 v222, v190, v191
v_cvt_pk_bf16_f32 v223, v192, v193
ds_read_b64_tr_b16 v[36:37], v1 offset:58368
v_cvt_pk_bf16_f32 v224, v194, v195
v_cvt_pk_bf16_f32 v225, v196, v197
v_mfma_f32_16x16x32_bf16 v[210:213], v[20:23], a[104:107], v[170:173]
v_accvgpr_write_b32 a96, v222
v_accvgpr_write_b32 a97, v223
v_accvgpr_write_b32 a98, v224
ds_read_b64_tr_b16 v[38:39], v1 offset:57352
v_accvgpr_write_b32 a99, v225
v_mfma_f32_16x16x32_bf16 v[214:217], v[22:25], a[108:111], v[174:177]
v_cvt_pk_bf16_f32 v226, v198, v199
v_cvt_pk_bf16_f32 v227, v200, v201
v_cvt_pk_bf16_f32 v228, v202, v203
v_cvt_pk_bf16_f32 v229, v204, v205
ds_read_b64_tr_b16 v[40:41], v1 offset:58376
v_mfma_f32_16x16x32_bf16 v[218:221], v[20:23], a[108:111], v[174:177]
v_accvgpr_write_b32 a100, v226
v_accvgpr_write_b32 a101, v227
v_accvgpr_write_b32 a102, v228
v_accvgpr_write_b32 a103, v229
v_mfma_f32_16x16x32_bf16 v[190:193], v[22:25], a[112:115], v[170:173]
s_waitcnt vmcnt(4) lgkmcnt(0)
v_cvt_pk_bf16_f32 v222, v206, v207
v_cvt_pk_bf16_f32 v223, v208, v209
v_cvt_pk_bf16_f32 v224, v210, v211
s_barrier
ds_read_b64_tr_b16 v[42:43], v1 offset:0
v_mfma_f32_16x16x32_bf16 v[178:181], v[186:189], v[26:29], v[178:181]
v_mfma_f32_16x16x32_bf16 v[182:185], v[186:189], v[30:33], v[182:185]
v_mfma_f32_16x16x32_bf16 v[178:181], v[186:189], v[34:37], v[178:181]
v_mfma_f32_16x16x32_bf16 v[182:185], v[186:189], v[38:41], v[182:185]
v_cvt_pk_bf16_f32 v225, v212, v213
v_mfma_f32_16x16x32_bf16 v[194:197], v[20:23], a[112:115], v[170:173]
ds_read_b64_tr_b16 v[44:45], v1 offset:1024
v_accvgpr_write_b32 a104, v222
v_accvgpr_write_b32 a105, v223
v_accvgpr_write_b32 a106, v224
v_accvgpr_write_b32 a107, v225
v_mfma_f32_16x16x32_bf16 v[198:201], v[22:25], a[116:119], v[174:177]
v_cvt_pk_bf16_f32 v226, v214, v215
ds_read_b64_tr_b16 v[46:47], v1 offset:8
v_cvt_pk_bf16_f32 v227, v216, v217
v_cvt_pk_bf16_f32 v228, v218, v219
v_cvt_pk_bf16_f32 v229, v220, v221
v_mfma_f32_16x16x32_bf16 v[202:205], v[20:23], a[116:119], v[174:177]
v_accvgpr_write_b32 a108, v226
v_accvgpr_write_b32 a109, v227
ds_read_b64_tr_b16 v[48:49], v1 offset:1032
v_accvgpr_write_b32 a110, v228
v_accvgpr_write_b32 a111, v229
v_mfma_f32_16x16x32_bf16 v[206:209], v[22:25], a[120:123], v[170:173]
v_cvt_pk_bf16_f32 v222, v190, v191
v_cvt_pk_bf16_f32 v223, v192, v193
ds_read_b64_tr_b16 v[50:51], v1 offset:8192
v_cvt_pk_bf16_f32 v224, v194, v195
v_cvt_pk_bf16_f32 v225, v196, v197
v_mfma_f32_16x16x32_bf16 v[210:213], v[20:23], a[120:123], v[170:173]
v_accvgpr_write_b32 a112, v222
v_accvgpr_write_b32 a113, v223
v_accvgpr_write_b32 a114, v224
ds_read_b64_tr_b16 v[52:53], v1 offset:9216
v_accvgpr_write_b32 a115, v225
v_mfma_f32_16x16x32_bf16 v[214:217], v[22:25], a[124:127], v[174:177]
v_cvt_pk_bf16_f32 v226, v198, v199
v_cvt_pk_bf16_f32 v227, v200, v201
v_cvt_pk_bf16_f32 v228, v202, v203
v_cvt_pk_bf16_f32 v229, v204, v205
ds_read_b64_tr_b16 v[54:55], v1 offset:8200
v_mfma_f32_16x16x32_bf16 v[218:221], v[20:23], a[124:127], v[174:177]
v_accvgpr_write_b32 a116, v226
v_accvgpr_write_b32 a117, v227
v_accvgpr_write_b32 a118, v228
v_accvgpr_write_b32 a119, v229
v_mfma_f32_16x16x32_bf16 v[190:193], v[22:25], a[128:131], v[170:173]
ds_read_b64_tr_b16 v[56:57], v1 offset:9224
v_cvt_pk_bf16_f32 v222, v206, v207
v_cvt_pk_bf16_f32 v223, v208, v209
v_cvt_pk_bf16_f32 v224, v210, v211
v_cvt_pk_bf16_f32 v225, v212, v213
v_mfma_f32_16x16x32_bf16 v[194:197], v[20:23], a[128:131], v[170:173]
s_waitcnt vmcnt(0) lgkmcnt(0)
v_accvgpr_write_b32 a120, v222
v_accvgpr_write_b32 a121, v223
v_accvgpr_write_b32 a122, v224
v_accvgpr_write_b32 a123, v225
s_barrier
ds_read_b64_tr_b16 v[58:59], v1 offset:16384
v_mfma_f32_16x16x32_bf16 v[178:181], v[186:189], v[42:45], v[178:181]
v_mfma_f32_16x16x32_bf16 v[182:185], v[186:189], v[46:49], v[182:185]
v_mfma_f32_16x16x32_bf16 v[178:181], v[186:189], v[50:53], v[178:181]
v_mfma_f32_16x16x32_bf16 v[182:185], v[186:189], v[54:57], v[182:185]
v_mfma_f32_16x16x32_bf16 v[198:201], v[22:25], a[132:135], v[174:177]
v_cvt_pk_bf16_f32 v226, v214, v215
ds_read_b64_tr_b16 v[60:61], v1 offset:17408
v_cvt_pk_bf16_f32 v227, v216, v217
v_cvt_pk_bf16_f32 v228, v218, v219
v_cvt_pk_bf16_f32 v229, v220, v221
v_mfma_f32_16x16x32_bf16 v[202:205], v[20:23], a[132:135], v[174:177]
v_accvgpr_write_b32 a124, v226
v_accvgpr_write_b32 a125, v227
ds_read_b64_tr_b16 v[62:63], v1 offset:16392
v_accvgpr_write_b32 a126, v228
v_accvgpr_write_b32 a127, v229
v_mfma_f32_16x16x32_bf16 v[206:209], v[22:25], a[136:139], v[170:173]
v_cvt_pk_bf16_f32 v222, v190, v191
v_cvt_pk_bf16_f32 v223, v192, v193
v_cvt_pk_bf16_f32 v224, v194, v195
ds_read_b64_tr_b16 v[64:65], v1 offset:17416
v_cvt_pk_bf16_f32 v225, v196, v197
v_mfma_f32_16x16x32_bf16 v[210:213], v[20:23], a[136:139], v[170:173]
v_accvgpr_write_b32 a128, v222
v_accvgpr_write_b32 a129, v223
v_accvgpr_write_b32 a130, v224
ds_read_b64_tr_b16 v[66:67], v1 offset:24576
v_accvgpr_write_b32 a131, v225
v_mfma_f32_16x16x32_bf16 v[214:217], v[22:25], a[140:143], v[174:177]
v_cvt_pk_bf16_f32 v226, v198, v199
v_cvt_pk_bf16_f32 v227, v200, v201
v_cvt_pk_bf16_f32 v228, v202, v203
v_cvt_pk_bf16_f32 v229, v204, v205
ds_read_b64_tr_b16 v[68:69], v1 offset:25600
v_mfma_f32_16x16x32_bf16 v[218:221], v[20:23], a[140:143], v[174:177]
s_waitcnt lgkmcnt(4)
v_accvgpr_write_b32 a132, v226
v_accvgpr_write_b32 a133, v227
v_accvgpr_write_b32 a134, v228
v_accvgpr_write_b32 a135, v229
v_cvt_pk_bf16_f32 v222, v206, v207
ds_read_b64_tr_b16 v[70:71], v1 offset:24584
v_cvt_pk_bf16_f32 v223, v208, v209
v_cvt_pk_bf16_f32 v224, v210, v211
v_cvt_pk_bf16_f32 v225, v212, v213
v_accvgpr_write_b32 a136, v222
v_accvgpr_write_b32 a137, v223
v_accvgpr_write_b32 a138, v224
ds_read_b64_tr_b16 v[72:73], v1 offset:25608
v_accvgpr_write_b32 a139, v225
v_cvt_pk_bf16_f32 v226, v214, v215
v_cvt_pk_bf16_f32 v227, v216, v217
v_cvt_pk_bf16_f32 v228, v218, v219
v_cvt_pk_bf16_f32 v229, v220, v221
v_accvgpr_write_b32 a140, v226
v_accvgpr_write_b32 a141, v227
v_accvgpr_write_b32 a142, v228
v_accvgpr_write_b32 a143, v229
v_mfma_f32_16x16x32_bf16 v[178:181], v[186:189], v[58:61], v[178:181]
s_waitcnt lgkmcnt(4)
v_mfma_f32_16x16x32_bf16 v[182:185], v[186:189], v[62:65], v[182:185]
s_waitcnt lgkmcnt(2)
v_mfma_f32_16x16x32_bf16 v[178:181], v[186:189], v[66:69], v[178:181]
s_waitcnt lgkmcnt(0)
v_mfma_f32_16x16x32_bf16 v[182:185], v[186:189], v[70:73], v[182:185]
s_nop 5
v_mul_f32 v178, 0xbae38e39, v178
v_mul_f32 v179, 0xbae38e39, v179
v_mul_f32 v180, 0xbae38e39, v180
v_mul_f32 v181, 0xbae38e39, v181
v_mul_f32 v182, 0xbae38e39, v182
v_mul_f32 v183, 0xbae38e39, v183
v_mul_f32 v184, 0xbae38e39, v184
v_mul_f32 v185, 0xbae38e39, v185
v_mfma_f32_16x16x32_bf16 v[190:193], v[22:25], a[144:147], v[178:181]
v_mfma_f32_16x16x32_bf16 v[194:197], v[20:23], a[144:147], v[178:181]
v_mfma_f32_16x16x32_bf16 v[198:201], v[22:25], a[148:151], v[182:185]
v_mfma_f32_16x16x32_bf16 v[202:205], v[20:23], a[148:151], v[182:185]
v_mfma_f32_16x16x32_bf16 v[206:209], v[22:25], a[152:155], v[178:181]
s_nop 3
v_cvt_pk_bf16_f32 v222, v190, v191
v_cvt_pk_bf16_f32 v223, v192, v193
v_cvt_pk_bf16_f32 v224, v194, v195
v_cvt_pk_bf16_f32 v225, v196, v197
v_mfma_f32_16x16x32_bf16 v[210:213], v[20:23], a[152:155], v[178:181]
v_accvgpr_write_b32 a144, v222
v_accvgpr_write_b32 a145, v223
v_accvgpr_write_b32 a146, v224
v_accvgpr_write_b32 a147, v225
v_mfma_f32_16x16x32_bf16 v[214:217], v[22:25], a[156:159], v[182:185]
v_cvt_pk_bf16_f32 v226, v198, v199
)"
R"(v_cvt_pk_bf16_f32 v227, v200, v201
v_cvt_pk_bf16_f32 v228, v202, v203
v_cvt_pk_bf16_f32 v229, v204, v205
v_mfma_f32_16x16x32_bf16 v[218:221], v[20:23], a[156:159], v[182:185]
v_accvgpr_write_b32 a148, v226
v_accvgpr_write_b32 a149, v227
v_accvgpr_write_b32 a150, v228
v_accvgpr_write_b32 a151, v229
v_mfma_f32_16x16x32_bf16 v[190:193], v[22:25], a[160:163], v[178:181]
v_cvt_pk_bf16_f32 v222, v206, v207
v_cvt_pk_bf16_f32 v223, v208, v209
v_cvt_pk_bf16_f32 v224, v210, v211
v_cvt_pk_bf16_f32 v225, v212, v213
v_mfma_f32_16x16x32_bf16 v[194:197], v[20:23], a[160:163], v[178:181]
v_accvgpr_write_b32 a152, v222
v_accvgpr_write_b32 a153, v223
v_accvgpr_write_b32 a154, v224
v_accvgpr_write_b32 a155, v225
v_mfma_f32_16x16x32_bf16 v[198:201], v[22:25], a[164:167], v[182:185]
v_cvt_pk_bf16_f32 v226, v214, v215
v_cvt_pk_bf16_f32 v227, v216, v217
v_cvt_pk_bf16_f32 v228, v218, v219
v_cvt_pk_bf16_f32 v229, v220, v221
v_mfma_f32_16x16x32_bf16 v[202:205], v[20:23], a[164:167], v[182:185]
v_accvgpr_write_b32 a156, v226
v_accvgpr_write_b32 a157, v227
v_accvgpr_write_b32 a158, v228
v_accvgpr_write_b32 a159, v229
v_mfma_f32_16x16x32_bf16 v[206:209], v[22:25], a[168:171], v[178:181]
v_cvt_pk_bf16_f32 v222, v190, v191
v_cvt_pk_bf16_f32 v223, v192, v193
v_cvt_pk_bf16_f32 v224, v194, v195
v_cvt_pk_bf16_f32 v225, v196, v197
v_mfma_f32_16x16x32_bf16 v[210:213], v[20:23], a[168:171], v[178:181]
v_accvgpr_write_b32 a160, v222
v_accvgpr_write_b32 a161, v223
v_accvgpr_write_b32 a162, v224
v_accvgpr_write_b32 a163, v225
v_mfma_f32_16x16x32_bf16 v[214:217], v[22:25], a[172:175], v[182:185]
v_cvt_pk_bf16_f32 v226, v198, v199
v_cvt_pk_bf16_f32 v227, v200, v201
v_cvt_pk_bf16_f32 v228, v202, v203
v_cvt_pk_bf16_f32 v229, v204, v205
v_mfma_f32_16x16x32_bf16 v[218:221], v[20:23], a[172:175], v[182:185]
v_accvgpr_write_b32 a164, v226
v_accvgpr_write_b32 a165, v227
v_accvgpr_write_b32 a166, v228
v_accvgpr_write_b32 a167, v229
v_mfma_f32_16x16x32_bf16 v[190:193], v[22:25], a[176:179], v[178:181]
v_cvt_pk_bf16_f32 v222, v206, v207
v_cvt_pk_bf16_f32 v223, v208, v209
v_cvt_pk_bf16_f32 v224, v210, v211
v_cvt_pk_bf16_f32 v225, v212, v213
v_mfma_f32_16x16x32_bf16 v[194:197], v[20:23], a[176:179], v[178:181]
v_accvgpr_write_b32 a168, v222
v_accvgpr_write_b32 a169, v223
v_accvgpr_write_b32 a170, v224
v_accvgpr_write_b32 a171, v225
v_mfma_f32_16x16x32_bf16 v[198:201], v[22:25], a[180:183], v[182:185]
v_cvt_pk_bf16_f32 v226, v214, v215
v_cvt_pk_bf16_f32 v227, v216, v217
v_cvt_pk_bf16_f32 v228, v218, v219
v_cvt_pk_bf16_f32 v229, v220, v221
v_mfma_f32_16x16x32_bf16 v[202:205], v[20:23], a[180:183], v[182:185]
v_accvgpr_write_b32 a172, v226
v_accvgpr_write_b32 a173, v227
v_accvgpr_write_b32 a174, v228
v_accvgpr_write_b32 a175, v229
v_mfma_f32_16x16x32_bf16 v[206:209], v[22:25], a[184:187], v[178:181]
v_cvt_pk_bf16_f32 v222, v190, v191
v_cvt_pk_bf16_f32 v223, v192, v193
v_cvt_pk_bf16_f32 v224, v194, v195
v_cvt_pk_bf16_f32 v225, v196, v197
v_mfma_f32_16x16x32_bf16 v[210:213], v[20:23], a[184:187], v[178:181]
v_accvgpr_write_b32 a176, v222
v_accvgpr_write_b32 a177, v223
v_accvgpr_write_b32 a178, v224
v_accvgpr_write_b32 a179, v225
v_mfma_f32_16x16x32_bf16 v[214:217], v[22:25], a[188:191], v[182:185]
v_cvt_pk_bf16_f32 v226, v198, v199
v_cvt_pk_bf16_f32 v227, v200, v201
v_cvt_pk_bf16_f32 v228, v202, v203
v_cvt_pk_bf16_f32 v229, v204, v205
v_mfma_f32_16x16x32_bf16 v[218:221], v[20:23], a[188:191], v[182:185]
v_accvgpr_write_b32 a180, v226
v_accvgpr_write_b32 a181, v227
v_accvgpr_write_b32 a182, v228
v_accvgpr_write_b32 a183, v229
v_mfma_f32_16x16x32_bf16 v[190:193], v[22:25], a[192:195], v[178:181]
v_cvt_pk_bf16_f32 v222, v206, v207
v_cvt_pk_bf16_f32 v223, v208, v209
v_cvt_pk_bf16_f32 v224, v210, v211
v_cvt_pk_bf16_f32 v225, v212, v213
v_mfma_f32_16x16x32_bf16 v[194:197], v[20:23], a[192:195], v[178:181]
v_accvgpr_write_b32 a184, v222
v_accvgpr_write_b32 a185, v223
v_accvgpr_write_b32 a186, v224
v_accvgpr_write_b32 a187, v225
v_mfma_f32_16x16x32_bf16 v[198:201], v[22:25], a[196:199], v[182:185]
v_cvt_pk_bf16_f32 v226, v214, v215
v_cvt_pk_bf16_f32 v227, v216, v217
v_cvt_pk_bf16_f32 v228, v218, v219
v_cvt_pk_bf16_f32 v229, v220, v221
v_mfma_f32_16x16x32_bf16 v[202:205], v[20:23], a[196:199], v[182:185]
v_accvgpr_write_b32 a188, v226
v_accvgpr_write_b32 a189, v227
v_accvgpr_write_b32 a190, v228
v_accvgpr_write_b32 a191, v229
v_mfma_f32_16x16x32_bf16 v[206:209], v[22:25], a[200:203], v[178:181]
v_cvt_pk_bf16_f32 v222, v190, v191
v_cvt_pk_bf16_f32 v223, v192, v193
v_cvt_pk_bf16_f32 v224, v194, v195
v_cvt_pk_bf16_f32 v225, v196, v197
v_mfma_f32_16x16x32_bf16 v[210:213], v[20:23], a[200:203], v[178:181]
v_accvgpr_write_b32 a192, v222
v_accvgpr_write_b32 a193, v223
v_accvgpr_write_b32 a194, v224
v_accvgpr_write_b32 a195, v225
v_mfma_f32_16x16x32_bf16 v[214:217], v[22:25], a[204:207], v[182:185]
v_cvt_pk_bf16_f32 v226, v198, v199
v_cvt_pk_bf16_f32 v227, v200, v201
v_cvt_pk_bf16_f32 v228, v202, v203
v_cvt_pk_bf16_f32 v229, v204, v205
v_mfma_f32_16x16x32_bf16 v[218:221], v[20:23], a[204:207], v[182:185]
v_accvgpr_write_b32 a196, v226
v_accvgpr_write_b32 a197, v227
v_accvgpr_write_b32 a198, v228
v_accvgpr_write_b32 a199, v229
v_mfma_f32_16x16x32_bf16 v[190:193], v[22:25], a[208:211], v[178:181]
v_cvt_pk_bf16_f32 v222, v206, v207
v_cvt_pk_bf16_f32 v223, v208, v209
v_cvt_pk_bf16_f32 v224, v210, v211
v_cvt_pk_bf16_f32 v225, v212, v213
v_mfma_f32_16x16x32_bf16 v[194:197], v[20:23], a[208:211], v[178:181]
v_accvgpr_write_b32 a200, v222
v_accvgpr_write_b32 a201, v223
v_accvgpr_write_b32 a202, v224
v_accvgpr_write_b32 a203, v225
v_mfma_f32_16x16x32_bf16 v[198:201], v[22:25], a[212:215], v[182:185]
v_cvt_pk_bf16_f32 v226, v214, v215
v_cvt_pk_bf16_f32 v227, v216, v217
v_cvt_pk_bf16_f32 v228, v218, v219
v_cvt_pk_bf16_f32 v229, v220, v221
v_mfma_f32_16x16x32_bf16 v[202:205], v[20:23], a[212:215], v[182:185]
v_accvgpr_write_b32 a204, v226
v_accvgpr_write_b32 a205, v227
v_accvgpr_write_b32 a206, v228
v_accvgpr_write_b32 a207, v229
v_mfma_f32_16x16x32_bf16 v[206:209], v[22:25], a[216:219], v[178:181]
v_cvt_pk_bf16_f32 v222, v190, v191
v_cvt_pk_bf16_f32 v223, v192, v193
v_cvt_pk_bf16_f32 v224, v194, v195
v_cvt_pk_bf16_f32 v225, v196, v197
v_mfma_f32_16x16x32_bf16 v[210:213], v[20:23], a[216:219], v[178:181]
v_accvgpr_write_b32 a208, v222
v_accvgpr_write_b32 a209, v223
v_accvgpr_write_b32 a210, v224
v_accvgpr_write_b32 a211, v225
v_mfma_f32_16x16x32_bf16 v[214:217], v[22:25], a[220:223], v[182:185]
v_cvt_pk_bf16_f32 v226, v198, v199
v_cvt_pk_bf16_f32 v227, v200, v201
v_cvt_pk_bf16_f32 v228, v202, v203
v_cvt_pk_bf16_f32 v229, v204, v205
v_mfma_f32_16x16x32_bf16 v[218:221], v[20:23], a[220:223], v[182:185]
v_accvgpr_write_b32 a212, v226
v_accvgpr_write_b32 a213, v227
v_accvgpr_write_b32 a214, v228
v_accvgpr_write_b32 a215, v229
v_mfma_f32_16x16x32_bf16 v[190:193], v[22:25], a[224:227], v[178:181]
v_cvt_pk_bf16_f32 v222, v206, v207
v_cvt_pk_bf16_f32 v223, v208, v209
v_cvt_pk_bf16_f32 v224, v210, v211
v_cvt_pk_bf16_f32 v225, v212, v213
v_mfma_f32_16x16x32_bf16 v[194:197], v[20:23], a[224:227], v[178:181]
v_accvgpr_write_b32 a216, v222
v_accvgpr_write_b32 a217, v223
v_accvgpr_write_b32 a218, v224
v_accvgpr_write_b32 a219, v225
v_mfma_f32_16x16x32_bf16 v[198:201], v[22:25], a[228:231], v[182:185]
v_cvt_pk_bf16_f32 v226, v214, v215
v_cvt_pk_bf16_f32 v227, v216, v217
v_cvt_pk_bf16_f32 v228, v218, v219
v_cvt_pk_bf16_f32 v229, v220, v221
v_mfma_f32_16x16x32_bf16 v[202:205], v[20:23], a[228:231], v[182:185]
v_accvgpr_write_b32 a220, v226
v_accvgpr_write_b32 a221, v227
v_accvgpr_write_b32 a222, v228
v_accvgpr_write_b32 a223, v229
v_mfma_f32_16x16x32_bf16 v[206:209], v[22:25], a[232:235], v[178:181]
v_cvt_pk_bf16_f32 v222, v190, v191
v_cvt_pk_bf16_f32 v223, v192, v193
v_cvt_pk_bf16_f32 v224, v194, v195
v_cvt_pk_bf16_f32 v225, v196, v197
v_mfma_f32_16x16x32_bf16 v[210:213], v[20:23], a[232:235], v[178:181]
v_accvgpr_write_b32 a224, v222
v_accvgpr_write_b32 a225, v223
v_accvgpr_write_b32 a226, v224
v_accvgpr_write_b32 a227, v225
v_mfma_f32_16x16x32_bf16 v[214:217], v[22:25], a[236:239], v[182:185]
v_cvt_pk_bf16_f32 v226, v198, v199
v_cvt_pk_bf16_f32 v227, v200, v201
v_cvt_pk_bf16_f32 v228, v202, v203
v_cvt_pk_bf16_f32 v229, v204, v205
v_mfma_f32_16x16x32_bf16 v[218:221], v[20:23], a[236:239], v[182:185]
v_accvgpr_write_b32 a228, v226
v_accvgpr_write_b32 a229, v227
v_accvgpr_write_b32 a230, v228
v_accvgpr_write_b32 a231, v229
v_mfma_f32_16x16x32_bf16 v[190:193], v[22:25], v[26:29], v[178:181]
v_cvt_pk_bf16_f32 v222, v206, v207
v_cvt_pk_bf16_f32 v223, v208, v209
v_cvt_pk_bf16_f32 v224, v210, v211
v_cvt_pk_bf16_f32 v225, v212, v213
v_mfma_f32_16x16x32_bf16 v[194:197], v[20:23], v[26:29], v[178:181]
v_accvgpr_write_b32 a232, v222
v_accvgpr_write_b32 a233, v223
v_accvgpr_write_b32 a234, v224
v_accvgpr_write_b32 a235, v225
v_mfma_f32_16x16x32_bf16 v[198:201], v[22:25], v[30:33], v[182:185]
v_cvt_pk_bf16_f32 v226, v214, v215
v_cvt_pk_bf16_f32 v227, v216, v217
v_cvt_pk_bf16_f32 v228, v218, v219
v_cvt_pk_bf16_f32 v229, v220, v221
v_mfma_f32_16x16x32_bf16 v[202:205], v[20:23], v[30:33], v[182:185]
v_accvgpr_write_b32 a236, v226
v_accvgpr_write_b32 a237, v227
v_accvgpr_write_b32 a238, v228
v_accvgpr_write_b32 a239, v229
v_mfma_f32_16x16x32_bf16 v[206:209], v[22:25], v[34:37], v[178:181]
v_cvt_pk_bf16_f32 v26, v190, v191
v_cvt_pk_bf16_f32 v27, v192, v193
v_cvt_pk_bf16_f32 v28, v194, v195
v_cvt_pk_bf16_f32 v29, v196, v197
v_mfma_f32_16x16x32_bf16 v[210:213], v[20:23], v[34:37], v[178:181]
v_mfma_f32_16x16x32_bf16 v[214:217], v[22:25], v[38:41], v[182:185]
v_cvt_pk_bf16_f32 v30, v198, v199
v_cvt_pk_bf16_f32 v31, v200, v201
v_cvt_pk_bf16_f32 v32, v202, v203
v_cvt_pk_bf16_f32 v33, v204, v205
v_mfma_f32_16x16x32_bf16 v[218:221], v[20:23], v[38:41], v[182:185]
v_mfma_f32_16x16x32_bf16 v[190:193], v[22:25], v[42:45], v[178:181]
v_cvt_pk_bf16_f32 v34, v206, v207
v_cvt_pk_bf16_f32 v35, v208, v209
v_cvt_pk_bf16_f32 v36, v210, v211
v_cvt_pk_bf16_f32 v37, v212, v213
v_mfma_f32_16x16x32_bf16 v[194:197], v[20:23], v[42:45], v[178:181]
v_mfma_f32_16x16x32_bf16 v[198:201], v[22:25], v[46:49], v[182:185]
v_cvt_pk_bf16_f32 v38, v214, v215
v_cvt_pk_bf16_f32 v39, v216, v217
v_cvt_pk_bf16_f32 v40, v218, v219
v_cvt_pk_bf16_f32 v41, v220, v221
v_mfma_f32_16x16x32_bf16 v[202:205], v[20:23], v[46:49], v[182:185]
v_mfma_f32_16x16x32_bf16 v[206:209], v[22:25], v[50:53], v[178:181]
v_cvt_pk_bf16_f32 v42, v190, v191
v_cvt_pk_bf16_f32 v43, v192, v193
v_cvt_pk_bf16_f32 v44, v194, v195
v_cvt_pk_bf16_f32 v45, v196, v197
v_mfma_f32_16x16x32_bf16 v[210:213], v[20:23], v[50:53], v[178:181]
v_mfma_f32_16x16x32_bf16 v[214:217], v[22:25], v[54:57], v[182:185]
v_cvt_pk_bf16_f32 v46, v198, v199
v_cvt_pk_bf16_f32 v47, v200, v201
v_cvt_pk_bf16_f32 v48, v202, v203
v_cvt_pk_bf16_f32 v49, v204, v205
v_mfma_f32_16x16x32_bf16 v[218:221], v[20:23], v[54:57], v[182:185]
v_mfma_f32_16x16x32_bf16 v[190:193], v[22:25], v[58:61], v[178:181]
v_cvt_pk_bf16_f32 v50, v206, v207
v_cvt_pk_bf16_f32 v51, v208, v209
v_cvt_pk_bf16_f32 v52, v210, v211
v_cvt_pk_bf16_f32 v53, v212, v213
v_mfma_f32_16x16x32_bf16 v[194:197], v[20:23], v[58:61], v[178:181]
v_mfma_f32_16x16x32_bf16 v[198:201], v[22:25], v[62:65], v[182:185]
v_cvt_pk_bf16_f32 v54, v214, v215
v_cvt_pk_bf16_f32 v55, v216, v217
v_cvt_pk_bf16_f32 v56, v218, v219
v_cvt_pk_bf16_f32 v57, v220, v221
v_mfma_f32_16x16x32_bf16 v[202:205], v[20:23], v[62:65], v[182:185]
v_mfma_f32_16x16x32_bf16 v[206:209], v[22:25], v[66:69], v[178:181]
v_cvt_pk_bf16_f32 v58, v190, v191
v_cvt_pk_bf16_f32 v59, v192, v193
v_cvt_pk_bf16_f32 v60, v194, v195
v_cvt_pk_bf16_f32 v61, v196, v197
v_mfma_f32_16x16x32_bf16 v[210:213], v[20:23], v[66:69], v[178:181]
v_mfma_f32_16x16x32_bf16 v[214:217], v[22:25], v[70:73], v[182:185]
v_cvt_pk_bf16_f32 v62, v198, v199
v_cvt_pk_bf16_f32 v63, v200, v201
v_cvt_pk_bf16_f32 v64, v202, v203
v_cvt_pk_bf16_f32 v65, v204, v205
v_mfma_f32_16x16x32_bf16 v[218:221], v[20:23], v[70:73], v[182:185]
v_cvt_pk_bf16_f32 v66, v206, v207
v_cvt_pk_bf16_f32 v67, v208, v209
v_cvt_pk_bf16_f32 v68, v210, v211
v_cvt_pk_bf16_f32 v69, v212, v213
v_cvt_pk_bf16_f32 v70, v214, v215
v_cvt_pk_bf16_f32 v71, v216, v217
s_nop 1
v_cvt_pk_bf16_f32 v72, v218, v219
v_cvt_pk_bf16_f32 v73, v220, v221
s_barrier
s_and_b32 s75, s36, 3
s_cmp_eq_u32 s75, 1
s_cbranch_scc1 .Lgu_w2
s_cmp_eq_u32 s75, 2
s_cbranch_scc1 .Lgu_w3
s_cmp_eq_u32 s75, 3
s_cbranch_scc1 .Lgu_w4
.Lgu_w1:
v_add_u32 v254, -1, v5
v_add_u32 v255, -1, v5
v_add_lshl_u32 v252, v7, 0, 7
v_bfi_b32 v252, s69, v252, v6
v_add_lshl_u32 v253, v5, 0, 7
v_bfi_b32 v253, s69, v253, v4
s_add_u32 m0, s45, 18432
s_xor_b32 s45, s45, 0x10000
s_mov_b32 m0, s45
s_barrier
global_load_lds_dwordx4 v2, s[38:39] offset:-2048 sc1
global_load_lds_dwordx4 v2, s[38:39] offset:-1024 sc1
global_load_lds_dwordx4 v2, s[38:39] offset:0 sc1
global_load_lds_dwordx4 v2, s[38:39] offset:1024 sc1
global_load_lds_dwordx4 v2, s[38:39] offset:2048 sc1
global_load_lds_dwordx4 v2, s[38:39] offset:3072 sc1
v_add_u32 v2, 18688, v2
s_mov_b32 s70, 32
s_waitcnt vmcnt(0) lgkmcnt(0)
s_nop 7
s_branch .Lgu_in5
.p2align 6
.Lgu_top6:
s_waitcnt lgkmcnt(5)
v_mfma_f32_16x16x32_bf16 v[74:77], a[0:3], v[106:109], 0
s_waitcnt lgkmcnt(4)
v_cvt_pk_bf16_f32 v162, v154, v155
v_cvt_pk_bf16_f32 v163, v156, v157
v_mfma_f32_16x16x32_bf16 v[74:77], a[8:11], v[110:113], v[74:77]
s_waitcnt lgkmcnt(3)
v_mfma_f32_16x16x32_bf16 v[74:77], a[16:19], v[114:117], v[74:77]
s_waitcnt lgkmcnt(2)
v_mfma_f32_16x16x32_bf16 v[74:77], a[24:27], v[118:121], v[74:77]
s_waitcnt lgkmcnt(1)
v_mul_f32 v158, v94, v102
v_mul_f32 v159, v95, v103
v_mfma_f32_16x16x32_bf16 v[74:77], a[32:35], v[122:125], v[74:77]
s_waitcnt lgkmcnt(0)
v_mul_f32 v160, v96, v104
v_mul_f32 v161, v97, v105
v_mfma_f32_16x16x32_bf16 v[74:77], a[40:43], v[126:129], v[74:77]
ds_read_b128 v[130:133], v8 offset:6144
v_cvt_pk_bf16_f32 v164, v158, v159
v_mfma_f32_16x16x32_bf16 v[82:85], a[144:147], v[106:109], 0
ds_read_b128 v[134:137], v8 offset:7168
v_cvt_pk_bf16_f32 v165, v160, v161
v_mfma_f32_16x16x32_bf16 v[82:85], a[152:155], v[110:113], v[82:85]
v_mfma_f32_16x16x32_bf16 v[82:85], a[160:163], v[114:117], v[82:85]
s_waitcnt vmcnt(6)
s_barrier
v_mfma_f32_16x16x32_bf16 v[82:85], a[168:171], v[118:121], v[82:85]
v_mfma_f32_16x16x32_bf16 v[82:85], a[176:179], v[122:125], v[82:85]
global_store_dwordx4 v3, v[162:165], s[38:39] sc1
v_mfma_f32_16x16x32_bf16 v[82:85], a[184:187], v[126:129], v[82:85]
ds_read_b128 v[138:141], v8 offset:8192
v_add_u32 v3, 147456, v3
v_mfma_f32_16x16x32_bf16 v[78:81], a[4:7], v[106:109], 0
v_mfma_f32_16x16x32_bf16 v[78:81], a[12:15], v[110:113], v[78:81]
v_mfma_f32_16x16x32_bf16 v[78:81], a[20:23], v[114:117], v[78:81]
ds_read_b128 v[142:145], v8 offset:9216
v_mfma_f32_16x16x32_bf16 v[78:81], a[28:31], v[118:121], v[78:81]
v_mfma_f32_16x16x32_bf16 v[78:81], a[36:39], v[122:125], v[78:81]
v_mfma_f32_16x16x32_bf16 v[78:81], a[44:47], v[126:129], v[78:81]
ds_read_b128 v[146:149], v8 offset:10240
v_mfma_f32_16x16x32_bf16 v[86:89], a[148:151], v[106:109], 0
v_mfma_f32_16x16x32_bf16 v[86:89], a[156:159], v[110:113], v[86:89]
v_mfma_f32_16x16x32_bf16 v[86:89], a[164:167], v[114:117], v[86:89]
ds_read_b128 v[150:153], v8 offset:11264
v_mfma_f32_16x16x32_bf16 v[86:89], a[172:175], v[118:121], v[86:89]
v_mfma_f32_16x16x32_bf16 v[86:89], a[180:183], v[122:125], v[86:89]
v_mfma_f32_16x16x32_bf16 v[86:89], a[188:191], v[126:129], v[86:89]
s_waitcnt lgkmcnt(5)
v_mfma_f32_16x16x32_bf16 v[74:77], a[48:51], v[130:133], v[74:77]
s_waitcnt lgkmcnt(4)
v_mfma_f32_16x16x32_bf16 v[74:77], a[56:59], v[134:137], v[74:77]
s_waitcnt lgkmcnt(3)
v_mfma_f32_16x16x32_bf16 v[74:77], a[64:67], v[138:141], v[74:77]
s_waitcnt lgkmcnt(2)
v_mfma_f32_16x16x32_bf16 v[74:77], a[72:75], v[142:145], v[74:77]
s_waitcnt lgkmcnt(1)
v_mfma_f32_16x16x32_bf16 v[74:77], a[80:83], v[146:149], v[74:77]
s_waitcnt lgkmcnt(0)
v_mfma_f32_16x16x32_bf16 v[74:77], a[88:91], v[150:153], v[74:77]
ds_read_b128 v[106:109], v8 offset:12288
v_mfma_f32_16x16x32_bf16 v[82:85], a[192:195], v[130:133], v[82:85]
v_mfma_f32_16x16x32_bf16 v[82:85], a[200:203], v[134:137], v[82:85]
v_mfma_f32_16x16x32_bf16 v[82:85], a[208:211], v[138:141], v[82:85]
ds_read_b128 v[110:113], v8 offset:13312
v_mfma_f32_16x16x32_bf16 v[82:85], a[216:219], v[142:145], v[82:85]
v_mfma_f32_16x16x32_bf16 v[82:85], a[224:227], v[146:149], v[82:85]
v_mfma_f32_16x16x32_bf16 v[82:85], a[232:235], v[150:153], v[82:85]
ds_read_b128 v[114:117], v8 offset:14336
v_mfma_f32_16x16x32_bf16 v[78:81], a[52:55], v[130:133], v[78:81]
v_mfma_f32_16x16x32_bf16 v[78:81], a[60:63], v[134:137], v[78:81]
)"
R"(v_mfma_f32_16x16x32_bf16 v[78:81], a[68:71], v[138:141], v[78:81]
ds_read_b128 v[118:121], v8 offset:15360
v_mfma_f32_16x16x32_bf16 v[78:81], a[76:79], v[142:145], v[78:81]
v_mfma_f32_16x16x32_bf16 v[78:81], a[84:87], v[146:149], v[78:81]
v_mfma_f32_16x16x32_bf16 v[78:81], a[92:95], v[150:153], v[78:81]
ds_read_b128 v[122:125], v8 offset:16384
v_mfma_f32_16x16x32_bf16 v[86:89], a[196:199], v[130:133], v[86:89]
v_mfma_f32_16x16x32_bf16 v[86:89], a[204:207], v[134:137], v[86:89]
v_mfma_f32_16x16x32_bf16 v[86:89], a[212:215], v[138:141], v[86:89]
ds_read_b128 v[126:129], v8 offset:17408
v_mfma_f32_16x16x32_bf16 v[86:89], a[220:223], v[142:145], v[86:89]
s_xor_b32 s45, s45, 0x10000
v_mfma_f32_16x16x32_bf16 v[86:89], a[228:231], v[146:149], v[86:89]
s_add_u32 m0, s45, 0
v_mfma_f32_16x16x32_bf16 v[86:89], a[236:239], v[150:153], v[86:89]
s_waitcnt lgkmcnt(5)
v_mfma_f32_16x16x32_bf16 v[74:77], a[96:99], v[106:109], v[74:77]
s_barrier
s_waitcnt lgkmcnt(4)
v_mfma_f32_16x16x32_bf16 v[74:77], a[104:107], v[110:113], v[74:77]
s_waitcnt lgkmcnt(3)
global_load_lds_dwordx4 v2, s[38:39] offset:-2048 sc1
v_mfma_f32_16x16x32_bf16 v[74:77], a[112:115], v[114:117], v[74:77]
s_waitcnt lgkmcnt(2)
v_mfma_f32_16x16x32_bf16 v[74:77], a[120:123], v[118:121], v[74:77]
s_waitcnt lgkmcnt(1)
v_mfma_f32_16x16x32_bf16 v[74:77], a[128:131], v[122:125], v[74:77]
s_waitcnt lgkmcnt(0)
global_load_lds_dwordx4 v2, s[38:39] offset:-1024 sc1
v_mfma_f32_16x16x32_bf16 v[74:77], a[136:139], v[126:129], v[74:77]
v_mfma_f32_16x16x32_bf16 v[82:85], v[26:29], v[106:109], v[82:85]
v_mfma_f32_16x16x32_bf16 v[82:85], v[34:37], v[110:113], v[82:85]
global_load_lds_dwordx4 v2, s[38:39] offset:0 sc1
v_mfma_f32_16x16x32_bf16 v[82:85], v[42:45], v[114:117], v[82:85]
v_mfma_f32_16x16x32_bf16 v[82:85], v[50:53], v[118:121], v[82:85]
v_mfma_f32_16x16x32_bf16 v[82:85], v[58:61], v[122:125], v[82:85]
global_load_lds_dwordx4 v2, s[38:39] offset:1024 sc1
v_mfma_f32_16x16x32_bf16 v[82:85], v[66:69], v[126:129], v[82:85]
v_mfma_f32_16x16x32_bf16 v[78:81], a[100:103], v[106:109], v[78:81]
s_waitcnt vmcnt(5)
s_barrier
v_mfma_f32_16x16x32_bf16 v[78:81], a[108:111], v[110:113], v[78:81]
global_load_lds_dwordx4 v2, s[38:39] offset:2048 sc1
v_mfma_f32_16x16x32_bf16 v[78:81], a[116:119], v[114:117], v[78:81]
v_mfma_f32_16x16x32_bf16 v[78:81], a[124:127], v[118:121], v[78:81]
ds_read_b128 v[130:133], v8 offset:18432
v_mfma_f32_16x16x32_bf16 v[78:81], a[132:135], v[122:125], v[78:81]
ds_read_b128 v[134:137], v8 offset:19456
global_load_lds_dwordx4 v2, s[38:39] offset:3072 sc1
v_mfma_f32_16x16x32_bf16 v[78:81], a[140:143], v[126:129], v[78:81]
ds_read_b128 v[138:141], v8 offset:20480
v_add_u32 v2, 18688, v2
v_mfma_f32_16x16x32_bf16 v[86:89], v[30:33], v[106:109], v[86:89]
ds_read_b128 v[142:145], v8 offset:21504
v_min_u32 v2, v2, v9
v_mfma_f32_16x16x32_bf16 v[86:89], v[38:41], v[110:113], v[86:89]
ds_read_b128 v[146:149], v8 offset:22528
v_mul_f32 v154, v74, v82
v_mfma_f32_16x16x32_bf16 v[86:89], v[46:49], v[114:117], v[86:89]
ds_read_b128 v[150:153], v8 offset:23552
v_mul_f32 v155, v75, v83
v_mfma_f32_16x16x32_bf16 v[86:89], v[54:57], v[118:121], v[86:89]
v_mul_f32 v156, v76, v84
v_mul_f32 v157, v77, v85
v_mfma_f32_16x16x32_bf16 v[86:89], v[62:65], v[122:125], v[86:89]
v_cvt_pk_bf16_f32 v162, v154, v155
v_cvt_pk_bf16_f32 v163, v156, v157
v_mfma_f32_16x16x32_bf16 v[86:89], v[70:73], v[126:129], v[86:89]
s_waitcnt lgkmcnt(5)
v_mfma_f32_16x16x32_bf16 v[90:93], a[0:3], v[130:133], 0
s_waitcnt lgkmcnt(4)
v_mfma_f32_16x16x32_bf16 v[90:93], a[8:11], v[134:137], v[90:93]
s_waitcnt lgkmcnt(3)
v_mfma_f32_16x16x32_bf16 v[90:93], a[16:19], v[138:141], v[90:93]
s_waitcnt lgkmcnt(2)
v_mfma_f32_16x16x32_bf16 v[90:93], a[24:27], v[142:145], v[90:93]
s_waitcnt lgkmcnt(1)
v_mul_f32 v158, v78, v86
v_mul_f32 v159, v79, v87
v_mfma_f32_16x16x32_bf16 v[90:93], a[32:35], v[146:149], v[90:93]
s_waitcnt lgkmcnt(0)
v_mul_f32 v160, v80, v88
v_mul_f32 v161, v81, v89
v_mfma_f32_16x16x32_bf16 v[90:93], a[40:43], v[150:153], v[90:93]
ds_read_b128 v[106:109], v8 offset:24576
v_cvt_pk_bf16_f32 v164, v158, v159
v_mfma_f32_16x16x32_bf16 v[98:101], a[144:147], v[130:133], 0
ds_read_b128 v[110:113], v8 offset:25600
v_cvt_pk_bf16_f32 v165, v160, v161
v_mfma_f32_16x16x32_bf16 v[98:101], a[152:155], v[134:137], v[98:101]
v_mfma_f32_16x16x32_bf16 v[98:101], a[160:163], v[138:141], v[98:101]
s_waitcnt vmcnt(6)
s_barrier
v_mfma_f32_16x16x32_bf16 v[98:101], a[168:171], v[142:145], v[98:101]
v_mfma_f32_16x16x32_bf16 v[98:101], a[176:179], v[146:149], v[98:101]
global_store_dwordx4 v3, v[162:165], s[38:39] sc1
v_mfma_f32_16x16x32_bf16 v[98:101], a[184:187], v[150:153], v[98:101]
ds_read_b128 v[114:117], v8 offset:26624
v_add_u32 v3, 147456, v3
v_mfma_f32_16x16x32_bf16 v[94:97], a[4:7], v[130:133], 0
v_mfma_f32_16x16x32_bf16 v[94:97], a[12:15], v[134:137], v[94:97]
v_mfma_f32_16x16x32_bf16 v[94:97], a[20:23], v[138:141], v[94:97]
ds_read_b128 v[118:121], v8 offset:27648
v_mfma_f32_16x16x32_bf16 v[94:97], a[28:31], v[142:145], v[94:97]
v_mfma_f32_16x16x32_bf16 v[94:97], a[36:39], v[146:149], v[94:97]
v_mfma_f32_16x16x32_bf16 v[94:97], a[44:47], v[150:153], v[94:97]
ds_read_b128 v[122:125], v8 offset:28672
v_mfma_f32_16x16x32_bf16 v[102:105], a[148:151], v[130:133], 0
v_mfma_f32_16x16x32_bf16 v[102:105], a[156:159], v[134:137], v[102:105]
v_mfma_f32_16x16x32_bf16 v[102:105], a[164:167], v[138:141], v[102:105]
ds_read_b128 v[126:129], v8 offset:29696
v_mfma_f32_16x16x32_bf16 v[102:105], a[172:175], v[142:145], v[102:105]
v_mfma_f32_16x16x32_bf16 v[102:105], a[180:183], v[146:149], v[102:105]
v_mfma_f32_16x16x32_bf16 v[102:105], a[188:191], v[150:153], v[102:105]
s_waitcnt lgkmcnt(5)
v_mfma_f32_16x16x32_bf16 v[90:93], a[48:51], v[106:109], v[90:93]
s_waitcnt lgkmcnt(4)
v_mfma_f32_16x16x32_bf16 v[90:93], a[56:59], v[110:113], v[90:93]
s_waitcnt lgkmcnt(3)
v_mfma_f32_16x16x32_bf16 v[90:93], a[64:67], v[114:117], v[90:93]
s_waitcnt lgkmcnt(2)
v_mfma_f32_16x16x32_bf16 v[90:93], a[72:75], v[118:121], v[90:93]
s_waitcnt lgkmcnt(1)
v_mfma_f32_16x16x32_bf16 v[90:93], a[80:83], v[122:125], v[90:93]
s_waitcnt lgkmcnt(0)
v_mfma_f32_16x16x32_bf16 v[90:93], a[88:91], v[126:129], v[90:93]
ds_read_b128 v[130:133], v8 offset:30720
v_mfma_f32_16x16x32_bf16 v[98:101], a[192:195], v[106:109], v[98:101]
v_mfma_f32_16x16x32_bf16 v[98:101], a[200:203], v[110:113], v[98:101]
v_mfma_f32_16x16x32_bf16 v[98:101], a[208:211], v[114:117], v[98:101]
ds_read_b128 v[134:137], v8 offset:31744
v_mfma_f32_16x16x32_bf16 v[98:101], a[216:219], v[118:121], v[98:101]
v_mfma_f32_16x16x32_bf16 v[98:101], a[224:227], v[122:125], v[98:101]
v_mfma_f32_16x16x32_bf16 v[98:101], a[232:235], v[126:129], v[98:101]
ds_read_b128 v[138:141], v8 offset:32768
v_mfma_f32_16x16x32_bf16 v[94:97], a[52:55], v[106:109], v[94:97]
v_mfma_f32_16x16x32_bf16 v[94:97], a[60:63], v[110:113], v[94:97]
v_mfma_f32_16x16x32_bf16 v[94:97], a[68:71], v[114:117], v[94:97]
ds_read_b128 v[142:145], v8 offset:33792
v_mfma_f32_16x16x32_bf16 v[94:97], a[76:79], v[118:121], v[94:97]
v_mfma_f32_16x16x32_bf16 v[94:97], a[84:87], v[122:125], v[94:97]
v_mfma_f32_16x16x32_bf16 v[94:97], a[92:95], v[126:129], v[94:97]
ds_read_b128 v[146:149], v8 offset:34816
v_mfma_f32_16x16x32_bf16 v[102:105], a[196:199], v[106:109], v[102:105]
.Lgu_in5:
v_mfma_f32_16x16x32_bf16 v[102:105], a[204:207], v[110:113], v[102:105]
v_mfma_f32_16x16x32_bf16 v[102:105], a[212:215], v[114:117], v[102:105]
ds_read_b128 v[150:153], v8 offset:35840
v_mfma_f32_16x16x32_bf16 v[102:105], a[220:223], v[118:121], v[102:105]
v_mfma_f32_16x16x32_bf16 v[102:105], a[228:231], v[122:125], v[102:105]
s_add_u32 m0, s45, 18432
v_mfma_f32_16x16x32_bf16 v[102:105], a[236:239], v[126:129], v[102:105]
s_waitcnt lgkmcnt(5)
v_mfma_f32_16x16x32_bf16 v[90:93], a[96:99], v[130:133], v[90:93]
s_barrier
s_waitcnt lgkmcnt(4)
v_mfma_f32_16x16x32_bf16 v[90:93], a[104:107], v[134:137], v[90:93]
s_waitcnt lgkmcnt(3)
global_load_lds_dwordx4 v2, s[38:39] offset:-2048 sc1
v_mfma_f32_16x16x32_bf16 v[90:93], a[112:115], v[138:141], v[90:93]
s_waitcnt lgkmcnt(2)
v_mfma_f32_16x16x32_bf16 v[90:93], a[120:123], v[142:145], v[90:93]
s_waitcnt lgkmcnt(1)
v_mfma_f32_16x16x32_bf16 v[90:93], a[128:131], v[146:149], v[90:93]
s_waitcnt lgkmcnt(0)
global_load_lds_dwordx4 v2, s[38:39] offset:-1024 sc1
v_mfma_f32_16x16x32_bf16 v[90:93], a[136:139], v[150:153], v[90:93]
v_mfma_f32_16x16x32_bf16 v[98:101], v[26:29], v[130:133], v[98:101]
v_mfma_f32_16x16x32_bf16 v[98:101], v[34:37], v[134:137], v[98:101]
global_load_lds_dwordx4 v2, s[38:39] offset:0 sc1
v_mfma_f32_16x16x32_bf16 v[98:101], v[42:45], v[138:141], v[98:101]
v_mfma_f32_16x16x32_bf16 v[98:101], v[50:53], v[142:145], v[98:101]
v_xor_b32 v8, 0x10000, v8
v_mfma_f32_16x16x32_bf16 v[98:101], v[58:61], v[146:149], v[98:101]
global_load_lds_dwordx4 v2, s[38:39] offset:1024 sc1
v_mfma_f32_16x16x32_bf16 v[98:101], v[66:69], v[150:153], v[98:101]
v_mfma_f32_16x16x32_bf16 v[94:97], a[100:103], v[130:133], v[94:97]
s_waitcnt vmcnt(5)
s_barrier
v_mfma_f32_16x16x32_bf16 v[94:97], a[108:111], v[134:137], v[94:97]
global_load_lds_dwordx4 v2, s[38:39] offset:2048 sc1
v_mfma_f32_16x16x32_bf16 v[94:97], a[116:119], v[138:141], v[94:97]
v_mfma_f32_16x16x32_bf16 v[94:97], a[124:127], v[142:145], v[94:97]
ds_read_b128 v[106:109], v8 offset:0
v_mfma_f32_16x16x32_bf16 v[94:97], a[132:135], v[146:149], v[94:97]
ds_read_b128 v[110:113], v8 offset:1024
global_load_lds_dwordx4 v2, s[38:39] offset:3072 sc1
v_mfma_f32_16x16x32_bf16 v[94:97], a[140:143], v[150:153], v[94:97]
ds_read_b128 v[114:117], v8 offset:2048
v_add_u32 v2, 18688, v2
v_mfma_f32_16x16x32_bf16 v[102:105], v[30:33], v[130:133], v[102:105]
ds_read_b128 v[118:121], v8 offset:3072
v_min_u32 v2, v2, v9
v_mfma_f32_16x16x32_bf16 v[102:105], v[38:41], v[134:137], v[102:105]
ds_read_b128 v[122:125], v8 offset:4096
v_mul_f32 v154, v90, v98
v_mfma_f32_16x16x32_bf16 v[102:105], v[46:49], v[138:141], v[102:105]
ds_read_b128 v[126:129], v8 offset:5120
v_mul_f32 v155, v91, v99
v_mfma_f32_16x16x32_bf16 v[102:105], v[54:57], v[142:145], v[102:105]
v_mul_f32 v156, v92, v100
v_mul_f32 v157, v93, v101
v_mfma_f32_16x16x32_bf16 v[102:105], v[62:65], v[146:149], v[102:105]
v_mfma_f32_16x16x32_bf16 v[102:105], v[70:73], v[150:153], v[102:105]
s_sub_u32 s70, s70, 1
s_cbranch_scc0 .Lgu_top6
s_waitcnt vmcnt(6) lgkmcnt(0)
s_barrier
v_mul_f32 v154, v90, v98
v_mul_f32 v155, v91, v99
v_mul_f32 v156, v92, v100
v_mul_f32 v157, v93, v101
v_cvt_pk_bf16_f32 v162, v154, v155
v_cvt_pk_bf16_f32 v163, v156, v157
v_mul_f32 v158, v94, v102
v_mul_f32 v159, v95, v103
v_mul_f32 v160, v96, v104
v_mul_f32 v161, v97, v105
v_cvt_pk_bf16_f32 v164, v158, v159
v_cvt_pk_bf16_f32 v165, v160, v161
global_store_dwordx4 v3, v[162:165], s[38:39] sc1
v_mov_b32 v231, 192
ds_read_b32 v231, v231 offset:36864
s_waitcnt vmcnt(0)
s_barrier
s_waitcnt lgkmcnt(0)
v_readfirstlane_b32 s62, v231
s_nop 3
s_max_i32 s62, s62, 2
s_min_i32 s62, s62, 66
s_add_u32 s62, s62, -2
s_branch .Lgu_fin
.Lgu_w2:
v_add_u32 v254, -1, v5
v_add_u32 v255, -1, v5
v_add_lshl_u32 v252, v7, 0, 7
v_bfi_b32 v252, s69, v252, v6
v_add_lshl_u32 v253, v5, 0, 7
v_bfi_b32 v253, s69, v253, v4
s_add_u32 m0, s45, 18432
s_xor_b32 s45, s45, 0x10000
s_mov_b32 m0, s45
s_barrier
global_load_lds_dwordx4 v2, s[38:39] offset:-2048 sc1
global_load_lds_dwordx4 v2, s[38:39] offset:-1024 sc1
global_load_lds_dwordx4 v2, s[38:39] offset:0 sc1
global_load_lds_dwordx4 v2, s[38:39] offset:1024 sc1
global_load_lds_dwordx4 v2, s[38:39] offset:2048 sc1
global_load_lds_dwordx4 v2, s[38:39] offset:3072 sc1
v_add_u32 v2, 18688, v2
s_mov_b32 s70, 32
s_waitcnt vmcnt(0) lgkmcnt(0)
s_nop 7
s_branch .Lgu_in8
.p2align 6
.Lgu_top9:
s_waitcnt lgkmcnt(5)
v_mfma_f32_16x16x32_bf16 v[74:77], a[0:3], v[106:109], 0
s_waitcnt lgkmcnt(4)
v_mul_f32 v157, v93, v101
v_cvt_pk_bf16_f32 v162, v154, v155
v_mfma_f32_16x16x32_bf16 v[74:77], a[8:11], v[110:113], v[74:77]
s_waitcnt lgkmcnt(3)
v_cvt_pk_bf16_f32 v163, v156, v157
v_mfma_f32_16x16x32_bf16 v[74:77], a[16:19], v[114:117], v[74:77]
s_waitcnt lgkmcnt(2)
v_mfma_f32_16x16x32_bf16 v[74:77], a[24:27], v[118:121], v[74:77]
s_waitcnt lgkmcnt(1)
v_mul_f32 v158, v94, v102
v_mul_f32 v159, v95, v103
v_mfma_f32_16x16x32_bf16 v[74:77], a[32:35], v[122:125], v[74:77]
s_waitcnt lgkmcnt(0)
v_mul_f32 v160, v96, v104
v_mul_f32 v161, v97, v105
v_mfma_f32_16x16x32_bf16 v[74:77], a[40:43], v[126:129], v[74:77]
ds_read_b128 v[130:133], v8 offset:6144
v_cvt_pk_bf16_f32 v164, v158, v159
v_mfma_f32_16x16x32_bf16 v[82:85], a[144:147], v[106:109], 0
ds_read_b128 v[134:137], v8 offset:7168
v_cvt_pk_bf16_f32 v165, v160, v161
v_mfma_f32_16x16x32_bf16 v[82:85], a[152:155], v[110:113], v[82:85]
v_mfma_f32_16x16x32_bf16 v[82:85], a[160:163], v[114:117], v[82:85]
s_waitcnt vmcnt(6)
s_barrier
v_mfma_f32_16x16x32_bf16 v[82:85], a[168:171], v[118:121], v[82:85]
s_mov_b64 exec, 1
global_store_dword v252, v7, s[38:39] sc1
s_mov_b64 exec, -1
v_mfma_f32_16x16x32_bf16 v[82:85], a[176:179], v[122:125], v[82:85]
v_add_u32 v7, 1, v7
v_add_lshl_u32 v252, v7, 0, 7
v_mfma_f32_16x16x32_bf16 v[82:85], a[184:187], v[126:129], v[82:85]
ds_read_b128 v[138:141], v8 offset:8192
v_bfi_b32 v252, s69, v252, v6
v_mfma_f32_16x16x32_bf16 v[78:81], a[4:7], v[106:109], 0
global_store_dwordx4 v3, v[162:165], s[38:39] sc1
v_mfma_f32_16x16x32_bf16 v[78:81], a[12:15], v[110:113], v[78:81]
v_add_u32 v3, 147456, v3
v_mfma_f32_16x16x32_bf16 v[78:81], a[20:23], v[114:117], v[78:81]
ds_read_b128 v[142:145], v8 offset:9216
v_mfma_f32_16x16x32_bf16 v[78:81], a[28:31], v[118:121], v[78:81]
v_mfma_f32_16x16x32_bf16 v[78:81], a[36:39], v[122:125], v[78:81]
v_mfma_f32_16x16x32_bf16 v[78:81], a[44:47], v[126:129], v[78:81]
ds_read_b128 v[146:149], v8 offset:10240
v_mfma_f32_16x16x32_bf16 v[86:89], a[148:151], v[106:109], 0
v_mfma_f32_16x16x32_bf16 v[86:89], a[156:159], v[110:113], v[86:89]
v_mfma_f32_16x16x32_bf16 v[86:89], a[164:167], v[114:117], v[86:89]
ds_read_b128 v[150:153], v8 offset:11264
v_mfma_f32_16x16x32_bf16 v[86:89], a[172:175], v[118:121], v[86:89]
v_mfma_f32_16x16x32_bf16 v[86:89], a[180:183], v[122:125], v[86:89]
v_mfma_f32_16x16x32_bf16 v[86:89], a[188:191], v[126:129], v[86:89]
s_waitcnt lgkmcnt(5)
v_mfma_f32_16x16x32_bf16 v[74:77], a[48:51], v[130:133], v[74:77]
s_waitcnt lgkmcnt(4)
v_mfma_f32_16x16x32_bf16 v[74:77], a[56:59], v[134:137], v[74:77]
s_waitcnt lgkmcnt(3)
v_mfma_f32_16x16x32_bf16 v[74:77], a[64:67], v[138:141], v[74:77]
s_waitcnt lgkmcnt(2)
v_mfma_f32_16x16x32_bf16 v[74:77], a[72:75], v[142:145], v[74:77]
s_waitcnt lgkmcnt(1)
v_mfma_f32_16x16x32_bf16 v[74:77], a[80:83], v[146:149], v[74:77]
s_waitcnt lgkmcnt(0)
v_mfma_f32_16x16x32_bf16 v[74:77], a[88:91], v[150:153], v[74:77]
ds_read_b128 v[106:109], v8 offset:12288
v_mfma_f32_16x16x32_bf16 v[82:85], a[192:195], v[130:133], v[82:85]
v_mfma_f32_16x16x32_bf16 v[82:85], a[200:203], v[134:137], v[82:85]
v_mfma_f32_16x16x32_bf16 v[82:85], a[208:211], v[138:141], v[82:85]
ds_read_b128 v[110:113], v8 offset:13312
v_mfma_f32_16x16x32_bf16 v[82:85], a[216:219], v[142:145], v[82:85]
v_mfma_f32_16x16x32_bf16 v[82:85], a[224:227], v[146:149], v[82:85]
v_mfma_f32_16x16x32_bf16 v[82:85], a[232:235], v[150:153], v[82:85]
ds_read_b128 v[114:117], v8 offset:14336
v_mfma_f32_16x16x32_bf16 v[78:81], a[52:55], v[130:133], v[78:81]
v_mfma_f32_16x16x32_bf16 v[78:81], a[60:63], v[134:137], v[78:81]
v_mfma_f32_16x16x32_bf16 v[78:81], a[68:71], v[138:141], v[78:81]
ds_read_b128 v[118:121], v8 offset:15360
v_mfma_f32_16x16x32_bf16 v[78:81], a[76:79], v[142:145], v[78:81]
v_mfma_f32_16x16x32_bf16 v[78:81], a[84:87], v[146:149], v[78:81]
v_mfma_f32_16x16x32_bf16 v[78:81], a[92:95], v[150:153], v[78:81]
ds_read_b128 v[122:125], v8 offset:16384
v_mfma_f32_16x16x32_bf16 v[86:89], a[196:199], v[130:133], v[86:89]
v_mfma_f32_16x16x32_bf16 v[86:89], a[204:207], v[134:137], v[86:89]
v_mfma_f32_16x16x32_bf16 v[86:89], a[212:215], v[138:141], v[86:89]
ds_read_b128 v[126:129], v8 offset:17408
v_mfma_f32_16x16x32_bf16 v[86:89], a[220:223], v[142:145], v[86:89]
s_xor_b32 s45, s45, 0x10000
v_mfma_f32_16x16x32_bf16 v[86:89], a[228:231], v[146:149], v[86:89]
s_add_u32 m0, s45, 0
v_mfma_f32_16x16x32_bf16 v[86:89], a[236:239], v[150:153], v[86:89]
s_waitcnt lgkmcnt(5)
v_mfma_f32_16x16x32_bf16 v[74:77], a[96:99], v[106:109], v[74:77]
s_barrier
s_waitcnt lgkmcnt(4)
v_mfma_f32_16x16x32_bf16 v[74:77], a[104:107], v[110:113], v[74:77]
s_waitcnt lgkmcnt(3)
v_mfma_f32_16x16x32_bf16 v[74:77], a[112:115], v[114:117], v[74:77]
s_waitcnt lgkmcnt(2)
global_load_lds_dwordx4 v2, s[38:39] offset:-2048 sc1
v_mfma_f32_16x16x32_bf16 v[74:77], a[120:123], v[118:121], v[74:77]
s_waitcnt lgkmcnt(1)
v_mfma_f32_16x16x32_bf16 v[74:77], a[128:131], v[122:125], v[74:77]
s_waitcnt lgkmcnt(0)
v_mfma_f32_16x16x32_bf16 v[74:77], a[136:139], v[126:129], v[74:77]
global_load_lds_dwordx4 v2, s[38:39] offset:-1024 sc1
v_mfma_f32_16x16x32_bf16 v[82:85], v[26:29], v[106:109], v[82:85]
v_mfma_f32_16x16x32_bf16 v[82:85], v[34:37], v[110:113], v[82:85]
v_mfma_f32_16x16x32_bf16 v[82:85], v[42:45], v[114:117], v[82:85]
global_load_lds_dwordx4 v2, s[38:39] offset:0 sc1
v_mfma_f32_16x16x32_bf16 v[82:85], v[50:53], v[118:121], v[82:85]
v_mfma_f32_16x16x32_bf16 v[82:85], v[58:61], v[122:125], v[82:85]
v_mfma_f32_16x16x32_bf16 v[82:85], v[66:69], v[126:129], v[82:85]
v_mfma_f32_16x16x32_bf16 v[78:81], a[100:103], v[106:109], v[78:81]
s_waitcnt vmcnt(5)
s_barrier
v_mfma_f32_16x16x32_bf16 v[78:81], a[108:111], v[110:113], v[78:81]
global_load_lds_dwordx4 v2, s[38:39] offset:1024 sc1
v_mfma_f32_16x16x32_bf16 v[78:81], a[116:119], v[114:117], v[78:81]
v_mfma_f32_16x16x32_bf16 v[78:81], a[124:127], v[118:121], v[78:81]
ds_read_b128 v[130:133], v8 offset:18432
v_mfma_f32_16x16x32_bf16 v[78:81], a[132:135], v[122:125], v[78:81]
ds_read_b128 v[134:137], v8 offset:19456
global_load_lds_dwordx4 v2, s[38:39] offset:2048 sc1
v_mfma_f32_16x16x32_bf16 v[78:81], a[140:143], v[126:129], v[78:81]
ds_read_b128 v[138:141], v8 offset:20480
v_mul_f32 v154, v74, v82
v_mfma_f32_16x16x32_bf16 v[86:89], v[30:33], v[106:109], v[86:89]
ds_read_b128 v[142:145], v8 offset:21504
v_mul_f32 v155, v75, v83
v_mfma_f32_16x16x32_bf16 v[86:89], v[38:41], v[110:113], v[86:89]
ds_read_b128 v[146:149], v8 offset:22528
global_load_lds_dwordx4 v2, s[38:39] offset:3072 sc1
v_mfma_f32_16x16x32_bf16 v[86:89], v[46:49], v[114:117], v[86:89]
ds_read_b128 v[150:153], v8 offset:23552
v_add_u32 v2, 18688, v2
v_mfma_f32_16x16x32_bf16 v[86:89], v[54:57], v[118:121], v[86:89]
v_min_u32 v2, v2, v9
v_mul_f32 v156, v76, v84
v_mfma_f32_16x16x32_bf16 v[86:89], v[62:65], v[122:125], v[86:89]
v_mul_f32 v157, v77, v85
v_cvt_pk_bf16_f32 v162, v154, v155
)"
R"(v_mfma_f32_16x16x32_bf16 v[86:89], v[70:73], v[126:129], v[86:89]
s_waitcnt lgkmcnt(5)
v_cvt_pk_bf16_f32 v163, v156, v157
v_mfma_f32_16x16x32_bf16 v[90:93], a[0:3], v[130:133], 0
s_waitcnt lgkmcnt(4)
v_mfma_f32_16x16x32_bf16 v[90:93], a[8:11], v[134:137], v[90:93]
s_waitcnt lgkmcnt(3)
v_mfma_f32_16x16x32_bf16 v[90:93], a[16:19], v[138:141], v[90:93]
s_waitcnt lgkmcnt(2)
v_mfma_f32_16x16x32_bf16 v[90:93], a[24:27], v[142:145], v[90:93]
s_waitcnt lgkmcnt(1)
v_mul_f32 v158, v78, v86
v_mul_f32 v159, v79, v87
v_mfma_f32_16x16x32_bf16 v[90:93], a[32:35], v[146:149], v[90:93]
s_waitcnt lgkmcnt(0)
v_mul_f32 v160, v80, v88
v_mul_f32 v161, v81, v89
v_mfma_f32_16x16x32_bf16 v[90:93], a[40:43], v[150:153], v[90:93]
ds_read_b128 v[106:109], v8 offset:24576
v_cvt_pk_bf16_f32 v164, v158, v159
v_mfma_f32_16x16x32_bf16 v[98:101], a[144:147], v[130:133], 0
ds_read_b128 v[110:113], v8 offset:25600
v_cvt_pk_bf16_f32 v165, v160, v161
v_mfma_f32_16x16x32_bf16 v[98:101], a[152:155], v[134:137], v[98:101]
v_mfma_f32_16x16x32_bf16 v[98:101], a[160:163], v[138:141], v[98:101]
s_waitcnt vmcnt(6)
s_barrier
v_mfma_f32_16x16x32_bf16 v[98:101], a[168:171], v[142:145], v[98:101]
s_mov_b64 exec, 1
global_store_dword v252, v7, s[38:39] sc1
s_mov_b64 exec, -1
v_mfma_f32_16x16x32_bf16 v[98:101], a[176:179], v[146:149], v[98:101]
v_add_u32 v7, 1, v7
v_add_lshl_u32 v252, v7, 0, 7
v_mfma_f32_16x16x32_bf16 v[98:101], a[184:187], v[150:153], v[98:101]
ds_read_b128 v[114:117], v8 offset:26624
v_bfi_b32 v252, s69, v252, v6
v_mfma_f32_16x16x32_bf16 v[94:97], a[4:7], v[130:133], 0
global_store_dwordx4 v3, v[162:165], s[38:39] sc1
v_mfma_f32_16x16x32_bf16 v[94:97], a[12:15], v[134:137], v[94:97]
v_add_u32 v3, 147456, v3
v_mfma_f32_16x16x32_bf16 v[94:97], a[20:23], v[138:141], v[94:97]
ds_read_b128 v[118:121], v8 offset:27648
v_mfma_f32_16x16x32_bf16 v[94:97], a[28:31], v[142:145], v[94:97]
v_mfma_f32_16x16x32_bf16 v[94:97], a[36:39], v[146:149], v[94:97]
v_mfma_f32_16x16x32_bf16 v[94:97], a[44:47], v[150:153], v[94:97]
ds_read_b128 v[122:125], v8 offset:28672
v_mfma_f32_16x16x32_bf16 v[102:105], a[148:151], v[130:133], 0
v_mfma_f32_16x16x32_bf16 v[102:105], a[156:159], v[134:137], v[102:105]
v_mfma_f32_16x16x32_bf16 v[102:105], a[164:167], v[138:141], v[102:105]
ds_read_b128 v[126:129], v8 offset:29696
v_mfma_f32_16x16x32_bf16 v[102:105], a[172:175], v[142:145], v[102:105]
v_mfma_f32_16x16x32_bf16 v[102:105], a[180:183], v[146:149], v[102:105]
v_mfma_f32_16x16x32_bf16 v[102:105], a[188:191], v[150:153], v[102:105]
s_waitcnt lgkmcnt(5)
v_mfma_f32_16x16x32_bf16 v[90:93], a[48:51], v[106:109], v[90:93]
s_waitcnt lgkmcnt(4)
v_mfma_f32_16x16x32_bf16 v[90:93], a[56:59], v[110:113], v[90:93]
s_waitcnt lgkmcnt(3)
v_mfma_f32_16x16x32_bf16 v[90:93], a[64:67], v[114:117], v[90:93]
s_waitcnt lgkmcnt(2)
v_mfma_f32_16x16x32_bf16 v[90:93], a[72:75], v[118:121], v[90:93]
s_waitcnt lgkmcnt(1)
v_mfma_f32_16x16x32_bf16 v[90:93], a[80:83], v[122:125], v[90:93]
s_waitcnt lgkmcnt(0)
v_mfma_f32_16x16x32_bf16 v[90:93], a[88:91], v[126:129], v[90:93]
ds_read_b128 v[130:133], v8 offset:30720
v_mfma_f32_16x16x32_bf16 v[98:101], a[192:195], v[106:109], v[98:101]
v_mfma_f32_16x16x32_bf16 v[98:101], a[200:203], v[110:113], v[98:101]
v_mfma_f32_16x16x32_bf16 v[98:101], a[208:211], v[114:117], v[98:101]
ds_read_b128 v[134:137], v8 offset:31744
v_mfma_f32_16x16x32_bf16 v[98:101], a[216:219], v[118:121], v[98:101]
v_mfma_f32_16x16x32_bf16 v[98:101], a[224:227], v[122:125], v[98:101]
v_mfma_f32_16x16x32_bf16 v[98:101], a[232:235], v[126:129], v[98:101]
ds_read_b128 v[138:141], v8 offset:32768
v_mfma_f32_16x16x32_bf16 v[94:97], a[52:55], v[106:109], v[94:97]
v_mfma_f32_16x16x32_bf16 v[94:97], a[60:63], v[110:113], v[94:97]
v_mfma_f32_16x16x32_bf16 v[94:97], a[68:71], v[114:117], v[94:97]
ds_read_b128 v[142:145], v8 offset:33792
v_mfma_f32_16x16x32_bf16 v[94:97], a[76:79], v[118:121], v[94:97]
v_mfma_f32_16x16x32_bf16 v[94:97], a[84:87], v[122:125], v[94:97]
v_mfma_f32_16x16x32_bf16 v[94:97], a[92:95], v[126:129], v[94:97]
ds_read_b128 v[146:149], v8 offset:34816
v_mfma_f32_16x16x32_bf16 v[102:105], a[196:199], v[106:109], v[102:105]
.Lgu_in8:
v_mfma_f32_16x16x32_bf16 v[102:105], a[204:207], v[110:113], v[102:105]
v_mfma_f32_16x16x32_bf16 v[102:105], a[212:215], v[114:117], v[102:105]
ds_read_b128 v[150:153], v8 offset:35840
v_mfma_f32_16x16x32_bf16 v[102:105], a[220:223], v[118:121], v[102:105]
v_mfma_f32_16x16x32_bf16 v[102:105], a[228:231], v[122:125], v[102:105]
s_add_u32 m0, s45, 18432
v_mfma_f32_16x16x32_bf16 v[102:105], a[236:239], v[126:129], v[102:105]
s_waitcnt lgkmcnt(5)
v_mfma_f32_16x16x32_bf16 v[90:93], a[96:99], v[130:133], v[90:93]
s_barrier
s_waitcnt lgkmcnt(4)
v_mfma_f32_16x16x32_bf16 v[90:93], a[104:107], v[134:137], v[90:93]
s_waitcnt lgkmcnt(3)
v_mfma_f32_16x16x32_bf16 v[90:93], a[112:115], v[138:141], v[90:93]
s_waitcnt lgkmcnt(2)
global_load_lds_dwordx4 v2, s[38:39] offset:-2048 sc1
v_mfma_f32_16x16x32_bf16 v[90:93], a[120:123], v[142:145], v[90:93]
s_waitcnt lgkmcnt(1)
v_mfma_f32_16x16x32_bf16 v[90:93], a[128:131], v[146:149], v[90:93]
s_waitcnt lgkmcnt(0)
v_mfma_f32_16x16x32_bf16 v[90:93], a[136:139], v[150:153], v[90:93]
global_load_lds_dwordx4 v2, s[38:39] offset:-1024 sc1
v_mfma_f32_16x16x32_bf16 v[98:101], v[26:29], v[130:133], v[98:101]
v_mfma_f32_16x16x32_bf16 v[98:101], v[34:37], v[134:137], v[98:101]
v_mfma_f32_16x16x32_bf16 v[98:101], v[42:45], v[138:141], v[98:101]
global_load_lds_dwordx4 v2, s[38:39] offset:0 sc1
v_mfma_f32_16x16x32_bf16 v[98:101], v[50:53], v[142:145], v[98:101]
v_xor_b32 v8, 0x10000, v8
v_mfma_f32_16x16x32_bf16 v[98:101], v[58:61], v[146:149], v[98:101]
v_mfma_f32_16x16x32_bf16 v[98:101], v[66:69], v[150:153], v[98:101]
v_mfma_f32_16x16x32_bf16 v[94:97], a[100:103], v[130:133], v[94:97]
s_waitcnt vmcnt(5)
s_barrier
v_mfma_f32_16x16x32_bf16 v[94:97], a[108:111], v[134:137], v[94:97]
global_load_lds_dwordx4 v2, s[38:39] offset:1024 sc1
v_mfma_f32_16x16x32_bf16 v[94:97], a[116:119], v[138:141], v[94:97]
v_mfma_f32_16x16x32_bf16 v[94:97], a[124:127], v[142:145], v[94:97]
ds_read_b128 v[106:109], v8 offset:0
v_mfma_f32_16x16x32_bf16 v[94:97], a[132:135], v[146:149], v[94:97]
ds_read_b128 v[110:113], v8 offset:1024
global_load_lds_dwordx4 v2, s[38:39] offset:2048 sc1
v_mfma_f32_16x16x32_bf16 v[94:97], a[140:143], v[150:153], v[94:97]
ds_read_b128 v[114:117], v8 offset:2048
v_mul_f32 v154, v90, v98
v_mfma_f32_16x16x32_bf16 v[102:105], v[30:33], v[130:133], v[102:105]
ds_read_b128 v[118:121], v8 offset:3072
v_mul_f32 v155, v91, v99
v_mfma_f32_16x16x32_bf16 v[102:105], v[38:41], v[134:137], v[102:105]
ds_read_b128 v[122:125], v8 offset:4096
global_load_lds_dwordx4 v2, s[38:39] offset:3072 sc1
v_mfma_f32_16x16x32_bf16 v[102:105], v[46:49], v[138:141], v[102:105]
ds_read_b128 v[126:129], v8 offset:5120
v_add_u32 v2, 18688, v2
v_mfma_f32_16x16x32_bf16 v[102:105], v[54:57], v[142:145], v[102:105]
v_min_u32 v2, v2, v9
v_mul_f32 v156, v92, v100
v_mfma_f32_16x16x32_bf16 v[102:105], v[62:65], v[146:149], v[102:105]
v_mfma_f32_16x16x32_bf16 v[102:105], v[70:73], v[150:153], v[102:105]
s_sub_u32 s70, s70, 1
s_cbranch_scc0 .Lgu_top9
s_waitcnt vmcnt(6) lgkmcnt(0)
s_barrier
s_mov_b64 exec, 1
global_store_dword v252, v7, s[38:39] sc1
s_mov_b64 exec, -1
v_mul_f32 v154, v90, v98
v_mul_f32 v155, v91, v99
v_mul_f32 v156, v92, v100
v_mul_f32 v157, v93, v101
v_cvt_pk_bf16_f32 v162, v154, v155
v_cvt_pk_bf16_f32 v163, v156, v157
v_mul_f32 v158, v94, v102
v_mul_f32 v159, v95, v103
v_mul_f32 v160, v96, v104
v_mul_f32 v161, v97, v105
v_cvt_pk_bf16_f32 v164, v158, v159
v_cvt_pk_bf16_f32 v165, v160, v161
global_store_dwordx4 v3, v[162:165], s[38:39] sc1
v_mov_b32 v231, 192
ds_read_b32 v231, v231 offset:36864
s_waitcnt vmcnt(0)
s_barrier
v_add_u32 v7, 8, v7
v_mbcnt_lo_u32_b32 v252, -1, 0
v_mbcnt_hi_u32_b32 v252, -1, v252
v_add_lshl_u32 v252, v252, 0, 7
v_bfi_b32 v252, s69, v252, v6
s_mov_b64 exec, 0xff
global_store_dword v252, v7, s[38:39] sc1
s_mov_b64 exec, -1
s_waitcnt lgkmcnt(0)
v_readfirstlane_b32 s62, v231
s_nop 3
s_max_i32 s62, s62, 2
s_min_i32 s62, s62, 66
s_add_u32 s62, s62, -1
s_branch .Lgu_fin
.Lgu_w3:
v_add_u32 v254, -1, v5
v_add_u32 v255, -1, v5
v_add_lshl_u32 v252, v7, 0, 7
v_bfi_b32 v252, s69, v252, v6
v_add_lshl_u32 v253, v5, 0, 7
v_bfi_b32 v253, s69, v253, v4
s_add_u32 m0, s45, 18432
v_add_lshl_u32 v253, v5, 0, 7
v_bfi_b32 v253, s69, v253, v4
global_load_dword v254, v253, s[38:39] sc1
s_waitcnt vmcnt(0)
v_sub_u32 v254, v254, v5
v_cmp_lt_u32 vcc, 0x7f, v254
s_cbranch_vccnz .Lgu_sp16
.Lgu_rt17:
v_add_u32 v5, 1, v5
s_xor_b32 s45, s45, 0x10000
s_mov_b32 m0, s45
s_barrier
global_load_lds_dwordx4 v2, s[38:39] offset:-2048 sc1
global_load_lds_dwordx4 v2, s[38:39] offset:-1024 sc1
global_load_lds_dwordx4 v2, s[38:39] offset:0 sc1
global_load_lds_dwordx4 v2, s[38:39] offset:1024 sc1
global_load_lds_dwordx4 v2, s[38:39] offset:2048 sc1
global_load_lds_dwordx4 v2, s[38:39] offset:3072 sc1
v_add_u32 v2, 18688, v2
s_mov_b32 s70, 32
s_waitcnt vmcnt(0) lgkmcnt(0)
s_nop 7
s_branch .Lgu_in15
.p2align 6
.Lgu_top18:
s_waitcnt lgkmcnt(5)
v_mfma_f32_16x16x32_bf16 v[74:77], a[0:3], v[106:109], 0
s_waitcnt lgkmcnt(4)
v_mul_f32 v157, v93, v101
v_cvt_pk_bf16_f32 v162, v154, v155
v_mfma_f32_16x16x32_bf16 v[74:77], a[8:11], v[110:113], v[74:77]
s_waitcnt lgkmcnt(3)
v_cvt_pk_bf16_f32 v163, v156, v157
v_mfma_f32_16x16x32_bf16 v[74:77], a[16:19], v[114:117], v[74:77]
s_waitcnt lgkmcnt(2)
global_load_dword v254, v253, s[38:39] sc1
v_mfma_f32_16x16x32_bf16 v[74:77], a[24:27], v[118:121], v[74:77]
s_waitcnt lgkmcnt(1)
v_mul_f32 v158, v94, v102
v_mul_f32 v159, v95, v103
v_mfma_f32_16x16x32_bf16 v[74:77], a[32:35], v[122:125], v[74:77]
s_waitcnt lgkmcnt(0)
v_mul_f32 v160, v96, v104
v_mul_f32 v161, v97, v105
v_mfma_f32_16x16x32_bf16 v[74:77], a[40:43], v[126:129], v[74:77]
ds_read_b128 v[130:133], v8 offset:6144
v_cvt_pk_bf16_f32 v164, v158, v159
v_mfma_f32_16x16x32_bf16 v[82:85], a[144:147], v[106:109], 0
ds_read_b128 v[134:137], v8 offset:7168
v_cvt_pk_bf16_f32 v165, v160, v161
v_mfma_f32_16x16x32_bf16 v[82:85], a[152:155], v[110:113], v[82:85]
v_mfma_f32_16x16x32_bf16 v[82:85], a[160:163], v[114:117], v[82:85]
s_waitcnt vmcnt(7)
s_barrier
v_mfma_f32_16x16x32_bf16 v[82:85], a[168:171], v[118:121], v[82:85]
v_mfma_f32_16x16x32_bf16 v[82:85], a[176:179], v[122:125], v[82:85]
v_mfma_f32_16x16x32_bf16 v[82:85], a[184:187], v[126:129], v[82:85]
ds_read_b128 v[138:141], v8 offset:8192
v_mfma_f32_16x16x32_bf16 v[78:81], a[4:7], v[106:109], 0
global_store_dwordx4 v3, v[162:165], s[38:39] sc1
v_mfma_f32_16x16x32_bf16 v[78:81], a[12:15], v[110:113], v[78:81]
v_add_u32 v3, 147456, v3
v_mfma_f32_16x16x32_bf16 v[78:81], a[20:23], v[114:117], v[78:81]
ds_read_b128 v[142:145], v8 offset:9216
v_mfma_f32_16x16x32_bf16 v[78:81], a[28:31], v[118:121], v[78:81]
v_mfma_f32_16x16x32_bf16 v[78:81], a[36:39], v[122:125], v[78:81]
v_mfma_f32_16x16x32_bf16 v[78:81], a[44:47], v[126:129], v[78:81]
ds_read_b128 v[146:149], v8 offset:10240
v_mfma_f32_16x16x32_bf16 v[86:89], a[148:151], v[106:109], 0
v_mfma_f32_16x16x32_bf16 v[86:89], a[156:159], v[110:113], v[86:89]
v_mfma_f32_16x16x32_bf16 v[86:89], a[164:167], v[114:117], v[86:89]
ds_read_b128 v[150:153], v8 offset:11264
v_mfma_f32_16x16x32_bf16 v[86:89], a[172:175], v[118:121], v[86:89]
v_mfma_f32_16x16x32_bf16 v[86:89], a[180:183], v[122:125], v[86:89]
v_mfma_f32_16x16x32_bf16 v[86:89], a[188:191], v[126:129], v[86:89]
s_waitcnt lgkmcnt(5)
v_mfma_f32_16x16x32_bf16 v[74:77], a[48:51], v[130:133], v[74:77]
s_waitcnt lgkmcnt(4)
v_mfma_f32_16x16x32_bf16 v[74:77], a[56:59], v[134:137], v[74:77]
s_waitcnt lgkmcnt(3)
v_mfma_f32_16x16x32_bf16 v[74:77], a[64:67], v[138:141], v[74:77]
s_waitcnt lgkmcnt(2)
v_mfma_f32_16x16x32_bf16 v[74:77], a[72:75], v[142:145], v[74:77]
s_waitcnt lgkmcnt(1)
v_mfma_f32_16x16x32_bf16 v[74:77], a[80:83], v[146:149], v[74:77]
s_waitcnt lgkmcnt(0)
v_mfma_f32_16x16x32_bf16 v[74:77], a[88:91], v[150:153], v[74:77]
ds_read_b128 v[106:109], v8 offset:12288
v_mfma_f32_16x16x32_bf16 v[82:85], a[192:195], v[130:133], v[82:85]
v_mfma_f32_16x16x32_bf16 v[82:85], a[200:203], v[134:137], v[82:85]
v_mfma_f32_16x16x32_bf16 v[82:85], a[208:211], v[138:141], v[82:85]
ds_read_b128 v[110:113], v8 offset:13312
v_mfma_f32_16x16x32_bf16 v[82:85], a[216:219], v[142:145], v[82:85]
v_mfma_f32_16x16x32_bf16 v[82:85], a[224:227], v[146:149], v[82:85]
v_mfma_f32_16x16x32_bf16 v[82:85], a[232:235], v[150:153], v[82:85]
ds_read_b128 v[114:117], v8 offset:14336
v_mfma_f32_16x16x32_bf16 v[78:81], a[52:55], v[130:133], v[78:81]
v_mfma_f32_16x16x32_bf16 v[78:81], a[60:63], v[134:137], v[78:81]
v_mfma_f32_16x16x32_bf16 v[78:81], a[68:71], v[138:141], v[78:81]
ds_read_b128 v[118:121], v8 offset:15360
v_mfma_f32_16x16x32_bf16 v[78:81], a[76:79], v[142:145], v[78:81]
v_mfma_f32_16x16x32_bf16 v[78:81], a[84:87], v[146:149], v[78:81]
v_mfma_f32_16x16x32_bf16 v[78:81], a[92:95], v[150:153], v[78:81]
ds_read_b128 v[122:125], v8 offset:16384
v_mfma_f32_16x16x32_bf16 v[86:89], a[196:199], v[130:133], v[86:89]
v_mfma_f32_16x16x32_bf16 v[86:89], a[204:207], v[134:137], v[86:89]
s_waitcnt vmcnt(1)
v_sub_u32 v254, v254, v5
v_cmp_lt_u32 vcc, 0x7f, v254
v_mfma_f32_16x16x32_bf16 v[86:89], a[212:215], v[138:141], v[86:89]
ds_read_b128 v[126:129], v8 offset:17408
v_mfma_f32_16x16x32_bf16 v[86:89], a[220:223], v[142:145], v[86:89]
s_xor_b32 s45, s45, 0x10000
v_mfma_f32_16x16x32_bf16 v[86:89], a[228:231], v[146:149], v[86:89]
s_cbranch_vccnz .Lgu_sp11
.Lgu_rt12:
s_add_u32 m0, s45, 0
v_mfma_f32_16x16x32_bf16 v[86:89], a[236:239], v[150:153], v[86:89]
s_waitcnt lgkmcnt(5)
v_mfma_f32_16x16x32_bf16 v[74:77], a[96:99], v[106:109], v[74:77]
s_barrier
s_waitcnt lgkmcnt(4)
v_mfma_f32_16x16x32_bf16 v[74:77], a[104:107], v[110:113], v[74:77]
s_waitcnt lgkmcnt(3)
v_add_u32 v5, 1, v5
v_add_lshl_u32 v253, v5, 0, 7
v_mfma_f32_16x16x32_bf16 v[74:77], a[112:115], v[114:117], v[74:77]
s_waitcnt lgkmcnt(2)
v_bfi_b32 v253, s69, v253, v4
v_mfma_f32_16x16x32_bf16 v[74:77], a[120:123], v[118:121], v[74:77]
s_waitcnt lgkmcnt(1)
global_load_lds_dwordx4 v2, s[38:39] offset:-2048 sc1
v_mfma_f32_16x16x32_bf16 v[74:77], a[128:131], v[122:125], v[74:77]
s_waitcnt lgkmcnt(0)
v_mfma_f32_16x16x32_bf16 v[74:77], a[136:139], v[126:129], v[74:77]
v_mfma_f32_16x16x32_bf16 v[82:85], v[26:29], v[106:109], v[82:85]
global_load_lds_dwordx4 v2, s[38:39] offset:-1024 sc1
v_mfma_f32_16x16x32_bf16 v[82:85], v[34:37], v[110:113], v[82:85]
v_mfma_f32_16x16x32_bf16 v[82:85], v[42:45], v[114:117], v[82:85]
v_mfma_f32_16x16x32_bf16 v[82:85], v[50:53], v[118:121], v[82:85]
global_load_lds_dwordx4 v2, s[38:39] offset:0 sc1
v_mfma_f32_16x16x32_bf16 v[82:85], v[58:61], v[122:125], v[82:85]
v_mfma_f32_16x16x32_bf16 v[82:85], v[66:69], v[126:129], v[82:85]
v_mfma_f32_16x16x32_bf16 v[78:81], a[100:103], v[106:109], v[78:81]
s_barrier
v_mfma_f32_16x16x32_bf16 v[78:81], a[108:111], v[110:113], v[78:81]
global_load_lds_dwordx4 v2, s[38:39] offset:1024 sc1
v_mfma_f32_16x16x32_bf16 v[78:81], a[116:119], v[114:117], v[78:81]
v_mfma_f32_16x16x32_bf16 v[78:81], a[124:127], v[118:121], v[78:81]
ds_read_b128 v[130:133], v8 offset:18432
v_mfma_f32_16x16x32_bf16 v[78:81], a[132:135], v[122:125], v[78:81]
ds_read_b128 v[134:137], v8 offset:19456
global_load_lds_dwordx4 v2, s[38:39] offset:2048 sc1
v_mfma_f32_16x16x32_bf16 v[78:81], a[140:143], v[126:129], v[78:81]
ds_read_b128 v[138:141], v8 offset:20480
v_mul_f32 v154, v74, v82
v_mfma_f32_16x16x32_bf16 v[86:89], v[30:33], v[106:109], v[86:89]
ds_read_b128 v[142:145], v8 offset:21504
v_mul_f32 v155, v75, v83
v_mfma_f32_16x16x32_bf16 v[86:89], v[38:41], v[110:113], v[86:89]
ds_read_b128 v[146:149], v8 offset:22528
global_load_lds_dwordx4 v2, s[38:39] offset:3072 sc1
v_mfma_f32_16x16x32_bf16 v[86:89], v[46:49], v[114:117], v[86:89]
ds_read_b128 v[150:153], v8 offset:23552
v_add_u32 v2, 18688, v2
v_mfma_f32_16x16x32_bf16 v[86:89], v[54:57], v[118:121], v[86:89]
v_min_u32 v2, v2, v9
v_mul_f32 v156, v76, v84
v_mfma_f32_16x16x32_bf16 v[86:89], v[62:65], v[122:125], v[86:89]
v_mul_f32 v157, v77, v85
v_cvt_pk_bf16_f32 v162, v154, v155
v_mfma_f32_16x16x32_bf16 v[86:89], v[70:73], v[126:129], v[86:89]
s_waitcnt lgkmcnt(5)
v_cvt_pk_bf16_f32 v163, v156, v157
v_mfma_f32_16x16x32_bf16 v[90:93], a[0:3], v[130:133], 0
s_waitcnt lgkmcnt(4)
v_mfma_f32_16x16x32_bf16 v[90:93], a[8:11], v[134:137], v[90:93]
s_waitcnt lgkmcnt(3)
v_mfma_f32_16x16x32_bf16 v[90:93], a[16:19], v[138:141], v[90:93]
s_waitcnt lgkmcnt(2)
global_load_dword v255, v253, s[38:39] sc1
v_mfma_f32_16x16x32_bf16 v[90:93], a[24:27], v[142:145], v[90:93]
s_waitcnt lgkmcnt(1)
v_mul_f32 v158, v78, v86
v_mul_f32 v159, v79, v87
v_mfma_f32_16x16x32_bf16 v[90:93], a[32:35], v[146:149], v[90:93]
s_waitcnt lgkmcnt(0)
v_mul_f32 v160, v80, v88
v_mul_f32 v161, v81, v89
v_mfma_f32_16x16x32_bf16 v[90:93], a[40:43], v[150:153], v[90:93]
ds_read_b128 v[106:109], v8 offset:24576
v_cvt_pk_bf16_f32 v164, v158, v159
v_mfma_f32_16x16x32_bf16 v[98:101], a[144:147], v[130:133], 0
ds_read_b128 v[110:113], v8 offset:25600
v_cvt_pk_bf16_f32 v165, v160, v161
v_mfma_f32_16x16x32_bf16 v[98:101], a[152:155], v[134:137], v[98:101]
v_mfma_f32_16x16x32_bf16 v[98:101], a[160:163], v[138:141], v[98:101]
s_waitcnt vmcnt(7)
s_barrier
v_mfma_f32_16x16x32_bf16 v[98:101], a[168:171], v[142:145], v[98:101]
v_mfma_f32_16x16x32_bf16 v[98:101], a[176:179], v[146:149], v[98:101]
v_mfma_f32_16x16x32_bf16 v[98:101], a[184:187], v[150:153], v[98:101]
ds_read_b128 v[114:117], v8 offset:26624
v_mfma_f32_16x16x32_bf16 v[94:97], a[4:7], v[130:133], 0
global_store_dwordx4 v3, v[162:165], s[38:39] sc1
v_mfma_f32_16x16x32_bf16 v[94:97], a[12:15], v[134:137], v[94:97]
v_add_u32 v3, 147456, v3
v_mfma_f32_16x16x32_bf16 v[94:97], a[20:23], v[138:141], v[94:97]
ds_read_b128 v[118:121], v8 offset:27648
v_mfma_f32_16x16x32_bf16 v[94:97], a[28:31], v[142:145], v[94:97]
)"
R"(v_mfma_f32_16x16x32_bf16 v[94:97], a[36:39], v[146:149], v[94:97]
v_mfma_f32_16x16x32_bf16 v[94:97], a[44:47], v[150:153], v[94:97]
ds_read_b128 v[122:125], v8 offset:28672
v_mfma_f32_16x16x32_bf16 v[102:105], a[148:151], v[130:133], 0
v_mfma_f32_16x16x32_bf16 v[102:105], a[156:159], v[134:137], v[102:105]
v_mfma_f32_16x16x32_bf16 v[102:105], a[164:167], v[138:141], v[102:105]
ds_read_b128 v[126:129], v8 offset:29696
v_mfma_f32_16x16x32_bf16 v[102:105], a[172:175], v[142:145], v[102:105]
v_mfma_f32_16x16x32_bf16 v[102:105], a[180:183], v[146:149], v[102:105]
v_mfma_f32_16x16x32_bf16 v[102:105], a[188:191], v[150:153], v[102:105]
s_waitcnt lgkmcnt(5)
v_mfma_f32_16x16x32_bf16 v[90:93], a[48:51], v[106:109], v[90:93]
s_waitcnt lgkmcnt(4)
v_mfma_f32_16x16x32_bf16 v[90:93], a[56:59], v[110:113], v[90:93]
s_waitcnt lgkmcnt(3)
v_mfma_f32_16x16x32_bf16 v[90:93], a[64:67], v[114:117], v[90:93]
s_waitcnt lgkmcnt(2)
v_mfma_f32_16x16x32_bf16 v[90:93], a[72:75], v[118:121], v[90:93]
s_waitcnt lgkmcnt(1)
v_mfma_f32_16x16x32_bf16 v[90:93], a[80:83], v[122:125], v[90:93]
s_waitcnt lgkmcnt(0)
v_mfma_f32_16x16x32_bf16 v[90:93], a[88:91], v[126:129], v[90:93]
ds_read_b128 v[130:133], v8 offset:30720
v_mfma_f32_16x16x32_bf16 v[98:101], a[192:195], v[106:109], v[98:101]
v_mfma_f32_16x16x32_bf16 v[98:101], a[200:203], v[110:113], v[98:101]
v_mfma_f32_16x16x32_bf16 v[98:101], a[208:211], v[114:117], v[98:101]
ds_read_b128 v[134:137], v8 offset:31744
v_mfma_f32_16x16x32_bf16 v[98:101], a[216:219], v[118:121], v[98:101]
v_mfma_f32_16x16x32_bf16 v[98:101], a[224:227], v[122:125], v[98:101]
v_mfma_f32_16x16x32_bf16 v[98:101], a[232:235], v[126:129], v[98:101]
ds_read_b128 v[138:141], v8 offset:32768
v_mfma_f32_16x16x32_bf16 v[94:97], a[52:55], v[106:109], v[94:97]
v_mfma_f32_16x16x32_bf16 v[94:97], a[60:63], v[110:113], v[94:97]
v_mfma_f32_16x16x32_bf16 v[94:97], a[68:71], v[114:117], v[94:97]
ds_read_b128 v[142:145], v8 offset:33792
v_mfma_f32_16x16x32_bf16 v[94:97], a[76:79], v[118:121], v[94:97]
v_mfma_f32_16x16x32_bf16 v[94:97], a[84:87], v[122:125], v[94:97]
v_mfma_f32_16x16x32_bf16 v[94:97], a[92:95], v[126:129], v[94:97]
ds_read_b128 v[146:149], v8 offset:34816
v_mfma_f32_16x16x32_bf16 v[102:105], a[196:199], v[106:109], v[102:105]
.Lgu_in15:
v_mfma_f32_16x16x32_bf16 v[102:105], a[204:207], v[110:113], v[102:105]
s_waitcnt vmcnt(1)
v_sub_u32 v255, v255, v5
v_cmp_lt_u32 vcc, 0x7f, v255
v_mfma_f32_16x16x32_bf16 v[102:105], a[212:215], v[114:117], v[102:105]
ds_read_b128 v[150:153], v8 offset:35840
v_mfma_f32_16x16x32_bf16 v[102:105], a[220:223], v[118:121], v[102:105]
v_mfma_f32_16x16x32_bf16 v[102:105], a[228:231], v[122:125], v[102:105]
s_cbranch_vccnz .Lgu_sp13
.Lgu_rt14:
s_add_u32 m0, s45, 18432
v_mfma_f32_16x16x32_bf16 v[102:105], a[236:239], v[126:129], v[102:105]
s_waitcnt lgkmcnt(5)
v_mfma_f32_16x16x32_bf16 v[90:93], a[96:99], v[130:133], v[90:93]
s_barrier
s_waitcnt lgkmcnt(4)
v_mfma_f32_16x16x32_bf16 v[90:93], a[104:107], v[134:137], v[90:93]
s_waitcnt lgkmcnt(3)
v_add_u32 v5, 1, v5
v_add_lshl_u32 v253, v5, 0, 7
v_mfma_f32_16x16x32_bf16 v[90:93], a[112:115], v[138:141], v[90:93]
s_waitcnt lgkmcnt(2)
v_bfi_b32 v253, s69, v253, v4
v_mfma_f32_16x16x32_bf16 v[90:93], a[120:123], v[142:145], v[90:93]
s_waitcnt lgkmcnt(1)
global_load_lds_dwordx4 v2, s[38:39] offset:-2048 sc1
v_mfma_f32_16x16x32_bf16 v[90:93], a[128:131], v[146:149], v[90:93]
s_waitcnt lgkmcnt(0)
v_mfma_f32_16x16x32_bf16 v[90:93], a[136:139], v[150:153], v[90:93]
v_mfma_f32_16x16x32_bf16 v[98:101], v[26:29], v[130:133], v[98:101]
global_load_lds_dwordx4 v2, s[38:39] offset:-1024 sc1
v_mfma_f32_16x16x32_bf16 v[98:101], v[34:37], v[134:137], v[98:101]
v_mfma_f32_16x16x32_bf16 v[98:101], v[42:45], v[138:141], v[98:101]
v_mfma_f32_16x16x32_bf16 v[98:101], v[50:53], v[142:145], v[98:101]
v_xor_b32 v8, 0x10000, v8
global_load_lds_dwordx4 v2, s[38:39] offset:0 sc1
v_mfma_f32_16x16x32_bf16 v[98:101], v[58:61], v[146:149], v[98:101]
v_mfma_f32_16x16x32_bf16 v[98:101], v[66:69], v[150:153], v[98:101]
v_mfma_f32_16x16x32_bf16 v[94:97], a[100:103], v[130:133], v[94:97]
s_barrier
v_mfma_f32_16x16x32_bf16 v[94:97], a[108:111], v[134:137], v[94:97]
global_load_lds_dwordx4 v2, s[38:39] offset:1024 sc1
v_mfma_f32_16x16x32_bf16 v[94:97], a[116:119], v[138:141], v[94:97]
v_mfma_f32_16x16x32_bf16 v[94:97], a[124:127], v[142:145], v[94:97]
ds_read_b128 v[106:109], v8 offset:0
v_mfma_f32_16x16x32_bf16 v[94:97], a[132:135], v[146:149], v[94:97]
ds_read_b128 v[110:113], v8 offset:1024
global_load_lds_dwordx4 v2, s[38:39] offset:2048 sc1
v_mfma_f32_16x16x32_bf16 v[94:97], a[140:143], v[150:153], v[94:97]
ds_read_b128 v[114:117], v8 offset:2048
v_mul_f32 v154, v90, v98
v_mfma_f32_16x16x32_bf16 v[102:105], v[30:33], v[130:133], v[102:105]
ds_read_b128 v[118:121], v8 offset:3072
v_mul_f32 v155, v91, v99
v_mfma_f32_16x16x32_bf16 v[102:105], v[38:41], v[134:137], v[102:105]
ds_read_b128 v[122:125], v8 offset:4096
global_load_lds_dwordx4 v2, s[38:39] offset:3072 sc1
v_mfma_f32_16x16x32_bf16 v[102:105], v[46:49], v[138:141], v[102:105]
ds_read_b128 v[126:129], v8 offset:5120
v_add_u32 v2, 18688, v2
v_mfma_f32_16x16x32_bf16 v[102:105], v[54:57], v[142:145], v[102:105]
v_min_u32 v2, v2, v9
v_mul_f32 v156, v92, v100
v_mfma_f32_16x16x32_bf16 v[102:105], v[62:65], v[146:149], v[102:105]
v_mfma_f32_16x16x32_bf16 v[102:105], v[70:73], v[150:153], v[102:105]
s_sub_u32 s70, s70, 1
s_cbranch_scc0 .Lgu_top18
s_waitcnt vmcnt(6) lgkmcnt(0)
s_barrier
v_mul_f32 v154, v90, v98
v_mul_f32 v155, v91, v99
v_mul_f32 v156, v92, v100
v_mul_f32 v157, v93, v101
v_cvt_pk_bf16_f32 v162, v154, v155
v_cvt_pk_bf16_f32 v163, v156, v157
v_mul_f32 v158, v94, v102
v_mul_f32 v159, v95, v103
v_mul_f32 v160, v96, v104
v_mul_f32 v161, v97, v105
v_cvt_pk_bf16_f32 v164, v158, v159
v_cvt_pk_bf16_f32 v165, v160, v161
global_store_dwordx4 v3, v[162:165], s[38:39] sc1
v_mov_b32 v231, 192
ds_read_b32 v231, v231 offset:36864
s_waitcnt vmcnt(0)
s_barrier
s_waitcnt lgkmcnt(0)
v_readfirstlane_b32 s62, v231
s_nop 3
s_max_i32 s62, s62, 2
s_min_i32 s62, s62, 66
s_add_u32 s62, s62, 0
s_branch .Lgu_fin
.Lgu_w4:
s_add_u32 s84, s54, 18688
s_addc_u32 s85, s55, 0
s_add_u32 s86, s52, 110592
s_addc_u32 s87, s53, 0
s_mov_b32 s80, 0
s_mov_b64 s[76:77], 0
s_mov_b32 s72, -14
v_add_u32 v230, -1, v17
v_add_u32 v226, -1, s37
v_add_u32 v227, -1, s37
v_add_u32 v228, -1, s37
v_add_u32 v229, -1, s37
v_add_lshl_u32 v252, v228, 0, 7
v_bfi_b32 v252, s69, v252, v18
v_add_lshl_u32 v253, v17, 0, 7
v_bfi_b32 v253, s69, v253, v16
v_add_u32 v254, -1, v5
v_add_u32 v255, -1, v5
s_add_u32 m0, s45, 18432
s_xor_b32 s45, s45, 0x10000
s_mov_b32 m0, s45
s_barrier
v_add_u32 v2, 18688, v2
s_mov_b32 s70, 32
s_waitcnt vmcnt(0) lgkmcnt(0)
s_nop 7
s_branch .Lgu_in24
.p2align 6
.Lgu_top25:
s_waitcnt lgkmcnt(5)
v_mfma_f32_16x16x32_bf16 v[74:77], a[0:3], v[106:109], 0
s_waitcnt lgkmcnt(4)
global_load_dword v230, v253, s[38:39] sc1
v_lshlrev_b32 v216, 16, v187
v_mfma_f32_16x16x32_bf16 v[74:77], a[8:11], v[110:113], v[74:77]
s_waitcnt lgkmcnt(3)
v_and_b32 v217, 0xffff0000, v187
v_add_f32_dpp v206, v210, v206 row_ror:8 row_mask:0xf bank_mask:0xf
v_mfma_f32_16x16x32_bf16 v[74:77], a[16:19], v[114:117], v[74:77]
s_waitcnt lgkmcnt(2)
v_add_f32_dpp v207, v211, v207 row_ror:8 row_mask:0xf bank_mask:0xf
v_add_f32_dpp v208, v212, v208 row_ror:8 row_mask:0xf bank_mask:0xf
v_mfma_f32_16x16x32_bf16 v[74:77], a[24:27], v[118:121], v[74:77]
s_waitcnt lgkmcnt(1)
v_mul_f32 v158, v94, v102
v_mul_f32 v159, v95, v103
v_mfma_f32_16x16x32_bf16 v[74:77], a[32:35], v[122:125], v[74:77]
s_waitcnt lgkmcnt(0)
v_mul_f32 v160, v96, v104
v_mul_f32 v161, v97, v105
v_mfma_f32_16x16x32_bf16 v[74:77], a[40:43], v[126:129], v[74:77]
ds_read_b128 v[130:133], v8 offset:6144
v_cvt_pk_bf16_f32 v164, v158, v159
v_mfma_f32_16x16x32_bf16 v[82:85], a[144:147], v[106:109], 0
ds_read_b128 v[134:137], v8 offset:7168
v_cvt_pk_bf16_f32 v165, v160, v161
v_mfma_f32_16x16x32_bf16 v[82:85], a[152:155], v[110:113], v[82:85]
v_mfma_f32_16x16x32_bf16 v[82:85], a[160:163], v[114:117], v[82:85]
s_waitcnt vmcnt(8)
s_barrier
v_mfma_f32_16x16x32_bf16 v[82:85], a[168:171], v[118:121], v[82:85]
v_add_f32_dpp v209, v213, v209 row_ror:8 row_mask:0xf bank_mask:0xf
v_fma_f32 v206, v206, v231, v214
v_mfma_f32_16x16x32_bf16 v[82:85], a[176:179], v[122:125], v[82:85]
v_fma_f32 v207, v207, v231, v215
v_fma_f32 v208, v208, v231, v216
v_mfma_f32_16x16x32_bf16 v[82:85], a[184:187], v[126:129], v[82:85]
ds_read_b128 v[138:141], v8 offset:8192
v_fma_f32 v209, v209, v231, v217
v_mfma_f32_16x16x32_bf16 v[78:81], a[4:7], v[106:109], 0
v_cvt_pk_bf16_f32 v222, v206, v207
v_cvt_pk_bf16_f32 v223, v208, v209
v_mfma_f32_16x16x32_bf16 v[78:81], a[12:15], v[110:113], v[78:81]
global_store_dwordx4 v3, v[162:165], s[38:39] sc1
v_mfma_f32_16x16x32_bf16 v[78:81], a[20:23], v[114:117], v[78:81]
ds_read_b128 v[142:145], v8 offset:9216
v_add_u32 v3, 147456, v3
v_mfma_f32_16x16x32_bf16 v[78:81], a[28:31], v[118:121], v[78:81]
ds_write_b64 v14, v[222:223]
v_mfma_f32_16x16x32_bf16 v[78:81], a[36:39], v[122:125], v[78:81]
s_waitcnt vmcnt(8)
ds_read_b64 v[224:225], v15
s_mov_b64 exec, 1
global_store_dword v252, v229, s[38:39] sc1
s_mov_b64 exec, -1
v_mfma_f32_16x16x32_bf16 v[78:81], a[44:47], v[126:129], v[78:81]
ds_read_b128 v[146:149], v8 offset:10240
v_mfma_f32_16x16x32_bf16 v[86:89], a[148:151], v[106:109], 0
v_mfma_f32_16x16x32_bf16 v[86:89], a[156:159], v[110:113], v[86:89]
v_mfma_f32_16x16x32_bf16 v[86:89], a[164:167], v[114:117], v[86:89]
ds_read_b128 v[150:153], v8 offset:11264
v_mfma_f32_16x16x32_bf16 v[86:89], a[172:175], v[118:121], v[86:89]
v_mfma_f32_16x16x32_bf16 v[86:89], a[180:183], v[122:125], v[86:89]
v_mfma_f32_16x16x32_bf16 v[86:89], a[188:191], v[126:129], v[86:89]
s_waitcnt lgkmcnt(2)
global_store_dwordx2 v13, v[224:225], s[58:59] sc1
v_mov_b32 v228, v226
v_mfma_f32_16x16x32_bf16 v[74:77], a[48:51], v[130:133], v[74:77]
v_mfma_f32_16x16x32_bf16 v[74:77], a[56:59], v[134:137], v[74:77]
v_mfma_f32_16x16x32_bf16 v[74:77], a[64:67], v[138:141], v[74:77]
v_mfma_f32_16x16x32_bf16 v[74:77], a[72:75], v[142:145], v[74:77]
s_waitcnt vmcnt(8)
v_add_f32 v200, v200, v202
s_waitcnt vmcnt(7)
v_lshlrev_b32 v206, 16, v188
v_add_f32 v201, v201, v203
s_waitcnt lgkmcnt(1)
v_mfma_f32_16x16x32_bf16 v[74:77], a[80:83], v[146:149], v[74:77]
s_waitcnt lgkmcnt(0)
v_and_b32 v207, 0xffff0000, v188
v_mul_f32 v200, 0x3ae38e39, v200
v_lshlrev_b32 v208, 16, v189
v_mfma_f32_16x16x32_bf16 v[74:77], a[88:91], v[150:153], v[74:77]
ds_read_b128 v[106:109], v8 offset:12288
v_mul_f32 v201, 0x3ae38e39, v201
v_and_b32 v209, 0xffff0000, v189
v_mfma_f32_16x16x32_bf16 v[82:85], a[192:195], v[130:133], v[82:85]
v_fma_f32 v232, -v200, v200, v201
v_lshlrev_b32 v210, 16, v190
v_max_f32 v232, 0, v232
v_mfma_f32_16x16x32_bf16 v[82:85], a[200:203], v[134:137], v[82:85]
v_and_b32 v211, 0xffff0000, v190
v_add_f32 v232, 0x3727c5ac, v232
v_mfma_f32_16x16x32_bf16 v[82:85], a[208:211], v[138:141], v[82:85]
ds_read_b128 v[110:113], v8 offset:13312
v_lshlrev_b32 v212, 16, v191
v_mfma_f32_16x16x32_bf16 v[82:85], a[216:219], v[142:145], v[82:85]
s_waitcnt vmcnt(3)
v_sub_u32 v230, v230, v17
v_cmp_lt_u32 vcc, 0x7f, v230
v_mfma_f32_16x16x32_bf16 v[82:85], a[224:227], v[146:149], v[82:85]
v_rcp_f32 v231, v232
v_mfma_f32_16x16x32_bf16 v[82:85], a[232:235], v[150:153], v[82:85]
ds_read_b128 v[114:117], v8 offset:14336
v_and_b32 v213, 0xffff0000, v191
v_mfma_f32_16x16x32_bf16 v[78:81], a[52:55], v[130:133], v[78:81]
s_and_b64 s[88:89], vcc, s[76:77]
s_cbranch_scc1 .Lgu_sp20
.Lgu_rt21:
v_mfma_f32_16x16x32_bf16 v[78:81], a[60:63], v[134:137], v[78:81]
s_cmp_eq_u64 vcc, 0
v_lshlrev_b32 v214, 16, v192
v_and_b32 v215, 0xffff0000, v192
v_mfma_f32_16x16x32_bf16 v[78:81], a[68:71], v[138:141], v[78:81]
s_cselect_b64 s[54:55], s[84:85], s[54:55]
ds_read_b128 v[118:121], v8 offset:15360
v_lshlrev_b32 v216, 16, v193
v_mfma_f32_16x16x32_bf16 v[78:81], a[76:79], v[142:145], v[78:81]
s_cselect_b64 s[52:53], s[86:87], s[52:53]
global_load_dwordx2 v[182:183], v12, s[54:55] offset:0 sc1
v_and_b32 v217, 0xffff0000, v193
v_mfma_f32_16x16x32_bf16 v[78:81], a[84:87], v[146:149], v[78:81]
s_cselect_b64 s[58:59], s[56:57], s[50:51]
global_load_dwordx2 v[184:185], v12, s[54:55] offset:128 sc1
v_lshlrev_b32 v218, 16, v194
v_mfma_f32_16x16x32_bf16 v[78:81], a[92:95], v[150:153], v[78:81]
s_cselect_b32 s82, 1, 0
ds_read_b128 v[122:125], v8 offset:16384
global_load_dwordx4 v[170:173], v10, s[52:53] offset:0 sc1
v_mfma_f32_16x16x32_bf16 v[86:89], a[196:199], v[130:133], v[86:89]
s_add_u32 s62, s62, s82
global_load_dwordx4 v[174:177], v10, s[52:53] offset:1024 sc1
v_and_b32 v219, 0xffff0000, v194
v_mfma_f32_16x16x32_bf16 v[86:89], a[204:207], v[134:137], v[86:89]
s_add_u32 s83, s62, s37
global_load_dwordx4 v[178:181], v10, s[52:53] offset:2048 sc1
v_add_u32 v17, s82, v17
v_add_u32 v226, -1, s83
v_mfma_f32_16x16x32_bf16 v[86:89], a[212:215], v[138:141], v[86:89]
s_cmp_lt_i32 s62, 32
ds_read_b128 v[126:129], v8 offset:17408
global_load_dwordx2 v[186:187], v11, s[54:55] sc1
v_add_lshl_u32 v253, v17, 0, 7
v_mfma_f32_16x16x32_bf16 v[86:89], a[220:223], v[142:145], v[86:89]
s_cselect_b64 s[56:57], s[46:47], s[48:49]
v_bfi_b32 v253, s69, v253, v16
v_lshlrev_b32 v220, 16, v195
v_mfma_f32_16x16x32_bf16 v[86:89], a[228:231], v[146:149], v[86:89]
s_mul_i32 s80, s62, 18432
v_and_b32 v221, 0xffff0000, v195
v_add_f32 v206, v206, v214
v_mfma_f32_16x16x32_bf16 v[86:89], a[236:239], v[150:153], v[86:89]
s_waitcnt lgkmcnt(5)
v_mfma_f32_16x16x32_bf16 v[74:77], a[96:99], v[106:109], v[74:77]
s_barrier
s_waitcnt lgkmcnt(4)
v_mfma_f32_16x16x32_bf16 v[74:77], a[104:107], v[110:113], v[74:77]
s_waitcnt lgkmcnt(3)
v_add_f32 v207, v207, v215
v_add_f32 v208, v208, v216
v_mfma_f32_16x16x32_bf16 v[74:77], a[112:115], v[114:117], v[74:77]
s_waitcnt lgkmcnt(2)
v_add_f32 v209, v209, v217
v_add_f32 v210, v210, v218
v_mfma_f32_16x16x32_bf16 v[74:77], a[120:123], v[118:121], v[74:77]
s_waitcnt lgkmcnt(1)
v_add_f32 v211, v211, v219
v_add_f32 v212, v212, v220
v_mfma_f32_16x16x32_bf16 v[74:77], a[128:131], v[122:125], v[74:77]
s_waitcnt lgkmcnt(0)
v_add_f32 v213, v213, v221
v_lshlrev_b32 v214, 16, v196
v_mfma_f32_16x16x32_bf16 v[74:77], a[136:139], v[126:129], v[74:77]
s_sub_u32 s56, s56, s80
v_and_b32 v215, 0xffff0000, v196
v_lshlrev_b32 v216, 16, v197
v_mfma_f32_16x16x32_bf16 v[82:85], v[26:29], v[106:109], v[82:85]
s_subb_u32 s57, s57, 0
v_and_b32 v217, 0xffff0000, v197
v_lshlrev_b32 v218, 16, v198
v_mfma_f32_16x16x32_bf16 v[82:85], v[34:37], v[110:113], v[82:85]
s_add_u32 s84, s54, 18688
v_and_b32 v219, 0xffff0000, v198
v_lshlrev_b32 v220, 16, v199
v_mfma_f32_16x16x32_bf16 v[82:85], v[42:45], v[114:117], v[82:85]
s_addc_u32 s85, s55, 0
v_and_b32 v221, 0xffff0000, v199
v_add_f32 v206, v206, v214
v_mfma_f32_16x16x32_bf16 v[82:85], v[50:53], v[118:121], v[82:85]
s_add_u32 s86, s52, 110592
v_add_f32 v207, v207, v215
v_add_f32 v208, v208, v216
v_mfma_f32_16x16x32_bf16 v[82:85], v[58:61], v[122:125], v[82:85]
s_addc_u32 s87, s53, 0
v_add_f32 v209, v209, v217
v_add_f32 v210, v210, v218
v_mfma_f32_16x16x32_bf16 v[82:85], v[66:69], v[126:129], v[82:85]
v_mfma_f32_16x16x32_bf16 v[78:81], a[100:103], v[106:109], v[78:81]
s_barrier
v_add_lshl_u32 v252, v228, 0, 7
v_mfma_f32_16x16x32_bf16 v[78:81], a[108:111], v[110:113], v[78:81]
s_add_u32 s72, s72, 1
v_bfi_b32 v252, s69, v252, v18
v_add_f32 v211, v211, v219
v_mfma_f32_16x16x32_bf16 v[78:81], a[116:119], v[114:117], v[78:81]
s_cmp_le_i32 s62, s72
v_add_f32 v212, v212, v220
v_add_f32 v213, v213, v221
v_mfma_f32_16x16x32_bf16 v[78:81], a[124:127], v[118:121], v[78:81]
s_cselect_b64 s[76:77], -1, 0
ds_read_b128 v[130:133], v8 offset:18432
v_lshlrev_b32 v214, 16, v204
v_mfma_f32_16x16x32_bf16 v[78:81], a[132:135], v[122:125], v[78:81]
ds_read_b128 v[134:137], v8 offset:19456
v_mul_f32 v154, v74, v82
v_mfma_f32_16x16x32_bf16 v[78:81], a[140:143], v[126:129], v[78:81]
ds_read_b128 v[138:141], v8 offset:20480
v_mul_f32 v155, v75, v83
v_mfma_f32_16x16x32_bf16 v[86:89], v[30:33], v[106:109], v[86:89]
ds_read_b128 v[142:145], v8 offset:21504
v_mul_f32 v156, v76, v84
v_mfma_f32_16x16x32_bf16 v[86:89], v[38:41], v[110:113], v[86:89]
ds_read_b128 v[146:149], v8 offset:22528
v_mul_f32 v157, v77, v85
v_mfma_f32_16x16x32_bf16 v[86:89], v[46:49], v[114:117], v[86:89]
ds_read_b128 v[150:153], v8 offset:23552
v_cvt_pk_bf16_f32 v162, v154, v155
v_mfma_f32_16x16x32_bf16 v[86:89], v[54:57], v[118:121], v[86:89]
v_cvt_pk_bf16_f32 v163, v156, v157
v_and_b32 v215, 0xffff0000, v204
v_mfma_f32_16x16x32_bf16 v[86:89], v[62:65], v[122:125], v[86:89]
v_lshlrev_b32 v216, 16, v205
v_and_b32 v217, 0xffff0000, v205
v_mfma_f32_16x16x32_bf16 v[86:89], v[70:73], v[126:129], v[86:89]
s_waitcnt lgkmcnt(5)
)"
R"(global_load_dword v230, v253, s[38:39] sc1
v_add_f32_dpp v206, v210, v206 row_ror:8 row_mask:0xf bank_mask:0xf
v_mfma_f32_16x16x32_bf16 v[90:93], a[0:3], v[130:133], 0
s_waitcnt lgkmcnt(4)
v_add_f32_dpp v207, v211, v207 row_ror:8 row_mask:0xf bank_mask:0xf
v_add_f32_dpp v208, v212, v208 row_ror:8 row_mask:0xf bank_mask:0xf
v_mfma_f32_16x16x32_bf16 v[90:93], a[8:11], v[134:137], v[90:93]
s_waitcnt lgkmcnt(3)
v_add_f32_dpp v209, v213, v209 row_ror:8 row_mask:0xf bank_mask:0xf
v_fma_f32 v206, v206, v231, v214
v_mfma_f32_16x16x32_bf16 v[90:93], a[16:19], v[138:141], v[90:93]
s_waitcnt lgkmcnt(2)
v_fma_f32 v207, v207, v231, v215
v_fma_f32 v208, v208, v231, v216
v_mfma_f32_16x16x32_bf16 v[90:93], a[24:27], v[142:145], v[90:93]
s_waitcnt lgkmcnt(1)
v_mul_f32 v158, v78, v86
v_mul_f32 v159, v79, v87
v_mfma_f32_16x16x32_bf16 v[90:93], a[32:35], v[146:149], v[90:93]
s_waitcnt lgkmcnt(0)
v_mul_f32 v160, v80, v88
v_mul_f32 v161, v81, v89
v_mfma_f32_16x16x32_bf16 v[90:93], a[40:43], v[150:153], v[90:93]
ds_read_b128 v[106:109], v8 offset:24576
v_cvt_pk_bf16_f32 v164, v158, v159
v_mfma_f32_16x16x32_bf16 v[98:101], a[144:147], v[130:133], 0
ds_read_b128 v[110:113], v8 offset:25600
v_cvt_pk_bf16_f32 v165, v160, v161
v_mfma_f32_16x16x32_bf16 v[98:101], a[152:155], v[134:137], v[98:101]
v_mfma_f32_16x16x32_bf16 v[98:101], a[160:163], v[138:141], v[98:101]
s_waitcnt vmcnt(9)
s_barrier
v_mfma_f32_16x16x32_bf16 v[98:101], a[168:171], v[142:145], v[98:101]
s_waitcnt vmcnt(7)
s_mov_b64 exec, 1
global_store_dword v252, v228, s[38:39] sc1
s_mov_b64 exec, -1
v_mfma_f32_16x16x32_bf16 v[98:101], a[176:179], v[146:149], v[98:101]
v_fma_f32 v209, v209, v231, v217
v_cvt_pk_bf16_f32 v222, v206, v207
v_mfma_f32_16x16x32_bf16 v[98:101], a[184:187], v[150:153], v[98:101]
ds_read_b128 v[114:117], v8 offset:26624
v_cvt_pk_bf16_f32 v223, v208, v209
v_mfma_f32_16x16x32_bf16 v[94:97], a[4:7], v[130:133], 0
ds_write_b64 v14, v[222:223]
v_mfma_f32_16x16x32_bf16 v[94:97], a[12:15], v[134:137], v[94:97]
global_store_dwordx4 v3, v[162:165], s[38:39] sc1
v_mfma_f32_16x16x32_bf16 v[94:97], a[20:23], v[138:141], v[94:97]
ds_read_b128 v[118:121], v8 offset:27648
v_add_u32 v3, 147456, v3
v_mfma_f32_16x16x32_bf16 v[94:97], a[28:31], v[142:145], v[94:97]
ds_read_b64 v[224:225], v15
v_mfma_f32_16x16x32_bf16 v[94:97], a[36:39], v[146:149], v[94:97]
v_mfma_f32_16x16x32_bf16 v[94:97], a[44:47], v[150:153], v[94:97]
ds_read_b128 v[122:125], v8 offset:28672
v_mfma_f32_16x16x32_bf16 v[102:105], a[148:151], v[130:133], 0
v_mfma_f32_16x16x32_bf16 v[102:105], a[156:159], v[134:137], v[102:105]
v_mfma_f32_16x16x32_bf16 v[102:105], a[164:167], v[138:141], v[102:105]
ds_read_b128 v[126:129], v8 offset:29696
v_mfma_f32_16x16x32_bf16 v[102:105], a[172:175], v[142:145], v[102:105]
v_mfma_f32_16x16x32_bf16 v[102:105], a[180:183], v[146:149], v[102:105]
s_waitcnt lgkmcnt(2)
global_store_dwordx2 v13, v[224:225], s[60:61] sc1
v_mov_b32 v229, v227
v_mfma_f32_16x16x32_bf16 v[102:105], a[188:191], v[150:153], v[102:105]
v_mfma_f32_16x16x32_bf16 v[90:93], a[48:51], v[106:109], v[90:93]
v_mfma_f32_16x16x32_bf16 v[90:93], a[56:59], v[110:113], v[90:93]
v_mfma_f32_16x16x32_bf16 v[90:93], a[64:67], v[114:117], v[90:93]
v_mfma_f32_16x16x32_bf16 v[90:93], a[72:75], v[118:121], v[90:93]
s_waitcnt vmcnt(8)
v_add_f32 v182, v182, v184
s_waitcnt vmcnt(7)
v_lshlrev_b32 v206, 16, v170
v_add_f32 v183, v183, v185
s_waitcnt lgkmcnt(1)
v_mfma_f32_16x16x32_bf16 v[90:93], a[80:83], v[122:125], v[90:93]
s_waitcnt lgkmcnt(0)
v_and_b32 v207, 0xffff0000, v170
v_mul_f32 v182, 0x3ae38e39, v182
v_lshlrev_b32 v208, 16, v171
v_mfma_f32_16x16x32_bf16 v[90:93], a[88:91], v[126:129], v[90:93]
ds_read_b128 v[130:133], v8 offset:30720
v_mul_f32 v183, 0x3ae38e39, v183
v_and_b32 v209, 0xffff0000, v171
v_mfma_f32_16x16x32_bf16 v[98:101], a[192:195], v[106:109], v[98:101]
v_fma_f32 v232, -v182, v182, v183
v_lshlrev_b32 v210, 16, v172
v_max_f32 v232, 0, v232
v_mfma_f32_16x16x32_bf16 v[98:101], a[200:203], v[110:113], v[98:101]
v_and_b32 v211, 0xffff0000, v172
v_add_f32 v232, 0x3727c5ac, v232
v_mfma_f32_16x16x32_bf16 v[98:101], a[208:211], v[114:117], v[98:101]
ds_read_b128 v[134:137], v8 offset:31744
v_lshlrev_b32 v212, 16, v173
.Lgu_in24:
v_mfma_f32_16x16x32_bf16 v[98:101], a[216:219], v[118:121], v[98:101]
s_waitcnt vmcnt(3)
v_sub_u32 v230, v230, v17
v_cmp_lt_u32 vcc, 0x7f, v230
v_mfma_f32_16x16x32_bf16 v[98:101], a[224:227], v[122:125], v[98:101]
v_rcp_f32 v231, v232
v_mfma_f32_16x16x32_bf16 v[98:101], a[232:235], v[126:129], v[98:101]
ds_read_b128 v[138:141], v8 offset:32768
v_and_b32 v213, 0xffff0000, v173
v_mfma_f32_16x16x32_bf16 v[94:97], a[52:55], v[106:109], v[94:97]
s_and_b64 s[88:89], vcc, s[76:77]
s_cbranch_scc1 .Lgu_sp22
.Lgu_rt23:
v_mfma_f32_16x16x32_bf16 v[94:97], a[60:63], v[110:113], v[94:97]
s_cmp_eq_u64 vcc, 0
v_lshlrev_b32 v214, 16, v174
v_and_b32 v215, 0xffff0000, v174
v_mfma_f32_16x16x32_bf16 v[94:97], a[68:71], v[114:117], v[94:97]
s_cselect_b64 s[54:55], s[84:85], s[54:55]
ds_read_b128 v[142:145], v8 offset:33792
v_lshlrev_b32 v216, 16, v175
v_mfma_f32_16x16x32_bf16 v[94:97], a[76:79], v[118:121], v[94:97]
s_cselect_b64 s[52:53], s[86:87], s[52:53]
global_load_dwordx2 v[200:201], v12, s[54:55] offset:0 sc1
v_and_b32 v217, 0xffff0000, v175
v_mfma_f32_16x16x32_bf16 v[94:97], a[84:87], v[122:125], v[94:97]
s_cselect_b64 s[60:61], s[56:57], s[50:51]
global_load_dwordx2 v[202:203], v12, s[54:55] offset:128 sc1
v_lshlrev_b32 v218, 16, v176
v_mfma_f32_16x16x32_bf16 v[94:97], a[92:95], v[126:129], v[94:97]
s_cselect_b32 s82, 1, 0
ds_read_b128 v[146:149], v8 offset:34816
global_load_dwordx4 v[188:191], v10, s[52:53] offset:0 sc1
v_mfma_f32_16x16x32_bf16 v[102:105], a[196:199], v[106:109], v[102:105]
s_add_u32 s62, s62, s82
global_load_dwordx4 v[192:195], v10, s[52:53] offset:1024 sc1
v_and_b32 v219, 0xffff0000, v176
v_mfma_f32_16x16x32_bf16 v[102:105], a[204:207], v[110:113], v[102:105]
s_add_u32 s83, s62, s37
global_load_dwordx4 v[196:199], v10, s[52:53] offset:2048 sc1
v_add_u32 v17, s82, v17
v_add_u32 v227, -1, s83
v_mfma_f32_16x16x32_bf16 v[102:105], a[212:215], v[114:117], v[102:105]
s_cmp_lt_i32 s62, 32
ds_read_b128 v[150:153], v8 offset:35840
global_load_dwordx2 v[204:205], v11, s[54:55] sc1
v_add_lshl_u32 v253, v17, 0, 7
v_mfma_f32_16x16x32_bf16 v[102:105], a[220:223], v[118:121], v[102:105]
s_cselect_b64 s[56:57], s[46:47], s[48:49]
v_bfi_b32 v253, s69, v253, v16
v_lshlrev_b32 v220, 16, v177
v_mfma_f32_16x16x32_bf16 v[102:105], a[228:231], v[122:125], v[102:105]
s_mul_i32 s80, s62, 18432
v_and_b32 v221, 0xffff0000, v177
v_add_f32 v206, v206, v214
v_mfma_f32_16x16x32_bf16 v[102:105], a[236:239], v[126:129], v[102:105]
s_waitcnt lgkmcnt(5)
v_mfma_f32_16x16x32_bf16 v[90:93], a[96:99], v[130:133], v[90:93]
s_barrier
s_waitcnt lgkmcnt(4)
v_mfma_f32_16x16x32_bf16 v[90:93], a[104:107], v[134:137], v[90:93]
s_waitcnt lgkmcnt(3)
v_add_f32 v207, v207, v215
v_add_f32 v208, v208, v216
v_mfma_f32_16x16x32_bf16 v[90:93], a[112:115], v[138:141], v[90:93]
s_waitcnt lgkmcnt(2)
v_add_f32 v209, v209, v217
v_add_f32 v210, v210, v218
v_mfma_f32_16x16x32_bf16 v[90:93], a[120:123], v[142:145], v[90:93]
s_waitcnt lgkmcnt(1)
v_add_f32 v211, v211, v219
v_add_f32 v212, v212, v220
v_mfma_f32_16x16x32_bf16 v[90:93], a[128:131], v[146:149], v[90:93]
s_waitcnt lgkmcnt(0)
v_add_f32 v213, v213, v221
v_lshlrev_b32 v214, 16, v178
v_mfma_f32_16x16x32_bf16 v[90:93], a[136:139], v[150:153], v[90:93]
s_sub_u32 s56, s56, s80
v_and_b32 v215, 0xffff0000, v178
v_lshlrev_b32 v216, 16, v179
v_mfma_f32_16x16x32_bf16 v[98:101], v[26:29], v[130:133], v[98:101]
s_subb_u32 s57, s57, 0
v_and_b32 v217, 0xffff0000, v179
v_lshlrev_b32 v218, 16, v180
v_mfma_f32_16x16x32_bf16 v[98:101], v[34:37], v[134:137], v[98:101]
s_add_u32 s84, s54, 18688
v_and_b32 v219, 0xffff0000, v180
v_lshlrev_b32 v220, 16, v181
v_mfma_f32_16x16x32_bf16 v[98:101], v[42:45], v[138:141], v[98:101]
s_addc_u32 s85, s55, 0
v_and_b32 v221, 0xffff0000, v181
v_add_f32 v206, v206, v214
v_mfma_f32_16x16x32_bf16 v[98:101], v[50:53], v[142:145], v[98:101]
s_add_u32 s86, s52, 110592
v_xor_b32 v8, 0x10000, v8
v_add_f32 v207, v207, v215
v_mfma_f32_16x16x32_bf16 v[98:101], v[58:61], v[146:149], v[98:101]
s_addc_u32 s87, s53, 0
v_add_f32 v208, v208, v216
v_add_f32 v209, v209, v217
v_mfma_f32_16x16x32_bf16 v[98:101], v[66:69], v[150:153], v[98:101]
v_mfma_f32_16x16x32_bf16 v[94:97], a[100:103], v[130:133], v[94:97]
s_barrier
v_add_lshl_u32 v252, v229, 0, 7
v_mfma_f32_16x16x32_bf16 v[94:97], a[108:111], v[134:137], v[94:97]
s_add_u32 s72, s72, 1
v_add_f32 v210, v210, v218
v_add_f32 v211, v211, v219
v_bfi_b32 v252, s69, v252, v18
v_mfma_f32_16x16x32_bf16 v[94:97], a[116:119], v[138:141], v[94:97]
s_cmp_le_i32 s62, s72
v_add_f32 v212, v212, v220
v_add_f32 v213, v213, v221
v_mfma_f32_16x16x32_bf16 v[94:97], a[124:127], v[142:145], v[94:97]
s_cselect_b64 s[76:77], -1, 0
ds_read_b128 v[106:109], v8 offset:0
v_lshlrev_b32 v214, 16, v186
v_mfma_f32_16x16x32_bf16 v[94:97], a[132:135], v[146:149], v[94:97]
ds_read_b128 v[110:113], v8 offset:1024
v_mul_f32 v154, v90, v98
v_mfma_f32_16x16x32_bf16 v[94:97], a[140:143], v[150:153], v[94:97]
ds_read_b128 v[114:117], v8 offset:2048
v_mul_f32 v155, v91, v99
v_mfma_f32_16x16x32_bf16 v[102:105], v[30:33], v[130:133], v[102:105]
ds_read_b128 v[118:121], v8 offset:3072
v_mul_f32 v156, v92, v100
v_mfma_f32_16x16x32_bf16 v[102:105], v[38:41], v[134:137], v[102:105]
ds_read_b128 v[122:125], v8 offset:4096
v_mul_f32 v157, v93, v101
v_mfma_f32_16x16x32_bf16 v[102:105], v[46:49], v[138:141], v[102:105]
ds_read_b128 v[126:129], v8 offset:5120
v_cvt_pk_bf16_f32 v162, v154, v155
v_mfma_f32_16x16x32_bf16 v[102:105], v[54:57], v[142:145], v[102:105]
v_cvt_pk_bf16_f32 v163, v156, v157
v_and_b32 v215, 0xffff0000, v186
v_mfma_f32_16x16x32_bf16 v[102:105], v[62:65], v[146:149], v[102:105]
v_mfma_f32_16x16x32_bf16 v[102:105], v[70:73], v[150:153], v[102:105]
s_sub_u32 s70, s70, 1
s_cbranch_scc0 .Lgu_top25
v_mov_b32 v232, s62
ds_write_b32 v1, v232 offset:36864
v_subrev_u32 v17, s62, v17
s_waitcnt vmcnt(7) lgkmcnt(0)
s_barrier
v_mul_f32 v154, v90, v98
v_mul_f32 v155, v91, v99
v_mul_f32 v156, v92, v100
v_mul_f32 v157, v93, v101
v_cvt_pk_bf16_f32 v162, v154, v155
v_cvt_pk_bf16_f32 v163, v156, v157
v_mul_f32 v158, v94, v102
v_mul_f32 v159, v95, v103
v_mul_f32 v160, v96, v104
v_mul_f32 v161, v97, v105
v_cvt_pk_bf16_f32 v164, v158, v159
v_cvt_pk_bf16_f32 v165, v160, v161
global_store_dwordx4 v3, v[162:165], s[38:39] sc1
v_mov_b32 v231, 192
ds_read_b32 v231, v231 offset:36864
s_waitcnt vmcnt(0)
s_barrier
s_waitcnt lgkmcnt(0)
v_readfirstlane_b32 s62, v231
s_nop 3
s_max_i32 s62, s62, 2
s_min_i32 s62, s62, 66
s_add_u32 s62, s62, 1
s_branch .Lgu_fin
.Lgu_fin:
s_cmp_gt_i32 s62, 63
s_cbranch_scc1 .Lgu_end
v_add_u32 v233, s62, v17
v_add_lshl_u32 v253, v233, 0, 7
v_bfi_b32 v253, s69, v253, v16
global_load_dword v230, v253, s[38:39] sc1
s_cmp_lt_i32 s62, 32
s_cselect_b64 s[56:57], s[46:47], s[48:49]
s_mul_i32 s80, s62, 18432
s_sub_u32 s56, s56, s80
s_subb_u32 s57, s57, 0
s_mov_b64 s[58:59], s[56:57]
s_mul_i32 s80, s62, 110592
s_mul_i32 s81, s62, 18688
s_mov_b64 s[52:53], s[38:39]
s_mov_b64 s[54:55], s[38:39]
s_add_u32 s52, s52, s80
s_addc_u32 s53, s53, 0
s_add_u32 s54, s54, s81
s_addc_u32 s55, s55, 0
s_waitcnt vmcnt(0)
v_sub_u32 v230, v230, v233
v_cmp_lt_u32 vcc, 0x7f, v230
s_cbranch_vccnz .Lgu_sp27
.Lgu_rt28:
global_load_dwordx2 v[182:183], v12, s[54:55] offset:0 sc1
global_load_dwordx2 v[184:185], v12, s[54:55] offset:128 sc1
global_load_dwordx4 v[170:173], v10, s[52:53] offset:0 sc1
global_load_dwordx4 v[174:177], v10, s[52:53] offset:1024 sc1
global_load_dwordx4 v[178:181], v10, s[52:53] offset:2048 sc1
global_load_dwordx2 v[186:187], v11, s[54:55] sc1
s_waitcnt vmcnt(4)
v_add_f32 v182, v182, v184
s_waitcnt vmcnt(3)
v_lshlrev_b32 v206, 16, v170
v_add_f32 v183, v183, v185
v_and_b32 v207, 0xffff0000, v170
v_mul_f32 v182, 0x3ae38e39, v182
v_lshlrev_b32 v208, 16, v171
v_mul_f32 v183, 0x3ae38e39, v183
v_and_b32 v209, 0xffff0000, v171
v_fma_f32 v232, -v182, v182, v183
v_lshlrev_b32 v210, 16, v172
v_max_f32 v232, 0, v232
v_and_b32 v211, 0xffff0000, v172
v_add_f32 v232, 0x3727c5ac, v232
v_lshlrev_b32 v212, 16, v173
v_rcp_f32 v231, v232
v_and_b32 v213, 0xffff0000, v173
s_waitcnt vmcnt(2)
v_lshlrev_b32 v214, 16, v174
v_and_b32 v215, 0xffff0000, v174
v_lshlrev_b32 v216, 16, v175
v_and_b32 v217, 0xffff0000, v175
v_lshlrev_b32 v218, 16, v176
v_and_b32 v219, 0xffff0000, v176
v_lshlrev_b32 v220, 16, v177
v_and_b32 v221, 0xffff0000, v177
v_add_f32 v206, v206, v214
v_add_f32 v207, v207, v215
v_add_f32 v208, v208, v216
v_add_f32 v209, v209, v217
v_add_f32 v210, v210, v218
v_add_f32 v211, v211, v219
v_add_f32 v212, v212, v220
v_add_f32 v213, v213, v221
s_waitcnt vmcnt(1)
v_lshlrev_b32 v214, 16, v178
v_and_b32 v215, 0xffff0000, v178
v_lshlrev_b32 v216, 16, v179
v_and_b32 v217, 0xffff0000, v179
v_lshlrev_b32 v218, 16, v180
v_and_b32 v219, 0xffff0000, v180
v_lshlrev_b32 v220, 16, v181
v_and_b32 v221, 0xffff0000, v181
v_add_f32 v206, v206, v214
v_add_f32 v207, v207, v215
v_add_f32 v208, v208, v216
v_add_f32 v209, v209, v217
v_add_f32 v210, v210, v218
v_add_f32 v211, v211, v219
v_add_f32 v212, v212, v220
v_add_f32 v213, v213, v221
s_waitcnt vmcnt(0)
v_lshlrev_b32 v214, 16, v186
v_and_b32 v215, 0xffff0000, v186
v_lshlrev_b32 v216, 16, v187
v_and_b32 v217, 0xffff0000, v187
v_add_f32_dpp v206, v210, v206 row_ror:8 row_mask:0xf bank_mask:0xf
v_add_f32_dpp v207, v211, v207 row_ror:8 row_mask:0xf bank_mask:0xf
v_add_f32_dpp v208, v212, v208 row_ror:8 row_mask:0xf bank_mask:0xf
v_add_f32_dpp v209, v213, v209 row_ror:8 row_mask:0xf bank_mask:0xf
v_fma_f32 v206, v206, v231, v214
v_fma_f32 v207, v207, v231, v215
v_fma_f32 v208, v208, v231, v216
v_fma_f32 v209, v209, v231, v217
v_cvt_pk_bf16_f32 v222, v206, v207
v_cvt_pk_bf16_f32 v223, v208, v209
ds_write_b64 v14, v[222:223]
ds_read_b64 v[224:225], v15
s_waitcnt lgkmcnt(0)
global_store_dwordx2 v13, v[224:225], s[58:59] sc1
s_add_u32 s62, s62, 4
s_branch .Lgu_fin
.Lgu_end:
s_endpgm
.Lgu_sp11:
s_sleep 1
v_add_lshl_u32 v253, v5, 0, 7
v_bfi_b32 v253, s69, v253, v4
global_load_dword v254, v253, s[38:39] sc1
s_add_u32 s71, s71, 1
s_waitcnt vmcnt(0)
v_sub_u32 v254, v254, v5
v_cmp_lt_u32 vcc, 0x7f, v254
s_cbranch_vccz .Lgu_rt12
s_cmp_lt_u32 s71, 3000000
s_cbranch_scc1 .Lgu_sp11
s_branch .Lgu_rt12
.Lgu_sp13:
s_sleep 1
v_add_lshl_u32 v253, v5, 0, 7
v_bfi_b32 v253, s69, v253, v4
global_load_dword v255, v253, s[38:39] sc1
s_add_u32 s71, s71, 1
s_waitcnt vmcnt(0)
v_sub_u32 v255, v255, v5
v_cmp_lt_u32 vcc, 0x7f, v255
s_cbranch_vccz .Lgu_rt14
s_cmp_lt_u32 s71, 3000000
s_cbranch_scc1 .Lgu_sp13
s_branch .Lgu_rt14
.Lgu_sp16:
s_sleep 1
v_add_lshl_u32 v253, v5, 0, 7
v_bfi_b32 v253, s69, v253, v4
)"
R"(global_load_dword v254, v253, s[38:39] sc1
s_add_u32 s71, s71, 1
s_waitcnt vmcnt(0)
v_sub_u32 v254, v254, v5
v_cmp_lt_u32 vcc, 0x7f, v254
s_cbranch_vccz .Lgu_rt17
s_cmp_lt_u32 s71, 3000000
s_cbranch_scc1 .Lgu_sp16
s_branch .Lgu_rt17
.Lgu_sp20:
s_sleep 1
v_add_lshl_u32 v253, v17, 0, 7
v_bfi_b32 v253, s69, v253, v16
global_load_dword v230, v253, s[38:39] sc1
s_add_u32 s71, s71, 1
s_waitcnt vmcnt(0)
v_sub_u32 v230, v230, v17
v_cmp_lt_u32 vcc, 0x7f, v230
s_and_b64 vcc, vcc, s[76:77]
s_cbranch_vccz .Lgu_rt21
s_cmp_lt_u32 s71, 3000000
s_cbranch_scc1 .Lgu_sp20
s_branch .Lgu_rt21
.Lgu_sp22:
s_sleep 1
v_add_lshl_u32 v253, v17, 0, 7
v_bfi_b32 v253, s69, v253, v16
global_load_dword v230, v253, s[38:39] sc1
s_add_u32 s71, s71, 1
s_waitcnt vmcnt(0)
v_sub_u32 v230, v230, v17
v_cmp_lt_u32 vcc, 0x7f, v230
s_and_b64 vcc, vcc, s[76:77]
s_cbranch_vccz .Lgu_rt23
s_cmp_lt_u32 s71, 3000000
s_cbranch_scc1 .Lgu_sp22
s_branch .Lgu_rt23
.Lgu_sp27:
s_sleep 1
v_add_lshl_u32 v253, v233, 0, 7
v_bfi_b32 v253, s69, v253, v16
global_load_dword v230, v253, s[38:39] sc1
s_add_u32 s71, s71, 1
s_waitcnt vmcnt(0)
v_sub_u32 v230, v230, v233
v_cmp_lt_u32 vcc, 0x7f, v230
s_cbranch_vccz .Lgu_rt28
s_cmp_lt_u32 s71, 3000000
s_cbranch_scc1 .Lgu_sp27
s_branch .Lgu_rt28
.Le3:
)"
    :: "{v[0:31]}"(va), "{s[36:51]}"(sa), "{s[52:67]}"(sb) : "v255", "a255", "s99", "vcc", "memory");
  }
}

extern "C" void kernel_launch(void* const* d_in, const int* in_sizes, int n_in,
                              void* d_out, int out_size, void* d_ws, size_t ws_size,
                              hipStream_t stream) {
  k_main<<<256, 256, 0, stream>>>((const char*)d_in[0], (const char*)d_in[1], (const char*)d_in[2], (const char*)d_in[4],
                                  (const char*)d_in[5], (const char*)d_in[6], (char*)d_ws, (char*)d_out);
}
```

```cpp
#include <hip/hip_runtime.h>
#include <hip/hip_bf16.h>
#include <stdint.h>
typedef __attribute__((ext_vector_type(32))) unsigned u32x32;
typedef __attribute__((ext_vector_type(16))) unsigned u32x16;
constexpr unsigned RP = 80u;
constexpr unsigned PAD = 12u;
constexpr unsigned XT = 18432u;
constexpr unsigned X1P = 18688u;
constexpr unsigned HT = 147456u;
constexpr unsigned PT = 110592u;
constexpr unsigned FL = 1024u;
constexpr unsigned FR = 118784u;
constexpr unsigned WS_FL = 16384u;
constexpr unsigned WS_ST = 491520u;
constexpr unsigned WS_O = 1048576u;
constexpr unsigned WS_X1 = 6946816u;
constexpr unsigned WS_X2 = 13000704u;
constexpr unsigned WS_XD = 15396864u;
constexpr unsigned WS_P = 17977344u;
constexpr unsigned WS_H = 53366784u;
constexpr unsigned WS_END = 100552704u;
constexpr unsigned FO = 0u;
constexpr unsigned FX1 = 8192u;
constexpr unsigned FX2 = 65536u;
constexpr unsigned FP = 10240u;
constexpr unsigned FH = 28672u;
__device__ __forceinline__ void touch(const void* p) {
  asm volatile("s_mov_b32 m0, 0x27f00\n\ts_nop 0\n\tglobal_load_lds_dword %0, off" :: "v"(p) : "memory");
}
__device__ __forceinline__ unsigned uni(unsigned v) { return __builtin_amdgcn_readfirstlane(v); }
#define TOUCH_CODE(K, B, N) do { unsigned long long pc; unsigned len; int rel;                   \
    asm volatile("s_getpc_b64 %0\n.Lpc" #K ":\n\ts_mov_b32 %1, .Le" #K "-(" B ")-4\n\ts_mov_b32 %2, (" B ")-.Lpc" #K : "=s"(pc), "=s"(len), "=s"(rel)); \
    touch((const char*)pc + rel + min(((N) * 64 + lane) * 128, len)); } while (0)
__global__ __launch_bounds__(256, 1) void k_main(const char* x0, const char* wqkv, const char* wo, const char* wg, const char* wu, const char* wd,
                                                 char* ws, char* out) {
  __shared__ __attribute__((aligned(1024))) char smem[163840];
  asm volatile("" :: "s"((unsigned)(size_t)(__attribute__((address_space(3))) const char*)smem) : "memory");
  unsigned lane = threadIdx.x & 63;
  const unsigned wave = uni(threadIdx.x >> 6), xc = blockIdx.x & 7, jj = blockIdx.x >> 3;
  if (wave == 3) { if (jj < 8) TOUCH_CODE(0, ".Lpc0", jj); else if (jj < 16) TOUCH_CODE(1, ".Lb1-2048", jj - 8); else if (jj < 24) TOUCH_CODE(2, ".Lb2-2048", jj - 16); else TOUCH_CODE(3, ".Lb3-2048", jj - 24); };
  const bool ev = !(xc & 1); const unsigned xh = xc >> 1;
  if (ev && jj >= 8) { const unsigned u = min((jj - 8) * 216 + threadIdx.x, 5183u), sg = u / 3; touch(wqkv + sg * 1152 + xh * 288 + min((u - 3 * sg) * 128, 284u)); }
  if (__builtin_expect(ev && jj < 8, 1)) {
    const unsigned qa_hg = xc + (jj >> 2), qa_rho = jj & 3, pq0 = wave < 2 ? wave * 5 : 2 + wave * 4;
    unsigned qx[5];
#pragma unroll
    for (unsigned j = 0; j < 5; ++j)
      qx[j] = ((qa_rho * 32 + 31) * 16 + ((pq0 + j) & 1) * 8 + (lane >> 3)) * 1152 + ((pq0 + j) >> 1) * 128 + (((lane ^ (lane >> 3)) & 7) << 4) + 4096 - j * 1024;
    const unsigned long long xb = (unsigned long long)(x0 - 2048);
    asm volatile("s_add_u32 m0, %6, 0\n\ts_sub_u32 vcc_lo, %7, 0\n\ts_subb_u32 vcc_hi, %8, 0\n\tglobal_load_lds_dwordx4 %1, vcc offset:-2048 sc1\n\tglobal_load_lds_dwordx4 %2, vcc offset:-1024 sc1\n\tglobal_load_lds_dwordx4 %3, vcc offset:0 sc1\n\tglobal_load_lds_dwordx4 %4, vcc offset:1024 sc1\n\ts_cmp_gt_u32 %9, 1\n\ts_cbranch_scc1 .Lqx0\n\tglobal_load_lds_dwordx4 %5, vcc offset:2048 sc1\n.Lqx0:\n\ts_add_u32 m0, %6, 18432\n\ts_sub_u32 vcc_lo, %7, 18432\n\ts_subb_u32 vcc_hi, %8, 0\n\tglobal_load_lds_dwordx4 %1, vcc offset:-2048 sc1\n\tglobal_load_lds_dwordx4 %2, vcc offset:-1024 sc1\n\tglobal_load_lds_dwordx4 %3, vcc offset:0 sc1\n\tglobal_load_lds_dwordx4 %4, vcc offset:1024 sc1\n\ts_cmp_gt_u32 %9, 1\n\ts_cbranch_scc1 .Lqx1\n\tglobal_load_lds_dwordx4 %5, vcc offset:2048 sc1\n.Lqx1:\n\t" : "+v"(lane) : "v"(qx[0]), "v"(qx[1]), "v"(qx[2]), "v"(qx[3]), "v"(qx[4]), "s"(pq0 * 1024 + 2048),
                 "s"((unsigned)xb), "s"((unsigned)(xb >> 32)), "s"(wave) : "vcc", "scc", "memory");
    const unsigned r = lane & 15, g = lane >> 4, q = r >> 2, p = lane & 3;
    const unsigned hg = qa_hg, rho = qa_rho, row0 = rho * RP + PAD, pc0 = wave < 2 ? wave * 5 : 2 + wave * 4, hb = wave * 36 + (wave & 1) * 4;
    auto oa = [&](unsigned b) { return 43520 + (b >> 4) * 256 + r * 16 + (b & 15); };
    const unsigned m0 = 4 * g + (r & 1), m1 = m0 + 2, hm = (r & 1) ? 0xFFFF0000u : 0xFFFFu, ninf = 0xFF800000u;
    u32x32 va, vb; u32x16 sa, sb;
    va[0] = (((0 + lane) * 5462u) >> 16) * (1152 - 192) + lane * 16 + 4096;
    va[1] = (((64 + lane) * 5462u) >> 16) * (1152 - 192) + lane * 16 + 4096;
    va[2] = (((128 + lane) * 5462u) >> 16) * (1152 - 192) + lane * 16 + 4096;
    va[3] = (((192 + lane) * 5462u) >> 16) * (1152 - 192) + lane * 16 + 4096;
    va[4] = (((256 + lane) * 5462u) >> 16) * (1152 - 192) + lane * 16 + 4096;
    va[5] = (((320 + lane) * 5462u) >> 16) * (1152 - 192) + lane * 16 + 4096;
    va[6] = (((384 + lane) * 5462u) >> 16) * (1152 - 192) + lane * 16 + 4096;
    va[7] = (((448 + lane) * 5462u) >> 16) * (1152 - 192) + lane * 16 + 4096;
    va[8] = (((512 + lane) * 5462u) >> 16) * (1152 - 192) + lane * 16 + 12288;
    va[9] = (8 * g + q) * 576 + (hg & 3) * 16 + hb + p * 8;
    va[10] = (8 * g + q) * 576 + (hg & 3) * 16 + 32 + (p & 1) * 72 + (wave == 0 ? (p >> 1) * 192 : 384);
    va[11] = qx[0];
    va[12] = qx[1];
    va[13] = qx[2];
    va[14] = qx[3];
    va[15] = qx[4];
    va[16] = WS_O + (row0 - 1) * XT + hg * 2304 + wave * 1024 + (wave < 2 ? lane : r) * 16;
    va[17] = 43520 + wave * 1024 + (wave < 2 ? lane : r) * 16;
    va[18] = oa(hb + g * 8);
    va[19] = ((g & 1) == (wave >> 1)) ? oa(32 + (wave & 1) * 4 + (wave >> 1) * 72) : 46336 + lane * 4;
    va[20] = g == 0 ? 40960 + ((wave >> 1) * 16 + r) * 8 + (wave & 1) * 4 : 46080;
    va[21] = 41216 + ((wave >> 1) * 16 + r) * 8 + (wave & 1) * 4;
    va[22] = lane * 8;
    va[23] = lane * 16;
    va[24] = 43008 + r * 4;
    va[25] = 43008 + g * 16;
    va[26] = 41984 + (q * 16 + r) * 16 + p * 4;
    va[27] = 43136 + (((m0 > r) ? 0 : 1) * 16 + m0) * 4;
    va[28] = 43136 + (((m1 > r) ? 0 : 1) * 16 + m1) * 4;
    va[29] = 43136 + (((m0 > r) ? 1 : 0) * 16 + m0) * 4;
    va[30] = 43136 + (((m1 > r) ? 1 : 0) * 16 + m1) * 4;
    va[31] = 0u;
    vb[0] = (4 * g + 0 > r && !((r ^ 0) & 1)) ? 0u : ninf;
    vb[1] = (4 * g + 1 > r && !((r ^ 1) & 1)) ? 0u : ninf;
    vb[2] = (4 * g + 2 > r && !((r ^ 2) & 1)) ? 0u : ninf;
    vb[3] = (4 * g + 3 > r && !((r ^ 3) & 1)) ? 0u : ninf;
    vb[4] = (4 * g + 0 <= r && !((r ^ 0) & 1)) ? 0u : ninf;
    vb[5] = (4 * g + 1 <= r && !((r ^ 1) & 1)) ? 0u : ninf;
    vb[6] = (4 * g + 2 <= r && !((r ^ 2) & 1)) ? 0u : ninf;
    vb[7] = (4 * g + 3 <= r && !((r ^ 3) & 1)) ? 0u : ninf;
    vb[8] = m0 > r ? hm : 0u;
    vb[9] = m1 > r ? hm : 0u;
    vb[10] = m0 > r ? 0u : hm;
    vb[11] = m1 > r ? 0u : hm;
    vb[12] = WS_FL + rho * FR + FX2 + min(lane, 35u) * FL;
    vb[13] = WS_FL + rho * FR + FX2 + min(lane, 35u) * FL;
    vb[14] = 0u - 29;
    vb[15] = WS_FL + rho * FR + FO + hg * FL;
    vb[16] = 0u - 2;
    vb[17] = WS_FL + lane * FL;
    vb[18] = 0u;
    vb[19] = r * 128 + (((0 + g) ^ (r & 7)) << 4);
    vb[20] = r * 128 + (((4 + g) ^ (r & 7)) << 4);
    sa[0] = 0 + wave; sa[2] = (unsigned)(unsigned long long)ws; sa[3] = (unsigned)((unsigned long long)ws >> 32);
    sa[4] = uni((unsigned)(unsigned long long)(wqkv + ((hg * 144) & ~63u) + wave * (16 * 3456) + 0 * 64 * 3456));
    sa[5] = uni((unsigned)((unsigned long long)(wqkv + ((hg * 144) & ~63u) + wave * (16 * 3456) + 0 * 64 * 3456) >> 32));
    sa[6] = uni((unsigned)(unsigned long long)(x0 - 2048 - 2 * XT));
    sa[7] = uni((unsigned)((unsigned long long)(x0 - 2048 - 2 * XT) >> 32));
    sa[8] = uni((unsigned)(unsigned long long)((ws + WS_X2) - x0 + (rho ? 32 : 31) * (long long)XT));
    sa[9] = uni((unsigned)((unsigned long long)((ws + WS_X2) - x0 + (rho ? 32 : 31) * (long long)XT) >> 32));
    sa[10] = uni((unsigned)(unsigned long long)(-(long long)XT));
    sa[11] = uni((unsigned)((unsigned long long)(-(long long)XT) >> 32));
    sa[12] = uni(47104 + wave * 9216);
    sa[13] = uni(pc0 * 1024 + 2048 + 0x10000);
    sa[14] = uni(rho ? 0u : XT);
    sa[15] = uni(rho ? 0u - FR : 0u);
    sb[0] = uni(rho ? 0u : ninf);
    sb[1] = uni(hg * 4 + rho);
    asm volatile(
R"(.Lb0:
s_movk_i32 s69, 0x380
s_mov_b32 s71, 0
s_mov_b32 s74, 0
s_load_dword s37, s[38:39], 0x0
s_nop 1
s_and_b32 s75, s36, 3
s_cmp_eq_u32 s75, 1
s_cbranch_scc1 .Lqa_w2
s_cmp_eq_u32 s75, 2
s_cbranch_scc1 .Lqa_w3
s_cmp_eq_u32 s75, 3
s_cbranch_scc1 .Lqa_w4
.Lqa_w1:
s_getpc_b64 s[90:91]
.Lqa_pc23:
s_add_u32 s90, s90, .Lqa_e8-.Lqa_pc23
s_addc_u32 s91, s91, 0
s_branch .Lqa_k15
.Lqa_bk22:
v_add_u32 v255, 0x10000, v9
v_add_u32 v162, 0x20000, v9
v_add_u32 v163, 0x10000, v10
v_add_u32 v164, 0x20000, v10
v_mov_b32 v106, 0x3F803F80
v_mov_b32 v107, 0x3F803F80
v_mov_b32 v108, 0x3F803F80
v_mov_b32 v109, 0x3F803F80
v_mov_b32 v141, 0
v_mov_b32 v145, 0
v_mov_b32 v149, 0
v_mov_b32 v153, 0
v_mov_b32 v165, 46080
s_waitcnt lgkmcnt(0)
s_mul_i32 s37, s37, 0x9E3779B1
s_add_u32 s37, s37, 0x7F4A7C15
v_add_u32 v46, s37, v46
v_add_u32 v48, s37, v48
v_add_u32 v50, s37, v50
ds_write_b32 v165, v141
s_mov_b64 s[76:77], 0
s_mov_b32 s73, 18
s_add_u32 m0, s48, 4096
s_nop 0
global_load_lds_dwordx4 v0, s[40:41] offset:-4096
global_load_lds_dwordx4 v1, s[40:41] offset:-3072
global_load_lds_dwordx4 v2, s[40:41] offset:-2048
global_load_lds_dwordx4 v3, s[40:41] offset:-1024
global_load_lds_dwordx4 v4, s[40:41] offset:0
global_load_lds_dwordx4 v5, s[40:41] offset:1024
global_load_lds_dwordx4 v6, s[40:41] offset:2048
global_load_lds_dwordx4 v7, s[40:41] offset:3072
s_add_u32 m0, s48, 12288
s_nop 0
global_load_lds_dwordx4 v8, s[40:41] offset:-4096
s_add_u32 s40, s40, 221184
s_addc_u32 s41, s41, 0
s_add_u32 m0, s48, 40960
s_nop 0
global_load_lds_dwordx4 v0, s[40:41] offset:-4096
global_load_lds_dwordx4 v1, s[40:41] offset:-3072
global_load_lds_dwordx4 v2, s[40:41] offset:-2048
global_load_lds_dwordx4 v3, s[40:41] offset:-1024
global_load_lds_dwordx4 v4, s[40:41] offset:0
global_load_lds_dwordx4 v5, s[40:41] offset:1024
global_load_lds_dwordx4 v6, s[40:41] offset:2048
global_load_lds_dwordx4 v7, s[40:41] offset:3072
s_add_u32 m0, s48, 49152
s_nop 0
global_load_lds_dwordx4 v8, s[40:41] offset:-4096
s_add_u32 s40, s40, 221184
s_addc_u32 s41, s41, 0
s_waitcnt vmcnt(9) lgkmcnt(0)
s_barrier
s_add_u32 m0, s48, 77824
ds_read_b64_tr_b16 a[0:1], v9 offset:47104
ds_read_b64_tr_b16 a[2:3], v9 offset:49408
global_load_lds_dwordx4 v0, s[40:41] offset:-4096
ds_read_b64_tr_b16 a[4:5], v9 offset:47296
ds_read_b64_tr_b16 a[6:7], v9 offset:49600
global_load_lds_dwordx4 v1, s[40:41] offset:-3072
ds_read_b64_tr_b16 a[8:9], v9 offset:47488
global_load_lds_dwordx4 v2, s[40:41] offset:-2048
ds_read_b64_tr_b16 a[10:11], v9 offset:49792
ds_read_b64_tr_b16 a[216:217], v10 offset:47104
global_load_lds_dwordx4 v3, s[40:41] offset:-1024
ds_read_b64_tr_b16 a[218:219], v10 offset:49408
global_load_lds_dwordx4 v4, s[40:41] offset:0
ds_read_b128 v[166:169], v51 offset:0
ds_read_b128 v[174:177], v51 offset:18432
global_load_lds_dwordx4 v5, s[40:41] offset:1024
ds_read_b64_tr_b16 a[12:13], v255 offset:0
global_load_lds_dwordx4 v6, s[40:41] offset:2048
ds_read_b64_tr_b16 a[14:15], v255 offset:2304
ds_read_b64_tr_b16 a[16:17], v255 offset:192
global_load_lds_dwordx4 v7, s[40:41] offset:3072
ds_read_b64_tr_b16 a[18:19], v255 offset:2496
s_add_u32 m0, s48, 86016
ds_read_b64_tr_b16 a[20:21], v255 offset:384
ds_read_b64_tr_b16 a[22:23], v255 offset:2688
global_load_lds_dwordx4 v8, s[40:41] offset:-4096
ds_read_b64_tr_b16 a[220:221], v163 offset:0
s_add_u32 s40, s40, 221184
ds_read_b64_tr_b16 a[222:223], v163 offset:2304
ds_read_b128 v[170:173], v52 offset:0
s_addc_u32 s41, s41, 0
ds_read_b128 v[178:181], v52 offset:18432
s_waitcnt vmcnt(9) lgkmcnt(0)
s_barrier
s_add_u32 m0, s48, 4096
ds_read_b64_tr_b16 a[24:25], v255 offset:18432
v_mfma_f32_16x16x32_bf16 v[86:89], a[0:3], v[106:109], 0
ds_read_b64_tr_b16 a[26:27], v255 offset:20736
v_mfma_f32_16x16x32_bf16 v[90:93], a[4:7], v[106:109], 0
global_load_lds_dwordx4 v0, s[40:41] offset:-4096
ds_read_b64_tr_b16 a[28:29], v255 offset:18624
v_mfma_f32_16x16x32_bf16 v[94:97], v[106:109], a[8:11], 0
ds_read_b64_tr_b16 a[30:31], v255 offset:20928
global_load_lds_dwordx4 v1, s[40:41] offset:-3072
v_mfma_f32_16x16x32_bf16 v[98:101], a[216:219], v[106:109], 0
ds_read_b64_tr_b16 a[32:33], v255 offset:18816
v_mfma_f32_16x16x32_bf16 v[110:113], a[0:3], v[166:169], 0
ds_read_b64_tr_b16 a[34:35], v255 offset:21120
global_load_lds_dwordx4 v2, s[40:41] offset:-2048
v_mfma_f32_16x16x32_bf16 v[114:117], a[4:7], v[166:169], 0
ds_read_b64_tr_b16 a[224:225], v163 offset:18432
v_mfma_f32_16x16x32_bf16 v[118:121], v[166:169], a[8:11], 0
global_load_lds_dwordx4 v3, s[40:41] offset:-1024
ds_read_b64_tr_b16 a[226:227], v163 offset:20736
v_mfma_f32_16x16x32_bf16 v[134:137], a[216:219], v[166:169], 0
ds_read_b128 v[182:185], v51 offset:2048
global_load_lds_dwordx4 v4, s[40:41] offset:0
v_mfma_f32_16x16x32_bf16 v[122:125], a[0:3], v[174:177], 0
ds_read_b128 v[190:193], v51 offset:20480
v_mfma_f32_16x16x32_bf16 v[126:129], a[4:7], v[174:177], 0
ds_read_b64_tr_b16 a[36:37], v255 offset:36864
global_load_lds_dwordx4 v5, s[40:41] offset:1024
v_mfma_f32_16x16x32_bf16 v[130:133], v[174:177], a[8:11], 0
ds_read_b64_tr_b16 a[38:39], v255 offset:39168
v_mfma_f32_16x16x32_bf16 v[158:161], a[216:219], v[174:177], 0
global_load_lds_dwordx4 v6, s[40:41] offset:2048
ds_read_b64_tr_b16 a[40:41], v255 offset:37056
v_mfma_f32_16x16x32_bf16 v[86:89], a[12:15], v[106:109], v[86:89]
ds_read_b64_tr_b16 a[42:43], v255 offset:39360
v_mfma_f32_16x16x32_bf16 v[90:93], a[16:19], v[106:109], v[90:93]
global_load_lds_dwordx4 v7, s[40:41] offset:3072
ds_read_b64_tr_b16 a[44:45], v255 offset:37248
v_mfma_f32_16x16x32_bf16 v[94:97], v[106:109], a[20:23], v[94:97]
s_add_u32 m0, s48, 12288
ds_read_b64_tr_b16 a[46:47], v255 offset:39552
v_mfma_f32_16x16x32_bf16 v[98:101], a[220:223], v[106:109], v[98:101]
ds_read_b64_tr_b16 a[228:229], v163 offset:36864
v_mfma_f32_16x16x32_bf16 v[110:113], a[12:15], v[170:173], v[110:113]
global_load_lds_dwordx4 v8, s[40:41] offset:-4096
ds_read_b64_tr_b16 a[230:231], v163 offset:39168
v_mfma_f32_16x16x32_bf16 v[114:117], a[16:19], v[170:173], v[114:117]
ds_read_b128 v[186:189], v52 offset:2048
v_mfma_f32_16x16x32_bf16 v[118:121], v[170:173], a[20:23], v[118:121]
s_add_u32 s40, s40, 221184
ds_read_b128 v[194:197], v52 offset:20480
v_mfma_f32_16x16x32_bf16 v[134:137], a[220:223], v[170:173], v[134:137]
v_mfma_f32_16x16x32_bf16 v[122:125], a[12:15], v[178:181], v[122:125]
s_addc_u32 s41, s41, 0
v_mfma_f32_16x16x32_bf16 v[126:129], a[16:19], v[178:181], v[126:129]
v_mfma_f32_16x16x32_bf16 v[130:133], v[178:181], a[20:23], v[130:133]
v_mfma_f32_16x16x32_bf16 v[158:161], a[220:223], v[178:181], v[158:161]
s_waitcnt vmcnt(9) lgkmcnt(0)
s_barrier
s_add_u32 m0, s48, 40960
ds_read_b64_tr_b16 a[48:49], v255 offset:55296
v_mfma_f32_16x16x32_bf16 v[86:89], a[24:27], v[106:109], v[86:89]
ds_read_b64_tr_b16 a[50:51], v255 offset:57600
v_mfma_f32_16x16x32_bf16 v[90:93], a[28:31], v[106:109], v[90:93]
global_load_lds_dwordx4 v0, s[40:41] offset:-4096
ds_read_b64_tr_b16 a[52:53], v255 offset:55488
v_mfma_f32_16x16x32_bf16 v[94:97], v[106:109], a[32:35], v[94:97]
ds_read_b64_tr_b16 a[54:55], v255 offset:57792
global_load_lds_dwordx4 v1, s[40:41] offset:-3072
v_mfma_f32_16x16x32_bf16 v[98:101], a[224:227], v[106:109], v[98:101]
ds_read_b64_tr_b16 a[56:57], v255 offset:55680
v_mfma_f32_16x16x32_bf16 v[110:113], a[24:27], v[182:185], v[110:113]
ds_read_b64_tr_b16 a[58:59], v255 offset:57984
global_load_lds_dwordx4 v2, s[40:41] offset:-2048
v_mfma_f32_16x16x32_bf16 v[114:117], a[28:31], v[182:185], v[114:117]
ds_read_b64_tr_b16 a[232:233], v163 offset:55296
v_mfma_f32_16x16x32_bf16 v[118:121], v[182:185], a[32:35], v[118:121]
global_load_lds_dwordx4 v3, s[40:41] offset:-1024
ds_read_b64_tr_b16 a[234:235], v163 offset:57600
v_mfma_f32_16x16x32_bf16 v[134:137], a[224:227], v[182:185], v[134:137]
ds_read_b128 v[166:169], v51 offset:4096
global_load_lds_dwordx4 v4, s[40:41] offset:0
v_mfma_f32_16x16x32_bf16 v[122:125], a[24:27], v[190:193], v[122:125]
ds_read_b128 v[174:177], v51 offset:22528
v_mfma_f32_16x16x32_bf16 v[126:129], a[28:31], v[190:193], v[126:129]
ds_read_b64_tr_b16 a[60:61], v162 offset:8192
global_load_lds_dwordx4 v5, s[40:41] offset:1024
v_mfma_f32_16x16x32_bf16 v[130:133], v[190:193], a[32:35], v[130:133]
ds_read_b64_tr_b16 a[62:63], v162 offset:10496
v_mfma_f32_16x16x32_bf16 v[158:161], a[224:227], v[190:193], v[158:161]
global_load_lds_dwordx4 v6, s[40:41] offset:2048
ds_read_b64_tr_b16 a[64:65], v162 offset:8384
v_mfma_f32_16x16x32_bf16 v[86:89], a[36:39], v[106:109], v[86:89]
ds_read_b64_tr_b16 a[66:67], v162 offset:10688
v_mfma_f32_16x16x32_bf16 v[90:93], a[40:43], v[106:109], v[90:93]
global_load_lds_dwordx4 v7, s[40:41] offset:3072
ds_read_b64_tr_b16 a[68:69], v162 offset:8576
v_mfma_f32_16x16x32_bf16 v[94:97], v[106:109], a[44:47], v[94:97]
s_add_u32 m0, s48, 49152
ds_read_b64_tr_b16 a[70:71], v162 offset:10880
v_mfma_f32_16x16x32_bf16 v[98:101], a[228:231], v[106:109], v[98:101]
ds_read_b64_tr_b16 a[236:237], v164 offset:8192
v_mfma_f32_16x16x32_bf16 v[110:113], a[36:39], v[186:189], v[110:113]
global_load_lds_dwordx4 v8, s[40:41] offset:-4096
ds_read_b64_tr_b16 a[238:239], v164 offset:10496
v_mfma_f32_16x16x32_bf16 v[114:117], a[40:43], v[186:189], v[114:117]
ds_read_b128 v[170:173], v52 offset:4096
v_mfma_f32_16x16x32_bf16 v[118:121], v[186:189], a[44:47], v[118:121]
s_add_u32 s40, s40, 221184
ds_read_b128 v[178:181], v52 offset:22528
v_mfma_f32_16x16x32_bf16 v[134:137], a[228:231], v[186:189], v[134:137]
v_mfma_f32_16x16x32_bf16 v[122:125], a[36:39], v[194:197], v[122:125]
s_addc_u32 s41, s41, 0
v_mfma_f32_16x16x32_bf16 v[126:129], a[40:43], v[194:197], v[126:129]
v_mfma_f32_16x16x32_bf16 v[130:133], v[194:197], a[44:47], v[130:133]
v_mfma_f32_16x16x32_bf16 v[158:161], a[228:231], v[194:197], v[158:161]
s_waitcnt vmcnt(9) lgkmcnt(0)
s_barrier
s_add_u32 m0, s48, 77824
ds_read_b64_tr_b16 a[72:73], v9 offset:47104
v_mfma_f32_16x16x32_bf16 v[86:89], a[48:51], v[106:109], v[86:89]
ds_read_b64_tr_b16 a[74:75], v9 offset:49408
v_mfma_f32_16x16x32_bf16 v[90:93], a[52:55], v[106:109], v[90:93]
global_load_lds_dwordx4 v0, s[40:41] offset:-4096
ds_read_b64_tr_b16 a[76:77], v9 offset:47296
v_mfma_f32_16x16x32_bf16 v[94:97], v[106:109], a[56:59], v[94:97]
ds_read_b64_tr_b16 a[78:79], v9 offset:49600
global_load_lds_dwordx4 v1, s[40:41] offset:-3072
v_mfma_f32_16x16x32_bf16 v[98:101], a[232:235], v[106:109], v[98:101]
ds_read_b64_tr_b16 a[80:81], v9 offset:47488
v_mfma_f32_16x16x32_bf16 v[110:113], a[48:51], v[166:169], v[110:113]
ds_read_b64_tr_b16 a[82:83], v9 offset:49792
global_load_lds_dwordx4 v2, s[40:41] offset:-2048
v_mfma_f32_16x16x32_bf16 v[114:117], a[52:55], v[166:169], v[114:117]
ds_read_b64_tr_b16 a[240:241], v10 offset:47104
v_mfma_f32_16x16x32_bf16 v[118:121], v[166:169], a[56:59], v[118:121]
global_load_lds_dwordx4 v3, s[40:41] offset:-1024
ds_read_b64_tr_b16 a[242:243], v10 offset:49408
v_mfma_f32_16x16x32_bf16 v[134:137], a[232:235], v[166:169], v[134:137]
ds_read_b128 v[182:185], v51 offset:6144
global_load_lds_dwordx4 v4, s[40:41] offset:0
v_mfma_f32_16x16x32_bf16 v[122:125], a[48:51], v[174:177], v[122:125]
ds_read_b128 v[190:193], v51 offset:24576
v_mfma_f32_16x16x32_bf16 v[126:129], a[52:55], v[174:177], v[126:129]
ds_read_b64_tr_b16 a[84:85], v255 offset:0
global_load_lds_dwordx4 v5, s[40:41] offset:1024
v_mfma_f32_16x16x32_bf16 v[130:133], v[174:177], a[56:59], v[130:133]
ds_read_b64_tr_b16 a[86:87], v255 offset:2304
v_mfma_f32_16x16x32_bf16 v[158:161], a[232:235], v[174:177], v[158:161]
global_load_lds_dwordx4 v6, s[40:41] offset:2048
ds_read_b64_tr_b16 a[88:89], v255 offset:192
v_mfma_f32_16x16x32_bf16 v[86:89], a[60:63], v[106:109], v[86:89]
ds_read_b64_tr_b16 a[90:91], v255 offset:2496
v_mfma_f32_16x16x32_bf16 v[90:93], a[64:67], v[106:109], v[90:93]
global_load_lds_dwordx4 v7, s[40:41] offset:3072
ds_read_b64_tr_b16 a[92:93], v255 offset:384
v_mfma_f32_16x16x32_bf16 v[94:97], v[106:109], a[68:71], v[94:97]
s_add_u32 m0, s48, 86016
ds_read_b64_tr_b16 a[94:95], v255 offset:2688
v_mfma_f32_16x16x32_bf16 v[98:101], a[236:239], v[106:109], v[98:101]
ds_read_b64_tr_b16 a[244:245], v163 offset:0
v_mfma_f32_16x16x32_bf16 v[110:113], a[60:63], v[170:173], v[110:113]
global_load_lds_dwordx4 v8, s[40:41] offset:-4096
ds_read_b64_tr_b16 a[246:247], v163 offset:2304
v_mfma_f32_16x16x32_bf16 v[114:117], a[64:67], v[170:173], v[114:117]
ds_read_b128 v[186:189], v52 offset:6144
v_mfma_f32_16x16x32_bf16 v[118:121], v[170:173], a[68:71], v[118:121]
s_add_u32 s40, s40, 221184
ds_read_b128 v[194:197], v52 offset:24576
v_mfma_f32_16x16x32_bf16 v[134:137], a[236:239], v[170:173], v[134:137]
v_mfma_f32_16x16x32_bf16 v[122:125], a[60:63], v[178:181], v[122:125]
s_addc_u32 s41, s41, 0
v_mfma_f32_16x16x32_bf16 v[126:129], a[64:67], v[178:181], v[126:129]
v_mfma_f32_16x16x32_bf16 v[130:133], v[178:181], a[68:71], v[130:133]
v_mfma_f32_16x16x32_bf16 v[158:161], a[236:239], v[178:181], v[158:161]
s_waitcnt vmcnt(9) lgkmcnt(0)
s_barrier
s_add_u32 m0, s48, 4096
ds_read_b64_tr_b16 a[96:97], v255 offset:18432
v_mfma_f32_16x16x32_bf16 v[86:89], a[72:75], v[106:109], v[86:89]
ds_read_b64_tr_b16 a[98:99], v255 offset:20736
v_mfma_f32_16x16x32_bf16 v[90:93], a[76:79], v[106:109], v[90:93]
global_load_lds_dwordx4 v0, s[40:41] offset:-4096
ds_read_b64_tr_b16 a[100:101], v255 offset:18624
v_mfma_f32_16x16x32_bf16 v[94:97], v[106:109], a[80:83], v[94:97]
ds_read_b64_tr_b16 a[102:103], v255 offset:20928
global_load_lds_dwordx4 v1, s[40:41] offset:-3072
v_mfma_f32_16x16x32_bf16 v[98:101], a[240:243], v[106:109], v[98:101]
ds_read_b64_tr_b16 a[104:105], v255 offset:18816
v_mfma_f32_16x16x32_bf16 v[110:113], a[72:75], v[182:185], v[110:113]
ds_read_b64_tr_b16 a[106:107], v255 offset:21120
global_load_lds_dwordx4 v2, s[40:41] offset:-2048
v_mfma_f32_16x16x32_bf16 v[114:117], a[76:79], v[182:185], v[114:117]
ds_read_b64_tr_b16 a[248:249], v163 offset:18432
v_mfma_f32_16x16x32_bf16 v[118:121], v[182:185], a[80:83], v[118:121]
global_load_lds_dwordx4 v3, s[40:41] offset:-1024
ds_read_b64_tr_b16 a[250:251], v163 offset:20736
v_mfma_f32_16x16x32_bf16 v[134:137], a[240:243], v[182:185], v[134:137]
ds_read_b128 v[166:169], v51 offset:8192
global_load_lds_dwordx4 v4, s[40:41] offset:0
v_mfma_f32_16x16x32_bf16 v[122:125], a[72:75], v[190:193], v[122:125]
ds_read_b128 v[174:177], v51 offset:26624
v_mfma_f32_16x16x32_bf16 v[126:129], a[76:79], v[190:193], v[126:129]
ds_read_b64_tr_b16 a[108:109], v255 offset:36864
global_load_lds_dwordx4 v5, s[40:41] offset:1024
v_mfma_f32_16x16x32_bf16 v[130:133], v[190:193], a[80:83], v[130:133]
ds_read_b64_tr_b16 a[110:111], v255 offset:39168
v_mfma_f32_16x16x32_bf16 v[158:161], a[240:243], v[190:193], v[158:161]
global_load_lds_dwordx4 v6, s[40:41] offset:2048
ds_read_b64_tr_b16 a[112:113], v255 offset:37056
v_mfma_f32_16x16x32_bf16 v[86:89], a[84:87], v[106:109], v[86:89]
ds_read_b64_tr_b16 a[114:115], v255 offset:39360
v_mfma_f32_16x16x32_bf16 v[90:93], a[88:91], v[106:109], v[90:93]
global_load_lds_dwordx4 v7, s[40:41] offset:3072
ds_read_b64_tr_b16 a[116:117], v255 offset:37248
v_mfma_f32_16x16x32_bf16 v[94:97], v[106:109], a[92:95], v[94:97]
s_add_u32 m0, s48, 12288
ds_read_b64_tr_b16 a[118:119], v255 offset:39552
v_mfma_f32_16x16x32_bf16 v[98:101], a[244:247], v[106:109], v[98:101]
ds_read_b64_tr_b16 a[252:253], v163 offset:36864
v_mfma_f32_16x16x32_bf16 v[110:113], a[84:87], v[186:189], v[110:113]
global_load_lds_dwordx4 v8, s[40:41] offset:-4096
ds_read_b64_tr_b16 a[254:255], v163 offset:39168
v_mfma_f32_16x16x32_bf16 v[114:117], a[88:91], v[186:189], v[114:117]
ds_read_b128 v[170:173], v52 offset:8192
v_mfma_f32_16x16x32_bf16 v[118:121], v[186:189], a[92:95], v[118:121]
s_add_u32 s40, s40, 221184
ds_read_b128 v[178:181], v52 offset:26624
v_mfma_f32_16x16x32_bf16 v[134:137], a[244:247], v[186:189], v[134:137]
v_mfma_f32_16x16x32_bf16 v[122:125], a[84:87], v[194:197], v[122:125]
s_addc_u32 s41, s41, 0
v_mfma_f32_16x16x32_bf16 v[126:129], a[88:91], v[194:197], v[126:129]
v_mfma_f32_16x16x32_bf16 v[130:133], v[194:197], a[92:95], v[130:133]
v_mfma_f32_16x16x32_bf16 v[158:161], a[244:247], v[194:197], v[158:161]
s_waitcnt vmcnt(9) lgkmcnt(0)
s_barrier
s_add_u32 m0, s48, 40960
ds_read_b64_tr_b16 a[120:121], v255 offset:55296
v_mfma_f32_16x16x32_bf16 v[86:89], a[96:99], v[106:109], v[86:89]
ds_read_b64_tr_b16 a[122:123], v255 offset:57600
v_mfma_f32_16x16x32_bf16 v[90:93], a[100:103], v[106:109], v[90:93]
global_load_lds_dwordx4 v0, s[40:41] offset:-4096
ds_read_b64_tr_b16 a[124:125], v255 offset:55488
v_mfma_f32_16x16x32_bf16 v[94:97], v[106:109], a[104:107], v[94:97]
ds_read_b64_tr_b16 a[126:127], v255 offset:57792
global_load_lds_dwordx4 v1, s[40:41] offset:-3072
v_mfma_f32_16x16x32_bf16 v[98:101], a[248:251], v[106:109], v[98:101]
ds_read_b64_tr_b16 a[128:129], v255 offset:55680
v_mfma_f32_16x16x32_bf16 v[110:113], a[96:99], v[166:169], v[110:113]
ds_read_b64_tr_b16 a[130:131], v255 offset:57984
global_load_lds_dwordx4 v2, s[40:41] offset:-2048
v_mfma_f32_16x16x32_bf16 v[114:117], a[100:103], v[166:169], v[114:117]
ds_read_b64_tr_b16 v[54:55], v163 offset:55296
v_mfma_f32_16x16x32_bf16 v[118:121], v[166:169], a[104:107], v[118:121]
global_load_lds_dwordx4 v3, s[40:41] offset:-1024
ds_read_b64_tr_b16 v[56:57], v163 offset:57600
v_mfma_f32_16x16x32_bf16 v[134:137], a[248:251], v[166:169], v[134:137]
ds_read_b128 v[182:185], v51 offset:10240
global_load_lds_dwordx4 v4, s[40:41] offset:0
v_mfma_f32_16x16x32_bf16 v[122:125], a[96:99], v[174:177], v[122:125]
ds_read_b128 v[190:193], v51 offset:28672
v_mfma_f32_16x16x32_bf16 v[126:129], a[100:103], v[174:177], v[126:129]
ds_read_b64_tr_b16 a[132:133], v162 offset:8192
global_load_lds_dwordx4 v5, s[40:41] offset:1024
v_mfma_f32_16x16x32_bf16 v[130:133], v[174:177], a[104:107], v[130:133]
ds_read_b64_tr_b16 a[134:135], v162 offset:10496
v_mfma_f32_16x16x32_bf16 v[158:161], a[248:251], v[174:177], v[158:161]
global_load_lds_dwordx4 v6, s[40:41] offset:2048
ds_read_b64_tr_b16 a[136:137], v162 offset:8384
v_mfma_f32_16x16x32_bf16 v[86:89], a[108:111], v[106:109], v[86:89]
ds_read_b64_tr_b16 a[138:139], v162 offset:10688
v_mfma_f32_16x16x32_bf16 v[90:93], a[112:115], v[106:109], v[90:93]
global_load_lds_dwordx4 v7, s[40:41] offset:3072
ds_read_b64_tr_b16 a[140:141], v162 offset:8576
v_mfma_f32_16x16x32_bf16 v[94:97], v[106:109], a[116:119], v[94:97]
s_add_u32 m0, s48, 49152
ds_read_b64_tr_b16 a[142:143], v162 offset:10880
v_mfma_f32_16x16x32_bf16 v[98:101], a[252:255], v[106:109], v[98:101]
ds_read_b64_tr_b16 v[58:59], v164 offset:8192
v_mfma_f32_16x16x32_bf16 v[110:113], a[108:111], v[170:173], v[110:113]
global_load_lds_dwordx4 v8, s[40:41] offset:-4096
ds_read_b64_tr_b16 v[60:61], v164 offset:10496
v_mfma_f32_16x16x32_bf16 v[114:117], a[112:115], v[170:173], v[114:117]
ds_read_b128 v[186:189], v52 offset:10240
v_mfma_f32_16x16x32_bf16 v[118:121], v[170:173], a[116:119], v[118:121]
s_add_u32 s40, s40, 221184
ds_read_b128 v[194:197], v52 offset:28672
v_mfma_f32_16x16x32_bf16 v[134:137], a[252:255], v[170:173], v[134:137]
v_mfma_f32_16x16x32_bf16 v[122:125], a[108:111], v[178:181], v[122:125]
s_addc_u32 s41, s41, 0
v_mfma_f32_16x16x32_bf16 v[126:129], a[112:115], v[178:181], v[126:129]
)"
R"(v_mfma_f32_16x16x32_bf16 v[130:133], v[178:181], a[116:119], v[130:133]
v_mfma_f32_16x16x32_bf16 v[158:161], a[252:255], v[178:181], v[158:161]
s_waitcnt vmcnt(9) lgkmcnt(0)
s_barrier
s_add_u32 m0, s48, 77824
ds_read_b64_tr_b16 a[144:145], v9 offset:47104
v_mfma_f32_16x16x32_bf16 v[86:89], a[120:123], v[106:109], v[86:89]
ds_read_b64_tr_b16 a[146:147], v9 offset:49408
v_mfma_f32_16x16x32_bf16 v[90:93], a[124:127], v[106:109], v[90:93]
global_load_lds_dwordx4 v0, s[40:41] offset:-4096
ds_read_b64_tr_b16 a[148:149], v9 offset:47296
v_mfma_f32_16x16x32_bf16 v[94:97], v[106:109], a[128:131], v[94:97]
ds_read_b64_tr_b16 a[150:151], v9 offset:49600
global_load_lds_dwordx4 v1, s[40:41] offset:-3072
v_mfma_f32_16x16x32_bf16 v[98:101], v[54:57], v[106:109], v[98:101]
ds_read_b64_tr_b16 a[152:153], v9 offset:47488
v_mfma_f32_16x16x32_bf16 v[110:113], a[120:123], v[182:185], v[110:113]
ds_read_b64_tr_b16 a[154:155], v9 offset:49792
global_load_lds_dwordx4 v2, s[40:41] offset:-2048
v_mfma_f32_16x16x32_bf16 v[114:117], a[124:127], v[182:185], v[114:117]
ds_read_b64_tr_b16 v[62:63], v10 offset:47104
v_mfma_f32_16x16x32_bf16 v[118:121], v[182:185], a[128:131], v[118:121]
global_load_lds_dwordx4 v3, s[40:41] offset:-1024
ds_read_b64_tr_b16 v[64:65], v10 offset:49408
v_mfma_f32_16x16x32_bf16 v[134:137], v[54:57], v[182:185], v[134:137]
ds_read_b128 v[166:169], v51 offset:12288
global_load_lds_dwordx4 v4, s[40:41] offset:0
v_mfma_f32_16x16x32_bf16 v[122:125], a[120:123], v[190:193], v[122:125]
ds_read_b128 v[174:177], v51 offset:30720
v_mfma_f32_16x16x32_bf16 v[126:129], a[124:127], v[190:193], v[126:129]
ds_read_b64_tr_b16 a[156:157], v255 offset:0
global_load_lds_dwordx4 v5, s[40:41] offset:1024
v_mfma_f32_16x16x32_bf16 v[130:133], v[190:193], a[128:131], v[130:133]
ds_read_b64_tr_b16 a[158:159], v255 offset:2304
v_mfma_f32_16x16x32_bf16 v[158:161], v[54:57], v[190:193], v[158:161]
global_load_lds_dwordx4 v6, s[40:41] offset:2048
ds_read_b64_tr_b16 a[160:161], v255 offset:192
v_mfma_f32_16x16x32_bf16 v[86:89], a[132:135], v[106:109], v[86:89]
ds_read_b64_tr_b16 a[162:163], v255 offset:2496
v_mfma_f32_16x16x32_bf16 v[90:93], a[136:139], v[106:109], v[90:93]
global_load_lds_dwordx4 v7, s[40:41] offset:3072
ds_read_b64_tr_b16 a[164:165], v255 offset:384
v_mfma_f32_16x16x32_bf16 v[94:97], v[106:109], a[140:143], v[94:97]
s_add_u32 m0, s48, 86016
ds_read_b64_tr_b16 a[166:167], v255 offset:2688
v_mfma_f32_16x16x32_bf16 v[98:101], v[58:61], v[106:109], v[98:101]
ds_read_b64_tr_b16 v[66:67], v163 offset:0
v_mfma_f32_16x16x32_bf16 v[110:113], a[132:135], v[186:189], v[110:113]
global_load_lds_dwordx4 v8, s[40:41] offset:-4096
ds_read_b64_tr_b16 v[68:69], v163 offset:2304
v_mfma_f32_16x16x32_bf16 v[114:117], a[136:139], v[186:189], v[114:117]
ds_read_b128 v[170:173], v52 offset:12288
v_mfma_f32_16x16x32_bf16 v[118:121], v[186:189], a[140:143], v[118:121]
s_sub_u32 s40, s40, 1769472
ds_read_b128 v[178:181], v52 offset:30720
v_mfma_f32_16x16x32_bf16 v[134:137], v[58:61], v[186:189], v[134:137]
v_mfma_f32_16x16x32_bf16 v[122:125], a[132:135], v[194:197], v[122:125]
s_subb_u32 s41, s41, 0
v_mfma_f32_16x16x32_bf16 v[126:129], a[136:139], v[194:197], v[126:129]
v_mfma_f32_16x16x32_bf16 v[130:133], v[194:197], a[140:143], v[130:133]
v_mfma_f32_16x16x32_bf16 v[158:161], v[58:61], v[194:197], v[158:161]
s_waitcnt vmcnt(9) lgkmcnt(0)
s_barrier
s_add_u32 m0, s49, 0
ds_read_b64_tr_b16 a[168:169], v255 offset:18432
v_mfma_f32_16x16x32_bf16 v[86:89], a[144:147], v[106:109], v[86:89]
ds_read_b64_tr_b16 a[170:171], v255 offset:20736
v_mfma_f32_16x16x32_bf16 v[90:93], a[148:151], v[106:109], v[90:93]
ds_read_b64_tr_b16 a[172:173], v255 offset:18624
v_mfma_f32_16x16x32_bf16 v[94:97], v[106:109], a[152:155], v[94:97]
ds_read_b64_tr_b16 a[174:175], v255 offset:20928
global_load_lds_dwordx4 v11, s[42:43] offset:-2048 sc1
v_mfma_f32_16x16x32_bf16 v[98:101], v[62:65], v[106:109], v[98:101]
ds_read_b64_tr_b16 a[176:177], v255 offset:18816
v_mfma_f32_16x16x32_bf16 v[110:113], a[144:147], v[166:169], v[110:113]
ds_read_b64_tr_b16 a[178:179], v255 offset:21120
v_mfma_f32_16x16x32_bf16 v[114:117], a[148:151], v[166:169], v[114:117]
ds_read_b64_tr_b16 v[70:71], v163 offset:18432
global_load_lds_dwordx4 v12, s[42:43] offset:-1024 sc1
v_mfma_f32_16x16x32_bf16 v[118:121], v[166:169], a[152:155], v[118:121]
ds_read_b64_tr_b16 v[72:73], v163 offset:20736
v_mfma_f32_16x16x32_bf16 v[134:137], v[62:65], v[166:169], v[134:137]
ds_read_b128 v[182:185], v51 offset:14336
v_mfma_f32_16x16x32_bf16 v[122:125], a[144:147], v[174:177], v[122:125]
ds_read_b128 v[190:193], v51 offset:32768
global_load_lds_dwordx4 v13, s[42:43] offset:0 sc1
v_mfma_f32_16x16x32_bf16 v[126:129], a[148:151], v[174:177], v[126:129]
ds_read_b64_tr_b16 a[180:181], v255 offset:36864
v_mfma_f32_16x16x32_bf16 v[130:133], v[174:177], a[152:155], v[130:133]
ds_read_b64_tr_b16 a[182:183], v255 offset:39168
v_mfma_f32_16x16x32_bf16 v[158:161], v[62:65], v[174:177], v[158:161]
ds_read_b64_tr_b16 a[184:185], v255 offset:37056
v_mfma_f32_16x16x32_bf16 v[86:89], a[156:159], v[106:109], v[86:89]
global_load_lds_dwordx4 v14, s[42:43] offset:1024 sc1
ds_read_b64_tr_b16 a[186:187], v255 offset:39360
v_mfma_f32_16x16x32_bf16 v[90:93], a[160:163], v[106:109], v[90:93]
ds_read_b64_tr_b16 a[188:189], v255 offset:37248
v_mfma_f32_16x16x32_bf16 v[94:97], v[106:109], a[164:167], v[94:97]
ds_read_b64_tr_b16 a[190:191], v255 offset:39552
v_mfma_f32_16x16x32_bf16 v[98:101], v[66:69], v[106:109], v[98:101]
global_load_lds_dwordx4 v15, s[42:43] offset:2048 sc1
ds_read_b64_tr_b16 v[74:75], v163 offset:36864
v_mfma_f32_16x16x32_bf16 v[110:113], a[156:159], v[170:173], v[110:113]
ds_read_b64_tr_b16 v[76:77], v163 offset:39168
v_mfma_f32_16x16x32_bf16 v[114:117], a[160:163], v[170:173], v[114:117]
ds_read_b128 v[186:189], v52 offset:14336
v_mfma_f32_16x16x32_bf16 v[118:121], v[170:173], a[164:167], v[118:121]
s_sub_u32 s42, s42, 18432
s_subb_u32 s43, s43, 0
ds_read_b128 v[194:197], v52 offset:32768
v_mfma_f32_16x16x32_bf16 v[134:137], v[66:69], v[170:173], v[134:137]
v_mfma_f32_16x16x32_bf16 v[122:125], a[156:159], v[178:181], v[122:125]
v_mfma_f32_16x16x32_bf16 v[126:129], a[160:163], v[178:181], v[126:129]
v_mfma_f32_16x16x32_bf16 v[130:133], v[178:181], a[164:167], v[130:133]
v_mfma_f32_16x16x32_bf16 v[158:161], v[66:69], v[178:181], v[158:161]
s_waitcnt vmcnt(5) lgkmcnt(0)
s_barrier
s_add_u32 m0, s49, 18432
ds_read_b64_tr_b16 a[192:193], v255 offset:55296
v_mfma_f32_16x16x32_bf16 v[86:89], a[168:171], v[106:109], v[86:89]
ds_read_b64_tr_b16 a[194:195], v255 offset:57600
v_mfma_f32_16x16x32_bf16 v[90:93], a[172:175], v[106:109], v[90:93]
ds_read_b64_tr_b16 a[196:197], v255 offset:55488
v_mfma_f32_16x16x32_bf16 v[94:97], v[106:109], a[176:179], v[94:97]
ds_read_b64_tr_b16 a[198:199], v255 offset:57792
global_load_lds_dwordx4 v11, s[42:43] offset:-2048 sc1
v_mfma_f32_16x16x32_bf16 v[98:101], v[70:73], v[106:109], v[98:101]
ds_read_b64_tr_b16 a[200:201], v255 offset:55680
v_mfma_f32_16x16x32_bf16 v[110:113], a[168:171], v[182:185], v[110:113]
ds_read_b64_tr_b16 a[202:203], v255 offset:57984
v_mfma_f32_16x16x32_bf16 v[114:117], a[172:175], v[182:185], v[114:117]
ds_read_b64_tr_b16 v[78:79], v163 offset:55296
global_load_lds_dwordx4 v12, s[42:43] offset:-1024 sc1
v_mfma_f32_16x16x32_bf16 v[118:121], v[182:185], a[176:179], v[118:121]
ds_read_b64_tr_b16 v[80:81], v163 offset:57600
v_mfma_f32_16x16x32_bf16 v[134:137], v[70:73], v[182:185], v[134:137]
ds_read_b128 v[166:169], v51 offset:16384
v_mfma_f32_16x16x32_bf16 v[122:125], a[168:171], v[190:193], v[122:125]
ds_read_b128 v[174:177], v51 offset:34816
global_load_lds_dwordx4 v13, s[42:43] offset:0 sc1
v_mfma_f32_16x16x32_bf16 v[126:129], a[172:175], v[190:193], v[126:129]
ds_read_b64_tr_b16 a[204:205], v162 offset:8192
v_mfma_f32_16x16x32_bf16 v[130:133], v[190:193], a[176:179], v[130:133]
ds_read_b64_tr_b16 a[206:207], v162 offset:10496
v_mfma_f32_16x16x32_bf16 v[158:161], v[70:73], v[190:193], v[158:161]
ds_read_b64_tr_b16 a[208:209], v162 offset:8384
global_load_lds_dwordx4 v14, s[42:43] offset:1024 sc1
v_mfma_f32_16x16x32_bf16 v[86:89], a[180:183], v[106:109], v[86:89]
ds_read_b64_tr_b16 a[210:211], v162 offset:10688
v_mfma_f32_16x16x32_bf16 v[90:93], a[184:187], v[106:109], v[90:93]
ds_read_b64_tr_b16 a[212:213], v162 offset:8576
v_mfma_f32_16x16x32_bf16 v[94:97], v[106:109], a[188:191], v[94:97]
ds_read_b64_tr_b16 a[214:215], v162 offset:10880
global_load_lds_dwordx4 v15, s[42:43] offset:2048 sc1
v_mfma_f32_16x16x32_bf16 v[98:101], v[74:77], v[106:109], v[98:101]
ds_read_b64_tr_b16 v[82:83], v164 offset:8192
v_mfma_f32_16x16x32_bf16 v[110:113], a[180:183], v[186:189], v[110:113]
ds_read_b64_tr_b16 v[84:85], v164 offset:10496
v_mfma_f32_16x16x32_bf16 v[114:117], a[184:187], v[186:189], v[114:117]
ds_read_b128 v[170:173], v52 offset:16384
s_sub_u32 s42, s42, 18432
s_subb_u32 s43, s43, 0
v_mfma_f32_16x16x32_bf16 v[118:121], v[186:189], a[188:191], v[118:121]
ds_read_b128 v[178:181], v52 offset:34816
v_mfma_f32_16x16x32_bf16 v[134:137], v[74:77], v[186:189], v[134:137]
v_mfma_f32_16x16x32_bf16 v[122:125], a[180:183], v[194:197], v[122:125]
v_mfma_f32_16x16x32_bf16 v[126:129], a[184:187], v[194:197], v[126:129]
v_mfma_f32_16x16x32_bf16 v[158:161], v[74:77], v[194:197], v[158:161]
s_waitcnt lgkmcnt(15)
v_mfma_f32_16x16x32_bf16 v[86:89], a[192:195], v[106:109], v[86:89]
v_mfma_f32_16x16x32_bf16 v[90:93], a[196:199], v[106:109], v[90:93]
s_waitcnt lgkmcnt(14)
v_mfma_f32_16x16x32_bf16 v[94:97], v[106:109], a[200:203], v[94:97]
s_waitcnt lgkmcnt(12)
v_mfma_f32_16x16x32_bf16 v[98:101], v[78:81], v[106:109], v[98:101]
s_waitcnt lgkmcnt(11)
v_mfma_f32_16x16x32_bf16 v[110:113], a[192:195], v[166:169], v[110:113]
v_mfma_f32_16x16x32_bf16 v[114:117], a[196:199], v[166:169], v[114:117]
v_mfma_f32_16x16x32_bf16 v[118:121], v[166:169], a[200:203], v[118:121]
v_mfma_f32_16x16x32_bf16 v[134:137], v[78:81], v[166:169], v[134:137]
s_waitcnt lgkmcnt(10)
v_mfma_f32_16x16x32_bf16 v[158:161], v[78:81], v[174:177], v[158:161]
s_waitcnt lgkmcnt(8)
v_mfma_f32_16x16x32_bf16 v[86:89], a[204:207], v[106:109], v[86:89]
s_waitcnt lgkmcnt(6)
v_mfma_f32_16x16x32_bf16 v[90:93], a[208:211], v[106:109], v[90:93]
s_waitcnt lgkmcnt(4)
v_mfma_f32_16x16x32_bf16 v[94:97], v[106:109], a[212:215], v[94:97]
s_waitcnt lgkmcnt(2)
v_mfma_f32_16x16x32_bf16 v[98:101], v[82:85], v[106:109], v[98:101]
s_waitcnt lgkmcnt(1)
v_mfma_f32_16x16x32_bf16 v[110:113], a[204:207], v[170:173], v[110:113]
v_mfma_f32_16x16x32_bf16 v[114:117], a[208:211], v[170:173], v[114:117]
v_mfma_f32_16x16x32_bf16 v[118:121], v[170:173], a[212:215], v[118:121]
v_mfma_f32_16x16x32_bf16 v[134:137], v[82:85], v[170:173], v[134:137]
s_waitcnt lgkmcnt(0)
v_mfma_f32_16x16x32_bf16 v[158:161], v[82:85], v[178:181], v[158:161]
s_barrier
ds_read_b32 v242, v24 offset:0
s_waitcnt lgkmcnt(0)
s_nop 2
v_fma_f32 v230, v98, v242, v134
v_fma_f32 v231, v99, v242, v135
v_fma_f32 v232, v100, v242, v136
v_fma_f32 v233, v101, v242, v137
v_cvt_pk_bf16_f32 v230, v230, v231
v_cvt_pk_bf16_f32 v231, v232, v233
ds_write_b64 v22, v[230:231] offset:40960
ds_read_b128 v[202:205], v52 offset:32768
ds_read_b128 v[206:209], v51 offset:34816
ds_read_b128 v[210:213], v52 offset:34816
v_mov_b32 v234, v32
v_mov_b32 v235, v33
v_mov_b32 v236, v34
v_mov_b32 v237, v35
v_mov_b32 v134, v158
v_mov_b32 v135, v159
v_mov_b32 v136, v160
v_mov_b32 v137, v161
s_waitcnt lgkmcnt(3)
s_barrier
ds_read_b32 v23, v24 offset:0
ds_read_b32 v148, v21
ds_read_b32 v26, v24 offset:192
ds_read_b32 v53, v27
ds_read_b32 v8, v28
ds_read_b128 v[226:229], v25 offset:0
ds_read_b64 v[158:159], v22 offset:41472
ds_read_b32 v140, v20
v_xor_b32 v51, 0x10000, v51
v_xor_b32 v52, 0x10000, v52
s_waitcnt lgkmcnt(7)
v_fma_f32 v238, v90, v23, v114
v_fma_f32 v239, v91, v23, v115
v_fma_f32 v240, v92, v23, v116
v_fma_f32 v241, v93, v23, v117
v_cvt_pk_bf16_f32 v146, v238, v239
v_cvt_pk_bf16_f32 v147, v240, v241
s_waitcnt lgkmcnt(0)
s_barrier
ds_read_b32 v242, v24 offset:64
v_fma_f32 v238, v94, v226, v118
v_fma_f32 v239, v94, v227, v119
v_fma_f32 v240, v94, v228, v120
v_fma_f32 v241, v94, v229, v121
v_cvt_pk_bf16_f32 v154, v238, v239
v_cvt_pk_bf16_f32 v155, v240, v241
v_fma_f32 v238, v86, v23, v110
v_fma_f32 v239, v87, v23, v111
v_fma_f32 v240, v88, v23, v112
v_fma_f32 v241, v89, v23, v113
v_cvt_pk_bf16_f32 v138, v238, v239
v_cvt_pk_bf16_f32 v139, v240, v241
s_waitcnt lgkmcnt(0)
v_fma_f32 v230, v98, v242, v134
v_fma_f32 v231, v99, v242, v135
v_fma_f32 v232, v100, v242, v136
v_fma_f32 v233, v101, v242, v137
v_cvt_pk_bf16_f32 v230, v230, v231
v_cvt_pk_bf16_f32 v231, v232, v233
ds_write_b64 v22, v[230:231] offset:40960
s_waitcnt vmcnt(5) lgkmcnt(0)
s_barrier
s_mov_b32 s70, 32
s_waitcnt lgkmcnt(0)
s_nop 1
s_branch .Lqa_in5
.p2align 6
.Lqa_top24:
s_waitcnt lgkmcnt(9)
v_mfma_f32_16x16x32_bf16 v[134:137], a[216:219], v[44:47], 0
s_waitcnt lgkmcnt(8)
ds_read_b128 v[162:165], v52 offset:4096
ds_read_b32 v8, v30
v_mfma_f32_16x16x32_bf16 v[134:137], a[220:223], v[102:105], v[134:137]
s_waitcnt lgkmcnt(9)
global_store_dwordx4 v16, v[230:233], s[38:39] sc1
v_mfma_f32_16x16x32_bf16 v[110:113], a[0:3], v[44:47], 0
s_waitcnt lgkmcnt(6)
ds_read_b128 v[166:169], v51 offset:6144
v_add_u32 v16, 18432, v16
v_mfma_f32_16x16x32_bf16 v[134:137], a[224:227], v[106:109], v[134:137]
s_waitcnt lgkmcnt(5)
ds_read_b128 v[170:173], v52 offset:6144
ds_read_b128 v[226:229], v25 offset:64
v_mfma_f32_16x16x32_bf16 v[134:137], a[228:231], v[0:3], v[134:137]
ds_read_b64 v[160:161], v22 offset:41472
ds_read_b32 v144, v20
v_mfma_f32_16x16x32_bf16 v[114:117], a[4:7], v[44:47], 0
s_waitcnt lgkmcnt(7)
ds_read_b128 v[174:177], v51 offset:8192
v_fma_f32 v238, v90, v23, v126
v_mfma_f32_16x16x32_bf16 v[134:137], a[232:235], v[4:7], v[134:137]
s_waitcnt lgkmcnt(7)
ds_read_b128 v[178:181], v52 offset:8192
v_fma_f32 v239, v91, v23, v127
v_mfma_f32_16x16x32_bf16 v[134:137], a[236:239], v[162:165], v[134:137]
v_fma_f32 v240, v92, v23, v128
v_fma_f32 v241, v93, v23, v129
v_mfma_f32_16x16x32_bf16 v[118:121], v[44:47], a[8:11], 0
s_waitcnt lgkmcnt(6)
ds_read_b128 v[182:185], v51 offset:10240
v_cvt_pk_bf16_f32 v150, v238, v239
v_mfma_f32_16x16x32_bf16 v[134:137], a[240:243], v[166:169], v[134:137]
s_waitcnt lgkmcnt(6)
ds_read_b128 v[186:189], v52 offset:10240
v_cvt_pk_bf16_f32 v151, v240, v241
v_mfma_f32_16x16x32_bf16 v[134:137], a[244:247], v[170:173], v[134:137]
s_nop 0
v_mfma_f32_16x16x32_bf16 v[214:217], v[150:153], v[138:141], v[32:35]
v_max_f32 v48, v218, v219
v_max_f32 v49, v220, v221
v_mfma_f32_16x16x32_bf16 v[110:113], a[12:15], v[102:105], v[110:113]
s_waitcnt lgkmcnt(6)
ds_read_b128 v[190:193], v51 offset:12288
v_fma_f32 v238, v94, v226, v130
s_waitcnt lgkmcnt(4)
v_mfma_f32_16x16x32_bf16 v[134:137], a[248:251], v[174:177], v[134:137]
s_xor_b32 s49, s49, 0x10000
ds_read_b128 v[194:197], v52 offset:12288
s_waitcnt lgkmcnt(4)
v_mfma_f32_16x16x32_bf16 v[134:137], a[252:255], v[178:181], v[134:137]
s_add_u32 m0, s49, 0
v_mul_f32 v26, 0x3eae1a93, v26
v_mul_f32 v9, v53, v26
v_mfma_f32_16x16x32_bf16 v[114:117], a[16:19], v[102:105], v[114:117]
s_waitcnt lgkmcnt(3)
ds_read_b128 v[198:201], v51 offset:14336
global_load_lds_dwordx4 v11, s[42:43] offset:-2048 sc1
v_mfma_f32_16x16x32_bf16 v[134:137], v[54:57], v[182:185], v[134:137]
s_waitcnt lgkmcnt(3)
ds_read_b128 v[202:205], v52 offset:14336
v_mul_f32 v10, v8, v26
v_mfma_f32_16x16x32_bf16 v[134:137], v[58:61], v[186:189], v[134:137]
v_max3_f32 v48, v214, v215, v48
v_max3_f32 v49, v216, v217, v49
v_mfma_f32_16x16x32_bf16 v[118:121], v[102:105], a[20:23], v[118:121]
s_waitcnt lgkmcnt(3)
ds_read_b128 v[206:209], v51 offset:16384
v_mul_f32 v48, v48, v9
v_mfma_f32_16x16x32_bf16 v[134:137], v[62:65], v[190:193], v[134:137]
s_waitcnt lgkmcnt(3)
ds_read_b128 v[210:213], v52 offset:16384
v_mul_f32 v49, v49, v10
v_mfma_f32_16x16x32_bf16 v[134:137], v[66:69], v[194:197], v[134:137]
v_max_f32 v50, v48, v49
v_mov_b32 v95, v50
v_mfma_f32_16x16x32_bf16 v[110:113], a[24:27], v[106:109], v[110:113]
s_waitcnt lgkmcnt(3)
v_permlane16_swap_b32 v50, v95
v_mfma_f32_16x16x32_bf16 v[134:137], v[70:73], v[198:201], v[134:137]
s_waitcnt lgkmcnt(2)
v_max_f32 v50, v50, v95
v_mov_b32 v95, v50
v_mfma_f32_16x16x32_bf16 v[134:137], v[74:77], v[202:205], v[134:137]
global_load_lds_dwordx4 v12, s[42:43] offset:-1024 sc1
v_fma_f32 v239, v94, v227, v131
v_mfma_f32_16x16x32_bf16 v[114:117], a[28:31], v[106:109], v[114:117]
s_waitcnt lgkmcnt(1)
v_permlane32_swap_b32 v50, v95
v_mfma_f32_16x16x32_bf16 v[134:137], v[78:81], v[206:209], v[134:137]
s_waitcnt lgkmcnt(0)
v_max_f32 v50, v50, v95
v_sub_f32 v48, v48, v50
v_mfma_f32_16x16x32_bf16 v[134:137], v[82:85], v[210:213], v[134:137]
v_sub_f32 v49, v49, v50
v_fma_f32 v240, v94, v228, v132
v_mfma_f32_16x16x32_bf16 v[118:121], v[106:109], a[32:35], v[118:121]
v_exp_f32 v96, v48
v_mfma_f32_16x16x32_bf16 v[110:113], a[36:39], v[0:3], v[110:113]
v_exp_f32 v97, v49
v_mfma_f32_16x16x32_bf16 v[114:117], a[40:43], v[0:3], v[114:117]
v_add_f32 v50, v96, v97
v_mov_b32 v95, v50
v_mfma_f32_16x16x32_bf16 v[118:121], v[0:3], a[44:47], v[118:121]
v_mul_f32 v96, v96, v53
v_fma_f32 v241, v94, v229, v133
v_mfma_f32_16x16x32_bf16 v[110:113], a[48:51], v[4:7], v[110:113]
global_load_lds_dwordx4 v13, s[42:43] offset:0 sc1
v_cvt_pk_bf16_f32 v156, v238, v239
v_mfma_f32_16x16x32_bf16 v[114:117], a[52:55], v[4:7], v[114:117]
v_permlane16_swap_b32 v50, v95
v_mfma_f32_16x16x32_bf16 v[118:121], v[4:7], a[56:59], v[118:121]
v_add_f32 v50, v50, v95
v_mov_b32 v95, v50
v_mfma_f32_16x16x32_bf16 v[110:113], a[60:63], v[162:165], v[110:113]
v_mul_f32 v97, v97, v8
v_cvt_pk_bf16_f32 v157, v240, v241
v_mfma_f32_16x16x32_bf16 v[114:117], a[64:67], v[162:165], v[114:117]
v_permlane32_swap_b32 v50, v95
v_mfma_f32_16x16x32_bf16 v[118:121], v[162:165], a[68:71], v[118:121]
v_mfma_f32_16x16x32_bf16 v[110:113], a[72:75], v[166:169], v[110:113]
s_barrier
v_mfma_f32_16x16x32_bf16 v[114:117], a[76:79], v[166:169], v[114:117]
ds_read_b32 v242, v24 offset:0
v_add_f32 v50, v50, v95
v_mfma_f32_16x16x32_bf16 v[118:121], v[166:169], a[80:83], v[118:121]
global_load_lds_dwordx4 v14, s[42:43] offset:1024 sc1
v_fma_f32 v238, v86, v23, v122
v_mfma_f32_16x16x32_bf16 v[110:113], a[84:87], v[170:173], v[110:113]
)"
R"(v_rcp_f32 v50, v50
v_mfma_f32_16x16x32_bf16 v[114:117], a[88:91], v[170:173], v[114:117]
v_mul_f32 v96, v96, v50
v_mul_f32 v97, v97, v50
v_mfma_f32_16x16x32_bf16 v[118:121], v[170:173], a[92:95], v[118:121]
v_cvt_pk_bf16_f32 v96, v96, v96
v_cvt_pk_bf16_f32 v97, v97, v97
v_mfma_f32_16x16x32_bf16 v[110:113], a[96:99], v[174:177], v[110:113]
v_and_b32 v224, v96, v40
v_and_b32 v225, v97, v41
v_mfma_f32_16x16x32_bf16 v[114:117], a[100:103], v[174:177], v[114:117]
v_and_b32 v222, v96, v42
v_and_b32 v223, v97, v43
v_mfma_f32_16x16x32_bf16 v[118:121], v[174:177], a[104:107], v[118:121]
s_nop 0
v_mfma_f32_16x16x32_bf16 v[218:221], v[154:157], v[222:225], 0
v_mfma_f32_16x16x32_bf16 v[214:217], v[158:161], v[222:225], 0
s_waitcnt lgkmcnt(0)
v_fma_f32 v230, v98, v242, v134
v_fma_f32 v231, v99, v242, v135
v_mfma_f32_16x16x32_bf16 v[110:113], a[108:111], v[178:181], v[110:113]
v_fma_f32 v232, v100, v242, v136
v_fma_f32 v233, v101, v242, v137
v_mfma_f32_16x16x32_bf16 v[114:117], a[112:115], v[178:181], v[114:117]
global_load_lds_dwordx4 v15, s[42:43] offset:2048 sc1
s_add_u32 s42, s42, s46
s_addc_u32 s43, s43, s47
v_mfma_f32_16x16x32_bf16 v[118:121], v[178:181], a[116:119], v[118:121]
v_cvt_pk_bf16_f32 v230, v230, v231
v_cvt_pk_bf16_f32 v231, v232, v233
v_mfma_f32_16x16x32_bf16 v[110:113], a[120:123], v[182:185], v[110:113]
v_cvt_pk_bf16_f32 v254, v218, v219
v_cvt_pk_bf16_f32 v255, v220, v221
v_mfma_f32_16x16x32_bf16 v[114:117], a[124:127], v[182:185], v[114:117]
ds_write_b64 v18, v[254:255]
v_mfma_f32_16x16x32_bf16 v[118:121], v[182:185], a[128:131], v[118:121]
v_cvt_pk_bf16_f32 v96, v214, v215
v_fma_f32 v239, v87, v23, v123
v_mfma_f32_16x16x32_bf16 v[110:113], a[132:135], v[186:189], v[110:113]
ds_write_b32 v19, v96
v_mfma_f32_16x16x32_bf16 v[114:117], a[136:139], v[186:189], v[114:117]
ds_write_b64 v22, v[230:231] offset:40960
v_mfma_f32_16x16x32_bf16 v[118:121], v[186:189], a[140:143], v[118:121]
v_fma_f32 v240, v88, v23, v124
v_fma_f32 v241, v89, v23, v125
v_mfma_f32_16x16x32_bf16 v[110:113], a[144:147], v[190:193], v[110:113]
v_cvt_pk_bf16_f32 v142, v238, v239
v_cvt_pk_bf16_f32 v143, v240, v241
v_mfma_f32_16x16x32_bf16 v[114:117], a[148:151], v[190:193], v[114:117]
v_mfma_f32_16x16x32_bf16 v[118:121], v[190:193], a[152:155], v[118:121]
v_mfma_f32_16x16x32_bf16 v[110:113], a[156:159], v[194:197], v[110:113]
v_mfma_f32_16x16x32_bf16 v[114:117], a[160:163], v[194:197], v[114:117]
v_mfma_f32_16x16x32_bf16 v[118:121], v[194:197], a[164:167], v[118:121]
v_mfma_f32_16x16x32_bf16 v[110:113], a[168:171], v[198:201], v[110:113]
v_mfma_f32_16x16x32_bf16 v[114:117], a[172:175], v[198:201], v[114:117]
v_mfma_f32_16x16x32_bf16 v[118:121], v[198:201], a[176:179], v[118:121]
v_mfma_f32_16x16x32_bf16 v[110:113], a[180:183], v[202:205], v[110:113]
v_mfma_f32_16x16x32_bf16 v[114:117], a[184:187], v[202:205], v[114:117]
s_waitcnt vmcnt(5) lgkmcnt(0)
s_barrier
v_mfma_f32_16x16x32_bf16 v[118:121], v[202:205], a[188:191], v[118:121]
ds_read_b128 v[44:47], v51 offset:18432
v_mfma_f32_16x16x32_bf16 v[110:113], a[192:195], v[206:209], v[110:113]
ds_read_b128 v[102:105], v52 offset:18432
ds_read_b128 v[230:233], v17 offset:0
v_mfma_f32_16x16x32_bf16 v[114:117], a[196:199], v[206:209], v[114:117]
ds_read_b32 v23, v24 offset:0
ds_read_b32 v148, v21
v_mfma_f32_16x16x32_bf16 v[218:221], v[150:153], v[142:145], v[36:39]
v_mfma_f32_16x16x32_bf16 v[118:121], v[206:209], a[200:203], v[118:121]
ds_read_b128 v[106:109], v51 offset:20480
ds_read_b32 v26, v24 offset:192
v_mfma_f32_16x16x32_bf16 v[110:113], a[204:207], v[210:213], v[110:113]
ds_read_b128 v[0:3], v52 offset:20480
ds_read_b32 v53, v27
v_mfma_f32_16x16x32_bf16 v[114:117], a[208:211], v[210:213], v[114:117]
ds_read_b32 v8, v28
ds_read_b128 v[226:229], v25 offset:0
v_mfma_f32_16x16x32_bf16 v[118:121], v[210:213], a[212:215], v[118:121]
s_waitcnt lgkmcnt(10)
ds_read_b128 v[4:7], v51 offset:22528
ds_read_b64 v[158:159], v22 offset:41472
v_mfma_f32_16x16x32_bf16 v[134:137], a[216:219], v[44:47], 0
s_waitcnt lgkmcnt(11)
ds_read_b128 v[162:165], v52 offset:22528
ds_read_b32 v140, v20
v_mfma_f32_16x16x32_bf16 v[134:137], a[220:223], v[102:105], v[134:137]
s_waitcnt lgkmcnt(12)
global_store_dwordx4 v16, v[230:233], s[38:39] sc1
v_mfma_f32_16x16x32_bf16 v[122:125], a[0:3], v[44:47], 0
s_waitcnt lgkmcnt(9)
ds_read_b128 v[166:169], v51 offset:24576
v_add_u32 v16, 18432, v16
v_mfma_f32_16x16x32_bf16 v[134:137], a[224:227], v[106:109], v[134:137]
s_waitcnt lgkmcnt(8)
ds_read_b128 v[170:173], v52 offset:24576
v_fma_f32 v238, v90, v23, v114
v_mfma_f32_16x16x32_bf16 v[134:137], a[228:231], v[0:3], v[134:137]
v_fma_f32 v239, v91, v23, v115
v_fma_f32 v240, v92, v23, v116
v_mfma_f32_16x16x32_bf16 v[126:129], a[4:7], v[44:47], 0
s_waitcnt lgkmcnt(5)
ds_read_b128 v[174:177], v51 offset:26624
v_fma_f32 v241, v93, v23, v117
v_mfma_f32_16x16x32_bf16 v[134:137], a[232:235], v[4:7], v[134:137]
s_waitcnt lgkmcnt(4)
ds_read_b128 v[178:181], v52 offset:26624
v_cvt_pk_bf16_f32 v146, v238, v239
v_mfma_f32_16x16x32_bf16 v[134:137], a[236:239], v[162:165], v[134:137]
v_cvt_pk_bf16_f32 v147, v240, v241
v_max_f32 v48, v218, v219
v_mfma_f32_16x16x32_bf16 v[130:133], v[44:47], a[8:11], 0
ds_read_b128 v[182:185], v51 offset:28672
v_mfma_f32_16x16x32_bf16 v[214:217], v[146:149], v[142:145], v[234:237]
s_waitcnt lgkmcnt(4)
v_max_f32 v49, v220, v221
v_fma_f32 v238, v94, v226, v118
v_mfma_f32_16x16x32_bf16 v[134:137], a[240:243], v[166:169], v[134:137]
s_waitcnt lgkmcnt(3)
ds_read_b128 v[186:189], v52 offset:28672
v_fma_f32 v239, v94, v227, v119
v_mfma_f32_16x16x32_bf16 v[134:137], a[244:247], v[170:173], v[134:137]
v_mul_f32 v26, 0x3eae1a93, v26
v_mul_f32 v9, v53, v26
v_mfma_f32_16x16x32_bf16 v[122:125], a[12:15], v[102:105], v[122:125]
s_waitcnt lgkmcnt(3)
ds_read_b128 v[190:193], v51 offset:30720
v_mul_f32 v10, v8, v26
v_mfma_f32_16x16x32_bf16 v[134:137], a[248:251], v[174:177], v[134:137]
s_add_u32 m0, s49, 18432
ds_read_b128 v[194:197], v52 offset:30720
s_waitcnt lgkmcnt(4)
v_mfma_f32_16x16x32_bf16 v[134:137], a[252:255], v[178:181], v[134:137]
global_load_lds_dwordx4 v11, s[42:43] offset:-2048 sc1
v_max3_f32 v48, v214, v215, v48
v_mfma_f32_16x16x32_bf16 v[126:129], a[16:19], v[102:105], v[126:129]
s_waitcnt lgkmcnt(3)
ds_read_b128 v[198:201], v51 offset:32768
v_max3_f32 v49, v216, v217, v49
v_mfma_f32_16x16x32_bf16 v[134:137], v[54:57], v[182:185], v[134:137]
s_waitcnt lgkmcnt(3)
ds_read_b128 v[202:205], v52 offset:32768
v_mul_f32 v48, v48, v9
v_mfma_f32_16x16x32_bf16 v[134:137], v[58:61], v[186:189], v[134:137]
v_mul_f32 v49, v49, v10
v_max_f32 v50, v48, v49
v_mfma_f32_16x16x32_bf16 v[130:133], v[102:105], a[20:23], v[130:133]
s_waitcnt lgkmcnt(3)
ds_read_b128 v[206:209], v51 offset:34816
v_mov_b32 v95, v50
v_mfma_f32_16x16x32_bf16 v[134:137], v[62:65], v[190:193], v[134:137]
s_waitcnt lgkmcnt(3)
ds_read_b128 v[210:213], v52 offset:34816
v_fma_f32 v240, v94, v228, v120
v_mfma_f32_16x16x32_bf16 v[134:137], v[66:69], v[194:197], v[134:137]
v_permlane16_swap_b32 v50, v95
v_mfma_f32_16x16x32_bf16 v[122:125], a[24:27], v[106:109], v[122:125]
s_waitcnt lgkmcnt(3)
v_max_f32 v50, v50, v95
v_mov_b32 v95, v50
v_mfma_f32_16x16x32_bf16 v[134:137], v[70:73], v[198:201], v[134:137]
s_waitcnt lgkmcnt(2)
global_load_lds_dwordx4 v12, s[42:43] offset:-1024 sc1
v_fma_f32 v241, v94, v229, v121
v_mfma_f32_16x16x32_bf16 v[134:137], v[74:77], v[202:205], v[134:137]
v_permlane32_swap_b32 v50, v95
v_mfma_f32_16x16x32_bf16 v[126:129], a[28:31], v[106:109], v[126:129]
s_waitcnt lgkmcnt(1)
v_max_f32 v50, v50, v95
v_sub_f32 v48, v48, v50
v_mfma_f32_16x16x32_bf16 v[134:137], v[78:81], v[206:209], v[134:137]
s_waitcnt lgkmcnt(0)
v_sub_f32 v49, v49, v50
v_cvt_pk_bf16_f32 v154, v238, v239
v_mfma_f32_16x16x32_bf16 v[134:137], v[82:85], v[210:213], v[134:137]
v_exp_f32 v96, v48
v_mfma_f32_16x16x32_bf16 v[130:133], v[106:109], a[32:35], v[130:133]
v_exp_f32 v97, v49
v_mfma_f32_16x16x32_bf16 v[122:125], a[36:39], v[0:3], v[122:125]
v_add_f32 v50, v96, v97
v_mov_b32 v95, v50
v_mfma_f32_16x16x32_bf16 v[126:129], a[40:43], v[0:3], v[126:129]
v_mul_f32 v96, v96, v53
v_cvt_pk_bf16_f32 v155, v240, v241
v_mfma_f32_16x16x32_bf16 v[130:133], v[0:3], a[44:47], v[130:133]
global_load_lds_dwordx4 v13, s[42:43] offset:0 sc1
v_fma_f32 v238, v86, v23, v110
v_mfma_f32_16x16x32_bf16 v[122:125], a[48:51], v[4:7], v[122:125]
v_permlane16_swap_b32 v50, v95
v_mfma_f32_16x16x32_bf16 v[126:129], a[52:55], v[4:7], v[126:129]
v_add_f32 v50, v50, v95
v_mov_b32 v95, v50
v_mfma_f32_16x16x32_bf16 v[130:133], v[4:7], a[56:59], v[130:133]
v_mul_f32 v97, v97, v8
v_fma_f32 v239, v87, v23, v111
v_mfma_f32_16x16x32_bf16 v[122:125], a[60:63], v[162:165], v[122:125]
v_permlane32_swap_b32 v50, v95
v_mfma_f32_16x16x32_bf16 v[126:129], a[64:67], v[162:165], v[126:129]
v_add_f32 v50, v50, v95
v_fma_f32 v240, v88, v23, v112
v_mfma_f32_16x16x32_bf16 v[130:133], v[162:165], a[68:71], v[130:133]
v_mfma_f32_16x16x32_bf16 v[122:125], a[72:75], v[166:169], v[122:125]
s_barrier
v_mfma_f32_16x16x32_bf16 v[126:129], a[76:79], v[166:169], v[126:129]
global_load_lds_dwordx4 v14, s[42:43] offset:1024 sc1
ds_read_b32 v242, v24 offset:64
v_mfma_f32_16x16x32_bf16 v[130:133], v[166:169], a[80:83], v[130:133]
v_rcp_f32 v50, v50
v_mfma_f32_16x16x32_bf16 v[122:125], a[84:87], v[170:173], v[122:125]
v_mul_f32 v96, v96, v50
v_mul_f32 v97, v97, v50
v_mfma_f32_16x16x32_bf16 v[126:129], a[88:91], v[170:173], v[126:129]
v_cvt_pk_bf16_f32 v96, v96, v96
v_cvt_pk_bf16_f32 v97, v97, v97
v_mfma_f32_16x16x32_bf16 v[130:133], v[170:173], a[92:95], v[130:133]
v_and_b32 v222, v96, v40
v_and_b32 v223, v97, v41
v_mfma_f32_16x16x32_bf16 v[122:125], a[96:99], v[174:177], v[122:125]
v_and_b32 v224, v96, v42
v_and_b32 v225, v97, v43
v_mfma_f32_16x16x32_bf16 v[126:129], a[100:103], v[174:177], v[126:129]
s_nop 0
v_mfma_f32_16x16x32_bf16 v[218:221], v[154:157], v[222:225], 0
v_mfma_f32_16x16x32_bf16 v[214:217], v[158:161], v[222:225], 0
s_waitcnt lgkmcnt(0)
v_fma_f32 v230, v98, v242, v134
v_fma_f32 v231, v99, v242, v135
v_mfma_f32_16x16x32_bf16 v[130:133], v[174:177], a[104:107], v[130:133]
v_fma_f32 v232, v100, v242, v136
v_fma_f32 v233, v101, v242, v137
v_mfma_f32_16x16x32_bf16 v[122:125], a[108:111], v[178:181], v[122:125]
global_load_lds_dwordx4 v15, s[42:43] offset:2048 sc1
s_sub_u32 s42, s42, 18432
s_subb_u32 s43, s43, 0
v_mfma_f32_16x16x32_bf16 v[126:129], a[112:115], v[178:181], v[126:129]
v_cvt_pk_bf16_f32 v230, v230, v231
v_cvt_pk_bf16_f32 v231, v232, v233
v_mfma_f32_16x16x32_bf16 v[130:133], v[178:181], a[116:119], v[130:133]
v_cvt_pk_bf16_f32 v254, v218, v219
v_cvt_pk_bf16_f32 v255, v220, v221
v_mfma_f32_16x16x32_bf16 v[122:125], a[120:123], v[182:185], v[122:125]
ds_write_b64 v18, v[254:255]
v_mfma_f32_16x16x32_bf16 v[126:129], a[124:127], v[182:185], v[126:129]
v_cvt_pk_bf16_f32 v96, v214, v215
v_fma_f32 v241, v89, v23, v113
v_mfma_f32_16x16x32_bf16 v[130:133], v[182:185], a[128:131], v[130:133]
ds_write_b32 v19, v96
v_mfma_f32_16x16x32_bf16 v[122:125], a[132:135], v[186:189], v[122:125]
ds_write_b64 v22, v[230:231] offset:40960
v_mfma_f32_16x16x32_bf16 v[126:129], a[136:139], v[186:189], v[126:129]
v_cvt_pk_bf16_f32 v138, v238, v239
v_cvt_pk_bf16_f32 v139, v240, v241
v_mfma_f32_16x16x32_bf16 v[130:133], v[186:189], a[140:143], v[130:133]
v_mfma_f32_16x16x32_bf16 v[122:125], a[144:147], v[190:193], v[122:125]
v_mfma_f32_16x16x32_bf16 v[126:129], a[148:151], v[190:193], v[126:129]
v_mfma_f32_16x16x32_bf16 v[130:133], v[190:193], a[152:155], v[130:133]
v_mfma_f32_16x16x32_bf16 v[122:125], a[156:159], v[194:197], v[122:125]
v_mfma_f32_16x16x32_bf16 v[126:129], a[160:163], v[194:197], v[126:129]
v_mfma_f32_16x16x32_bf16 v[130:133], v[194:197], a[164:167], v[130:133]
v_mfma_f32_16x16x32_bf16 v[122:125], a[168:171], v[198:201], v[122:125]
v_mfma_f32_16x16x32_bf16 v[126:129], a[172:175], v[198:201], v[126:129]
v_xor_b32 v51, 0x10000, v51
v_xor_b32 v52, 0x10000, v52
v_mfma_f32_16x16x32_bf16 v[130:133], v[198:201], a[176:179], v[130:133]
v_mfma_f32_16x16x32_bf16 v[122:125], a[180:183], v[202:205], v[122:125]
v_mfma_f32_16x16x32_bf16 v[126:129], a[184:187], v[202:205], v[126:129]
s_waitcnt vmcnt(5) lgkmcnt(0)
s_barrier
.Lqa_in5:
v_mfma_f32_16x16x32_bf16 v[130:133], v[202:205], a[188:191], v[130:133]
s_cmp_eq_u32 s70, s73
s_cbranch_scc1 .Lqa_hk6
.Lqa_hr7:
ds_read_b128 v[44:47], v51 offset:0
v_mfma_f32_16x16x32_bf16 v[122:125], a[192:195], v[206:209], v[122:125]
ds_read_b128 v[102:105], v52 offset:0
ds_read_b128 v[230:233], v17 offset:0
v_mfma_f32_16x16x32_bf16 v[126:129], a[196:199], v[206:209], v[126:129]
ds_read_b32 v23, v24 offset:64
ds_read_b32 v152, v21
v_mfma_f32_16x16x32_bf16 v[218:221], v[146:149], v[138:141], v[36:39]
v_mfma_f32_16x16x32_bf16 v[130:133], v[206:209], a[200:203], v[130:133]
ds_read_b128 v[106:109], v51 offset:2048
ds_read_b32 v26, v24 offset:128
v_mfma_f32_16x16x32_bf16 v[122:125], a[204:207], v[210:213], v[122:125]
ds_read_b128 v[0:3], v52 offset:2048
ds_read_b32 v53, v29
v_mfma_f32_16x16x32_bf16 v[126:129], a[208:211], v[210:213], v[126:129]
v_mfma_f32_16x16x32_bf16 v[130:133], v[210:213], a[212:215], v[130:133]
s_sub_u32 s70, s70, 1
ds_read_b128 v[4:7], v51 offset:4096
s_cbranch_scc0 .Lqa_top24
s_waitcnt lgkmcnt(0)
ds_read_b32 v8, v30
global_store_dwordx4 v16, v[230:233], s[38:39] sc1
v_add_u32 v16, 18432, v16
ds_read_b128 v[226:229], v25 offset:64
ds_read_b64 v[160:161], v22 offset:41472
ds_read_b32 v144, v20
v_fma_f32 v238, v90, v23, v126
v_fma_f32 v239, v91, v23, v127
v_fma_f32 v240, v92, v23, v128
v_fma_f32 v241, v93, v23, v129
v_cvt_pk_bf16_f32 v150, v238, v239
v_cvt_pk_bf16_f32 v151, v240, v241
s_nop 1
v_mfma_f32_16x16x32_bf16 v[214:217], v[150:153], v[138:141], v[32:35]
s_waitcnt lgkmcnt(2)
v_max_f32 v48, v218, v219
v_max_f32 v49, v220, v221
v_fma_f32 v238, v94, v226, v130
v_mul_f32 v26, 0x3eae1a93, v26
v_mul_f32 v9, v53, v26
v_mul_f32 v10, v8, v26
s_nop 0
v_max3_f32 v48, v214, v215, v48
v_max3_f32 v49, v216, v217, v49
v_mul_f32 v48, v48, v9
v_mul_f32 v49, v49, v10
v_max_f32 v50, v48, v49
v_mov_b32 v95, v50
s_nop 1
v_permlane16_swap_b32 v50, v95
v_max_f32 v50, v50, v95
v_mov_b32 v95, v50
v_fma_f32 v239, v94, v227, v131
s_nop 0
v_permlane32_swap_b32 v50, v95
v_max_f32 v50, v50, v95
v_sub_f32 v48, v48, v50
v_sub_f32 v49, v49, v50
v_fma_f32 v240, v94, v228, v132
v_exp_f32 v96, v48
v_exp_f32 v97, v49
s_nop 0
v_add_f32 v50, v96, v97
v_mov_b32 v95, v50
v_mul_f32 v96, v96, v53
v_fma_f32 v241, v94, v229, v133
v_cvt_pk_bf16_f32 v156, v238, v239
v_permlane16_swap_b32 v50, v95
v_add_f32 v50, v50, v95
v_mov_b32 v95, v50
v_mul_f32 v97, v97, v8
v_cvt_pk_bf16_f32 v157, v240, v241
v_permlane32_swap_b32 v50, v95
s_waitcnt lgkmcnt(0)
s_barrier
v_add_f32 v50, v50, v95
v_fma_f32 v238, v86, v23, v122
v_rcp_f32 v50, v50
s_nop 0
v_mul_f32 v96, v96, v50
v_mul_f32 v97, v97, v50
v_cvt_pk_bf16_f32 v96, v96, v96
v_cvt_pk_bf16_f32 v97, v97, v97
v_and_b32 v224, v96, v40
v_and_b32 v225, v97, v41
v_and_b32 v222, v96, v42
v_and_b32 v223, v97, v43
s_nop 1
v_mfma_f32_16x16x32_bf16 v[218:221], v[154:157], v[222:225], 0
v_mfma_f32_16x16x32_bf16 v[214:217], v[158:161], v[222:225], 0
s_nop 6
v_cvt_pk_bf16_f32 v254, v218, v219
v_cvt_pk_bf16_f32 v255, v220, v221
ds_write_b64 v18, v[254:255]
v_cvt_pk_bf16_f32 v96, v214, v215
v_fma_f32 v239, v87, v23, v123
ds_write_b32 v19, v96
v_fma_f32 v240, v88, v23, v124
v_fma_f32 v241, v89, v23, v125
v_cvt_pk_bf16_f32 v142, v238, v239
v_cvt_pk_bf16_f32 v143, v240, v241
s_waitcnt vmcnt(0)
s_waitcnt lgkmcnt(0)
s_barrier
ds_read_b128 v[230:233], v17 offset:0
s_waitcnt lgkmcnt(0)
global_store_dwordx4 v16, v[230:233], s[38:39] sc1
s_waitcnt vmcnt(0)
s_barrier
s_endpgm
.Lqa_w2:
s_getpc_b64 s[90:91]
.Lqa_pc44:
s_add_u32 s90, s90, .Lqa_e29-.Lqa_pc44
s_addc_u32 s91, s91, 0
s_branch .Lqa_k36
.Lqa_bk43:
v_add_u32 v255, 0x10000, v9
v_add_u32 v162, 0x20000, v9
v_add_u32 v163, 0x10000, v10
v_add_u32 v164, 0x20000, v10
v_mov_b32 v106, 0x3F803F80
v_mov_b32 v107, 0x3F803F80
v_mov_b32 v108, 0x3F803F80
v_mov_b32 v109, 0x3F803F80
v_mov_b32 v141, 0
v_mov_b32 v145, 0
)"
R"(v_mov_b32 v149, 0
v_mov_b32 v153, 0
v_mov_b32 v165, 46080
s_waitcnt lgkmcnt(0)
s_mul_i32 s37, s37, 0x9E3779B1
s_add_u32 s37, s37, 0x7F4A7C15
v_add_u32 v46, s37, v46
v_add_u32 v48, s37, v48
v_add_u32 v50, s37, v50
ds_write_b32 v165, v141
s_mov_b64 s[76:77], 0
s_mov_b32 s73, 18
s_add_u32 m0, s48, 4096
s_nop 0
global_load_lds_dwordx4 v0, s[40:41] offset:-4096
global_load_lds_dwordx4 v1, s[40:41] offset:-3072
global_load_lds_dwordx4 v2, s[40:41] offset:-2048
global_load_lds_dwordx4 v3, s[40:41] offset:-1024
global_load_lds_dwordx4 v4, s[40:41] offset:0
global_load_lds_dwordx4 v5, s[40:41] offset:1024
global_load_lds_dwordx4 v6, s[40:41] offset:2048
global_load_lds_dwordx4 v7, s[40:41] offset:3072
s_add_u32 m0, s48, 12288
s_nop 0
global_load_lds_dwordx4 v8, s[40:41] offset:-4096
s_add_u32 s40, s40, 221184
s_addc_u32 s41, s41, 0
s_add_u32 m0, s48, 40960
s_nop 0
global_load_lds_dwordx4 v0, s[40:41] offset:-4096
global_load_lds_dwordx4 v1, s[40:41] offset:-3072
global_load_lds_dwordx4 v2, s[40:41] offset:-2048
global_load_lds_dwordx4 v3, s[40:41] offset:-1024
global_load_lds_dwordx4 v4, s[40:41] offset:0
global_load_lds_dwordx4 v5, s[40:41] offset:1024
global_load_lds_dwordx4 v6, s[40:41] offset:2048
global_load_lds_dwordx4 v7, s[40:41] offset:3072
s_add_u32 m0, s48, 49152
s_nop 0
global_load_lds_dwordx4 v8, s[40:41] offset:-4096
s_add_u32 s40, s40, 221184
s_addc_u32 s41, s41, 0
s_waitcnt vmcnt(9) lgkmcnt(0)
s_barrier
s_add_u32 m0, s48, 77824
ds_read_b64_tr_b16 a[0:1], v9 offset:47104
ds_read_b64_tr_b16 a[2:3], v9 offset:49408
global_load_lds_dwordx4 v0, s[40:41] offset:-4096
ds_read_b64_tr_b16 a[4:5], v9 offset:47296
ds_read_b64_tr_b16 a[6:7], v9 offset:49600
global_load_lds_dwordx4 v1, s[40:41] offset:-3072
ds_read_b64_tr_b16 a[8:9], v9 offset:47488
global_load_lds_dwordx4 v2, s[40:41] offset:-2048
ds_read_b64_tr_b16 a[10:11], v9 offset:49792
ds_read_b64_tr_b16 a[216:217], v10 offset:47104
global_load_lds_dwordx4 v3, s[40:41] offset:-1024
ds_read_b64_tr_b16 a[218:219], v10 offset:49408
global_load_lds_dwordx4 v4, s[40:41] offset:0
ds_read_b128 v[166:169], v51 offset:0
ds_read_b128 v[174:177], v51 offset:18432
global_load_lds_dwordx4 v5, s[40:41] offset:1024
ds_read_b64_tr_b16 a[12:13], v255 offset:0
global_load_lds_dwordx4 v6, s[40:41] offset:2048
ds_read_b64_tr_b16 a[14:15], v255 offset:2304
ds_read_b64_tr_b16 a[16:17], v255 offset:192
global_load_lds_dwordx4 v7, s[40:41] offset:3072
ds_read_b64_tr_b16 a[18:19], v255 offset:2496
s_add_u32 m0, s48, 86016
ds_read_b64_tr_b16 a[20:21], v255 offset:384
ds_read_b64_tr_b16 a[22:23], v255 offset:2688
global_load_lds_dwordx4 v8, s[40:41] offset:-4096
ds_read_b64_tr_b16 a[220:221], v163 offset:0
s_add_u32 s40, s40, 221184
ds_read_b64_tr_b16 a[222:223], v163 offset:2304
ds_read_b128 v[170:173], v52 offset:0
s_addc_u32 s41, s41, 0
ds_read_b128 v[178:181], v52 offset:18432
s_waitcnt vmcnt(9) lgkmcnt(0)
s_barrier
s_add_u32 m0, s48, 4096
ds_read_b64_tr_b16 a[24:25], v255 offset:18432
v_mfma_f32_16x16x32_bf16 v[86:89], a[0:3], v[106:109], 0
ds_read_b64_tr_b16 a[26:27], v255 offset:20736
v_mfma_f32_16x16x32_bf16 v[90:93], a[4:7], v[106:109], 0
global_load_lds_dwordx4 v0, s[40:41] offset:-4096
ds_read_b64_tr_b16 a[28:29], v255 offset:18624
v_mfma_f32_16x16x32_bf16 v[94:97], v[106:109], a[8:11], 0
ds_read_b64_tr_b16 a[30:31], v255 offset:20928
global_load_lds_dwordx4 v1, s[40:41] offset:-3072
v_mfma_f32_16x16x32_bf16 v[102:105], v[106:109], a[216:219], 0
ds_read_b64_tr_b16 a[32:33], v255 offset:18816
v_mfma_f32_16x16x32_bf16 v[110:113], a[0:3], v[166:169], 0
ds_read_b64_tr_b16 a[34:35], v255 offset:21120
global_load_lds_dwordx4 v2, s[40:41] offset:-2048
v_mfma_f32_16x16x32_bf16 v[114:117], a[4:7], v[166:169], 0
ds_read_b64_tr_b16 a[224:225], v163 offset:18432
v_mfma_f32_16x16x32_bf16 v[118:121], v[166:169], a[8:11], 0
global_load_lds_dwordx4 v3, s[40:41] offset:-1024
ds_read_b64_tr_b16 a[226:227], v163 offset:20736
v_mfma_f32_16x16x32_bf16 v[134:137], v[166:169], a[216:219], 0
ds_read_b128 v[182:185], v51 offset:2048
global_load_lds_dwordx4 v4, s[40:41] offset:0
v_mfma_f32_16x16x32_bf16 v[122:125], a[0:3], v[174:177], 0
ds_read_b128 v[190:193], v51 offset:20480
v_mfma_f32_16x16x32_bf16 v[126:129], a[4:7], v[174:177], 0
ds_read_b64_tr_b16 a[36:37], v255 offset:36864
global_load_lds_dwordx4 v5, s[40:41] offset:1024
v_mfma_f32_16x16x32_bf16 v[130:133], v[174:177], a[8:11], 0
ds_read_b64_tr_b16 a[38:39], v255 offset:39168
v_mfma_f32_16x16x32_bf16 v[158:161], v[174:177], a[216:219], 0
global_load_lds_dwordx4 v6, s[40:41] offset:2048
ds_read_b64_tr_b16 a[40:41], v255 offset:37056
v_mfma_f32_16x16x32_bf16 v[86:89], a[12:15], v[106:109], v[86:89]
ds_read_b64_tr_b16 a[42:43], v255 offset:39360
v_mfma_f32_16x16x32_bf16 v[90:93], a[16:19], v[106:109], v[90:93]
global_load_lds_dwordx4 v7, s[40:41] offset:3072
ds_read_b64_tr_b16 a[44:45], v255 offset:37248
v_mfma_f32_16x16x32_bf16 v[94:97], v[106:109], a[20:23], v[94:97]
s_add_u32 m0, s48, 12288
ds_read_b64_tr_b16 a[46:47], v255 offset:39552
v_mfma_f32_16x16x32_bf16 v[102:105], v[106:109], a[220:223], v[102:105]
ds_read_b64_tr_b16 a[228:229], v163 offset:36864
v_mfma_f32_16x16x32_bf16 v[110:113], a[12:15], v[170:173], v[110:113]
global_load_lds_dwordx4 v8, s[40:41] offset:-4096
ds_read_b64_tr_b16 a[230:231], v163 offset:39168
v_mfma_f32_16x16x32_bf16 v[114:117], a[16:19], v[170:173], v[114:117]
ds_read_b128 v[186:189], v52 offset:2048
v_mfma_f32_16x16x32_bf16 v[118:121], v[170:173], a[20:23], v[118:121]
s_add_u32 s40, s40, 221184
ds_read_b128 v[194:197], v52 offset:20480
v_mfma_f32_16x16x32_bf16 v[134:137], v[170:173], a[220:223], v[134:137]
v_mfma_f32_16x16x32_bf16 v[122:125], a[12:15], v[178:181], v[122:125]
s_addc_u32 s41, s41, 0
v_mfma_f32_16x16x32_bf16 v[126:129], a[16:19], v[178:181], v[126:129]
v_mfma_f32_16x16x32_bf16 v[130:133], v[178:181], a[20:23], v[130:133]
v_mfma_f32_16x16x32_bf16 v[158:161], v[178:181], a[220:223], v[158:161]
s_waitcnt vmcnt(9) lgkmcnt(0)
s_barrier
s_add_u32 m0, s48, 40960
ds_read_b64_tr_b16 a[48:49], v255 offset:55296
v_mfma_f32_16x16x32_bf16 v[86:89], a[24:27], v[106:109], v[86:89]
ds_read_b64_tr_b16 a[50:51], v255 offset:57600
v_mfma_f32_16x16x32_bf16 v[90:93], a[28:31], v[106:109], v[90:93]
global_load_lds_dwordx4 v0, s[40:41] offset:-4096
ds_read_b64_tr_b16 a[52:53], v255 offset:55488
v_mfma_f32_16x16x32_bf16 v[94:97], v[106:109], a[32:35], v[94:97]
ds_read_b64_tr_b16 a[54:55], v255 offset:57792
global_load_lds_dwordx4 v1, s[40:41] offset:-3072
v_mfma_f32_16x16x32_bf16 v[102:105], v[106:109], a[224:227], v[102:105]
ds_read_b64_tr_b16 a[56:57], v255 offset:55680
v_mfma_f32_16x16x32_bf16 v[110:113], a[24:27], v[182:185], v[110:113]
ds_read_b64_tr_b16 a[58:59], v255 offset:57984
global_load_lds_dwordx4 v2, s[40:41] offset:-2048
v_mfma_f32_16x16x32_bf16 v[114:117], a[28:31], v[182:185], v[114:117]
ds_read_b64_tr_b16 a[232:233], v163 offset:55296
v_mfma_f32_16x16x32_bf16 v[118:121], v[182:185], a[32:35], v[118:121]
global_load_lds_dwordx4 v3, s[40:41] offset:-1024
ds_read_b64_tr_b16 a[234:235], v163 offset:57600
v_mfma_f32_16x16x32_bf16 v[134:137], v[182:185], a[224:227], v[134:137]
ds_read_b128 v[166:169], v51 offset:4096
global_load_lds_dwordx4 v4, s[40:41] offset:0
v_mfma_f32_16x16x32_bf16 v[122:125], a[24:27], v[190:193], v[122:125]
ds_read_b128 v[174:177], v51 offset:22528
v_mfma_f32_16x16x32_bf16 v[126:129], a[28:31], v[190:193], v[126:129]
ds_read_b64_tr_b16 a[60:61], v162 offset:8192
global_load_lds_dwordx4 v5, s[40:41] offset:1024
v_mfma_f32_16x16x32_bf16 v[130:133], v[190:193], a[32:35], v[130:133]
ds_read_b64_tr_b16 a[62:63], v162 offset:10496
v_mfma_f32_16x16x32_bf16 v[158:161], v[190:193], a[224:227], v[158:161]
global_load_lds_dwordx4 v6, s[40:41] offset:2048
ds_read_b64_tr_b16 a[64:65], v162 offset:8384
v_mfma_f32_16x16x32_bf16 v[86:89], a[36:39], v[106:109], v[86:89]
ds_read_b64_tr_b16 a[66:67], v162 offset:10688
v_mfma_f32_16x16x32_bf16 v[90:93], a[40:43], v[106:109], v[90:93]
global_load_lds_dwordx4 v7, s[40:41] offset:3072
ds_read_b64_tr_b16 a[68:69], v162 offset:8576
v_mfma_f32_16x16x32_bf16 v[94:97], v[106:109], a[44:47], v[94:97]
s_add_u32 m0, s48, 49152
ds_read_b64_tr_b16 a[70:71], v162 offset:10880
v_mfma_f32_16x16x32_bf16 v[102:105], v[106:109], a[228:231], v[102:105]
ds_read_b64_tr_b16 a[236:237], v164 offset:8192
v_mfma_f32_16x16x32_bf16 v[110:113], a[36:39], v[186:189], v[110:113]
global_load_lds_dwordx4 v8, s[40:41] offset:-4096
ds_read_b64_tr_b16 a[238:239], v164 offset:10496
v_mfma_f32_16x16x32_bf16 v[114:117], a[40:43], v[186:189], v[114:117]
ds_read_b128 v[170:173], v52 offset:4096
v_mfma_f32_16x16x32_bf16 v[118:121], v[186:189], a[44:47], v[118:121]
s_add_u32 s40, s40, 221184
ds_read_b128 v[178:181], v52 offset:22528
v_mfma_f32_16x16x32_bf16 v[134:137], v[186:189], a[228:231], v[134:137]
v_mfma_f32_16x16x32_bf16 v[122:125], a[36:39], v[194:197], v[122:125]
s_addc_u32 s41, s41, 0
v_mfma_f32_16x16x32_bf16 v[126:129], a[40:43], v[194:197], v[126:129]
v_mfma_f32_16x16x32_bf16 v[130:133], v[194:197], a[44:47], v[130:133]
v_mfma_f32_16x16x32_bf16 v[158:161], v[194:197], a[228:231], v[158:161]
s_waitcnt vmcnt(9) lgkmcnt(0)
s_barrier
s_add_u32 m0, s48, 77824
ds_read_b64_tr_b16 a[72:73], v9 offset:47104
v_mfma_f32_16x16x32_bf16 v[86:89], a[48:51], v[106:109], v[86:89]
ds_read_b64_tr_b16 a[74:75], v9 offset:49408
v_mfma_f32_16x16x32_bf16 v[90:93], a[52:55], v[106:109], v[90:93]
global_load_lds_dwordx4 v0, s[40:41] offset:-4096
ds_read_b64_tr_b16 a[76:77], v9 offset:47296
v_mfma_f32_16x16x32_bf16 v[94:97], v[106:109], a[56:59], v[94:97]
ds_read_b64_tr_b16 a[78:79], v9 offset:49600
global_load_lds_dwordx4 v1, s[40:41] offset:-3072
v_mfma_f32_16x16x32_bf16 v[102:105], v[106:109], a[232:235], v[102:105]
ds_read_b64_tr_b16 a[80:81], v9 offset:47488
v_mfma_f32_16x16x32_bf16 v[110:113], a[48:51], v[166:169], v[110:113]
ds_read_b64_tr_b16 a[82:83], v9 offset:49792
global_load_lds_dwordx4 v2, s[40:41] offset:-2048
v_mfma_f32_16x16x32_bf16 v[114:117], a[52:55], v[166:169], v[114:117]
ds_read_b64_tr_b16 a[240:241], v10 offset:47104
v_mfma_f32_16x16x32_bf16 v[118:121], v[166:169], a[56:59], v[118:121]
global_load_lds_dwordx4 v3, s[40:41] offset:-1024
ds_read_b64_tr_b16 a[242:243], v10 offset:49408
v_mfma_f32_16x16x32_bf16 v[134:137], v[166:169], a[232:235], v[134:137]
ds_read_b128 v[182:185], v51 offset:6144
global_load_lds_dwordx4 v4, s[40:41] offset:0
v_mfma_f32_16x16x32_bf16 v[122:125], a[48:51], v[174:177], v[122:125]
ds_read_b128 v[190:193], v51 offset:24576
v_mfma_f32_16x16x32_bf16 v[126:129], a[52:55], v[174:177], v[126:129]
ds_read_b64_tr_b16 a[84:85], v255 offset:0
global_load_lds_dwordx4 v5, s[40:41] offset:1024
v_mfma_f32_16x16x32_bf16 v[130:133], v[174:177], a[56:59], v[130:133]
ds_read_b64_tr_b16 a[86:87], v255 offset:2304
v_mfma_f32_16x16x32_bf16 v[158:161], v[174:177], a[232:235], v[158:161]
global_load_lds_dwordx4 v6, s[40:41] offset:2048
ds_read_b64_tr_b16 a[88:89], v255 offset:192
v_mfma_f32_16x16x32_bf16 v[86:89], a[60:63], v[106:109], v[86:89]
ds_read_b64_tr_b16 a[90:91], v255 offset:2496
v_mfma_f32_16x16x32_bf16 v[90:93], a[64:67], v[106:109], v[90:93]
global_load_lds_dwordx4 v7, s[40:41] offset:3072
ds_read_b64_tr_b16 a[92:93], v255 offset:384
v_mfma_f32_16x16x32_bf16 v[94:97], v[106:109], a[68:71], v[94:97]
s_add_u32 m0, s48, 86016
ds_read_b64_tr_b16 a[94:95], v255 offset:2688
v_mfma_f32_16x16x32_bf16 v[102:105], v[106:109], a[236:239], v[102:105]
ds_read_b64_tr_b16 a[244:245], v163 offset:0
v_mfma_f32_16x16x32_bf16 v[110:113], a[60:63], v[170:173], v[110:113]
global_load_lds_dwordx4 v8, s[40:41] offset:-4096
ds_read_b64_tr_b16 a[246:247], v163 offset:2304
v_mfma_f32_16x16x32_bf16 v[114:117], a[64:67], v[170:173], v[114:117]
ds_read_b128 v[186:189], v52 offset:6144
v_mfma_f32_16x16x32_bf16 v[118:121], v[170:173], a[68:71], v[118:121]
s_add_u32 s40, s40, 221184
ds_read_b128 v[194:197], v52 offset:24576
v_mfma_f32_16x16x32_bf16 v[134:137], v[170:173], a[236:239], v[134:137]
v_mfma_f32_16x16x32_bf16 v[122:125], a[60:63], v[178:181], v[122:125]
s_addc_u32 s41, s41, 0
v_mfma_f32_16x16x32_bf16 v[126:129], a[64:67], v[178:181], v[126:129]
v_mfma_f32_16x16x32_bf16 v[130:133], v[178:181], a[68:71], v[130:133]
v_mfma_f32_16x16x32_bf16 v[158:161], v[178:181], a[236:239], v[158:161]
s_waitcnt vmcnt(9) lgkmcnt(0)
s_barrier
s_add_u32 m0, s48, 4096
ds_read_b64_tr_b16 a[96:97], v255 offset:18432
v_mfma_f32_16x16x32_bf16 v[86:89], a[72:75], v[106:109], v[86:89]
ds_read_b64_tr_b16 a[98:99], v255 offset:20736
v_mfma_f32_16x16x32_bf16 v[90:93], a[76:79], v[106:109], v[90:93]
global_load_lds_dwordx4 v0, s[40:41] offset:-4096
ds_read_b64_tr_b16 a[100:101], v255 offset:18624
v_mfma_f32_16x16x32_bf16 v[94:97], v[106:109], a[80:83], v[94:97]
ds_read_b64_tr_b16 a[102:103], v255 offset:20928
global_load_lds_dwordx4 v1, s[40:41] offset:-3072
v_mfma_f32_16x16x32_bf16 v[102:105], v[106:109], a[240:243], v[102:105]
ds_read_b64_tr_b16 a[104:105], v255 offset:18816
v_mfma_f32_16x16x32_bf16 v[110:113], a[72:75], v[182:185], v[110:113]
ds_read_b64_tr_b16 a[106:107], v255 offset:21120
global_load_lds_dwordx4 v2, s[40:41] offset:-2048
v_mfma_f32_16x16x32_bf16 v[114:117], a[76:79], v[182:185], v[114:117]
ds_read_b64_tr_b16 a[248:249], v163 offset:18432
v_mfma_f32_16x16x32_bf16 v[118:121], v[182:185], a[80:83], v[118:121]
global_load_lds_dwordx4 v3, s[40:41] offset:-1024
ds_read_b64_tr_b16 a[250:251], v163 offset:20736
v_mfma_f32_16x16x32_bf16 v[134:137], v[182:185], a[240:243], v[134:137]
ds_read_b128 v[166:169], v51 offset:8192
global_load_lds_dwordx4 v4, s[40:41] offset:0
v_mfma_f32_16x16x32_bf16 v[122:125], a[72:75], v[190:193], v[122:125]
ds_read_b128 v[174:177], v51 offset:26624
v_mfma_f32_16x16x32_bf16 v[126:129], a[76:79], v[190:193], v[126:129]
ds_read_b64_tr_b16 a[108:109], v255 offset:36864
global_load_lds_dwordx4 v5, s[40:41] offset:1024
v_mfma_f32_16x16x32_bf16 v[130:133], v[190:193], a[80:83], v[130:133]
ds_read_b64_tr_b16 a[110:111], v255 offset:39168
v_mfma_f32_16x16x32_bf16 v[158:161], v[190:193], a[240:243], v[158:161]
global_load_lds_dwordx4 v6, s[40:41] offset:2048
ds_read_b64_tr_b16 a[112:113], v255 offset:37056
v_mfma_f32_16x16x32_bf16 v[86:89], a[84:87], v[106:109], v[86:89]
ds_read_b64_tr_b16 a[114:115], v255 offset:39360
v_mfma_f32_16x16x32_bf16 v[90:93], a[88:91], v[106:109], v[90:93]
global_load_lds_dwordx4 v7, s[40:41] offset:3072
ds_read_b64_tr_b16 a[116:117], v255 offset:37248
v_mfma_f32_16x16x32_bf16 v[94:97], v[106:109], a[92:95], v[94:97]
s_add_u32 m0, s48, 12288
ds_read_b64_tr_b16 a[118:119], v255 offset:39552
v_mfma_f32_16x16x32_bf16 v[102:105], v[106:109], a[244:247], v[102:105]
ds_read_b64_tr_b16 a[252:253], v163 offset:36864
v_mfma_f32_16x16x32_bf16 v[110:113], a[84:87], v[186:189], v[110:113]
global_load_lds_dwordx4 v8, s[40:41] offset:-4096
ds_read_b64_tr_b16 a[254:255], v163 offset:39168
v_mfma_f32_16x16x32_bf16 v[114:117], a[88:91], v[186:189], v[114:117]
ds_read_b128 v[170:173], v52 offset:8192
v_mfma_f32_16x16x32_bf16 v[118:121], v[186:189], a[92:95], v[118:121]
s_add_u32 s40, s40, 221184
ds_read_b128 v[178:181], v52 offset:26624
v_mfma_f32_16x16x32_bf16 v[134:137], v[186:189], a[244:247], v[134:137]
v_mfma_f32_16x16x32_bf16 v[122:125], a[84:87], v[194:197], v[122:125]
s_addc_u32 s41, s41, 0
v_mfma_f32_16x16x32_bf16 v[126:129], a[88:91], v[194:197], v[126:129]
v_mfma_f32_16x16x32_bf16 v[130:133], v[194:197], a[92:95], v[130:133]
v_mfma_f32_16x16x32_bf16 v[158:161], v[194:197], a[244:247], v[158:161]
s_waitcnt vmcnt(9) lgkmcnt(0)
s_barrier
s_add_u32 m0, s48, 40960
ds_read_b64_tr_b16 a[120:121], v255 offset:55296
v_mfma_f32_16x16x32_bf16 v[86:89], a[96:99], v[106:109], v[86:89]
ds_read_b64_tr_b16 a[122:123], v255 offset:57600
v_mfma_f32_16x16x32_bf16 v[90:93], a[100:103], v[106:109], v[90:93]
global_load_lds_dwordx4 v0, s[40:41] offset:-4096
ds_read_b64_tr_b16 a[124:125], v255 offset:55488
v_mfma_f32_16x16x32_bf16 v[94:97], v[106:109], a[104:107], v[94:97]
ds_read_b64_tr_b16 a[126:127], v255 offset:57792
global_load_lds_dwordx4 v1, s[40:41] offset:-3072
v_mfma_f32_16x16x32_bf16 v[102:105], v[106:109], a[248:251], v[102:105]
ds_read_b64_tr_b16 a[128:129], v255 offset:55680
v_mfma_f32_16x16x32_bf16 v[110:113], a[96:99], v[166:169], v[110:113]
ds_read_b64_tr_b16 a[130:131], v255 offset:57984
global_load_lds_dwordx4 v2, s[40:41] offset:-2048
v_mfma_f32_16x16x32_bf16 v[114:117], a[100:103], v[166:169], v[114:117]
ds_read_b64_tr_b16 v[54:55], v163 offset:55296
v_mfma_f32_16x16x32_bf16 v[118:121], v[166:169], a[104:107], v[118:121]
global_load_lds_dwordx4 v3, s[40:41] offset:-1024
ds_read_b64_tr_b16 v[56:57], v163 offset:57600
v_mfma_f32_16x16x32_bf16 v[134:137], v[166:169], a[248:251], v[134:137]
ds_read_b128 v[182:185], v51 offset:10240
global_load_lds_dwordx4 v4, s[40:41] offset:0
v_mfma_f32_16x16x32_bf16 v[122:125], a[96:99], v[174:177], v[122:125]
ds_read_b128 v[190:193], v51 offset:28672
v_mfma_f32_16x16x32_bf16 v[126:129], a[100:103], v[174:177], v[126:129]
ds_read_b64_tr_b16 a[132:133], v162 offset:8192
global_load_lds_dwordx4 v5, s[40:41] offset:1024
v_mfma_f32_16x16x32_bf16 v[130:133], v[174:177], a[104:107], v[130:133]
ds_read_b64_tr_b16 a[134:135], v162 offset:10496
v_mfma_f32_16x16x32_bf16 v[158:161], v[174:177], a[248:251], v[158:161]
global_load_lds_dwordx4 v6, s[40:41] offset:2048
ds_read_b64_tr_b16 a[136:137], v162 offset:8384
v_mfma_f32_16x16x32_bf16 v[86:89], a[108:111], v[106:109], v[86:89]
ds_read_b64_tr_b16 a[138:139], v162 offset:10688
v_mfma_f32_16x16x32_bf16 v[90:93], a[112:115], v[106:109], v[90:93]
global_load_lds_dwordx4 v7, s[40:41] offset:3072
ds_read_b64_tr_b16 a[140:141], v162 offset:8576
v_mfma_f32_16x16x32_bf16 v[94:97], v[106:109], a[116:119], v[94:97]
s_add_u32 m0, s48, 49152
ds_read_b64_tr_b16 a[142:143], v162 offset:10880
v_mfma_f32_16x16x32_bf16 v[102:105], v[106:109], a[252:255], v[102:105]
ds_read_b64_tr_b16 v[58:59], v164 offset:8192
v_mfma_f32_16x16x32_bf16 v[110:113], a[108:111], v[170:173], v[110:113]
global_load_lds_dwordx4 v8, s[40:41] offset:-4096
ds_read_b64_tr_b16 v[60:61], v164 offset:10496
v_mfma_f32_16x16x32_bf16 v[114:117], a[112:115], v[170:173], v[114:117]
ds_read_b128 v[186:189], v52 offset:10240
v_mfma_f32_16x16x32_bf16 v[118:121], v[170:173], a[116:119], v[118:121]
s_add_u32 s40, s40, 221184
ds_read_b128 v[194:197], v52 offset:28672
v_mfma_f32_16x16x32_bf16 v[134:137], v[170:173], a[252:255], v[134:137]
v_mfma_f32_16x16x32_bf16 v[122:125], a[108:111], v[178:181], v[122:125]
s_addc_u32 s41, s41, 0
v_mfma_f32_16x16x32_bf16 v[126:129], a[112:115], v[178:181], v[126:129]
v_mfma_f32_16x16x32_bf16 v[130:133], v[178:181], a[116:119], v[130:133]
v_mfma_f32_16x16x32_bf16 v[158:161], v[178:181], a[252:255], v[158:161]
s_waitcnt vmcnt(9) lgkmcnt(0)
s_barrier
s_add_u32 m0, s48, 77824
ds_read_b64_tr_b16 a[144:145], v9 offset:47104
v_mfma_f32_16x16x32_bf16 v[86:89], a[120:123], v[106:109], v[86:89]
ds_read_b64_tr_b16 a[146:147], v9 offset:49408
v_mfma_f32_16x16x32_bf16 v[90:93], a[124:127], v[106:109], v[90:93]
global_load_lds_dwordx4 v0, s[40:41] offset:-4096
ds_read_b64_tr_b16 a[148:149], v9 offset:47296
v_mfma_f32_16x16x32_bf16 v[94:97], v[106:109], a[128:131], v[94:97]
ds_read_b64_tr_b16 a[150:151], v9 offset:49600
global_load_lds_dwordx4 v1, s[40:41] offset:-3072
v_mfma_f32_16x16x32_bf16 v[102:105], v[106:109], v[54:57], v[102:105]
ds_read_b64_tr_b16 a[152:153], v9 offset:47488
v_mfma_f32_16x16x32_bf16 v[110:113], a[120:123], v[182:185], v[110:113]
ds_read_b64_tr_b16 a[154:155], v9 offset:49792
global_load_lds_dwordx4 v2, s[40:41] offset:-2048
v_mfma_f32_16x16x32_bf16 v[114:117], a[124:127], v[182:185], v[114:117]
ds_read_b64_tr_b16 v[62:63], v10 offset:47104
v_mfma_f32_16x16x32_bf16 v[118:121], v[182:185], a[128:131], v[118:121]
global_load_lds_dwordx4 v3, s[40:41] offset:-1024
ds_read_b64_tr_b16 v[64:65], v10 offset:49408
v_mfma_f32_16x16x32_bf16 v[134:137], v[182:185], v[54:57], v[134:137]
ds_read_b128 v[166:169], v51 offset:12288
global_load_lds_dwordx4 v4, s[40:41] offset:0
v_mfma_f32_16x16x32_bf16 v[122:125], a[120:123], v[190:193], v[122:125]
ds_read_b128 v[174:177], v51 offset:30720
v_mfma_f32_16x16x32_bf16 v[126:129], a[124:127], v[190:193], v[126:129]
)"
R"(ds_read_b64_tr_b16 a[156:157], v255 offset:0
global_load_lds_dwordx4 v5, s[40:41] offset:1024
v_mfma_f32_16x16x32_bf16 v[130:133], v[190:193], a[128:131], v[130:133]
ds_read_b64_tr_b16 a[158:159], v255 offset:2304
v_mfma_f32_16x16x32_bf16 v[158:161], v[190:193], v[54:57], v[158:161]
global_load_lds_dwordx4 v6, s[40:41] offset:2048
ds_read_b64_tr_b16 a[160:161], v255 offset:192
v_mfma_f32_16x16x32_bf16 v[86:89], a[132:135], v[106:109], v[86:89]
ds_read_b64_tr_b16 a[162:163], v255 offset:2496
v_mfma_f32_16x16x32_bf16 v[90:93], a[136:139], v[106:109], v[90:93]
global_load_lds_dwordx4 v7, s[40:41] offset:3072
ds_read_b64_tr_b16 a[164:165], v255 offset:384
v_mfma_f32_16x16x32_bf16 v[94:97], v[106:109], a[140:143], v[94:97]
s_add_u32 m0, s48, 86016
ds_read_b64_tr_b16 a[166:167], v255 offset:2688
v_mfma_f32_16x16x32_bf16 v[102:105], v[106:109], v[58:61], v[102:105]
ds_read_b64_tr_b16 v[66:67], v163 offset:0
v_mfma_f32_16x16x32_bf16 v[110:113], a[132:135], v[186:189], v[110:113]
global_load_lds_dwordx4 v8, s[40:41] offset:-4096
ds_read_b64_tr_b16 v[68:69], v163 offset:2304
v_mfma_f32_16x16x32_bf16 v[114:117], a[136:139], v[186:189], v[114:117]
ds_read_b128 v[170:173], v52 offset:12288
v_mfma_f32_16x16x32_bf16 v[118:121], v[186:189], a[140:143], v[118:121]
s_sub_u32 s40, s40, 1769472
ds_read_b128 v[178:181], v52 offset:30720
v_mfma_f32_16x16x32_bf16 v[134:137], v[186:189], v[58:61], v[134:137]
v_mfma_f32_16x16x32_bf16 v[122:125], a[132:135], v[194:197], v[122:125]
s_subb_u32 s41, s41, 0
v_mfma_f32_16x16x32_bf16 v[126:129], a[136:139], v[194:197], v[126:129]
v_mfma_f32_16x16x32_bf16 v[130:133], v[194:197], a[140:143], v[130:133]
v_mfma_f32_16x16x32_bf16 v[158:161], v[194:197], v[58:61], v[158:161]
s_waitcnt vmcnt(9) lgkmcnt(0)
s_barrier
s_add_u32 m0, s49, 0
ds_read_b64_tr_b16 a[168:169], v255 offset:18432
v_mfma_f32_16x16x32_bf16 v[86:89], a[144:147], v[106:109], v[86:89]
ds_read_b64_tr_b16 a[170:171], v255 offset:20736
v_mfma_f32_16x16x32_bf16 v[90:93], a[148:151], v[106:109], v[90:93]
ds_read_b64_tr_b16 a[172:173], v255 offset:18624
v_mfma_f32_16x16x32_bf16 v[94:97], v[106:109], a[152:155], v[94:97]
ds_read_b64_tr_b16 a[174:175], v255 offset:20928
global_load_lds_dwordx4 v11, s[42:43] offset:-2048 sc1
v_mfma_f32_16x16x32_bf16 v[102:105], v[106:109], v[62:65], v[102:105]
ds_read_b64_tr_b16 a[176:177], v255 offset:18816
v_mfma_f32_16x16x32_bf16 v[110:113], a[144:147], v[166:169], v[110:113]
ds_read_b64_tr_b16 a[178:179], v255 offset:21120
v_mfma_f32_16x16x32_bf16 v[114:117], a[148:151], v[166:169], v[114:117]
ds_read_b64_tr_b16 v[70:71], v163 offset:18432
global_load_lds_dwordx4 v12, s[42:43] offset:-1024 sc1
v_mfma_f32_16x16x32_bf16 v[118:121], v[166:169], a[152:155], v[118:121]
ds_read_b64_tr_b16 v[72:73], v163 offset:20736
v_mfma_f32_16x16x32_bf16 v[134:137], v[166:169], v[62:65], v[134:137]
ds_read_b128 v[182:185], v51 offset:14336
v_mfma_f32_16x16x32_bf16 v[122:125], a[144:147], v[174:177], v[122:125]
ds_read_b128 v[190:193], v51 offset:32768
global_load_lds_dwordx4 v13, s[42:43] offset:0 sc1
v_mfma_f32_16x16x32_bf16 v[126:129], a[148:151], v[174:177], v[126:129]
ds_read_b64_tr_b16 a[180:181], v255 offset:36864
v_mfma_f32_16x16x32_bf16 v[130:133], v[174:177], a[152:155], v[130:133]
ds_read_b64_tr_b16 a[182:183], v255 offset:39168
v_mfma_f32_16x16x32_bf16 v[158:161], v[174:177], v[62:65], v[158:161]
ds_read_b64_tr_b16 a[184:185], v255 offset:37056
v_mfma_f32_16x16x32_bf16 v[86:89], a[156:159], v[106:109], v[86:89]
global_load_lds_dwordx4 v14, s[42:43] offset:1024 sc1
ds_read_b64_tr_b16 a[186:187], v255 offset:39360
v_mfma_f32_16x16x32_bf16 v[90:93], a[160:163], v[106:109], v[90:93]
ds_read_b64_tr_b16 a[188:189], v255 offset:37248
v_mfma_f32_16x16x32_bf16 v[94:97], v[106:109], a[164:167], v[94:97]
ds_read_b64_tr_b16 a[190:191], v255 offset:39552
v_mfma_f32_16x16x32_bf16 v[102:105], v[106:109], v[66:69], v[102:105]
global_load_lds_dwordx4 v15, s[42:43] offset:2048 sc1
ds_read_b64_tr_b16 v[74:75], v163 offset:36864
v_mfma_f32_16x16x32_bf16 v[110:113], a[156:159], v[170:173], v[110:113]
ds_read_b64_tr_b16 v[76:77], v163 offset:39168
v_mfma_f32_16x16x32_bf16 v[114:117], a[160:163], v[170:173], v[114:117]
ds_read_b128 v[186:189], v52 offset:14336
v_mfma_f32_16x16x32_bf16 v[118:121], v[170:173], a[164:167], v[118:121]
s_sub_u32 s42, s42, 18432
s_subb_u32 s43, s43, 0
ds_read_b128 v[194:197], v52 offset:32768
v_mfma_f32_16x16x32_bf16 v[134:137], v[170:173], v[66:69], v[134:137]
v_mfma_f32_16x16x32_bf16 v[122:125], a[156:159], v[178:181], v[122:125]
v_mfma_f32_16x16x32_bf16 v[126:129], a[160:163], v[178:181], v[126:129]
v_mfma_f32_16x16x32_bf16 v[130:133], v[178:181], a[164:167], v[130:133]
v_mfma_f32_16x16x32_bf16 v[158:161], v[178:181], v[66:69], v[158:161]
s_waitcnt vmcnt(5) lgkmcnt(0)
s_barrier
s_add_u32 m0, s49, 18432
ds_read_b64_tr_b16 a[192:193], v255 offset:55296
v_mfma_f32_16x16x32_bf16 v[86:89], a[168:171], v[106:109], v[86:89]
ds_read_b64_tr_b16 a[194:195], v255 offset:57600
v_mfma_f32_16x16x32_bf16 v[90:93], a[172:175], v[106:109], v[90:93]
ds_read_b64_tr_b16 a[196:197], v255 offset:55488
v_mfma_f32_16x16x32_bf16 v[94:97], v[106:109], a[176:179], v[94:97]
ds_read_b64_tr_b16 a[198:199], v255 offset:57792
global_load_lds_dwordx4 v11, s[42:43] offset:-2048 sc1
v_mfma_f32_16x16x32_bf16 v[102:105], v[106:109], v[70:73], v[102:105]
ds_read_b64_tr_b16 a[200:201], v255 offset:55680
v_mfma_f32_16x16x32_bf16 v[110:113], a[168:171], v[182:185], v[110:113]
ds_read_b64_tr_b16 a[202:203], v255 offset:57984
v_mfma_f32_16x16x32_bf16 v[114:117], a[172:175], v[182:185], v[114:117]
ds_read_b64_tr_b16 v[78:79], v163 offset:55296
global_load_lds_dwordx4 v12, s[42:43] offset:-1024 sc1
v_mfma_f32_16x16x32_bf16 v[118:121], v[182:185], a[176:179], v[118:121]
ds_read_b64_tr_b16 v[80:81], v163 offset:57600
v_mfma_f32_16x16x32_bf16 v[134:137], v[182:185], v[70:73], v[134:137]
ds_read_b128 v[166:169], v51 offset:16384
v_mfma_f32_16x16x32_bf16 v[122:125], a[168:171], v[190:193], v[122:125]
ds_read_b128 v[174:177], v51 offset:34816
global_load_lds_dwordx4 v13, s[42:43] offset:0 sc1
v_mfma_f32_16x16x32_bf16 v[126:129], a[172:175], v[190:193], v[126:129]
ds_read_b64_tr_b16 a[204:205], v162 offset:8192
v_mfma_f32_16x16x32_bf16 v[130:133], v[190:193], a[176:179], v[130:133]
ds_read_b64_tr_b16 a[206:207], v162 offset:10496
v_mfma_f32_16x16x32_bf16 v[158:161], v[190:193], v[70:73], v[158:161]
ds_read_b64_tr_b16 a[208:209], v162 offset:8384
global_load_lds_dwordx4 v14, s[42:43] offset:1024 sc1
v_mfma_f32_16x16x32_bf16 v[86:89], a[180:183], v[106:109], v[86:89]
ds_read_b64_tr_b16 a[210:211], v162 offset:10688
v_mfma_f32_16x16x32_bf16 v[90:93], a[184:187], v[106:109], v[90:93]
ds_read_b64_tr_b16 a[212:213], v162 offset:8576
v_mfma_f32_16x16x32_bf16 v[94:97], v[106:109], a[188:191], v[94:97]
ds_read_b64_tr_b16 a[214:215], v162 offset:10880
global_load_lds_dwordx4 v15, s[42:43] offset:2048 sc1
v_mfma_f32_16x16x32_bf16 v[102:105], v[106:109], v[74:77], v[102:105]
ds_read_b64_tr_b16 v[82:83], v164 offset:8192
v_mfma_f32_16x16x32_bf16 v[110:113], a[180:183], v[186:189], v[110:113]
ds_read_b64_tr_b16 v[84:85], v164 offset:10496
v_mfma_f32_16x16x32_bf16 v[114:117], a[184:187], v[186:189], v[114:117]
ds_read_b128 v[170:173], v52 offset:16384
s_sub_u32 s42, s42, 18432
s_subb_u32 s43, s43, 0
v_mfma_f32_16x16x32_bf16 v[118:121], v[186:189], a[188:191], v[118:121]
ds_read_b128 v[178:181], v52 offset:34816
v_mfma_f32_16x16x32_bf16 v[134:137], v[186:189], v[74:77], v[134:137]
v_mfma_f32_16x16x32_bf16 v[122:125], a[180:183], v[194:197], v[122:125]
v_mfma_f32_16x16x32_bf16 v[126:129], a[184:187], v[194:197], v[126:129]
v_mfma_f32_16x16x32_bf16 v[158:161], v[194:197], v[74:77], v[158:161]
s_waitcnt lgkmcnt(15)
v_mfma_f32_16x16x32_bf16 v[86:89], a[192:195], v[106:109], v[86:89]
v_mfma_f32_16x16x32_bf16 v[90:93], a[196:199], v[106:109], v[90:93]
s_waitcnt lgkmcnt(14)
v_mfma_f32_16x16x32_bf16 v[94:97], v[106:109], a[200:203], v[94:97]
s_waitcnt lgkmcnt(12)
v_mfma_f32_16x16x32_bf16 v[102:105], v[106:109], v[78:81], v[102:105]
s_waitcnt lgkmcnt(11)
v_mfma_f32_16x16x32_bf16 v[110:113], a[192:195], v[166:169], v[110:113]
v_mfma_f32_16x16x32_bf16 v[114:117], a[196:199], v[166:169], v[114:117]
v_mfma_f32_16x16x32_bf16 v[118:121], v[166:169], a[200:203], v[118:121]
v_mfma_f32_16x16x32_bf16 v[134:137], v[166:169], v[78:81], v[134:137]
s_waitcnt lgkmcnt(10)
v_mfma_f32_16x16x32_bf16 v[158:161], v[174:177], v[78:81], v[158:161]
s_waitcnt lgkmcnt(8)
v_mfma_f32_16x16x32_bf16 v[86:89], a[204:207], v[106:109], v[86:89]
s_waitcnt lgkmcnt(6)
v_mfma_f32_16x16x32_bf16 v[90:93], a[208:211], v[106:109], v[90:93]
s_waitcnt lgkmcnt(4)
v_mfma_f32_16x16x32_bf16 v[94:97], v[106:109], a[212:215], v[94:97]
s_waitcnt lgkmcnt(2)
v_mfma_f32_16x16x32_bf16 v[102:105], v[106:109], v[82:85], v[102:105]
s_waitcnt lgkmcnt(1)
v_mfma_f32_16x16x32_bf16 v[110:113], a[204:207], v[170:173], v[110:113]
v_mfma_f32_16x16x32_bf16 v[114:117], a[208:211], v[170:173], v[114:117]
v_mfma_f32_16x16x32_bf16 v[118:121], v[170:173], a[212:215], v[118:121]
v_mfma_f32_16x16x32_bf16 v[134:137], v[170:173], v[82:85], v[134:137]
s_waitcnt lgkmcnt(0)
v_mfma_f32_16x16x32_bf16 v[158:161], v[178:181], v[82:85], v[158:161]
s_barrier
ds_read_b128 v[242:245], v25 offset:0
s_waitcnt lgkmcnt(0)
s_nop 2
v_fma_f32 v230, v102, v242, v134
v_fma_f32 v231, v102, v243, v135
v_fma_f32 v232, v102, v244, v136
v_fma_f32 v233, v102, v245, v137
v_cvt_pk_bf16_f32 v230, v230, v231
v_cvt_pk_bf16_f32 v231, v232, v233
ds_write_b64 v22, v[230:231] offset:41472
ds_read_b128 v[202:205], v52 offset:32768
ds_read_b128 v[206:209], v51 offset:34816
ds_read_b128 v[210:213], v52 offset:34816
v_mov_b32 v234, v32
v_mov_b32 v235, v33
v_mov_b32 v236, v34
v_mov_b32 v237, v35
v_mov_b32 v134, v158
v_mov_b32 v135, v159
v_mov_b32 v136, v160
v_mov_b32 v137, v161
s_waitcnt lgkmcnt(3)
s_barrier
ds_read_b32 v23, v24 offset:0
ds_read_b32 v148, v21
ds_read_b32 v26, v24 offset:192
ds_read_b32 v53, v27
ds_read_b32 v95, v28
ds_read_b128 v[226:229], v25 offset:0
ds_read_b64 v[158:159], v22 offset:41472
ds_read_b32 v140, v20
v_xor_b32 v51, 0x10000, v51
v_xor_b32 v52, 0x10000, v52
s_waitcnt lgkmcnt(7)
v_fma_f32 v238, v90, v23, v114
v_fma_f32 v239, v91, v23, v115
v_fma_f32 v240, v92, v23, v116
v_fma_f32 v241, v93, v23, v117
v_cvt_pk_bf16_f32 v146, v238, v239
v_cvt_pk_bf16_f32 v147, v240, v241
s_waitcnt lgkmcnt(0)
s_barrier
ds_read_b128 v[242:245], v25 offset:64
v_fma_f32 v238, v94, v226, v118
v_fma_f32 v239, v94, v227, v119
v_fma_f32 v240, v94, v228, v120
v_fma_f32 v241, v94, v229, v121
v_cvt_pk_bf16_f32 v154, v238, v239
v_cvt_pk_bf16_f32 v155, v240, v241
v_fma_f32 v238, v86, v23, v110
v_fma_f32 v239, v87, v23, v111
v_fma_f32 v240, v88, v23, v112
v_fma_f32 v241, v89, v23, v113
v_cvt_pk_bf16_f32 v138, v238, v239
v_cvt_pk_bf16_f32 v139, v240, v241
s_waitcnt lgkmcnt(0)
v_fma_f32 v230, v102, v242, v134
v_fma_f32 v231, v102, v243, v135
v_fma_f32 v232, v102, v244, v136
v_fma_f32 v233, v102, v245, v137
v_cvt_pk_bf16_f32 v230, v230, v231
v_cvt_pk_bf16_f32 v231, v232, v233
ds_write_b64 v22, v[230:231] offset:41472
s_waitcnt vmcnt(5) lgkmcnt(0)
s_barrier
s_mov_b32 s70, 32
s_waitcnt lgkmcnt(0)
s_nop 1
s_branch .Lqa_in26
.p2align 6
.Lqa_top45:
s_waitcnt lgkmcnt(9)
v_mfma_f32_16x16x32_bf16 v[134:137], v[44:47], a[216:219], 0
s_waitcnt lgkmcnt(8)
ds_read_b128 v[162:165], v52 offset:4096
ds_read_b32 v95, v30
v_mfma_f32_16x16x32_bf16 v[134:137], v[96:99], a[220:223], v[134:137]
ds_read_b128 v[226:229], v25 offset:64
ds_read_b64 v[160:161], v22 offset:41472
v_mfma_f32_16x16x32_bf16 v[110:113], a[0:3], v[44:47], 0
s_waitcnt lgkmcnt(8)
ds_read_b128 v[166:169], v51 offset:6144
ds_read_b32 v144, v20
v_mfma_f32_16x16x32_bf16 v[134:137], v[104:107], a[224:227], v[134:137]
s_waitcnt lgkmcnt(8)
ds_read_b128 v[170:173], v52 offset:6144
v_fma_f32 v238, v90, v23, v126
v_mfma_f32_16x16x32_bf16 v[134:137], v[0:3], a[228:231], v[134:137]
global_store_dwordx4 v16, v[230:233], s[38:39] sc1
v_mfma_f32_16x16x32_bf16 v[114:117], a[4:7], v[44:47], 0
s_waitcnt lgkmcnt(7)
ds_read_b128 v[174:177], v51 offset:8192
v_add_u32 v16, 18432, v16
v_mfma_f32_16x16x32_bf16 v[134:137], v[4:7], a[232:235], v[134:137]
s_waitcnt lgkmcnt(7)
ds_read_b128 v[178:181], v52 offset:8192
v_fma_f32 v239, v91, v23, v127
v_mfma_f32_16x16x32_bf16 v[134:137], v[162:165], a[236:239], v[134:137]
v_fma_f32 v240, v92, v23, v128
v_fma_f32 v241, v93, v23, v129
v_mfma_f32_16x16x32_bf16 v[118:121], v[44:47], a[8:11], 0
s_waitcnt lgkmcnt(4)
ds_read_b128 v[182:185], v51 offset:10240
v_cvt_pk_bf16_f32 v150, v238, v239
v_mfma_f32_16x16x32_bf16 v[134:137], v[166:169], a[240:243], v[134:137]
s_waitcnt lgkmcnt(3)
ds_read_b128 v[186:189], v52 offset:10240
v_cvt_pk_bf16_f32 v151, v240, v241
v_mfma_f32_16x16x32_bf16 v[134:137], v[170:173], a[244:247], v[134:137]
s_nop 0
v_mfma_f32_16x16x32_bf16 v[214:217], v[150:153], v[138:141], v[32:35]
v_max_f32 v109, v218, v219
v_max_f32 v254, v220, v221
v_mfma_f32_16x16x32_bf16 v[110:113], a[12:15], v[96:99], v[110:113]
s_waitcnt lgkmcnt(3)
ds_read_b128 v[190:193], v51 offset:12288
v_fma_f32 v238, v94, v226, v130
v_mfma_f32_16x16x32_bf16 v[134:137], v[174:177], a[248:251], v[134:137]
s_xor_b32 s49, s49, 0x10000
ds_read_b128 v[194:197], v52 offset:12288
s_waitcnt lgkmcnt(4)
v_mfma_f32_16x16x32_bf16 v[134:137], v[178:181], a[252:255], v[134:137]
s_add_u32 m0, s49, 0
v_mul_f32 v26, 0x3eae1a93, v26
v_mul_f32 v103, v53, v26
v_mfma_f32_16x16x32_bf16 v[114:117], a[16:19], v[96:99], v[114:117]
s_waitcnt lgkmcnt(3)
ds_read_b128 v[198:201], v51 offset:14336
v_mul_f32 v108, v95, v26
v_mfma_f32_16x16x32_bf16 v[134:137], v[182:185], v[54:57], v[134:137]
s_waitcnt lgkmcnt(3)
ds_read_b128 v[202:205], v52 offset:14336
v_max3_f32 v109, v214, v215, v109
v_mfma_f32_16x16x32_bf16 v[134:137], v[186:189], v[58:61], v[134:137]
global_load_lds_dwordx4 v11, s[42:43] offset:-2048 sc1
v_max3_f32 v254, v216, v217, v254
v_mfma_f32_16x16x32_bf16 v[118:121], v[96:99], a[20:23], v[118:121]
s_waitcnt lgkmcnt(3)
ds_read_b128 v[206:209], v51 offset:16384
v_mul_f32 v109, v109, v103
v_mfma_f32_16x16x32_bf16 v[134:137], v[190:193], v[62:65], v[134:137]
s_waitcnt lgkmcnt(3)
ds_read_b128 v[210:213], v52 offset:16384
v_mul_f32 v254, v254, v108
v_mfma_f32_16x16x32_bf16 v[134:137], v[194:197], v[66:69], v[134:137]
v_max_f32 v255, v109, v254
v_mov_b32 v8, v255
v_mfma_f32_16x16x32_bf16 v[110:113], a[24:27], v[104:107], v[110:113]
s_waitcnt lgkmcnt(3)
v_permlane16_swap_b32 v255, v8
v_mfma_f32_16x16x32_bf16 v[134:137], v[198:201], v[70:73], v[134:137]
s_waitcnt lgkmcnt(2)
v_max_f32 v255, v255, v8
v_mov_b32 v8, v255
v_mfma_f32_16x16x32_bf16 v[134:137], v[202:205], v[74:77], v[134:137]
s_nop 0
v_permlane32_swap_b32 v255, v8
v_mfma_f32_16x16x32_bf16 v[114:117], a[28:31], v[104:107], v[114:117]
s_waitcnt lgkmcnt(1)
v_max_f32 v255, v255, v8
v_sub_f32 v109, v109, v255
v_mfma_f32_16x16x32_bf16 v[134:137], v[206:209], v[78:81], v[134:137]
s_waitcnt lgkmcnt(0)
global_load_lds_dwordx4 v12, s[42:43] offset:-1024 sc1
v_sub_f32 v254, v254, v255
v_mfma_f32_16x16x32_bf16 v[134:137], v[210:213], v[82:85], v[134:137]
v_exp_f32 v9, v109
v_mfma_f32_16x16x32_bf16 v[118:121], v[104:107], a[32:35], v[118:121]
v_exp_f32 v10, v254
v_mfma_f32_16x16x32_bf16 v[110:113], a[36:39], v[0:3], v[110:113]
v_add_f32 v255, v9, v10
v_mov_b32 v8, v255
v_mfma_f32_16x16x32_bf16 v[114:117], a[40:43], v[0:3], v[114:117]
v_mul_f32 v9, v9, v53
v_fma_f32 v239, v94, v227, v131
v_mfma_f32_16x16x32_bf16 v[118:121], v[0:3], a[44:47], v[118:121]
v_permlane16_swap_b32 v255, v8
v_mfma_f32_16x16x32_bf16 v[110:113], a[48:51], v[4:7], v[110:113]
v_add_f32 v255, v255, v8
v_mov_b32 v8, v255
v_mfma_f32_16x16x32_bf16 v[114:117], a[52:55], v[4:7], v[114:117]
v_mul_f32 v10, v10, v95
v_fma_f32 v240, v94, v228, v132
v_mfma_f32_16x16x32_bf16 v[118:121], v[4:7], a[56:59], v[118:121]
global_load_lds_dwordx4 v13, s[42:43] offset:0 sc1
v_fma_f32 v241, v94, v229, v133
v_mfma_f32_16x16x32_bf16 v[110:113], a[60:63], v[162:165], v[110:113]
v_permlane32_swap_b32 v255, v8
v_mfma_f32_16x16x32_bf16 v[114:117], a[64:67], v[162:165], v[114:117]
v_add_f32 v255, v255, v8
v_cvt_pk_bf16_f32 v156, v238, v239
v_mfma_f32_16x16x32_bf16 v[118:121], v[162:165], a[68:71], v[118:121]
v_mfma_f32_16x16x32_bf16 v[110:113], a[72:75], v[166:169], v[110:113]
s_barrier
v_mfma_f32_16x16x32_bf16 v[114:117], a[76:79], v[166:169], v[114:117]
ds_read_b128 v[242:245], v25 offset:0
v_cvt_pk_bf16_f32 v157, v240, v241
v_mfma_f32_16x16x32_bf16 v[118:121], v[166:169], a[80:83], v[118:121]
v_rcp_f32 v255, v255
v_mfma_f32_16x16x32_bf16 v[110:113], a[84:87], v[170:173], v[110:113]
v_mul_f32 v9, v9, v255
v_mul_f32 v10, v10, v255
v_mfma_f32_16x16x32_bf16 v[114:117], a[88:91], v[170:173], v[114:117]
global_load_lds_dwordx4 v14, s[42:43] offset:1024 sc1
v_cvt_pk_bf16_f32 v9, v9, v9
v_mfma_f32_16x16x32_bf16 v[118:121], v[170:173], a[92:95], v[118:121]
v_cvt_pk_bf16_f32 v10, v10, v10
v_and_b32 v224, v9, v40
v_mfma_f32_16x16x32_bf16 v[110:113], a[96:99], v[174:177], v[110:113]
v_and_b32 v225, v10, v41
v_and_b32 v222, v9, v42
v_mfma_f32_16x16x32_bf16 v[114:117], a[100:103], v[174:177], v[114:117]
s_waitcnt lgkmcnt(0)
v_and_b32 v223, v10, v43
v_fma_f32 v230, v102, v242, v134
v_mfma_f32_16x16x32_bf16 v[118:121], v[174:177], a[104:107], v[118:121]
v_mfma_f32_16x16x32_bf16 v[218:221], v[154:157], v[222:225], 0
v_mfma_f32_16x16x32_bf16 v[214:217], v[158:161], v[222:225], 0
v_fma_f32 v231, v102, v243, v135
v_fma_f32 v232, v102, v244, v136
v_mfma_f32_16x16x32_bf16 v[110:113], a[108:111], v[178:181], v[110:113]
v_fma_f32 v233, v102, v245, v137
v_cvt_pk_bf16_f32 v230, v230, v231
v_mfma_f32_16x16x32_bf16 v[114:117], a[112:115], v[178:181], v[114:117]
v_cvt_pk_bf16_f32 v231, v232, v233
v_fma_f32 v238, v86, v23, v122
v_mfma_f32_16x16x32_bf16 v[118:121], v[178:181], a[116:119], v[118:121]
ds_write_b64 v22, v[230:231] offset:41472
v_mfma_f32_16x16x32_bf16 v[110:113], a[120:123], v[182:185], v[110:113]
global_load_lds_dwordx4 v15, s[42:43] offset:2048 sc1
s_add_u32 s42, s42, s46
s_addc_u32 s43, s43, s47
)"
R"(v_mfma_f32_16x16x32_bf16 v[114:117], a[124:127], v[182:185], v[114:117]
v_cvt_pk_bf16_f32 v100, v218, v219
v_cvt_pk_bf16_f32 v101, v220, v221
v_mfma_f32_16x16x32_bf16 v[118:121], v[182:185], a[128:131], v[118:121]
ds_write_b64 v18, v[100:101]
v_mfma_f32_16x16x32_bf16 v[110:113], a[132:135], v[186:189], v[110:113]
v_cvt_pk_bf16_f32 v9, v216, v217
v_fma_f32 v239, v87, v23, v123
v_mfma_f32_16x16x32_bf16 v[114:117], a[136:139], v[186:189], v[114:117]
ds_write_b32 v19, v9
v_mfma_f32_16x16x32_bf16 v[118:121], v[186:189], a[140:143], v[118:121]
v_fma_f32 v240, v88, v23, v124
v_fma_f32 v241, v89, v23, v125
v_mfma_f32_16x16x32_bf16 v[110:113], a[144:147], v[190:193], v[110:113]
v_cvt_pk_bf16_f32 v142, v238, v239
v_cvt_pk_bf16_f32 v143, v240, v241
v_mfma_f32_16x16x32_bf16 v[114:117], a[148:151], v[190:193], v[114:117]
v_mfma_f32_16x16x32_bf16 v[118:121], v[190:193], a[152:155], v[118:121]
v_mfma_f32_16x16x32_bf16 v[110:113], a[156:159], v[194:197], v[110:113]
v_mfma_f32_16x16x32_bf16 v[114:117], a[160:163], v[194:197], v[114:117]
v_mfma_f32_16x16x32_bf16 v[118:121], v[194:197], a[164:167], v[118:121]
v_mfma_f32_16x16x32_bf16 v[110:113], a[168:171], v[198:201], v[110:113]
v_mfma_f32_16x16x32_bf16 v[114:117], a[172:175], v[198:201], v[114:117]
v_mfma_f32_16x16x32_bf16 v[118:121], v[198:201], a[176:179], v[118:121]
v_mfma_f32_16x16x32_bf16 v[110:113], a[180:183], v[202:205], v[110:113]
v_mfma_f32_16x16x32_bf16 v[114:117], a[184:187], v[202:205], v[114:117]
s_waitcnt vmcnt(5) lgkmcnt(0)
s_barrier
v_mfma_f32_16x16x32_bf16 v[118:121], v[202:205], a[188:191], v[118:121]
ds_read_b128 v[44:47], v51 offset:18432
v_mfma_f32_16x16x32_bf16 v[110:113], a[192:195], v[206:209], v[110:113]
ds_read_b128 v[96:99], v52 offset:18432
ds_read_b128 v[230:233], v17 offset:0
v_mfma_f32_16x16x32_bf16 v[114:117], a[196:199], v[206:209], v[114:117]
ds_read_b32 v23, v24 offset:0
ds_read_b32 v148, v21
v_mfma_f32_16x16x32_bf16 v[218:221], v[150:153], v[142:145], v[36:39]
v_mfma_f32_16x16x32_bf16 v[118:121], v[206:209], a[200:203], v[118:121]
ds_read_b128 v[104:107], v51 offset:20480
ds_read_b32 v26, v24 offset:192
v_mfma_f32_16x16x32_bf16 v[110:113], a[204:207], v[210:213], v[110:113]
ds_read_b128 v[0:3], v52 offset:20480
ds_read_b32 v53, v27
v_mfma_f32_16x16x32_bf16 v[114:117], a[208:211], v[210:213], v[114:117]
ds_read_b32 v95, v28
ds_read_b128 v[226:229], v25 offset:0
v_mfma_f32_16x16x32_bf16 v[118:121], v[210:213], a[212:215], v[118:121]
s_waitcnt lgkmcnt(10)
ds_read_b128 v[4:7], v51 offset:22528
ds_read_b64 v[158:159], v22 offset:41472
v_mfma_f32_16x16x32_bf16 v[134:137], v[44:47], a[216:219], 0
s_waitcnt lgkmcnt(11)
ds_read_b128 v[162:165], v52 offset:22528
ds_read_b32 v140, v20
v_mfma_f32_16x16x32_bf16 v[134:137], v[96:99], a[220:223], v[134:137]
v_max_f32 v109, v218, v219
v_max_f32 v254, v220, v221
v_mfma_f32_16x16x32_bf16 v[122:125], a[0:3], v[44:47], 0
s_waitcnt lgkmcnt(11)
ds_read_b128 v[166:169], v51 offset:24576
v_fma_f32 v238, v90, v23, v114
s_waitcnt lgkmcnt(10)
v_mfma_f32_16x16x32_bf16 v[134:137], v[104:107], a[224:227], v[134:137]
s_waitcnt lgkmcnt(8)
ds_read_b128 v[170:173], v52 offset:24576
v_fma_f32 v239, v91, v23, v115
v_mfma_f32_16x16x32_bf16 v[134:137], v[0:3], a[228:231], v[134:137]
global_store_dwordx4 v16, v[230:233], s[38:39] sc1
v_mfma_f32_16x16x32_bf16 v[126:129], a[4:7], v[44:47], 0
s_waitcnt lgkmcnt(5)
ds_read_b128 v[174:177], v51 offset:26624
v_add_u32 v16, 18432, v16
v_mfma_f32_16x16x32_bf16 v[134:137], v[4:7], a[232:235], v[134:137]
s_waitcnt lgkmcnt(4)
ds_read_b128 v[178:181], v52 offset:26624
v_fma_f32 v240, v92, v23, v116
v_mfma_f32_16x16x32_bf16 v[134:137], v[162:165], a[236:239], v[134:137]
v_fma_f32 v241, v93, v23, v117
v_cvt_pk_bf16_f32 v146, v238, v239
v_mfma_f32_16x16x32_bf16 v[130:133], v[44:47], a[8:11], 0
s_waitcnt lgkmcnt(3)
ds_read_b128 v[182:185], v51 offset:28672
v_cvt_pk_bf16_f32 v147, v240, v241
v_mfma_f32_16x16x32_bf16 v[134:137], v[166:169], a[240:243], v[134:137]
ds_read_b128 v[186:189], v52 offset:28672
v_mfma_f32_16x16x32_bf16 v[214:217], v[146:149], v[142:145], v[234:237]
s_waitcnt lgkmcnt(4)
v_mul_f32 v26, 0x3eae1a93, v26
v_mul_f32 v103, v53, v26
v_mfma_f32_16x16x32_bf16 v[134:137], v[170:173], a[244:247], v[134:137]
v_mul_f32 v108, v95, v26
v_fma_f32 v238, v94, v226, v118
v_mfma_f32_16x16x32_bf16 v[122:125], a[12:15], v[96:99], v[122:125]
s_waitcnt lgkmcnt(3)
ds_read_b128 v[190:193], v51 offset:30720
v_fma_f32 v239, v94, v227, v119
v_mfma_f32_16x16x32_bf16 v[134:137], v[174:177], a[248:251], v[134:137]
s_add_u32 m0, s49, 18432
ds_read_b128 v[194:197], v52 offset:30720
s_waitcnt lgkmcnt(4)
v_mfma_f32_16x16x32_bf16 v[134:137], v[178:181], a[252:255], v[134:137]
v_max3_f32 v109, v214, v215, v109
v_max3_f32 v254, v216, v217, v254
v_mfma_f32_16x16x32_bf16 v[126:129], a[16:19], v[96:99], v[126:129]
s_waitcnt lgkmcnt(3)
ds_read_b128 v[198:201], v51 offset:32768
v_mul_f32 v109, v109, v103
v_mfma_f32_16x16x32_bf16 v[134:137], v[182:185], v[54:57], v[134:137]
s_waitcnt lgkmcnt(3)
ds_read_b128 v[202:205], v52 offset:32768
global_load_lds_dwordx4 v11, s[42:43] offset:-2048 sc1
v_mfma_f32_16x16x32_bf16 v[134:137], v[186:189], v[58:61], v[134:137]
v_mul_f32 v254, v254, v108
v_max_f32 v255, v109, v254
v_mfma_f32_16x16x32_bf16 v[130:133], v[96:99], a[20:23], v[130:133]
s_waitcnt lgkmcnt(3)
ds_read_b128 v[206:209], v51 offset:34816
v_mov_b32 v8, v255
v_mfma_f32_16x16x32_bf16 v[134:137], v[190:193], v[62:65], v[134:137]
s_waitcnt lgkmcnt(3)
ds_read_b128 v[210:213], v52 offset:34816
v_fma_f32 v240, v94, v228, v120
v_mfma_f32_16x16x32_bf16 v[134:137], v[194:197], v[66:69], v[134:137]
v_permlane16_swap_b32 v255, v8
v_mfma_f32_16x16x32_bf16 v[122:125], a[24:27], v[104:107], v[122:125]
s_waitcnt lgkmcnt(3)
v_max_f32 v255, v255, v8
v_mov_b32 v8, v255
v_mfma_f32_16x16x32_bf16 v[134:137], v[198:201], v[70:73], v[134:137]
s_waitcnt lgkmcnt(2)
v_permlane32_swap_b32 v255, v8
v_mfma_f32_16x16x32_bf16 v[134:137], v[202:205], v[74:77], v[134:137]
v_max_f32 v255, v255, v8
v_sub_f32 v109, v109, v255
v_mfma_f32_16x16x32_bf16 v[126:129], a[28:31], v[104:107], v[126:129]
s_waitcnt lgkmcnt(1)
global_load_lds_dwordx4 v12, s[42:43] offset:-1024 sc1
v_sub_f32 v254, v254, v255
v_mfma_f32_16x16x32_bf16 v[134:137], v[206:209], v[78:81], v[134:137]
s_waitcnt lgkmcnt(0)
v_exp_f32 v9, v109
v_mfma_f32_16x16x32_bf16 v[134:137], v[210:213], v[82:85], v[134:137]
v_exp_f32 v10, v254
v_mfma_f32_16x16x32_bf16 v[130:133], v[104:107], a[32:35], v[130:133]
v_add_f32 v255, v9, v10
v_mov_b32 v8, v255
v_mfma_f32_16x16x32_bf16 v[122:125], a[36:39], v[0:3], v[122:125]
v_mul_f32 v9, v9, v53
v_fma_f32 v241, v94, v229, v121
v_mfma_f32_16x16x32_bf16 v[126:129], a[40:43], v[0:3], v[126:129]
v_permlane16_swap_b32 v255, v8
v_mfma_f32_16x16x32_bf16 v[130:133], v[0:3], a[44:47], v[130:133]
v_add_f32 v255, v255, v8
v_mov_b32 v8, v255
v_mfma_f32_16x16x32_bf16 v[122:125], a[48:51], v[4:7], v[122:125]
v_mul_f32 v10, v10, v95
v_cvt_pk_bf16_f32 v154, v238, v239
v_mfma_f32_16x16x32_bf16 v[126:129], a[52:55], v[4:7], v[126:129]
global_load_lds_dwordx4 v13, s[42:43] offset:0 sc1
v_cvt_pk_bf16_f32 v155, v240, v241
v_mfma_f32_16x16x32_bf16 v[130:133], v[4:7], a[56:59], v[130:133]
v_permlane32_swap_b32 v255, v8
v_mfma_f32_16x16x32_bf16 v[122:125], a[60:63], v[162:165], v[122:125]
v_add_f32 v255, v255, v8
v_fma_f32 v238, v86, v23, v110
v_mfma_f32_16x16x32_bf16 v[126:129], a[64:67], v[162:165], v[126:129]
v_rcp_f32 v255, v255
v_mfma_f32_16x16x32_bf16 v[130:133], v[162:165], a[68:71], v[130:133]
v_mfma_f32_16x16x32_bf16 v[122:125], a[72:75], v[166:169], v[122:125]
s_barrier
v_mfma_f32_16x16x32_bf16 v[126:129], a[76:79], v[166:169], v[126:129]
ds_read_b128 v[242:245], v25 offset:64
v_mul_f32 v9, v9, v255
v_mfma_f32_16x16x32_bf16 v[130:133], v[166:169], a[80:83], v[130:133]
v_mul_f32 v10, v10, v255
v_cvt_pk_bf16_f32 v9, v9, v9
v_mfma_f32_16x16x32_bf16 v[122:125], a[84:87], v[170:173], v[122:125]
global_load_lds_dwordx4 v14, s[42:43] offset:1024 sc1
v_cvt_pk_bf16_f32 v10, v10, v10
v_mfma_f32_16x16x32_bf16 v[126:129], a[88:91], v[170:173], v[126:129]
v_and_b32 v222, v9, v40
v_and_b32 v223, v10, v41
v_mfma_f32_16x16x32_bf16 v[130:133], v[170:173], a[92:95], v[130:133]
v_and_b32 v224, v9, v42
v_and_b32 v225, v10, v43
v_mfma_f32_16x16x32_bf16 v[122:125], a[96:99], v[174:177], v[122:125]
s_nop 0
v_mfma_f32_16x16x32_bf16 v[218:221], v[154:157], v[222:225], 0
v_mfma_f32_16x16x32_bf16 v[214:217], v[158:161], v[222:225], 0
v_fma_f32 v239, v87, v23, v111
v_fma_f32 v240, v88, v23, v112
v_mfma_f32_16x16x32_bf16 v[126:129], a[100:103], v[174:177], v[126:129]
s_waitcnt lgkmcnt(0)
v_fma_f32 v230, v102, v242, v134
v_fma_f32 v231, v102, v243, v135
v_mfma_f32_16x16x32_bf16 v[130:133], v[174:177], a[104:107], v[130:133]
v_fma_f32 v232, v102, v244, v136
v_fma_f32 v233, v102, v245, v137
v_mfma_f32_16x16x32_bf16 v[122:125], a[108:111], v[178:181], v[122:125]
v_cvt_pk_bf16_f32 v230, v230, v231
v_cvt_pk_bf16_f32 v231, v232, v233
v_mfma_f32_16x16x32_bf16 v[126:129], a[112:115], v[178:181], v[126:129]
v_cvt_pk_bf16_f32 v100, v218, v219
v_cvt_pk_bf16_f32 v101, v220, v221
v_mfma_f32_16x16x32_bf16 v[130:133], v[178:181], a[116:119], v[130:133]
global_load_lds_dwordx4 v15, s[42:43] offset:2048 sc1
s_sub_u32 s42, s42, 18432
s_subb_u32 s43, s43, 0
v_mfma_f32_16x16x32_bf16 v[122:125], a[120:123], v[182:185], v[122:125]
ds_write_b64 v18, v[100:101]
v_mfma_f32_16x16x32_bf16 v[126:129], a[124:127], v[182:185], v[126:129]
v_cvt_pk_bf16_f32 v9, v216, v217
v_fma_f32 v241, v89, v23, v113
v_mfma_f32_16x16x32_bf16 v[130:133], v[182:185], a[128:131], v[130:133]
ds_write_b32 v19, v9
v_mfma_f32_16x16x32_bf16 v[122:125], a[132:135], v[186:189], v[122:125]
ds_write_b64 v22, v[230:231] offset:41472
v_mfma_f32_16x16x32_bf16 v[126:129], a[136:139], v[186:189], v[126:129]
v_cvt_pk_bf16_f32 v138, v238, v239
v_cvt_pk_bf16_f32 v139, v240, v241
v_mfma_f32_16x16x32_bf16 v[130:133], v[186:189], a[140:143], v[130:133]
v_mfma_f32_16x16x32_bf16 v[122:125], a[144:147], v[190:193], v[122:125]
v_mfma_f32_16x16x32_bf16 v[126:129], a[148:151], v[190:193], v[126:129]
v_mfma_f32_16x16x32_bf16 v[130:133], v[190:193], a[152:155], v[130:133]
v_mfma_f32_16x16x32_bf16 v[122:125], a[156:159], v[194:197], v[122:125]
v_mfma_f32_16x16x32_bf16 v[126:129], a[160:163], v[194:197], v[126:129]
v_mfma_f32_16x16x32_bf16 v[130:133], v[194:197], a[164:167], v[130:133]
v_mfma_f32_16x16x32_bf16 v[122:125], a[168:171], v[198:201], v[122:125]
v_mfma_f32_16x16x32_bf16 v[126:129], a[172:175], v[198:201], v[126:129]
v_xor_b32 v51, 0x10000, v51
v_xor_b32 v52, 0x10000, v52
v_mfma_f32_16x16x32_bf16 v[130:133], v[198:201], a[176:179], v[130:133]
v_mfma_f32_16x16x32_bf16 v[122:125], a[180:183], v[202:205], v[122:125]
v_mfma_f32_16x16x32_bf16 v[126:129], a[184:187], v[202:205], v[126:129]
s_waitcnt vmcnt(5) lgkmcnt(0)
s_barrier
.Lqa_in26:
v_mfma_f32_16x16x32_bf16 v[130:133], v[202:205], a[188:191], v[130:133]
s_cmp_eq_u32 s70, s73
s_cbranch_scc1 .Lqa_hk27
.Lqa_hr28:
ds_read_b128 v[44:47], v51 offset:0
v_mfma_f32_16x16x32_bf16 v[122:125], a[192:195], v[206:209], v[122:125]
ds_read_b128 v[96:99], v52 offset:0
ds_read_b128 v[230:233], v17 offset:0
v_mfma_f32_16x16x32_bf16 v[126:129], a[196:199], v[206:209], v[126:129]
ds_read_b32 v23, v24 offset:64
ds_read_b32 v152, v21
v_mfma_f32_16x16x32_bf16 v[218:221], v[146:149], v[138:141], v[36:39]
v_mfma_f32_16x16x32_bf16 v[130:133], v[206:209], a[200:203], v[130:133]
ds_read_b128 v[104:107], v51 offset:2048
ds_read_b32 v26, v24 offset:128
v_mfma_f32_16x16x32_bf16 v[122:125], a[204:207], v[210:213], v[122:125]
ds_read_b128 v[0:3], v52 offset:2048
ds_read_b32 v53, v29
v_mfma_f32_16x16x32_bf16 v[126:129], a[208:211], v[210:213], v[126:129]
v_mfma_f32_16x16x32_bf16 v[130:133], v[210:213], a[212:215], v[130:133]
s_sub_u32 s70, s70, 1
ds_read_b128 v[4:7], v51 offset:4096
s_cbranch_scc0 .Lqa_top45
s_waitcnt lgkmcnt(0)
ds_read_b32 v95, v30
ds_read_b128 v[226:229], v25 offset:64
ds_read_b64 v[160:161], v22 offset:41472
ds_read_b32 v144, v20
v_fma_f32 v238, v90, v23, v126
global_store_dwordx4 v16, v[230:233], s[38:39] sc1
v_add_u32 v16, 18432, v16
v_fma_f32 v239, v91, v23, v127
v_fma_f32 v240, v92, v23, v128
v_fma_f32 v241, v93, v23, v129
v_cvt_pk_bf16_f32 v150, v238, v239
v_cvt_pk_bf16_f32 v151, v240, v241
s_nop 1
v_mfma_f32_16x16x32_bf16 v[214:217], v[150:153], v[138:141], v[32:35]
s_waitcnt lgkmcnt(2)
v_max_f32 v109, v218, v219
v_max_f32 v254, v220, v221
v_fma_f32 v238, v94, v226, v130
v_mul_f32 v26, 0x3eae1a93, v26
v_mul_f32 v103, v53, v26
v_mul_f32 v108, v95, v26
s_nop 0
v_max3_f32 v109, v214, v215, v109
v_max3_f32 v254, v216, v217, v254
v_mul_f32 v109, v109, v103
v_mul_f32 v254, v254, v108
v_max_f32 v255, v109, v254
v_mov_b32 v8, v255
s_nop 1
v_permlane16_swap_b32 v255, v8
v_max_f32 v255, v255, v8
v_mov_b32 v8, v255
s_nop 1
v_permlane32_swap_b32 v255, v8
v_max_f32 v255, v255, v8
v_sub_f32 v109, v109, v255
v_sub_f32 v254, v254, v255
v_exp_f32 v9, v109
v_exp_f32 v10, v254
s_nop 0
v_add_f32 v255, v9, v10
v_mov_b32 v8, v255
v_mul_f32 v9, v9, v53
v_fma_f32 v239, v94, v227, v131
v_permlane16_swap_b32 v255, v8
v_add_f32 v255, v255, v8
v_mov_b32 v8, v255
v_mul_f32 v10, v10, v95
v_fma_f32 v240, v94, v228, v132
v_fma_f32 v241, v94, v229, v133
v_permlane32_swap_b32 v255, v8
v_add_f32 v255, v255, v8
v_cvt_pk_bf16_f32 v156, v238, v239
s_waitcnt lgkmcnt(0)
s_barrier
v_cvt_pk_bf16_f32 v157, v240, v241
v_rcp_f32 v255, v255
s_nop 0
v_mul_f32 v9, v9, v255
v_mul_f32 v10, v10, v255
v_cvt_pk_bf16_f32 v9, v9, v9
v_cvt_pk_bf16_f32 v10, v10, v10
v_and_b32 v224, v9, v40
v_and_b32 v225, v10, v41
v_and_b32 v222, v9, v42
v_and_b32 v223, v10, v43
s_nop 1
v_mfma_f32_16x16x32_bf16 v[218:221], v[154:157], v[222:225], 0
v_mfma_f32_16x16x32_bf16 v[214:217], v[158:161], v[222:225], 0
v_fma_f32 v238, v86, v23, v122
s_nop 5
v_cvt_pk_bf16_f32 v100, v218, v219
v_cvt_pk_bf16_f32 v101, v220, v221
ds_write_b64 v18, v[100:101]
v_cvt_pk_bf16_f32 v9, v216, v217
v_fma_f32 v239, v87, v23, v123
ds_write_b32 v19, v9
v_fma_f32 v240, v88, v23, v124
v_fma_f32 v241, v89, v23, v125
v_cvt_pk_bf16_f32 v142, v238, v239
v_cvt_pk_bf16_f32 v143, v240, v241
s_waitcnt vmcnt(0)
s_waitcnt lgkmcnt(0)
s_barrier
ds_read_b128 v[230:233], v17 offset:0
s_waitcnt lgkmcnt(0)
global_store_dwordx4 v16, v[230:233], s[38:39] sc1
s_waitcnt vmcnt(0)
s_barrier
s_endpgm
.Lqa_w3:
s_getpc_b64 s[90:91]
.Lqa_pc69:
s_add_u32 s90, s90, .Lqa_e54-.Lqa_pc69
s_addc_u32 s91, s91, 0
s_branch .Lqa_k61
.Lqa_bk68:
v_add_u32 v255, 0x10000, v9
v_add_u32 v162, 0x20000, v9
v_add_u32 v163, 0x10000, v10
v_add_u32 v164, 0x20000, v10
v_mov_b32 v106, 0x3F803F80
v_mov_b32 v107, 0x3F803F80
v_mov_b32 v108, 0x3F803F80
v_mov_b32 v109, 0x3F803F80
v_mov_b32 v141, 0
v_mov_b32 v145, 0
v_mov_b32 v149, 0
v_mov_b32 v153, 0
v_mov_b32 v165, 46080
s_waitcnt lgkmcnt(0)
s_mul_i32 s37, s37, 0x9E3779B1
s_add_u32 s37, s37, 0x7F4A7C15
v_add_u32 v46, s37, v46
v_add_u32 v48, s37, v48
v_add_u32 v50, s37, v50
ds_write_b32 v165, v141
s_mov_b64 s[76:77], 0
s_mov_b32 s73, 18
s_add_u32 m0, s48, 4096
s_nop 0
global_load_lds_dwordx4 v0, s[40:41] offset:-4096
global_load_lds_dwordx4 v1, s[40:41] offset:-3072
global_load_lds_dwordx4 v2, s[40:41] offset:-2048
global_load_lds_dwordx4 v3, s[40:41] offset:-1024
global_load_lds_dwordx4 v4, s[40:41] offset:0
global_load_lds_dwordx4 v5, s[40:41] offset:1024
global_load_lds_dwordx4 v6, s[40:41] offset:2048
global_load_lds_dwordx4 v7, s[40:41] offset:3072
s_add_u32 m0, s48, 12288
s_nop 0
global_load_lds_dwordx4 v8, s[40:41] offset:-4096
s_add_u32 s40, s40, 221184
s_addc_u32 s41, s41, 0
s_add_u32 m0, s48, 40960
s_nop 0
global_load_lds_dwordx4 v0, s[40:41] offset:-4096
global_load_lds_dwordx4 v1, s[40:41] offset:-3072
global_load_lds_dwordx4 v2, s[40:41] offset:-2048
)"
R"(global_load_lds_dwordx4 v3, s[40:41] offset:-1024
global_load_lds_dwordx4 v4, s[40:41] offset:0
global_load_lds_dwordx4 v5, s[40:41] offset:1024
global_load_lds_dwordx4 v6, s[40:41] offset:2048
global_load_lds_dwordx4 v7, s[40:41] offset:3072
s_add_u32 m0, s48, 49152
s_nop 0
global_load_lds_dwordx4 v8, s[40:41] offset:-4096
s_add_u32 s40, s40, 221184
s_addc_u32 s41, s41, 0
s_waitcnt vmcnt(9) lgkmcnt(0)
s_barrier
s_add_u32 m0, s48, 77824
ds_read_b64_tr_b16 a[0:1], v9 offset:47104
ds_read_b64_tr_b16 a[2:3], v9 offset:49408
global_load_lds_dwordx4 v0, s[40:41] offset:-4096
ds_read_b64_tr_b16 a[4:5], v9 offset:47296
global_load_lds_dwordx4 v1, s[40:41] offset:-3072
ds_read_b64_tr_b16 a[6:7], v9 offset:49600
global_load_lds_dwordx4 v2, s[40:41] offset:-2048
ds_read_b64_tr_b16 a[8:9], v9 offset:47488
global_load_lds_dwordx4 v3, s[40:41] offset:-1024
ds_read_b64_tr_b16 a[10:11], v9 offset:49792
ds_read_b128 v[166:169], v51 offset:0
global_load_lds_dwordx4 v4, s[40:41] offset:0
ds_read_b128 v[174:177], v51 offset:18432
global_load_lds_dwordx4 v5, s[40:41] offset:1024
ds_read_b64_tr_b16 a[12:13], v255 offset:0
global_load_lds_dwordx4 v6, s[40:41] offset:2048
ds_read_b64_tr_b16 a[14:15], v255 offset:2304
global_load_lds_dwordx4 v7, s[40:41] offset:3072
ds_read_b64_tr_b16 a[16:17], v255 offset:192
ds_read_b64_tr_b16 a[18:19], v255 offset:2496
s_add_u32 m0, s48, 86016
ds_read_b64_tr_b16 a[20:21], v255 offset:384
global_load_lds_dwordx4 v8, s[40:41] offset:-4096
ds_read_b64_tr_b16 a[22:23], v255 offset:2688
s_add_u32 s40, s40, 221184
ds_read_b128 v[170:173], v52 offset:0
s_addc_u32 s41, s41, 0
ds_read_b128 v[178:181], v52 offset:18432
s_waitcnt vmcnt(9) lgkmcnt(0)
s_barrier
s_add_u32 m0, s48, 4096
ds_read_b64_tr_b16 a[24:25], v255 offset:18432
v_mfma_f32_16x16x32_bf16 v[86:89], a[0:3], v[106:109], 0
v_mfma_f32_16x16x32_bf16 v[90:93], a[4:7], v[106:109], 0
global_load_lds_dwordx4 v0, s[40:41] offset:-4096
ds_read_b64_tr_b16 a[26:27], v255 offset:20736
v_mfma_f32_16x16x32_bf16 v[94:97], v[106:109], a[8:11], 0
ds_read_b64_tr_b16 a[28:29], v255 offset:18624
global_load_lds_dwordx4 v1, s[40:41] offset:-3072
v_mfma_f32_16x16x32_bf16 v[110:113], a[0:3], v[166:169], 0
ds_read_b64_tr_b16 a[30:31], v255 offset:20928
v_mfma_f32_16x16x32_bf16 v[114:117], a[4:7], v[166:169], 0
global_load_lds_dwordx4 v2, s[40:41] offset:-2048
ds_read_b64_tr_b16 a[32:33], v255 offset:18816
v_mfma_f32_16x16x32_bf16 v[118:121], v[166:169], a[8:11], 0
ds_read_b64_tr_b16 a[34:35], v255 offset:21120
global_load_lds_dwordx4 v3, s[40:41] offset:-1024
v_mfma_f32_16x16x32_bf16 v[134:137], v[166:169], v[166:169], 0
ds_read_b128 v[182:185], v51 offset:2048
v_mfma_f32_16x16x32_bf16 v[122:125], a[0:3], v[174:177], 0
global_load_lds_dwordx4 v4, s[40:41] offset:0
ds_read_b128 v[190:193], v51 offset:20480
v_mfma_f32_16x16x32_bf16 v[126:129], a[4:7], v[174:177], 0
ds_read_b64_tr_b16 a[36:37], v255 offset:36864
global_load_lds_dwordx4 v5, s[40:41] offset:1024
v_mfma_f32_16x16x32_bf16 v[130:133], v[174:177], a[8:11], 0
v_mfma_f32_16x16x32_bf16 v[158:161], v[174:177], v[174:177], 0
ds_read_b64_tr_b16 a[38:39], v255 offset:39168
global_load_lds_dwordx4 v6, s[40:41] offset:2048
v_mfma_f32_16x16x32_bf16 v[86:89], a[12:15], v[106:109], v[86:89]
ds_read_b64_tr_b16 a[40:41], v255 offset:37056
v_mfma_f32_16x16x32_bf16 v[90:93], a[16:19], v[106:109], v[90:93]
global_load_lds_dwordx4 v7, s[40:41] offset:3072
ds_read_b64_tr_b16 a[42:43], v255 offset:39360
v_mfma_f32_16x16x32_bf16 v[94:97], v[106:109], a[20:23], v[94:97]
s_add_u32 m0, s48, 12288
ds_read_b64_tr_b16 a[44:45], v255 offset:37248
v_mfma_f32_16x16x32_bf16 v[110:113], a[12:15], v[170:173], v[110:113]
ds_read_b64_tr_b16 a[46:47], v255 offset:39552
v_mfma_f32_16x16x32_bf16 v[114:117], a[16:19], v[170:173], v[114:117]
global_load_lds_dwordx4 v8, s[40:41] offset:-4096
ds_read_b128 v[186:189], v52 offset:2048
v_mfma_f32_16x16x32_bf16 v[118:121], v[170:173], a[20:23], v[118:121]
s_add_u32 s40, s40, 221184
ds_read_b128 v[194:197], v52 offset:20480
v_mfma_f32_16x16x32_bf16 v[134:137], v[170:173], v[170:173], v[134:137]
v_mfma_f32_16x16x32_bf16 v[122:125], a[12:15], v[178:181], v[122:125]
v_mfma_f32_16x16x32_bf16 v[126:129], a[16:19], v[178:181], v[126:129]
s_addc_u32 s41, s41, 0
v_mfma_f32_16x16x32_bf16 v[130:133], v[178:181], a[20:23], v[130:133]
v_mfma_f32_16x16x32_bf16 v[158:161], v[178:181], v[178:181], v[158:161]
s_waitcnt vmcnt(9) lgkmcnt(0)
s_barrier
s_add_u32 m0, s48, 40960
ds_read_b64_tr_b16 a[48:49], v255 offset:55296
v_mfma_f32_16x16x32_bf16 v[86:89], a[24:27], v[106:109], v[86:89]
v_mfma_f32_16x16x32_bf16 v[90:93], a[28:31], v[106:109], v[90:93]
global_load_lds_dwordx4 v0, s[40:41] offset:-4096
ds_read_b64_tr_b16 a[50:51], v255 offset:57600
v_mfma_f32_16x16x32_bf16 v[94:97], v[106:109], a[32:35], v[94:97]
ds_read_b64_tr_b16 a[52:53], v255 offset:55488
global_load_lds_dwordx4 v1, s[40:41] offset:-3072
v_mfma_f32_16x16x32_bf16 v[110:113], a[24:27], v[182:185], v[110:113]
ds_read_b64_tr_b16 a[54:55], v255 offset:57792
v_mfma_f32_16x16x32_bf16 v[114:117], a[28:31], v[182:185], v[114:117]
global_load_lds_dwordx4 v2, s[40:41] offset:-2048
ds_read_b64_tr_b16 a[56:57], v255 offset:55680
v_mfma_f32_16x16x32_bf16 v[118:121], v[182:185], a[32:35], v[118:121]
ds_read_b64_tr_b16 a[58:59], v255 offset:57984
global_load_lds_dwordx4 v3, s[40:41] offset:-1024
v_mfma_f32_16x16x32_bf16 v[134:137], v[182:185], v[182:185], v[134:137]
ds_read_b128 v[166:169], v51 offset:4096
v_mfma_f32_16x16x32_bf16 v[122:125], a[24:27], v[190:193], v[122:125]
global_load_lds_dwordx4 v4, s[40:41] offset:0
ds_read_b128 v[174:177], v51 offset:22528
v_mfma_f32_16x16x32_bf16 v[126:129], a[28:31], v[190:193], v[126:129]
ds_read_b64_tr_b16 a[60:61], v162 offset:8192
global_load_lds_dwordx4 v5, s[40:41] offset:1024
v_mfma_f32_16x16x32_bf16 v[130:133], v[190:193], a[32:35], v[130:133]
v_mfma_f32_16x16x32_bf16 v[158:161], v[190:193], v[190:193], v[158:161]
ds_read_b64_tr_b16 a[62:63], v162 offset:10496
global_load_lds_dwordx4 v6, s[40:41] offset:2048
v_mfma_f32_16x16x32_bf16 v[86:89], a[36:39], v[106:109], v[86:89]
ds_read_b64_tr_b16 a[64:65], v162 offset:8384
v_mfma_f32_16x16x32_bf16 v[90:93], a[40:43], v[106:109], v[90:93]
global_load_lds_dwordx4 v7, s[40:41] offset:3072
ds_read_b64_tr_b16 a[66:67], v162 offset:10688
v_mfma_f32_16x16x32_bf16 v[94:97], v[106:109], a[44:47], v[94:97]
s_add_u32 m0, s48, 49152
ds_read_b64_tr_b16 a[68:69], v162 offset:8576
v_mfma_f32_16x16x32_bf16 v[110:113], a[36:39], v[186:189], v[110:113]
ds_read_b64_tr_b16 a[70:71], v162 offset:10880
v_mfma_f32_16x16x32_bf16 v[114:117], a[40:43], v[186:189], v[114:117]
global_load_lds_dwordx4 v8, s[40:41] offset:-4096
ds_read_b128 v[170:173], v52 offset:4096
v_mfma_f32_16x16x32_bf16 v[118:121], v[186:189], a[44:47], v[118:121]
s_add_u32 s40, s40, 221184
ds_read_b128 v[178:181], v52 offset:22528
v_mfma_f32_16x16x32_bf16 v[134:137], v[186:189], v[186:189], v[134:137]
v_mfma_f32_16x16x32_bf16 v[122:125], a[36:39], v[194:197], v[122:125]
v_mfma_f32_16x16x32_bf16 v[126:129], a[40:43], v[194:197], v[126:129]
s_addc_u32 s41, s41, 0
v_mfma_f32_16x16x32_bf16 v[130:133], v[194:197], a[44:47], v[130:133]
v_mfma_f32_16x16x32_bf16 v[158:161], v[194:197], v[194:197], v[158:161]
s_waitcnt vmcnt(9) lgkmcnt(0)
s_barrier
s_add_u32 m0, s48, 77824
ds_read_b64_tr_b16 a[72:73], v9 offset:47104
v_mfma_f32_16x16x32_bf16 v[86:89], a[48:51], v[106:109], v[86:89]
v_mfma_f32_16x16x32_bf16 v[90:93], a[52:55], v[106:109], v[90:93]
global_load_lds_dwordx4 v0, s[40:41] offset:-4096
ds_read_b64_tr_b16 a[74:75], v9 offset:49408
v_mfma_f32_16x16x32_bf16 v[94:97], v[106:109], a[56:59], v[94:97]
ds_read_b64_tr_b16 a[76:77], v9 offset:47296
global_load_lds_dwordx4 v1, s[40:41] offset:-3072
v_mfma_f32_16x16x32_bf16 v[110:113], a[48:51], v[166:169], v[110:113]
ds_read_b64_tr_b16 a[78:79], v9 offset:49600
v_mfma_f32_16x16x32_bf16 v[114:117], a[52:55], v[166:169], v[114:117]
global_load_lds_dwordx4 v2, s[40:41] offset:-2048
ds_read_b64_tr_b16 a[80:81], v9 offset:47488
v_mfma_f32_16x16x32_bf16 v[118:121], v[166:169], a[56:59], v[118:121]
ds_read_b64_tr_b16 a[82:83], v9 offset:49792
global_load_lds_dwordx4 v3, s[40:41] offset:-1024
v_mfma_f32_16x16x32_bf16 v[134:137], v[166:169], v[166:169], v[134:137]
ds_read_b128 v[182:185], v51 offset:6144
v_mfma_f32_16x16x32_bf16 v[122:125], a[48:51], v[174:177], v[122:125]
global_load_lds_dwordx4 v4, s[40:41] offset:0
ds_read_b128 v[190:193], v51 offset:24576
v_mfma_f32_16x16x32_bf16 v[126:129], a[52:55], v[174:177], v[126:129]
ds_read_b64_tr_b16 a[84:85], v255 offset:0
global_load_lds_dwordx4 v5, s[40:41] offset:1024
v_mfma_f32_16x16x32_bf16 v[130:133], v[174:177], a[56:59], v[130:133]
v_mfma_f32_16x16x32_bf16 v[158:161], v[174:177], v[174:177], v[158:161]
ds_read_b64_tr_b16 a[86:87], v255 offset:2304
global_load_lds_dwordx4 v6, s[40:41] offset:2048
v_mfma_f32_16x16x32_bf16 v[86:89], a[60:63], v[106:109], v[86:89]
ds_read_b64_tr_b16 a[88:89], v255 offset:192
v_mfma_f32_16x16x32_bf16 v[90:93], a[64:67], v[106:109], v[90:93]
global_load_lds_dwordx4 v7, s[40:41] offset:3072
ds_read_b64_tr_b16 a[90:91], v255 offset:2496
v_mfma_f32_16x16x32_bf16 v[94:97], v[106:109], a[68:71], v[94:97]
s_add_u32 m0, s48, 86016
ds_read_b64_tr_b16 a[92:93], v255 offset:384
v_mfma_f32_16x16x32_bf16 v[110:113], a[60:63], v[170:173], v[110:113]
ds_read_b64_tr_b16 a[94:95], v255 offset:2688
v_mfma_f32_16x16x32_bf16 v[114:117], a[64:67], v[170:173], v[114:117]
global_load_lds_dwordx4 v8, s[40:41] offset:-4096
ds_read_b128 v[186:189], v52 offset:6144
v_mfma_f32_16x16x32_bf16 v[118:121], v[170:173], a[68:71], v[118:121]
s_add_u32 s40, s40, 221184
ds_read_b128 v[194:197], v52 offset:24576
v_mfma_f32_16x16x32_bf16 v[134:137], v[170:173], v[170:173], v[134:137]
v_mfma_f32_16x16x32_bf16 v[122:125], a[60:63], v[178:181], v[122:125]
v_mfma_f32_16x16x32_bf16 v[126:129], a[64:67], v[178:181], v[126:129]
s_addc_u32 s41, s41, 0
v_mfma_f32_16x16x32_bf16 v[130:133], v[178:181], a[68:71], v[130:133]
v_mfma_f32_16x16x32_bf16 v[158:161], v[178:181], v[178:181], v[158:161]
s_waitcnt vmcnt(9) lgkmcnt(0)
s_barrier
s_add_u32 m0, s48, 4096
ds_read_b64_tr_b16 a[96:97], v255 offset:18432
v_mfma_f32_16x16x32_bf16 v[86:89], a[72:75], v[106:109], v[86:89]
v_mfma_f32_16x16x32_bf16 v[90:93], a[76:79], v[106:109], v[90:93]
global_load_lds_dwordx4 v0, s[40:41] offset:-4096
ds_read_b64_tr_b16 a[98:99], v255 offset:20736
v_mfma_f32_16x16x32_bf16 v[94:97], v[106:109], a[80:83], v[94:97]
ds_read_b64_tr_b16 a[100:101], v255 offset:18624
global_load_lds_dwordx4 v1, s[40:41] offset:-3072
v_mfma_f32_16x16x32_bf16 v[110:113], a[72:75], v[182:185], v[110:113]
ds_read_b64_tr_b16 a[102:103], v255 offset:20928
v_mfma_f32_16x16x32_bf16 v[114:117], a[76:79], v[182:185], v[114:117]
global_load_lds_dwordx4 v2, s[40:41] offset:-2048
ds_read_b64_tr_b16 a[104:105], v255 offset:18816
v_mfma_f32_16x16x32_bf16 v[118:121], v[182:185], a[80:83], v[118:121]
ds_read_b64_tr_b16 a[106:107], v255 offset:21120
global_load_lds_dwordx4 v3, s[40:41] offset:-1024
v_mfma_f32_16x16x32_bf16 v[134:137], v[182:185], v[182:185], v[134:137]
ds_read_b128 v[166:169], v51 offset:8192
v_mfma_f32_16x16x32_bf16 v[122:125], a[72:75], v[190:193], v[122:125]
global_load_lds_dwordx4 v4, s[40:41] offset:0
ds_read_b128 v[174:177], v51 offset:26624
v_mfma_f32_16x16x32_bf16 v[126:129], a[76:79], v[190:193], v[126:129]
ds_read_b64_tr_b16 a[108:109], v255 offset:36864
global_load_lds_dwordx4 v5, s[40:41] offset:1024
v_mfma_f32_16x16x32_bf16 v[130:133], v[190:193], a[80:83], v[130:133]
v_mfma_f32_16x16x32_bf16 v[158:161], v[190:193], v[190:193], v[158:161]
ds_read_b64_tr_b16 a[110:111], v255 offset:39168
global_load_lds_dwordx4 v6, s[40:41] offset:2048
v_mfma_f32_16x16x32_bf16 v[86:89], a[84:87], v[106:109], v[86:89]
ds_read_b64_tr_b16 a[112:113], v255 offset:37056
v_mfma_f32_16x16x32_bf16 v[90:93], a[88:91], v[106:109], v[90:93]
global_load_lds_dwordx4 v7, s[40:41] offset:3072
ds_read_b64_tr_b16 a[114:115], v255 offset:39360
v_mfma_f32_16x16x32_bf16 v[94:97], v[106:109], a[92:95], v[94:97]
s_add_u32 m0, s48, 12288
ds_read_b64_tr_b16 a[116:117], v255 offset:37248
v_mfma_f32_16x16x32_bf16 v[110:113], a[84:87], v[186:189], v[110:113]
ds_read_b64_tr_b16 a[118:119], v255 offset:39552
v_mfma_f32_16x16x32_bf16 v[114:117], a[88:91], v[186:189], v[114:117]
global_load_lds_dwordx4 v8, s[40:41] offset:-4096
ds_read_b128 v[170:173], v52 offset:8192
v_mfma_f32_16x16x32_bf16 v[118:121], v[186:189], a[92:95], v[118:121]
s_add_u32 s40, s40, 221184
ds_read_b128 v[178:181], v52 offset:26624
v_mfma_f32_16x16x32_bf16 v[134:137], v[186:189], v[186:189], v[134:137]
v_mfma_f32_16x16x32_bf16 v[122:125], a[84:87], v[194:197], v[122:125]
v_mfma_f32_16x16x32_bf16 v[126:129], a[88:91], v[194:197], v[126:129]
s_addc_u32 s41, s41, 0
v_mfma_f32_16x16x32_bf16 v[130:133], v[194:197], a[92:95], v[130:133]
v_mfma_f32_16x16x32_bf16 v[158:161], v[194:197], v[194:197], v[158:161]
s_waitcnt vmcnt(9) lgkmcnt(0)
s_barrier
s_add_u32 m0, s48, 40960
ds_read_b64_tr_b16 a[120:121], v255 offset:55296
v_mfma_f32_16x16x32_bf16 v[86:89], a[96:99], v[106:109], v[86:89]
v_mfma_f32_16x16x32_bf16 v[90:93], a[100:103], v[106:109], v[90:93]
global_load_lds_dwordx4 v0, s[40:41] offset:-4096
ds_read_b64_tr_b16 a[122:123], v255 offset:57600
v_mfma_f32_16x16x32_bf16 v[94:97], v[106:109], a[104:107], v[94:97]
ds_read_b64_tr_b16 a[124:125], v255 offset:55488
global_load_lds_dwordx4 v1, s[40:41] offset:-3072
v_mfma_f32_16x16x32_bf16 v[110:113], a[96:99], v[166:169], v[110:113]
ds_read_b64_tr_b16 a[126:127], v255 offset:57792
v_mfma_f32_16x16x32_bf16 v[114:117], a[100:103], v[166:169], v[114:117]
global_load_lds_dwordx4 v2, s[40:41] offset:-2048
ds_read_b64_tr_b16 a[128:129], v255 offset:55680
v_mfma_f32_16x16x32_bf16 v[118:121], v[166:169], a[104:107], v[118:121]
ds_read_b64_tr_b16 a[130:131], v255 offset:57984
global_load_lds_dwordx4 v3, s[40:41] offset:-1024
v_mfma_f32_16x16x32_bf16 v[134:137], v[166:169], v[166:169], v[134:137]
ds_read_b128 v[182:185], v51 offset:10240
v_mfma_f32_16x16x32_bf16 v[122:125], a[96:99], v[174:177], v[122:125]
global_load_lds_dwordx4 v4, s[40:41] offset:0
ds_read_b128 v[190:193], v51 offset:28672
v_mfma_f32_16x16x32_bf16 v[126:129], a[100:103], v[174:177], v[126:129]
ds_read_b64_tr_b16 a[132:133], v162 offset:8192
global_load_lds_dwordx4 v5, s[40:41] offset:1024
v_mfma_f32_16x16x32_bf16 v[130:133], v[174:177], a[104:107], v[130:133]
v_mfma_f32_16x16x32_bf16 v[158:161], v[174:177], v[174:177], v[158:161]
ds_read_b64_tr_b16 a[134:135], v162 offset:10496
global_load_lds_dwordx4 v6, s[40:41] offset:2048
v_mfma_f32_16x16x32_bf16 v[86:89], a[108:111], v[106:109], v[86:89]
ds_read_b64_tr_b16 a[136:137], v162 offset:8384
v_mfma_f32_16x16x32_bf16 v[90:93], a[112:115], v[106:109], v[90:93]
global_load_lds_dwordx4 v7, s[40:41] offset:3072
ds_read_b64_tr_b16 a[138:139], v162 offset:10688
v_mfma_f32_16x16x32_bf16 v[94:97], v[106:109], a[116:119], v[94:97]
s_add_u32 m0, s48, 49152
ds_read_b64_tr_b16 a[140:141], v162 offset:8576
v_mfma_f32_16x16x32_bf16 v[110:113], a[108:111], v[170:173], v[110:113]
ds_read_b64_tr_b16 a[142:143], v162 offset:10880
v_mfma_f32_16x16x32_bf16 v[114:117], a[112:115], v[170:173], v[114:117]
global_load_lds_dwordx4 v8, s[40:41] offset:-4096
ds_read_b128 v[186:189], v52 offset:10240
v_mfma_f32_16x16x32_bf16 v[118:121], v[170:173], a[116:119], v[118:121]
s_add_u32 s40, s40, 221184
ds_read_b128 v[194:197], v52 offset:28672
v_mfma_f32_16x16x32_bf16 v[134:137], v[170:173], v[170:173], v[134:137]
v_mfma_f32_16x16x32_bf16 v[122:125], a[108:111], v[178:181], v[122:125]
v_mfma_f32_16x16x32_bf16 v[126:129], a[112:115], v[178:181], v[126:129]
s_addc_u32 s41, s41, 0
v_mfma_f32_16x16x32_bf16 v[130:133], v[178:181], a[116:119], v[130:133]
v_mfma_f32_16x16x32_bf16 v[158:161], v[178:181], v[178:181], v[158:161]
s_waitcnt vmcnt(9) lgkmcnt(0)
s_barrier
s_add_u32 m0, s48, 77824
ds_read_b64_tr_b16 a[144:145], v9 offset:47104
v_mfma_f32_16x16x32_bf16 v[86:89], a[120:123], v[106:109], v[86:89]
v_mfma_f32_16x16x32_bf16 v[90:93], a[124:127], v[106:109], v[90:93]
global_load_lds_dwordx4 v0, s[40:41] offset:-4096
ds_read_b64_tr_b16 a[146:147], v9 offset:49408
v_mfma_f32_16x16x32_bf16 v[94:97], v[106:109], a[128:131], v[94:97]
ds_read_b64_tr_b16 a[148:149], v9 offset:47296
global_load_lds_dwordx4 v1, s[40:41] offset:-3072
v_mfma_f32_16x16x32_bf16 v[110:113], a[120:123], v[182:185], v[110:113]
ds_read_b64_tr_b16 a[150:151], v9 offset:49600
v_mfma_f32_16x16x32_bf16 v[114:117], a[124:127], v[182:185], v[114:117]
global_load_lds_dwordx4 v2, s[40:41] offset:-2048
ds_read_b64_tr_b16 a[152:153], v9 offset:47488
v_mfma_f32_16x16x32_bf16 v[118:121], v[182:185], a[128:131], v[118:121]
ds_read_b64_tr_b16 a[154:155], v9 offset:49792
global_load_lds_dwordx4 v3, s[40:41] offset:-1024
v_mfma_f32_16x16x32_bf16 v[134:137], v[182:185], v[182:185], v[134:137]
ds_read_b128 v[166:169], v51 offset:12288
v_mfma_f32_16x16x32_bf16 v[122:125], a[120:123], v[190:193], v[122:125]
global_load_lds_dwordx4 v4, s[40:41] offset:0
ds_read_b128 v[174:177], v51 offset:30720
v_mfma_f32_16x16x32_bf16 v[126:129], a[124:127], v[190:193], v[126:129]
ds_read_b64_tr_b16 a[156:157], v255 offset:0
global_load_lds_dwordx4 v5, s[40:41] offset:1024
v_mfma_f32_16x16x32_bf16 v[130:133], v[190:193], a[128:131], v[130:133]
v_mfma_f32_16x16x32_bf16 v[158:161], v[190:193], v[190:193], v[158:161]
ds_read_b64_tr_b16 a[158:159], v255 offset:2304
global_load_lds_dwordx4 v6, s[40:41] offset:2048
v_mfma_f32_16x16x32_bf16 v[86:89], a[132:135], v[106:109], v[86:89]
ds_read_b64_tr_b16 a[160:161], v255 offset:192
v_mfma_f32_16x16x32_bf16 v[90:93], a[136:139], v[106:109], v[90:93]
global_load_lds_dwordx4 v7, s[40:41] offset:3072
ds_read_b64_tr_b16 a[162:163], v255 offset:2496
v_mfma_f32_16x16x32_bf16 v[94:97], v[106:109], a[140:143], v[94:97]
s_add_u32 m0, s48, 86016
ds_read_b64_tr_b16 a[164:165], v255 offset:384
v_mfma_f32_16x16x32_bf16 v[110:113], a[132:135], v[186:189], v[110:113]
ds_read_b64_tr_b16 a[166:167], v255 offset:2688
v_mfma_f32_16x16x32_bf16 v[114:117], a[136:139], v[186:189], v[114:117]
global_load_lds_dwordx4 v8, s[40:41] offset:-4096
ds_read_b128 v[170:173], v52 offset:12288
v_mfma_f32_16x16x32_bf16 v[118:121], v[186:189], a[140:143], v[118:121]
s_sub_u32 s40, s40, 1769472
ds_read_b128 v[178:181], v52 offset:30720
v_mfma_f32_16x16x32_bf16 v[134:137], v[186:189], v[186:189], v[134:137]
v_mfma_f32_16x16x32_bf16 v[122:125], a[132:135], v[194:197], v[122:125]
v_mfma_f32_16x16x32_bf16 v[126:129], a[136:139], v[194:197], v[126:129]
s_subb_u32 s41, s41, 0
v_mfma_f32_16x16x32_bf16 v[130:133], v[194:197], a[140:143], v[130:133]
v_mfma_f32_16x16x32_bf16 v[158:161], v[194:197], v[194:197], v[158:161]
s_waitcnt vmcnt(9) lgkmcnt(0)
s_barrier
s_add_u32 m0, s49, 0
ds_read_b64_tr_b16 a[168:169], v255 offset:18432
v_mfma_f32_16x16x32_bf16 v[86:89], a[144:147], v[106:109], v[86:89]
v_mfma_f32_16x16x32_bf16 v[90:93], a[148:151], v[106:109], v[90:93]
ds_read_b64_tr_b16 a[170:171], v255 offset:20736
v_mfma_f32_16x16x32_bf16 v[94:97], v[106:109], a[152:155], v[94:97]
ds_read_b64_tr_b16 a[172:173], v255 offset:18624
v_mfma_f32_16x16x32_bf16 v[110:113], a[144:147], v[166:169], v[110:113]
global_load_lds_dwordx4 v11, s[42:43] offset:-2048 sc1
ds_read_b64_tr_b16 a[174:175], v255 offset:20928
v_mfma_f32_16x16x32_bf16 v[114:117], a[148:151], v[166:169], v[114:117]
ds_read_b64_tr_b16 a[176:177], v255 offset:18816
v_mfma_f32_16x16x32_bf16 v[118:121], v[166:169], a[152:155], v[118:121]
ds_read_b64_tr_b16 a[178:179], v255 offset:21120
v_mfma_f32_16x16x32_bf16 v[134:137], v[166:169], v[166:169], v[134:137]
global_load_lds_dwordx4 v12, s[42:43] offset:-1024 sc1
ds_read_b128 v[182:185], v51 offset:14336
v_mfma_f32_16x16x32_bf16 v[122:125], a[144:147], v[174:177], v[122:125]
ds_read_b128 v[190:193], v51 offset:32768
v_mfma_f32_16x16x32_bf16 v[126:129], a[148:151], v[174:177], v[126:129]
ds_read_b64_tr_b16 a[180:181], v255 offset:36864
v_mfma_f32_16x16x32_bf16 v[130:133], v[174:177], a[152:155], v[130:133]
global_load_lds_dwordx4 v13, s[42:43] offset:0 sc1
v_mfma_f32_16x16x32_bf16 v[158:161], v[174:177], v[174:177], v[158:161]
ds_read_b64_tr_b16 a[182:183], v255 offset:39168
v_mfma_f32_16x16x32_bf16 v[86:89], a[156:159], v[106:109], v[86:89]
ds_read_b64_tr_b16 a[184:185], v255 offset:37056
v_mfma_f32_16x16x32_bf16 v[90:93], a[160:163], v[106:109], v[90:93]
ds_read_b64_tr_b16 a[186:187], v255 offset:39360
v_mfma_f32_16x16x32_bf16 v[94:97], v[106:109], a[164:167], v[94:97]
global_load_lds_dwordx4 v14, s[42:43] offset:1024 sc1
ds_read_b64_tr_b16 a[188:189], v255 offset:37248
v_mfma_f32_16x16x32_bf16 v[110:113], a[156:159], v[170:173], v[110:113]
ds_read_b64_tr_b16 a[190:191], v255 offset:39552
v_mfma_f32_16x16x32_bf16 v[114:117], a[160:163], v[170:173], v[114:117]
ds_read_b128 v[186:189], v52 offset:14336
v_mfma_f32_16x16x32_bf16 v[118:121], v[170:173], a[164:167], v[118:121]
s_sub_u32 s42, s42, 18432
s_subb_u32 s43, s43, 0
)"
R"(ds_read_b128 v[194:197], v52 offset:32768
v_mfma_f32_16x16x32_bf16 v[134:137], v[170:173], v[170:173], v[134:137]
v_mfma_f32_16x16x32_bf16 v[122:125], a[156:159], v[178:181], v[122:125]
v_mfma_f32_16x16x32_bf16 v[126:129], a[160:163], v[178:181], v[126:129]
v_mfma_f32_16x16x32_bf16 v[130:133], v[178:181], a[164:167], v[130:133]
v_mfma_f32_16x16x32_bf16 v[158:161], v[178:181], v[178:181], v[158:161]
s_waitcnt vmcnt(4) lgkmcnt(0)
s_barrier
s_add_u32 m0, s49, 18432
ds_read_b64_tr_b16 a[192:193], v255 offset:55296
v_mfma_f32_16x16x32_bf16 v[86:89], a[168:171], v[106:109], v[86:89]
v_mfma_f32_16x16x32_bf16 v[90:93], a[172:175], v[106:109], v[90:93]
ds_read_b64_tr_b16 a[194:195], v255 offset:57600
v_mfma_f32_16x16x32_bf16 v[94:97], v[106:109], a[176:179], v[94:97]
ds_read_b64_tr_b16 a[196:197], v255 offset:55488
v_mfma_f32_16x16x32_bf16 v[110:113], a[168:171], v[182:185], v[110:113]
global_load_lds_dwordx4 v11, s[42:43] offset:-2048 sc1
ds_read_b64_tr_b16 a[198:199], v255 offset:57792
v_mfma_f32_16x16x32_bf16 v[114:117], a[172:175], v[182:185], v[114:117]
ds_read_b64_tr_b16 a[200:201], v255 offset:55680
v_mfma_f32_16x16x32_bf16 v[118:121], v[182:185], a[176:179], v[118:121]
ds_read_b64_tr_b16 a[202:203], v255 offset:57984
v_mfma_f32_16x16x32_bf16 v[134:137], v[182:185], v[182:185], v[134:137]
global_load_lds_dwordx4 v12, s[42:43] offset:-1024 sc1
ds_read_b128 v[166:169], v51 offset:16384
v_mfma_f32_16x16x32_bf16 v[122:125], a[168:171], v[190:193], v[122:125]
ds_read_b128 v[174:177], v51 offset:34816
v_mfma_f32_16x16x32_bf16 v[126:129], a[172:175], v[190:193], v[126:129]
ds_read_b64_tr_b16 a[204:205], v162 offset:8192
v_mfma_f32_16x16x32_bf16 v[130:133], v[190:193], a[176:179], v[130:133]
global_load_lds_dwordx4 v13, s[42:43] offset:0 sc1
ds_read_b64_tr_b16 a[206:207], v162 offset:10496
v_mfma_f32_16x16x32_bf16 v[158:161], v[190:193], v[190:193], v[158:161]
ds_read_b64_tr_b16 a[208:209], v162 offset:8384
v_mfma_f32_16x16x32_bf16 v[86:89], a[180:183], v[106:109], v[86:89]
ds_read_b64_tr_b16 a[210:211], v162 offset:10688
v_mfma_f32_16x16x32_bf16 v[90:93], a[184:187], v[106:109], v[90:93]
global_load_lds_dwordx4 v14, s[42:43] offset:1024 sc1
ds_read_b64_tr_b16 a[212:213], v162 offset:8576
v_mfma_f32_16x16x32_bf16 v[94:97], v[106:109], a[188:191], v[94:97]
ds_read_b64_tr_b16 a[214:215], v162 offset:10880
v_mfma_f32_16x16x32_bf16 v[110:113], a[180:183], v[186:189], v[110:113]
ds_read_b128 v[170:173], v52 offset:16384
v_mfma_f32_16x16x32_bf16 v[114:117], a[184:187], v[186:189], v[114:117]
s_sub_u32 s42, s42, 18432
s_subb_u32 s43, s43, 0
ds_read_b128 v[178:181], v52 offset:34816
v_mfma_f32_16x16x32_bf16 v[118:121], v[186:189], a[188:191], v[118:121]
v_mfma_f32_16x16x32_bf16 v[134:137], v[186:189], v[186:189], v[134:137]
v_mfma_f32_16x16x32_bf16 v[122:125], a[180:183], v[194:197], v[122:125]
v_mfma_f32_16x16x32_bf16 v[126:129], a[184:187], v[194:197], v[126:129]
v_mfma_f32_16x16x32_bf16 v[158:161], v[194:197], v[194:197], v[158:161]
s_waitcnt lgkmcnt(14)
v_mfma_f32_16x16x32_bf16 v[86:89], a[192:195], v[106:109], v[86:89]
s_waitcnt lgkmcnt(12)
v_mfma_f32_16x16x32_bf16 v[90:93], a[196:199], v[106:109], v[90:93]
s_waitcnt lgkmcnt(10)
v_mfma_f32_16x16x32_bf16 v[94:97], v[106:109], a[200:203], v[94:97]
s_waitcnt lgkmcnt(9)
v_mfma_f32_16x16x32_bf16 v[110:113], a[192:195], v[166:169], v[110:113]
v_mfma_f32_16x16x32_bf16 v[114:117], a[196:199], v[166:169], v[114:117]
v_mfma_f32_16x16x32_bf16 v[118:121], v[166:169], a[200:203], v[118:121]
v_mfma_f32_16x16x32_bf16 v[134:137], v[166:169], v[166:169], v[134:137]
s_waitcnt lgkmcnt(8)
v_mfma_f32_16x16x32_bf16 v[158:161], v[174:177], v[174:177], v[158:161]
s_waitcnt lgkmcnt(6)
v_mfma_f32_16x16x32_bf16 v[86:89], a[204:207], v[106:109], v[86:89]
s_waitcnt lgkmcnt(4)
v_mfma_f32_16x16x32_bf16 v[90:93], a[208:211], v[106:109], v[90:93]
s_waitcnt lgkmcnt(2)
v_mfma_f32_16x16x32_bf16 v[94:97], v[106:109], a[212:215], v[94:97]
s_waitcnt lgkmcnt(1)
v_mfma_f32_16x16x32_bf16 v[110:113], a[204:207], v[170:173], v[110:113]
v_mfma_f32_16x16x32_bf16 v[114:117], a[208:211], v[170:173], v[114:117]
v_mfma_f32_16x16x32_bf16 v[118:121], v[170:173], a[212:215], v[118:121]
v_mfma_f32_16x16x32_bf16 v[134:137], v[170:173], v[170:173], v[134:137]
s_waitcnt lgkmcnt(0)
v_mfma_f32_16x16x32_bf16 v[158:161], v[178:181], v[178:181], v[158:161]
s_nop 5
ds_write_b128 v23, v[134:137] offset:41984
s_waitcnt lgkmcnt(0)
s_barrier
ds_read_b128 v[170:173], v52 offset:32768
ds_read_b128 v[174:177], v51 offset:34816
ds_read_b128 v[178:181], v52 offset:34816
v_mov_b32 v202, v32
v_mov_b32 v203, v33
v_mov_b32 v204, v34
v_mov_b32 v205, v35
v_mov_b32 v134, v158
v_mov_b32 v135, v159
v_mov_b32 v136, v160
v_mov_b32 v137, v161
s_barrier
ds_read_b32 v26, v24 offset:0
ds_read_b32 v148, v21
ds_read_b32 v95, v24 offset:192
ds_read_b32 v255, v27
ds_read_b32 v8, v28
ds_read_b128 v[194:197], v25 offset:0
ds_read_b64 v[158:159], v22 offset:41472
ds_read_b32 v140, v20
v_xor_b32 v51, 0x10000, v51
v_xor_b32 v52, 0x10000, v52
ds_write_b128 v23, v[134:137] offset:41984
s_waitcnt lgkmcnt(8)
v_fma_f32 v206, v90, v26, v114
v_fma_f32 v207, v91, v26, v115
v_fma_f32 v208, v92, v26, v116
v_fma_f32 v209, v93, v26, v117
v_cvt_pk_bf16_f32 v146, v206, v207
v_cvt_pk_bf16_f32 v147, v208, v209
s_waitcnt lgkmcnt(0)
s_barrier
v_fma_f32 v206, v94, v194, v118
v_fma_f32 v207, v94, v195, v119
v_fma_f32 v208, v94, v196, v120
v_fma_f32 v209, v94, v197, v121
v_cvt_pk_bf16_f32 v154, v206, v207
v_cvt_pk_bf16_f32 v155, v208, v209
v_fma_f32 v206, v86, v26, v110
v_fma_f32 v207, v87, v26, v111
v_fma_f32 v208, v88, v26, v112
v_fma_f32 v209, v89, v26, v113
v_cvt_pk_bf16_f32 v138, v206, v207
v_cvt_pk_bf16_f32 v139, v208, v209
s_waitcnt vmcnt(4) lgkmcnt(0)
s_barrier
s_mov_b32 s70, 32
s_waitcnt lgkmcnt(0)
s_nop 1
s_branch .Lqa_in47
.p2align 6
.Lqa_top70:
s_waitcnt lgkmcnt(9)
v_mfma_f32_16x16x32_bf16 v[134:137], v[0:3], v[0:3], 0
s_waitcnt lgkmcnt(8)
ds_read_b128 v[54:57], v52 offset:4096
ds_read_b32 v8, v30
v_mfma_f32_16x16x32_bf16 v[134:137], v[4:7], v[4:7], v[134:137]
ds_read_b128 v[194:197], v25 offset:64
ds_read_b64 v[160:161], v22 offset:41472
v_mfma_f32_16x16x32_bf16 v[110:113], a[0:3], v[0:3], 0
s_waitcnt lgkmcnt(8)
ds_read_b128 v[58:61], v51 offset:6144
ds_read_b32 v144, v20
v_mfma_f32_16x16x32_bf16 v[134:137], v[96:99], v[96:99], v[134:137]
s_waitcnt lgkmcnt(8)
ds_read_b128 v[62:65], v52 offset:6144
v_add_u32 v46, 1, v46
v_mfma_f32_16x16x32_bf16 v[134:137], v[100:103], v[100:103], v[134:137]
s_mov_b64 exec, 65535
global_store_dwordx4 v16, v[198:201], s[38:39] sc1
s_mov_b64 exec, -1
v_mfma_f32_16x16x32_bf16 v[114:117], a[4:7], v[0:3], 0
s_waitcnt lgkmcnt(7)
ds_read_b128 v[66:69], v51 offset:8192
v_add_u32 v16, 18432, v16
v_mfma_f32_16x16x32_bf16 v[134:137], v[104:107], v[104:107], v[134:137]
s_waitcnt lgkmcnt(7)
ds_read_b128 v[70:73], v52 offset:8192
v_fma_f32 v206, v90, v26, v126
v_mfma_f32_16x16x32_bf16 v[134:137], v[54:57], v[54:57], v[134:137]
v_fma_f32 v207, v91, v26, v127
v_fma_f32 v208, v92, v26, v128
v_mfma_f32_16x16x32_bf16 v[118:121], v[0:3], a[8:11], 0
s_waitcnt lgkmcnt(4)
ds_read_b128 v[74:77], v51 offset:10240
v_fma_f32 v209, v93, v26, v129
v_mfma_f32_16x16x32_bf16 v[134:137], v[58:61], v[58:61], v[134:137]
s_waitcnt lgkmcnt(3)
ds_read_b128 v[78:81], v52 offset:10240
v_cvt_pk_bf16_f32 v150, v206, v207
v_mfma_f32_16x16x32_bf16 v[134:137], v[62:65], v[62:65], v[134:137]
v_cvt_pk_bf16_f32 v151, v208, v209
v_max_f32 v47, v186, v187
v_mfma_f32_16x16x32_bf16 v[110:113], a[12:15], v[4:7], v[110:113]
ds_read_b128 v[82:85], v51 offset:12288
v_mfma_f32_16x16x32_bf16 v[182:185], v[150:153], v[138:141], v[32:35]
s_waitcnt lgkmcnt(4)
v_max_f32 v48, v188, v189
v_fma_f32 v206, v94, v194, v130
v_mfma_f32_16x16x32_bf16 v[134:137], v[66:69], v[66:69], v[134:137]
s_xor_b32 s49, s49, 0x10000
ds_read_b128 v[162:165], v52 offset:12288
s_waitcnt lgkmcnt(4)
v_mfma_f32_16x16x32_bf16 v[134:137], v[70:73], v[70:73], v[134:137]
s_add_u32 m0, s49, 0
v_mul_f32 v95, 0x3eae1a93, v95
v_mul_f32 v9, v255, v95
v_mfma_f32_16x16x32_bf16 v[114:117], a[16:19], v[4:7], v[114:117]
s_waitcnt lgkmcnt(3)
ds_read_b128 v[166:169], v51 offset:14336
v_mul_f32 v10, v8, v95
v_mfma_f32_16x16x32_bf16 v[134:137], v[74:77], v[74:77], v[134:137]
s_waitcnt lgkmcnt(3)
ds_read_b128 v[170:173], v52 offset:14336
v_max3_f32 v47, v182, v183, v47
v_mfma_f32_16x16x32_bf16 v[134:137], v[78:81], v[78:81], v[134:137]
v_max3_f32 v48, v184, v185, v48
v_mul_f32 v47, v47, v9
v_mfma_f32_16x16x32_bf16 v[118:121], v[4:7], a[20:23], v[118:121]
s_waitcnt lgkmcnt(3)
ds_read_b128 v[174:177], v51 offset:16384
v_mul_f32 v48, v48, v10
v_mfma_f32_16x16x32_bf16 v[134:137], v[82:85], v[82:85], v[134:137]
s_waitcnt lgkmcnt(3)
ds_read_b128 v[178:181], v52 offset:16384
global_load_lds_dwordx4 v11, s[42:43] offset:-2048 sc1
v_mfma_f32_16x16x32_bf16 v[134:137], v[162:165], v[162:165], v[134:137]
v_max_f32 v49, v47, v48
v_mov_b32 v50, v49
v_mfma_f32_16x16x32_bf16 v[110:113], a[24:27], v[96:99], v[110:113]
s_waitcnt lgkmcnt(3)
v_permlane16_swap_b32 v49, v50
v_mfma_f32_16x16x32_bf16 v[134:137], v[166:169], v[166:169], v[134:137]
s_waitcnt lgkmcnt(2)
v_max_f32 v49, v49, v50
v_mov_b32 v50, v49
v_mfma_f32_16x16x32_bf16 v[134:137], v[170:173], v[170:173], v[134:137]
s_nop 0
v_permlane32_swap_b32 v49, v50
v_mfma_f32_16x16x32_bf16 v[114:117], a[28:31], v[96:99], v[114:117]
s_waitcnt lgkmcnt(1)
v_max_f32 v49, v49, v50
v_sub_f32 v47, v47, v49
v_mfma_f32_16x16x32_bf16 v[134:137], v[174:177], v[174:177], v[134:137]
s_waitcnt lgkmcnt(0)
v_sub_f32 v48, v48, v49
v_fma_f32 v207, v94, v195, v131
v_mfma_f32_16x16x32_bf16 v[134:137], v[178:181], v[178:181], v[134:137]
v_exp_f32 v210, v47
v_mfma_f32_16x16x32_bf16 v[118:121], v[96:99], a[32:35], v[118:121]
global_load_lds_dwordx4 v12, s[42:43] offset:-1024 sc1
v_fma_f32 v208, v94, v196, v132
v_mfma_f32_16x16x32_bf16 v[110:113], a[36:39], v[100:103], v[110:113]
v_exp_f32 v211, v48
v_mfma_f32_16x16x32_bf16 v[114:117], a[40:43], v[100:103], v[114:117]
v_add_f32 v49, v210, v211
v_mov_b32 v50, v49
v_mfma_f32_16x16x32_bf16 v[118:121], v[100:103], a[44:47], v[118:121]
ds_write_b128 v23, v[134:137] offset:41984
v_mfma_f32_16x16x32_bf16 v[110:113], a[48:51], v[104:107], v[110:113]
v_mul_f32 v210, v210, v255
v_fma_f32 v209, v94, v197, v133
v_mfma_f32_16x16x32_bf16 v[114:117], a[52:55], v[104:107], v[114:117]
v_permlane16_swap_b32 v49, v50
v_mfma_f32_16x16x32_bf16 v[118:121], v[104:107], a[56:59], v[118:121]
v_add_f32 v49, v49, v50
v_mov_b32 v50, v49
v_mfma_f32_16x16x32_bf16 v[110:113], a[60:63], v[54:57], v[110:113]
v_mul_f32 v211, v211, v8
v_cvt_pk_bf16_f32 v156, v206, v207
v_mfma_f32_16x16x32_bf16 v[114:117], a[64:67], v[54:57], v[114:117]
global_load_lds_dwordx4 v13, s[42:43] offset:0 sc1
v_cvt_pk_bf16_f32 v157, v208, v209
v_mfma_f32_16x16x32_bf16 v[118:121], v[54:57], a[68:71], v[118:121]
v_mfma_f32_16x16x32_bf16 v[110:113], a[72:75], v[58:61], v[110:113]
s_waitcnt lgkmcnt(0)
s_barrier
v_mfma_f32_16x16x32_bf16 v[114:117], a[76:79], v[58:61], v[114:117]
v_permlane32_swap_b32 v49, v50
v_mfma_f32_16x16x32_bf16 v[118:121], v[58:61], a[80:83], v[118:121]
v_add_f32 v49, v49, v50
v_fma_f32 v206, v86, v26, v122
v_mfma_f32_16x16x32_bf16 v[110:113], a[84:87], v[62:65], v[110:113]
v_rcp_f32 v49, v49
v_mfma_f32_16x16x32_bf16 v[114:117], a[88:91], v[62:65], v[114:117]
v_mul_f32 v210, v210, v49
v_mul_f32 v211, v211, v49
v_mfma_f32_16x16x32_bf16 v[118:121], v[62:65], a[92:95], v[118:121]
v_cvt_pk_bf16_f32 v210, v210, v210
v_cvt_pk_bf16_f32 v211, v211, v211
v_mfma_f32_16x16x32_bf16 v[110:113], a[96:99], v[66:69], v[110:113]
global_load_lds_dwordx4 v14, s[42:43] offset:1024 sc1
s_add_u32 s42, s42, s46
s_addc_u32 s43, s43, s47
v_mfma_f32_16x16x32_bf16 v[114:117], a[100:103], v[66:69], v[114:117]
v_and_b32 v192, v210, v40
v_and_b32 v193, v211, v41
v_mfma_f32_16x16x32_bf16 v[118:121], v[66:69], a[104:107], v[118:121]
v_and_b32 v190, v210, v42
v_and_b32 v191, v211, v43
v_mfma_f32_16x16x32_bf16 v[110:113], a[108:111], v[70:73], v[110:113]
s_nop 0
v_mfma_f32_16x16x32_bf16 v[186:189], v[154:157], v[190:193], 0
v_mfma_f32_16x16x32_bf16 v[182:185], v[158:161], v[190:193], 0
v_fma_f32 v207, v87, v26, v123
v_fma_f32 v208, v88, v26, v124
v_mfma_f32_16x16x32_bf16 v[114:117], a[112:115], v[70:73], v[114:117]
v_fma_f32 v209, v89, v26, v125
v_cvt_pk_bf16_f32 v142, v206, v207
v_mfma_f32_16x16x32_bf16 v[118:121], v[70:73], a[116:119], v[118:121]
v_cvt_pk_bf16_f32 v143, v208, v209
v_mfma_f32_16x16x32_bf16 v[110:113], a[120:123], v[74:77], v[110:113]
v_mfma_f32_16x16x32_bf16 v[114:117], a[124:127], v[74:77], v[114:117]
v_cvt_pk_bf16_f32 v108, v186, v187
v_cvt_pk_bf16_f32 v109, v188, v189
v_mfma_f32_16x16x32_bf16 v[118:121], v[74:77], a[128:131], v[118:121]
ds_write_b64 v18, v[108:109]
v_mfma_f32_16x16x32_bf16 v[110:113], a[132:135], v[78:81], v[110:113]
v_cvt_pk_bf16_f32 v210, v182, v183
v_mfma_f32_16x16x32_bf16 v[114:117], a[136:139], v[78:81], v[114:117]
ds_write_b32 v19, v210
v_mfma_f32_16x16x32_bf16 v[118:121], v[78:81], a[140:143], v[118:121]
v_mfma_f32_16x16x32_bf16 v[110:113], a[144:147], v[82:85], v[110:113]
v_mfma_f32_16x16x32_bf16 v[114:117], a[148:151], v[82:85], v[114:117]
v_mfma_f32_16x16x32_bf16 v[118:121], v[82:85], a[152:155], v[118:121]
v_mfma_f32_16x16x32_bf16 v[110:113], a[156:159], v[162:165], v[110:113]
v_add_lshl_u32 v253, v46, 1, 7
v_bfi_b32 v253, s69, v253, v44
v_mfma_f32_16x16x32_bf16 v[114:117], a[160:163], v[162:165], v[114:117]
global_load_dword v254, v253, s[38:39] sc1
v_mfma_f32_16x16x32_bf16 v[118:121], v[162:165], a[164:167], v[118:121]
s_waitcnt vmcnt(6)
v_sub_u32 v53, v53, v46
v_cmp_lt_u32 vcc, 0x7f, v53
v_mfma_f32_16x16x32_bf16 v[110:113], a[168:171], v[166:169], v[110:113]
v_mfma_f32_16x16x32_bf16 v[114:117], a[172:175], v[166:169], v[114:117]
v_mfma_f32_16x16x32_bf16 v[118:121], v[166:169], a[176:179], v[118:121]
s_and_b64 vcc, vcc, s[76:77]
s_cbranch_vccnz .Lqa_sp50
.Lqa_rt51:
v_mfma_f32_16x16x32_bf16 v[110:113], a[180:183], v[170:173], v[110:113]
v_mfma_f32_16x16x32_bf16 v[114:117], a[184:187], v[170:173], v[114:117]
s_waitcnt vmcnt(5) lgkmcnt(0)
s_barrier
v_mfma_f32_16x16x32_bf16 v[118:121], v[170:173], a[188:191], v[118:121]
ds_read_b128 v[0:3], v51 offset:18432
v_mfma_f32_16x16x32_bf16 v[110:113], a[192:195], v[174:177], v[110:113]
ds_read_b128 v[4:7], v52 offset:18432
ds_read_b128 v[198:201], v17 offset:0
v_mfma_f32_16x16x32_bf16 v[114:117], a[196:199], v[174:177], v[114:117]
ds_read_b32 v26, v24 offset:0
ds_read_b32 v148, v21
v_mfma_f32_16x16x32_bf16 v[186:189], v[150:153], v[142:145], v[36:39]
v_mfma_f32_16x16x32_bf16 v[118:121], v[174:177], a[200:203], v[118:121]
ds_read_b128 v[96:99], v51 offset:20480
ds_read_b32 v95, v24 offset:192
v_mfma_f32_16x16x32_bf16 v[110:113], a[204:207], v[178:181], v[110:113]
ds_read_b128 v[100:103], v52 offset:20480
ds_read_b32 v255, v27
v_mfma_f32_16x16x32_bf16 v[114:117], a[208:211], v[178:181], v[114:117]
ds_read_b32 v8, v28
ds_read_b128 v[194:197], v25 offset:0
v_mfma_f32_16x16x32_bf16 v[118:121], v[178:181], a[212:215], v[118:121]
s_waitcnt lgkmcnt(10)
ds_read_b128 v[104:107], v51 offset:22528
ds_read_b64 v[158:159], v22 offset:41472
v_mfma_f32_16x16x32_bf16 v[134:137], v[0:3], v[0:3], 0
s_waitcnt lgkmcnt(11)
ds_read_b128 v[54:57], v52 offset:22528
ds_read_b32 v140, v20
v_mfma_f32_16x16x32_bf16 v[134:137], v[4:7], v[4:7], v[134:137]
v_add_u32 v46, 1, v46
v_max_f32 v47, v186, v187
v_mfma_f32_16x16x32_bf16 v[122:125], a[0:3], v[0:3], 0
s_waitcnt lgkmcnt(11)
ds_read_b128 v[58:61], v51 offset:24576
v_fma_f32 v206, v90, v26, v114
s_waitcnt lgkmcnt(10)
v_mfma_f32_16x16x32_bf16 v[134:137], v[96:99], v[96:99], v[134:137]
s_waitcnt lgkmcnt(8)
ds_read_b128 v[62:65], v52 offset:24576
v_fma_f32 v207, v91, v26, v115
v_mfma_f32_16x16x32_bf16 v[134:137], v[100:103], v[100:103], v[134:137]
s_mov_b64 exec, 65535
global_store_dwordx4 v16, v[198:201], s[38:39] sc1
s_mov_b64 exec, -1
v_mfma_f32_16x16x32_bf16 v[126:129], a[4:7], v[0:3], 0
s_waitcnt lgkmcnt(5)
ds_read_b128 v[66:69], v51 offset:26624
v_add_u32 v16, 18432, v16
v_mfma_f32_16x16x32_bf16 v[134:137], v[104:107], v[104:107], v[134:137]
s_waitcnt lgkmcnt(4)
ds_read_b128 v[70:73], v52 offset:26624
v_fma_f32 v208, v92, v26, v116
v_mfma_f32_16x16x32_bf16 v[134:137], v[54:57], v[54:57], v[134:137]
v_fma_f32 v209, v93, v26, v117
v_cvt_pk_bf16_f32 v146, v206, v207
v_mfma_f32_16x16x32_bf16 v[130:133], v[0:3], a[8:11], 0
s_waitcnt lgkmcnt(3)
ds_read_b128 v[74:77], v51 offset:28672
v_cvt_pk_bf16_f32 v147, v208, v209
v_mfma_f32_16x16x32_bf16 v[134:137], v[58:61], v[58:61], v[134:137]
ds_read_b128 v[78:81], v52 offset:28672
v_mfma_f32_16x16x32_bf16 v[182:185], v[146:149], v[142:145], v[202:205]
s_waitcnt lgkmcnt(4)
v_max_f32 v48, v188, v189
v_fma_f32 v206, v94, v194, v118
v_mfma_f32_16x16x32_bf16 v[134:137], v[62:65], v[62:65], v[134:137]
v_fma_f32 v207, v94, v195, v119
v_fma_f32 v208, v94, v196, v120
v_mfma_f32_16x16x32_bf16 v[122:125], a[12:15], v[4:7], v[122:125]
s_waitcnt lgkmcnt(3)
ds_read_b128 v[82:85], v51 offset:30720
v_mul_f32 v95, 0x3eae1a93, v95
v_mfma_f32_16x16x32_bf16 v[134:137], v[66:69], v[66:69], v[134:137]
s_add_u32 m0, s49, 18432
ds_read_b128 v[162:165], v52 offset:30720
)"
R"(s_waitcnt lgkmcnt(4)
v_mfma_f32_16x16x32_bf16 v[134:137], v[70:73], v[70:73], v[134:137]
v_mul_f32 v9, v255, v95
v_mul_f32 v10, v8, v95
v_mfma_f32_16x16x32_bf16 v[126:129], a[16:19], v[4:7], v[126:129]
s_waitcnt lgkmcnt(3)
ds_read_b128 v[166:169], v51 offset:32768
v_max3_f32 v47, v182, v183, v47
v_mfma_f32_16x16x32_bf16 v[134:137], v[74:77], v[74:77], v[134:137]
s_waitcnt lgkmcnt(3)
ds_read_b128 v[170:173], v52 offset:32768
v_max3_f32 v48, v184, v185, v48
v_mfma_f32_16x16x32_bf16 v[134:137], v[78:81], v[78:81], v[134:137]
v_mul_f32 v47, v47, v9
v_mul_f32 v48, v48, v10
v_mfma_f32_16x16x32_bf16 v[130:133], v[4:7], a[20:23], v[130:133]
s_waitcnt lgkmcnt(3)
ds_read_b128 v[174:177], v51 offset:34816
global_load_lds_dwordx4 v11, s[42:43] offset:-2048 sc1
v_mfma_f32_16x16x32_bf16 v[134:137], v[82:85], v[82:85], v[134:137]
s_waitcnt lgkmcnt(3)
ds_read_b128 v[178:181], v52 offset:34816
v_max_f32 v49, v47, v48
v_mfma_f32_16x16x32_bf16 v[134:137], v[162:165], v[162:165], v[134:137]
v_mov_b32 v50, v49
v_fma_f32 v209, v94, v197, v121
v_mfma_f32_16x16x32_bf16 v[122:125], a[24:27], v[96:99], v[122:125]
s_waitcnt lgkmcnt(3)
v_permlane16_swap_b32 v49, v50
v_mfma_f32_16x16x32_bf16 v[134:137], v[166:169], v[166:169], v[134:137]
s_waitcnt lgkmcnt(2)
v_max_f32 v49, v49, v50
v_mov_b32 v50, v49
v_mfma_f32_16x16x32_bf16 v[134:137], v[170:173], v[170:173], v[134:137]
s_nop 0
v_permlane32_swap_b32 v49, v50
v_mfma_f32_16x16x32_bf16 v[126:129], a[28:31], v[96:99], v[126:129]
s_waitcnt lgkmcnt(1)
v_max_f32 v49, v49, v50
v_sub_f32 v47, v47, v49
v_mfma_f32_16x16x32_bf16 v[134:137], v[174:177], v[174:177], v[134:137]
s_waitcnt lgkmcnt(0)
v_sub_f32 v48, v48, v49
v_cvt_pk_bf16_f32 v154, v206, v207
v_mfma_f32_16x16x32_bf16 v[134:137], v[178:181], v[178:181], v[134:137]
global_load_lds_dwordx4 v12, s[42:43] offset:-1024 sc1
v_cvt_pk_bf16_f32 v155, v208, v209
v_mfma_f32_16x16x32_bf16 v[130:133], v[96:99], a[32:35], v[130:133]
v_exp_f32 v210, v47
v_mfma_f32_16x16x32_bf16 v[122:125], a[36:39], v[100:103], v[122:125]
v_exp_f32 v211, v48
v_mfma_f32_16x16x32_bf16 v[126:129], a[40:43], v[100:103], v[126:129]
v_add_f32 v49, v210, v211
v_mov_b32 v50, v49
v_mfma_f32_16x16x32_bf16 v[130:133], v[100:103], a[44:47], v[130:133]
ds_write_b128 v23, v[134:137] offset:41984
v_mfma_f32_16x16x32_bf16 v[122:125], a[48:51], v[104:107], v[122:125]
v_mul_f32 v210, v210, v255
v_fma_f32 v206, v86, v26, v110
v_mfma_f32_16x16x32_bf16 v[126:129], a[52:55], v[104:107], v[126:129]
v_permlane16_swap_b32 v49, v50
v_mfma_f32_16x16x32_bf16 v[130:133], v[104:107], a[56:59], v[130:133]
v_add_f32 v49, v49, v50
v_mov_b32 v50, v49
v_mfma_f32_16x16x32_bf16 v[122:125], a[60:63], v[54:57], v[122:125]
global_load_lds_dwordx4 v13, s[42:43] offset:0 sc1
v_mul_f32 v211, v211, v8
v_mfma_f32_16x16x32_bf16 v[126:129], a[64:67], v[54:57], v[126:129]
v_permlane32_swap_b32 v49, v50
v_mfma_f32_16x16x32_bf16 v[130:133], v[54:57], a[68:71], v[130:133]
v_mfma_f32_16x16x32_bf16 v[122:125], a[72:75], v[58:61], v[122:125]
s_waitcnt lgkmcnt(0)
s_barrier
v_mfma_f32_16x16x32_bf16 v[126:129], a[76:79], v[58:61], v[126:129]
v_add_f32 v49, v49, v50
v_fma_f32 v207, v87, v26, v111
v_mfma_f32_16x16x32_bf16 v[130:133], v[58:61], a[80:83], v[130:133]
v_rcp_f32 v49, v49
v_mfma_f32_16x16x32_bf16 v[122:125], a[84:87], v[62:65], v[122:125]
v_mul_f32 v210, v210, v49
v_mul_f32 v211, v211, v49
v_mfma_f32_16x16x32_bf16 v[126:129], a[88:91], v[62:65], v[126:129]
v_cvt_pk_bf16_f32 v210, v210, v210
v_cvt_pk_bf16_f32 v211, v211, v211
v_mfma_f32_16x16x32_bf16 v[130:133], v[62:65], a[92:95], v[130:133]
global_load_lds_dwordx4 v14, s[42:43] offset:1024 sc1
s_sub_u32 s42, s42, 18432
s_subb_u32 s43, s43, 0
v_mfma_f32_16x16x32_bf16 v[122:125], a[96:99], v[66:69], v[122:125]
v_and_b32 v190, v210, v40
v_and_b32 v191, v211, v41
v_mfma_f32_16x16x32_bf16 v[126:129], a[100:103], v[66:69], v[126:129]
v_and_b32 v192, v210, v42
v_and_b32 v193, v211, v43
v_mfma_f32_16x16x32_bf16 v[130:133], v[66:69], a[104:107], v[130:133]
s_nop 0
v_mfma_f32_16x16x32_bf16 v[186:189], v[154:157], v[190:193], 0
v_mfma_f32_16x16x32_bf16 v[182:185], v[158:161], v[190:193], 0
v_fma_f32 v208, v88, v26, v112
v_fma_f32 v209, v89, v26, v113
v_mfma_f32_16x16x32_bf16 v[122:125], a[108:111], v[70:73], v[122:125]
v_cvt_pk_bf16_f32 v138, v206, v207
v_cvt_pk_bf16_f32 v139, v208, v209
v_mfma_f32_16x16x32_bf16 v[126:129], a[112:115], v[70:73], v[126:129]
v_mfma_f32_16x16x32_bf16 v[130:133], v[70:73], a[116:119], v[130:133]
v_mfma_f32_16x16x32_bf16 v[122:125], a[120:123], v[74:77], v[122:125]
v_cvt_pk_bf16_f32 v108, v186, v187
v_cvt_pk_bf16_f32 v109, v188, v189
v_mfma_f32_16x16x32_bf16 v[126:129], a[124:127], v[74:77], v[126:129]
ds_write_b64 v18, v[108:109]
v_mfma_f32_16x16x32_bf16 v[130:133], v[74:77], a[128:131], v[130:133]
v_cvt_pk_bf16_f32 v210, v182, v183
v_mfma_f32_16x16x32_bf16 v[122:125], a[132:135], v[78:81], v[122:125]
ds_write_b32 v19, v210
v_mfma_f32_16x16x32_bf16 v[126:129], a[136:139], v[78:81], v[126:129]
v_mfma_f32_16x16x32_bf16 v[130:133], v[78:81], a[140:143], v[130:133]
v_mfma_f32_16x16x32_bf16 v[122:125], a[144:147], v[82:85], v[122:125]
v_mfma_f32_16x16x32_bf16 v[126:129], a[148:151], v[82:85], v[126:129]
v_mfma_f32_16x16x32_bf16 v[130:133], v[82:85], a[152:155], v[130:133]
v_mfma_f32_16x16x32_bf16 v[122:125], a[156:159], v[162:165], v[122:125]
v_add_lshl_u32 v253, v46, 1, 7
v_bfi_b32 v253, s69, v253, v45
v_mfma_f32_16x16x32_bf16 v[126:129], a[160:163], v[162:165], v[126:129]
global_load_dword v53, v253, s[38:39] sc1
v_mfma_f32_16x16x32_bf16 v[130:133], v[162:165], a[164:167], v[130:133]
s_waitcnt vmcnt(6)
v_sub_u32 v254, v254, v46
v_cmp_lt_u32 vcc, 0x7f, v254
v_mfma_f32_16x16x32_bf16 v[122:125], a[168:171], v[166:169], v[122:125]
v_mfma_f32_16x16x32_bf16 v[126:129], a[172:175], v[166:169], v[126:129]
v_xor_b32 v51, 0x10000, v51
v_xor_b32 v52, 0x10000, v52
v_mfma_f32_16x16x32_bf16 v[130:133], v[166:169], a[176:179], v[130:133]
s_and_b64 vcc, vcc, s[76:77]
s_cbranch_vccnz .Lqa_sp52
.Lqa_rt53:
v_mfma_f32_16x16x32_bf16 v[122:125], a[180:183], v[170:173], v[122:125]
v_mfma_f32_16x16x32_bf16 v[126:129], a[184:187], v[170:173], v[126:129]
s_waitcnt vmcnt(5) lgkmcnt(0)
s_barrier
.Lqa_in47:
v_mfma_f32_16x16x32_bf16 v[130:133], v[170:173], a[188:191], v[130:133]
s_cmp_eq_u32 s70, s73
s_cbranch_scc1 .Lqa_hk48
.Lqa_hr49:
ds_read_b128 v[0:3], v51 offset:0
v_mfma_f32_16x16x32_bf16 v[122:125], a[192:195], v[174:177], v[122:125]
ds_read_b128 v[4:7], v52 offset:0
ds_read_b128 v[198:201], v17 offset:0
v_mfma_f32_16x16x32_bf16 v[126:129], a[196:199], v[174:177], v[126:129]
ds_read_b32 v26, v24 offset:64
ds_read_b32 v152, v21
v_mfma_f32_16x16x32_bf16 v[186:189], v[146:149], v[138:141], v[36:39]
v_mfma_f32_16x16x32_bf16 v[130:133], v[174:177], a[200:203], v[130:133]
ds_read_b128 v[96:99], v51 offset:2048
ds_read_b32 v95, v24 offset:128
v_mfma_f32_16x16x32_bf16 v[122:125], a[204:207], v[178:181], v[122:125]
ds_read_b128 v[100:103], v52 offset:2048
ds_read_b32 v255, v29
v_mfma_f32_16x16x32_bf16 v[126:129], a[208:211], v[178:181], v[126:129]
v_mfma_f32_16x16x32_bf16 v[130:133], v[178:181], a[212:215], v[130:133]
s_sub_u32 s70, s70, 1
ds_read_b128 v[104:107], v51 offset:4096
s_cbranch_scc0 .Lqa_top70
s_waitcnt lgkmcnt(0)
ds_read_b32 v8, v30
ds_read_b128 v[194:197], v25 offset:64
ds_read_b64 v[160:161], v22 offset:41472
ds_read_b32 v144, v20
s_mov_b64 exec, 65535
global_store_dwordx4 v16, v[198:201], s[38:39] sc1
s_mov_b64 exec, -1
v_add_u32 v16, 18432, v16
v_fma_f32 v206, v90, v26, v126
v_fma_f32 v207, v91, v26, v127
v_fma_f32 v208, v92, v26, v128
v_fma_f32 v209, v93, v26, v129
v_cvt_pk_bf16_f32 v150, v206, v207
v_cvt_pk_bf16_f32 v151, v208, v209
v_max_f32 v47, v186, v187
s_nop 0
v_mfma_f32_16x16x32_bf16 v[182:185], v[150:153], v[138:141], v[32:35]
s_waitcnt lgkmcnt(2)
v_max_f32 v48, v188, v189
v_fma_f32 v206, v94, v194, v130
v_mul_f32 v95, 0x3eae1a93, v95
v_mul_f32 v9, v255, v95
v_mul_f32 v10, v8, v95
s_nop 1
v_max3_f32 v47, v182, v183, v47
v_max3_f32 v48, v184, v185, v48
v_mul_f32 v47, v47, v9
v_mul_f32 v48, v48, v10
v_max_f32 v49, v47, v48
v_mov_b32 v50, v49
s_nop 1
v_permlane16_swap_b32 v49, v50
v_max_f32 v49, v49, v50
v_mov_b32 v50, v49
s_nop 1
v_permlane32_swap_b32 v49, v50
v_max_f32 v49, v49, v50
v_sub_f32 v47, v47, v49
v_sub_f32 v48, v48, v49
v_fma_f32 v207, v94, v195, v131
v_exp_f32 v210, v47
v_fma_f32 v208, v94, v196, v132
v_exp_f32 v211, v48
s_nop 0
v_add_f32 v49, v210, v211
v_mov_b32 v50, v49
v_mul_f32 v210, v210, v255
v_fma_f32 v209, v94, v197, v133
v_permlane16_swap_b32 v49, v50
v_add_f32 v49, v49, v50
v_mov_b32 v50, v49
v_mul_f32 v211, v211, v8
v_cvt_pk_bf16_f32 v156, v206, v207
v_cvt_pk_bf16_f32 v157, v208, v209
s_waitcnt lgkmcnt(0)
s_barrier
v_permlane32_swap_b32 v49, v50
v_add_f32 v49, v49, v50
v_fma_f32 v206, v86, v26, v122
v_rcp_f32 v49, v49
s_nop 0
v_mul_f32 v210, v210, v49
v_mul_f32 v211, v211, v49
v_cvt_pk_bf16_f32 v210, v210, v210
v_cvt_pk_bf16_f32 v211, v211, v211
v_and_b32 v192, v210, v40
v_and_b32 v193, v211, v41
v_and_b32 v190, v210, v42
v_and_b32 v191, v211, v43
s_nop 1
v_mfma_f32_16x16x32_bf16 v[186:189], v[154:157], v[190:193], 0
v_mfma_f32_16x16x32_bf16 v[182:185], v[158:161], v[190:193], 0
v_fma_f32 v207, v87, v26, v123
v_fma_f32 v208, v88, v26, v124
v_fma_f32 v209, v89, v26, v125
v_cvt_pk_bf16_f32 v142, v206, v207
v_cvt_pk_bf16_f32 v143, v208, v209
s_nop 1
v_cvt_pk_bf16_f32 v108, v186, v187
v_cvt_pk_bf16_f32 v109, v188, v189
ds_write_b64 v18, v[108:109]
v_cvt_pk_bf16_f32 v210, v182, v183
ds_write_b32 v19, v210
s_waitcnt vmcnt(0)
s_waitcnt lgkmcnt(0)
s_barrier
ds_read_b128 v[198:201], v17 offset:0
s_waitcnt lgkmcnt(0)
global_store_dwordx4 v16, v[198:201], s[38:39] sc1
s_waitcnt vmcnt(0)
s_barrier
s_endpgm
.Lqa_w4:
s_getpc_b64 s[90:91]
.Lqa_pc90:
s_add_u32 s90, s90, .Lqa_e75-.Lqa_pc90
s_addc_u32 s91, s91, 0
s_branch .Lqa_k82
.Lqa_bk89:
v_add_u32 v255, 0x10000, v9
v_add_u32 v162, 0x20000, v9
v_add_u32 v163, 0x10000, v10
v_add_u32 v164, 0x20000, v10
v_mov_b32 v106, 0x3F803F80
v_mov_b32 v107, 0x3F803F80
v_mov_b32 v108, 0x3F803F80
v_mov_b32 v109, 0x3F803F80
v_mov_b32 v141, 0
v_mov_b32 v145, 0
v_mov_b32 v149, 0
v_mov_b32 v153, 0
v_mov_b32 v165, 46080
s_waitcnt lgkmcnt(0)
s_mul_i32 s37, s37, 0x9E3779B1
s_add_u32 s37, s37, 0x7F4A7C15
v_add_u32 v46, s37, v46
v_add_u32 v48, s37, v48
v_add_u32 v50, s37, v50
ds_write_b32 v165, v141
s_mov_b64 s[76:77], 0
s_mov_b32 s73, 18
s_add_u32 m0, s48, 4096
s_nop 0
global_load_lds_dwordx4 v0, s[40:41] offset:-4096
global_load_lds_dwordx4 v1, s[40:41] offset:-3072
global_load_lds_dwordx4 v2, s[40:41] offset:-2048
global_load_lds_dwordx4 v3, s[40:41] offset:-1024
global_load_lds_dwordx4 v4, s[40:41] offset:0
global_load_lds_dwordx4 v5, s[40:41] offset:1024
global_load_lds_dwordx4 v6, s[40:41] offset:2048
global_load_lds_dwordx4 v7, s[40:41] offset:3072
s_add_u32 m0, s48, 12288
s_nop 0
global_load_lds_dwordx4 v8, s[40:41] offset:-4096
s_add_u32 s40, s40, 221184
s_addc_u32 s41, s41, 0
s_add_u32 m0, s48, 40960
s_nop 0
global_load_lds_dwordx4 v0, s[40:41] offset:-4096
global_load_lds_dwordx4 v1, s[40:41] offset:-3072
global_load_lds_dwordx4 v2, s[40:41] offset:-2048
global_load_lds_dwordx4 v3, s[40:41] offset:-1024
global_load_lds_dwordx4 v4, s[40:41] offset:0
global_load_lds_dwordx4 v5, s[40:41] offset:1024
global_load_lds_dwordx4 v6, s[40:41] offset:2048
global_load_lds_dwordx4 v7, s[40:41] offset:3072
s_add_u32 m0, s48, 49152
s_nop 0
global_load_lds_dwordx4 v8, s[40:41] offset:-4096
s_add_u32 s40, s40, 221184
s_addc_u32 s41, s41, 0
s_waitcnt vmcnt(9) lgkmcnt(0)
s_barrier
s_add_u32 m0, s48, 77824
ds_read_b64_tr_b16 a[0:1], v9 offset:47104
ds_read_b64_tr_b16 a[2:3], v9 offset:49408
global_load_lds_dwordx4 v0, s[40:41] offset:-4096
ds_read_b64_tr_b16 a[4:5], v9 offset:47296
global_load_lds_dwordx4 v1, s[40:41] offset:-3072
ds_read_b64_tr_b16 a[6:7], v9 offset:49600
global_load_lds_dwordx4 v2, s[40:41] offset:-2048
ds_read_b64_tr_b16 a[8:9], v9 offset:47488
global_load_lds_dwordx4 v3, s[40:41] offset:-1024
ds_read_b64_tr_b16 a[10:11], v9 offset:49792
ds_read_b128 v[166:169], v51 offset:0
global_load_lds_dwordx4 v4, s[40:41] offset:0
ds_read_b128 v[174:177], v51 offset:18432
global_load_lds_dwordx4 v5, s[40:41] offset:1024
ds_read_b64_tr_b16 a[12:13], v255 offset:0
global_load_lds_dwordx4 v6, s[40:41] offset:2048
ds_read_b64_tr_b16 a[14:15], v255 offset:2304
global_load_lds_dwordx4 v7, s[40:41] offset:3072
ds_read_b64_tr_b16 a[16:17], v255 offset:192
ds_read_b64_tr_b16 a[18:19], v255 offset:2496
s_add_u32 m0, s48, 86016
ds_read_b64_tr_b16 a[20:21], v255 offset:384
global_load_lds_dwordx4 v8, s[40:41] offset:-4096
ds_read_b64_tr_b16 a[22:23], v255 offset:2688
s_add_u32 s40, s40, 221184
ds_read_b128 v[170:173], v52 offset:0
s_addc_u32 s41, s41, 0
ds_read_b128 v[178:181], v52 offset:18432
s_waitcnt vmcnt(9) lgkmcnt(0)
s_barrier
s_add_u32 m0, s48, 4096
ds_read_b64_tr_b16 a[24:25], v255 offset:18432
v_mfma_f32_16x16x32_bf16 v[86:89], a[0:3], v[106:109], 0
v_mfma_f32_16x16x32_bf16 v[90:93], a[4:7], v[106:109], 0
global_load_lds_dwordx4 v0, s[40:41] offset:-4096
ds_read_b64_tr_b16 a[26:27], v255 offset:20736
v_mfma_f32_16x16x32_bf16 v[94:97], v[106:109], a[8:11], 0
ds_read_b64_tr_b16 a[28:29], v255 offset:18624
global_load_lds_dwordx4 v1, s[40:41] offset:-3072
v_mfma_f32_16x16x32_bf16 v[110:113], a[0:3], v[166:169], 0
ds_read_b64_tr_b16 a[30:31], v255 offset:20928
v_mfma_f32_16x16x32_bf16 v[114:117], a[4:7], v[166:169], 0
global_load_lds_dwordx4 v2, s[40:41] offset:-2048
ds_read_b64_tr_b16 a[32:33], v255 offset:18816
v_mfma_f32_16x16x32_bf16 v[118:121], v[166:169], a[8:11], 0
ds_read_b64_tr_b16 a[34:35], v255 offset:21120
global_load_lds_dwordx4 v3, s[40:41] offset:-1024
v_mfma_f32_16x16x32_bf16 v[134:137], v[106:109], v[166:169], 0
ds_read_b128 v[182:185], v51 offset:2048
v_mfma_f32_16x16x32_bf16 v[122:125], a[0:3], v[174:177], 0
global_load_lds_dwordx4 v4, s[40:41] offset:0
ds_read_b128 v[190:193], v51 offset:20480
v_mfma_f32_16x16x32_bf16 v[126:129], a[4:7], v[174:177], 0
ds_read_b64_tr_b16 a[36:37], v255 offset:36864
global_load_lds_dwordx4 v5, s[40:41] offset:1024
v_mfma_f32_16x16x32_bf16 v[130:133], v[174:177], a[8:11], 0
v_mfma_f32_16x16x32_bf16 v[158:161], v[106:109], v[174:177], 0
ds_read_b64_tr_b16 a[38:39], v255 offset:39168
global_load_lds_dwordx4 v6, s[40:41] offset:2048
v_mfma_f32_16x16x32_bf16 v[86:89], a[12:15], v[106:109], v[86:89]
ds_read_b64_tr_b16 a[40:41], v255 offset:37056
v_mfma_f32_16x16x32_bf16 v[90:93], a[16:19], v[106:109], v[90:93]
global_load_lds_dwordx4 v7, s[40:41] offset:3072
ds_read_b64_tr_b16 a[42:43], v255 offset:39360
v_mfma_f32_16x16x32_bf16 v[94:97], v[106:109], a[20:23], v[94:97]
s_add_u32 m0, s48, 12288
ds_read_b64_tr_b16 a[44:45], v255 offset:37248
v_mfma_f32_16x16x32_bf16 v[110:113], a[12:15], v[170:173], v[110:113]
ds_read_b64_tr_b16 a[46:47], v255 offset:39552
v_mfma_f32_16x16x32_bf16 v[114:117], a[16:19], v[170:173], v[114:117]
global_load_lds_dwordx4 v8, s[40:41] offset:-4096
ds_read_b128 v[186:189], v52 offset:2048
v_mfma_f32_16x16x32_bf16 v[118:121], v[170:173], a[20:23], v[118:121]
s_add_u32 s40, s40, 221184
ds_read_b128 v[194:197], v52 offset:20480
v_mfma_f32_16x16x32_bf16 v[134:137], v[106:109], v[170:173], v[134:137]
v_mfma_f32_16x16x32_bf16 v[122:125], a[12:15], v[178:181], v[122:125]
v_mfma_f32_16x16x32_bf16 v[126:129], a[16:19], v[178:181], v[126:129]
s_addc_u32 s41, s41, 0
v_mfma_f32_16x16x32_bf16 v[130:133], v[178:181], a[20:23], v[130:133]
v_mfma_f32_16x16x32_bf16 v[158:161], v[106:109], v[178:181], v[158:161]
)"
R"(s_waitcnt vmcnt(9) lgkmcnt(0)
s_barrier
s_add_u32 m0, s48, 40960
ds_read_b64_tr_b16 a[48:49], v255 offset:55296
v_mfma_f32_16x16x32_bf16 v[86:89], a[24:27], v[106:109], v[86:89]
v_mfma_f32_16x16x32_bf16 v[90:93], a[28:31], v[106:109], v[90:93]
global_load_lds_dwordx4 v0, s[40:41] offset:-4096
ds_read_b64_tr_b16 a[50:51], v255 offset:57600
v_mfma_f32_16x16x32_bf16 v[94:97], v[106:109], a[32:35], v[94:97]
ds_read_b64_tr_b16 a[52:53], v255 offset:55488
global_load_lds_dwordx4 v1, s[40:41] offset:-3072
v_mfma_f32_16x16x32_bf16 v[110:113], a[24:27], v[182:185], v[110:113]
ds_read_b64_tr_b16 a[54:55], v255 offset:57792
v_mfma_f32_16x16x32_bf16 v[114:117], a[28:31], v[182:185], v[114:117]
global_load_lds_dwordx4 v2, s[40:41] offset:-2048
ds_read_b64_tr_b16 a[56:57], v255 offset:55680
v_mfma_f32_16x16x32_bf16 v[118:121], v[182:185], a[32:35], v[118:121]
ds_read_b64_tr_b16 a[58:59], v255 offset:57984
global_load_lds_dwordx4 v3, s[40:41] offset:-1024
v_mfma_f32_16x16x32_bf16 v[134:137], v[106:109], v[182:185], v[134:137]
ds_read_b128 v[166:169], v51 offset:4096
v_mfma_f32_16x16x32_bf16 v[122:125], a[24:27], v[190:193], v[122:125]
global_load_lds_dwordx4 v4, s[40:41] offset:0
ds_read_b128 v[174:177], v51 offset:22528
v_mfma_f32_16x16x32_bf16 v[126:129], a[28:31], v[190:193], v[126:129]
ds_read_b64_tr_b16 a[60:61], v162 offset:8192
global_load_lds_dwordx4 v5, s[40:41] offset:1024
v_mfma_f32_16x16x32_bf16 v[130:133], v[190:193], a[32:35], v[130:133]
v_mfma_f32_16x16x32_bf16 v[158:161], v[106:109], v[190:193], v[158:161]
ds_read_b64_tr_b16 a[62:63], v162 offset:10496
global_load_lds_dwordx4 v6, s[40:41] offset:2048
v_mfma_f32_16x16x32_bf16 v[86:89], a[36:39], v[106:109], v[86:89]
ds_read_b64_tr_b16 a[64:65], v162 offset:8384
v_mfma_f32_16x16x32_bf16 v[90:93], a[40:43], v[106:109], v[90:93]
global_load_lds_dwordx4 v7, s[40:41] offset:3072
ds_read_b64_tr_b16 a[66:67], v162 offset:10688
v_mfma_f32_16x16x32_bf16 v[94:97], v[106:109], a[44:47], v[94:97]
s_add_u32 m0, s48, 49152
ds_read_b64_tr_b16 a[68:69], v162 offset:8576
v_mfma_f32_16x16x32_bf16 v[110:113], a[36:39], v[186:189], v[110:113]
ds_read_b64_tr_b16 a[70:71], v162 offset:10880
v_mfma_f32_16x16x32_bf16 v[114:117], a[40:43], v[186:189], v[114:117]
global_load_lds_dwordx4 v8, s[40:41] offset:-4096
ds_read_b128 v[170:173], v52 offset:4096
v_mfma_f32_16x16x32_bf16 v[118:121], v[186:189], a[44:47], v[118:121]
s_add_u32 s40, s40, 221184
ds_read_b128 v[178:181], v52 offset:22528
v_mfma_f32_16x16x32_bf16 v[134:137], v[106:109], v[186:189], v[134:137]
v_mfma_f32_16x16x32_bf16 v[122:125], a[36:39], v[194:197], v[122:125]
v_mfma_f32_16x16x32_bf16 v[126:129], a[40:43], v[194:197], v[126:129]
s_addc_u32 s41, s41, 0
v_mfma_f32_16x16x32_bf16 v[130:133], v[194:197], a[44:47], v[130:133]
v_mfma_f32_16x16x32_bf16 v[158:161], v[106:109], v[194:197], v[158:161]
s_waitcnt vmcnt(9) lgkmcnt(0)
s_barrier
s_add_u32 m0, s48, 77824
ds_read_b64_tr_b16 a[72:73], v9 offset:47104
v_mfma_f32_16x16x32_bf16 v[86:89], a[48:51], v[106:109], v[86:89]
v_mfma_f32_16x16x32_bf16 v[90:93], a[52:55], v[106:109], v[90:93]
global_load_lds_dwordx4 v0, s[40:41] offset:-4096
ds_read_b64_tr_b16 a[74:75], v9 offset:49408
v_mfma_f32_16x16x32_bf16 v[94:97], v[106:109], a[56:59], v[94:97]
ds_read_b64_tr_b16 a[76:77], v9 offset:47296
global_load_lds_dwordx4 v1, s[40:41] offset:-3072
v_mfma_f32_16x16x32_bf16 v[110:113], a[48:51], v[166:169], v[110:113]
ds_read_b64_tr_b16 a[78:79], v9 offset:49600
v_mfma_f32_16x16x32_bf16 v[114:117], a[52:55], v[166:169], v[114:117]
global_load_lds_dwordx4 v2, s[40:41] offset:-2048
ds_read_b64_tr_b16 a[80:81], v9 offset:47488
v_mfma_f32_16x16x32_bf16 v[118:121], v[166:169], a[56:59], v[118:121]
ds_read_b64_tr_b16 a[82:83], v9 offset:49792
global_load_lds_dwordx4 v3, s[40:41] offset:-1024
v_mfma_f32_16x16x32_bf16 v[134:137], v[106:109], v[166:169], v[134:137]
ds_read_b128 v[182:185], v51 offset:6144
v_mfma_f32_16x16x32_bf16 v[122:125], a[48:51], v[174:177], v[122:125]
global_load_lds_dwordx4 v4, s[40:41] offset:0
ds_read_b128 v[190:193], v51 offset:24576
v_mfma_f32_16x16x32_bf16 v[126:129], a[52:55], v[174:177], v[126:129]
ds_read_b64_tr_b16 a[84:85], v255 offset:0
global_load_lds_dwordx4 v5, s[40:41] offset:1024
v_mfma_f32_16x16x32_bf16 v[130:133], v[174:177], a[56:59], v[130:133]
v_mfma_f32_16x16x32_bf16 v[158:161], v[106:109], v[174:177], v[158:161]
ds_read_b64_tr_b16 a[86:87], v255 offset:2304
global_load_lds_dwordx4 v6, s[40:41] offset:2048
v_mfma_f32_16x16x32_bf16 v[86:89], a[60:63], v[106:109], v[86:89]
ds_read_b64_tr_b16 a[88:89], v255 offset:192
v_mfma_f32_16x16x32_bf16 v[90:93], a[64:67], v[106:109], v[90:93]
global_load_lds_dwordx4 v7, s[40:41] offset:3072
ds_read_b64_tr_b16 a[90:91], v255 offset:2496
v_mfma_f32_16x16x32_bf16 v[94:97], v[106:109], a[68:71], v[94:97]
s_add_u32 m0, s48, 86016
ds_read_b64_tr_b16 a[92:93], v255 offset:384
v_mfma_f32_16x16x32_bf16 v[110:113], a[60:63], v[170:173], v[110:113]
ds_read_b64_tr_b16 a[94:95], v255 offset:2688
v_mfma_f32_16x16x32_bf16 v[114:117], a[64:67], v[170:173], v[114:117]
global_load_lds_dwordx4 v8, s[40:41] offset:-4096
ds_read_b128 v[186:189], v52 offset:6144
v_mfma_f32_16x16x32_bf16 v[118:121], v[170:173], a[68:71], v[118:121]
s_add_u32 s40, s40, 221184
ds_read_b128 v[194:197], v52 offset:24576
v_mfma_f32_16x16x32_bf16 v[134:137], v[106:109], v[170:173], v[134:137]
v_mfma_f32_16x16x32_bf16 v[122:125], a[60:63], v[178:181], v[122:125]
v_mfma_f32_16x16x32_bf16 v[126:129], a[64:67], v[178:181], v[126:129]
s_addc_u32 s41, s41, 0
v_mfma_f32_16x16x32_bf16 v[130:133], v[178:181], a[68:71], v[130:133]
v_mfma_f32_16x16x32_bf16 v[158:161], v[106:109], v[178:181], v[158:161]
s_waitcnt vmcnt(9) lgkmcnt(0)
s_barrier
s_add_u32 m0, s48, 4096
ds_read_b64_tr_b16 a[96:97], v255 offset:18432
v_mfma_f32_16x16x32_bf16 v[86:89], a[72:75], v[106:109], v[86:89]
v_mfma_f32_16x16x32_bf16 v[90:93], a[76:79], v[106:109], v[90:93]
global_load_lds_dwordx4 v0, s[40:41] offset:-4096
ds_read_b64_tr_b16 a[98:99], v255 offset:20736
v_mfma_f32_16x16x32_bf16 v[94:97], v[106:109], a[80:83], v[94:97]
ds_read_b64_tr_b16 a[100:101], v255 offset:18624
global_load_lds_dwordx4 v1, s[40:41] offset:-3072
v_mfma_f32_16x16x32_bf16 v[110:113], a[72:75], v[182:185], v[110:113]
ds_read_b64_tr_b16 a[102:103], v255 offset:20928
v_mfma_f32_16x16x32_bf16 v[114:117], a[76:79], v[182:185], v[114:117]
global_load_lds_dwordx4 v2, s[40:41] offset:-2048
ds_read_b64_tr_b16 a[104:105], v255 offset:18816
v_mfma_f32_16x16x32_bf16 v[118:121], v[182:185], a[80:83], v[118:121]
ds_read_b64_tr_b16 a[106:107], v255 offset:21120
global_load_lds_dwordx4 v3, s[40:41] offset:-1024
v_mfma_f32_16x16x32_bf16 v[134:137], v[106:109], v[182:185], v[134:137]
ds_read_b128 v[166:169], v51 offset:8192
v_mfma_f32_16x16x32_bf16 v[122:125], a[72:75], v[190:193], v[122:125]
global_load_lds_dwordx4 v4, s[40:41] offset:0
ds_read_b128 v[174:177], v51 offset:26624
v_mfma_f32_16x16x32_bf16 v[126:129], a[76:79], v[190:193], v[126:129]
ds_read_b64_tr_b16 a[108:109], v255 offset:36864
global_load_lds_dwordx4 v5, s[40:41] offset:1024
v_mfma_f32_16x16x32_bf16 v[130:133], v[190:193], a[80:83], v[130:133]
v_mfma_f32_16x16x32_bf16 v[158:161], v[106:109], v[190:193], v[158:161]
ds_read_b64_tr_b16 a[110:111], v255 offset:39168
global_load_lds_dwordx4 v6, s[40:41] offset:2048
v_mfma_f32_16x16x32_bf16 v[86:89], a[84:87], v[106:109], v[86:89]
ds_read_b64_tr_b16 a[112:113], v255 offset:37056
v_mfma_f32_16x16x32_bf16 v[90:93], a[88:91], v[106:109], v[90:93]
global_load_lds_dwordx4 v7, s[40:41] offset:3072
ds_read_b64_tr_b16 a[114:115], v255 offset:39360
v_mfma_f32_16x16x32_bf16 v[94:97], v[106:109], a[92:95], v[94:97]
s_add_u32 m0, s48, 12288
ds_read_b64_tr_b16 a[116:117], v255 offset:37248
v_mfma_f32_16x16x32_bf16 v[110:113], a[84:87], v[186:189], v[110:113]
ds_read_b64_tr_b16 a[118:119], v255 offset:39552
v_mfma_f32_16x16x32_bf16 v[114:117], a[88:91], v[186:189], v[114:117]
global_load_lds_dwordx4 v8, s[40:41] offset:-4096
ds_read_b128 v[170:173], v52 offset:8192
v_mfma_f32_16x16x32_bf16 v[118:121], v[186:189], a[92:95], v[118:121]
s_add_u32 s40, s40, 221184
ds_read_b128 v[178:181], v52 offset:26624
v_mfma_f32_16x16x32_bf16 v[134:137], v[106:109], v[186:189], v[134:137]
v_mfma_f32_16x16x32_bf16 v[122:125], a[84:87], v[194:197], v[122:125]
v_mfma_f32_16x16x32_bf16 v[126:129], a[88:91], v[194:197], v[126:129]
s_addc_u32 s41, s41, 0
v_mfma_f32_16x16x32_bf16 v[130:133], v[194:197], a[92:95], v[130:133]
v_mfma_f32_16x16x32_bf16 v[158:161], v[106:109], v[194:197], v[158:161]
s_waitcnt vmcnt(9) lgkmcnt(0)
s_barrier
s_add_u32 m0, s48, 40960
ds_read_b64_tr_b16 a[120:121], v255 offset:55296
v_mfma_f32_16x16x32_bf16 v[86:89], a[96:99], v[106:109], v[86:89]
v_mfma_f32_16x16x32_bf16 v[90:93], a[100:103], v[106:109], v[90:93]
global_load_lds_dwordx4 v0, s[40:41] offset:-4096
ds_read_b64_tr_b16 a[122:123], v255 offset:57600
v_mfma_f32_16x16x32_bf16 v[94:97], v[106:109], a[104:107], v[94:97]
ds_read_b64_tr_b16 a[124:125], v255 offset:55488
global_load_lds_dwordx4 v1, s[40:41] offset:-3072
v_mfma_f32_16x16x32_bf16 v[110:113], a[96:99], v[166:169], v[110:113]
ds_read_b64_tr_b16 a[126:127], v255 offset:57792
v_mfma_f32_16x16x32_bf16 v[114:117], a[100:103], v[166:169], v[114:117]
global_load_lds_dwordx4 v2, s[40:41] offset:-2048
ds_read_b64_tr_b16 a[128:129], v255 offset:55680
v_mfma_f32_16x16x32_bf16 v[118:121], v[166:169], a[104:107], v[118:121]
ds_read_b64_tr_b16 a[130:131], v255 offset:57984
global_load_lds_dwordx4 v3, s[40:41] offset:-1024
v_mfma_f32_16x16x32_bf16 v[134:137], v[106:109], v[166:169], v[134:137]
ds_read_b128 v[182:185], v51 offset:10240
v_mfma_f32_16x16x32_bf16 v[122:125], a[96:99], v[174:177], v[122:125]
global_load_lds_dwordx4 v4, s[40:41] offset:0
ds_read_b128 v[190:193], v51 offset:28672
v_mfma_f32_16x16x32_bf16 v[126:129], a[100:103], v[174:177], v[126:129]
ds_read_b64_tr_b16 a[132:133], v162 offset:8192
global_load_lds_dwordx4 v5, s[40:41] offset:1024
v_mfma_f32_16x16x32_bf16 v[130:133], v[174:177], a[104:107], v[130:133]
v_mfma_f32_16x16x32_bf16 v[158:161], v[106:109], v[174:177], v[158:161]
ds_read_b64_tr_b16 a[134:135], v162 offset:10496
global_load_lds_dwordx4 v6, s[40:41] offset:2048
v_mfma_f32_16x16x32_bf16 v[86:89], a[108:111], v[106:109], v[86:89]
ds_read_b64_tr_b16 a[136:137], v162 offset:8384
v_mfma_f32_16x16x32_bf16 v[90:93], a[112:115], v[106:109], v[90:93]
global_load_lds_dwordx4 v7, s[40:41] offset:3072
ds_read_b64_tr_b16 a[138:139], v162 offset:10688
v_mfma_f32_16x16x32_bf16 v[94:97], v[106:109], a[116:119], v[94:97]
s_add_u32 m0, s48, 49152
ds_read_b64_tr_b16 a[140:141], v162 offset:8576
v_mfma_f32_16x16x32_bf16 v[110:113], a[108:111], v[170:173], v[110:113]
ds_read_b64_tr_b16 a[142:143], v162 offset:10880
v_mfma_f32_16x16x32_bf16 v[114:117], a[112:115], v[170:173], v[114:117]
global_load_lds_dwordx4 v8, s[40:41] offset:-4096
ds_read_b128 v[186:189], v52 offset:10240
v_mfma_f32_16x16x32_bf16 v[118:121], v[170:173], a[116:119], v[118:121]
s_add_u32 s40, s40, 221184
ds_read_b128 v[194:197], v52 offset:28672
v_mfma_f32_16x16x32_bf16 v[134:137], v[106:109], v[170:173], v[134:137]
v_mfma_f32_16x16x32_bf16 v[122:125], a[108:111], v[178:181], v[122:125]
v_mfma_f32_16x16x32_bf16 v[126:129], a[112:115], v[178:181], v[126:129]
s_addc_u32 s41, s41, 0
v_mfma_f32_16x16x32_bf16 v[130:133], v[178:181], a[116:119], v[130:133]
v_mfma_f32_16x16x32_bf16 v[158:161], v[106:109], v[178:181], v[158:161]
s_waitcnt vmcnt(9) lgkmcnt(0)
s_barrier
s_add_u32 m0, s48, 77824
ds_read_b64_tr_b16 a[144:145], v9 offset:47104
v_mfma_f32_16x16x32_bf16 v[86:89], a[120:123], v[106:109], v[86:89]
v_mfma_f32_16x16x32_bf16 v[90:93], a[124:127], v[106:109], v[90:93]
global_load_lds_dwordx4 v0, s[40:41] offset:-4096
ds_read_b64_tr_b16 a[146:147], v9 offset:49408
v_mfma_f32_16x16x32_bf16 v[94:97], v[106:109], a[128:131], v[94:97]
ds_read_b64_tr_b16 a[148:149], v9 offset:47296
global_load_lds_dwordx4 v1, s[40:41] offset:-3072
v_mfma_f32_16x16x32_bf16 v[110:113], a[120:123], v[182:185], v[110:113]
ds_read_b64_tr_b16 a[150:151], v9 offset:49600
v_mfma_f32_16x16x32_bf16 v[114:117], a[124:127], v[182:185], v[114:117]
global_load_lds_dwordx4 v2, s[40:41] offset:-2048
ds_read_b64_tr_b16 a[152:153], v9 offset:47488
v_mfma_f32_16x16x32_bf16 v[118:121], v[182:185], a[128:131], v[118:121]
ds_read_b64_tr_b16 a[154:155], v9 offset:49792
global_load_lds_dwordx4 v3, s[40:41] offset:-1024
v_mfma_f32_16x16x32_bf16 v[134:137], v[106:109], v[182:185], v[134:137]
ds_read_b128 v[166:169], v51 offset:12288
v_mfma_f32_16x16x32_bf16 v[122:125], a[120:123], v[190:193], v[122:125]
global_load_lds_dwordx4 v4, s[40:41] offset:0
ds_read_b128 v[174:177], v51 offset:30720
v_mfma_f32_16x16x32_bf16 v[126:129], a[124:127], v[190:193], v[126:129]
ds_read_b64_tr_b16 a[156:157], v255 offset:0
global_load_lds_dwordx4 v5, s[40:41] offset:1024
v_mfma_f32_16x16x32_bf16 v[130:133], v[190:193], a[128:131], v[130:133]
v_mfma_f32_16x16x32_bf16 v[158:161], v[106:109], v[190:193], v[158:161]
ds_read_b64_tr_b16 a[158:159], v255 offset:2304
global_load_lds_dwordx4 v6, s[40:41] offset:2048
v_mfma_f32_16x16x32_bf16 v[86:89], a[132:135], v[106:109], v[86:89]
ds_read_b64_tr_b16 a[160:161], v255 offset:192
v_mfma_f32_16x16x32_bf16 v[90:93], a[136:139], v[106:109], v[90:93]
global_load_lds_dwordx4 v7, s[40:41] offset:3072
ds_read_b64_tr_b16 a[162:163], v255 offset:2496
v_mfma_f32_16x16x32_bf16 v[94:97], v[106:109], a[140:143], v[94:97]
s_add_u32 m0, s48, 86016
ds_read_b64_tr_b16 a[164:165], v255 offset:384
v_mfma_f32_16x16x32_bf16 v[110:113], a[132:135], v[186:189], v[110:113]
ds_read_b64_tr_b16 a[166:167], v255 offset:2688
v_mfma_f32_16x16x32_bf16 v[114:117], a[136:139], v[186:189], v[114:117]
global_load_lds_dwordx4 v8, s[40:41] offset:-4096
ds_read_b128 v[170:173], v52 offset:12288
v_mfma_f32_16x16x32_bf16 v[118:121], v[186:189], a[140:143], v[118:121]
s_sub_u32 s40, s40, 1769472
ds_read_b128 v[178:181], v52 offset:30720
v_mfma_f32_16x16x32_bf16 v[134:137], v[106:109], v[186:189], v[134:137]
v_mfma_f32_16x16x32_bf16 v[122:125], a[132:135], v[194:197], v[122:125]
v_mfma_f32_16x16x32_bf16 v[126:129], a[136:139], v[194:197], v[126:129]
s_subb_u32 s41, s41, 0
v_mfma_f32_16x16x32_bf16 v[130:133], v[194:197], a[140:143], v[130:133]
v_mfma_f32_16x16x32_bf16 v[158:161], v[106:109], v[194:197], v[158:161]
s_waitcnt vmcnt(9) lgkmcnt(0)
s_barrier
s_add_u32 m0, s49, 0
ds_read_b64_tr_b16 a[168:169], v255 offset:18432
v_mfma_f32_16x16x32_bf16 v[86:89], a[144:147], v[106:109], v[86:89]
v_mfma_f32_16x16x32_bf16 v[90:93], a[148:151], v[106:109], v[90:93]
ds_read_b64_tr_b16 a[170:171], v255 offset:20736
v_mfma_f32_16x16x32_bf16 v[94:97], v[106:109], a[152:155], v[94:97]
ds_read_b64_tr_b16 a[172:173], v255 offset:18624
v_mfma_f32_16x16x32_bf16 v[110:113], a[144:147], v[166:169], v[110:113]
global_load_lds_dwordx4 v11, s[42:43] offset:-2048 sc1
ds_read_b64_tr_b16 a[174:175], v255 offset:20928
v_mfma_f32_16x16x32_bf16 v[114:117], a[148:151], v[166:169], v[114:117]
ds_read_b64_tr_b16 a[176:177], v255 offset:18816
v_mfma_f32_16x16x32_bf16 v[118:121], v[166:169], a[152:155], v[118:121]
ds_read_b64_tr_b16 a[178:179], v255 offset:21120
v_mfma_f32_16x16x32_bf16 v[134:137], v[106:109], v[166:169], v[134:137]
global_load_lds_dwordx4 v12, s[42:43] offset:-1024 sc1
ds_read_b128 v[182:185], v51 offset:14336
v_mfma_f32_16x16x32_bf16 v[122:125], a[144:147], v[174:177], v[122:125]
ds_read_b128 v[190:193], v51 offset:32768
v_mfma_f32_16x16x32_bf16 v[126:129], a[148:151], v[174:177], v[126:129]
ds_read_b64_tr_b16 a[180:181], v255 offset:36864
v_mfma_f32_16x16x32_bf16 v[130:133], v[174:177], a[152:155], v[130:133]
global_load_lds_dwordx4 v13, s[42:43] offset:0 sc1
v_mfma_f32_16x16x32_bf16 v[158:161], v[106:109], v[174:177], v[158:161]
ds_read_b64_tr_b16 a[182:183], v255 offset:39168
v_mfma_f32_16x16x32_bf16 v[86:89], a[156:159], v[106:109], v[86:89]
ds_read_b64_tr_b16 a[184:185], v255 offset:37056
v_mfma_f32_16x16x32_bf16 v[90:93], a[160:163], v[106:109], v[90:93]
ds_read_b64_tr_b16 a[186:187], v255 offset:39360
v_mfma_f32_16x16x32_bf16 v[94:97], v[106:109], a[164:167], v[94:97]
global_load_lds_dwordx4 v14, s[42:43] offset:1024 sc1
ds_read_b64_tr_b16 a[188:189], v255 offset:37248
v_mfma_f32_16x16x32_bf16 v[110:113], a[156:159], v[170:173], v[110:113]
ds_read_b64_tr_b16 a[190:191], v255 offset:39552
v_mfma_f32_16x16x32_bf16 v[114:117], a[160:163], v[170:173], v[114:117]
ds_read_b128 v[186:189], v52 offset:14336
v_mfma_f32_16x16x32_bf16 v[118:121], v[170:173], a[164:167], v[118:121]
s_sub_u32 s42, s42, 18432
s_subb_u32 s43, s43, 0
ds_read_b128 v[194:197], v52 offset:32768
v_mfma_f32_16x16x32_bf16 v[134:137], v[106:109], v[170:173], v[134:137]
v_mfma_f32_16x16x32_bf16 v[122:125], a[156:159], v[178:181], v[122:125]
v_mfma_f32_16x16x32_bf16 v[126:129], a[160:163], v[178:181], v[126:129]
v_mfma_f32_16x16x32_bf16 v[130:133], v[178:181], a[164:167], v[130:133]
v_mfma_f32_16x16x32_bf16 v[158:161], v[106:109], v[178:181], v[158:161]
s_waitcnt vmcnt(4) lgkmcnt(0)
s_barrier
s_add_u32 m0, s49, 18432
ds_read_b64_tr_b16 a[192:193], v255 offset:55296
v_mfma_f32_16x16x32_bf16 v[86:89], a[168:171], v[106:109], v[86:89]
v_mfma_f32_16x16x32_bf16 v[90:93], a[172:175], v[106:109], v[90:93]
ds_read_b64_tr_b16 a[194:195], v255 offset:57600
v_mfma_f32_16x16x32_bf16 v[94:97], v[106:109], a[176:179], v[94:97]
ds_read_b64_tr_b16 a[196:197], v255 offset:55488
v_mfma_f32_16x16x32_bf16 v[110:113], a[168:171], v[182:185], v[110:113]
global_load_lds_dwordx4 v11, s[42:43] offset:-2048 sc1
ds_read_b64_tr_b16 a[198:199], v255 offset:57792
v_mfma_f32_16x16x32_bf16 v[114:117], a[172:175], v[182:185], v[114:117]
ds_read_b64_tr_b16 a[200:201], v255 offset:55680
v_mfma_f32_16x16x32_bf16 v[118:121], v[182:185], a[176:179], v[118:121]
ds_read_b64_tr_b16 a[202:203], v255 offset:57984
v_mfma_f32_16x16x32_bf16 v[134:137], v[106:109], v[182:185], v[134:137]
global_load_lds_dwordx4 v12, s[42:43] offset:-1024 sc1
ds_read_b128 v[166:169], v51 offset:16384
v_mfma_f32_16x16x32_bf16 v[122:125], a[168:171], v[190:193], v[122:125]
ds_read_b128 v[174:177], v51 offset:34816
v_mfma_f32_16x16x32_bf16 v[126:129], a[172:175], v[190:193], v[126:129]
ds_read_b64_tr_b16 a[204:205], v162 offset:8192
v_mfma_f32_16x16x32_bf16 v[130:133], v[190:193], a[176:179], v[130:133]
global_load_lds_dwordx4 v13, s[42:43] offset:0 sc1
ds_read_b64_tr_b16 a[206:207], v162 offset:10496
v_mfma_f32_16x16x32_bf16 v[158:161], v[106:109], v[190:193], v[158:161]
ds_read_b64_tr_b16 a[208:209], v162 offset:8384
v_mfma_f32_16x16x32_bf16 v[86:89], a[180:183], v[106:109], v[86:89]
ds_read_b64_tr_b16 a[210:211], v162 offset:10688
v_mfma_f32_16x16x32_bf16 v[90:93], a[184:187], v[106:109], v[90:93]
global_load_lds_dwordx4 v14, s[42:43] offset:1024 sc1
ds_read_b64_tr_b16 a[212:213], v162 offset:8576
v_mfma_f32_16x16x32_bf16 v[94:97], v[106:109], a[188:191], v[94:97]
ds_read_b64_tr_b16 a[214:215], v162 offset:10880
v_mfma_f32_16x16x32_bf16 v[110:113], a[180:183], v[186:189], v[110:113]
ds_read_b128 v[170:173], v52 offset:16384
v_mfma_f32_16x16x32_bf16 v[114:117], a[184:187], v[186:189], v[114:117]
s_sub_u32 s42, s42, 18432
s_subb_u32 s43, s43, 0
ds_read_b128 v[178:181], v52 offset:34816
v_mfma_f32_16x16x32_bf16 v[118:121], v[186:189], a[188:191], v[118:121]
v_mfma_f32_16x16x32_bf16 v[134:137], v[106:109], v[186:189], v[134:137]
v_mfma_f32_16x16x32_bf16 v[122:125], a[180:183], v[194:197], v[122:125]
v_mfma_f32_16x16x32_bf16 v[126:129], a[184:187], v[194:197], v[126:129]
v_mfma_f32_16x16x32_bf16 v[158:161], v[106:109], v[194:197], v[158:161]
s_waitcnt lgkmcnt(14)
v_mfma_f32_16x16x32_bf16 v[86:89], a[192:195], v[106:109], v[86:89]
s_waitcnt lgkmcnt(12)
v_mfma_f32_16x16x32_bf16 v[90:93], a[196:199], v[106:109], v[90:93]
s_waitcnt lgkmcnt(10)
v_mfma_f32_16x16x32_bf16 v[94:97], v[106:109], a[200:203], v[94:97]
s_waitcnt lgkmcnt(9)
v_mfma_f32_16x16x32_bf16 v[110:113], a[192:195], v[166:169], v[110:113]
v_mfma_f32_16x16x32_bf16 v[114:117], a[196:199], v[166:169], v[114:117]
v_mfma_f32_16x16x32_bf16 v[118:121], v[166:169], a[200:203], v[118:121]
v_mfma_f32_16x16x32_bf16 v[134:137], v[106:109], v[166:169], v[134:137]
s_waitcnt lgkmcnt(8)
v_mfma_f32_16x16x32_bf16 v[158:161], v[106:109], v[174:177], v[158:161]
s_waitcnt lgkmcnt(6)
v_mfma_f32_16x16x32_bf16 v[86:89], a[204:207], v[106:109], v[86:89]
s_waitcnt lgkmcnt(4)
v_mfma_f32_16x16x32_bf16 v[90:93], a[208:211], v[106:109], v[90:93]
s_waitcnt lgkmcnt(2)
v_mfma_f32_16x16x32_bf16 v[94:97], v[106:109], a[212:215], v[94:97]
s_waitcnt lgkmcnt(1)
v_mfma_f32_16x16x32_bf16 v[110:113], a[204:207], v[170:173], v[110:113]
v_mfma_f32_16x16x32_bf16 v[114:117], a[208:211], v[170:173], v[114:117]
v_mfma_f32_16x16x32_bf16 v[118:121], v[170:173], a[212:215], v[118:121]
v_mfma_f32_16x16x32_bf16 v[134:137], v[106:109], v[170:173], v[134:137]
s_waitcnt lgkmcnt(0)
v_mfma_f32_16x16x32_bf16 v[158:161], v[106:109], v[178:181], v[158:161]
s_nop 5
v_mul_f32 v202, 0x3ae38e39, v134
v_cvt_pk_bf16_f32 v216, -v202, -v202
v_lshlrev_b32 v216, 16, v216
ds_write_b32 v24, v216 offset:0
s_waitcnt lgkmcnt(0)
s_barrier
ds_read_b32 v216, v26
s_waitcnt lgkmcnt(0)
v_mul_f32 v216, 0x3ae38e39, v216
v_fma_f32 v203, -v202, v202, v216
v_max_f32 v203, 0, v203
v_cvt_pk_bf16_f32 v203, v203, v203
v_lshlrev_b32 v203, 16, v203
v_add_f32 v203, 0x3727c5ac, v203
v_cvt_pk_bf16_f32 v203, v203, v203
)"
R"(v_lshlrev_b32 v203, 16, v203
v_sqrt_f32 v203, v203
s_nop 0
v_cvt_pk_bf16_f32 v203, v203, v203
v_lshlrev_b32 v203, 16, v203
v_rcp_f32 v216, v203
s_nop 0
ds_write_b32 v24, v216 offset:128
ds_read_b128 v[174:177], v52 offset:32768
ds_read_b128 v[178:181], v51 offset:34816
ds_read_b128 v[182:185], v52 offset:34816
v_mov_b32 v206, v32
v_mov_b32 v207, v33
v_mov_b32 v208, v34
v_mov_b32 v209, v35
v_mov_b32 v134, v158
v_mov_b32 v135, v159
v_mov_b32 v136, v160
v_mov_b32 v137, v161
s_waitcnt lgkmcnt(3)
s_barrier
ds_read_b32 v23, v24 offset:0
ds_read_b32 v148, v21
ds_read_b32 v95, v24 offset:192
ds_read_b32 v254, v27
ds_read_b32 v255, v28
ds_read_b128 v[198:201], v25 offset:0
ds_read_b64 v[158:159], v22 offset:41472
ds_read_b32 v140, v20
v_xor_b32 v51, 0x10000, v51
v_xor_b32 v52, 0x10000, v52
v_mul_f32 v202, 0x3ae38e39, v134
v_cvt_pk_bf16_f32 v216, -v202, -v202
v_lshlrev_b32 v216, 16, v216
ds_write_b32 v24, v216 offset:64
s_waitcnt lgkmcnt(8)
v_fma_f32 v210, v90, v23, v114
v_fma_f32 v211, v91, v23, v115
v_fma_f32 v212, v92, v23, v116
v_fma_f32 v213, v93, v23, v117
v_cvt_pk_bf16_f32 v146, v210, v211
v_cvt_pk_bf16_f32 v147, v212, v213
s_waitcnt lgkmcnt(0)
s_barrier
ds_read_b32 v216, v26
v_fma_f32 v210, v94, v198, v118
v_fma_f32 v211, v94, v199, v119
v_fma_f32 v212, v94, v200, v120
v_fma_f32 v213, v94, v201, v121
v_cvt_pk_bf16_f32 v154, v210, v211
v_cvt_pk_bf16_f32 v155, v212, v213
v_fma_f32 v210, v86, v23, v110
v_fma_f32 v211, v87, v23, v111
v_fma_f32 v212, v88, v23, v112
v_fma_f32 v213, v89, v23, v113
v_cvt_pk_bf16_f32 v138, v210, v211
v_cvt_pk_bf16_f32 v139, v212, v213
s_waitcnt lgkmcnt(0)
v_mul_f32 v216, 0x3ae38e39, v216
v_fma_f32 v203, -v202, v202, v216
v_max_f32 v203, 0, v203
v_cvt_pk_bf16_f32 v203, v203, v203
v_lshlrev_b32 v203, 16, v203
v_add_f32 v203, 0x3727c5ac, v203
v_cvt_pk_bf16_f32 v203, v203, v203
v_lshlrev_b32 v203, 16, v203
v_sqrt_f32 v203, v203
s_nop 0
v_cvt_pk_bf16_f32 v203, v203, v203
v_lshlrev_b32 v203, 16, v203
v_rcp_f32 v216, v203
s_nop 0
ds_write_b32 v24, v216 offset:192
v_add_lshl_u32 v252, v48, 0, 7
v_bfi_b32 v252, s69, v252, v47
s_waitcnt vmcnt(4) lgkmcnt(0)
s_barrier
s_mov_b32 s70, 32
s_waitcnt lgkmcnt(0)
s_nop 1
s_branch .Lqa_in72
.p2align 6
.Lqa_top91:
s_waitcnt lgkmcnt(5)
v_mfma_f32_16x16x32_bf16 v[134:137], v[106:109], v[0:3], 0
s_waitcnt lgkmcnt(4)
ds_read_b128 v[58:61], v52 offset:4096
v_bfi_b32 v252, s69, v252, v47
v_mfma_f32_16x16x32_bf16 v[134:137], v[106:109], v[4:7], v[134:137]
ds_read_b32 v152, v21
ds_read_b32 v95, v24 offset:128
v_mfma_f32_16x16x32_bf16 v[110:113], a[0:3], v[0:3], 0
s_waitcnt lgkmcnt(5)
ds_read_b128 v[62:65], v51 offset:6144
ds_read_b32 v254, v29
v_mfma_f32_16x16x32_bf16 v[134:137], v[106:109], v[96:99], v[134:137]
s_waitcnt lgkmcnt(6)
ds_read_b128 v[66:69], v52 offset:6144
ds_read_b32 v255, v30
v_mfma_f32_16x16x32_bf16 v[134:137], v[106:109], v[100:103], v[134:137]
ds_read_b128 v[198:201], v25 offset:64
ds_read_b64 v[160:161], v22 offset:41472
v_mfma_f32_16x16x32_bf16 v[114:117], a[4:7], v[0:3], 0
s_waitcnt lgkmcnt(9)
ds_read_b128 v[70:73], v51 offset:8192
ds_read_b32 v144, v20
v_mfma_f32_16x16x32_bf16 v[134:137], v[106:109], v[54:57], v[134:137]
s_waitcnt lgkmcnt(10)
ds_read_b128 v[74:77], v52 offset:8192
v_fma_f32 v210, v90, v23, v126
v_mfma_f32_16x16x32_bf16 v[134:137], v[106:109], v[58:61], v[134:137]
v_fma_f32 v211, v91, v23, v127
v_fma_f32 v212, v92, v23, v128
v_mfma_f32_16x16x32_bf16 v[118:121], v[0:3], a[8:11], 0
s_waitcnt lgkmcnt(8)
ds_read_b128 v[78:81], v51 offset:10240
v_fma_f32 v213, v93, v23, v129
v_mfma_f32_16x16x32_bf16 v[134:137], v[106:109], v[62:65], v[134:137]
s_waitcnt lgkmcnt(7)
ds_read_b128 v[82:85], v52 offset:10240
v_cvt_pk_bf16_f32 v150, v210, v211
v_mfma_f32_16x16x32_bf16 v[134:137], v[106:109], v[66:69], v[134:137]
v_cvt_pk_bf16_f32 v151, v212, v213
v_max_f32 v10, v190, v191
v_mfma_f32_16x16x32_bf16 v[110:113], a[12:15], v[4:7], v[110:113]
ds_read_b128 v[162:165], v51 offset:12288
v_mfma_f32_16x16x32_bf16 v[186:189], v[150:153], v[138:141], v[32:35]
s_waitcnt lgkmcnt(7)
v_max_f32 v44, v192, v193
v_fma_f32 v210, v94, v198, v130
s_waitcnt lgkmcnt(5)
v_mfma_f32_16x16x32_bf16 v[134:137], v[106:109], v[70:73], v[134:137]
s_xor_b32 s49, s49, 0x10000
ds_read_b128 v[166:169], v52 offset:12288
s_waitcnt lgkmcnt(4)
v_mfma_f32_16x16x32_bf16 v[134:137], v[106:109], v[74:77], v[134:137]
s_add_u32 m0, s49, 0
v_mul_f32 v95, 0x3eae1a93, v95
v_mul_f32 v8, v254, v95
v_mfma_f32_16x16x32_bf16 v[114:117], a[16:19], v[4:7], v[114:117]
s_waitcnt lgkmcnt(3)
ds_read_b128 v[170:173], v51 offset:14336
v_mul_f32 v9, v255, v95
v_mfma_f32_16x16x32_bf16 v[134:137], v[106:109], v[78:81], v[134:137]
s_waitcnt lgkmcnt(3)
ds_read_b128 v[174:177], v52 offset:14336
v_max3_f32 v10, v186, v187, v10
v_mfma_f32_16x16x32_bf16 v[134:137], v[106:109], v[82:85], v[134:137]
v_max3_f32 v44, v188, v189, v44
v_mul_f32 v10, v10, v8
v_mfma_f32_16x16x32_bf16 v[118:121], v[4:7], a[20:23], v[118:121]
s_waitcnt lgkmcnt(3)
ds_read_b128 v[178:181], v51 offset:16384
v_mul_f32 v44, v44, v9
v_mfma_f32_16x16x32_bf16 v[134:137], v[106:109], v[162:165], v[134:137]
s_waitcnt lgkmcnt(3)
ds_read_b128 v[182:185], v52 offset:16384
v_max_f32 v45, v10, v44
v_mfma_f32_16x16x32_bf16 v[134:137], v[106:109], v[166:169], v[134:137]
v_mov_b32 v46, v45
v_fma_f32 v211, v94, v199, v131
v_mfma_f32_16x16x32_bf16 v[110:113], a[24:27], v[96:99], v[110:113]
s_waitcnt lgkmcnt(3)
global_load_lds_dwordx4 v11, s[42:43] offset:-2048 sc1
v_fma_f32 v212, v94, v200, v132
v_mfma_f32_16x16x32_bf16 v[134:137], v[106:109], v[170:173], v[134:137]
s_waitcnt lgkmcnt(2)
v_permlane16_swap_b32 v45, v46
v_mfma_f32_16x16x32_bf16 v[134:137], v[106:109], v[174:177], v[134:137]
v_max_f32 v45, v45, v46
v_mov_b32 v46, v45
v_mfma_f32_16x16x32_bf16 v[114:117], a[28:31], v[96:99], v[114:117]
s_waitcnt lgkmcnt(1)
v_permlane32_swap_b32 v45, v46
v_mfma_f32_16x16x32_bf16 v[134:137], v[106:109], v[178:181], v[134:137]
s_waitcnt lgkmcnt(0)
v_max_f32 v45, v45, v46
v_sub_f32 v10, v10, v45
v_mfma_f32_16x16x32_bf16 v[134:137], v[106:109], v[182:185], v[134:137]
v_sub_f32 v44, v44, v45
v_fma_f32 v213, v94, v201, v133
v_mfma_f32_16x16x32_bf16 v[118:121], v[96:99], a[32:35], v[118:121]
v_exp_f32 v214, v10
v_mfma_f32_16x16x32_bf16 v[110:113], a[36:39], v[100:103], v[110:113]
v_exp_f32 v215, v44
v_mfma_f32_16x16x32_bf16 v[114:117], a[40:43], v[100:103], v[114:117]
s_nop 0
v_mul_f32 v202, 0x3ae38e39, v134
v_cvt_pk_bf16_f32 v216, -v202, -v202
v_mfma_f32_16x16x32_bf16 v[118:121], v[100:103], a[44:47], v[118:121]
v_lshlrev_b32 v216, 16, v216
global_load_lds_dwordx4 v12, s[42:43] offset:-1024 sc1
v_mfma_f32_16x16x32_bf16 v[110:113], a[48:51], v[54:57], v[110:113]
ds_write_b32 v24, v216 offset:0
v_mfma_f32_16x16x32_bf16 v[114:117], a[52:55], v[54:57], v[114:117]
v_add_f32 v45, v214, v215
v_mov_b32 v46, v45
v_mfma_f32_16x16x32_bf16 v[118:121], v[54:57], a[56:59], v[118:121]
v_mul_f32 v214, v214, v254
v_cvt_pk_bf16_f32 v156, v210, v211
v_mfma_f32_16x16x32_bf16 v[110:113], a[60:63], v[58:61], v[110:113]
v_permlane16_swap_b32 v45, v46
v_mfma_f32_16x16x32_bf16 v[114:117], a[64:67], v[58:61], v[114:117]
v_add_f32 v45, v45, v46
v_mov_b32 v46, v45
v_mfma_f32_16x16x32_bf16 v[118:121], v[58:61], a[68:71], v[118:121]
v_mfma_f32_16x16x32_bf16 v[110:113], a[72:75], v[62:65], v[110:113]
s_waitcnt lgkmcnt(0)
s_barrier
v_mfma_f32_16x16x32_bf16 v[114:117], a[76:79], v[62:65], v[114:117]
global_load_lds_dwordx4 v13, s[42:43] offset:0 sc1
ds_read_b32 v216, v26
v_mfma_f32_16x16x32_bf16 v[118:121], v[62:65], a[80:83], v[118:121]
v_mul_f32 v215, v215, v255
v_cvt_pk_bf16_f32 v157, v212, v213
v_mfma_f32_16x16x32_bf16 v[110:113], a[84:87], v[66:69], v[110:113]
v_permlane32_swap_b32 v45, v46
v_mfma_f32_16x16x32_bf16 v[114:117], a[88:91], v[66:69], v[114:117]
v_add_f32 v45, v45, v46
v_fma_f32 v210, v86, v23, v122
v_mfma_f32_16x16x32_bf16 v[118:121], v[66:69], a[92:95], v[118:121]
v_rcp_f32 v45, v45
v_mfma_f32_16x16x32_bf16 v[110:113], a[96:99], v[70:73], v[110:113]
v_mul_f32 v214, v214, v45
v_mul_f32 v215, v215, v45
v_mfma_f32_16x16x32_bf16 v[114:117], a[100:103], v[70:73], v[114:117]
v_cvt_pk_bf16_f32 v214, v214, v214
v_cvt_pk_bf16_f32 v215, v215, v215
v_mfma_f32_16x16x32_bf16 v[118:121], v[70:73], a[104:107], v[118:121]
v_and_b32 v196, v214, v40
v_and_b32 v197, v215, v41
v_mfma_f32_16x16x32_bf16 v[110:113], a[108:111], v[74:77], v[110:113]
global_load_lds_dwordx4 v14, s[42:43] offset:1024 sc1
s_add_u32 s42, s42, s46
s_addc_u32 s43, s43, s47
v_mfma_f32_16x16x32_bf16 v[114:117], a[112:115], v[74:77], v[114:117]
v_and_b32 v194, v214, v42
v_and_b32 v195, v215, v43
v_mfma_f32_16x16x32_bf16 v[118:121], v[74:77], a[116:119], v[118:121]
s_nop 0
v_mfma_f32_16x16x32_bf16 v[190:193], v[154:157], v[194:197], 0
v_mfma_f32_16x16x32_bf16 v[186:189], v[158:161], v[194:197], 0
s_waitcnt lgkmcnt(0)
v_mul_f32 v216, 0x3ae38e39, v216
v_fma_f32 v203, -v202, v202, v216
v_mfma_f32_16x16x32_bf16 v[110:113], a[120:123], v[78:81], v[110:113]
v_max_f32 v203, 0, v203
v_cvt_pk_bf16_f32 v203, v203, v203
v_mfma_f32_16x16x32_bf16 v[114:117], a[124:127], v[78:81], v[114:117]
v_lshlrev_b32 v203, 16, v203
v_add_f32 v203, 0x3727c5ac, v203
v_mfma_f32_16x16x32_bf16 v[118:121], v[78:81], a[128:131], v[118:121]
v_cvt_pk_bf16_f32 v203, v203, v203
v_lshlrev_b32 v203, 16, v203
v_mfma_f32_16x16x32_bf16 v[110:113], a[132:135], v[82:85], v[110:113]
v_cvt_pk_bf16_f32 v104, v190, v191
v_cvt_pk_bf16_f32 v105, v192, v193
v_mfma_f32_16x16x32_bf16 v[114:117], a[136:139], v[82:85], v[114:117]
ds_write_b64 v18, v[104:105]
v_mfma_f32_16x16x32_bf16 v[118:121], v[82:85], a[140:143], v[118:121]
v_cvt_pk_bf16_f32 v214, v188, v189
v_fma_f32 v211, v87, v23, v123
v_mfma_f32_16x16x32_bf16 v[110:113], a[144:147], v[162:165], v[110:113]
ds_write_b32 v19, v214
v_mfma_f32_16x16x32_bf16 v[114:117], a[148:151], v[162:165], v[114:117]
v_sqrt_f32 v203, v203
v_mfma_f32_16x16x32_bf16 v[118:121], v[162:165], a[152:155], v[118:121]
v_cvt_pk_bf16_f32 v203, v203, v203
v_lshlrev_b32 v203, 16, v203
v_mfma_f32_16x16x32_bf16 v[110:113], a[156:159], v[166:169], v[110:113]
v_rcp_f32 v216, v203
v_mfma_f32_16x16x32_bf16 v[114:117], a[160:163], v[166:169], v[114:117]
ds_write_b32 v24, v216 offset:128
v_mfma_f32_16x16x32_bf16 v[118:121], v[166:169], a[164:167], v[118:121]
v_fma_f32 v212, v88, v23, v124
v_fma_f32 v213, v89, v23, v125
v_mfma_f32_16x16x32_bf16 v[110:113], a[168:171], v[170:173], v[110:113]
v_cvt_pk_bf16_f32 v142, v210, v211
v_cvt_pk_bf16_f32 v143, v212, v213
v_mfma_f32_16x16x32_bf16 v[114:117], a[172:175], v[170:173], v[114:117]
v_mfma_f32_16x16x32_bf16 v[118:121], v[170:173], a[176:179], v[118:121]
v_mfma_f32_16x16x32_bf16 v[110:113], a[180:183], v[174:177], v[110:113]
v_mfma_f32_16x16x32_bf16 v[114:117], a[184:187], v[174:177], v[114:117]
s_waitcnt vmcnt(5) lgkmcnt(0)
s_barrier
v_mfma_f32_16x16x32_bf16 v[118:121], v[174:177], a[188:191], v[118:121]
ds_read_b128 v[0:3], v51 offset:18432
v_mfma_f32_16x16x32_bf16 v[110:113], a[192:195], v[178:181], v[110:113]
ds_read_b128 v[4:7], v52 offset:18432
ds_read_b32 v23, v24 offset:0
v_mfma_f32_16x16x32_bf16 v[114:117], a[196:199], v[178:181], v[114:117]
s_mov_b64 exec, 1
global_store_dword v252, v48, s[38:39] sc1
s_mov_b64 exec, -1
v_mfma_f32_16x16x32_bf16 v[190:193], v[150:153], v[142:145], v[36:39]
v_mfma_f32_16x16x32_bf16 v[118:121], v[178:181], a[200:203], v[118:121]
ds_read_b128 v[96:99], v51 offset:20480
v_add_u32 v48, 1, v48
v_mfma_f32_16x16x32_bf16 v[110:113], a[204:207], v[182:185], v[110:113]
ds_read_b128 v[100:103], v52 offset:20480
v_add_lshl_u32 v252, v48, 0, 7
v_mfma_f32_16x16x32_bf16 v[114:117], a[208:211], v[182:185], v[114:117]
v_bfi_b32 v252, s69, v252, v47
ds_read_b32 v148, v21
v_mfma_f32_16x16x32_bf16 v[118:121], v[182:185], a[212:215], v[118:121]
s_waitcnt lgkmcnt(5)
ds_read_b128 v[54:57], v51 offset:22528
ds_read_b32 v95, v24 offset:192
v_mfma_f32_16x16x32_bf16 v[134:137], v[106:109], v[0:3], 0
s_waitcnt lgkmcnt(6)
ds_read_b128 v[58:61], v52 offset:22528
ds_read_b32 v254, v27
v_mfma_f32_16x16x32_bf16 v[134:137], v[106:109], v[4:7], v[134:137]
ds_read_b32 v255, v28
ds_read_b128 v[198:201], v25 offset:0
v_mfma_f32_16x16x32_bf16 v[122:125], a[0:3], v[0:3], 0
s_waitcnt lgkmcnt(8)
ds_read_b128 v[62:65], v51 offset:24576
ds_read_b64 v[158:159], v22 offset:41472
v_mfma_f32_16x16x32_bf16 v[134:137], v[106:109], v[96:99], v[134:137]
s_waitcnt lgkmcnt(9)
ds_read_b128 v[66:69], v52 offset:24576
ds_read_b32 v140, v20
v_mfma_f32_16x16x32_bf16 v[134:137], v[106:109], v[100:103], v[134:137]
v_fma_f32 v210, v90, v23, v114
v_fma_f32 v211, v91, v23, v115
v_mfma_f32_16x16x32_bf16 v[126:129], a[4:7], v[0:3], 0
s_waitcnt lgkmcnt(9)
ds_read_b128 v[70:73], v51 offset:26624
v_fma_f32 v212, v92, v23, v116
v_mfma_f32_16x16x32_bf16 v[134:137], v[106:109], v[54:57], v[134:137]
s_waitcnt lgkmcnt(8)
ds_read_b128 v[74:77], v52 offset:26624
v_fma_f32 v213, v93, v23, v117
v_mfma_f32_16x16x32_bf16 v[134:137], v[106:109], v[58:61], v[134:137]
v_cvt_pk_bf16_f32 v146, v210, v211
v_cvt_pk_bf16_f32 v147, v212, v213
v_mfma_f32_16x16x32_bf16 v[130:133], v[0:3], a[8:11], 0
ds_read_b128 v[78:81], v51 offset:28672
v_mfma_f32_16x16x32_bf16 v[186:189], v[146:149], v[142:145], v[206:209]
s_waitcnt lgkmcnt(6)
v_max_f32 v10, v190, v191
v_max_f32 v44, v192, v193
v_mfma_f32_16x16x32_bf16 v[134:137], v[106:109], v[62:65], v[134:137]
s_waitcnt lgkmcnt(4)
ds_read_b128 v[82:85], v52 offset:28672
v_fma_f32 v210, v94, v198, v118
v_mfma_f32_16x16x32_bf16 v[134:137], v[106:109], v[66:69], v[134:137]
v_mul_f32 v95, 0x3eae1a93, v95
v_mul_f32 v8, v254, v95
v_mfma_f32_16x16x32_bf16 v[122:125], a[12:15], v[4:7], v[122:125]
s_waitcnt lgkmcnt(3)
ds_read_b128 v[162:165], v51 offset:30720
v_mul_f32 v9, v255, v95
v_mfma_f32_16x16x32_bf16 v[134:137], v[106:109], v[70:73], v[134:137]
s_add_u32 m0, s49, 18432
ds_read_b128 v[166:169], v52 offset:30720
s_waitcnt lgkmcnt(4)
v_mfma_f32_16x16x32_bf16 v[134:137], v[106:109], v[74:77], v[134:137]
v_max3_f32 v10, v186, v187, v10
v_max3_f32 v44, v188, v189, v44
v_mfma_f32_16x16x32_bf16 v[126:129], a[16:19], v[4:7], v[126:129]
s_waitcnt lgkmcnt(3)
ds_read_b128 v[170:173], v51 offset:32768
v_mul_f32 v10, v10, v8
v_mfma_f32_16x16x32_bf16 v[134:137], v[106:109], v[78:81], v[134:137]
s_waitcnt lgkmcnt(3)
ds_read_b128 v[174:177], v52 offset:32768
v_mul_f32 v44, v44, v9
v_mfma_f32_16x16x32_bf16 v[134:137], v[106:109], v[82:85], v[134:137]
v_max_f32 v45, v10, v44
v_mov_b32 v46, v45
v_mfma_f32_16x16x32_bf16 v[130:133], v[4:7], a[20:23], v[130:133]
s_waitcnt lgkmcnt(3)
ds_read_b128 v[178:181], v51 offset:34816
v_fma_f32 v211, v94, v199, v119
v_mfma_f32_16x16x32_bf16 v[134:137], v[106:109], v[162:165], v[134:137]
s_waitcnt lgkmcnt(3)
ds_read_b128 v[182:185], v52 offset:34816
v_fma_f32 v212, v94, v200, v120
v_mfma_f32_16x16x32_bf16 v[134:137], v[106:109], v[166:169], v[134:137]
global_load_lds_dwordx4 v11, s[42:43] offset:-2048 sc1
v_fma_f32 v213, v94, v201, v121
v_mfma_f32_16x16x32_bf16 v[122:125], a[24:27], v[96:99], v[122:125]
s_waitcnt lgkmcnt(3)
v_permlane16_swap_b32 v45, v46
v_mfma_f32_16x16x32_bf16 v[134:137], v[106:109], v[170:173], v[134:137]
s_waitcnt lgkmcnt(2)
v_max_f32 v45, v45, v46
v_mov_b32 v46, v45
v_mfma_f32_16x16x32_bf16 v[134:137], v[106:109], v[174:177], v[134:137]
s_nop 0
v_permlane32_swap_b32 v45, v46
v_mfma_f32_16x16x32_bf16 v[126:129], a[28:31], v[96:99], v[126:129]
s_waitcnt lgkmcnt(1)
v_max_f32 v45, v45, v46
v_sub_f32 v10, v10, v45
v_mfma_f32_16x16x32_bf16 v[134:137], v[106:109], v[178:181], v[134:137]
s_waitcnt lgkmcnt(0)
v_sub_f32 v44, v44, v45
)"
R"(v_cvt_pk_bf16_f32 v154, v210, v211
v_mfma_f32_16x16x32_bf16 v[134:137], v[106:109], v[182:185], v[134:137]
v_exp_f32 v214, v10
v_mfma_f32_16x16x32_bf16 v[130:133], v[96:99], a[32:35], v[130:133]
v_exp_f32 v215, v44
v_mfma_f32_16x16x32_bf16 v[122:125], a[36:39], v[100:103], v[122:125]
global_load_lds_dwordx4 v12, s[42:43] offset:-1024 sc1
v_add_f32 v45, v214, v215
v_mfma_f32_16x16x32_bf16 v[126:129], a[40:43], v[100:103], v[126:129]
s_nop 0
v_mul_f32 v202, 0x3ae38e39, v134
v_cvt_pk_bf16_f32 v216, -v202, -v202
v_mfma_f32_16x16x32_bf16 v[130:133], v[100:103], a[44:47], v[130:133]
v_lshlrev_b32 v216, 16, v216
v_mov_b32 v46, v45
v_mfma_f32_16x16x32_bf16 v[122:125], a[48:51], v[54:57], v[122:125]
ds_write_b32 v24, v216 offset:64
v_mfma_f32_16x16x32_bf16 v[126:129], a[52:55], v[54:57], v[126:129]
v_mul_f32 v214, v214, v254
v_cvt_pk_bf16_f32 v155, v212, v213
v_mfma_f32_16x16x32_bf16 v[130:133], v[54:57], a[56:59], v[130:133]
v_permlane16_swap_b32 v45, v46
v_mfma_f32_16x16x32_bf16 v[122:125], a[60:63], v[58:61], v[122:125]
v_add_f32 v45, v45, v46
v_mov_b32 v46, v45
v_mfma_f32_16x16x32_bf16 v[126:129], a[64:67], v[58:61], v[126:129]
v_mul_f32 v215, v215, v255
v_fma_f32 v210, v86, v23, v110
v_mfma_f32_16x16x32_bf16 v[130:133], v[58:61], a[68:71], v[130:133]
v_mfma_f32_16x16x32_bf16 v[122:125], a[72:75], v[62:65], v[122:125]
s_waitcnt lgkmcnt(0)
s_barrier
v_mfma_f32_16x16x32_bf16 v[126:129], a[76:79], v[62:65], v[126:129]
global_load_lds_dwordx4 v13, s[42:43] offset:0 sc1
ds_read_b32 v216, v26
v_mfma_f32_16x16x32_bf16 v[130:133], v[62:65], a[80:83], v[130:133]
v_permlane32_swap_b32 v45, v46
v_mfma_f32_16x16x32_bf16 v[122:125], a[84:87], v[66:69], v[122:125]
v_add_f32 v45, v45, v46
v_fma_f32 v211, v87, v23, v111
v_mfma_f32_16x16x32_bf16 v[126:129], a[88:91], v[66:69], v[126:129]
v_rcp_f32 v45, v45
v_mfma_f32_16x16x32_bf16 v[130:133], v[66:69], a[92:95], v[130:133]
v_mul_f32 v214, v214, v45
v_mul_f32 v215, v215, v45
v_mfma_f32_16x16x32_bf16 v[122:125], a[96:99], v[70:73], v[122:125]
v_cvt_pk_bf16_f32 v214, v214, v214
v_cvt_pk_bf16_f32 v215, v215, v215
v_mfma_f32_16x16x32_bf16 v[126:129], a[100:103], v[70:73], v[126:129]
v_and_b32 v194, v214, v40
v_and_b32 v195, v215, v41
v_mfma_f32_16x16x32_bf16 v[130:133], v[70:73], a[104:107], v[130:133]
v_and_b32 v196, v214, v42
v_and_b32 v197, v215, v43
v_mfma_f32_16x16x32_bf16 v[122:125], a[108:111], v[74:77], v[122:125]
global_load_lds_dwordx4 v14, s[42:43] offset:1024 sc1
s_sub_u32 s42, s42, 18432
s_subb_u32 s43, s43, 0
v_mfma_f32_16x16x32_bf16 v[190:193], v[154:157], v[194:197], 0
v_mfma_f32_16x16x32_bf16 v[186:189], v[158:161], v[194:197], 0
s_waitcnt lgkmcnt(0)
v_mul_f32 v216, 0x3ae38e39, v216
v_fma_f32 v203, -v202, v202, v216
v_mfma_f32_16x16x32_bf16 v[126:129], a[112:115], v[74:77], v[126:129]
v_max_f32 v203, 0, v203
v_cvt_pk_bf16_f32 v203, v203, v203
v_mfma_f32_16x16x32_bf16 v[130:133], v[74:77], a[116:119], v[130:133]
v_lshlrev_b32 v203, 16, v203
v_add_f32 v203, 0x3727c5ac, v203
v_mfma_f32_16x16x32_bf16 v[122:125], a[120:123], v[78:81], v[122:125]
v_cvt_pk_bf16_f32 v203, v203, v203
v_lshlrev_b32 v203, 16, v203
v_mfma_f32_16x16x32_bf16 v[126:129], a[124:127], v[78:81], v[126:129]
v_cvt_pk_bf16_f32 v104, v190, v191
v_cvt_pk_bf16_f32 v105, v192, v193
v_mfma_f32_16x16x32_bf16 v[130:133], v[78:81], a[128:131], v[130:133]
ds_write_b64 v18, v[104:105]
v_mfma_f32_16x16x32_bf16 v[122:125], a[132:135], v[82:85], v[122:125]
v_cvt_pk_bf16_f32 v214, v188, v189
v_fma_f32 v212, v88, v23, v112
v_mfma_f32_16x16x32_bf16 v[126:129], a[136:139], v[82:85], v[126:129]
ds_write_b32 v19, v214
v_mfma_f32_16x16x32_bf16 v[130:133], v[82:85], a[140:143], v[130:133]
v_sqrt_f32 v203, v203
v_mfma_f32_16x16x32_bf16 v[122:125], a[144:147], v[162:165], v[122:125]
v_cvt_pk_bf16_f32 v203, v203, v203
v_lshlrev_b32 v203, 16, v203
v_mfma_f32_16x16x32_bf16 v[126:129], a[148:151], v[162:165], v[126:129]
v_rcp_f32 v216, v203
v_mfma_f32_16x16x32_bf16 v[130:133], v[162:165], a[152:155], v[130:133]
ds_write_b32 v24, v216 offset:192
v_mfma_f32_16x16x32_bf16 v[122:125], a[156:159], v[166:169], v[122:125]
v_fma_f32 v213, v89, v23, v113
v_cvt_pk_bf16_f32 v138, v210, v211
v_mfma_f32_16x16x32_bf16 v[126:129], a[160:163], v[166:169], v[126:129]
v_cvt_pk_bf16_f32 v139, v212, v213
v_mfma_f32_16x16x32_bf16 v[130:133], v[166:169], a[164:167], v[130:133]
v_mfma_f32_16x16x32_bf16 v[122:125], a[168:171], v[170:173], v[122:125]
v_mfma_f32_16x16x32_bf16 v[126:129], a[172:175], v[170:173], v[126:129]
v_xor_b32 v51, 0x10000, v51
v_xor_b32 v52, 0x10000, v52
v_mfma_f32_16x16x32_bf16 v[130:133], v[170:173], a[176:179], v[130:133]
v_mfma_f32_16x16x32_bf16 v[122:125], a[180:183], v[174:177], v[122:125]
v_mfma_f32_16x16x32_bf16 v[126:129], a[184:187], v[174:177], v[126:129]
s_waitcnt vmcnt(5) lgkmcnt(0)
s_barrier
.Lqa_in72:
v_mfma_f32_16x16x32_bf16 v[130:133], v[174:177], a[188:191], v[130:133]
s_cmp_eq_u32 s70, s73
s_cbranch_scc1 .Lqa_hk73
.Lqa_hr74:
ds_read_b128 v[0:3], v51 offset:0
v_mfma_f32_16x16x32_bf16 v[122:125], a[192:195], v[178:181], v[122:125]
ds_read_b128 v[4:7], v52 offset:0
ds_read_b32 v23, v24 offset:64
v_mfma_f32_16x16x32_bf16 v[126:129], a[196:199], v[178:181], v[126:129]
s_mov_b64 exec, 1
global_store_dword v252, v48, s[38:39] sc1
s_mov_b64 exec, -1
v_mfma_f32_16x16x32_bf16 v[190:193], v[146:149], v[138:141], v[36:39]
v_mfma_f32_16x16x32_bf16 v[130:133], v[178:181], a[200:203], v[130:133]
ds_read_b128 v[96:99], v51 offset:2048
v_add_u32 v48, 1, v48
v_mfma_f32_16x16x32_bf16 v[122:125], a[204:207], v[182:185], v[122:125]
ds_read_b128 v[100:103], v52 offset:2048
v_add_lshl_u32 v252, v48, 0, 7
v_mfma_f32_16x16x32_bf16 v[126:129], a[208:211], v[182:185], v[126:129]
v_mfma_f32_16x16x32_bf16 v[130:133], v[182:185], a[212:215], v[130:133]
s_sub_u32 s70, s70, 1
ds_read_b128 v[54:57], v51 offset:4096
s_cbranch_scc0 .Lqa_top91
s_waitcnt lgkmcnt(0)
v_bfi_b32 v252, s69, v252, v47
ds_read_b32 v152, v21
ds_read_b32 v95, v24 offset:128
ds_read_b32 v254, v29
ds_read_b32 v255, v30
ds_read_b128 v[198:201], v25 offset:64
ds_read_b64 v[160:161], v22 offset:41472
ds_read_b32 v144, v20
v_fma_f32 v210, v90, v23, v126
v_fma_f32 v211, v91, v23, v127
v_fma_f32 v212, v92, v23, v128
v_fma_f32 v213, v93, v23, v129
v_cvt_pk_bf16_f32 v150, v210, v211
v_cvt_pk_bf16_f32 v151, v212, v213
v_max_f32 v10, v190, v191
s_waitcnt lgkmcnt(6)
v_mfma_f32_16x16x32_bf16 v[186:189], v[150:153], v[138:141], v[32:35]
s_waitcnt lgkmcnt(2)
v_max_f32 v44, v192, v193
v_fma_f32 v210, v94, v198, v130
v_mul_f32 v95, 0x3eae1a93, v95
v_mul_f32 v8, v254, v95
v_mul_f32 v9, v255, v95
s_nop 1
v_max3_f32 v10, v186, v187, v10
v_max3_f32 v44, v188, v189, v44
v_mul_f32 v10, v10, v8
v_mul_f32 v44, v44, v9
v_max_f32 v45, v10, v44
v_mov_b32 v46, v45
v_fma_f32 v211, v94, v199, v131
v_fma_f32 v212, v94, v200, v132
v_permlane16_swap_b32 v45, v46
v_max_f32 v45, v45, v46
v_mov_b32 v46, v45
s_nop 1
v_permlane32_swap_b32 v45, v46
v_max_f32 v45, v45, v46
v_sub_f32 v10, v10, v45
v_sub_f32 v44, v44, v45
v_fma_f32 v213, v94, v201, v133
v_exp_f32 v214, v10
v_exp_f32 v215, v44
s_nop 0
v_add_f32 v45, v214, v215
v_mov_b32 v46, v45
v_mul_f32 v214, v214, v254
v_cvt_pk_bf16_f32 v156, v210, v211
v_permlane16_swap_b32 v45, v46
v_add_f32 v45, v45, v46
v_mov_b32 v46, v45
s_waitcnt lgkmcnt(0)
s_barrier
v_mul_f32 v215, v215, v255
v_cvt_pk_bf16_f32 v157, v212, v213
v_permlane32_swap_b32 v45, v46
v_add_f32 v45, v45, v46
v_fma_f32 v210, v86, v23, v122
v_rcp_f32 v45, v45
s_nop 0
v_mul_f32 v214, v214, v45
v_mul_f32 v215, v215, v45
v_cvt_pk_bf16_f32 v214, v214, v214
v_cvt_pk_bf16_f32 v215, v215, v215
v_and_b32 v196, v214, v40
v_and_b32 v197, v215, v41
v_and_b32 v194, v214, v42
v_and_b32 v195, v215, v43
s_nop 1
v_mfma_f32_16x16x32_bf16 v[190:193], v[154:157], v[194:197], 0
v_mfma_f32_16x16x32_bf16 v[186:189], v[158:161], v[194:197], 0
s_nop 6
v_cvt_pk_bf16_f32 v104, v190, v191
v_cvt_pk_bf16_f32 v105, v192, v193
ds_write_b64 v18, v[104:105]
v_cvt_pk_bf16_f32 v214, v188, v189
v_fma_f32 v211, v87, v23, v123
ds_write_b32 v19, v214
v_fma_f32 v212, v88, v23, v124
v_fma_f32 v213, v89, v23, v125
v_cvt_pk_bf16_f32 v142, v210, v211
v_cvt_pk_bf16_f32 v143, v212, v213
s_waitcnt vmcnt(0)
s_waitcnt lgkmcnt(0)
s_barrier
s_mov_b64 exec, 1
global_store_dword v252, v48, s[38:39] sc1
s_mov_b64 exec, -1
s_waitcnt vmcnt(1)
s_barrier
v_add_u32 v48, 8, v48
v_mbcnt_lo_u32_b32 v252, -1, 0
v_mbcnt_hi_u32_b32 v252, -1, v252
v_add_lshl_u32 v252, v252, 0, 7
v_bfi_b32 v252, s69, v252, v47
s_mov_b64 exec, 0xff
global_store_dword v252, v48, s[38:39] sc1
s_mov_b64 exec, -1
s_cmp_lg_u32 s53, 0
s_cbranch_scc1 .Lqa_n93
v_add_lshl_u32 v253, v50, 0, 7
v_bfi_b32 v253, s69, v253, v49
global_load_dword v53, v253, s[38:39] sc1
s_waitcnt vmcnt(0)
v_sub_u32 v53, v53, v50
v_cmp_lt_u32 vcc, 0x7f, v53
s_cbranch_vccnz .Lqa_sp94
.Lqa_rt95:
v_add_u32 v49, 118784, v49
v_add_lshl_u32 v253, v50, 0, 7
v_bfi_b32 v253, s69, v253, v49
global_load_dword v53, v253, s[38:39] sc1
s_waitcnt vmcnt(0)
v_sub_u32 v53, v53, v50
v_cmp_lt_u32 vcc, 0x7f, v53
s_cbranch_vccnz .Lqa_sp96
.Lqa_rt97:
v_add_u32 v49, 118784, v49
v_add_lshl_u32 v253, v50, 0, 7
v_bfi_b32 v253, s69, v253, v49
global_load_dword v53, v253, s[38:39] sc1
s_waitcnt vmcnt(0)
v_sub_u32 v53, v53, v50
v_cmp_lt_u32 vcc, 0x7f, v53
s_cbranch_vccnz .Lqa_sp98
.Lqa_rt99:
v_add_u32 v49, 118784, v49
v_add_lshl_u32 v253, v50, 0, 7
v_bfi_b32 v253, s69, v253, v49
global_load_dword v53, v253, s[38:39] sc1
s_waitcnt vmcnt(0)
v_sub_u32 v53, v53, v50
v_cmp_lt_u32 vcc, 0x7f, v53
s_cbranch_vccnz .Lqa_sp100
.Lqa_rt101:
v_add_u32 v49, 118784, v49
s_load_dword s75, s[38:39], 0x0
s_waitcnt vmcnt(0) lgkmcnt(0)
s_add_u32 s75, s75, 1
v_mov_b32 v53, s75
v_mov_b32 v49, 0
global_store_dword v49, v53, s[38:39] sc1
.Lqa_n93:
s_endpgm
.Lqa_hk6:
s_setpc_b64 s[90:91]
.Lqa_k15:
s_branch .Lqa_k16
.Lqa_e8:
s_add_u32 s42, s42, s50
s_addc_u32 s43, s43, 0
s_mov_b64 s[76:77], -1
s_mov_b64 s[46:47], s[44:45]
s_mov_b32 s73, 17
s_add_u32 s90, s90, .Lqa_e9-.Lqa_e8
s_addc_u32 s91, s91, 0
s_branch .Lqa_hr7
.Lqa_k16:
s_branch .Lqa_k17
.Lqa_e9:
s_mov_b32 s46, -18432
s_mov_b32 s47, -1
v_or_b32 v234, s52, v234
v_or_b32 v235, s52, v235
v_or_b32 v236, s52, v236
v_or_b32 v237, s52, v237
s_mov_b32 s73, 16
s_add_u32 s90, s90, .Lqa_e10-.Lqa_e9
s_addc_u32 s91, s91, 0
s_branch .Lqa_hr7
.Lqa_k17:
s_branch .Lqa_k18
.Lqa_e10:
v_mov_b32 v234, v32
v_mov_b32 v235, v33
v_mov_b32 v236, v34
v_mov_b32 v237, v35
s_mov_b32 s73, 15
s_add_u32 s90, s90, .Lqa_e11-.Lqa_e10
s_addc_u32 s91, s91, 0
s_branch .Lqa_hr7
.Lqa_k18:
s_branch .Lqa_k19
.Lqa_e11:
v_subrev_u32 v16, 18432, v16
s_mov_b32 s73, 3
s_add_u32 s90, s90, .Lqa_e12-.Lqa_e11
s_addc_u32 s91, s91, 0
s_branch .Lqa_hr7
.Lqa_k19:
s_branch .Lqa_k20
.Lqa_e12:
s_mov_b32 s73, 2
s_add_u32 s90, s90, .Lqa_e13-.Lqa_e12
s_addc_u32 s91, s91, 0
s_branch .Lqa_hr7
.Lqa_k20:
s_branch .Lqa_k21
.Lqa_e13:
s_add_u32 s46, s46, s50
s_addc_u32 s47, s47, 0
s_mov_b32 s73, 0
s_add_u32 s90, s90, .Lqa_e14-.Lqa_e13
s_addc_u32 s91, s91, 0
s_branch .Lqa_hr7
.Lqa_k21:
s_branch .Lqa_bk22
.Lqa_e14:
v_or_b32 v32, s52, v32
v_or_b32 v33, s52, v33
v_or_b32 v34, s52, v34
v_or_b32 v35, s52, v35
s_mov_b32 s73, 999
s_nop 0
s_addc_u32 s91, s91, 0
s_branch .Lqa_hr7
.Lqa_hk27:
s_setpc_b64 s[90:91]
.Lqa_k36:
s_branch .Lqa_k37
.Lqa_e29:
s_add_u32 s42, s42, s50
s_addc_u32 s43, s43, 0
s_mov_b64 s[76:77], -1
s_mov_b64 s[46:47], s[44:45]
s_mov_b32 s73, 17
s_add_u32 s90, s90, .Lqa_e30-.Lqa_e29
s_addc_u32 s91, s91, 0
s_branch .Lqa_hr28
.Lqa_k37:
s_branch .Lqa_k38
.Lqa_e30:
s_mov_b32 s46, -18432
s_mov_b32 s47, -1
v_or_b32 v234, s52, v234
v_or_b32 v235, s52, v235
v_or_b32 v236, s52, v236
v_or_b32 v237, s52, v237
s_mov_b32 s73, 16
s_add_u32 s90, s90, .Lqa_e31-.Lqa_e30
s_addc_u32 s91, s91, 0
s_branch .Lqa_hr28
.Lqa_k38:
s_branch .Lqa_k39
.Lqa_e31:
v_mov_b32 v234, v32
v_mov_b32 v235, v33
v_mov_b32 v236, v34
v_mov_b32 v237, v35
s_mov_b32 s73, 15
s_add_u32 s90, s90, .Lqa_e32-.Lqa_e31
s_addc_u32 s91, s91, 0
s_branch .Lqa_hr28
.Lqa_k39:
s_branch .Lqa_k40
.Lqa_e32:
v_subrev_u32 v16, 18432, v16
s_mov_b32 s73, 3
s_add_u32 s90, s90, .Lqa_e33-.Lqa_e32
s_addc_u32 s91, s91, 0
s_branch .Lqa_hr28
.Lqa_k40:
s_branch .Lqa_k41
.Lqa_e33:
s_mov_b32 s73, 2
s_add_u32 s90, s90, .Lqa_e34-.Lqa_e33
s_addc_u32 s91, s91, 0
s_branch .Lqa_hr28
)"
R"(.Lqa_k41:
s_branch .Lqa_k42
.Lqa_e34:
s_add_u32 s46, s46, s50
s_addc_u32 s47, s47, 0
s_mov_b32 s73, 0
s_add_u32 s90, s90, .Lqa_e35-.Lqa_e34
s_addc_u32 s91, s91, 0
s_branch .Lqa_hr28
.Lqa_k42:
s_branch .Lqa_bk43
.Lqa_e35:
v_or_b32 v32, s52, v32
v_or_b32 v33, s52, v33
v_or_b32 v34, s52, v34
v_or_b32 v35, s52, v35
s_mov_b32 s73, 999
s_nop 0
s_addc_u32 s91, s91, 0
s_branch .Lqa_hr28
.Lqa_sp50:
s_sleep 1
v_add_lshl_u32 v253, v46, 0, 7
v_bfi_b32 v253, s69, v253, v44
global_load_dword v53, v253, s[38:39] sc1
s_add_u32 s71, s71, 1
s_waitcnt vmcnt(0)
v_sub_u32 v53, v53, v46
v_cmp_lt_u32 vcc, 0x7f, v53
s_and_b64 vcc, vcc, s[76:77]
s_cbranch_vccz .Lqa_rt51
s_cmp_lt_u32 s71, 3000000
s_cbranch_scc1 .Lqa_sp50
s_branch .Lqa_rt51
.Lqa_sp52:
s_sleep 1
v_add_lshl_u32 v253, v46, 0, 7
v_bfi_b32 v253, s69, v253, v44
global_load_dword v254, v253, s[38:39] sc1
s_add_u32 s71, s71, 1
s_waitcnt vmcnt(0)
v_sub_u32 v254, v254, v46
v_cmp_lt_u32 vcc, 0x7f, v254
s_and_b64 vcc, vcc, s[76:77]
s_cbranch_vccz .Lqa_rt53
s_cmp_lt_u32 s71, 3000000
s_cbranch_scc1 .Lqa_sp52
s_branch .Lqa_rt53
.Lqa_hk48:
s_setpc_b64 s[90:91]
.Lqa_k61:
s_branch .Lqa_k62
.Lqa_e54:
s_add_u32 s42, s42, s50
s_addc_u32 s43, s43, 0
s_mov_b64 s[76:77], -1
s_mov_b64 s[46:47], s[44:45]
s_mov_b32 s73, 17
s_add_u32 s90, s90, .Lqa_e55-.Lqa_e54
s_addc_u32 s91, s91, 0
s_branch .Lqa_hr49
.Lqa_k62:
s_branch .Lqa_k63
.Lqa_e55:
s_mov_b32 s46, -18432
s_mov_b32 s47, -1
v_or_b32 v202, s52, v202
v_or_b32 v203, s52, v203
v_or_b32 v204, s52, v204
v_or_b32 v205, s52, v205
s_mov_b32 s73, 16
s_add_u32 s90, s90, .Lqa_e56-.Lqa_e55
s_addc_u32 s91, s91, 0
s_branch .Lqa_hr49
.Lqa_k63:
s_branch .Lqa_k64
.Lqa_e56:
v_mov_b32 v202, v32
v_mov_b32 v203, v33
v_mov_b32 v204, v34
v_mov_b32 v205, v35
s_mov_b32 s73, 15
s_add_u32 s90, s90, .Lqa_e57-.Lqa_e56
s_addc_u32 s91, s91, 0
s_branch .Lqa_hr49
.Lqa_k64:
s_branch .Lqa_k65
.Lqa_e57:
v_subrev_u32 v16, 18432, v16
s_mov_b32 s73, 3
s_add_u32 s90, s90, .Lqa_e58-.Lqa_e57
s_addc_u32 s91, s91, 0
s_branch .Lqa_hr49
.Lqa_k65:
s_branch .Lqa_k66
.Lqa_e58:
v_add_u32 v45, s51, v45
s_mov_b32 s73, 2
s_add_u32 s90, s90, .Lqa_e59-.Lqa_e58
s_addc_u32 s91, s91, 0
s_branch .Lqa_hr49
.Lqa_k66:
s_branch .Lqa_k67
.Lqa_e59:
v_add_u32 v44, s51, v44
v_add_u32 v46, -32, v46
s_add_u32 s46, s46, s50
s_addc_u32 s47, s47, 0
s_mov_b32 s73, 0
s_add_u32 s90, s90, .Lqa_e60-.Lqa_e59
s_addc_u32 s91, s91, 0
s_branch .Lqa_hr49
.Lqa_k67:
s_branch .Lqa_bk68
.Lqa_e60:
v_or_b32 v32, s52, v32
v_or_b32 v33, s52, v33
v_or_b32 v34, s52, v34
v_or_b32 v35, s52, v35
s_mov_b32 s73, 999
s_nop 0
s_addc_u32 s91, s91, 0
s_branch .Lqa_hr49
.Lqa_hk73:
s_setpc_b64 s[90:91]
.Lqa_k82:
s_branch .Lqa_k83
.Lqa_e75:
s_add_u32 s42, s42, s50
s_addc_u32 s43, s43, 0
s_mov_b64 s[76:77], -1
s_mov_b64 s[46:47], s[44:45]
s_mov_b32 s73, 17
s_add_u32 s90, s90, .Lqa_e76-.Lqa_e75
s_addc_u32 s91, s91, 0
s_branch .Lqa_hr74
.Lqa_k83:
s_branch .Lqa_k84
.Lqa_e76:
s_mov_b32 s46, -18432
s_mov_b32 s47, -1
v_or_b32 v206, s52, v206
v_or_b32 v207, s52, v207
v_or_b32 v208, s52, v208
v_or_b32 v209, s52, v209
s_mov_b32 s73, 16
s_add_u32 s90, s90, .Lqa_e77-.Lqa_e76
s_addc_u32 s91, s91, 0
s_branch .Lqa_hr74
.Lqa_k84:
s_branch .Lqa_k85
.Lqa_e77:
v_mov_b32 v206, v32
v_mov_b32 v207, v33
v_mov_b32 v208, v34
v_mov_b32 v209, v35
s_mov_b32 s73, 15
s_add_u32 s90, s90, .Lqa_e78-.Lqa_e77
s_addc_u32 s91, s91, 0
s_branch .Lqa_hr74
.Lqa_k85:
s_branch .Lqa_k86
.Lqa_e78:
v_add_u32 v48, -1, v48
v_add_lshl_u32 v252, v48, 0, 7
v_bfi_b32 v252, s69, v252, v47
s_mov_b32 s73, 3
s_add_u32 s90, s90, .Lqa_e79-.Lqa_e78
s_addc_u32 s91, s91, 0
s_branch .Lqa_hr74
.Lqa_k86:
s_branch .Lqa_k87
.Lqa_e79:
s_mov_b32 s73, 2
s_add_u32 s90, s90, .Lqa_e80-.Lqa_e79
s_addc_u32 s91, s91, 0
s_branch .Lqa_hr74
.Lqa_k87:
s_branch .Lqa_k88
.Lqa_e80:
s_add_u32 s46, s46, s50
s_addc_u32 s47, s47, 0
s_mov_b32 s73, 0
s_add_u32 s90, s90, .Lqa_e81-.Lqa_e80
s_addc_u32 s91, s91, 0
s_branch .Lqa_hr74
.Lqa_k88:
s_branch .Lqa_bk89
.Lqa_e81:
v_or_b32 v32, s52, v32
v_or_b32 v33, s52, v33
v_or_b32 v34, s52, v34
v_or_b32 v35, s52, v35
s_mov_b32 s73, 999
s_nop 0
s_addc_u32 s91, s91, 0
s_branch .Lqa_hr74
.Lqa_sp94:
s_sleep 1
v_add_lshl_u32 v253, v50, 0, 7
v_bfi_b32 v253, s69, v253, v49
global_load_dword v53, v253, s[38:39] sc1
s_add_u32 s71, s71, 1
s_waitcnt vmcnt(0)
v_sub_u32 v53, v53, v50
v_cmp_lt_u32 vcc, 0x7f, v53
s_cbranch_vccz .Lqa_rt95
s_cmp_lt_u32 s71, 3000000
s_cbranch_scc1 .Lqa_sp94
s_branch .Lqa_rt95
.Lqa_sp96:
s_sleep 1
v_add_lshl_u32 v253, v50, 0, 7
v_bfi_b32 v253, s69, v253, v49
global_load_dword v53, v253, s[38:39] sc1
s_add_u32 s71, s71, 1
s_waitcnt vmcnt(0)
v_sub_u32 v53, v53, v50
v_cmp_lt_u32 vcc, 0x7f, v53
s_cbranch_vccz .Lqa_rt97
s_cmp_lt_u32 s71, 3000000
s_cbranch_scc1 .Lqa_sp96
s_branch .Lqa_rt97
.Lqa_sp98:
s_sleep 1
v_add_lshl_u32 v253, v50, 0, 7
v_bfi_b32 v253, s69, v253, v49
global_load_dword v53, v253, s[38:39] sc1
s_add_u32 s71, s71, 1
s_waitcnt vmcnt(0)
v_sub_u32 v53, v53, v50
v_cmp_lt_u32 vcc, 0x7f, v53
s_cbranch_vccz .Lqa_rt99
s_cmp_lt_u32 s71, 3000000
s_cbranch_scc1 .Lqa_sp98
s_branch .Lqa_rt99
.Lqa_sp100:
s_sleep 1
v_add_lshl_u32 v253, v50, 0, 7
v_bfi_b32 v253, s69, v253, v49
global_load_dword v53, v253, s[38:39] sc1
s_add_u32 s71, s71, 1
s_waitcnt vmcnt(0)
v_sub_u32 v53, v53, v50
v_cmp_lt_u32 vcc, 0x7f, v53
s_cbranch_vccz .Lqa_rt101
s_cmp_lt_u32 s71, 3000000
s_cbranch_scc1 .Lqa_sp100
s_branch .Lqa_rt101
.Le0:
)"
    :: "{v[0:31]}"(va), "{v[32:63]}"(vb), "{s[36:51]}"(sa), "{s[52:67]}"(sb) : "v255", "a255", "s99", "vcc", "memory");
    return;
  }
  if (wave == 2) {
    const unsigned o = min((lane & 7) * 1048576u, 5308412u);
    touch(wg + o); touch(wu + o); touch(wd + o); touch(out + min(o, 2359292u));
    touch(ws + min(lane * 2097152u, WS_END - 4));
    if (blockIdx.x - 64 < 464 * FL / 8192) touch(ws + WS_FL + ((blockIdx.x - 64) * 64 + lane) * 128);
  }
  if (ev) touch(wo + min((jj - 8) * 216 + threadIdx.x, 5183u) * 128);
  if (!ev && wave != 2) touch(x0 + min((xh * 32 + jj) * 144 + min(wave, 2u) * 64 + lane, 18431u) * 128);
  if (ev && jj < 10) {
    const unsigned wo_rho = xc >> 1, wo_cw = jj & 1;
    const unsigned r = lane & 15, g = lane >> 4, q = r >> 2, p = lane & 3;
    const unsigned rho = wo_rho, cw = wo_cw, row0 = rho * RP + PAD, pc0 = wave * 5;
    auto wv = [&](unsigned j) { const unsigned b = j * 1024 + lane * 16, rw = b / 576; return rw * 576 + b + 4096 - (j & 7) * 1024; };
    u32x32 va; u32x16 sa;
    va[0] = wv(0);
    va[1] = wv(1);
    va[2] = wv(2);
    va[3] = wv(3);
    va[4] = wv(4);
    va[5] = wv(5);
    va[6] = wv(6);
    va[7] = wv(7);
    va[8] = wv(8);
    va[9] = (8 * g + q) * 576 + cw * 64 + wave * 128 + p * 16;
    va[10] = (8 * g + q) * 576 + (cw ? 0 : 512) + p * 16 + (wave >> 1) * 8;
    va[11] = WS_O + (row0 - 2) * XT + pc0 * 1024 + 2048 + lane * 16;
    va[12] = WS_O + (row0 + 63) * XT + pc0 * 1024 + 2048 + lane * 16;
    va[13] = WS_X1 + (row0 - 5) * X1P + (cw * 10 + wave * 2) * 1024 + lane * 16;
    va[14] = WS_X1 + (row0 - 5) * X1P + (8 + cw) * 1024 + lane * 16;
    va[15] = WS_X1 + (row0 - 7) * X1P + XT + cw * 128 + r * 8;
    va[16] = ((rho * 32 + 31) * 16 + 0 + (lane >> 3)) * 1152 + (cw * 5 + wave) * 128 + (((lane ^ (lane >> 3)) & 7) << 4) - 0;
    va[17] = ((rho * 32 + 31) * 16 + 8 + (lane >> 3)) * 1152 + (cw * 5 + wave) * 128 + (((lane ^ (lane >> 3)) & 7) << 4) - 1024;
    va[18] = ((rho * 32 + 31) * 16 + r) * 1152 + (8 + cw) * 64 + g * 16;
    va[19] = 47104 + wave * 2048 + r * 128 + (((0 + g) ^ (r & 7)) << 4);
    va[20] = 47104 + wave * 2048 + r * 128 + (((4 + g) ^ (r & 7)) << 4);
    va[21] = WS_FL + rho * FR + FO + (lane & 7) * FL;
    va[22] = 0u - 2;
    va[23] = WS_FL + rho * FR + FX1 + cw * FL;
    va[24] = 0u - 6;
    va[25] = lane * 16;
    va[26] = lane * 16;
    va[27] = lane * 8;
    sa[0] = 4 + wave; sa[2] = (unsigned)(unsigned long long)ws; sa[3] = (unsigned)((unsigned long long)ws >> 32);
    sa[4] = uni((unsigned)(unsigned long long)(wo + wave * 16 * 1152 + cw * 576));
    sa[5] = uni((unsigned)((unsigned long long)(wo + wave * 16 * 1152 + cw * 576) >> 32));
    sa[6] = uni((unsigned)(unsigned long long)(x0));
    sa[7] = uni((unsigned)((unsigned long long)(x0) >> 32));
    sa[8] = uni((unsigned)(unsigned long long)(x0));
    sa[9] = uni((unsigned)((unsigned long long)(x0) >> 32));
    sa[10] = uni((unsigned)(unsigned long long)(ws + WS_X2));
    sa[11] = uni((unsigned)((unsigned long long)(ws + WS_X2) >> 32));
    sa[12] = uni(wave * 9216);
    sa[13] = uni(pc0 * 1024 + 2048);
    asm volatile(
R"(.Lb1:
s_movk_i32 s69, 0x380
s_mov_b32 s71, 0
s_mov_b32 s74, 0
s_load_dword s37, s[38:39], 0x0
s_nop 1
s_and_b32 s75, s36, 3
s_cmp_eq_u32 s75, 1
s_cbranch_scc1 .Lwo_w2
s_cmp_eq_u32 s75, 2
s_cbranch_scc1 .Lwo_w3
s_cmp_eq_u32 s75, 3
s_cbranch_scc1 .Lwo_w4
.Lwo_w1:
s_add_u32 m0, s48, 4096
s_nop 0
global_load_lds_dwordx4 v0, s[40:41] offset:-4096
global_load_lds_dwordx4 v1, s[40:41] offset:-3072
global_load_lds_dwordx4 v2, s[40:41] offset:-2048
global_load_lds_dwordx4 v3, s[40:41] offset:-1024
global_load_lds_dwordx4 v4, s[40:41] offset:0
global_load_lds_dwordx4 v5, s[40:41] offset:1024
global_load_lds_dwordx4 v6, s[40:41] offset:2048
global_load_lds_dwordx4 v7, s[40:41] offset:3072
s_add_u32 m0, s48, 12288
s_nop 0
global_load_lds_dwordx4 v8, s[40:41] offset:-4096
s_add_u32 s40, s40, 73728
s_addc_u32 s41, s41, 0
s_add_u32 m0, s48, 40960
s_nop 0
global_load_lds_dwordx4 v0, s[40:41] offset:-4096
global_load_lds_dwordx4 v1, s[40:41] offset:-3072
global_load_lds_dwordx4 v2, s[40:41] offset:-2048
global_load_lds_dwordx4 v3, s[40:41] offset:-1024
global_load_lds_dwordx4 v4, s[40:41] offset:0
global_load_lds_dwordx4 v5, s[40:41] offset:1024
global_load_lds_dwordx4 v6, s[40:41] offset:2048
global_load_lds_dwordx4 v7, s[40:41] offset:3072
s_add_u32 m0, s48, 49152
s_nop 0
global_load_lds_dwordx4 v8, s[40:41] offset:-4096
s_add_u32 s40, s40, 73728
s_addc_u32 s41, s41, 0
s_add_u32 m0, s48, 77824
s_nop 0
global_load_lds_dwordx4 v0, s[40:41] offset:-4096
global_load_lds_dwordx4 v1, s[40:41] offset:-3072
global_load_lds_dwordx4 v2, s[40:41] offset:-2048
global_load_lds_dwordx4 v3, s[40:41] offset:-1024
global_load_lds_dwordx4 v4, s[40:41] offset:0
global_load_lds_dwordx4 v5, s[40:41] offset:1024
global_load_lds_dwordx4 v6, s[40:41] offset:2048
global_load_lds_dwordx4 v7, s[40:41] offset:3072
s_add_u32 m0, s48, 86016
s_nop 0
global_load_lds_dwordx4 v8, s[40:41] offset:-4096
s_add_u32 s40, s40, 73728
s_addc_u32 s41, s41, 0
v_add_u32 v204, 0x10000, v9
v_add_u32 v205, 0x20000, v9
v_add_u32 v206, 0x10000, v10
v_add_u32 v207, 0x20000, v10
s_waitcnt lgkmcnt(0)
s_mul_i32 s37, s37, 0x9E3779B1
s_add_u32 s37, s37, 0x7F4A7C15
v_add_u32 v22, s37, v22
v_add_u32 v24, s37, v24
s_waitcnt vmcnt(18)
s_barrier
s_add_u32 m0, s48, 114688
ds_read_b64_tr_b16 a[0:1], v9 offset:0
ds_read_b64_tr_b16 a[2:3], v9 offset:2304
global_load_lds_dwordx4 v0, s[40:41] offset:-4096
ds_read_b64_tr_b16 a[4:5], v9 offset:8
ds_read_b64_tr_b16 a[6:7], v9 offset:2312
global_load_lds_dwordx4 v1, s[40:41] offset:-3072
ds_read_b64_tr_b16 a[8:9], v9 offset:64
global_load_lds_dwordx4 v2, s[40:41] offset:-2048
ds_read_b64_tr_b16 a[10:11], v9 offset:2368
ds_read_b64_tr_b16 a[12:13], v9 offset:72
global_load_lds_dwordx4 v3, s[40:41] offset:-1024
ds_read_b64_tr_b16 a[14:15], v9 offset:2376
global_load_lds_dwordx4 v4, s[40:41] offset:0
ds_read_b64_tr_b16 v[60:61], v10 offset:0
ds_read_b64_tr_b16 v[62:63], v10 offset:2304
global_load_lds_dwordx4 v5, s[40:41] offset:1024
ds_read_b64_tr_b16 a[16:17], v9 offset:18432
global_load_lds_dwordx4 v6, s[40:41] offset:2048
ds_read_b64_tr_b16 a[18:19], v9 offset:20736
ds_read_b64_tr_b16 a[20:21], v9 offset:18440
global_load_lds_dwordx4 v7, s[40:41] offset:3072
ds_read_b64_tr_b16 a[22:23], v9 offset:20744
s_add_u32 m0, s48, 122880
ds_read_b64_tr_b16 a[24:25], v9 offset:18496
ds_read_b64_tr_b16 a[26:27], v9 offset:20800
global_load_lds_dwordx4 v8, s[40:41] offset:-4096
ds_read_b64_tr_b16 a[28:29], v9 offset:18504
s_add_u32 s40, s40, 73728
ds_read_b64_tr_b16 a[30:31], v9 offset:20808
ds_read_b64_tr_b16 v[64:65], v10 offset:18432
s_addc_u32 s41, s41, 0
ds_read_b64_tr_b16 v[66:67], v10 offset:20736
s_waitcnt vmcnt(18) lgkmcnt(0)
s_barrier
s_add_u32 m0, s48, 4096
ds_read_b64_tr_b16 a[32:33], v9 offset:36864
ds_read_b64_tr_b16 a[34:35], v9 offset:39168
global_load_lds_dwordx4 v0, s[40:41] offset:-4096
ds_read_b64_tr_b16 a[36:37], v9 offset:36872
ds_read_b64_tr_b16 a[38:39], v9 offset:39176
global_load_lds_dwordx4 v1, s[40:41] offset:-3072
ds_read_b64_tr_b16 a[40:41], v9 offset:36928
global_load_lds_dwordx4 v2, s[40:41] offset:-2048
ds_read_b64_tr_b16 a[42:43], v9 offset:39232
ds_read_b64_tr_b16 a[44:45], v9 offset:36936
global_load_lds_dwordx4 v3, s[40:41] offset:-1024
ds_read_b64_tr_b16 a[46:47], v9 offset:39240
global_load_lds_dwordx4 v4, s[40:41] offset:0
ds_read_b64_tr_b16 v[68:69], v10 offset:36864
ds_read_b64_tr_b16 v[70:71], v10 offset:39168
global_load_lds_dwordx4 v5, s[40:41] offset:1024
ds_read_b64_tr_b16 a[48:49], v9 offset:55296
global_load_lds_dwordx4 v6, s[40:41] offset:2048
ds_read_b64_tr_b16 a[50:51], v9 offset:57600
ds_read_b64_tr_b16 a[52:53], v9 offset:55304
global_load_lds_dwordx4 v7, s[40:41] offset:3072
ds_read_b64_tr_b16 a[54:55], v9 offset:57608
s_add_u32 m0, s48, 12288
ds_read_b64_tr_b16 a[56:57], v9 offset:55360
ds_read_b64_tr_b16 a[58:59], v9 offset:57664
global_load_lds_dwordx4 v8, s[40:41] offset:-4096
ds_read_b64_tr_b16 a[60:61], v9 offset:55368
s_add_u32 s40, s40, 73728
ds_read_b64_tr_b16 a[62:63], v9 offset:57672
ds_read_b64_tr_b16 v[72:73], v10 offset:55296
s_addc_u32 s41, s41, 0
ds_read_b64_tr_b16 v[74:75], v10 offset:57600
s_waitcnt vmcnt(18) lgkmcnt(0)
s_barrier
s_add_u32 m0, s48, 40960
ds_read_b64_tr_b16 a[64:65], v204 offset:8192
ds_read_b64_tr_b16 a[66:67], v204 offset:10496
global_load_lds_dwordx4 v0, s[40:41] offset:-4096
ds_read_b64_tr_b16 a[68:69], v204 offset:8200
ds_read_b64_tr_b16 a[70:71], v204 offset:10504
global_load_lds_dwordx4 v1, s[40:41] offset:-3072
ds_read_b64_tr_b16 a[72:73], v204 offset:8256
global_load_lds_dwordx4 v2, s[40:41] offset:-2048
ds_read_b64_tr_b16 a[74:75], v204 offset:10560
ds_read_b64_tr_b16 a[76:77], v204 offset:8264
global_load_lds_dwordx4 v3, s[40:41] offset:-1024
ds_read_b64_tr_b16 a[78:79], v204 offset:10568
global_load_lds_dwordx4 v4, s[40:41] offset:0
ds_read_b64_tr_b16 v[76:77], v206 offset:8192
ds_read_b64_tr_b16 v[78:79], v206 offset:10496
global_load_lds_dwordx4 v5, s[40:41] offset:1024
ds_read_b64_tr_b16 a[80:81], v204 offset:26624
global_load_lds_dwordx4 v6, s[40:41] offset:2048
ds_read_b64_tr_b16 a[82:83], v204 offset:28928
ds_read_b64_tr_b16 a[84:85], v204 offset:26632
global_load_lds_dwordx4 v7, s[40:41] offset:3072
ds_read_b64_tr_b16 a[86:87], v204 offset:28936
s_add_u32 m0, s48, 49152
ds_read_b64_tr_b16 a[88:89], v204 offset:26688
ds_read_b64_tr_b16 a[90:91], v204 offset:28992
global_load_lds_dwordx4 v8, s[40:41] offset:-4096
ds_read_b64_tr_b16 a[92:93], v204 offset:26696
s_add_u32 s40, s40, 73728
ds_read_b64_tr_b16 a[94:95], v204 offset:29000
ds_read_b64_tr_b16 v[80:81], v206 offset:26624
s_addc_u32 s41, s41, 0
ds_read_b64_tr_b16 v[82:83], v206 offset:28928
s_waitcnt vmcnt(18) lgkmcnt(0)
s_barrier
s_add_u32 m0, s48, 77824
ds_read_b64_tr_b16 a[96:97], v204 offset:45056
ds_read_b64_tr_b16 a[98:99], v204 offset:47360
global_load_lds_dwordx4 v0, s[40:41] offset:-4096
ds_read_b64_tr_b16 a[100:101], v204 offset:45064
ds_read_b64_tr_b16 a[102:103], v204 offset:47368
global_load_lds_dwordx4 v1, s[40:41] offset:-3072
ds_read_b64_tr_b16 a[104:105], v204 offset:45120
global_load_lds_dwordx4 v2, s[40:41] offset:-2048
ds_read_b64_tr_b16 a[106:107], v204 offset:47424
ds_read_b64_tr_b16 a[108:109], v204 offset:45128
global_load_lds_dwordx4 v3, s[40:41] offset:-1024
ds_read_b64_tr_b16 a[110:111], v204 offset:47432
global_load_lds_dwordx4 v4, s[40:41] offset:0
ds_read_b64_tr_b16 v[84:85], v206 offset:45056
ds_read_b64_tr_b16 v[86:87], v206 offset:47360
global_load_lds_dwordx4 v5, s[40:41] offset:1024
ds_read_b64_tr_b16 a[112:113], v204 offset:63488
global_load_lds_dwordx4 v6, s[40:41] offset:2048
ds_read_b64_tr_b16 a[114:115], v205 offset:256
ds_read_b64_tr_b16 a[116:117], v204 offset:63496
global_load_lds_dwordx4 v7, s[40:41] offset:3072
ds_read_b64_tr_b16 a[118:119], v205 offset:264
s_add_u32 m0, s48, 86016
ds_read_b64_tr_b16 a[120:121], v204 offset:63552
ds_read_b64_tr_b16 a[122:123], v205 offset:320
global_load_lds_dwordx4 v8, s[40:41] offset:-4096
ds_read_b64_tr_b16 a[124:125], v204 offset:63560
s_add_u32 s40, s40, 73728
ds_read_b64_tr_b16 a[126:127], v205 offset:328
ds_read_b64_tr_b16 v[88:89], v206 offset:63488
s_addc_u32 s41, s41, 0
ds_read_b64_tr_b16 v[90:91], v207 offset:256
s_waitcnt vmcnt(18) lgkmcnt(0)
s_barrier
s_add_u32 m0, s48, 114688
ds_read_b64_tr_b16 a[128:129], v9 offset:0
ds_read_b64_tr_b16 a[130:131], v9 offset:2304
global_load_lds_dwordx4 v0, s[40:41] offset:-4096
ds_read_b64_tr_b16 a[132:133], v9 offset:8
global_load_lds_dwordx4 v1, s[40:41] offset:-3072
ds_read_b64_tr_b16 a[134:135], v9 offset:2312
ds_read_b64_tr_b16 a[136:137], v9 offset:64
global_load_lds_dwordx4 v2, s[40:41] offset:-2048
ds_read_b64_tr_b16 a[138:139], v9 offset:2368
global_load_lds_dwordx4 v3, s[40:41] offset:-1024
ds_read_b64_tr_b16 a[140:141], v9 offset:72
global_load_lds_dwordx4 v4, s[40:41] offset:0
ds_read_b64_tr_b16 a[142:143], v9 offset:2376
ds_read_b64_tr_b16 v[92:93], v10 offset:0
global_load_lds_dwordx4 v5, s[40:41] offset:1024
ds_read_b64_tr_b16 v[94:95], v10 offset:2304
global_load_lds_dwordx4 v6, s[40:41] offset:2048
ds_read_b64_tr_b16 a[144:145], v9 offset:18432
ds_read_b64_tr_b16 a[146:147], v9 offset:20736
global_load_lds_dwordx4 v7, s[40:41] offset:3072
ds_read_b64_tr_b16 a[148:149], v9 offset:18440
s_add_u32 m0, s48, 122880
ds_read_b64_tr_b16 a[150:151], v9 offset:20744
global_load_lds_dwordx4 v8, s[40:41] offset:-4096
ds_read_b64_tr_b16 a[152:153], v9 offset:18496
ds_read_b64_tr_b16 a[154:155], v9 offset:20800
s_add_u32 s40, s40, 73728
ds_read_b64_tr_b16 a[156:157], v9 offset:18504
s_addc_u32 s41, s41, 0
ds_read_b64_tr_b16 a[158:159], v9 offset:20808
s_waitcnt vmcnt(18) lgkmcnt(0)
s_barrier
s_add_u32 m0, s48, 4096
ds_read_b64_tr_b16 a[160:161], v9 offset:36864
ds_read_b64_tr_b16 a[162:163], v9 offset:39168
global_load_lds_dwordx4 v0, s[40:41] offset:-4096
ds_read_b64_tr_b16 a[164:165], v9 offset:36872
global_load_lds_dwordx4 v1, s[40:41] offset:-3072
ds_read_b64_tr_b16 a[166:167], v9 offset:39176
global_load_lds_dwordx4 v2, s[40:41] offset:-2048
ds_read_b64_tr_b16 a[168:169], v9 offset:36928
global_load_lds_dwordx4 v3, s[40:41] offset:-1024
ds_read_b64_tr_b16 a[170:171], v9 offset:39232
ds_read_b64_tr_b16 a[172:173], v9 offset:36936
global_load_lds_dwordx4 v4, s[40:41] offset:0
ds_read_b64_tr_b16 a[174:175], v9 offset:39240
global_load_lds_dwordx4 v5, s[40:41] offset:1024
ds_read_b64_tr_b16 a[176:177], v9 offset:55296
global_load_lds_dwordx4 v6, s[40:41] offset:2048
ds_read_b64_tr_b16 a[178:179], v9 offset:57600
global_load_lds_dwordx4 v7, s[40:41] offset:3072
ds_read_b64_tr_b16 a[180:181], v9 offset:55304
ds_read_b64_tr_b16 a[182:183], v9 offset:57608
s_add_u32 m0, s48, 12288
ds_read_b64_tr_b16 a[184:185], v9 offset:55360
global_load_lds_dwordx4 v8, s[40:41] offset:-4096
ds_read_b64_tr_b16 a[186:187], v9 offset:57664
s_sub_u32 s40, s40, 589824
ds_read_b64_tr_b16 a[188:189], v9 offset:55368
s_subb_u32 s41, s41, 0
ds_read_b64_tr_b16 a[190:191], v9 offset:57672
s_waitcnt vmcnt(18) lgkmcnt(0)
s_barrier
ds_read_b64_tr_b16 a[192:193], v204 offset:8192
ds_read_b64_tr_b16 a[194:195], v204 offset:10496
ds_read_b64_tr_b16 a[196:197], v204 offset:8200
ds_read_b64_tr_b16 a[198:199], v204 offset:10504
ds_read_b64_tr_b16 a[200:201], v204 offset:8256
ds_read_b64_tr_b16 a[202:203], v204 offset:10560
ds_read_b64_tr_b16 a[204:205], v204 offset:8264
ds_read_b64_tr_b16 a[206:207], v204 offset:10568
ds_read_b64_tr_b16 a[208:209], v204 offset:26624
ds_read_b64_tr_b16 a[210:211], v204 offset:28928
ds_read_b64_tr_b16 a[212:213], v204 offset:26632
ds_read_b64_tr_b16 a[214:215], v204 offset:28936
ds_read_b64_tr_b16 a[216:217], v204 offset:26688
ds_read_b64_tr_b16 a[218:219], v204 offset:28992
ds_read_b64_tr_b16 a[220:221], v204 offset:26696
ds_read_b64_tr_b16 a[222:223], v204 offset:29000
s_waitcnt vmcnt(9) lgkmcnt(0)
s_barrier
ds_read_b64_tr_b16 a[224:225], v204 offset:45056
ds_read_b64_tr_b16 a[226:227], v204 offset:47360
ds_read_b64_tr_b16 a[228:229], v204 offset:45064
ds_read_b64_tr_b16 a[230:231], v204 offset:47368
ds_read_b64_tr_b16 a[232:233], v204 offset:45120
ds_read_b64_tr_b16 a[234:235], v204 offset:47424
ds_read_b64_tr_b16 a[236:237], v204 offset:45128
ds_read_b64_tr_b16 a[238:239], v204 offset:47432
ds_read_b64_tr_b16 a[240:241], v204 offset:63488
ds_read_b64_tr_b16 a[242:243], v205 offset:256
ds_read_b64_tr_b16 a[244:245], v204 offset:63496
ds_read_b64_tr_b16 a[246:247], v205 offset:264
ds_read_b64_tr_b16 a[248:249], v204 offset:63552
ds_read_b64_tr_b16 a[250:251], v205 offset:320
ds_read_b64_tr_b16 a[252:253], v204 offset:63560
ds_read_b64_tr_b16 a[254:255], v205 offset:328
s_waitcnt vmcnt(0) lgkmcnt(0)
s_barrier
ds_read_b64_tr_b16 v[28:29], v9 offset:0
ds_read_b64_tr_b16 v[30:31], v9 offset:2304
ds_read_b64_tr_b16 v[32:33], v9 offset:8
ds_read_b64_tr_b16 v[34:35], v9 offset:2312
ds_read_b64_tr_b16 v[36:37], v9 offset:64
ds_read_b64_tr_b16 v[38:39], v9 offset:2368
ds_read_b64_tr_b16 v[40:41], v9 offset:72
ds_read_b64_tr_b16 v[42:43], v9 offset:2376
ds_read_b64_tr_b16 v[44:45], v9 offset:18432
ds_read_b64_tr_b16 v[46:47], v9 offset:20736
ds_read_b64_tr_b16 v[48:49], v9 offset:18440
ds_read_b64_tr_b16 v[50:51], v9 offset:20744
ds_read_b64_tr_b16 v[52:53], v9 offset:18496
ds_read_b64_tr_b16 v[54:55], v9 offset:20800
ds_read_b64_tr_b16 v[56:57], v9 offset:18504
ds_read_b64_tr_b16 v[58:59], v9 offset:20808
s_waitcnt lgkmcnt(0)
s_barrier
v_add_lshl_u32 v252, v24, 0, 7
v_bfi_b32 v252, s69, v252, v23
v_add_lshl_u32 v253, v22, 0, 7
v_bfi_b32 v253, s69, v253, v21
s_mov_b32 s92, 0x01010101
s_mov_b32 s93, 0x01010101
s_mov_b64 s[76:77], 0
s_add_u32 m0, s49, 18432
v_add_u32 v24, 1, v24
v_add_lshl_u32 v252, v24, 0, 7
v_bfi_b32 v252, s69, v252, v23
v_add_u32 v13, 18688, v13
s_xor_b32 s49, s49, 0x10000
s_mov_b32 m0, 47104
s_add_u32 m0, s49, 0
v_add_u32 v11, 18432, v11
v_add_u32 v24, 1, v24
v_add_lshl_u32 v252, v24, 0, 7
v_bfi_b32 v252, s69, v252, v23
s_mov_b32 m0, 55296
v_add_u32 v13, 18688, v13
s_add_u32 m0, s49, 18432
s_mov_b64 s[76:77], -1
s_mov_b32 s70, 33
s_waitcnt vmcnt(0) lgkmcnt(0)
s_nop 7
s_branch .Lwo_in5
.p2align 6
.Lwo_top6:
s_waitcnt lgkmcnt(2)
v_mfma_f32_16x16x32_bf16 v[96:99], a[0:3], v[132:135], v[96:99]
ds_read_b128 v[144:147], v25 offset:3072
v_mfma_f32_16x16x32_bf16 v[100:103], a[4:7], v[132:135], v[100:103]
v_mfma_f32_16x16x32_bf16 v[104:107], a[8:11], v[132:135], v[104:107]
s_waitcnt vmcnt(7)
s_barrier
v_mfma_f32_16x16x32_bf16 v[108:111], a[12:15], v[132:135], v[108:111]
v_cvt_pk_bf16_f32 v164, v112, v113
v_cvt_pk_bf16_f32 v165, v114, v115
s_mov_b64 exec, 1
global_store_dword v252, v24, s[38:39] sc1
s_mov_b64 exec, -1
v_mfma_f32_16x16x32_bf16 v[128:131], v[60:63], v[132:135], 0
s_waitcnt lgkmcnt(2)
v_cvt_pk_bf16_f32 v166, v116, v117
v_cvt_pk_bf16_f32 v167, v118, v119
v_add_u32 v24, 1, v24
v_add_lshl_u32 v252, v24, 0, 7
v_mfma_f32_16x16x32_bf16 v[96:99], a[16:19], v[136:139], v[96:99]
ds_read_b128 v[132:135], v25 offset:4096
v_bfi_b32 v252, s69, v252, v23
v_mfma_f32_16x16x32_bf16 v[100:103], a[20:23], v[136:139], v[100:103]
global_store_dwordx4 v13, v[164:167], s[38:39] offset:0 sc1
v_mfma_f32_16x16x32_bf16 v[104:107], a[24:27], v[136:139], v[104:107]
v_cvt_pk_bf16_f32 v168, v120, v121
v_cvt_pk_bf16_f32 v169, v122, v123
v_mfma_f32_16x16x32_bf16 v[108:111], a[28:31], v[136:139], v[108:111]
v_cvt_pk_bf16_f32 v170, v124, v125
v_cvt_pk_bf16_f32 v171, v126, v127
v_mfma_f32_16x16x32_bf16 v[128:131], v[64:67], v[136:139], v[128:131]
s_waitcnt lgkmcnt(2)
global_store_dwordx4 v13, v[168:171], s[38:39] offset:1024 sc1
v_mfma_f32_16x16x32_bf16 v[96:99], a[32:35], v[140:143], v[96:99]
s_cmp_eq_u32 s70, 32
ds_read_b128 v[136:139], v25 offset:5120
v_add_u32 v13, 18688, v13
v_mfma_f32_16x16x32_bf16 v[100:103], a[36:39], v[140:143], v[100:103]
s_cselect_b64 s[42:43], s[44:45], s[42:43]
v_add_f32 v254, v112, v113
v_mul_f32 v255, v112, v112
v_mfma_f32_16x16x32_bf16 v[104:107], a[40:43], v[140:143], v[104:107]
s_cmp_eq_u32 s70, 16
)"
R"(v_fmac_f32 v255, v113, v113
v_add_f32 v254, v254, v114
v_mfma_f32_16x16x32_bf16 v[108:111], a[44:47], v[140:143], v[108:111]
s_cselect_b64 s[42:43], s[46:47], s[42:43]
v_fmac_f32 v255, v114, v114
v_add_f32 v254, v254, v115
v_mfma_f32_16x16x32_bf16 v[128:131], v[68:71], v[140:143], v[128:131]
s_waitcnt lgkmcnt(2)
v_fmac_f32 v255, v115, v115
v_add_f32 v254, v254, v116
v_mfma_f32_16x16x32_bf16 v[96:99], a[48:51], v[144:147], v[96:99]
s_xor_b32 s49, s49, 0x10000
ds_read_b128 v[140:143], v25 offset:6144
v_fmac_f32 v255, v116, v116
v_mfma_f32_16x16x32_bf16 v[100:103], a[52:55], v[144:147], v[100:103]
s_mov_b32 m0, 47104
v_add_f32 v254, v254, v117
v_fmac_f32 v255, v117, v117
v_mfma_f32_16x16x32_bf16 v[104:107], a[56:59], v[144:147], v[104:107]
v_add_f32 v254, v254, v118
v_fmac_f32 v255, v118, v118
v_mfma_f32_16x16x32_bf16 v[108:111], a[60:63], v[144:147], v[108:111]
global_load_lds_dwordx4 v16, s[42:43] offset:0 sc1
v_add_f32 v254, v254, v119
v_mfma_f32_16x16x32_bf16 v[128:131], v[72:75], v[144:147], v[128:131]
s_waitcnt lgkmcnt(2)
v_fmac_f32 v255, v119, v119
v_add_f32 v254, v254, v120
v_mfma_f32_16x16x32_bf16 v[96:99], a[64:67], v[132:135], v[96:99]
ds_read_b128 v[144:147], v25 offset:7168
v_fmac_f32 v255, v120, v120
v_mfma_f32_16x16x32_bf16 v[100:103], a[68:71], v[132:135], v[100:103]
v_add_f32 v254, v254, v121
v_fmac_f32 v255, v121, v121
v_mfma_f32_16x16x32_bf16 v[104:107], a[72:75], v[132:135], v[104:107]
v_add_f32 v254, v254, v122
v_fmac_f32 v255, v122, v122
v_mfma_f32_16x16x32_bf16 v[108:111], a[76:79], v[132:135], v[108:111]
v_add_f32 v254, v254, v123
v_fmac_f32 v255, v123, v123
v_mfma_f32_16x16x32_bf16 v[128:131], v[76:79], v[132:135], v[128:131]
s_waitcnt vmcnt(9)
ds_read_b128 v[148:151], v19 offset:8192
ds_read_b128 v[152:155], v20 offset:8192
s_waitcnt lgkmcnt(4)
v_mfma_f32_16x16x32_bf16 v[96:99], a[80:83], v[136:139], v[96:99]
ds_read_b128 v[132:135], v25 offset:8192
global_load_lds_dwordx4 v17, s[42:43] offset:1024 sc1
v_mfma_f32_16x16x32_bf16 v[100:103], a[84:87], v[136:139], v[100:103]
v_add_f32 v254, v254, v124
v_fmac_f32 v255, v124, v124
v_mfma_f32_16x16x32_bf16 v[104:107], a[88:91], v[136:139], v[104:107]
v_add_f32 v254, v254, v125
v_fmac_f32 v255, v125, v125
v_mfma_f32_16x16x32_bf16 v[108:111], a[92:95], v[136:139], v[108:111]
v_add_f32 v254, v254, v126
v_fmac_f32 v255, v126, v126
v_mfma_f32_16x16x32_bf16 v[128:131], v[80:83], v[136:139], v[128:131]
s_waitcnt lgkmcnt(4)
v_add_f32 v254, v254, v127
v_fmac_f32 v255, v127, v127
v_mfma_f32_16x16x32_bf16 v[96:99], a[96:99], v[140:143], v[96:99]
s_add_u32 m0, s49, 0
ds_read_b128 v[136:139], v25 offset:9216
v_mfma_f32_16x16x32_bf16 v[100:103], a[100:103], v[140:143], v[100:103]
ds_write_b64 v27, v[254:255] offset:45056
v_mfma_f32_16x16x32_bf16 v[104:107], a[104:107], v[140:143], v[104:107]
s_waitcnt lgkmcnt(4)
v_lshlrev_b32 v112, 16, v148
v_and_b32 v113, 0xffff0000, v148
v_mfma_f32_16x16x32_bf16 v[108:111], a[108:111], v[140:143], v[108:111]
s_sub_u32 s42, s42, 18432
v_lshlrev_b32 v114, 16, v149
v_and_b32 v115, 0xffff0000, v149
v_mfma_f32_16x16x32_bf16 v[128:131], v[84:87], v[140:143], v[128:131]
v_lshlrev_b32 v116, 16, v150
v_and_b32 v117, 0xffff0000, v150
v_mfma_f32_16x16x32_bf16 v[96:99], a[112:115], v[144:147], v[96:99]
s_subb_u32 s43, s43, 0
ds_read_b128 v[140:143], v25 offset:10240
v_lshlrev_b32 v118, 16, v151
v_mfma_f32_16x16x32_bf16 v[100:103], a[116:119], v[144:147], v[100:103]
s_waitcnt lgkmcnt(4)
v_and_b32 v119, 0xffff0000, v151
v_lshlrev_b32 v120, 16, v152
v_mfma_f32_16x16x32_bf16 v[104:107], a[120:123], v[144:147], v[104:107]
v_and_b32 v121, 0xffff0000, v152
v_lshlrev_b32 v122, 16, v153
v_mfma_f32_16x16x32_bf16 v[108:111], a[124:127], v[144:147], v[108:111]
v_and_b32 v123, 0xffff0000, v153
v_lshlrev_b32 v124, 16, v154
v_mfma_f32_16x16x32_bf16 v[128:131], v[88:91], v[144:147], v[128:131]
s_waitcnt lgkmcnt(3)
v_and_b32 v125, 0xffff0000, v154
v_lshlrev_b32 v126, 16, v155
v_mfma_f32_16x16x32_bf16 v[96:99], a[128:131], v[132:135], v[96:99]
ds_read_b128 v[144:147], v25 offset:11264
v_and_b32 v127, 0xffff0000, v155
v_mfma_f32_16x16x32_bf16 v[100:103], a[132:135], v[132:135], v[100:103]
v_mfma_f32_16x16x32_bf16 v[104:107], a[136:139], v[132:135], v[104:107]
v_mfma_f32_16x16x32_bf16 v[108:111], a[140:143], v[132:135], v[108:111]
v_mfma_f32_16x16x32_bf16 v[128:131], v[92:95], v[132:135], v[128:131]
s_waitcnt lgkmcnt(3)
v_mfma_f32_16x16x32_bf16 v[96:99], a[144:147], v[136:139], v[96:99]
ds_read_b128 v[132:135], v25 offset:12288
v_mfma_f32_16x16x32_bf16 v[100:103], a[148:151], v[136:139], v[100:103]
v_mfma_f32_16x16x32_bf16 v[104:107], a[152:155], v[136:139], v[104:107]
v_mfma_f32_16x16x32_bf16 v[108:111], a[156:159], v[136:139], v[108:111]
s_waitcnt lgkmcnt(2)
v_mfma_f32_16x16x32_bf16 v[96:99], a[160:163], v[140:143], v[96:99]
ds_read_b128 v[136:139], v25 offset:13312
v_mfma_f32_16x16x32_bf16 v[100:103], a[164:167], v[140:143], v[100:103]
v_mfma_f32_16x16x32_bf16 v[104:107], a[168:171], v[140:143], v[104:107]
ds_write_b128 v26, v[128:131] offset:36864
v_mfma_f32_16x16x32_bf16 v[108:111], a[172:175], v[140:143], v[108:111]
s_waitcnt lgkmcnt(3)
v_mfma_f32_16x16x32_bf16 v[96:99], a[176:179], v[144:147], v[96:99]
ds_read_b128 v[140:143], v25 offset:14336
v_mfma_f32_16x16x32_bf16 v[100:103], a[180:183], v[144:147], v[100:103]
v_mfma_f32_16x16x32_bf16 v[104:107], a[184:187], v[144:147], v[104:107]
s_barrier
v_mfma_f32_16x16x32_bf16 v[108:111], a[188:191], v[144:147], v[108:111]
s_waitcnt lgkmcnt(3)
global_load_lds_dwordx4 v11, s[38:39] offset:-2048 sc1
v_mfma_f32_16x16x32_bf16 v[96:99], a[192:195], v[132:135], v[96:99]
ds_read_b128 v[144:147], v25 offset:15360
v_mfma_f32_16x16x32_bf16 v[100:103], a[196:199], v[132:135], v[100:103]
v_mfma_f32_16x16x32_bf16 v[104:107], a[200:203], v[132:135], v[104:107]
global_load_lds_dwordx4 v11, s[38:39] offset:-1024 sc1
v_mfma_f32_16x16x32_bf16 v[108:111], a[204:207], v[132:135], v[108:111]
s_waitcnt lgkmcnt(3)
v_mfma_f32_16x16x32_bf16 v[96:99], a[208:211], v[136:139], v[96:99]
ds_read_b128 v[132:135], v25 offset:16384
v_mfma_f32_16x16x32_bf16 v[100:103], a[212:215], v[136:139], v[100:103]
global_load_lds_dwordx4 v11, s[38:39] offset:0 sc1
v_mfma_f32_16x16x32_bf16 v[104:107], a[216:219], v[136:139], v[104:107]
v_mfma_f32_16x16x32_bf16 v[108:111], a[220:223], v[136:139], v[108:111]
s_waitcnt lgkmcnt(2)
v_mfma_f32_16x16x32_bf16 v[96:99], a[224:227], v[140:143], v[96:99]
ds_read_b128 v[136:139], v25 offset:17408
global_load_lds_dwordx4 v11, s[38:39] offset:1024 sc1
v_mfma_f32_16x16x32_bf16 v[100:103], a[228:231], v[140:143], v[100:103]
v_mfma_f32_16x16x32_bf16 v[104:107], a[232:235], v[140:143], v[104:107]
v_mfma_f32_16x16x32_bf16 v[108:111], a[236:239], v[140:143], v[108:111]
s_waitcnt lgkmcnt(2)
global_load_lds_dwordx4 v11, s[38:39] offset:2048 sc1
v_add_u32 v11, 18432, v11
v_mfma_f32_16x16x32_bf16 v[96:99], a[240:243], v[144:147], v[96:99]
v_min_u32 v11, v11, v12
v_mfma_f32_16x16x32_bf16 v[100:103], a[244:247], v[144:147], v[100:103]
v_mfma_f32_16x16x32_bf16 v[104:107], a[248:251], v[144:147], v[104:107]
s_waitcnt vmcnt(10)
s_barrier
v_mfma_f32_16x16x32_bf16 v[108:111], a[252:255], v[144:147], v[108:111]
s_waitcnt lgkmcnt(1)
ds_read_b128 v[140:143], v25 offset:18432
v_mfma_f32_16x16x32_bf16 v[96:99], v[28:31], v[132:135], v[96:99]
ds_read_b128 v[144:147], v25 offset:19456
v_mfma_f32_16x16x32_bf16 v[100:103], v[32:35], v[132:135], v[100:103]
v_mfma_f32_16x16x32_bf16 v[104:107], v[36:39], v[132:135], v[104:107]
v_mfma_f32_16x16x32_bf16 v[108:111], v[40:43], v[132:135], v[108:111]
s_waitcnt lgkmcnt(2)
v_mfma_f32_16x16x32_bf16 v[96:99], v[44:47], v[136:139], v[96:99]
ds_read_b128 v[132:135], v25 offset:20480
v_mfma_f32_16x16x32_bf16 v[100:103], v[48:51], v[136:139], v[100:103]
v_mfma_f32_16x16x32_bf16 v[104:107], v[52:55], v[136:139], v[104:107]
v_mfma_f32_16x16x32_bf16 v[108:111], v[56:59], v[136:139], v[108:111]
s_waitcnt lgkmcnt(2)
v_mfma_f32_16x16x32_bf16 v[112:115], a[0:3], v[140:143], v[112:115]
ds_read_b128 v[136:139], v25 offset:21504
v_mfma_f32_16x16x32_bf16 v[116:119], a[4:7], v[140:143], v[116:119]
v_mfma_f32_16x16x32_bf16 v[120:123], a[8:11], v[140:143], v[120:123]
s_waitcnt vmcnt(7)
s_barrier
v_mfma_f32_16x16x32_bf16 v[124:127], a[12:15], v[140:143], v[124:127]
s_mov_b64 exec, 1
global_store_dword v252, v24, s[38:39] sc1
s_mov_b64 exec, -1
v_add_u32 v24, 1, v24
v_add_lshl_u32 v252, v24, 0, 7
v_mfma_f32_16x16x32_bf16 v[128:131], v[60:63], v[140:143], 0
s_waitcnt lgkmcnt(2)
v_bfi_b32 v252, s69, v252, v23
v_cvt_pk_bf16_f32 v164, v96, v97
v_mfma_f32_16x16x32_bf16 v[112:115], a[16:19], v[144:147], v[112:115]
ds_read_b128 v[140:143], v25 offset:22528
v_cvt_pk_bf16_f32 v165, v98, v99
v_mfma_f32_16x16x32_bf16 v[116:119], a[20:23], v[144:147], v[116:119]
v_cvt_pk_bf16_f32 v166, v100, v101
v_cvt_pk_bf16_f32 v167, v102, v103
v_mfma_f32_16x16x32_bf16 v[120:123], a[24:27], v[144:147], v[120:123]
global_store_dwordx4 v13, v[164:167], s[38:39] offset:0 sc1
v_mfma_f32_16x16x32_bf16 v[124:127], a[28:31], v[144:147], v[124:127]
v_cvt_pk_bf16_f32 v168, v104, v105
v_cvt_pk_bf16_f32 v169, v106, v107
v_mfma_f32_16x16x32_bf16 v[128:131], v[64:67], v[144:147], v[128:131]
s_waitcnt lgkmcnt(2)
v_cvt_pk_bf16_f32 v170, v108, v109
v_cvt_pk_bf16_f32 v171, v110, v111
v_mfma_f32_16x16x32_bf16 v[112:115], a[32:35], v[132:135], v[112:115]
s_mov_b32 m0, 55296
ds_read_b128 v[144:147], v25 offset:23552
v_add_f32 v254, v96, v97
v_mfma_f32_16x16x32_bf16 v[116:119], a[36:39], v[132:135], v[116:119]
global_store_dwordx4 v13, v[168:171], s[38:39] offset:1024 sc1
v_mfma_f32_16x16x32_bf16 v[120:123], a[40:43], v[132:135], v[120:123]
v_add_u32 v13, 18688, v13
v_mul_f32 v255, v96, v96
v_mfma_f32_16x16x32_bf16 v[124:127], a[44:47], v[132:135], v[124:127]
v_fmac_f32 v255, v97, v97
v_add_f32 v254, v254, v98
v_mfma_f32_16x16x32_bf16 v[128:131], v[68:71], v[132:135], v[128:131]
s_waitcnt lgkmcnt(2)
v_fmac_f32 v255, v98, v98
v_add_f32 v254, v254, v99
v_mfma_f32_16x16x32_bf16 v[112:115], a[48:51], v[136:139], v[112:115]
ds_read_b128 v[132:135], v25 offset:24576
v_fmac_f32 v255, v99, v99
v_mfma_f32_16x16x32_bf16 v[116:119], a[52:55], v[136:139], v[116:119]
v_add_f32 v254, v254, v100
v_fmac_f32 v255, v100, v100
v_mfma_f32_16x16x32_bf16 v[120:123], a[56:59], v[136:139], v[120:123]
v_add_f32 v254, v254, v101
v_fmac_f32 v255, v101, v101
v_mfma_f32_16x16x32_bf16 v[124:127], a[60:63], v[136:139], v[124:127]
global_load_lds_dwordx4 v16, s[42:43] offset:0 sc1
v_add_f32 v254, v254, v102
v_mfma_f32_16x16x32_bf16 v[128:131], v[72:75], v[136:139], v[128:131]
s_waitcnt lgkmcnt(2)
v_fmac_f32 v255, v102, v102
v_add_f32 v254, v254, v103
v_mfma_f32_16x16x32_bf16 v[112:115], a[64:67], v[140:143], v[112:115]
ds_read_b128 v[136:139], v25 offset:25600
v_fmac_f32 v255, v103, v103
v_mfma_f32_16x16x32_bf16 v[116:119], a[68:71], v[140:143], v[116:119]
v_add_f32 v254, v254, v104
v_fmac_f32 v255, v104, v104
v_mfma_f32_16x16x32_bf16 v[120:123], a[72:75], v[140:143], v[120:123]
v_add_f32 v254, v254, v105
v_fmac_f32 v255, v105, v105
v_mfma_f32_16x16x32_bf16 v[124:127], a[76:79], v[140:143], v[124:127]
v_add_f32 v254, v254, v106
v_fmac_f32 v255, v106, v106
v_mfma_f32_16x16x32_bf16 v[128:131], v[76:79], v[140:143], v[128:131]
s_waitcnt vmcnt(9)
ds_read_b128 v[148:151], v19 offset:0
ds_read_b128 v[152:155], v20 offset:0
s_waitcnt lgkmcnt(4)
v_mfma_f32_16x16x32_bf16 v[112:115], a[80:83], v[144:147], v[112:115]
ds_read_b128 v[140:143], v25 offset:26624
global_load_lds_dwordx4 v17, s[42:43] offset:1024 sc1
v_mfma_f32_16x16x32_bf16 v[116:119], a[84:87], v[144:147], v[116:119]
v_add_f32 v254, v254, v107
v_fmac_f32 v255, v107, v107
v_mfma_f32_16x16x32_bf16 v[120:123], a[88:91], v[144:147], v[120:123]
v_add_f32 v254, v254, v108
v_fmac_f32 v255, v108, v108
v_mfma_f32_16x16x32_bf16 v[124:127], a[92:95], v[144:147], v[124:127]
v_add_f32 v254, v254, v109
v_fmac_f32 v255, v109, v109
v_mfma_f32_16x16x32_bf16 v[128:131], v[80:83], v[144:147], v[128:131]
s_waitcnt lgkmcnt(4)
v_add_f32 v254, v254, v110
v_fmac_f32 v255, v110, v110
v_mfma_f32_16x16x32_bf16 v[112:115], a[96:99], v[132:135], v[112:115]
s_add_u32 m0, s49, 18432
ds_read_b128 v[144:147], v25 offset:27648
v_add_f32 v254, v254, v111
v_mfma_f32_16x16x32_bf16 v[116:119], a[100:103], v[132:135], v[116:119]
v_fmac_f32 v255, v111, v111
v_mfma_f32_16x16x32_bf16 v[120:123], a[104:107], v[132:135], v[120:123]
ds_write_b64 v27, v[254:255] offset:43008
v_mfma_f32_16x16x32_bf16 v[124:127], a[108:111], v[132:135], v[124:127]
s_sub_u32 s42, s42, 18432
s_waitcnt lgkmcnt(4)
v_lshlrev_b32 v96, 16, v148
v_and_b32 v97, 0xffff0000, v148
v_mfma_f32_16x16x32_bf16 v[128:131], v[84:87], v[132:135], v[128:131]
v_lshlrev_b32 v98, 16, v149
v_and_b32 v99, 0xffff0000, v149
v_mfma_f32_16x16x32_bf16 v[112:115], a[112:115], v[136:139], v[112:115]
s_subb_u32 s43, s43, 0
ds_read_b128 v[132:135], v25 offset:28672
v_lshlrev_b32 v100, 16, v150
v_mfma_f32_16x16x32_bf16 v[116:119], a[116:119], v[136:139], v[116:119]
v_and_b32 v101, 0xffff0000, v150
v_lshlrev_b32 v102, 16, v151
v_mfma_f32_16x16x32_bf16 v[120:123], a[120:123], v[136:139], v[120:123]
s_waitcnt lgkmcnt(4)
v_and_b32 v103, 0xffff0000, v151
v_lshlrev_b32 v104, 16, v152
v_mfma_f32_16x16x32_bf16 v[124:127], a[124:127], v[136:139], v[124:127]
v_and_b32 v105, 0xffff0000, v152
v_lshlrev_b32 v106, 16, v153
v_mfma_f32_16x16x32_bf16 v[128:131], v[88:91], v[136:139], v[128:131]
s_waitcnt lgkmcnt(3)
v_and_b32 v107, 0xffff0000, v153
v_lshlrev_b32 v108, 16, v154
v_mfma_f32_16x16x32_bf16 v[112:115], a[128:131], v[140:143], v[112:115]
ds_read_b128 v[136:139], v25 offset:29696
v_and_b32 v109, 0xffff0000, v154
v_mfma_f32_16x16x32_bf16 v[116:119], a[132:135], v[140:143], v[116:119]
v_lshlrev_b32 v110, 16, v155
v_and_b32 v111, 0xffff0000, v155
v_mfma_f32_16x16x32_bf16 v[120:123], a[136:139], v[140:143], v[120:123]
v_mfma_f32_16x16x32_bf16 v[124:127], a[140:143], v[140:143], v[124:127]
v_mfma_f32_16x16x32_bf16 v[128:131], v[92:95], v[140:143], v[128:131]
s_waitcnt lgkmcnt(3)
v_mfma_f32_16x16x32_bf16 v[112:115], a[144:147], v[144:147], v[112:115]
ds_read_b128 v[140:143], v25 offset:30720
v_mfma_f32_16x16x32_bf16 v[116:119], a[148:151], v[144:147], v[116:119]
v_mfma_f32_16x16x32_bf16 v[120:123], a[152:155], v[144:147], v[120:123]
v_mfma_f32_16x16x32_bf16 v[124:127], a[156:159], v[144:147], v[124:127]
s_waitcnt lgkmcnt(2)
v_mfma_f32_16x16x32_bf16 v[112:115], a[160:163], v[132:135], v[112:115]
ds_read_b128 v[144:147], v25 offset:31744
v_mfma_f32_16x16x32_bf16 v[116:119], a[164:167], v[132:135], v[116:119]
v_mfma_f32_16x16x32_bf16 v[120:123], a[168:171], v[132:135], v[120:123]
ds_write_b128 v26, v[128:131] offset:39936
v_mfma_f32_16x16x32_bf16 v[124:127], a[172:175], v[132:135], v[124:127]
s_waitcnt lgkmcnt(3)
v_mfma_f32_16x16x32_bf16 v[112:115], a[176:179], v[136:139], v[112:115]
ds_read_b128 v[132:135], v25 offset:32768
v_mfma_f32_16x16x32_bf16 v[116:119], a[180:183], v[136:139], v[116:119]
v_mfma_f32_16x16x32_bf16 v[120:123], a[184:187], v[136:139], v[120:123]
s_barrier
v_mfma_f32_16x16x32_bf16 v[124:127], a[188:191], v[136:139], v[124:127]
s_waitcnt lgkmcnt(3)
global_load_lds_dwordx4 v11, s[38:39] offset:-2048 sc1
v_mfma_f32_16x16x32_bf16 v[112:115], a[192:195], v[140:143], v[112:115]
s_mov_b64 s[76:77], -1
ds_read_b128 v[136:139], v25 offset:33792
v_mfma_f32_16x16x32_bf16 v[116:119], a[196:199], v[140:143], v[116:119]
v_mfma_f32_16x16x32_bf16 v[120:123], a[200:203], v[140:143], v[120:123]
global_load_lds_dwordx4 v11, s[38:39] offset:-1024 sc1
.Lwo_in5:
v_mfma_f32_16x16x32_bf16 v[124:127], a[204:207], v[140:143], v[124:127]
s_waitcnt lgkmcnt(3)
v_mfma_f32_16x16x32_bf16 v[112:115], a[208:211], v[144:147], v[112:115]
ds_read_b128 v[140:143], v25 offset:34816
v_mfma_f32_16x16x32_bf16 v[116:119], a[212:215], v[144:147], v[116:119]
global_load_lds_dwordx4 v11, s[38:39] offset:0 sc1
v_mfma_f32_16x16x32_bf16 v[120:123], a[216:219], v[144:147], v[120:123]
v_mfma_f32_16x16x32_bf16 v[124:127], a[220:223], v[144:147], v[124:127]
s_waitcnt lgkmcnt(2)
v_mfma_f32_16x16x32_bf16 v[112:115], a[224:227], v[132:135], v[112:115]
ds_read_b128 v[144:147], v25 offset:35840
global_load_lds_dwordx4 v11, s[38:39] offset:1024 sc1
v_mfma_f32_16x16x32_bf16 v[116:119], a[228:231], v[132:135], v[116:119]
v_mfma_f32_16x16x32_bf16 v[120:123], a[232:235], v[132:135], v[120:123]
v_mfma_f32_16x16x32_bf16 v[124:127], a[236:239], v[132:135], v[124:127]
s_waitcnt lgkmcnt(2)
v_xor_b32 v25, 0x10000, v25
global_load_lds_dwordx4 v11, s[38:39] offset:2048 sc1
v_mfma_f32_16x16x32_bf16 v[112:115], a[240:243], v[136:139], v[112:115]
v_add_u32 v11, 18432, v11
v_min_u32 v11, v11, v12
v_mfma_f32_16x16x32_bf16 v[116:119], a[244:247], v[136:139], v[116:119]
v_mfma_f32_16x16x32_bf16 v[120:123], a[248:251], v[136:139], v[120:123]
s_waitcnt vmcnt(10)
s_barrier
v_mfma_f32_16x16x32_bf16 v[124:127], a[252:255], v[136:139], v[124:127]
s_waitcnt lgkmcnt(1)
ds_read_b128 v[132:135], v25 offset:0
v_mfma_f32_16x16x32_bf16 v[112:115], v[28:31], v[140:143], v[112:115]
ds_read_b128 v[136:139], v25 offset:1024
v_mfma_f32_16x16x32_bf16 v[116:119], v[32:35], v[140:143], v[116:119]
v_mfma_f32_16x16x32_bf16 v[120:123], v[36:39], v[140:143], v[120:123]
v_mfma_f32_16x16x32_bf16 v[124:127], v[40:43], v[140:143], v[124:127]
s_waitcnt lgkmcnt(2)
v_mfma_f32_16x16x32_bf16 v[112:115], v[44:47], v[144:147], v[112:115]
ds_read_b128 v[140:143], v25 offset:2048
v_mfma_f32_16x16x32_bf16 v[116:119], v[48:51], v[144:147], v[116:119]
v_mfma_f32_16x16x32_bf16 v[120:123], v[52:55], v[144:147], v[120:123]
v_mfma_f32_16x16x32_bf16 v[124:127], v[56:59], v[144:147], v[124:127]
s_sub_u32 s70, s70, 1
s_cbranch_scc0 .Lwo_top6
s_waitcnt vmcnt(7)
s_barrier
s_mov_b64 exec, 1
global_store_dword v252, v24, s[38:39] sc1
s_mov_b64 exec, -1
v_add_u32 v24, 1, v24
v_add_lshl_u32 v252, v24, 0, 7
v_bfi_b32 v252, s69, v252, v23
v_cvt_pk_bf16_f32 v164, v112, v113
v_cvt_pk_bf16_f32 v165, v114, v115
v_cvt_pk_bf16_f32 v166, v116, v117
v_cvt_pk_bf16_f32 v167, v118, v119
global_store_dwordx4 v13, v[164:167], s[38:39] offset:0 sc1
v_cvt_pk_bf16_f32 v168, v120, v121
v_cvt_pk_bf16_f32 v169, v122, v123
v_cvt_pk_bf16_f32 v170, v124, v125
v_cvt_pk_bf16_f32 v171, v126, v127
global_store_dwordx4 v13, v[168:171], s[38:39] offset:1024 sc1
v_add_f32 v254, v112, v113
v_mul_f32 v255, v112, v112
v_fmac_f32 v255, v113, v113
v_add_f32 v254, v254, v114
v_fmac_f32 v255, v114, v114
)"
R"(v_add_f32 v254, v254, v115
v_fmac_f32 v255, v115, v115
v_add_f32 v254, v254, v116
v_fmac_f32 v255, v116, v116
v_add_f32 v254, v254, v117
v_fmac_f32 v255, v117, v117
v_add_f32 v254, v254, v118
v_fmac_f32 v255, v118, v118
v_add_f32 v254, v254, v119
v_fmac_f32 v255, v119, v119
v_add_f32 v254, v254, v120
v_fmac_f32 v255, v120, v120
v_add_f32 v254, v254, v121
v_fmac_f32 v255, v121, v121
v_add_f32 v254, v254, v122
v_fmac_f32 v255, v122, v122
v_add_f32 v254, v254, v123
v_fmac_f32 v255, v123, v123
v_add_f32 v254, v254, v124
v_fmac_f32 v255, v124, v124
v_add_f32 v254, v254, v125
v_fmac_f32 v255, v125, v125
v_add_f32 v254, v254, v126
v_fmac_f32 v255, v126, v126
v_add_f32 v254, v254, v127
v_fmac_f32 v255, v127, v127
ds_write_b64 v27, v[254:255] offset:45056
s_waitcnt vmcnt(0) lgkmcnt(0)
s_barrier
s_mov_b64 exec, 1
global_store_dword v252, v24, s[38:39] sc1
s_mov_b64 exec, -1
s_waitcnt vmcnt(0)
s_barrier
v_add_u32 v24, 8, v24
v_mbcnt_lo_u32_b32 v252, -1, 0
v_mbcnt_hi_u32_b32 v252, -1, v252
v_add_lshl_u32 v252, v252, 0, 7
v_bfi_b32 v252, s69, v252, v23
s_mov_b64 exec, 0xff
global_store_dword v252, v24, s[38:39] sc1
s_mov_b64 exec, -1
s_endpgm
.Lwo_w2:
s_add_u32 m0, s48, 4096
s_nop 0
global_load_lds_dwordx4 v0, s[40:41] offset:-4096
global_load_lds_dwordx4 v1, s[40:41] offset:-3072
global_load_lds_dwordx4 v2, s[40:41] offset:-2048
global_load_lds_dwordx4 v3, s[40:41] offset:-1024
global_load_lds_dwordx4 v4, s[40:41] offset:0
global_load_lds_dwordx4 v5, s[40:41] offset:1024
global_load_lds_dwordx4 v6, s[40:41] offset:2048
global_load_lds_dwordx4 v7, s[40:41] offset:3072
s_add_u32 m0, s48, 12288
s_nop 0
global_load_lds_dwordx4 v8, s[40:41] offset:-4096
s_add_u32 s40, s40, 73728
s_addc_u32 s41, s41, 0
s_add_u32 m0, s48, 40960
s_nop 0
global_load_lds_dwordx4 v0, s[40:41] offset:-4096
global_load_lds_dwordx4 v1, s[40:41] offset:-3072
global_load_lds_dwordx4 v2, s[40:41] offset:-2048
global_load_lds_dwordx4 v3, s[40:41] offset:-1024
global_load_lds_dwordx4 v4, s[40:41] offset:0
global_load_lds_dwordx4 v5, s[40:41] offset:1024
global_load_lds_dwordx4 v6, s[40:41] offset:2048
global_load_lds_dwordx4 v7, s[40:41] offset:3072
s_add_u32 m0, s48, 49152
s_nop 0
global_load_lds_dwordx4 v8, s[40:41] offset:-4096
s_add_u32 s40, s40, 73728
s_addc_u32 s41, s41, 0
s_add_u32 m0, s48, 77824
s_nop 0
global_load_lds_dwordx4 v0, s[40:41] offset:-4096
global_load_lds_dwordx4 v1, s[40:41] offset:-3072
global_load_lds_dwordx4 v2, s[40:41] offset:-2048
global_load_lds_dwordx4 v3, s[40:41] offset:-1024
global_load_lds_dwordx4 v4, s[40:41] offset:0
global_load_lds_dwordx4 v5, s[40:41] offset:1024
global_load_lds_dwordx4 v6, s[40:41] offset:2048
global_load_lds_dwordx4 v7, s[40:41] offset:3072
s_add_u32 m0, s48, 86016
s_nop 0
global_load_lds_dwordx4 v8, s[40:41] offset:-4096
s_add_u32 s40, s40, 73728
s_addc_u32 s41, s41, 0
v_add_u32 v204, 0x10000, v9
v_add_u32 v205, 0x20000, v9
v_add_u32 v206, 0x10000, v10
v_add_u32 v207, 0x20000, v10
s_waitcnt lgkmcnt(0)
s_mul_i32 s37, s37, 0x9E3779B1
s_add_u32 s37, s37, 0x7F4A7C15
v_add_u32 v22, s37, v22
v_add_u32 v24, s37, v24
s_waitcnt vmcnt(18)
s_barrier
s_add_u32 m0, s48, 114688
ds_read_b64_tr_b16 a[0:1], v9 offset:0
ds_read_b64_tr_b16 a[2:3], v9 offset:2304
global_load_lds_dwordx4 v0, s[40:41] offset:-4096
ds_read_b64_tr_b16 a[4:5], v9 offset:8
global_load_lds_dwordx4 v1, s[40:41] offset:-3072
ds_read_b64_tr_b16 a[6:7], v9 offset:2312
global_load_lds_dwordx4 v2, s[40:41] offset:-2048
ds_read_b64_tr_b16 a[8:9], v9 offset:64
global_load_lds_dwordx4 v3, s[40:41] offset:-1024
ds_read_b64_tr_b16 a[10:11], v9 offset:2368
ds_read_b64_tr_b16 a[12:13], v9 offset:72
global_load_lds_dwordx4 v4, s[40:41] offset:0
ds_read_b64_tr_b16 a[14:15], v9 offset:2376
global_load_lds_dwordx4 v5, s[40:41] offset:1024
ds_read_b64_tr_b16 a[16:17], v9 offset:18432
global_load_lds_dwordx4 v6, s[40:41] offset:2048
ds_read_b64_tr_b16 a[18:19], v9 offset:20736
global_load_lds_dwordx4 v7, s[40:41] offset:3072
ds_read_b64_tr_b16 a[20:21], v9 offset:18440
ds_read_b64_tr_b16 a[22:23], v9 offset:20744
s_add_u32 m0, s48, 122880
ds_read_b64_tr_b16 a[24:25], v9 offset:18496
global_load_lds_dwordx4 v8, s[40:41] offset:-4096
ds_read_b64_tr_b16 a[26:27], v9 offset:20800
s_add_u32 s40, s40, 73728
ds_read_b64_tr_b16 a[28:29], v9 offset:18504
s_addc_u32 s41, s41, 0
ds_read_b64_tr_b16 a[30:31], v9 offset:20808
s_waitcnt vmcnt(18) lgkmcnt(0)
s_barrier
s_add_u32 m0, s48, 4096
ds_read_b64_tr_b16 a[32:33], v9 offset:36864
ds_read_b64_tr_b16 a[34:35], v9 offset:39168
global_load_lds_dwordx4 v0, s[40:41] offset:-4096
ds_read_b64_tr_b16 a[36:37], v9 offset:36872
global_load_lds_dwordx4 v1, s[40:41] offset:-3072
ds_read_b64_tr_b16 a[38:39], v9 offset:39176
global_load_lds_dwordx4 v2, s[40:41] offset:-2048
ds_read_b64_tr_b16 a[40:41], v9 offset:36928
global_load_lds_dwordx4 v3, s[40:41] offset:-1024
ds_read_b64_tr_b16 a[42:43], v9 offset:39232
ds_read_b64_tr_b16 a[44:45], v9 offset:36936
global_load_lds_dwordx4 v4, s[40:41] offset:0
ds_read_b64_tr_b16 a[46:47], v9 offset:39240
global_load_lds_dwordx4 v5, s[40:41] offset:1024
ds_read_b64_tr_b16 a[48:49], v9 offset:55296
global_load_lds_dwordx4 v6, s[40:41] offset:2048
ds_read_b64_tr_b16 a[50:51], v9 offset:57600
global_load_lds_dwordx4 v7, s[40:41] offset:3072
ds_read_b64_tr_b16 a[52:53], v9 offset:55304
ds_read_b64_tr_b16 a[54:55], v9 offset:57608
s_add_u32 m0, s48, 12288
ds_read_b64_tr_b16 a[56:57], v9 offset:55360
global_load_lds_dwordx4 v8, s[40:41] offset:-4096
ds_read_b64_tr_b16 a[58:59], v9 offset:57664
s_add_u32 s40, s40, 73728
ds_read_b64_tr_b16 a[60:61], v9 offset:55368
s_addc_u32 s41, s41, 0
ds_read_b64_tr_b16 a[62:63], v9 offset:57672
s_waitcnt vmcnt(18) lgkmcnt(0)
s_barrier
s_add_u32 m0, s48, 40960
ds_read_b64_tr_b16 a[64:65], v204 offset:8192
ds_read_b64_tr_b16 a[66:67], v204 offset:10496
global_load_lds_dwordx4 v0, s[40:41] offset:-4096
ds_read_b64_tr_b16 a[68:69], v204 offset:8200
global_load_lds_dwordx4 v1, s[40:41] offset:-3072
ds_read_b64_tr_b16 a[70:71], v204 offset:10504
global_load_lds_dwordx4 v2, s[40:41] offset:-2048
ds_read_b64_tr_b16 a[72:73], v204 offset:8256
global_load_lds_dwordx4 v3, s[40:41] offset:-1024
ds_read_b64_tr_b16 a[74:75], v204 offset:10560
ds_read_b64_tr_b16 a[76:77], v204 offset:8264
global_load_lds_dwordx4 v4, s[40:41] offset:0
ds_read_b64_tr_b16 a[78:79], v204 offset:10568
global_load_lds_dwordx4 v5, s[40:41] offset:1024
ds_read_b64_tr_b16 a[80:81], v204 offset:26624
global_load_lds_dwordx4 v6, s[40:41] offset:2048
ds_read_b64_tr_b16 a[82:83], v204 offset:28928
global_load_lds_dwordx4 v7, s[40:41] offset:3072
ds_read_b64_tr_b16 a[84:85], v204 offset:26632
ds_read_b64_tr_b16 a[86:87], v204 offset:28936
s_add_u32 m0, s48, 49152
ds_read_b64_tr_b16 a[88:89], v204 offset:26688
global_load_lds_dwordx4 v8, s[40:41] offset:-4096
ds_read_b64_tr_b16 a[90:91], v204 offset:28992
s_add_u32 s40, s40, 73728
ds_read_b64_tr_b16 a[92:93], v204 offset:26696
s_addc_u32 s41, s41, 0
ds_read_b64_tr_b16 a[94:95], v204 offset:29000
s_waitcnt vmcnt(18) lgkmcnt(0)
s_barrier
s_add_u32 m0, s48, 77824
ds_read_b64_tr_b16 a[96:97], v204 offset:45056
ds_read_b64_tr_b16 a[98:99], v204 offset:47360
global_load_lds_dwordx4 v0, s[40:41] offset:-4096
ds_read_b64_tr_b16 a[100:101], v204 offset:45064
global_load_lds_dwordx4 v1, s[40:41] offset:-3072
ds_read_b64_tr_b16 a[102:103], v204 offset:47368
global_load_lds_dwordx4 v2, s[40:41] offset:-2048
ds_read_b64_tr_b16 a[104:105], v204 offset:45120
global_load_lds_dwordx4 v3, s[40:41] offset:-1024
ds_read_b64_tr_b16 a[106:107], v204 offset:47424
ds_read_b64_tr_b16 a[108:109], v204 offset:45128
global_load_lds_dwordx4 v4, s[40:41] offset:0
ds_read_b64_tr_b16 a[110:111], v204 offset:47432
global_load_lds_dwordx4 v5, s[40:41] offset:1024
ds_read_b64_tr_b16 a[112:113], v204 offset:63488
global_load_lds_dwordx4 v6, s[40:41] offset:2048
ds_read_b64_tr_b16 a[114:115], v205 offset:256
global_load_lds_dwordx4 v7, s[40:41] offset:3072
ds_read_b64_tr_b16 a[116:117], v204 offset:63496
ds_read_b64_tr_b16 a[118:119], v205 offset:264
s_add_u32 m0, s48, 86016
ds_read_b64_tr_b16 a[120:121], v204 offset:63552
global_load_lds_dwordx4 v8, s[40:41] offset:-4096
ds_read_b64_tr_b16 a[122:123], v205 offset:320
s_add_u32 s40, s40, 73728
ds_read_b64_tr_b16 a[124:125], v204 offset:63560
s_addc_u32 s41, s41, 0
ds_read_b64_tr_b16 a[126:127], v205 offset:328
s_waitcnt vmcnt(18) lgkmcnt(0)
s_barrier
s_add_u32 m0, s48, 114688
ds_read_b64_tr_b16 a[128:129], v9 offset:0
ds_read_b64_tr_b16 a[130:131], v9 offset:2304
global_load_lds_dwordx4 v0, s[40:41] offset:-4096
ds_read_b64_tr_b16 a[132:133], v9 offset:8
global_load_lds_dwordx4 v1, s[40:41] offset:-3072
ds_read_b64_tr_b16 a[134:135], v9 offset:2312
ds_read_b64_tr_b16 a[136:137], v9 offset:64
global_load_lds_dwordx4 v2, s[40:41] offset:-2048
ds_read_b64_tr_b16 a[138:139], v9 offset:2368
global_load_lds_dwordx4 v3, s[40:41] offset:-1024
ds_read_b64_tr_b16 a[140:141], v9 offset:72
global_load_lds_dwordx4 v4, s[40:41] offset:0
ds_read_b64_tr_b16 a[142:143], v9 offset:2376
ds_read_b64_tr_b16 a[144:145], v9 offset:18432
global_load_lds_dwordx4 v5, s[40:41] offset:1024
ds_read_b64_tr_b16 a[146:147], v9 offset:20736
global_load_lds_dwordx4 v6, s[40:41] offset:2048
ds_read_b64_tr_b16 a[148:149], v9 offset:18440
ds_read_b64_tr_b16 a[150:151], v9 offset:20744
global_load_lds_dwordx4 v7, s[40:41] offset:3072
ds_read_b64_tr_b16 a[152:153], v9 offset:18496
s_add_u32 m0, s48, 122880
ds_read_b64_tr_b16 a[154:155], v9 offset:20800
global_load_lds_dwordx4 v8, s[40:41] offset:-4096
ds_read_b64_tr_b16 a[156:157], v9 offset:18504
ds_read_b64_tr_b16 a[158:159], v9 offset:20808
s_add_u32 s40, s40, 73728
ds_read_b64_tr_b16 v[60:61], v10 offset:18432
s_addc_u32 s41, s41, 0
ds_read_b64_tr_b16 v[62:63], v10 offset:20736
s_waitcnt vmcnt(18) lgkmcnt(0)
s_barrier
s_add_u32 m0, s48, 4096
ds_read_b64_tr_b16 a[160:161], v9 offset:36864
ds_read_b64_tr_b16 a[162:163], v9 offset:39168
global_load_lds_dwordx4 v0, s[40:41] offset:-4096
ds_read_b64_tr_b16 a[164:165], v9 offset:36872
ds_read_b64_tr_b16 a[166:167], v9 offset:39176
global_load_lds_dwordx4 v1, s[40:41] offset:-3072
ds_read_b64_tr_b16 a[168:169], v9 offset:36928
global_load_lds_dwordx4 v2, s[40:41] offset:-2048
ds_read_b64_tr_b16 a[170:171], v9 offset:39232
ds_read_b64_tr_b16 a[172:173], v9 offset:36936
global_load_lds_dwordx4 v3, s[40:41] offset:-1024
ds_read_b64_tr_b16 a[174:175], v9 offset:39240
global_load_lds_dwordx4 v4, s[40:41] offset:0
ds_read_b64_tr_b16 v[64:65], v10 offset:36864
ds_read_b64_tr_b16 v[66:67], v10 offset:39168
global_load_lds_dwordx4 v5, s[40:41] offset:1024
ds_read_b64_tr_b16 a[176:177], v9 offset:55296
global_load_lds_dwordx4 v6, s[40:41] offset:2048
ds_read_b64_tr_b16 a[178:179], v9 offset:57600
ds_read_b64_tr_b16 a[180:181], v9 offset:55304
global_load_lds_dwordx4 v7, s[40:41] offset:3072
ds_read_b64_tr_b16 a[182:183], v9 offset:57608
s_add_u32 m0, s48, 12288
ds_read_b64_tr_b16 a[184:185], v9 offset:55360
ds_read_b64_tr_b16 a[186:187], v9 offset:57664
global_load_lds_dwordx4 v8, s[40:41] offset:-4096
ds_read_b64_tr_b16 a[188:189], v9 offset:55368
s_sub_u32 s40, s40, 589824
ds_read_b64_tr_b16 a[190:191], v9 offset:57672
ds_read_b64_tr_b16 v[68:69], v10 offset:55296
s_subb_u32 s41, s41, 0
ds_read_b64_tr_b16 v[70:71], v10 offset:57600
s_waitcnt vmcnt(18) lgkmcnt(0)
s_barrier
ds_read_b64_tr_b16 a[192:193], v204 offset:8192
ds_read_b64_tr_b16 a[194:195], v204 offset:10496
ds_read_b64_tr_b16 a[196:197], v204 offset:8200
ds_read_b64_tr_b16 a[198:199], v204 offset:10504
ds_read_b64_tr_b16 a[200:201], v204 offset:8256
ds_read_b64_tr_b16 a[202:203], v204 offset:10560
ds_read_b64_tr_b16 a[204:205], v204 offset:8264
ds_read_b64_tr_b16 a[206:207], v204 offset:10568
ds_read_b64_tr_b16 v[72:73], v206 offset:8192
ds_read_b64_tr_b16 v[74:75], v206 offset:10496
ds_read_b64_tr_b16 a[208:209], v204 offset:26624
ds_read_b64_tr_b16 a[210:211], v204 offset:28928
ds_read_b64_tr_b16 a[212:213], v204 offset:26632
ds_read_b64_tr_b16 a[214:215], v204 offset:28936
ds_read_b64_tr_b16 a[216:217], v204 offset:26688
ds_read_b64_tr_b16 a[218:219], v204 offset:28992
ds_read_b64_tr_b16 a[220:221], v204 offset:26696
ds_read_b64_tr_b16 a[222:223], v204 offset:29000
ds_read_b64_tr_b16 v[76:77], v206 offset:26624
ds_read_b64_tr_b16 v[78:79], v206 offset:28928
s_waitcnt vmcnt(9) lgkmcnt(0)
s_barrier
ds_read_b64_tr_b16 a[224:225], v204 offset:45056
ds_read_b64_tr_b16 a[226:227], v204 offset:47360
ds_read_b64_tr_b16 a[228:229], v204 offset:45064
ds_read_b64_tr_b16 a[230:231], v204 offset:47368
ds_read_b64_tr_b16 a[232:233], v204 offset:45120
ds_read_b64_tr_b16 a[234:235], v204 offset:47424
ds_read_b64_tr_b16 a[236:237], v204 offset:45128
ds_read_b64_tr_b16 a[238:239], v204 offset:47432
ds_read_b64_tr_b16 v[80:81], v206 offset:45056
ds_read_b64_tr_b16 v[82:83], v206 offset:47360
ds_read_b64_tr_b16 a[240:241], v204 offset:63488
ds_read_b64_tr_b16 a[242:243], v205 offset:256
ds_read_b64_tr_b16 a[244:245], v204 offset:63496
ds_read_b64_tr_b16 a[246:247], v205 offset:264
ds_read_b64_tr_b16 a[248:249], v204 offset:63552
ds_read_b64_tr_b16 a[250:251], v205 offset:320
ds_read_b64_tr_b16 a[252:253], v204 offset:63560
ds_read_b64_tr_b16 a[254:255], v205 offset:328
ds_read_b64_tr_b16 v[84:85], v206 offset:63488
ds_read_b64_tr_b16 v[86:87], v207 offset:256
s_waitcnt vmcnt(0) lgkmcnt(0)
s_barrier
ds_read_b64_tr_b16 v[28:29], v9 offset:0
ds_read_b64_tr_b16 v[30:31], v9 offset:2304
ds_read_b64_tr_b16 v[32:33], v9 offset:8
ds_read_b64_tr_b16 v[34:35], v9 offset:2312
ds_read_b64_tr_b16 v[36:37], v9 offset:64
ds_read_b64_tr_b16 v[38:39], v9 offset:2368
ds_read_b64_tr_b16 v[40:41], v9 offset:72
ds_read_b64_tr_b16 v[42:43], v9 offset:2376
ds_read_b64_tr_b16 v[88:89], v10 offset:0
ds_read_b64_tr_b16 v[90:91], v10 offset:2304
ds_read_b64_tr_b16 v[44:45], v9 offset:18432
ds_read_b64_tr_b16 v[46:47], v9 offset:20736
ds_read_b64_tr_b16 v[48:49], v9 offset:18440
ds_read_b64_tr_b16 v[50:51], v9 offset:20744
ds_read_b64_tr_b16 v[52:53], v9 offset:18496
ds_read_b64_tr_b16 v[54:55], v9 offset:20800
ds_read_b64_tr_b16 v[56:57], v9 offset:18504
ds_read_b64_tr_b16 v[58:59], v9 offset:20808
ds_read_b64_tr_b16 v[92:93], v10 offset:18432
ds_read_b64_tr_b16 v[94:95], v10 offset:20736
s_waitcnt lgkmcnt(0)
s_barrier
v_add_lshl_u32 v252, v24, 0, 7
v_bfi_b32 v252, s69, v252, v23
v_add_lshl_u32 v253, v22, 0, 7
v_bfi_b32 v253, s69, v253, v21
s_mov_b32 s92, 0x01010101
s_mov_b32 s93, 0x01010101
s_mov_b64 s[76:77], 0
s_add_u32 m0, s49, 18432
v_add_u32 v13, 18688, v13
s_xor_b32 s49, s49, 0x10000
s_mov_b32 m0, 49152
s_add_u32 m0, s49, 0
v_add_u32 v22, 1, v22
v_add_lshl_u32 v253, v22, 0, 7
v_bfi_b32 v253, s69, v253, v21
v_add_u32 v11, 18432, v11
s_mov_b32 m0, 57344
v_add_u32 v13, 18688, v13
s_add_u32 m0, s49, 18432
s_mov_b64 s[76:77], -1
v_add_u32 v22, 1, v22
v_add_lshl_u32 v253, v22, 0, 7
v_bfi_b32 v253, s69, v253, v21
s_mov_b32 s70, 33
s_waitcnt vmcnt(0) lgkmcnt(0)
s_nop 7
s_branch .Lwo_in12
.p2align 6
.Lwo_top13:
s_waitcnt lgkmcnt(2)
v_mfma_f32_16x16x32_bf16 v[96:99], a[144:147], v[132:135], v[96:99]
ds_read_b128 v[144:147], v25 offset:12288
v_mfma_f32_16x16x32_bf16 v[100:103], a[148:151], v[132:135], v[100:103]
v_mfma_f32_16x16x32_bf16 v[104:107], a[152:155], v[132:135], v[104:107]
s_waitcnt vmcnt(8)
s_barrier
v_mfma_f32_16x16x32_bf16 v[108:111], a[156:159], v[132:135], v[108:111]
v_cvt_pk_bf16_f32 v164, v112, v113
v_cvt_pk_bf16_f32 v165, v114, v115
v_mfma_f32_16x16x32_bf16 v[128:131], v[60:63], v[132:135], 0
s_waitcnt lgkmcnt(2)
v_cvt_pk_bf16_f32 v166, v116, v117
)"
R"(v_cvt_pk_bf16_f32 v167, v118, v119
v_mfma_f32_16x16x32_bf16 v[96:99], a[160:163], v[136:139], v[96:99]
ds_read_b128 v[132:135], v25 offset:13312
v_add_f32 v254, v112, v113
v_mfma_f32_16x16x32_bf16 v[100:103], a[164:167], v[136:139], v[100:103]
global_store_dwordx4 v13, v[164:167], s[38:39] offset:0 sc1
v_mfma_f32_16x16x32_bf16 v[104:107], a[168:171], v[136:139], v[104:107]
v_cvt_pk_bf16_f32 v168, v120, v121
v_cvt_pk_bf16_f32 v169, v122, v123
v_mfma_f32_16x16x32_bf16 v[108:111], a[172:175], v[136:139], v[108:111]
v_cvt_pk_bf16_f32 v170, v124, v125
v_cvt_pk_bf16_f32 v171, v126, v127
v_mfma_f32_16x16x32_bf16 v[128:131], v[64:67], v[136:139], v[128:131]
s_waitcnt lgkmcnt(2)
v_mul_f32 v255, v112, v112
v_fmac_f32 v255, v113, v113
v_mfma_f32_16x16x32_bf16 v[96:99], a[176:179], v[140:143], v[96:99]
s_cmp_eq_u32 s70, 32
ds_read_b128 v[136:139], v25 offset:14336
v_add_f32 v254, v254, v114
v_mfma_f32_16x16x32_bf16 v[100:103], a[180:183], v[140:143], v[100:103]
s_cselect_b64 s[42:43], s[44:45], s[42:43]
global_store_dwordx4 v13, v[168:171], s[38:39] offset:1024 sc1
v_mfma_f32_16x16x32_bf16 v[104:107], a[184:187], v[140:143], v[104:107]
s_cmp_eq_u32 s70, 16
v_add_u32 v13, 18688, v13
v_fmac_f32 v255, v114, v114
v_mfma_f32_16x16x32_bf16 v[108:111], a[188:191], v[140:143], v[108:111]
s_cselect_b64 s[42:43], s[46:47], s[42:43]
v_add_f32 v254, v254, v115
v_fmac_f32 v255, v115, v115
v_mfma_f32_16x16x32_bf16 v[128:131], v[68:71], v[140:143], v[128:131]
s_waitcnt lgkmcnt(2)
v_add_f32 v254, v254, v116
v_fmac_f32 v255, v116, v116
v_mfma_f32_16x16x32_bf16 v[96:99], a[192:195], v[144:147], v[96:99]
s_xor_b32 s49, s49, 0x10000
ds_read_b128 v[140:143], v25 offset:15360
v_add_f32 v254, v254, v117
v_mfma_f32_16x16x32_bf16 v[100:103], a[196:199], v[144:147], v[100:103]
s_mov_b32 m0, 49152
v_fmac_f32 v255, v117, v117
v_add_f32 v254, v254, v118
v_mfma_f32_16x16x32_bf16 v[104:107], a[200:203], v[144:147], v[104:107]
v_fmac_f32 v255, v118, v118
v_add_f32 v254, v254, v119
v_mfma_f32_16x16x32_bf16 v[108:111], a[204:207], v[144:147], v[108:111]
v_fmac_f32 v255, v119, v119
v_add_f32 v254, v254, v120
v_mfma_f32_16x16x32_bf16 v[128:131], v[72:75], v[144:147], v[128:131]
s_waitcnt lgkmcnt(2)
v_fmac_f32 v255, v120, v120
v_add_f32 v254, v254, v121
v_mfma_f32_16x16x32_bf16 v[96:99], a[208:211], v[132:135], v[96:99]
ds_read_b128 v[144:147], v25 offset:16384
global_load_lds_dwordx4 v16, s[42:43] offset:0 sc1
v_mfma_f32_16x16x32_bf16 v[100:103], a[212:215], v[132:135], v[100:103]
v_fmac_f32 v255, v121, v121
v_add_f32 v254, v254, v122
v_mfma_f32_16x16x32_bf16 v[104:107], a[216:219], v[132:135], v[104:107]
v_fmac_f32 v255, v122, v122
v_add_f32 v254, v254, v123
v_mfma_f32_16x16x32_bf16 v[108:111], a[220:223], v[132:135], v[108:111]
v_fmac_f32 v255, v123, v123
v_add_f32 v254, v254, v124
v_mfma_f32_16x16x32_bf16 v[128:131], v[76:79], v[132:135], v[128:131]
s_waitcnt vmcnt(9)
ds_read_b128 v[148:151], v19 offset:8192
ds_read_b128 v[152:155], v20 offset:8192
s_waitcnt lgkmcnt(4)
v_mfma_f32_16x16x32_bf16 v[96:99], a[224:227], v[136:139], v[96:99]
ds_read_b128 v[132:135], v25 offset:17408
v_fmac_f32 v255, v124, v124
v_mfma_f32_16x16x32_bf16 v[100:103], a[228:231], v[136:139], v[100:103]
v_add_f32 v254, v254, v125
v_fmac_f32 v255, v125, v125
v_mfma_f32_16x16x32_bf16 v[104:107], a[232:235], v[136:139], v[104:107]
global_load_lds_dwordx4 v17, s[42:43] offset:1024 sc1
v_add_f32 v254, v254, v126
v_mfma_f32_16x16x32_bf16 v[108:111], a[236:239], v[136:139], v[108:111]
v_fmac_f32 v255, v126, v126
v_add_f32 v254, v254, v127
v_mfma_f32_16x16x32_bf16 v[128:131], v[80:83], v[136:139], v[128:131]
s_waitcnt lgkmcnt(4)
v_fmac_f32 v255, v127, v127
v_mfma_f32_16x16x32_bf16 v[96:99], a[240:243], v[140:143], v[96:99]
ds_read_b128 v[136:139], v25 offset:0
v_mfma_f32_16x16x32_bf16 v[100:103], a[244:247], v[140:143], v[100:103]
ds_write_b64 v27, v[254:255] offset:45568
v_mfma_f32_16x16x32_bf16 v[104:107], a[248:251], v[140:143], v[104:107]
s_add_u32 m0, s49, 0
s_waitcnt lgkmcnt(4)
v_lshlrev_b32 v112, 16, v148
v_and_b32 v113, 0xffff0000, v148
v_mfma_f32_16x16x32_bf16 v[108:111], a[252:255], v[140:143], v[108:111]
v_lshlrev_b32 v114, 16, v149
v_and_b32 v115, 0xffff0000, v149
v_mfma_f32_16x16x32_bf16 v[128:131], v[84:87], v[140:143], v[128:131]
v_lshlrev_b32 v116, 16, v150
v_and_b32 v117, 0xffff0000, v150
v_mfma_f32_16x16x32_bf16 v[96:99], v[28:31], v[144:147], v[96:99]
s_sub_u32 s42, s42, 18432
ds_read_b128 v[140:143], v25 offset:1024
v_lshlrev_b32 v118, 16, v151
v_mfma_f32_16x16x32_bf16 v[100:103], v[32:35], v[144:147], v[100:103]
s_subb_u32 s43, s43, 0
v_and_b32 v119, 0xffff0000, v151
s_waitcnt lgkmcnt(4)
v_lshlrev_b32 v120, 16, v152
v_mfma_f32_16x16x32_bf16 v[104:107], v[36:39], v[144:147], v[104:107]
v_and_b32 v121, 0xffff0000, v152
v_lshlrev_b32 v122, 16, v153
v_mfma_f32_16x16x32_bf16 v[108:111], v[40:43], v[144:147], v[108:111]
v_and_b32 v123, 0xffff0000, v153
v_lshlrev_b32 v124, 16, v154
v_mfma_f32_16x16x32_bf16 v[128:131], v[88:91], v[144:147], v[128:131]
s_waitcnt lgkmcnt(3)
v_and_b32 v125, 0xffff0000, v154
v_lshlrev_b32 v126, 16, v155
v_mfma_f32_16x16x32_bf16 v[96:99], v[44:47], v[132:135], v[96:99]
ds_read_b128 v[144:147], v25 offset:2048
v_and_b32 v127, 0xffff0000, v155
v_mfma_f32_16x16x32_bf16 v[100:103], v[48:51], v[132:135], v[100:103]
v_mfma_f32_16x16x32_bf16 v[104:107], v[52:55], v[132:135], v[104:107]
v_mfma_f32_16x16x32_bf16 v[108:111], v[56:59], v[132:135], v[108:111]
v_mfma_f32_16x16x32_bf16 v[128:131], v[92:95], v[132:135], v[128:131]
s_waitcnt lgkmcnt(3)
v_mfma_f32_16x16x32_bf16 v[96:99], a[0:3], v[136:139], v[96:99]
ds_read_b128 v[132:135], v25 offset:3072
v_mfma_f32_16x16x32_bf16 v[100:103], a[4:7], v[136:139], v[100:103]
v_mfma_f32_16x16x32_bf16 v[104:107], a[8:11], v[136:139], v[104:107]
v_mfma_f32_16x16x32_bf16 v[108:111], a[12:15], v[136:139], v[108:111]
s_waitcnt lgkmcnt(2)
v_mfma_f32_16x16x32_bf16 v[96:99], a[16:19], v[140:143], v[96:99]
ds_read_b128 v[136:139], v25 offset:4096
v_mfma_f32_16x16x32_bf16 v[100:103], a[20:23], v[140:143], v[100:103]
s_waitcnt vmcnt(6)
v_sub_u32 v200, v200, v22
v_cmp_lt_u32 vcc, 0x7f, v200
v_mfma_f32_16x16x32_bf16 v[104:107], a[24:27], v[140:143], v[104:107]
v_mfma_f32_16x16x32_bf16 v[108:111], a[28:31], v[140:143], v[108:111]
s_waitcnt lgkmcnt(2)
v_mfma_f32_16x16x32_bf16 v[96:99], a[32:35], v[144:147], v[96:99]
s_and_b64 vcc, vcc, s[76:77]
s_cbranch_vccnz .Lwo_sp8
.Lwo_rt9:
ds_read_b128 v[140:143], v25 offset:5120
v_mfma_f32_16x16x32_bf16 v[100:103], a[36:39], v[144:147], v[100:103]
v_mfma_f32_16x16x32_bf16 v[104:107], a[40:43], v[144:147], v[104:107]
s_barrier
v_mfma_f32_16x16x32_bf16 v[108:111], a[44:47], v[144:147], v[108:111]
s_waitcnt lgkmcnt(2)
ds_write_b128 v26, v[128:131] offset:37888
v_mfma_f32_16x16x32_bf16 v[96:99], a[48:51], v[132:135], v[96:99]
ds_read_b128 v[144:147], v25 offset:6144
v_add_u32 v22, 1, v22
v_mfma_f32_16x16x32_bf16 v[100:103], a[52:55], v[132:135], v[100:103]
v_add_lshl_u32 v253, v22, 0, 7
v_bfi_b32 v253, s69, v253, v21
v_mfma_f32_16x16x32_bf16 v[104:107], a[56:59], v[132:135], v[104:107]
global_load_lds_dwordx4 v11, s[38:39] offset:-2048 sc1
v_mfma_f32_16x16x32_bf16 v[108:111], a[60:63], v[132:135], v[108:111]
s_waitcnt lgkmcnt(3)
v_mfma_f32_16x16x32_bf16 v[96:99], a[64:67], v[136:139], v[96:99]
ds_read_b128 v[132:135], v25 offset:7168
v_mfma_f32_16x16x32_bf16 v[100:103], a[68:71], v[136:139], v[100:103]
global_load_lds_dwordx4 v11, s[38:39] offset:-1024 sc1
v_mfma_f32_16x16x32_bf16 v[104:107], a[72:75], v[136:139], v[104:107]
v_mfma_f32_16x16x32_bf16 v[108:111], a[76:79], v[136:139], v[108:111]
s_waitcnt lgkmcnt(3)
v_mfma_f32_16x16x32_bf16 v[96:99], a[80:83], v[140:143], v[96:99]
ds_read_b128 v[136:139], v25 offset:8192
global_load_lds_dwordx4 v11, s[38:39] offset:0 sc1
v_mfma_f32_16x16x32_bf16 v[100:103], a[84:87], v[140:143], v[100:103]
v_mfma_f32_16x16x32_bf16 v[104:107], a[88:91], v[140:143], v[104:107]
v_mfma_f32_16x16x32_bf16 v[108:111], a[92:95], v[140:143], v[108:111]
s_waitcnt lgkmcnt(2)
global_load_dword v201, v253, s[38:39] sc1
global_load_lds_dwordx4 v11, s[38:39] offset:1024 sc1
v_mfma_f32_16x16x32_bf16 v[96:99], a[96:99], v[144:147], v[96:99]
v_mfma_f32_16x16x32_bf16 v[100:103], a[100:103], v[144:147], v[100:103]
v_mfma_f32_16x16x32_bf16 v[104:107], a[104:107], v[144:147], v[104:107]
s_waitcnt vmcnt(9)
s_barrier
v_mfma_f32_16x16x32_bf16 v[108:111], a[108:111], v[144:147], v[108:111]
s_waitcnt lgkmcnt(1)
ds_read_b128 v[140:143], v25 offset:27648
global_load_lds_dwordx4 v11, s[38:39] offset:2048 sc1
v_mfma_f32_16x16x32_bf16 v[96:99], a[112:115], v[132:135], v[96:99]
ds_read_b128 v[144:147], v25 offset:28672
v_add_u32 v11, 18432, v11
v_mfma_f32_16x16x32_bf16 v[100:103], a[116:119], v[132:135], v[100:103]
v_min_u32 v11, v11, v12
v_mfma_f32_16x16x32_bf16 v[104:107], a[120:123], v[132:135], v[104:107]
v_mfma_f32_16x16x32_bf16 v[108:111], a[124:127], v[132:135], v[108:111]
s_waitcnt lgkmcnt(2)
v_mfma_f32_16x16x32_bf16 v[96:99], a[128:131], v[136:139], v[96:99]
ds_read_b128 v[132:135], v25 offset:29696
v_mfma_f32_16x16x32_bf16 v[100:103], a[132:135], v[136:139], v[100:103]
v_mfma_f32_16x16x32_bf16 v[104:107], a[136:139], v[136:139], v[104:107]
v_mfma_f32_16x16x32_bf16 v[108:111], a[140:143], v[136:139], v[108:111]
s_waitcnt lgkmcnt(2)
v_mfma_f32_16x16x32_bf16 v[112:115], a[144:147], v[140:143], v[112:115]
ds_read_b128 v[136:139], v25 offset:30720
v_mfma_f32_16x16x32_bf16 v[116:119], a[148:151], v[140:143], v[116:119]
v_mfma_f32_16x16x32_bf16 v[120:123], a[152:155], v[140:143], v[120:123]
s_waitcnt vmcnt(8)
s_barrier
v_mfma_f32_16x16x32_bf16 v[124:127], a[156:159], v[140:143], v[124:127]
v_cvt_pk_bf16_f32 v164, v96, v97
v_cvt_pk_bf16_f32 v165, v98, v99
v_mfma_f32_16x16x32_bf16 v[128:131], v[60:63], v[140:143], 0
s_waitcnt lgkmcnt(2)
v_cvt_pk_bf16_f32 v166, v100, v101
v_cvt_pk_bf16_f32 v167, v102, v103
v_mfma_f32_16x16x32_bf16 v[112:115], a[160:163], v[144:147], v[112:115]
ds_read_b128 v[140:143], v25 offset:31744
v_add_f32 v254, v96, v97
v_mfma_f32_16x16x32_bf16 v[116:119], a[164:167], v[144:147], v[116:119]
global_store_dwordx4 v13, v[164:167], s[38:39] offset:0 sc1
v_mfma_f32_16x16x32_bf16 v[120:123], a[168:171], v[144:147], v[120:123]
v_cvt_pk_bf16_f32 v168, v104, v105
v_cvt_pk_bf16_f32 v169, v106, v107
v_mfma_f32_16x16x32_bf16 v[124:127], a[172:175], v[144:147], v[124:127]
v_cvt_pk_bf16_f32 v170, v108, v109
v_cvt_pk_bf16_f32 v171, v110, v111
v_mfma_f32_16x16x32_bf16 v[128:131], v[64:67], v[144:147], v[128:131]
s_waitcnt lgkmcnt(2)
v_mul_f32 v255, v96, v96
v_fmac_f32 v255, v97, v97
v_mfma_f32_16x16x32_bf16 v[112:115], a[176:179], v[132:135], v[112:115]
s_mov_b32 m0, 57344
ds_read_b128 v[144:147], v25 offset:32768
v_add_f32 v254, v254, v98
v_mfma_f32_16x16x32_bf16 v[116:119], a[180:183], v[132:135], v[116:119]
global_store_dwordx4 v13, v[168:171], s[38:39] offset:1024 sc1
v_mfma_f32_16x16x32_bf16 v[120:123], a[184:187], v[132:135], v[120:123]
v_add_u32 v13, 18688, v13
v_fmac_f32 v255, v98, v98
v_mfma_f32_16x16x32_bf16 v[124:127], a[188:191], v[132:135], v[124:127]
v_add_f32 v254, v254, v99
v_fmac_f32 v255, v99, v99
v_mfma_f32_16x16x32_bf16 v[128:131], v[68:71], v[132:135], v[128:131]
s_waitcnt lgkmcnt(2)
v_add_f32 v254, v254, v100
v_fmac_f32 v255, v100, v100
v_mfma_f32_16x16x32_bf16 v[112:115], a[192:195], v[136:139], v[112:115]
ds_read_b128 v[132:135], v25 offset:33792
v_add_f32 v254, v254, v101
v_mfma_f32_16x16x32_bf16 v[116:119], a[196:199], v[136:139], v[116:119]
v_fmac_f32 v255, v101, v101
v_add_f32 v254, v254, v102
v_mfma_f32_16x16x32_bf16 v[120:123], a[200:203], v[136:139], v[120:123]
v_fmac_f32 v255, v102, v102
v_add_f32 v254, v254, v103
v_mfma_f32_16x16x32_bf16 v[124:127], a[204:207], v[136:139], v[124:127]
v_fmac_f32 v255, v103, v103
v_add_f32 v254, v254, v104
v_mfma_f32_16x16x32_bf16 v[128:131], v[72:75], v[136:139], v[128:131]
s_waitcnt lgkmcnt(2)
v_fmac_f32 v255, v104, v104
v_add_f32 v254, v254, v105
v_mfma_f32_16x16x32_bf16 v[112:115], a[208:211], v[140:143], v[112:115]
ds_read_b128 v[136:139], v25 offset:34816
global_load_lds_dwordx4 v16, s[42:43] offset:0 sc1
v_mfma_f32_16x16x32_bf16 v[116:119], a[212:215], v[140:143], v[116:119]
v_fmac_f32 v255, v105, v105
v_add_f32 v254, v254, v106
v_mfma_f32_16x16x32_bf16 v[120:123], a[216:219], v[140:143], v[120:123]
v_fmac_f32 v255, v106, v106
v_add_f32 v254, v254, v107
v_mfma_f32_16x16x32_bf16 v[124:127], a[220:223], v[140:143], v[124:127]
v_fmac_f32 v255, v107, v107
v_add_f32 v254, v254, v108
v_mfma_f32_16x16x32_bf16 v[128:131], v[76:79], v[140:143], v[128:131]
s_waitcnt vmcnt(9)
ds_read_b128 v[148:151], v19 offset:0
ds_read_b128 v[152:155], v20 offset:0
s_waitcnt lgkmcnt(4)
v_mfma_f32_16x16x32_bf16 v[112:115], a[224:227], v[144:147], v[112:115]
ds_read_b128 v[140:143], v25 offset:35840
v_fmac_f32 v255, v108, v108
v_mfma_f32_16x16x32_bf16 v[116:119], a[228:231], v[144:147], v[116:119]
v_add_f32 v254, v254, v109
v_fmac_f32 v255, v109, v109
v_mfma_f32_16x16x32_bf16 v[120:123], a[232:235], v[144:147], v[120:123]
global_load_lds_dwordx4 v17, s[42:43] offset:1024 sc1
v_add_f32 v254, v254, v110
v_mfma_f32_16x16x32_bf16 v[124:127], a[236:239], v[144:147], v[124:127]
v_fmac_f32 v255, v110, v110
v_add_f32 v254, v254, v111
v_mfma_f32_16x16x32_bf16 v[128:131], v[80:83], v[144:147], v[128:131]
s_waitcnt lgkmcnt(4)
v_fmac_f32 v255, v111, v111
v_mfma_f32_16x16x32_bf16 v[112:115], a[240:243], v[132:135], v[112:115]
ds_read_b128 v[144:147], v25 offset:18432
v_mfma_f32_16x16x32_bf16 v[116:119], a[244:247], v[132:135], v[116:119]
ds_write_b64 v27, v[254:255] offset:43520
v_mfma_f32_16x16x32_bf16 v[120:123], a[248:251], v[132:135], v[120:123]
s_add_u32 m0, s49, 18432
s_waitcnt lgkmcnt(4)
v_lshlrev_b32 v96, 16, v148
v_and_b32 v97, 0xffff0000, v148
v_mfma_f32_16x16x32_bf16 v[124:127], a[252:255], v[132:135], v[124:127]
v_lshlrev_b32 v98, 16, v149
v_and_b32 v99, 0xffff0000, v149
v_mfma_f32_16x16x32_bf16 v[128:131], v[84:87], v[132:135], v[128:131]
v_lshlrev_b32 v100, 16, v150
v_and_b32 v101, 0xffff0000, v150
v_mfma_f32_16x16x32_bf16 v[112:115], v[28:31], v[136:139], v[112:115]
s_sub_u32 s42, s42, 18432
ds_read_b128 v[132:135], v25 offset:19456
v_lshlrev_b32 v102, 16, v151
v_mfma_f32_16x16x32_bf16 v[116:119], v[32:35], v[136:139], v[116:119]
s_subb_u32 s43, s43, 0
v_and_b32 v103, 0xffff0000, v151
s_waitcnt lgkmcnt(4)
v_lshlrev_b32 v104, 16, v152
v_mfma_f32_16x16x32_bf16 v[120:123], v[36:39], v[136:139], v[120:123]
v_and_b32 v105, 0xffff0000, v152
v_lshlrev_b32 v106, 16, v153
v_mfma_f32_16x16x32_bf16 v[124:127], v[40:43], v[136:139], v[124:127]
v_and_b32 v107, 0xffff0000, v153
v_lshlrev_b32 v108, 16, v154
v_mfma_f32_16x16x32_bf16 v[128:131], v[88:91], v[136:139], v[128:131]
s_waitcnt lgkmcnt(3)
v_and_b32 v109, 0xffff0000, v154
v_lshlrev_b32 v110, 16, v155
v_mfma_f32_16x16x32_bf16 v[112:115], v[44:47], v[140:143], v[112:115]
ds_read_b128 v[136:139], v25 offset:20480
v_and_b32 v111, 0xffff0000, v155
v_mfma_f32_16x16x32_bf16 v[116:119], v[48:51], v[140:143], v[116:119]
v_mfma_f32_16x16x32_bf16 v[120:123], v[52:55], v[140:143], v[120:123]
v_mfma_f32_16x16x32_bf16 v[124:127], v[56:59], v[140:143], v[124:127]
v_mfma_f32_16x16x32_bf16 v[128:131], v[92:95], v[140:143], v[128:131]
s_waitcnt lgkmcnt(3)
v_mfma_f32_16x16x32_bf16 v[112:115], a[0:3], v[144:147], v[112:115]
ds_read_b128 v[140:143], v25 offset:21504
v_mfma_f32_16x16x32_bf16 v[116:119], a[4:7], v[144:147], v[116:119]
v_mfma_f32_16x16x32_bf16 v[120:123], a[8:11], v[144:147], v[120:123]
v_mfma_f32_16x16x32_bf16 v[124:127], a[12:15], v[144:147], v[124:127]
s_waitcnt lgkmcnt(2)
v_mfma_f32_16x16x32_bf16 v[112:115], a[16:19], v[132:135], v[112:115]
ds_read_b128 v[144:147], v25 offset:22528
v_mfma_f32_16x16x32_bf16 v[116:119], a[20:23], v[132:135], v[116:119]
s_waitcnt vmcnt(6)
v_sub_u32 v201, v201, v22
v_cmp_lt_u32 vcc, 0x7f, v201
v_mfma_f32_16x16x32_bf16 v[120:123], a[24:27], v[132:135], v[120:123]
v_mfma_f32_16x16x32_bf16 v[124:127], a[28:31], v[132:135], v[124:127]
s_waitcnt lgkmcnt(2)
v_mfma_f32_16x16x32_bf16 v[112:115], a[32:35], v[136:139], v[112:115]
s_and_b64 vcc, vcc, s[76:77]
s_cbranch_vccnz .Lwo_sp10
.Lwo_rt11:
ds_read_b128 v[132:135], v25 offset:23552
v_mfma_f32_16x16x32_bf16 v[116:119], a[36:39], v[136:139], v[116:119]
v_mfma_f32_16x16x32_bf16 v[120:123], a[40:43], v[136:139], v[120:123]
s_barrier
v_mfma_f32_16x16x32_bf16 v[124:127], a[44:47], v[136:139], v[124:127]
s_waitcnt lgkmcnt(2)
ds_write_b128 v26, v[128:131] offset:40960
v_mfma_f32_16x16x32_bf16 v[112:115], a[48:51], v[140:143], v[112:115]
s_mov_b64 s[76:77], -1
ds_read_b128 v[136:139], v25 offset:24576
v_add_u32 v22, 1, v22
v_mfma_f32_16x16x32_bf16 v[116:119], a[52:55], v[140:143], v[116:119]
v_add_lshl_u32 v253, v22, 0, 7
v_bfi_b32 v253, s69, v253, v21
v_mfma_f32_16x16x32_bf16 v[120:123], a[56:59], v[140:143], v[120:123]
global_load_lds_dwordx4 v11, s[38:39] offset:-2048 sc1
.Lwo_in12:
v_mfma_f32_16x16x32_bf16 v[124:127], a[60:63], v[140:143], v[124:127]
s_waitcnt lgkmcnt(3)
v_mfma_f32_16x16x32_bf16 v[112:115], a[64:67], v[144:147], v[112:115]
ds_read_b128 v[140:143], v25 offset:25600
v_mfma_f32_16x16x32_bf16 v[116:119], a[68:71], v[144:147], v[116:119]
global_load_lds_dwordx4 v11, s[38:39] offset:-1024 sc1
v_mfma_f32_16x16x32_bf16 v[120:123], a[72:75], v[144:147], v[120:123]
v_mfma_f32_16x16x32_bf16 v[124:127], a[76:79], v[144:147], v[124:127]
s_waitcnt lgkmcnt(3)
v_mfma_f32_16x16x32_bf16 v[112:115], a[80:83], v[132:135], v[112:115]
ds_read_b128 v[144:147], v25 offset:26624
global_load_lds_dwordx4 v11, s[38:39] offset:0 sc1
v_mfma_f32_16x16x32_bf16 v[116:119], a[84:87], v[132:135], v[116:119]
v_mfma_f32_16x16x32_bf16 v[120:123], a[88:91], v[132:135], v[120:123]
v_mfma_f32_16x16x32_bf16 v[124:127], a[92:95], v[132:135], v[124:127]
s_waitcnt lgkmcnt(2)
v_xor_b32 v25, 0x10000, v25
global_load_dword v200, v253, s[38:39] sc1
v_mfma_f32_16x16x32_bf16 v[112:115], a[96:99], v[136:139], v[112:115]
global_load_lds_dwordx4 v11, s[38:39] offset:1024 sc1
v_mfma_f32_16x16x32_bf16 v[116:119], a[100:103], v[136:139], v[116:119]
v_mfma_f32_16x16x32_bf16 v[120:123], a[104:107], v[136:139], v[120:123]
s_waitcnt vmcnt(9)
s_barrier
v_mfma_f32_16x16x32_bf16 v[124:127], a[108:111], v[136:139], v[124:127]
s_waitcnt lgkmcnt(1)
ds_read_b128 v[132:135], v25 offset:9216
global_load_lds_dwordx4 v11, s[38:39] offset:2048 sc1
)"
R"(v_mfma_f32_16x16x32_bf16 v[112:115], a[112:115], v[140:143], v[112:115]
ds_read_b128 v[136:139], v25 offset:10240
v_add_u32 v11, 18432, v11
v_mfma_f32_16x16x32_bf16 v[116:119], a[116:119], v[140:143], v[116:119]
v_min_u32 v11, v11, v12
v_mfma_f32_16x16x32_bf16 v[120:123], a[120:123], v[140:143], v[120:123]
v_mfma_f32_16x16x32_bf16 v[124:127], a[124:127], v[140:143], v[124:127]
s_waitcnt lgkmcnt(2)
v_mfma_f32_16x16x32_bf16 v[112:115], a[128:131], v[144:147], v[112:115]
ds_read_b128 v[140:143], v25 offset:11264
v_mfma_f32_16x16x32_bf16 v[116:119], a[132:135], v[144:147], v[116:119]
v_mfma_f32_16x16x32_bf16 v[120:123], a[136:139], v[144:147], v[120:123]
v_mfma_f32_16x16x32_bf16 v[124:127], a[140:143], v[144:147], v[124:127]
s_sub_u32 s70, s70, 1
s_cbranch_scc0 .Lwo_top13
s_waitcnt vmcnt(8)
s_barrier
v_cvt_pk_bf16_f32 v164, v112, v113
v_cvt_pk_bf16_f32 v165, v114, v115
v_cvt_pk_bf16_f32 v166, v116, v117
v_cvt_pk_bf16_f32 v167, v118, v119
global_store_dwordx4 v13, v[164:167], s[38:39] offset:0 sc1
v_cvt_pk_bf16_f32 v168, v120, v121
v_cvt_pk_bf16_f32 v169, v122, v123
v_cvt_pk_bf16_f32 v170, v124, v125
v_cvt_pk_bf16_f32 v171, v126, v127
global_store_dwordx4 v13, v[168:171], s[38:39] offset:1024 sc1
v_add_f32 v254, v112, v113
v_mul_f32 v255, v112, v112
v_fmac_f32 v255, v113, v113
v_add_f32 v254, v254, v114
v_fmac_f32 v255, v114, v114
v_add_f32 v254, v254, v115
v_fmac_f32 v255, v115, v115
v_add_f32 v254, v254, v116
v_fmac_f32 v255, v116, v116
v_add_f32 v254, v254, v117
v_fmac_f32 v255, v117, v117
v_add_f32 v254, v254, v118
v_fmac_f32 v255, v118, v118
v_add_f32 v254, v254, v119
v_fmac_f32 v255, v119, v119
v_add_f32 v254, v254, v120
v_fmac_f32 v255, v120, v120
v_add_f32 v254, v254, v121
v_fmac_f32 v255, v121, v121
v_add_f32 v254, v254, v122
v_fmac_f32 v255, v122, v122
v_add_f32 v254, v254, v123
v_fmac_f32 v255, v123, v123
v_add_f32 v254, v254, v124
v_fmac_f32 v255, v124, v124
v_add_f32 v254, v254, v125
v_fmac_f32 v255, v125, v125
v_add_f32 v254, v254, v126
v_fmac_f32 v255, v126, v126
v_add_f32 v254, v254, v127
v_fmac_f32 v255, v127, v127
ds_write_b64 v27, v[254:255] offset:45568
s_waitcnt vmcnt(0) lgkmcnt(0)
s_barrier
s_barrier
s_endpgm
.Lwo_w3:
s_add_u32 m0, s48, 4096
s_nop 0
global_load_lds_dwordx4 v0, s[40:41] offset:-4096
global_load_lds_dwordx4 v1, s[40:41] offset:-3072
global_load_lds_dwordx4 v2, s[40:41] offset:-2048
global_load_lds_dwordx4 v3, s[40:41] offset:-1024
global_load_lds_dwordx4 v4, s[40:41] offset:0
global_load_lds_dwordx4 v5, s[40:41] offset:1024
global_load_lds_dwordx4 v6, s[40:41] offset:2048
global_load_lds_dwordx4 v7, s[40:41] offset:3072
s_add_u32 m0, s48, 12288
s_nop 0
global_load_lds_dwordx4 v8, s[40:41] offset:-4096
s_add_u32 s40, s40, 73728
s_addc_u32 s41, s41, 0
s_add_u32 m0, s48, 40960
s_nop 0
global_load_lds_dwordx4 v0, s[40:41] offset:-4096
global_load_lds_dwordx4 v1, s[40:41] offset:-3072
global_load_lds_dwordx4 v2, s[40:41] offset:-2048
global_load_lds_dwordx4 v3, s[40:41] offset:-1024
global_load_lds_dwordx4 v4, s[40:41] offset:0
global_load_lds_dwordx4 v5, s[40:41] offset:1024
global_load_lds_dwordx4 v6, s[40:41] offset:2048
global_load_lds_dwordx4 v7, s[40:41] offset:3072
s_add_u32 m0, s48, 49152
s_nop 0
global_load_lds_dwordx4 v8, s[40:41] offset:-4096
s_add_u32 s40, s40, 73728
s_addc_u32 s41, s41, 0
s_add_u32 m0, s48, 77824
s_nop 0
global_load_lds_dwordx4 v0, s[40:41] offset:-4096
global_load_lds_dwordx4 v1, s[40:41] offset:-3072
global_load_lds_dwordx4 v2, s[40:41] offset:-2048
global_load_lds_dwordx4 v3, s[40:41] offset:-1024
global_load_lds_dwordx4 v4, s[40:41] offset:0
global_load_lds_dwordx4 v5, s[40:41] offset:1024
global_load_lds_dwordx4 v6, s[40:41] offset:2048
global_load_lds_dwordx4 v7, s[40:41] offset:3072
s_add_u32 m0, s48, 86016
s_nop 0
global_load_lds_dwordx4 v8, s[40:41] offset:-4096
s_add_u32 s40, s40, 73728
s_addc_u32 s41, s41, 0
v_add_u32 v204, 0x10000, v9
v_add_u32 v205, 0x20000, v9
v_add_u32 v206, 0x10000, v10
v_add_u32 v207, 0x20000, v10
s_waitcnt lgkmcnt(0)
s_mul_i32 s37, s37, 0x9E3779B1
s_add_u32 s37, s37, 0x7F4A7C15
v_add_u32 v22, s37, v22
v_add_u32 v24, s37, v24
s_waitcnt vmcnt(18)
s_barrier
s_add_u32 m0, s48, 114688
ds_read_b64_tr_b16 a[0:1], v9 offset:0
ds_read_b64_tr_b16 a[2:3], v9 offset:2304
global_load_lds_dwordx4 v0, s[40:41] offset:-4096
ds_read_b64_tr_b16 a[4:5], v9 offset:8
ds_read_b64_tr_b16 a[6:7], v9 offset:2312
global_load_lds_dwordx4 v1, s[40:41] offset:-3072
ds_read_b64_tr_b16 a[8:9], v9 offset:64
global_load_lds_dwordx4 v2, s[40:41] offset:-2048
ds_read_b64_tr_b16 a[10:11], v9 offset:2368
ds_read_b64_tr_b16 a[12:13], v9 offset:72
global_load_lds_dwordx4 v3, s[40:41] offset:-1024
ds_read_b64_tr_b16 a[14:15], v9 offset:2376
global_load_lds_dwordx4 v4, s[40:41] offset:0
ds_read_b64_tr_b16 v[60:61], v10 offset:0
ds_read_b64_tr_b16 v[62:63], v10 offset:2304
global_load_lds_dwordx4 v5, s[40:41] offset:1024
ds_read_b64_tr_b16 a[16:17], v9 offset:18432
global_load_lds_dwordx4 v6, s[40:41] offset:2048
ds_read_b64_tr_b16 a[18:19], v9 offset:20736
ds_read_b64_tr_b16 a[20:21], v9 offset:18440
global_load_lds_dwordx4 v7, s[40:41] offset:3072
ds_read_b64_tr_b16 a[22:23], v9 offset:20744
s_add_u32 m0, s48, 122880
ds_read_b64_tr_b16 a[24:25], v9 offset:18496
ds_read_b64_tr_b16 a[26:27], v9 offset:20800
global_load_lds_dwordx4 v8, s[40:41] offset:-4096
ds_read_b64_tr_b16 a[28:29], v9 offset:18504
s_add_u32 s40, s40, 73728
ds_read_b64_tr_b16 a[30:31], v9 offset:20808
ds_read_b64_tr_b16 v[64:65], v10 offset:18432
s_addc_u32 s41, s41, 0
ds_read_b64_tr_b16 v[66:67], v10 offset:20736
s_waitcnt vmcnt(18) lgkmcnt(0)
s_barrier
s_add_u32 m0, s48, 4096
ds_read_b64_tr_b16 a[32:33], v9 offset:36864
ds_read_b64_tr_b16 a[34:35], v9 offset:39168
global_load_lds_dwordx4 v0, s[40:41] offset:-4096
ds_read_b64_tr_b16 a[36:37], v9 offset:36872
ds_read_b64_tr_b16 a[38:39], v9 offset:39176
global_load_lds_dwordx4 v1, s[40:41] offset:-3072
ds_read_b64_tr_b16 a[40:41], v9 offset:36928
global_load_lds_dwordx4 v2, s[40:41] offset:-2048
ds_read_b64_tr_b16 a[42:43], v9 offset:39232
ds_read_b64_tr_b16 a[44:45], v9 offset:36936
global_load_lds_dwordx4 v3, s[40:41] offset:-1024
ds_read_b64_tr_b16 a[46:47], v9 offset:39240
global_load_lds_dwordx4 v4, s[40:41] offset:0
ds_read_b64_tr_b16 v[68:69], v10 offset:36864
ds_read_b64_tr_b16 v[70:71], v10 offset:39168
global_load_lds_dwordx4 v5, s[40:41] offset:1024
ds_read_b64_tr_b16 a[48:49], v9 offset:55296
global_load_lds_dwordx4 v6, s[40:41] offset:2048
ds_read_b64_tr_b16 a[50:51], v9 offset:57600
ds_read_b64_tr_b16 a[52:53], v9 offset:55304
global_load_lds_dwordx4 v7, s[40:41] offset:3072
ds_read_b64_tr_b16 a[54:55], v9 offset:57608
s_add_u32 m0, s48, 12288
ds_read_b64_tr_b16 a[56:57], v9 offset:55360
ds_read_b64_tr_b16 a[58:59], v9 offset:57664
global_load_lds_dwordx4 v8, s[40:41] offset:-4096
ds_read_b64_tr_b16 a[60:61], v9 offset:55368
s_add_u32 s40, s40, 73728
ds_read_b64_tr_b16 a[62:63], v9 offset:57672
ds_read_b64_tr_b16 v[72:73], v10 offset:55296
s_addc_u32 s41, s41, 0
ds_read_b64_tr_b16 v[74:75], v10 offset:57600
s_waitcnt vmcnt(18) lgkmcnt(0)
s_barrier
s_add_u32 m0, s48, 40960
ds_read_b64_tr_b16 a[64:65], v204 offset:8192
ds_read_b64_tr_b16 a[66:67], v204 offset:10496
global_load_lds_dwordx4 v0, s[40:41] offset:-4096
ds_read_b64_tr_b16 a[68:69], v204 offset:8200
ds_read_b64_tr_b16 a[70:71], v204 offset:10504
global_load_lds_dwordx4 v1, s[40:41] offset:-3072
ds_read_b64_tr_b16 a[72:73], v204 offset:8256
global_load_lds_dwordx4 v2, s[40:41] offset:-2048
ds_read_b64_tr_b16 a[74:75], v204 offset:10560
ds_read_b64_tr_b16 a[76:77], v204 offset:8264
global_load_lds_dwordx4 v3, s[40:41] offset:-1024
ds_read_b64_tr_b16 a[78:79], v204 offset:10568
global_load_lds_dwordx4 v4, s[40:41] offset:0
ds_read_b64_tr_b16 v[76:77], v206 offset:8192
ds_read_b64_tr_b16 v[78:79], v206 offset:10496
global_load_lds_dwordx4 v5, s[40:41] offset:1024
ds_read_b64_tr_b16 a[80:81], v204 offset:26624
global_load_lds_dwordx4 v6, s[40:41] offset:2048
ds_read_b64_tr_b16 a[82:83], v204 offset:28928
ds_read_b64_tr_b16 a[84:85], v204 offset:26632
global_load_lds_dwordx4 v7, s[40:41] offset:3072
ds_read_b64_tr_b16 a[86:87], v204 offset:28936
s_add_u32 m0, s48, 49152
ds_read_b64_tr_b16 a[88:89], v204 offset:26688
ds_read_b64_tr_b16 a[90:91], v204 offset:28992
global_load_lds_dwordx4 v8, s[40:41] offset:-4096
ds_read_b64_tr_b16 a[92:93], v204 offset:26696
s_add_u32 s40, s40, 73728
ds_read_b64_tr_b16 a[94:95], v204 offset:29000
ds_read_b64_tr_b16 v[80:81], v206 offset:26624
s_addc_u32 s41, s41, 0
ds_read_b64_tr_b16 v[82:83], v206 offset:28928
s_waitcnt vmcnt(18) lgkmcnt(0)
s_barrier
s_add_u32 m0, s48, 77824
ds_read_b64_tr_b16 a[96:97], v204 offset:45056
ds_read_b64_tr_b16 a[98:99], v204 offset:47360
global_load_lds_dwordx4 v0, s[40:41] offset:-4096
ds_read_b64_tr_b16 a[100:101], v204 offset:45064
ds_read_b64_tr_b16 a[102:103], v204 offset:47368
global_load_lds_dwordx4 v1, s[40:41] offset:-3072
ds_read_b64_tr_b16 a[104:105], v204 offset:45120
global_load_lds_dwordx4 v2, s[40:41] offset:-2048
ds_read_b64_tr_b16 a[106:107], v204 offset:47424
ds_read_b64_tr_b16 a[108:109], v204 offset:45128
global_load_lds_dwordx4 v3, s[40:41] offset:-1024
ds_read_b64_tr_b16 a[110:111], v204 offset:47432
global_load_lds_dwordx4 v4, s[40:41] offset:0
ds_read_b64_tr_b16 v[84:85], v206 offset:45056
ds_read_b64_tr_b16 v[86:87], v206 offset:47360
global_load_lds_dwordx4 v5, s[40:41] offset:1024
ds_read_b64_tr_b16 a[112:113], v204 offset:63488
global_load_lds_dwordx4 v6, s[40:41] offset:2048
ds_read_b64_tr_b16 a[114:115], v205 offset:256
ds_read_b64_tr_b16 a[116:117], v204 offset:63496
global_load_lds_dwordx4 v7, s[40:41] offset:3072
ds_read_b64_tr_b16 a[118:119], v205 offset:264
s_add_u32 m0, s48, 86016
ds_read_b64_tr_b16 a[120:121], v204 offset:63552
ds_read_b64_tr_b16 a[122:123], v205 offset:320
global_load_lds_dwordx4 v8, s[40:41] offset:-4096
ds_read_b64_tr_b16 a[124:125], v204 offset:63560
s_add_u32 s40, s40, 73728
ds_read_b64_tr_b16 a[126:127], v205 offset:328
ds_read_b64_tr_b16 v[88:89], v206 offset:63488
s_addc_u32 s41, s41, 0
ds_read_b64_tr_b16 v[90:91], v207 offset:256
s_waitcnt vmcnt(18) lgkmcnt(0)
s_barrier
s_add_u32 m0, s48, 114688
ds_read_b64_tr_b16 a[128:129], v9 offset:0
ds_read_b64_tr_b16 a[130:131], v9 offset:2304
global_load_lds_dwordx4 v0, s[40:41] offset:-4096
ds_read_b64_tr_b16 a[132:133], v9 offset:8
global_load_lds_dwordx4 v1, s[40:41] offset:-3072
ds_read_b64_tr_b16 a[134:135], v9 offset:2312
ds_read_b64_tr_b16 a[136:137], v9 offset:64
global_load_lds_dwordx4 v2, s[40:41] offset:-2048
ds_read_b64_tr_b16 a[138:139], v9 offset:2368
global_load_lds_dwordx4 v3, s[40:41] offset:-1024
ds_read_b64_tr_b16 a[140:141], v9 offset:72
global_load_lds_dwordx4 v4, s[40:41] offset:0
ds_read_b64_tr_b16 a[142:143], v9 offset:2376
ds_read_b64_tr_b16 v[92:93], v10 offset:0
global_load_lds_dwordx4 v5, s[40:41] offset:1024
ds_read_b64_tr_b16 v[94:95], v10 offset:2304
global_load_lds_dwordx4 v6, s[40:41] offset:2048
ds_read_b64_tr_b16 a[144:145], v9 offset:18432
ds_read_b64_tr_b16 a[146:147], v9 offset:20736
global_load_lds_dwordx4 v7, s[40:41] offset:3072
ds_read_b64_tr_b16 a[148:149], v9 offset:18440
s_add_u32 m0, s48, 122880
ds_read_b64_tr_b16 a[150:151], v9 offset:20744
global_load_lds_dwordx4 v8, s[40:41] offset:-4096
ds_read_b64_tr_b16 a[152:153], v9 offset:18496
ds_read_b64_tr_b16 a[154:155], v9 offset:20800
s_add_u32 s40, s40, 73728
ds_read_b64_tr_b16 a[156:157], v9 offset:18504
s_addc_u32 s41, s41, 0
ds_read_b64_tr_b16 a[158:159], v9 offset:20808
s_waitcnt vmcnt(18) lgkmcnt(0)
s_barrier
s_add_u32 m0, s48, 4096
ds_read_b64_tr_b16 a[160:161], v9 offset:36864
ds_read_b64_tr_b16 a[162:163], v9 offset:39168
global_load_lds_dwordx4 v0, s[40:41] offset:-4096
ds_read_b64_tr_b16 a[164:165], v9 offset:36872
global_load_lds_dwordx4 v1, s[40:41] offset:-3072
ds_read_b64_tr_b16 a[166:167], v9 offset:39176
global_load_lds_dwordx4 v2, s[40:41] offset:-2048
ds_read_b64_tr_b16 a[168:169], v9 offset:36928
global_load_lds_dwordx4 v3, s[40:41] offset:-1024
ds_read_b64_tr_b16 a[170:171], v9 offset:39232
ds_read_b64_tr_b16 a[172:173], v9 offset:36936
global_load_lds_dwordx4 v4, s[40:41] offset:0
ds_read_b64_tr_b16 a[174:175], v9 offset:39240
global_load_lds_dwordx4 v5, s[40:41] offset:1024
ds_read_b64_tr_b16 a[176:177], v9 offset:55296
global_load_lds_dwordx4 v6, s[40:41] offset:2048
ds_read_b64_tr_b16 a[178:179], v9 offset:57600
global_load_lds_dwordx4 v7, s[40:41] offset:3072
ds_read_b64_tr_b16 a[180:181], v9 offset:55304
ds_read_b64_tr_b16 a[182:183], v9 offset:57608
s_add_u32 m0, s48, 12288
ds_read_b64_tr_b16 a[184:185], v9 offset:55360
global_load_lds_dwordx4 v8, s[40:41] offset:-4096
ds_read_b64_tr_b16 a[186:187], v9 offset:57664
s_sub_u32 s40, s40, 589824
ds_read_b64_tr_b16 a[188:189], v9 offset:55368
s_subb_u32 s41, s41, 0
ds_read_b64_tr_b16 a[190:191], v9 offset:57672
s_waitcnt vmcnt(18) lgkmcnt(0)
s_barrier
ds_read_b64_tr_b16 a[192:193], v204 offset:8192
ds_read_b64_tr_b16 a[194:195], v204 offset:10496
ds_read_b64_tr_b16 a[196:197], v204 offset:8200
ds_read_b64_tr_b16 a[198:199], v204 offset:10504
ds_read_b64_tr_b16 a[200:201], v204 offset:8256
ds_read_b64_tr_b16 a[202:203], v204 offset:10560
ds_read_b64_tr_b16 a[204:205], v204 offset:8264
ds_read_b64_tr_b16 a[206:207], v204 offset:10568
ds_read_b64_tr_b16 a[208:209], v204 offset:26624
ds_read_b64_tr_b16 a[210:211], v204 offset:28928
ds_read_b64_tr_b16 a[212:213], v204 offset:26632
ds_read_b64_tr_b16 a[214:215], v204 offset:28936
ds_read_b64_tr_b16 a[216:217], v204 offset:26688
ds_read_b64_tr_b16 a[218:219], v204 offset:28992
ds_read_b64_tr_b16 a[220:221], v204 offset:26696
ds_read_b64_tr_b16 a[222:223], v204 offset:29000
s_waitcnt vmcnt(9) lgkmcnt(0)
s_barrier
ds_read_b64_tr_b16 a[224:225], v204 offset:45056
ds_read_b64_tr_b16 a[226:227], v204 offset:47360
ds_read_b64_tr_b16 a[228:229], v204 offset:45064
ds_read_b64_tr_b16 a[230:231], v204 offset:47368
ds_read_b64_tr_b16 a[232:233], v204 offset:45120
ds_read_b64_tr_b16 a[234:235], v204 offset:47424
ds_read_b64_tr_b16 a[236:237], v204 offset:45128
ds_read_b64_tr_b16 a[238:239], v204 offset:47432
ds_read_b64_tr_b16 a[240:241], v204 offset:63488
ds_read_b64_tr_b16 a[242:243], v205 offset:256
ds_read_b64_tr_b16 a[244:245], v204 offset:63496
ds_read_b64_tr_b16 a[246:247], v205 offset:264
ds_read_b64_tr_b16 a[248:249], v204 offset:63552
ds_read_b64_tr_b16 a[250:251], v205 offset:320
ds_read_b64_tr_b16 a[252:253], v204 offset:63560
ds_read_b64_tr_b16 a[254:255], v205 offset:328
s_waitcnt vmcnt(0) lgkmcnt(0)
s_barrier
ds_read_b64_tr_b16 v[28:29], v9 offset:0
ds_read_b64_tr_b16 v[30:31], v9 offset:2304
ds_read_b64_tr_b16 v[32:33], v9 offset:8
ds_read_b64_tr_b16 v[34:35], v9 offset:2312
ds_read_b64_tr_b16 v[36:37], v9 offset:64
ds_read_b64_tr_b16 v[38:39], v9 offset:2368
ds_read_b64_tr_b16 v[40:41], v9 offset:72
ds_read_b64_tr_b16 v[42:43], v9 offset:2376
ds_read_b64_tr_b16 v[44:45], v9 offset:18432
ds_read_b64_tr_b16 v[46:47], v9 offset:20736
ds_read_b64_tr_b16 v[48:49], v9 offset:18440
ds_read_b64_tr_b16 v[50:51], v9 offset:20744
ds_read_b64_tr_b16 v[52:53], v9 offset:18496
ds_read_b64_tr_b16 v[54:55], v9 offset:20800
ds_read_b64_tr_b16 v[56:57], v9 offset:18504
ds_read_b64_tr_b16 v[58:59], v9 offset:20808
s_waitcnt lgkmcnt(0)
s_barrier
v_add_lshl_u32 v252, v24, 0, 7
v_bfi_b32 v252, s69, v252, v23
v_add_lshl_u32 v253, v22, 0, 7
v_bfi_b32 v253, s69, v253, v21
s_mov_b32 s92, 0x01010101
s_mov_b32 s93, 0x01010101
s_mov_b64 s[76:77], 0
s_add_u32 m0, s49, 18432
v_add_u32 v15, 18688, v15
v_add_u32 v13, 18688, v13
s_xor_b32 s49, s49, 0x10000
s_mov_b32 m0, 51200
s_add_u32 m0, s49, 0
v_add_u32 v11, 18432, v11
v_add_u32 v15, 18688, v15
s_mov_b32 m0, 59392
v_add_u32 v13, 18688, v13
s_add_u32 m0, s49, 18432
s_mov_b64 s[76:77], -1
s_mov_b32 s70, 33
s_waitcnt vmcnt(0) lgkmcnt(0)
s_nop 7
)"
R"(s_branch .Lwo_in15
.p2align 6
.Lwo_top16:
s_waitcnt lgkmcnt(2)
v_mfma_f32_16x16x32_bf16 v[96:99], a[0:3], v[132:135], v[96:99]
ds_read_b128 v[144:147], v25 offset:3072
v_mfma_f32_16x16x32_bf16 v[100:103], a[4:7], v[132:135], v[100:103]
v_mfma_f32_16x16x32_bf16 v[104:107], a[8:11], v[132:135], v[104:107]
s_waitcnt vmcnt(7)
s_barrier
v_mfma_f32_16x16x32_bf16 v[108:111], a[12:15], v[132:135], v[108:111]
v_cvt_pk_bf16_f32 v164, v112, v113
v_cvt_pk_bf16_f32 v165, v114, v115
v_mfma_f32_16x16x32_bf16 v[128:131], v[60:63], v[132:135], 0
s_waitcnt lgkmcnt(2)
v_cvt_pk_bf16_f32 v166, v116, v117
v_cvt_pk_bf16_f32 v167, v118, v119
s_mov_b64 exec, 65535
global_store_dwordx2 v15, v[176:177], s[38:39] sc1
s_mov_b64 exec, -1
v_mfma_f32_16x16x32_bf16 v[96:99], a[16:19], v[136:139], v[96:99]
ds_read_b128 v[132:135], v25 offset:4096
v_add_f32 v254, v112, v113
v_add_u32 v15, 18688, v15
v_mfma_f32_16x16x32_bf16 v[100:103], a[20:23], v[136:139], v[100:103]
v_mul_f32 v255, v112, v112
v_fmac_f32 v255, v113, v113
v_mfma_f32_16x16x32_bf16 v[104:107], a[24:27], v[136:139], v[104:107]
global_store_dwordx4 v13, v[164:167], s[38:39] offset:0 sc1
v_mfma_f32_16x16x32_bf16 v[108:111], a[28:31], v[136:139], v[108:111]
v_cvt_pk_bf16_f32 v168, v120, v121
v_cvt_pk_bf16_f32 v169, v122, v123
v_mfma_f32_16x16x32_bf16 v[128:131], v[64:67], v[136:139], v[128:131]
s_waitcnt lgkmcnt(2)
v_cvt_pk_bf16_f32 v170, v124, v125
v_cvt_pk_bf16_f32 v171, v126, v127
v_mfma_f32_16x16x32_bf16 v[96:99], a[32:35], v[140:143], v[96:99]
s_cmp_eq_u32 s70, 32
ds_read_b128 v[136:139], v25 offset:5120
v_add_f32 v254, v254, v114
v_mfma_f32_16x16x32_bf16 v[100:103], a[36:39], v[140:143], v[100:103]
s_cselect_b64 s[42:43], s[44:45], s[42:43]
global_store_dwordx4 v13, v[168:171], s[38:39] offset:1024 sc1
v_mfma_f32_16x16x32_bf16 v[104:107], a[40:43], v[140:143], v[104:107]
s_cmp_eq_u32 s70, 16
v_add_u32 v13, 18688, v13
v_fmac_f32 v255, v114, v114
v_mfma_f32_16x16x32_bf16 v[108:111], a[44:47], v[140:143], v[108:111]
s_cselect_b64 s[42:43], s[46:47], s[42:43]
v_add_f32 v254, v254, v115
v_fmac_f32 v255, v115, v115
v_mfma_f32_16x16x32_bf16 v[128:131], v[68:71], v[140:143], v[128:131]
s_waitcnt lgkmcnt(2)
v_add_f32 v254, v254, v116
v_fmac_f32 v255, v116, v116
v_mfma_f32_16x16x32_bf16 v[96:99], a[48:51], v[144:147], v[96:99]
s_xor_b32 s49, s49, 0x10000
ds_read_b128 v[140:143], v25 offset:6144
v_add_f32 v254, v254, v117
v_mfma_f32_16x16x32_bf16 v[100:103], a[52:55], v[144:147], v[100:103]
s_mov_b32 m0, 51200
v_fmac_f32 v255, v117, v117
v_add_f32 v254, v254, v118
v_mfma_f32_16x16x32_bf16 v[104:107], a[56:59], v[144:147], v[104:107]
v_fmac_f32 v255, v118, v118
v_add_f32 v254, v254, v119
v_mfma_f32_16x16x32_bf16 v[108:111], a[60:63], v[144:147], v[108:111]
v_fmac_f32 v255, v119, v119
v_add_f32 v254, v254, v120
v_mfma_f32_16x16x32_bf16 v[128:131], v[72:75], v[144:147], v[128:131]
s_waitcnt lgkmcnt(2)
v_fmac_f32 v255, v120, v120
v_add_f32 v254, v254, v121
v_mfma_f32_16x16x32_bf16 v[96:99], a[64:67], v[132:135], v[96:99]
ds_read_b128 v[144:147], v25 offset:7168
v_fmac_f32 v255, v121, v121
v_mfma_f32_16x16x32_bf16 v[100:103], a[68:71], v[132:135], v[100:103]
v_add_f32 v254, v254, v122
v_fmac_f32 v255, v122, v122
v_mfma_f32_16x16x32_bf16 v[104:107], a[72:75], v[132:135], v[104:107]
global_load_lds_dwordx4 v16, s[42:43] offset:0 sc1
v_add_f32 v254, v254, v123
v_mfma_f32_16x16x32_bf16 v[108:111], a[76:79], v[132:135], v[108:111]
v_fmac_f32 v255, v123, v123
v_add_f32 v254, v254, v124
v_mfma_f32_16x16x32_bf16 v[128:131], v[76:79], v[132:135], v[128:131]
s_waitcnt vmcnt(9)
ds_read_b128 v[148:151], v19 offset:8192
ds_read_b128 v[152:155], v20 offset:8192
s_waitcnt lgkmcnt(4)
v_mfma_f32_16x16x32_bf16 v[96:99], a[80:83], v[136:139], v[96:99]
ds_read_b128 v[132:135], v25 offset:8192
v_fmac_f32 v255, v124, v124
v_mfma_f32_16x16x32_bf16 v[100:103], a[84:87], v[136:139], v[100:103]
v_add_f32 v254, v254, v125
v_fmac_f32 v255, v125, v125
v_mfma_f32_16x16x32_bf16 v[104:107], a[88:91], v[136:139], v[104:107]
v_add_f32 v254, v254, v126
v_fmac_f32 v255, v126, v126
v_mfma_f32_16x16x32_bf16 v[108:111], a[92:95], v[136:139], v[108:111]
v_add_f32 v254, v254, v127
v_fmac_f32 v255, v127, v127
v_mfma_f32_16x16x32_bf16 v[128:131], v[80:83], v[136:139], v[128:131]
s_waitcnt lgkmcnt(4)
global_load_lds_dwordx4 v17, s[42:43] offset:1024 sc1
v_mfma_f32_16x16x32_bf16 v[96:99], a[96:99], v[140:143], v[96:99]
ds_read_b128 v[136:139], v25 offset:9216
v_mfma_f32_16x16x32_bf16 v[100:103], a[100:103], v[140:143], v[100:103]
ds_write_b64 v27, v[254:255] offset:46080
v_mfma_f32_16x16x32_bf16 v[104:107], a[104:107], v[140:143], v[104:107]
s_waitcnt lgkmcnt(4)
v_lshlrev_b32 v112, 16, v148
v_and_b32 v113, 0xffff0000, v148
v_mfma_f32_16x16x32_bf16 v[108:111], a[108:111], v[140:143], v[108:111]
v_lshlrev_b32 v114, 16, v149
v_and_b32 v115, 0xffff0000, v149
v_mfma_f32_16x16x32_bf16 v[128:131], v[84:87], v[140:143], v[128:131]
v_lshlrev_b32 v116, 16, v150
v_and_b32 v117, 0xffff0000, v150
v_mfma_f32_16x16x32_bf16 v[96:99], a[112:115], v[144:147], v[96:99]
s_add_u32 m0, s49, 0
ds_read_b128 v[140:143], v25 offset:10240
v_lshlrev_b32 v118, 16, v151
v_mfma_f32_16x16x32_bf16 v[100:103], a[116:119], v[144:147], v[100:103]
s_waitcnt lgkmcnt(4)
v_and_b32 v119, 0xffff0000, v151
v_lshlrev_b32 v120, 16, v152
v_mfma_f32_16x16x32_bf16 v[104:107], a[120:123], v[144:147], v[104:107]
s_sub_u32 s42, s42, 18432
v_and_b32 v121, 0xffff0000, v152
v_lshlrev_b32 v122, 16, v153
v_mfma_f32_16x16x32_bf16 v[108:111], a[124:127], v[144:147], v[108:111]
s_subb_u32 s43, s43, 0
v_and_b32 v123, 0xffff0000, v153
v_lshlrev_b32 v124, 16, v154
v_mfma_f32_16x16x32_bf16 v[128:131], v[88:91], v[144:147], v[128:131]
s_waitcnt lgkmcnt(3)
v_and_b32 v125, 0xffff0000, v154
v_lshlrev_b32 v126, 16, v155
v_mfma_f32_16x16x32_bf16 v[96:99], a[128:131], v[132:135], v[96:99]
ds_read_b128 v[144:147], v25 offset:11264
v_and_b32 v127, 0xffff0000, v155
v_mfma_f32_16x16x32_bf16 v[100:103], a[132:135], v[132:135], v[100:103]
v_mfma_f32_16x16x32_bf16 v[104:107], a[136:139], v[132:135], v[104:107]
v_mfma_f32_16x16x32_bf16 v[108:111], a[140:143], v[132:135], v[108:111]
v_mfma_f32_16x16x32_bf16 v[128:131], v[92:95], v[132:135], v[128:131]
s_waitcnt lgkmcnt(3)
ds_read_b64 v[176:177], v27 offset:43008
ds_read_b64 v[180:181], v27 offset:43520
v_mfma_f32_16x16x32_bf16 v[96:99], a[144:147], v[136:139], v[96:99]
ds_read_b128 v[132:135], v25 offset:12288
ds_read_b64 v[184:185], v27 offset:44032
v_mfma_f32_16x16x32_bf16 v[100:103], a[148:151], v[136:139], v[100:103]
ds_read_b64 v[188:189], v27 offset:44544
v_mfma_f32_16x16x32_bf16 v[104:107], a[152:155], v[136:139], v[104:107]
v_mfma_f32_16x16x32_bf16 v[108:111], a[156:159], v[136:139], v[108:111]
s_waitcnt lgkmcnt(6)
v_mfma_f32_16x16x32_bf16 v[96:99], a[160:163], v[140:143], v[96:99]
ds_read_b128 v[136:139], v25 offset:13312
v_mfma_f32_16x16x32_bf16 v[100:103], a[164:167], v[140:143], v[100:103]
v_mfma_f32_16x16x32_bf16 v[104:107], a[168:171], v[140:143], v[104:107]
v_mfma_f32_16x16x32_bf16 v[108:111], a[172:175], v[140:143], v[108:111]
s_waitcnt lgkmcnt(4)
v_add_f32 v176, v176, v180
v_add_f32 v177, v177, v181
v_mfma_f32_16x16x32_bf16 v[96:99], a[176:179], v[144:147], v[96:99]
s_waitcnt lgkmcnt(1)
ds_read_b128 v[140:143], v25 offset:14336
v_add_f32 v184, v184, v188
v_mfma_f32_16x16x32_bf16 v[100:103], a[180:183], v[144:147], v[100:103]
v_mfma_f32_16x16x32_bf16 v[104:107], a[184:187], v[144:147], v[104:107]
s_barrier
v_mfma_f32_16x16x32_bf16 v[108:111], a[188:191], v[144:147], v[108:111]
global_load_lds_dwordx4 v11, s[38:39] offset:-2048 sc1
v_add_f32 v185, v185, v189
v_mfma_f32_16x16x32_bf16 v[96:99], a[192:195], v[132:135], v[96:99]
ds_read_b128 v[144:147], v25 offset:15360
v_add_f32 v176, v176, v184
v_mfma_f32_16x16x32_bf16 v[100:103], a[196:199], v[132:135], v[100:103]
v_add_f32 v177, v177, v185
v_mov_b32 v202, v176
v_mfma_f32_16x16x32_bf16 v[104:107], a[200:203], v[132:135], v[104:107]
ds_write_b128 v26, v[128:131] offset:38912
v_mfma_f32_16x16x32_bf16 v[108:111], a[204:207], v[132:135], v[108:111]
s_waitcnt lgkmcnt(3)
global_load_lds_dwordx4 v11, s[38:39] offset:-1024 sc1
v_mov_b32 v203, v177
v_mfma_f32_16x16x32_bf16 v[96:99], a[208:211], v[136:139], v[96:99]
ds_read_b128 v[132:135], v25 offset:16384
v_mfma_f32_16x16x32_bf16 v[100:103], a[212:215], v[136:139], v[100:103]
v_permlane16_swap_b32 v176, v202
v_mfma_f32_16x16x32_bf16 v[104:107], a[216:219], v[136:139], v[104:107]
global_load_lds_dwordx4 v11, s[38:39] offset:0 sc1
v_mfma_f32_16x16x32_bf16 v[108:111], a[220:223], v[136:139], v[108:111]
s_waitcnt lgkmcnt(3)
v_permlane16_swap_b32 v177, v203
v_mfma_f32_16x16x32_bf16 v[96:99], a[224:227], v[140:143], v[96:99]
ds_read_b128 v[136:139], v25 offset:17408
v_add_f32 v176, v176, v202
v_mfma_f32_16x16x32_bf16 v[100:103], a[228:231], v[140:143], v[100:103]
global_load_lds_dwordx4 v11, s[38:39] offset:1024 sc1
v_add_f32 v177, v177, v203
v_mfma_f32_16x16x32_bf16 v[104:107], a[232:235], v[140:143], v[104:107]
v_mov_b32 v202, v176
v_mov_b32 v203, v177
v_mfma_f32_16x16x32_bf16 v[108:111], a[236:239], v[140:143], v[108:111]
s_waitcnt lgkmcnt(3)
v_permlane32_swap_b32 v176, v202
v_mfma_f32_16x16x32_bf16 v[96:99], a[240:243], v[144:147], v[96:99]
global_load_lds_dwordx4 v11, s[38:39] offset:2048 sc1
v_add_u32 v11, 18432, v11
v_mfma_f32_16x16x32_bf16 v[100:103], a[244:247], v[144:147], v[100:103]
v_mfma_f32_16x16x32_bf16 v[104:107], a[248:251], v[144:147], v[104:107]
s_waitcnt vmcnt(10) lgkmcnt(2)
s_barrier
v_mfma_f32_16x16x32_bf16 v[108:111], a[252:255], v[144:147], v[108:111]
s_waitcnt lgkmcnt(1)
ds_read_b128 v[140:143], v25 offset:18432
v_min_u32 v11, v11, v12
v_mfma_f32_16x16x32_bf16 v[96:99], v[28:31], v[132:135], v[96:99]
ds_read_b128 v[144:147], v25 offset:19456
v_mfma_f32_16x16x32_bf16 v[100:103], v[32:35], v[132:135], v[100:103]
v_permlane32_swap_b32 v177, v203
v_mfma_f32_16x16x32_bf16 v[104:107], v[36:39], v[132:135], v[104:107]
v_add_f32 v176, v176, v202
v_add_f32 v177, v177, v203
v_mfma_f32_16x16x32_bf16 v[108:111], v[40:43], v[132:135], v[108:111]
s_waitcnt lgkmcnt(2)
v_mfma_f32_16x16x32_bf16 v[96:99], v[44:47], v[136:139], v[96:99]
ds_read_b128 v[132:135], v25 offset:20480
v_mfma_f32_16x16x32_bf16 v[100:103], v[48:51], v[136:139], v[100:103]
v_mfma_f32_16x16x32_bf16 v[104:107], v[52:55], v[136:139], v[104:107]
v_mfma_f32_16x16x32_bf16 v[108:111], v[56:59], v[136:139], v[108:111]
s_waitcnt lgkmcnt(2)
v_mfma_f32_16x16x32_bf16 v[112:115], a[0:3], v[140:143], v[112:115]
ds_read_b128 v[136:139], v25 offset:21504
v_mfma_f32_16x16x32_bf16 v[116:119], a[4:7], v[140:143], v[116:119]
v_mfma_f32_16x16x32_bf16 v[120:123], a[8:11], v[140:143], v[120:123]
s_waitcnt vmcnt(7)
s_barrier
v_mfma_f32_16x16x32_bf16 v[124:127], a[12:15], v[140:143], v[124:127]
v_cvt_pk_bf16_f32 v164, v96, v97
v_cvt_pk_bf16_f32 v165, v98, v99
v_mfma_f32_16x16x32_bf16 v[128:131], v[60:63], v[140:143], 0
s_waitcnt lgkmcnt(2)
v_cvt_pk_bf16_f32 v166, v100, v101
v_cvt_pk_bf16_f32 v167, v102, v103
s_mov_b64 exec, 65535
global_store_dwordx2 v15, v[176:177], s[38:39] sc1
s_mov_b64 exec, -1
v_mfma_f32_16x16x32_bf16 v[112:115], a[16:19], v[144:147], v[112:115]
ds_read_b128 v[140:143], v25 offset:22528
v_add_u32 v15, 18688, v15
v_mfma_f32_16x16x32_bf16 v[116:119], a[20:23], v[144:147], v[116:119]
v_add_f32 v254, v96, v97
v_mul_f32 v255, v96, v96
v_mfma_f32_16x16x32_bf16 v[120:123], a[24:27], v[144:147], v[120:123]
global_store_dwordx4 v13, v[164:167], s[38:39] offset:0 sc1
v_mfma_f32_16x16x32_bf16 v[124:127], a[28:31], v[144:147], v[124:127]
v_cvt_pk_bf16_f32 v168, v104, v105
v_cvt_pk_bf16_f32 v169, v106, v107
v_mfma_f32_16x16x32_bf16 v[128:131], v[64:67], v[144:147], v[128:131]
s_waitcnt lgkmcnt(2)
v_cvt_pk_bf16_f32 v170, v108, v109
v_cvt_pk_bf16_f32 v171, v110, v111
v_mfma_f32_16x16x32_bf16 v[112:115], a[32:35], v[132:135], v[112:115]
s_mov_b32 m0, 59392
ds_read_b128 v[144:147], v25 offset:23552
v_fmac_f32 v255, v97, v97
v_mfma_f32_16x16x32_bf16 v[116:119], a[36:39], v[132:135], v[116:119]
global_store_dwordx4 v13, v[168:171], s[38:39] offset:1024 sc1
v_mfma_f32_16x16x32_bf16 v[120:123], a[40:43], v[132:135], v[120:123]
v_add_u32 v13, 18688, v13
v_add_f32 v254, v254, v98
v_mfma_f32_16x16x32_bf16 v[124:127], a[44:47], v[132:135], v[124:127]
v_fmac_f32 v255, v98, v98
v_add_f32 v254, v254, v99
v_mfma_f32_16x16x32_bf16 v[128:131], v[68:71], v[132:135], v[128:131]
s_waitcnt lgkmcnt(2)
v_fmac_f32 v255, v99, v99
v_add_f32 v254, v254, v100
v_mfma_f32_16x16x32_bf16 v[112:115], a[48:51], v[136:139], v[112:115]
ds_read_b128 v[132:135], v25 offset:24576
v_fmac_f32 v255, v100, v100
v_mfma_f32_16x16x32_bf16 v[116:119], a[52:55], v[136:139], v[116:119]
v_add_f32 v254, v254, v101
v_fmac_f32 v255, v101, v101
v_mfma_f32_16x16x32_bf16 v[120:123], a[56:59], v[136:139], v[120:123]
v_add_f32 v254, v254, v102
v_fmac_f32 v255, v102, v102
v_mfma_f32_16x16x32_bf16 v[124:127], a[60:63], v[136:139], v[124:127]
v_add_f32 v254, v254, v103
v_fmac_f32 v255, v103, v103
v_mfma_f32_16x16x32_bf16 v[128:131], v[72:75], v[136:139], v[128:131]
s_waitcnt lgkmcnt(2)
v_add_f32 v254, v254, v104
v_fmac_f32 v255, v104, v104
v_mfma_f32_16x16x32_bf16 v[112:115], a[64:67], v[140:143], v[112:115]
ds_read_b128 v[136:139], v25 offset:25600
v_add_f32 v254, v254, v105
v_mfma_f32_16x16x32_bf16 v[116:119], a[68:71], v[140:143], v[116:119]
v_fmac_f32 v255, v105, v105
v_add_f32 v254, v254, v106
v_mfma_f32_16x16x32_bf16 v[120:123], a[72:75], v[140:143], v[120:123]
global_load_lds_dwordx4 v16, s[42:43] offset:0 sc1
v_fmac_f32 v255, v106, v106
v_mfma_f32_16x16x32_bf16 v[124:127], a[76:79], v[140:143], v[124:127]
v_add_f32 v254, v254, v107
v_fmac_f32 v255, v107, v107
v_mfma_f32_16x16x32_bf16 v[128:131], v[76:79], v[140:143], v[128:131]
s_waitcnt vmcnt(9)
ds_read_b128 v[148:151], v19 offset:0
ds_read_b128 v[152:155], v20 offset:0
s_waitcnt lgkmcnt(4)
v_mfma_f32_16x16x32_bf16 v[112:115], a[80:83], v[144:147], v[112:115]
ds_read_b128 v[140:143], v25 offset:26624
v_add_f32 v254, v254, v108
v_mfma_f32_16x16x32_bf16 v[116:119], a[84:87], v[144:147], v[116:119]
v_fmac_f32 v255, v108, v108
v_add_f32 v254, v254, v109
v_mfma_f32_16x16x32_bf16 v[120:123], a[88:91], v[144:147], v[120:123]
v_fmac_f32 v255, v109, v109
v_add_f32 v254, v254, v110
v_mfma_f32_16x16x32_bf16 v[124:127], a[92:95], v[144:147], v[124:127]
v_fmac_f32 v255, v110, v110
v_add_f32 v254, v254, v111
v_mfma_f32_16x16x32_bf16 v[128:131], v[80:83], v[144:147], v[128:131]
s_waitcnt lgkmcnt(4)
global_load_lds_dwordx4 v17, s[42:43] offset:1024 sc1
v_fmac_f32 v255, v111, v111
v_mfma_f32_16x16x32_bf16 v[112:115], a[96:99], v[132:135], v[112:115]
ds_read_b128 v[144:147], v25 offset:27648
v_mfma_f32_16x16x32_bf16 v[116:119], a[100:103], v[132:135], v[116:119]
ds_write_b64 v27, v[254:255] offset:44032
v_mfma_f32_16x16x32_bf16 v[120:123], a[104:107], v[132:135], v[120:123]
s_waitcnt lgkmcnt(4)
v_lshlrev_b32 v96, 16, v148
v_and_b32 v97, 0xffff0000, v148
v_mfma_f32_16x16x32_bf16 v[124:127], a[108:111], v[132:135], v[124:127]
v_lshlrev_b32 v98, 16, v149
v_and_b32 v99, 0xffff0000, v149
v_mfma_f32_16x16x32_bf16 v[128:131], v[84:87], v[132:135], v[128:131]
v_lshlrev_b32 v100, 16, v150
v_and_b32 v101, 0xffff0000, v150
v_mfma_f32_16x16x32_bf16 v[112:115], a[112:115], v[136:139], v[112:115]
s_add_u32 m0, s49, 18432
ds_read_b128 v[132:135], v25 offset:28672
v_lshlrev_b32 v102, 16, v151
v_mfma_f32_16x16x32_bf16 v[116:119], a[116:119], v[136:139], v[116:119]
s_waitcnt lgkmcnt(4)
v_and_b32 v103, 0xffff0000, v151
v_lshlrev_b32 v104, 16, v152
v_mfma_f32_16x16x32_bf16 v[120:123], a[120:123], v[136:139], v[120:123]
s_sub_u32 s42, s42, 18432
v_and_b32 v105, 0xffff0000, v152
v_lshlrev_b32 v106, 16, v153
v_mfma_f32_16x16x32_bf16 v[124:127], a[124:127], v[136:139], v[124:127]
s_subb_u32 s43, s43, 0
v_and_b32 v107, 0xffff0000, v153
v_lshlrev_b32 v108, 16, v154
v_mfma_f32_16x16x32_bf16 v[128:131], v[88:91], v[136:139], v[128:131]
s_waitcnt lgkmcnt(3)
v_and_b32 v109, 0xffff0000, v154
v_lshlrev_b32 v110, 16, v155
v_mfma_f32_16x16x32_bf16 v[112:115], a[128:131], v[140:143], v[112:115]
ds_read_b128 v[136:139], v25 offset:29696
v_and_b32 v111, 0xffff0000, v155
v_mfma_f32_16x16x32_bf16 v[116:119], a[132:135], v[140:143], v[116:119]
v_mfma_f32_16x16x32_bf16 v[120:123], a[136:139], v[140:143], v[120:123]
v_mfma_f32_16x16x32_bf16 v[124:127], a[140:143], v[140:143], v[124:127]
v_mfma_f32_16x16x32_bf16 v[128:131], v[92:95], v[140:143], v[128:131]
s_waitcnt lgkmcnt(3)
ds_read_b64 v[176:177], v27 offset:45056
ds_read_b64 v[180:181], v27 offset:45568
v_mfma_f32_16x16x32_bf16 v[112:115], a[144:147], v[144:147], v[112:115]
ds_read_b128 v[140:143], v25 offset:30720
ds_read_b64 v[184:185], v27 offset:46080
v_mfma_f32_16x16x32_bf16 v[116:119], a[148:151], v[144:147], v[116:119]
ds_read_b64 v[188:189], v27 offset:46592
v_mfma_f32_16x16x32_bf16 v[120:123], a[152:155], v[144:147], v[120:123]
v_mfma_f32_16x16x32_bf16 v[124:127], a[156:159], v[144:147], v[124:127]
s_waitcnt lgkmcnt(6)
v_mfma_f32_16x16x32_bf16 v[112:115], a[160:163], v[132:135], v[112:115]
ds_read_b128 v[144:147], v25 offset:31744
v_mfma_f32_16x16x32_bf16 v[116:119], a[164:167], v[132:135], v[116:119]
v_mfma_f32_16x16x32_bf16 v[120:123], a[168:171], v[132:135], v[120:123]
v_mfma_f32_16x16x32_bf16 v[124:127], a[172:175], v[132:135], v[124:127]
s_waitcnt lgkmcnt(4)
v_add_f32 v176, v176, v180
v_add_f32 v177, v177, v181
v_mfma_f32_16x16x32_bf16 v[112:115], a[176:179], v[136:139], v[112:115]
s_waitcnt lgkmcnt(1)
ds_read_b128 v[132:135], v25 offset:32768
v_add_f32 v184, v184, v188
v_mfma_f32_16x16x32_bf16 v[116:119], a[180:183], v[136:139], v[116:119]
v_mfma_f32_16x16x32_bf16 v[120:123], a[184:187], v[136:139], v[120:123]
s_barrier
v_mfma_f32_16x16x32_bf16 v[124:127], a[188:191], v[136:139], v[124:127]
global_load_lds_dwordx4 v11, s[38:39] offset:-2048 sc1
v_add_f32 v185, v185, v189
)"
R"(v_mfma_f32_16x16x32_bf16 v[112:115], a[192:195], v[140:143], v[112:115]
s_mov_b64 s[76:77], -1
ds_read_b128 v[136:139], v25 offset:33792
v_add_f32 v176, v176, v184
v_mfma_f32_16x16x32_bf16 v[116:119], a[196:199], v[140:143], v[116:119]
v_add_f32 v177, v177, v185
v_mov_b32 v202, v176
v_mfma_f32_16x16x32_bf16 v[120:123], a[200:203], v[140:143], v[120:123]
ds_write_b128 v26, v[128:131] offset:41984
.Lwo_in15:
v_mfma_f32_16x16x32_bf16 v[124:127], a[204:207], v[140:143], v[124:127]
s_waitcnt lgkmcnt(3)
global_load_lds_dwordx4 v11, s[38:39] offset:-1024 sc1
v_mov_b32 v203, v177
v_mfma_f32_16x16x32_bf16 v[112:115], a[208:211], v[144:147], v[112:115]
ds_read_b128 v[140:143], v25 offset:34816
v_mfma_f32_16x16x32_bf16 v[116:119], a[212:215], v[144:147], v[116:119]
v_permlane16_swap_b32 v176, v202
v_mfma_f32_16x16x32_bf16 v[120:123], a[216:219], v[144:147], v[120:123]
global_load_lds_dwordx4 v11, s[38:39] offset:0 sc1
v_mfma_f32_16x16x32_bf16 v[124:127], a[220:223], v[144:147], v[124:127]
s_waitcnt lgkmcnt(3)
v_permlane16_swap_b32 v177, v203
v_mfma_f32_16x16x32_bf16 v[112:115], a[224:227], v[132:135], v[112:115]
ds_read_b128 v[144:147], v25 offset:35840
v_add_f32 v176, v176, v202
v_mfma_f32_16x16x32_bf16 v[116:119], a[228:231], v[132:135], v[116:119]
global_load_lds_dwordx4 v11, s[38:39] offset:1024 sc1
v_add_f32 v177, v177, v203
v_mfma_f32_16x16x32_bf16 v[120:123], a[232:235], v[132:135], v[120:123]
v_mov_b32 v202, v176
v_mov_b32 v203, v177
v_mfma_f32_16x16x32_bf16 v[124:127], a[236:239], v[132:135], v[124:127]
s_waitcnt lgkmcnt(3)
v_xor_b32 v25, 0x10000, v25
v_mfma_f32_16x16x32_bf16 v[112:115], a[240:243], v[136:139], v[112:115]
global_load_lds_dwordx4 v11, s[38:39] offset:2048 sc1
v_add_u32 v11, 18432, v11
v_mfma_f32_16x16x32_bf16 v[116:119], a[244:247], v[136:139], v[116:119]
v_mfma_f32_16x16x32_bf16 v[120:123], a[248:251], v[136:139], v[120:123]
s_waitcnt vmcnt(10) lgkmcnt(2)
s_barrier
v_mfma_f32_16x16x32_bf16 v[124:127], a[252:255], v[136:139], v[124:127]
s_waitcnt lgkmcnt(1)
ds_read_b128 v[132:135], v25 offset:0
v_min_u32 v11, v11, v12
v_mfma_f32_16x16x32_bf16 v[112:115], v[28:31], v[140:143], v[112:115]
ds_read_b128 v[136:139], v25 offset:1024
v_mfma_f32_16x16x32_bf16 v[116:119], v[32:35], v[140:143], v[116:119]
v_permlane32_swap_b32 v176, v202
v_mfma_f32_16x16x32_bf16 v[120:123], v[36:39], v[140:143], v[120:123]
v_permlane32_swap_b32 v177, v203
v_mfma_f32_16x16x32_bf16 v[124:127], v[40:43], v[140:143], v[124:127]
s_waitcnt lgkmcnt(2)
v_add_f32 v176, v176, v202
v_add_f32 v177, v177, v203
v_mfma_f32_16x16x32_bf16 v[112:115], v[44:47], v[144:147], v[112:115]
ds_read_b128 v[140:143], v25 offset:2048
v_mfma_f32_16x16x32_bf16 v[116:119], v[48:51], v[144:147], v[116:119]
v_mfma_f32_16x16x32_bf16 v[120:123], v[52:55], v[144:147], v[120:123]
v_mfma_f32_16x16x32_bf16 v[124:127], v[56:59], v[144:147], v[124:127]
s_sub_u32 s70, s70, 1
s_cbranch_scc0 .Lwo_top16
s_waitcnt vmcnt(7)
s_barrier
s_mov_b64 exec, 65535
global_store_dwordx2 v15, v[176:177], s[38:39] sc1
s_mov_b64 exec, -1
v_add_u32 v15, 18688, v15
v_cvt_pk_bf16_f32 v164, v112, v113
v_cvt_pk_bf16_f32 v165, v114, v115
v_cvt_pk_bf16_f32 v166, v116, v117
v_cvt_pk_bf16_f32 v167, v118, v119
global_store_dwordx4 v13, v[164:167], s[38:39] offset:0 sc1
v_cvt_pk_bf16_f32 v168, v120, v121
v_cvt_pk_bf16_f32 v169, v122, v123
v_cvt_pk_bf16_f32 v170, v124, v125
v_cvt_pk_bf16_f32 v171, v126, v127
global_store_dwordx4 v13, v[168:171], s[38:39] offset:1024 sc1
v_add_f32 v254, v112, v113
v_mul_f32 v255, v112, v112
v_fmac_f32 v255, v113, v113
v_add_f32 v254, v254, v114
v_fmac_f32 v255, v114, v114
v_add_f32 v254, v254, v115
v_fmac_f32 v255, v115, v115
v_add_f32 v254, v254, v116
v_fmac_f32 v255, v116, v116
v_add_f32 v254, v254, v117
v_fmac_f32 v255, v117, v117
v_add_f32 v254, v254, v118
v_fmac_f32 v255, v118, v118
v_add_f32 v254, v254, v119
v_fmac_f32 v255, v119, v119
v_add_f32 v254, v254, v120
v_fmac_f32 v255, v120, v120
v_add_f32 v254, v254, v121
v_fmac_f32 v255, v121, v121
v_add_f32 v254, v254, v122
v_fmac_f32 v255, v122, v122
v_add_f32 v254, v254, v123
v_fmac_f32 v255, v123, v123
v_add_f32 v254, v254, v124
v_fmac_f32 v255, v124, v124
v_add_f32 v254, v254, v125
v_fmac_f32 v255, v125, v125
v_add_f32 v254, v254, v126
v_fmac_f32 v255, v126, v126
v_add_f32 v254, v254, v127
v_fmac_f32 v255, v127, v127
ds_write_b64 v27, v[254:255] offset:46080
s_waitcnt vmcnt(0) lgkmcnt(0)
s_barrier
ds_read_b64 v[176:177], v27 offset:43008
ds_read_b64 v[180:181], v27 offset:43520
ds_read_b64 v[184:185], v27 offset:44032
ds_read_b64 v[188:189], v27 offset:44544
s_waitcnt lgkmcnt(2)
v_add_f32 v176, v176, v180
v_add_f32 v177, v177, v181
s_waitcnt lgkmcnt(0)
v_add_f32 v184, v184, v188
v_add_f32 v185, v185, v189
v_add_f32 v176, v176, v184
v_add_f32 v177, v177, v185
v_mov_b32 v202, v176
v_mov_b32 v203, v177
s_nop 0
v_permlane16_swap_b32 v176, v202
v_permlane16_swap_b32 v177, v203
v_add_f32 v176, v176, v202
v_add_f32 v177, v177, v203
v_mov_b32 v202, v176
v_mov_b32 v203, v177
s_nop 0
v_permlane32_swap_b32 v176, v202
v_permlane32_swap_b32 v177, v203
v_add_f32 v176, v176, v202
v_add_f32 v177, v177, v203
s_mov_b64 exec, 65535
global_store_dwordx2 v15, v[176:177], s[38:39] sc1
s_mov_b64 exec, -1
v_add_u32 v15, 18688, v15
ds_read_b64 v[176:177], v27 offset:45056
ds_read_b64 v[180:181], v27 offset:45568
ds_read_b64 v[184:185], v27 offset:46080
ds_read_b64 v[188:189], v27 offset:46592
s_waitcnt lgkmcnt(2)
v_add_f32 v176, v176, v180
v_add_f32 v177, v177, v181
s_waitcnt lgkmcnt(0)
v_add_f32 v184, v184, v188
v_add_f32 v185, v185, v189
v_add_f32 v176, v176, v184
v_add_f32 v177, v177, v185
v_mov_b32 v202, v176
v_mov_b32 v203, v177
s_nop 0
v_permlane16_swap_b32 v176, v202
v_permlane16_swap_b32 v177, v203
v_add_f32 v176, v176, v202
v_add_f32 v177, v177, v203
v_mov_b32 v202, v176
v_mov_b32 v203, v177
s_nop 0
v_permlane32_swap_b32 v176, v202
v_permlane32_swap_b32 v177, v203
v_add_f32 v176, v176, v202
v_add_f32 v177, v177, v203
s_mov_b64 exec, 65535
global_store_dwordx2 v15, v[176:177], s[38:39] sc1
s_mov_b64 exec, -1
v_add_u32 v15, 18688, v15
s_waitcnt vmcnt(0)
s_barrier
s_endpgm
.Lwo_w4:
s_add_u32 m0, s48, 4096
s_nop 0
global_load_lds_dwordx4 v0, s[40:41] offset:-4096
global_load_lds_dwordx4 v1, s[40:41] offset:-3072
global_load_lds_dwordx4 v2, s[40:41] offset:-2048
global_load_lds_dwordx4 v3, s[40:41] offset:-1024
global_load_lds_dwordx4 v4, s[40:41] offset:0
global_load_lds_dwordx4 v5, s[40:41] offset:1024
global_load_lds_dwordx4 v6, s[40:41] offset:2048
global_load_lds_dwordx4 v7, s[40:41] offset:3072
s_add_u32 m0, s48, 12288
s_nop 0
global_load_lds_dwordx4 v8, s[40:41] offset:-4096
s_add_u32 s40, s40, 73728
s_addc_u32 s41, s41, 0
s_add_u32 m0, s48, 40960
s_nop 0
global_load_lds_dwordx4 v0, s[40:41] offset:-4096
global_load_lds_dwordx4 v1, s[40:41] offset:-3072
global_load_lds_dwordx4 v2, s[40:41] offset:-2048
global_load_lds_dwordx4 v3, s[40:41] offset:-1024
global_load_lds_dwordx4 v4, s[40:41] offset:0
global_load_lds_dwordx4 v5, s[40:41] offset:1024
global_load_lds_dwordx4 v6, s[40:41] offset:2048
global_load_lds_dwordx4 v7, s[40:41] offset:3072
s_add_u32 m0, s48, 49152
s_nop 0
global_load_lds_dwordx4 v8, s[40:41] offset:-4096
s_add_u32 s40, s40, 73728
s_addc_u32 s41, s41, 0
s_add_u32 m0, s48, 77824
s_nop 0
global_load_lds_dwordx4 v0, s[40:41] offset:-4096
global_load_lds_dwordx4 v1, s[40:41] offset:-3072
global_load_lds_dwordx4 v2, s[40:41] offset:-2048
global_load_lds_dwordx4 v3, s[40:41] offset:-1024
global_load_lds_dwordx4 v4, s[40:41] offset:0
global_load_lds_dwordx4 v5, s[40:41] offset:1024
global_load_lds_dwordx4 v6, s[40:41] offset:2048
global_load_lds_dwordx4 v7, s[40:41] offset:3072
s_add_u32 m0, s48, 86016
s_nop 0
global_load_lds_dwordx4 v8, s[40:41] offset:-4096
s_add_u32 s40, s40, 73728
s_addc_u32 s41, s41, 0
v_add_u32 v204, 0x10000, v9
v_add_u32 v205, 0x20000, v9
v_add_u32 v206, 0x10000, v10
v_add_u32 v207, 0x20000, v10
s_waitcnt lgkmcnt(0)
s_mul_i32 s37, s37, 0x9E3779B1
s_add_u32 s37, s37, 0x7F4A7C15
v_add_u32 v22, s37, v22
v_add_u32 v24, s37, v24
s_waitcnt vmcnt(18)
s_barrier
s_add_u32 m0, s48, 114688
ds_read_b64_tr_b16 a[0:1], v9 offset:0
ds_read_b64_tr_b16 a[2:3], v9 offset:2304
global_load_lds_dwordx4 v0, s[40:41] offset:-4096
ds_read_b64_tr_b16 a[4:5], v9 offset:8
global_load_lds_dwordx4 v1, s[40:41] offset:-3072
ds_read_b64_tr_b16 a[6:7], v9 offset:2312
global_load_lds_dwordx4 v2, s[40:41] offset:-2048
ds_read_b64_tr_b16 a[8:9], v9 offset:64
global_load_lds_dwordx4 v3, s[40:41] offset:-1024
ds_read_b64_tr_b16 a[10:11], v9 offset:2368
ds_read_b64_tr_b16 a[12:13], v9 offset:72
global_load_lds_dwordx4 v4, s[40:41] offset:0
ds_read_b64_tr_b16 a[14:15], v9 offset:2376
global_load_lds_dwordx4 v5, s[40:41] offset:1024
ds_read_b64_tr_b16 a[16:17], v9 offset:18432
global_load_lds_dwordx4 v6, s[40:41] offset:2048
ds_read_b64_tr_b16 a[18:19], v9 offset:20736
global_load_lds_dwordx4 v7, s[40:41] offset:3072
ds_read_b64_tr_b16 a[20:21], v9 offset:18440
ds_read_b64_tr_b16 a[22:23], v9 offset:20744
s_add_u32 m0, s48, 122880
ds_read_b64_tr_b16 a[24:25], v9 offset:18496
global_load_lds_dwordx4 v8, s[40:41] offset:-4096
ds_read_b64_tr_b16 a[26:27], v9 offset:20800
s_add_u32 s40, s40, 73728
ds_read_b64_tr_b16 a[28:29], v9 offset:18504
s_addc_u32 s41, s41, 0
ds_read_b64_tr_b16 a[30:31], v9 offset:20808
s_waitcnt vmcnt(18) lgkmcnt(0)
s_barrier
s_add_u32 m0, s48, 4096
ds_read_b64_tr_b16 a[32:33], v9 offset:36864
ds_read_b64_tr_b16 a[34:35], v9 offset:39168
global_load_lds_dwordx4 v0, s[40:41] offset:-4096
ds_read_b64_tr_b16 a[36:37], v9 offset:36872
global_load_lds_dwordx4 v1, s[40:41] offset:-3072
ds_read_b64_tr_b16 a[38:39], v9 offset:39176
global_load_lds_dwordx4 v2, s[40:41] offset:-2048
ds_read_b64_tr_b16 a[40:41], v9 offset:36928
global_load_lds_dwordx4 v3, s[40:41] offset:-1024
ds_read_b64_tr_b16 a[42:43], v9 offset:39232
ds_read_b64_tr_b16 a[44:45], v9 offset:36936
global_load_lds_dwordx4 v4, s[40:41] offset:0
ds_read_b64_tr_b16 a[46:47], v9 offset:39240
global_load_lds_dwordx4 v5, s[40:41] offset:1024
ds_read_b64_tr_b16 a[48:49], v9 offset:55296
global_load_lds_dwordx4 v6, s[40:41] offset:2048
ds_read_b64_tr_b16 a[50:51], v9 offset:57600
global_load_lds_dwordx4 v7, s[40:41] offset:3072
ds_read_b64_tr_b16 a[52:53], v9 offset:55304
ds_read_b64_tr_b16 a[54:55], v9 offset:57608
s_add_u32 m0, s48, 12288
ds_read_b64_tr_b16 a[56:57], v9 offset:55360
global_load_lds_dwordx4 v8, s[40:41] offset:-4096
ds_read_b64_tr_b16 a[58:59], v9 offset:57664
s_add_u32 s40, s40, 73728
ds_read_b64_tr_b16 a[60:61], v9 offset:55368
s_addc_u32 s41, s41, 0
ds_read_b64_tr_b16 a[62:63], v9 offset:57672
s_waitcnt vmcnt(18) lgkmcnt(0)
s_barrier
s_add_u32 m0, s48, 40960
ds_read_b64_tr_b16 a[64:65], v204 offset:8192
ds_read_b64_tr_b16 a[66:67], v204 offset:10496
global_load_lds_dwordx4 v0, s[40:41] offset:-4096
ds_read_b64_tr_b16 a[68:69], v204 offset:8200
global_load_lds_dwordx4 v1, s[40:41] offset:-3072
ds_read_b64_tr_b16 a[70:71], v204 offset:10504
global_load_lds_dwordx4 v2, s[40:41] offset:-2048
ds_read_b64_tr_b16 a[72:73], v204 offset:8256
global_load_lds_dwordx4 v3, s[40:41] offset:-1024
ds_read_b64_tr_b16 a[74:75], v204 offset:10560
ds_read_b64_tr_b16 a[76:77], v204 offset:8264
global_load_lds_dwordx4 v4, s[40:41] offset:0
ds_read_b64_tr_b16 a[78:79], v204 offset:10568
global_load_lds_dwordx4 v5, s[40:41] offset:1024
ds_read_b64_tr_b16 a[80:81], v204 offset:26624
global_load_lds_dwordx4 v6, s[40:41] offset:2048
ds_read_b64_tr_b16 a[82:83], v204 offset:28928
global_load_lds_dwordx4 v7, s[40:41] offset:3072
ds_read_b64_tr_b16 a[84:85], v204 offset:26632
ds_read_b64_tr_b16 a[86:87], v204 offset:28936
s_add_u32 m0, s48, 49152
ds_read_b64_tr_b16 a[88:89], v204 offset:26688
global_load_lds_dwordx4 v8, s[40:41] offset:-4096
ds_read_b64_tr_b16 a[90:91], v204 offset:28992
s_add_u32 s40, s40, 73728
ds_read_b64_tr_b16 a[92:93], v204 offset:26696
s_addc_u32 s41, s41, 0
ds_read_b64_tr_b16 a[94:95], v204 offset:29000
s_waitcnt vmcnt(18) lgkmcnt(0)
s_barrier
s_add_u32 m0, s48, 77824
ds_read_b64_tr_b16 a[96:97], v204 offset:45056
ds_read_b64_tr_b16 a[98:99], v204 offset:47360
global_load_lds_dwordx4 v0, s[40:41] offset:-4096
ds_read_b64_tr_b16 a[100:101], v204 offset:45064
global_load_lds_dwordx4 v1, s[40:41] offset:-3072
ds_read_b64_tr_b16 a[102:103], v204 offset:47368
global_load_lds_dwordx4 v2, s[40:41] offset:-2048
ds_read_b64_tr_b16 a[104:105], v204 offset:45120
global_load_lds_dwordx4 v3, s[40:41] offset:-1024
ds_read_b64_tr_b16 a[106:107], v204 offset:47424
ds_read_b64_tr_b16 a[108:109], v204 offset:45128
global_load_lds_dwordx4 v4, s[40:41] offset:0
ds_read_b64_tr_b16 a[110:111], v204 offset:47432
global_load_lds_dwordx4 v5, s[40:41] offset:1024
ds_read_b64_tr_b16 a[112:113], v204 offset:63488
global_load_lds_dwordx4 v6, s[40:41] offset:2048
ds_read_b64_tr_b16 a[114:115], v205 offset:256
global_load_lds_dwordx4 v7, s[40:41] offset:3072
ds_read_b64_tr_b16 a[116:117], v204 offset:63496
ds_read_b64_tr_b16 a[118:119], v205 offset:264
s_add_u32 m0, s48, 86016
ds_read_b64_tr_b16 a[120:121], v204 offset:63552
global_load_lds_dwordx4 v8, s[40:41] offset:-4096
ds_read_b64_tr_b16 a[122:123], v205 offset:320
s_add_u32 s40, s40, 73728
ds_read_b64_tr_b16 a[124:125], v204 offset:63560
s_addc_u32 s41, s41, 0
ds_read_b64_tr_b16 a[126:127], v205 offset:328
s_waitcnt vmcnt(18) lgkmcnt(0)
s_barrier
s_add_u32 m0, s48, 114688
ds_read_b64_tr_b16 a[128:129], v9 offset:0
ds_read_b64_tr_b16 a[130:131], v9 offset:2304
global_load_lds_dwordx4 v0, s[40:41] offset:-4096
ds_read_b64_tr_b16 a[132:133], v9 offset:8
global_load_lds_dwordx4 v1, s[40:41] offset:-3072
ds_read_b64_tr_b16 a[134:135], v9 offset:2312
ds_read_b64_tr_b16 a[136:137], v9 offset:64
global_load_lds_dwordx4 v2, s[40:41] offset:-2048
ds_read_b64_tr_b16 a[138:139], v9 offset:2368
global_load_lds_dwordx4 v3, s[40:41] offset:-1024
ds_read_b64_tr_b16 a[140:141], v9 offset:72
global_load_lds_dwordx4 v4, s[40:41] offset:0
ds_read_b64_tr_b16 a[142:143], v9 offset:2376
ds_read_b64_tr_b16 a[144:145], v9 offset:18432
global_load_lds_dwordx4 v5, s[40:41] offset:1024
ds_read_b64_tr_b16 a[146:147], v9 offset:20736
global_load_lds_dwordx4 v6, s[40:41] offset:2048
ds_read_b64_tr_b16 a[148:149], v9 offset:18440
ds_read_b64_tr_b16 a[150:151], v9 offset:20744
global_load_lds_dwordx4 v7, s[40:41] offset:3072
ds_read_b64_tr_b16 a[152:153], v9 offset:18496
s_add_u32 m0, s48, 122880
ds_read_b64_tr_b16 a[154:155], v9 offset:20800
global_load_lds_dwordx4 v8, s[40:41] offset:-4096
ds_read_b64_tr_b16 a[156:157], v9 offset:18504
ds_read_b64_tr_b16 a[158:159], v9 offset:20808
s_add_u32 s40, s40, 73728
ds_read_b64_tr_b16 v[60:61], v10 offset:18432
s_addc_u32 s41, s41, 0
ds_read_b64_tr_b16 v[62:63], v10 offset:20736
s_waitcnt vmcnt(18) lgkmcnt(0)
s_barrier
s_add_u32 m0, s48, 4096
ds_read_b64_tr_b16 a[160:161], v9 offset:36864
ds_read_b64_tr_b16 a[162:163], v9 offset:39168
global_load_lds_dwordx4 v0, s[40:41] offset:-4096
ds_read_b64_tr_b16 a[164:165], v9 offset:36872
ds_read_b64_tr_b16 a[166:167], v9 offset:39176
global_load_lds_dwordx4 v1, s[40:41] offset:-3072
ds_read_b64_tr_b16 a[168:169], v9 offset:36928
global_load_lds_dwordx4 v2, s[40:41] offset:-2048
ds_read_b64_tr_b16 a[170:171], v9 offset:39232
)"
R"(ds_read_b64_tr_b16 a[172:173], v9 offset:36936
global_load_lds_dwordx4 v3, s[40:41] offset:-1024
ds_read_b64_tr_b16 a[174:175], v9 offset:39240
global_load_lds_dwordx4 v4, s[40:41] offset:0
ds_read_b64_tr_b16 v[64:65], v10 offset:36864
ds_read_b64_tr_b16 v[66:67], v10 offset:39168
global_load_lds_dwordx4 v5, s[40:41] offset:1024
ds_read_b64_tr_b16 a[176:177], v9 offset:55296
global_load_lds_dwordx4 v6, s[40:41] offset:2048
ds_read_b64_tr_b16 a[178:179], v9 offset:57600
ds_read_b64_tr_b16 a[180:181], v9 offset:55304
global_load_lds_dwordx4 v7, s[40:41] offset:3072
ds_read_b64_tr_b16 a[182:183], v9 offset:57608
s_add_u32 m0, s48, 12288
ds_read_b64_tr_b16 a[184:185], v9 offset:55360
ds_read_b64_tr_b16 a[186:187], v9 offset:57664
global_load_lds_dwordx4 v8, s[40:41] offset:-4096
ds_read_b64_tr_b16 a[188:189], v9 offset:55368
s_sub_u32 s40, s40, 589824
ds_read_b64_tr_b16 a[190:191], v9 offset:57672
ds_read_b64_tr_b16 v[68:69], v10 offset:55296
s_subb_u32 s41, s41, 0
ds_read_b64_tr_b16 v[70:71], v10 offset:57600
s_waitcnt vmcnt(18) lgkmcnt(0)
s_barrier
ds_read_b64_tr_b16 a[192:193], v204 offset:8192
ds_read_b64_tr_b16 a[194:195], v204 offset:10496
ds_read_b64_tr_b16 a[196:197], v204 offset:8200
ds_read_b64_tr_b16 a[198:199], v204 offset:10504
ds_read_b64_tr_b16 a[200:201], v204 offset:8256
ds_read_b64_tr_b16 a[202:203], v204 offset:10560
ds_read_b64_tr_b16 a[204:205], v204 offset:8264
ds_read_b64_tr_b16 a[206:207], v204 offset:10568
ds_read_b64_tr_b16 v[72:73], v206 offset:8192
ds_read_b64_tr_b16 v[74:75], v206 offset:10496
ds_read_b64_tr_b16 a[208:209], v204 offset:26624
ds_read_b64_tr_b16 a[210:211], v204 offset:28928
ds_read_b64_tr_b16 a[212:213], v204 offset:26632
ds_read_b64_tr_b16 a[214:215], v204 offset:28936
ds_read_b64_tr_b16 a[216:217], v204 offset:26688
ds_read_b64_tr_b16 a[218:219], v204 offset:28992
ds_read_b64_tr_b16 a[220:221], v204 offset:26696
ds_read_b64_tr_b16 a[222:223], v204 offset:29000
ds_read_b64_tr_b16 v[76:77], v206 offset:26624
ds_read_b64_tr_b16 v[78:79], v206 offset:28928
s_waitcnt vmcnt(9) lgkmcnt(0)
s_barrier
ds_read_b64_tr_b16 a[224:225], v204 offset:45056
ds_read_b64_tr_b16 a[226:227], v204 offset:47360
ds_read_b64_tr_b16 a[228:229], v204 offset:45064
ds_read_b64_tr_b16 a[230:231], v204 offset:47368
ds_read_b64_tr_b16 a[232:233], v204 offset:45120
ds_read_b64_tr_b16 a[234:235], v204 offset:47424
ds_read_b64_tr_b16 a[236:237], v204 offset:45128
ds_read_b64_tr_b16 a[238:239], v204 offset:47432
ds_read_b64_tr_b16 v[80:81], v206 offset:45056
ds_read_b64_tr_b16 v[82:83], v206 offset:47360
ds_read_b64_tr_b16 a[240:241], v204 offset:63488
ds_read_b64_tr_b16 a[242:243], v205 offset:256
ds_read_b64_tr_b16 a[244:245], v204 offset:63496
ds_read_b64_tr_b16 a[246:247], v205 offset:264
ds_read_b64_tr_b16 a[248:249], v204 offset:63552
ds_read_b64_tr_b16 a[250:251], v205 offset:320
ds_read_b64_tr_b16 a[252:253], v204 offset:63560
ds_read_b64_tr_b16 a[254:255], v205 offset:328
ds_read_b64_tr_b16 v[84:85], v206 offset:63488
ds_read_b64_tr_b16 v[86:87], v207 offset:256
s_waitcnt vmcnt(0) lgkmcnt(0)
s_barrier
ds_read_b64_tr_b16 v[28:29], v9 offset:0
ds_read_b64_tr_b16 v[30:31], v9 offset:2304
ds_read_b64_tr_b16 v[32:33], v9 offset:8
ds_read_b64_tr_b16 v[34:35], v9 offset:2312
ds_read_b64_tr_b16 v[36:37], v9 offset:64
ds_read_b64_tr_b16 v[38:39], v9 offset:2368
ds_read_b64_tr_b16 v[40:41], v9 offset:72
ds_read_b64_tr_b16 v[42:43], v9 offset:2376
ds_read_b64_tr_b16 v[88:89], v10 offset:0
ds_read_b64_tr_b16 v[90:91], v10 offset:2304
ds_read_b64_tr_b16 v[44:45], v9 offset:18432
ds_read_b64_tr_b16 v[46:47], v9 offset:20736
ds_read_b64_tr_b16 v[48:49], v9 offset:18440
ds_read_b64_tr_b16 v[50:51], v9 offset:20744
ds_read_b64_tr_b16 v[52:53], v9 offset:18496
ds_read_b64_tr_b16 v[54:55], v9 offset:20800
ds_read_b64_tr_b16 v[56:57], v9 offset:18504
ds_read_b64_tr_b16 v[58:59], v9 offset:20808
ds_read_b64_tr_b16 v[92:93], v10 offset:18432
ds_read_b64_tr_b16 v[94:95], v10 offset:20736
s_waitcnt lgkmcnt(0)
s_barrier
v_add_lshl_u32 v252, v24, 0, 7
v_bfi_b32 v252, s69, v252, v23
v_add_lshl_u32 v253, v22, 0, 7
v_bfi_b32 v253, s69, v253, v21
s_mov_b32 s92, 0x01010101
s_mov_b32 s93, 0x01010101
s_mov_b64 s[76:77], 0
s_add_u32 m0, s49, 18432
v_add_u32 v14, 18688, v14
s_xor_b32 s49, s49, 0x10000
s_mov_b32 m0, 53248
v_add_u32 v13, 18688, v13
s_add_u32 m0, s49, 0
v_add_u32 v11, 18432, v11
s_mov_b32 m0, 61440
v_add_u32 v14, 18688, v14
v_add_u32 v13, 18688, v13
s_add_u32 m0, s49, 18432
s_mov_b64 s[76:77], -1
s_mov_b32 s70, 33
s_waitcnt vmcnt(0) lgkmcnt(0)
s_nop 7
s_branch .Lwo_in18
.p2align 6
.Lwo_top19:
s_waitcnt lgkmcnt(5)
v_mfma_f32_16x16x32_bf16 v[96:99], a[144:147], v[132:135], v[96:99]
s_waitcnt lgkmcnt(4)
ds_read_b128 v[144:147], v25 offset:12288
v_add_f32 v176, v176, v184
v_mfma_f32_16x16x32_bf16 v[100:103], a[148:151], v[132:135], v[100:103]
v_mfma_f32_16x16x32_bf16 v[104:107], a[152:155], v[132:135], v[104:107]
s_waitcnt vmcnt(6)
s_barrier
v_mfma_f32_16x16x32_bf16 v[108:111], a[156:159], v[132:135], v[108:111]
v_add_f32 v177, v177, v185
v_add_f32 v178, v178, v186
v_mfma_f32_16x16x32_bf16 v[128:131], v[60:63], v[132:135], 0
s_waitcnt lgkmcnt(3)
v_add_f32 v179, v179, v187
v_add_f32 v176, v176, v188
v_mfma_f32_16x16x32_bf16 v[96:99], a[160:163], v[136:139], v[96:99]
ds_read_b128 v[132:135], v25 offset:13312
v_add_f32 v177, v177, v189
v_mfma_f32_16x16x32_bf16 v[100:103], a[164:167], v[136:139], v[100:103]
v_add_f32 v178, v178, v190
v_add_f32 v179, v179, v191
v_mfma_f32_16x16x32_bf16 v[104:107], a[168:171], v[136:139], v[104:107]
s_waitcnt lgkmcnt(3)
v_add_f32 v180, v180, v192
v_add_f32 v181, v181, v193
v_mfma_f32_16x16x32_bf16 v[108:111], a[172:175], v[136:139], v[108:111]
v_add_f32 v182, v182, v194
v_add_f32 v183, v183, v195
v_mfma_f32_16x16x32_bf16 v[128:131], v[64:67], v[136:139], v[128:131]
s_waitcnt lgkmcnt(2)
v_cvt_pk_bf16_f32 v172, v176, v177
v_cvt_pk_bf16_f32 v173, v178, v179
v_mfma_f32_16x16x32_bf16 v[96:99], a[176:179], v[140:143], v[96:99]
s_cmp_eq_u32 s70, 32
ds_read_b128 v[136:139], v25 offset:14336
v_cvt_pk_bf16_f32 v174, v180, v181
v_mfma_f32_16x16x32_bf16 v[100:103], a[180:183], v[140:143], v[100:103]
s_cselect_b64 s[42:43], s[44:45], s[42:43]
v_cvt_pk_bf16_f32 v175, v182, v183
v_cvt_pk_bf16_f32 v164, v112, v113
v_mfma_f32_16x16x32_bf16 v[104:107], a[184:187], v[140:143], v[104:107]
s_cmp_eq_u32 s70, 16
global_store_dwordx4 v14, v[172:175], s[38:39] sc1
v_mfma_f32_16x16x32_bf16 v[108:111], a[188:191], v[140:143], v[108:111]
s_cselect_b64 s[42:43], s[46:47], s[42:43]
v_add_u32 v14, 18688, v14
v_cvt_pk_bf16_f32 v165, v114, v115
v_mfma_f32_16x16x32_bf16 v[128:131], v[68:71], v[140:143], v[128:131]
s_waitcnt lgkmcnt(2)
v_cvt_pk_bf16_f32 v166, v116, v117
v_cvt_pk_bf16_f32 v167, v118, v119
v_mfma_f32_16x16x32_bf16 v[96:99], a[192:195], v[144:147], v[96:99]
s_xor_b32 s49, s49, 0x10000
ds_read_b128 v[140:143], v25 offset:15360
v_add_f32 v254, v112, v113
v_mfma_f32_16x16x32_bf16 v[100:103], a[196:199], v[144:147], v[100:103]
s_mov_b32 m0, 53248
global_store_dwordx4 v13, v[164:167], s[38:39] offset:0 sc1
v_mfma_f32_16x16x32_bf16 v[104:107], a[200:203], v[144:147], v[104:107]
v_cvt_pk_bf16_f32 v168, v120, v121
v_cvt_pk_bf16_f32 v169, v122, v123
v_mfma_f32_16x16x32_bf16 v[108:111], a[204:207], v[144:147], v[108:111]
v_cvt_pk_bf16_f32 v170, v124, v125
v_cvt_pk_bf16_f32 v171, v126, v127
v_mfma_f32_16x16x32_bf16 v[128:131], v[72:75], v[144:147], v[128:131]
s_waitcnt lgkmcnt(2)
global_store_dwordx4 v13, v[168:171], s[38:39] offset:1024 sc1
v_mfma_f32_16x16x32_bf16 v[96:99], a[208:211], v[132:135], v[96:99]
ds_read_b128 v[144:147], v25 offset:16384
v_add_u32 v13, 18688, v13
v_mfma_f32_16x16x32_bf16 v[100:103], a[212:215], v[132:135], v[100:103]
v_mul_f32 v255, v112, v112
v_fmac_f32 v255, v113, v113
v_mfma_f32_16x16x32_bf16 v[104:107], a[216:219], v[132:135], v[104:107]
v_add_f32 v254, v254, v114
v_fmac_f32 v255, v114, v114
v_mfma_f32_16x16x32_bf16 v[108:111], a[220:223], v[132:135], v[108:111]
v_add_f32 v254, v254, v115
v_fmac_f32 v255, v115, v115
v_mfma_f32_16x16x32_bf16 v[128:131], v[76:79], v[132:135], v[128:131]
s_waitcnt vmcnt(7)
global_load_lds_dwordx4 v16, s[42:43] offset:0 sc1
ds_read_b128 v[148:151], v19 offset:8192
s_waitcnt lgkmcnt(3)
v_mfma_f32_16x16x32_bf16 v[96:99], a[224:227], v[136:139], v[96:99]
ds_read_b128 v[132:135], v25 offset:17408
ds_read_b128 v[152:155], v20 offset:8192
v_mfma_f32_16x16x32_bf16 v[100:103], a[228:231], v[136:139], v[100:103]
v_add_f32 v254, v254, v116
v_fmac_f32 v255, v116, v116
v_mfma_f32_16x16x32_bf16 v[104:107], a[232:235], v[136:139], v[104:107]
v_add_f32 v254, v254, v117
v_fmac_f32 v255, v117, v117
v_mfma_f32_16x16x32_bf16 v[108:111], a[236:239], v[136:139], v[108:111]
v_add_f32 v254, v254, v118
v_fmac_f32 v255, v118, v118
v_mfma_f32_16x16x32_bf16 v[128:131], v[80:83], v[136:139], v[128:131]
s_waitcnt lgkmcnt(4)
v_add_f32 v254, v254, v119
v_fmac_f32 v255, v119, v119
v_mfma_f32_16x16x32_bf16 v[96:99], a[240:243], v[140:143], v[96:99]
ds_read_b128 v[136:139], v25 offset:0
v_add_f32 v254, v254, v120
v_mfma_f32_16x16x32_bf16 v[100:103], a[244:247], v[140:143], v[100:103]
global_load_lds_dwordx4 v17, s[42:43] offset:1024 sc1
v_fmac_f32 v255, v120, v120
v_mfma_f32_16x16x32_bf16 v[104:107], a[248:251], v[140:143], v[104:107]
v_add_f32 v254, v254, v121
v_fmac_f32 v255, v121, v121
v_mfma_f32_16x16x32_bf16 v[108:111], a[252:255], v[140:143], v[108:111]
v_add_f32 v254, v254, v122
v_fmac_f32 v255, v122, v122
v_mfma_f32_16x16x32_bf16 v[128:131], v[84:87], v[140:143], v[128:131]
s_waitcnt lgkmcnt(4)
v_add_f32 v254, v254, v123
v_fmac_f32 v255, v123, v123
v_mfma_f32_16x16x32_bf16 v[96:99], v[28:31], v[144:147], v[96:99]
ds_read_b128 v[140:143], v25 offset:1024
v_add_f32 v254, v254, v124
v_mfma_f32_16x16x32_bf16 v[100:103], v[32:35], v[144:147], v[100:103]
s_add_u32 m0, s49, 0
v_fmac_f32 v255, v124, v124
v_add_f32 v254, v254, v125
v_mfma_f32_16x16x32_bf16 v[104:107], v[36:39], v[144:147], v[104:107]
v_fmac_f32 v255, v125, v125
v_add_f32 v254, v254, v126
v_mfma_f32_16x16x32_bf16 v[108:111], v[40:43], v[144:147], v[108:111]
v_fmac_f32 v255, v126, v126
v_add_f32 v254, v254, v127
v_mfma_f32_16x16x32_bf16 v[128:131], v[88:91], v[144:147], v[128:131]
s_waitcnt lgkmcnt(3)
v_fmac_f32 v255, v127, v127
v_add_f32 v254, v254, v176
v_mfma_f32_16x16x32_bf16 v[96:99], v[44:47], v[132:135], v[96:99]
ds_read_b128 v[144:147], v25 offset:2048
v_fmac_f32 v255, v176, v176
v_mfma_f32_16x16x32_bf16 v[100:103], v[48:51], v[132:135], v[100:103]
v_add_f32 v254, v254, v177
v_fmac_f32 v255, v177, v177
v_mfma_f32_16x16x32_bf16 v[104:107], v[52:55], v[132:135], v[104:107]
v_add_f32 v254, v254, v178
v_fmac_f32 v255, v178, v178
v_mfma_f32_16x16x32_bf16 v[108:111], v[56:59], v[132:135], v[108:111]
v_add_f32 v254, v254, v179
v_fmac_f32 v255, v179, v179
v_mfma_f32_16x16x32_bf16 v[128:131], v[92:95], v[132:135], v[128:131]
s_waitcnt lgkmcnt(2)
v_add_f32 v254, v254, v180
v_fmac_f32 v255, v180, v180
v_mfma_f32_16x16x32_bf16 v[96:99], a[0:3], v[136:139], v[96:99]
ds_read_b128 v[132:135], v25 offset:3072
v_add_f32 v254, v254, v181
v_mfma_f32_16x16x32_bf16 v[100:103], a[4:7], v[136:139], v[100:103]
v_fmac_f32 v255, v181, v181
v_add_f32 v254, v254, v182
v_mfma_f32_16x16x32_bf16 v[104:107], a[8:11], v[136:139], v[104:107]
v_fmac_f32 v255, v182, v182
v_add_f32 v254, v254, v183
v_mfma_f32_16x16x32_bf16 v[108:111], a[12:15], v[136:139], v[108:111]
s_waitcnt lgkmcnt(2)
v_fmac_f32 v255, v183, v183
v_mfma_f32_16x16x32_bf16 v[96:99], a[16:19], v[140:143], v[96:99]
ds_read_b128 v[136:139], v25 offset:4096
v_mfma_f32_16x16x32_bf16 v[100:103], a[20:23], v[140:143], v[100:103]
ds_write_b64 v27, v[254:255] offset:46592
v_mfma_f32_16x16x32_bf16 v[104:107], a[24:27], v[140:143], v[104:107]
v_lshlrev_b32 v176, 16, v156
v_and_b32 v177, 0xffff0000, v156
v_mfma_f32_16x16x32_bf16 v[108:111], a[28:31], v[140:143], v[108:111]
s_waitcnt lgkmcnt(3)
v_lshlrev_b32 v178, 16, v157
v_and_b32 v179, 0xffff0000, v157
v_mfma_f32_16x16x32_bf16 v[96:99], a[32:35], v[144:147], v[96:99]
ds_read_b128 v[140:143], v25 offset:5120
v_lshlrev_b32 v180, 16, v158
v_mfma_f32_16x16x32_bf16 v[100:103], a[36:39], v[144:147], v[100:103]
v_mfma_f32_16x16x32_bf16 v[104:107], a[40:43], v[144:147], v[104:107]
s_barrier
v_mfma_f32_16x16x32_bf16 v[108:111], a[44:47], v[144:147], v[108:111]
s_waitcnt lgkmcnt(3)
v_and_b32 v181, 0xffff0000, v158
v_lshlrev_b32 v182, 16, v159
v_mfma_f32_16x16x32_bf16 v[96:99], a[48:51], v[132:135], v[96:99]
ds_read_b128 v[144:147], v25 offset:6144
global_load_lds_dwordx4 v11, s[38:39] offset:-2048 sc1
v_mfma_f32_16x16x32_bf16 v[100:103], a[52:55], v[132:135], v[100:103]
v_and_b32 v183, 0xffff0000, v159
v_add_f32 v180, v180, v128
v_mfma_f32_16x16x32_bf16 v[104:107], a[56:59], v[132:135], v[104:107]
v_add_f32 v181, v181, v129
v_add_f32 v182, v182, v130
v_mfma_f32_16x16x32_bf16 v[108:111], a[60:63], v[132:135], v[108:111]
s_waitcnt lgkmcnt(3)
global_load_lds_dwordx4 v11, s[38:39] offset:-1024 sc1
v_add_f32 v183, v183, v131
v_mfma_f32_16x16x32_bf16 v[96:99], a[64:67], v[136:139], v[96:99]
ds_read_b128 v[132:135], v25 offset:7168
global_load_dwordx4 v[156:159], v18, s[42:43] sc1
v_mfma_f32_16x16x32_bf16 v[100:103], a[68:71], v[136:139], v[100:103]
s_sub_u32 s42, s42, 18432
v_lshlrev_b32 v112, 16, v148
v_and_b32 v113, 0xffff0000, v148
v_mfma_f32_16x16x32_bf16 v[104:107], a[72:75], v[136:139], v[104:107]
s_subb_u32 s43, s43, 0
global_load_lds_dwordx4 v11, s[38:39] offset:0 sc1
v_add_u32 v11, 18432, v11
v_mfma_f32_16x16x32_bf16 v[108:111], a[76:79], v[136:139], v[108:111]
s_waitcnt lgkmcnt(2)
v_min_u32 v11, v11, v12
v_lshlrev_b32 v114, 16, v149
v_mfma_f32_16x16x32_bf16 v[96:99], a[80:83], v[140:143], v[96:99]
ds_read_b128 v[136:139], v25 offset:8192
v_and_b32 v115, 0xffff0000, v149
v_mfma_f32_16x16x32_bf16 v[100:103], a[84:87], v[140:143], v[100:103]
v_lshlrev_b32 v116, 16, v150
v_and_b32 v117, 0xffff0000, v150
v_mfma_f32_16x16x32_bf16 v[104:107], a[88:91], v[140:143], v[104:107]
v_lshlrev_b32 v118, 16, v151
v_and_b32 v119, 0xffff0000, v151
v_mfma_f32_16x16x32_bf16 v[108:111], a[92:95], v[140:143], v[108:111]
s_waitcnt lgkmcnt(2)
v_lshlrev_b32 v120, 16, v152
v_and_b32 v121, 0xffff0000, v152
v_mfma_f32_16x16x32_bf16 v[96:99], a[96:99], v[144:147], v[96:99]
v_lshlrev_b32 v122, 16, v153
v_and_b32 v123, 0xffff0000, v153
v_mfma_f32_16x16x32_bf16 v[100:103], a[100:103], v[144:147], v[100:103]
v_mfma_f32_16x16x32_bf16 v[104:107], a[104:107], v[144:147], v[104:107]
s_waitcnt vmcnt(9)
s_barrier
v_mfma_f32_16x16x32_bf16 v[108:111], a[108:111], v[144:147], v[108:111]
s_waitcnt lgkmcnt(1)
ds_read_b128 v[140:143], v25 offset:27648
ds_read_b128 v[184:187], v26 offset:36864
v_mfma_f32_16x16x32_bf16 v[96:99], a[112:115], v[132:135], v[96:99]
ds_read_b128 v[144:147], v25 offset:28672
ds_read_b128 v[188:191], v26 offset:37888
v_mfma_f32_16x16x32_bf16 v[100:103], a[116:119], v[132:135], v[100:103]
ds_read_b128 v[192:195], v26 offset:38912
v_lshlrev_b32 v124, 16, v154
v_mfma_f32_16x16x32_bf16 v[104:107], a[120:123], v[132:135], v[104:107]
v_and_b32 v125, 0xffff0000, v154
v_lshlrev_b32 v126, 16, v155
v_mfma_f32_16x16x32_bf16 v[108:111], a[124:127], v[132:135], v[108:111]
s_waitcnt lgkmcnt(5)
v_and_b32 v127, 0xffff0000, v155
v_mfma_f32_16x16x32_bf16 v[96:99], a[128:131], v[136:139], v[96:99]
ds_read_b128 v[132:135], v25 offset:29696
v_mfma_f32_16x16x32_bf16 v[100:103], a[132:135], v[136:139], v[100:103]
v_mfma_f32_16x16x32_bf16 v[104:107], a[136:139], v[136:139], v[104:107]
s_waitcnt lgkmcnt(4)
v_add_f32 v176, v176, v184
v_add_f32 v177, v177, v185
v_mfma_f32_16x16x32_bf16 v[108:111], a[140:143], v[136:139], v[108:111]
v_add_f32 v178, v178, v186
v_add_f32 v179, v179, v187
v_mfma_f32_16x16x32_bf16 v[112:115], a[144:147], v[140:143], v[112:115]
s_waitcnt lgkmcnt(2)
ds_read_b128 v[136:139], v25 offset:30720
v_add_f32 v176, v176, v188
v_mfma_f32_16x16x32_bf16 v[116:119], a[148:151], v[140:143], v[116:119]
v_mfma_f32_16x16x32_bf16 v[120:123], a[152:155], v[140:143], v[120:123]
s_waitcnt vmcnt(6)
s_barrier
v_mfma_f32_16x16x32_bf16 v[124:127], a[156:159], v[140:143], v[124:127]
v_add_f32 v177, v177, v189
v_add_f32 v178, v178, v190
v_mfma_f32_16x16x32_bf16 v[128:131], v[60:63], v[140:143], 0
s_waitcnt lgkmcnt(2)
v_add_f32 v179, v179, v191
v_add_f32 v180, v180, v192
v_mfma_f32_16x16x32_bf16 v[112:115], a[160:163], v[144:147], v[112:115]
ds_read_b128 v[140:143], v25 offset:31744
v_add_f32 v181, v181, v193
v_mfma_f32_16x16x32_bf16 v[116:119], a[164:167], v[144:147], v[116:119]
v_add_f32 v182, v182, v194
v_add_f32 v183, v183, v195
v_mfma_f32_16x16x32_bf16 v[120:123], a[168:171], v[144:147], v[120:123]
v_cvt_pk_bf16_f32 v172, v176, v177
v_cvt_pk_bf16_f32 v173, v178, v179
v_mfma_f32_16x16x32_bf16 v[124:127], a[172:175], v[144:147], v[124:127]
v_cvt_pk_bf16_f32 v174, v180, v181
v_cvt_pk_bf16_f32 v175, v182, v183
)"
R"(v_mfma_f32_16x16x32_bf16 v[128:131], v[64:67], v[144:147], v[128:131]
s_waitcnt lgkmcnt(2)
global_store_dwordx4 v14, v[172:175], s[38:39] sc1
v_mfma_f32_16x16x32_bf16 v[112:115], a[176:179], v[132:135], v[112:115]
s_mov_b32 m0, 61440
ds_read_b128 v[144:147], v25 offset:32768
v_add_u32 v14, 18688, v14
v_mfma_f32_16x16x32_bf16 v[116:119], a[180:183], v[132:135], v[116:119]
v_cvt_pk_bf16_f32 v164, v96, v97
v_cvt_pk_bf16_f32 v165, v98, v99
v_mfma_f32_16x16x32_bf16 v[120:123], a[184:187], v[132:135], v[120:123]
v_cvt_pk_bf16_f32 v166, v100, v101
v_cvt_pk_bf16_f32 v167, v102, v103
v_mfma_f32_16x16x32_bf16 v[124:127], a[188:191], v[132:135], v[124:127]
global_store_dwordx4 v13, v[164:167], s[38:39] offset:0 sc1
v_mfma_f32_16x16x32_bf16 v[128:131], v[68:71], v[132:135], v[128:131]
s_waitcnt lgkmcnt(2)
v_cvt_pk_bf16_f32 v168, v104, v105
v_cvt_pk_bf16_f32 v169, v106, v107
v_mfma_f32_16x16x32_bf16 v[112:115], a[192:195], v[136:139], v[112:115]
ds_read_b128 v[132:135], v25 offset:33792
v_cvt_pk_bf16_f32 v170, v108, v109
v_mfma_f32_16x16x32_bf16 v[116:119], a[196:199], v[136:139], v[116:119]
v_cvt_pk_bf16_f32 v171, v110, v111
v_add_f32 v254, v96, v97
v_mfma_f32_16x16x32_bf16 v[120:123], a[200:203], v[136:139], v[120:123]
global_store_dwordx4 v13, v[168:171], s[38:39] offset:1024 sc1
v_mfma_f32_16x16x32_bf16 v[124:127], a[204:207], v[136:139], v[124:127]
v_add_u32 v13, 18688, v13
v_mul_f32 v255, v96, v96
v_mfma_f32_16x16x32_bf16 v[128:131], v[72:75], v[136:139], v[128:131]
s_waitcnt lgkmcnt(2)
v_fmac_f32 v255, v97, v97
v_add_f32 v254, v254, v98
v_mfma_f32_16x16x32_bf16 v[112:115], a[208:211], v[140:143], v[112:115]
ds_read_b128 v[136:139], v25 offset:34816
v_fmac_f32 v255, v98, v98
v_mfma_f32_16x16x32_bf16 v[116:119], a[212:215], v[140:143], v[116:119]
v_add_f32 v254, v254, v99
v_fmac_f32 v255, v99, v99
v_mfma_f32_16x16x32_bf16 v[120:123], a[216:219], v[140:143], v[120:123]
v_add_f32 v254, v254, v100
v_fmac_f32 v255, v100, v100
v_mfma_f32_16x16x32_bf16 v[124:127], a[220:223], v[140:143], v[124:127]
v_add_f32 v254, v254, v101
v_fmac_f32 v255, v101, v101
v_mfma_f32_16x16x32_bf16 v[128:131], v[76:79], v[140:143], v[128:131]
s_waitcnt vmcnt(7)
global_load_lds_dwordx4 v16, s[42:43] offset:0 sc1
ds_read_b128 v[148:151], v19 offset:0
s_waitcnt lgkmcnt(3)
v_mfma_f32_16x16x32_bf16 v[112:115], a[224:227], v[144:147], v[112:115]
ds_read_b128 v[140:143], v25 offset:35840
ds_read_b128 v[152:155], v20 offset:0
v_mfma_f32_16x16x32_bf16 v[116:119], a[228:231], v[144:147], v[116:119]
v_add_f32 v254, v254, v102
v_fmac_f32 v255, v102, v102
v_mfma_f32_16x16x32_bf16 v[120:123], a[232:235], v[144:147], v[120:123]
v_add_f32 v254, v254, v103
v_fmac_f32 v255, v103, v103
v_mfma_f32_16x16x32_bf16 v[124:127], a[236:239], v[144:147], v[124:127]
v_add_f32 v254, v254, v104
v_fmac_f32 v255, v104, v104
v_mfma_f32_16x16x32_bf16 v[128:131], v[80:83], v[144:147], v[128:131]
s_waitcnt lgkmcnt(4)
v_add_f32 v254, v254, v105
v_fmac_f32 v255, v105, v105
v_mfma_f32_16x16x32_bf16 v[112:115], a[240:243], v[132:135], v[112:115]
ds_read_b128 v[144:147], v25 offset:18432
v_add_f32 v254, v254, v106
v_mfma_f32_16x16x32_bf16 v[116:119], a[244:247], v[132:135], v[116:119]
global_load_lds_dwordx4 v17, s[42:43] offset:1024 sc1
v_fmac_f32 v255, v106, v106
v_mfma_f32_16x16x32_bf16 v[120:123], a[248:251], v[132:135], v[120:123]
v_add_f32 v254, v254, v107
v_fmac_f32 v255, v107, v107
v_mfma_f32_16x16x32_bf16 v[124:127], a[252:255], v[132:135], v[124:127]
v_add_f32 v254, v254, v108
v_fmac_f32 v255, v108, v108
v_mfma_f32_16x16x32_bf16 v[128:131], v[84:87], v[132:135], v[128:131]
s_waitcnt lgkmcnt(4)
v_add_f32 v254, v254, v109
v_fmac_f32 v255, v109, v109
v_mfma_f32_16x16x32_bf16 v[112:115], v[28:31], v[136:139], v[112:115]
ds_read_b128 v[132:135], v25 offset:19456
v_add_f32 v254, v254, v110
v_mfma_f32_16x16x32_bf16 v[116:119], v[32:35], v[136:139], v[116:119]
s_add_u32 m0, s49, 18432
v_fmac_f32 v255, v110, v110
v_add_f32 v254, v254, v111
v_mfma_f32_16x16x32_bf16 v[120:123], v[36:39], v[136:139], v[120:123]
v_fmac_f32 v255, v111, v111
v_add_f32 v254, v254, v176
v_mfma_f32_16x16x32_bf16 v[124:127], v[40:43], v[136:139], v[124:127]
v_fmac_f32 v255, v176, v176
v_add_f32 v254, v254, v177
v_mfma_f32_16x16x32_bf16 v[128:131], v[88:91], v[136:139], v[128:131]
s_waitcnt lgkmcnt(3)
v_fmac_f32 v255, v177, v177
v_add_f32 v254, v254, v178
v_mfma_f32_16x16x32_bf16 v[112:115], v[44:47], v[140:143], v[112:115]
ds_read_b128 v[136:139], v25 offset:20480
v_fmac_f32 v255, v178, v178
v_mfma_f32_16x16x32_bf16 v[116:119], v[48:51], v[140:143], v[116:119]
v_add_f32 v254, v254, v179
v_fmac_f32 v255, v179, v179
v_mfma_f32_16x16x32_bf16 v[120:123], v[52:55], v[140:143], v[120:123]
v_add_f32 v254, v254, v180
v_fmac_f32 v255, v180, v180
v_mfma_f32_16x16x32_bf16 v[124:127], v[56:59], v[140:143], v[124:127]
v_add_f32 v254, v254, v181
v_fmac_f32 v255, v181, v181
v_mfma_f32_16x16x32_bf16 v[128:131], v[92:95], v[140:143], v[128:131]
s_waitcnt lgkmcnt(2)
v_add_f32 v254, v254, v182
v_fmac_f32 v255, v182, v182
v_mfma_f32_16x16x32_bf16 v[112:115], a[0:3], v[144:147], v[112:115]
ds_read_b128 v[140:143], v25 offset:21504
v_add_f32 v254, v254, v183
v_mfma_f32_16x16x32_bf16 v[116:119], a[4:7], v[144:147], v[116:119]
v_fmac_f32 v255, v183, v183
v_mfma_f32_16x16x32_bf16 v[120:123], a[8:11], v[144:147], v[120:123]
ds_write_b64 v27, v[254:255] offset:44544
v_mfma_f32_16x16x32_bf16 v[124:127], a[12:15], v[144:147], v[124:127]
s_waitcnt lgkmcnt(3)
v_lshlrev_b32 v176, 16, v160
v_and_b32 v177, 0xffff0000, v160
v_mfma_f32_16x16x32_bf16 v[112:115], a[16:19], v[132:135], v[112:115]
ds_read_b128 v[144:147], v25 offset:22528
v_lshlrev_b32 v178, 16, v161
v_mfma_f32_16x16x32_bf16 v[116:119], a[20:23], v[132:135], v[116:119]
v_and_b32 v179, 0xffff0000, v161
v_lshlrev_b32 v180, 16, v162
v_mfma_f32_16x16x32_bf16 v[120:123], a[24:27], v[132:135], v[120:123]
v_and_b32 v181, 0xffff0000, v162
v_lshlrev_b32 v182, 16, v163
v_mfma_f32_16x16x32_bf16 v[124:127], a[28:31], v[132:135], v[124:127]
s_waitcnt lgkmcnt(3)
v_and_b32 v183, 0xffff0000, v163
v_add_f32 v180, v180, v128
v_mfma_f32_16x16x32_bf16 v[112:115], a[32:35], v[136:139], v[112:115]
ds_read_b128 v[132:135], v25 offset:23552
v_add_f32 v181, v181, v129
v_mfma_f32_16x16x32_bf16 v[116:119], a[36:39], v[136:139], v[116:119]
v_mfma_f32_16x16x32_bf16 v[120:123], a[40:43], v[136:139], v[120:123]
s_barrier
v_mfma_f32_16x16x32_bf16 v[124:127], a[44:47], v[136:139], v[124:127]
s_waitcnt lgkmcnt(3)
v_add_f32 v182, v182, v130
v_add_f32 v183, v183, v131
v_mfma_f32_16x16x32_bf16 v[112:115], a[48:51], v[140:143], v[112:115]
s_mov_b64 s[76:77], -1
ds_read_b128 v[136:139], v25 offset:24576
global_load_lds_dwordx4 v11, s[38:39] offset:-2048 sc1
v_mfma_f32_16x16x32_bf16 v[116:119], a[52:55], v[140:143], v[116:119]
global_load_dwordx4 v[160:163], v18, s[42:43] sc1
v_lshlrev_b32 v96, 16, v148
v_mfma_f32_16x16x32_bf16 v[120:123], a[56:59], v[140:143], v[120:123]
s_sub_u32 s42, s42, 18432
v_and_b32 v97, 0xffff0000, v148
v_lshlrev_b32 v98, 16, v149
.Lwo_in18:
v_mfma_f32_16x16x32_bf16 v[124:127], a[60:63], v[140:143], v[124:127]
s_waitcnt lgkmcnt(2)
global_load_lds_dwordx4 v11, s[38:39] offset:-1024 sc1
v_and_b32 v99, 0xffff0000, v149
v_mfma_f32_16x16x32_bf16 v[112:115], a[64:67], v[144:147], v[112:115]
s_subb_u32 s43, s43, 0
ds_read_b128 v[140:143], v25 offset:25600
v_lshlrev_b32 v100, 16, v150
v_mfma_f32_16x16x32_bf16 v[116:119], a[68:71], v[144:147], v[116:119]
v_and_b32 v101, 0xffff0000, v150
v_lshlrev_b32 v102, 16, v151
v_mfma_f32_16x16x32_bf16 v[120:123], a[72:75], v[144:147], v[120:123]
global_load_lds_dwordx4 v11, s[38:39] offset:0 sc1
v_add_u32 v11, 18432, v11
v_mfma_f32_16x16x32_bf16 v[124:127], a[76:79], v[144:147], v[124:127]
s_waitcnt lgkmcnt(2)
v_min_u32 v11, v11, v12
v_and_b32 v103, 0xffff0000, v151
v_mfma_f32_16x16x32_bf16 v[112:115], a[80:83], v[132:135], v[112:115]
ds_read_b128 v[144:147], v25 offset:26624
v_lshlrev_b32 v104, 16, v152
v_mfma_f32_16x16x32_bf16 v[116:119], a[84:87], v[132:135], v[116:119]
v_and_b32 v105, 0xffff0000, v152
v_lshlrev_b32 v106, 16, v153
v_mfma_f32_16x16x32_bf16 v[120:123], a[88:91], v[132:135], v[120:123]
v_and_b32 v107, 0xffff0000, v153
v_lshlrev_b32 v108, 16, v154
v_mfma_f32_16x16x32_bf16 v[124:127], a[92:95], v[132:135], v[124:127]
s_waitcnt lgkmcnt(2)
v_xor_b32 v25, 0x10000, v25
v_and_b32 v109, 0xffff0000, v154
v_mfma_f32_16x16x32_bf16 v[112:115], a[96:99], v[136:139], v[112:115]
v_lshlrev_b32 v110, 16, v155
v_and_b32 v111, 0xffff0000, v155
v_mfma_f32_16x16x32_bf16 v[116:119], a[100:103], v[136:139], v[116:119]
v_mfma_f32_16x16x32_bf16 v[120:123], a[104:107], v[136:139], v[120:123]
s_waitcnt vmcnt(9)
s_barrier
v_mfma_f32_16x16x32_bf16 v[124:127], a[108:111], v[136:139], v[124:127]
s_waitcnt lgkmcnt(1)
ds_read_b128 v[132:135], v25 offset:9216
ds_read_b128 v[184:187], v26 offset:39936
v_mfma_f32_16x16x32_bf16 v[112:115], a[112:115], v[140:143], v[112:115]
ds_read_b128 v[136:139], v25 offset:10240
ds_read_b128 v[188:191], v26 offset:40960
v_mfma_f32_16x16x32_bf16 v[116:119], a[116:119], v[140:143], v[116:119]
ds_read_b128 v[192:195], v26 offset:41984
v_mfma_f32_16x16x32_bf16 v[120:123], a[120:123], v[140:143], v[120:123]
v_mfma_f32_16x16x32_bf16 v[124:127], a[124:127], v[140:143], v[124:127]
s_waitcnt lgkmcnt(5)
v_mfma_f32_16x16x32_bf16 v[112:115], a[128:131], v[144:147], v[112:115]
ds_read_b128 v[140:143], v25 offset:11264
v_mfma_f32_16x16x32_bf16 v[116:119], a[132:135], v[144:147], v[116:119]
v_mfma_f32_16x16x32_bf16 v[120:123], a[136:139], v[144:147], v[120:123]
v_mfma_f32_16x16x32_bf16 v[124:127], a[140:143], v[144:147], v[124:127]
s_sub_u32 s70, s70, 1
s_cbranch_scc0 .Lwo_top19
s_waitcnt vmcnt(6)
s_barrier
s_waitcnt lgkmcnt(4)
v_add_f32 v176, v176, v184
v_add_f32 v177, v177, v185
v_add_f32 v178, v178, v186
v_add_f32 v179, v179, v187
s_waitcnt lgkmcnt(2)
v_add_f32 v176, v176, v188
v_add_f32 v177, v177, v189
v_add_f32 v178, v178, v190
v_add_f32 v179, v179, v191
s_waitcnt lgkmcnt(1)
v_add_f32 v180, v180, v192
v_add_f32 v181, v181, v193
v_add_f32 v182, v182, v194
v_add_f32 v183, v183, v195
v_cvt_pk_bf16_f32 v172, v176, v177
v_cvt_pk_bf16_f32 v173, v178, v179
v_cvt_pk_bf16_f32 v174, v180, v181
v_cvt_pk_bf16_f32 v175, v182, v183
v_cvt_pk_bf16_f32 v164, v112, v113
global_store_dwordx4 v14, v[172:175], s[38:39] sc1
v_cvt_pk_bf16_f32 v165, v114, v115
v_cvt_pk_bf16_f32 v166, v116, v117
v_cvt_pk_bf16_f32 v167, v118, v119
global_store_dwordx4 v13, v[164:167], s[38:39] offset:0 sc1
v_cvt_pk_bf16_f32 v168, v120, v121
v_cvt_pk_bf16_f32 v169, v122, v123
v_cvt_pk_bf16_f32 v170, v124, v125
v_cvt_pk_bf16_f32 v171, v126, v127
global_store_dwordx4 v13, v[168:171], s[38:39] offset:1024 sc1
v_add_f32 v254, v112, v113
v_mul_f32 v255, v112, v112
v_fmac_f32 v255, v113, v113
v_add_f32 v254, v254, v114
v_fmac_f32 v255, v114, v114
v_add_f32 v254, v254, v115
v_fmac_f32 v255, v115, v115
v_add_f32 v254, v254, v116
v_fmac_f32 v255, v116, v116
v_add_f32 v254, v254, v117
v_fmac_f32 v255, v117, v117
v_add_f32 v254, v254, v118
v_fmac_f32 v255, v118, v118
v_add_f32 v254, v254, v119
v_fmac_f32 v255, v119, v119
v_add_f32 v254, v254, v120
v_fmac_f32 v255, v120, v120
v_add_f32 v254, v254, v121
v_fmac_f32 v255, v121, v121
v_add_f32 v254, v254, v122
v_fmac_f32 v255, v122, v122
v_add_f32 v254, v254, v123
v_fmac_f32 v255, v123, v123
v_add_f32 v254, v254, v124
v_fmac_f32 v255, v124, v124
v_add_f32 v254, v254, v125
v_fmac_f32 v255, v125, v125
v_add_f32 v254, v254, v126
v_fmac_f32 v255, v126, v126
v_add_f32 v254, v254, v127
v_fmac_f32 v255, v127, v127
v_add_f32 v254, v254, v176
v_fmac_f32 v255, v176, v176
v_add_f32 v254, v254, v177
v_fmac_f32 v255, v177, v177
v_add_f32 v254, v254, v178
v_fmac_f32 v255, v178, v178
v_add_f32 v254, v254, v179
v_fmac_f32 v255, v179, v179
v_add_f32 v254, v254, v180
v_fmac_f32 v255, v180, v180
v_add_f32 v254, v254, v181
v_fmac_f32 v255, v181, v181
v_add_f32 v254, v254, v182
v_fmac_f32 v255, v182, v182
v_add_f32 v254, v254, v183
v_fmac_f32 v255, v183, v183
ds_write_b64 v27, v[254:255] offset:46592
s_waitcnt vmcnt(0) lgkmcnt(0)
s_barrier
s_barrier
s_endpgm
.Lwo_sp8:
s_sleep 1
v_add_lshl_u32 v253, v22, 0, 7
v_bfi_b32 v253, s69, v253, v21
global_load_dword v200, v253, s[38:39] sc1
s_add_u32 s71, s71, 1
s_waitcnt vmcnt(0)
v_sub_u32 v200, v200, v22
v_cmp_lt_u32 vcc, 0x7f, v200
s_and_b64 vcc, vcc, s[76:77]
s_cbranch_vccz .Lwo_rt9
s_cmp_lt_u32 s71, 3000000
s_cbranch_scc1 .Lwo_sp8
s_branch .Lwo_rt9
.Lwo_sp10:
s_sleep 1
v_add_lshl_u32 v253, v22, 0, 7
v_bfi_b32 v253, s69, v253, v21
global_load_dword v201, v253, s[38:39] sc1
s_add_u32 s71, s71, 1
s_waitcnt vmcnt(0)
v_sub_u32 v201, v201, v22
v_cmp_lt_u32 vcc, 0x7f, v201
s_and_b64 vcc, vcc, s[76:77]
s_cbranch_vccz .Lwo_rt11
s_cmp_lt_u32 s71, 3000000
s_cbranch_scc1 .Lwo_sp10
s_branch .Lwo_rt11
.Le1:
)"
    :: "{v[0:31]}"(va), "{s[36:51]}"(sa) : "v255", "a255", "s99", "vcc", "memory");
  } else if (jj < (ev ? 16 : 12)) {
    const unsigned dn_i = ev ? 48 + xh * 6 + jj - 10 : xh * 12 + jj, pf_g = (xh & 1) * 18 + (ev ? 10 : 0), pf_n = ev ? 24 : 48, pf_w = (ev ? jj - 10 : jj) * 4 + wave, pf_m = ev ? 8 : 10;
    const unsigned r = lane & 15, g = lane >> 4, q = r >> 2, p = lane & 3;
    const unsigned tri = dn_i / 3, nu = dn_i % 3, kap = tri >> 2, rho = tri & 3, row0 = rho * RP + PAD;
    auto pf = [&](unsigned k) { const unsigned u = k * pf_n + pf_w, b = u / pf_m, pg = pf_g + u - b * pf_m; return b < 16 ? (64 + b * 32 + (lane >> 1)) * 9216 + pg / 6 * 1536 + (pg % 6) * 128 + (lane & 1) * 768 : 0u; };
    auto wv = [&](unsigned j) { const unsigned b = j * 1024 + lane * 16, rw = b / 384; return rw * (1152 - 384) + b + 4096 - j * 1024; };
    u32x32 va; u32x16 sa;
    va[0] = wv(0);
    va[1] = wv(1);
    va[2] = wv(2);
    va[3] = wv(3);
    va[4] = wv(4);
    va[5] = wv(5);
    va[6] = (8 * g + q) * 384 + wave * 64 + p * 16 + (kap & 1) * 8;
    va[7] = (8 * g + q) * 384 + wave * 64 + p * 16 + 8 - (kap & 1) * 8;
    va[8] = (8 * g + q) * 384 + 256 + wave * 32 + (p ^ (kap & 1)) * 8;
    va[9] = WS_H + row0 * HT + (kap * 24 + wave * 6 + 2) * 1024 + lane * 16;
    va[10] = WS_H + (row0 + 63) * HT + (kap * 24 + wave * 6 + 2) * 1024 + lane * 16;
    va[11] = WS_P + (row0 - 1) * PT + (((nu * 3 + (wave >> 1)) * 4 + q) * 6 + kap) * 512 + (wave & 1) * 256 + g * 64 + p * 16;
    va[12] = WS_P + (row0 - 1) * PT + (((nu * 3 + 2) * 4 + q) * 6 + kap) * 512 + wave * 128 + (g >> 1) * 64 + p * 16;
    va[13] = WS_FL + rho * FR + FH + (kap * 6 + min(lane, 5u)) * FL;
    va[14] = 0u;
    va[15] = WS_FL + rho * FR + FP + (nu * 6 + kap) * FL;
    va[16] = 0u - 2;
    va[17] = lane * 16;
    va[18] = pf(0);
    va[19] = pf(1);
    va[20] = pf(2);
    va[21] = pf(3);
    va[22] = pf(4);
    va[23] = pf(5);
    sa[0] = 8 + wave; sa[2] = (unsigned)(unsigned long long)ws; sa[3] = (unsigned)((unsigned long long)ws >> 32);
    sa[4] = uni((unsigned)(unsigned long long)(wu));
    sa[5] = uni((unsigned)((unsigned long long)(wu) >> 32));
    sa[6] = uni((unsigned)(unsigned long long)(wd + (kap * 768 + wave * 16) * 1152 + nu * 384));
    sa[7] = uni((unsigned)((unsigned long long)(wd + (kap * 768 + wave * 16) * 1152 + nu * 384) >> 32));
    sa[8] = uni(wave * 6144);
    sa[9] = uni(wave * 6144 + 2048);
    asm volatile(
R"(.Lb2:
s_movk_i32 s69, 0x380
s_mov_b32 s71, 0
s_mov_b32 s74, 0
s_load_dword s37, s[38:39], 0x0
s_nop 1
s_sleep 70
s_mov_b32 m0, 0x27f00
s_nop 0
global_load_lds_dword v18, s[40:41]
global_load_lds_dword v19, s[40:41]
global_load_lds_dword v20, s[40:41]
global_load_lds_dword v21, s[40:41]
global_load_lds_dword v22, s[40:41]
global_load_lds_dword v23, s[40:41]
s_waitcnt vmcnt(0)
s_add_u32 m0, s44, 4096
s_nop 0
global_load_lds_dwordx4 v0, s[42:43] offset:-4096 nt
global_load_lds_dwordx4 v1, s[42:43] offset:-3072 nt
global_load_lds_dwordx4 v2, s[42:43] offset:-2048 nt
global_load_lds_dwordx4 v3, s[42:43] offset:-1024 nt
global_load_lds_dwordx4 v4, s[42:43] offset:0 nt
global_load_lds_dwordx4 v5, s[42:43] offset:1024 nt
s_add_u32 s42, s42, 73728
s_addc_u32 s43, s43, 0
s_add_u32 m0, s44, 28672
s_nop 0
global_load_lds_dwordx4 v0, s[42:43] offset:-4096 nt
global_load_lds_dwordx4 v1, s[42:43] offset:-3072 nt
global_load_lds_dwordx4 v2, s[42:43] offset:-2048 nt
global_load_lds_dwordx4 v3, s[42:43] offset:-1024 nt
global_load_lds_dwordx4 v4, s[42:43] offset:0 nt
global_load_lds_dwordx4 v5, s[42:43] offset:1024 nt
s_add_u32 s42, s42, 73728
s_addc_u32 s43, s43, 0
s_add_u32 m0, s44, 53248
s_nop 0
global_load_lds_dwordx4 v0, s[42:43] offset:-4096 nt
global_load_lds_dwordx4 v1, s[42:43] offset:-3072 nt
global_load_lds_dwordx4 v2, s[42:43] offset:-2048 nt
global_load_lds_dwordx4 v3, s[42:43] offset:-1024 nt
global_load_lds_dwordx4 v4, s[42:43] offset:0 nt
global_load_lds_dwordx4 v5, s[42:43] offset:1024 nt
s_add_u32 s42, s42, 73728
s_addc_u32 s43, s43, 0
s_add_u32 m0, s44, 77824
s_nop 0
global_load_lds_dwordx4 v0, s[42:43] offset:-4096 nt
global_load_lds_dwordx4 v1, s[42:43] offset:-3072 nt
global_load_lds_dwordx4 v2, s[42:43] offset:-2048 nt
global_load_lds_dwordx4 v3, s[42:43] offset:-1024 nt
global_load_lds_dwordx4 v4, s[42:43] offset:0 nt
global_load_lds_dwordx4 v5, s[42:43] offset:1024 nt
s_add_u32 s42, s42, 73728
s_addc_u32 s43, s43, 0
s_add_u32 m0, s44, 102400
s_nop 0
global_load_lds_dwordx4 v0, s[42:43] offset:-4096 nt
global_load_lds_dwordx4 v1, s[42:43] offset:-3072 nt
global_load_lds_dwordx4 v2, s[42:43] offset:-2048 nt
global_load_lds_dwordx4 v3, s[42:43] offset:-1024 nt
global_load_lds_dwordx4 v4, s[42:43] offset:0 nt
global_load_lds_dwordx4 v5, s[42:43] offset:1024 nt
s_add_u32 s42, s42, 73728
s_addc_u32 s43, s43, 0
v_add_u32 v136, 0x10000, v6
v_add_u32 v137, 0x20000, v6
v_add_u32 v138, 0x10000, v7
v_add_u32 v139, 0x20000, v7
v_add_u32 v140, 0x10000, v8
v_add_u32 v141, 0x20000, v8
s_waitcnt lgkmcnt(0)
s_mul_i32 s37, s37, 0x9E3779B1
s_add_u32 s37, s37, 0x7F4A7C15
v_add_u32 v14, s37, v14
v_add_u32 v16, s37, v16
s_waitcnt vmcnt(24)
s_barrier
s_add_u32 m0, s44, 126976
ds_read_b64_tr_b16 a[0:1], v6 offset:0
ds_read_b64_tr_b16 a[2:3], v6 offset:1536
global_load_lds_dwordx4 v0, s[42:43] offset:-4096 nt
ds_read_b64_tr_b16 a[4:5], v7 offset:0
global_load_lds_dwordx4 v1, s[42:43] offset:-3072 nt
ds_read_b64_tr_b16 a[6:7], v7 offset:1536
global_load_lds_dwordx4 v2, s[42:43] offset:-2048 nt
ds_read_b64_tr_b16 a[8:9], v8 offset:0
ds_read_b64_tr_b16 a[10:11], v8 offset:1536
global_load_lds_dwordx4 v3, s[42:43] offset:-1024 nt
ds_read_b64_tr_b16 a[12:13], v6 offset:12288
global_load_lds_dwordx4 v4, s[42:43] offset:0 nt
ds_read_b64_tr_b16 a[14:15], v6 offset:13824
global_load_lds_dwordx4 v5, s[42:43] offset:1024 nt
ds_read_b64_tr_b16 a[16:17], v7 offset:12288
ds_read_b64_tr_b16 a[18:19], v7 offset:13824
s_add_u32 s42, s42, 73728
ds_read_b64_tr_b16 a[20:21], v8 offset:12288
s_addc_u32 s43, s43, 0
ds_read_b64_tr_b16 a[22:23], v8 offset:13824
s_waitcnt vmcnt(24) lgkmcnt(0)
s_barrier
s_add_u32 m0, s44, 4096
ds_read_b64_tr_b16 a[24:25], v6 offset:24576
ds_read_b64_tr_b16 a[26:27], v6 offset:26112
global_load_lds_dwordx4 v0, s[42:43] offset:-4096 nt
ds_read_b64_tr_b16 a[28:29], v7 offset:24576
global_load_lds_dwordx4 v1, s[42:43] offset:-3072 nt
ds_read_b64_tr_b16 a[30:31], v7 offset:26112
global_load_lds_dwordx4 v2, s[42:43] offset:-2048 nt
ds_read_b64_tr_b16 a[32:33], v8 offset:24576
ds_read_b64_tr_b16 a[34:35], v8 offset:26112
global_load_lds_dwordx4 v3, s[42:43] offset:-1024 nt
ds_read_b64_tr_b16 a[36:37], v6 offset:36864
global_load_lds_dwordx4 v4, s[42:43] offset:0 nt
ds_read_b64_tr_b16 a[38:39], v6 offset:38400
global_load_lds_dwordx4 v5, s[42:43] offset:1024 nt
ds_read_b64_tr_b16 a[40:41], v7 offset:36864
ds_read_b64_tr_b16 a[42:43], v7 offset:38400
s_add_u32 s42, s42, 73728
ds_read_b64_tr_b16 a[44:45], v8 offset:36864
s_addc_u32 s43, s43, 0
ds_read_b64_tr_b16 a[46:47], v8 offset:38400
s_waitcnt vmcnt(24) lgkmcnt(0)
s_barrier
s_add_u32 m0, s44, 28672
ds_read_b64_tr_b16 a[48:49], v6 offset:49152
ds_read_b64_tr_b16 a[50:51], v6 offset:50688
global_load_lds_dwordx4 v0, s[42:43] offset:-4096 nt
ds_read_b64_tr_b16 a[52:53], v7 offset:49152
global_load_lds_dwordx4 v1, s[42:43] offset:-3072 nt
ds_read_b64_tr_b16 a[54:55], v7 offset:50688
global_load_lds_dwordx4 v2, s[42:43] offset:-2048 nt
ds_read_b64_tr_b16 a[56:57], v8 offset:49152
ds_read_b64_tr_b16 a[58:59], v8 offset:50688
global_load_lds_dwordx4 v3, s[42:43] offset:-1024 nt
ds_read_b64_tr_b16 a[60:61], v6 offset:61440
global_load_lds_dwordx4 v4, s[42:43] offset:0 nt
ds_read_b64_tr_b16 a[62:63], v6 offset:62976
global_load_lds_dwordx4 v5, s[42:43] offset:1024 nt
ds_read_b64_tr_b16 a[64:65], v7 offset:61440
ds_read_b64_tr_b16 a[66:67], v7 offset:62976
s_add_u32 s42, s42, 73728
ds_read_b64_tr_b16 a[68:69], v8 offset:61440
s_addc_u32 s43, s43, 0
ds_read_b64_tr_b16 a[70:71], v8 offset:62976
s_waitcnt vmcnt(24) lgkmcnt(0)
s_barrier
s_add_u32 m0, s44, 53248
ds_read_b64_tr_b16 a[72:73], v136 offset:8192
ds_read_b64_tr_b16 a[74:75], v136 offset:9728
global_load_lds_dwordx4 v0, s[42:43] offset:-4096 nt
ds_read_b64_tr_b16 a[76:77], v138 offset:8192
global_load_lds_dwordx4 v1, s[42:43] offset:-3072 nt
ds_read_b64_tr_b16 a[78:79], v138 offset:9728
global_load_lds_dwordx4 v2, s[42:43] offset:-2048 nt
ds_read_b64_tr_b16 a[80:81], v140 offset:8192
ds_read_b64_tr_b16 a[82:83], v140 offset:9728
global_load_lds_dwordx4 v3, s[42:43] offset:-1024 nt
ds_read_b64_tr_b16 a[84:85], v136 offset:20480
global_load_lds_dwordx4 v4, s[42:43] offset:0 nt
ds_read_b64_tr_b16 a[86:87], v136 offset:22016
global_load_lds_dwordx4 v5, s[42:43] offset:1024 nt
ds_read_b64_tr_b16 a[88:89], v138 offset:20480
ds_read_b64_tr_b16 a[90:91], v138 offset:22016
s_add_u32 s42, s42, 73728
ds_read_b64_tr_b16 a[92:93], v140 offset:20480
s_addc_u32 s43, s43, 0
ds_read_b64_tr_b16 a[94:95], v140 offset:22016
s_waitcnt vmcnt(24) lgkmcnt(0)
s_barrier
s_add_u32 m0, s44, 77824
ds_read_b64_tr_b16 a[96:97], v136 offset:32768
ds_read_b64_tr_b16 a[98:99], v136 offset:34304
global_load_lds_dwordx4 v0, s[42:43] offset:-4096 nt
ds_read_b64_tr_b16 a[100:101], v138 offset:32768
global_load_lds_dwordx4 v1, s[42:43] offset:-3072 nt
ds_read_b64_tr_b16 a[102:103], v138 offset:34304
global_load_lds_dwordx4 v2, s[42:43] offset:-2048 nt
ds_read_b64_tr_b16 a[104:105], v140 offset:32768
ds_read_b64_tr_b16 a[106:107], v140 offset:34304
global_load_lds_dwordx4 v3, s[42:43] offset:-1024 nt
ds_read_b64_tr_b16 a[108:109], v136 offset:45056
global_load_lds_dwordx4 v4, s[42:43] offset:0 nt
ds_read_b64_tr_b16 a[110:111], v136 offset:46592
global_load_lds_dwordx4 v5, s[42:43] offset:1024 nt
ds_read_b64_tr_b16 a[112:113], v138 offset:45056
ds_read_b64_tr_b16 a[114:115], v138 offset:46592
s_add_u32 s42, s42, 73728
ds_read_b64_tr_b16 a[116:117], v140 offset:45056
s_addc_u32 s43, s43, 0
ds_read_b64_tr_b16 a[118:119], v140 offset:46592
s_waitcnt vmcnt(24) lgkmcnt(0)
s_barrier
s_add_u32 m0, s44, 102400
ds_read_b64_tr_b16 a[120:121], v136 offset:57344
ds_read_b64_tr_b16 a[122:123], v136 offset:58880
global_load_lds_dwordx4 v0, s[42:43] offset:-4096 nt
ds_read_b64_tr_b16 a[124:125], v138 offset:57344
global_load_lds_dwordx4 v1, s[42:43] offset:-3072 nt
ds_read_b64_tr_b16 a[126:127], v138 offset:58880
global_load_lds_dwordx4 v2, s[42:43] offset:-2048 nt
ds_read_b64_tr_b16 a[128:129], v140 offset:57344
ds_read_b64_tr_b16 a[130:131], v140 offset:58880
global_load_lds_dwordx4 v3, s[42:43] offset:-1024 nt
ds_read_b64_tr_b16 a[132:133], v137 offset:4096
global_load_lds_dwordx4 v4, s[42:43] offset:0 nt
ds_read_b64_tr_b16 a[134:135], v137 offset:5632
global_load_lds_dwordx4 v5, s[42:43] offset:1024 nt
ds_read_b64_tr_b16 a[136:137], v139 offset:4096
ds_read_b64_tr_b16 a[138:139], v139 offset:5632
s_add_u32 s42, s42, 73728
ds_read_b64_tr_b16 a[140:141], v141 offset:4096
s_addc_u32 s43, s43, 0
ds_read_b64_tr_b16 a[142:143], v141 offset:5632
s_waitcnt vmcnt(24) lgkmcnt(0)
s_barrier
s_add_u32 m0, s44, 126976
ds_read_b64_tr_b16 a[144:145], v6 offset:0
ds_read_b64_tr_b16 a[146:147], v6 offset:1536
global_load_lds_dwordx4 v0, s[42:43] offset:-4096 nt
ds_read_b64_tr_b16 a[148:149], v7 offset:0
global_load_lds_dwordx4 v1, s[42:43] offset:-3072 nt
ds_read_b64_tr_b16 a[150:151], v7 offset:1536
global_load_lds_dwordx4 v2, s[42:43] offset:-2048 nt
ds_read_b64_tr_b16 a[152:153], v8 offset:0
ds_read_b64_tr_b16 a[154:155], v8 offset:1536
global_load_lds_dwordx4 v3, s[42:43] offset:-1024 nt
ds_read_b64_tr_b16 a[156:157], v6 offset:12288
global_load_lds_dwordx4 v4, s[42:43] offset:0 nt
ds_read_b64_tr_b16 a[158:159], v6 offset:13824
global_load_lds_dwordx4 v5, s[42:43] offset:1024 nt
ds_read_b64_tr_b16 a[160:161], v7 offset:12288
ds_read_b64_tr_b16 a[162:163], v7 offset:13824
s_sub_u32 s42, s42, 811008
ds_read_b64_tr_b16 a[164:165], v8 offset:12288
s_subb_u32 s43, s43, 0
ds_read_b64_tr_b16 a[166:167], v8 offset:13824
s_waitcnt vmcnt(24) lgkmcnt(0)
s_barrier
ds_read_b64_tr_b16 a[168:169], v6 offset:24576
ds_read_b64_tr_b16 a[170:171], v6 offset:26112
ds_read_b64_tr_b16 a[172:173], v7 offset:24576
ds_read_b64_tr_b16 a[174:175], v7 offset:26112
ds_read_b64_tr_b16 a[176:177], v8 offset:24576
ds_read_b64_tr_b16 a[178:179], v8 offset:26112
ds_read_b64_tr_b16 a[180:181], v6 offset:36864
ds_read_b64_tr_b16 a[182:183], v6 offset:38400
ds_read_b64_tr_b16 a[184:185], v7 offset:36864
ds_read_b64_tr_b16 a[186:187], v7 offset:38400
ds_read_b64_tr_b16 a[188:189], v8 offset:36864
ds_read_b64_tr_b16 a[190:191], v8 offset:38400
s_waitcnt vmcnt(18) lgkmcnt(0)
s_barrier
ds_read_b64_tr_b16 a[192:193], v6 offset:49152
ds_read_b64_tr_b16 a[194:195], v6 offset:50688
ds_read_b64_tr_b16 a[196:197], v7 offset:49152
ds_read_b64_tr_b16 a[198:199], v7 offset:50688
ds_read_b64_tr_b16 a[200:201], v8 offset:49152
ds_read_b64_tr_b16 a[202:203], v8 offset:50688
ds_read_b64_tr_b16 a[204:205], v6 offset:61440
ds_read_b64_tr_b16 a[206:207], v6 offset:62976
ds_read_b64_tr_b16 a[208:209], v7 offset:61440
ds_read_b64_tr_b16 a[210:211], v7 offset:62976
ds_read_b64_tr_b16 a[212:213], v8 offset:61440
ds_read_b64_tr_b16 a[214:215], v8 offset:62976
s_waitcnt vmcnt(12) lgkmcnt(0)
s_barrier
ds_read_b64_tr_b16 a[216:217], v136 offset:8192
ds_read_b64_tr_b16 a[218:219], v136 offset:9728
ds_read_b64_tr_b16 a[220:221], v138 offset:8192
ds_read_b64_tr_b16 a[222:223], v138 offset:9728
ds_read_b64_tr_b16 a[224:225], v140 offset:8192
ds_read_b64_tr_b16 a[226:227], v140 offset:9728
ds_read_b64_tr_b16 a[228:229], v136 offset:20480
ds_read_b64_tr_b16 a[230:231], v136 offset:22016
ds_read_b64_tr_b16 a[232:233], v138 offset:20480
ds_read_b64_tr_b16 a[234:235], v138 offset:22016
ds_read_b64_tr_b16 a[236:237], v140 offset:20480
ds_read_b64_tr_b16 a[238:239], v140 offset:22016
s_waitcnt vmcnt(6) lgkmcnt(0)
s_barrier
ds_read_b64_tr_b16 a[240:241], v136 offset:32768
ds_read_b64_tr_b16 a[242:243], v136 offset:34304
ds_read_b64_tr_b16 a[244:245], v138 offset:32768
ds_read_b64_tr_b16 a[246:247], v138 offset:34304
ds_read_b64_tr_b16 a[248:249], v140 offset:32768
ds_read_b64_tr_b16 a[250:251], v140 offset:34304
ds_read_b64_tr_b16 a[252:253], v136 offset:45056
ds_read_b64_tr_b16 a[254:255], v136 offset:46592
ds_read_b64_tr_b16 v[24:25], v138 offset:45056
ds_read_b64_tr_b16 v[26:27], v138 offset:46592
ds_read_b64_tr_b16 v[28:29], v140 offset:45056
ds_read_b64_tr_b16 v[30:31], v140 offset:46592
s_waitcnt vmcnt(0) lgkmcnt(0)
s_barrier
ds_read_b64_tr_b16 v[32:33], v136 offset:57344
ds_read_b64_tr_b16 v[34:35], v136 offset:58880
ds_read_b64_tr_b16 v[36:37], v138 offset:57344
ds_read_b64_tr_b16 v[38:39], v138 offset:58880
ds_read_b64_tr_b16 v[40:41], v140 offset:57344
ds_read_b64_tr_b16 v[42:43], v140 offset:58880
ds_read_b64_tr_b16 v[44:45], v137 offset:4096
ds_read_b64_tr_b16 v[46:47], v137 offset:5632
ds_read_b64_tr_b16 v[48:49], v139 offset:4096
ds_read_b64_tr_b16 v[50:51], v139 offset:5632
ds_read_b64_tr_b16 v[52:53], v141 offset:4096
ds_read_b64_tr_b16 v[54:55], v141 offset:5632
s_waitcnt lgkmcnt(0)
s_barrier
s_and_b32 s75, s36, 3
s_cmp_eq_u32 s75, 1
s_cbranch_scc1 .Ldn_w2
s_cmp_eq_u32 s75, 2
s_cbranch_scc1 .Ldn_w3
s_cmp_eq_u32 s75, 3
s_cbranch_scc1 .Ldn_w4
.Ldn_w1:
v_add_u32 v254, -1, v14
v_add_u32 v255, -1, v14
v_add_lshl_u32 v252, v16, 0, 7
v_bfi_b32 v252, s69, v252, v15
v_add_lshl_u32 v253, v14, 0, 7
v_bfi_b32 v253, s69, v253, v13
s_mov_b32 s76, 0xffff
s_mov_b32 s77, 0xffff
s_add_u32 m0, s45, 24576
s_xor_b32 s45, s45, 0x10000
s_mov_b32 m0, s45
s_barrier
global_load_lds_dwordx4 v9, s[38:39] offset:-2048 sc1
global_load_lds_dwordx4 v9, s[38:39] offset:-1024 sc1
global_load_lds_dwordx4 v9, s[38:39] offset:0 sc1
global_load_lds_dwordx4 v9, s[38:39] offset:1024 sc1
global_load_lds_dwordx4 v9, s[38:39] offset:2048 sc1
global_load_lds_dwordx4 v9, s[38:39] offset:3072 sc1
v_add_u32 v9, 147456, v9
s_mov_b32 s70, 32
s_waitcnt vmcnt(0) lgkmcnt(0)
s_nop 7
s_branch .Ldn_in5
.p2align 6
.Ldn_top6:
s_waitcnt lgkmcnt(5)
v_mfma_f32_16x16x32_bf16 v[56:59], a[0:3], v[80:83], 0
s_waitcnt lgkmcnt(4)
v_mfma_f32_16x16x32_bf16 v[56:59], a[12:15], v[84:87], v[56:59]
s_waitcnt lgkmcnt(3)
v_mfma_f32_16x16x32_bf16 v[56:59], a[24:27], v[88:91], v[56:59]
s_waitcnt lgkmcnt(2)
v_mfma_f32_16x16x32_bf16 v[56:59], a[36:39], v[92:95], v[56:59]
s_waitcnt lgkmcnt(1)
ds_read_b128 v[104:107], v17 offset:6144
v_cvt_pk_bf16_f32 v132, v76, v77
v_mfma_f32_16x16x32_bf16 v[56:59], a[48:51], v[96:99], v[56:59]
s_waitcnt lgkmcnt(1)
v_mfma_f32_16x16x32_bf16 v[56:59], a[60:63], v[100:103], v[56:59]
s_waitcnt vmcnt(6)
s_barrier
v_mfma_f32_16x16x32_bf16 v[60:63], a[4:7], v[80:83], 0
ds_read_b128 v[108:111], v17 offset:7168
v_cvt_pk_bf16_f32 v134, v76, v77
v_mfma_f32_16x16x32_bf16 v[60:63], a[16:19], v[84:87], v[60:63]
global_store_dwordx4 v11, v[128:131], s[38:39] sc1
v_mfma_f32_16x16x32_bf16 v[60:63], a[28:31], v[88:91], v[60:63]
ds_read_b128 v[112:115], v17 offset:8192
v_add_u32 v11, 110592, v11
v_mfma_f32_16x16x32_bf16 v[60:63], a[40:43], v[92:95], v[60:63]
v_cvt_pk_bf16_f32 v133, v78, v79
v_cvt_pk_bf16_f32 v135, v78, v79
v_mfma_f32_16x16x32_bf16 v[60:63], a[52:55], v[96:99], v[60:63]
ds_read_b128 v[116:119], v17 offset:9216
v_mfma_f32_16x16x32_bf16 v[60:63], a[64:67], v[100:103], v[60:63]
v_permlane16_swap_b32 v132, v134
v_mfma_f32_16x16x32_bf16 v[64:67], a[8:11], v[80:83], 0
ds_read_b128 v[120:123], v17 offset:10240
v_mfma_f32_16x16x32_bf16 v[64:67], a[20:23], v[84:87], v[64:67]
v_permlane16_swap_b32 v133, v135
v_mfma_f32_16x16x32_bf16 v[64:67], a[32:35], v[88:91], v[64:67]
ds_read_b128 v[124:127], v17 offset:11264
v_mfma_f32_16x16x32_bf16 v[64:67], a[44:47], v[92:95], v[64:67]
s_mov_b64 exec, s[76:77]
global_store_dwordx4 v12, v[132:135], s[38:39] sc1
s_mov_b64 exec, -1
v_mfma_f32_16x16x32_bf16 v[64:67], a[56:59], v[96:99], v[64:67]
v_add_u32 v12, 110592, v12
v_mfma_f32_16x16x32_bf16 v[64:67], a[68:71], v[100:103], v[64:67]
s_waitcnt lgkmcnt(5)
v_mfma_f32_16x16x32_bf16 v[56:59], a[72:75], v[104:107], v[56:59]
s_waitcnt lgkmcnt(4)
v_mfma_f32_16x16x32_bf16 v[56:59], a[84:87], v[108:111], v[56:59]
s_waitcnt lgkmcnt(3)
v_mfma_f32_16x16x32_bf16 v[56:59], a[96:99], v[112:115], v[56:59]
s_waitcnt lgkmcnt(2)
v_mfma_f32_16x16x32_bf16 v[56:59], a[108:111], v[116:119], v[56:59]
s_waitcnt lgkmcnt(1)
v_mfma_f32_16x16x32_bf16 v[56:59], a[120:123], v[120:123], v[56:59]
s_waitcnt lgkmcnt(0)
ds_read_b128 v[80:83], v17 offset:12288
v_mfma_f32_16x16x32_bf16 v[56:59], a[132:135], v[124:127], v[56:59]
)"
R"(v_mfma_f32_16x16x32_bf16 v[60:63], a[76:79], v[104:107], v[60:63]
ds_read_b128 v[84:87], v17 offset:13312
v_mfma_f32_16x16x32_bf16 v[60:63], a[88:91], v[108:111], v[60:63]
v_mfma_f32_16x16x32_bf16 v[60:63], a[100:103], v[112:115], v[60:63]
ds_read_b128 v[88:91], v17 offset:14336
v_mfma_f32_16x16x32_bf16 v[60:63], a[112:115], v[116:119], v[60:63]
v_mfma_f32_16x16x32_bf16 v[60:63], a[124:127], v[120:123], v[60:63]
ds_read_b128 v[92:95], v17 offset:15360
v_mfma_f32_16x16x32_bf16 v[60:63], a[136:139], v[124:127], v[60:63]
v_mfma_f32_16x16x32_bf16 v[64:67], a[80:83], v[104:107], v[64:67]
ds_read_b128 v[96:99], v17 offset:16384
v_mfma_f32_16x16x32_bf16 v[64:67], a[92:95], v[108:111], v[64:67]
v_mfma_f32_16x16x32_bf16 v[64:67], a[104:107], v[112:115], v[64:67]
ds_read_b128 v[100:103], v17 offset:17408
v_mfma_f32_16x16x32_bf16 v[64:67], a[116:119], v[116:119], v[64:67]
v_mfma_f32_16x16x32_bf16 v[64:67], a[128:131], v[120:123], v[64:67]
v_mfma_f32_16x16x32_bf16 v[64:67], a[140:143], v[124:127], v[64:67]
s_waitcnt lgkmcnt(5)
v_mfma_f32_16x16x32_bf16 v[56:59], a[144:147], v[80:83], v[56:59]
s_waitcnt lgkmcnt(4)
v_mfma_f32_16x16x32_bf16 v[56:59], a[156:159], v[84:87], v[56:59]
s_waitcnt lgkmcnt(3)
v_mfma_f32_16x16x32_bf16 v[56:59], a[168:171], v[88:91], v[56:59]
s_waitcnt lgkmcnt(2)
v_mfma_f32_16x16x32_bf16 v[56:59], a[180:183], v[92:95], v[56:59]
s_waitcnt lgkmcnt(1)
v_mfma_f32_16x16x32_bf16 v[56:59], a[192:195], v[96:99], v[56:59]
s_xor_b32 s45, s45, 0x10000
ds_read_b128 v[104:107], v17 offset:18432
s_waitcnt lgkmcnt(1)
v_mfma_f32_16x16x32_bf16 v[56:59], a[204:207], v[100:103], v[56:59]
s_add_u32 m0, s45, 0
v_mfma_f32_16x16x32_bf16 v[60:63], a[148:151], v[80:83], v[60:63]
ds_read_b128 v[108:111], v17 offset:19456
ds_read_b128 v[112:115], v17 offset:20480
v_mfma_f32_16x16x32_bf16 v[60:63], a[160:163], v[84:87], v[60:63]
v_mfma_f32_16x16x32_bf16 v[60:63], a[172:175], v[88:91], v[60:63]
s_barrier
v_mfma_f32_16x16x32_bf16 v[60:63], a[184:187], v[92:95], v[60:63]
global_load_lds_dwordx4 v9, s[38:39] offset:-2048 sc1
v_mfma_f32_16x16x32_bf16 v[60:63], a[196:199], v[96:99], v[60:63]
ds_read_b128 v[116:119], v17 offset:21504
v_mfma_f32_16x16x32_bf16 v[60:63], a[208:211], v[100:103], v[60:63]
v_mfma_f32_16x16x32_bf16 v[64:67], a[152:155], v[80:83], v[64:67]
ds_read_b128 v[120:123], v17 offset:22528
global_load_lds_dwordx4 v9, s[38:39] offset:-1024 sc1
v_mfma_f32_16x16x32_bf16 v[64:67], a[164:167], v[84:87], v[64:67]
v_mfma_f32_16x16x32_bf16 v[64:67], a[176:179], v[88:91], v[64:67]
ds_read_b128 v[124:127], v17 offset:23552
v_mfma_f32_16x16x32_bf16 v[64:67], a[188:191], v[92:95], v[64:67]
global_load_lds_dwordx4 v9, s[38:39] offset:0 sc1
v_mfma_f32_16x16x32_bf16 v[64:67], a[200:203], v[96:99], v[64:67]
v_mfma_f32_16x16x32_bf16 v[64:67], a[212:215], v[100:103], v[64:67]
s_waitcnt lgkmcnt(5)
v_mfma_f32_16x16x32_bf16 v[56:59], a[216:219], v[104:107], v[56:59]
s_waitcnt lgkmcnt(4)
global_load_lds_dwordx4 v9, s[38:39] offset:1024 sc1
v_mfma_f32_16x16x32_bf16 v[56:59], a[228:231], v[108:111], v[56:59]
s_waitcnt lgkmcnt(3)
v_mfma_f32_16x16x32_bf16 v[56:59], a[240:243], v[112:115], v[56:59]
s_waitcnt lgkmcnt(2)
v_mfma_f32_16x16x32_bf16 v[56:59], a[252:255], v[116:119], v[56:59]
s_waitcnt lgkmcnt(1)
global_load_lds_dwordx4 v9, s[38:39] offset:2048 sc1
v_mfma_f32_16x16x32_bf16 v[56:59], v[32:35], v[120:123], v[56:59]
s_waitcnt lgkmcnt(0)
v_mfma_f32_16x16x32_bf16 v[56:59], v[44:47], v[124:127], v[56:59]
v_mfma_f32_16x16x32_bf16 v[60:63], a[220:223], v[104:107], v[60:63]
global_load_lds_dwordx4 v9, s[38:39] offset:3072 sc1
v_add_u32 v9, 147456, v9
v_mfma_f32_16x16x32_bf16 v[60:63], a[232:235], v[108:111], v[60:63]
v_mfma_f32_16x16x32_bf16 v[60:63], a[244:247], v[112:115], v[60:63]
s_waitcnt vmcnt(8)
s_barrier
v_mfma_f32_16x16x32_bf16 v[60:63], v[24:27], v[116:119], v[60:63]
ds_read_b128 v[80:83], v17 offset:24576
v_min_u32 v9, v9, v10
v_mfma_f32_16x16x32_bf16 v[60:63], v[36:39], v[120:123], v[60:63]
ds_read_b128 v[84:87], v17 offset:25600
v_cvt_pk_bf16_f32 v128, v56, v57
v_mfma_f32_16x16x32_bf16 v[60:63], v[48:51], v[124:127], v[60:63]
ds_read_b128 v[88:91], v17 offset:26624
v_cvt_pk_bf16_f32 v129, v58, v59
v_mfma_f32_16x16x32_bf16 v[64:67], a[224:227], v[104:107], v[64:67]
ds_read_b128 v[92:95], v17 offset:27648
v_mfma_f32_16x16x32_bf16 v[64:67], a[236:239], v[108:111], v[64:67]
ds_read_b128 v[96:99], v17 offset:28672
v_mfma_f32_16x16x32_bf16 v[64:67], a[248:251], v[112:115], v[64:67]
ds_read_b128 v[100:103], v17 offset:29696
v_mfma_f32_16x16x32_bf16 v[64:67], v[28:31], v[116:119], v[64:67]
v_cvt_pk_bf16_f32 v130, v60, v61
v_cvt_pk_bf16_f32 v131, v62, v63
v_mfma_f32_16x16x32_bf16 v[64:67], v[40:43], v[120:123], v[64:67]
v_mfma_f32_16x16x32_bf16 v[64:67], v[52:55], v[124:127], v[64:67]
s_waitcnt lgkmcnt(5)
v_mfma_f32_16x16x32_bf16 v[68:71], a[0:3], v[80:83], 0
s_waitcnt lgkmcnt(4)
v_mfma_f32_16x16x32_bf16 v[68:71], a[12:15], v[84:87], v[68:71]
s_waitcnt lgkmcnt(3)
v_mfma_f32_16x16x32_bf16 v[68:71], a[24:27], v[88:91], v[68:71]
s_waitcnt lgkmcnt(2)
v_mfma_f32_16x16x32_bf16 v[68:71], a[36:39], v[92:95], v[68:71]
s_waitcnt lgkmcnt(1)
ds_read_b128 v[104:107], v17 offset:30720
v_cvt_pk_bf16_f32 v132, v64, v65
v_mfma_f32_16x16x32_bf16 v[68:71], a[48:51], v[96:99], v[68:71]
s_waitcnt lgkmcnt(1)
v_mfma_f32_16x16x32_bf16 v[68:71], a[60:63], v[100:103], v[68:71]
s_waitcnt vmcnt(6)
s_barrier
v_mfma_f32_16x16x32_bf16 v[72:75], a[4:7], v[80:83], 0
ds_read_b128 v[108:111], v17 offset:31744
v_cvt_pk_bf16_f32 v134, v64, v65
v_mfma_f32_16x16x32_bf16 v[72:75], a[16:19], v[84:87], v[72:75]
global_store_dwordx4 v11, v[128:131], s[38:39] sc1
v_mfma_f32_16x16x32_bf16 v[72:75], a[28:31], v[88:91], v[72:75]
ds_read_b128 v[112:115], v17 offset:32768
v_add_u32 v11, 110592, v11
v_mfma_f32_16x16x32_bf16 v[72:75], a[40:43], v[92:95], v[72:75]
v_cvt_pk_bf16_f32 v133, v66, v67
v_cvt_pk_bf16_f32 v135, v66, v67
v_mfma_f32_16x16x32_bf16 v[72:75], a[52:55], v[96:99], v[72:75]
ds_read_b128 v[116:119], v17 offset:33792
v_mfma_f32_16x16x32_bf16 v[72:75], a[64:67], v[100:103], v[72:75]
v_permlane16_swap_b32 v132, v134
v_mfma_f32_16x16x32_bf16 v[76:79], a[8:11], v[80:83], 0
ds_read_b128 v[120:123], v17 offset:34816
v_mfma_f32_16x16x32_bf16 v[76:79], a[20:23], v[84:87], v[76:79]
v_permlane16_swap_b32 v133, v135
v_mfma_f32_16x16x32_bf16 v[76:79], a[32:35], v[88:91], v[76:79]
ds_read_b128 v[124:127], v17 offset:35840
v_mfma_f32_16x16x32_bf16 v[76:79], a[44:47], v[92:95], v[76:79]
s_mov_b64 exec, s[76:77]
global_store_dwordx4 v12, v[132:135], s[38:39] sc1
s_mov_b64 exec, -1
v_mfma_f32_16x16x32_bf16 v[76:79], a[56:59], v[96:99], v[76:79]
v_add_u32 v12, 110592, v12
v_mfma_f32_16x16x32_bf16 v[76:79], a[68:71], v[100:103], v[76:79]
s_waitcnt lgkmcnt(5)
v_mfma_f32_16x16x32_bf16 v[68:71], a[72:75], v[104:107], v[68:71]
s_waitcnt lgkmcnt(4)
v_mfma_f32_16x16x32_bf16 v[68:71], a[84:87], v[108:111], v[68:71]
s_waitcnt lgkmcnt(3)
v_mfma_f32_16x16x32_bf16 v[68:71], a[96:99], v[112:115], v[68:71]
s_waitcnt lgkmcnt(2)
v_mfma_f32_16x16x32_bf16 v[68:71], a[108:111], v[116:119], v[68:71]
s_waitcnt lgkmcnt(1)
v_mfma_f32_16x16x32_bf16 v[68:71], a[120:123], v[120:123], v[68:71]
s_waitcnt lgkmcnt(0)
ds_read_b128 v[80:83], v17 offset:36864
v_mfma_f32_16x16x32_bf16 v[68:71], a[132:135], v[124:127], v[68:71]
v_mfma_f32_16x16x32_bf16 v[72:75], a[76:79], v[104:107], v[72:75]
ds_read_b128 v[84:87], v17 offset:37888
v_mfma_f32_16x16x32_bf16 v[72:75], a[88:91], v[108:111], v[72:75]
v_mfma_f32_16x16x32_bf16 v[72:75], a[100:103], v[112:115], v[72:75]
ds_read_b128 v[88:91], v17 offset:38912
v_mfma_f32_16x16x32_bf16 v[72:75], a[112:115], v[116:119], v[72:75]
v_mfma_f32_16x16x32_bf16 v[72:75], a[124:127], v[120:123], v[72:75]
ds_read_b128 v[92:95], v17 offset:39936
v_mfma_f32_16x16x32_bf16 v[72:75], a[136:139], v[124:127], v[72:75]
v_mfma_f32_16x16x32_bf16 v[76:79], a[80:83], v[104:107], v[76:79]
ds_read_b128 v[96:99], v17 offset:40960
v_mfma_f32_16x16x32_bf16 v[76:79], a[92:95], v[108:111], v[76:79]
v_mfma_f32_16x16x32_bf16 v[76:79], a[104:107], v[112:115], v[76:79]
ds_read_b128 v[100:103], v17 offset:41984
v_mfma_f32_16x16x32_bf16 v[76:79], a[116:119], v[116:119], v[76:79]
v_mfma_f32_16x16x32_bf16 v[76:79], a[128:131], v[120:123], v[76:79]
v_mfma_f32_16x16x32_bf16 v[76:79], a[140:143], v[124:127], v[76:79]
s_waitcnt lgkmcnt(5)
v_mfma_f32_16x16x32_bf16 v[68:71], a[144:147], v[80:83], v[68:71]
s_waitcnt lgkmcnt(4)
v_mfma_f32_16x16x32_bf16 v[68:71], a[156:159], v[84:87], v[68:71]
s_waitcnt lgkmcnt(3)
v_mfma_f32_16x16x32_bf16 v[68:71], a[168:171], v[88:91], v[68:71]
.Ldn_in5:
s_waitcnt lgkmcnt(2)
v_mfma_f32_16x16x32_bf16 v[68:71], a[180:183], v[92:95], v[68:71]
s_waitcnt lgkmcnt(1)
v_mfma_f32_16x16x32_bf16 v[68:71], a[192:195], v[96:99], v[68:71]
s_waitcnt lgkmcnt(0)
ds_read_b128 v[104:107], v17 offset:43008
v_mfma_f32_16x16x32_bf16 v[68:71], a[204:207], v[100:103], v[68:71]
s_add_u32 m0, s45, 24576
v_mfma_f32_16x16x32_bf16 v[72:75], a[148:151], v[80:83], v[72:75]
ds_read_b128 v[108:111], v17 offset:44032
ds_read_b128 v[112:115], v17 offset:45056
v_mfma_f32_16x16x32_bf16 v[72:75], a[160:163], v[84:87], v[72:75]
v_mfma_f32_16x16x32_bf16 v[72:75], a[172:175], v[88:91], v[72:75]
s_barrier
v_mfma_f32_16x16x32_bf16 v[72:75], a[184:187], v[92:95], v[72:75]
global_load_lds_dwordx4 v9, s[38:39] offset:-2048 sc1
v_mfma_f32_16x16x32_bf16 v[72:75], a[196:199], v[96:99], v[72:75]
ds_read_b128 v[116:119], v17 offset:46080
v_mfma_f32_16x16x32_bf16 v[72:75], a[208:211], v[100:103], v[72:75]
v_mfma_f32_16x16x32_bf16 v[76:79], a[152:155], v[80:83], v[76:79]
ds_read_b128 v[120:123], v17 offset:47104
global_load_lds_dwordx4 v9, s[38:39] offset:-1024 sc1
v_mfma_f32_16x16x32_bf16 v[76:79], a[164:167], v[84:87], v[76:79]
v_mfma_f32_16x16x32_bf16 v[76:79], a[176:179], v[88:91], v[76:79]
ds_read_b128 v[124:127], v17 offset:48128
v_mfma_f32_16x16x32_bf16 v[76:79], a[188:191], v[92:95], v[76:79]
global_load_lds_dwordx4 v9, s[38:39] offset:0 sc1
v_mfma_f32_16x16x32_bf16 v[76:79], a[200:203], v[96:99], v[76:79]
v_mfma_f32_16x16x32_bf16 v[76:79], a[212:215], v[100:103], v[76:79]
s_waitcnt lgkmcnt(5)
v_mfma_f32_16x16x32_bf16 v[68:71], a[216:219], v[104:107], v[68:71]
s_waitcnt lgkmcnt(4)
global_load_lds_dwordx4 v9, s[38:39] offset:1024 sc1
v_mfma_f32_16x16x32_bf16 v[68:71], a[228:231], v[108:111], v[68:71]
s_waitcnt lgkmcnt(3)
v_mfma_f32_16x16x32_bf16 v[68:71], a[240:243], v[112:115], v[68:71]
s_waitcnt lgkmcnt(2)
v_mfma_f32_16x16x32_bf16 v[68:71], a[252:255], v[116:119], v[68:71]
s_waitcnt lgkmcnt(1)
global_load_lds_dwordx4 v9, s[38:39] offset:2048 sc1
v_mfma_f32_16x16x32_bf16 v[68:71], v[32:35], v[120:123], v[68:71]
s_waitcnt lgkmcnt(0)
v_mfma_f32_16x16x32_bf16 v[68:71], v[44:47], v[124:127], v[68:71]
v_xor_b32 v17, 0x10000, v17
v_mfma_f32_16x16x32_bf16 v[72:75], a[220:223], v[104:107], v[72:75]
global_load_lds_dwordx4 v9, s[38:39] offset:3072 sc1
v_add_u32 v9, 147456, v9
v_mfma_f32_16x16x32_bf16 v[72:75], a[232:235], v[108:111], v[72:75]
v_mfma_f32_16x16x32_bf16 v[72:75], a[244:247], v[112:115], v[72:75]
s_waitcnt vmcnt(8)
s_barrier
v_mfma_f32_16x16x32_bf16 v[72:75], v[24:27], v[116:119], v[72:75]
ds_read_b128 v[80:83], v17 offset:0
v_min_u32 v9, v9, v10
v_mfma_f32_16x16x32_bf16 v[72:75], v[36:39], v[120:123], v[72:75]
ds_read_b128 v[84:87], v17 offset:1024
v_cvt_pk_bf16_f32 v128, v68, v69
v_mfma_f32_16x16x32_bf16 v[72:75], v[48:51], v[124:127], v[72:75]
ds_read_b128 v[88:91], v17 offset:2048
v_cvt_pk_bf16_f32 v129, v70, v71
v_mfma_f32_16x16x32_bf16 v[76:79], a[224:227], v[104:107], v[76:79]
ds_read_b128 v[92:95], v17 offset:3072
v_mfma_f32_16x16x32_bf16 v[76:79], a[236:239], v[108:111], v[76:79]
ds_read_b128 v[96:99], v17 offset:4096
v_mfma_f32_16x16x32_bf16 v[76:79], a[248:251], v[112:115], v[76:79]
ds_read_b128 v[100:103], v17 offset:5120
v_mfma_f32_16x16x32_bf16 v[76:79], v[28:31], v[116:119], v[76:79]
v_cvt_pk_bf16_f32 v130, v72, v73
v_cvt_pk_bf16_f32 v131, v74, v75
v_mfma_f32_16x16x32_bf16 v[76:79], v[40:43], v[120:123], v[76:79]
v_mfma_f32_16x16x32_bf16 v[76:79], v[52:55], v[124:127], v[76:79]
s_sub_u32 s70, s70, 1
s_cbranch_scc0 .Ldn_top6
v_cvt_pk_bf16_f32 v128, v68, v69
v_cvt_pk_bf16_f32 v129, v70, v71
v_cvt_pk_bf16_f32 v130, v72, v73
v_cvt_pk_bf16_f32 v131, v74, v75
global_store_dwordx4 v11, v[128:131], s[38:39] sc1
v_add_u32 v11, 110592, v11
v_cvt_pk_bf16_f32 v132, v76, v77
v_cvt_pk_bf16_f32 v134, v76, v77
v_cvt_pk_bf16_f32 v133, v78, v79
v_cvt_pk_bf16_f32 v135, v78, v79
v_permlane16_swap_b32 v132, v134
s_nop 0
v_permlane16_swap_b32 v133, v135
s_mov_b64 exec, s[76:77]
global_store_dwordx4 v12, v[132:135], s[38:39] sc1
s_mov_b64 exec, -1
v_add_u32 v12, 110592, v12
s_waitcnt vmcnt(2)
s_barrier
s_waitcnt vmcnt(0)
s_barrier
s_endpgm
.Ldn_w2:
v_add_u32 v254, -1, v14
v_add_u32 v255, -1, v14
v_add_lshl_u32 v252, v16, 0, 7
v_bfi_b32 v252, s69, v252, v15
v_add_lshl_u32 v253, v14, 0, 7
v_bfi_b32 v253, s69, v253, v13
s_mov_b32 s76, 0xffff
s_mov_b32 s77, 0xffff
s_add_u32 m0, s45, 24576
s_xor_b32 s45, s45, 0x10000
s_mov_b32 m0, s45
s_barrier
global_load_lds_dwordx4 v9, s[38:39] offset:-2048 sc1
global_load_lds_dwordx4 v9, s[38:39] offset:-1024 sc1
global_load_lds_dwordx4 v9, s[38:39] offset:0 sc1
global_load_lds_dwordx4 v9, s[38:39] offset:1024 sc1
global_load_lds_dwordx4 v9, s[38:39] offset:2048 sc1
global_load_lds_dwordx4 v9, s[38:39] offset:3072 sc1
v_add_u32 v9, 147456, v9
s_mov_b32 s70, 32
s_waitcnt vmcnt(0) lgkmcnt(0)
s_nop 7
s_branch .Ldn_in8
.p2align 6
.Ldn_top9:
s_waitcnt lgkmcnt(5)
v_mfma_f32_16x16x32_bf16 v[56:59], a[0:3], v[80:83], 0
s_waitcnt lgkmcnt(4)
v_mfma_f32_16x16x32_bf16 v[56:59], a[12:15], v[84:87], v[56:59]
s_waitcnt lgkmcnt(3)
v_mfma_f32_16x16x32_bf16 v[56:59], a[24:27], v[88:91], v[56:59]
s_waitcnt lgkmcnt(2)
v_mfma_f32_16x16x32_bf16 v[56:59], a[36:39], v[92:95], v[56:59]
s_waitcnt lgkmcnt(1)
ds_read_b128 v[104:107], v17 offset:6144
v_cvt_pk_bf16_f32 v132, v76, v77
v_mfma_f32_16x16x32_bf16 v[56:59], a[48:51], v[96:99], v[56:59]
s_waitcnt lgkmcnt(1)
v_mfma_f32_16x16x32_bf16 v[56:59], a[60:63], v[100:103], v[56:59]
s_waitcnt vmcnt(6)
s_barrier
v_mfma_f32_16x16x32_bf16 v[60:63], a[4:7], v[80:83], 0
ds_read_b128 v[108:111], v17 offset:7168
v_cvt_pk_bf16_f32 v134, v76, v77
v_mfma_f32_16x16x32_bf16 v[60:63], a[16:19], v[84:87], v[60:63]
v_cvt_pk_bf16_f32 v133, v78, v79
v_cvt_pk_bf16_f32 v135, v78, v79
v_mfma_f32_16x16x32_bf16 v[60:63], a[28:31], v[88:91], v[60:63]
ds_read_b128 v[112:115], v17 offset:8192
v_mfma_f32_16x16x32_bf16 v[60:63], a[40:43], v[92:95], v[60:63]
global_store_dwordx4 v11, v[128:131], s[38:39] sc1
v_mfma_f32_16x16x32_bf16 v[60:63], a[52:55], v[96:99], v[60:63]
ds_read_b128 v[116:119], v17 offset:9216
v_add_u32 v11, 110592, v11
v_mfma_f32_16x16x32_bf16 v[60:63], a[64:67], v[100:103], v[60:63]
v_permlane16_swap_b32 v132, v134
v_mfma_f32_16x16x32_bf16 v[64:67], a[8:11], v[80:83], 0
ds_read_b128 v[120:123], v17 offset:10240
v_mfma_f32_16x16x32_bf16 v[64:67], a[20:23], v[84:87], v[64:67]
v_permlane16_swap_b32 v133, v135
v_mfma_f32_16x16x32_bf16 v[64:67], a[32:35], v[88:91], v[64:67]
ds_read_b128 v[124:127], v17 offset:11264
v_mfma_f32_16x16x32_bf16 v[64:67], a[44:47], v[92:95], v[64:67]
s_mov_b64 exec, s[76:77]
global_store_dwordx4 v12, v[132:135], s[38:39] sc1
s_mov_b64 exec, -1
v_mfma_f32_16x16x32_bf16 v[64:67], a[56:59], v[96:99], v[64:67]
v_add_u32 v12, 110592, v12
v_mfma_f32_16x16x32_bf16 v[64:67], a[68:71], v[100:103], v[64:67]
s_waitcnt lgkmcnt(5)
v_mfma_f32_16x16x32_bf16 v[56:59], a[72:75], v[104:107], v[56:59]
s_waitcnt lgkmcnt(4)
v_mfma_f32_16x16x32_bf16 v[56:59], a[84:87], v[108:111], v[56:59]
s_waitcnt lgkmcnt(3)
v_mfma_f32_16x16x32_bf16 v[56:59], a[96:99], v[112:115], v[56:59]
s_waitcnt lgkmcnt(2)
v_mfma_f32_16x16x32_bf16 v[56:59], a[108:111], v[116:119], v[56:59]
s_waitcnt lgkmcnt(1)
v_mfma_f32_16x16x32_bf16 v[56:59], a[120:123], v[120:123], v[56:59]
s_waitcnt lgkmcnt(0)
ds_read_b128 v[80:83], v17 offset:12288
v_mfma_f32_16x16x32_bf16 v[56:59], a[132:135], v[124:127], v[56:59]
v_mfma_f32_16x16x32_bf16 v[60:63], a[76:79], v[104:107], v[60:63]
ds_read_b128 v[84:87], v17 offset:13312
v_mfma_f32_16x16x32_bf16 v[60:63], a[88:91], v[108:111], v[60:63]
v_mfma_f32_16x16x32_bf16 v[60:63], a[100:103], v[112:115], v[60:63]
ds_read_b128 v[88:91], v17 offset:14336
v_mfma_f32_16x16x32_bf16 v[60:63], a[112:115], v[116:119], v[60:63]
v_mfma_f32_16x16x32_bf16 v[60:63], a[124:127], v[120:123], v[60:63]
ds_read_b128 v[92:95], v17 offset:15360
v_mfma_f32_16x16x32_bf16 v[60:63], a[136:139], v[124:127], v[60:63]
v_mfma_f32_16x16x32_bf16 v[64:67], a[80:83], v[104:107], v[64:67]
ds_read_b128 v[96:99], v17 offset:16384
v_mfma_f32_16x16x32_bf16 v[64:67], a[92:95], v[108:111], v[64:67]
v_mfma_f32_16x16x32_bf16 v[64:67], a[104:107], v[112:115], v[64:67]
ds_read_b128 v[100:103], v17 offset:17408
v_mfma_f32_16x16x32_bf16 v[64:67], a[116:119], v[116:119], v[64:67]
v_mfma_f32_16x16x32_bf16 v[64:67], a[128:131], v[120:123], v[64:67]
v_mfma_f32_16x16x32_bf16 v[64:67], a[140:143], v[124:127], v[64:67]
s_waitcnt lgkmcnt(5)
v_mfma_f32_16x16x32_bf16 v[56:59], a[144:147], v[80:83], v[56:59]
s_waitcnt lgkmcnt(4)
v_mfma_f32_16x16x32_bf16 v[56:59], a[156:159], v[84:87], v[56:59]
s_waitcnt lgkmcnt(3)
v_mfma_f32_16x16x32_bf16 v[56:59], a[168:171], v[88:91], v[56:59]
s_waitcnt lgkmcnt(2)
v_mfma_f32_16x16x32_bf16 v[56:59], a[180:183], v[92:95], v[56:59]
s_waitcnt lgkmcnt(1)
v_mfma_f32_16x16x32_bf16 v[56:59], a[192:195], v[96:99], v[56:59]
s_xor_b32 s45, s45, 0x10000
ds_read_b128 v[104:107], v17 offset:18432
s_waitcnt lgkmcnt(1)
v_mfma_f32_16x16x32_bf16 v[56:59], a[204:207], v[100:103], v[56:59]
s_add_u32 m0, s45, 0
v_mfma_f32_16x16x32_bf16 v[60:63], a[148:151], v[80:83], v[60:63]
ds_read_b128 v[108:111], v17 offset:19456
ds_read_b128 v[112:115], v17 offset:20480
v_mfma_f32_16x16x32_bf16 v[60:63], a[160:163], v[84:87], v[60:63]
v_mfma_f32_16x16x32_bf16 v[60:63], a[172:175], v[88:91], v[60:63]
s_barrier
v_mfma_f32_16x16x32_bf16 v[60:63], a[184:187], v[92:95], v[60:63]
v_mfma_f32_16x16x32_bf16 v[60:63], a[196:199], v[96:99], v[60:63]
ds_read_b128 v[116:119], v17 offset:21504
global_load_lds_dwordx4 v9, s[38:39] offset:-2048 sc1
v_mfma_f32_16x16x32_bf16 v[60:63], a[208:211], v[100:103], v[60:63]
v_mfma_f32_16x16x32_bf16 v[64:67], a[152:155], v[80:83], v[64:67]
ds_read_b128 v[120:123], v17 offset:22528
v_mfma_f32_16x16x32_bf16 v[64:67], a[164:167], v[84:87], v[64:67]
global_load_lds_dwordx4 v9, s[38:39] offset:-1024 sc1
)"
R"(v_mfma_f32_16x16x32_bf16 v[64:67], a[176:179], v[88:91], v[64:67]
ds_read_b128 v[124:127], v17 offset:23552
v_mfma_f32_16x16x32_bf16 v[64:67], a[188:191], v[92:95], v[64:67]
v_mfma_f32_16x16x32_bf16 v[64:67], a[200:203], v[96:99], v[64:67]
global_load_lds_dwordx4 v9, s[38:39] offset:0 sc1
v_mfma_f32_16x16x32_bf16 v[64:67], a[212:215], v[100:103], v[64:67]
s_waitcnt lgkmcnt(5)
v_mfma_f32_16x16x32_bf16 v[56:59], a[216:219], v[104:107], v[56:59]
s_waitcnt lgkmcnt(4)
v_mfma_f32_16x16x32_bf16 v[56:59], a[228:231], v[108:111], v[56:59]
s_waitcnt lgkmcnt(3)
global_load_lds_dwordx4 v9, s[38:39] offset:1024 sc1
v_mfma_f32_16x16x32_bf16 v[56:59], a[240:243], v[112:115], v[56:59]
s_waitcnt lgkmcnt(2)
v_mfma_f32_16x16x32_bf16 v[56:59], a[252:255], v[116:119], v[56:59]
s_waitcnt lgkmcnt(1)
v_mfma_f32_16x16x32_bf16 v[56:59], v[32:35], v[120:123], v[56:59]
s_waitcnt lgkmcnt(0)
global_load_lds_dwordx4 v9, s[38:39] offset:2048 sc1
v_mfma_f32_16x16x32_bf16 v[56:59], v[44:47], v[124:127], v[56:59]
v_mfma_f32_16x16x32_bf16 v[60:63], a[220:223], v[104:107], v[60:63]
v_mfma_f32_16x16x32_bf16 v[60:63], a[232:235], v[108:111], v[60:63]
v_mfma_f32_16x16x32_bf16 v[60:63], a[244:247], v[112:115], v[60:63]
s_waitcnt vmcnt(7)
s_barrier
v_mfma_f32_16x16x32_bf16 v[60:63], v[24:27], v[116:119], v[60:63]
ds_read_b128 v[80:83], v17 offset:24576
global_load_lds_dwordx4 v9, s[38:39] offset:3072 sc1
v_mfma_f32_16x16x32_bf16 v[60:63], v[36:39], v[120:123], v[60:63]
ds_read_b128 v[84:87], v17 offset:25600
v_add_u32 v9, 147456, v9
v_mfma_f32_16x16x32_bf16 v[60:63], v[48:51], v[124:127], v[60:63]
ds_read_b128 v[88:91], v17 offset:26624
v_min_u32 v9, v9, v10
v_mfma_f32_16x16x32_bf16 v[64:67], a[224:227], v[104:107], v[64:67]
ds_read_b128 v[92:95], v17 offset:27648
v_cvt_pk_bf16_f32 v128, v56, v57
v_mfma_f32_16x16x32_bf16 v[64:67], a[236:239], v[108:111], v[64:67]
ds_read_b128 v[96:99], v17 offset:28672
v_cvt_pk_bf16_f32 v129, v58, v59
v_mfma_f32_16x16x32_bf16 v[64:67], a[248:251], v[112:115], v[64:67]
ds_read_b128 v[100:103], v17 offset:29696
v_mfma_f32_16x16x32_bf16 v[64:67], v[28:31], v[116:119], v[64:67]
v_cvt_pk_bf16_f32 v130, v60, v61
v_cvt_pk_bf16_f32 v131, v62, v63
v_mfma_f32_16x16x32_bf16 v[64:67], v[40:43], v[120:123], v[64:67]
v_mfma_f32_16x16x32_bf16 v[64:67], v[52:55], v[124:127], v[64:67]
s_waitcnt lgkmcnt(5)
v_mfma_f32_16x16x32_bf16 v[68:71], a[0:3], v[80:83], 0
s_waitcnt lgkmcnt(4)
v_mfma_f32_16x16x32_bf16 v[68:71], a[12:15], v[84:87], v[68:71]
s_waitcnt lgkmcnt(3)
v_mfma_f32_16x16x32_bf16 v[68:71], a[24:27], v[88:91], v[68:71]
s_waitcnt lgkmcnt(2)
v_mfma_f32_16x16x32_bf16 v[68:71], a[36:39], v[92:95], v[68:71]
s_waitcnt lgkmcnt(1)
ds_read_b128 v[104:107], v17 offset:30720
v_cvt_pk_bf16_f32 v132, v64, v65
v_mfma_f32_16x16x32_bf16 v[68:71], a[48:51], v[96:99], v[68:71]
s_waitcnt lgkmcnt(1)
v_mfma_f32_16x16x32_bf16 v[68:71], a[60:63], v[100:103], v[68:71]
s_waitcnt vmcnt(6)
s_barrier
v_mfma_f32_16x16x32_bf16 v[72:75], a[4:7], v[80:83], 0
ds_read_b128 v[108:111], v17 offset:31744
v_cvt_pk_bf16_f32 v134, v64, v65
v_mfma_f32_16x16x32_bf16 v[72:75], a[16:19], v[84:87], v[72:75]
v_cvt_pk_bf16_f32 v133, v66, v67
v_cvt_pk_bf16_f32 v135, v66, v67
v_mfma_f32_16x16x32_bf16 v[72:75], a[28:31], v[88:91], v[72:75]
ds_read_b128 v[112:115], v17 offset:32768
v_mfma_f32_16x16x32_bf16 v[72:75], a[40:43], v[92:95], v[72:75]
global_store_dwordx4 v11, v[128:131], s[38:39] sc1
v_mfma_f32_16x16x32_bf16 v[72:75], a[52:55], v[96:99], v[72:75]
ds_read_b128 v[116:119], v17 offset:33792
v_add_u32 v11, 110592, v11
v_mfma_f32_16x16x32_bf16 v[72:75], a[64:67], v[100:103], v[72:75]
v_permlane16_swap_b32 v132, v134
v_mfma_f32_16x16x32_bf16 v[76:79], a[8:11], v[80:83], 0
ds_read_b128 v[120:123], v17 offset:34816
v_mfma_f32_16x16x32_bf16 v[76:79], a[20:23], v[84:87], v[76:79]
v_permlane16_swap_b32 v133, v135
v_mfma_f32_16x16x32_bf16 v[76:79], a[32:35], v[88:91], v[76:79]
ds_read_b128 v[124:127], v17 offset:35840
v_mfma_f32_16x16x32_bf16 v[76:79], a[44:47], v[92:95], v[76:79]
s_mov_b64 exec, s[76:77]
global_store_dwordx4 v12, v[132:135], s[38:39] sc1
s_mov_b64 exec, -1
v_mfma_f32_16x16x32_bf16 v[76:79], a[56:59], v[96:99], v[76:79]
v_add_u32 v12, 110592, v12
v_mfma_f32_16x16x32_bf16 v[76:79], a[68:71], v[100:103], v[76:79]
s_waitcnt lgkmcnt(5)
v_mfma_f32_16x16x32_bf16 v[68:71], a[72:75], v[104:107], v[68:71]
s_waitcnt lgkmcnt(4)
v_mfma_f32_16x16x32_bf16 v[68:71], a[84:87], v[108:111], v[68:71]
s_waitcnt lgkmcnt(3)
v_mfma_f32_16x16x32_bf16 v[68:71], a[96:99], v[112:115], v[68:71]
s_waitcnt lgkmcnt(2)
v_mfma_f32_16x16x32_bf16 v[68:71], a[108:111], v[116:119], v[68:71]
s_waitcnt lgkmcnt(1)
v_mfma_f32_16x16x32_bf16 v[68:71], a[120:123], v[120:123], v[68:71]
s_waitcnt lgkmcnt(0)
ds_read_b128 v[80:83], v17 offset:36864
v_mfma_f32_16x16x32_bf16 v[68:71], a[132:135], v[124:127], v[68:71]
v_mfma_f32_16x16x32_bf16 v[72:75], a[76:79], v[104:107], v[72:75]
ds_read_b128 v[84:87], v17 offset:37888
v_mfma_f32_16x16x32_bf16 v[72:75], a[88:91], v[108:111], v[72:75]
v_mfma_f32_16x16x32_bf16 v[72:75], a[100:103], v[112:115], v[72:75]
ds_read_b128 v[88:91], v17 offset:38912
v_mfma_f32_16x16x32_bf16 v[72:75], a[112:115], v[116:119], v[72:75]
v_mfma_f32_16x16x32_bf16 v[72:75], a[124:127], v[120:123], v[72:75]
ds_read_b128 v[92:95], v17 offset:39936
v_mfma_f32_16x16x32_bf16 v[72:75], a[136:139], v[124:127], v[72:75]
v_mfma_f32_16x16x32_bf16 v[76:79], a[80:83], v[104:107], v[76:79]
ds_read_b128 v[96:99], v17 offset:40960
v_mfma_f32_16x16x32_bf16 v[76:79], a[92:95], v[108:111], v[76:79]
v_mfma_f32_16x16x32_bf16 v[76:79], a[104:107], v[112:115], v[76:79]
ds_read_b128 v[100:103], v17 offset:41984
v_mfma_f32_16x16x32_bf16 v[76:79], a[116:119], v[116:119], v[76:79]
v_mfma_f32_16x16x32_bf16 v[76:79], a[128:131], v[120:123], v[76:79]
v_mfma_f32_16x16x32_bf16 v[76:79], a[140:143], v[124:127], v[76:79]
s_waitcnt lgkmcnt(5)
v_mfma_f32_16x16x32_bf16 v[68:71], a[144:147], v[80:83], v[68:71]
s_waitcnt lgkmcnt(4)
v_mfma_f32_16x16x32_bf16 v[68:71], a[156:159], v[84:87], v[68:71]
s_waitcnt lgkmcnt(3)
v_mfma_f32_16x16x32_bf16 v[68:71], a[168:171], v[88:91], v[68:71]
.Ldn_in8:
s_waitcnt lgkmcnt(2)
v_mfma_f32_16x16x32_bf16 v[68:71], a[180:183], v[92:95], v[68:71]
s_waitcnt lgkmcnt(1)
v_mfma_f32_16x16x32_bf16 v[68:71], a[192:195], v[96:99], v[68:71]
s_waitcnt lgkmcnt(0)
ds_read_b128 v[104:107], v17 offset:43008
v_mfma_f32_16x16x32_bf16 v[68:71], a[204:207], v[100:103], v[68:71]
s_add_u32 m0, s45, 24576
v_mfma_f32_16x16x32_bf16 v[72:75], a[148:151], v[80:83], v[72:75]
ds_read_b128 v[108:111], v17 offset:44032
ds_read_b128 v[112:115], v17 offset:45056
v_mfma_f32_16x16x32_bf16 v[72:75], a[160:163], v[84:87], v[72:75]
v_mfma_f32_16x16x32_bf16 v[72:75], a[172:175], v[88:91], v[72:75]
s_barrier
v_mfma_f32_16x16x32_bf16 v[72:75], a[184:187], v[92:95], v[72:75]
v_mfma_f32_16x16x32_bf16 v[72:75], a[196:199], v[96:99], v[72:75]
ds_read_b128 v[116:119], v17 offset:46080
global_load_lds_dwordx4 v9, s[38:39] offset:-2048 sc1
v_mfma_f32_16x16x32_bf16 v[72:75], a[208:211], v[100:103], v[72:75]
v_mfma_f32_16x16x32_bf16 v[76:79], a[152:155], v[80:83], v[76:79]
ds_read_b128 v[120:123], v17 offset:47104
v_mfma_f32_16x16x32_bf16 v[76:79], a[164:167], v[84:87], v[76:79]
global_load_lds_dwordx4 v9, s[38:39] offset:-1024 sc1
v_mfma_f32_16x16x32_bf16 v[76:79], a[176:179], v[88:91], v[76:79]
ds_read_b128 v[124:127], v17 offset:48128
v_mfma_f32_16x16x32_bf16 v[76:79], a[188:191], v[92:95], v[76:79]
v_mfma_f32_16x16x32_bf16 v[76:79], a[200:203], v[96:99], v[76:79]
global_load_lds_dwordx4 v9, s[38:39] offset:0 sc1
v_mfma_f32_16x16x32_bf16 v[76:79], a[212:215], v[100:103], v[76:79]
s_waitcnt lgkmcnt(5)
v_mfma_f32_16x16x32_bf16 v[68:71], a[216:219], v[104:107], v[68:71]
s_waitcnt lgkmcnt(4)
v_mfma_f32_16x16x32_bf16 v[68:71], a[228:231], v[108:111], v[68:71]
s_waitcnt lgkmcnt(3)
global_load_lds_dwordx4 v9, s[38:39] offset:1024 sc1
v_mfma_f32_16x16x32_bf16 v[68:71], a[240:243], v[112:115], v[68:71]
s_waitcnt lgkmcnt(2)
v_mfma_f32_16x16x32_bf16 v[68:71], a[252:255], v[116:119], v[68:71]
s_waitcnt lgkmcnt(1)
v_mfma_f32_16x16x32_bf16 v[68:71], v[32:35], v[120:123], v[68:71]
s_waitcnt lgkmcnt(0)
global_load_lds_dwordx4 v9, s[38:39] offset:2048 sc1
v_mfma_f32_16x16x32_bf16 v[68:71], v[44:47], v[124:127], v[68:71]
v_xor_b32 v17, 0x10000, v17
v_mfma_f32_16x16x32_bf16 v[72:75], a[220:223], v[104:107], v[72:75]
v_mfma_f32_16x16x32_bf16 v[72:75], a[232:235], v[108:111], v[72:75]
v_mfma_f32_16x16x32_bf16 v[72:75], a[244:247], v[112:115], v[72:75]
s_waitcnt vmcnt(7)
s_barrier
v_mfma_f32_16x16x32_bf16 v[72:75], v[24:27], v[116:119], v[72:75]
ds_read_b128 v[80:83], v17 offset:0
global_load_lds_dwordx4 v9, s[38:39] offset:3072 sc1
v_mfma_f32_16x16x32_bf16 v[72:75], v[36:39], v[120:123], v[72:75]
ds_read_b128 v[84:87], v17 offset:1024
v_add_u32 v9, 147456, v9
v_mfma_f32_16x16x32_bf16 v[72:75], v[48:51], v[124:127], v[72:75]
ds_read_b128 v[88:91], v17 offset:2048
v_min_u32 v9, v9, v10
v_mfma_f32_16x16x32_bf16 v[76:79], a[224:227], v[104:107], v[76:79]
ds_read_b128 v[92:95], v17 offset:3072
v_cvt_pk_bf16_f32 v128, v68, v69
v_mfma_f32_16x16x32_bf16 v[76:79], a[236:239], v[108:111], v[76:79]
ds_read_b128 v[96:99], v17 offset:4096
v_cvt_pk_bf16_f32 v129, v70, v71
v_mfma_f32_16x16x32_bf16 v[76:79], a[248:251], v[112:115], v[76:79]
ds_read_b128 v[100:103], v17 offset:5120
v_mfma_f32_16x16x32_bf16 v[76:79], v[28:31], v[116:119], v[76:79]
v_cvt_pk_bf16_f32 v130, v72, v73
v_cvt_pk_bf16_f32 v131, v74, v75
v_mfma_f32_16x16x32_bf16 v[76:79], v[40:43], v[120:123], v[76:79]
v_mfma_f32_16x16x32_bf16 v[76:79], v[52:55], v[124:127], v[76:79]
s_sub_u32 s70, s70, 1
s_cbranch_scc0 .Ldn_top9
v_cvt_pk_bf16_f32 v128, v68, v69
v_cvt_pk_bf16_f32 v129, v70, v71
v_cvt_pk_bf16_f32 v130, v72, v73
v_cvt_pk_bf16_f32 v131, v74, v75
global_store_dwordx4 v11, v[128:131], s[38:39] sc1
v_add_u32 v11, 110592, v11
v_cvt_pk_bf16_f32 v132, v76, v77
v_cvt_pk_bf16_f32 v134, v76, v77
v_cvt_pk_bf16_f32 v133, v78, v79
v_cvt_pk_bf16_f32 v135, v78, v79
v_permlane16_swap_b32 v132, v134
s_nop 0
v_permlane16_swap_b32 v133, v135
s_mov_b64 exec, s[76:77]
global_store_dwordx4 v12, v[132:135], s[38:39] sc1
s_mov_b64 exec, -1
v_add_u32 v12, 110592, v12
s_waitcnt vmcnt(2)
s_barrier
s_waitcnt vmcnt(0)
s_barrier
s_endpgm
.Ldn_w3:
v_add_u32 v254, -1, v14
v_add_u32 v255, -1, v14
v_add_lshl_u32 v252, v16, 0, 7
v_bfi_b32 v252, s69, v252, v15
v_add_lshl_u32 v253, v14, 0, 7
v_bfi_b32 v253, s69, v253, v13
s_mov_b32 s76, 0xffff
s_mov_b32 s77, 0xffff
s_add_u32 m0, s45, 24576
s_xor_b32 s45, s45, 0x10000
s_mov_b32 m0, s45
s_barrier
global_load_lds_dwordx4 v9, s[38:39] offset:-2048 sc1
global_load_lds_dwordx4 v9, s[38:39] offset:-1024 sc1
global_load_lds_dwordx4 v9, s[38:39] offset:0 sc1
global_load_lds_dwordx4 v9, s[38:39] offset:1024 sc1
global_load_lds_dwordx4 v9, s[38:39] offset:2048 sc1
global_load_lds_dwordx4 v9, s[38:39] offset:3072 sc1
v_add_u32 v9, 147456, v9
s_mov_b32 s70, 32
s_waitcnt vmcnt(0) lgkmcnt(0)
s_nop 7
s_branch .Ldn_in11
.p2align 6
.Ldn_top12:
s_waitcnt lgkmcnt(5)
v_mfma_f32_16x16x32_bf16 v[56:59], a[0:3], v[80:83], 0
s_waitcnt lgkmcnt(4)
v_mfma_f32_16x16x32_bf16 v[56:59], a[12:15], v[84:87], v[56:59]
s_waitcnt lgkmcnt(3)
v_mfma_f32_16x16x32_bf16 v[56:59], a[24:27], v[88:91], v[56:59]
s_waitcnt lgkmcnt(2)
v_mfma_f32_16x16x32_bf16 v[56:59], a[36:39], v[92:95], v[56:59]
s_waitcnt lgkmcnt(1)
ds_read_b128 v[104:107], v17 offset:6144
v_cvt_pk_bf16_f32 v132, v76, v77
v_mfma_f32_16x16x32_bf16 v[56:59], a[48:51], v[96:99], v[56:59]
s_waitcnt lgkmcnt(1)
v_mfma_f32_16x16x32_bf16 v[56:59], a[60:63], v[100:103], v[56:59]
s_waitcnt vmcnt(6)
s_barrier
v_mfma_f32_16x16x32_bf16 v[60:63], a[4:7], v[80:83], 0
ds_read_b128 v[108:111], v17 offset:7168
s_mov_b64 exec, 1
global_store_dword v252, v16, s[38:39] sc1
s_mov_b64 exec, -1
v_mfma_f32_16x16x32_bf16 v[60:63], a[16:19], v[84:87], v[60:63]
v_add_u32 v16, 1, v16
v_add_lshl_u32 v252, v16, 0, 7
v_bfi_b32 v252, s69, v252, v15
v_mfma_f32_16x16x32_bf16 v[60:63], a[28:31], v[88:91], v[60:63]
ds_read_b128 v[112:115], v17 offset:8192
v_cvt_pk_bf16_f32 v134, v76, v77
v_mfma_f32_16x16x32_bf16 v[60:63], a[40:43], v[92:95], v[60:63]
global_store_dwordx4 v11, v[128:131], s[38:39] sc1
v_mfma_f32_16x16x32_bf16 v[60:63], a[52:55], v[96:99], v[60:63]
ds_read_b128 v[116:119], v17 offset:9216
v_add_u32 v11, 110592, v11
v_mfma_f32_16x16x32_bf16 v[60:63], a[64:67], v[100:103], v[60:63]
v_cvt_pk_bf16_f32 v133, v78, v79
v_cvt_pk_bf16_f32 v135, v78, v79
v_mfma_f32_16x16x32_bf16 v[64:67], a[8:11], v[80:83], 0
ds_read_b128 v[120:123], v17 offset:10240
v_mfma_f32_16x16x32_bf16 v[64:67], a[20:23], v[84:87], v[64:67]
v_permlane16_swap_b32 v132, v134
v_mfma_f32_16x16x32_bf16 v[64:67], a[32:35], v[88:91], v[64:67]
ds_read_b128 v[124:127], v17 offset:11264
v_mfma_f32_16x16x32_bf16 v[64:67], a[44:47], v[92:95], v[64:67]
v_permlane16_swap_b32 v133, v135
v_mfma_f32_16x16x32_bf16 v[64:67], a[56:59], v[96:99], v[64:67]
s_mov_b64 exec, s[76:77]
global_store_dwordx4 v12, v[132:135], s[38:39] sc1
s_mov_b64 exec, -1
v_mfma_f32_16x16x32_bf16 v[64:67], a[68:71], v[100:103], v[64:67]
s_waitcnt lgkmcnt(5)
v_add_u32 v12, 110592, v12
v_mfma_f32_16x16x32_bf16 v[56:59], a[72:75], v[104:107], v[56:59]
s_waitcnt lgkmcnt(4)
v_mfma_f32_16x16x32_bf16 v[56:59], a[84:87], v[108:111], v[56:59]
s_waitcnt lgkmcnt(3)
v_mfma_f32_16x16x32_bf16 v[56:59], a[96:99], v[112:115], v[56:59]
s_waitcnt lgkmcnt(2)
v_mfma_f32_16x16x32_bf16 v[56:59], a[108:111], v[116:119], v[56:59]
s_waitcnt lgkmcnt(1)
v_mfma_f32_16x16x32_bf16 v[56:59], a[120:123], v[120:123], v[56:59]
s_waitcnt lgkmcnt(0)
ds_read_b128 v[80:83], v17 offset:12288
v_mfma_f32_16x16x32_bf16 v[56:59], a[132:135], v[124:127], v[56:59]
v_mfma_f32_16x16x32_bf16 v[60:63], a[76:79], v[104:107], v[60:63]
ds_read_b128 v[84:87], v17 offset:13312
v_mfma_f32_16x16x32_bf16 v[60:63], a[88:91], v[108:111], v[60:63]
v_mfma_f32_16x16x32_bf16 v[60:63], a[100:103], v[112:115], v[60:63]
ds_read_b128 v[88:91], v17 offset:14336
v_mfma_f32_16x16x32_bf16 v[60:63], a[112:115], v[116:119], v[60:63]
v_mfma_f32_16x16x32_bf16 v[60:63], a[124:127], v[120:123], v[60:63]
ds_read_b128 v[92:95], v17 offset:15360
v_mfma_f32_16x16x32_bf16 v[60:63], a[136:139], v[124:127], v[60:63]
v_mfma_f32_16x16x32_bf16 v[64:67], a[80:83], v[104:107], v[64:67]
ds_read_b128 v[96:99], v17 offset:16384
v_mfma_f32_16x16x32_bf16 v[64:67], a[92:95], v[108:111], v[64:67]
v_mfma_f32_16x16x32_bf16 v[64:67], a[104:107], v[112:115], v[64:67]
ds_read_b128 v[100:103], v17 offset:17408
v_mfma_f32_16x16x32_bf16 v[64:67], a[116:119], v[116:119], v[64:67]
v_mfma_f32_16x16x32_bf16 v[64:67], a[128:131], v[120:123], v[64:67]
v_mfma_f32_16x16x32_bf16 v[64:67], a[140:143], v[124:127], v[64:67]
s_waitcnt lgkmcnt(5)
v_mfma_f32_16x16x32_bf16 v[56:59], a[144:147], v[80:83], v[56:59]
s_waitcnt lgkmcnt(4)
v_mfma_f32_16x16x32_bf16 v[56:59], a[156:159], v[84:87], v[56:59]
s_waitcnt lgkmcnt(3)
v_mfma_f32_16x16x32_bf16 v[56:59], a[168:171], v[88:91], v[56:59]
s_waitcnt lgkmcnt(2)
v_mfma_f32_16x16x32_bf16 v[56:59], a[180:183], v[92:95], v[56:59]
s_waitcnt lgkmcnt(1)
v_mfma_f32_16x16x32_bf16 v[56:59], a[192:195], v[96:99], v[56:59]
s_xor_b32 s45, s45, 0x10000
ds_read_b128 v[104:107], v17 offset:18432
s_waitcnt lgkmcnt(1)
v_mfma_f32_16x16x32_bf16 v[56:59], a[204:207], v[100:103], v[56:59]
s_add_u32 m0, s45, 0
v_mfma_f32_16x16x32_bf16 v[60:63], a[148:151], v[80:83], v[60:63]
ds_read_b128 v[108:111], v17 offset:19456
ds_read_b128 v[112:115], v17 offset:20480
v_mfma_f32_16x16x32_bf16 v[60:63], a[160:163], v[84:87], v[60:63]
v_mfma_f32_16x16x32_bf16 v[60:63], a[172:175], v[88:91], v[60:63]
s_barrier
v_mfma_f32_16x16x32_bf16 v[60:63], a[184:187], v[92:95], v[60:63]
v_mfma_f32_16x16x32_bf16 v[60:63], a[196:199], v[96:99], v[60:63]
ds_read_b128 v[116:119], v17 offset:21504
v_mfma_f32_16x16x32_bf16 v[60:63], a[208:211], v[100:103], v[60:63]
global_load_lds_dwordx4 v9, s[38:39] offset:-2048 sc1
v_mfma_f32_16x16x32_bf16 v[64:67], a[152:155], v[80:83], v[64:67]
ds_read_b128 v[120:123], v17 offset:22528
v_mfma_f32_16x16x32_bf16 v[64:67], a[164:167], v[84:87], v[64:67]
v_mfma_f32_16x16x32_bf16 v[64:67], a[176:179], v[88:91], v[64:67]
ds_read_b128 v[124:127], v17 offset:23552
global_load_lds_dwordx4 v9, s[38:39] offset:-1024 sc1
v_mfma_f32_16x16x32_bf16 v[64:67], a[188:191], v[92:95], v[64:67]
v_mfma_f32_16x16x32_bf16 v[64:67], a[200:203], v[96:99], v[64:67]
v_mfma_f32_16x16x32_bf16 v[64:67], a[212:215], v[100:103], v[64:67]
s_waitcnt lgkmcnt(5)
global_load_lds_dwordx4 v9, s[38:39] offset:0 sc1
v_mfma_f32_16x16x32_bf16 v[56:59], a[216:219], v[104:107], v[56:59]
s_waitcnt lgkmcnt(4)
v_mfma_f32_16x16x32_bf16 v[56:59], a[228:231], v[108:111], v[56:59]
s_waitcnt lgkmcnt(3)
v_mfma_f32_16x16x32_bf16 v[56:59], a[240:243], v[112:115], v[56:59]
s_waitcnt lgkmcnt(2)
global_load_lds_dwordx4 v9, s[38:39] offset:1024 sc1
v_mfma_f32_16x16x32_bf16 v[56:59], a[252:255], v[116:119], v[56:59]
s_waitcnt lgkmcnt(1)
v_mfma_f32_16x16x32_bf16 v[56:59], v[32:35], v[120:123], v[56:59]
s_waitcnt lgkmcnt(0)
v_mfma_f32_16x16x32_bf16 v[56:59], v[44:47], v[124:127], v[56:59]
global_load_lds_dwordx4 v9, s[38:39] offset:2048 sc1
v_mfma_f32_16x16x32_bf16 v[60:63], a[220:223], v[104:107], v[60:63]
v_mfma_f32_16x16x32_bf16 v[60:63], a[232:235], v[108:111], v[60:63]
v_mfma_f32_16x16x32_bf16 v[60:63], a[244:247], v[112:115], v[60:63]
s_waitcnt vmcnt(8)
s_barrier
v_mfma_f32_16x16x32_bf16 v[60:63], v[24:27], v[116:119], v[60:63]
ds_read_b128 v[80:83], v17 offset:24576
global_load_lds_dwordx4 v9, s[38:39] offset:3072 sc1
v_mfma_f32_16x16x32_bf16 v[60:63], v[36:39], v[120:123], v[60:63]
ds_read_b128 v[84:87], v17 offset:25600
v_add_u32 v9, 147456, v9
v_mfma_f32_16x16x32_bf16 v[60:63], v[48:51], v[124:127], v[60:63]
ds_read_b128 v[88:91], v17 offset:26624
v_min_u32 v9, v9, v10
v_mfma_f32_16x16x32_bf16 v[64:67], a[224:227], v[104:107], v[64:67]
ds_read_b128 v[92:95], v17 offset:27648
v_cvt_pk_bf16_f32 v128, v56, v57
v_mfma_f32_16x16x32_bf16 v[64:67], a[236:239], v[108:111], v[64:67]
ds_read_b128 v[96:99], v17 offset:28672
v_cvt_pk_bf16_f32 v129, v58, v59
v_mfma_f32_16x16x32_bf16 v[64:67], a[248:251], v[112:115], v[64:67]
)"
R"(ds_read_b128 v[100:103], v17 offset:29696
v_mfma_f32_16x16x32_bf16 v[64:67], v[28:31], v[116:119], v[64:67]
v_cvt_pk_bf16_f32 v130, v60, v61
v_cvt_pk_bf16_f32 v131, v62, v63
v_mfma_f32_16x16x32_bf16 v[64:67], v[40:43], v[120:123], v[64:67]
v_mfma_f32_16x16x32_bf16 v[64:67], v[52:55], v[124:127], v[64:67]
s_waitcnt lgkmcnt(5)
v_mfma_f32_16x16x32_bf16 v[68:71], a[0:3], v[80:83], 0
s_waitcnt lgkmcnt(4)
v_mfma_f32_16x16x32_bf16 v[68:71], a[12:15], v[84:87], v[68:71]
s_waitcnt lgkmcnt(3)
v_mfma_f32_16x16x32_bf16 v[68:71], a[24:27], v[88:91], v[68:71]
s_waitcnt lgkmcnt(2)
v_mfma_f32_16x16x32_bf16 v[68:71], a[36:39], v[92:95], v[68:71]
s_waitcnt lgkmcnt(1)
ds_read_b128 v[104:107], v17 offset:30720
v_cvt_pk_bf16_f32 v132, v64, v65
v_mfma_f32_16x16x32_bf16 v[68:71], a[48:51], v[96:99], v[68:71]
s_waitcnt lgkmcnt(1)
v_mfma_f32_16x16x32_bf16 v[68:71], a[60:63], v[100:103], v[68:71]
s_waitcnt vmcnt(6)
s_barrier
v_mfma_f32_16x16x32_bf16 v[72:75], a[4:7], v[80:83], 0
ds_read_b128 v[108:111], v17 offset:31744
v_cvt_pk_bf16_f32 v134, v64, v65
v_mfma_f32_16x16x32_bf16 v[72:75], a[16:19], v[84:87], v[72:75]
v_cvt_pk_bf16_f32 v133, v66, v67
v_cvt_pk_bf16_f32 v135, v66, v67
v_mfma_f32_16x16x32_bf16 v[72:75], a[28:31], v[88:91], v[72:75]
ds_read_b128 v[112:115], v17 offset:32768
s_mov_b64 exec, 1
global_store_dword v252, v16, s[38:39] sc1
s_mov_b64 exec, -1
v_mfma_f32_16x16x32_bf16 v[72:75], a[40:43], v[92:95], v[72:75]
global_store_dwordx4 v11, v[128:131], s[38:39] sc1
v_add_u32 v16, 1, v16
v_mfma_f32_16x16x32_bf16 v[72:75], a[52:55], v[96:99], v[72:75]
ds_read_b128 v[116:119], v17 offset:33792
v_add_u32 v11, 110592, v11
v_add_lshl_u32 v252, v16, 0, 7
v_mfma_f32_16x16x32_bf16 v[72:75], a[64:67], v[100:103], v[72:75]
v_permlane16_swap_b32 v132, v134
v_bfi_b32 v252, s69, v252, v15
v_mfma_f32_16x16x32_bf16 v[76:79], a[8:11], v[80:83], 0
ds_read_b128 v[120:123], v17 offset:34816
v_mfma_f32_16x16x32_bf16 v[76:79], a[20:23], v[84:87], v[76:79]
v_permlane16_swap_b32 v133, v135
v_mfma_f32_16x16x32_bf16 v[76:79], a[32:35], v[88:91], v[76:79]
ds_read_b128 v[124:127], v17 offset:35840
v_mfma_f32_16x16x32_bf16 v[76:79], a[44:47], v[92:95], v[76:79]
s_mov_b64 exec, s[76:77]
global_store_dwordx4 v12, v[132:135], s[38:39] sc1
s_mov_b64 exec, -1
v_mfma_f32_16x16x32_bf16 v[76:79], a[56:59], v[96:99], v[76:79]
v_add_u32 v12, 110592, v12
v_mfma_f32_16x16x32_bf16 v[76:79], a[68:71], v[100:103], v[76:79]
s_waitcnt lgkmcnt(5)
v_mfma_f32_16x16x32_bf16 v[68:71], a[72:75], v[104:107], v[68:71]
s_waitcnt lgkmcnt(4)
v_mfma_f32_16x16x32_bf16 v[68:71], a[84:87], v[108:111], v[68:71]
s_waitcnt lgkmcnt(3)
v_mfma_f32_16x16x32_bf16 v[68:71], a[96:99], v[112:115], v[68:71]
s_waitcnt lgkmcnt(2)
v_mfma_f32_16x16x32_bf16 v[68:71], a[108:111], v[116:119], v[68:71]
s_waitcnt lgkmcnt(1)
v_mfma_f32_16x16x32_bf16 v[68:71], a[120:123], v[120:123], v[68:71]
s_waitcnt lgkmcnt(0)
ds_read_b128 v[80:83], v17 offset:36864
v_mfma_f32_16x16x32_bf16 v[68:71], a[132:135], v[124:127], v[68:71]
v_mfma_f32_16x16x32_bf16 v[72:75], a[76:79], v[104:107], v[72:75]
ds_read_b128 v[84:87], v17 offset:37888
v_mfma_f32_16x16x32_bf16 v[72:75], a[88:91], v[108:111], v[72:75]
v_mfma_f32_16x16x32_bf16 v[72:75], a[100:103], v[112:115], v[72:75]
ds_read_b128 v[88:91], v17 offset:38912
v_mfma_f32_16x16x32_bf16 v[72:75], a[112:115], v[116:119], v[72:75]
v_mfma_f32_16x16x32_bf16 v[72:75], a[124:127], v[120:123], v[72:75]
ds_read_b128 v[92:95], v17 offset:39936
v_mfma_f32_16x16x32_bf16 v[72:75], a[136:139], v[124:127], v[72:75]
v_mfma_f32_16x16x32_bf16 v[76:79], a[80:83], v[104:107], v[76:79]
ds_read_b128 v[96:99], v17 offset:40960
v_mfma_f32_16x16x32_bf16 v[76:79], a[92:95], v[108:111], v[76:79]
v_mfma_f32_16x16x32_bf16 v[76:79], a[104:107], v[112:115], v[76:79]
ds_read_b128 v[100:103], v17 offset:41984
v_mfma_f32_16x16x32_bf16 v[76:79], a[116:119], v[116:119], v[76:79]
v_mfma_f32_16x16x32_bf16 v[76:79], a[128:131], v[120:123], v[76:79]
v_mfma_f32_16x16x32_bf16 v[76:79], a[140:143], v[124:127], v[76:79]
s_waitcnt lgkmcnt(5)
v_mfma_f32_16x16x32_bf16 v[68:71], a[144:147], v[80:83], v[68:71]
s_waitcnt lgkmcnt(4)
v_mfma_f32_16x16x32_bf16 v[68:71], a[156:159], v[84:87], v[68:71]
s_waitcnt lgkmcnt(3)
v_mfma_f32_16x16x32_bf16 v[68:71], a[168:171], v[88:91], v[68:71]
.Ldn_in11:
s_waitcnt lgkmcnt(2)
v_mfma_f32_16x16x32_bf16 v[68:71], a[180:183], v[92:95], v[68:71]
s_waitcnt lgkmcnt(1)
v_mfma_f32_16x16x32_bf16 v[68:71], a[192:195], v[96:99], v[68:71]
s_waitcnt lgkmcnt(0)
ds_read_b128 v[104:107], v17 offset:43008
v_mfma_f32_16x16x32_bf16 v[68:71], a[204:207], v[100:103], v[68:71]
s_add_u32 m0, s45, 24576
v_mfma_f32_16x16x32_bf16 v[72:75], a[148:151], v[80:83], v[72:75]
ds_read_b128 v[108:111], v17 offset:44032
ds_read_b128 v[112:115], v17 offset:45056
v_mfma_f32_16x16x32_bf16 v[72:75], a[160:163], v[84:87], v[72:75]
v_mfma_f32_16x16x32_bf16 v[72:75], a[172:175], v[88:91], v[72:75]
s_barrier
v_mfma_f32_16x16x32_bf16 v[72:75], a[184:187], v[92:95], v[72:75]
v_mfma_f32_16x16x32_bf16 v[72:75], a[196:199], v[96:99], v[72:75]
ds_read_b128 v[116:119], v17 offset:46080
v_mfma_f32_16x16x32_bf16 v[72:75], a[208:211], v[100:103], v[72:75]
global_load_lds_dwordx4 v9, s[38:39] offset:-2048 sc1
v_mfma_f32_16x16x32_bf16 v[76:79], a[152:155], v[80:83], v[76:79]
ds_read_b128 v[120:123], v17 offset:47104
v_mfma_f32_16x16x32_bf16 v[76:79], a[164:167], v[84:87], v[76:79]
v_mfma_f32_16x16x32_bf16 v[76:79], a[176:179], v[88:91], v[76:79]
ds_read_b128 v[124:127], v17 offset:48128
global_load_lds_dwordx4 v9, s[38:39] offset:-1024 sc1
v_mfma_f32_16x16x32_bf16 v[76:79], a[188:191], v[92:95], v[76:79]
v_mfma_f32_16x16x32_bf16 v[76:79], a[200:203], v[96:99], v[76:79]
v_mfma_f32_16x16x32_bf16 v[76:79], a[212:215], v[100:103], v[76:79]
s_waitcnt lgkmcnt(5)
global_load_lds_dwordx4 v9, s[38:39] offset:0 sc1
v_mfma_f32_16x16x32_bf16 v[68:71], a[216:219], v[104:107], v[68:71]
s_waitcnt lgkmcnt(4)
v_mfma_f32_16x16x32_bf16 v[68:71], a[228:231], v[108:111], v[68:71]
s_waitcnt lgkmcnt(3)
v_mfma_f32_16x16x32_bf16 v[68:71], a[240:243], v[112:115], v[68:71]
s_waitcnt lgkmcnt(2)
global_load_lds_dwordx4 v9, s[38:39] offset:1024 sc1
v_mfma_f32_16x16x32_bf16 v[68:71], a[252:255], v[116:119], v[68:71]
s_waitcnt lgkmcnt(1)
v_mfma_f32_16x16x32_bf16 v[68:71], v[32:35], v[120:123], v[68:71]
s_waitcnt lgkmcnt(0)
v_mfma_f32_16x16x32_bf16 v[68:71], v[44:47], v[124:127], v[68:71]
v_xor_b32 v17, 0x10000, v17
global_load_lds_dwordx4 v9, s[38:39] offset:2048 sc1
v_mfma_f32_16x16x32_bf16 v[72:75], a[220:223], v[104:107], v[72:75]
v_mfma_f32_16x16x32_bf16 v[72:75], a[232:235], v[108:111], v[72:75]
v_mfma_f32_16x16x32_bf16 v[72:75], a[244:247], v[112:115], v[72:75]
s_waitcnt vmcnt(8)
s_barrier
v_mfma_f32_16x16x32_bf16 v[72:75], v[24:27], v[116:119], v[72:75]
ds_read_b128 v[80:83], v17 offset:0
global_load_lds_dwordx4 v9, s[38:39] offset:3072 sc1
v_mfma_f32_16x16x32_bf16 v[72:75], v[36:39], v[120:123], v[72:75]
ds_read_b128 v[84:87], v17 offset:1024
v_add_u32 v9, 147456, v9
v_mfma_f32_16x16x32_bf16 v[72:75], v[48:51], v[124:127], v[72:75]
ds_read_b128 v[88:91], v17 offset:2048
v_min_u32 v9, v9, v10
v_mfma_f32_16x16x32_bf16 v[76:79], a[224:227], v[104:107], v[76:79]
ds_read_b128 v[92:95], v17 offset:3072
v_cvt_pk_bf16_f32 v128, v68, v69
v_mfma_f32_16x16x32_bf16 v[76:79], a[236:239], v[108:111], v[76:79]
ds_read_b128 v[96:99], v17 offset:4096
v_cvt_pk_bf16_f32 v129, v70, v71
v_mfma_f32_16x16x32_bf16 v[76:79], a[248:251], v[112:115], v[76:79]
ds_read_b128 v[100:103], v17 offset:5120
v_mfma_f32_16x16x32_bf16 v[76:79], v[28:31], v[116:119], v[76:79]
v_cvt_pk_bf16_f32 v130, v72, v73
v_cvt_pk_bf16_f32 v131, v74, v75
v_mfma_f32_16x16x32_bf16 v[76:79], v[40:43], v[120:123], v[76:79]
v_mfma_f32_16x16x32_bf16 v[76:79], v[52:55], v[124:127], v[76:79]
s_sub_u32 s70, s70, 1
s_cbranch_scc0 .Ldn_top12
v_cvt_pk_bf16_f32 v128, v68, v69
v_cvt_pk_bf16_f32 v129, v70, v71
v_cvt_pk_bf16_f32 v130, v72, v73
v_cvt_pk_bf16_f32 v131, v74, v75
global_store_dwordx4 v11, v[128:131], s[38:39] sc1
v_add_u32 v11, 110592, v11
v_cvt_pk_bf16_f32 v132, v76, v77
v_cvt_pk_bf16_f32 v134, v76, v77
v_cvt_pk_bf16_f32 v133, v78, v79
v_cvt_pk_bf16_f32 v135, v78, v79
v_permlane16_swap_b32 v132, v134
s_nop 0
v_permlane16_swap_b32 v133, v135
s_mov_b64 exec, s[76:77]
global_store_dwordx4 v12, v[132:135], s[38:39] sc1
s_mov_b64 exec, -1
v_add_u32 v12, 110592, v12
s_waitcnt vmcnt(2)
s_barrier
s_mov_b64 exec, 1
global_store_dword v252, v16, s[38:39] sc1
s_mov_b64 exec, -1
v_add_u32 v16, 1, v16
v_add_lshl_u32 v252, v16, 0, 7
v_bfi_b32 v252, s69, v252, v15
s_waitcnt vmcnt(1)
s_barrier
s_mov_b64 exec, 1
global_store_dword v252, v16, s[38:39] sc1
s_mov_b64 exec, -1
s_endpgm
.Ldn_w4:
v_add_u32 v254, -1, v14
v_add_u32 v255, -1, v14
v_add_lshl_u32 v252, v16, 0, 7
v_bfi_b32 v252, s69, v252, v15
v_add_lshl_u32 v253, v14, 0, 7
v_bfi_b32 v253, s69, v253, v13
s_mov_b32 s76, 0xffff
s_mov_b32 s77, 0xffff
s_add_u32 m0, s45, 24576
v_add_lshl_u32 v253, v14, 0, 7
v_bfi_b32 v253, s69, v253, v13
global_load_dword v254, v253, s[38:39] sc1
s_waitcnt vmcnt(0)
v_sub_u32 v254, v254, v14
v_cmp_lt_u32 vcc, 0x7f, v254
s_cbranch_vccnz .Ldn_sp19
.Ldn_rt20:
v_add_u32 v14, 1, v14
s_xor_b32 s45, s45, 0x10000
s_mov_b32 m0, s45
s_barrier
global_load_lds_dwordx4 v9, s[38:39] offset:-2048 sc1
global_load_lds_dwordx4 v9, s[38:39] offset:-1024 sc1
global_load_lds_dwordx4 v9, s[38:39] offset:0 sc1
global_load_lds_dwordx4 v9, s[38:39] offset:1024 sc1
global_load_lds_dwordx4 v9, s[38:39] offset:2048 sc1
global_load_lds_dwordx4 v9, s[38:39] offset:3072 sc1
v_add_u32 v9, 147456, v9
s_mov_b32 s70, 32
s_waitcnt vmcnt(0) lgkmcnt(0)
s_nop 7
s_branch .Ldn_in18
.p2align 6
.Ldn_top21:
s_waitcnt lgkmcnt(5)
v_mfma_f32_16x16x32_bf16 v[56:59], a[0:3], v[80:83], 0
s_waitcnt lgkmcnt(4)
v_mfma_f32_16x16x32_bf16 v[56:59], a[12:15], v[84:87], v[56:59]
s_waitcnt lgkmcnt(3)
global_load_dword v254, v253, s[38:39] sc1
v_mfma_f32_16x16x32_bf16 v[56:59], a[24:27], v[88:91], v[56:59]
s_waitcnt lgkmcnt(2)
v_mfma_f32_16x16x32_bf16 v[56:59], a[36:39], v[92:95], v[56:59]
s_waitcnt lgkmcnt(1)
ds_read_b128 v[104:107], v17 offset:6144
v_cvt_pk_bf16_f32 v132, v76, v77
v_mfma_f32_16x16x32_bf16 v[56:59], a[48:51], v[96:99], v[56:59]
s_waitcnt lgkmcnt(1)
v_mfma_f32_16x16x32_bf16 v[56:59], a[60:63], v[100:103], v[56:59]
s_waitcnt vmcnt(7)
s_barrier
v_mfma_f32_16x16x32_bf16 v[60:63], a[4:7], v[80:83], 0
ds_read_b128 v[108:111], v17 offset:7168
v_cvt_pk_bf16_f32 v134, v76, v77
v_mfma_f32_16x16x32_bf16 v[60:63], a[16:19], v[84:87], v[60:63]
v_cvt_pk_bf16_f32 v133, v78, v79
v_cvt_pk_bf16_f32 v135, v78, v79
v_mfma_f32_16x16x32_bf16 v[60:63], a[28:31], v[88:91], v[60:63]
ds_read_b128 v[112:115], v17 offset:8192
v_mfma_f32_16x16x32_bf16 v[60:63], a[40:43], v[92:95], v[60:63]
v_permlane16_swap_b32 v132, v134
v_mfma_f32_16x16x32_bf16 v[60:63], a[52:55], v[96:99], v[60:63]
ds_read_b128 v[116:119], v17 offset:9216
v_mfma_f32_16x16x32_bf16 v[60:63], a[64:67], v[100:103], v[60:63]
global_store_dwordx4 v11, v[128:131], s[38:39] sc1
v_mfma_f32_16x16x32_bf16 v[64:67], a[8:11], v[80:83], 0
ds_read_b128 v[120:123], v17 offset:10240
v_add_u32 v11, 110592, v11
v_mfma_f32_16x16x32_bf16 v[64:67], a[20:23], v[84:87], v[64:67]
v_permlane16_swap_b32 v133, v135
v_mfma_f32_16x16x32_bf16 v[64:67], a[32:35], v[88:91], v[64:67]
ds_read_b128 v[124:127], v17 offset:11264
v_mfma_f32_16x16x32_bf16 v[64:67], a[44:47], v[92:95], v[64:67]
s_mov_b64 exec, s[76:77]
global_store_dwordx4 v12, v[132:135], s[38:39] sc1
s_mov_b64 exec, -1
v_mfma_f32_16x16x32_bf16 v[64:67], a[56:59], v[96:99], v[64:67]
v_add_u32 v12, 110592, v12
v_mfma_f32_16x16x32_bf16 v[64:67], a[68:71], v[100:103], v[64:67]
s_waitcnt lgkmcnt(5)
v_mfma_f32_16x16x32_bf16 v[56:59], a[72:75], v[104:107], v[56:59]
s_waitcnt lgkmcnt(4)
v_mfma_f32_16x16x32_bf16 v[56:59], a[84:87], v[108:111], v[56:59]
s_waitcnt lgkmcnt(3)
v_mfma_f32_16x16x32_bf16 v[56:59], a[96:99], v[112:115], v[56:59]
s_waitcnt lgkmcnt(2)
v_mfma_f32_16x16x32_bf16 v[56:59], a[108:111], v[116:119], v[56:59]
s_waitcnt lgkmcnt(1)
v_mfma_f32_16x16x32_bf16 v[56:59], a[120:123], v[120:123], v[56:59]
s_waitcnt lgkmcnt(0)
ds_read_b128 v[80:83], v17 offset:12288
v_mfma_f32_16x16x32_bf16 v[56:59], a[132:135], v[124:127], v[56:59]
v_mfma_f32_16x16x32_bf16 v[60:63], a[76:79], v[104:107], v[60:63]
ds_read_b128 v[84:87], v17 offset:13312
v_mfma_f32_16x16x32_bf16 v[60:63], a[88:91], v[108:111], v[60:63]
v_mfma_f32_16x16x32_bf16 v[60:63], a[100:103], v[112:115], v[60:63]
ds_read_b128 v[88:91], v17 offset:14336
v_mfma_f32_16x16x32_bf16 v[60:63], a[112:115], v[116:119], v[60:63]
v_mfma_f32_16x16x32_bf16 v[60:63], a[124:127], v[120:123], v[60:63]
ds_read_b128 v[92:95], v17 offset:15360
v_mfma_f32_16x16x32_bf16 v[60:63], a[136:139], v[124:127], v[60:63]
v_mfma_f32_16x16x32_bf16 v[64:67], a[80:83], v[104:107], v[64:67]
ds_read_b128 v[96:99], v17 offset:16384
v_mfma_f32_16x16x32_bf16 v[64:67], a[92:95], v[108:111], v[64:67]
v_mfma_f32_16x16x32_bf16 v[64:67], a[104:107], v[112:115], v[64:67]
ds_read_b128 v[100:103], v17 offset:17408
v_mfma_f32_16x16x32_bf16 v[64:67], a[116:119], v[116:119], v[64:67]
v_mfma_f32_16x16x32_bf16 v[64:67], a[128:131], v[120:123], v[64:67]
v_mfma_f32_16x16x32_bf16 v[64:67], a[140:143], v[124:127], v[64:67]
s_waitcnt lgkmcnt(5)
v_mfma_f32_16x16x32_bf16 v[56:59], a[144:147], v[80:83], v[56:59]
s_waitcnt lgkmcnt(4)
v_mfma_f32_16x16x32_bf16 v[56:59], a[156:159], v[84:87], v[56:59]
s_waitcnt lgkmcnt(3)
v_mfma_f32_16x16x32_bf16 v[56:59], a[168:171], v[88:91], v[56:59]
s_waitcnt lgkmcnt(2)
v_mfma_f32_16x16x32_bf16 v[56:59], a[180:183], v[92:95], v[56:59]
s_waitcnt vmcnt(2)
v_sub_u32 v254, v254, v14
v_cmp_lt_u32 vcc, 0x7f, v254
s_waitcnt lgkmcnt(1)
v_mfma_f32_16x16x32_bf16 v[56:59], a[192:195], v[96:99], v[56:59]
s_xor_b32 s45, s45, 0x10000
ds_read_b128 v[104:107], v17 offset:18432
s_waitcnt lgkmcnt(1)
v_mfma_f32_16x16x32_bf16 v[56:59], a[204:207], v[100:103], v[56:59]
s_add_u32 m0, s45, 0
v_mfma_f32_16x16x32_bf16 v[60:63], a[148:151], v[80:83], v[60:63]
s_cbranch_vccnz .Ldn_sp14
.Ldn_rt15:
ds_read_b128 v[108:111], v17 offset:19456
ds_read_b128 v[112:115], v17 offset:20480
v_mfma_f32_16x16x32_bf16 v[60:63], a[160:163], v[84:87], v[60:63]
v_mfma_f32_16x16x32_bf16 v[60:63], a[172:175], v[88:91], v[60:63]
s_barrier
v_mfma_f32_16x16x32_bf16 v[60:63], a[184:187], v[92:95], v[60:63]
v_add_u32 v14, 1, v14
v_add_lshl_u32 v253, v14, 0, 7
v_mfma_f32_16x16x32_bf16 v[60:63], a[196:199], v[96:99], v[60:63]
ds_read_b128 v[116:119], v17 offset:21504
v_bfi_b32 v253, s69, v253, v13
v_mfma_f32_16x16x32_bf16 v[60:63], a[208:211], v[100:103], v[60:63]
v_mfma_f32_16x16x32_bf16 v[64:67], a[152:155], v[80:83], v[64:67]
ds_read_b128 v[120:123], v17 offset:22528
global_load_lds_dwordx4 v9, s[38:39] offset:-2048 sc1
v_mfma_f32_16x16x32_bf16 v[64:67], a[164:167], v[84:87], v[64:67]
v_mfma_f32_16x16x32_bf16 v[64:67], a[176:179], v[88:91], v[64:67]
ds_read_b128 v[124:127], v17 offset:23552
v_mfma_f32_16x16x32_bf16 v[64:67], a[188:191], v[92:95], v[64:67]
global_load_lds_dwordx4 v9, s[38:39] offset:-1024 sc1
v_mfma_f32_16x16x32_bf16 v[64:67], a[200:203], v[96:99], v[64:67]
v_mfma_f32_16x16x32_bf16 v[64:67], a[212:215], v[100:103], v[64:67]
s_waitcnt lgkmcnt(5)
v_mfma_f32_16x16x32_bf16 v[56:59], a[216:219], v[104:107], v[56:59]
s_waitcnt lgkmcnt(4)
global_load_lds_dwordx4 v9, s[38:39] offset:0 sc1
v_mfma_f32_16x16x32_bf16 v[56:59], a[228:231], v[108:111], v[56:59]
s_waitcnt lgkmcnt(3)
v_mfma_f32_16x16x32_bf16 v[56:59], a[240:243], v[112:115], v[56:59]
s_waitcnt lgkmcnt(2)
v_mfma_f32_16x16x32_bf16 v[56:59], a[252:255], v[116:119], v[56:59]
s_waitcnt lgkmcnt(1)
global_load_lds_dwordx4 v9, s[38:39] offset:1024 sc1
v_mfma_f32_16x16x32_bf16 v[56:59], v[32:35], v[120:123], v[56:59]
s_waitcnt lgkmcnt(0)
v_mfma_f32_16x16x32_bf16 v[56:59], v[44:47], v[124:127], v[56:59]
v_mfma_f32_16x16x32_bf16 v[60:63], a[220:223], v[104:107], v[60:63]
global_load_lds_dwordx4 v9, s[38:39] offset:2048 sc1
v_mfma_f32_16x16x32_bf16 v[60:63], a[232:235], v[108:111], v[60:63]
v_mfma_f32_16x16x32_bf16 v[60:63], a[244:247], v[112:115], v[60:63]
s_barrier
v_mfma_f32_16x16x32_bf16 v[60:63], v[24:27], v[116:119], v[60:63]
ds_read_b128 v[80:83], v17 offset:24576
global_load_lds_dwordx4 v9, s[38:39] offset:3072 sc1
v_mfma_f32_16x16x32_bf16 v[60:63], v[36:39], v[120:123], v[60:63]
ds_read_b128 v[84:87], v17 offset:25600
v_add_u32 v9, 147456, v9
v_mfma_f32_16x16x32_bf16 v[60:63], v[48:51], v[124:127], v[60:63]
ds_read_b128 v[88:91], v17 offset:26624
v_min_u32 v9, v9, v10
v_mfma_f32_16x16x32_bf16 v[64:67], a[224:227], v[104:107], v[64:67]
ds_read_b128 v[92:95], v17 offset:27648
v_cvt_pk_bf16_f32 v128, v56, v57
v_mfma_f32_16x16x32_bf16 v[64:67], a[236:239], v[108:111], v[64:67]
ds_read_b128 v[96:99], v17 offset:28672
v_cvt_pk_bf16_f32 v129, v58, v59
v_mfma_f32_16x16x32_bf16 v[64:67], a[248:251], v[112:115], v[64:67]
ds_read_b128 v[100:103], v17 offset:29696
v_mfma_f32_16x16x32_bf16 v[64:67], v[28:31], v[116:119], v[64:67]
v_cvt_pk_bf16_f32 v130, v60, v61
v_cvt_pk_bf16_f32 v131, v62, v63
v_mfma_f32_16x16x32_bf16 v[64:67], v[40:43], v[120:123], v[64:67]
v_mfma_f32_16x16x32_bf16 v[64:67], v[52:55], v[124:127], v[64:67]
s_waitcnt lgkmcnt(5)
v_mfma_f32_16x16x32_bf16 v[68:71], a[0:3], v[80:83], 0
s_waitcnt lgkmcnt(4)
v_mfma_f32_16x16x32_bf16 v[68:71], a[12:15], v[84:87], v[68:71]
s_waitcnt lgkmcnt(3)
global_load_dword v255, v253, s[38:39] sc1
v_mfma_f32_16x16x32_bf16 v[68:71], a[24:27], v[88:91], v[68:71]
s_waitcnt lgkmcnt(2)
v_mfma_f32_16x16x32_bf16 v[68:71], a[36:39], v[92:95], v[68:71]
)"
R"(s_waitcnt lgkmcnt(1)
ds_read_b128 v[104:107], v17 offset:30720
v_cvt_pk_bf16_f32 v132, v64, v65
v_mfma_f32_16x16x32_bf16 v[68:71], a[48:51], v[96:99], v[68:71]
s_waitcnt lgkmcnt(1)
v_mfma_f32_16x16x32_bf16 v[68:71], a[60:63], v[100:103], v[68:71]
s_waitcnt vmcnt(7)
s_barrier
v_mfma_f32_16x16x32_bf16 v[72:75], a[4:7], v[80:83], 0
ds_read_b128 v[108:111], v17 offset:31744
v_cvt_pk_bf16_f32 v134, v64, v65
v_mfma_f32_16x16x32_bf16 v[72:75], a[16:19], v[84:87], v[72:75]
v_cvt_pk_bf16_f32 v133, v66, v67
v_cvt_pk_bf16_f32 v135, v66, v67
v_mfma_f32_16x16x32_bf16 v[72:75], a[28:31], v[88:91], v[72:75]
ds_read_b128 v[112:115], v17 offset:32768
v_mfma_f32_16x16x32_bf16 v[72:75], a[40:43], v[92:95], v[72:75]
v_permlane16_swap_b32 v132, v134
v_mfma_f32_16x16x32_bf16 v[72:75], a[52:55], v[96:99], v[72:75]
ds_read_b128 v[116:119], v17 offset:33792
v_mfma_f32_16x16x32_bf16 v[72:75], a[64:67], v[100:103], v[72:75]
global_store_dwordx4 v11, v[128:131], s[38:39] sc1
v_mfma_f32_16x16x32_bf16 v[76:79], a[8:11], v[80:83], 0
ds_read_b128 v[120:123], v17 offset:34816
v_add_u32 v11, 110592, v11
v_mfma_f32_16x16x32_bf16 v[76:79], a[20:23], v[84:87], v[76:79]
v_permlane16_swap_b32 v133, v135
v_mfma_f32_16x16x32_bf16 v[76:79], a[32:35], v[88:91], v[76:79]
ds_read_b128 v[124:127], v17 offset:35840
v_mfma_f32_16x16x32_bf16 v[76:79], a[44:47], v[92:95], v[76:79]
s_mov_b64 exec, s[76:77]
global_store_dwordx4 v12, v[132:135], s[38:39] sc1
s_mov_b64 exec, -1
v_mfma_f32_16x16x32_bf16 v[76:79], a[56:59], v[96:99], v[76:79]
v_add_u32 v12, 110592, v12
v_mfma_f32_16x16x32_bf16 v[76:79], a[68:71], v[100:103], v[76:79]
s_waitcnt lgkmcnt(5)
v_mfma_f32_16x16x32_bf16 v[68:71], a[72:75], v[104:107], v[68:71]
s_waitcnt lgkmcnt(4)
v_mfma_f32_16x16x32_bf16 v[68:71], a[84:87], v[108:111], v[68:71]
s_waitcnt lgkmcnt(3)
v_mfma_f32_16x16x32_bf16 v[68:71], a[96:99], v[112:115], v[68:71]
s_waitcnt lgkmcnt(2)
v_mfma_f32_16x16x32_bf16 v[68:71], a[108:111], v[116:119], v[68:71]
s_waitcnt lgkmcnt(1)
v_mfma_f32_16x16x32_bf16 v[68:71], a[120:123], v[120:123], v[68:71]
s_waitcnt lgkmcnt(0)
ds_read_b128 v[80:83], v17 offset:36864
v_mfma_f32_16x16x32_bf16 v[68:71], a[132:135], v[124:127], v[68:71]
v_mfma_f32_16x16x32_bf16 v[72:75], a[76:79], v[104:107], v[72:75]
ds_read_b128 v[84:87], v17 offset:37888
v_mfma_f32_16x16x32_bf16 v[72:75], a[88:91], v[108:111], v[72:75]
v_mfma_f32_16x16x32_bf16 v[72:75], a[100:103], v[112:115], v[72:75]
ds_read_b128 v[88:91], v17 offset:38912
v_mfma_f32_16x16x32_bf16 v[72:75], a[112:115], v[116:119], v[72:75]
v_mfma_f32_16x16x32_bf16 v[72:75], a[124:127], v[120:123], v[72:75]
ds_read_b128 v[92:95], v17 offset:39936
v_mfma_f32_16x16x32_bf16 v[72:75], a[136:139], v[124:127], v[72:75]
v_mfma_f32_16x16x32_bf16 v[76:79], a[80:83], v[104:107], v[76:79]
ds_read_b128 v[96:99], v17 offset:40960
v_mfma_f32_16x16x32_bf16 v[76:79], a[92:95], v[108:111], v[76:79]
v_mfma_f32_16x16x32_bf16 v[76:79], a[104:107], v[112:115], v[76:79]
ds_read_b128 v[100:103], v17 offset:41984
v_mfma_f32_16x16x32_bf16 v[76:79], a[116:119], v[116:119], v[76:79]
v_mfma_f32_16x16x32_bf16 v[76:79], a[128:131], v[120:123], v[76:79]
v_mfma_f32_16x16x32_bf16 v[76:79], a[140:143], v[124:127], v[76:79]
s_waitcnt lgkmcnt(5)
v_mfma_f32_16x16x32_bf16 v[68:71], a[144:147], v[80:83], v[68:71]
s_waitcnt lgkmcnt(4)
v_mfma_f32_16x16x32_bf16 v[68:71], a[156:159], v[84:87], v[68:71]
s_waitcnt lgkmcnt(3)
v_mfma_f32_16x16x32_bf16 v[68:71], a[168:171], v[88:91], v[68:71]
.Ldn_in18:
s_waitcnt lgkmcnt(2)
v_mfma_f32_16x16x32_bf16 v[68:71], a[180:183], v[92:95], v[68:71]
s_waitcnt vmcnt(2)
v_sub_u32 v255, v255, v14
v_cmp_lt_u32 vcc, 0x7f, v255
s_waitcnt lgkmcnt(1)
v_mfma_f32_16x16x32_bf16 v[68:71], a[192:195], v[96:99], v[68:71]
s_waitcnt lgkmcnt(0)
ds_read_b128 v[104:107], v17 offset:43008
v_mfma_f32_16x16x32_bf16 v[68:71], a[204:207], v[100:103], v[68:71]
s_add_u32 m0, s45, 24576
v_mfma_f32_16x16x32_bf16 v[72:75], a[148:151], v[80:83], v[72:75]
s_cbranch_vccnz .Ldn_sp16
.Ldn_rt17:
ds_read_b128 v[108:111], v17 offset:44032
ds_read_b128 v[112:115], v17 offset:45056
v_mfma_f32_16x16x32_bf16 v[72:75], a[160:163], v[84:87], v[72:75]
v_mfma_f32_16x16x32_bf16 v[72:75], a[172:175], v[88:91], v[72:75]
s_barrier
v_mfma_f32_16x16x32_bf16 v[72:75], a[184:187], v[92:95], v[72:75]
v_add_u32 v14, 1, v14
v_add_lshl_u32 v253, v14, 0, 7
v_mfma_f32_16x16x32_bf16 v[72:75], a[196:199], v[96:99], v[72:75]
ds_read_b128 v[116:119], v17 offset:46080
v_bfi_b32 v253, s69, v253, v13
v_mfma_f32_16x16x32_bf16 v[72:75], a[208:211], v[100:103], v[72:75]
v_mfma_f32_16x16x32_bf16 v[76:79], a[152:155], v[80:83], v[76:79]
ds_read_b128 v[120:123], v17 offset:47104
global_load_lds_dwordx4 v9, s[38:39] offset:-2048 sc1
v_mfma_f32_16x16x32_bf16 v[76:79], a[164:167], v[84:87], v[76:79]
v_mfma_f32_16x16x32_bf16 v[76:79], a[176:179], v[88:91], v[76:79]
ds_read_b128 v[124:127], v17 offset:48128
v_mfma_f32_16x16x32_bf16 v[76:79], a[188:191], v[92:95], v[76:79]
global_load_lds_dwordx4 v9, s[38:39] offset:-1024 sc1
v_mfma_f32_16x16x32_bf16 v[76:79], a[200:203], v[96:99], v[76:79]
v_mfma_f32_16x16x32_bf16 v[76:79], a[212:215], v[100:103], v[76:79]
s_waitcnt lgkmcnt(5)
v_mfma_f32_16x16x32_bf16 v[68:71], a[216:219], v[104:107], v[68:71]
s_waitcnt lgkmcnt(4)
global_load_lds_dwordx4 v9, s[38:39] offset:0 sc1
v_mfma_f32_16x16x32_bf16 v[68:71], a[228:231], v[108:111], v[68:71]
s_waitcnt lgkmcnt(3)
v_mfma_f32_16x16x32_bf16 v[68:71], a[240:243], v[112:115], v[68:71]
s_waitcnt lgkmcnt(2)
v_mfma_f32_16x16x32_bf16 v[68:71], a[252:255], v[116:119], v[68:71]
s_waitcnt lgkmcnt(1)
global_load_lds_dwordx4 v9, s[38:39] offset:1024 sc1
v_mfma_f32_16x16x32_bf16 v[68:71], v[32:35], v[120:123], v[68:71]
s_waitcnt lgkmcnt(0)
v_mfma_f32_16x16x32_bf16 v[68:71], v[44:47], v[124:127], v[68:71]
v_xor_b32 v17, 0x10000, v17
v_mfma_f32_16x16x32_bf16 v[72:75], a[220:223], v[104:107], v[72:75]
global_load_lds_dwordx4 v9, s[38:39] offset:2048 sc1
v_mfma_f32_16x16x32_bf16 v[72:75], a[232:235], v[108:111], v[72:75]
v_mfma_f32_16x16x32_bf16 v[72:75], a[244:247], v[112:115], v[72:75]
s_barrier
v_mfma_f32_16x16x32_bf16 v[72:75], v[24:27], v[116:119], v[72:75]
ds_read_b128 v[80:83], v17 offset:0
global_load_lds_dwordx4 v9, s[38:39] offset:3072 sc1
v_mfma_f32_16x16x32_bf16 v[72:75], v[36:39], v[120:123], v[72:75]
ds_read_b128 v[84:87], v17 offset:1024
v_add_u32 v9, 147456, v9
v_mfma_f32_16x16x32_bf16 v[72:75], v[48:51], v[124:127], v[72:75]
ds_read_b128 v[88:91], v17 offset:2048
v_min_u32 v9, v9, v10
v_mfma_f32_16x16x32_bf16 v[76:79], a[224:227], v[104:107], v[76:79]
ds_read_b128 v[92:95], v17 offset:3072
v_cvt_pk_bf16_f32 v128, v68, v69
v_mfma_f32_16x16x32_bf16 v[76:79], a[236:239], v[108:111], v[76:79]
ds_read_b128 v[96:99], v17 offset:4096
v_cvt_pk_bf16_f32 v129, v70, v71
v_mfma_f32_16x16x32_bf16 v[76:79], a[248:251], v[112:115], v[76:79]
ds_read_b128 v[100:103], v17 offset:5120
v_mfma_f32_16x16x32_bf16 v[76:79], v[28:31], v[116:119], v[76:79]
v_cvt_pk_bf16_f32 v130, v72, v73
v_cvt_pk_bf16_f32 v131, v74, v75
v_mfma_f32_16x16x32_bf16 v[76:79], v[40:43], v[120:123], v[76:79]
v_mfma_f32_16x16x32_bf16 v[76:79], v[52:55], v[124:127], v[76:79]
s_sub_u32 s70, s70, 1
s_cbranch_scc0 .Ldn_top21
v_cvt_pk_bf16_f32 v128, v68, v69
v_cvt_pk_bf16_f32 v129, v70, v71
v_cvt_pk_bf16_f32 v130, v72, v73
v_cvt_pk_bf16_f32 v131, v74, v75
global_store_dwordx4 v11, v[128:131], s[38:39] sc1
v_add_u32 v11, 110592, v11
v_cvt_pk_bf16_f32 v132, v76, v77
v_cvt_pk_bf16_f32 v134, v76, v77
v_cvt_pk_bf16_f32 v133, v78, v79
v_cvt_pk_bf16_f32 v135, v78, v79
v_permlane16_swap_b32 v132, v134
s_nop 0
v_permlane16_swap_b32 v133, v135
s_mov_b64 exec, s[76:77]
global_store_dwordx4 v12, v[132:135], s[38:39] sc1
s_mov_b64 exec, -1
v_add_u32 v12, 110592, v12
s_waitcnt vmcnt(2)
s_barrier
s_waitcnt vmcnt(0)
s_barrier
s_endpgm
.Ldn_sp14:
s_sleep 1
v_add_lshl_u32 v253, v14, 0, 7
v_bfi_b32 v253, s69, v253, v13
global_load_dword v254, v253, s[38:39] sc1
s_add_u32 s71, s71, 1
s_waitcnt vmcnt(0)
v_sub_u32 v254, v254, v14
v_cmp_lt_u32 vcc, 0x7f, v254
s_cbranch_vccz .Ldn_rt15
s_cmp_lt_u32 s71, 3000000
s_cbranch_scc1 .Ldn_sp14
s_branch .Ldn_rt15
.Ldn_sp16:
s_sleep 1
v_add_lshl_u32 v253, v14, 0, 7
v_bfi_b32 v253, s69, v253, v13
global_load_dword v255, v253, s[38:39] sc1
s_add_u32 s71, s71, 1
s_waitcnt vmcnt(0)
v_sub_u32 v255, v255, v14
v_cmp_lt_u32 vcc, 0x7f, v255
s_cbranch_vccz .Ldn_rt17
s_cmp_lt_u32 s71, 3000000
s_cbranch_scc1 .Ldn_sp16
s_branch .Ldn_rt17
.Ldn_sp19:
s_sleep 1
v_add_lshl_u32 v253, v14, 0, 7
v_bfi_b32 v253, s69, v253, v13
global_load_dword v254, v253, s[38:39] sc1
s_add_u32 s71, s71, 1
s_waitcnt vmcnt(0)
v_sub_u32 v254, v254, v14
v_cmp_lt_u32 vcc, 0x7f, v254
s_cbranch_vccz .Ldn_rt20
s_cmp_lt_u32 s71, 3000000
s_cbranch_scc1 .Ldn_sp19
s_branch .Ldn_rt20
.Le2:
)"
    :: "{v[0:31]}"(va), "{s[36:51]}"(sa) : "v255", "a255", "s99", "vcc", "memory");
  } else {
    const unsigned gu_i = xh * 36 + (ev ? jj + 4 : jj - 12);
    const unsigned r = lane & 15, g = lane >> 4, q = r >> 2, p = lane & 3;
    const unsigned ii = gu_i, gam = (ii % 72) >> 1, rho = ii / 72 * 2 + (ii & 1), row0 = rho * RP + PAD, pc0 = wave * 6;
    const unsigned bk = gam >> 2, qd = gam & 3, od = (lane >> 3) & 1, un = g * 8 + (lane & 7), jr = qd * 4 + p, jc = (un >> 2) * 8 + od * 4;
    u32x32 va; u32x16 sa, sb;
    va[0] = g * 9216 + ((r ^ (g << 2)) << 4) + 4096 + (((r ^ (g << 2)) >> 3) & 1) * 640;
    va[1] = (8 * g + q) * 256 + (((4 * wave + p) ^ (q << 2)) << 4);
    va[2] = WS_X1 + row0 * X1P + pc0 * 1024 + 2048 + lane * 16;
    va[3] = WS_H + (row0 - 1) * HT + (gam / 6 * 24 + (gam % 6) * 2 + (wave >> 1) * 12 + (wave & 1)) * 1024 + lane * 16;
    va[4] = WS_FL + rho * FR + FX1 + (lane & 1) * FL;
    va[5] = 0u;
    va[6] = WS_FL + rho * FR + FH + gam * FL;
    va[7] = 0u - 2;
    va[8] = lane * 16;
    va[9] = WS_X1 + (row0 + 63) * X1P + pc0 * 1024 + 2048 + lane * 16;
    va[10] = WS_P + row0 * PT + gam * 3072 + od * 512 + un * 16;
    va[11] = WS_X1 + row0 * X1P + bk * 2048 + (un >> 2) * 256 + jr * 16 + od * 8;
    va[12] = WS_X1 + row0 * X1P + XT + jr * 8;
    va[13] = ((rho * 32 + 31) * 16 + qd * 4 + g) * 1152 + bk * 128 + r * 8;
    va[14] = 49152 + wave * 1024 + p * 144 + jc * 2;
    va[15] = 49152 + wave * 1024 + g * 144 + r * 8;
    va[16] = WS_FL + rho * FR + FP + (bk / 3 * 6 + min(lane, 5u)) * FL;
    va[17] = 0u;
    va[18] = WS_FL + rho * FR + FX2 + gam * FL;
    va[19] = 0u;
    va[20] = 0u;
    va[21] = 0u;
    va[22] = (g == q && p < 2) ? (0x3F80u << (16 * p)) : 0u;
    va[23] = (g == q && p >= 2) ? (0x3F80u << (16 * (p - 2))) : 0u;
    va[24] = 0u;
    va[25] = 0u;
    sa[0] = 12 + wave; sa[2] = (unsigned)(unsigned long long)ws; sa[3] = (unsigned)((unsigned long long)ws >> 32);
    sa[4] = uni((unsigned)(unsigned long long)(wg + gam / 6 * 1536 + (gam % 6) * 128 + wave * (16 * 9216)));
    sa[5] = uni((unsigned)((unsigned long long)(wg + gam / 6 * 1536 + (gam % 6) * 128 + wave * (16 * 9216)) >> 32));
    sa[6] = uni((unsigned)(unsigned long long)(wu + gam / 6 * 1536 + (gam % 6) * 128 + wave * (16 * 9216)));
    sa[7] = uni((unsigned)((unsigned long long)(wu + gam / 6 * 1536 + (gam % 6) * 128 + wave * (16 * 9216)) >> 32));
    sa[8] = uni(wave * 4096);
    sa[9] = uni(pc0 * 1024 + 2048);
    sa[10] = uni((unsigned)(unsigned long long)(ws + WS_X2));
    sa[11] = uni((unsigned)((unsigned long long)(ws + WS_X2) >> 32));
    sa[12] = uni((unsigned)(unsigned long long)(out + 32 * XT));
    sa[13] = uni((unsigned)((unsigned long long)(out + 32 * XT) >> 32));
    sa[14] = uni((unsigned)(unsigned long long)(ws + WS_XD));
    sa[15] = uni((unsigned)((unsigned long long)(ws + WS_XD) >> 32));
    sb[0] = uni((unsigned)(unsigned long long)(ws - PT));
    sb[1] = uni((unsigned)((unsigned long long)(ws - PT) >> 32));
    sb[2] = uni((unsigned)(unsigned long long)(ws - X1P));
    sb[3] = uni((unsigned)((unsigned long long)(ws - X1P) >> 32));
    sb[4] = uni((unsigned)(unsigned long long)(ws + WS_X2));
    sb[5] = uni((unsigned)((unsigned long long)(ws + WS_X2) >> 32));
    sb[6] = uni((unsigned)(unsigned long long)(ws + WS_XD));
    sb[7] = uni((unsigned)((unsigned long long)(ws + WS_XD) >> 32));
    sb[8] = uni((unsigned)(unsigned long long)(ws + WS_XD));
    sb[9] = uni((unsigned)((unsigned long long)(ws + WS_XD) >> 32));
    sb[10] = uni(0u);
    asm volatile(
R"(.Lb3:
s_movk_i32 s69, 0x380
s_mov_b32 s71, 0
s_mov_b32 s74, 0
s_load_dword s37, s[38:39], 0x0
s_nop 1
v_add_u32 v166, 35840, v0
v_add_u32 v167, 71680, v0
v_add_u32 v168, 107520, v0
s_sleep 20
s_add_u32 m0, s44, 4096
s_nop 0
global_load_lds_dwordx4 v0, s[40:41] offset:-4096 nt
global_load_lds_dwordx4 v166, s[40:41] offset:-3072 nt
global_load_lds_dwordx4 v167, s[40:41] offset:-2048 nt
global_load_lds_dwordx4 v168, s[40:41] offset:-1024 nt
s_add_u32 s40, s40, 589824
s_addc_u32 s41, s41, 0
s_add_u32 m0, s44, 20480
s_nop 0
global_load_lds_dwordx4 v0, s[40:41] offset:-4096 nt
global_load_lds_dwordx4 v166, s[40:41] offset:-3072 nt
global_load_lds_dwordx4 v167, s[40:41] offset:-2048 nt
global_load_lds_dwordx4 v168, s[40:41] offset:-1024 nt
s_add_u32 s40, s40, 589824
s_addc_u32 s41, s41, 0
s_add_u32 m0, s44, 36864
s_nop 0
global_load_lds_dwordx4 v0, s[40:41] offset:-4096 nt
global_load_lds_dwordx4 v166, s[40:41] offset:-3072 nt
global_load_lds_dwordx4 v167, s[40:41] offset:-2048 nt
global_load_lds_dwordx4 v168, s[40:41] offset:-1024 nt
s_add_u32 s40, s40, 589824
s_addc_u32 s41, s41, 0
v_add_u32 v169, 0x10000, v1
v_mov_b32 v186, 0x3F803F80
v_mov_b32 v187, 0x3F803F80
v_mov_b32 v188, 0x3F803F80
v_mov_b32 v189, 0x3F803F80
s_waitcnt lgkmcnt(0)
s_mul_i32 s37, s37, 0x9E3779B1
s_add_u32 s37, s37, 0x7F4A7C15
v_add_u32 v5, s37, v5
v_add_u32 v7, s37, v7
v_add_u32 v17, s37, v17
s_waitcnt vmcnt(8)
s_barrier
s_add_u32 m0, s44, 53248
ds_read_b64_tr_b16 a[0:1], v1 offset:0
ds_read_b64_tr_b16 a[2:3], v1 offset:1024
global_load_lds_dwordx4 v0, s[40:41] offset:-4096 nt
ds_read_b64_tr_b16 a[4:5], v1 offset:8
global_load_lds_dwordx4 v166, s[40:41] offset:-3072 nt
ds_read_b64_tr_b16 a[6:7], v1 offset:1032
global_load_lds_dwordx4 v167, s[40:41] offset:-2048 nt
ds_read_b64_tr_b16 a[8:9], v1 offset:8192
global_load_lds_dwordx4 v168, s[40:41] offset:-1024 nt
ds_read_b64_tr_b16 a[10:11], v1 offset:9216
s_add_u32 s40, s40, 589824
ds_read_b64_tr_b16 a[12:13], v1 offset:8200
s_addc_u32 s41, s41, 0
ds_read_b64_tr_b16 a[14:15], v1 offset:9224
s_waitcnt vmcnt(8) lgkmcnt(0)
s_barrier
s_add_u32 m0, s44, 4096
ds_read_b64_tr_b16 a[16:17], v1 offset:16384
ds_read_b64_tr_b16 a[18:19], v1 offset:17408
global_load_lds_dwordx4 v0, s[40:41] offset:-4096 nt
ds_read_b64_tr_b16 a[20:21], v1 offset:16392
ds_read_b64_tr_b16 a[22:23], v1 offset:17416
global_load_lds_dwordx4 v166, s[40:41] offset:-3072 nt
ds_read_b64_tr_b16 a[24:25], v1 offset:24576
ds_read_b64_tr_b16 a[26:27], v1 offset:25600
global_load_lds_dwordx4 v167, s[40:41] offset:-2048 nt
ds_read_b64_tr_b16 a[28:29], v1 offset:24584
global_load_lds_dwordx4 v168, s[40:41] offset:-1024 nt
ds_read_b64_tr_b16 a[30:31], v1 offset:25608
v_mfma_f32_16x16x32_bf16 v[170:173], v[186:189], a[0:3], 0
s_add_u32 s40, s40, 589824
v_mfma_f32_16x16x32_bf16 v[174:177], v[186:189], a[4:7], 0
v_mfma_f32_16x16x32_bf16 v[170:173], v[186:189], a[8:11], v[170:173]
s_addc_u32 s41, s41, 0
v_mfma_f32_16x16x32_bf16 v[174:177], v[186:189], a[12:15], v[174:177]
s_waitcnt vmcnt(8) lgkmcnt(0)
s_barrier
s_add_u32 m0, s44, 20480
ds_read_b64_tr_b16 a[32:33], v1 offset:32768
ds_read_b64_tr_b16 a[34:35], v1 offset:33792
global_load_lds_dwordx4 v0, s[40:41] offset:-4096 nt
ds_read_b64_tr_b16 a[36:37], v1 offset:32776
ds_read_b64_tr_b16 a[38:39], v1 offset:33800
global_load_lds_dwordx4 v166, s[40:41] offset:-3072 nt
ds_read_b64_tr_b16 a[40:41], v1 offset:40960
ds_read_b64_tr_b16 a[42:43], v1 offset:41984
global_load_lds_dwordx4 v167, s[40:41] offset:-2048 nt
ds_read_b64_tr_b16 a[44:45], v1 offset:40968
global_load_lds_dwordx4 v168, s[40:41] offset:-1024 nt
ds_read_b64_tr_b16 a[46:47], v1 offset:41992
v_mfma_f32_16x16x32_bf16 v[170:173], v[186:189], a[16:19], v[170:173]
s_add_u32 s40, s40, 589824
v_mfma_f32_16x16x32_bf16 v[174:177], v[186:189], a[20:23], v[174:177]
v_mfma_f32_16x16x32_bf16 v[170:173], v[186:189], a[24:27], v[170:173]
s_addc_u32 s41, s41, 0
v_mfma_f32_16x16x32_bf16 v[174:177], v[186:189], a[28:31], v[174:177]
s_waitcnt vmcnt(8) lgkmcnt(0)
s_barrier
s_add_u32 m0, s44, 36864
ds_read_b64_tr_b16 a[48:49], v1 offset:49152
ds_read_b64_tr_b16 a[50:51], v1 offset:50176
global_load_lds_dwordx4 v0, s[40:41] offset:-4096 nt
ds_read_b64_tr_b16 a[52:53], v1 offset:49160
ds_read_b64_tr_b16 a[54:55], v1 offset:50184
global_load_lds_dwordx4 v166, s[40:41] offset:-3072 nt
ds_read_b64_tr_b16 a[56:57], v1 offset:57344
ds_read_b64_tr_b16 a[58:59], v1 offset:58368
global_load_lds_dwordx4 v167, s[40:41] offset:-2048 nt
ds_read_b64_tr_b16 a[60:61], v1 offset:57352
global_load_lds_dwordx4 v168, s[40:41] offset:-1024 nt
ds_read_b64_tr_b16 a[62:63], v1 offset:58376
v_mfma_f32_16x16x32_bf16 v[170:173], v[186:189], a[32:35], v[170:173]
s_add_u32 s40, s40, 589824
v_mfma_f32_16x16x32_bf16 v[174:177], v[186:189], a[36:39], v[174:177]
v_mfma_f32_16x16x32_bf16 v[170:173], v[186:189], a[40:43], v[170:173]
s_addc_u32 s41, s41, 0
v_mfma_f32_16x16x32_bf16 v[174:177], v[186:189], a[44:47], v[174:177]
s_waitcnt vmcnt(8) lgkmcnt(0)
s_barrier
s_add_u32 m0, s44, 53248
ds_read_b64_tr_b16 a[64:65], v1 offset:0
ds_read_b64_tr_b16 a[66:67], v1 offset:1024
global_load_lds_dwordx4 v0, s[40:41] offset:-4096 nt
ds_read_b64_tr_b16 a[68:69], v1 offset:8
ds_read_b64_tr_b16 a[70:71], v1 offset:1032
global_load_lds_dwordx4 v166, s[40:41] offset:-3072 nt
ds_read_b64_tr_b16 a[72:73], v1 offset:8192
ds_read_b64_tr_b16 a[74:75], v1 offset:9216
global_load_lds_dwordx4 v167, s[40:41] offset:-2048 nt
ds_read_b64_tr_b16 a[76:77], v1 offset:8200
global_load_lds_dwordx4 v168, s[40:41] offset:-1024 nt
ds_read_b64_tr_b16 a[78:79], v1 offset:9224
v_mfma_f32_16x16x32_bf16 v[170:173], v[186:189], a[48:51], v[170:173]
s_add_u32 s40, s40, 589824
v_mfma_f32_16x16x32_bf16 v[174:177], v[186:189], a[52:55], v[174:177]
v_mfma_f32_16x16x32_bf16 v[170:173], v[186:189], a[56:59], v[170:173]
s_addc_u32 s41, s41, 0
v_mfma_f32_16x16x32_bf16 v[174:177], v[186:189], a[60:63], v[174:177]
s_waitcnt vmcnt(8) lgkmcnt(0)
s_barrier
s_add_u32 m0, s44, 4096
ds_read_b64_tr_b16 a[80:81], v1 offset:16384
ds_read_b64_tr_b16 a[82:83], v1 offset:17408
global_load_lds_dwordx4 v0, s[40:41] offset:-4096 nt
ds_read_b64_tr_b16 a[84:85], v1 offset:16392
ds_read_b64_tr_b16 a[86:87], v1 offset:17416
global_load_lds_dwordx4 v166, s[40:41] offset:-3072 nt
ds_read_b64_tr_b16 a[88:89], v1 offset:24576
ds_read_b64_tr_b16 a[90:91], v1 offset:25600
global_load_lds_dwordx4 v167, s[40:41] offset:-2048 nt
ds_read_b64_tr_b16 a[92:93], v1 offset:24584
ds_read_b64_tr_b16 a[94:95], v1 offset:25608
global_load_lds_dwordx4 v168, s[40:41] offset:-1024 nt
v_mfma_f32_16x16x32_bf16 v[170:173], v[186:189], a[64:67], v[170:173]
v_mfma_f32_16x16x32_bf16 v[174:177], v[186:189], a[68:71], v[174:177]
s_mov_b64 s[40:41], s[42:43]
v_mfma_f32_16x16x32_bf16 v[170:173], v[186:189], a[72:75], v[170:173]
v_mfma_f32_16x16x32_bf16 v[174:177], v[186:189], a[76:79], v[174:177]
s_waitcnt vmcnt(8) lgkmcnt(0)
s_barrier
s_add_u32 m0, s44, 20480
ds_read_b64_tr_b16 a[96:97], v1 offset:32768
ds_read_b64_tr_b16 a[98:99], v1 offset:33792
global_load_lds_dwordx4 v0, s[40:41] offset:-4096 nt
ds_read_b64_tr_b16 a[100:101], v1 offset:32776
ds_read_b64_tr_b16 a[102:103], v1 offset:33800
global_load_lds_dwordx4 v166, s[40:41] offset:-3072 nt
ds_read_b64_tr_b16 a[104:105], v1 offset:40960
ds_read_b64_tr_b16 a[106:107], v1 offset:41984
global_load_lds_dwordx4 v167, s[40:41] offset:-2048 nt
ds_read_b64_tr_b16 a[108:109], v1 offset:40968
global_load_lds_dwordx4 v168, s[40:41] offset:-1024 nt
ds_read_b64_tr_b16 a[110:111], v1 offset:41992
v_mfma_f32_16x16x32_bf16 v[170:173], v[186:189], a[80:83], v[170:173]
s_add_u32 s40, s40, 589824
v_mfma_f32_16x16x32_bf16 v[174:177], v[186:189], a[84:87], v[174:177]
v_mfma_f32_16x16x32_bf16 v[170:173], v[186:189], a[88:91], v[170:173]
s_addc_u32 s41, s41, 0
v_mfma_f32_16x16x32_bf16 v[174:177], v[186:189], a[92:95], v[174:177]
s_waitcnt vmcnt(8) lgkmcnt(0)
s_barrier
s_add_u32 m0, s44, 36864
ds_read_b64_tr_b16 a[112:113], v1 offset:49152
ds_read_b64_tr_b16 a[114:115], v1 offset:50176
global_load_lds_dwordx4 v0, s[40:41] offset:-4096 nt
ds_read_b64_tr_b16 a[116:117], v1 offset:49160
ds_read_b64_tr_b16 a[118:119], v1 offset:50184
global_load_lds_dwordx4 v166, s[40:41] offset:-3072 nt
ds_read_b64_tr_b16 a[120:121], v1 offset:57344
ds_read_b64_tr_b16 a[122:123], v1 offset:58368
global_load_lds_dwordx4 v167, s[40:41] offset:-2048 nt
ds_read_b64_tr_b16 a[124:125], v1 offset:57352
global_load_lds_dwordx4 v168, s[40:41] offset:-1024 nt
ds_read_b64_tr_b16 a[126:127], v1 offset:58376
v_mfma_f32_16x16x32_bf16 v[170:173], v[186:189], a[96:99], v[170:173]
s_add_u32 s40, s40, 589824
v_mfma_f32_16x16x32_bf16 v[174:177], v[186:189], a[100:103], v[174:177]
v_mfma_f32_16x16x32_bf16 v[170:173], v[186:189], a[104:107], v[170:173]
s_addc_u32 s41, s41, 0
v_mfma_f32_16x16x32_bf16 v[174:177], v[186:189], a[108:111], v[174:177]
s_waitcnt vmcnt(8) lgkmcnt(0)
s_barrier
s_add_u32 m0, s44, 53248
ds_read_b64_tr_b16 a[128:129], v1 offset:0
ds_read_b64_tr_b16 a[130:131], v1 offset:1024
global_load_lds_dwordx4 v0, s[40:41] offset:-4096 nt
ds_read_b64_tr_b16 a[132:133], v1 offset:8
ds_read_b64_tr_b16 a[134:135], v1 offset:1032
global_load_lds_dwordx4 v166, s[40:41] offset:-3072 nt
ds_read_b64_tr_b16 a[136:137], v1 offset:8192
ds_read_b64_tr_b16 a[138:139], v1 offset:9216
global_load_lds_dwordx4 v167, s[40:41] offset:-2048 nt
ds_read_b64_tr_b16 a[140:141], v1 offset:8200
global_load_lds_dwordx4 v168, s[40:41] offset:-1024 nt
ds_read_b64_tr_b16 a[142:143], v1 offset:9224
v_mfma_f32_16x16x32_bf16 v[170:173], v[186:189], a[112:115], v[170:173]
s_add_u32 s40, s40, 589824
v_mfma_f32_16x16x32_bf16 v[174:177], v[186:189], a[116:119], v[174:177]
v_mfma_f32_16x16x32_bf16 v[170:173], v[186:189], a[120:123], v[170:173]
s_addc_u32 s41, s41, 0
v_mfma_f32_16x16x32_bf16 v[174:177], v[186:189], a[124:127], v[174:177]
s_waitcnt vmcnt(8) lgkmcnt(0)
s_barrier
s_add_u32 m0, s44, 4096
ds_read_b64_tr_b16 a[144:145], v1 offset:16384
ds_read_b64_tr_b16 a[146:147], v1 offset:17408
global_load_lds_dwordx4 v0, s[40:41] offset:-4096 nt
ds_read_b64_tr_b16 a[148:149], v1 offset:16392
ds_read_b64_tr_b16 a[150:151], v1 offset:17416
global_load_lds_dwordx4 v166, s[40:41] offset:-3072 nt
ds_read_b64_tr_b16 a[152:153], v1 offset:24576
ds_read_b64_tr_b16 a[154:155], v1 offset:25600
global_load_lds_dwordx4 v167, s[40:41] offset:-2048 nt
ds_read_b64_tr_b16 a[156:157], v1 offset:24584
global_load_lds_dwordx4 v168, s[40:41] offset:-1024 nt
ds_read_b64_tr_b16 a[158:159], v1 offset:25608
v_mfma_f32_16x16x32_bf16 v[170:173], v[186:189], a[128:131], v[170:173]
s_add_u32 s40, s40, 589824
v_mfma_f32_16x16x32_bf16 v[174:177], v[186:189], a[132:135], v[174:177]
v_mfma_f32_16x16x32_bf16 v[170:173], v[186:189], a[136:139], v[170:173]
s_addc_u32 s41, s41, 0
v_mfma_f32_16x16x32_bf16 v[174:177], v[186:189], a[140:143], v[174:177]
s_waitcnt vmcnt(8) lgkmcnt(0)
s_barrier
s_add_u32 m0, s44, 20480
ds_read_b64_tr_b16 a[160:161], v1 offset:32768
v_mfma_f32_16x16x32_bf16 v[178:181], v[186:189], a[144:147], 0
v_mfma_f32_16x16x32_bf16 v[182:185], v[186:189], a[148:151], 0
v_mfma_f32_16x16x32_bf16 v[178:181], v[186:189], a[152:155], v[178:181]
v_mfma_f32_16x16x32_bf16 v[182:185], v[186:189], a[156:159], v[182:185]
v_mul_f32 v170, 0xbae38e39, v170
v_mul_f32 v171, 0xbae38e39, v171
ds_read_b64_tr_b16 a[162:163], v1 offset:33792
v_mul_f32 v172, 0xbae38e39, v172
global_load_lds_dwordx4 v0, s[40:41] offset:-4096 nt
v_mul_f32 v173, 0xbae38e39, v173
v_mul_f32 v174, 0xbae38e39, v174
v_mul_f32 v175, 0xbae38e39, v175
v_mul_f32 v176, 0xbae38e39, v176
v_mul_f32 v177, 0xbae38e39, v177
ds_read_b64_tr_b16 a[164:165], v1 offset:32776
v_mfma_f32_16x16x32_bf16 v[190:193], v[22:25], a[0:3], v[170:173]
v_mfma_f32_16x16x32_bf16 v[194:197], v[20:23], a[0:3], v[170:173]
global_load_lds_dwordx4 v166, s[40:41] offset:-3072 nt
v_mfma_f32_16x16x32_bf16 v[198:201], v[22:25], a[4:7], v[174:177]
v_mfma_f32_16x16x32_bf16 v[202:205], v[20:23], a[4:7], v[174:177]
v_mfma_f32_16x16x32_bf16 v[206:209], v[22:25], a[8:11], v[170:173]
s_nop 2
v_cvt_pk_bf16_f32 v222, v190, v191
ds_read_b64_tr_b16 a[166:167], v1 offset:33800
v_cvt_pk_bf16_f32 v223, v192, v193
v_cvt_pk_bf16_f32 v224, v194, v195
v_cvt_pk_bf16_f32 v225, v196, v197
global_load_lds_dwordx4 v167, s[40:41] offset:-2048 nt
v_mfma_f32_16x16x32_bf16 v[210:213], v[20:23], a[8:11], v[170:173]
v_accvgpr_write_b32 a0, v222
ds_read_b64_tr_b16 a[168:169], v1 offset:40960
v_accvgpr_write_b32 a1, v223
v_accvgpr_write_b32 a2, v224
v_accvgpr_write_b32 a3, v225
v_mfma_f32_16x16x32_bf16 v[214:217], v[22:25], a[12:15], v[174:177]
v_cvt_pk_bf16_f32 v226, v198, v199
v_cvt_pk_bf16_f32 v227, v200, v201
global_load_lds_dwordx4 v168, s[40:41] offset:-1024 nt
ds_read_b64_tr_b16 a[170:171], v1 offset:41984
v_cvt_pk_bf16_f32 v228, v202, v203
v_cvt_pk_bf16_f32 v229, v204, v205
v_mfma_f32_16x16x32_bf16 v[218:221], v[20:23], a[12:15], v[174:177]
s_add_u32 s40, s40, 589824
v_accvgpr_write_b32 a4, v226
v_accvgpr_write_b32 a5, v227
v_accvgpr_write_b32 a6, v228
ds_read_b64_tr_b16 a[172:173], v1 offset:40968
v_accvgpr_write_b32 a7, v229
v_mfma_f32_16x16x32_bf16 v[190:193], v[22:25], a[16:19], v[170:173]
v_cvt_pk_bf16_f32 v222, v206, v207
v_cvt_pk_bf16_f32 v223, v208, v209
v_cvt_pk_bf16_f32 v224, v210, v211
v_cvt_pk_bf16_f32 v225, v212, v213
ds_read_b64_tr_b16 a[174:175], v1 offset:41992
v_mfma_f32_16x16x32_bf16 v[194:197], v[20:23], a[16:19], v[170:173]
s_addc_u32 s41, s41, 0
v_accvgpr_write_b32 a8, v222
v_accvgpr_write_b32 a9, v223
v_accvgpr_write_b32 a10, v224
v_accvgpr_write_b32 a11, v225
v_mfma_f32_16x16x32_bf16 v[198:201], v[22:25], a[20:23], v[174:177]
s_waitcnt vmcnt(8) lgkmcnt(0)
v_cvt_pk_bf16_f32 v226, v214, v215
v_cvt_pk_bf16_f32 v227, v216, v217
v_cvt_pk_bf16_f32 v228, v218, v219
s_barrier
s_add_u32 m0, s44, 36864
ds_read_b64_tr_b16 a[176:177], v1 offset:49152
v_mfma_f32_16x16x32_bf16 v[178:181], v[186:189], a[160:163], v[178:181]
v_mfma_f32_16x16x32_bf16 v[182:185], v[186:189], a[164:167], v[182:185]
v_mfma_f32_16x16x32_bf16 v[178:181], v[186:189], a[168:171], v[178:181]
v_mfma_f32_16x16x32_bf16 v[182:185], v[186:189], a[172:175], v[182:185]
v_cvt_pk_bf16_f32 v229, v220, v221
v_mfma_f32_16x16x32_bf16 v[202:205], v[20:23], a[20:23], v[174:177]
ds_read_b64_tr_b16 a[178:179], v1 offset:50176
v_accvgpr_write_b32 a12, v226
global_load_lds_dwordx4 v0, s[40:41] offset:-4096 nt
v_accvgpr_write_b32 a13, v227
v_accvgpr_write_b32 a14, v228
v_accvgpr_write_b32 a15, v229
v_mfma_f32_16x16x32_bf16 v[206:209], v[22:25], a[24:27], v[170:173]
v_cvt_pk_bf16_f32 v222, v190, v191
ds_read_b64_tr_b16 a[180:181], v1 offset:49160
v_cvt_pk_bf16_f32 v223, v192, v193
v_cvt_pk_bf16_f32 v224, v194, v195
global_load_lds_dwordx4 v166, s[40:41] offset:-3072 nt
v_cvt_pk_bf16_f32 v225, v196, v197
v_mfma_f32_16x16x32_bf16 v[210:213], v[20:23], a[24:27], v[170:173]
v_accvgpr_write_b32 a16, v222
v_accvgpr_write_b32 a17, v223
ds_read_b64_tr_b16 a[182:183], v1 offset:50184
v_accvgpr_write_b32 a18, v224
v_accvgpr_write_b32 a19, v225
v_mfma_f32_16x16x32_bf16 v[214:217], v[22:25], a[28:31], v[174:177]
global_load_lds_dwordx4 v167, s[40:41] offset:-2048 nt
v_cvt_pk_bf16_f32 v226, v198, v199
v_cvt_pk_bf16_f32 v227, v200, v201
ds_read_b64_tr_b16 a[184:185], v1 offset:57344
v_cvt_pk_bf16_f32 v228, v202, v203
v_cvt_pk_bf16_f32 v229, v204, v205
v_mfma_f32_16x16x32_bf16 v[218:221], v[20:23], a[28:31], v[174:177]
v_accvgpr_write_b32 a20, v226
v_accvgpr_write_b32 a21, v227
v_accvgpr_write_b32 a22, v228
global_load_lds_dwordx4 v168, s[40:41] offset:-1024 nt
ds_read_b64_tr_b16 a[186:187], v1 offset:58368
v_accvgpr_write_b32 a23, v229
v_mfma_f32_16x16x32_bf16 v[190:193], v[22:25], a[32:35], v[170:173]
s_add_u32 s40, s40, 589824
v_cvt_pk_bf16_f32 v222, v206, v207
v_cvt_pk_bf16_f32 v223, v208, v209
v_cvt_pk_bf16_f32 v224, v210, v211
v_cvt_pk_bf16_f32 v225, v212, v213
ds_read_b64_tr_b16 a[188:189], v1 offset:57352
v_mfma_f32_16x16x32_bf16 v[194:197], v[20:23], a[32:35], v[170:173]
v_accvgpr_write_b32 a24, v222
v_accvgpr_write_b32 a25, v223
v_accvgpr_write_b32 a26, v224
v_accvgpr_write_b32 a27, v225
v_mfma_f32_16x16x32_bf16 v[198:201], v[22:25], a[36:39], v[174:177]
s_addc_u32 s41, s41, 0
ds_read_b64_tr_b16 a[190:191], v1 offset:58376
v_cvt_pk_bf16_f32 v226, v214, v215
v_cvt_pk_bf16_f32 v227, v216, v217
v_cvt_pk_bf16_f32 v228, v218, v219
v_cvt_pk_bf16_f32 v229, v220, v221
v_mfma_f32_16x16x32_bf16 v[202:205], v[20:23], a[36:39], v[174:177]
s_waitcnt vmcnt(8) lgkmcnt(0)
v_accvgpr_write_b32 a28, v226
v_accvgpr_write_b32 a29, v227
v_accvgpr_write_b32 a30, v228
v_accvgpr_write_b32 a31, v229
s_barrier
s_add_u32 m0, s44, 53248
ds_read_b64_tr_b16 a[192:193], v1 offset:0
v_mfma_f32_16x16x32_bf16 v[178:181], v[186:189], a[176:179], v[178:181]
v_mfma_f32_16x16x32_bf16 v[182:185], v[186:189], a[180:183], v[182:185]
v_mfma_f32_16x16x32_bf16 v[178:181], v[186:189], a[184:187], v[178:181]
v_mfma_f32_16x16x32_bf16 v[182:185], v[186:189], a[188:191], v[182:185]
v_mfma_f32_16x16x32_bf16 v[206:209], v[22:25], a[40:43], v[170:173]
v_cvt_pk_bf16_f32 v222, v190, v191
ds_read_b64_tr_b16 a[194:195], v1 offset:1024
v_cvt_pk_bf16_f32 v223, v192, v193
global_load_lds_dwordx4 v0, s[40:41] offset:-4096 nt
v_cvt_pk_bf16_f32 v224, v194, v195
v_cvt_pk_bf16_f32 v225, v196, v197
)"
R"(v_mfma_f32_16x16x32_bf16 v[210:213], v[20:23], a[40:43], v[170:173]
v_accvgpr_write_b32 a32, v222
v_accvgpr_write_b32 a33, v223
ds_read_b64_tr_b16 a[196:197], v1 offset:8
v_accvgpr_write_b32 a34, v224
v_accvgpr_write_b32 a35, v225
global_load_lds_dwordx4 v166, s[40:41] offset:-3072 nt
v_mfma_f32_16x16x32_bf16 v[214:217], v[22:25], a[44:47], v[174:177]
v_cvt_pk_bf16_f32 v226, v198, v199
v_cvt_pk_bf16_f32 v227, v200, v201
v_cvt_pk_bf16_f32 v228, v202, v203
ds_read_b64_tr_b16 a[198:199], v1 offset:1032
v_cvt_pk_bf16_f32 v229, v204, v205
v_mfma_f32_16x16x32_bf16 v[218:221], v[20:23], a[44:47], v[174:177]
v_accvgpr_write_b32 a36, v226
global_load_lds_dwordx4 v167, s[40:41] offset:-2048 nt
v_accvgpr_write_b32 a37, v227
v_accvgpr_write_b32 a38, v228
ds_read_b64_tr_b16 a[200:201], v1 offset:8192
v_accvgpr_write_b32 a39, v229
v_mfma_f32_16x16x32_bf16 v[190:193], v[22:25], a[48:51], v[170:173]
v_cvt_pk_bf16_f32 v222, v206, v207
v_cvt_pk_bf16_f32 v223, v208, v209
v_cvt_pk_bf16_f32 v224, v210, v211
v_cvt_pk_bf16_f32 v225, v212, v213
global_load_lds_dwordx4 v168, s[40:41] offset:-1024 nt
ds_read_b64_tr_b16 a[202:203], v1 offset:9216
v_mfma_f32_16x16x32_bf16 v[194:197], v[20:23], a[48:51], v[170:173]
v_accvgpr_write_b32 a40, v222
v_accvgpr_write_b32 a41, v223
v_accvgpr_write_b32 a42, v224
v_accvgpr_write_b32 a43, v225
v_mfma_f32_16x16x32_bf16 v[198:201], v[22:25], a[52:55], v[174:177]
s_add_u32 s40, s40, 589824
ds_read_b64_tr_b16 a[204:205], v1 offset:8200
v_cvt_pk_bf16_f32 v226, v214, v215
v_cvt_pk_bf16_f32 v227, v216, v217
v_cvt_pk_bf16_f32 v228, v218, v219
v_cvt_pk_bf16_f32 v229, v220, v221
v_mfma_f32_16x16x32_bf16 v[202:205], v[20:23], a[52:55], v[174:177]
s_addc_u32 s41, s41, 0
v_accvgpr_write_b32 a44, v226
ds_read_b64_tr_b16 a[206:207], v1 offset:9224
v_accvgpr_write_b32 a45, v227
v_accvgpr_write_b32 a46, v228
v_accvgpr_write_b32 a47, v229
v_mfma_f32_16x16x32_bf16 v[206:209], v[22:25], a[56:59], v[170:173]
v_cvt_pk_bf16_f32 v222, v190, v191
v_cvt_pk_bf16_f32 v223, v192, v193
v_cvt_pk_bf16_f32 v224, v194, v195
v_cvt_pk_bf16_f32 v225, v196, v197
v_mfma_f32_16x16x32_bf16 v[210:213], v[20:23], a[56:59], v[170:173]
s_waitcnt vmcnt(8) lgkmcnt(0)
s_barrier
s_add_u32 m0, s44, 4096
ds_read_b64_tr_b16 a[208:209], v1 offset:16384
v_mfma_f32_16x16x32_bf16 v[178:181], v[186:189], a[192:195], v[178:181]
v_mfma_f32_16x16x32_bf16 v[182:185], v[186:189], a[196:199], v[182:185]
v_mfma_f32_16x16x32_bf16 v[178:181], v[186:189], a[200:203], v[178:181]
v_mfma_f32_16x16x32_bf16 v[182:185], v[186:189], a[204:207], v[182:185]
v_accvgpr_write_b32 a48, v222
v_accvgpr_write_b32 a49, v223
ds_read_b64_tr_b16 a[210:211], v1 offset:17408
v_accvgpr_write_b32 a50, v224
global_load_lds_dwordx4 v0, s[40:41] offset:-4096 nt
v_accvgpr_write_b32 a51, v225
v_mfma_f32_16x16x32_bf16 v[214:217], v[22:25], a[60:63], v[174:177]
v_cvt_pk_bf16_f32 v226, v198, v199
v_cvt_pk_bf16_f32 v227, v200, v201
v_cvt_pk_bf16_f32 v228, v202, v203
ds_read_b64_tr_b16 a[212:213], v1 offset:16392
v_cvt_pk_bf16_f32 v229, v204, v205
v_mfma_f32_16x16x32_bf16 v[218:221], v[20:23], a[60:63], v[174:177]
global_load_lds_dwordx4 v166, s[40:41] offset:-3072 nt
v_accvgpr_write_b32 a52, v226
v_accvgpr_write_b32 a53, v227
v_accvgpr_write_b32 a54, v228
v_accvgpr_write_b32 a55, v229
ds_read_b64_tr_b16 a[214:215], v1 offset:17416
v_mfma_f32_16x16x32_bf16 v[190:193], v[22:25], a[64:67], v[170:173]
v_cvt_pk_bf16_f32 v222, v206, v207
v_cvt_pk_bf16_f32 v223, v208, v209
global_load_lds_dwordx4 v167, s[40:41] offset:-2048 nt
v_cvt_pk_bf16_f32 v224, v210, v211
v_cvt_pk_bf16_f32 v225, v212, v213
ds_read_b64_tr_b16 a[216:217], v1 offset:24576
v_mfma_f32_16x16x32_bf16 v[194:197], v[20:23], a[64:67], v[170:173]
v_accvgpr_write_b32 a56, v222
v_accvgpr_write_b32 a57, v223
v_accvgpr_write_b32 a58, v224
v_accvgpr_write_b32 a59, v225
v_mfma_f32_16x16x32_bf16 v[198:201], v[22:25], a[68:71], v[174:177]
global_load_lds_dwordx4 v168, s[40:41] offset:-1024 nt
ds_read_b64_tr_b16 a[218:219], v1 offset:25600
v_cvt_pk_bf16_f32 v226, v214, v215
v_cvt_pk_bf16_f32 v227, v216, v217
v_cvt_pk_bf16_f32 v228, v218, v219
v_cvt_pk_bf16_f32 v229, v220, v221
v_mfma_f32_16x16x32_bf16 v[202:205], v[20:23], a[68:71], v[174:177]
s_add_u32 s40, s40, 589824
v_accvgpr_write_b32 a60, v226
ds_read_b64_tr_b16 a[220:221], v1 offset:24584
v_accvgpr_write_b32 a61, v227
v_accvgpr_write_b32 a62, v228
v_accvgpr_write_b32 a63, v229
v_mfma_f32_16x16x32_bf16 v[206:209], v[22:25], a[72:75], v[170:173]
s_addc_u32 s41, s41, 0
v_cvt_pk_bf16_f32 v222, v190, v191
v_cvt_pk_bf16_f32 v223, v192, v193
ds_read_b64_tr_b16 a[222:223], v1 offset:25608
v_cvt_pk_bf16_f32 v224, v194, v195
v_cvt_pk_bf16_f32 v225, v196, v197
v_mfma_f32_16x16x32_bf16 v[210:213], v[20:23], a[72:75], v[170:173]
v_accvgpr_write_b32 a64, v222
v_accvgpr_write_b32 a65, v223
v_accvgpr_write_b32 a66, v224
v_accvgpr_write_b32 a67, v225
v_mfma_f32_16x16x32_bf16 v[214:217], v[22:25], a[76:79], v[174:177]
s_waitcnt vmcnt(8) lgkmcnt(0)
v_cvt_pk_bf16_f32 v226, v198, v199
s_barrier
s_add_u32 m0, s44, 20480
ds_read_b64_tr_b16 a[224:225], v1 offset:32768
v_mfma_f32_16x16x32_bf16 v[178:181], v[186:189], a[208:211], v[178:181]
v_mfma_f32_16x16x32_bf16 v[182:185], v[186:189], a[212:215], v[182:185]
v_mfma_f32_16x16x32_bf16 v[178:181], v[186:189], a[216:219], v[178:181]
v_mfma_f32_16x16x32_bf16 v[182:185], v[186:189], a[220:223], v[182:185]
v_cvt_pk_bf16_f32 v227, v200, v201
v_cvt_pk_bf16_f32 v228, v202, v203
ds_read_b64_tr_b16 a[226:227], v1 offset:33792
v_cvt_pk_bf16_f32 v229, v204, v205
global_load_lds_dwordx4 v0, s[40:41] offset:-4096 nt
v_mfma_f32_16x16x32_bf16 v[218:221], v[20:23], a[76:79], v[174:177]
v_accvgpr_write_b32 a68, v226
v_accvgpr_write_b32 a69, v227
v_accvgpr_write_b32 a70, v228
v_accvgpr_write_b32 a71, v229
ds_read_b64_tr_b16 a[228:229], v1 offset:32776
v_mfma_f32_16x16x32_bf16 v[190:193], v[22:25], a[80:83], v[170:173]
v_cvt_pk_bf16_f32 v222, v206, v207
global_load_lds_dwordx4 v166, s[40:41] offset:-3072 nt
v_cvt_pk_bf16_f32 v223, v208, v209
v_cvt_pk_bf16_f32 v224, v210, v211
v_cvt_pk_bf16_f32 v225, v212, v213
v_mfma_f32_16x16x32_bf16 v[194:197], v[20:23], a[80:83], v[170:173]
ds_read_b64_tr_b16 a[230:231], v1 offset:33800
v_accvgpr_write_b32 a72, v222
v_accvgpr_write_b32 a73, v223
v_accvgpr_write_b32 a74, v224
global_load_lds_dwordx4 v167, s[40:41] offset:-2048 nt
v_accvgpr_write_b32 a75, v225
v_mfma_f32_16x16x32_bf16 v[198:201], v[22:25], a[84:87], v[174:177]
ds_read_b64_tr_b16 a[232:233], v1 offset:40960
v_cvt_pk_bf16_f32 v226, v214, v215
v_cvt_pk_bf16_f32 v227, v216, v217
v_cvt_pk_bf16_f32 v228, v218, v219
v_cvt_pk_bf16_f32 v229, v220, v221
v_mfma_f32_16x16x32_bf16 v[202:205], v[20:23], a[84:87], v[174:177]
v_accvgpr_write_b32 a76, v226
global_load_lds_dwordx4 v168, s[40:41] offset:-1024 nt
ds_read_b64_tr_b16 a[234:235], v1 offset:41984
v_accvgpr_write_b32 a77, v227
v_accvgpr_write_b32 a78, v228
v_accvgpr_write_b32 a79, v229
v_mfma_f32_16x16x32_bf16 v[206:209], v[22:25], a[88:91], v[170:173]
s_sub_u32 s40, s40, 10027008
v_cvt_pk_bf16_f32 v222, v190, v191
v_cvt_pk_bf16_f32 v223, v192, v193
ds_read_b64_tr_b16 a[236:237], v1 offset:40968
v_cvt_pk_bf16_f32 v224, v194, v195
v_cvt_pk_bf16_f32 v225, v196, v197
v_mfma_f32_16x16x32_bf16 v[210:213], v[20:23], a[88:91], v[170:173]
s_subb_u32 s41, s41, 0
v_accvgpr_write_b32 a80, v222
v_accvgpr_write_b32 a81, v223
v_accvgpr_write_b32 a82, v224
ds_read_b64_tr_b16 a[238:239], v1 offset:41992
v_accvgpr_write_b32 a83, v225
v_mfma_f32_16x16x32_bf16 v[214:217], v[22:25], a[92:95], v[174:177]
v_cvt_pk_bf16_f32 v226, v198, v199
v_cvt_pk_bf16_f32 v227, v200, v201
v_cvt_pk_bf16_f32 v228, v202, v203
v_cvt_pk_bf16_f32 v229, v204, v205
v_mfma_f32_16x16x32_bf16 v[218:221], v[20:23], a[92:95], v[174:177]
s_waitcnt vmcnt(8) lgkmcnt(0)
v_accvgpr_write_b32 a84, v226
v_accvgpr_write_b32 a85, v227
s_barrier
ds_read_b64_tr_b16 v[26:27], v1 offset:49152
v_mfma_f32_16x16x32_bf16 v[178:181], v[186:189], a[224:227], v[178:181]
v_mfma_f32_16x16x32_bf16 v[182:185], v[186:189], a[228:231], v[182:185]
v_mfma_f32_16x16x32_bf16 v[178:181], v[186:189], a[232:235], v[178:181]
v_mfma_f32_16x16x32_bf16 v[182:185], v[186:189], a[236:239], v[182:185]
v_accvgpr_write_b32 a86, v228
v_accvgpr_write_b32 a87, v229
ds_read_b64_tr_b16 v[28:29], v1 offset:50176
v_mfma_f32_16x16x32_bf16 v[190:193], v[22:25], a[96:99], v[170:173]
v_cvt_pk_bf16_f32 v222, v206, v207
v_cvt_pk_bf16_f32 v223, v208, v209
v_cvt_pk_bf16_f32 v224, v210, v211
v_cvt_pk_bf16_f32 v225, v212, v213
v_mfma_f32_16x16x32_bf16 v[194:197], v[20:23], a[96:99], v[170:173]
ds_read_b64_tr_b16 v[30:31], v1 offset:49160
v_accvgpr_write_b32 a88, v222
v_accvgpr_write_b32 a89, v223
v_accvgpr_write_b32 a90, v224
v_accvgpr_write_b32 a91, v225
v_mfma_f32_16x16x32_bf16 v[198:201], v[22:25], a[100:103], v[174:177]
v_cvt_pk_bf16_f32 v226, v214, v215
ds_read_b64_tr_b16 v[32:33], v1 offset:50184
v_cvt_pk_bf16_f32 v227, v216, v217
v_cvt_pk_bf16_f32 v228, v218, v219
v_cvt_pk_bf16_f32 v229, v220, v221
v_mfma_f32_16x16x32_bf16 v[202:205], v[20:23], a[100:103], v[174:177]
v_accvgpr_write_b32 a92, v226
ds_read_b64_tr_b16 v[34:35], v1 offset:57344
v_accvgpr_write_b32 a93, v227
v_accvgpr_write_b32 a94, v228
v_accvgpr_write_b32 a95, v229
v_mfma_f32_16x16x32_bf16 v[206:209], v[22:25], a[104:107], v[170:173]
v_cvt_pk_bf16_f32 v222, v190, v191
v_cvt_pk_bf16_f32 v223, v192, v193
ds_read_b64_tr_b16 v[36:37], v1 offset:58368
v_cvt_pk_bf16_f32 v224, v194, v195
v_cvt_pk_bf16_f32 v225, v196, v197
v_mfma_f32_16x16x32_bf16 v[210:213], v[20:23], a[104:107], v[170:173]
v_accvgpr_write_b32 a96, v222
v_accvgpr_write_b32 a97, v223
v_accvgpr_write_b32 a98, v224
ds_read_b64_tr_b16 v[38:39], v1 offset:57352
v_accvgpr_write_b32 a99, v225
v_mfma_f32_16x16x32_bf16 v[214:217], v[22:25], a[108:111], v[174:177]
v_cvt_pk_bf16_f32 v226, v198, v199
v_cvt_pk_bf16_f32 v227, v200, v201
v_cvt_pk_bf16_f32 v228, v202, v203
v_cvt_pk_bf16_f32 v229, v204, v205
ds_read_b64_tr_b16 v[40:41], v1 offset:58376
v_mfma_f32_16x16x32_bf16 v[218:221], v[20:23], a[108:111], v[174:177]
v_accvgpr_write_b32 a100, v226
v_accvgpr_write_b32 a101, v227
v_accvgpr_write_b32 a102, v228
v_accvgpr_write_b32 a103, v229
v_mfma_f32_16x16x32_bf16 v[190:193], v[22:25], a[112:115], v[170:173]
s_waitcnt vmcnt(4) lgkmcnt(0)
v_cvt_pk_bf16_f32 v222, v206, v207
v_cvt_pk_bf16_f32 v223, v208, v209
v_cvt_pk_bf16_f32 v224, v210, v211
s_barrier
ds_read_b64_tr_b16 v[42:43], v1 offset:0
v_mfma_f32_16x16x32_bf16 v[178:181], v[186:189], v[26:29], v[178:181]
v_mfma_f32_16x16x32_bf16 v[182:185], v[186:189], v[30:33], v[182:185]
v_mfma_f32_16x16x32_bf16 v[178:181], v[186:189], v[34:37], v[178:181]
v_mfma_f32_16x16x32_bf16 v[182:185], v[186:189], v[38:41], v[182:185]
v_cvt_pk_bf16_f32 v225, v212, v213
v_mfma_f32_16x16x32_bf16 v[194:197], v[20:23], a[112:115], v[170:173]
ds_read_b64_tr_b16 v[44:45], v1 offset:1024
v_accvgpr_write_b32 a104, v222
v_accvgpr_write_b32 a105, v223
v_accvgpr_write_b32 a106, v224
v_accvgpr_write_b32 a107, v225
v_mfma_f32_16x16x32_bf16 v[198:201], v[22:25], a[116:119], v[174:177]
v_cvt_pk_bf16_f32 v226, v214, v215
ds_read_b64_tr_b16 v[46:47], v1 offset:8
v_cvt_pk_bf16_f32 v227, v216, v217
v_cvt_pk_bf16_f32 v228, v218, v219
v_cvt_pk_bf16_f32 v229, v220, v221
v_mfma_f32_16x16x32_bf16 v[202:205], v[20:23], a[116:119], v[174:177]
v_accvgpr_write_b32 a108, v226
v_accvgpr_write_b32 a109, v227
ds_read_b64_tr_b16 v[48:49], v1 offset:1032
v_accvgpr_write_b32 a110, v228
v_accvgpr_write_b32 a111, v229
v_mfma_f32_16x16x32_bf16 v[206:209], v[22:25], a[120:123], v[170:173]
v_cvt_pk_bf16_f32 v222, v190, v191
v_cvt_pk_bf16_f32 v223, v192, v193
ds_read_b64_tr_b16 v[50:51], v1 offset:8192
v_cvt_pk_bf16_f32 v224, v194, v195
v_cvt_pk_bf16_f32 v225, v196, v197
v_mfma_f32_16x16x32_bf16 v[210:213], v[20:23], a[120:123], v[170:173]
v_accvgpr_write_b32 a112, v222
v_accvgpr_write_b32 a113, v223
v_accvgpr_write_b32 a114, v224
ds_read_b64_tr_b16 v[52:53], v1 offset:9216
v_accvgpr_write_b32 a115, v225
v_mfma_f32_16x16x32_bf16 v[214:217], v[22:25], a[124:127], v[174:177]
v_cvt_pk_bf16_f32 v226, v198, v199
v_cvt_pk_bf16_f32 v227, v200, v201
v_cvt_pk_bf16_f32 v228, v202, v203
v_cvt_pk_bf16_f32 v229, v204, v205
ds_read_b64_tr_b16 v[54:55], v1 offset:8200
v_mfma_f32_16x16x32_bf16 v[218:221], v[20:23], a[124:127], v[174:177]
v_accvgpr_write_b32 a116, v226
v_accvgpr_write_b32 a117, v227
v_accvgpr_write_b32 a118, v228
v_accvgpr_write_b32 a119, v229
v_mfma_f32_16x16x32_bf16 v[190:193], v[22:25], a[128:131], v[170:173]
ds_read_b64_tr_b16 v[56:57], v1 offset:9224
v_cvt_pk_bf16_f32 v222, v206, v207
v_cvt_pk_bf16_f32 v223, v208, v209
v_cvt_pk_bf16_f32 v224, v210, v211
v_cvt_pk_bf16_f32 v225, v212, v213
v_mfma_f32_16x16x32_bf16 v[194:197], v[20:23], a[128:131], v[170:173]
s_waitcnt vmcnt(0) lgkmcnt(0)
v_accvgpr_write_b32 a120, v222
v_accvgpr_write_b32 a121, v223
v_accvgpr_write_b32 a122, v224
v_accvgpr_write_b32 a123, v225
s_barrier
ds_read_b64_tr_b16 v[58:59], v1 offset:16384
v_mfma_f32_16x16x32_bf16 v[178:181], v[186:189], v[42:45], v[178:181]
v_mfma_f32_16x16x32_bf16 v[182:185], v[186:189], v[46:49], v[182:185]
v_mfma_f32_16x16x32_bf16 v[178:181], v[186:189], v[50:53], v[178:181]
v_mfma_f32_16x16x32_bf16 v[182:185], v[186:189], v[54:57], v[182:185]
v_mfma_f32_16x16x32_bf16 v[198:201], v[22:25], a[132:135], v[174:177]
v_cvt_pk_bf16_f32 v226, v214, v215
ds_read_b64_tr_b16 v[60:61], v1 offset:17408
v_cvt_pk_bf16_f32 v227, v216, v217
v_cvt_pk_bf16_f32 v228, v218, v219
v_cvt_pk_bf16_f32 v229, v220, v221
v_mfma_f32_16x16x32_bf16 v[202:205], v[20:23], a[132:135], v[174:177]
v_accvgpr_write_b32 a124, v226
v_accvgpr_write_b32 a125, v227
ds_read_b64_tr_b16 v[62:63], v1 offset:16392
v_accvgpr_write_b32 a126, v228
v_accvgpr_write_b32 a127, v229
v_mfma_f32_16x16x32_bf16 v[206:209], v[22:25], a[136:139], v[170:173]
v_cvt_pk_bf16_f32 v222, v190, v191
v_cvt_pk_bf16_f32 v223, v192, v193
v_cvt_pk_bf16_f32 v224, v194, v195
ds_read_b64_tr_b16 v[64:65], v1 offset:17416
v_cvt_pk_bf16_f32 v225, v196, v197
v_mfma_f32_16x16x32_bf16 v[210:213], v[20:23], a[136:139], v[170:173]
v_accvgpr_write_b32 a128, v222
v_accvgpr_write_b32 a129, v223
v_accvgpr_write_b32 a130, v224
ds_read_b64_tr_b16 v[66:67], v1 offset:24576
v_accvgpr_write_b32 a131, v225
v_mfma_f32_16x16x32_bf16 v[214:217], v[22:25], a[140:143], v[174:177]
v_cvt_pk_bf16_f32 v226, v198, v199
v_cvt_pk_bf16_f32 v227, v200, v201
v_cvt_pk_bf16_f32 v228, v202, v203
v_cvt_pk_bf16_f32 v229, v204, v205
ds_read_b64_tr_b16 v[68:69], v1 offset:25600
v_mfma_f32_16x16x32_bf16 v[218:221], v[20:23], a[140:143], v[174:177]
s_waitcnt lgkmcnt(4)
v_accvgpr_write_b32 a132, v226
v_accvgpr_write_b32 a133, v227
v_accvgpr_write_b32 a134, v228
v_accvgpr_write_b32 a135, v229
v_cvt_pk_bf16_f32 v222, v206, v207
ds_read_b64_tr_b16 v[70:71], v1 offset:24584
v_cvt_pk_bf16_f32 v223, v208, v209
v_cvt_pk_bf16_f32 v224, v210, v211
v_cvt_pk_bf16_f32 v225, v212, v213
v_accvgpr_write_b32 a136, v222
v_accvgpr_write_b32 a137, v223
v_accvgpr_write_b32 a138, v224
ds_read_b64_tr_b16 v[72:73], v1 offset:25608
v_accvgpr_write_b32 a139, v225
v_cvt_pk_bf16_f32 v226, v214, v215
v_cvt_pk_bf16_f32 v227, v216, v217
v_cvt_pk_bf16_f32 v228, v218, v219
v_cvt_pk_bf16_f32 v229, v220, v221
v_accvgpr_write_b32 a140, v226
v_accvgpr_write_b32 a141, v227
v_accvgpr_write_b32 a142, v228
v_accvgpr_write_b32 a143, v229
v_mfma_f32_16x16x32_bf16 v[178:181], v[186:189], v[58:61], v[178:181]
s_waitcnt lgkmcnt(4)
v_mfma_f32_16x16x32_bf16 v[182:185], v[186:189], v[62:65], v[182:185]
s_waitcnt lgkmcnt(2)
v_mfma_f32_16x16x32_bf16 v[178:181], v[186:189], v[66:69], v[178:181]
s_waitcnt lgkmcnt(0)
v_mfma_f32_16x16x32_bf16 v[182:185], v[186:189], v[70:73], v[182:185]
s_nop 5
v_mul_f32 v178, 0xbae38e39, v178
v_mul_f32 v179, 0xbae38e39, v179
v_mul_f32 v180, 0xbae38e39, v180
v_mul_f32 v181, 0xbae38e39, v181
v_mul_f32 v182, 0xbae38e39, v182
v_mul_f32 v183, 0xbae38e39, v183
v_mul_f32 v184, 0xbae38e39, v184
v_mul_f32 v185, 0xbae38e39, v185
v_mfma_f32_16x16x32_bf16 v[190:193], v[22:25], a[144:147], v[178:181]
v_mfma_f32_16x16x32_bf16 v[194:197], v[20:23], a[144:147], v[178:181]
v_mfma_f32_16x16x32_bf16 v[198:201], v[22:25], a[148:151], v[182:185]
v_mfma_f32_16x16x32_bf16 v[202:205], v[20:23], a[148:151], v[182:185]
v_mfma_f32_16x16x32_bf16 v[206:209], v[22:25], a[152:155], v[178:181]
s_nop 3
v_cvt_pk_bf16_f32 v222, v190, v191
v_cvt_pk_bf16_f32 v223, v192, v193
v_cvt_pk_bf16_f32 v224, v194, v195
v_cvt_pk_bf16_f32 v225, v196, v197
v_mfma_f32_16x16x32_bf16 v[210:213], v[20:23], a[152:155], v[178:181]
v_accvgpr_write_b32 a144, v222
v_accvgpr_write_b32 a145, v223
v_accvgpr_write_b32 a146, v224
v_accvgpr_write_b32 a147, v225
v_mfma_f32_16x16x32_bf16 v[214:217], v[22:25], a[156:159], v[182:185]
v_cvt_pk_bf16_f32 v226, v198, v199
)"
R"(v_cvt_pk_bf16_f32 v227, v200, v201
v_cvt_pk_bf16_f32 v228, v202, v203
v_cvt_pk_bf16_f32 v229, v204, v205
v_mfma_f32_16x16x32_bf16 v[218:221], v[20:23], a[156:159], v[182:185]
v_accvgpr_write_b32 a148, v226
v_accvgpr_write_b32 a149, v227
v_accvgpr_write_b32 a150, v228
v_accvgpr_write_b32 a151, v229
v_mfma_f32_16x16x32_bf16 v[190:193], v[22:25], a[160:163], v[178:181]
v_cvt_pk_bf16_f32 v222, v206, v207
v_cvt_pk_bf16_f32 v223, v208, v209
v_cvt_pk_bf16_f32 v224, v210, v211
v_cvt_pk_bf16_f32 v225, v212, v213
v_mfma_f32_16x16x32_bf16 v[194:197], v[20:23], a[160:163], v[178:181]
v_accvgpr_write_b32 a152, v222
v_accvgpr_write_b32 a153, v223
v_accvgpr_write_b32 a154, v224
v_accvgpr_write_b32 a155, v225
v_mfma_f32_16x16x32_bf16 v[198:201], v[22:25], a[164:167], v[182:185]
v_cvt_pk_bf16_f32 v226, v214, v215
v_cvt_pk_bf16_f32 v227, v216, v217
v_cvt_pk_bf16_f32 v228, v218, v219
v_cvt_pk_bf16_f32 v229, v220, v221
v_mfma_f32_16x16x32_bf16 v[202:205], v[20:23], a[164:167], v[182:185]
v_accvgpr_write_b32 a156, v226
v_accvgpr_write_b32 a157, v227
v_accvgpr_write_b32 a158, v228
v_accvgpr_write_b32 a159, v229
v_mfma_f32_16x16x32_bf16 v[206:209], v[22:25], a[168:171], v[178:181]
v_cvt_pk_bf16_f32 v222, v190, v191
v_cvt_pk_bf16_f32 v223, v192, v193
v_cvt_pk_bf16_f32 v224, v194, v195
v_cvt_pk_bf16_f32 v225, v196, v197
v_mfma_f32_16x16x32_bf16 v[210:213], v[20:23], a[168:171], v[178:181]
v_accvgpr_write_b32 a160, v222
v_accvgpr_write_b32 a161, v223
v_accvgpr_write_b32 a162, v224
v_accvgpr_write_b32 a163, v225
v_mfma_f32_16x16x32_bf16 v[214:217], v[22:25], a[172:175], v[182:185]
v_cvt_pk_bf16_f32 v226, v198, v199
v_cvt_pk_bf16_f32 v227, v200, v201
v_cvt_pk_bf16_f32 v228, v202, v203
v_cvt_pk_bf16_f32 v229, v204, v205
v_mfma_f32_16x16x32_bf16 v[218:221], v[20:23], a[172:175], v[182:185]
v_accvgpr_write_b32 a164, v226
v_accvgpr_write_b32 a165, v227
v_accvgpr_write_b32 a166, v228
v_accvgpr_write_b32 a167, v229
v_mfma_f32_16x16x32_bf16 v[190:193], v[22:25], a[176:179], v[178:181]
v_cvt_pk_bf16_f32 v222, v206, v207
v_cvt_pk_bf16_f32 v223, v208, v209
v_cvt_pk_bf16_f32 v224, v210, v211
v_cvt_pk_bf16_f32 v225, v212, v213
v_mfma_f32_16x16x32_bf16 v[194:197], v[20:23], a[176:179], v[178:181]
v_accvgpr_write_b32 a168, v222
v_accvgpr_write_b32 a169, v223
v_accvgpr_write_b32 a170, v224
v_accvgpr_write_b32 a171, v225
v_mfma_f32_16x16x32_bf16 v[198:201], v[22:25], a[180:183], v[182:185]
v_cvt_pk_bf16_f32 v226, v214, v215
v_cvt_pk_bf16_f32 v227, v216, v217
v_cvt_pk_bf16_f32 v228, v218, v219
v_cvt_pk_bf16_f32 v229, v220, v221
v_mfma_f32_16x16x32_bf16 v[202:205], v[20:23], a[180:183], v[182:185]
v_accvgpr_write_b32 a172, v226
v_accvgpr_write_b32 a173, v227
v_accvgpr_write_b32 a174, v228
v_accvgpr_write_b32 a175, v229
v_mfma_f32_16x16x32_bf16 v[206:209], v[22:25], a[184:187], v[178:181]
v_cvt_pk_bf16_f32 v222, v190, v191
v_cvt_pk_bf16_f32 v223, v192, v193
v_cvt_pk_bf16_f32 v224, v194, v195
v_cvt_pk_bf16_f32 v225, v196, v197
v_mfma_f32_16x16x32_bf16 v[210:213], v[20:23], a[184:187], v[178:181]
v_accvgpr_write_b32 a176, v222
v_accvgpr_write_b32 a177, v223
v_accvgpr_write_b32 a178, v224
v_accvgpr_write_b32 a179, v225
v_mfma_f32_16x16x32_bf16 v[214:217], v[22:25], a[188:191], v[182:185]
v_cvt_pk_bf16_f32 v226, v198, v199
v_cvt_pk_bf16_f32 v227, v200, v201
v_cvt_pk_bf16_f32 v228, v202, v203
v_cvt_pk_bf16_f32 v229, v204, v205
v_mfma_f32_16x16x32_bf16 v[218:221], v[20:23], a[188:191], v[182:185]
v_accvgpr_write_b32 a180, v226
v_accvgpr_write_b32 a181, v227
v_accvgpr_write_b32 a182, v228
v_accvgpr_write_b32 a183, v229
v_mfma_f32_16x16x32_bf16 v[190:193], v[22:25], a[192:195], v[178:181]
v_cvt_pk_bf16_f32 v222, v206, v207
v_cvt_pk_bf16_f32 v223, v208, v209
v_cvt_pk_bf16_f32 v224, v210, v211
v_cvt_pk_bf16_f32 v225, v212, v213
v_mfma_f32_16x16x32_bf16 v[194:197], v[20:23], a[192:195], v[178:181]
v_accvgpr_write_b32 a184, v222
v_accvgpr_write_b32 a185, v223
v_accvgpr_write_b32 a186, v224
v_accvgpr_write_b32 a187, v225
v_mfma_f32_16x16x32_bf16 v[198:201], v[22:25], a[196:199], v[182:185]
v_cvt_pk_bf16_f32 v226, v214, v215
v_cvt_pk_bf16_f32 v227, v216, v217
v_cvt_pk_bf16_f32 v228, v218, v219
v_cvt_pk_bf16_f32 v229, v220, v221
v_mfma_f32_16x16x32_bf16 v[202:205], v[20:23], a[196:199], v[182:185]
v_accvgpr_write_b32 a188, v226
v_accvgpr_write_b32 a189, v227
v_accvgpr_write_b32 a190, v228
v_accvgpr_write_b32 a191, v229
v_mfma_f32_16x16x32_bf16 v[206:209], v[22:25], a[200:203], v[178:181]
v_cvt_pk_bf16_f32 v222, v190, v191
v_cvt_pk_bf16_f32 v223, v192, v193
v_cvt_pk_bf16_f32 v224, v194, v195
v_cvt_pk_bf16_f32 v225, v196, v197
v_mfma_f32_16x16x32_bf16 v[210:213], v[20:23], a[200:203], v[178:181]
v_accvgpr_write_b32 a192, v222
v_accvgpr_write_b32 a193, v223
v_accvgpr_write_b32 a194, v224
v_accvgpr_write_b32 a195, v225
v_mfma_f32_16x16x32_bf16 v[214:217], v[22:25], a[204:207], v[182:185]
v_cvt_pk_bf16_f32 v226, v198, v199
v_cvt_pk_bf16_f32 v227, v200, v201
v_cvt_pk_bf16_f32 v228, v202, v203
v_cvt_pk_bf16_f32 v229, v204, v205
v_mfma_f32_16x16x32_bf16 v[218:221], v[20:23], a[204:207], v[182:185]
v_accvgpr_write_b32 a196, v226
v_accvgpr_write_b32 a197, v227
v_accvgpr_write_b32 a198, v228
v_accvgpr_write_b32 a199, v229
v_mfma_f32_16x16x32_bf16 v[190:193], v[22:25], a[208:211], v[178:181]
v_cvt_pk_bf16_f32 v222, v206, v207
v_cvt_pk_bf16_f32 v223, v208, v209
v_cvt_pk_bf16_f32 v224, v210, v211
v_cvt_pk_bf16_f32 v225, v212, v213
v_mfma_f32_16x16x32_bf16 v[194:197], v[20:23], a[208:211], v[178:181]
v_accvgpr_write_b32 a200, v222
v_accvgpr_write_b32 a201, v223
v_accvgpr_write_b32 a202, v224
v_accvgpr_write_b32 a203, v225
v_mfma_f32_16x16x32_bf16 v[198:201], v[22:25], a[212:215], v[182:185]
v_cvt_pk_bf16_f32 v226, v214, v215
v_cvt_pk_bf16_f32 v227, v216, v217
v_cvt_pk_bf16_f32 v228, v218, v219
v_cvt_pk_bf16_f32 v229, v220, v221
v_mfma_f32_16x16x32_bf16 v[202:205], v[20:23], a[212:215], v[182:185]
v_accvgpr_write_b32 a204, v226
v_accvgpr_write_b32 a205, v227
v_accvgpr_write_b32 a206, v228
v_accvgpr_write_b32 a207, v229
v_mfma_f32_16x16x32_bf16 v[206:209], v[22:25], a[216:219], v[178:181]
v_cvt_pk_bf16_f32 v222, v190, v191
v_cvt_pk_bf16_f32 v223, v192, v193
v_cvt_pk_bf16_f32 v224, v194, v195
v_cvt_pk_bf16_f32 v225, v196, v197
v_mfma_f32_16x16x32_bf16 v[210:213], v[20:23], a[216:219], v[178:181]
v_accvgpr_write_b32 a208, v222
v_accvgpr_write_b32 a209, v223
v_accvgpr_write_b32 a210, v224
v_accvgpr_write_b32 a211, v225
v_mfma_f32_16x16x32_bf16 v[214:217], v[22:25], a[220:223], v[182:185]
v_cvt_pk_bf16_f32 v226, v198, v199
v_cvt_pk_bf16_f32 v227, v200, v201
v_cvt_pk_bf16_f32 v228, v202, v203
v_cvt_pk_bf16_f32 v229, v204, v205
v_mfma_f32_16x16x32_bf16 v[218:221], v[20:23], a[220:223], v[182:185]
v_accvgpr_write_b32 a212, v226
v_accvgpr_write_b32 a213, v227
v_accvgpr_write_b32 a214, v228
v_accvgpr_write_b32 a215, v229
v_mfma_f32_16x16x32_bf16 v[190:193], v[22:25], a[224:227], v[178:181]
v_cvt_pk_bf16_f32 v222, v206, v207
v_cvt_pk_bf16_f32 v223, v208, v209
v_cvt_pk_bf16_f32 v224, v210, v211
v_cvt_pk_bf16_f32 v225, v212, v213
v_mfma_f32_16x16x32_bf16 v[194:197], v[20:23], a[224:227], v[178:181]
v_accvgpr_write_b32 a216, v222
v_accvgpr_write_b32 a217, v223
v_accvgpr_write_b32 a218, v224
v_accvgpr_write_b32 a219, v225
v_mfma_f32_16x16x32_bf16 v[198:201], v[22:25], a[228:231], v[182:185]
v_cvt_pk_bf16_f32 v226, v214, v215
v_cvt_pk_bf16_f32 v227, v216, v217
v_cvt_pk_bf16_f32 v228, v218, v219
v_cvt_pk_bf16_f32 v229, v220, v221
v_mfma_f32_16x16x32_bf16 v[202:205], v[20:23], a[228:231], v[182:185]
v_accvgpr_write_b32 a220, v226
v_accvgpr_write_b32 a221, v227
v_accvgpr_write_b32 a222, v228
v_accvgpr_write_b32 a223, v229
v_mfma_f32_16x16x32_bf16 v[206:209], v[22:25], a[232:235], v[178:181]
v_cvt_pk_bf16_f32 v222, v190, v191
v_cvt_pk_bf16_f32 v223, v192, v193
v_cvt_pk_bf16_f32 v224, v194, v195
v_cvt_pk_bf16_f32 v225, v196, v197
v_mfma_f32_16x16x32_bf16 v[210:213], v[20:23], a[232:235], v[178:181]
v_accvgpr_write_b32 a224, v222
v_accvgpr_write_b32 a225, v223
v_accvgpr_write_b32 a226, v224
v_accvgpr_write_b32 a227, v225
v_mfma_f32_16x16x32_bf16 v[214:217], v[22:25], a[236:239], v[182:185]
v_cvt_pk_bf16_f32 v226, v198, v199
v_cvt_pk_bf16_f32 v227, v200, v201
v_cvt_pk_bf16_f32 v228, v202, v203
v_cvt_pk_bf16_f32 v229, v204, v205
v_mfma_f32_16x16x32_bf16 v[218:221], v[20:23], a[236:239], v[182:185]
v_accvgpr_write_b32 a228, v226
v_accvgpr_write_b32 a229, v227
v_accvgpr_write_b32 a230, v228
v_accvgpr_write_b32 a231, v229
v_mfma_f32_16x16x32_bf16 v[190:193], v[22:25], v[26:29], v[178:181]
v_cvt_pk_bf16_f32 v222, v206, v207
v_cvt_pk_bf16_f32 v223, v208, v209
v_cvt_pk_bf16_f32 v224, v210, v211
v_cvt_pk_bf16_f32 v225, v212, v213
v_mfma_f32_16x16x32_bf16 v[194:197], v[20:23], v[26:29], v[178:181]
v_accvgpr_write_b32 a232, v222
v_accvgpr_write_b32 a233, v223
v_accvgpr_write_b32 a234, v224
v_accvgpr_write_b32 a235, v225
v_mfma_f32_16x16x32_bf16 v[198:201], v[22:25], v[30:33], v[182:185]
v_cvt_pk_bf16_f32 v226, v214, v215
v_cvt_pk_bf16_f32 v227, v216, v217
v_cvt_pk_bf16_f32 v228, v218, v219
v_cvt_pk_bf16_f32 v229, v220, v221
v_mfma_f32_16x16x32_bf16 v[202:205], v[20:23], v[30:33], v[182:185]
v_accvgpr_write_b32 a236, v226
v_accvgpr_write_b32 a237, v227
v_accvgpr_write_b32 a238, v228
v_accvgpr_write_b32 a239, v229
v_mfma_f32_16x16x32_bf16 v[206:209], v[22:25], v[34:37], v[178:181]
v_cvt_pk_bf16_f32 v26, v190, v191
v_cvt_pk_bf16_f32 v27, v192, v193
v_cvt_pk_bf16_f32 v28, v194, v195
v_cvt_pk_bf16_f32 v29, v196, v197
v_mfma_f32_16x16x32_bf16 v[210:213], v[20:23], v[34:37], v[178:181]
v_mfma_f32_16x16x32_bf16 v[214:217], v[22:25], v[38:41], v[182:185]
v_cvt_pk_bf16_f32 v30, v198, v199
v_cvt_pk_bf16_f32 v31, v200, v201
v_cvt_pk_bf16_f32 v32, v202, v203
v_cvt_pk_bf16_f32 v33, v204, v205
v_mfma_f32_16x16x32_bf16 v[218:221], v[20:23], v[38:41], v[182:185]
v_mfma_f32_16x16x32_bf16 v[190:193], v[22:25], v[42:45], v[178:181]
v_cvt_pk_bf16_f32 v34, v206, v207
v_cvt_pk_bf16_f32 v35, v208, v209
v_cvt_pk_bf16_f32 v36, v210, v211
v_cvt_pk_bf16_f32 v37, v212, v213
v_mfma_f32_16x16x32_bf16 v[194:197], v[20:23], v[42:45], v[178:181]
v_mfma_f32_16x16x32_bf16 v[198:201], v[22:25], v[46:49], v[182:185]
v_cvt_pk_bf16_f32 v38, v214, v215
v_cvt_pk_bf16_f32 v39, v216, v217
v_cvt_pk_bf16_f32 v40, v218, v219
v_cvt_pk_bf16_f32 v41, v220, v221
v_mfma_f32_16x16x32_bf16 v[202:205], v[20:23], v[46:49], v[182:185]
v_mfma_f32_16x16x32_bf16 v[206:209], v[22:25], v[50:53], v[178:181]
v_cvt_pk_bf16_f32 v42, v190, v191
v_cvt_pk_bf16_f32 v43, v192, v193
v_cvt_pk_bf16_f32 v44, v194, v195
v_cvt_pk_bf16_f32 v45, v196, v197
v_mfma_f32_16x16x32_bf16 v[210:213], v[20:23], v[50:53], v[178:181]
v_mfma_f32_16x16x32_bf16 v[214:217], v[22:25], v[54:57], v[182:185]
v_cvt_pk_bf16_f32 v46, v198, v199
v_cvt_pk_bf16_f32 v47, v200, v201
v_cvt_pk_bf16_f32 v48, v202, v203
v_cvt_pk_bf16_f32 v49, v204, v205
v_mfma_f32_16x16x32_bf16 v[218:221], v[20:23], v[54:57], v[182:185]
v_mfma_f32_16x16x32_bf16 v[190:193], v[22:25], v[58:61], v[178:181]
v_cvt_pk_bf16_f32 v50, v206, v207
v_cvt_pk_bf16_f32 v51, v208, v209
v_cvt_pk_bf16_f32 v52, v210, v211
v_cvt_pk_bf16_f32 v53, v212, v213
v_mfma_f32_16x16x32_bf16 v[194:197], v[20:23], v[58:61], v[178:181]
v_mfma_f32_16x16x32_bf16 v[198:201], v[22:25], v[62:65], v[182:185]
v_cvt_pk_bf16_f32 v54, v214, v215
v_cvt_pk_bf16_f32 v55, v216, v217
v_cvt_pk_bf16_f32 v56, v218, v219
v_cvt_pk_bf16_f32 v57, v220, v221
v_mfma_f32_16x16x32_bf16 v[202:205], v[20:23], v[62:65], v[182:185]
v_mfma_f32_16x16x32_bf16 v[206:209], v[22:25], v[66:69], v[178:181]
v_cvt_pk_bf16_f32 v58, v190, v191
v_cvt_pk_bf16_f32 v59, v192, v193
v_cvt_pk_bf16_f32 v60, v194, v195
v_cvt_pk_bf16_f32 v61, v196, v197
v_mfma_f32_16x16x32_bf16 v[210:213], v[20:23], v[66:69], v[178:181]
v_mfma_f32_16x16x32_bf16 v[214:217], v[22:25], v[70:73], v[182:185]
v_cvt_pk_bf16_f32 v62, v198, v199
v_cvt_pk_bf16_f32 v63, v200, v201
v_cvt_pk_bf16_f32 v64, v202, v203
v_cvt_pk_bf16_f32 v65, v204, v205
v_mfma_f32_16x16x32_bf16 v[218:221], v[20:23], v[70:73], v[182:185]
v_cvt_pk_bf16_f32 v66, v206, v207
v_cvt_pk_bf16_f32 v67, v208, v209
v_cvt_pk_bf16_f32 v68, v210, v211
v_cvt_pk_bf16_f32 v69, v212, v213
v_cvt_pk_bf16_f32 v70, v214, v215
v_cvt_pk_bf16_f32 v71, v216, v217
s_nop 1
v_cvt_pk_bf16_f32 v72, v218, v219
v_cvt_pk_bf16_f32 v73, v220, v221
s_barrier
s_and_b32 s75, s36, 3
s_cmp_eq_u32 s75, 1
s_cbranch_scc1 .Lgu_w2
s_cmp_eq_u32 s75, 2
s_cbranch_scc1 .Lgu_w3
s_cmp_eq_u32 s75, 3
s_cbranch_scc1 .Lgu_w4
.Lgu_w1:
v_add_u32 v254, -1, v5
v_add_u32 v255, -1, v5
v_add_lshl_u32 v252, v7, 0, 7
v_bfi_b32 v252, s69, v252, v6
v_add_lshl_u32 v253, v5, 0, 7
v_bfi_b32 v253, s69, v253, v4
s_add_u32 m0, s45, 18432
s_xor_b32 s45, s45, 0x10000
s_mov_b32 m0, s45
s_barrier
global_load_lds_dwordx4 v2, s[38:39] offset:-2048 sc1
global_load_lds_dwordx4 v2, s[38:39] offset:-1024 sc1
global_load_lds_dwordx4 v2, s[38:39] offset:0 sc1
global_load_lds_dwordx4 v2, s[38:39] offset:1024 sc1
global_load_lds_dwordx4 v2, s[38:39] offset:2048 sc1
global_load_lds_dwordx4 v2, s[38:39] offset:3072 sc1
v_add_u32 v2, 18688, v2
s_mov_b32 s70, 32
s_waitcnt vmcnt(0) lgkmcnt(0)
s_nop 7
s_branch .Lgu_in5
.p2align 6
.Lgu_top6:
s_waitcnt lgkmcnt(5)
v_mfma_f32_16x16x32_bf16 v[74:77], a[0:3], v[106:109], 0
s_waitcnt lgkmcnt(4)
v_cvt_pk_bf16_f32 v162, v154, v155
v_cvt_pk_bf16_f32 v163, v156, v157
v_mfma_f32_16x16x32_bf16 v[74:77], a[8:11], v[110:113], v[74:77]
s_waitcnt lgkmcnt(3)
v_mfma_f32_16x16x32_bf16 v[74:77], a[16:19], v[114:117], v[74:77]
s_waitcnt lgkmcnt(2)
v_mfma_f32_16x16x32_bf16 v[74:77], a[24:27], v[118:121], v[74:77]
s_waitcnt lgkmcnt(1)
v_mul_f32 v158, v94, v102
v_mul_f32 v159, v95, v103
v_mfma_f32_16x16x32_bf16 v[74:77], a[32:35], v[122:125], v[74:77]
s_waitcnt lgkmcnt(0)
v_mul_f32 v160, v96, v104
v_mul_f32 v161, v97, v105
v_mfma_f32_16x16x32_bf16 v[74:77], a[40:43], v[126:129], v[74:77]
ds_read_b128 v[130:133], v8 offset:6144
v_cvt_pk_bf16_f32 v164, v158, v159
v_mfma_f32_16x16x32_bf16 v[82:85], a[144:147], v[106:109], 0
ds_read_b128 v[134:137], v8 offset:7168
v_cvt_pk_bf16_f32 v165, v160, v161
v_mfma_f32_16x16x32_bf16 v[82:85], a[152:155], v[110:113], v[82:85]
v_mfma_f32_16x16x32_bf16 v[82:85], a[160:163], v[114:117], v[82:85]
s_waitcnt vmcnt(6)
s_barrier
v_mfma_f32_16x16x32_bf16 v[82:85], a[168:171], v[118:121], v[82:85]
v_mfma_f32_16x16x32_bf16 v[82:85], a[176:179], v[122:125], v[82:85]
global_store_dwordx4 v3, v[162:165], s[38:39] sc1
v_mfma_f32_16x16x32_bf16 v[82:85], a[184:187], v[126:129], v[82:85]
ds_read_b128 v[138:141], v8 offset:8192
v_add_u32 v3, 147456, v3
v_mfma_f32_16x16x32_bf16 v[78:81], a[4:7], v[106:109], 0
v_mfma_f32_16x16x32_bf16 v[78:81], a[12:15], v[110:113], v[78:81]
v_mfma_f32_16x16x32_bf16 v[78:81], a[20:23], v[114:117], v[78:81]
ds_read_b128 v[142:145], v8 offset:9216
v_mfma_f32_16x16x32_bf16 v[78:81], a[28:31], v[118:121], v[78:81]
v_mfma_f32_16x16x32_bf16 v[78:81], a[36:39], v[122:125], v[78:81]
v_mfma_f32_16x16x32_bf16 v[78:81], a[44:47], v[126:129], v[78:81]
ds_read_b128 v[146:149], v8 offset:10240
v_mfma_f32_16x16x32_bf16 v[86:89], a[148:151], v[106:109], 0
v_mfma_f32_16x16x32_bf16 v[86:89], a[156:159], v[110:113], v[86:89]
v_mfma_f32_16x16x32_bf16 v[86:89], a[164:167], v[114:117], v[86:89]
ds_read_b128 v[150:153], v8 offset:11264
v_mfma_f32_16x16x32_bf16 v[86:89], a[172:175], v[118:121], v[86:89]
v_mfma_f32_16x16x32_bf16 v[86:89], a[180:183], v[122:125], v[86:89]
v_mfma_f32_16x16x32_bf16 v[86:89], a[188:191], v[126:129], v[86:89]
s_waitcnt lgkmcnt(5)
v_mfma_f32_16x16x32_bf16 v[74:77], a[48:51], v[130:133], v[74:77]
s_waitcnt lgkmcnt(4)
v_mfma_f32_16x16x32_bf16 v[74:77], a[56:59], v[134:137], v[74:77]
s_waitcnt lgkmcnt(3)
v_mfma_f32_16x16x32_bf16 v[74:77], a[64:67], v[138:141], v[74:77]
s_waitcnt lgkmcnt(2)
v_mfma_f32_16x16x32_bf16 v[74:77], a[72:75], v[142:145], v[74:77]
s_waitcnt lgkmcnt(1)
v_mfma_f32_16x16x32_bf16 v[74:77], a[80:83], v[146:149], v[74:77]
s_waitcnt lgkmcnt(0)
v_mfma_f32_16x16x32_bf16 v[74:77], a[88:91], v[150:153], v[74:77]
ds_read_b128 v[106:109], v8 offset:12288
v_mfma_f32_16x16x32_bf16 v[82:85], a[192:195], v[130:133], v[82:85]
v_mfma_f32_16x16x32_bf16 v[82:85], a[200:203], v[134:137], v[82:85]
v_mfma_f32_16x16x32_bf16 v[82:85], a[208:211], v[138:141], v[82:85]
ds_read_b128 v[110:113], v8 offset:13312
v_mfma_f32_16x16x32_bf16 v[82:85], a[216:219], v[142:145], v[82:85]
v_mfma_f32_16x16x32_bf16 v[82:85], a[224:227], v[146:149], v[82:85]
v_mfma_f32_16x16x32_bf16 v[82:85], a[232:235], v[150:153], v[82:85]
ds_read_b128 v[114:117], v8 offset:14336
v_mfma_f32_16x16x32_bf16 v[78:81], a[52:55], v[130:133], v[78:81]
v_mfma_f32_16x16x32_bf16 v[78:81], a[60:63], v[134:137], v[78:81]
)"
R"(v_mfma_f32_16x16x32_bf16 v[78:81], a[68:71], v[138:141], v[78:81]
ds_read_b128 v[118:121], v8 offset:15360
v_mfma_f32_16x16x32_bf16 v[78:81], a[76:79], v[142:145], v[78:81]
v_mfma_f32_16x16x32_bf16 v[78:81], a[84:87], v[146:149], v[78:81]
v_mfma_f32_16x16x32_bf16 v[78:81], a[92:95], v[150:153], v[78:81]
ds_read_b128 v[122:125], v8 offset:16384
v_mfma_f32_16x16x32_bf16 v[86:89], a[196:199], v[130:133], v[86:89]
v_mfma_f32_16x16x32_bf16 v[86:89], a[204:207], v[134:137], v[86:89]
v_mfma_f32_16x16x32_bf16 v[86:89], a[212:215], v[138:141], v[86:89]
ds_read_b128 v[126:129], v8 offset:17408
v_mfma_f32_16x16x32_bf16 v[86:89], a[220:223], v[142:145], v[86:89]
s_xor_b32 s45, s45, 0x10000
v_mfma_f32_16x16x32_bf16 v[86:89], a[228:231], v[146:149], v[86:89]
s_add_u32 m0, s45, 0
v_mfma_f32_16x16x32_bf16 v[86:89], a[236:239], v[150:153], v[86:89]
s_waitcnt lgkmcnt(5)
v_mfma_f32_16x16x32_bf16 v[74:77], a[96:99], v[106:109], v[74:77]
s_barrier
s_waitcnt lgkmcnt(4)
v_mfma_f32_16x16x32_bf16 v[74:77], a[104:107], v[110:113], v[74:77]
s_waitcnt lgkmcnt(3)
global_load_lds_dwordx4 v2, s[38:39] offset:-2048 sc1
v_mfma_f32_16x16x32_bf16 v[74:77], a[112:115], v[114:117], v[74:77]
s_waitcnt lgkmcnt(2)
v_mfma_f32_16x16x32_bf16 v[74:77], a[120:123], v[118:121], v[74:77]
s_waitcnt lgkmcnt(1)
v_mfma_f32_16x16x32_bf16 v[74:77], a[128:131], v[122:125], v[74:77]
s_waitcnt lgkmcnt(0)
global_load_lds_dwordx4 v2, s[38:39] offset:-1024 sc1
v_mfma_f32_16x16x32_bf16 v[74:77], a[136:139], v[126:129], v[74:77]
v_mfma_f32_16x16x32_bf16 v[82:85], v[26:29], v[106:109], v[82:85]
v_mfma_f32_16x16x32_bf16 v[82:85], v[34:37], v[110:113], v[82:85]
global_load_lds_dwordx4 v2, s[38:39] offset:0 sc1
v_mfma_f32_16x16x32_bf16 v[82:85], v[42:45], v[114:117], v[82:85]
v_mfma_f32_16x16x32_bf16 v[82:85], v[50:53], v[118:121], v[82:85]
v_mfma_f32_16x16x32_bf16 v[82:85], v[58:61], v[122:125], v[82:85]
global_load_lds_dwordx4 v2, s[38:39] offset:1024 sc1
v_mfma_f32_16x16x32_bf16 v[82:85], v[66:69], v[126:129], v[82:85]
v_mfma_f32_16x16x32_bf16 v[78:81], a[100:103], v[106:109], v[78:81]
s_waitcnt vmcnt(5)
s_barrier
v_mfma_f32_16x16x32_bf16 v[78:81], a[108:111], v[110:113], v[78:81]
global_load_lds_dwordx4 v2, s[38:39] offset:2048 sc1
v_mfma_f32_16x16x32_bf16 v[78:81], a[116:119], v[114:117], v[78:81]
v_mfma_f32_16x16x32_bf16 v[78:81], a[124:127], v[118:121], v[78:81]
ds_read_b128 v[130:133], v8 offset:18432
v_mfma_f32_16x16x32_bf16 v[78:81], a[132:135], v[122:125], v[78:81]
ds_read_b128 v[134:137], v8 offset:19456
global_load_lds_dwordx4 v2, s[38:39] offset:3072 sc1
v_mfma_f32_16x16x32_bf16 v[78:81], a[140:143], v[126:129], v[78:81]
ds_read_b128 v[138:141], v8 offset:20480
v_add_u32 v2, 18688, v2
v_mfma_f32_16x16x32_bf16 v[86:89], v[30:33], v[106:109], v[86:89]
ds_read_b128 v[142:145], v8 offset:21504
v_min_u32 v2, v2, v9
v_mfma_f32_16x16x32_bf16 v[86:89], v[38:41], v[110:113], v[86:89]
ds_read_b128 v[146:149], v8 offset:22528
v_mul_f32 v154, v74, v82
v_mfma_f32_16x16x32_bf16 v[86:89], v[46:49], v[114:117], v[86:89]
ds_read_b128 v[150:153], v8 offset:23552
v_mul_f32 v155, v75, v83
v_mfma_f32_16x16x32_bf16 v[86:89], v[54:57], v[118:121], v[86:89]
v_mul_f32 v156, v76, v84
v_mul_f32 v157, v77, v85
v_mfma_f32_16x16x32_bf16 v[86:89], v[62:65], v[122:125], v[86:89]
v_cvt_pk_bf16_f32 v162, v154, v155
v_cvt_pk_bf16_f32 v163, v156, v157
v_mfma_f32_16x16x32_bf16 v[86:89], v[70:73], v[126:129], v[86:89]
s_waitcnt lgkmcnt(5)
v_mfma_f32_16x16x32_bf16 v[90:93], a[0:3], v[130:133], 0
s_waitcnt lgkmcnt(4)
v_mfma_f32_16x16x32_bf16 v[90:93], a[8:11], v[134:137], v[90:93]
s_waitcnt lgkmcnt(3)
v_mfma_f32_16x16x32_bf16 v[90:93], a[16:19], v[138:141], v[90:93]
s_waitcnt lgkmcnt(2)
v_mfma_f32_16x16x32_bf16 v[90:93], a[24:27], v[142:145], v[90:93]
s_waitcnt lgkmcnt(1)
v_mul_f32 v158, v78, v86
v_mul_f32 v159, v79, v87
v_mfma_f32_16x16x32_bf16 v[90:93], a[32:35], v[146:149], v[90:93]
s_waitcnt lgkmcnt(0)
v_mul_f32 v160, v80, v88
v_mul_f32 v161, v81, v89
v_mfma_f32_16x16x32_bf16 v[90:93], a[40:43], v[150:153], v[90:93]
ds_read_b128 v[106:109], v8 offset:24576
v_cvt_pk_bf16_f32 v164, v158, v159
v_mfma_f32_16x16x32_bf16 v[98:101], a[144:147], v[130:133], 0
ds_read_b128 v[110:113], v8 offset:25600
v_cvt_pk_bf16_f32 v165, v160, v161
v_mfma_f32_16x16x32_bf16 v[98:101], a[152:155], v[134:137], v[98:101]
v_mfma_f32_16x16x32_bf16 v[98:101], a[160:163], v[138:141], v[98:101]
s_waitcnt vmcnt(6)
s_barrier
v_mfma_f32_16x16x32_bf16 v[98:101], a[168:171], v[142:145], v[98:101]
v_mfma_f32_16x16x32_bf16 v[98:101], a[176:179], v[146:149], v[98:101]
global_store_dwordx4 v3, v[162:165], s[38:39] sc1
v_mfma_f32_16x16x32_bf16 v[98:101], a[184:187], v[150:153], v[98:101]
ds_read_b128 v[114:117], v8 offset:26624
v_add_u32 v3, 147456, v3
v_mfma_f32_16x16x32_bf16 v[94:97], a[4:7], v[130:133], 0
v_mfma_f32_16x16x32_bf16 v[94:97], a[12:15], v[134:137], v[94:97]
v_mfma_f32_16x16x32_bf16 v[94:97], a[20:23], v[138:141], v[94:97]
ds_read_b128 v[118:121], v8 offset:27648
v_mfma_f32_16x16x32_bf16 v[94:97], a[28:31], v[142:145], v[94:97]
v_mfma_f32_16x16x32_bf16 v[94:97], a[36:39], v[146:149], v[94:97]
v_mfma_f32_16x16x32_bf16 v[94:97], a[44:47], v[150:153], v[94:97]
ds_read_b128 v[122:125], v8 offset:28672
v_mfma_f32_16x16x32_bf16 v[102:105], a[148:151], v[130:133], 0
v_mfma_f32_16x16x32_bf16 v[102:105], a[156:159], v[134:137], v[102:105]
v_mfma_f32_16x16x32_bf16 v[102:105], a[164:167], v[138:141], v[102:105]
ds_read_b128 v[126:129], v8 offset:29696
v_mfma_f32_16x16x32_bf16 v[102:105], a[172:175], v[142:145], v[102:105]
v_mfma_f32_16x16x32_bf16 v[102:105], a[180:183], v[146:149], v[102:105]
v_mfma_f32_16x16x32_bf16 v[102:105], a[188:191], v[150:153], v[102:105]
s_waitcnt lgkmcnt(5)
v_mfma_f32_16x16x32_bf16 v[90:93], a[48:51], v[106:109], v[90:93]
s_waitcnt lgkmcnt(4)
v_mfma_f32_16x16x32_bf16 v[90:93], a[56:59], v[110:113], v[90:93]
s_waitcnt lgkmcnt(3)
v_mfma_f32_16x16x32_bf16 v[90:93], a[64:67], v[114:117], v[90:93]
s_waitcnt lgkmcnt(2)
v_mfma_f32_16x16x32_bf16 v[90:93], a[72:75], v[118:121], v[90:93]
s_waitcnt lgkmcnt(1)
v_mfma_f32_16x16x32_bf16 v[90:93], a[80:83], v[122:125], v[90:93]
s_waitcnt lgkmcnt(0)
v_mfma_f32_16x16x32_bf16 v[90:93], a[88:91], v[126:129], v[90:93]
ds_read_b128 v[130:133], v8 offset:30720
v_mfma_f32_16x16x32_bf16 v[98:101], a[192:195], v[106:109], v[98:101]
v_mfma_f32_16x16x32_bf16 v[98:101], a[200:203], v[110:113], v[98:101]
v_mfma_f32_16x16x32_bf16 v[98:101], a[208:211], v[114:117], v[98:101]
ds_read_b128 v[134:137], v8 offset:31744
v_mfma_f32_16x16x32_bf16 v[98:101], a[216:219], v[118:121], v[98:101]
v_mfma_f32_16x16x32_bf16 v[98:101], a[224:227], v[122:125], v[98:101]
v_mfma_f32_16x16x32_bf16 v[98:101], a[232:235], v[126:129], v[98:101]
ds_read_b128 v[138:141], v8 offset:32768
v_mfma_f32_16x16x32_bf16 v[94:97], a[52:55], v[106:109], v[94:97]
v_mfma_f32_16x16x32_bf16 v[94:97], a[60:63], v[110:113], v[94:97]
v_mfma_f32_16x16x32_bf16 v[94:97], a[68:71], v[114:117], v[94:97]
ds_read_b128 v[142:145], v8 offset:33792
v_mfma_f32_16x16x32_bf16 v[94:97], a[76:79], v[118:121], v[94:97]
v_mfma_f32_16x16x32_bf16 v[94:97], a[84:87], v[122:125], v[94:97]
v_mfma_f32_16x16x32_bf16 v[94:97], a[92:95], v[126:129], v[94:97]
ds_read_b128 v[146:149], v8 offset:34816
v_mfma_f32_16x16x32_bf16 v[102:105], a[196:199], v[106:109], v[102:105]
.Lgu_in5:
v_mfma_f32_16x16x32_bf16 v[102:105], a[204:207], v[110:113], v[102:105]
v_mfma_f32_16x16x32_bf16 v[102:105], a[212:215], v[114:117], v[102:105]
ds_read_b128 v[150:153], v8 offset:35840
v_mfma_f32_16x16x32_bf16 v[102:105], a[220:223], v[118:121], v[102:105]
v_mfma_f32_16x16x32_bf16 v[102:105], a[228:231], v[122:125], v[102:105]
s_add_u32 m0, s45, 18432
v_mfma_f32_16x16x32_bf16 v[102:105], a[236:239], v[126:129], v[102:105]
s_waitcnt lgkmcnt(5)
v_mfma_f32_16x16x32_bf16 v[90:93], a[96:99], v[130:133], v[90:93]
s_barrier
s_waitcnt lgkmcnt(4)
v_mfma_f32_16x16x32_bf16 v[90:93], a[104:107], v[134:137], v[90:93]
s_waitcnt lgkmcnt(3)
global_load_lds_dwordx4 v2, s[38:39] offset:-2048 sc1
v_mfma_f32_16x16x32_bf16 v[90:93], a[112:115], v[138:141], v[90:93]
s_waitcnt lgkmcnt(2)
v_mfma_f32_16x16x32_bf16 v[90:93], a[120:123], v[142:145], v[90:93]
s_waitcnt lgkmcnt(1)
v_mfma_f32_16x16x32_bf16 v[90:93], a[128:131], v[146:149], v[90:93]
s_waitcnt lgkmcnt(0)
global_load_lds_dwordx4 v2, s[38:39] offset:-1024 sc1
v_mfma_f32_16x16x32_bf16 v[90:93], a[136:139], v[150:153], v[90:93]
v_mfma_f32_16x16x32_bf16 v[98:101], v[26:29], v[130:133], v[98:101]
v_mfma_f32_16x16x32_bf16 v[98:101], v[34:37], v[134:137], v[98:101]
global_load_lds_dwordx4 v2, s[38:39] offset:0 sc1
v_mfma_f32_16x16x32_bf16 v[98:101], v[42:45], v[138:141], v[98:101]
v_mfma_f32_16x16x32_bf16 v[98:101], v[50:53], v[142:145], v[98:101]
v_xor_b32 v8, 0x10000, v8
v_mfma_f32_16x16x32_bf16 v[98:101], v[58:61], v[146:149], v[98:101]
global_load_lds_dwordx4 v2, s[38:39] offset:1024 sc1
v_mfma_f32_16x16x32_bf16 v[98:101], v[66:69], v[150:153], v[98:101]
v_mfma_f32_16x16x32_bf16 v[94:97], a[100:103], v[130:133], v[94:97]
s_waitcnt vmcnt(5)
s_barrier
v_mfma_f32_16x16x32_bf16 v[94:97], a[108:111], v[134:137], v[94:97]
global_load_lds_dwordx4 v2, s[38:39] offset:2048 sc1
v_mfma_f32_16x16x32_bf16 v[94:97], a[116:119], v[138:141], v[94:97]
v_mfma_f32_16x16x32_bf16 v[94:97], a[124:127], v[142:145], v[94:97]
ds_read_b128 v[106:109], v8 offset:0
v_mfma_f32_16x16x32_bf16 v[94:97], a[132:135], v[146:149], v[94:97]
ds_read_b128 v[110:113], v8 offset:1024
global_load_lds_dwordx4 v2, s[38:39] offset:3072 sc1
v_mfma_f32_16x16x32_bf16 v[94:97], a[140:143], v[150:153], v[94:97]
ds_read_b128 v[114:117], v8 offset:2048
v_add_u32 v2, 18688, v2
v_mfma_f32_16x16x32_bf16 v[102:105], v[30:33], v[130:133], v[102:105]
ds_read_b128 v[118:121], v8 offset:3072
v_min_u32 v2, v2, v9
v_mfma_f32_16x16x32_bf16 v[102:105], v[38:41], v[134:137], v[102:105]
ds_read_b128 v[122:125], v8 offset:4096
v_mul_f32 v154, v90, v98
v_mfma_f32_16x16x32_bf16 v[102:105], v[46:49], v[138:141], v[102:105]
ds_read_b128 v[126:129], v8 offset:5120
v_mul_f32 v155, v91, v99
v_mfma_f32_16x16x32_bf16 v[102:105], v[54:57], v[142:145], v[102:105]
v_mul_f32 v156, v92, v100
v_mul_f32 v157, v93, v101
v_mfma_f32_16x16x32_bf16 v[102:105], v[62:65], v[146:149], v[102:105]
v_mfma_f32_16x16x32_bf16 v[102:105], v[70:73], v[150:153], v[102:105]
s_sub_u32 s70, s70, 1
s_cbranch_scc0 .Lgu_top6
s_waitcnt vmcnt(6) lgkmcnt(0)
s_barrier
v_mul_f32 v154, v90, v98
v_mul_f32 v155, v91, v99
v_mul_f32 v156, v92, v100
v_mul_f32 v157, v93, v101
v_cvt_pk_bf16_f32 v162, v154, v155
v_cvt_pk_bf16_f32 v163, v156, v157
v_mul_f32 v158, v94, v102
v_mul_f32 v159, v95, v103
v_mul_f32 v160, v96, v104
v_mul_f32 v161, v97, v105
v_cvt_pk_bf16_f32 v164, v158, v159
v_cvt_pk_bf16_f32 v165, v160, v161
global_store_dwordx4 v3, v[162:165], s[38:39] sc1
v_mov_b32 v231, 192
ds_read_b32 v231, v231 offset:36864
s_waitcnt vmcnt(0)
s_barrier
s_waitcnt lgkmcnt(0)
v_readfirstlane_b32 s62, v231
s_nop 3
s_max_i32 s62, s62, 2
s_min_i32 s62, s62, 66
s_add_u32 s62, s62, -2
s_branch .Lgu_fin
.Lgu_w2:
v_add_u32 v254, -1, v5
v_add_u32 v255, -1, v5
v_add_lshl_u32 v252, v7, 0, 7
v_bfi_b32 v252, s69, v252, v6
v_add_lshl_u32 v253, v5, 0, 7
v_bfi_b32 v253, s69, v253, v4
s_add_u32 m0, s45, 18432
s_xor_b32 s45, s45, 0x10000
s_mov_b32 m0, s45
s_barrier
global_load_lds_dwordx4 v2, s[38:39] offset:-2048 sc1
global_load_lds_dwordx4 v2, s[38:39] offset:-1024 sc1
global_load_lds_dwordx4 v2, s[38:39] offset:0 sc1
global_load_lds_dwordx4 v2, s[38:39] offset:1024 sc1
global_load_lds_dwordx4 v2, s[38:39] offset:2048 sc1
global_load_lds_dwordx4 v2, s[38:39] offset:3072 sc1
v_add_u32 v2, 18688, v2
s_mov_b32 s70, 32
s_waitcnt vmcnt(0) lgkmcnt(0)
s_nop 7
s_branch .Lgu_in8
.p2align 6
.Lgu_top9:
s_waitcnt lgkmcnt(5)
v_mfma_f32_16x16x32_bf16 v[74:77], a[0:3], v[106:109], 0
s_waitcnt lgkmcnt(4)
v_mul_f32 v157, v93, v101
v_cvt_pk_bf16_f32 v162, v154, v155
v_mfma_f32_16x16x32_bf16 v[74:77], a[8:11], v[110:113], v[74:77]
s_waitcnt lgkmcnt(3)
v_cvt_pk_bf16_f32 v163, v156, v157
v_mfma_f32_16x16x32_bf16 v[74:77], a[16:19], v[114:117], v[74:77]
s_waitcnt lgkmcnt(2)
v_mfma_f32_16x16x32_bf16 v[74:77], a[24:27], v[118:121], v[74:77]
s_waitcnt lgkmcnt(1)
v_mul_f32 v158, v94, v102
v_mul_f32 v159, v95, v103
v_mfma_f32_16x16x32_bf16 v[74:77], a[32:35], v[122:125], v[74:77]
s_waitcnt lgkmcnt(0)
v_mul_f32 v160, v96, v104
v_mul_f32 v161, v97, v105
v_mfma_f32_16x16x32_bf16 v[74:77], a[40:43], v[126:129], v[74:77]
ds_read_b128 v[130:133], v8 offset:6144
v_cvt_pk_bf16_f32 v164, v158, v159
v_mfma_f32_16x16x32_bf16 v[82:85], a[144:147], v[106:109], 0
ds_read_b128 v[134:137], v8 offset:7168
v_cvt_pk_bf16_f32 v165, v160, v161
v_mfma_f32_16x16x32_bf16 v[82:85], a[152:155], v[110:113], v[82:85]
v_mfma_f32_16x16x32_bf16 v[82:85], a[160:163], v[114:117], v[82:85]
s_waitcnt vmcnt(6)
s_barrier
v_mfma_f32_16x16x32_bf16 v[82:85], a[168:171], v[118:121], v[82:85]
s_mov_b64 exec, 1
global_store_dword v252, v7, s[38:39] sc1
s_mov_b64 exec, -1
v_mfma_f32_16x16x32_bf16 v[82:85], a[176:179], v[122:125], v[82:85]
v_add_u32 v7, 1, v7
v_add_lshl_u32 v252, v7, 0, 7
v_mfma_f32_16x16x32_bf16 v[82:85], a[184:187], v[126:129], v[82:85]
ds_read_b128 v[138:141], v8 offset:8192
v_bfi_b32 v252, s69, v252, v6
v_mfma_f32_16x16x32_bf16 v[78:81], a[4:7], v[106:109], 0
global_store_dwordx4 v3, v[162:165], s[38:39] sc1
v_mfma_f32_16x16x32_bf16 v[78:81], a[12:15], v[110:113], v[78:81]
v_add_u32 v3, 147456, v3
v_mfma_f32_16x16x32_bf16 v[78:81], a[20:23], v[114:117], v[78:81]
ds_read_b128 v[142:145], v8 offset:9216
v_mfma_f32_16x16x32_bf16 v[78:81], a[28:31], v[118:121], v[78:81]
v_mfma_f32_16x16x32_bf16 v[78:81], a[36:39], v[122:125], v[78:81]
v_mfma_f32_16x16x32_bf16 v[78:81], a[44:47], v[126:129], v[78:81]
ds_read_b128 v[146:149], v8 offset:10240
v_mfma_f32_16x16x32_bf16 v[86:89], a[148:151], v[106:109], 0
v_mfma_f32_16x16x32_bf16 v[86:89], a[156:159], v[110:113], v[86:89]
v_mfma_f32_16x16x32_bf16 v[86:89], a[164:167], v[114:117], v[86:89]
ds_read_b128 v[150:153], v8 offset:11264
v_mfma_f32_16x16x32_bf16 v[86:89], a[172:175], v[118:121], v[86:89]
v_mfma_f32_16x16x32_bf16 v[86:89], a[180:183], v[122:125], v[86:89]
v_mfma_f32_16x16x32_bf16 v[86:89], a[188:191], v[126:129], v[86:89]
s_waitcnt lgkmcnt(5)
v_mfma_f32_16x16x32_bf16 v[74:77], a[48:51], v[130:133], v[74:77]
s_waitcnt lgkmcnt(4)
v_mfma_f32_16x16x32_bf16 v[74:77], a[56:59], v[134:137], v[74:77]
s_waitcnt lgkmcnt(3)
v_mfma_f32_16x16x32_bf16 v[74:77], a[64:67], v[138:141], v[74:77]
s_waitcnt lgkmcnt(2)
v_mfma_f32_16x16x32_bf16 v[74:77], a[72:75], v[142:145], v[74:77]
s_waitcnt lgkmcnt(1)
v_mfma_f32_16x16x32_bf16 v[74:77], a[80:83], v[146:149], v[74:77]
s_waitcnt lgkmcnt(0)
v_mfma_f32_16x16x32_bf16 v[74:77], a[88:91], v[150:153], v[74:77]
ds_read_b128 v[106:109], v8 offset:12288
v_mfma_f32_16x16x32_bf16 v[82:85], a[192:195], v[130:133], v[82:85]
v_mfma_f32_16x16x32_bf16 v[82:85], a[200:203], v[134:137], v[82:85]
v_mfma_f32_16x16x32_bf16 v[82:85], a[208:211], v[138:141], v[82:85]
ds_read_b128 v[110:113], v8 offset:13312
v_mfma_f32_16x16x32_bf16 v[82:85], a[216:219], v[142:145], v[82:85]
v_mfma_f32_16x16x32_bf16 v[82:85], a[224:227], v[146:149], v[82:85]
v_mfma_f32_16x16x32_bf16 v[82:85], a[232:235], v[150:153], v[82:85]
ds_read_b128 v[114:117], v8 offset:14336
v_mfma_f32_16x16x32_bf16 v[78:81], a[52:55], v[130:133], v[78:81]
v_mfma_f32_16x16x32_bf16 v[78:81], a[60:63], v[134:137], v[78:81]
v_mfma_f32_16x16x32_bf16 v[78:81], a[68:71], v[138:141], v[78:81]
ds_read_b128 v[118:121], v8 offset:15360
v_mfma_f32_16x16x32_bf16 v[78:81], a[76:79], v[142:145], v[78:81]
v_mfma_f32_16x16x32_bf16 v[78:81], a[84:87], v[146:149], v[78:81]
v_mfma_f32_16x16x32_bf16 v[78:81], a[92:95], v[150:153], v[78:81]
ds_read_b128 v[122:125], v8 offset:16384
v_mfma_f32_16x16x32_bf16 v[86:89], a[196:199], v[130:133], v[86:89]
v_mfma_f32_16x16x32_bf16 v[86:89], a[204:207], v[134:137], v[86:89]
v_mfma_f32_16x16x32_bf16 v[86:89], a[212:215], v[138:141], v[86:89]
ds_read_b128 v[126:129], v8 offset:17408
v_mfma_f32_16x16x32_bf16 v[86:89], a[220:223], v[142:145], v[86:89]
s_xor_b32 s45, s45, 0x10000
v_mfma_f32_16x16x32_bf16 v[86:89], a[228:231], v[146:149], v[86:89]
s_add_u32 m0, s45, 0
v_mfma_f32_16x16x32_bf16 v[86:89], a[236:239], v[150:153], v[86:89]
s_waitcnt lgkmcnt(5)
v_mfma_f32_16x16x32_bf16 v[74:77], a[96:99], v[106:109], v[74:77]
s_barrier
s_waitcnt lgkmcnt(4)
v_mfma_f32_16x16x32_bf16 v[74:77], a[104:107], v[110:113], v[74:77]
s_waitcnt lgkmcnt(3)
v_mfma_f32_16x16x32_bf16 v[74:77], a[112:115], v[114:117], v[74:77]
s_waitcnt lgkmcnt(2)
global_load_lds_dwordx4 v2, s[38:39] offset:-2048 sc1
v_mfma_f32_16x16x32_bf16 v[74:77], a[120:123], v[118:121], v[74:77]
s_waitcnt lgkmcnt(1)
v_mfma_f32_16x16x32_bf16 v[74:77], a[128:131], v[122:125], v[74:77]
s_waitcnt lgkmcnt(0)
v_mfma_f32_16x16x32_bf16 v[74:77], a[136:139], v[126:129], v[74:77]
global_load_lds_dwordx4 v2, s[38:39] offset:-1024 sc1
v_mfma_f32_16x16x32_bf16 v[82:85], v[26:29], v[106:109], v[82:85]
v_mfma_f32_16x16x32_bf16 v[82:85], v[34:37], v[110:113], v[82:85]
v_mfma_f32_16x16x32_bf16 v[82:85], v[42:45], v[114:117], v[82:85]
global_load_lds_dwordx4 v2, s[38:39] offset:0 sc1
v_mfma_f32_16x16x32_bf16 v[82:85], v[50:53], v[118:121], v[82:85]
v_mfma_f32_16x16x32_bf16 v[82:85], v[58:61], v[122:125], v[82:85]
v_mfma_f32_16x16x32_bf16 v[82:85], v[66:69], v[126:129], v[82:85]
v_mfma_f32_16x16x32_bf16 v[78:81], a[100:103], v[106:109], v[78:81]
s_waitcnt vmcnt(5)
s_barrier
v_mfma_f32_16x16x32_bf16 v[78:81], a[108:111], v[110:113], v[78:81]
global_load_lds_dwordx4 v2, s[38:39] offset:1024 sc1
v_mfma_f32_16x16x32_bf16 v[78:81], a[116:119], v[114:117], v[78:81]
v_mfma_f32_16x16x32_bf16 v[78:81], a[124:127], v[118:121], v[78:81]
ds_read_b128 v[130:133], v8 offset:18432
v_mfma_f32_16x16x32_bf16 v[78:81], a[132:135], v[122:125], v[78:81]
ds_read_b128 v[134:137], v8 offset:19456
global_load_lds_dwordx4 v2, s[38:39] offset:2048 sc1
v_mfma_f32_16x16x32_bf16 v[78:81], a[140:143], v[126:129], v[78:81]
ds_read_b128 v[138:141], v8 offset:20480
v_mul_f32 v154, v74, v82
v_mfma_f32_16x16x32_bf16 v[86:89], v[30:33], v[106:109], v[86:89]
ds_read_b128 v[142:145], v8 offset:21504
v_mul_f32 v155, v75, v83
v_mfma_f32_16x16x32_bf16 v[86:89], v[38:41], v[110:113], v[86:89]
ds_read_b128 v[146:149], v8 offset:22528
global_load_lds_dwordx4 v2, s[38:39] offset:3072 sc1
v_mfma_f32_16x16x32_bf16 v[86:89], v[46:49], v[114:117], v[86:89]
ds_read_b128 v[150:153], v8 offset:23552
v_add_u32 v2, 18688, v2
v_mfma_f32_16x16x32_bf16 v[86:89], v[54:57], v[118:121], v[86:89]
v_min_u32 v2, v2, v9
v_mul_f32 v156, v76, v84
v_mfma_f32_16x16x32_bf16 v[86:89], v[62:65], v[122:125], v[86:89]
v_mul_f32 v157, v77, v85
v_cvt_pk_bf16_f32 v162, v154, v155
)"
R"(v_mfma_f32_16x16x32_bf16 v[86:89], v[70:73], v[126:129], v[86:89]
s_waitcnt lgkmcnt(5)
v_cvt_pk_bf16_f32 v163, v156, v157
v_mfma_f32_16x16x32_bf16 v[90:93], a[0:3], v[130:133], 0
s_waitcnt lgkmcnt(4)
v_mfma_f32_16x16x32_bf16 v[90:93], a[8:11], v[134:137], v[90:93]
s_waitcnt lgkmcnt(3)
v_mfma_f32_16x16x32_bf16 v[90:93], a[16:19], v[138:141], v[90:93]
s_waitcnt lgkmcnt(2)
v_mfma_f32_16x16x32_bf16 v[90:93], a[24:27], v[142:145], v[90:93]
s_waitcnt lgkmcnt(1)
v_mul_f32 v158, v78, v86
v_mul_f32 v159, v79, v87
v_mfma_f32_16x16x32_bf16 v[90:93], a[32:35], v[146:149], v[90:93]
s_waitcnt lgkmcnt(0)
v_mul_f32 v160, v80, v88
v_mul_f32 v161, v81, v89
v_mfma_f32_16x16x32_bf16 v[90:93], a[40:43], v[150:153], v[90:93]
ds_read_b128 v[106:109], v8 offset:24576
v_cvt_pk_bf16_f32 v164, v158, v159
v_mfma_f32_16x16x32_bf16 v[98:101], a[144:147], v[130:133], 0
ds_read_b128 v[110:113], v8 offset:25600
v_cvt_pk_bf16_f32 v165, v160, v161
v_mfma_f32_16x16x32_bf16 v[98:101], a[152:155], v[134:137], v[98:101]
v_mfma_f32_16x16x32_bf16 v[98:101], a[160:163], v[138:141], v[98:101]
s_waitcnt vmcnt(6)
s_barrier
v_mfma_f32_16x16x32_bf16 v[98:101], a[168:171], v[142:145], v[98:101]
s_mov_b64 exec, 1
global_store_dword v252, v7, s[38:39] sc1
s_mov_b64 exec, -1
v_mfma_f32_16x16x32_bf16 v[98:101], a[176:179], v[146:149], v[98:101]
v_add_u32 v7, 1, v7
v_add_lshl_u32 v252, v7, 0, 7
v_mfma_f32_16x16x32_bf16 v[98:101], a[184:187], v[150:153], v[98:101]
ds_read_b128 v[114:117], v8 offset:26624
v_bfi_b32 v252, s69, v252, v6
v_mfma_f32_16x16x32_bf16 v[94:97], a[4:7], v[130:133], 0
global_store_dwordx4 v3, v[162:165], s[38:39] sc1
v_mfma_f32_16x16x32_bf16 v[94:97], a[12:15], v[134:137], v[94:97]
v_add_u32 v3, 147456, v3
v_mfma_f32_16x16x32_bf16 v[94:97], a[20:23], v[138:141], v[94:97]
ds_read_b128 v[118:121], v8 offset:27648
v_mfma_f32_16x16x32_bf16 v[94:97], a[28:31], v[142:145], v[94:97]
v_mfma_f32_16x16x32_bf16 v[94:97], a[36:39], v[146:149], v[94:97]
v_mfma_f32_16x16x32_bf16 v[94:97], a[44:47], v[150:153], v[94:97]
ds_read_b128 v[122:125], v8 offset:28672
v_mfma_f32_16x16x32_bf16 v[102:105], a[148:151], v[130:133], 0
v_mfma_f32_16x16x32_bf16 v[102:105], a[156:159], v[134:137], v[102:105]
v_mfma_f32_16x16x32_bf16 v[102:105], a[164:167], v[138:141], v[102:105]
ds_read_b128 v[126:129], v8 offset:29696
v_mfma_f32_16x16x32_bf16 v[102:105], a[172:175], v[142:145], v[102:105]
v_mfma_f32_16x16x32_bf16 v[102:105], a[180:183], v[146:149], v[102:105]
v_mfma_f32_16x16x32_bf16 v[102:105], a[188:191], v[150:153], v[102:105]
s_waitcnt lgkmcnt(5)
v_mfma_f32_16x16x32_bf16 v[90:93], a[48:51], v[106:109], v[90:93]
s_waitcnt lgkmcnt(4)
v_mfma_f32_16x16x32_bf16 v[90:93], a[56:59], v[110:113], v[90:93]
s_waitcnt lgkmcnt(3)
v_mfma_f32_16x16x32_bf16 v[90:93], a[64:67], v[114:117], v[90:93]
s_waitcnt lgkmcnt(2)
v_mfma_f32_16x16x32_bf16 v[90:93], a[72:75], v[118:121], v[90:93]
s_waitcnt lgkmcnt(1)
v_mfma_f32_16x16x32_bf16 v[90:93], a[80:83], v[122:125], v[90:93]
s_waitcnt lgkmcnt(0)
v_mfma_f32_16x16x32_bf16 v[90:93], a[88:91], v[126:129], v[90:93]
ds_read_b128 v[130:133], v8 offset:30720
v_mfma_f32_16x16x32_bf16 v[98:101], a[192:195], v[106:109], v[98:101]
v_mfma_f32_16x16x32_bf16 v[98:101], a[200:203], v[110:113], v[98:101]
v_mfma_f32_16x16x32_bf16 v[98:101], a[208:211], v[114:117], v[98:101]
ds_read_b128 v[134:137], v8 offset:31744
v_mfma_f32_16x16x32_bf16 v[98:101], a[216:219], v[118:121], v[98:101]
v_mfma_f32_16x16x32_bf16 v[98:101], a[224:227], v[122:125], v[98:101]
v_mfma_f32_16x16x32_bf16 v[98:101], a[232:235], v[126:129], v[98:101]
ds_read_b128 v[138:141], v8 offset:32768
v_mfma_f32_16x16x32_bf16 v[94:97], a[52:55], v[106:109], v[94:97]
v_mfma_f32_16x16x32_bf16 v[94:97], a[60:63], v[110:113], v[94:97]
v_mfma_f32_16x16x32_bf16 v[94:97], a[68:71], v[114:117], v[94:97]
ds_read_b128 v[142:145], v8 offset:33792
v_mfma_f32_16x16x32_bf16 v[94:97], a[76:79], v[118:121], v[94:97]
v_mfma_f32_16x16x32_bf16 v[94:97], a[84:87], v[122:125], v[94:97]
v_mfma_f32_16x16x32_bf16 v[94:97], a[92:95], v[126:129], v[94:97]
ds_read_b128 v[146:149], v8 offset:34816
v_mfma_f32_16x16x32_bf16 v[102:105], a[196:199], v[106:109], v[102:105]
.Lgu_in8:
v_mfma_f32_16x16x32_bf16 v[102:105], a[204:207], v[110:113], v[102:105]
v_mfma_f32_16x16x32_bf16 v[102:105], a[212:215], v[114:117], v[102:105]
ds_read_b128 v[150:153], v8 offset:35840
v_mfma_f32_16x16x32_bf16 v[102:105], a[220:223], v[118:121], v[102:105]
v_mfma_f32_16x16x32_bf16 v[102:105], a[228:231], v[122:125], v[102:105]
s_add_u32 m0, s45, 18432
v_mfma_f32_16x16x32_bf16 v[102:105], a[236:239], v[126:129], v[102:105]
s_waitcnt lgkmcnt(5)
v_mfma_f32_16x16x32_bf16 v[90:93], a[96:99], v[130:133], v[90:93]
s_barrier
s_waitcnt lgkmcnt(4)
v_mfma_f32_16x16x32_bf16 v[90:93], a[104:107], v[134:137], v[90:93]
s_waitcnt lgkmcnt(3)
v_mfma_f32_16x16x32_bf16 v[90:93], a[112:115], v[138:141], v[90:93]
s_waitcnt lgkmcnt(2)
global_load_lds_dwordx4 v2, s[38:39] offset:-2048 sc1
v_mfma_f32_16x16x32_bf16 v[90:93], a[120:123], v[142:145], v[90:93]
s_waitcnt lgkmcnt(1)
v_mfma_f32_16x16x32_bf16 v[90:93], a[128:131], v[146:149], v[90:93]
s_waitcnt lgkmcnt(0)
v_mfma_f32_16x16x32_bf16 v[90:93], a[136:139], v[150:153], v[90:93]
global_load_lds_dwordx4 v2, s[38:39] offset:-1024 sc1
v_mfma_f32_16x16x32_bf16 v[98:101], v[26:29], v[130:133], v[98:101]
v_mfma_f32_16x16x32_bf16 v[98:101], v[34:37], v[134:137], v[98:101]
v_mfma_f32_16x16x32_bf16 v[98:101], v[42:45], v[138:141], v[98:101]
global_load_lds_dwordx4 v2, s[38:39] offset:0 sc1
v_mfma_f32_16x16x32_bf16 v[98:101], v[50:53], v[142:145], v[98:101]
v_xor_b32 v8, 0x10000, v8
v_mfma_f32_16x16x32_bf16 v[98:101], v[58:61], v[146:149], v[98:101]
v_mfma_f32_16x16x32_bf16 v[98:101], v[66:69], v[150:153], v[98:101]
v_mfma_f32_16x16x32_bf16 v[94:97], a[100:103], v[130:133], v[94:97]
s_waitcnt vmcnt(5)
s_barrier
v_mfma_f32_16x16x32_bf16 v[94:97], a[108:111], v[134:137], v[94:97]
global_load_lds_dwordx4 v2, s[38:39] offset:1024 sc1
v_mfma_f32_16x16x32_bf16 v[94:97], a[116:119], v[138:141], v[94:97]
v_mfma_f32_16x16x32_bf16 v[94:97], a[124:127], v[142:145], v[94:97]
ds_read_b128 v[106:109], v8 offset:0
v_mfma_f32_16x16x32_bf16 v[94:97], a[132:135], v[146:149], v[94:97]
ds_read_b128 v[110:113], v8 offset:1024
global_load_lds_dwordx4 v2, s[38:39] offset:2048 sc1
v_mfma_f32_16x16x32_bf16 v[94:97], a[140:143], v[150:153], v[94:97]
ds_read_b128 v[114:117], v8 offset:2048
v_mul_f32 v154, v90, v98
v_mfma_f32_16x16x32_bf16 v[102:105], v[30:33], v[130:133], v[102:105]
ds_read_b128 v[118:121], v8 offset:3072
v_mul_f32 v155, v91, v99
v_mfma_f32_16x16x32_bf16 v[102:105], v[38:41], v[134:137], v[102:105]
ds_read_b128 v[122:125], v8 offset:4096
global_load_lds_dwordx4 v2, s[38:39] offset:3072 sc1
v_mfma_f32_16x16x32_bf16 v[102:105], v[46:49], v[138:141], v[102:105]
ds_read_b128 v[126:129], v8 offset:5120
v_add_u32 v2, 18688, v2
v_mfma_f32_16x16x32_bf16 v[102:105], v[54:57], v[142:145], v[102:105]
v_min_u32 v2, v2, v9
v_mul_f32 v156, v92, v100
v_mfma_f32_16x16x32_bf16 v[102:105], v[62:65], v[146:149], v[102:105]
v_mfma_f32_16x16x32_bf16 v[102:105], v[70:73], v[150:153], v[102:105]
s_sub_u32 s70, s70, 1
s_cbranch_scc0 .Lgu_top9
s_waitcnt vmcnt(6) lgkmcnt(0)
s_barrier
s_mov_b64 exec, 1
global_store_dword v252, v7, s[38:39] sc1
s_mov_b64 exec, -1
v_mul_f32 v154, v90, v98
v_mul_f32 v155, v91, v99
v_mul_f32 v156, v92, v100
v_mul_f32 v157, v93, v101
v_cvt_pk_bf16_f32 v162, v154, v155
v_cvt_pk_bf16_f32 v163, v156, v157
v_mul_f32 v158, v94, v102
v_mul_f32 v159, v95, v103
v_mul_f32 v160, v96, v104
v_mul_f32 v161, v97, v105
v_cvt_pk_bf16_f32 v164, v158, v159
v_cvt_pk_bf16_f32 v165, v160, v161
global_store_dwordx4 v3, v[162:165], s[38:39] sc1
v_mov_b32 v231, 192
ds_read_b32 v231, v231 offset:36864
s_waitcnt vmcnt(0)
s_barrier
v_add_u32 v7, 8, v7
v_mbcnt_lo_u32_b32 v252, -1, 0
v_mbcnt_hi_u32_b32 v252, -1, v252
v_add_lshl_u32 v252, v252, 0, 7
v_bfi_b32 v252, s69, v252, v6
s_mov_b64 exec, 0xff
global_store_dword v252, v7, s[38:39] sc1
s_mov_b64 exec, -1
s_waitcnt lgkmcnt(0)
v_readfirstlane_b32 s62, v231
s_nop 3
s_max_i32 s62, s62, 2
s_min_i32 s62, s62, 66
s_add_u32 s62, s62, -1
s_branch .Lgu_fin
.Lgu_w3:
v_add_u32 v254, -1, v5
v_add_u32 v255, -1, v5
v_add_lshl_u32 v252, v7, 0, 7
v_bfi_b32 v252, s69, v252, v6
v_add_lshl_u32 v253, v5, 0, 7
v_bfi_b32 v253, s69, v253, v4
s_add_u32 m0, s45, 18432
v_add_lshl_u32 v253, v5, 0, 7
v_bfi_b32 v253, s69, v253, v4
global_load_dword v254, v253, s[38:39] sc1
s_waitcnt vmcnt(0)
v_sub_u32 v254, v254, v5
v_cmp_lt_u32 vcc, 0x7f, v254
s_cbranch_vccnz .Lgu_sp16
.Lgu_rt17:
v_add_u32 v5, 1, v5
s_xor_b32 s45, s45, 0x10000
s_mov_b32 m0, s45
s_barrier
global_load_lds_dwordx4 v2, s[38:39] offset:-2048 sc1
global_load_lds_dwordx4 v2, s[38:39] offset:-1024 sc1
global_load_lds_dwordx4 v2, s[38:39] offset:0 sc1
global_load_lds_dwordx4 v2, s[38:39] offset:1024 sc1
global_load_lds_dwordx4 v2, s[38:39] offset:2048 sc1
global_load_lds_dwordx4 v2, s[38:39] offset:3072 sc1
v_add_u32 v2, 18688, v2
s_mov_b32 s70, 32
s_waitcnt vmcnt(0) lgkmcnt(0)
s_nop 7
s_branch .Lgu_in15
.p2align 6
.Lgu_top18:
s_waitcnt lgkmcnt(5)
v_mfma_f32_16x16x32_bf16 v[74:77], a[0:3], v[106:109], 0
s_waitcnt lgkmcnt(4)
v_mul_f32 v157, v93, v101
v_cvt_pk_bf16_f32 v162, v154, v155
v_mfma_f32_16x16x32_bf16 v[74:77], a[8:11], v[110:113], v[74:77]
s_waitcnt lgkmcnt(3)
v_cvt_pk_bf16_f32 v163, v156, v157
v_mfma_f32_16x16x32_bf16 v[74:77], a[16:19], v[114:117], v[74:77]
s_waitcnt lgkmcnt(2)
global_load_dword v254, v253, s[38:39] sc1
v_mfma_f32_16x16x32_bf16 v[74:77], a[24:27], v[118:121], v[74:77]
s_waitcnt lgkmcnt(1)
v_mul_f32 v158, v94, v102
v_mul_f32 v159, v95, v103
v_mfma_f32_16x16x32_bf16 v[74:77], a[32:35], v[122:125], v[74:77]
s_waitcnt lgkmcnt(0)
v_mul_f32 v160, v96, v104
v_mul_f32 v161, v97, v105
v_mfma_f32_16x16x32_bf16 v[74:77], a[40:43], v[126:129], v[74:77]
ds_read_b128 v[130:133], v8 offset:6144
v_cvt_pk_bf16_f32 v164, v158, v159
v_mfma_f32_16x16x32_bf16 v[82:85], a[144:147], v[106:109], 0
ds_read_b128 v[134:137], v8 offset:7168
v_cvt_pk_bf16_f32 v165, v160, v161
v_mfma_f32_16x16x32_bf16 v[82:85], a[152:155], v[110:113], v[82:85]
v_mfma_f32_16x16x32_bf16 v[82:85], a[160:163], v[114:117], v[82:85]
s_waitcnt vmcnt(7)
s_barrier
v_mfma_f32_16x16x32_bf16 v[82:85], a[168:171], v[118:121], v[82:85]
v_mfma_f32_16x16x32_bf16 v[82:85], a[176:179], v[122:125], v[82:85]
v_mfma_f32_16x16x32_bf16 v[82:85], a[184:187], v[126:129], v[82:85]
ds_read_b128 v[138:141], v8 offset:8192
v_mfma_f32_16x16x32_bf16 v[78:81], a[4:7], v[106:109], 0
global_store_dwordx4 v3, v[162:165], s[38:39] sc1
v_mfma_f32_16x16x32_bf16 v[78:81], a[12:15], v[110:113], v[78:81]
v_add_u32 v3, 147456, v3
v_mfma_f32_16x16x32_bf16 v[78:81], a[20:23], v[114:117], v[78:81]
ds_read_b128 v[142:145], v8 offset:9216
v_mfma_f32_16x16x32_bf16 v[78:81], a[28:31], v[118:121], v[78:81]
v_mfma_f32_16x16x32_bf16 v[78:81], a[36:39], v[122:125], v[78:81]
v_mfma_f32_16x16x32_bf16 v[78:81], a[44:47], v[126:129], v[78:81]
ds_read_b128 v[146:149], v8 offset:10240
v_mfma_f32_16x16x32_bf16 v[86:89], a[148:151], v[106:109], 0
v_mfma_f32_16x16x32_bf16 v[86:89], a[156:159], v[110:113], v[86:89]
v_mfma_f32_16x16x32_bf16 v[86:89], a[164:167], v[114:117], v[86:89]
ds_read_b128 v[150:153], v8 offset:11264
v_mfma_f32_16x16x32_bf16 v[86:89], a[172:175], v[118:121], v[86:89]
v_mfma_f32_16x16x32_bf16 v[86:89], a[180:183], v[122:125], v[86:89]
v_mfma_f32_16x16x32_bf16 v[86:89], a[188:191], v[126:129], v[86:89]
s_waitcnt lgkmcnt(5)
v_mfma_f32_16x16x32_bf16 v[74:77], a[48:51], v[130:133], v[74:77]
s_waitcnt lgkmcnt(4)
v_mfma_f32_16x16x32_bf16 v[74:77], a[56:59], v[134:137], v[74:77]
s_waitcnt lgkmcnt(3)
v_mfma_f32_16x16x32_bf16 v[74:77], a[64:67], v[138:141], v[74:77]
s_waitcnt lgkmcnt(2)
v_mfma_f32_16x16x32_bf16 v[74:77], a[72:75], v[142:145], v[74:77]
s_waitcnt lgkmcnt(1)
v_mfma_f32_16x16x32_bf16 v[74:77], a[80:83], v[146:149], v[74:77]
s_waitcnt lgkmcnt(0)
v_mfma_f32_16x16x32_bf16 v[74:77], a[88:91], v[150:153], v[74:77]
ds_read_b128 v[106:109], v8 offset:12288
v_mfma_f32_16x16x32_bf16 v[82:85], a[192:195], v[130:133], v[82:85]
v_mfma_f32_16x16x32_bf16 v[82:85], a[200:203], v[134:137], v[82:85]
v_mfma_f32_16x16x32_bf16 v[82:85], a[208:211], v[138:141], v[82:85]
ds_read_b128 v[110:113], v8 offset:13312
v_mfma_f32_16x16x32_bf16 v[82:85], a[216:219], v[142:145], v[82:85]
v_mfma_f32_16x16x32_bf16 v[82:85], a[224:227], v[146:149], v[82:85]
v_mfma_f32_16x16x32_bf16 v[82:85], a[232:235], v[150:153], v[82:85]
ds_read_b128 v[114:117], v8 offset:14336
v_mfma_f32_16x16x32_bf16 v[78:81], a[52:55], v[130:133], v[78:81]
v_mfma_f32_16x16x32_bf16 v[78:81], a[60:63], v[134:137], v[78:81]
v_mfma_f32_16x16x32_bf16 v[78:81], a[68:71], v[138:141], v[78:81]
ds_read_b128 v[118:121], v8 offset:15360
v_mfma_f32_16x16x32_bf16 v[78:81], a[76:79], v[142:145], v[78:81]
v_mfma_f32_16x16x32_bf16 v[78:81], a[84:87], v[146:149], v[78:81]
v_mfma_f32_16x16x32_bf16 v[78:81], a[92:95], v[150:153], v[78:81]
ds_read_b128 v[122:125], v8 offset:16384
v_mfma_f32_16x16x32_bf16 v[86:89], a[196:199], v[130:133], v[86:89]
v_mfma_f32_16x16x32_bf16 v[86:89], a[204:207], v[134:137], v[86:89]
s_waitcnt vmcnt(1)
v_sub_u32 v254, v254, v5
v_cmp_lt_u32 vcc, 0x7f, v254
v_mfma_f32_16x16x32_bf16 v[86:89], a[212:215], v[138:141], v[86:89]
ds_read_b128 v[126:129], v8 offset:17408
v_mfma_f32_16x16x32_bf16 v[86:89], a[220:223], v[142:145], v[86:89]
s_xor_b32 s45, s45, 0x10000
v_mfma_f32_16x16x32_bf16 v[86:89], a[228:231], v[146:149], v[86:89]
s_cbranch_vccnz .Lgu_sp11
.Lgu_rt12:
s_add_u32 m0, s45, 0
v_mfma_f32_16x16x32_bf16 v[86:89], a[236:239], v[150:153], v[86:89]
s_waitcnt lgkmcnt(5)
v_mfma_f32_16x16x32_bf16 v[74:77], a[96:99], v[106:109], v[74:77]
s_barrier
s_waitcnt lgkmcnt(4)
v_mfma_f32_16x16x32_bf16 v[74:77], a[104:107], v[110:113], v[74:77]
s_waitcnt lgkmcnt(3)
v_add_u32 v5, 1, v5
v_add_lshl_u32 v253, v5, 0, 7
v_mfma_f32_16x16x32_bf16 v[74:77], a[112:115], v[114:117], v[74:77]
s_waitcnt lgkmcnt(2)
v_bfi_b32 v253, s69, v253, v4
v_mfma_f32_16x16x32_bf16 v[74:77], a[120:123], v[118:121], v[74:77]
s_waitcnt lgkmcnt(1)
global_load_lds_dwordx4 v2, s[38:39] offset:-2048 sc1
v_mfma_f32_16x16x32_bf16 v[74:77], a[128:131], v[122:125], v[74:77]
s_waitcnt lgkmcnt(0)
v_mfma_f32_16x16x32_bf16 v[74:77], a[136:139], v[126:129], v[74:77]
v_mfma_f32_16x16x32_bf16 v[82:85], v[26:29], v[106:109], v[82:85]
global_load_lds_dwordx4 v2, s[38:39] offset:-1024 sc1
v_mfma_f32_16x16x32_bf16 v[82:85], v[34:37], v[110:113], v[82:85]
v_mfma_f32_16x16x32_bf16 v[82:85], v[42:45], v[114:117], v[82:85]
v_mfma_f32_16x16x32_bf16 v[82:85], v[50:53], v[118:121], v[82:85]
global_load_lds_dwordx4 v2, s[38:39] offset:0 sc1
v_mfma_f32_16x16x32_bf16 v[82:85], v[58:61], v[122:125], v[82:85]
v_mfma_f32_16x16x32_bf16 v[82:85], v[66:69], v[126:129], v[82:85]
v_mfma_f32_16x16x32_bf16 v[78:81], a[100:103], v[106:109], v[78:81]
s_barrier
v_mfma_f32_16x16x32_bf16 v[78:81], a[108:111], v[110:113], v[78:81]
global_load_lds_dwordx4 v2, s[38:39] offset:1024 sc1
v_mfma_f32_16x16x32_bf16 v[78:81], a[116:119], v[114:117], v[78:81]
v_mfma_f32_16x16x32_bf16 v[78:81], a[124:127], v[118:121], v[78:81]
ds_read_b128 v[130:133], v8 offset:18432
v_mfma_f32_16x16x32_bf16 v[78:81], a[132:135], v[122:125], v[78:81]
ds_read_b128 v[134:137], v8 offset:19456
global_load_lds_dwordx4 v2, s[38:39] offset:2048 sc1
v_mfma_f32_16x16x32_bf16 v[78:81], a[140:143], v[126:129], v[78:81]
ds_read_b128 v[138:141], v8 offset:20480
v_mul_f32 v154, v74, v82
v_mfma_f32_16x16x32_bf16 v[86:89], v[30:33], v[106:109], v[86:89]
ds_read_b128 v[142:145], v8 offset:21504
v_mul_f32 v155, v75, v83
v_mfma_f32_16x16x32_bf16 v[86:89], v[38:41], v[110:113], v[86:89]
ds_read_b128 v[146:149], v8 offset:22528
global_load_lds_dwordx4 v2, s[38:39] offset:3072 sc1
v_mfma_f32_16x16x32_bf16 v[86:89], v[46:49], v[114:117], v[86:89]
ds_read_b128 v[150:153], v8 offset:23552
v_add_u32 v2, 18688, v2
v_mfma_f32_16x16x32_bf16 v[86:89], v[54:57], v[118:121], v[86:89]
v_min_u32 v2, v2, v9
v_mul_f32 v156, v76, v84
v_mfma_f32_16x16x32_bf16 v[86:89], v[62:65], v[122:125], v[86:89]
v_mul_f32 v157, v77, v85
v_cvt_pk_bf16_f32 v162, v154, v155
v_mfma_f32_16x16x32_bf16 v[86:89], v[70:73], v[126:129], v[86:89]
s_waitcnt lgkmcnt(5)
v_cvt_pk_bf16_f32 v163, v156, v157
v_mfma_f32_16x16x32_bf16 v[90:93], a[0:3], v[130:133], 0
s_waitcnt lgkmcnt(4)
v_mfma_f32_16x16x32_bf16 v[90:93], a[8:11], v[134:137], v[90:93]
s_waitcnt lgkmcnt(3)
v_mfma_f32_16x16x32_bf16 v[90:93], a[16:19], v[138:141], v[90:93]
s_waitcnt lgkmcnt(2)
global_load_dword v255, v253, s[38:39] sc1
v_mfma_f32_16x16x32_bf16 v[90:93], a[24:27], v[142:145], v[90:93]
s_waitcnt lgkmcnt(1)
v_mul_f32 v158, v78, v86
v_mul_f32 v159, v79, v87
v_mfma_f32_16x16x32_bf16 v[90:93], a[32:35], v[146:149], v[90:93]
s_waitcnt lgkmcnt(0)
v_mul_f32 v160, v80, v88
v_mul_f32 v161, v81, v89
v_mfma_f32_16x16x32_bf16 v[90:93], a[40:43], v[150:153], v[90:93]
ds_read_b128 v[106:109], v8 offset:24576
v_cvt_pk_bf16_f32 v164, v158, v159
v_mfma_f32_16x16x32_bf16 v[98:101], a[144:147], v[130:133], 0
ds_read_b128 v[110:113], v8 offset:25600
v_cvt_pk_bf16_f32 v165, v160, v161
v_mfma_f32_16x16x32_bf16 v[98:101], a[152:155], v[134:137], v[98:101]
v_mfma_f32_16x16x32_bf16 v[98:101], a[160:163], v[138:141], v[98:101]
s_waitcnt vmcnt(7)
s_barrier
v_mfma_f32_16x16x32_bf16 v[98:101], a[168:171], v[142:145], v[98:101]
v_mfma_f32_16x16x32_bf16 v[98:101], a[176:179], v[146:149], v[98:101]
v_mfma_f32_16x16x32_bf16 v[98:101], a[184:187], v[150:153], v[98:101]
ds_read_b128 v[114:117], v8 offset:26624
v_mfma_f32_16x16x32_bf16 v[94:97], a[4:7], v[130:133], 0
global_store_dwordx4 v3, v[162:165], s[38:39] sc1
v_mfma_f32_16x16x32_bf16 v[94:97], a[12:15], v[134:137], v[94:97]
v_add_u32 v3, 147456, v3
v_mfma_f32_16x16x32_bf16 v[94:97], a[20:23], v[138:141], v[94:97]
ds_read_b128 v[118:121], v8 offset:27648
v_mfma_f32_16x16x32_bf16 v[94:97], a[28:31], v[142:145], v[94:97]
)"
R"(v_mfma_f32_16x16x32_bf16 v[94:97], a[36:39], v[146:149], v[94:97]
v_mfma_f32_16x16x32_bf16 v[94:97], a[44:47], v[150:153], v[94:97]
ds_read_b128 v[122:125], v8 offset:28672
v_mfma_f32_16x16x32_bf16 v[102:105], a[148:151], v[130:133], 0
v_mfma_f32_16x16x32_bf16 v[102:105], a[156:159], v[134:137], v[102:105]
v_mfma_f32_16x16x32_bf16 v[102:105], a[164:167], v[138:141], v[102:105]
ds_read_b128 v[126:129], v8 offset:29696
v_mfma_f32_16x16x32_bf16 v[102:105], a[172:175], v[142:145], v[102:105]
v_mfma_f32_16x16x32_bf16 v[102:105], a[180:183], v[146:149], v[102:105]
v_mfma_f32_16x16x32_bf16 v[102:105], a[188:191], v[150:153], v[102:105]
s_waitcnt lgkmcnt(5)
v_mfma_f32_16x16x32_bf16 v[90:93], a[48:51], v[106:109], v[90:93]
s_waitcnt lgkmcnt(4)
v_mfma_f32_16x16x32_bf16 v[90:93], a[56:59], v[110:113], v[90:93]
s_waitcnt lgkmcnt(3)
v_mfma_f32_16x16x32_bf16 v[90:93], a[64:67], v[114:117], v[90:93]
s_waitcnt lgkmcnt(2)
v_mfma_f32_16x16x32_bf16 v[90:93], a[72:75], v[118:121], v[90:93]
s_waitcnt lgkmcnt(1)
v_mfma_f32_16x16x32_bf16 v[90:93], a[80:83], v[122:125], v[90:93]
s_waitcnt lgkmcnt(0)
v_mfma_f32_16x16x32_bf16 v[90:93], a[88:91], v[126:129], v[90:93]
ds_read_b128 v[130:133], v8 offset:30720
v_mfma_f32_16x16x32_bf16 v[98:101], a[192:195], v[106:109], v[98:101]
v_mfma_f32_16x16x32_bf16 v[98:101], a[200:203], v[110:113], v[98:101]
v_mfma_f32_16x16x32_bf16 v[98:101], a[208:211], v[114:117], v[98:101]
ds_read_b128 v[134:137], v8 offset:31744
v_mfma_f32_16x16x32_bf16 v[98:101], a[216:219], v[118:121], v[98:101]
v_mfma_f32_16x16x32_bf16 v[98:101], a[224:227], v[122:125], v[98:101]
v_mfma_f32_16x16x32_bf16 v[98:101], a[232:235], v[126:129], v[98:101]
ds_read_b128 v[138:141], v8 offset:32768
v_mfma_f32_16x16x32_bf16 v[94:97], a[52:55], v[106:109], v[94:97]
v_mfma_f32_16x16x32_bf16 v[94:97], a[60:63], v[110:113], v[94:97]
v_mfma_f32_16x16x32_bf16 v[94:97], a[68:71], v[114:117], v[94:97]
ds_read_b128 v[142:145], v8 offset:33792
v_mfma_f32_16x16x32_bf16 v[94:97], a[76:79], v[118:121], v[94:97]
v_mfma_f32_16x16x32_bf16 v[94:97], a[84:87], v[122:125], v[94:97]
v_mfma_f32_16x16x32_bf16 v[94:97], a[92:95], v[126:129], v[94:97]
ds_read_b128 v[146:149], v8 offset:34816
v_mfma_f32_16x16x32_bf16 v[102:105], a[196:199], v[106:109], v[102:105]
.Lgu_in15:
v_mfma_f32_16x16x32_bf16 v[102:105], a[204:207], v[110:113], v[102:105]
s_waitcnt vmcnt(1)
v_sub_u32 v255, v255, v5
v_cmp_lt_u32 vcc, 0x7f, v255
v_mfma_f32_16x16x32_bf16 v[102:105], a[212:215], v[114:117], v[102:105]
ds_read_b128 v[150:153], v8 offset:35840
v_mfma_f32_16x16x32_bf16 v[102:105], a[220:223], v[118:121], v[102:105]
v_mfma_f32_16x16x32_bf16 v[102:105], a[228:231], v[122:125], v[102:105]
s_cbranch_vccnz .Lgu_sp13
.Lgu_rt14:
s_add_u32 m0, s45, 18432
v_mfma_f32_16x16x32_bf16 v[102:105], a[236:239], v[126:129], v[102:105]
s_waitcnt lgkmcnt(5)
v_mfma_f32_16x16x32_bf16 v[90:93], a[96:99], v[130:133], v[90:93]
s_barrier
s_waitcnt lgkmcnt(4)
v_mfma_f32_16x16x32_bf16 v[90:93], a[104:107], v[134:137], v[90:93]
s_waitcnt lgkmcnt(3)
v_add_u32 v5, 1, v5
v_add_lshl_u32 v253, v5, 0, 7
v_mfma_f32_16x16x32_bf16 v[90:93], a[112:115], v[138:141], v[90:93]
s_waitcnt lgkmcnt(2)
v_bfi_b32 v253, s69, v253, v4
v_mfma_f32_16x16x32_bf16 v[90:93], a[120:123], v[142:145], v[90:93]
s_waitcnt lgkmcnt(1)
global_load_lds_dwordx4 v2, s[38:39] offset:-2048 sc1
v_mfma_f32_16x16x32_bf16 v[90:93], a[128:131], v[146:149], v[90:93]
s_waitcnt lgkmcnt(0)
v_mfma_f32_16x16x32_bf16 v[90:93], a[136:139], v[150:153], v[90:93]
v_mfma_f32_16x16x32_bf16 v[98:101], v[26:29], v[130:133], v[98:101]
global_load_lds_dwordx4 v2, s[38:39] offset:-1024 sc1
v_mfma_f32_16x16x32_bf16 v[98:101], v[34:37], v[134:137], v[98:101]
v_mfma_f32_16x16x32_bf16 v[98:101], v[42:45], v[138:141], v[98:101]
v_mfma_f32_16x16x32_bf16 v[98:101], v[50:53], v[142:145], v[98:101]
v_xor_b32 v8, 0x10000, v8
global_load_lds_dwordx4 v2, s[38:39] offset:0 sc1
v_mfma_f32_16x16x32_bf16 v[98:101], v[58:61], v[146:149], v[98:101]
v_mfma_f32_16x16x32_bf16 v[98:101], v[66:69], v[150:153], v[98:101]
v_mfma_f32_16x16x32_bf16 v[94:97], a[100:103], v[130:133], v[94:97]
s_barrier
v_mfma_f32_16x16x32_bf16 v[94:97], a[108:111], v[134:137], v[94:97]
global_load_lds_dwordx4 v2, s[38:39] offset:1024 sc1
v_mfma_f32_16x16x32_bf16 v[94:97], a[116:119], v[138:141], v[94:97]
v_mfma_f32_16x16x32_bf16 v[94:97], a[124:127], v[142:145], v[94:97]
ds_read_b128 v[106:109], v8 offset:0
v_mfma_f32_16x16x32_bf16 v[94:97], a[132:135], v[146:149], v[94:97]
ds_read_b128 v[110:113], v8 offset:1024
global_load_lds_dwordx4 v2, s[38:39] offset:2048 sc1
v_mfma_f32_16x16x32_bf16 v[94:97], a[140:143], v[150:153], v[94:97]
ds_read_b128 v[114:117], v8 offset:2048
v_mul_f32 v154, v90, v98
v_mfma_f32_16x16x32_bf16 v[102:105], v[30:33], v[130:133], v[102:105]
ds_read_b128 v[118:121], v8 offset:3072
v_mul_f32 v155, v91, v99
v_mfma_f32_16x16x32_bf16 v[102:105], v[38:41], v[134:137], v[102:105]
ds_read_b128 v[122:125], v8 offset:4096
global_load_lds_dwordx4 v2, s[38:39] offset:3072 sc1
v_mfma_f32_16x16x32_bf16 v[102:105], v[46:49], v[138:141], v[102:105]
ds_read_b128 v[126:129], v8 offset:5120
v_add_u32 v2, 18688, v2
v_mfma_f32_16x16x32_bf16 v[102:105], v[54:57], v[142:145], v[102:105]
v_min_u32 v2, v2, v9
v_mul_f32 v156, v92, v100
v_mfma_f32_16x16x32_bf16 v[102:105], v[62:65], v[146:149], v[102:105]
v_mfma_f32_16x16x32_bf16 v[102:105], v[70:73], v[150:153], v[102:105]
s_sub_u32 s70, s70, 1
s_cbranch_scc0 .Lgu_top18
s_waitcnt vmcnt(6) lgkmcnt(0)
s_barrier
v_mul_f32 v154, v90, v98
v_mul_f32 v155, v91, v99
v_mul_f32 v156, v92, v100
v_mul_f32 v157, v93, v101
v_cvt_pk_bf16_f32 v162, v154, v155
v_cvt_pk_bf16_f32 v163, v156, v157
v_mul_f32 v158, v94, v102
v_mul_f32 v159, v95, v103
v_mul_f32 v160, v96, v104
v_mul_f32 v161, v97, v105
v_cvt_pk_bf16_f32 v164, v158, v159
v_cvt_pk_bf16_f32 v165, v160, v161
global_store_dwordx4 v3, v[162:165], s[38:39] sc1
v_mov_b32 v231, 192
ds_read_b32 v231, v231 offset:36864
s_waitcnt vmcnt(0)
s_barrier
s_waitcnt lgkmcnt(0)
v_readfirstlane_b32 s62, v231
s_nop 3
s_max_i32 s62, s62, 2
s_min_i32 s62, s62, 66
s_add_u32 s62, s62, 0
s_branch .Lgu_fin
.Lgu_w4:
s_add_u32 s84, s54, 18688
s_addc_u32 s85, s55, 0
s_add_u32 s86, s52, 110592
s_addc_u32 s87, s53, 0
s_mov_b32 s80, 0
s_mov_b64 s[76:77], 0
s_mov_b32 s72, -14
v_add_u32 v230, -1, v17
v_add_u32 v226, -1, s37
v_add_u32 v227, -1, s37
v_add_u32 v228, -1, s37
v_add_u32 v229, -1, s37
v_add_lshl_u32 v252, v228, 0, 7
v_bfi_b32 v252, s69, v252, v18
v_add_lshl_u32 v253, v17, 0, 7
v_bfi_b32 v253, s69, v253, v16
v_add_u32 v254, -1, v5
v_add_u32 v255, -1, v5
s_add_u32 m0, s45, 18432
s_xor_b32 s45, s45, 0x10000
s_mov_b32 m0, s45
s_barrier
v_add_u32 v2, 18688, v2
s_mov_b32 s70, 32
s_waitcnt vmcnt(0) lgkmcnt(0)
s_nop 7
s_branch .Lgu_in24
.p2align 6
.Lgu_top25:
s_waitcnt lgkmcnt(5)
v_mfma_f32_16x16x32_bf16 v[74:77], a[0:3], v[106:109], 0
s_waitcnt lgkmcnt(4)
global_load_dword v230, v253, s[38:39] sc1
v_lshlrev_b32 v216, 16, v187
v_mfma_f32_16x16x32_bf16 v[74:77], a[8:11], v[110:113], v[74:77]
s_waitcnt lgkmcnt(3)
v_and_b32 v217, 0xffff0000, v187
v_add_f32_dpp v206, v210, v206 row_ror:8 row_mask:0xf bank_mask:0xf
v_mfma_f32_16x16x32_bf16 v[74:77], a[16:19], v[114:117], v[74:77]
s_waitcnt lgkmcnt(2)
v_add_f32_dpp v207, v211, v207 row_ror:8 row_mask:0xf bank_mask:0xf
v_add_f32_dpp v208, v212, v208 row_ror:8 row_mask:0xf bank_mask:0xf
v_mfma_f32_16x16x32_bf16 v[74:77], a[24:27], v[118:121], v[74:77]
s_waitcnt lgkmcnt(1)
v_mul_f32 v158, v94, v102
v_mul_f32 v159, v95, v103
v_mfma_f32_16x16x32_bf16 v[74:77], a[32:35], v[122:125], v[74:77]
s_waitcnt lgkmcnt(0)
v_mul_f32 v160, v96, v104
v_mul_f32 v161, v97, v105
v_mfma_f32_16x16x32_bf16 v[74:77], a[40:43], v[126:129], v[74:77]
ds_read_b128 v[130:133], v8 offset:6144
v_cvt_pk_bf16_f32 v164, v158, v159
v_mfma_f32_16x16x32_bf16 v[82:85], a[144:147], v[106:109], 0
ds_read_b128 v[134:137], v8 offset:7168
v_cvt_pk_bf16_f32 v165, v160, v161
v_mfma_f32_16x16x32_bf16 v[82:85], a[152:155], v[110:113], v[82:85]
v_mfma_f32_16x16x32_bf16 v[82:85], a[160:163], v[114:117], v[82:85]
s_waitcnt vmcnt(8)
s_barrier
v_mfma_f32_16x16x32_bf16 v[82:85], a[168:171], v[118:121], v[82:85]
v_add_f32_dpp v209, v213, v209 row_ror:8 row_mask:0xf bank_mask:0xf
v_fma_f32 v206, v206, v231, v214
v_mfma_f32_16x16x32_bf16 v[82:85], a[176:179], v[122:125], v[82:85]
v_fma_f32 v207, v207, v231, v215
v_fma_f32 v208, v208, v231, v216
v_mfma_f32_16x16x32_bf16 v[82:85], a[184:187], v[126:129], v[82:85]
ds_read_b128 v[138:141], v8 offset:8192
v_fma_f32 v209, v209, v231, v217
v_mfma_f32_16x16x32_bf16 v[78:81], a[4:7], v[106:109], 0
v_cvt_pk_bf16_f32 v222, v206, v207
v_cvt_pk_bf16_f32 v223, v208, v209
v_mfma_f32_16x16x32_bf16 v[78:81], a[12:15], v[110:113], v[78:81]
global_store_dwordx4 v3, v[162:165], s[38:39] sc1
v_mfma_f32_16x16x32_bf16 v[78:81], a[20:23], v[114:117], v[78:81]
ds_read_b128 v[142:145], v8 offset:9216
v_add_u32 v3, 147456, v3
v_mfma_f32_16x16x32_bf16 v[78:81], a[28:31], v[118:121], v[78:81]
ds_write_b64 v14, v[222:223]
v_mfma_f32_16x16x32_bf16 v[78:81], a[36:39], v[122:125], v[78:81]
s_waitcnt vmcnt(8)
ds_read_b64 v[224:225], v15
s_mov_b64 exec, 1
global_store_dword v252, v229, s[38:39] sc1
s_mov_b64 exec, -1
v_mfma_f32_16x16x32_bf16 v[78:81], a[44:47], v[126:129], v[78:81]
ds_read_b128 v[146:149], v8 offset:10240
v_mfma_f32_16x16x32_bf16 v[86:89], a[148:151], v[106:109], 0
v_mfma_f32_16x16x32_bf16 v[86:89], a[156:159], v[110:113], v[86:89]
v_mfma_f32_16x16x32_bf16 v[86:89], a[164:167], v[114:117], v[86:89]
ds_read_b128 v[150:153], v8 offset:11264
v_mfma_f32_16x16x32_bf16 v[86:89], a[172:175], v[118:121], v[86:89]
v_mfma_f32_16x16x32_bf16 v[86:89], a[180:183], v[122:125], v[86:89]
v_mfma_f32_16x16x32_bf16 v[86:89], a[188:191], v[126:129], v[86:89]
s_waitcnt lgkmcnt(2)
global_store_dwordx2 v13, v[224:225], s[58:59] sc1
v_mov_b32 v228, v226
v_mfma_f32_16x16x32_bf16 v[74:77], a[48:51], v[130:133], v[74:77]
v_mfma_f32_16x16x32_bf16 v[74:77], a[56:59], v[134:137], v[74:77]
v_mfma_f32_16x16x32_bf16 v[74:77], a[64:67], v[138:141], v[74:77]
v_mfma_f32_16x16x32_bf16 v[74:77], a[72:75], v[142:145], v[74:77]
s_waitcnt vmcnt(8)
v_add_f32 v200, v200, v202
s_waitcnt vmcnt(7)
v_lshlrev_b32 v206, 16, v188
v_add_f32 v201, v201, v203
s_waitcnt lgkmcnt(1)
v_mfma_f32_16x16x32_bf16 v[74:77], a[80:83], v[146:149], v[74:77]
s_waitcnt lgkmcnt(0)
v_and_b32 v207, 0xffff0000, v188
v_mul_f32 v200, 0x3ae38e39, v200
v_lshlrev_b32 v208, 16, v189
v_mfma_f32_16x16x32_bf16 v[74:77], a[88:91], v[150:153], v[74:77]
ds_read_b128 v[106:109], v8 offset:12288
v_mul_f32 v201, 0x3ae38e39, v201
v_and_b32 v209, 0xffff0000, v189
v_mfma_f32_16x16x32_bf16 v[82:85], a[192:195], v[130:133], v[82:85]
v_fma_f32 v232, -v200, v200, v201
v_lshlrev_b32 v210, 16, v190
v_max_f32 v232, 0, v232
v_mfma_f32_16x16x32_bf16 v[82:85], a[200:203], v[134:137], v[82:85]
v_and_b32 v211, 0xffff0000, v190
v_add_f32 v232, 0x3727c5ac, v232
v_mfma_f32_16x16x32_bf16 v[82:85], a[208:211], v[138:141], v[82:85]
ds_read_b128 v[110:113], v8 offset:13312
v_lshlrev_b32 v212, 16, v191
v_mfma_f32_16x16x32_bf16 v[82:85], a[216:219], v[142:145], v[82:85]
s_waitcnt vmcnt(3)
v_sub_u32 v230, v230, v17
v_cmp_lt_u32 vcc, 0x7f, v230
v_mfma_f32_16x16x32_bf16 v[82:85], a[224:227], v[146:149], v[82:85]
v_rcp_f32 v231, v232
v_mfma_f32_16x16x32_bf16 v[82:85], a[232:235], v[150:153], v[82:85]
ds_read_b128 v[114:117], v8 offset:14336
v_and_b32 v213, 0xffff0000, v191
v_mfma_f32_16x16x32_bf16 v[78:81], a[52:55], v[130:133], v[78:81]
s_and_b64 s[88:89], vcc, s[76:77]
s_cbranch_scc1 .Lgu_sp20
.Lgu_rt21:
v_mfma_f32_16x16x32_bf16 v[78:81], a[60:63], v[134:137], v[78:81]
s_cmp_eq_u64 vcc, 0
v_lshlrev_b32 v214, 16, v192
v_and_b32 v215, 0xffff0000, v192
v_mfma_f32_16x16x32_bf16 v[78:81], a[68:71], v[138:141], v[78:81]
s_cselect_b64 s[54:55], s[84:85], s[54:55]
ds_read_b128 v[118:121], v8 offset:15360
v_lshlrev_b32 v216, 16, v193
v_mfma_f32_16x16x32_bf16 v[78:81], a[76:79], v[142:145], v[78:81]
s_cselect_b64 s[52:53], s[86:87], s[52:53]
global_load_dwordx2 v[182:183], v12, s[54:55] offset:0 sc1
v_and_b32 v217, 0xffff0000, v193
v_mfma_f32_16x16x32_bf16 v[78:81], a[84:87], v[146:149], v[78:81]
s_cselect_b64 s[58:59], s[56:57], s[50:51]
global_load_dwordx2 v[184:185], v12, s[54:55] offset:128 sc1
v_lshlrev_b32 v218, 16, v194
v_mfma_f32_16x16x32_bf16 v[78:81], a[92:95], v[150:153], v[78:81]
s_cselect_b32 s82, 1, 0
ds_read_b128 v[122:125], v8 offset:16384
global_load_dwordx4 v[170:173], v10, s[52:53] offset:0 sc1
v_mfma_f32_16x16x32_bf16 v[86:89], a[196:199], v[130:133], v[86:89]
s_add_u32 s62, s62, s82
global_load_dwordx4 v[174:177], v10, s[52:53] offset:1024 sc1
v_and_b32 v219, 0xffff0000, v194
v_mfma_f32_16x16x32_bf16 v[86:89], a[204:207], v[134:137], v[86:89]
s_add_u32 s83, s62, s37
global_load_dwordx4 v[178:181], v10, s[52:53] offset:2048 sc1
v_add_u32 v17, s82, v17
v_add_u32 v226, -1, s83
v_mfma_f32_16x16x32_bf16 v[86:89], a[212:215], v[138:141], v[86:89]
s_cmp_lt_i32 s62, 32
ds_read_b128 v[126:129], v8 offset:17408
global_load_dwordx2 v[186:187], v11, s[54:55] sc1
v_add_lshl_u32 v253, v17, 0, 7
v_mfma_f32_16x16x32_bf16 v[86:89], a[220:223], v[142:145], v[86:89]
s_cselect_b64 s[56:57], s[46:47], s[48:49]
v_bfi_b32 v253, s69, v253, v16
v_lshlrev_b32 v220, 16, v195
v_mfma_f32_16x16x32_bf16 v[86:89], a[228:231], v[146:149], v[86:89]
s_mul_i32 s80, s62, 18432
v_and_b32 v221, 0xffff0000, v195
v_add_f32 v206, v206, v214
v_mfma_f32_16x16x32_bf16 v[86:89], a[236:239], v[150:153], v[86:89]
s_waitcnt lgkmcnt(5)
v_mfma_f32_16x16x32_bf16 v[74:77], a[96:99], v[106:109], v[74:77]
s_barrier
s_waitcnt lgkmcnt(4)
v_mfma_f32_16x16x32_bf16 v[74:77], a[104:107], v[110:113], v[74:77]
s_waitcnt lgkmcnt(3)
v_add_f32 v207, v207, v215
v_add_f32 v208, v208, v216
v_mfma_f32_16x16x32_bf16 v[74:77], a[112:115], v[114:117], v[74:77]
s_waitcnt lgkmcnt(2)
v_add_f32 v209, v209, v217
v_add_f32 v210, v210, v218
v_mfma_f32_16x16x32_bf16 v[74:77], a[120:123], v[118:121], v[74:77]
s_waitcnt lgkmcnt(1)
v_add_f32 v211, v211, v219
v_add_f32 v212, v212, v220
v_mfma_f32_16x16x32_bf16 v[74:77], a[128:131], v[122:125], v[74:77]
s_waitcnt lgkmcnt(0)
v_add_f32 v213, v213, v221
v_lshlrev_b32 v214, 16, v196
v_mfma_f32_16x16x32_bf16 v[74:77], a[136:139], v[126:129], v[74:77]
s_sub_u32 s56, s56, s80
v_and_b32 v215, 0xffff0000, v196
v_lshlrev_b32 v216, 16, v197
v_mfma_f32_16x16x32_bf16 v[82:85], v[26:29], v[106:109], v[82:85]
s_subb_u32 s57, s57, 0
v_and_b32 v217, 0xffff0000, v197
v_lshlrev_b32 v218, 16, v198
v_mfma_f32_16x16x32_bf16 v[82:85], v[34:37], v[110:113], v[82:85]
s_add_u32 s84, s54, 18688
v_and_b32 v219, 0xffff0000, v198
v_lshlrev_b32 v220, 16, v199
v_mfma_f32_16x16x32_bf16 v[82:85], v[42:45], v[114:117], v[82:85]
s_addc_u32 s85, s55, 0
v_and_b32 v221, 0xffff0000, v199
v_add_f32 v206, v206, v214
v_mfma_f32_16x16x32_bf16 v[82:85], v[50:53], v[118:121], v[82:85]
s_add_u32 s86, s52, 110592
v_add_f32 v207, v207, v215
v_add_f32 v208, v208, v216
v_mfma_f32_16x16x32_bf16 v[82:85], v[58:61], v[122:125], v[82:85]
s_addc_u32 s87, s53, 0
v_add_f32 v209, v209, v217
v_add_f32 v210, v210, v218
v_mfma_f32_16x16x32_bf16 v[82:85], v[66:69], v[126:129], v[82:85]
v_mfma_f32_16x16x32_bf16 v[78:81], a[100:103], v[106:109], v[78:81]
s_barrier
v_add_lshl_u32 v252, v228, 0, 7
v_mfma_f32_16x16x32_bf16 v[78:81], a[108:111], v[110:113], v[78:81]
s_add_u32 s72, s72, 1
v_bfi_b32 v252, s69, v252, v18
v_add_f32 v211, v211, v219
v_mfma_f32_16x16x32_bf16 v[78:81], a[116:119], v[114:117], v[78:81]
s_cmp_le_i32 s62, s72
v_add_f32 v212, v212, v220
v_add_f32 v213, v213, v221
v_mfma_f32_16x16x32_bf16 v[78:81], a[124:127], v[118:121], v[78:81]
s_cselect_b64 s[76:77], -1, 0
ds_read_b128 v[130:133], v8 offset:18432
v_lshlrev_b32 v214, 16, v204
v_mfma_f32_16x16x32_bf16 v[78:81], a[132:135], v[122:125], v[78:81]
ds_read_b128 v[134:137], v8 offset:19456
v_mul_f32 v154, v74, v82
v_mfma_f32_16x16x32_bf16 v[78:81], a[140:143], v[126:129], v[78:81]
ds_read_b128 v[138:141], v8 offset:20480
v_mul_f32 v155, v75, v83
v_mfma_f32_16x16x32_bf16 v[86:89], v[30:33], v[106:109], v[86:89]
ds_read_b128 v[142:145], v8 offset:21504
v_mul_f32 v156, v76, v84
v_mfma_f32_16x16x32_bf16 v[86:89], v[38:41], v[110:113], v[86:89]
ds_read_b128 v[146:149], v8 offset:22528
v_mul_f32 v157, v77, v85
v_mfma_f32_16x16x32_bf16 v[86:89], v[46:49], v[114:117], v[86:89]
ds_read_b128 v[150:153], v8 offset:23552
v_cvt_pk_bf16_f32 v162, v154, v155
v_mfma_f32_16x16x32_bf16 v[86:89], v[54:57], v[118:121], v[86:89]
v_cvt_pk_bf16_f32 v163, v156, v157
v_and_b32 v215, 0xffff0000, v204
v_mfma_f32_16x16x32_bf16 v[86:89], v[62:65], v[122:125], v[86:89]
v_lshlrev_b32 v216, 16, v205
v_and_b32 v217, 0xffff0000, v205
v_mfma_f32_16x16x32_bf16 v[86:89], v[70:73], v[126:129], v[86:89]
s_waitcnt lgkmcnt(5)
)"
R"(global_load_dword v230, v253, s[38:39] sc1
v_add_f32_dpp v206, v210, v206 row_ror:8 row_mask:0xf bank_mask:0xf
v_mfma_f32_16x16x32_bf16 v[90:93], a[0:3], v[130:133], 0
s_waitcnt lgkmcnt(4)
v_add_f32_dpp v207, v211, v207 row_ror:8 row_mask:0xf bank_mask:0xf
v_add_f32_dpp v208, v212, v208 row_ror:8 row_mask:0xf bank_mask:0xf
v_mfma_f32_16x16x32_bf16 v[90:93], a[8:11], v[134:137], v[90:93]
s_waitcnt lgkmcnt(3)
v_add_f32_dpp v209, v213, v209 row_ror:8 row_mask:0xf bank_mask:0xf
v_fma_f32 v206, v206, v231, v214
v_mfma_f32_16x16x32_bf16 v[90:93], a[16:19], v[138:141], v[90:93]
s_waitcnt lgkmcnt(2)
v_fma_f32 v207, v207, v231, v215
v_fma_f32 v208, v208, v231, v216
v_mfma_f32_16x16x32_bf16 v[90:93], a[24:27], v[142:145], v[90:93]
s_waitcnt lgkmcnt(1)
v_mul_f32 v158, v78, v86
v_mul_f32 v159, v79, v87
v_mfma_f32_16x16x32_bf16 v[90:93], a[32:35], v[146:149], v[90:93]
s_waitcnt lgkmcnt(0)
v_mul_f32 v160, v80, v88
v_mul_f32 v161, v81, v89
v_mfma_f32_16x16x32_bf16 v[90:93], a[40:43], v[150:153], v[90:93]
ds_read_b128 v[106:109], v8 offset:24576
v_cvt_pk_bf16_f32 v164, v158, v159
v_mfma_f32_16x16x32_bf16 v[98:101], a[144:147], v[130:133], 0
ds_read_b128 v[110:113], v8 offset:25600
v_cvt_pk_bf16_f32 v165, v160, v161
v_mfma_f32_16x16x32_bf16 v[98:101], a[152:155], v[134:137], v[98:101]
v_mfma_f32_16x16x32_bf16 v[98:101], a[160:163], v[138:141], v[98:101]
s_waitcnt vmcnt(9)
s_barrier
v_mfma_f32_16x16x32_bf16 v[98:101], a[168:171], v[142:145], v[98:101]
s_waitcnt vmcnt(7)
s_mov_b64 exec, 1
global_store_dword v252, v228, s[38:39] sc1
s_mov_b64 exec, -1
v_mfma_f32_16x16x32_bf16 v[98:101], a[176:179], v[146:149], v[98:101]
v_fma_f32 v209, v209, v231, v217
v_cvt_pk_bf16_f32 v222, v206, v207
v_mfma_f32_16x16x32_bf16 v[98:101], a[184:187], v[150:153], v[98:101]
ds_read_b128 v[114:117], v8 offset:26624
v_cvt_pk_bf16_f32 v223, v208, v209
v_mfma_f32_16x16x32_bf16 v[94:97], a[4:7], v[130:133], 0
ds_write_b64 v14, v[222:223]
v_mfma_f32_16x16x32_bf16 v[94:97], a[12:15], v[134:137], v[94:97]
global_store_dwordx4 v3, v[162:165], s[38:39] sc1
v_mfma_f32_16x16x32_bf16 v[94:97], a[20:23], v[138:141], v[94:97]
ds_read_b128 v[118:121], v8 offset:27648
v_add_u32 v3, 147456, v3
v_mfma_f32_16x16x32_bf16 v[94:97], a[28:31], v[142:145], v[94:97]
ds_read_b64 v[224:225], v15
v_mfma_f32_16x16x32_bf16 v[94:97], a[36:39], v[146:149], v[94:97]
v_mfma_f32_16x16x32_bf16 v[94:97], a[44:47], v[150:153], v[94:97]
ds_read_b128 v[122:125], v8 offset:28672
v_mfma_f32_16x16x32_bf16 v[102:105], a[148:151], v[130:133], 0
v_mfma_f32_16x16x32_bf16 v[102:105], a[156:159], v[134:137], v[102:105]
v_mfma_f32_16x16x32_bf16 v[102:105], a[164:167], v[138:141], v[102:105]
ds_read_b128 v[126:129], v8 offset:29696
v_mfma_f32_16x16x32_bf16 v[102:105], a[172:175], v[142:145], v[102:105]
v_mfma_f32_16x16x32_bf16 v[102:105], a[180:183], v[146:149], v[102:105]
s_waitcnt lgkmcnt(2)
global_store_dwordx2 v13, v[224:225], s[60:61] sc1
v_mov_b32 v229, v227
v_mfma_f32_16x16x32_bf16 v[102:105], a[188:191], v[150:153], v[102:105]
v_mfma_f32_16x16x32_bf16 v[90:93], a[48:51], v[106:109], v[90:93]
v_mfma_f32_16x16x32_bf16 v[90:93], a[56:59], v[110:113], v[90:93]
v_mfma_f32_16x16x32_bf16 v[90:93], a[64:67], v[114:117], v[90:93]
v_mfma_f32_16x16x32_bf16 v[90:93], a[72:75], v[118:121], v[90:93]
s_waitcnt vmcnt(8)
v_add_f32 v182, v182, v184
s_waitcnt vmcnt(7)
v_lshlrev_b32 v206, 16, v170
v_add_f32 v183, v183, v185
s_waitcnt lgkmcnt(1)
v_mfma_f32_16x16x32_bf16 v[90:93], a[80:83], v[122:125], v[90:93]
s_waitcnt lgkmcnt(0)
v_and_b32 v207, 0xffff0000, v170
v_mul_f32 v182, 0x3ae38e39, v182
v_lshlrev_b32 v208, 16, v171
v_mfma_f32_16x16x32_bf16 v[90:93], a[88:91], v[126:129], v[90:93]
ds_read_b128 v[130:133], v8 offset:30720
v_mul_f32 v183, 0x3ae38e39, v183
v_and_b32 v209, 0xffff0000, v171
v_mfma_f32_16x16x32_bf16 v[98:101], a[192:195], v[106:109], v[98:101]
v_fma_f32 v232, -v182, v182, v183
v_lshlrev_b32 v210, 16, v172
v_max_f32 v232, 0, v232
v_mfma_f32_16x16x32_bf16 v[98:101], a[200:203], v[110:113], v[98:101]
v_and_b32 v211, 0xffff0000, v172
v_add_f32 v232, 0x3727c5ac, v232
v_mfma_f32_16x16x32_bf16 v[98:101], a[208:211], v[114:117], v[98:101]
ds_read_b128 v[134:137], v8 offset:31744
v_lshlrev_b32 v212, 16, v173
.Lgu_in24:
v_mfma_f32_16x16x32_bf16 v[98:101], a[216:219], v[118:121], v[98:101]
s_waitcnt vmcnt(3)
v_sub_u32 v230, v230, v17
v_cmp_lt_u32 vcc, 0x7f, v230
v_mfma_f32_16x16x32_bf16 v[98:101], a[224:227], v[122:125], v[98:101]
v_rcp_f32 v231, v232
v_mfma_f32_16x16x32_bf16 v[98:101], a[232:235], v[126:129], v[98:101]
ds_read_b128 v[138:141], v8 offset:32768
v_and_b32 v213, 0xffff0000, v173
v_mfma_f32_16x16x32_bf16 v[94:97], a[52:55], v[106:109], v[94:97]
s_and_b64 s[88:89], vcc, s[76:77]
s_cbranch_scc1 .Lgu_sp22
.Lgu_rt23:
v_mfma_f32_16x16x32_bf16 v[94:97], a[60:63], v[110:113], v[94:97]
s_cmp_eq_u64 vcc, 0
v_lshlrev_b32 v214, 16, v174
v_and_b32 v215, 0xffff0000, v174
v_mfma_f32_16x16x32_bf16 v[94:97], a[68:71], v[114:117], v[94:97]
s_cselect_b64 s[54:55], s[84:85], s[54:55]
ds_read_b128 v[142:145], v8 offset:33792
v_lshlrev_b32 v216, 16, v175
v_mfma_f32_16x16x32_bf16 v[94:97], a[76:79], v[118:121], v[94:97]
s_cselect_b64 s[52:53], s[86:87], s[52:53]
global_load_dwordx2 v[200:201], v12, s[54:55] offset:0 sc1
v_and_b32 v217, 0xffff0000, v175
v_mfma_f32_16x16x32_bf16 v[94:97], a[84:87], v[122:125], v[94:97]
s_cselect_b64 s[60:61], s[56:57], s[50:51]
global_load_dwordx2 v[202:203], v12, s[54:55] offset:128 sc1
v_lshlrev_b32 v218, 16, v176
v_mfma_f32_16x16x32_bf16 v[94:97], a[92:95], v[126:129], v[94:97]
s_cselect_b32 s82, 1, 0
ds_read_b128 v[146:149], v8 offset:34816
global_load_dwordx4 v[188:191], v10, s[52:53] offset:0 sc1
v_mfma_f32_16x16x32_bf16 v[102:105], a[196:199], v[106:109], v[102:105]
s_add_u32 s62, s62, s82
global_load_dwordx4 v[192:195], v10, s[52:53] offset:1024 sc1
v_and_b32 v219, 0xffff0000, v176
v_mfma_f32_16x16x32_bf16 v[102:105], a[204:207], v[110:113], v[102:105]
s_add_u32 s83, s62, s37
global_load_dwordx4 v[196:199], v10, s[52:53] offset:2048 sc1
v_add_u32 v17, s82, v17
v_add_u32 v227, -1, s83
v_mfma_f32_16x16x32_bf16 v[102:105], a[212:215], v[114:117], v[102:105]
s_cmp_lt_i32 s62, 32
ds_read_b128 v[150:153], v8 offset:35840
global_load_dwordx2 v[204:205], v11, s[54:55] sc1
v_add_lshl_u32 v253, v17, 0, 7
v_mfma_f32_16x16x32_bf16 v[102:105], a[220:223], v[118:121], v[102:105]
s_cselect_b64 s[56:57], s[46:47], s[48:49]
v_bfi_b32 v253, s69, v253, v16
v_lshlrev_b32 v220, 16, v177
v_mfma_f32_16x16x32_bf16 v[102:105], a[228:231], v[122:125], v[102:105]
s_mul_i32 s80, s62, 18432
v_and_b32 v221, 0xffff0000, v177
v_add_f32 v206, v206, v214
v_mfma_f32_16x16x32_bf16 v[102:105], a[236:239], v[126:129], v[102:105]
s_waitcnt lgkmcnt(5)
v_mfma_f32_16x16x32_bf16 v[90:93], a[96:99], v[130:133], v[90:93]
s_barrier
s_waitcnt lgkmcnt(4)
v_mfma_f32_16x16x32_bf16 v[90:93], a[104:107], v[134:137], v[90:93]
s_waitcnt lgkmcnt(3)
v_add_f32 v207, v207, v215
v_add_f32 v208, v208, v216
v_mfma_f32_16x16x32_bf16 v[90:93], a[112:115], v[138:141], v[90:93]
s_waitcnt lgkmcnt(2)
v_add_f32 v209, v209, v217
v_add_f32 v210, v210, v218
v_mfma_f32_16x16x32_bf16 v[90:93], a[120:123], v[142:145], v[90:93]
s_waitcnt lgkmcnt(1)
v_add_f32 v211, v211, v219
v_add_f32 v212, v212, v220
v_mfma_f32_16x16x32_bf16 v[90:93], a[128:131], v[146:149], v[90:93]
s_waitcnt lgkmcnt(0)
v_add_f32 v213, v213, v221
v_lshlrev_b32 v214, 16, v178
v_mfma_f32_16x16x32_bf16 v[90:93], a[136:139], v[150:153], v[90:93]
s_sub_u32 s56, s56, s80
v_and_b32 v215, 0xffff0000, v178
v_lshlrev_b32 v216, 16, v179
v_mfma_f32_16x16x32_bf16 v[98:101], v[26:29], v[130:133], v[98:101]
s_subb_u32 s57, s57, 0
v_and_b32 v217, 0xffff0000, v179
v_lshlrev_b32 v218, 16, v180
v_mfma_f32_16x16x32_bf16 v[98:101], v[34:37], v[134:137], v[98:101]
s_add_u32 s84, s54, 18688
v_and_b32 v219, 0xffff0000, v180
v_lshlrev_b32 v220, 16, v181
v_mfma_f32_16x16x32_bf16 v[98:101], v[42:45], v[138:141], v[98:101]
s_addc_u32 s85, s55, 0
v_and_b32 v221, 0xffff0000, v181
v_add_f32 v206, v206, v214
v_mfma_f32_16x16x32_bf16 v[98:101], v[50:53], v[142:145], v[98:101]
s_add_u32 s86, s52, 110592
v_xor_b32 v8, 0x10000, v8
v_add_f32 v207, v207, v215
v_mfma_f32_16x16x32_bf16 v[98:101], v[58:61], v[146:149], v[98:101]
s_addc_u32 s87, s53, 0
v_add_f32 v208, v208, v216
v_add_f32 v209, v209, v217
v_mfma_f32_16x16x32_bf16 v[98:101], v[66:69], v[150:153], v[98:101]
v_mfma_f32_16x16x32_bf16 v[94:97], a[100:103], v[130:133], v[94:97]
s_barrier
v_add_lshl_u32 v252, v229, 0, 7
v_mfma_f32_16x16x32_bf16 v[94:97], a[108:111], v[134:137], v[94:97]
s_add_u32 s72, s72, 1
v_add_f32 v210, v210, v218
v_add_f32 v211, v211, v219
v_bfi_b32 v252, s69, v252, v18
v_mfma_f32_16x16x32_bf16 v[94:97], a[116:119], v[138:141], v[94:97]
s_cmp_le_i32 s62, s72
v_add_f32 v212, v212, v220
v_add_f32 v213, v213, v221
v_mfma_f32_16x16x32_bf16 v[94:97], a[124:127], v[142:145], v[94:97]
s_cselect_b64 s[76:77], -1, 0
ds_read_b128 v[106:109], v8 offset:0
v_lshlrev_b32 v214, 16, v186
v_mfma_f32_16x16x32_bf16 v[94:97], a[132:135], v[146:149], v[94:97]
ds_read_b128 v[110:113], v8 offset:1024
v_mul_f32 v154, v90, v98
v_mfma_f32_16x16x32_bf16 v[94:97], a[140:143], v[150:153], v[94:97]
ds_read_b128 v[114:117], v8 offset:2048
v_mul_f32 v155, v91, v99
v_mfma_f32_16x16x32_bf16 v[102:105], v[30:33], v[130:133], v[102:105]
ds_read_b128 v[118:121], v8 offset:3072
v_mul_f32 v156, v92, v100
v_mfma_f32_16x16x32_bf16 v[102:105], v[38:41], v[134:137], v[102:105]
ds_read_b128 v[122:125], v8 offset:4096
v_mul_f32 v157, v93, v101
v_mfma_f32_16x16x32_bf16 v[102:105], v[46:49], v[138:141], v[102:105]
ds_read_b128 v[126:129], v8 offset:5120
v_cvt_pk_bf16_f32 v162, v154, v155
v_mfma_f32_16x16x32_bf16 v[102:105], v[54:57], v[142:145], v[102:105]
v_cvt_pk_bf16_f32 v163, v156, v157
v_and_b32 v215, 0xffff0000, v186
v_mfma_f32_16x16x32_bf16 v[102:105], v[62:65], v[146:149], v[102:105]
v_mfma_f32_16x16x32_bf16 v[102:105], v[70:73], v[150:153], v[102:105]
s_sub_u32 s70, s70, 1
s_cbranch_scc0 .Lgu_top25
v_mov_b32 v232, s62
ds_write_b32 v1, v232 offset:36864
v_subrev_u32 v17, s62, v17
s_waitcnt vmcnt(7) lgkmcnt(0)
s_barrier
v_mul_f32 v154, v90, v98
v_mul_f32 v155, v91, v99
v_mul_f32 v156, v92, v100
v_mul_f32 v157, v93, v101
v_cvt_pk_bf16_f32 v162, v154, v155
v_cvt_pk_bf16_f32 v163, v156, v157
v_mul_f32 v158, v94, v102
v_mul_f32 v159, v95, v103
v_mul_f32 v160, v96, v104
v_mul_f32 v161, v97, v105
v_cvt_pk_bf16_f32 v164, v158, v159
v_cvt_pk_bf16_f32 v165, v160, v161
global_store_dwordx4 v3, v[162:165], s[38:39] sc1
v_mov_b32 v231, 192
ds_read_b32 v231, v231 offset:36864
s_waitcnt vmcnt(0)
s_barrier
s_waitcnt lgkmcnt(0)
v_readfirstlane_b32 s62, v231
s_nop 3
s_max_i32 s62, s62, 2
s_min_i32 s62, s62, 66
s_add_u32 s62, s62, 1
s_branch .Lgu_fin
.Lgu_fin:
s_cmp_gt_i32 s62, 63
s_cbranch_scc1 .Lgu_end
v_add_u32 v233, s62, v17
v_add_lshl_u32 v253, v233, 0, 7
v_bfi_b32 v253, s69, v253, v16
global_load_dword v230, v253, s[38:39] sc1
s_cmp_lt_i32 s62, 32
s_cselect_b64 s[56:57], s[46:47], s[48:49]
s_mul_i32 s80, s62, 18432
s_sub_u32 s56, s56, s80
s_subb_u32 s57, s57, 0
s_mov_b64 s[58:59], s[56:57]
s_mul_i32 s80, s62, 110592
s_mul_i32 s81, s62, 18688
s_mov_b64 s[52:53], s[38:39]
s_mov_b64 s[54:55], s[38:39]
s_add_u32 s52, s52, s80
s_addc_u32 s53, s53, 0
s_add_u32 s54, s54, s81
s_addc_u32 s55, s55, 0
s_waitcnt vmcnt(0)
v_sub_u32 v230, v230, v233
v_cmp_lt_u32 vcc, 0x7f, v230
s_cbranch_vccnz .Lgu_sp27
.Lgu_rt28:
global_load_dwordx2 v[182:183], v12, s[54:55] offset:0 sc1
global_load_dwordx2 v[184:185], v12, s[54:55] offset:128 sc1
global_load_dwordx4 v[170:173], v10, s[52:53] offset:0 sc1
global_load_dwordx4 v[174:177], v10, s[52:53] offset:1024 sc1
global_load_dwordx4 v[178:181], v10, s[52:53] offset:2048 sc1
global_load_dwordx2 v[186:187], v11, s[54:55] sc1
s_waitcnt vmcnt(4)
v_add_f32 v182, v182, v184
s_waitcnt vmcnt(3)
v_lshlrev_b32 v206, 16, v170
v_add_f32 v183, v183, v185
v_and_b32 v207, 0xffff0000, v170
v_mul_f32 v182, 0x3ae38e39, v182
v_lshlrev_b32 v208, 16, v171
v_mul_f32 v183, 0x3ae38e39, v183
v_and_b32 v209, 0xffff0000, v171
v_fma_f32 v232, -v182, v182, v183
v_lshlrev_b32 v210, 16, v172
v_max_f32 v232, 0, v232
v_and_b32 v211, 0xffff0000, v172
v_add_f32 v232, 0x3727c5ac, v232
v_lshlrev_b32 v212, 16, v173
v_rcp_f32 v231, v232
v_and_b32 v213, 0xffff0000, v173
s_waitcnt vmcnt(2)
v_lshlrev_b32 v214, 16, v174
v_and_b32 v215, 0xffff0000, v174
v_lshlrev_b32 v216, 16, v175
v_and_b32 v217, 0xffff0000, v175
v_lshlrev_b32 v218, 16, v176
v_and_b32 v219, 0xffff0000, v176
v_lshlrev_b32 v220, 16, v177
v_and_b32 v221, 0xffff0000, v177
v_add_f32 v206, v206, v214
v_add_f32 v207, v207, v215
v_add_f32 v208, v208, v216
v_add_f32 v209, v209, v217
v_add_f32 v210, v210, v218
v_add_f32 v211, v211, v219
v_add_f32 v212, v212, v220
v_add_f32 v213, v213, v221
s_waitcnt vmcnt(1)
v_lshlrev_b32 v214, 16, v178
v_and_b32 v215, 0xffff0000, v178
v_lshlrev_b32 v216, 16, v179
v_and_b32 v217, 0xffff0000, v179
v_lshlrev_b32 v218, 16, v180
v_and_b32 v219, 0xffff0000, v180
v_lshlrev_b32 v220, 16, v181
v_and_b32 v221, 0xffff0000, v181
v_add_f32 v206, v206, v214
v_add_f32 v207, v207, v215
v_add_f32 v208, v208, v216
v_add_f32 v209, v209, v217
v_add_f32 v210, v210, v218
v_add_f32 v211, v211, v219
v_add_f32 v212, v212, v220
v_add_f32 v213, v213, v221
s_waitcnt vmcnt(0)
v_lshlrev_b32 v214, 16, v186
v_and_b32 v215, 0xffff0000, v186
v_lshlrev_b32 v216, 16, v187
v_and_b32 v217, 0xffff0000, v187
v_add_f32_dpp v206, v210, v206 row_ror:8 row_mask:0xf bank_mask:0xf
v_add_f32_dpp v207, v211, v207 row_ror:8 row_mask:0xf bank_mask:0xf
v_add_f32_dpp v208, v212, v208 row_ror:8 row_mask:0xf bank_mask:0xf
v_add_f32_dpp v209, v213, v209 row_ror:8 row_mask:0xf bank_mask:0xf
v_fma_f32 v206, v206, v231, v214
v_fma_f32 v207, v207, v231, v215
v_fma_f32 v208, v208, v231, v216
v_fma_f32 v209, v209, v231, v217
v_cvt_pk_bf16_f32 v222, v206, v207
v_cvt_pk_bf16_f32 v223, v208, v209
ds_write_b64 v14, v[222:223]
ds_read_b64 v[224:225], v15
s_waitcnt lgkmcnt(0)
global_store_dwordx2 v13, v[224:225], s[58:59] sc1
s_add_u32 s62, s62, 4
s_branch .Lgu_fin
.Lgu_end:
s_endpgm
.Lgu_sp11:
s_sleep 1
v_add_lshl_u32 v253, v5, 0, 7
v_bfi_b32 v253, s69, v253, v4
global_load_dword v254, v253, s[38:39] sc1
s_add_u32 s71, s71, 1
s_waitcnt vmcnt(0)
v_sub_u32 v254, v254, v5
v_cmp_lt_u32 vcc, 0x7f, v254
s_cbranch_vccz .Lgu_rt12
s_cmp_lt_u32 s71, 3000000
s_cbranch_scc1 .Lgu_sp11
s_branch .Lgu_rt12
.Lgu_sp13:
s_sleep 1
v_add_lshl_u32 v253, v5, 0, 7
v_bfi_b32 v253, s69, v253, v4
global_load_dword v255, v253, s[38:39] sc1
s_add_u32 s71, s71, 1
s_waitcnt vmcnt(0)
v_sub_u32 v255, v255, v5
v_cmp_lt_u32 vcc, 0x7f, v255
s_cbranch_vccz .Lgu_rt14
s_cmp_lt_u32 s71, 3000000
s_cbranch_scc1 .Lgu_sp13
s_branch .Lgu_rt14
.Lgu_sp16:
s_sleep 1
v_add_lshl_u32 v253, v5, 0, 7
v_bfi_b32 v253, s69, v253, v4
)"
R"(global_load_dword v254, v253, s[38:39] sc1
s_add_u32 s71, s71, 1
s_waitcnt vmcnt(0)
v_sub_u32 v254, v254, v5
v_cmp_lt_u32 vcc, 0x7f, v254
s_cbranch_vccz .Lgu_rt17
s_cmp_lt_u32 s71, 3000000
s_cbranch_scc1 .Lgu_sp16
s_branch .Lgu_rt17
.Lgu_sp20:
s_sleep 1
v_add_lshl_u32 v253, v17, 0, 7
v_bfi_b32 v253, s69, v253, v16
global_load_dword v230, v253, s[38:39] sc1
s_add_u32 s71, s71, 1
s_waitcnt vmcnt(0)
v_sub_u32 v230, v230, v17
v_cmp_lt_u32 vcc, 0x7f, v230
s_and_b64 vcc, vcc, s[76:77]
s_cbranch_vccz .Lgu_rt21
s_cmp_lt_u32 s71, 3000000
s_cbranch_scc1 .Lgu_sp20
s_branch .Lgu_rt21
.Lgu_sp22:
s_sleep 1
v_add_lshl_u32 v253, v17, 0, 7
v_bfi_b32 v253, s69, v253, v16
global_load_dword v230, v253, s[38:39] sc1
s_add_u32 s71, s71, 1
s_waitcnt vmcnt(0)
v_sub_u32 v230, v230, v17
v_cmp_lt_u32 vcc, 0x7f, v230
s_and_b64 vcc, vcc, s[76:77]
s_cbranch_vccz .Lgu_rt23
s_cmp_lt_u32 s71, 3000000
s_cbranch_scc1 .Lgu_sp22
s_branch .Lgu_rt23
.Lgu_sp27:
s_sleep 1
v_add_lshl_u32 v253, v233, 0, 7
v_bfi_b32 v253, s69, v253, v16
global_load_dword v230, v253, s[38:39] sc1
s_add_u32 s71, s71, 1
s_waitcnt vmcnt(0)
v_sub_u32 v230, v230, v233
v_cmp_lt_u32 vcc, 0x7f, v230
s_cbranch_vccz .Lgu_rt28
s_cmp_lt_u32 s71, 3000000
s_cbranch_scc1 .Lgu_sp27
s_branch .Lgu_rt28
.Le3:
)"
    :: "{v[0:31]}"(va), "{s[36:51]}"(sa), "{s[52:67]}"(sb) : "v255", "a255", "s99", "vcc", "memory");
  }
}

extern "C" void kernel_launch(void* const* d_in, const int* in_sizes, int n_in,
                              void* d_out, int out_size, void* d_ws, size_t ws_size,
                              hipStream_t stream) {
  k_main<<<256, 256, 0, stream>>>((const char*)d_in[0], (const char*)d_in[1], (const char*)d_in[2], (const char*)d_in[4],
                                  (const char*)d_in[5], (const char*)d_in[6], (char*)d_ws, (char*)d_out);
}
```

```cpp
#include <hip/hip_runtime.h>
#include <hip/hip_bf16.h>
#include <stdint.h>
typedef __attribute__((ext_vector_type(32))) unsigned u32x32;
typedef __attribute__((ext_vector_type(16))) unsigned u32x16;
constexpr unsigned RP = 80u;
constexpr unsigned PAD = 12u;
constexpr unsigned XT = 18432u;
constexpr unsigned X1P = 18688u;
constexpr unsigned HT = 147456u;
constexpr unsigned PT = 110592u;
constexpr unsigned FL = 1024u;
constexpr unsigned FR = 118784u;
constexpr unsigned WS_FL = 16384u;
constexpr unsigned WS_ST = 491520u;
constexpr unsigned WS_O = 1048576u;
constexpr unsigned WS_X1 = 6946816u;
constexpr unsigned WS_X2 = 13000704u;
constexpr unsigned WS_XD = 15396864u;
constexpr unsigned WS_P = 17977344u;
constexpr unsigned WS_H = 53366784u;
constexpr unsigned WS_END = 100552704u;
constexpr unsigned FO = 0u;
constexpr unsigned FX1 = 8192u;
constexpr unsigned FX2 = 65536u;
constexpr unsigned FP = 10240u;
constexpr unsigned FH = 28672u;
__device__ __forceinline__ void touch(const void* p) {
  asm volatile("s_mov_b32 m0, 0x27f00\n\ts_nop 0\n\tglobal_load_lds_dword %0, off" :: "v"(p) : "memory");
}
__device__ __forceinline__ unsigned uni(unsigned v) { return __builtin_amdgcn_readfirstlane(v); }
#define TOUCH_CODE(K, B, N) do { unsigned long long pc; unsigned len; int rel;                   \
    asm volatile("s_getpc_b64 %0\n.Lpc" #K ":\n\ts_mov_b32 %1, .Le" #K "-(" B ")-4\n\ts_mov_b32 %2, (" B ")-.Lpc" #K : "=s"(pc), "=s"(len), "=s"(rel)); \
    touch((const char*)pc + rel + min(((N) * 64 + lane) * 128, len)); } while (0)
__global__ __launch_bounds__(256, 1) void k_main(const char* x0, const char* wqkv, const char* wo, const char* wg, const char* wu, const char* wd,
                                                 char* ws, char* out) {
  __shared__ __attribute__((aligned(1024))) char smem[163840];
  asm volatile("" :: "s"((unsigned)(size_t)(__attribute__((address_space(3))) const char*)smem) : "memory");
  unsigned lane = threadIdx.x & 63;
  const unsigned wave = uni(threadIdx.x >> 6), xc = blockIdx.x & 7, jj = blockIdx.x >> 3;
  if (wave == 3) { if (jj < 8) TOUCH_CODE(0, ".Lpc0", jj); else if (jj < 16) TOUCH_CODE(1, ".Lb1-2048", jj - 8); else if (jj < 24) TOUCH_CODE(2, ".Lb2-2048", jj - 16); else TOUCH_CODE(3, ".Lb3-2048", jj - 24); };
  const bool ev = !(xc & 1); const unsigned xh = xc >> 1;
  if (ev && jj >= 8) { const unsigned u = min((jj - 8) * 216 + threadIdx.x, 5183u), sg = u / 3; touch(wqkv + sg * 1152 + xh * 288 + min((u - 3 * sg) * 128, 284u)); }
  if (__builtin_expect(ev && jj < 8, 1)) {
    const unsigned qa_hg = xc + (jj >> 2), qa_rho = jj & 3, pq0 = wave < 2 ? wave * 5 : 2 + wave * 4;
    unsigned qx[5];
#pragma unroll
    for (unsigned j = 0; j < 5; ++j)
      qx[j] = ((qa_rho * 32 + 31) * 16 + ((pq0 + j) & 1) * 8 + (lane >> 3)) * 1152 + ((pq0 + j) >> 1) * 128 + (((lane ^ (lane >> 3)) & 7) << 4) + 4096 - j * 1024;
    const unsigned long long xb = (unsigned long long)(x0 - 2048);
    asm volatile("s_add_u32 m0, %6, 0\n\ts_sub_u32 vcc_lo, %7, 0\n\ts_subb_u32 vcc_hi, %8, 0\n\tglobal_load_lds_dwordx4 %1, vcc offset:-2048 sc1\n\tglobal_load_lds_dwordx4 %2, vcc offset:-1024 sc1\n\tglobal_load_lds_dwordx4 %3, vcc offset:0 sc1\n\tglobal_load_lds_dwordx4 %4, vcc offset:1024 sc1\n\ts_cmp_gt_u32 %9, 1\n\ts_cbranch_scc1 .Lqx0\n\tglobal_load_lds_dwordx4 %5, vcc offset:2048 sc1\n.Lqx0:\n\ts_add_u32 m0, %6, 18432\n\ts_sub_u32 vcc_lo, %7, 18432\n\ts_subb_u32 vcc_hi, %8, 0\n\tglobal_load_lds_dwordx4 %1, vcc offset:-2048 sc1\n\tglobal_load_lds_dwordx4 %2, vcc offset:-1024 sc1\n\tglobal_load_lds_dwordx4 %3, vcc offset:0 sc1\n\tglobal_load_lds_dwordx4 %4, vcc offset:1024 sc1\n\ts_cmp_gt_u32 %9, 1\n\ts_cbranch_scc1 .Lqx1\n\tglobal_load_lds_dwordx4 %5, vcc offset:2048 sc1\n.Lqx1:\n\t" : "+v"(lane) : "v"(qx[0]), "v"(qx[1]), "v"(qx[2]), "v"(qx[3]), "v"(qx[4]), "s"(pq0 * 1024 + 2048),
                 "s"((unsigned)xb), "s"((unsigned)(xb >> 32)), "s"(wave) : "vcc", "scc", "memory");
    const unsigned r = lane & 15, g = lane >> 4, q = r >> 2, p = lane & 3;
    const unsigned hg = qa_hg, rho = qa_rho, row0 = rho * RP + PAD, pc0 = wave < 2 ? wave * 5 : 2 + wave * 4, hb = wave * 36 + (wave & 1) * 4;
    auto oa = [&](unsigned b) { return 43520 + (b >> 4) * 256 + r * 16 + (b & 15); };
    const unsigned m0 = 4 * g + (r & 1), m1 = m0 + 2, hm = (r & 1) ? 0xFFFF0000u : 0xFFFFu, ninf = 0xFF800000u;
    u32x32 va, vb; u32x16 sa, sb;
    va[0] = (((0 + lane) * 5462u) >> 16) * (1152 - 192) + lane * 16 + 4096;
    va[1] = (((64 + lane) * 5462u) >> 16) * (1152 - 192) + lane * 16 + 4096;
    va[2] = (((128 + lane) * 5462u) >> 16) * (1152 - 192) + lane * 16 + 4096;
    va[3] = (((192 + lane) * 5462u) >> 16) * (1152 - 192) + lane * 16 + 4096;
    va[4] = (((256 + lane) * 5462u) >> 16) * (1152 - 192) + lane * 16 + 4096;
    va[5] = (((320 + lane) * 5462u) >> 16) * (1152 - 192) + lane * 16 + 4096;
    va[6] = (((384 + lane) * 5462u) >> 16) * (1152 - 192) + lane * 16 + 4096;
    va[7] = (((448 + lane) * 5462u) >> 16) * (1152 - 192) + lane * 16 + 4096;
    va[8] = (((512 + lane) * 5462u) >> 16) * (1152 - 192) + lane * 16 + 12288;
    va[9] = (8 * g + q) * 576 + (hg & 3) * 16 + hb + p * 8;
    va[10] = (8 * g + q) * 576 + (hg & 3) * 16 + 32 + (p & 1) * 72 + (wave == 0 ? (p >> 1) * 192 : 384);
    va[11] = qx[0];
    va[12] = qx[1];
    va[13] = qx[2];
    va[14] = qx[3];
    va[15] = qx[4];
    va[16] = WS_O + (row0 - 1) * XT + hg * 2304 + wave * 1024 + (wave < 2 ? lane : r) * 16;
    va[17] = 43520 + wave * 1024 + (wave < 2 ? lane : r) * 16;
    va[18] = oa(hb + g * 8);
    va[19] = ((g & 1) == (wave >> 1)) ? oa(32 + (wave & 1) * 4 + (wave >> 1) * 72) : 46336 + lane * 4;
    va[20] = g == 0 ? 40960 + ((wave >> 1) * 16 + r) * 8 + (wave & 1) * 4 : 46080;
    va[21] = 41216 + ((wave >> 1) * 16 + r) * 8 + (wave & 1) * 4;
    va[22] = lane * 8;
    va[23] = lane * 16;
    va[24] = 43008 + r * 4;
    va[25] = 43008 + g * 16;
    va[26] = 41984 + (q * 16 + r) * 16 + p * 4;
    va[27] = 43136 + (((m0 > r) ? 0 : 1) * 16 + m0) * 4;
    va[28] = 43136 + (((m1 > r) ? 0 : 1) * 16 + m1) * 4;
    va[29] = 43136 + (((m0 > r) ? 1 : 0) * 16 + m0) * 4;
    va[30] = 43136 + (((m1 > r) ? 1 : 0) * 16 + m1) * 4;
    va[31] = 0u;
    vb[0] = (4 * g + 0 > r && !((r ^ 0) & 1)) ? 0u : ninf;
    vb[1] = (4 * g + 1 > r && !((r ^ 1) & 1)) ? 0u : ninf;
    vb[2] = (4 * g + 2 > r && !((r ^ 2) & 1)) ? 0u : ninf;
    vb[3] = (4 * g + 3 > r && !((r ^ 3) & 1)) ? 0u : ninf;
    vb[4] = (4 * g + 0 <= r && !((r ^ 0) & 1)) ? 0u : ninf;
    vb[5] = (4 * g + 1 <= r && !((r ^ 1) & 1)) ? 0u : ninf;
    vb[6] = (4 * g + 2 <= r && !((r ^ 2) & 1)) ? 0u : ninf;
    vb[7] = (4 * g + 3 <= r && !((r ^ 3) & 1)) ? 0u : ninf;
    vb[8] = m0 > r ? hm : 0u;
    vb[9] = m1 > r ? hm : 0u;
    vb[10] = m0 > r ? 0u : hm;
    vb[11] = m1 > r ? 0u : hm;
    vb[12] = WS_FL + rho * FR + FX2 + min(lane, 35u) * FL;
    vb[13] = WS_FL + rho * FR + FX2 + min(lane, 35u) * FL;
    vb[14] = 0u - 29;
    vb[15] = WS_FL + rho * FR + FO + hg * FL;
    vb[16] = 0u - 2;
    vb[17] = WS_FL + lane * FL;
    vb[18] = 0u;
    vb[19] = r * 128 + (((0 + g) ^ (r & 7)) << 4);
    vb[20] = r * 128 + (((4 + g) ^ (r & 7)) << 4);
    sa[0] = 0 + wave; sa[2] = (unsigned)(unsigned long long)ws; sa[3] = (unsigned)((unsigned long long)ws >> 32);
    sa[4] = uni((unsigned)(unsigned long long)(wqkv + ((hg * 144) & ~63u) + wave * (16 * 3456) + 0 * 64 * 3456));
    sa[5] = uni((unsigned)((unsigned long long)(wqkv + ((hg * 144) & ~63u) + wave * (16 * 3456) + 0 * 64 * 3456) >> 32));
    sa[6] = uni((unsigned)(unsigned long long)(x0 - 2048 - 2 * XT));
    sa[7] = uni((unsigned)((unsigned long long)(x0 - 2048 - 2 * XT) >> 32));
    sa[8] = uni((unsigned)(unsigned long long)((ws + WS_X2) - x0 + (rho ? 32 : 31) * (long long)XT));
    sa[9] = uni((unsigned)((unsigned long long)((ws + WS_X2) - x0 + (rho ? 32 : 31) * (long long)XT) >> 32));
    sa[10] = uni((unsigned)(unsigned long long)(-(long long)XT));
    sa[11] = uni((unsigned)((unsigned long long)(-(long long)XT) >> 32));
    sa[12] = uni(47104 + wave * 9216);
    sa[13] = uni(pc0 * 1024 + 2048 + 0x10000);
    sa[14] = uni(rho ? 0u : XT);
    sa[15] = uni(rho ? 0u - FR : 0u);
    sb[0] = uni(rho ? 0u : ninf);
    sb[1] = uni(hg * 4 + rho);
    asm volatile(
R"(.Lb0:
s_movk_i32 s69, 0x380
s_mov_b32 s71, 0
s_mov_b32 s74, 0
s_load_dword s37, s[38:39], 0x0
s_nop 1
s_and_b32 s75, s36, 3
s_cmp_eq_u32 s75, 1
s_cbranch_scc1 .Lqa_w2
s_cmp_eq_u32 s75, 2
s_cbranch_scc1 .Lqa_w3
s_cmp_eq_u32 s75, 3
s_cbranch_scc1 .Lqa_w4
.Lqa_w1:
s_getpc_b64 s[90:91]
.Lqa_pc23:
s_add_u32 s90, s90, .Lqa_e8-.Lqa_pc23
s_addc_u32 s91, s91, 0
s_branch .Lqa_k15
.Lqa_bk22:
v_add_u32 v255, 0x10000, v9
v_add_u32 v162, 0x20000, v9
v_add_u32 v163, 0x10000, v10
v_add_u32 v164, 0x20000, v10
v_mov_b32 v106, 0x3F803F80
v_mov_b32 v107, 0x3F803F80
v_mov_b32 v108, 0x3F803F80
v_mov_b32 v109, 0x3F803F80
v_mov_b32 v141, 0
v_mov_b32 v145, 0
v_mov_b32 v149, 0
v_mov_b32 v153, 0
v_mov_b32 v165, 46080
s_waitcnt lgkmcnt(0)
s_mul_i32 s37, s37, 0x9E3779B1
s_add_u32 s37, s37, 0x7F4A7C15
v_add_u32 v46, s37, v46
v_add_u32 v48, s37, v48
v_add_u32 v50, s37, v50
ds_write_b32 v165, v141
s_mov_b64 s[76:77], 0
s_mov_b32 s73, 18
s_add_u32 m0, s48, 4096
s_nop 0
global_load_lds_dwordx4 v0, s[40:41] offset:-4096
global_load_lds_dwordx4 v1, s[40:41] offset:-3072
global_load_lds_dwordx4 v2, s[40:41] offset:-2048
global_load_lds_dwordx4 v3, s[40:41] offset:-1024
global_load_lds_dwordx4 v4, s[40:41] offset:0
global_load_lds_dwordx4 v5, s[40:41] offset:1024
global_load_lds_dwordx4 v6, s[40:41] offset:2048
global_load_lds_dwordx4 v7, s[40:41] offset:3072
s_add_u32 m0, s48, 12288
s_nop 0
global_load_lds_dwordx4 v8, s[40:41] offset:-4096
s_add_u32 s40, s40, 221184
s_addc_u32 s41, s41, 0
s_add_u32 m0, s48, 40960
s_nop 0
global_load_lds_dwordx4 v0, s[40:41] offset:-4096
global_load_lds_dwordx4 v1, s[40:41] offset:-3072
global_load_lds_dwordx4 v2, s[40:41] offset:-2048
global_load_lds_dwordx4 v3, s[40:41] offset:-1024
global_load_lds_dwordx4 v4, s[40:41] offset:0
global_load_lds_dwordx4 v5, s[40:41] offset:1024
global_load_lds_dwordx4 v6, s[40:41] offset:2048
global_load_lds_dwordx4 v7, s[40:41] offset:3072
s_add_u32 m0, s48, 49152
s_nop 0
global_load_lds_dwordx4 v8, s[40:41] offset:-4096
s_add_u32 s40, s40, 221184
s_addc_u32 s41, s41, 0
s_waitcnt vmcnt(9) lgkmcnt(0)
s_barrier
s_add_u32 m0, s48, 77824
ds_read_b64_tr_b16 a[0:1], v9 offset:47104
ds_read_b64_tr_b16 a[2:3], v9 offset:49408
global_load_lds_dwordx4 v0, s[40:41] offset:-4096
ds_read_b64_tr_b16 a[4:5], v9 offset:47296
ds_read_b64_tr_b16 a[6:7], v9 offset:49600
global_load_lds_dwordx4 v1, s[40:41] offset:-3072
ds_read_b64_tr_b16 a[8:9], v9 offset:47488
global_load_lds_dwordx4 v2, s[40:41] offset:-2048
ds_read_b64_tr_b16 a[10:11], v9 offset:49792
ds_read_b64_tr_b16 a[216:217], v10 offset:47104
global_load_lds_dwordx4 v3, s[40:41] offset:-1024
ds_read_b64_tr_b16 a[218:219], v10 offset:49408
global_load_lds_dwordx4 v4, s[40:41] offset:0
ds_read_b128 v[166:169], v51 offset:0
ds_read_b128 v[174:177], v51 offset:18432
global_load_lds_dwordx4 v5, s[40:41] offset:1024
ds_read_b64_tr_b16 a[12:13], v255 offset:0
global_load_lds_dwordx4 v6, s[40:41] offset:2048
ds_read_b64_tr_b16 a[14:15], v255 offset:2304
ds_read_b64_tr_b16 a[16:17], v255 offset:192
global_load_lds_dwordx4 v7, s[40:41] offset:3072
ds_read_b64_tr_b16 a[18:19], v255 offset:2496
s_add_u32 m0, s48, 86016
ds_read_b64_tr_b16 a[20:21], v255 offset:384
ds_read_b64_tr_b16 a[22:23], v255 offset:2688
global_load_lds_dwordx4 v8, s[40:41] offset:-4096
ds_read_b64_tr_b16 a[220:221], v163 offset:0
s_add_u32 s40, s40, 221184
ds_read_b64_tr_b16 a[222:223], v163 offset:2304
ds_read_b128 v[170:173], v52 offset:0
s_addc_u32 s41, s41, 0
ds_read_b128 v[178:181], v52 offset:18432
s_waitcnt vmcnt(9) lgkmcnt(0)
s_barrier
s_add_u32 m0, s48, 4096
ds_read_b64_tr_b16 a[24:25], v255 offset:18432
v_mfma_f32_16x16x32_bf16 v[86:89], a[0:3], v[106:109], 0
ds_read_b64_tr_b16 a[26:27], v255 offset:20736
v_mfma_f32_16x16x32_bf16 v[90:93], a[4:7], v[106:109], 0
global_load_lds_dwordx4 v0, s[40:41] offset:-4096
ds_read_b64_tr_b16 a[28:29], v255 offset:18624
v_mfma_f32_16x16x32_bf16 v[94:97], v[106:109], a[8:11], 0
ds_read_b64_tr_b16 a[30:31], v255 offset:20928
global_load_lds_dwordx4 v1, s[40:41] offset:-3072
v_mfma_f32_16x16x32_bf16 v[98:101], a[216:219], v[106:109], 0
ds_read_b64_tr_b16 a[32:33], v255 offset:18816
v_mfma_f32_16x16x32_bf16 v[110:113], a[0:3], v[166:169], 0
ds_read_b64_tr_b16 a[34:35], v255 offset:21120
global_load_lds_dwordx4 v2, s[40:41] offset:-2048
v_mfma_f32_16x16x32_bf16 v[114:117], a[4:7], v[166:169], 0
ds_read_b64_tr_b16 a[224:225], v163 offset:18432
v_mfma_f32_16x16x32_bf16 v[118:121], v[166:169], a[8:11], 0
global_load_lds_dwordx4 v3, s[40:41] offset:-1024
ds_read_b64_tr_b16 a[226:227], v163 offset:20736
v_mfma_f32_16x16x32_bf16 v[134:137], a[216:219], v[166:169], 0
ds_read_b128 v[182:185], v51 offset:2048
global_load_lds_dwordx4 v4, s[40:41] offset:0
v_mfma_f32_16x16x32_bf16 v[122:125], a[0:3], v[174:177], 0
ds_read_b128 v[190:193], v51 offset:20480
v_mfma_f32_16x16x32_bf16 v[126:129], a[4:7], v[174:177], 0
ds_read_b64_tr_b16 a[36:37], v255 offset:36864
global_load_lds_dwordx4 v5, s[40:41] offset:1024
v_mfma_f32_16x16x32_bf16 v[130:133], v[174:177], a[8:11], 0
ds_read_b64_tr_b16 a[38:39], v255 offset:39168
v_mfma_f32_16x16x32_bf16 v[158:161], a[216:219], v[174:177], 0
global_load_lds_dwordx4 v6, s[40:41] offset:2048
ds_read_b64_tr_b16 a[40:41], v255 offset:37056
v_mfma_f32_16x16x32_bf16 v[86:89], a[12:15], v[106:109], v[86:89]
ds_read_b64_tr_b16 a[42:43], v255 offset:39360
v_mfma_f32_16x16x32_bf16 v[90:93], a[16:19], v[106:109], v[90:93]
global_load_lds_dwordx4 v7, s[40:41] offset:3072
ds_read_b64_tr_b16 a[44:45], v255 offset:37248
v_mfma_f32_16x16x32_bf16 v[94:97], v[106:109], a[20:23], v[94:97]
s_add_u32 m0, s48, 12288
ds_read_b64_tr_b16 a[46:47], v255 offset:39552
v_mfma_f32_16x16x32_bf16 v[98:101], a[220:223], v[106:109], v[98:101]
ds_read_b64_tr_b16 a[228:229], v163 offset:36864
v_mfma_f32_16x16x32_bf16 v[110:113], a[12:15], v[170:173], v[110:113]
global_load_lds_dwordx4 v8, s[40:41] offset:-4096
ds_read_b64_tr_b16 a[230:231], v163 offset:39168
v_mfma_f32_16x16x32_bf16 v[114:117], a[16:19], v[170:173], v[114:117]
ds_read_b128 v[186:189], v52 offset:2048
v_mfma_f32_16x16x32_bf16 v[118:121], v[170:173], a[20:23], v[118:121]
s_add_u32 s40, s40, 221184
ds_read_b128 v[194:197], v52 offset:20480
v_mfma_f32_16x16x32_bf16 v[134:137], a[220:223], v[170:173], v[134:137]
v_mfma_f32_16x16x32_bf16 v[122:125], a[12:15], v[178:181], v[122:125]
s_addc_u32 s41, s41, 0
v_mfma_f32_16x16x32_bf16 v[126:129], a[16:19], v[178:181], v[126:129]
v_mfma_f32_16x16x32_bf16 v[130:133], v[178:181], a[20:23], v[130:133]
v_mfma_f32_16x16x32_bf16 v[158:161], a[220:223], v[178:181], v[158:161]
s_waitcnt vmcnt(9) lgkmcnt(0)
s_barrier
s_add_u32 m0, s48, 40960
ds_read_b64_tr_b16 a[48:49], v255 offset:55296
v_mfma_f32_16x16x32_bf16 v[86:89], a[24:27], v[106:109], v[86:89]
ds_read_b64_tr_b16 a[50:51], v255 offset:57600
v_mfma_f32_16x16x32_bf16 v[90:93], a[28:31], v[106:109], v[90:93]
global_load_lds_dwordx4 v0, s[40:41] offset:-4096
ds_read_b64_tr_b16 a[52:53], v255 offset:55488
v_mfma_f32_16x16x32_bf16 v[94:97], v[106:109], a[32:35], v[94:97]
ds_read_b64_tr_b16 a[54:55], v255 offset:57792
global_load_lds_dwordx4 v1, s[40:41] offset:-3072
v_mfma_f32_16x16x32_bf16 v[98:101], a[224:227], v[106:109], v[98:101]
ds_read_b64_tr_b16 a[56:57], v255 offset:55680
v_mfma_f32_16x16x32_bf16 v[110:113], a[24:27], v[182:185], v[110:113]
ds_read_b64_tr_b16 a[58:59], v255 offset:57984
global_load_lds_dwordx4 v2, s[40:41] offset:-2048
v_mfma_f32_16x16x32_bf16 v[114:117], a[28:31], v[182:185], v[114:117]
ds_read_b64_tr_b16 a[232:233], v163 offset:55296
v_mfma_f32_16x16x32_bf16 v[118:121], v[182:185], a[32:35], v[118:121]
global_load_lds_dwordx4 v3, s[40:41] offset:-1024
ds_read_b64_tr_b16 a[234:235], v163 offset:57600
v_mfma_f32_16x16x32_bf16 v[134:137], a[224:227], v[182:185], v[134:137]
ds_read_b128 v[166:169], v51 offset:4096
global_load_lds_dwordx4 v4, s[40:41] offset:0
v_mfma_f32_16x16x32_bf16 v[122:125], a[24:27], v[190:193], v[122:125]
ds_read_b128 v[174:177], v51 offset:22528
v_mfma_f32_16x16x32_bf16 v[126:129], a[28:31], v[190:193], v[126:129]
ds_read_b64_tr_b16 a[60:61], v162 offset:8192
global_load_lds_dwordx4 v5, s[40:41] offset:1024
v_mfma_f32_16x16x32_bf16 v[130:133], v[190:193], a[32:35], v[130:133]
ds_read_b64_tr_b16 a[62:63], v162 offset:10496
v_mfma_f32_16x16x32_bf16 v[158:161], a[224:227], v[190:193], v[158:161]
global_load_lds_dwordx4 v6, s[40:41] offset:2048
ds_read_b64_tr_b16 a[64:65], v162 offset:8384
v_mfma_f32_16x16x32_bf16 v[86:89], a[36:39], v[106:109], v[86:89]
ds_read_b64_tr_b16 a[66:67], v162 offset:10688
v_mfma_f32_16x16x32_bf16 v[90:93], a[40:43], v[106:109], v[90:93]
global_load_lds_dwordx4 v7, s[40:41] offset:3072
ds_read_b64_tr_b16 a[68:69], v162 offset:8576
v_mfma_f32_16x16x32_bf16 v[94:97], v[106:109], a[44:47], v[94:97]
s_add_u32 m0, s48, 49152
ds_read_b64_tr_b16 a[70:71], v162 offset:10880
v_mfma_f32_16x16x32_bf16 v[98:101], a[228:231], v[106:109], v[98:101]
ds_read_b64_tr_b16 a[236:237], v164 offset:8192
v_mfma_f32_16x16x32_bf16 v[110:113], a[36:39], v[186:189], v[110:113]
global_load_lds_dwordx4 v8, s[40:41] offset:-4096
ds_read_b64_tr_b16 a[238:239], v164 offset:10496
v_mfma_f32_16x16x32_bf16 v[114:117], a[40:43], v[186:189], v[114:117]
ds_read_b128 v[170:173], v52 offset:4096
v_mfma_f32_16x16x32_bf16 v[118:121], v[186:189], a[44:47], v[118:121]
s_add_u32 s40, s40, 221184
ds_read_b128 v[178:181], v52 offset:22528
v_mfma_f32_16x16x32_bf16 v[134:137], a[228:231], v[186:189], v[134:137]
v_mfma_f32_16x16x32_bf16 v[122:125], a[36:39], v[194:197], v[122:125]
s_addc_u32 s41, s41, 0
v_mfma_f32_16x16x32_bf16 v[126:129], a[40:43], v[194:197], v[126:129]
v_mfma_f32_16x16x32_bf16 v[130:133], v[194:197], a[44:47], v[130:133]
v_mfma_f32_16x16x32_bf16 v[158:161], a[228:231], v[194:197], v[158:161]
s_waitcnt vmcnt(9) lgkmcnt(0)
s_barrier
s_add_u32 m0, s48, 77824
ds_read_b64_tr_b16 a[72:73], v9 offset:47104
v_mfma_f32_16x16x32_bf16 v[86:89], a[48:51], v[106:109], v[86:89]
ds_read_b64_tr_b16 a[74:75], v9 offset:49408
v_mfma_f32_16x16x32_bf16 v[90:93], a[52:55], v[106:109], v[90:93]
global_load_lds_dwordx4 v0, s[40:41] offset:-4096
ds_read_b64_tr_b16 a[76:77], v9 offset:47296
v_mfma_f32_16x16x32_bf16 v[94:97], v[106:109], a[56:59], v[94:97]
ds_read_b64_tr_b16 a[78:79], v9 offset:49600
global_load_lds_dwordx4 v1, s[40:41] offset:-3072
v_mfma_f32_16x16x32_bf16 v[98:101], a[232:235], v[106:109], v[98:101]
ds_read_b64_tr_b16 a[80:81], v9 offset:47488
v_mfma_f32_16x16x32_bf16 v[110:113], a[48:51], v[166:169], v[110:113]
ds_read_b64_tr_b16 a[82:83], v9 offset:49792
global_load_lds_dwordx4 v2, s[40:41] offset:-2048
v_mfma_f32_16x16x32_bf16 v[114:117], a[52:55], v[166:169], v[114:117]
ds_read_b64_tr_b16 a[240:241], v10 offset:47104
v_mfma_f32_16x16x32_bf16 v[118:121], v[166:169], a[56:59], v[118:121]
global_load_lds_dwordx4 v3, s[40:41] offset:-1024
ds_read_b64_tr_b16 a[242:243], v10 offset:49408
v_mfma_f32_16x16x32_bf16 v[134:137], a[232:235], v[166:169], v[134:137]
ds_read_b128 v[182:185], v51 offset:6144
global_load_lds_dwordx4 v4, s[40:41] offset:0
v_mfma_f32_16x16x32_bf16 v[122:125], a[48:51], v[174:177], v[122:125]
ds_read_b128 v[190:193], v51 offset:24576
v_mfma_f32_16x16x32_bf16 v[126:129], a[52:55], v[174:177], v[126:129]
ds_read_b64_tr_b16 a[84:85], v255 offset:0
global_load_lds_dwordx4 v5, s[40:41] offset:1024
v_mfma_f32_16x16x32_bf16 v[130:133], v[174:177], a[56:59], v[130:133]
ds_read_b64_tr_b16 a[86:87], v255 offset:2304
v_mfma_f32_16x16x32_bf16 v[158:161], a[232:235], v[174:177], v[158:161]
global_load_lds_dwordx4 v6, s[40:41] offset:2048
ds_read_b64_tr_b16 a[88:89], v255 offset:192
v_mfma_f32_16x16x32_bf16 v[86:89], a[60:63], v[106:109], v[86:89]
ds_read_b64_tr_b16 a[90:91], v255 offset:2496
v_mfma_f32_16x16x32_bf16 v[90:93], a[64:67], v[106:109], v[90:93]
global_load_lds_dwordx4 v7, s[40:41] offset:3072
ds_read_b64_tr_b16 a[92:93], v255 offset:384
v_mfma_f32_16x16x32_bf16 v[94:97], v[106:109], a[68:71], v[94:97]
s_add_u32 m0, s48, 86016
ds_read_b64_tr_b16 a[94:95], v255 offset:2688
v_mfma_f32_16x16x32_bf16 v[98:101], a[236:239], v[106:109], v[98:101]
ds_read_b64_tr_b16 a[244:245], v163 offset:0
v_mfma_f32_16x16x32_bf16 v[110:113], a[60:63], v[170:173], v[110:113]
global_load_lds_dwordx4 v8, s[40:41] offset:-4096
ds_read_b64_tr_b16 a[246:247], v163 offset:2304
v_mfma_f32_16x16x32_bf16 v[114:117], a[64:67], v[170:173], v[114:117]
ds_read_b128 v[186:189], v52 offset:6144
v_mfma_f32_16x16x32_bf16 v[118:121], v[170:173], a[68:71], v[118:121]
s_add_u32 s40, s40, 221184
ds_read_b128 v[194:197], v52 offset:24576
v_mfma_f32_16x16x32_bf16 v[134:137], a[236:239], v[170:173], v[134:137]
v_mfma_f32_16x16x32_bf16 v[122:125], a[60:63], v[178:181], v[122:125]
s_addc_u32 s41, s41, 0
v_mfma_f32_16x16x32_bf16 v[126:129], a[64:67], v[178:181], v[126:129]
v_mfma_f32_16x16x32_bf16 v[130:133], v[178:181], a[68:71], v[130:133]
v_mfma_f32_16x16x32_bf16 v[158:161], a[236:239], v[178:181], v[158:161]
s_waitcnt vmcnt(9) lgkmcnt(0)
s_barrier
s_add_u32 m0, s48, 4096
ds_read_b64_tr_b16 a[96:97], v255 offset:18432
v_mfma_f32_16x16x32_bf16 v[86:89], a[72:75], v[106:109], v[86:89]
ds_read_b64_tr_b16 a[98:99], v255 offset:20736
v_mfma_f32_16x16x32_bf16 v[90:93], a[76:79], v[106:109], v[90:93]
global_load_lds_dwordx4 v0, s[40:41] offset:-4096
ds_read_b64_tr_b16 a[100:101], v255 offset:18624
v_mfma_f32_16x16x32_bf16 v[94:97], v[106:109], a[80:83], v[94:97]
ds_read_b64_tr_b16 a[102:103], v255 offset:20928
global_load_lds_dwordx4 v1, s[40:41] offset:-3072
v_mfma_f32_16x16x32_bf16 v[98:101], a[240:243], v[106:109], v[98:101]
ds_read_b64_tr_b16 a[104:105], v255 offset:18816
v_mfma_f32_16x16x32_bf16 v[110:113], a[72:75], v[182:185], v[110:113]
ds_read_b64_tr_b16 a[106:107], v255 offset:21120
global_load_lds_dwordx4 v2, s[40:41] offset:-2048
v_mfma_f32_16x16x32_bf16 v[114:117], a[76:79], v[182:185], v[114:117]
ds_read_b64_tr_b16 a[248:249], v163 offset:18432
v_mfma_f32_16x16x32_bf16 v[118:121], v[182:185], a[80:83], v[118:121]
global_load_lds_dwordx4 v3, s[40:41] offset:-1024
ds_read_b64_tr_b16 a[250:251], v163 offset:20736
v_mfma_f32_16x16x32_bf16 v[134:137], a[240:243], v[182:185], v[134:137]
ds_read_b128 v[166:169], v51 offset:8192
global_load_lds_dwordx4 v4, s[40:41] offset:0
v_mfma_f32_16x16x32_bf16 v[122:125], a[72:75], v[190:193], v[122:125]
ds_read_b128 v[174:177], v51 offset:26624
v_mfma_f32_16x16x32_bf16 v[126:129], a[76:79], v[190:193], v[126:129]
ds_read_b64_tr_b16 a[108:109], v255 offset:36864
global_load_lds_dwordx4 v5, s[40:41] offset:1024
v_mfma_f32_16x16x32_bf16 v[130:133], v[190:193], a[80:83], v[130:133]
ds_read_b64_tr_b16 a[110:111], v255 offset:39168
v_mfma_f32_16x16x32_bf16 v[158:161], a[240:243], v[190:193], v[158:161]
global_load_lds_dwordx4 v6, s[40:41] offset:2048
ds_read_b64_tr_b16 a[112:113], v255 offset:37056
v_mfma_f32_16x16x32_bf16 v[86:89], a[84:87], v[106:109], v[86:89]
ds_read_b64_tr_b16 a[114:115], v255 offset:39360
v_mfma_f32_16x16x32_bf16 v[90:93], a[88:91], v[106:109], v[90:93]
global_load_lds_dwordx4 v7, s[40:41] offset:3072
ds_read_b64_tr_b16 a[116:117], v255 offset:37248
v_mfma_f32_16x16x32_bf16 v[94:97], v[106:109], a[92:95], v[94:97]
s_add_u32 m0, s48, 12288
ds_read_b64_tr_b16 a[118:119], v255 offset:39552
v_mfma_f32_16x16x32_bf16 v[98:101], a[244:247], v[106:109], v[98:101]
ds_read_b64_tr_b16 a[252:253], v163 offset:36864
v_mfma_f32_16x16x32_bf16 v[110:113], a[84:87], v[186:189], v[110:113]
global_load_lds_dwordx4 v8, s[40:41] offset:-4096
ds_read_b64_tr_b16 a[254:255], v163 offset:39168
v_mfma_f32_16x16x32_bf16 v[114:117], a[88:91], v[186:189], v[114:117]
ds_read_b128 v[170:173], v52 offset:8192
v_mfma_f32_16x16x32_bf16 v[118:121], v[186:189], a[92:95], v[118:121]
s_add_u32 s40, s40, 221184
ds_read_b128 v[178:181], v52 offset:26624
v_mfma_f32_16x16x32_bf16 v[134:137], a[244:247], v[186:189], v[134:137]
v_mfma_f32_16x16x32_bf16 v[122:125], a[84:87], v[194:197], v[122:125]
s_addc_u32 s41, s41, 0
v_mfma_f32_16x16x32_bf16 v[126:129], a[88:91], v[194:197], v[126:129]
v_mfma_f32_16x16x32_bf16 v[130:133], v[194:197], a[92:95], v[130:133]
v_mfma_f32_16x16x32_bf16 v[158:161], a[244:247], v[194:197], v[158:161]
s_waitcnt vmcnt(9) lgkmcnt(0)
s_barrier
s_add_u32 m0, s48, 40960
ds_read_b64_tr_b16 a[120:121], v255 offset:55296
v_mfma_f32_16x16x32_bf16 v[86:89], a[96:99], v[106:109], v[86:89]
ds_read_b64_tr_b16 a[122:123], v255 offset:57600
v_mfma_f32_16x16x32_bf16 v[90:93], a[100:103], v[106:109], v[90:93]
global_load_lds_dwordx4 v0, s[40:41] offset:-4096
ds_read_b64_tr_b16 a[124:125], v255 offset:55488
v_mfma_f32_16x16x32_bf16 v[94:97], v[106:109], a[104:107], v[94:97]
ds_read_b64_tr_b16 a[126:127], v255 offset:57792
global_load_lds_dwordx4 v1, s[40:41] offset:-3072
v_mfma_f32_16x16x32_bf16 v[98:101], a[248:251], v[106:109], v[98:101]
ds_read_b64_tr_b16 a[128:129], v255 offset:55680
v_mfma_f32_16x16x32_bf16 v[110:113], a[96:99], v[166:169], v[110:113]
ds_read_b64_tr_b16 a[130:131], v255 offset:57984
global_load_lds_dwordx4 v2, s[40:41] offset:-2048
v_mfma_f32_16x16x32_bf16 v[114:117], a[100:103], v[166:169], v[114:117]
ds_read_b64_tr_b16 v[54:55], v163 offset:55296
v_mfma_f32_16x16x32_bf16 v[118:121], v[166:169], a[104:107], v[118:121]
global_load_lds_dwordx4 v3, s[40:41] offset:-1024
ds_read_b64_tr_b16 v[56:57], v163 offset:57600
v_mfma_f32_16x16x32_bf16 v[134:137], a[248:251], v[166:169], v[134:137]
ds_read_b128 v[182:185], v51 offset:10240
global_load_lds_dwordx4 v4, s[40:41] offset:0
v_mfma_f32_16x16x32_bf16 v[122:125], a[96:99], v[174:177], v[122:125]
ds_read_b128 v[190:193], v51 offset:28672
v_mfma_f32_16x16x32_bf16 v[126:129], a[100:103], v[174:177], v[126:129]
ds_read_b64_tr_b16 a[132:133], v162 offset:8192
global_load_lds_dwordx4 v5, s[40:41] offset:1024
v_mfma_f32_16x16x32_bf16 v[130:133], v[174:177], a[104:107], v[130:133]
ds_read_b64_tr_b16 a[134:135], v162 offset:10496
v_mfma_f32_16x16x32_bf16 v[158:161], a[248:251], v[174:177], v[158:161]
global_load_lds_dwordx4 v6, s[40:41] offset:2048
ds_read_b64_tr_b16 a[136:137], v162 offset:8384
v_mfma_f32_16x16x32_bf16 v[86:89], a[108:111], v[106:109], v[86:89]
ds_read_b64_tr_b16 a[138:139], v162 offset:10688
v_mfma_f32_16x16x32_bf16 v[90:93], a[112:115], v[106:109], v[90:93]
global_load_lds_dwordx4 v7, s[40:41] offset:3072
ds_read_b64_tr_b16 a[140:141], v162 offset:8576
v_mfma_f32_16x16x32_bf16 v[94:97], v[106:109], a[116:119], v[94:97]
s_add_u32 m0, s48, 49152
ds_read_b64_tr_b16 a[142:143], v162 offset:10880
v_mfma_f32_16x16x32_bf16 v[98:101], a[252:255], v[106:109], v[98:101]
ds_read_b64_tr_b16 v[58:59], v164 offset:8192
v_mfma_f32_16x16x32_bf16 v[110:113], a[108:111], v[170:173], v[110:113]
global_load_lds_dwordx4 v8, s[40:41] offset:-4096
ds_read_b64_tr_b16 v[60:61], v164 offset:10496
v_mfma_f32_16x16x32_bf16 v[114:117], a[112:115], v[170:173], v[114:117]
ds_read_b128 v[186:189], v52 offset:10240
v_mfma_f32_16x16x32_bf16 v[118:121], v[170:173], a[116:119], v[118:121]
s_add_u32 s40, s40, 221184
ds_read_b128 v[194:197], v52 offset:28672
v_mfma_f32_16x16x32_bf16 v[134:137], a[252:255], v[170:173], v[134:137]
v_mfma_f32_16x16x32_bf16 v[122:125], a[108:111], v[178:181], v[122:125]
s_addc_u32 s41, s41, 0
v_mfma_f32_16x16x32_bf16 v[126:129], a[112:115], v[178:181], v[126:129]
)"
R"(v_mfma_f32_16x16x32_bf16 v[130:133], v[178:181], a[116:119], v[130:133]
v_mfma_f32_16x16x32_bf16 v[158:161], a[252:255], v[178:181], v[158:161]
s_waitcnt vmcnt(9) lgkmcnt(0)
s_barrier
s_add_u32 m0, s48, 77824
ds_read_b64_tr_b16 a[144:145], v9 offset:47104
v_mfma_f32_16x16x32_bf16 v[86:89], a[120:123], v[106:109], v[86:89]
ds_read_b64_tr_b16 a[146:147], v9 offset:49408
v_mfma_f32_16x16x32_bf16 v[90:93], a[124:127], v[106:109], v[90:93]
global_load_lds_dwordx4 v0, s[40:41] offset:-4096
ds_read_b64_tr_b16 a[148:149], v9 offset:47296
v_mfma_f32_16x16x32_bf16 v[94:97], v[106:109], a[128:131], v[94:97]
ds_read_b64_tr_b16 a[150:151], v9 offset:49600
global_load_lds_dwordx4 v1, s[40:41] offset:-3072
v_mfma_f32_16x16x32_bf16 v[98:101], v[54:57], v[106:109], v[98:101]
ds_read_b64_tr_b16 a[152:153], v9 offset:47488
v_mfma_f32_16x16x32_bf16 v[110:113], a[120:123], v[182:185], v[110:113]
ds_read_b64_tr_b16 a[154:155], v9 offset:49792
global_load_lds_dwordx4 v2, s[40:41] offset:-2048
v_mfma_f32_16x16x32_bf16 v[114:117], a[124:127], v[182:185], v[114:117]
ds_read_b64_tr_b16 v[62:63], v10 offset:47104
v_mfma_f32_16x16x32_bf16 v[118:121], v[182:185], a[128:131], v[118:121]
global_load_lds_dwordx4 v3, s[40:41] offset:-1024
ds_read_b64_tr_b16 v[64:65], v10 offset:49408
v_mfma_f32_16x16x32_bf16 v[134:137], v[54:57], v[182:185], v[134:137]
ds_read_b128 v[166:169], v51 offset:12288
global_load_lds_dwordx4 v4, s[40:41] offset:0
v_mfma_f32_16x16x32_bf16 v[122:125], a[120:123], v[190:193], v[122:125]
ds_read_b128 v[174:177], v51 offset:30720
v_mfma_f32_16x16x32_bf16 v[126:129], a[124:127], v[190:193], v[126:129]
ds_read_b64_tr_b16 a[156:157], v255 offset:0
global_load_lds_dwordx4 v5, s[40:41] offset:1024
v_mfma_f32_16x16x32_bf16 v[130:133], v[190:193], a[128:131], v[130:133]
ds_read_b64_tr_b16 a[158:159], v255 offset:2304
v_mfma_f32_16x16x32_bf16 v[158:161], v[54:57], v[190:193], v[158:161]
global_load_lds_dwordx4 v6, s[40:41] offset:2048
ds_read_b64_tr_b16 a[160:161], v255 offset:192
v_mfma_f32_16x16x32_bf16 v[86:89], a[132:135], v[106:109], v[86:89]
ds_read_b64_tr_b16 a[162:163], v255 offset:2496
v_mfma_f32_16x16x32_bf16 v[90:93], a[136:139], v[106:109], v[90:93]
global_load_lds_dwordx4 v7, s[40:41] offset:3072
ds_read_b64_tr_b16 a[164:165], v255 offset:384
v_mfma_f32_16x16x32_bf16 v[94:97], v[106:109], a[140:143], v[94:97]
s_add_u32 m0, s48, 86016
ds_read_b64_tr_b16 a[166:167], v255 offset:2688
v_mfma_f32_16x16x32_bf16 v[98:101], v[58:61], v[106:109], v[98:101]
ds_read_b64_tr_b16 v[66:67], v163 offset:0
v_mfma_f32_16x16x32_bf16 v[110:113], a[132:135], v[186:189], v[110:113]
global_load_lds_dwordx4 v8, s[40:41] offset:-4096
ds_read_b64_tr_b16 v[68:69], v163 offset:2304
v_mfma_f32_16x16x32_bf16 v[114:117], a[136:139], v[186:189], v[114:117]
ds_read_b128 v[170:173], v52 offset:12288
v_mfma_f32_16x16x32_bf16 v[118:121], v[186:189], a[140:143], v[118:121]
s_sub_u32 s40, s40, 1769472
ds_read_b128 v[178:181], v52 offset:30720
v_mfma_f32_16x16x32_bf16 v[134:137], v[58:61], v[186:189], v[134:137]
v_mfma_f32_16x16x32_bf16 v[122:125], a[132:135], v[194:197], v[122:125]
s_subb_u32 s41, s41, 0
v_mfma_f32_16x16x32_bf16 v[126:129], a[136:139], v[194:197], v[126:129]
v_mfma_f32_16x16x32_bf16 v[130:133], v[194:197], a[140:143], v[130:133]
v_mfma_f32_16x16x32_bf16 v[158:161], v[58:61], v[194:197], v[158:161]
s_waitcnt vmcnt(9) lgkmcnt(0)
s_barrier
s_add_u32 m0, s49, 0
ds_read_b64_tr_b16 a[168:169], v255 offset:18432
v_mfma_f32_16x16x32_bf16 v[86:89], a[144:147], v[106:109], v[86:89]
ds_read_b64_tr_b16 a[170:171], v255 offset:20736
v_mfma_f32_16x16x32_bf16 v[90:93], a[148:151], v[106:109], v[90:93]
ds_read_b64_tr_b16 a[172:173], v255 offset:18624
v_mfma_f32_16x16x32_bf16 v[94:97], v[106:109], a[152:155], v[94:97]
ds_read_b64_tr_b16 a[174:175], v255 offset:20928
global_load_lds_dwordx4 v11, s[42:43] offset:-2048 sc1
v_mfma_f32_16x16x32_bf16 v[98:101], v[62:65], v[106:109], v[98:101]
ds_read_b64_tr_b16 a[176:177], v255 offset:18816
v_mfma_f32_16x16x32_bf16 v[110:113], a[144:147], v[166:169], v[110:113]
ds_read_b64_tr_b16 a[178:179], v255 offset:21120
v_mfma_f32_16x16x32_bf16 v[114:117], a[148:151], v[166:169], v[114:117]
ds_read_b64_tr_b16 v[70:71], v163 offset:18432
global_load_lds_dwordx4 v12, s[42:43] offset:-1024 sc1
v_mfma_f32_16x16x32_bf16 v[118:121], v[166:169], a[152:155], v[118:121]
ds_read_b64_tr_b16 v[72:73], v163 offset:20736
v_mfma_f32_16x16x32_bf16 v[134:137], v[62:65], v[166:169], v[134:137]
ds_read_b128 v[182:185], v51 offset:14336
v_mfma_f32_16x16x32_bf16 v[122:125], a[144:147], v[174:177], v[122:125]
ds_read_b128 v[190:193], v51 offset:32768
global_load_lds_dwordx4 v13, s[42:43] offset:0 sc1
v_mfma_f32_16x16x32_bf16 v[126:129], a[148:151], v[174:177], v[126:129]
ds_read_b64_tr_b16 a[180:181], v255 offset:36864
v_mfma_f32_16x16x32_bf16 v[130:133], v[174:177], a[152:155], v[130:133]
ds_read_b64_tr_b16 a[182:183], v255 offset:39168
v_mfma_f32_16x16x32_bf16 v[158:161], v[62:65], v[174:177], v[158:161]
ds_read_b64_tr_b16 a[184:185], v255 offset:37056
v_mfma_f32_16x16x32_bf16 v[86:89], a[156:159], v[106:109], v[86:89]
global_load_lds_dwordx4 v14, s[42:43] offset:1024 sc1
ds_read_b64_tr_b16 a[186:187], v255 offset:39360
v_mfma_f32_16x16x32_bf16 v[90:93], a[160:163], v[106:109], v[90:93]
ds_read_b64_tr_b16 a[188:189], v255 offset:37248
v_mfma_f32_16x16x32_bf16 v[94:97], v[106:109], a[164:167], v[94:97]
ds_read_b64_tr_b16 a[190:191], v255 offset:39552
v_mfma_f32_16x16x32_bf16 v[98:101], v[66:69], v[106:109], v[98:101]
global_load_lds_dwordx4 v15, s[42:43] offset:2048 sc1
ds_read_b64_tr_b16 v[74:75], v163 offset:36864
v_mfma_f32_16x16x32_bf16 v[110:113], a[156:159], v[170:173], v[110:113]
ds_read_b64_tr_b16 v[76:77], v163 offset:39168
v_mfma_f32_16x16x32_bf16 v[114:117], a[160:163], v[170:173], v[114:117]
ds_read_b128 v[186:189], v52 offset:14336
v_mfma_f32_16x16x32_bf16 v[118:121], v[170:173], a[164:167], v[118:121]
s_sub_u32 s42, s42, 18432
s_subb_u32 s43, s43, 0
ds_read_b128 v[194:197], v52 offset:32768
v_mfma_f32_16x16x32_bf16 v[134:137], v[66:69], v[170:173], v[134:137]
v_mfma_f32_16x16x32_bf16 v[122:125], a[156:159], v[178:181], v[122:125]
v_mfma_f32_16x16x32_bf16 v[126:129], a[160:163], v[178:181], v[126:129]
v_mfma_f32_16x16x32_bf16 v[130:133], v[178:181], a[164:167], v[130:133]
v_mfma_f32_16x16x32_bf16 v[158:161], v[66:69], v[178:181], v[158:161]
s_waitcnt vmcnt(5) lgkmcnt(0)
s_barrier
s_add_u32 m0, s49, 18432
ds_read_b64_tr_b16 a[192:193], v255 offset:55296
v_mfma_f32_16x16x32_bf16 v[86:89], a[168:171], v[106:109], v[86:89]
ds_read_b64_tr_b16 a[194:195], v255 offset:57600
v_mfma_f32_16x16x32_bf16 v[90:93], a[172:175], v[106:109], v[90:93]
ds_read_b64_tr_b16 a[196:197], v255 offset:55488
v_mfma_f32_16x16x32_bf16 v[94:97], v[106:109], a[176:179], v[94:97]
ds_read_b64_tr_b16 a[198:199], v255 offset:57792
global_load_lds_dwordx4 v11, s[42:43] offset:-2048 sc1
v_mfma_f32_16x16x32_bf16 v[98:101], v[70:73], v[106:109], v[98:101]
ds_read_b64_tr_b16 a[200:201], v255 offset:55680
v_mfma_f32_16x16x32_bf16 v[110:113], a[168:171], v[182:185], v[110:113]
ds_read_b64_tr_b16 a[202:203], v255 offset:57984
v_mfma_f32_16x16x32_bf16 v[114:117], a[172:175], v[182:185], v[114:117]
ds_read_b64_tr_b16 v[78:79], v163 offset:55296
global_load_lds_dwordx4 v12, s[42:43] offset:-1024 sc1
v_mfma_f32_16x16x32_bf16 v[118:121], v[182:185], a[176:179], v[118:121]
ds_read_b64_tr_b16 v[80:81], v163 offset:57600
v_mfma_f32_16x16x32_bf16 v[134:137], v[70:73], v[182:185], v[134:137]
ds_read_b128 v[166:169], v51 offset:16384
v_mfma_f32_16x16x32_bf16 v[122:125], a[168:171], v[190:193], v[122:125]
ds_read_b128 v[174:177], v51 offset:34816
global_load_lds_dwordx4 v13, s[42:43] offset:0 sc1
v_mfma_f32_16x16x32_bf16 v[126:129], a[172:175], v[190:193], v[126:129]
ds_read_b64_tr_b16 a[204:205], v162 offset:8192
v_mfma_f32_16x16x32_bf16 v[130:133], v[190:193], a[176:179], v[130:133]
ds_read_b64_tr_b16 a[206:207], v162 offset:10496
v_mfma_f32_16x16x32_bf16 v[158:161], v[70:73], v[190:193], v[158:161]
ds_read_b64_tr_b16 a[208:209], v162 offset:8384
global_load_lds_dwordx4 v14, s[42:43] offset:1024 sc1
v_mfma_f32_16x16x32_bf16 v[86:89], a[180:183], v[106:109], v[86:89]
ds_read_b64_tr_b16 a[210:211], v162 offset:10688
v_mfma_f32_16x16x32_bf16 v[90:93], a[184:187], v[106:109], v[90:93]
ds_read_b64_tr_b16 a[212:213], v162 offset:8576
v_mfma_f32_16x16x32_bf16 v[94:97], v[106:109], a[188:191], v[94:97]
ds_read_b64_tr_b16 a[214:215], v162 offset:10880
global_load_lds_dwordx4 v15, s[42:43] offset:2048 sc1
v_mfma_f32_16x16x32_bf16 v[98:101], v[74:77], v[106:109], v[98:101]
ds_read_b64_tr_b16 v[82:83], v164 offset:8192
v_mfma_f32_16x16x32_bf16 v[110:113], a[180:183], v[186:189], v[110:113]
ds_read_b64_tr_b16 v[84:85], v164 offset:10496
v_mfma_f32_16x16x32_bf16 v[114:117], a[184:187], v[186:189], v[114:117]
ds_read_b128 v[170:173], v52 offset:16384
s_sub_u32 s42, s42, 18432
s_subb_u32 s43, s43, 0
v_mfma_f32_16x16x32_bf16 v[118:121], v[186:189], a[188:191], v[118:121]
ds_read_b128 v[178:181], v52 offset:34816
v_mfma_f32_16x16x32_bf16 v[134:137], v[74:77], v[186:189], v[134:137]
v_mfma_f32_16x16x32_bf16 v[122:125], a[180:183], v[194:197], v[122:125]
v_mfma_f32_16x16x32_bf16 v[126:129], a[184:187], v[194:197], v[126:129]
v_mfma_f32_16x16x32_bf16 v[158:161], v[74:77], v[194:197], v[158:161]
s_waitcnt lgkmcnt(15)
v_mfma_f32_16x16x32_bf16 v[86:89], a[192:195], v[106:109], v[86:89]
v_mfma_f32_16x16x32_bf16 v[90:93], a[196:199], v[106:109], v[90:93]
s_waitcnt lgkmcnt(14)
v_mfma_f32_16x16x32_bf16 v[94:97], v[106:109], a[200:203], v[94:97]
s_waitcnt lgkmcnt(12)
v_mfma_f32_16x16x32_bf16 v[98:101], v[78:81], v[106:109], v[98:101]
s_waitcnt lgkmcnt(11)
v_mfma_f32_16x16x32_bf16 v[110:113], a[192:195], v[166:169], v[110:113]
v_mfma_f32_16x16x32_bf16 v[114:117], a[196:199], v[166:169], v[114:117]
v_mfma_f32_16x16x32_bf16 v[118:121], v[166:169], a[200:203], v[118:121]
v_mfma_f32_16x16x32_bf16 v[134:137], v[78:81], v[166:169], v[134:137]
s_waitcnt lgkmcnt(10)
v_mfma_f32_16x16x32_bf16 v[158:161], v[78:81], v[174:177], v[158:161]
s_waitcnt lgkmcnt(8)
v_mfma_f32_16x16x32_bf16 v[86:89], a[204:207], v[106:109], v[86:89]
s_waitcnt lgkmcnt(6)
v_mfma_f32_16x16x32_bf16 v[90:93], a[208:211], v[106:109], v[90:93]
s_waitcnt lgkmcnt(4)
v_mfma_f32_16x16x32_bf16 v[94:97], v[106:109], a[212:215], v[94:97]
s_waitcnt lgkmcnt(2)
v_mfma_f32_16x16x32_bf16 v[98:101], v[82:85], v[106:109], v[98:101]
s_waitcnt lgkmcnt(1)
v_mfma_f32_16x16x32_bf16 v[110:113], a[204:207], v[170:173], v[110:113]
v_mfma_f32_16x16x32_bf16 v[114:117], a[208:211], v[170:173], v[114:117]
v_mfma_f32_16x16x32_bf16 v[118:121], v[170:173], a[212:215], v[118:121]
v_mfma_f32_16x16x32_bf16 v[134:137], v[82:85], v[170:173], v[134:137]
s_waitcnt lgkmcnt(0)
v_mfma_f32_16x16x32_bf16 v[158:161], v[82:85], v[178:181], v[158:161]
s_barrier
ds_read_b32 v242, v24 offset:0
s_waitcnt lgkmcnt(0)
s_nop 2
v_fma_f32 v230, v98, v242, v134
v_fma_f32 v231, v99, v242, v135
v_fma_f32 v232, v100, v242, v136
v_fma_f32 v233, v101, v242, v137
v_cvt_pk_bf16_f32 v230, v230, v231
v_cvt_pk_bf16_f32 v231, v232, v233
ds_write_b64 v22, v[230:231] offset:40960
ds_read_b128 v[202:205], v52 offset:32768
ds_read_b128 v[206:209], v51 offset:34816
ds_read_b128 v[210:213], v52 offset:34816
v_mov_b32 v234, v32
v_mov_b32 v235, v33
v_mov_b32 v236, v34
v_mov_b32 v237, v35
v_mov_b32 v134, v158
v_mov_b32 v135, v159
v_mov_b32 v136, v160
v_mov_b32 v137, v161
s_waitcnt lgkmcnt(3)
s_barrier
ds_read_b32 v23, v24 offset:0
ds_read_b32 v148, v21
ds_read_b32 v26, v24 offset:192
ds_read_b32 v53, v27
ds_read_b32 v8, v28
ds_read_b128 v[226:229], v25 offset:0
ds_read_b64 v[158:159], v22 offset:41472
ds_read_b32 v140, v20
v_xor_b32 v51, 0x10000, v51
v_xor_b32 v52, 0x10000, v52
s_waitcnt lgkmcnt(7)
v_fma_f32 v238, v90, v23, v114
v_fma_f32 v239, v91, v23, v115
v_fma_f32 v240, v92, v23, v116
v_fma_f32 v241, v93, v23, v117
v_cvt_pk_bf16_f32 v146, v238, v239
v_cvt_pk_bf16_f32 v147, v240, v241
s_waitcnt lgkmcnt(0)
s_barrier
ds_read_b32 v242, v24 offset:64
v_fma_f32 v238, v94, v226, v118
v_fma_f32 v239, v94, v227, v119
v_fma_f32 v240, v94, v228, v120
v_fma_f32 v241, v94, v229, v121
v_cvt_pk_bf16_f32 v154, v238, v239
v_cvt_pk_bf16_f32 v155, v240, v241
v_fma_f32 v238, v86, v23, v110
v_fma_f32 v239, v87, v23, v111
v_fma_f32 v240, v88, v23, v112
v_fma_f32 v241, v89, v23, v113
v_cvt_pk_bf16_f32 v138, v238, v239
v_cvt_pk_bf16_f32 v139, v240, v241
s_waitcnt lgkmcnt(0)
v_fma_f32 v230, v98, v242, v134
v_fma_f32 v231, v99, v242, v135
v_fma_f32 v232, v100, v242, v136
v_fma_f32 v233, v101, v242, v137
v_cvt_pk_bf16_f32 v230, v230, v231
v_cvt_pk_bf16_f32 v231, v232, v233
ds_write_b64 v22, v[230:231] offset:40960
s_waitcnt vmcnt(5) lgkmcnt(0)
s_barrier
s_mov_b32 s70, 32
s_waitcnt lgkmcnt(0)
s_nop 1
s_branch .Lqa_in5
.p2align 6
.Lqa_top24:
s_waitcnt lgkmcnt(9)
v_mfma_f32_16x16x32_bf16 v[134:137], a[216:219], v[44:47], 0
s_waitcnt lgkmcnt(8)
ds_read_b128 v[162:165], v52 offset:4096
ds_read_b32 v8, v30
v_mfma_f32_16x16x32_bf16 v[134:137], a[220:223], v[102:105], v[134:137]
s_waitcnt lgkmcnt(9)
global_store_dwordx4 v16, v[230:233], s[38:39] sc1
v_mfma_f32_16x16x32_bf16 v[110:113], a[0:3], v[44:47], 0
s_waitcnt lgkmcnt(6)
ds_read_b128 v[166:169], v51 offset:6144
v_add_u32 v16, 18432, v16
v_mfma_f32_16x16x32_bf16 v[134:137], a[224:227], v[106:109], v[134:137]
s_waitcnt lgkmcnt(5)
ds_read_b128 v[170:173], v52 offset:6144
ds_read_b128 v[226:229], v25 offset:64
v_mfma_f32_16x16x32_bf16 v[134:137], a[228:231], v[0:3], v[134:137]
ds_read_b64 v[160:161], v22 offset:41472
ds_read_b32 v144, v20
v_mfma_f32_16x16x32_bf16 v[114:117], a[4:7], v[44:47], 0
s_waitcnt lgkmcnt(7)
ds_read_b128 v[174:177], v51 offset:8192
v_fma_f32 v238, v90, v23, v126
v_mfma_f32_16x16x32_bf16 v[134:137], a[232:235], v[4:7], v[134:137]
s_waitcnt lgkmcnt(7)
ds_read_b128 v[178:181], v52 offset:8192
v_fma_f32 v239, v91, v23, v127
v_mfma_f32_16x16x32_bf16 v[134:137], a[236:239], v[162:165], v[134:137]
v_fma_f32 v240, v92, v23, v128
v_fma_f32 v241, v93, v23, v129
v_mfma_f32_16x16x32_bf16 v[118:121], v[44:47], a[8:11], 0
s_waitcnt lgkmcnt(6)
ds_read_b128 v[182:185], v51 offset:10240
v_cvt_pk_bf16_f32 v150, v238, v239
v_mfma_f32_16x16x32_bf16 v[134:137], a[240:243], v[166:169], v[134:137]
s_waitcnt lgkmcnt(6)
ds_read_b128 v[186:189], v52 offset:10240
v_cvt_pk_bf16_f32 v151, v240, v241
v_mfma_f32_16x16x32_bf16 v[134:137], a[244:247], v[170:173], v[134:137]
s_nop 0
v_mfma_f32_16x16x32_bf16 v[214:217], v[150:153], v[138:141], v[32:35]
v_max_f32 v48, v218, v219
v_max_f32 v49, v220, v221
v_mfma_f32_16x16x32_bf16 v[110:113], a[12:15], v[102:105], v[110:113]
s_waitcnt lgkmcnt(6)
ds_read_b128 v[190:193], v51 offset:12288
v_fma_f32 v238, v94, v226, v130
s_waitcnt lgkmcnt(4)
v_mfma_f32_16x16x32_bf16 v[134:137], a[248:251], v[174:177], v[134:137]
s_xor_b32 s49, s49, 0x10000
ds_read_b128 v[194:197], v52 offset:12288
s_waitcnt lgkmcnt(4)
v_mfma_f32_16x16x32_bf16 v[134:137], a[252:255], v[178:181], v[134:137]
s_add_u32 m0, s49, 0
v_mul_f32 v26, 0x3eae1a93, v26
v_mul_f32 v9, v53, v26
v_mfma_f32_16x16x32_bf16 v[114:117], a[16:19], v[102:105], v[114:117]
s_waitcnt lgkmcnt(3)
ds_read_b128 v[198:201], v51 offset:14336
global_load_lds_dwordx4 v11, s[42:43] offset:-2048 sc1
v_mfma_f32_16x16x32_bf16 v[134:137], v[54:57], v[182:185], v[134:137]
s_waitcnt lgkmcnt(3)
ds_read_b128 v[202:205], v52 offset:14336
v_mul_f32 v10, v8, v26
v_mfma_f32_16x16x32_bf16 v[134:137], v[58:61], v[186:189], v[134:137]
v_max3_f32 v48, v214, v215, v48
v_max3_f32 v49, v216, v217, v49
v_mfma_f32_16x16x32_bf16 v[118:121], v[102:105], a[20:23], v[118:121]
s_waitcnt lgkmcnt(3)
ds_read_b128 v[206:209], v51 offset:16384
v_mul_f32 v48, v48, v9
v_mfma_f32_16x16x32_bf16 v[134:137], v[62:65], v[190:193], v[134:137]
s_waitcnt lgkmcnt(3)
ds_read_b128 v[210:213], v52 offset:16384
v_mul_f32 v49, v49, v10
v_mfma_f32_16x16x32_bf16 v[134:137], v[66:69], v[194:197], v[134:137]
v_max_f32 v50, v48, v49
v_mov_b32 v95, v50
v_mfma_f32_16x16x32_bf16 v[110:113], a[24:27], v[106:109], v[110:113]
s_waitcnt lgkmcnt(3)
v_permlane16_swap_b32 v50, v95
v_mfma_f32_16x16x32_bf16 v[134:137], v[70:73], v[198:201], v[134:137]
s_waitcnt lgkmcnt(2)
v_max_f32 v50, v50, v95
v_mov_b32 v95, v50
v_mfma_f32_16x16x32_bf16 v[134:137], v[74:77], v[202:205], v[134:137]
global_load_lds_dwordx4 v12, s[42:43] offset:-1024 sc1
v_fma_f32 v239, v94, v227, v131
v_mfma_f32_16x16x32_bf16 v[114:117], a[28:31], v[106:109], v[114:117]
s_waitcnt lgkmcnt(1)
v_permlane32_swap_b32 v50, v95
v_mfma_f32_16x16x32_bf16 v[134:137], v[78:81], v[206:209], v[134:137]
s_waitcnt lgkmcnt(0)
v_max_f32 v50, v50, v95
v_sub_f32 v48, v48, v50
v_mfma_f32_16x16x32_bf16 v[134:137], v[82:85], v[210:213], v[134:137]
v_sub_f32 v49, v49, v50
v_fma_f32 v240, v94, v228, v132
v_mfma_f32_16x16x32_bf16 v[118:121], v[106:109], a[32:35], v[118:121]
v_exp_f32 v96, v48
v_mfma_f32_16x16x32_bf16 v[110:113], a[36:39], v[0:3], v[110:113]
v_exp_f32 v97, v49
v_mfma_f32_16x16x32_bf16 v[114:117], a[40:43], v[0:3], v[114:117]
v_add_f32 v50, v96, v97
v_mov_b32 v95, v50
v_mfma_f32_16x16x32_bf16 v[118:121], v[0:3], a[44:47], v[118:121]
v_mul_f32 v96, v96, v53
v_fma_f32 v241, v94, v229, v133
v_mfma_f32_16x16x32_bf16 v[110:113], a[48:51], v[4:7], v[110:113]
global_load_lds_dwordx4 v13, s[42:43] offset:0 sc1
v_cvt_pk_bf16_f32 v156, v238, v239
v_mfma_f32_16x16x32_bf16 v[114:117], a[52:55], v[4:7], v[114:117]
v_permlane16_swap_b32 v50, v95
v_mfma_f32_16x16x32_bf16 v[118:121], v[4:7], a[56:59], v[118:121]
v_add_f32 v50, v50, v95
v_mov_b32 v95, v50
v_mfma_f32_16x16x32_bf16 v[110:113], a[60:63], v[162:165], v[110:113]
v_mul_f32 v97, v97, v8
v_cvt_pk_bf16_f32 v157, v240, v241
v_mfma_f32_16x16x32_bf16 v[114:117], a[64:67], v[162:165], v[114:117]
v_permlane32_swap_b32 v50, v95
v_mfma_f32_16x16x32_bf16 v[118:121], v[162:165], a[68:71], v[118:121]
v_mfma_f32_16x16x32_bf16 v[110:113], a[72:75], v[166:169], v[110:113]
s_barrier
v_mfma_f32_16x16x32_bf16 v[114:117], a[76:79], v[166:169], v[114:117]
ds_read_b32 v242, v24 offset:0
v_add_f32 v50, v50, v95
v_mfma_f32_16x16x32_bf16 v[118:121], v[166:169], a[80:83], v[118:121]
global_load_lds_dwordx4 v14, s[42:43] offset:1024 sc1
v_fma_f32 v238, v86, v23, v122
v_mfma_f32_16x16x32_bf16 v[110:113], a[84:87], v[170:173], v[110:113]
)"
R"(v_rcp_f32 v50, v50
v_mfma_f32_16x16x32_bf16 v[114:117], a[88:91], v[170:173], v[114:117]
v_mul_f32 v96, v96, v50
v_mul_f32 v97, v97, v50
v_mfma_f32_16x16x32_bf16 v[118:121], v[170:173], a[92:95], v[118:121]
v_cvt_pk_bf16_f32 v96, v96, v96
v_cvt_pk_bf16_f32 v97, v97, v97
v_mfma_f32_16x16x32_bf16 v[110:113], a[96:99], v[174:177], v[110:113]
v_and_b32 v224, v96, v40
v_and_b32 v225, v97, v41
v_mfma_f32_16x16x32_bf16 v[114:117], a[100:103], v[174:177], v[114:117]
v_and_b32 v222, v96, v42
v_and_b32 v223, v97, v43
v_mfma_f32_16x16x32_bf16 v[118:121], v[174:177], a[104:107], v[118:121]
s_nop 0
v_mfma_f32_16x16x32_bf16 v[218:221], v[154:157], v[222:225], 0
v_mfma_f32_16x16x32_bf16 v[214:217], v[158:161], v[222:225], 0
s_waitcnt lgkmcnt(0)
v_fma_f32 v230, v98, v242, v134
v_fma_f32 v231, v99, v242, v135
v_mfma_f32_16x16x32_bf16 v[110:113], a[108:111], v[178:181], v[110:113]
v_fma_f32 v232, v100, v242, v136
v_fma_f32 v233, v101, v242, v137
v_mfma_f32_16x16x32_bf16 v[114:117], a[112:115], v[178:181], v[114:117]
global_load_lds_dwordx4 v15, s[42:43] offset:2048 sc1
s_add_u32 s42, s42, s46
s_addc_u32 s43, s43, s47
v_mfma_f32_16x16x32_bf16 v[118:121], v[178:181], a[116:119], v[118:121]
v_cvt_pk_bf16_f32 v230, v230, v231
v_cvt_pk_bf16_f32 v231, v232, v233
v_mfma_f32_16x16x32_bf16 v[110:113], a[120:123], v[182:185], v[110:113]
v_cvt_pk_bf16_f32 v254, v218, v219
v_cvt_pk_bf16_f32 v255, v220, v221
v_mfma_f32_16x16x32_bf16 v[114:117], a[124:127], v[182:185], v[114:117]
ds_write_b64 v18, v[254:255]
v_mfma_f32_16x16x32_bf16 v[118:121], v[182:185], a[128:131], v[118:121]
v_cvt_pk_bf16_f32 v96, v214, v215
v_fma_f32 v239, v87, v23, v123
v_mfma_f32_16x16x32_bf16 v[110:113], a[132:135], v[186:189], v[110:113]
ds_write_b32 v19, v96
v_mfma_f32_16x16x32_bf16 v[114:117], a[136:139], v[186:189], v[114:117]
ds_write_b64 v22, v[230:231] offset:40960
v_mfma_f32_16x16x32_bf16 v[118:121], v[186:189], a[140:143], v[118:121]
v_fma_f32 v240, v88, v23, v124
v_fma_f32 v241, v89, v23, v125
v_mfma_f32_16x16x32_bf16 v[110:113], a[144:147], v[190:193], v[110:113]
v_cvt_pk_bf16_f32 v142, v238, v239
v_cvt_pk_bf16_f32 v143, v240, v241
v_mfma_f32_16x16x32_bf16 v[114:117], a[148:151], v[190:193], v[114:117]
v_mfma_f32_16x16x32_bf16 v[118:121], v[190:193], a[152:155], v[118:121]
v_mfma_f32_16x16x32_bf16 v[110:113], a[156:159], v[194:197], v[110:113]
v_mfma_f32_16x16x32_bf16 v[114:117], a[160:163], v[194:197], v[114:117]
v_mfma_f32_16x16x32_bf16 v[118:121], v[194:197], a[164:167], v[118:121]
v_mfma_f32_16x16x32_bf16 v[110:113], a[168:171], v[198:201], v[110:113]
v_mfma_f32_16x16x32_bf16 v[114:117], a[172:175], v[198:201], v[114:117]
v_mfma_f32_16x16x32_bf16 v[118:121], v[198:201], a[176:179], v[118:121]
v_mfma_f32_16x16x32_bf16 v[110:113], a[180:183], v[202:205], v[110:113]
v_mfma_f32_16x16x32_bf16 v[114:117], a[184:187], v[202:205], v[114:117]
s_waitcnt vmcnt(5) lgkmcnt(0)
s_barrier
v_mfma_f32_16x16x32_bf16 v[118:121], v[202:205], a[188:191], v[118:121]
ds_read_b128 v[44:47], v51 offset:18432
v_mfma_f32_16x16x32_bf16 v[110:113], a[192:195], v[206:209], v[110:113]
ds_read_b128 v[102:105], v52 offset:18432
ds_read_b128 v[230:233], v17 offset:0
v_mfma_f32_16x16x32_bf16 v[114:117], a[196:199], v[206:209], v[114:117]
ds_read_b32 v23, v24 offset:0
ds_read_b32 v148, v21
v_mfma_f32_16x16x32_bf16 v[218:221], v[150:153], v[142:145], v[36:39]
v_mfma_f32_16x16x32_bf16 v[118:121], v[206:209], a[200:203], v[118:121]
ds_read_b128 v[106:109], v51 offset:20480
ds_read_b32 v26, v24 offset:192
v_mfma_f32_16x16x32_bf16 v[110:113], a[204:207], v[210:213], v[110:113]
ds_read_b128 v[0:3], v52 offset:20480
ds_read_b32 v53, v27
v_mfma_f32_16x16x32_bf16 v[114:117], a[208:211], v[210:213], v[114:117]
ds_read_b32 v8, v28
ds_read_b128 v[226:229], v25 offset:0
v_mfma_f32_16x16x32_bf16 v[118:121], v[210:213], a[212:215], v[118:121]
s_waitcnt lgkmcnt(10)
ds_read_b128 v[4:7], v51 offset:22528
ds_read_b64 v[158:159], v22 offset:41472
v_mfma_f32_16x16x32_bf16 v[134:137], a[216:219], v[44:47], 0
s_waitcnt lgkmcnt(11)
ds_read_b128 v[162:165], v52 offset:22528
ds_read_b32 v140, v20
v_mfma_f32_16x16x32_bf16 v[134:137], a[220:223], v[102:105], v[134:137]
s_waitcnt lgkmcnt(12)
global_store_dwordx4 v16, v[230:233], s[38:39] sc1
v_mfma_f32_16x16x32_bf16 v[122:125], a[0:3], v[44:47], 0
s_waitcnt lgkmcnt(9)
ds_read_b128 v[166:169], v51 offset:24576
v_add_u32 v16, 18432, v16
v_mfma_f32_16x16x32_bf16 v[134:137], a[224:227], v[106:109], v[134:137]
s_waitcnt lgkmcnt(8)
ds_read_b128 v[170:173], v52 offset:24576
v_fma_f32 v238, v90, v23, v114
v_mfma_f32_16x16x32_bf16 v[134:137], a[228:231], v[0:3], v[134:137]
v_fma_f32 v239, v91, v23, v115
v_fma_f32 v240, v92, v23, v116
v_mfma_f32_16x16x32_bf16 v[126:129], a[4:7], v[44:47], 0
s_waitcnt lgkmcnt(5)
ds_read_b128 v[174:177], v51 offset:26624
v_fma_f32 v241, v93, v23, v117
v_mfma_f32_16x16x32_bf16 v[134:137], a[232:235], v[4:7], v[134:137]
s_waitcnt lgkmcnt(4)
ds_read_b128 v[178:181], v52 offset:26624
v_cvt_pk_bf16_f32 v146, v238, v239
v_mfma_f32_16x16x32_bf16 v[134:137], a[236:239], v[162:165], v[134:137]
v_cvt_pk_bf16_f32 v147, v240, v241
v_max_f32 v48, v218, v219
v_mfma_f32_16x16x32_bf16 v[130:133], v[44:47], a[8:11], 0
ds_read_b128 v[182:185], v51 offset:28672
v_mfma_f32_16x16x32_bf16 v[214:217], v[146:149], v[142:145], v[234:237]
s_waitcnt lgkmcnt(4)
v_max_f32 v49, v220, v221
v_fma_f32 v238, v94, v226, v118
v_mfma_f32_16x16x32_bf16 v[134:137], a[240:243], v[166:169], v[134:137]
s_waitcnt lgkmcnt(3)
ds_read_b128 v[186:189], v52 offset:28672
v_fma_f32 v239, v94, v227, v119
v_mfma_f32_16x16x32_bf16 v[134:137], a[244:247], v[170:173], v[134:137]
v_mul_f32 v26, 0x3eae1a93, v26
v_mul_f32 v9, v53, v26
v_mfma_f32_16x16x32_bf16 v[122:125], a[12:15], v[102:105], v[122:125]
s_waitcnt lgkmcnt(3)
ds_read_b128 v[190:193], v51 offset:30720
v_mul_f32 v10, v8, v26
v_mfma_f32_16x16x32_bf16 v[134:137], a[248:251], v[174:177], v[134:137]
s_add_u32 m0, s49, 18432
ds_read_b128 v[194:197], v52 offset:30720
s_waitcnt lgkmcnt(4)
v_mfma_f32_16x16x32_bf16 v[134:137], a[252:255], v[178:181], v[134:137]
global_load_lds_dwordx4 v11, s[42:43] offset:-2048 sc1
v_max3_f32 v48, v214, v215, v48
v_mfma_f32_16x16x32_bf16 v[126:129], a[16:19], v[102:105], v[126:129]
s_waitcnt lgkmcnt(3)
ds_read_b128 v[198:201], v51 offset:32768
v_max3_f32 v49, v216, v217, v49
v_mfma_f32_16x16x32_bf16 v[134:137], v[54:57], v[182:185], v[134:137]
s_waitcnt lgkmcnt(3)
ds_read_b128 v[202:205], v52 offset:32768
v_mul_f32 v48, v48, v9
v_mfma_f32_16x16x32_bf16 v[134:137], v[58:61], v[186:189], v[134:137]
v_mul_f32 v49, v49, v10
v_max_f32 v50, v48, v49
v_mfma_f32_16x16x32_bf16 v[130:133], v[102:105], a[20:23], v[130:133]
s_waitcnt lgkmcnt(3)
ds_read_b128 v[206:209], v51 offset:34816
v_mov_b32 v95, v50
v_mfma_f32_16x16x32_bf16 v[134:137], v[62:65], v[190:193], v[134:137]
s_waitcnt lgkmcnt(3)
ds_read_b128 v[210:213], v52 offset:34816
v_fma_f32 v240, v94, v228, v120
v_mfma_f32_16x16x32_bf16 v[134:137], v[66:69], v[194:197], v[134:137]
v_permlane16_swap_b32 v50, v95
v_mfma_f32_16x16x32_bf16 v[122:125], a[24:27], v[106:109], v[122:125]
s_waitcnt lgkmcnt(3)
v_max_f32 v50, v50, v95
v_mov_b32 v95, v50
v_mfma_f32_16x16x32_bf16 v[134:137], v[70:73], v[198:201], v[134:137]
s_waitcnt lgkmcnt(2)
global_load_lds_dwordx4 v12, s[42:43] offset:-1024 sc1
v_fma_f32 v241, v94, v229, v121
v_mfma_f32_16x16x32_bf16 v[134:137], v[74:77], v[202:205], v[134:137]
v_permlane32_swap_b32 v50, v95
v_mfma_f32_16x16x32_bf16 v[126:129], a[28:31], v[106:109], v[126:129]
s_waitcnt lgkmcnt(1)
v_max_f32 v50, v50, v95
v_sub_f32 v48, v48, v50
v_mfma_f32_16x16x32_bf16 v[134:137], v[78:81], v[206:209], v[134:137]
s_waitcnt lgkmcnt(0)
v_sub_f32 v49, v49, v50
v_cvt_pk_bf16_f32 v154, v238, v239
v_mfma_f32_16x16x32_bf16 v[134:137], v[82:85], v[210:213], v[134:137]
v_exp_f32 v96, v48
v_mfma_f32_16x16x32_bf16 v[130:133], v[106:109], a[32:35], v[130:133]
v_exp_f32 v97, v49
v_mfma_f32_16x16x32_bf16 v[122:125], a[36:39], v[0:3], v[122:125]
v_add_f32 v50, v96, v97
v_mov_b32 v95, v50
v_mfma_f32_16x16x32_bf16 v[126:129], a[40:43], v[0:3], v[126:129]
v_mul_f32 v96, v96, v53
v_cvt_pk_bf16_f32 v155, v240, v241
v_mfma_f32_16x16x32_bf16 v[130:133], v[0:3], a[44:47], v[130:133]
global_load_lds_dwordx4 v13, s[42:43] offset:0 sc1
v_fma_f32 v238, v86, v23, v110
v_mfma_f32_16x16x32_bf16 v[122:125], a[48:51], v[4:7], v[122:125]
v_permlane16_swap_b32 v50, v95
v_mfma_f32_16x16x32_bf16 v[126:129], a[52:55], v[4:7], v[126:129]
v_add_f32 v50, v50, v95
v_mov_b32 v95, v50
v_mfma_f32_16x16x32_bf16 v[130:133], v[4:7], a[56:59], v[130:133]
v_mul_f32 v97, v97, v8
v_fma_f32 v239, v87, v23, v111
v_mfma_f32_16x16x32_bf16 v[122:125], a[60:63], v[162:165], v[122:125]
v_permlane32_swap_b32 v50, v95
v_mfma_f32_16x16x32_bf16 v[126:129], a[64:67], v[162:165], v[126:129]
v_add_f32 v50, v50, v95
v_fma_f32 v240, v88, v23, v112
v_mfma_f32_16x16x32_bf16 v[130:133], v[162:165], a[68:71], v[130:133]
v_mfma_f32_16x16x32_bf16 v[122:125], a[72:75], v[166:169], v[122:125]
s_barrier
v_mfma_f32_16x16x32_bf16 v[126:129], a[76:79], v[166:169], v[126:129]
global_load_lds_dwordx4 v14, s[42:43] offset:1024 sc1
ds_read_b32 v242, v24 offset:64
v_mfma_f32_16x16x32_bf16 v[130:133], v[166:169], a[80:83], v[130:133]
v_rcp_f32 v50, v50
v_mfma_f32_16x16x32_bf16 v[122:125], a[84:87], v[170:173], v[122:125]
v_mul_f32 v96, v96, v50
v_mul_f32 v97, v97, v50
v_mfma_f32_16x16x32_bf16 v[126:129], a[88:91], v[170:173], v[126:129]
v_cvt_pk_bf16_f32 v96, v96, v96
v_cvt_pk_bf16_f32 v97, v97, v97
v_mfma_f32_16x16x32_bf16 v[130:133], v[170:173], a[92:95], v[130:133]
v_and_b32 v222, v96, v40
v_and_b32 v223, v97, v41
v_mfma_f32_16x16x32_bf16 v[122:125], a[96:99], v[174:177], v[122:125]
v_and_b32 v224, v96, v42
v_and_b32 v225, v97, v43
v_mfma_f32_16x16x32_bf16 v[126:129], a[100:103], v[174:177], v[126:129]
s_nop 0
v_mfma_f32_16x16x32_bf16 v[218:221], v[154:157], v[222:225], 0
v_mfma_f32_16x16x32_bf16 v[214:217], v[158:161], v[222:225], 0
s_waitcnt lgkmcnt(0)
v_fma_f32 v230, v98, v242, v134
v_fma_f32 v231, v99, v242, v135
v_mfma_f32_16x16x32_bf16 v[130:133], v[174:177], a[104:107], v[130:133]
v_fma_f32 v232, v100, v242, v136
v_fma_f32 v233, v101, v242, v137
v_mfma_f32_16x16x32_bf16 v[122:125], a[108:111], v[178:181], v[122:125]
global_load_lds_dwordx4 v15, s[42:43] offset:2048 sc1
s_sub_u32 s42, s42, 18432
s_subb_u32 s43, s43, 0
v_mfma_f32_16x16x32_bf16 v[126:129], a[112:115], v[178:181], v[126:129]
v_cvt_pk_bf16_f32 v230, v230, v231
v_cvt_pk_bf16_f32 v231, v232, v233
v_mfma_f32_16x16x32_bf16 v[130:133], v[178:181], a[116:119], v[130:133]
v_cvt_pk_bf16_f32 v254, v218, v219
v_cvt_pk_bf16_f32 v255, v220, v221
v_mfma_f32_16x16x32_bf16 v[122:125], a[120:123], v[182:185], v[122:125]
ds_write_b64 v18, v[254:255]
v_mfma_f32_16x16x32_bf16 v[126:129], a[124:127], v[182:185], v[126:129]
v_cvt_pk_bf16_f32 v96, v214, v215
v_fma_f32 v241, v89, v23, v113
v_mfma_f32_16x16x32_bf16 v[130:133], v[182:185], a[128:131], v[130:133]
ds_write_b32 v19, v96
v_mfma_f32_16x16x32_bf16 v[122:125], a[132:135], v[186:189], v[122:125]
ds_write_b64 v22, v[230:231] offset:40960
v_mfma_f32_16x16x32_bf16 v[126:129], a[136:139], v[186:189], v[126:129]
v_cvt_pk_bf16_f32 v138, v238, v239
v_cvt_pk_bf16_f32 v139, v240, v241
v_mfma_f32_16x16x32_bf16 v[130:133], v[186:189], a[140:143], v[130:133]
v_mfma_f32_16x16x32_bf16 v[122:125], a[144:147], v[190:193], v[122:125]
v_mfma_f32_16x16x32_bf16 v[126:129], a[148:151], v[190:193], v[126:129]
v_mfma_f32_16x16x32_bf16 v[130:133], v[190:193], a[152:155], v[130:133]
v_mfma_f32_16x16x32_bf16 v[122:125], a[156:159], v[194:197], v[122:125]
v_mfma_f32_16x16x32_bf16 v[126:129], a[160:163], v[194:197], v[126:129]
v_mfma_f32_16x16x32_bf16 v[130:133], v[194:197], a[164:167], v[130:133]
v_mfma_f32_16x16x32_bf16 v[122:125], a[168:171], v[198:201], v[122:125]
v_mfma_f32_16x16x32_bf16 v[126:129], a[172:175], v[198:201], v[126:129]
v_xor_b32 v51, 0x10000, v51
v_xor_b32 v52, 0x10000, v52
v_mfma_f32_16x16x32_bf16 v[130:133], v[198:201], a[176:179], v[130:133]
v_mfma_f32_16x16x32_bf16 v[122:125], a[180:183], v[202:205], v[122:125]
v_mfma_f32_16x16x32_bf16 v[126:129], a[184:187], v[202:205], v[126:129]
s_waitcnt vmcnt(5) lgkmcnt(0)
s_barrier
.Lqa_in5:
v_mfma_f32_16x16x32_bf16 v[130:133], v[202:205], a[188:191], v[130:133]
s_cmp_eq_u32 s70, s73
s_cbranch_scc1 .Lqa_hk6
.Lqa_hr7:
ds_read_b128 v[44:47], v51 offset:0
v_mfma_f32_16x16x32_bf16 v[122:125], a[192:195], v[206:209], v[122:125]
ds_read_b128 v[102:105], v52 offset:0
ds_read_b128 v[230:233], v17 offset:0
v_mfma_f32_16x16x32_bf16 v[126:129], a[196:199], v[206:209], v[126:129]
ds_read_b32 v23, v24 offset:64
ds_read_b32 v152, v21
v_mfma_f32_16x16x32_bf16 v[218:221], v[146:149], v[138:141], v[36:39]
v_mfma_f32_16x16x32_bf16 v[130:133], v[206:209], a[200:203], v[130:133]
ds_read_b128 v[106:109], v51 offset:2048
ds_read_b32 v26, v24 offset:128
v_mfma_f32_16x16x32_bf16 v[122:125], a[204:207], v[210:213], v[122:125]
ds_read_b128 v[0:3], v52 offset:2048
ds_read_b32 v53, v29
v_mfma_f32_16x16x32_bf16 v[126:129], a[208:211], v[210:213], v[126:129]
v_mfma_f32_16x16x32_bf16 v[130:133], v[210:213], a[212:215], v[130:133]
s_sub_u32 s70, s70, 1
ds_read_b128 v[4:7], v51 offset:4096
s_cbranch_scc0 .Lqa_top24
s_waitcnt lgkmcnt(0)
ds_read_b32 v8, v30
global_store_dwordx4 v16, v[230:233], s[38:39] sc1
v_add_u32 v16, 18432, v16
ds_read_b128 v[226:229], v25 offset:64
ds_read_b64 v[160:161], v22 offset:41472
ds_read_b32 v144, v20
v_fma_f32 v238, v90, v23, v126
v_fma_f32 v239, v91, v23, v127
v_fma_f32 v240, v92, v23, v128
v_fma_f32 v241, v93, v23, v129
v_cvt_pk_bf16_f32 v150, v238, v239
v_cvt_pk_bf16_f32 v151, v240, v241
s_nop 1
v_mfma_f32_16x16x32_bf16 v[214:217], v[150:153], v[138:141], v[32:35]
s_waitcnt lgkmcnt(2)
v_max_f32 v48, v218, v219
v_max_f32 v49, v220, v221
v_fma_f32 v238, v94, v226, v130
v_mul_f32 v26, 0x3eae1a93, v26
v_mul_f32 v9, v53, v26
v_mul_f32 v10, v8, v26
s_nop 0
v_max3_f32 v48, v214, v215, v48
v_max3_f32 v49, v216, v217, v49
v_mul_f32 v48, v48, v9
v_mul_f32 v49, v49, v10
v_max_f32 v50, v48, v49
v_mov_b32 v95, v50
s_nop 1
v_permlane16_swap_b32 v50, v95
v_max_f32 v50, v50, v95
v_mov_b32 v95, v50
v_fma_f32 v239, v94, v227, v131
s_nop 0
v_permlane32_swap_b32 v50, v95
v_max_f32 v50, v50, v95
v_sub_f32 v48, v48, v50
v_sub_f32 v49, v49, v50
v_fma_f32 v240, v94, v228, v132
v_exp_f32 v96, v48
v_exp_f32 v97, v49
s_nop 0
v_add_f32 v50, v96, v97
v_mov_b32 v95, v50
v_mul_f32 v96, v96, v53
v_fma_f32 v241, v94, v229, v133
v_cvt_pk_bf16_f32 v156, v238, v239
v_permlane16_swap_b32 v50, v95
v_add_f32 v50, v50, v95
v_mov_b32 v95, v50
v_mul_f32 v97, v97, v8
v_cvt_pk_bf16_f32 v157, v240, v241
v_permlane32_swap_b32 v50, v95
s_waitcnt lgkmcnt(0)
s_barrier
v_add_f32 v50, v50, v95
v_fma_f32 v238, v86, v23, v122
v_rcp_f32 v50, v50
s_nop 0
v_mul_f32 v96, v96, v50
v_mul_f32 v97, v97, v50
v_cvt_pk_bf16_f32 v96, v96, v96
v_cvt_pk_bf16_f32 v97, v97, v97
v_and_b32 v224, v96, v40
v_and_b32 v225, v97, v41
v_and_b32 v222, v96, v42
v_and_b32 v223, v97, v43
s_nop 1
v_mfma_f32_16x16x32_bf16 v[218:221], v[154:157], v[222:225], 0
v_mfma_f32_16x16x32_bf16 v[214:217], v[158:161], v[222:225], 0
s_nop 6
v_cvt_pk_bf16_f32 v254, v218, v219
v_cvt_pk_bf16_f32 v255, v220, v221
ds_write_b64 v18, v[254:255]
v_cvt_pk_bf16_f32 v96, v214, v215
v_fma_f32 v239, v87, v23, v123
ds_write_b32 v19, v96
v_fma_f32 v240, v88, v23, v124
v_fma_f32 v241, v89, v23, v125
v_cvt_pk_bf16_f32 v142, v238, v239
v_cvt_pk_bf16_f32 v143, v240, v241
s_waitcnt vmcnt(0)
s_waitcnt lgkmcnt(0)
s_barrier
ds_read_b128 v[230:233], v17 offset:0
s_waitcnt lgkmcnt(0)
global_store_dwordx4 v16, v[230:233], s[38:39] sc1
s_waitcnt vmcnt(0)
s_barrier
s_endpgm
.Lqa_w2:
s_getpc_b64 s[90:91]
.Lqa_pc44:
s_add_u32 s90, s90, .Lqa_e29-.Lqa_pc44
s_addc_u32 s91, s91, 0
s_branch .Lqa_k36
.Lqa_bk43:
v_add_u32 v255, 0x10000, v9
v_add_u32 v162, 0x20000, v9
v_add_u32 v163, 0x10000, v10
v_add_u32 v164, 0x20000, v10
v_mov_b32 v106, 0x3F803F80
v_mov_b32 v107, 0x3F803F80
v_mov_b32 v108, 0x3F803F80
v_mov_b32 v109, 0x3F803F80
v_mov_b32 v141, 0
v_mov_b32 v145, 0
)"
R"(v_mov_b32 v149, 0
v_mov_b32 v153, 0
v_mov_b32 v165, 46080
s_waitcnt lgkmcnt(0)
s_mul_i32 s37, s37, 0x9E3779B1
s_add_u32 s37, s37, 0x7F4A7C15
v_add_u32 v46, s37, v46
v_add_u32 v48, s37, v48
v_add_u32 v50, s37, v50
ds_write_b32 v165, v141
s_mov_b64 s[76:77], 0
s_mov_b32 s73, 18
s_add_u32 m0, s48, 4096
s_nop 0
global_load_lds_dwordx4 v0, s[40:41] offset:-4096
global_load_lds_dwordx4 v1, s[40:41] offset:-3072
global_load_lds_dwordx4 v2, s[40:41] offset:-2048
global_load_lds_dwordx4 v3, s[40:41] offset:-1024
global_load_lds_dwordx4 v4, s[40:41] offset:0
global_load_lds_dwordx4 v5, s[40:41] offset:1024
global_load_lds_dwordx4 v6, s[40:41] offset:2048
global_load_lds_dwordx4 v7, s[40:41] offset:3072
s_add_u32 m0, s48, 12288
s_nop 0
global_load_lds_dwordx4 v8, s[40:41] offset:-4096
s_add_u32 s40, s40, 221184
s_addc_u32 s41, s41, 0
s_add_u32 m0, s48, 40960
s_nop 0
global_load_lds_dwordx4 v0, s[40:41] offset:-4096
global_load_lds_dwordx4 v1, s[40:41] offset:-3072
global_load_lds_dwordx4 v2, s[40:41] offset:-2048
global_load_lds_dwordx4 v3, s[40:41] offset:-1024
global_load_lds_dwordx4 v4, s[40:41] offset:0
global_load_lds_dwordx4 v5, s[40:41] offset:1024
global_load_lds_dwordx4 v6, s[40:41] offset:2048
global_load_lds_dwordx4 v7, s[40:41] offset:3072
s_add_u32 m0, s48, 49152
s_nop 0
global_load_lds_dwordx4 v8, s[40:41] offset:-4096
s_add_u32 s40, s40, 221184
s_addc_u32 s41, s41, 0
s_waitcnt vmcnt(9) lgkmcnt(0)
s_barrier
s_add_u32 m0, s48, 77824
ds_read_b64_tr_b16 a[0:1], v9 offset:47104
ds_read_b64_tr_b16 a[2:3], v9 offset:49408
global_load_lds_dwordx4 v0, s[40:41] offset:-4096
ds_read_b64_tr_b16 a[4:5], v9 offset:47296
ds_read_b64_tr_b16 a[6:7], v9 offset:49600
global_load_lds_dwordx4 v1, s[40:41] offset:-3072
ds_read_b64_tr_b16 a[8:9], v9 offset:47488
global_load_lds_dwordx4 v2, s[40:41] offset:-2048
ds_read_b64_tr_b16 a[10:11], v9 offset:49792
ds_read_b64_tr_b16 a[216:217], v10 offset:47104
global_load_lds_dwordx4 v3, s[40:41] offset:-1024
ds_read_b64_tr_b16 a[218:219], v10 offset:49408
global_load_lds_dwordx4 v4, s[40:41] offset:0
ds_read_b128 v[166:169], v51 offset:0
ds_read_b128 v[174:177], v51 offset:18432
global_load_lds_dwordx4 v5, s[40:41] offset:1024
ds_read_b64_tr_b16 a[12:13], v255 offset:0
global_load_lds_dwordx4 v6, s[40:41] offset:2048
ds_read_b64_tr_b16 a[14:15], v255 offset:2304
ds_read_b64_tr_b16 a[16:17], v255 offset:192
global_load_lds_dwordx4 v7, s[40:41] offset:3072
ds_read_b64_tr_b16 a[18:19], v255 offset:2496
s_add_u32 m0, s48, 86016
ds_read_b64_tr_b16 a[20:21], v255 offset:384
ds_read_b64_tr_b16 a[22:23], v255 offset:2688
global_load_lds_dwordx4 v8, s[40:41] offset:-4096
ds_read_b64_tr_b16 a[220:221], v163 offset:0
s_add_u32 s40, s40, 221184
ds_read_b64_tr_b16 a[222:223], v163 offset:2304
ds_read_b128 v[170:173], v52 offset:0
s_addc_u32 s41, s41, 0
ds_read_b128 v[178:181], v52 offset:18432
s_waitcnt vmcnt(9) lgkmcnt(0)
s_barrier
s_add_u32 m0, s48, 4096
ds_read_b64_tr_b16 a[24:25], v255 offset:18432
v_mfma_f32_16x16x32_bf16 v[86:89], a[0:3], v[106:109], 0
ds_read_b64_tr_b16 a[26:27], v255 offset:20736
v_mfma_f32_16x16x32_bf16 v[90:93], a[4:7], v[106:109], 0
global_load_lds_dwordx4 v0, s[40:41] offset:-4096
ds_read_b64_tr_b16 a[28:29], v255 offset:18624
v_mfma_f32_16x16x32_bf16 v[94:97], v[106:109], a[8:11], 0
ds_read_b64_tr_b16 a[30:31], v255 offset:20928
global_load_lds_dwordx4 v1, s[40:41] offset:-3072
v_mfma_f32_16x16x32_bf16 v[102:105], v[106:109], a[216:219], 0
ds_read_b64_tr_b16 a[32:33], v255 offset:18816
v_mfma_f32_16x16x32_bf16 v[110:113], a[0:3], v[166:169], 0
ds_read_b64_tr_b16 a[34:35], v255 offset:21120
global_load_lds_dwordx4 v2, s[40:41] offset:-2048
v_mfma_f32_16x16x32_bf16 v[114:117], a[4:7], v[166:169], 0
ds_read_b64_tr_b16 a[224:225], v163 offset:18432
v_mfma_f32_16x16x32_bf16 v[118:121], v[166:169], a[8:11], 0
global_load_lds_dwordx4 v3, s[40:41] offset:-1024
ds_read_b64_tr_b16 a[226:227], v163 offset:20736
v_mfma_f32_16x16x32_bf16 v[134:137], v[166:169], a[216:219], 0
ds_read_b128 v[182:185], v51 offset:2048
global_load_lds_dwordx4 v4, s[40:41] offset:0
v_mfma_f32_16x16x32_bf16 v[122:125], a[0:3], v[174:177], 0
ds_read_b128 v[190:193], v51 offset:20480
v_mfma_f32_16x16x32_bf16 v[126:129], a[4:7], v[174:177], 0
ds_read_b64_tr_b16 a[36:37], v255 offset:36864
global_load_lds_dwordx4 v5, s[40:41] offset:1024
v_mfma_f32_16x16x32_bf16 v[130:133], v[174:177], a[8:11], 0
ds_read_b64_tr_b16 a[38:39], v255 offset:39168
v_mfma_f32_16x16x32_bf16 v[158:161], v[174:177], a[216:219], 0
global_load_lds_dwordx4 v6, s[40:41] offset:2048
ds_read_b64_tr_b16 a[40:41], v255 offset:37056
v_mfma_f32_16x16x32_bf16 v[86:89], a[12:15], v[106:109], v[86:89]
ds_read_b64_tr_b16 a[42:43], v255 offset:39360
v_mfma_f32_16x16x32_bf16 v[90:93], a[16:19], v[106:109], v[90:93]
global_load_lds_dwordx4 v7, s[40:41] offset:3072
ds_read_b64_tr_b16 a[44:45], v255 offset:37248
v_mfma_f32_16x16x32_bf16 v[94:97], v[106:109], a[20:23], v[94:97]
s_add_u32 m0, s48, 12288
ds_read_b64_tr_b16 a[46:47], v255 offset:39552
v_mfma_f32_16x16x32_bf16 v[102:105], v[106:109], a[220:223], v[102:105]
ds_read_b64_tr_b16 a[228:229], v163 offset:36864
v_mfma_f32_16x16x32_bf16 v[110:113], a[12:15], v[170:173], v[110:113]
global_load_lds_dwordx4 v8, s[40:41] offset:-4096
ds_read_b64_tr_b16 a[230:231], v163 offset:39168
v_mfma_f32_16x16x32_bf16 v[114:117], a[16:19], v[170:173], v[114:117]
ds_read_b128 v[186:189], v52 offset:2048
v_mfma_f32_16x16x32_bf16 v[118:121], v[170:173], a[20:23], v[118:121]
s_add_u32 s40, s40, 221184
ds_read_b128 v[194:197], v52 offset:20480
v_mfma_f32_16x16x32_bf16 v[134:137], v[170:173], a[220:223], v[134:137]
v_mfma_f32_16x16x32_bf16 v[122:125], a[12:15], v[178:181], v[122:125]
s_addc_u32 s41, s41, 0
v_mfma_f32_16x16x32_bf16 v[126:129], a[16:19], v[178:181], v[126:129]
v_mfma_f32_16x16x32_bf16 v[130:133], v[178:181], a[20:23], v[130:133]
v_mfma_f32_16x16x32_bf16 v[158:161], v[178:181], a[220:223], v[158:161]
s_waitcnt vmcnt(9) lgkmcnt(0)
s_barrier
s_add_u32 m0, s48, 40960
ds_read_b64_tr_b16 a[48:49], v255 offset:55296
v_mfma_f32_16x16x32_bf16 v[86:89], a[24:27], v[106:109], v[86:89]
ds_read_b64_tr_b16 a[50:51], v255 offset:57600
v_mfma_f32_16x16x32_bf16 v[90:93], a[28:31], v[106:109], v[90:93]
global_load_lds_dwordx4 v0, s[40:41] offset:-4096
ds_read_b64_tr_b16 a[52:53], v255 offset:55488
v_mfma_f32_16x16x32_bf16 v[94:97], v[106:109], a[32:35], v[94:97]
ds_read_b64_tr_b16 a[54:55], v255 offset:57792
global_load_lds_dwordx4 v1, s[40:41] offset:-3072
v_mfma_f32_16x16x32_bf16 v[102:105], v[106:109], a[224:227], v[102:105]
ds_read_b64_tr_b16 a[56:57], v255 offset:55680
v_mfma_f32_16x16x32_bf16 v[110:113], a[24:27], v[182:185], v[110:113]
ds_read_b64_tr_b16 a[58:59], v255 offset:57984
global_load_lds_dwordx4 v2, s[40:41] offset:-2048
v_mfma_f32_16x16x32_bf16 v[114:117], a[28:31], v[182:185], v[114:117]
ds_read_b64_tr_b16 a[232:233], v163 offset:55296
v_mfma_f32_16x16x32_bf16 v[118:121], v[182:185], a[32:35], v[118:121]
global_load_lds_dwordx4 v3, s[40:41] offset:-1024
ds_read_b64_tr_b16 a[234:235], v163 offset:57600
v_mfma_f32_16x16x32_bf16 v[134:137], v[182:185], a[224:227], v[134:137]
ds_read_b128 v[166:169], v51 offset:4096
global_load_lds_dwordx4 v4, s[40:41] offset:0
v_mfma_f32_16x16x32_bf16 v[122:125], a[24:27], v[190:193], v[122:125]
ds_read_b128 v[174:177], v51 offset:22528
v_mfma_f32_16x16x32_bf16 v[126:129], a[28:31], v[190:193], v[126:129]
ds_read_b64_tr_b16 a[60:61], v162 offset:8192
global_load_lds_dwordx4 v5, s[40:41] offset:1024
v_mfma_f32_16x16x32_bf16 v[130:133], v[190:193], a[32:35], v[130:133]
ds_read_b64_tr_b16 a[62:63], v162 offset:10496
v_mfma_f32_16x16x32_bf16 v[158:161], v[190:193], a[224:227], v[158:161]
global_load_lds_dwordx4 v6, s[40:41] offset:2048
ds_read_b64_tr_b16 a[64:65], v162 offset:8384
v_mfma_f32_16x16x32_bf16 v[86:89], a[36:39], v[106:109], v[86:89]
ds_read_b64_tr_b16 a[66:67], v162 offset:10688
v_mfma_f32_16x16x32_bf16 v[90:93], a[40:43], v[106:109], v[90:93]
global_load_lds_dwordx4 v7, s[40:41] offset:3072
ds_read_b64_tr_b16 a[68:69], v162 offset:8576
v_mfma_f32_16x16x32_bf16 v[94:97], v[106:109], a[44:47], v[94:97]
s_add_u32 m0, s48, 49152
ds_read_b64_tr_b16 a[70:71], v162 offset:10880
v_mfma_f32_16x16x32_bf16 v[102:105], v[106:109], a[228:231], v[102:105]
ds_read_b64_tr_b16 a[236:237], v164 offset:8192
v_mfma_f32_16x16x32_bf16 v[110:113], a[36:39], v[186:189], v[110:113]
global_load_lds_dwordx4 v8, s[40:41] offset:-4096
ds_read_b64_tr_b16 a[238:239], v164 offset:10496
v_mfma_f32_16x16x32_bf16 v[114:117], a[40:43], v[186:189], v[114:117]
ds_read_b128 v[170:173], v52 offset:4096
v_mfma_f32_16x16x32_bf16 v[118:121], v[186:189], a[44:47], v[118:121]
s_add_u32 s40, s40, 221184
ds_read_b128 v[178:181], v52 offset:22528
v_mfma_f32_16x16x32_bf16 v[134:137], v[186:189], a[228:231], v[134:137]
v_mfma_f32_16x16x32_bf16 v[122:125], a[36:39], v[194:197], v[122:125]
s_addc_u32 s41, s41, 0
v_mfma_f32_16x16x32_bf16 v[126:129], a[40:43], v[194:197], v[126:129]
v_mfma_f32_16x16x32_bf16 v[130:133], v[194:197], a[44:47], v[130:133]
v_mfma_f32_16x16x32_bf16 v[158:161], v[194:197], a[228:231], v[158:161]
s_waitcnt vmcnt(9) lgkmcnt(0)
s_barrier
s_add_u32 m0, s48, 77824
ds_read_b64_tr_b16 a[72:73], v9 offset:47104
v_mfma_f32_16x16x32_bf16 v[86:89], a[48:51], v[106:109], v[86:89]
ds_read_b64_tr_b16 a[74:75], v9 offset:49408
v_mfma_f32_16x16x32_bf16 v[90:93], a[52:55], v[106:109], v[90:93]
global_load_lds_dwordx4 v0, s[40:41] offset:-4096
ds_read_b64_tr_b16 a[76:77], v9 offset:47296
v_mfma_f32_16x16x32_bf16 v[94:97], v[106:109], a[56:59], v[94:97]
ds_read_b64_tr_b16 a[78:79], v9 offset:49600
global_load_lds_dwordx4 v1, s[40:41] offset:-3072
v_mfma_f32_16x16x32_bf16 v[102:105], v[106:109], a[232:235], v[102:105]
ds_read_b64_tr_b16 a[80:81], v9 offset:47488
v_mfma_f32_16x16x32_bf16 v[110:113], a[48:51], v[166:169], v[110:113]
ds_read_b64_tr_b16 a[82:83], v9 offset:49792
global_load_lds_dwordx4 v2, s[40:41] offset:-2048
v_mfma_f32_16x16x32_bf16 v[114:117], a[52:55], v[166:169], v[114:117]
ds_read_b64_tr_b16 a[240:241], v10 offset:47104
v_mfma_f32_16x16x32_bf16 v[118:121], v[166:169], a[56:59], v[118:121]
global_load_lds_dwordx4 v3, s[40:41] offset:-1024
ds_read_b64_tr_b16 a[242:243], v10 offset:49408
v_mfma_f32_16x16x32_bf16 v[134:137], v[166:169], a[232:235], v[134:137]
ds_read_b128 v[182:185], v51 offset:6144
global_load_lds_dwordx4 v4, s[40:41] offset:0
v_mfma_f32_16x16x32_bf16 v[122:125], a[48:51], v[174:177], v[122:125]
ds_read_b128 v[190:193], v51 offset:24576
v_mfma_f32_16x16x32_bf16 v[126:129], a[52:55], v[174:177], v[126:129]
ds_read_b64_tr_b16 a[84:85], v255 offset:0
global_load_lds_dwordx4 v5, s[40:41] offset:1024
v_mfma_f32_16x16x32_bf16 v[130:133], v[174:177], a[56:59], v[130:133]
ds_read_b64_tr_b16 a[86:87], v255 offset:2304
v_mfma_f32_16x16x32_bf16 v[158:161], v[174:177], a[232:235], v[158:161]
global_load_lds_dwordx4 v6, s[40:41] offset:2048
ds_read_b64_tr_b16 a[88:89], v255 offset:192
v_mfma_f32_16x16x32_bf16 v[86:89], a[60:63], v[106:109], v[86:89]
ds_read_b64_tr_b16 a[90:91], v255 offset:2496
v_mfma_f32_16x16x32_bf16 v[90:93], a[64:67], v[106:109], v[90:93]
global_load_lds_dwordx4 v7, s[40:41] offset:3072
ds_read_b64_tr_b16 a[92:93], v255 offset:384
v_mfma_f32_16x16x32_bf16 v[94:97], v[106:109], a[68:71], v[94:97]
s_add_u32 m0, s48, 86016
ds_read_b64_tr_b16 a[94:95], v255 offset:2688
v_mfma_f32_16x16x32_bf16 v[102:105], v[106:109], a[236:239], v[102:105]
ds_read_b64_tr_b16 a[244:245], v163 offset:0
v_mfma_f32_16x16x32_bf16 v[110:113], a[60:63], v[170:173], v[110:113]
global_load_lds_dwordx4 v8, s[40:41] offset:-4096
ds_read_b64_tr_b16 a[246:247], v163 offset:2304
v_mfma_f32_16x16x32_bf16 v[114:117], a[64:67], v[170:173], v[114:117]
ds_read_b128 v[186:189], v52 offset:6144
v_mfma_f32_16x16x32_bf16 v[118:121], v[170:173], a[68:71], v[118:121]
s_add_u32 s40, s40, 221184
ds_read_b128 v[194:197], v52 offset:24576
v_mfma_f32_16x16x32_bf16 v[134:137], v[170:173], a[236:239], v[134:137]
v_mfma_f32_16x16x32_bf16 v[122:125], a[60:63], v[178:181], v[122:125]
s_addc_u32 s41, s41, 0
v_mfma_f32_16x16x32_bf16 v[126:129], a[64:67], v[178:181], v[126:129]
v_mfma_f32_16x16x32_bf16 v[130:133], v[178:181], a[68:71], v[130:133]
v_mfma_f32_16x16x32_bf16 v[158:161], v[178:181], a[236:239], v[158:161]
s_waitcnt vmcnt(9) lgkmcnt(0)
s_barrier
s_add_u32 m0, s48, 4096
ds_read_b64_tr_b16 a[96:97], v255 offset:18432
v_mfma_f32_16x16x32_bf16 v[86:89], a[72:75], v[106:109], v[86:89]
ds_read_b64_tr_b16 a[98:99], v255 offset:20736
v_mfma_f32_16x16x32_bf16 v[90:93], a[76:79], v[106:109], v[90:93]
global_load_lds_dwordx4 v0, s[40:41] offset:-4096
ds_read_b64_tr_b16 a[100:101], v255 offset:18624
v_mfma_f32_16x16x32_bf16 v[94:97], v[106:109], a[80:83], v[94:97]
ds_read_b64_tr_b16 a[102:103], v255 offset:20928
global_load_lds_dwordx4 v1, s[40:41] offset:-3072
v_mfma_f32_16x16x32_bf16 v[102:105], v[106:109], a[240:243], v[102:105]
ds_read_b64_tr_b16 a[104:105], v255 offset:18816
v_mfma_f32_16x16x32_bf16 v[110:113], a[72:75], v[182:185], v[110:113]
ds_read_b64_tr_b16 a[106:107], v255 offset:21120
global_load_lds_dwordx4 v2, s[40:41] offset:-2048
v_mfma_f32_16x16x32_bf16 v[114:117], a[76:79], v[182:185], v[114:117]
ds_read_b64_tr_b16 a[248:249], v163 offset:18432
v_mfma_f32_16x16x32_bf16 v[118:121], v[182:185], a[80:83], v[118:121]
global_load_lds_dwordx4 v3, s[40:41] offset:-1024
ds_read_b64_tr_b16 a[250:251], v163 offset:20736
v_mfma_f32_16x16x32_bf16 v[134:137], v[182:185], a[240:243], v[134:137]
ds_read_b128 v[166:169], v51 offset:8192
global_load_lds_dwordx4 v4, s[40:41] offset:0
v_mfma_f32_16x16x32_bf16 v[122:125], a[72:75], v[190:193], v[122:125]
ds_read_b128 v[174:177], v51 offset:26624
v_mfma_f32_16x16x32_bf16 v[126:129], a[76:79], v[190:193], v[126:129]
ds_read_b64_tr_b16 a[108:109], v255 offset:36864
global_load_lds_dwordx4 v5, s[40:41] offset:1024
v_mfma_f32_16x16x32_bf16 v[130:133], v[190:193], a[80:83], v[130:133]
ds_read_b64_tr_b16 a[110:111], v255 offset:39168
v_mfma_f32_16x16x32_bf16 v[158:161], v[190:193], a[240:243], v[158:161]
global_load_lds_dwordx4 v6, s[40:41] offset:2048
ds_read_b64_tr_b16 a[112:113], v255 offset:37056
v_mfma_f32_16x16x32_bf16 v[86:89], a[84:87], v[106:109], v[86:89]
ds_read_b64_tr_b16 a[114:115], v255 offset:39360
v_mfma_f32_16x16x32_bf16 v[90:93], a[88:91], v[106:109], v[90:93]
global_load_lds_dwordx4 v7, s[40:41] offset:3072
ds_read_b64_tr_b16 a[116:117], v255 offset:37248
v_mfma_f32_16x16x32_bf16 v[94:97], v[106:109], a[92:95], v[94:97]
s_add_u32 m0, s48, 12288
ds_read_b64_tr_b16 a[118:119], v255 offset:39552
v_mfma_f32_16x16x32_bf16 v[102:105], v[106:109], a[244:247], v[102:105]
ds_read_b64_tr_b16 a[252:253], v163 offset:36864
v_mfma_f32_16x16x32_bf16 v[110:113], a[84:87], v[186:189], v[110:113]
global_load_lds_dwordx4 v8, s[40:41] offset:-4096
ds_read_b64_tr_b16 a[254:255], v163 offset:39168
v_mfma_f32_16x16x32_bf16 v[114:117], a[88:91], v[186:189], v[114:117]
ds_read_b128 v[170:173], v52 offset:8192
v_mfma_f32_16x16x32_bf16 v[118:121], v[186:189], a[92:95], v[118:121]
s_add_u32 s40, s40, 221184
ds_read_b128 v[178:181], v52 offset:26624
v_mfma_f32_16x16x32_bf16 v[134:137], v[186:189], a[244:247], v[134:137]
v_mfma_f32_16x16x32_bf16 v[122:125], a[84:87], v[194:197], v[122:125]
s_addc_u32 s41, s41, 0
v_mfma_f32_16x16x32_bf16 v[126:129], a[88:91], v[194:197], v[126:129]
v_mfma_f32_16x16x32_bf16 v[130:133], v[194:197], a[92:95], v[130:133]
v_mfma_f32_16x16x32_bf16 v[158:161], v[194:197], a[244:247], v[158:161]
s_waitcnt vmcnt(9) lgkmcnt(0)
s_barrier
s_add_u32 m0, s48, 40960
ds_read_b64_tr_b16 a[120:121], v255 offset:55296
v_mfma_f32_16x16x32_bf16 v[86:89], a[96:99], v[106:109], v[86:89]
ds_read_b64_tr_b16 a[122:123], v255 offset:57600
v_mfma_f32_16x16x32_bf16 v[90:93], a[100:103], v[106:109], v[90:93]
global_load_lds_dwordx4 v0, s[40:41] offset:-4096
ds_read_b64_tr_b16 a[124:125], v255 offset:55488
v_mfma_f32_16x16x32_bf16 v[94:97], v[106:109], a[104:107], v[94:97]
ds_read_b64_tr_b16 a[126:127], v255 offset:57792
global_load_lds_dwordx4 v1, s[40:41] offset:-3072
v_mfma_f32_16x16x32_bf16 v[102:105], v[106:109], a[248:251], v[102:105]
ds_read_b64_tr_b16 a[128:129], v255 offset:55680
v_mfma_f32_16x16x32_bf16 v[110:113], a[96:99], v[166:169], v[110:113]
ds_read_b64_tr_b16 a[130:131], v255 offset:57984
global_load_lds_dwordx4 v2, s[40:41] offset:-2048
v_mfma_f32_16x16x32_bf16 v[114:117], a[100:103], v[166:169], v[114:117]
ds_read_b64_tr_b16 v[54:55], v163 offset:55296
v_mfma_f32_16x16x32_bf16 v[118:121], v[166:169], a[104:107], v[118:121]
global_load_lds_dwordx4 v3, s[40:41] offset:-1024
ds_read_b64_tr_b16 v[56:57], v163 offset:57600
v_mfma_f32_16x16x32_bf16 v[134:137], v[166:169], a[248:251], v[134:137]
ds_read_b128 v[182:185], v51 offset:10240
global_load_lds_dwordx4 v4, s[40:41] offset:0
v_mfma_f32_16x16x32_bf16 v[122:125], a[96:99], v[174:177], v[122:125]
ds_read_b128 v[190:193], v51 offset:28672
v_mfma_f32_16x16x32_bf16 v[126:129], a[100:103], v[174:177], v[126:129]
ds_read_b64_tr_b16 a[132:133], v162 offset:8192
global_load_lds_dwordx4 v5, s[40:41] offset:1024
v_mfma_f32_16x16x32_bf16 v[130:133], v[174:177], a[104:107], v[130:133]
ds_read_b64_tr_b16 a[134:135], v162 offset:10496
v_mfma_f32_16x16x32_bf16 v[158:161], v[174:177], a[248:251], v[158:161]
global_load_lds_dwordx4 v6, s[40:41] offset:2048
ds_read_b64_tr_b16 a[136:137], v162 offset:8384
v_mfma_f32_16x16x32_bf16 v[86:89], a[108:111], v[106:109], v[86:89]
ds_read_b64_tr_b16 a[138:139], v162 offset:10688
v_mfma_f32_16x16x32_bf16 v[90:93], a[112:115], v[106:109], v[90:93]
global_load_lds_dwordx4 v7, s[40:41] offset:3072
ds_read_b64_tr_b16 a[140:141], v162 offset:8576
v_mfma_f32_16x16x32_bf16 v[94:97], v[106:109], a[116:119], v[94:97]
s_add_u32 m0, s48, 49152
ds_read_b64_tr_b16 a[142:143], v162 offset:10880
v_mfma_f32_16x16x32_bf16 v[102:105], v[106:109], a[252:255], v[102:105]
ds_read_b64_tr_b16 v[58:59], v164 offset:8192
v_mfma_f32_16x16x32_bf16 v[110:113], a[108:111], v[170:173], v[110:113]
global_load_lds_dwordx4 v8, s[40:41] offset:-4096
ds_read_b64_tr_b16 v[60:61], v164 offset:10496
v_mfma_f32_16x16x32_bf16 v[114:117], a[112:115], v[170:173], v[114:117]
ds_read_b128 v[186:189], v52 offset:10240
v_mfma_f32_16x16x32_bf16 v[118:121], v[170:173], a[116:119], v[118:121]
s_add_u32 s40, s40, 221184
ds_read_b128 v[194:197], v52 offset:28672
v_mfma_f32_16x16x32_bf16 v[134:137], v[170:173], a[252:255], v[134:137]
v_mfma_f32_16x16x32_bf16 v[122:125], a[108:111], v[178:181], v[122:125]
s_addc_u32 s41, s41, 0
v_mfma_f32_16x16x32_bf16 v[126:129], a[112:115], v[178:181], v[126:129]
v_mfma_f32_16x16x32_bf16 v[130:133], v[178:181], a[116:119], v[130:133]
v_mfma_f32_16x16x32_bf16 v[158:161], v[178:181], a[252:255], v[158:161]
s_waitcnt vmcnt(9) lgkmcnt(0)
s_barrier
s_add_u32 m0, s48, 77824
ds_read_b64_tr_b16 a[144:145], v9 offset:47104
v_mfma_f32_16x16x32_bf16 v[86:89], a[120:123], v[106:109], v[86:89]
ds_read_b64_tr_b16 a[146:147], v9 offset:49408
v_mfma_f32_16x16x32_bf16 v[90:93], a[124:127], v[106:109], v[90:93]
global_load_lds_dwordx4 v0, s[40:41] offset:-4096
ds_read_b64_tr_b16 a[148:149], v9 offset:47296
v_mfma_f32_16x16x32_bf16 v[94:97], v[106:109], a[128:131], v[94:97]
ds_read_b64_tr_b16 a[150:151], v9 offset:49600
global_load_lds_dwordx4 v1, s[40:41] offset:-3072
v_mfma_f32_16x16x32_bf16 v[102:105], v[106:109], v[54:57], v[102:105]
ds_read_b64_tr_b16 a[152:153], v9 offset:47488
v_mfma_f32_16x16x32_bf16 v[110:113], a[120:123], v[182:185], v[110:113]
ds_read_b64_tr_b16 a[154:155], v9 offset:49792
global_load_lds_dwordx4 v2, s[40:41] offset:-2048
v_mfma_f32_16x16x32_bf16 v[114:117], a[124:127], v[182:185], v[114:117]
ds_read_b64_tr_b16 v[62:63], v10 offset:47104
v_mfma_f32_16x16x32_bf16 v[118:121], v[182:185], a[128:131], v[118:121]
global_load_lds_dwordx4 v3, s[40:41] offset:-1024
ds_read_b64_tr_b16 v[64:65], v10 offset:49408
v_mfma_f32_16x16x32_bf16 v[134:137], v[182:185], v[54:57], v[134:137]
ds_read_b128 v[166:169], v51 offset:12288
global_load_lds_dwordx4 v4, s[40:41] offset:0
v_mfma_f32_16x16x32_bf16 v[122:125], a[120:123], v[190:193], v[122:125]
ds_read_b128 v[174:177], v51 offset:30720
v_mfma_f32_16x16x32_bf16 v[126:129], a[124:127], v[190:193], v[126:129]
)"
R"(ds_read_b64_tr_b16 a[156:157], v255 offset:0
global_load_lds_dwordx4 v5, s[40:41] offset:1024
v_mfma_f32_16x16x32_bf16 v[130:133], v[190:193], a[128:131], v[130:133]
ds_read_b64_tr_b16 a[158:159], v255 offset:2304
v_mfma_f32_16x16x32_bf16 v[158:161], v[190:193], v[54:57], v[158:161]
global_load_lds_dwordx4 v6, s[40:41] offset:2048
ds_read_b64_tr_b16 a[160:161], v255 offset:192
v_mfma_f32_16x16x32_bf16 v[86:89], a[132:135], v[106:109], v[86:89]
ds_read_b64_tr_b16 a[162:163], v255 offset:2496
v_mfma_f32_16x16x32_bf16 v[90:93], a[136:139], v[106:109], v[90:93]
global_load_lds_dwordx4 v7, s[40:41] offset:3072
ds_read_b64_tr_b16 a[164:165], v255 offset:384
v_mfma_f32_16x16x32_bf16 v[94:97], v[106:109], a[140:143], v[94:97]
s_add_u32 m0, s48, 86016
ds_read_b64_tr_b16 a[166:167], v255 offset:2688
v_mfma_f32_16x16x32_bf16 v[102:105], v[106:109], v[58:61], v[102:105]
ds_read_b64_tr_b16 v[66:67], v163 offset:0
v_mfma_f32_16x16x32_bf16 v[110:113], a[132:135], v[186:189], v[110:113]
global_load_lds_dwordx4 v8, s[40:41] offset:-4096
ds_read_b64_tr_b16 v[68:69], v163 offset:2304
v_mfma_f32_16x16x32_bf16 v[114:117], a[136:139], v[186:189], v[114:117]
ds_read_b128 v[170:173], v52 offset:12288
v_mfma_f32_16x16x32_bf16 v[118:121], v[186:189], a[140:143], v[118:121]
s_sub_u32 s40, s40, 1769472
ds_read_b128 v[178:181], v52 offset:30720
v_mfma_f32_16x16x32_bf16 v[134:137], v[186:189], v[58:61], v[134:137]
v_mfma_f32_16x16x32_bf16 v[122:125], a[132:135], v[194:197], v[122:125]
s_subb_u32 s41, s41, 0
v_mfma_f32_16x16x32_bf16 v[126:129], a[136:139], v[194:197], v[126:129]
v_mfma_f32_16x16x32_bf16 v[130:133], v[194:197], a[140:143], v[130:133]
v_mfma_f32_16x16x32_bf16 v[158:161], v[194:197], v[58:61], v[158:161]
s_waitcnt vmcnt(9) lgkmcnt(0)
s_barrier
s_add_u32 m0, s49, 0
ds_read_b64_tr_b16 a[168:169], v255 offset:18432
v_mfma_f32_16x16x32_bf16 v[86:89], a[144:147], v[106:109], v[86:89]
ds_read_b64_tr_b16 a[170:171], v255 offset:20736
v_mfma_f32_16x16x32_bf16 v[90:93], a[148:151], v[106:109], v[90:93]
ds_read_b64_tr_b16 a[172:173], v255 offset:18624
v_mfma_f32_16x16x32_bf16 v[94:97], v[106:109], a[152:155], v[94:97]
ds_read_b64_tr_b16 a[174:175], v255 offset:20928
global_load_lds_dwordx4 v11, s[42:43] offset:-2048 sc1
v_mfma_f32_16x16x32_bf16 v[102:105], v[106:109], v[62:65], v[102:105]
ds_read_b64_tr_b16 a[176:177], v255 offset:18816
v_mfma_f32_16x16x32_bf16 v[110:113], a[144:147], v[166:169], v[110:113]
ds_read_b64_tr_b16 a[178:179], v255 offset:21120
v_mfma_f32_16x16x32_bf16 v[114:117], a[148:151], v[166:169], v[114:117]
ds_read_b64_tr_b16 v[70:71], v163 offset:18432
global_load_lds_dwordx4 v12, s[42:43] offset:-1024 sc1
v_mfma_f32_16x16x32_bf16 v[118:121], v[166:169], a[152:155], v[118:121]
ds_read_b64_tr_b16 v[72:73], v163 offset:20736
v_mfma_f32_16x16x32_bf16 v[134:137], v[166:169], v[62:65], v[134:137]
ds_read_b128 v[182:185], v51 offset:14336
v_mfma_f32_16x16x32_bf16 v[122:125], a[144:147], v[174:177], v[122:125]
ds_read_b128 v[190:193], v51 offset:32768
global_load_lds_dwordx4 v13, s[42:43] offset:0 sc1
v_mfma_f32_16x16x32_bf16 v[126:129], a[148:151], v[174:177], v[126:129]
ds_read_b64_tr_b16 a[180:181], v255 offset:36864
v_mfma_f32_16x16x32_bf16 v[130:133], v[174:177], a[152:155], v[130:133]
ds_read_b64_tr_b16 a[182:183], v255 offset:39168
v_mfma_f32_16x16x32_bf16 v[158:161], v[174:177], v[62:65], v[158:161]
ds_read_b64_tr_b16 a[184:185], v255 offset:37056
v_mfma_f32_16x16x32_bf16 v[86:89], a[156:159], v[106:109], v[86:89]
global_load_lds_dwordx4 v14, s[42:43] offset:1024 sc1
ds_read_b64_tr_b16 a[186:187], v255 offset:39360
v_mfma_f32_16x16x32_bf16 v[90:93], a[160:163], v[106:109], v[90:93]
ds_read_b64_tr_b16 a[188:189], v255 offset:37248
v_mfma_f32_16x16x32_bf16 v[94:97], v[106:109], a[164:167], v[94:97]
ds_read_b64_tr_b16 a[190:191], v255 offset:39552
v_mfma_f32_16x16x32_bf16 v[102:105], v[106:109], v[66:69], v[102:105]
global_load_lds_dwordx4 v15, s[42:43] offset:2048 sc1
ds_read_b64_tr_b16 v[74:75], v163 offset:36864
v_mfma_f32_16x16x32_bf16 v[110:113], a[156:159], v[170:173], v[110:113]
ds_read_b64_tr_b16 v[76:77], v163 offset:39168
v_mfma_f32_16x16x32_bf16 v[114:117], a[160:163], v[170:173], v[114:117]
ds_read_b128 v[186:189], v52 offset:14336
v_mfma_f32_16x16x32_bf16 v[118:121], v[170:173], a[164:167], v[118:121]
s_sub_u32 s42, s42, 18432
s_subb_u32 s43, s43, 0
ds_read_b128 v[194:197], v52 offset:32768
v_mfma_f32_16x16x32_bf16 v[134:137], v[170:173], v[66:69], v[134:137]
v_mfma_f32_16x16x32_bf16 v[122:125], a[156:159], v[178:181], v[122:125]
v_mfma_f32_16x16x32_bf16 v[126:129], a[160:163], v[178:181], v[126:129]
v_mfma_f32_16x16x32_bf16 v[130:133], v[178:181], a[164:167], v[130:133]
v_mfma_f32_16x16x32_bf16 v[158:161], v[178:181], v[66:69], v[158:161]
s_waitcnt vmcnt(5) lgkmcnt(0)
s_barrier
s_add_u32 m0, s49, 18432
ds_read_b64_tr_b16 a[192:193], v255 offset:55296
v_mfma_f32_16x16x32_bf16 v[86:89], a[168:171], v[106:109], v[86:89]
ds_read_b64_tr_b16 a[194:195], v255 offset:57600
v_mfma_f32_16x16x32_bf16 v[90:93], a[172:175], v[106:109], v[90:93]
ds_read_b64_tr_b16 a[196:197], v255 offset:55488
v_mfma_f32_16x16x32_bf16 v[94:97], v[106:109], a[176:179], v[94:97]
ds_read_b64_tr_b16 a[198:199], v255 offset:57792
global_load_lds_dwordx4 v11, s[42:43] offset:-2048 sc1
v_mfma_f32_16x16x32_bf16 v[102:105], v[106:109], v[70:73], v[102:105]
ds_read_b64_tr_b16 a[200:201], v255 offset:55680
v_mfma_f32_16x16x32_bf16 v[110:113], a[168:171], v[182:185], v[110:113]
ds_read_b64_tr_b16 a[202:203], v255 offset:57984
v_mfma_f32_16x16x32_bf16 v[114:117], a[172:175], v[182:185], v[114:117]
ds_read_b64_tr_b16 v[78:79], v163 offset:55296
global_load_lds_dwordx4 v12, s[42:43] offset:-1024 sc1
v_mfma_f32_16x16x32_bf16 v[118:121], v[182:185], a[176:179], v[118:121]
ds_read_b64_tr_b16 v[80:81], v163 offset:57600
v_mfma_f32_16x16x32_bf16 v[134:137], v[182:185], v[70:73], v[134:137]
ds_read_b128 v[166:169], v51 offset:16384
v_mfma_f32_16x16x32_bf16 v[122:125], a[168:171], v[190:193], v[122:125]
ds_read_b128 v[174:177], v51 offset:34816
global_load_lds_dwordx4 v13, s[42:43] offset:0 sc1
v_mfma_f32_16x16x32_bf16 v[126:129], a[172:175], v[190:193], v[126:129]
ds_read_b64_tr_b16 a[204:205], v162 offset:8192
v_mfma_f32_16x16x32_bf16 v[130:133], v[190:193], a[176:179], v[130:133]
ds_read_b64_tr_b16 a[206:207], v162 offset:10496
v_mfma_f32_16x16x32_bf16 v[158:161], v[190:193], v[70:73], v[158:161]
ds_read_b64_tr_b16 a[208:209], v162 offset:8384
global_load_lds_dwordx4 v14, s[42:43] offset:1024 sc1
v_mfma_f32_16x16x32_bf16 v[86:89], a[180:183], v[106:109], v[86:89]
ds_read_b64_tr_b16 a[210:211], v162 offset:10688
v_mfma_f32_16x16x32_bf16 v[90:93], a[184:187], v[106:109], v[90:93]
ds_read_b64_tr_b16 a[212:213], v162 offset:8576
v_mfma_f32_16x16x32_bf16 v[94:97], v[106:109], a[188:191], v[94:97]
ds_read_b64_tr_b16 a[214:215], v162 offset:10880
global_load_lds_dwordx4 v15, s[42:43] offset:2048 sc1
v_mfma_f32_16x16x32_bf16 v[102:105], v[106:109], v[74:77], v[102:105]
ds_read_b64_tr_b16 v[82:83], v164 offset:8192
v_mfma_f32_16x16x32_bf16 v[110:113], a[180:183], v[186:189], v[110:113]
ds_read_b64_tr_b16 v[84:85], v164 offset:10496
v_mfma_f32_16x16x32_bf16 v[114:117], a[184:187], v[186:189], v[114:117]
ds_read_b128 v[170:173], v52 offset:16384
s_sub_u32 s42, s42, 18432
s_subb_u32 s43, s43, 0
v_mfma_f32_16x16x32_bf16 v[118:121], v[186:189], a[188:191], v[118:121]
ds_read_b128 v[178:181], v52 offset:34816
v_mfma_f32_16x16x32_bf16 v[134:137], v[186:189], v[74:77], v[134:137]
v_mfma_f32_16x16x32_bf16 v[122:125], a[180:183], v[194:197], v[122:125]
v_mfma_f32_16x16x32_bf16 v[126:129], a[184:187], v[194:197], v[126:129]
v_mfma_f32_16x16x32_bf16 v[158:161], v[194:197], v[74:77], v[158:161]
s_waitcnt lgkmcnt(15)
v_mfma_f32_16x16x32_bf16 v[86:89], a[192:195], v[106:109], v[86:89]
v_mfma_f32_16x16x32_bf16 v[90:93], a[196:199], v[106:109], v[90:93]
s_waitcnt lgkmcnt(14)
v_mfma_f32_16x16x32_bf16 v[94:97], v[106:109], a[200:203], v[94:97]
s_waitcnt lgkmcnt(12)
v_mfma_f32_16x16x32_bf16 v[102:105], v[106:109], v[78:81], v[102:105]
s_waitcnt lgkmcnt(11)
v_mfma_f32_16x16x32_bf16 v[110:113], a[192:195], v[166:169], v[110:113]
v_mfma_f32_16x16x32_bf16 v[114:117], a[196:199], v[166:169], v[114:117]
v_mfma_f32_16x16x32_bf16 v[118:121], v[166:169], a[200:203], v[118:121]
v_mfma_f32_16x16x32_bf16 v[134:137], v[166:169], v[78:81], v[134:137]
s_waitcnt lgkmcnt(10)
v_mfma_f32_16x16x32_bf16 v[158:161], v[174:177], v[78:81], v[158:161]
s_waitcnt lgkmcnt(8)
v_mfma_f32_16x16x32_bf16 v[86:89], a[204:207], v[106:109], v[86:89]
s_waitcnt lgkmcnt(6)
v_mfma_f32_16x16x32_bf16 v[90:93], a[208:211], v[106:109], v[90:93]
s_waitcnt lgkmcnt(4)
v_mfma_f32_16x16x32_bf16 v[94:97], v[106:109], a[212:215], v[94:97]
s_waitcnt lgkmcnt(2)
v_mfma_f32_16x16x32_bf16 v[102:105], v[106:109], v[82:85], v[102:105]
s_waitcnt lgkmcnt(1)
v_mfma_f32_16x16x32_bf16 v[110:113], a[204:207], v[170:173], v[110:113]
v_mfma_f32_16x16x32_bf16 v[114:117], a[208:211], v[170:173], v[114:117]
v_mfma_f32_16x16x32_bf16 v[118:121], v[170:173], a[212:215], v[118:121]
v_mfma_f32_16x16x32_bf16 v[134:137], v[170:173], v[82:85], v[134:137]
s_waitcnt lgkmcnt(0)
v_mfma_f32_16x16x32_bf16 v[158:161], v[178:181], v[82:85], v[158:161]
s_barrier
ds_read_b128 v[242:245], v25 offset:0
s_waitcnt lgkmcnt(0)
s_nop 2
v_fma_f32 v230, v102, v242, v134
v_fma_f32 v231, v102, v243, v135
v_fma_f32 v232, v102, v244, v136
v_fma_f32 v233, v102, v245, v137
v_cvt_pk_bf16_f32 v230, v230, v231
v_cvt_pk_bf16_f32 v231, v232, v233
ds_write_b64 v22, v[230:231] offset:41472
ds_read_b128 v[202:205], v52 offset:32768
ds_read_b128 v[206:209], v51 offset:34816
ds_read_b128 v[210:213], v52 offset:34816
v_mov_b32 v234, v32
v_mov_b32 v235, v33
v_mov_b32 v236, v34
v_mov_b32 v237, v35
v_mov_b32 v134, v158
v_mov_b32 v135, v159
v_mov_b32 v136, v160
v_mov_b32 v137, v161
s_waitcnt lgkmcnt(3)
s_barrier
ds_read_b32 v23, v24 offset:0
ds_read_b32 v148, v21
ds_read_b32 v26, v24 offset:192
ds_read_b32 v53, v27
ds_read_b32 v95, v28
ds_read_b128 v[226:229], v25 offset:0
ds_read_b64 v[158:159], v22 offset:41472
ds_read_b32 v140, v20
v_xor_b32 v51, 0x10000, v51
v_xor_b32 v52, 0x10000, v52
s_waitcnt lgkmcnt(7)
v_fma_f32 v238, v90, v23, v114
v_fma_f32 v239, v91, v23, v115
v_fma_f32 v240, v92, v23, v116
v_fma_f32 v241, v93, v23, v117
v_cvt_pk_bf16_f32 v146, v238, v239
v_cvt_pk_bf16_f32 v147, v240, v241
s_waitcnt lgkmcnt(0)
s_barrier
ds_read_b128 v[242:245], v25 offset:64
v_fma_f32 v238, v94, v226, v118
v_fma_f32 v239, v94, v227, v119
v_fma_f32 v240, v94, v228, v120
v_fma_f32 v241, v94, v229, v121
v_cvt_pk_bf16_f32 v154, v238, v239
v_cvt_pk_bf16_f32 v155, v240, v241
v_fma_f32 v238, v86, v23, v110
v_fma_f32 v239, v87, v23, v111
v_fma_f32 v240, v88, v23, v112
v_fma_f32 v241, v89, v23, v113
v_cvt_pk_bf16_f32 v138, v238, v239
v_cvt_pk_bf16_f32 v139, v240, v241
s_waitcnt lgkmcnt(0)
v_fma_f32 v230, v102, v242, v134
v_fma_f32 v231, v102, v243, v135
v_fma_f32 v232, v102, v244, v136
v_fma_f32 v233, v102, v245, v137
v_cvt_pk_bf16_f32 v230, v230, v231
v_cvt_pk_bf16_f32 v231, v232, v233
ds_write_b64 v22, v[230:231] offset:41472
s_waitcnt vmcnt(5) lgkmcnt(0)
s_barrier
s_mov_b32 s70, 32
s_waitcnt lgkmcnt(0)
s_nop 1
s_branch .Lqa_in26
.p2align 6
.Lqa_top45:
s_waitcnt lgkmcnt(9)
v_mfma_f32_16x16x32_bf16 v[134:137], v[44:47], a[216:219], 0
s_waitcnt lgkmcnt(8)
ds_read_b128 v[162:165], v52 offset:4096
ds_read_b32 v95, v30
v_mfma_f32_16x16x32_bf16 v[134:137], v[96:99], a[220:223], v[134:137]
ds_read_b128 v[226:229], v25 offset:64
ds_read_b64 v[160:161], v22 offset:41472
v_mfma_f32_16x16x32_bf16 v[110:113], a[0:3], v[44:47], 0
s_waitcnt lgkmcnt(8)
ds_read_b128 v[166:169], v51 offset:6144
ds_read_b32 v144, v20
v_mfma_f32_16x16x32_bf16 v[134:137], v[104:107], a[224:227], v[134:137]
s_waitcnt lgkmcnt(8)
ds_read_b128 v[170:173], v52 offset:6144
v_fma_f32 v238, v90, v23, v126
v_mfma_f32_16x16x32_bf16 v[134:137], v[0:3], a[228:231], v[134:137]
global_store_dwordx4 v16, v[230:233], s[38:39] sc1
v_mfma_f32_16x16x32_bf16 v[114:117], a[4:7], v[44:47], 0
s_waitcnt lgkmcnt(7)
ds_read_b128 v[174:177], v51 offset:8192
v_add_u32 v16, 18432, v16
v_mfma_f32_16x16x32_bf16 v[134:137], v[4:7], a[232:235], v[134:137]
s_waitcnt lgkmcnt(7)
ds_read_b128 v[178:181], v52 offset:8192
v_fma_f32 v239, v91, v23, v127
v_mfma_f32_16x16x32_bf16 v[134:137], v[162:165], a[236:239], v[134:137]
v_fma_f32 v240, v92, v23, v128
v_fma_f32 v241, v93, v23, v129
v_mfma_f32_16x16x32_bf16 v[118:121], v[44:47], a[8:11], 0
s_waitcnt lgkmcnt(4)
ds_read_b128 v[182:185], v51 offset:10240
v_cvt_pk_bf16_f32 v150, v238, v239
v_mfma_f32_16x16x32_bf16 v[134:137], v[166:169], a[240:243], v[134:137]
s_waitcnt lgkmcnt(3)
ds_read_b128 v[186:189], v52 offset:10240
v_cvt_pk_bf16_f32 v151, v240, v241
v_mfma_f32_16x16x32_bf16 v[134:137], v[170:173], a[244:247], v[134:137]
s_nop 0
v_mfma_f32_16x16x32_bf16 v[214:217], v[150:153], v[138:141], v[32:35]
v_max_f32 v109, v218, v219
v_max_f32 v254, v220, v221
v_mfma_f32_16x16x32_bf16 v[110:113], a[12:15], v[96:99], v[110:113]
s_waitcnt lgkmcnt(3)
ds_read_b128 v[190:193], v51 offset:12288
v_fma_f32 v238, v94, v226, v130
v_mfma_f32_16x16x32_bf16 v[134:137], v[174:177], a[248:251], v[134:137]
s_xor_b32 s49, s49, 0x10000
ds_read_b128 v[194:197], v52 offset:12288
s_waitcnt lgkmcnt(4)
v_mfma_f32_16x16x32_bf16 v[134:137], v[178:181], a[252:255], v[134:137]
s_add_u32 m0, s49, 0
v_mul_f32 v26, 0x3eae1a93, v26
v_mul_f32 v103, v53, v26
v_mfma_f32_16x16x32_bf16 v[114:117], a[16:19], v[96:99], v[114:117]
s_waitcnt lgkmcnt(3)
ds_read_b128 v[198:201], v51 offset:14336
v_mul_f32 v108, v95, v26
v_mfma_f32_16x16x32_bf16 v[134:137], v[182:185], v[54:57], v[134:137]
s_waitcnt lgkmcnt(3)
ds_read_b128 v[202:205], v52 offset:14336
v_max3_f32 v109, v214, v215, v109
v_mfma_f32_16x16x32_bf16 v[134:137], v[186:189], v[58:61], v[134:137]
global_load_lds_dwordx4 v11, s[42:43] offset:-2048 sc1
v_max3_f32 v254, v216, v217, v254
v_mfma_f32_16x16x32_bf16 v[118:121], v[96:99], a[20:23], v[118:121]
s_waitcnt lgkmcnt(3)
ds_read_b128 v[206:209], v51 offset:16384
v_mul_f32 v109, v109, v103
v_mfma_f32_16x16x32_bf16 v[134:137], v[190:193], v[62:65], v[134:137]
s_waitcnt lgkmcnt(3)
ds_read_b128 v[210:213], v52 offset:16384
v_mul_f32 v254, v254, v108
v_mfma_f32_16x16x32_bf16 v[134:137], v[194:197], v[66:69], v[134:137]
v_max_f32 v255, v109, v254
v_mov_b32 v8, v255
v_mfma_f32_16x16x32_bf16 v[110:113], a[24:27], v[104:107], v[110:113]
s_waitcnt lgkmcnt(3)
v_permlane16_swap_b32 v255, v8
v_mfma_f32_16x16x32_bf16 v[134:137], v[198:201], v[70:73], v[134:137]
s_waitcnt lgkmcnt(2)
v_max_f32 v255, v255, v8
v_mov_b32 v8, v255
v_mfma_f32_16x16x32_bf16 v[134:137], v[202:205], v[74:77], v[134:137]
s_nop 0
v_permlane32_swap_b32 v255, v8
v_mfma_f32_16x16x32_bf16 v[114:117], a[28:31], v[104:107], v[114:117]
s_waitcnt lgkmcnt(1)
v_max_f32 v255, v255, v8
v_sub_f32 v109, v109, v255
v_mfma_f32_16x16x32_bf16 v[134:137], v[206:209], v[78:81], v[134:137]
s_waitcnt lgkmcnt(0)
global_load_lds_dwordx4 v12, s[42:43] offset:-1024 sc1
v_sub_f32 v254, v254, v255
v_mfma_f32_16x16x32_bf16 v[134:137], v[210:213], v[82:85], v[134:137]
v_exp_f32 v9, v109
v_mfma_f32_16x16x32_bf16 v[118:121], v[104:107], a[32:35], v[118:121]
v_exp_f32 v10, v254
v_mfma_f32_16x16x32_bf16 v[110:113], a[36:39], v[0:3], v[110:113]
v_add_f32 v255, v9, v10
v_mov_b32 v8, v255
v_mfma_f32_16x16x32_bf16 v[114:117], a[40:43], v[0:3], v[114:117]
v_mul_f32 v9, v9, v53
v_fma_f32 v239, v94, v227, v131
v_mfma_f32_16x16x32_bf16 v[118:121], v[0:3], a[44:47], v[118:121]
v_permlane16_swap_b32 v255, v8
v_mfma_f32_16x16x32_bf16 v[110:113], a[48:51], v[4:7], v[110:113]
v_add_f32 v255, v255, v8
v_mov_b32 v8, v255
v_mfma_f32_16x16x32_bf16 v[114:117], a[52:55], v[4:7], v[114:117]
v_mul_f32 v10, v10, v95
v_fma_f32 v240, v94, v228, v132
v_mfma_f32_16x16x32_bf16 v[118:121], v[4:7], a[56:59], v[118:121]
global_load_lds_dwordx4 v13, s[42:43] offset:0 sc1
v_fma_f32 v241, v94, v229, v133
v_mfma_f32_16x16x32_bf16 v[110:113], a[60:63], v[162:165], v[110:113]
v_permlane32_swap_b32 v255, v8
v_mfma_f32_16x16x32_bf16 v[114:117], a[64:67], v[162:165], v[114:117]
v_add_f32 v255, v255, v8
v_cvt_pk_bf16_f32 v156, v238, v239
v_mfma_f32_16x16x32_bf16 v[118:121], v[162:165], a[68:71], v[118:121]
v_mfma_f32_16x16x32_bf16 v[110:113], a[72:75], v[166:169], v[110:113]
s_barrier
v_mfma_f32_16x16x32_bf16 v[114:117], a[76:79], v[166:169], v[114:117]
ds_read_b128 v[242:245], v25 offset:0
v_cvt_pk_bf16_f32 v157, v240, v241
v_mfma_f32_16x16x32_bf16 v[118:121], v[166:169], a[80:83], v[118:121]
v_rcp_f32 v255, v255
v_mfma_f32_16x16x32_bf16 v[110:113], a[84:87], v[170:173], v[110:113]
v_mul_f32 v9, v9, v255
v_mul_f32 v10, v10, v255
v_mfma_f32_16x16x32_bf16 v[114:117], a[88:91], v[170:173], v[114:117]
global_load_lds_dwordx4 v14, s[42:43] offset:1024 sc1
v_cvt_pk_bf16_f32 v9, v9, v9
v_mfma_f32_16x16x32_bf16 v[118:121], v[170:173], a[92:95], v[118:121]
v_cvt_pk_bf16_f32 v10, v10, v10
v_and_b32 v224, v9, v40
v_mfma_f32_16x16x32_bf16 v[110:113], a[96:99], v[174:177], v[110:113]
v_and_b32 v225, v10, v41
v_and_b32 v222, v9, v42
v_mfma_f32_16x16x32_bf16 v[114:117], a[100:103], v[174:177], v[114:117]
s_waitcnt lgkmcnt(0)
v_and_b32 v223, v10, v43
v_fma_f32 v230, v102, v242, v134
v_mfma_f32_16x16x32_bf16 v[118:121], v[174:177], a[104:107], v[118:121]
v_mfma_f32_16x16x32_bf16 v[218:221], v[154:157], v[222:225], 0
v_mfma_f32_16x16x32_bf16 v[214:217], v[158:161], v[222:225], 0
v_fma_f32 v231, v102, v243, v135
v_fma_f32 v232, v102, v244, v136
v_mfma_f32_16x16x32_bf16 v[110:113], a[108:111], v[178:181], v[110:113]
v_fma_f32 v233, v102, v245, v137
v_cvt_pk_bf16_f32 v230, v230, v231
v_mfma_f32_16x16x32_bf16 v[114:117], a[112:115], v[178:181], v[114:117]
v_cvt_pk_bf16_f32 v231, v232, v233
v_fma_f32 v238, v86, v23, v122
v_mfma_f32_16x16x32_bf16 v[118:121], v[178:181], a[116:119], v[118:121]
ds_write_b64 v22, v[230:231] offset:41472
v_mfma_f32_16x16x32_bf16 v[110:113], a[120:123], v[182:185], v[110:113]
global_load_lds_dwordx4 v15, s[42:43] offset:2048 sc1
s_add_u32 s42, s42, s46
s_addc_u32 s43, s43, s47
)"
R"(v_mfma_f32_16x16x32_bf16 v[114:117], a[124:127], v[182:185], v[114:117]
v_cvt_pk_bf16_f32 v100, v218, v219
v_cvt_pk_bf16_f32 v101, v220, v221
v_mfma_f32_16x16x32_bf16 v[118:121], v[182:185], a[128:131], v[118:121]
ds_write_b64 v18, v[100:101]
v_mfma_f32_16x16x32_bf16 v[110:113], a[132:135], v[186:189], v[110:113]
v_cvt_pk_bf16_f32 v9, v216, v217
v_fma_f32 v239, v87, v23, v123
v_mfma_f32_16x16x32_bf16 v[114:117], a[136:139], v[186:189], v[114:117]
ds_write_b32 v19, v9
v_mfma_f32_16x16x32_bf16 v[118:121], v[186:189], a[140:143], v[118:121]
v_fma_f32 v240, v88, v23, v124
v_fma_f32 v241, v89, v23, v125
v_mfma_f32_16x16x32_bf16 v[110:113], a[144:147], v[190:193], v[110:113]
v_cvt_pk_bf16_f32 v142, v238, v239
v_cvt_pk_bf16_f32 v143, v240, v241
v_mfma_f32_16x16x32_bf16 v[114:117], a[148:151], v[190:193], v[114:117]
v_mfma_f32_16x16x32_bf16 v[118:121], v[190:193], a[152:155], v[118:121]
v_mfma_f32_16x16x32_bf16 v[110:113], a[156:159], v[194:197], v[110:113]
v_mfma_f32_16x16x32_bf16 v[114:117], a[160:163], v[194:197], v[114:117]
v_mfma_f32_16x16x32_bf16 v[118:121], v[194:197], a[164:167], v[118:121]
v_mfma_f32_16x16x32_bf16 v[110:113], a[168:171], v[198:201], v[110:113]
v_mfma_f32_16x16x32_bf16 v[114:117], a[172:175], v[198:201], v[114:117]
v_mfma_f32_16x16x32_bf16 v[118:121], v[198:201], a[176:179], v[118:121]
v_mfma_f32_16x16x32_bf16 v[110:113], a[180:183], v[202:205], v[110:113]
v_mfma_f32_16x16x32_bf16 v[114:117], a[184:187], v[202:205], v[114:117]
s_waitcnt vmcnt(5) lgkmcnt(0)
s_barrier
v_mfma_f32_16x16x32_bf16 v[118:121], v[202:205], a[188:191], v[118:121]
ds_read_b128 v[44:47], v51 offset:18432
v_mfma_f32_16x16x32_bf16 v[110:113], a[192:195], v[206:209], v[110:113]
ds_read_b128 v[96:99], v52 offset:18432
ds_read_b128 v[230:233], v17 offset:0
v_mfma_f32_16x16x32_bf16 v[114:117], a[196:199], v[206:209], v[114:117]
ds_read_b32 v23, v24 offset:0
ds_read_b32 v148, v21
v_mfma_f32_16x16x32_bf16 v[218:221], v[150:153], v[142:145], v[36:39]
v_mfma_f32_16x16x32_bf16 v[118:121], v[206:209], a[200:203], v[118:121]
ds_read_b128 v[104:107], v51 offset:20480
ds_read_b32 v26, v24 offset:192
v_mfma_f32_16x16x32_bf16 v[110:113], a[204:207], v[210:213], v[110:113]
ds_read_b128 v[0:3], v52 offset:20480
ds_read_b32 v53, v27
v_mfma_f32_16x16x32_bf16 v[114:117], a[208:211], v[210:213], v[114:117]
ds_read_b32 v95, v28
ds_read_b128 v[226:229], v25 offset:0
v_mfma_f32_16x16x32_bf16 v[118:121], v[210:213], a[212:215], v[118:121]
s_waitcnt lgkmcnt(10)
ds_read_b128 v[4:7], v51 offset:22528
ds_read_b64 v[158:159], v22 offset:41472
v_mfma_f32_16x16x32_bf16 v[134:137], v[44:47], a[216:219], 0
s_waitcnt lgkmcnt(11)
ds_read_b128 v[162:165], v52 offset:22528
ds_read_b32 v140, v20
v_mfma_f32_16x16x32_bf16 v[134:137], v[96:99], a[220:223], v[134:137]
v_max_f32 v109, v218, v219
v_max_f32 v254, v220, v221
v_mfma_f32_16x16x32_bf16 v[122:125], a[0:3], v[44:47], 0
s_waitcnt lgkmcnt(11)
ds_read_b128 v[166:169], v51 offset:24576
v_fma_f32 v238, v90, v23, v114
s_waitcnt lgkmcnt(10)
v_mfma_f32_16x16x32_bf16 v[134:137], v[104:107], a[224:227], v[134:137]
s_waitcnt lgkmcnt(8)
ds_read_b128 v[170:173], v52 offset:24576
v_fma_f32 v239, v91, v23, v115
v_mfma_f32_16x16x32_bf16 v[134:137], v[0:3], a[228:231], v[134:137]
global_store_dwordx4 v16, v[230:233], s[38:39] sc1
v_mfma_f32_16x16x32_bf16 v[126:129], a[4:7], v[44:47], 0
s_waitcnt lgkmcnt(5)
ds_read_b128 v[174:177], v51 offset:26624
v_add_u32 v16, 18432, v16
v_mfma_f32_16x16x32_bf16 v[134:137], v[4:7], a[232:235], v[134:137]
s_waitcnt lgkmcnt(4)
ds_read_b128 v[178:181], v52 offset:26624
v_fma_f32 v240, v92, v23, v116
v_mfma_f32_16x16x32_bf16 v[134:137], v[162:165], a[236:239], v[134:137]
v_fma_f32 v241, v93, v23, v117
v_cvt_pk_bf16_f32 v146, v238, v239
v_mfma_f32_16x16x32_bf16 v[130:133], v[44:47], a[8:11], 0
s_waitcnt lgkmcnt(3)
ds_read_b128 v[182:185], v51 offset:28672
v_cvt_pk_bf16_f32 v147, v240, v241
v_mfma_f32_16x16x32_bf16 v[134:137], v[166:169], a[240:243], v[134:137]
ds_read_b128 v[186:189], v52 offset:28672
v_mfma_f32_16x16x32_bf16 v[214:217], v[146:149], v[142:145], v[234:237]
s_waitcnt lgkmcnt(4)
v_mul_f32 v26, 0x3eae1a93, v26
v_mul_f32 v103, v53, v26
v_mfma_f32_16x16x32_bf16 v[134:137], v[170:173], a[244:247], v[134:137]
v_mul_f32 v108, v95, v26
v_fma_f32 v238, v94, v226, v118
v_mfma_f32_16x16x32_bf16 v[122:125], a[12:15], v[96:99], v[122:125]
s_waitcnt lgkmcnt(3)
ds_read_b128 v[190:193], v51 offset:30720
v_fma_f32 v239, v94, v227, v119
v_mfma_f32_16x16x32_bf16 v[134:137], v[174:177], a[248:251], v[134:137]
s_add_u32 m0, s49, 18432
ds_read_b128 v[194:197], v52 offset:30720
s_waitcnt lgkmcnt(4)
v_mfma_f32_16x16x32_bf16 v[134:137], v[178:181], a[252:255], v[134:137]
v_max3_f32 v109, v214, v215, v109
v_max3_f32 v254, v216, v217, v254
v_mfma_f32_16x16x32_bf16 v[126:129], a[16:19], v[96:99], v[126:129]
s_waitcnt lgkmcnt(3)
ds_read_b128 v[198:201], v51 offset:32768
v_mul_f32 v109, v109, v103
v_mfma_f32_16x16x32_bf16 v[134:137], v[182:185], v[54:57], v[134:137]
s_waitcnt lgkmcnt(3)
ds_read_b128 v[202:205], v52 offset:32768
global_load_lds_dwordx4 v11, s[42:43] offset:-2048 sc1
v_mfma_f32_16x16x32_bf16 v[134:137], v[186:189], v[58:61], v[134:137]
v_mul_f32 v254, v254, v108
v_max_f32 v255, v109, v254
v_mfma_f32_16x16x32_bf16 v[130:133], v[96:99], a[20:23], v[130:133]
s_waitcnt lgkmcnt(3)
ds_read_b128 v[206:209], v51 offset:34816
v_mov_b32 v8, v255
v_mfma_f32_16x16x32_bf16 v[134:137], v[190:193], v[62:65], v[134:137]
s_waitcnt lgkmcnt(3)
ds_read_b128 v[210:213], v52 offset:34816
v_fma_f32 v240, v94, v228, v120
v_mfma_f32_16x16x32_bf16 v[134:137], v[194:197], v[66:69], v[134:137]
v_permlane16_swap_b32 v255, v8
v_mfma_f32_16x16x32_bf16 v[122:125], a[24:27], v[104:107], v[122:125]
s_waitcnt lgkmcnt(3)
v_max_f32 v255, v255, v8
v_mov_b32 v8, v255
v_mfma_f32_16x16x32_bf16 v[134:137], v[198:201], v[70:73], v[134:137]
s_waitcnt lgkmcnt(2)
v_permlane32_swap_b32 v255, v8
v_mfma_f32_16x16x32_bf16 v[134:137], v[202:205], v[74:77], v[134:137]
v_max_f32 v255, v255, v8
v_sub_f32 v109, v109, v255
v_mfma_f32_16x16x32_bf16 v[126:129], a[28:31], v[104:107], v[126:129]
s_waitcnt lgkmcnt(1)
global_load_lds_dwordx4 v12, s[42:43] offset:-1024 sc1
v_sub_f32 v254, v254, v255
v_mfma_f32_16x16x32_bf16 v[134:137], v[206:209], v[78:81], v[134:137]
s_waitcnt lgkmcnt(0)
v_exp_f32 v9, v109
v_mfma_f32_16x16x32_bf16 v[134:137], v[210:213], v[82:85], v[134:137]
v_exp_f32 v10, v254
v_mfma_f32_16x16x32_bf16 v[130:133], v[104:107], a[32:35], v[130:133]
v_add_f32 v255, v9, v10
v_mov_b32 v8, v255
v_mfma_f32_16x16x32_bf16 v[122:125], a[36:39], v[0:3], v[122:125]
v_mul_f32 v9, v9, v53
v_fma_f32 v241, v94, v229, v121
v_mfma_f32_16x16x32_bf16 v[126:129], a[40:43], v[0:3], v[126:129]
v_permlane16_swap_b32 v255, v8
v_mfma_f32_16x16x32_bf16 v[130:133], v[0:3], a[44:47], v[130:133]
v_add_f32 v255, v255, v8
v_mov_b32 v8, v255
v_mfma_f32_16x16x32_bf16 v[122:125], a[48:51], v[4:7], v[122:125]
v_mul_f32 v10, v10, v95
v_cvt_pk_bf16_f32 v154, v238, v239
v_mfma_f32_16x16x32_bf16 v[126:129], a[52:55], v[4:7], v[126:129]
global_load_lds_dwordx4 v13, s[42:43] offset:0 sc1
v_cvt_pk_bf16_f32 v155, v240, v241
v_mfma_f32_16x16x32_bf16 v[130:133], v[4:7], a[56:59], v[130:133]
v_permlane32_swap_b32 v255, v8
v_mfma_f32_16x16x32_bf16 v[122:125], a[60:63], v[162:165], v[122:125]
v_add_f32 v255, v255, v8
v_fma_f32 v238, v86, v23, v110
v_mfma_f32_16x16x32_bf16 v[126:129], a[64:67], v[162:165], v[126:129]
v_rcp_f32 v255, v255
v_mfma_f32_16x16x32_bf16 v[130:133], v[162:165], a[68:71], v[130:133]
v_mfma_f32_16x16x32_bf16 v[122:125], a[72:75], v[166:169], v[122:125]
s_barrier
v_mfma_f32_16x16x32_bf16 v[126:129], a[76:79], v[166:169], v[126:129]
ds_read_b128 v[242:245], v25 offset:64
v_mul_f32 v9, v9, v255
v_mfma_f32_16x16x32_bf16 v[130:133], v[166:169], a[80:83], v[130:133]
v_mul_f32 v10, v10, v255
v_cvt_pk_bf16_f32 v9, v9, v9
v_mfma_f32_16x16x32_bf16 v[122:125], a[84:87], v[170:173], v[122:125]
global_load_lds_dwordx4 v14, s[42:43] offset:1024 sc1
v_cvt_pk_bf16_f32 v10, v10, v10
v_mfma_f32_16x16x32_bf16 v[126:129], a[88:91], v[170:173], v[126:129]
v_and_b32 v222, v9, v40
v_and_b32 v223, v10, v41
v_mfma_f32_16x16x32_bf16 v[130:133], v[170:173], a[92:95], v[130:133]
v_and_b32 v224, v9, v42
v_and_b32 v225, v10, v43
v_mfma_f32_16x16x32_bf16 v[122:125], a[96:99], v[174:177], v[122:125]
s_nop 0
v_mfma_f32_16x16x32_bf16 v[218:221], v[154:157], v[222:225], 0
v_mfma_f32_16x16x32_bf16 v[214:217], v[158:161], v[222:225], 0
v_fma_f32 v239, v87, v23, v111
v_fma_f32 v240, v88, v23, v112
v_mfma_f32_16x16x32_bf16 v[126:129], a[100:103], v[174:177], v[126:129]
s_waitcnt lgkmcnt(0)
v_fma_f32 v230, v102, v242, v134
v_fma_f32 v231, v102, v243, v135
v_mfma_f32_16x16x32_bf16 v[130:133], v[174:177], a[104:107], v[130:133]
v_fma_f32 v232, v102, v244, v136
v_fma_f32 v233, v102, v245, v137
v_mfma_f32_16x16x32_bf16 v[122:125], a[108:111], v[178:181], v[122:125]
v_cvt_pk_bf16_f32 v230, v230, v231
v_cvt_pk_bf16_f32 v231, v232, v233
v_mfma_f32_16x16x32_bf16 v[126:129], a[112:115], v[178:181], v[126:129]
v_cvt_pk_bf16_f32 v100, v218, v219
v_cvt_pk_bf16_f32 v101, v220, v221
v_mfma_f32_16x16x32_bf16 v[130:133], v[178:181], a[116:119], v[130:133]
global_load_lds_dwordx4 v15, s[42:43] offset:2048 sc1
s_sub_u32 s42, s42, 18432
s_subb_u32 s43, s43, 0
v_mfma_f32_16x16x32_bf16 v[122:125], a[120:123], v[182:185], v[122:125]
ds_write_b64 v18, v[100:101]
v_mfma_f32_16x16x32_bf16 v[126:129], a[124:127], v[182:185], v[126:129]
v_cvt_pk_bf16_f32 v9, v216, v217
v_fma_f32 v241, v89, v23, v113
v_mfma_f32_16x16x32_bf16 v[130:133], v[182:185], a[128:131], v[130:133]
ds_write_b32 v19, v9
v_mfma_f32_16x16x32_bf16 v[122:125], a[132:135], v[186:189], v[122:125]
ds_write_b64 v22, v[230:231] offset:41472
v_mfma_f32_16x16x32_bf16 v[126:129], a[136:139], v[186:189], v[126:129]
v_cvt_pk_bf16_f32 v138, v238, v239
v_cvt_pk_bf16_f32 v139, v240, v241
v_mfma_f32_16x16x32_bf16 v[130:133], v[186:189], a[140:143], v[130:133]
v_mfma_f32_16x16x32_bf16 v[122:125], a[144:147], v[190:193], v[122:125]
v_mfma_f32_16x16x32_bf16 v[126:129], a[148:151], v[190:193], v[126:129]
v_mfma_f32_16x16x32_bf16 v[130:133], v[190:193], a[152:155], v[130:133]
v_mfma_f32_16x16x32_bf16 v[122:125], a[156:159], v[194:197], v[122:125]
v_mfma_f32_16x16x32_bf16 v[126:129], a[160:163], v[194:197], v[126:129]
v_mfma_f32_16x16x32_bf16 v[130:133], v[194:197], a[164:167], v[130:133]
v_mfma_f32_16x16x32_bf16 v[122:125], a[168:171], v[198:201], v[122:125]
v_mfma_f32_16x16x32_bf16 v[126:129], a[172:175], v[198:201], v[126:129]
v_xor_b32 v51, 0x10000, v51
v_xor_b32 v52, 0x10000, v52
v_mfma_f32_16x16x32_bf16 v[130:133], v[198:201], a[176:179], v[130:133]
v_mfma_f32_16x16x32_bf16 v[122:125], a[180:183], v[202:205], v[122:125]
v_mfma_f32_16x16x32_bf16 v[126:129], a[184:187], v[202:205], v[126:129]
s_waitcnt vmcnt(5) lgkmcnt(0)
s_barrier
.Lqa_in26:
v_mfma_f32_16x16x32_bf16 v[130:133], v[202:205], a[188:191], v[130:133]
s_cmp_eq_u32 s70, s73
s_cbranch_scc1 .Lqa_hk27
.Lqa_hr28:
ds_read_b128 v[44:47], v51 offset:0
v_mfma_f32_16x16x32_bf16 v[122:125], a[192:195], v[206:209], v[122:125]
ds_read_b128 v[96:99], v52 offset:0
ds_read_b128 v[230:233], v17 offset:0
v_mfma_f32_16x16x32_bf16 v[126:129], a[196:199], v[206:209], v[126:129]
ds_read_b32 v23, v24 offset:64
ds_read_b32 v152, v21
v_mfma_f32_16x16x32_bf16 v[218:221], v[146:149], v[138:141], v[36:39]
v_mfma_f32_16x16x32_bf16 v[130:133], v[206:209], a[200:203], v[130:133]
ds_read_b128 v[104:107], v51 offset:2048
ds_read_b32 v26, v24 offset:128
v_mfma_f32_16x16x32_bf16 v[122:125], a[204:207], v[210:213], v[122:125]
ds_read_b128 v[0:3], v52 offset:2048
ds_read_b32 v53, v29
v_mfma_f32_16x16x32_bf16 v[126:129], a[208:211], v[210:213], v[126:129]
v_mfma_f32_16x16x32_bf16 v[130:133], v[210:213], a[212:215], v[130:133]
s_sub_u32 s70, s70, 1
ds_read_b128 v[4:7], v51 offset:4096
s_cbranch_scc0 .Lqa_top45
s_waitcnt lgkmcnt(0)
ds_read_b32 v95, v30
ds_read_b128 v[226:229], v25 offset:64
ds_read_b64 v[160:161], v22 offset:41472
ds_read_b32 v144, v20
v_fma_f32 v238, v90, v23, v126
global_store_dwordx4 v16, v[230:233], s[38:39] sc1
v_add_u32 v16, 18432, v16
v_fma_f32 v239, v91, v23, v127
v_fma_f32 v240, v92, v23, v128
v_fma_f32 v241, v93, v23, v129
v_cvt_pk_bf16_f32 v150, v238, v239
v_cvt_pk_bf16_f32 v151, v240, v241
s_nop 1
v_mfma_f32_16x16x32_bf16 v[214:217], v[150:153], v[138:141], v[32:35]
s_waitcnt lgkmcnt(2)
v_max_f32 v109, v218, v219
v_max_f32 v254, v220, v221
v_fma_f32 v238, v94, v226, v130
v_mul_f32 v26, 0x3eae1a93, v26
v_mul_f32 v103, v53, v26
v_mul_f32 v108, v95, v26
s_nop 0
v_max3_f32 v109, v214, v215, v109
v_max3_f32 v254, v216, v217, v254
v_mul_f32 v109, v109, v103
v_mul_f32 v254, v254, v108
v_max_f32 v255, v109, v254
v_mov_b32 v8, v255
s_nop 1
v_permlane16_swap_b32 v255, v8
v_max_f32 v255, v255, v8
v_mov_b32 v8, v255
s_nop 1
v_permlane32_swap_b32 v255, v8
v_max_f32 v255, v255, v8
v_sub_f32 v109, v109, v255
v_sub_f32 v254, v254, v255
v_exp_f32 v9, v109
v_exp_f32 v10, v254
s_nop 0
v_add_f32 v255, v9, v10
v_mov_b32 v8, v255
v_mul_f32 v9, v9, v53
v_fma_f32 v239, v94, v227, v131
v_permlane16_swap_b32 v255, v8
v_add_f32 v255, v255, v8
v_mov_b32 v8, v255
v_mul_f32 v10, v10, v95
v_fma_f32 v240, v94, v228, v132
v_fma_f32 v241, v94, v229, v133
v_permlane32_swap_b32 v255, v8
v_add_f32 v255, v255, v8
v_cvt_pk_bf16_f32 v156, v238, v239
s_waitcnt lgkmcnt(0)
s_barrier
v_cvt_pk_bf16_f32 v157, v240, v241
v_rcp_f32 v255, v255
s_nop 0
v_mul_f32 v9, v9, v255
v_mul_f32 v10, v10, v255
v_cvt_pk_bf16_f32 v9, v9, v9
v_cvt_pk_bf16_f32 v10, v10, v10
v_and_b32 v224, v9, v40
v_and_b32 v225, v10, v41
v_and_b32 v222, v9, v42
v_and_b32 v223, v10, v43
s_nop 1
v_mfma_f32_16x16x32_bf16 v[218:221], v[154:157], v[222:225], 0
v_mfma_f32_16x16x32_bf16 v[214:217], v[158:161], v[222:225], 0
v_fma_f32 v238, v86, v23, v122
s_nop 5
v_cvt_pk_bf16_f32 v100, v218, v219
v_cvt_pk_bf16_f32 v101, v220, v221
ds_write_b64 v18, v[100:101]
v_cvt_pk_bf16_f32 v9, v216, v217
v_fma_f32 v239, v87, v23, v123
ds_write_b32 v19, v9
v_fma_f32 v240, v88, v23, v124
v_fma_f32 v241, v89, v23, v125
v_cvt_pk_bf16_f32 v142, v238, v239
v_cvt_pk_bf16_f32 v143, v240, v241
s_waitcnt vmcnt(0)
s_waitcnt lgkmcnt(0)
s_barrier
ds_read_b128 v[230:233], v17 offset:0
s_waitcnt lgkmcnt(0)
global_store_dwordx4 v16, v[230:233], s[38:39] sc1
s_waitcnt vmcnt(0)
s_barrier
s_endpgm
.Lqa_w3:
s_getpc_b64 s[90:91]
.Lqa_pc69:
s_add_u32 s90, s90, .Lqa_e54-.Lqa_pc69
s_addc_u32 s91, s91, 0
s_branch .Lqa_k61
.Lqa_bk68:
v_add_u32 v255, 0x10000, v9
v_add_u32 v162, 0x20000, v9
v_add_u32 v163, 0x10000, v10
v_add_u32 v164, 0x20000, v10
v_mov_b32 v106, 0x3F803F80
v_mov_b32 v107, 0x3F803F80
v_mov_b32 v108, 0x3F803F80
v_mov_b32 v109, 0x3F803F80
v_mov_b32 v141, 0
v_mov_b32 v145, 0
v_mov_b32 v149, 0
v_mov_b32 v153, 0
v_mov_b32 v165, 46080
s_waitcnt lgkmcnt(0)
s_mul_i32 s37, s37, 0x9E3779B1
s_add_u32 s37, s37, 0x7F4A7C15
v_add_u32 v46, s37, v46
v_add_u32 v48, s37, v48
v_add_u32 v50, s37, v50
ds_write_b32 v165, v141
s_mov_b64 s[76:77], 0
s_mov_b32 s73, 18
s_add_u32 m0, s48, 4096
s_nop 0
global_load_lds_dwordx4 v0, s[40:41] offset:-4096
global_load_lds_dwordx4 v1, s[40:41] offset:-3072
global_load_lds_dwordx4 v2, s[40:41] offset:-2048
global_load_lds_dwordx4 v3, s[40:41] offset:-1024
global_load_lds_dwordx4 v4, s[40:41] offset:0
global_load_lds_dwordx4 v5, s[40:41] offset:1024
global_load_lds_dwordx4 v6, s[40:41] offset:2048
global_load_lds_dwordx4 v7, s[40:41] offset:3072
s_add_u32 m0, s48, 12288
s_nop 0
global_load_lds_dwordx4 v8, s[40:41] offset:-4096
s_add_u32 s40, s40, 221184
s_addc_u32 s41, s41, 0
s_add_u32 m0, s48, 40960
s_nop 0
global_load_lds_dwordx4 v0, s[40:41] offset:-4096
global_load_lds_dwordx4 v1, s[40:41] offset:-3072
global_load_lds_dwordx4 v2, s[40:41] offset:-2048
)"
R"(global_load_lds_dwordx4 v3, s[40:41] offset:-1024
global_load_lds_dwordx4 v4, s[40:41] offset:0
global_load_lds_dwordx4 v5, s[40:41] offset:1024
global_load_lds_dwordx4 v6, s[40:41] offset:2048
global_load_lds_dwordx4 v7, s[40:41] offset:3072
s_add_u32 m0, s48, 49152
s_nop 0
global_load_lds_dwordx4 v8, s[40:41] offset:-4096
s_add_u32 s40, s40, 221184
s_addc_u32 s41, s41, 0
s_waitcnt vmcnt(9) lgkmcnt(0)
s_barrier
s_add_u32 m0, s48, 77824
ds_read_b64_tr_b16 a[0:1], v9 offset:47104
ds_read_b64_tr_b16 a[2:3], v9 offset:49408
global_load_lds_dwordx4 v0, s[40:41] offset:-4096
ds_read_b64_tr_b16 a[4:5], v9 offset:47296
global_load_lds_dwordx4 v1, s[40:41] offset:-3072
ds_read_b64_tr_b16 a[6:7], v9 offset:49600
global_load_lds_dwordx4 v2, s[40:41] offset:-2048
ds_read_b64_tr_b16 a[8:9], v9 offset:47488
global_load_lds_dwordx4 v3, s[40:41] offset:-1024
ds_read_b64_tr_b16 a[10:11], v9 offset:49792
ds_read_b128 v[166:169], v51 offset:0
global_load_lds_dwordx4 v4, s[40:41] offset:0
ds_read_b128 v[174:177], v51 offset:18432
global_load_lds_dwordx4 v5, s[40:41] offset:1024
ds_read_b64_tr_b16 a[12:13], v255 offset:0
global_load_lds_dwordx4 v6, s[40:41] offset:2048
ds_read_b64_tr_b16 a[14:15], v255 offset:2304
global_load_lds_dwordx4 v7, s[40:41] offset:3072
ds_read_b64_tr_b16 a[16:17], v255 offset:192
ds_read_b64_tr_b16 a[18:19], v255 offset:2496
s_add_u32 m0, s48, 86016
ds_read_b64_tr_b16 a[20:21], v255 offset:384
global_load_lds_dwordx4 v8, s[40:41] offset:-4096
ds_read_b64_tr_b16 a[22:23], v255 offset:2688
s_add_u32 s40, s40, 221184
ds_read_b128 v[170:173], v52 offset:0
s_addc_u32 s41, s41, 0
ds_read_b128 v[178:181], v52 offset:18432
s_waitcnt vmcnt(9) lgkmcnt(0)
s_barrier
s_add_u32 m0, s48, 4096
ds_read_b64_tr_b16 a[24:25], v255 offset:18432
v_mfma_f32_16x16x32_bf16 v[86:89], a[0:3], v[106:109], 0
v_mfma_f32_16x16x32_bf16 v[90:93], a[4:7], v[106:109], 0
global_load_lds_dwordx4 v0, s[40:41] offset:-4096
ds_read_b64_tr_b16 a[26:27], v255 offset:20736
v_mfma_f32_16x16x32_bf16 v[94:97], v[106:109], a[8:11], 0
ds_read_b64_tr_b16 a[28:29], v255 offset:18624
global_load_lds_dwordx4 v1, s[40:41] offset:-3072
v_mfma_f32_16x16x32_bf16 v[110:113], a[0:3], v[166:169], 0
ds_read_b64_tr_b16 a[30:31], v255 offset:20928
v_mfma_f32_16x16x32_bf16 v[114:117], a[4:7], v[166:169], 0
global_load_lds_dwordx4 v2, s[40:41] offset:-2048
ds_read_b64_tr_b16 a[32:33], v255 offset:18816
v_mfma_f32_16x16x32_bf16 v[118:121], v[166:169], a[8:11], 0
ds_read_b64_tr_b16 a[34:35], v255 offset:21120
global_load_lds_dwordx4 v3, s[40:41] offset:-1024
v_mfma_f32_16x16x32_bf16 v[134:137], v[166:169], v[166:169], 0
ds_read_b128 v[182:185], v51 offset:2048
v_mfma_f32_16x16x32_bf16 v[122:125], a[0:3], v[174:177], 0
global_load_lds_dwordx4 v4, s[40:41] offset:0
ds_read_b128 v[190:193], v51 offset:20480
v_mfma_f32_16x16x32_bf16 v[126:129], a[4:7], v[174:177], 0
ds_read_b64_tr_b16 a[36:37], v255 offset:36864
global_load_lds_dwordx4 v5, s[40:41] offset:1024
v_mfma_f32_16x16x32_bf16 v[130:133], v[174:177], a[8:11], 0
v_mfma_f32_16x16x32_bf16 v[158:161], v[174:177], v[174:177], 0
ds_read_b64_tr_b16 a[38:39], v255 offset:39168
global_load_lds_dwordx4 v6, s[40:41] offset:2048
v_mfma_f32_16x16x32_bf16 v[86:89], a[12:15], v[106:109], v[86:89]
ds_read_b64_tr_b16 a[40:41], v255 offset:37056
v_mfma_f32_16x16x32_bf16 v[90:93], a[16:19], v[106:109], v[90:93]
global_load_lds_dwordx4 v7, s[40:41] offset:3072
ds_read_b64_tr_b16 a[42:43], v255 offset:39360
v_mfma_f32_16x16x32_bf16 v[94:97], v[106:109], a[20:23], v[94:97]
s_add_u32 m0, s48, 12288
ds_read_b64_tr_b16 a[44:45], v255 offset:37248
v_mfma_f32_16x16x32_bf16 v[110:113], a[12:15], v[170:173], v[110:113]
ds_read_b64_tr_b16 a[46:47], v255 offset:39552
v_mfma_f32_16x16x32_bf16 v[114:117], a[16:19], v[170:173], v[114:117]
global_load_lds_dwordx4 v8, s[40:41] offset:-4096
ds_read_b128 v[186:189], v52 offset:2048
v_mfma_f32_16x16x32_bf16 v[118:121], v[170:173], a[20:23], v[118:121]
s_add_u32 s40, s40, 221184
ds_read_b128 v[194:197], v52 offset:20480
v_mfma_f32_16x16x32_bf16 v[134:137], v[170:173], v[170:173], v[134:137]
v_mfma_f32_16x16x32_bf16 v[122:125], a[12:15], v[178:181], v[122:125]
v_mfma_f32_16x16x32_bf16 v[126:129], a[16:19], v[178:181], v[126:129]
s_addc_u32 s41, s41, 0
v_mfma_f32_16x16x32_bf16 v[130:133], v[178:181], a[20:23], v[130:133]
v_mfma_f32_16x16x32_bf16 v[158:161], v[178:181], v[178:181], v[158:161]
s_waitcnt vmcnt(9) lgkmcnt(0)
s_barrier
s_add_u32 m0, s48, 40960
ds_read_b64_tr_b16 a[48:49], v255 offset:55296
v_mfma_f32_16x16x32_bf16 v[86:89], a[24:27], v[106:109], v[86:89]
v_mfma_f32_16x16x32_bf16 v[90:93], a[28:31], v[106:109], v[90:93]
global_load_lds_dwordx4 v0, s[40:41] offset:-4096
ds_read_b64_tr_b16 a[50:51], v255 offset:57600
v_mfma_f32_16x16x32_bf16 v[94:97], v[106:109], a[32:35], v[94:97]
ds_read_b64_tr_b16 a[52:53], v255 offset:55488
global_load_lds_dwordx4 v1, s[40:41] offset:-3072
v_mfma_f32_16x16x32_bf16 v[110:113], a[24:27], v[182:185], v[110:113]
ds_read_b64_tr_b16 a[54:55], v255 offset:57792
v_mfma_f32_16x16x32_bf16 v[114:117], a[28:31], v[182:185], v[114:117]
global_load_lds_dwordx4 v2, s[40:41] offset:-2048
ds_read_b64_tr_b16 a[56:57], v255 offset:55680
v_mfma_f32_16x16x32_bf16 v[118:121], v[182:185], a[32:35], v[118:121]
ds_read_b64_tr_b16 a[58:59], v255 offset:57984
global_load_lds_dwordx4 v3, s[40:41] offset:-1024
v_mfma_f32_16x16x32_bf16 v[134:137], v[182:185], v[182:185], v[134:137]
ds_read_b128 v[166:169], v51 offset:4096
v_mfma_f32_16x16x32_bf16 v[122:125], a[24:27], v[190:193], v[122:125]
global_load_lds_dwordx4 v4, s[40:41] offset:0
ds_read_b128 v[174:177], v51 offset:22528
v_mfma_f32_16x16x32_bf16 v[126:129], a[28:31], v[190:193], v[126:129]
ds_read_b64_tr_b16 a[60:61], v162 offset:8192
global_load_lds_dwordx4 v5, s[40:41] offset:1024
v_mfma_f32_16x16x32_bf16 v[130:133], v[190:193], a[32:35], v[130:133]
v_mfma_f32_16x16x32_bf16 v[158:161], v[190:193], v[190:193], v[158:161]
ds_read_b64_tr_b16 a[62:63], v162 offset:10496
global_load_lds_dwordx4 v6, s[40:41] offset:2048
v_mfma_f32_16x16x32_bf16 v[86:89], a[36:39], v[106:109], v[86:89]
ds_read_b64_tr_b16 a[64:65], v162 offset:8384
v_mfma_f32_16x16x32_bf16 v[90:93], a[40:43], v[106:109], v[90:93]
global_load_lds_dwordx4 v7, s[40:41] offset:3072
ds_read_b64_tr_b16 a[66:67], v162 offset:10688
v_mfma_f32_16x16x32_bf16 v[94:97], v[106:109], a[44:47], v[94:97]
s_add_u32 m0, s48, 49152
ds_read_b64_tr_b16 a[68:69], v162 offset:8576
v_mfma_f32_16x16x32_bf16 v[110:113], a[36:39], v[186:189], v[110:113]
ds_read_b64_tr_b16 a[70:71], v162 offset:10880
v_mfma_f32_16x16x32_bf16 v[114:117], a[40:43], v[186:189], v[114:117]
global_load_lds_dwordx4 v8, s[40:41] offset:-4096
ds_read_b128 v[170:173], v52 offset:4096
v_mfma_f32_16x16x32_bf16 v[118:121], v[186:189], a[44:47], v[118:121]
s_add_u32 s40, s40, 221184
ds_read_b128 v[178:181], v52 offset:22528
v_mfma_f32_16x16x32_bf16 v[134:137], v[186:189], v[186:189], v[134:137]
v_mfma_f32_16x16x32_bf16 v[122:125], a[36:39], v[194:197], v[122:125]
v_mfma_f32_16x16x32_bf16 v[126:129], a[40:43], v[194:197], v[126:129]
s_addc_u32 s41, s41, 0
v_mfma_f32_16x16x32_bf16 v[130:133], v[194:197], a[44:47], v[130:133]
v_mfma_f32_16x16x32_bf16 v[158:161], v[194:197], v[194:197], v[158:161]
s_waitcnt vmcnt(9) lgkmcnt(0)
s_barrier
s_add_u32 m0, s48, 77824
ds_read_b64_tr_b16 a[72:73], v9 offset:47104
v_mfma_f32_16x16x32_bf16 v[86:89], a[48:51], v[106:109], v[86:89]
v_mfma_f32_16x16x32_bf16 v[90:93], a[52:55], v[106:109], v[90:93]
global_load_lds_dwordx4 v0, s[40:41] offset:-4096
ds_read_b64_tr_b16 a[74:75], v9 offset:49408
v_mfma_f32_16x16x32_bf16 v[94:97], v[106:109], a[56:59], v[94:97]
ds_read_b64_tr_b16 a[76:77], v9 offset:47296
global_load_lds_dwordx4 v1, s[40:41] offset:-3072
v_mfma_f32_16x16x32_bf16 v[110:113], a[48:51], v[166:169], v[110:113]
ds_read_b64_tr_b16 a[78:79], v9 offset:49600
v_mfma_f32_16x16x32_bf16 v[114:117], a[52:55], v[166:169], v[114:117]
global_load_lds_dwordx4 v2, s[40:41] offset:-2048
ds_read_b64_tr_b16 a[80:81], v9 offset:47488
v_mfma_f32_16x16x32_bf16 v[118:121], v[166:169], a[56:59], v[118:121]
ds_read_b64_tr_b16 a[82:83], v9 offset:49792
global_load_lds_dwordx4 v3, s[40:41] offset:-1024
v_mfma_f32_16x16x32_bf16 v[134:137], v[166:169], v[166:169], v[134:137]
ds_read_b128 v[182:185], v51 offset:6144
v_mfma_f32_16x16x32_bf16 v[122:125], a[48:51], v[174:177], v[122:125]
global_load_lds_dwordx4 v4, s[40:41] offset:0
ds_read_b128 v[190:193], v51 offset:24576
v_mfma_f32_16x16x32_bf16 v[126:129], a[52:55], v[174:177], v[126:129]
ds_read_b64_tr_b16 a[84:85], v255 offset:0
global_load_lds_dwordx4 v5, s[40:41] offset:1024
v_mfma_f32_16x16x32_bf16 v[130:133], v[174:177], a[56:59], v[130:133]
v_mfma_f32_16x16x32_bf16 v[158:161], v[174:177], v[174:177], v[158:161]
ds_read_b64_tr_b16 a[86:87], v255 offset:2304
global_load_lds_dwordx4 v6, s[40:41] offset:2048
v_mfma_f32_16x16x32_bf16 v[86:89], a[60:63], v[106:109], v[86:89]
ds_read_b64_tr_b16 a[88:89], v255 offset:192
v_mfma_f32_16x16x32_bf16 v[90:93], a[64:67], v[106:109], v[90:93]
global_load_lds_dwordx4 v7, s[40:41] offset:3072
ds_read_b64_tr_b16 a[90:91], v255 offset:2496
v_mfma_f32_16x16x32_bf16 v[94:97], v[106:109], a[68:71], v[94:97]
s_add_u32 m0, s48, 86016
ds_read_b64_tr_b16 a[92:93], v255 offset:384
v_mfma_f32_16x16x32_bf16 v[110:113], a[60:63], v[170:173], v[110:113]
ds_read_b64_tr_b16 a[94:95], v255 offset:2688
v_mfma_f32_16x16x32_bf16 v[114:117], a[64:67], v[170:173], v[114:117]
global_load_lds_dwordx4 v8, s[40:41] offset:-4096
ds_read_b128 v[186:189], v52 offset:6144
v_mfma_f32_16x16x32_bf16 v[118:121], v[170:173], a[68:71], v[118:121]
s_add_u32 s40, s40, 221184
ds_read_b128 v[194:197], v52 offset:24576
v_mfma_f32_16x16x32_bf16 v[134:137], v[170:173], v[170:173], v[134:137]
v_mfma_f32_16x16x32_bf16 v[122:125], a[60:63], v[178:181], v[122:125]
v_mfma_f32_16x16x32_bf16 v[126:129], a[64:67], v[178:181], v[126:129]
s_addc_u32 s41, s41, 0
v_mfma_f32_16x16x32_bf16 v[130:133], v[178:181], a[68:71], v[130:133]
v_mfma_f32_16x16x32_bf16 v[158:161], v[178:181], v[178:181], v[158:161]
s_waitcnt vmcnt(9) lgkmcnt(0)
s_barrier
s_add_u32 m0, s48, 4096
ds_read_b64_tr_b16 a[96:97], v255 offset:18432
v_mfma_f32_16x16x32_bf16 v[86:89], a[72:75], v[106:109], v[86:89]
v_mfma_f32_16x16x32_bf16 v[90:93], a[76:79], v[106:109], v[90:93]
global_load_lds_dwordx4 v0, s[40:41] offset:-4096
ds_read_b64_tr_b16 a[98:99], v255 offset:20736
v_mfma_f32_16x16x32_bf16 v[94:97], v[106:109], a[80:83], v[94:97]
ds_read_b64_tr_b16 a[100:101], v255 offset:18624
global_load_lds_dwordx4 v1, s[40:41] offset:-3072
v_mfma_f32_16x16x32_bf16 v[110:113], a[72:75], v[182:185], v[110:113]
ds_read_b64_tr_b16 a[102:103], v255 offset:20928
v_mfma_f32_16x16x32_bf16 v[114:117], a[76:79], v[182:185], v[114:117]
global_load_lds_dwordx4 v2, s[40:41] offset:-2048
ds_read_b64_tr_b16 a[104:105], v255 offset:18816
v_mfma_f32_16x16x32_bf16 v[118:121], v[182:185], a[80:83], v[118:121]
ds_read_b64_tr_b16 a[106:107], v255 offset:21120
global_load_lds_dwordx4 v3, s[40:41] offset:-1024
v_mfma_f32_16x16x32_bf16 v[134:137], v[182:185], v[182:185], v[134:137]
ds_read_b128 v[166:169], v51 offset:8192
v_mfma_f32_16x16x32_bf16 v[122:125], a[72:75], v[190:193], v[122:125]
global_load_lds_dwordx4 v4, s[40:41] offset:0
ds_read_b128 v[174:177], v51 offset:26624
v_mfma_f32_16x16x32_bf16 v[126:129], a[76:79], v[190:193], v[126:129]
ds_read_b64_tr_b16 a[108:109], v255 offset:36864
global_load_lds_dwordx4 v5, s[40:41] offset:1024
v_mfma_f32_16x16x32_bf16 v[130:133], v[190:193], a[80:83], v[130:133]
v_mfma_f32_16x16x32_bf16 v[158:161], v[190:193], v[190:193], v[158:161]
ds_read_b64_tr_b16 a[110:111], v255 offset:39168
global_load_lds_dwordx4 v6, s[40:41] offset:2048
v_mfma_f32_16x16x32_bf16 v[86:89], a[84:87], v[106:109], v[86:89]
ds_read_b64_tr_b16 a[112:113], v255 offset:37056
v_mfma_f32_16x16x32_bf16 v[90:93], a[88:91], v[106:109], v[90:93]
global_load_lds_dwordx4 v7, s[40:41] offset:3072
ds_read_b64_tr_b16 a[114:115], v255 offset:39360
v_mfma_f32_16x16x32_bf16 v[94:97], v[106:109], a[92:95], v[94:97]
s_add_u32 m0, s48, 12288
ds_read_b64_tr_b16 a[116:117], v255 offset:37248
v_mfma_f32_16x16x32_bf16 v[110:113], a[84:87], v[186:189], v[110:113]
ds_read_b64_tr_b16 a[118:119], v255 offset:39552
v_mfma_f32_16x16x32_bf16 v[114:117], a[88:91], v[186:189], v[114:117]
global_load_lds_dwordx4 v8, s[40:41] offset:-4096
ds_read_b128 v[170:173], v52 offset:8192
v_mfma_f32_16x16x32_bf16 v[118:121], v[186:189], a[92:95], v[118:121]
s_add_u32 s40, s40, 221184
ds_read_b128 v[178:181], v52 offset:26624
v_mfma_f32_16x16x32_bf16 v[134:137], v[186:189], v[186:189], v[134:137]
v_mfma_f32_16x16x32_bf16 v[122:125], a[84:87], v[194:197], v[122:125]
v_mfma_f32_16x16x32_bf16 v[126:129], a[88:91], v[194:197], v[126:129]
s_addc_u32 s41, s41, 0
v_mfma_f32_16x16x32_bf16 v[130:133], v[194:197], a[92:95], v[130:133]
v_mfma_f32_16x16x32_bf16 v[158:161], v[194:197], v[194:197], v[158:161]
s_waitcnt vmcnt(9) lgkmcnt(0)
s_barrier
s_add_u32 m0, s48, 40960
ds_read_b64_tr_b16 a[120:121], v255 offset:55296
v_mfma_f32_16x16x32_bf16 v[86:89], a[96:99], v[106:109], v[86:89]
v_mfma_f32_16x16x32_bf16 v[90:93], a[100:103], v[106:109], v[90:93]
global_load_lds_dwordx4 v0, s[40:41] offset:-4096
ds_read_b64_tr_b16 a[122:123], v255 offset:57600
v_mfma_f32_16x16x32_bf16 v[94:97], v[106:109], a[104:107], v[94:97]
ds_read_b64_tr_b16 a[124:125], v255 offset:55488
global_load_lds_dwordx4 v1, s[40:41] offset:-3072
v_mfma_f32_16x16x32_bf16 v[110:113], a[96:99], v[166:169], v[110:113]
ds_read_b64_tr_b16 a[126:127], v255 offset:57792
v_mfma_f32_16x16x32_bf16 v[114:117], a[100:103], v[166:169], v[114:117]
global_load_lds_dwordx4 v2, s[40:41] offset:-2048
ds_read_b64_tr_b16 a[128:129], v255 offset:55680
v_mfma_f32_16x16x32_bf16 v[118:121], v[166:169], a[104:107], v[118:121]
ds_read_b64_tr_b16 a[130:131], v255 offset:57984
global_load_lds_dwordx4 v3, s[40:41] offset:-1024
v_mfma_f32_16x16x32_bf16 v[134:137], v[166:169], v[166:169], v[134:137]
ds_read_b128 v[182:185], v51 offset:10240
v_mfma_f32_16x16x32_bf16 v[122:125], a[96:99], v[174:177], v[122:125]
global_load_lds_dwordx4 v4, s[40:41] offset:0
ds_read_b128 v[190:193], v51 offset:28672
v_mfma_f32_16x16x32_bf16 v[126:129], a[100:103], v[174:177], v[126:129]
ds_read_b64_tr_b16 a[132:133], v162 offset:8192
global_load_lds_dwordx4 v5, s[40:41] offset:1024
v_mfma_f32_16x16x32_bf16 v[130:133], v[174:177], a[104:107], v[130:133]
v_mfma_f32_16x16x32_bf16 v[158:161], v[174:177], v[174:177], v[158:161]
ds_read_b64_tr_b16 a[134:135], v162 offset:10496
global_load_lds_dwordx4 v6, s[40:41] offset:2048
v_mfma_f32_16x16x32_bf16 v[86:89], a[108:111], v[106:109], v[86:89]
ds_read_b64_tr_b16 a[136:137], v162 offset:8384
v_mfma_f32_16x16x32_bf16 v[90:93], a[112:115], v[106:109], v[90:93]
global_load_lds_dwordx4 v7, s[40:41] offset:3072
ds_read_b64_tr_b16 a[138:139], v162 offset:10688
v_mfma_f32_16x16x32_bf16 v[94:97], v[106:109], a[116:119], v[94:97]
s_add_u32 m0, s48, 49152
ds_read_b64_tr_b16 a[140:141], v162 offset:8576
v_mfma_f32_16x16x32_bf16 v[110:113], a[108:111], v[170:173], v[110:113]
ds_read_b64_tr_b16 a[142:143], v162 offset:10880
v_mfma_f32_16x16x32_bf16 v[114:117], a[112:115], v[170:173], v[114:117]
global_load_lds_dwordx4 v8, s[40:41] offset:-4096
ds_read_b128 v[186:189], v52 offset:10240
v_mfma_f32_16x16x32_bf16 v[118:121], v[170:173], a[116:119], v[118:121]
s_add_u32 s40, s40, 221184
ds_read_b128 v[194:197], v52 offset:28672
v_mfma_f32_16x16x32_bf16 v[134:137], v[170:173], v[170:173], v[134:137]
v_mfma_f32_16x16x32_bf16 v[122:125], a[108:111], v[178:181], v[122:125]
v_mfma_f32_16x16x32_bf16 v[126:129], a[112:115], v[178:181], v[126:129]
s_addc_u32 s41, s41, 0
v_mfma_f32_16x16x32_bf16 v[130:133], v[178:181], a[116:119], v[130:133]
v_mfma_f32_16x16x32_bf16 v[158:161], v[178:181], v[178:181], v[158:161]
s_waitcnt vmcnt(9) lgkmcnt(0)
s_barrier
s_add_u32 m0, s48, 77824
ds_read_b64_tr_b16 a[144:145], v9 offset:47104
v_mfma_f32_16x16x32_bf16 v[86:89], a[120:123], v[106:109], v[86:89]
v_mfma_f32_16x16x32_bf16 v[90:93], a[124:127], v[106:109], v[90:93]
global_load_lds_dwordx4 v0, s[40:41] offset:-4096
ds_read_b64_tr_b16 a[146:147], v9 offset:49408
v_mfma_f32_16x16x32_bf16 v[94:97], v[106:109], a[128:131], v[94:97]
ds_read_b64_tr_b16 a[148:149], v9 offset:47296
global_load_lds_dwordx4 v1, s[40:41] offset:-3072
v_mfma_f32_16x16x32_bf16 v[110:113], a[120:123], v[182:185], v[110:113]
ds_read_b64_tr_b16 a[150:151], v9 offset:49600
v_mfma_f32_16x16x32_bf16 v[114:117], a[124:127], v[182:185], v[114:117]
global_load_lds_dwordx4 v2, s[40:41] offset:-2048
ds_read_b64_tr_b16 a[152:153], v9 offset:47488
v_mfma_f32_16x16x32_bf16 v[118:121], v[182:185], a[128:131], v[118:121]
ds_read_b64_tr_b16 a[154:155], v9 offset:49792
global_load_lds_dwordx4 v3, s[40:41] offset:-1024
v_mfma_f32_16x16x32_bf16 v[134:137], v[182:185], v[182:185], v[134:137]
ds_read_b128 v[166:169], v51 offset:12288
v_mfma_f32_16x16x32_bf16 v[122:125], a[120:123], v[190:193], v[122:125]
global_load_lds_dwordx4 v4, s[40:41] offset:0
ds_read_b128 v[174:177], v51 offset:30720
v_mfma_f32_16x16x32_bf16 v[126:129], a[124:127], v[190:193], v[126:129]
ds_read_b64_tr_b16 a[156:157], v255 offset:0
global_load_lds_dwordx4 v5, s[40:41] offset:1024
v_mfma_f32_16x16x32_bf16 v[130:133], v[190:193], a[128:131], v[130:133]
v_mfma_f32_16x16x32_bf16 v[158:161], v[190:193], v[190:193], v[158:161]
ds_read_b64_tr_b16 a[158:159], v255 offset:2304
global_load_lds_dwordx4 v6, s[40:41] offset:2048
v_mfma_f32_16x16x32_bf16 v[86:89], a[132:135], v[106:109], v[86:89]
ds_read_b64_tr_b16 a[160:161], v255 offset:192
v_mfma_f32_16x16x32_bf16 v[90:93], a[136:139], v[106:109], v[90:93]
global_load_lds_dwordx4 v7, s[40:41] offset:3072
ds_read_b64_tr_b16 a[162:163], v255 offset:2496
v_mfma_f32_16x16x32_bf16 v[94:97], v[106:109], a[140:143], v[94:97]
s_add_u32 m0, s48, 86016
ds_read_b64_tr_b16 a[164:165], v255 offset:384
v_mfma_f32_16x16x32_bf16 v[110:113], a[132:135], v[186:189], v[110:113]
ds_read_b64_tr_b16 a[166:167], v255 offset:2688
v_mfma_f32_16x16x32_bf16 v[114:117], a[136:139], v[186:189], v[114:117]
global_load_lds_dwordx4 v8, s[40:41] offset:-4096
ds_read_b128 v[170:173], v52 offset:12288
v_mfma_f32_16x16x32_bf16 v[118:121], v[186:189], a[140:143], v[118:121]
s_sub_u32 s40, s40, 1769472
ds_read_b128 v[178:181], v52 offset:30720
v_mfma_f32_16x16x32_bf16 v[134:137], v[186:189], v[186:189], v[134:137]
v_mfma_f32_16x16x32_bf16 v[122:125], a[132:135], v[194:197], v[122:125]
v_mfma_f32_16x16x32_bf16 v[126:129], a[136:139], v[194:197], v[126:129]
s_subb_u32 s41, s41, 0
v_mfma_f32_16x16x32_bf16 v[130:133], v[194:197], a[140:143], v[130:133]
v_mfma_f32_16x16x32_bf16 v[158:161], v[194:197], v[194:197], v[158:161]
s_waitcnt vmcnt(9) lgkmcnt(0)
s_barrier
s_add_u32 m0, s49, 0
ds_read_b64_tr_b16 a[168:169], v255 offset:18432
v_mfma_f32_16x16x32_bf16 v[86:89], a[144:147], v[106:109], v[86:89]
v_mfma_f32_16x16x32_bf16 v[90:93], a[148:151], v[106:109], v[90:93]
ds_read_b64_tr_b16 a[170:171], v255 offset:20736
v_mfma_f32_16x16x32_bf16 v[94:97], v[106:109], a[152:155], v[94:97]
ds_read_b64_tr_b16 a[172:173], v255 offset:18624
v_mfma_f32_16x16x32_bf16 v[110:113], a[144:147], v[166:169], v[110:113]
global_load_lds_dwordx4 v11, s[42:43] offset:-2048 sc1
ds_read_b64_tr_b16 a[174:175], v255 offset:20928
v_mfma_f32_16x16x32_bf16 v[114:117], a[148:151], v[166:169], v[114:117]
ds_read_b64_tr_b16 a[176:177], v255 offset:18816
v_mfma_f32_16x16x32_bf16 v[118:121], v[166:169], a[152:155], v[118:121]
ds_read_b64_tr_b16 a[178:179], v255 offset:21120
v_mfma_f32_16x16x32_bf16 v[134:137], v[166:169], v[166:169], v[134:137]
global_load_lds_dwordx4 v12, s[42:43] offset:-1024 sc1
ds_read_b128 v[182:185], v51 offset:14336
v_mfma_f32_16x16x32_bf16 v[122:125], a[144:147], v[174:177], v[122:125]
ds_read_b128 v[190:193], v51 offset:32768
v_mfma_f32_16x16x32_bf16 v[126:129], a[148:151], v[174:177], v[126:129]
ds_read_b64_tr_b16 a[180:181], v255 offset:36864
v_mfma_f32_16x16x32_bf16 v[130:133], v[174:177], a[152:155], v[130:133]
global_load_lds_dwordx4 v13, s[42:43] offset:0 sc1
v_mfma_f32_16x16x32_bf16 v[158:161], v[174:177], v[174:177], v[158:161]
ds_read_b64_tr_b16 a[182:183], v255 offset:39168
v_mfma_f32_16x16x32_bf16 v[86:89], a[156:159], v[106:109], v[86:89]
ds_read_b64_tr_b16 a[184:185], v255 offset:37056
v_mfma_f32_16x16x32_bf16 v[90:93], a[160:163], v[106:109], v[90:93]
ds_read_b64_tr_b16 a[186:187], v255 offset:39360
v_mfma_f32_16x16x32_bf16 v[94:97], v[106:109], a[164:167], v[94:97]
global_load_lds_dwordx4 v14, s[42:43] offset:1024 sc1
ds_read_b64_tr_b16 a[188:189], v255 offset:37248
v_mfma_f32_16x16x32_bf16 v[110:113], a[156:159], v[170:173], v[110:113]
ds_read_b64_tr_b16 a[190:191], v255 offset:39552
v_mfma_f32_16x16x32_bf16 v[114:117], a[160:163], v[170:173], v[114:117]
ds_read_b128 v[186:189], v52 offset:14336
v_mfma_f32_16x16x32_bf16 v[118:121], v[170:173], a[164:167], v[118:121]
s_sub_u32 s42, s42, 18432
s_subb_u32 s43, s43, 0
)"
R"(ds_read_b128 v[194:197], v52 offset:32768
v_mfma_f32_16x16x32_bf16 v[134:137], v[170:173], v[170:173], v[134:137]
v_mfma_f32_16x16x32_bf16 v[122:125], a[156:159], v[178:181], v[122:125]
v_mfma_f32_16x16x32_bf16 v[126:129], a[160:163], v[178:181], v[126:129]
v_mfma_f32_16x16x32_bf16 v[130:133], v[178:181], a[164:167], v[130:133]
v_mfma_f32_16x16x32_bf16 v[158:161], v[178:181], v[178:181], v[158:161]
s_waitcnt vmcnt(4) lgkmcnt(0)
s_barrier
s_add_u32 m0, s49, 18432
ds_read_b64_tr_b16 a[192:193], v255 offset:55296
v_mfma_f32_16x16x32_bf16 v[86:89], a[168:171], v[106:109], v[86:89]
v_mfma_f32_16x16x32_bf16 v[90:93], a[172:175], v[106:109], v[90:93]
ds_read_b64_tr_b16 a[194:195], v255 offset:57600
v_mfma_f32_16x16x32_bf16 v[94:97], v[106:109], a[176:179], v[94:97]
ds_read_b64_tr_b16 a[196:197], v255 offset:55488
v_mfma_f32_16x16x32_bf16 v[110:113], a[168:171], v[182:185], v[110:113]
global_load_lds_dwordx4 v11, s[42:43] offset:-2048 sc1
ds_read_b64_tr_b16 a[198:199], v255 offset:57792
v_mfma_f32_16x16x32_bf16 v[114:117], a[172:175], v[182:185], v[114:117]
ds_read_b64_tr_b16 a[200:201], v255 offset:55680
v_mfma_f32_16x16x32_bf16 v[118:121], v[182:185], a[176:179], v[118:121]
ds_read_b64_tr_b16 a[202:203], v255 offset:57984
v_mfma_f32_16x16x32_bf16 v[134:137], v[182:185], v[182:185], v[134:137]
global_load_lds_dwordx4 v12, s[42:43] offset:-1024 sc1
ds_read_b128 v[166:169], v51 offset:16384
v_mfma_f32_16x16x32_bf16 v[122:125], a[168:171], v[190:193], v[122:125]
ds_read_b128 v[174:177], v51 offset:34816
v_mfma_f32_16x16x32_bf16 v[126:129], a[172:175], v[190:193], v[126:129]
ds_read_b64_tr_b16 a[204:205], v162 offset:8192
v_mfma_f32_16x16x32_bf16 v[130:133], v[190:193], a[176:179], v[130:133]
global_load_lds_dwordx4 v13, s[42:43] offset:0 sc1
ds_read_b64_tr_b16 a[206:207], v162 offset:10496
v_mfma_f32_16x16x32_bf16 v[158:161], v[190:193], v[190:193], v[158:161]
ds_read_b64_tr_b16 a[208:209], v162 offset:8384
v_mfma_f32_16x16x32_bf16 v[86:89], a[180:183], v[106:109], v[86:89]
ds_read_b64_tr_b16 a[210:211], v162 offset:10688
v_mfma_f32_16x16x32_bf16 v[90:93], a[184:187], v[106:109], v[90:93]
global_load_lds_dwordx4 v14, s[42:43] offset:1024 sc1
ds_read_b64_tr_b16 a[212:213], v162 offset:8576
v_mfma_f32_16x16x32_bf16 v[94:97], v[106:109], a[188:191], v[94:97]
ds_read_b64_tr_b16 a[214:215], v162 offset:10880
v_mfma_f32_16x16x32_bf16 v[110:113], a[180:183], v[186:189], v[110:113]
ds_read_b128 v[170:173], v52 offset:16384
v_mfma_f32_16x16x32_bf16 v[114:117], a[184:187], v[186:189], v[114:117]
s_sub_u32 s42, s42, 18432
s_subb_u32 s43, s43, 0
ds_read_b128 v[178:181], v52 offset:34816
v_mfma_f32_16x16x32_bf16 v[118:121], v[186:189], a[188:191], v[118:121]
v_mfma_f32_16x16x32_bf16 v[134:137], v[186:189], v[186:189], v[134:137]
v_mfma_f32_16x16x32_bf16 v[122:125], a[180:183], v[194:197], v[122:125]
v_mfma_f32_16x16x32_bf16 v[126:129], a[184:187], v[194:197], v[126:129]
v_mfma_f32_16x16x32_bf16 v[158:161], v[194:197], v[194:197], v[158:161]
s_waitcnt lgkmcnt(14)
v_mfma_f32_16x16x32_bf16 v[86:89], a[192:195], v[106:109], v[86:89]
s_waitcnt lgkmcnt(12)
v_mfma_f32_16x16x32_bf16 v[90:93], a[196:199], v[106:109], v[90:93]
s_waitcnt lgkmcnt(10)
v_mfma_f32_16x16x32_bf16 v[94:97], v[106:109], a[200:203], v[94:97]
s_waitcnt lgkmcnt(9)
v_mfma_f32_16x16x32_bf16 v[110:113], a[192:195], v[166:169], v[110:113]
v_mfma_f32_16x16x32_bf16 v[114:117], a[196:199], v[166:169], v[114:117]
v_mfma_f32_16x16x32_bf16 v[118:121], v[166:169], a[200:203], v[118:121]
v_mfma_f32_16x16x32_bf16 v[134:137], v[166:169], v[166:169], v[134:137]
s_waitcnt lgkmcnt(8)
v_mfma_f32_16x16x32_bf16 v[158:161], v[174:177], v[174:177], v[158:161]
s_waitcnt lgkmcnt(6)
v_mfma_f32_16x16x32_bf16 v[86:89], a[204:207], v[106:109], v[86:89]
s_waitcnt lgkmcnt(4)
v_mfma_f32_16x16x32_bf16 v[90:93], a[208:211], v[106:109], v[90:93]
s_waitcnt lgkmcnt(2)
v_mfma_f32_16x16x32_bf16 v[94:97], v[106:109], a[212:215], v[94:97]
s_waitcnt lgkmcnt(1)
v_mfma_f32_16x16x32_bf16 v[110:113], a[204:207], v[170:173], v[110:113]
v_mfma_f32_16x16x32_bf16 v[114:117], a[208:211], v[170:173], v[114:117]
v_mfma_f32_16x16x32_bf16 v[118:121], v[170:173], a[212:215], v[118:121]
v_mfma_f32_16x16x32_bf16 v[134:137], v[170:173], v[170:173], v[134:137]
s_waitcnt lgkmcnt(0)
v_mfma_f32_16x16x32_bf16 v[158:161], v[178:181], v[178:181], v[158:161]
s_nop 5
ds_write_b128 v23, v[134:137] offset:41984
s_waitcnt lgkmcnt(0)
s_barrier
ds_read_b128 v[170:173], v52 offset:32768
ds_read_b128 v[174:177], v51 offset:34816
ds_read_b128 v[178:181], v52 offset:34816
v_mov_b32 v202, v32
v_mov_b32 v203, v33
v_mov_b32 v204, v34
v_mov_b32 v205, v35
v_mov_b32 v134, v158
v_mov_b32 v135, v159
v_mov_b32 v136, v160
v_mov_b32 v137, v161
s_barrier
ds_read_b32 v26, v24 offset:0
ds_read_b32 v148, v21
ds_read_b32 v95, v24 offset:192
ds_read_b32 v255, v27
ds_read_b32 v8, v28
ds_read_b128 v[194:197], v25 offset:0
ds_read_b64 v[158:159], v22 offset:41472
ds_read_b32 v140, v20
v_xor_b32 v51, 0x10000, v51
v_xor_b32 v52, 0x10000, v52
ds_write_b128 v23, v[134:137] offset:41984
s_waitcnt lgkmcnt(8)
v_fma_f32 v206, v90, v26, v114
v_fma_f32 v207, v91, v26, v115
v_fma_f32 v208, v92, v26, v116
v_fma_f32 v209, v93, v26, v117
v_cvt_pk_bf16_f32 v146, v206, v207
v_cvt_pk_bf16_f32 v147, v208, v209
s_waitcnt lgkmcnt(0)
s_barrier
v_fma_f32 v206, v94, v194, v118
v_fma_f32 v207, v94, v195, v119
v_fma_f32 v208, v94, v196, v120
v_fma_f32 v209, v94, v197, v121
v_cvt_pk_bf16_f32 v154, v206, v207
v_cvt_pk_bf16_f32 v155, v208, v209
v_fma_f32 v206, v86, v26, v110
v_fma_f32 v207, v87, v26, v111
v_fma_f32 v208, v88, v26, v112
v_fma_f32 v209, v89, v26, v113
v_cvt_pk_bf16_f32 v138, v206, v207
v_cvt_pk_bf16_f32 v139, v208, v209
s_waitcnt vmcnt(4) lgkmcnt(0)
s_barrier
s_mov_b32 s70, 32
s_waitcnt lgkmcnt(0)
s_nop 1
s_branch .Lqa_in47
.p2align 6
.Lqa_top70:
s_waitcnt lgkmcnt(9)
v_mfma_f32_16x16x32_bf16 v[134:137], v[0:3], v[0:3], 0
s_waitcnt lgkmcnt(8)
ds_read_b128 v[54:57], v52 offset:4096
ds_read_b32 v8, v30
v_mfma_f32_16x16x32_bf16 v[134:137], v[4:7], v[4:7], v[134:137]
ds_read_b128 v[194:197], v25 offset:64
ds_read_b64 v[160:161], v22 offset:41472
v_mfma_f32_16x16x32_bf16 v[110:113], a[0:3], v[0:3], 0
s_waitcnt lgkmcnt(8)
ds_read_b128 v[58:61], v51 offset:6144
ds_read_b32 v144, v20
v_mfma_f32_16x16x32_bf16 v[134:137], v[96:99], v[96:99], v[134:137]
s_waitcnt lgkmcnt(8)
ds_read_b128 v[62:65], v52 offset:6144
v_add_u32 v46, 1, v46
v_mfma_f32_16x16x32_bf16 v[134:137], v[100:103], v[100:103], v[134:137]
s_mov_b64 exec, 65535
global_store_dwordx4 v16, v[198:201], s[38:39] sc1
s_mov_b64 exec, -1
v_mfma_f32_16x16x32_bf16 v[114:117], a[4:7], v[0:3], 0
s_waitcnt lgkmcnt(7)
ds_read_b128 v[66:69], v51 offset:8192
v_add_u32 v16, 18432, v16
v_mfma_f32_16x16x32_bf16 v[134:137], v[104:107], v[104:107], v[134:137]
s_waitcnt lgkmcnt(7)
ds_read_b128 v[70:73], v52 offset:8192
v_fma_f32 v206, v90, v26, v126
v_mfma_f32_16x16x32_bf16 v[134:137], v[54:57], v[54:57], v[134:137]
v_fma_f32 v207, v91, v26, v127
v_fma_f32 v208, v92, v26, v128
v_mfma_f32_16x16x32_bf16 v[118:121], v[0:3], a[8:11], 0
s_waitcnt lgkmcnt(4)
ds_read_b128 v[74:77], v51 offset:10240
v_fma_f32 v209, v93, v26, v129
v_mfma_f32_16x16x32_bf16 v[134:137], v[58:61], v[58:61], v[134:137]
s_waitcnt lgkmcnt(3)
ds_read_b128 v[78:81], v52 offset:10240
v_cvt_pk_bf16_f32 v150, v206, v207
v_mfma_f32_16x16x32_bf16 v[134:137], v[62:65], v[62:65], v[134:137]
v_cvt_pk_bf16_f32 v151, v208, v209
v_max_f32 v47, v186, v187
v_mfma_f32_16x16x32_bf16 v[110:113], a[12:15], v[4:7], v[110:113]
ds_read_b128 v[82:85], v51 offset:12288
v_mfma_f32_16x16x32_bf16 v[182:185], v[150:153], v[138:141], v[32:35]
s_waitcnt lgkmcnt(4)
v_max_f32 v48, v188, v189
v_fma_f32 v206, v94, v194, v130
v_mfma_f32_16x16x32_bf16 v[134:137], v[66:69], v[66:69], v[134:137]
s_xor_b32 s49, s49, 0x10000
ds_read_b128 v[162:165], v52 offset:12288
s_waitcnt lgkmcnt(4)
v_mfma_f32_16x16x32_bf16 v[134:137], v[70:73], v[70:73], v[134:137]
s_add_u32 m0, s49, 0
v_mul_f32 v95, 0x3eae1a93, v95
v_mul_f32 v9, v255, v95
v_mfma_f32_16x16x32_bf16 v[114:117], a[16:19], v[4:7], v[114:117]
s_waitcnt lgkmcnt(3)
ds_read_b128 v[166:169], v51 offset:14336
v_mul_f32 v10, v8, v95
v_mfma_f32_16x16x32_bf16 v[134:137], v[74:77], v[74:77], v[134:137]
s_waitcnt lgkmcnt(3)
ds_read_b128 v[170:173], v52 offset:14336
v_max3_f32 v47, v182, v183, v47
v_mfma_f32_16x16x32_bf16 v[134:137], v[78:81], v[78:81], v[134:137]
v_max3_f32 v48, v184, v185, v48
v_mul_f32 v47, v47, v9
v_mfma_f32_16x16x32_bf16 v[118:121], v[4:7], a[20:23], v[118:121]
s_waitcnt lgkmcnt(3)
ds_read_b128 v[174:177], v51 offset:16384
v_mul_f32 v48, v48, v10
v_mfma_f32_16x16x32_bf16 v[134:137], v[82:85], v[82:85], v[134:137]
s_waitcnt lgkmcnt(3)
ds_read_b128 v[178:181], v52 offset:16384
global_load_lds_dwordx4 v11, s[42:43] offset:-2048 sc1
v_mfma_f32_16x16x32_bf16 v[134:137], v[162:165], v[162:165], v[134:137]
v_max_f32 v49, v47, v48
v_mov_b32 v50, v49
v_mfma_f32_16x16x32_bf16 v[110:113], a[24:27], v[96:99], v[110:113]
s_waitcnt lgkmcnt(3)
v_permlane16_swap_b32 v49, v50
v_mfma_f32_16x16x32_bf16 v[134:137], v[166:169], v[166:169], v[134:137]
s_waitcnt lgkmcnt(2)
v_max_f32 v49, v49, v50
v_mov_b32 v50, v49
v_mfma_f32_16x16x32_bf16 v[134:137], v[170:173], v[170:173], v[134:137]
s_nop 0
v_permlane32_swap_b32 v49, v50
v_mfma_f32_16x16x32_bf16 v[114:117], a[28:31], v[96:99], v[114:117]
s_waitcnt lgkmcnt(1)
v_max_f32 v49, v49, v50
v_sub_f32 v47, v47, v49
v_mfma_f32_16x16x32_bf16 v[134:137], v[174:177], v[174:177], v[134:137]
s_waitcnt lgkmcnt(0)
v_sub_f32 v48, v48, v49
v_fma_f32 v207, v94, v195, v131
v_mfma_f32_16x16x32_bf16 v[134:137], v[178:181], v[178:181], v[134:137]
v_exp_f32 v210, v47
v_mfma_f32_16x16x32_bf16 v[118:121], v[96:99], a[32:35], v[118:121]
global_load_lds_dwordx4 v12, s[42:43] offset:-1024 sc1
v_fma_f32 v208, v94, v196, v132
v_mfma_f32_16x16x32_bf16 v[110:113], a[36:39], v[100:103], v[110:113]
v_exp_f32 v211, v48
v_mfma_f32_16x16x32_bf16 v[114:117], a[40:43], v[100:103], v[114:117]
v_add_f32 v49, v210, v211
v_mov_b32 v50, v49
v_mfma_f32_16x16x32_bf16 v[118:121], v[100:103], a[44:47], v[118:121]
ds_write_b128 v23, v[134:137] offset:41984
v_mfma_f32_16x16x32_bf16 v[110:113], a[48:51], v[104:107], v[110:113]
v_mul_f32 v210, v210, v255
v_fma_f32 v209, v94, v197, v133
v_mfma_f32_16x16x32_bf16 v[114:117], a[52:55], v[104:107], v[114:117]
v_permlane16_swap_b32 v49, v50
v_mfma_f32_16x16x32_bf16 v[118:121], v[104:107], a[56:59], v[118:121]
v_add_f32 v49, v49, v50
v_mov_b32 v50, v49
v_mfma_f32_16x16x32_bf16 v[110:113], a[60:63], v[54:57], v[110:113]
v_mul_f32 v211, v211, v8
v_cvt_pk_bf16_f32 v156, v206, v207
v_mfma_f32_16x16x32_bf16 v[114:117], a[64:67], v[54:57], v[114:117]
global_load_lds_dwordx4 v13, s[42:43] offset:0 sc1
v_cvt_pk_bf16_f32 v157, v208, v209
v_mfma_f32_16x16x32_bf16 v[118:121], v[54:57], a[68:71], v[118:121]
v_mfma_f32_16x16x32_bf16 v[110:113], a[72:75], v[58:61], v[110:113]
s_waitcnt lgkmcnt(0)
s_barrier
v_mfma_f32_16x16x32_bf16 v[114:117], a[76:79], v[58:61], v[114:117]
v_permlane32_swap_b32 v49, v50
v_mfma_f32_16x16x32_bf16 v[118:121], v[58:61], a[80:83], v[118:121]
v_add_f32 v49, v49, v50
v_fma_f32 v206, v86, v26, v122
v_mfma_f32_16x16x32_bf16 v[110:113], a[84:87], v[62:65], v[110:113]
v_rcp_f32 v49, v49
v_mfma_f32_16x16x32_bf16 v[114:117], a[88:91], v[62:65], v[114:117]
v_mul_f32 v210, v210, v49
v_mul_f32 v211, v211, v49
v_mfma_f32_16x16x32_bf16 v[118:121], v[62:65], a[92:95], v[118:121]
v_cvt_pk_bf16_f32 v210, v210, v210
v_cvt_pk_bf16_f32 v211, v211, v211
v_mfma_f32_16x16x32_bf16 v[110:113], a[96:99], v[66:69], v[110:113]
global_load_lds_dwordx4 v14, s[42:43] offset:1024 sc1
s_add_u32 s42, s42, s46
s_addc_u32 s43, s43, s47
v_mfma_f32_16x16x32_bf16 v[114:117], a[100:103], v[66:69], v[114:117]
v_and_b32 v192, v210, v40
v_and_b32 v193, v211, v41
v_mfma_f32_16x16x32_bf16 v[118:121], v[66:69], a[104:107], v[118:121]
v_and_b32 v190, v210, v42
v_and_b32 v191, v211, v43
v_mfma_f32_16x16x32_bf16 v[110:113], a[108:111], v[70:73], v[110:113]
s_nop 0
v_mfma_f32_16x16x32_bf16 v[186:189], v[154:157], v[190:193], 0
v_mfma_f32_16x16x32_bf16 v[182:185], v[158:161], v[190:193], 0
v_fma_f32 v207, v87, v26, v123
v_fma_f32 v208, v88, v26, v124
v_mfma_f32_16x16x32_bf16 v[114:117], a[112:115], v[70:73], v[114:117]
v_fma_f32 v209, v89, v26, v125
v_cvt_pk_bf16_f32 v142, v206, v207
v_mfma_f32_16x16x32_bf16 v[118:121], v[70:73], a[116:119], v[118:121]
v_cvt_pk_bf16_f32 v143, v208, v209
v_mfma_f32_16x16x32_bf16 v[110:113], a[120:123], v[74:77], v[110:113]
v_mfma_f32_16x16x32_bf16 v[114:117], a[124:127], v[74:77], v[114:117]
v_cvt_pk_bf16_f32 v108, v186, v187
v_cvt_pk_bf16_f32 v109, v188, v189
v_mfma_f32_16x16x32_bf16 v[118:121], v[74:77], a[128:131], v[118:121]
ds_write_b64 v18, v[108:109]
v_mfma_f32_16x16x32_bf16 v[110:113], a[132:135], v[78:81], v[110:113]
v_cvt_pk_bf16_f32 v210, v182, v183
v_mfma_f32_16x16x32_bf16 v[114:117], a[136:139], v[78:81], v[114:117]
ds_write_b32 v19, v210
v_mfma_f32_16x16x32_bf16 v[118:121], v[78:81], a[140:143], v[118:121]
v_mfma_f32_16x16x32_bf16 v[110:113], a[144:147], v[82:85], v[110:113]
v_mfma_f32_16x16x32_bf16 v[114:117], a[148:151], v[82:85], v[114:117]
v_mfma_f32_16x16x32_bf16 v[118:121], v[82:85], a[152:155], v[118:121]
v_mfma_f32_16x16x32_bf16 v[110:113], a[156:159], v[162:165], v[110:113]
v_add_lshl_u32 v253, v46, 1, 7
v_bfi_b32 v253, s69, v253, v44
v_mfma_f32_16x16x32_bf16 v[114:117], a[160:163], v[162:165], v[114:117]
global_load_dword v254, v253, s[38:39] sc1
v_mfma_f32_16x16x32_bf16 v[118:121], v[162:165], a[164:167], v[118:121]
s_waitcnt vmcnt(6)
v_sub_u32 v53, v53, v46
v_cmp_lt_u32 vcc, 0x7f, v53
v_mfma_f32_16x16x32_bf16 v[110:113], a[168:171], v[166:169], v[110:113]
v_mfma_f32_16x16x32_bf16 v[114:117], a[172:175], v[166:169], v[114:117]
v_mfma_f32_16x16x32_bf16 v[118:121], v[166:169], a[176:179], v[118:121]
s_and_b64 vcc, vcc, s[76:77]
s_cbranch_vccnz .Lqa_sp50
.Lqa_rt51:
v_mfma_f32_16x16x32_bf16 v[110:113], a[180:183], v[170:173], v[110:113]
v_mfma_f32_16x16x32_bf16 v[114:117], a[184:187], v[170:173], v[114:117]
s_waitcnt vmcnt(5) lgkmcnt(0)
s_barrier
v_mfma_f32_16x16x32_bf16 v[118:121], v[170:173], a[188:191], v[118:121]
ds_read_b128 v[0:3], v51 offset:18432
v_mfma_f32_16x16x32_bf16 v[110:113], a[192:195], v[174:177], v[110:113]
ds_read_b128 v[4:7], v52 offset:18432
ds_read_b128 v[198:201], v17 offset:0
v_mfma_f32_16x16x32_bf16 v[114:117], a[196:199], v[174:177], v[114:117]
ds_read_b32 v26, v24 offset:0
ds_read_b32 v148, v21
v_mfma_f32_16x16x32_bf16 v[186:189], v[150:153], v[142:145], v[36:39]
v_mfma_f32_16x16x32_bf16 v[118:121], v[174:177], a[200:203], v[118:121]
ds_read_b128 v[96:99], v51 offset:20480
ds_read_b32 v95, v24 offset:192
v_mfma_f32_16x16x32_bf16 v[110:113], a[204:207], v[178:181], v[110:113]
ds_read_b128 v[100:103], v52 offset:20480
ds_read_b32 v255, v27
v_mfma_f32_16x16x32_bf16 v[114:117], a[208:211], v[178:181], v[114:117]
ds_read_b32 v8, v28
ds_read_b128 v[194:197], v25 offset:0
v_mfma_f32_16x16x32_bf16 v[118:121], v[178:181], a[212:215], v[118:121]
s_waitcnt lgkmcnt(10)
ds_read_b128 v[104:107], v51 offset:22528
ds_read_b64 v[158:159], v22 offset:41472
v_mfma_f32_16x16x32_bf16 v[134:137], v[0:3], v[0:3], 0
s_waitcnt lgkmcnt(11)
ds_read_b128 v[54:57], v52 offset:22528
ds_read_b32 v140, v20
v_mfma_f32_16x16x32_bf16 v[134:137], v[4:7], v[4:7], v[134:137]
v_add_u32 v46, 1, v46
v_max_f32 v47, v186, v187
v_mfma_f32_16x16x32_bf16 v[122:125], a[0:3], v[0:3], 0
s_waitcnt lgkmcnt(11)
ds_read_b128 v[58:61], v51 offset:24576
v_fma_f32 v206, v90, v26, v114
s_waitcnt lgkmcnt(10)
v_mfma_f32_16x16x32_bf16 v[134:137], v[96:99], v[96:99], v[134:137]
s_waitcnt lgkmcnt(8)
ds_read_b128 v[62:65], v52 offset:24576
v_fma_f32 v207, v91, v26, v115
v_mfma_f32_16x16x32_bf16 v[134:137], v[100:103], v[100:103], v[134:137]
s_mov_b64 exec, 65535
global_store_dwordx4 v16, v[198:201], s[38:39] sc1
s_mov_b64 exec, -1
v_mfma_f32_16x16x32_bf16 v[126:129], a[4:7], v[0:3], 0
s_waitcnt lgkmcnt(5)
ds_read_b128 v[66:69], v51 offset:26624
v_add_u32 v16, 18432, v16
v_mfma_f32_16x16x32_bf16 v[134:137], v[104:107], v[104:107], v[134:137]
s_waitcnt lgkmcnt(4)
ds_read_b128 v[70:73], v52 offset:26624
v_fma_f32 v208, v92, v26, v116
v_mfma_f32_16x16x32_bf16 v[134:137], v[54:57], v[54:57], v[134:137]
v_fma_f32 v209, v93, v26, v117
v_cvt_pk_bf16_f32 v146, v206, v207
v_mfma_f32_16x16x32_bf16 v[130:133], v[0:3], a[8:11], 0
s_waitcnt lgkmcnt(3)
ds_read_b128 v[74:77], v51 offset:28672
v_cvt_pk_bf16_f32 v147, v208, v209
v_mfma_f32_16x16x32_bf16 v[134:137], v[58:61], v[58:61], v[134:137]
ds_read_b128 v[78:81], v52 offset:28672
v_mfma_f32_16x16x32_bf16 v[182:185], v[146:149], v[142:145], v[202:205]
s_waitcnt lgkmcnt(4)
v_max_f32 v48, v188, v189
v_fma_f32 v206, v94, v194, v118
v_mfma_f32_16x16x32_bf16 v[134:137], v[62:65], v[62:65], v[134:137]
v_fma_f32 v207, v94, v195, v119
v_fma_f32 v208, v94, v196, v120
v_mfma_f32_16x16x32_bf16 v[122:125], a[12:15], v[4:7], v[122:125]
s_waitcnt lgkmcnt(3)
ds_read_b128 v[82:85], v51 offset:30720
v_mul_f32 v95, 0x3eae1a93, v95
v_mfma_f32_16x16x32_bf16 v[134:137], v[66:69], v[66:69], v[134:137]
s_add_u32 m0, s49, 18432
ds_read_b128 v[162:165], v52 offset:30720
)"
R"(s_waitcnt lgkmcnt(4)
v_mfma_f32_16x16x32_bf16 v[134:137], v[70:73], v[70:73], v[134:137]
v_mul_f32 v9, v255, v95
v_mul_f32 v10, v8, v95
v_mfma_f32_16x16x32_bf16 v[126:129], a[16:19], v[4:7], v[126:129]
s_waitcnt lgkmcnt(3)
ds_read_b128 v[166:169], v51 offset:32768
v_max3_f32 v47, v182, v183, v47
v_mfma_f32_16x16x32_bf16 v[134:137], v[74:77], v[74:77], v[134:137]
s_waitcnt lgkmcnt(3)
ds_read_b128 v[170:173], v52 offset:32768
v_max3_f32 v48, v184, v185, v48
v_mfma_f32_16x16x32_bf16 v[134:137], v[78:81], v[78:81], v[134:137]
v_mul_f32 v47, v47, v9
v_mul_f32 v48, v48, v10
v_mfma_f32_16x16x32_bf16 v[130:133], v[4:7], a[20:23], v[130:133]
s_waitcnt lgkmcnt(3)
ds_read_b128 v[174:177], v51 offset:34816
global_load_lds_dwordx4 v11, s[42:43] offset:-2048 sc1
v_mfma_f32_16x16x32_bf16 v[134:137], v[82:85], v[82:85], v[134:137]
s_waitcnt lgkmcnt(3)
ds_read_b128 v[178:181], v52 offset:34816
v_max_f32 v49, v47, v48
v_mfma_f32_16x16x32_bf16 v[134:137], v[162:165], v[162:165], v[134:137]
v_mov_b32 v50, v49
v_fma_f32 v209, v94, v197, v121
v_mfma_f32_16x16x32_bf16 v[122:125], a[24:27], v[96:99], v[122:125]
s_waitcnt lgkmcnt(3)
v_permlane16_swap_b32 v49, v50
v_mfma_f32_16x16x32_bf16 v[134:137], v[166:169], v[166:169], v[134:137]
s_waitcnt lgkmcnt(2)
v_max_f32 v49, v49, v50
v_mov_b32 v50, v49
v_mfma_f32_16x16x32_bf16 v[134:137], v[170:173], v[170:173], v[134:137]
s_nop 0
v_permlane32_swap_b32 v49, v50
v_mfma_f32_16x16x32_bf16 v[126:129], a[28:31], v[96:99], v[126:129]
s_waitcnt lgkmcnt(1)
v_max_f32 v49, v49, v50
v_sub_f32 v47, v47, v49
v_mfma_f32_16x16x32_bf16 v[134:137], v[174:177], v[174:177], v[134:137]
s_waitcnt lgkmcnt(0)
v_sub_f32 v48, v48, v49
v_cvt_pk_bf16_f32 v154, v206, v207
v_mfma_f32_16x16x32_bf16 v[134:137], v[178:181], v[178:181], v[134:137]
global_load_lds_dwordx4 v12, s[42:43] offset:-1024 sc1
v_cvt_pk_bf16_f32 v155, v208, v209
v_mfma_f32_16x16x32_bf16 v[130:133], v[96:99], a[32:35], v[130:133]
v_exp_f32 v210, v47
v_mfma_f32_16x16x32_bf16 v[122:125], a[36:39], v[100:103], v[122:125]
v_exp_f32 v211, v48
v_mfma_f32_16x16x32_bf16 v[126:129], a[40:43], v[100:103], v[126:129]
v_add_f32 v49, v210, v211
v_mov_b32 v50, v49
v_mfma_f32_16x16x32_bf16 v[130:133], v[100:103], a[44:47], v[130:133]
ds_write_b128 v23, v[134:137] offset:41984
v_mfma_f32_16x16x32_bf16 v[122:125], a[48:51], v[104:107], v[122:125]
v_mul_f32 v210, v210, v255
v_fma_f32 v206, v86, v26, v110
v_mfma_f32_16x16x32_bf16 v[126:129], a[52:55], v[104:107], v[126:129]
v_permlane16_swap_b32 v49, v50
v_mfma_f32_16x16x32_bf16 v[130:133], v[104:107], a[56:59], v[130:133]
v_add_f32 v49, v49, v50
v_mov_b32 v50, v49
v_mfma_f32_16x16x32_bf16 v[122:125], a[60:63], v[54:57], v[122:125]
global_load_lds_dwordx4 v13, s[42:43] offset:0 sc1
v_mul_f32 v211, v211, v8
v_mfma_f32_16x16x32_bf16 v[126:129], a[64:67], v[54:57], v[126:129]
v_permlane32_swap_b32 v49, v50
v_mfma_f32_16x16x32_bf16 v[130:133], v[54:57], a[68:71], v[130:133]
v_mfma_f32_16x16x32_bf16 v[122:125], a[72:75], v[58:61], v[122:125]
s_waitcnt lgkmcnt(0)
s_barrier
v_mfma_f32_16x16x32_bf16 v[126:129], a[76:79], v[58:61], v[126:129]
v_add_f32 v49, v49, v50
v_fma_f32 v207, v87, v26, v111
v_mfma_f32_16x16x32_bf16 v[130:133], v[58:61], a[80:83], v[130:133]
v_rcp_f32 v49, v49
v_mfma_f32_16x16x32_bf16 v[122:125], a[84:87], v[62:65], v[122:125]
v_mul_f32 v210, v210, v49
v_mul_f32 v211, v211, v49
v_mfma_f32_16x16x32_bf16 v[126:129], a[88:91], v[62:65], v[126:129]
v_cvt_pk_bf16_f32 v210, v210, v210
v_cvt_pk_bf16_f32 v211, v211, v211
v_mfma_f32_16x16x32_bf16 v[130:133], v[62:65], a[92:95], v[130:133]
global_load_lds_dwordx4 v14, s[42:43] offset:1024 sc1
s_sub_u32 s42, s42, 18432
s_subb_u32 s43, s43, 0
v_mfma_f32_16x16x32_bf16 v[122:125], a[96:99], v[66:69], v[122:125]
v_and_b32 v190, v210, v40
v_and_b32 v191, v211, v41
v_mfma_f32_16x16x32_bf16 v[126:129], a[100:103], v[66:69], v[126:129]
v_and_b32 v192, v210, v42
v_and_b32 v193, v211, v43
v_mfma_f32_16x16x32_bf16 v[130:133], v[66:69], a[104:107], v[130:133]
s_nop 0
v_mfma_f32_16x16x32_bf16 v[186:189], v[154:157], v[190:193], 0
v_mfma_f32_16x16x32_bf16 v[182:185], v[158:161], v[190:193], 0
v_fma_f32 v208, v88, v26, v112
v_fma_f32 v209, v89, v26, v113
v_mfma_f32_16x16x32_bf16 v[122:125], a[108:111], v[70:73], v[122:125]
v_cvt_pk_bf16_f32 v138, v206, v207
v_cvt_pk_bf16_f32 v139, v208, v209
v_mfma_f32_16x16x32_bf16 v[126:129], a[112:115], v[70:73], v[126:129]
v_mfma_f32_16x16x32_bf16 v[130:133], v[70:73], a[116:119], v[130:133]
v_mfma_f32_16x16x32_bf16 v[122:125], a[120:123], v[74:77], v[122:125]
v_cvt_pk_bf16_f32 v108, v186, v187
v_cvt_pk_bf16_f32 v109, v188, v189
v_mfma_f32_16x16x32_bf16 v[126:129], a[124:127], v[74:77], v[126:129]
ds_write_b64 v18, v[108:109]
v_mfma_f32_16x16x32_bf16 v[130:133], v[74:77], a[128:131], v[130:133]
v_cvt_pk_bf16_f32 v210, v182, v183
v_mfma_f32_16x16x32_bf16 v[122:125], a[132:135], v[78:81], v[122:125]
ds_write_b32 v19, v210
v_mfma_f32_16x16x32_bf16 v[126:129], a[136:139], v[78:81], v[126:129]
v_mfma_f32_16x16x32_bf16 v[130:133], v[78:81], a[140:143], v[130:133]
v_mfma_f32_16x16x32_bf16 v[122:125], a[144:147], v[82:85], v[122:125]
v_mfma_f32_16x16x32_bf16 v[126:129], a[148:151], v[82:85], v[126:129]
v_mfma_f32_16x16x32_bf16 v[130:133], v[82:85], a[152:155], v[130:133]
v_mfma_f32_16x16x32_bf16 v[122:125], a[156:159], v[162:165], v[122:125]
v_add_lshl_u32 v253, v46, 1, 7
v_bfi_b32 v253, s69, v253, v45
v_mfma_f32_16x16x32_bf16 v[126:129], a[160:163], v[162:165], v[126:129]
global_load_dword v53, v253, s[38:39] sc1
v_mfma_f32_16x16x32_bf16 v[130:133], v[162:165], a[164:167], v[130:133]
s_waitcnt vmcnt(6)
v_sub_u32 v254, v254, v46
v_cmp_lt_u32 vcc, 0x7f, v254
v_mfma_f32_16x16x32_bf16 v[122:125], a[168:171], v[166:169], v[122:125]
v_mfma_f32_16x16x32_bf16 v[126:129], a[172:175], v[166:169], v[126:129]
v_xor_b32 v51, 0x10000, v51
v_xor_b32 v52, 0x10000, v52
v_mfma_f32_16x16x32_bf16 v[130:133], v[166:169], a[176:179], v[130:133]
s_and_b64 vcc, vcc, s[76:77]
s_cbranch_vccnz .Lqa_sp52
.Lqa_rt53:
v_mfma_f32_16x16x32_bf16 v[122:125], a[180:183], v[170:173], v[122:125]
v_mfma_f32_16x16x32_bf16 v[126:129], a[184:187], v[170:173], v[126:129]
s_waitcnt vmcnt(5) lgkmcnt(0)
s_barrier
.Lqa_in47:
v_mfma_f32_16x16x32_bf16 v[130:133], v[170:173], a[188:191], v[130:133]
s_cmp_eq_u32 s70, s73
s_cbranch_scc1 .Lqa_hk48
.Lqa_hr49:
ds_read_b128 v[0:3], v51 offset:0
v_mfma_f32_16x16x32_bf16 v[122:125], a[192:195], v[174:177], v[122:125]
ds_read_b128 v[4:7], v52 offset:0
ds_read_b128 v[198:201], v17 offset:0
v_mfma_f32_16x16x32_bf16 v[126:129], a[196:199], v[174:177], v[126:129]
ds_read_b32 v26, v24 offset:64
ds_read_b32 v152, v21
v_mfma_f32_16x16x32_bf16 v[186:189], v[146:149], v[138:141], v[36:39]
v_mfma_f32_16x16x32_bf16 v[130:133], v[174:177], a[200:203], v[130:133]
ds_read_b128 v[96:99], v51 offset:2048
ds_read_b32 v95, v24 offset:128
v_mfma_f32_16x16x32_bf16 v[122:125], a[204:207], v[178:181], v[122:125]
ds_read_b128 v[100:103], v52 offset:2048
ds_read_b32 v255, v29
v_mfma_f32_16x16x32_bf16 v[126:129], a[208:211], v[178:181], v[126:129]
v_mfma_f32_16x16x32_bf16 v[130:133], v[178:181], a[212:215], v[130:133]
s_sub_u32 s70, s70, 1
ds_read_b128 v[104:107], v51 offset:4096
s_cbranch_scc0 .Lqa_top70
s_waitcnt lgkmcnt(0)
ds_read_b32 v8, v30
ds_read_b128 v[194:197], v25 offset:64
ds_read_b64 v[160:161], v22 offset:41472
ds_read_b32 v144, v20
s_mov_b64 exec, 65535
global_store_dwordx4 v16, v[198:201], s[38:39] sc1
s_mov_b64 exec, -1
v_add_u32 v16, 18432, v16
v_fma_f32 v206, v90, v26, v126
v_fma_f32 v207, v91, v26, v127
v_fma_f32 v208, v92, v26, v128
v_fma_f32 v209, v93, v26, v129
v_cvt_pk_bf16_f32 v150, v206, v207
v_cvt_pk_bf16_f32 v151, v208, v209
v_max_f32 v47, v186, v187
s_nop 0
v_mfma_f32_16x16x32_bf16 v[182:185], v[150:153], v[138:141], v[32:35]
s_waitcnt lgkmcnt(2)
v_max_f32 v48, v188, v189
v_fma_f32 v206, v94, v194, v130
v_mul_f32 v95, 0x3eae1a93, v95
v_mul_f32 v9, v255, v95
v_mul_f32 v10, v8, v95
s_nop 1
v_max3_f32 v47, v182, v183, v47
v_max3_f32 v48, v184, v185, v48
v_mul_f32 v47, v47, v9
v_mul_f32 v48, v48, v10
v_max_f32 v49, v47, v48
v_mov_b32 v50, v49
s_nop 1
v_permlane16_swap_b32 v49, v50
v_max_f32 v49, v49, v50
v_mov_b32 v50, v49
s_nop 1
v_permlane32_swap_b32 v49, v50
v_max_f32 v49, v49, v50
v_sub_f32 v47, v47, v49
v_sub_f32 v48, v48, v49
v_fma_f32 v207, v94, v195, v131
v_exp_f32 v210, v47
v_fma_f32 v208, v94, v196, v132
v_exp_f32 v211, v48
s_nop 0
v_add_f32 v49, v210, v211
v_mov_b32 v50, v49
v_mul_f32 v210, v210, v255
v_fma_f32 v209, v94, v197, v133
v_permlane16_swap_b32 v49, v50
v_add_f32 v49, v49, v50
v_mov_b32 v50, v49
v_mul_f32 v211, v211, v8
v_cvt_pk_bf16_f32 v156, v206, v207
v_cvt_pk_bf16_f32 v157, v208, v209
s_waitcnt lgkmcnt(0)
s_barrier
v_permlane32_swap_b32 v49, v50
v_add_f32 v49, v49, v50
v_fma_f32 v206, v86, v26, v122
v_rcp_f32 v49, v49
s_nop 0
v_mul_f32 v210, v210, v49
v_mul_f32 v211, v211, v49
v_cvt_pk_bf16_f32 v210, v210, v210
v_cvt_pk_bf16_f32 v211, v211, v211
v_and_b32 v192, v210, v40
v_and_b32 v193, v211, v41
v_and_b32 v190, v210, v42
v_and_b32 v191, v211, v43
s_nop 1
v_mfma_f32_16x16x32_bf16 v[186:189], v[154:157], v[190:193], 0
v_mfma_f32_16x16x32_bf16 v[182:185], v[158:161], v[190:193], 0
v_fma_f32 v207, v87, v26, v123
v_fma_f32 v208, v88, v26, v124
v_fma_f32 v209, v89, v26, v125
v_cvt_pk_bf16_f32 v142, v206, v207
v_cvt_pk_bf16_f32 v143, v208, v209
s_nop 1
v_cvt_pk_bf16_f32 v108, v186, v187
v_cvt_pk_bf16_f32 v109, v188, v189
ds_write_b64 v18, v[108:109]
v_cvt_pk_bf16_f32 v210, v182, v183
ds_write_b32 v19, v210
s_waitcnt vmcnt(0)
s_waitcnt lgkmcnt(0)
s_barrier
ds_read_b128 v[198:201], v17 offset:0
s_waitcnt lgkmcnt(0)
global_store_dwordx4 v16, v[198:201], s[38:39] sc1
s_waitcnt vmcnt(0)
s_barrier
s_endpgm
.Lqa_w4:
s_getpc_b64 s[90:91]
.Lqa_pc90:
s_add_u32 s90, s90, .Lqa_e75-.Lqa_pc90
s_addc_u32 s91, s91, 0
s_branch .Lqa_k82
.Lqa_bk89:
v_add_u32 v255, 0x10000, v9
v_add_u32 v162, 0x20000, v9
v_add_u32 v163, 0x10000, v10
v_add_u32 v164, 0x20000, v10
v_mov_b32 v106, 0x3F803F80
v_mov_b32 v107, 0x3F803F80
v_mov_b32 v108, 0x3F803F80
v_mov_b32 v109, 0x3F803F80
v_mov_b32 v141, 0
v_mov_b32 v145, 0
v_mov_b32 v149, 0
v_mov_b32 v153, 0
v_mov_b32 v165, 46080
s_waitcnt lgkmcnt(0)
s_mul_i32 s37, s37, 0x9E3779B1
s_add_u32 s37, s37, 0x7F4A7C15
v_add_u32 v46, s37, v46
v_add_u32 v48, s37, v48
v_add_u32 v50, s37, v50
ds_write_b32 v165, v141
s_mov_b64 s[76:77], 0
s_mov_b32 s73, 18
s_add_u32 m0, s48, 4096
s_nop 0
global_load_lds_dwordx4 v0, s[40:41] offset:-4096
global_load_lds_dwordx4 v1, s[40:41] offset:-3072
global_load_lds_dwordx4 v2, s[40:41] offset:-2048
global_load_lds_dwordx4 v3, s[40:41] offset:-1024
global_load_lds_dwordx4 v4, s[40:41] offset:0
global_load_lds_dwordx4 v5, s[40:41] offset:1024
global_load_lds_dwordx4 v6, s[40:41] offset:2048
global_load_lds_dwordx4 v7, s[40:41] offset:3072
s_add_u32 m0, s48, 12288
s_nop 0
global_load_lds_dwordx4 v8, s[40:41] offset:-4096
s_add_u32 s40, s40, 221184
s_addc_u32 s41, s41, 0
s_add_u32 m0, s48, 40960
s_nop 0
global_load_lds_dwordx4 v0, s[40:41] offset:-4096
global_load_lds_dwordx4 v1, s[40:41] offset:-3072
global_load_lds_dwordx4 v2, s[40:41] offset:-2048
global_load_lds_dwordx4 v3, s[40:41] offset:-1024
global_load_lds_dwordx4 v4, s[40:41] offset:0
global_load_lds_dwordx4 v5, s[40:41] offset:1024
global_load_lds_dwordx4 v6, s[40:41] offset:2048
global_load_lds_dwordx4 v7, s[40:41] offset:3072
s_add_u32 m0, s48, 49152
s_nop 0
global_load_lds_dwordx4 v8, s[40:41] offset:-4096
s_add_u32 s40, s40, 221184
s_addc_u32 s41, s41, 0
s_waitcnt vmcnt(9) lgkmcnt(0)
s_barrier
s_add_u32 m0, s48, 77824
ds_read_b64_tr_b16 a[0:1], v9 offset:47104
ds_read_b64_tr_b16 a[2:3], v9 offset:49408
global_load_lds_dwordx4 v0, s[40:41] offset:-4096
ds_read_b64_tr_b16 a[4:5], v9 offset:47296
global_load_lds_dwordx4 v1, s[40:41] offset:-3072
ds_read_b64_tr_b16 a[6:7], v9 offset:49600
global_load_lds_dwordx4 v2, s[40:41] offset:-2048
ds_read_b64_tr_b16 a[8:9], v9 offset:47488
global_load_lds_dwordx4 v3, s[40:41] offset:-1024
ds_read_b64_tr_b16 a[10:11], v9 offset:49792
ds_read_b128 v[166:169], v51 offset:0
global_load_lds_dwordx4 v4, s[40:41] offset:0
ds_read_b128 v[174:177], v51 offset:18432
global_load_lds_dwordx4 v5, s[40:41] offset:1024
ds_read_b64_tr_b16 a[12:13], v255 offset:0
global_load_lds_dwordx4 v6, s[40:41] offset:2048
ds_read_b64_tr_b16 a[14:15], v255 offset:2304
global_load_lds_dwordx4 v7, s[40:41] offset:3072
ds_read_b64_tr_b16 a[16:17], v255 offset:192
ds_read_b64_tr_b16 a[18:19], v255 offset:2496
s_add_u32 m0, s48, 86016
ds_read_b64_tr_b16 a[20:21], v255 offset:384
global_load_lds_dwordx4 v8, s[40:41] offset:-4096
ds_read_b64_tr_b16 a[22:23], v255 offset:2688
s_add_u32 s40, s40, 221184
ds_read_b128 v[170:173], v52 offset:0
s_addc_u32 s41, s41, 0
ds_read_b128 v[178:181], v52 offset:18432
s_waitcnt vmcnt(9) lgkmcnt(0)
s_barrier
s_add_u32 m0, s48, 4096
ds_read_b64_tr_b16 a[24:25], v255 offset:18432
v_mfma_f32_16x16x32_bf16 v[86:89], a[0:3], v[106:109], 0
v_mfma_f32_16x16x32_bf16 v[90:93], a[4:7], v[106:109], 0
global_load_lds_dwordx4 v0, s[40:41] offset:-4096
ds_read_b64_tr_b16 a[26:27], v255 offset:20736
v_mfma_f32_16x16x32_bf16 v[94:97], v[106:109], a[8:11], 0
ds_read_b64_tr_b16 a[28:29], v255 offset:18624
global_load_lds_dwordx4 v1, s[40:41] offset:-3072
v_mfma_f32_16x16x32_bf16 v[110:113], a[0:3], v[166:169], 0
ds_read_b64_tr_b16 a[30:31], v255 offset:20928
v_mfma_f32_16x16x32_bf16 v[114:117], a[4:7], v[166:169], 0
global_load_lds_dwordx4 v2, s[40:41] offset:-2048
ds_read_b64_tr_b16 a[32:33], v255 offset:18816
v_mfma_f32_16x16x32_bf16 v[118:121], v[166:169], a[8:11], 0
ds_read_b64_tr_b16 a[34:35], v255 offset:21120
global_load_lds_dwordx4 v3, s[40:41] offset:-1024
v_mfma_f32_16x16x32_bf16 v[134:137], v[106:109], v[166:169], 0
ds_read_b128 v[182:185], v51 offset:2048
v_mfma_f32_16x16x32_bf16 v[122:125], a[0:3], v[174:177], 0
global_load_lds_dwordx4 v4, s[40:41] offset:0
ds_read_b128 v[190:193], v51 offset:20480
v_mfma_f32_16x16x32_bf16 v[126:129], a[4:7], v[174:177], 0
ds_read_b64_tr_b16 a[36:37], v255 offset:36864
global_load_lds_dwordx4 v5, s[40:41] offset:1024
v_mfma_f32_16x16x32_bf16 v[130:133], v[174:177], a[8:11], 0
v_mfma_f32_16x16x32_bf16 v[158:161], v[106:109], v[174:177], 0
ds_read_b64_tr_b16 a[38:39], v255 offset:39168
global_load_lds_dwordx4 v6, s[40:41] offset:2048
v_mfma_f32_16x16x32_bf16 v[86:89], a[12:15], v[106:109], v[86:89]
ds_read_b64_tr_b16 a[40:41], v255 offset:37056
v_mfma_f32_16x16x32_bf16 v[90:93], a[16:19], v[106:109], v[90:93]
global_load_lds_dwordx4 v7, s[40:41] offset:3072
ds_read_b64_tr_b16 a[42:43], v255 offset:39360
v_mfma_f32_16x16x32_bf16 v[94:97], v[106:109], a[20:23], v[94:97]
s_add_u32 m0, s48, 12288
ds_read_b64_tr_b16 a[44:45], v255 offset:37248
v_mfma_f32_16x16x32_bf16 v[110:113], a[12:15], v[170:173], v[110:113]
ds_read_b64_tr_b16 a[46:47], v255 offset:39552
v_mfma_f32_16x16x32_bf16 v[114:117], a[16:19], v[170:173], v[114:117]
global_load_lds_dwordx4 v8, s[40:41] offset:-4096
ds_read_b128 v[186:189], v52 offset:2048
v_mfma_f32_16x16x32_bf16 v[118:121], v[170:173], a[20:23], v[118:121]
s_add_u32 s40, s40, 221184
ds_read_b128 v[194:197], v52 offset:20480
v_mfma_f32_16x16x32_bf16 v[134:137], v[106:109], v[170:173], v[134:137]
v_mfma_f32_16x16x32_bf16 v[122:125], a[12:15], v[178:181], v[122:125]
v_mfma_f32_16x16x32_bf16 v[126:129], a[16:19], v[178:181], v[126:129]
s_addc_u32 s41, s41, 0
v_mfma_f32_16x16x32_bf16 v[130:133], v[178:181], a[20:23], v[130:133]
v_mfma_f32_16x16x32_bf16 v[158:161], v[106:109], v[178:181], v[158:161]
)"
R"(s_waitcnt vmcnt(9) lgkmcnt(0)
s_barrier
s_add_u32 m0, s48, 40960
ds_read_b64_tr_b16 a[48:49], v255 offset:55296
v_mfma_f32_16x16x32_bf16 v[86:89], a[24:27], v[106:109], v[86:89]
v_mfma_f32_16x16x32_bf16 v[90:93], a[28:31], v[106:109], v[90:93]
global_load_lds_dwordx4 v0, s[40:41] offset:-4096
ds_read_b64_tr_b16 a[50:51], v255 offset:57600
v_mfma_f32_16x16x32_bf16 v[94:97], v[106:109], a[32:35], v[94:97]
ds_read_b64_tr_b16 a[52:53], v255 offset:55488
global_load_lds_dwordx4 v1, s[40:41] offset:-3072
v_mfma_f32_16x16x32_bf16 v[110:113], a[24:27], v[182:185], v[110:113]
ds_read_b64_tr_b16 a[54:55], v255 offset:57792
v_mfma_f32_16x16x32_bf16 v[114:117], a[28:31], v[182:185], v[114:117]
global_load_lds_dwordx4 v2, s[40:41] offset:-2048
ds_read_b64_tr_b16 a[56:57], v255 offset:55680
v_mfma_f32_16x16x32_bf16 v[118:121], v[182:185], a[32:35], v[118:121]
ds_read_b64_tr_b16 a[58:59], v255 offset:57984
global_load_lds_dwordx4 v3, s[40:41] offset:-1024
v_mfma_f32_16x16x32_bf16 v[134:137], v[106:109], v[182:185], v[134:137]
ds_read_b128 v[166:169], v51 offset:4096
v_mfma_f32_16x16x32_bf16 v[122:125], a[24:27], v[190:193], v[122:125]
global_load_lds_dwordx4 v4, s[40:41] offset:0
ds_read_b128 v[174:177], v51 offset:22528
v_mfma_f32_16x16x32_bf16 v[126:129], a[28:31], v[190:193], v[126:129]
ds_read_b64_tr_b16 a[60:61], v162 offset:8192
global_load_lds_dwordx4 v5, s[40:41] offset:1024
v_mfma_f32_16x16x32_bf16 v[130:133], v[190:193], a[32:35], v[130:133]
v_mfma_f32_16x16x32_bf16 v[158:161], v[106:109], v[190:193], v[158:161]
ds_read_b64_tr_b16 a[62:63], v162 offset:10496
global_load_lds_dwordx4 v6, s[40:41] offset:2048
v_mfma_f32_16x16x32_bf16 v[86:89], a[36:39], v[106:109], v[86:89]
ds_read_b64_tr_b16 a[64:65], v162 offset:8384
v_mfma_f32_16x16x32_bf16 v[90:93], a[40:43], v[106:109], v[90:93]
global_load_lds_dwordx4 v7, s[40:41] offset:3072
ds_read_b64_tr_b16 a[66:67], v162 offset:10688
v_mfma_f32_16x16x32_bf16 v[94:97], v[106:109], a[44:47], v[94:97]
s_add_u32 m0, s48, 49152
ds_read_b64_tr_b16 a[68:69], v162 offset:8576
v_mfma_f32_16x16x32_bf16 v[110:113], a[36:39], v[186:189], v[110:113]
ds_read_b64_tr_b16 a[70:71], v162 offset:10880
v_mfma_f32_16x16x32_bf16 v[114:117], a[40:43], v[186:189], v[114:117]
global_load_lds_dwordx4 v8, s[40:41] offset:-4096
ds_read_b128 v[170:173], v52 offset:4096
v_mfma_f32_16x16x32_bf16 v[118:121], v[186:189], a[44:47], v[118:121]
s_add_u32 s40, s40, 221184
ds_read_b128 v[178:181], v52 offset:22528
v_mfma_f32_16x16x32_bf16 v[134:137], v[106:109], v[186:189], v[134:137]
v_mfma_f32_16x16x32_bf16 v[122:125], a[36:39], v[194:197], v[122:125]
v_mfma_f32_16x16x32_bf16 v[126:129], a[40:43], v[194:197], v[126:129]
s_addc_u32 s41, s41, 0
v_mfma_f32_16x16x32_bf16 v[130:133], v[194:197], a[44:47], v[130:133]
v_mfma_f32_16x16x32_bf16 v[158:161], v[106:109], v[194:197], v[158:161]
s_waitcnt vmcnt(9) lgkmcnt(0)
s_barrier
s_add_u32 m0, s48, 77824
ds_read_b64_tr_b16 a[72:73], v9 offset:47104
v_mfma_f32_16x16x32_bf16 v[86:89], a[48:51], v[106:109], v[86:89]
v_mfma_f32_16x16x32_bf16 v[90:93], a[52:55], v[106:109], v[90:93]
global_load_lds_dwordx4 v0, s[40:41] offset:-4096
ds_read_b64_tr_b16 a[74:75], v9 offset:49408
v_mfma_f32_16x16x32_bf16 v[94:97], v[106:109], a[56:59], v[94:97]
ds_read_b64_tr_b16 a[76:77], v9 offset:47296
global_load_lds_dwordx4 v1, s[40:41] offset:-3072
v_mfma_f32_16x16x32_bf16 v[110:113], a[48:51], v[166:169], v[110:113]
ds_read_b64_tr_b16 a[78:79], v9 offset:49600
v_mfma_f32_16x16x32_bf16 v[114:117], a[52:55], v[166:169], v[114:117]
global_load_lds_dwordx4 v2, s[40:41] offset:-2048
ds_read_b64_tr_b16 a[80:81], v9 offset:47488
v_mfma_f32_16x16x32_bf16 v[118:121], v[166:169], a[56:59], v[118:121]
ds_read_b64_tr_b16 a[82:83], v9 offset:49792
global_load_lds_dwordx4 v3, s[40:41] offset:-1024
v_mfma_f32_16x16x32_bf16 v[134:137], v[106:109], v[166:169], v[134:137]
ds_read_b128 v[182:185], v51 offset:6144
v_mfma_f32_16x16x32_bf16 v[122:125], a[48:51], v[174:177], v[122:125]
global_load_lds_dwordx4 v4, s[40:41] offset:0
ds_read_b128 v[190:193], v51 offset:24576
v_mfma_f32_16x16x32_bf16 v[126:129], a[52:55], v[174:177], v[126:129]
ds_read_b64_tr_b16 a[84:85], v255 offset:0
global_load_lds_dwordx4 v5, s[40:41] offset:1024
v_mfma_f32_16x16x32_bf16 v[130:133], v[174:177], a[56:59], v[130:133]
v_mfma_f32_16x16x32_bf16 v[158:161], v[106:109], v[174:177], v[158:161]
ds_read_b64_tr_b16 a[86:87], v255 offset:2304
global_load_lds_dwordx4 v6, s[40:41] offset:2048
v_mfma_f32_16x16x32_bf16 v[86:89], a[60:63], v[106:109], v[86:89]
ds_read_b64_tr_b16 a[88:89], v255 offset:192
v_mfma_f32_16x16x32_bf16 v[90:93], a[64:67], v[106:109], v[90:93]
global_load_lds_dwordx4 v7, s[40:41] offset:3072
ds_read_b64_tr_b16 a[90:91], v255 offset:2496
v_mfma_f32_16x16x32_bf16 v[94:97], v[106:109], a[68:71], v[94:97]
s_add_u32 m0, s48, 86016
ds_read_b64_tr_b16 a[92:93], v255 offset:384
v_mfma_f32_16x16x32_bf16 v[110:113], a[60:63], v[170:173], v[110:113]
ds_read_b64_tr_b16 a[94:95], v255 offset:2688
v_mfma_f32_16x16x32_bf16 v[114:117], a[64:67], v[170:173], v[114:117]
global_load_lds_dwordx4 v8, s[40:41] offset:-4096
ds_read_b128 v[186:189], v52 offset:6144
v_mfma_f32_16x16x32_bf16 v[118:121], v[170:173], a[68:71], v[118:121]
s_add_u32 s40, s40, 221184
ds_read_b128 v[194:197], v52 offset:24576
v_mfma_f32_16x16x32_bf16 v[134:137], v[106:109], v[170:173], v[134:137]
v_mfma_f32_16x16x32_bf16 v[122:125], a[60:63], v[178:181], v[122:125]
v_mfma_f32_16x16x32_bf16 v[126:129], a[64:67], v[178:181], v[126:129]
s_addc_u32 s41, s41, 0
v_mfma_f32_16x16x32_bf16 v[130:133], v[178:181], a[68:71], v[130:133]
v_mfma_f32_16x16x32_bf16 v[158:161], v[106:109], v[178:181], v[158:161]
s_waitcnt vmcnt(9) lgkmcnt(0)
s_barrier
s_add_u32 m0, s48, 4096
ds_read_b64_tr_b16 a[96:97], v255 offset:18432
v_mfma_f32_16x16x32_bf16 v[86:89], a[72:75], v[106:109], v[86:89]
v_mfma_f32_16x16x32_bf16 v[90:93], a[76:79], v[106:109], v[90:93]
global_load_lds_dwordx4 v0, s[40:41] offset:-4096
ds_read_b64_tr_b16 a[98:99], v255 offset:20736
v_mfma_f32_16x16x32_bf16 v[94:97], v[106:109], a[80:83], v[94:97]
ds_read_b64_tr_b16 a[100:101], v255 offset:18624
global_load_lds_dwordx4 v1, s[40:41] offset:-3072
v_mfma_f32_16x16x32_bf16 v[110:113], a[72:75], v[182:185], v[110:113]
ds_read_b64_tr_b16 a[102:103], v255 offset:20928
v_mfma_f32_16x16x32_bf16 v[114:117], a[76:79], v[182:185], v[114:117]
global_load_lds_dwordx4 v2, s[40:41] offset:-2048
ds_read_b64_tr_b16 a[104:105], v255 offset:18816
v_mfma_f32_16x16x32_bf16 v[118:121], v[182:185], a[80:83], v[118:121]
ds_read_b64_tr_b16 a[106:107], v255 offset:21120
global_load_lds_dwordx4 v3, s[40:41] offset:-1024
v_mfma_f32_16x16x32_bf16 v[134:137], v[106:109], v[182:185], v[134:137]
ds_read_b128 v[166:169], v51 offset:8192
v_mfma_f32_16x16x32_bf16 v[122:125], a[72:75], v[190:193], v[122:125]
global_load_lds_dwordx4 v4, s[40:41] offset:0
ds_read_b128 v[174:177], v51 offset:26624
v_mfma_f32_16x16x32_bf16 v[126:129], a[76:79], v[190:193], v[126:129]
ds_read_b64_tr_b16 a[108:109], v255 offset:36864
global_load_lds_dwordx4 v5, s[40:41] offset:1024
v_mfma_f32_16x16x32_bf16 v[130:133], v[190:193], a[80:83], v[130:133]
v_mfma_f32_16x16x32_bf16 v[158:161], v[106:109], v[190:193], v[158:161]
ds_read_b64_tr_b16 a[110:111], v255 offset:39168
global_load_lds_dwordx4 v6, s[40:41] offset:2048
v_mfma_f32_16x16x32_bf16 v[86:89], a[84:87], v[106:109], v[86:89]
ds_read_b64_tr_b16 a[112:113], v255 offset:37056
v_mfma_f32_16x16x32_bf16 v[90:93], a[88:91], v[106:109], v[90:93]
global_load_lds_dwordx4 v7, s[40:41] offset:3072
ds_read_b64_tr_b16 a[114:115], v255 offset:39360
v_mfma_f32_16x16x32_bf16 v[94:97], v[106:109], a[92:95], v[94:97]
s_add_u32 m0, s48, 12288
ds_read_b64_tr_b16 a[116:117], v255 offset:37248
v_mfma_f32_16x16x32_bf16 v[110:113], a[84:87], v[186:189], v[110:113]
ds_read_b64_tr_b16 a[118:119], v255 offset:39552
v_mfma_f32_16x16x32_bf16 v[114:117], a[88:91], v[186:189], v[114:117]
global_load_lds_dwordx4 v8, s[40:41] offset:-4096
ds_read_b128 v[170:173], v52 offset:8192
v_mfma_f32_16x16x32_bf16 v[118:121], v[186:189], a[92:95], v[118:121]
s_add_u32 s40, s40, 221184
ds_read_b128 v[178:181], v52 offset:26624
v_mfma_f32_16x16x32_bf16 v[134:137], v[106:109], v[186:189], v[134:137]
v_mfma_f32_16x16x32_bf16 v[122:125], a[84:87], v[194:197], v[122:125]
v_mfma_f32_16x16x32_bf16 v[126:129], a[88:91], v[194:197], v[126:129]
s_addc_u32 s41, s41, 0
v_mfma_f32_16x16x32_bf16 v[130:133], v[194:197], a[92:95], v[130:133]
v_mfma_f32_16x16x32_bf16 v[158:161], v[106:109], v[194:197], v[158:161]
s_waitcnt vmcnt(9) lgkmcnt(0)
s_barrier
s_add_u32 m0, s48, 40960
ds_read_b64_tr_b16 a[120:121], v255 offset:55296
v_mfma_f32_16x16x32_bf16 v[86:89], a[96:99], v[106:109], v[86:89]
v_mfma_f32_16x16x32_bf16 v[90:93], a[100:103], v[106:109], v[90:93]
global_load_lds_dwordx4 v0, s[40:41] offset:-4096
ds_read_b64_tr_b16 a[122:123], v255 offset:57600
v_mfma_f32_16x16x32_bf16 v[94:97], v[106:109], a[104:107], v[94:97]
ds_read_b64_tr_b16 a[124:125], v255 offset:55488
global_load_lds_dwordx4 v1, s[40:41] offset:-3072
v_mfma_f32_16x16x32_bf16 v[110:113], a[96:99], v[166:169], v[110:113]
ds_read_b64_tr_b16 a[126:127], v255 offset:57792
v_mfma_f32_16x16x32_bf16 v[114:117], a[100:103], v[166:169], v[114:117]
global_load_lds_dwordx4 v2, s[40:41] offset:-2048
ds_read_b64_tr_b16 a[128:129], v255 offset:55680
v_mfma_f32_16x16x32_bf16 v[118:121], v[166:169], a[104:107], v[118:121]
ds_read_b64_tr_b16 a[130:131], v255 offset:57984
global_load_lds_dwordx4 v3, s[40:41] offset:-1024
v_mfma_f32_16x16x32_bf16 v[134:137], v[106:109], v[166:169], v[134:137]
ds_read_b128 v[182:185], v51 offset:10240
v_mfma_f32_16x16x32_bf16 v[122:125], a[96:99], v[174:177], v[122:125]
global_load_lds_dwordx4 v4, s[40:41] offset:0
ds_read_b128 v[190:193], v51 offset:28672
v_mfma_f32_16x16x32_bf16 v[126:129], a[100:103], v[174:177], v[126:129]
ds_read_b64_tr_b16 a[132:133], v162 offset:8192
global_load_lds_dwordx4 v5, s[40:41] offset:1024
v_mfma_f32_16x16x32_bf16 v[130:133], v[174:177], a[104:107], v[130:133]
v_mfma_f32_16x16x32_bf16 v[158:161], v[106:109], v[174:177], v[158:161]
ds_read_b64_tr_b16 a[134:135], v162 offset:10496
global_load_lds_dwordx4 v6, s[40:41] offset:2048
v_mfma_f32_16x16x32_bf16 v[86:89], a[108:111], v[106:109], v[86:89]
ds_read_b64_tr_b16 a[136:137], v162 offset:8384
v_mfma_f32_16x16x32_bf16 v[90:93], a[112:115], v[106:109], v[90:93]
global_load_lds_dwordx4 v7, s[40:41] offset:3072
ds_read_b64_tr_b16 a[138:139], v162 offset:10688
v_mfma_f32_16x16x32_bf16 v[94:97], v[106:109], a[116:119], v[94:97]
s_add_u32 m0, s48, 49152
ds_read_b64_tr_b16 a[140:141], v162 offset:8576
v_mfma_f32_16x16x32_bf16 v[110:113], a[108:111], v[170:173], v[110:113]
ds_read_b64_tr_b16 a[142:143], v162 offset:10880
v_mfma_f32_16x16x32_bf16 v[114:117], a[112:115], v[170:173], v[114:117]
global_load_lds_dwordx4 v8, s[40:41] offset:-4096
ds_read_b128 v[186:189], v52 offset:10240
v_mfma_f32_16x16x32_bf16 v[118:121], v[170:173], a[116:119], v[118:121]
s_add_u32 s40, s40, 221184
ds_read_b128 v[194:197], v52 offset:28672
v_mfma_f32_16x16x32_bf16 v[134:137], v[106:109], v[170:173], v[134:137]
v_mfma_f32_16x16x32_bf16 v[122:125], a[108:111], v[178:181], v[122:125]
v_mfma_f32_16x16x32_bf16 v[126:129], a[112:115], v[178:181], v[126:129]
s_addc_u32 s41, s41, 0
v_mfma_f32_16x16x32_bf16 v[130:133], v[178:181], a[116:119], v[130:133]
v_mfma_f32_16x16x32_bf16 v[158:161], v[106:109], v[178:181], v[158:161]
s_waitcnt vmcnt(9) lgkmcnt(0)
s_barrier
s_add_u32 m0, s48, 77824
ds_read_b64_tr_b16 a[144:145], v9 offset:47104
v_mfma_f32_16x16x32_bf16 v[86:89], a[120:123], v[106:109], v[86:89]
v_mfma_f32_16x16x32_bf16 v[90:93], a[124:127], v[106:109], v[90:93]
global_load_lds_dwordx4 v0, s[40:41] offset:-4096
ds_read_b64_tr_b16 a[146:147], v9 offset:49408
v_mfma_f32_16x16x32_bf16 v[94:97], v[106:109], a[128:131], v[94:97]
ds_read_b64_tr_b16 a[148:149], v9 offset:47296
global_load_lds_dwordx4 v1, s[40:41] offset:-3072
v_mfma_f32_16x16x32_bf16 v[110:113], a[120:123], v[182:185], v[110:113]
ds_read_b64_tr_b16 a[150:151], v9 offset:49600
v_mfma_f32_16x16x32_bf16 v[114:117], a[124:127], v[182:185], v[114:117]
global_load_lds_dwordx4 v2, s[40:41] offset:-2048
ds_read_b64_tr_b16 a[152:153], v9 offset:47488
v_mfma_f32_16x16x32_bf16 v[118:121], v[182:185], a[128:131], v[118:121]
ds_read_b64_tr_b16 a[154:155], v9 offset:49792
global_load_lds_dwordx4 v3, s[40:41] offset:-1024
v_mfma_f32_16x16x32_bf16 v[134:137], v[106:109], v[182:185], v[134:137]
ds_read_b128 v[166:169], v51 offset:12288
v_mfma_f32_16x16x32_bf16 v[122:125], a[120:123], v[190:193], v[122:125]
global_load_lds_dwordx4 v4, s[40:41] offset:0
ds_read_b128 v[174:177], v51 offset:30720
v_mfma_f32_16x16x32_bf16 v[126:129], a[124:127], v[190:193], v[126:129]
ds_read_b64_tr_b16 a[156:157], v255 offset:0
global_load_lds_dwordx4 v5, s[40:41] offset:1024
v_mfma_f32_16x16x32_bf16 v[130:133], v[190:193], a[128:131], v[130:133]
v_mfma_f32_16x16x32_bf16 v[158:161], v[106:109], v[190:193], v[158:161]
ds_read_b64_tr_b16 a[158:159], v255 offset:2304
global_load_lds_dwordx4 v6, s[40:41] offset:2048
v_mfma_f32_16x16x32_bf16 v[86:89], a[132:135], v[106:109], v[86:89]
ds_read_b64_tr_b16 a[160:161], v255 offset:192
v_mfma_f32_16x16x32_bf16 v[90:93], a[136:139], v[106:109], v[90:93]
global_load_lds_dwordx4 v7, s[40:41] offset:3072
ds_read_b64_tr_b16 a[162:163], v255 offset:2496
v_mfma_f32_16x16x32_bf16 v[94:97], v[106:109], a[140:143], v[94:97]
s_add_u32 m0, s48, 86016
ds_read_b64_tr_b16 a[164:165], v255 offset:384
v_mfma_f32_16x16x32_bf16 v[110:113], a[132:135], v[186:189], v[110:113]
ds_read_b64_tr_b16 a[166:167], v255 offset:2688
v_mfma_f32_16x16x32_bf16 v[114:117], a[136:139], v[186:189], v[114:117]
global_load_lds_dwordx4 v8, s[40:41] offset:-4096
ds_read_b128 v[170:173], v52 offset:12288
v_mfma_f32_16x16x32_bf16 v[118:121], v[186:189], a[140:143], v[118:121]
s_sub_u32 s40, s40, 1769472
ds_read_b128 v[178:181], v52 offset:30720
v_mfma_f32_16x16x32_bf16 v[134:137], v[106:109], v[186:189], v[134:137]
v_mfma_f32_16x16x32_bf16 v[122:125], a[132:135], v[194:197], v[122:125]
v_mfma_f32_16x16x32_bf16 v[126:129], a[136:139], v[194:197], v[126:129]
s_subb_u32 s41, s41, 0
v_mfma_f32_16x16x32_bf16 v[130:133], v[194:197], a[140:143], v[130:133]
v_mfma_f32_16x16x32_bf16 v[158:161], v[106:109], v[194:197], v[158:161]
s_waitcnt vmcnt(9) lgkmcnt(0)
s_barrier
s_add_u32 m0, s49, 0
ds_read_b64_tr_b16 a[168:169], v255 offset:18432
v_mfma_f32_16x16x32_bf16 v[86:89], a[144:147], v[106:109], v[86:89]
v_mfma_f32_16x16x32_bf16 v[90:93], a[148:151], v[106:109], v[90:93]
ds_read_b64_tr_b16 a[170:171], v255 offset:20736
v_mfma_f32_16x16x32_bf16 v[94:97], v[106:109], a[152:155], v[94:97]
ds_read_b64_tr_b16 a[172:173], v255 offset:18624
v_mfma_f32_16x16x32_bf16 v[110:113], a[144:147], v[166:169], v[110:113]
global_load_lds_dwordx4 v11, s[42:43] offset:-2048 sc1
ds_read_b64_tr_b16 a[174:175], v255 offset:20928
v_mfma_f32_16x16x32_bf16 v[114:117], a[148:151], v[166:169], v[114:117]
ds_read_b64_tr_b16 a[176:177], v255 offset:18816
v_mfma_f32_16x16x32_bf16 v[118:121], v[166:169], a[152:155], v[118:121]
ds_read_b64_tr_b16 a[178:179], v255 offset:21120
v_mfma_f32_16x16x32_bf16 v[134:137], v[106:109], v[166:169], v[134:137]
global_load_lds_dwordx4 v12, s[42:43] offset:-1024 sc1
ds_read_b128 v[182:185], v51 offset:14336
v_mfma_f32_16x16x32_bf16 v[122:125], a[144:147], v[174:177], v[122:125]
ds_read_b128 v[190:193], v51 offset:32768
v_mfma_f32_16x16x32_bf16 v[126:129], a[148:151], v[174:177], v[126:129]
ds_read_b64_tr_b16 a[180:181], v255 offset:36864
v_mfma_f32_16x16x32_bf16 v[130:133], v[174:177], a[152:155], v[130:133]
global_load_lds_dwordx4 v13, s[42:43] offset:0 sc1
v_mfma_f32_16x16x32_bf16 v[158:161], v[106:109], v[174:177], v[158:161]
ds_read_b64_tr_b16 a[182:183], v255 offset:39168
v_mfma_f32_16x16x32_bf16 v[86:89], a[156:159], v[106:109], v[86:89]
ds_read_b64_tr_b16 a[184:185], v255 offset:37056
v_mfma_f32_16x16x32_bf16 v[90:93], a[160:163], v[106:109], v[90:93]
ds_read_b64_tr_b16 a[186:187], v255 offset:39360
v_mfma_f32_16x16x32_bf16 v[94:97], v[106:109], a[164:167], v[94:97]
global_load_lds_dwordx4 v14, s[42:43] offset:1024 sc1
ds_read_b64_tr_b16 a[188:189], v255 offset:37248
v_mfma_f32_16x16x32_bf16 v[110:113], a[156:159], v[170:173], v[110:113]
ds_read_b64_tr_b16 a[190:191], v255 offset:39552
v_mfma_f32_16x16x32_bf16 v[114:117], a[160:163], v[170:173], v[114:117]
ds_read_b128 v[186:189], v52 offset:14336
v_mfma_f32_16x16x32_bf16 v[118:121], v[170:173], a[164:167], v[118:121]
s_sub_u32 s42, s42, 18432
s_subb_u32 s43, s43, 0
ds_read_b128 v[194:197], v52 offset:32768
v_mfma_f32_16x16x32_bf16 v[134:137], v[106:109], v[170:173], v[134:137]
v_mfma_f32_16x16x32_bf16 v[122:125], a[156:159], v[178:181], v[122:125]
v_mfma_f32_16x16x32_bf16 v[126:129], a[160:163], v[178:181], v[126:129]
v_mfma_f32_16x16x32_bf16 v[130:133], v[178:181], a[164:167], v[130:133]
v_mfma_f32_16x16x32_bf16 v[158:161], v[106:109], v[178:181], v[158:161]
s_waitcnt vmcnt(4) lgkmcnt(0)
s_barrier
s_add_u32 m0, s49, 18432
ds_read_b64_tr_b16 a[192:193], v255 offset:55296
v_mfma_f32_16x16x32_bf16 v[86:89], a[168:171], v[106:109], v[86:89]
v_mfma_f32_16x16x32_bf16 v[90:93], a[172:175], v[106:109], v[90:93]
ds_read_b64_tr_b16 a[194:195], v255 offset:57600
v_mfma_f32_16x16x32_bf16 v[94:97], v[106:109], a[176:179], v[94:97]
ds_read_b64_tr_b16 a[196:197], v255 offset:55488
v_mfma_f32_16x16x32_bf16 v[110:113], a[168:171], v[182:185], v[110:113]
global_load_lds_dwordx4 v11, s[42:43] offset:-2048 sc1
ds_read_b64_tr_b16 a[198:199], v255 offset:57792
v_mfma_f32_16x16x32_bf16 v[114:117], a[172:175], v[182:185], v[114:117]
ds_read_b64_tr_b16 a[200:201], v255 offset:55680
v_mfma_f32_16x16x32_bf16 v[118:121], v[182:185], a[176:179], v[118:121]
ds_read_b64_tr_b16 a[202:203], v255 offset:57984
v_mfma_f32_16x16x32_bf16 v[134:137], v[106:109], v[182:185], v[134:137]
global_load_lds_dwordx4 v12, s[42:43] offset:-1024 sc1
ds_read_b128 v[166:169], v51 offset:16384
v_mfma_f32_16x16x32_bf16 v[122:125], a[168:171], v[190:193], v[122:125]
ds_read_b128 v[174:177], v51 offset:34816
v_mfma_f32_16x16x32_bf16 v[126:129], a[172:175], v[190:193], v[126:129]
ds_read_b64_tr_b16 a[204:205], v162 offset:8192
v_mfma_f32_16x16x32_bf16 v[130:133], v[190:193], a[176:179], v[130:133]
global_load_lds_dwordx4 v13, s[42:43] offset:0 sc1
ds_read_b64_tr_b16 a[206:207], v162 offset:10496
v_mfma_f32_16x16x32_bf16 v[158:161], v[106:109], v[190:193], v[158:161]
ds_read_b64_tr_b16 a[208:209], v162 offset:8384
v_mfma_f32_16x16x32_bf16 v[86:89], a[180:183], v[106:109], v[86:89]
ds_read_b64_tr_b16 a[210:211], v162 offset:10688
v_mfma_f32_16x16x32_bf16 v[90:93], a[184:187], v[106:109], v[90:93]
global_load_lds_dwordx4 v14, s[42:43] offset:1024 sc1
ds_read_b64_tr_b16 a[212:213], v162 offset:8576
v_mfma_f32_16x16x32_bf16 v[94:97], v[106:109], a[188:191], v[94:97]
ds_read_b64_tr_b16 a[214:215], v162 offset:10880
v_mfma_f32_16x16x32_bf16 v[110:113], a[180:183], v[186:189], v[110:113]
ds_read_b128 v[170:173], v52 offset:16384
v_mfma_f32_16x16x32_bf16 v[114:117], a[184:187], v[186:189], v[114:117]
s_sub_u32 s42, s42, 18432
s_subb_u32 s43, s43, 0
ds_read_b128 v[178:181], v52 offset:34816
v_mfma_f32_16x16x32_bf16 v[118:121], v[186:189], a[188:191], v[118:121]
v_mfma_f32_16x16x32_bf16 v[134:137], v[106:109], v[186:189], v[134:137]
v_mfma_f32_16x16x32_bf16 v[122:125], a[180:183], v[194:197], v[122:125]
v_mfma_f32_16x16x32_bf16 v[126:129], a[184:187], v[194:197], v[126:129]
v_mfma_f32_16x16x32_bf16 v[158:161], v[106:109], v[194:197], v[158:161]
s_waitcnt lgkmcnt(14)
v_mfma_f32_16x16x32_bf16 v[86:89], a[192:195], v[106:109], v[86:89]
s_waitcnt lgkmcnt(12)
v_mfma_f32_16x16x32_bf16 v[90:93], a[196:199], v[106:109], v[90:93]
s_waitcnt lgkmcnt(10)
v_mfma_f32_16x16x32_bf16 v[94:97], v[106:109], a[200:203], v[94:97]
s_waitcnt lgkmcnt(9)
v_mfma_f32_16x16x32_bf16 v[110:113], a[192:195], v[166:169], v[110:113]
v_mfma_f32_16x16x32_bf16 v[114:117], a[196:199], v[166:169], v[114:117]
v_mfma_f32_16x16x32_bf16 v[118:121], v[166:169], a[200:203], v[118:121]
v_mfma_f32_16x16x32_bf16 v[134:137], v[106:109], v[166:169], v[134:137]
s_waitcnt lgkmcnt(8)
v_mfma_f32_16x16x32_bf16 v[158:161], v[106:109], v[174:177], v[158:161]
s_waitcnt lgkmcnt(6)
v_mfma_f32_16x16x32_bf16 v[86:89], a[204:207], v[106:109], v[86:89]
s_waitcnt lgkmcnt(4)
v_mfma_f32_16x16x32_bf16 v[90:93], a[208:211], v[106:109], v[90:93]
s_waitcnt lgkmcnt(2)
v_mfma_f32_16x16x32_bf16 v[94:97], v[106:109], a[212:215], v[94:97]
s_waitcnt lgkmcnt(1)
v_mfma_f32_16x16x32_bf16 v[110:113], a[204:207], v[170:173], v[110:113]
v_mfma_f32_16x16x32_bf16 v[114:117], a[208:211], v[170:173], v[114:117]
v_mfma_f32_16x16x32_bf16 v[118:121], v[170:173], a[212:215], v[118:121]
v_mfma_f32_16x16x32_bf16 v[134:137], v[106:109], v[170:173], v[134:137]
s_waitcnt lgkmcnt(0)
v_mfma_f32_16x16x32_bf16 v[158:161], v[106:109], v[178:181], v[158:161]
s_nop 5
v_mul_f32 v202, 0x3ae38e39, v134
v_cvt_pk_bf16_f32 v216, -v202, -v202
v_lshlrev_b32 v216, 16, v216
ds_write_b32 v24, v216 offset:0
s_waitcnt lgkmcnt(0)
s_barrier
ds_read_b32 v216, v26
s_waitcnt lgkmcnt(0)
v_mul_f32 v216, 0x3ae38e39, v216
v_fma_f32 v203, -v202, v202, v216
v_max_f32 v203, 0, v203
v_cvt_pk_bf16_f32 v203, v203, v203
v_lshlrev_b32 v203, 16, v203
v_add_f32 v203, 0x3727c5ac, v203
v_cvt_pk_bf16_f32 v203, v203, v203
)"
R"(v_lshlrev_b32 v203, 16, v203
v_sqrt_f32 v203, v203
s_nop 0
v_cvt_pk_bf16_f32 v203, v203, v203
v_lshlrev_b32 v203, 16, v203
v_rcp_f32 v216, v203
s_nop 0
ds_write_b32 v24, v216 offset:128
ds_read_b128 v[174:177], v52 offset:32768
ds_read_b128 v[178:181], v51 offset:34816
ds_read_b128 v[182:185], v52 offset:34816
v_mov_b32 v206, v32
v_mov_b32 v207, v33
v_mov_b32 v208, v34
v_mov_b32 v209, v35
v_mov_b32 v134, v158
v_mov_b32 v135, v159
v_mov_b32 v136, v160
v_mov_b32 v137, v161
s_waitcnt lgkmcnt(3)
s_barrier
ds_read_b32 v23, v24 offset:0
ds_read_b32 v148, v21
ds_read_b32 v95, v24 offset:192
ds_read_b32 v254, v27
ds_read_b32 v255, v28
ds_read_b128 v[198:201], v25 offset:0
ds_read_b64 v[158:159], v22 offset:41472
ds_read_b32 v140, v20
v_xor_b32 v51, 0x10000, v51
v_xor_b32 v52, 0x10000, v52
v_mul_f32 v202, 0x3ae38e39, v134
v_cvt_pk_bf16_f32 v216, -v202, -v202
v_lshlrev_b32 v216, 16, v216
ds_write_b32 v24, v216 offset:64
s_waitcnt lgkmcnt(8)
v_fma_f32 v210, v90, v23, v114
v_fma_f32 v211, v91, v23, v115
v_fma_f32 v212, v92, v23, v116
v_fma_f32 v213, v93, v23, v117
v_cvt_pk_bf16_f32 v146, v210, v211
v_cvt_pk_bf16_f32 v147, v212, v213
s_waitcnt lgkmcnt(0)
s_barrier
ds_read_b32 v216, v26
v_fma_f32 v210, v94, v198, v118
v_fma_f32 v211, v94, v199, v119
v_fma_f32 v212, v94, v200, v120
v_fma_f32 v213, v94, v201, v121
v_cvt_pk_bf16_f32 v154, v210, v211
v_cvt_pk_bf16_f32 v155, v212, v213
v_fma_f32 v210, v86, v23, v110
v_fma_f32 v211, v87, v23, v111
v_fma_f32 v212, v88, v23, v112
v_fma_f32 v213, v89, v23, v113
v_cvt_pk_bf16_f32 v138, v210, v211
v_cvt_pk_bf16_f32 v139, v212, v213
s_waitcnt lgkmcnt(0)
v_mul_f32 v216, 0x3ae38e39, v216
v_fma_f32 v203, -v202, v202, v216
v_max_f32 v203, 0, v203
v_cvt_pk_bf16_f32 v203, v203, v203
v_lshlrev_b32 v203, 16, v203
v_add_f32 v203, 0x3727c5ac, v203
v_cvt_pk_bf16_f32 v203, v203, v203
v_lshlrev_b32 v203, 16, v203
v_sqrt_f32 v203, v203
s_nop 0
v_cvt_pk_bf16_f32 v203, v203, v203
v_lshlrev_b32 v203, 16, v203
v_rcp_f32 v216, v203
s_nop 0
ds_write_b32 v24, v216 offset:192
v_add_lshl_u32 v252, v48, 0, 7
v_bfi_b32 v252, s69, v252, v47
s_waitcnt vmcnt(4) lgkmcnt(0)
s_barrier
s_mov_b32 s70, 32
s_waitcnt lgkmcnt(0)
s_nop 1
s_branch .Lqa_in72
.p2align 6
.Lqa_top91:
s_waitcnt lgkmcnt(5)
v_mfma_f32_16x16x32_bf16 v[134:137], v[106:109], v[0:3], 0
s_waitcnt lgkmcnt(4)
ds_read_b128 v[58:61], v52 offset:4096
v_bfi_b32 v252, s69, v252, v47
v_mfma_f32_16x16x32_bf16 v[134:137], v[106:109], v[4:7], v[134:137]
ds_read_b32 v152, v21
ds_read_b32 v95, v24 offset:128
v_mfma_f32_16x16x32_bf16 v[110:113], a[0:3], v[0:3], 0
s_waitcnt lgkmcnt(5)
ds_read_b128 v[62:65], v51 offset:6144
ds_read_b32 v254, v29
v_mfma_f32_16x16x32_bf16 v[134:137], v[106:109], v[96:99], v[134:137]
s_waitcnt lgkmcnt(6)
ds_read_b128 v[66:69], v52 offset:6144
ds_read_b32 v255, v30
v_mfma_f32_16x16x32_bf16 v[134:137], v[106:109], v[100:103], v[134:137]
ds_read_b128 v[198:201], v25 offset:64
ds_read_b64 v[160:161], v22 offset:41472
v_mfma_f32_16x16x32_bf16 v[114:117], a[4:7], v[0:3], 0
s_waitcnt lgkmcnt(9)
ds_read_b128 v[70:73], v51 offset:8192
ds_read_b32 v144, v20
v_mfma_f32_16x16x32_bf16 v[134:137], v[106:109], v[54:57], v[134:137]
s_waitcnt lgkmcnt(10)
ds_read_b128 v[74:77], v52 offset:8192
v_fma_f32 v210, v90, v23, v126
v_mfma_f32_16x16x32_bf16 v[134:137], v[106:109], v[58:61], v[134:137]
v_fma_f32 v211, v91, v23, v127
v_fma_f32 v212, v92, v23, v128
v_mfma_f32_16x16x32_bf16 v[118:121], v[0:3], a[8:11], 0
s_waitcnt lgkmcnt(8)
ds_read_b128 v[78:81], v51 offset:10240
v_fma_f32 v213, v93, v23, v129
v_mfma_f32_16x16x32_bf16 v[134:137], v[106:109], v[62:65], v[134:137]
s_waitcnt lgkmcnt(7)
ds_read_b128 v[82:85], v52 offset:10240
v_cvt_pk_bf16_f32 v150, v210, v211
v_mfma_f32_16x16x32_bf16 v[134:137], v[106:109], v[66:69], v[134:137]
v_cvt_pk_bf16_f32 v151, v212, v213
v_max_f32 v10, v190, v191
v_mfma_f32_16x16x32_bf16 v[110:113], a[12:15], v[4:7], v[110:113]
ds_read_b128 v[162:165], v51 offset:12288
v_mfma_f32_16x16x32_bf16 v[186:189], v[150:153], v[138:141], v[32:35]
s_waitcnt lgkmcnt(7)
v_max_f32 v44, v192, v193
v_fma_f32 v210, v94, v198, v130
s_waitcnt lgkmcnt(5)
v_mfma_f32_16x16x32_bf16 v[134:137], v[106:109], v[70:73], v[134:137]
s_xor_b32 s49, s49, 0x10000
ds_read_b128 v[166:169], v52 offset:12288
s_waitcnt lgkmcnt(4)
v_mfma_f32_16x16x32_bf16 v[134:137], v[106:109], v[74:77], v[134:137]
s_add_u32 m0, s49, 0
v_mul_f32 v95, 0x3eae1a93, v95
v_mul_f32 v8, v254, v95
v_mfma_f32_16x16x32_bf16 v[114:117], a[16:19], v[4:7], v[114:117]
s_waitcnt lgkmcnt(3)
ds_read_b128 v[170:173], v51 offset:14336
v_mul_f32 v9, v255, v95
v_mfma_f32_16x16x32_bf16 v[134:137], v[106:109], v[78:81], v[134:137]
s_waitcnt lgkmcnt(3)
ds_read_b128 v[174:177], v52 offset:14336
v_max3_f32 v10, v186, v187, v10
v_mfma_f32_16x16x32_bf16 v[134:137], v[106:109], v[82:85], v[134:137]
v_max3_f32 v44, v188, v189, v44
v_mul_f32 v10, v10, v8
v_mfma_f32_16x16x32_bf16 v[118:121], v[4:7], a[20:23], v[118:121]
s_waitcnt lgkmcnt(3)
ds_read_b128 v[178:181], v51 offset:16384
v_mul_f32 v44, v44, v9
v_mfma_f32_16x16x32_bf16 v[134:137], v[106:109], v[162:165], v[134:137]
s_waitcnt lgkmcnt(3)
ds_read_b128 v[182:185], v52 offset:16384
v_max_f32 v45, v10, v44
v_mfma_f32_16x16x32_bf16 v[134:137], v[106:109], v[166:169], v[134:137]
v_mov_b32 v46, v45
v_fma_f32 v211, v94, v199, v131
v_mfma_f32_16x16x32_bf16 v[110:113], a[24:27], v[96:99], v[110:113]
s_waitcnt lgkmcnt(3)
global_load_lds_dwordx4 v11, s[42:43] offset:-2048 sc1
v_fma_f32 v212, v94, v200, v132
v_mfma_f32_16x16x32_bf16 v[134:137], v[106:109], v[170:173], v[134:137]
s_waitcnt lgkmcnt(2)
v_permlane16_swap_b32 v45, v46
v_mfma_f32_16x16x32_bf16 v[134:137], v[106:109], v[174:177], v[134:137]
v_max_f32 v45, v45, v46
v_mov_b32 v46, v45
v_mfma_f32_16x16x32_bf16 v[114:117], a[28:31], v[96:99], v[114:117]
s_waitcnt lgkmcnt(1)
v_permlane32_swap_b32 v45, v46
v_mfma_f32_16x16x32_bf16 v[134:137], v[106:109], v[178:181], v[134:137]
s_waitcnt lgkmcnt(0)
v_max_f32 v45, v45, v46
v_sub_f32 v10, v10, v45
v_mfma_f32_16x16x32_bf16 v[134:137], v[106:109], v[182:185], v[134:137]
v_sub_f32 v44, v44, v45
v_fma_f32 v213, v94, v201, v133
v_mfma_f32_16x16x32_bf16 v[118:121], v[96:99], a[32:35], v[118:121]
v_exp_f32 v214, v10
v_mfma_f32_16x16x32_bf16 v[110:113], a[36:39], v[100:103], v[110:113]
v_exp_f32 v215, v44
v_mfma_f32_16x16x32_bf16 v[114:117], a[40:43], v[100:103], v[114:117]
s_nop 0
v_mul_f32 v202, 0x3ae38e39, v134
v_cvt_pk_bf16_f32 v216, -v202, -v202
v_mfma_f32_16x16x32_bf16 v[118:121], v[100:103], a[44:47], v[118:121]
v_lshlrev_b32 v216, 16, v216
global_load_lds_dwordx4 v12, s[42:43] offset:-1024 sc1
v_mfma_f32_16x16x32_bf16 v[110:113], a[48:51], v[54:57], v[110:113]
ds_write_b32 v24, v216 offset:0
v_mfma_f32_16x16x32_bf16 v[114:117], a[52:55], v[54:57], v[114:117]
v_add_f32 v45, v214, v215
v_mov_b32 v46, v45
v_mfma_f32_16x16x32_bf16 v[118:121], v[54:57], a[56:59], v[118:121]
v_mul_f32 v214, v214, v254
v_cvt_pk_bf16_f32 v156, v210, v211
v_mfma_f32_16x16x32_bf16 v[110:113], a[60:63], v[58:61], v[110:113]
v_permlane16_swap_b32 v45, v46
v_mfma_f32_16x16x32_bf16 v[114:117], a[64:67], v[58:61], v[114:117]
v_add_f32 v45, v45, v46
v_mov_b32 v46, v45
v_mfma_f32_16x16x32_bf16 v[118:121], v[58:61], a[68:71], v[118:121]
v_mfma_f32_16x16x32_bf16 v[110:113], a[72:75], v[62:65], v[110:113]
s_waitcnt lgkmcnt(0)
s_barrier
v_mfma_f32_16x16x32_bf16 v[114:117], a[76:79], v[62:65], v[114:117]
global_load_lds_dwordx4 v13, s[42:43] offset:0 sc1
ds_read_b32 v216, v26
v_mfma_f32_16x16x32_bf16 v[118:121], v[62:65], a[80:83], v[118:121]
v_mul_f32 v215, v215, v255
v_cvt_pk_bf16_f32 v157, v212, v213
v_mfma_f32_16x16x32_bf16 v[110:113], a[84:87], v[66:69], v[110:113]
v_permlane32_swap_b32 v45, v46
v_mfma_f32_16x16x32_bf16 v[114:117], a[88:91], v[66:69], v[114:117]
v_add_f32 v45, v45, v46
v_fma_f32 v210, v86, v23, v122
v_mfma_f32_16x16x32_bf16 v[118:121], v[66:69], a[92:95], v[118:121]
v_rcp_f32 v45, v45
v_mfma_f32_16x16x32_bf16 v[110:113], a[96:99], v[70:73], v[110:113]
v_mul_f32 v214, v214, v45
v_mul_f32 v215, v215, v45
v_mfma_f32_16x16x32_bf16 v[114:117], a[100:103], v[70:73], v[114:117]
v_cvt_pk_bf16_f32 v214, v214, v214
v_cvt_pk_bf16_f32 v215, v215, v215
v_mfma_f32_16x16x32_bf16 v[118:121], v[70:73], a[104:107], v[118:121]
v_and_b32 v196, v214, v40
v_and_b32 v197, v215, v41
v_mfma_f32_16x16x32_bf16 v[110:113], a[108:111], v[74:77], v[110:113]
global_load_lds_dwordx4 v14, s[42:43] offset:1024 sc1
s_add_u32 s42, s42, s46
s_addc_u32 s43, s43, s47
v_mfma_f32_16x16x32_bf16 v[114:117], a[112:115], v[74:77], v[114:117]
v_and_b32 v194, v214, v42
v_and_b32 v195, v215, v43
v_mfma_f32_16x16x32_bf16 v[118:121], v[74:77], a[116:119], v[118:121]
s_nop 0
v_mfma_f32_16x16x32_bf16 v[190:193], v[154:157], v[194:197], 0
v_mfma_f32_16x16x32_bf16 v[186:189], v[158:161], v[194:197], 0
s_waitcnt lgkmcnt(0)
v_mul_f32 v216, 0x3ae38e39, v216
v_fma_f32 v203, -v202, v202, v216
v_mfma_f32_16x16x32_bf16 v[110:113], a[120:123], v[78:81], v[110:113]
v_max_f32 v203, 0, v203
v_cvt_pk_bf16_f32 v203, v203, v203
v_mfma_f32_16x16x32_bf16 v[114:117], a[124:127], v[78:81], v[114:117]
v_lshlrev_b32 v203, 16, v203
v_add_f32 v203, 0x3727c5ac, v203
v_mfma_f32_16x16x32_bf16 v[118:121], v[78:81], a[128:131], v[118:121]
v_cvt_pk_bf16_f32 v203, v203, v203
v_lshlrev_b32 v203, 16, v203
v_mfma_f32_16x16x32_bf16 v[110:113], a[132:135], v[82:85], v[110:113]
v_cvt_pk_bf16_f32 v104, v190, v191
v_cvt_pk_bf16_f32 v105, v192, v193
v_mfma_f32_16x16x32_bf16 v[114:117], a[136:139], v[82:85], v[114:117]
ds_write_b64 v18, v[104:105]
v_mfma_f32_16x16x32_bf16 v[118:121], v[82:85], a[140:143], v[118:121]
v_cvt_pk_bf16_f32 v214, v188, v189
v_fma_f32 v211, v87, v23, v123
v_mfma_f32_16x16x32_bf16 v[110:113], a[144:147], v[162:165], v[110:113]
ds_write_b32 v19, v214
v_mfma_f32_16x16x32_bf16 v[114:117], a[148:151], v[162:165], v[114:117]
v_sqrt_f32 v203, v203
v_mfma_f32_16x16x32_bf16 v[118:121], v[162:165], a[152:155], v[118:121]
v_cvt_pk_bf16_f32 v203, v203, v203
v_lshlrev_b32 v203, 16, v203
v_mfma_f32_16x16x32_bf16 v[110:113], a[156:159], v[166:169], v[110:113]
v_rcp_f32 v216, v203
v_mfma_f32_16x16x32_bf16 v[114:117], a[160:163], v[166:169], v[114:117]
ds_write_b32 v24, v216 offset:128
v_mfma_f32_16x16x32_bf16 v[118:121], v[166:169], a[164:167], v[118:121]
v_fma_f32 v212, v88, v23, v124
v_fma_f32 v213, v89, v23, v125
v_mfma_f32_16x16x32_bf16 v[110:113], a[168:171], v[170:173], v[110:113]
v_cvt_pk_bf16_f32 v142, v210, v211
v_cvt_pk_bf16_f32 v143, v212, v213
v_mfma_f32_16x16x32_bf16 v[114:117], a[172:175], v[170:173], v[114:117]
v_mfma_f32_16x16x32_bf16 v[118:121], v[170:173], a[176:179], v[118:121]
v_mfma_f32_16x16x32_bf16 v[110:113], a[180:183], v[174:177], v[110:113]
v_mfma_f32_16x16x32_bf16 v[114:117], a[184:187], v[174:177], v[114:117]
s_waitcnt vmcnt(5) lgkmcnt(0)
s_barrier
v_mfma_f32_16x16x32_bf16 v[118:121], v[174:177], a[188:191], v[118:121]
ds_read_b128 v[0:3], v51 offset:18432
v_mfma_f32_16x16x32_bf16 v[110:113], a[192:195], v[178:181], v[110:113]
ds_read_b128 v[4:7], v52 offset:18432
ds_read_b32 v23, v24 offset:0
v_mfma_f32_16x16x32_bf16 v[114:117], a[196:199], v[178:181], v[114:117]
s_mov_b64 exec, 1
global_store_dword v252, v48, s[38:39] sc1
s_mov_b64 exec, -1
v_mfma_f32_16x16x32_bf16 v[190:193], v[150:153], v[142:145], v[36:39]
v_mfma_f32_16x16x32_bf16 v[118:121], v[178:181], a[200:203], v[118:121]
ds_read_b128 v[96:99], v51 offset:20480
v_add_u32 v48, 1, v48
v_mfma_f32_16x16x32_bf16 v[110:113], a[204:207], v[182:185], v[110:113]
ds_read_b128 v[100:103], v52 offset:20480
v_add_lshl_u32 v252, v48, 0, 7
v_mfma_f32_16x16x32_bf16 v[114:117], a[208:211], v[182:185], v[114:117]
v_bfi_b32 v252, s69, v252, v47
ds_read_b32 v148, v21
v_mfma_f32_16x16x32_bf16 v[118:121], v[182:185], a[212:215], v[118:121]
s_waitcnt lgkmcnt(5)
ds_read_b128 v[54:57], v51 offset:22528
ds_read_b32 v95, v24 offset:192
v_mfma_f32_16x16x32_bf16 v[134:137], v[106:109], v[0:3], 0
s_waitcnt lgkmcnt(6)
ds_read_b128 v[58:61], v52 offset:22528
ds_read_b32 v254, v27
v_mfma_f32_16x16x32_bf16 v[134:137], v[106:109], v[4:7], v[134:137]
ds_read_b32 v255, v28
ds_read_b128 v[198:201], v25 offset:0
v_mfma_f32_16x16x32_bf16 v[122:125], a[0:3], v[0:3], 0
s_waitcnt lgkmcnt(8)
ds_read_b128 v[62:65], v51 offset:24576
ds_read_b64 v[158:159], v22 offset:41472
v_mfma_f32_16x16x32_bf16 v[134:137], v[106:109], v[96:99], v[134:137]
s_waitcnt lgkmcnt(9)
ds_read_b128 v[66:69], v52 offset:24576
ds_read_b32 v140, v20
v_mfma_f32_16x16x32_bf16 v[134:137], v[106:109], v[100:103], v[134:137]
v_fma_f32 v210, v90, v23, v114
v_fma_f32 v211, v91, v23, v115
v_mfma_f32_16x16x32_bf16 v[126:129], a[4:7], v[0:3], 0
s_waitcnt lgkmcnt(9)
ds_read_b128 v[70:73], v51 offset:26624
v_fma_f32 v212, v92, v23, v116
v_mfma_f32_16x16x32_bf16 v[134:137], v[106:109], v[54:57], v[134:137]
s_waitcnt lgkmcnt(8)
ds_read_b128 v[74:77], v52 offset:26624
v_fma_f32 v213, v93, v23, v117
v_mfma_f32_16x16x32_bf16 v[134:137], v[106:109], v[58:61], v[134:137]
v_cvt_pk_bf16_f32 v146, v210, v211
v_cvt_pk_bf16_f32 v147, v212, v213
v_mfma_f32_16x16x32_bf16 v[130:133], v[0:3], a[8:11], 0
ds_read_b128 v[78:81], v51 offset:28672
v_mfma_f32_16x16x32_bf16 v[186:189], v[146:149], v[142:145], v[206:209]
s_waitcnt lgkmcnt(6)
v_max_f32 v10, v190, v191
v_max_f32 v44, v192, v193
v_mfma_f32_16x16x32_bf16 v[134:137], v[106:109], v[62:65], v[134:137]
s_waitcnt lgkmcnt(4)
ds_read_b128 v[82:85], v52 offset:28672
v_fma_f32 v210, v94, v198, v118
v_mfma_f32_16x16x32_bf16 v[134:137], v[106:109], v[66:69], v[134:137]
v_mul_f32 v95, 0x3eae1a93, v95
v_mul_f32 v8, v254, v95
v_mfma_f32_16x16x32_bf16 v[122:125], a[12:15], v[4:7], v[122:125]
s_waitcnt lgkmcnt(3)
ds_read_b128 v[162:165], v51 offset:30720
v_mul_f32 v9, v255, v95
v_mfma_f32_16x16x32_bf16 v[134:137], v[106:109], v[70:73], v[134:137]
s_add_u32 m0, s49, 18432
ds_read_b128 v[166:169], v52 offset:30720
s_waitcnt lgkmcnt(4)
v_mfma_f32_16x16x32_bf16 v[134:137], v[106:109], v[74:77], v[134:137]
v_max3_f32 v10, v186, v187, v10
v_max3_f32 v44, v188, v189, v44
v_mfma_f32_16x16x32_bf16 v[126:129], a[16:19], v[4:7], v[126:129]
s_waitcnt lgkmcnt(3)
ds_read_b128 v[170:173], v51 offset:32768
v_mul_f32 v10, v10, v8
v_mfma_f32_16x16x32_bf16 v[134:137], v[106:109], v[78:81], v[134:137]
s_waitcnt lgkmcnt(3)
ds_read_b128 v[174:177], v52 offset:32768
v_mul_f32 v44, v44, v9
v_mfma_f32_16x16x32_bf16 v[134:137], v[106:109], v[82:85], v[134:137]
v_max_f32 v45, v10, v44
v_mov_b32 v46, v45
v_mfma_f32_16x16x32_bf16 v[130:133], v[4:7], a[20:23], v[130:133]
s_waitcnt lgkmcnt(3)
ds_read_b128 v[178:181], v51 offset:34816
v_fma_f32 v211, v94, v199, v119
v_mfma_f32_16x16x32_bf16 v[134:137], v[106:109], v[162:165], v[134:137]
s_waitcnt lgkmcnt(3)
ds_read_b128 v[182:185], v52 offset:34816
v_fma_f32 v212, v94, v200, v120
v_mfma_f32_16x16x32_bf16 v[134:137], v[106:109], v[166:169], v[134:137]
global_load_lds_dwordx4 v11, s[42:43] offset:-2048 sc1
v_fma_f32 v213, v94, v201, v121
v_mfma_f32_16x16x32_bf16 v[122:125], a[24:27], v[96:99], v[122:125]
s_waitcnt lgkmcnt(3)
v_permlane16_swap_b32 v45, v46
v_mfma_f32_16x16x32_bf16 v[134:137], v[106:109], v[170:173], v[134:137]
s_waitcnt lgkmcnt(2)
v_max_f32 v45, v45, v46
v_mov_b32 v46, v45
v_mfma_f32_16x16x32_bf16 v[134:137], v[106:109], v[174:177], v[134:137]
s_nop 0
v_permlane32_swap_b32 v45, v46
v_mfma_f32_16x16x32_bf16 v[126:129], a[28:31], v[96:99], v[126:129]
s_waitcnt lgkmcnt(1)
v_max_f32 v45, v45, v46
v_sub_f32 v10, v10, v45
v_mfma_f32_16x16x32_bf16 v[134:137], v[106:109], v[178:181], v[134:137]
s_waitcnt lgkmcnt(0)
v_sub_f32 v44, v44, v45
)"
R"(v_cvt_pk_bf16_f32 v154, v210, v211
v_mfma_f32_16x16x32_bf16 v[134:137], v[106:109], v[182:185], v[134:137]
v_exp_f32 v214, v10
v_mfma_f32_16x16x32_bf16 v[130:133], v[96:99], a[32:35], v[130:133]
v_exp_f32 v215, v44
v_mfma_f32_16x16x32_bf16 v[122:125], a[36:39], v[100:103], v[122:125]
global_load_lds_dwordx4 v12, s[42:43] offset:-1024 sc1
v_add_f32 v45, v214, v215
v_mfma_f32_16x16x32_bf16 v[126:129], a[40:43], v[100:103], v[126:129]
s_nop 0
v_mul_f32 v202, 0x3ae38e39, v134
v_cvt_pk_bf16_f32 v216, -v202, -v202
v_mfma_f32_16x16x32_bf16 v[130:133], v[100:103], a[44:47], v[130:133]
v_lshlrev_b32 v216, 16, v216
v_mov_b32 v46, v45
v_mfma_f32_16x16x32_bf16 v[122:125], a[48:51], v[54:57], v[122:125]
ds_write_b32 v24, v216 offset:64
v_mfma_f32_16x16x32_bf16 v[126:129], a[52:55], v[54:57], v[126:129]
v_mul_f32 v214, v214, v254
v_cvt_pk_bf16_f32 v155, v212, v213
v_mfma_f32_16x16x32_bf16 v[130:133], v[54:57], a[56:59], v[130:133]
v_permlane16_swap_b32 v45, v46
v_mfma_f32_16x16x32_bf16 v[122:125], a[60:63], v[58:61], v[122:125]
v_add_f32 v45, v45, v46
v_mov_b32 v46, v45
v_mfma_f32_16x16x32_bf16 v[126:129], a[64:67], v[58:61], v[126:129]
v_mul_f32 v215, v215, v255
v_fma_f32 v210, v86, v23, v110
v_mfma_f32_16x16x32_bf16 v[130:133], v[58:61], a[68:71], v[130:133]
v_mfma_f32_16x16x32_bf16 v[122:125], a[72:75], v[62:65], v[122:125]
s_waitcnt lgkmcnt(0)
s_barrier
v_mfma_f32_16x16x32_bf16 v[126:129], a[76:79], v[62:65], v[126:129]
global_load_lds_dwordx4 v13, s[42:43] offset:0 sc1
ds_read_b32 v216, v26
v_mfma_f32_16x16x32_bf16 v[130:133], v[62:65], a[80:83], v[130:133]
v_permlane32_swap_b32 v45, v46
v_mfma_f32_16x16x32_bf16 v[122:125], a[84:87], v[66:69], v[122:125]
v_add_f32 v45, v45, v46
v_fma_f32 v211, v87, v23, v111
v_mfma_f32_16x16x32_bf16 v[126:129], a[88:91], v[66:69], v[126:129]
v_rcp_f32 v45, v45
v_mfma_f32_16x16x32_bf16 v[130:133], v[66:69], a[92:95], v[130:133]
v_mul_f32 v214, v214, v45
v_mul_f32 v215, v215, v45
v_mfma_f32_16x16x32_bf16 v[122:125], a[96:99], v[70:73], v[122:125]
v_cvt_pk_bf16_f32 v214, v214, v214
v_cvt_pk_bf16_f32 v215, v215, v215
v_mfma_f32_16x16x32_bf16 v[126:129], a[100:103], v[70:73], v[126:129]
v_and_b32 v194, v214, v40
v_and_b32 v195, v215, v41
v_mfma_f32_16x16x32_bf16 v[130:133], v[70:73], a[104:107], v[130:133]
v_and_b32 v196, v214, v42
v_and_b32 v197, v215, v43
v_mfma_f32_16x16x32_bf16 v[122:125], a[108:111], v[74:77], v[122:125]
global_load_lds_dwordx4 v14, s[42:43] offset:1024 sc1
s_sub_u32 s42, s42, 18432
s_subb_u32 s43, s43, 0
v_mfma_f32_16x16x32_bf16 v[190:193], v[154:157], v[194:197], 0
v_mfma_f32_16x16x32_bf16 v[186:189], v[158:161], v[194:197], 0
s_waitcnt lgkmcnt(0)
v_mul_f32 v216, 0x3ae38e39, v216
v_fma_f32 v203, -v202, v202, v216
v_mfma_f32_16x16x32_bf16 v[126:129], a[112:115], v[74:77], v[126:129]
v_max_f32 v203, 0, v203
v_cvt_pk_bf16_f32 v203, v203, v203
v_mfma_f32_16x16x32_bf16 v[130:133], v[74:77], a[116:119], v[130:133]
v_lshlrev_b32 v203, 16, v203
v_add_f32 v203, 0x3727c5ac, v203
v_mfma_f32_16x16x32_bf16 v[122:125], a[120:123], v[78:81], v[122:125]
v_cvt_pk_bf16_f32 v203, v203, v203
v_lshlrev_b32 v203, 16, v203
v_mfma_f32_16x16x32_bf16 v[126:129], a[124:127], v[78:81], v[126:129]
v_cvt_pk_bf16_f32 v104, v190, v191
v_cvt_pk_bf16_f32 v105, v192, v193
v_mfma_f32_16x16x32_bf16 v[130:133], v[78:81], a[128:131], v[130:133]
ds_write_b64 v18, v[104:105]
v_mfma_f32_16x16x32_bf16 v[122:125], a[132:135], v[82:85], v[122:125]
v_cvt_pk_bf16_f32 v214, v188, v189
v_fma_f32 v212, v88, v23, v112
v_mfma_f32_16x16x32_bf16 v[126:129], a[136:139], v[82:85], v[126:129]
ds_write_b32 v19, v214
v_mfma_f32_16x16x32_bf16 v[130:133], v[82:85], a[140:143], v[130:133]
v_sqrt_f32 v203, v203
v_mfma_f32_16x16x32_bf16 v[122:125], a[144:147], v[162:165], v[122:125]
v_cvt_pk_bf16_f32 v203, v203, v203
v_lshlrev_b32 v203, 16, v203
v_mfma_f32_16x16x32_bf16 v[126:129], a[148:151], v[162:165], v[126:129]
v_rcp_f32 v216, v203
v_mfma_f32_16x16x32_bf16 v[130:133], v[162:165], a[152:155], v[130:133]
ds_write_b32 v24, v216 offset:192
v_mfma_f32_16x16x32_bf16 v[122:125], a[156:159], v[166:169], v[122:125]
v_fma_f32 v213, v89, v23, v113
v_cvt_pk_bf16_f32 v138, v210, v211
v_mfma_f32_16x16x32_bf16 v[126:129], a[160:163], v[166:169], v[126:129]
v_cvt_pk_bf16_f32 v139, v212, v213
v_mfma_f32_16x16x32_bf16 v[130:133], v[166:169], a[164:167], v[130:133]
v_mfma_f32_16x16x32_bf16 v[122:125], a[168:171], v[170:173], v[122:125]
v_mfma_f32_16x16x32_bf16 v[126:129], a[172:175], v[170:173], v[126:129]
v_xor_b32 v51, 0x10000, v51
v_xor_b32 v52, 0x10000, v52
v_mfma_f32_16x16x32_bf16 v[130:133], v[170:173], a[176:179], v[130:133]
v_mfma_f32_16x16x32_bf16 v[122:125], a[180:183], v[174:177], v[122:125]
v_mfma_f32_16x16x32_bf16 v[126:129], a[184:187], v[174:177], v[126:129]
s_waitcnt vmcnt(5) lgkmcnt(0)
s_barrier
.Lqa_in72:
v_mfma_f32_16x16x32_bf16 v[130:133], v[174:177], a[188:191], v[130:133]
s_cmp_eq_u32 s70, s73
s_cbranch_scc1 .Lqa_hk73
.Lqa_hr74:
ds_read_b128 v[0:3], v51 offset:0
v_mfma_f32_16x16x32_bf16 v[122:125], a[192:195], v[178:181], v[122:125]
ds_read_b128 v[4:7], v52 offset:0
ds_read_b32 v23, v24 offset:64
v_mfma_f32_16x16x32_bf16 v[126:129], a[196:199], v[178:181], v[126:129]
s_mov_b64 exec, 1
global_store_dword v252, v48, s[38:39] sc1
s_mov_b64 exec, -1
v_mfma_f32_16x16x32_bf16 v[190:193], v[146:149], v[138:141], v[36:39]
v_mfma_f32_16x16x32_bf16 v[130:133], v[178:181], a[200:203], v[130:133]
ds_read_b128 v[96:99], v51 offset:2048
v_add_u32 v48, 1, v48
v_mfma_f32_16x16x32_bf16 v[122:125], a[204:207], v[182:185], v[122:125]
ds_read_b128 v[100:103], v52 offset:2048
v_add_lshl_u32 v252, v48, 0, 7
v_mfma_f32_16x16x32_bf16 v[126:129], a[208:211], v[182:185], v[126:129]
v_mfma_f32_16x16x32_bf16 v[130:133], v[182:185], a[212:215], v[130:133]
s_sub_u32 s70, s70, 1
ds_read_b128 v[54:57], v51 offset:4096
s_cbranch_scc0 .Lqa_top91
s_waitcnt lgkmcnt(0)
v_bfi_b32 v252, s69, v252, v47
ds_read_b32 v152, v21
ds_read_b32 v95, v24 offset:128
ds_read_b32 v254, v29
ds_read_b32 v255, v30
ds_read_b128 v[198:201], v25 offset:64
ds_read_b64 v[160:161], v22 offset:41472
ds_read_b32 v144, v20
v_fma_f32 v210, v90, v23, v126
v_fma_f32 v211, v91, v23, v127
v_fma_f32 v212, v92, v23, v128
v_fma_f32 v213, v93, v23, v129
v_cvt_pk_bf16_f32 v150, v210, v211
v_cvt_pk_bf16_f32 v151, v212, v213
v_max_f32 v10, v190, v191
s_waitcnt lgkmcnt(6)
v_mfma_f32_16x16x32_bf16 v[186:189], v[150:153], v[138:141], v[32:35]
s_waitcnt lgkmcnt(2)
v_max_f32 v44, v192, v193
v_fma_f32 v210, v94, v198, v130
v_mul_f32 v95, 0x3eae1a93, v95
v_mul_f32 v8, v254, v95
v_mul_f32 v9, v255, v95
s_nop 1
v_max3_f32 v10, v186, v187, v10
v_max3_f32 v44, v188, v189, v44
v_mul_f32 v10, v10, v8
v_mul_f32 v44, v44, v9
v_max_f32 v45, v10, v44
v_mov_b32 v46, v45
v_fma_f32 v211, v94, v199, v131
v_fma_f32 v212, v94, v200, v132
v_permlane16_swap_b32 v45, v46
v_max_f32 v45, v45, v46
v_mov_b32 v46, v45
s_nop 1
v_permlane32_swap_b32 v45, v46
v_max_f32 v45, v45, v46
v_sub_f32 v10, v10, v45
v_sub_f32 v44, v44, v45
v_fma_f32 v213, v94, v201, v133
v_exp_f32 v214, v10
v_exp_f32 v215, v44
s_nop 0
v_add_f32 v45, v214, v215
v_mov_b32 v46, v45
v_mul_f32 v214, v214, v254
v_cvt_pk_bf16_f32 v156, v210, v211
v_permlane16_swap_b32 v45, v46
v_add_f32 v45, v45, v46
v_mov_b32 v46, v45
s_waitcnt lgkmcnt(0)
s_barrier
v_mul_f32 v215, v215, v255
v_cvt_pk_bf16_f32 v157, v212, v213
v_permlane32_swap_b32 v45, v46
v_add_f32 v45, v45, v46
v_fma_f32 v210, v86, v23, v122
v_rcp_f32 v45, v45
s_nop 0
v_mul_f32 v214, v214, v45
v_mul_f32 v215, v215, v45
v_cvt_pk_bf16_f32 v214, v214, v214
v_cvt_pk_bf16_f32 v215, v215, v215
v_and_b32 v196, v214, v40
v_and_b32 v197, v215, v41
v_and_b32 v194, v214, v42
v_and_b32 v195, v215, v43
s_nop 1
v_mfma_f32_16x16x32_bf16 v[190:193], v[154:157], v[194:197], 0
v_mfma_f32_16x16x32_bf16 v[186:189], v[158:161], v[194:197], 0
s_nop 6
v_cvt_pk_bf16_f32 v104, v190, v191
v_cvt_pk_bf16_f32 v105, v192, v193
ds_write_b64 v18, v[104:105]
v_cvt_pk_bf16_f32 v214, v188, v189
v_fma_f32 v211, v87, v23, v123
ds_write_b32 v19, v214
v_fma_f32 v212, v88, v23, v124
v_fma_f32 v213, v89, v23, v125
v_cvt_pk_bf16_f32 v142, v210, v211
v_cvt_pk_bf16_f32 v143, v212, v213
s_waitcnt vmcnt(0)
s_waitcnt lgkmcnt(0)
s_barrier
s_mov_b64 exec, 1
global_store_dword v252, v48, s[38:39] sc1
s_mov_b64 exec, -1
s_waitcnt vmcnt(1)
s_barrier
v_add_u32 v48, 8, v48
v_mbcnt_lo_u32_b32 v252, -1, 0
v_mbcnt_hi_u32_b32 v252, -1, v252
v_add_lshl_u32 v252, v252, 0, 7
v_bfi_b32 v252, s69, v252, v47
s_mov_b64 exec, 0xff
global_store_dword v252, v48, s[38:39] sc1
s_mov_b64 exec, -1
s_cmp_lg_u32 s53, 0
s_cbranch_scc1 .Lqa_n93
v_add_lshl_u32 v253, v50, 0, 7
v_bfi_b32 v253, s69, v253, v49
global_load_dword v53, v253, s[38:39] sc1
s_waitcnt vmcnt(0)
v_sub_u32 v53, v53, v50
v_cmp_lt_u32 vcc, 0x7f, v53
s_cbranch_vccnz .Lqa_sp94
.Lqa_rt95:
v_add_u32 v49, 118784, v49
v_add_lshl_u32 v253, v50, 0, 7
v_bfi_b32 v253, s69, v253, v49
global_load_dword v53, v253, s[38:39] sc1
s_waitcnt vmcnt(0)
v_sub_u32 v53, v53, v50
v_cmp_lt_u32 vcc, 0x7f, v53
s_cbranch_vccnz .Lqa_sp96
.Lqa_rt97:
v_add_u32 v49, 118784, v49
v_add_lshl_u32 v253, v50, 0, 7
v_bfi_b32 v253, s69, v253, v49
global_load_dword v53, v253, s[38:39] sc1
s_waitcnt vmcnt(0)
v_sub_u32 v53, v53, v50
v_cmp_lt_u32 vcc, 0x7f, v53
s_cbranch_vccnz .Lqa_sp98
.Lqa_rt99:
v_add_u32 v49, 118784, v49
v_add_lshl_u32 v253, v50, 0, 7
v_bfi_b32 v253, s69, v253, v49
global_load_dword v53, v253, s[38:39] sc1
s_waitcnt vmcnt(0)
v_sub_u32 v53, v53, v50
v_cmp_lt_u32 vcc, 0x7f, v53
s_cbranch_vccnz .Lqa_sp100
.Lqa_rt101:
v_add_u32 v49, 118784, v49
s_load_dword s75, s[38:39], 0x0
s_waitcnt vmcnt(0) lgkmcnt(0)
s_add_u32 s75, s75, 1
v_mov_b32 v53, s75
v_mov_b32 v49, 0
global_store_dword v49, v53, s[38:39] sc1
.Lqa_n93:
s_endpgm
.Lqa_hk6:
s_setpc_b64 s[90:91]
.Lqa_k15:
s_branch .Lqa_k16
.Lqa_e8:
s_add_u32 s42, s42, s50
s_addc_u32 s43, s43, 0
s_mov_b64 s[76:77], -1
s_mov_b64 s[46:47], s[44:45]
s_mov_b32 s73, 17
s_add_u32 s90, s90, .Lqa_e9-.Lqa_e8
s_addc_u32 s91, s91, 0
s_branch .Lqa_hr7
.Lqa_k16:
s_branch .Lqa_k17
.Lqa_e9:
s_mov_b32 s46, -18432
s_mov_b32 s47, -1
v_or_b32 v234, s52, v234
v_or_b32 v235, s52, v235
v_or_b32 v236, s52, v236
v_or_b32 v237, s52, v237
s_mov_b32 s73, 16
s_add_u32 s90, s90, .Lqa_e10-.Lqa_e9
s_addc_u32 s91, s91, 0
s_branch .Lqa_hr7
.Lqa_k17:
s_branch .Lqa_k18
.Lqa_e10:
v_mov_b32 v234, v32
v_mov_b32 v235, v33
v_mov_b32 v236, v34
v_mov_b32 v237, v35
s_mov_b32 s73, 15
s_add_u32 s90, s90, .Lqa_e11-.Lqa_e10
s_addc_u32 s91, s91, 0
s_branch .Lqa_hr7
.Lqa_k18:
s_branch .Lqa_k19
.Lqa_e11:
v_subrev_u32 v16, 18432, v16
s_mov_b32 s73, 3
s_add_u32 s90, s90, .Lqa_e12-.Lqa_e11
s_addc_u32 s91, s91, 0
s_branch .Lqa_hr7
.Lqa_k19:
s_branch .Lqa_k20
.Lqa_e12:
s_mov_b32 s73, 2
s_add_u32 s90, s90, .Lqa_e13-.Lqa_e12
s_addc_u32 s91, s91, 0
s_branch .Lqa_hr7
.Lqa_k20:
s_branch .Lqa_k21
.Lqa_e13:
s_add_u32 s46, s46, s50
s_addc_u32 s47, s47, 0
s_mov_b32 s73, 0
s_add_u32 s90, s90, .Lqa_e14-.Lqa_e13
s_addc_u32 s91, s91, 0
s_branch .Lqa_hr7
.Lqa_k21:
s_branch .Lqa_bk22
.Lqa_e14:
v_or_b32 v32, s52, v32
v_or_b32 v33, s52, v33
v_or_b32 v34, s52, v34
v_or_b32 v35, s52, v35
s_mov_b32 s73, 999
s_nop 0
s_addc_u32 s91, s91, 0
s_branch .Lqa_hr7
.Lqa_hk27:
s_setpc_b64 s[90:91]
.Lqa_k36:
s_branch .Lqa_k37
.Lqa_e29:
s_add_u32 s42, s42, s50
s_addc_u32 s43, s43, 0
s_mov_b64 s[76:77], -1
s_mov_b64 s[46:47], s[44:45]
s_mov_b32 s73, 17
s_add_u32 s90, s90, .Lqa_e30-.Lqa_e29
s_addc_u32 s91, s91, 0
s_branch .Lqa_hr28
.Lqa_k37:
s_branch .Lqa_k38
.Lqa_e30:
s_mov_b32 s46, -18432
s_mov_b32 s47, -1
v_or_b32 v234, s52, v234
v_or_b32 v235, s52, v235
v_or_b32 v236, s52, v236
v_or_b32 v237, s52, v237
s_mov_b32 s73, 16
s_add_u32 s90, s90, .Lqa_e31-.Lqa_e30
s_addc_u32 s91, s91, 0
s_branch .Lqa_hr28
.Lqa_k38:
s_branch .Lqa_k39
.Lqa_e31:
v_mov_b32 v234, v32
v_mov_b32 v235, v33
v_mov_b32 v236, v34
v_mov_b32 v237, v35
s_mov_b32 s73, 15
s_add_u32 s90, s90, .Lqa_e32-.Lqa_e31
s_addc_u32 s91, s91, 0
s_branch .Lqa_hr28
.Lqa_k39:
s_branch .Lqa_k40
.Lqa_e32:
v_subrev_u32 v16, 18432, v16
s_mov_b32 s73, 3
s_add_u32 s90, s90, .Lqa_e33-.Lqa_e32
s_addc_u32 s91, s91, 0
s_branch .Lqa_hr28
.Lqa_k40:
s_branch .Lqa_k41
.Lqa_e33:
s_mov_b32 s73, 2
s_add_u32 s90, s90, .Lqa_e34-.Lqa_e33
s_addc_u32 s91, s91, 0
s_branch .Lqa_hr28
)"
R"(.Lqa_k41:
s_branch .Lqa_k42
.Lqa_e34:
s_add_u32 s46, s46, s50
s_addc_u32 s47, s47, 0
s_mov_b32 s73, 0
s_add_u32 s90, s90, .Lqa_e35-.Lqa_e34
s_addc_u32 s91, s91, 0
s_branch .Lqa_hr28
.Lqa_k42:
s_branch .Lqa_bk43
.Lqa_e35:
v_or_b32 v32, s52, v32
v_or_b32 v33, s52, v33
v_or_b32 v34, s52, v34
v_or_b32 v35, s52, v35
s_mov_b32 s73, 999
s_nop 0
s_addc_u32 s91, s91, 0
s_branch .Lqa_hr28
.Lqa_sp50:
s_sleep 1
v_add_lshl_u32 v253, v46, 0, 7
v_bfi_b32 v253, s69, v253, v44
global_load_dword v53, v253, s[38:39] sc1
s_add_u32 s71, s71, 1
s_waitcnt vmcnt(0)
v_sub_u32 v53, v53, v46
v_cmp_lt_u32 vcc, 0x7f, v53
s_and_b64 vcc, vcc, s[76:77]
s_cbranch_vccz .Lqa_rt51
s_cmp_lt_u32 s71, 3000000
s_cbranch_scc1 .Lqa_sp50
s_branch .Lqa_rt51
.Lqa_sp52:
s_sleep 1
v_add_lshl_u32 v253, v46, 0, 7
v_bfi_b32 v253, s69, v253, v44
global_load_dword v254, v253, s[38:39] sc1
s_add_u32 s71, s71, 1
s_waitcnt vmcnt(0)
v_sub_u32 v254, v254, v46
v_cmp_lt_u32 vcc, 0x7f, v254
s_and_b64 vcc, vcc, s[76:77]
s_cbranch_vccz .Lqa_rt53
s_cmp_lt_u32 s71, 3000000
s_cbranch_scc1 .Lqa_sp52
s_branch .Lqa_rt53
.Lqa_hk48:
s_setpc_b64 s[90:91]
.Lqa_k61:
s_branch .Lqa_k62
.Lqa_e54:
s_add_u32 s42, s42, s50
s_addc_u32 s43, s43, 0
s_mov_b64 s[76:77], -1
s_mov_b64 s[46:47], s[44:45]
s_mov_b32 s73, 17
s_add_u32 s90, s90, .Lqa_e55-.Lqa_e54
s_addc_u32 s91, s91, 0
s_branch .Lqa_hr49
.Lqa_k62:
s_branch .Lqa_k63
.Lqa_e55:
s_mov_b32 s46, -18432
s_mov_b32 s47, -1
v_or_b32 v202, s52, v202
v_or_b32 v203, s52, v203
v_or_b32 v204, s52, v204
v_or_b32 v205, s52, v205
s_mov_b32 s73, 16
s_add_u32 s90, s90, .Lqa_e56-.Lqa_e55
s_addc_u32 s91, s91, 0
s_branch .Lqa_hr49
.Lqa_k63:
s_branch .Lqa_k64
.Lqa_e56:
v_mov_b32 v202, v32
v_mov_b32 v203, v33
v_mov_b32 v204, v34
v_mov_b32 v205, v35
s_mov_b32 s73, 15
s_add_u32 s90, s90, .Lqa_e57-.Lqa_e56
s_addc_u32 s91, s91, 0
s_branch .Lqa_hr49
.Lqa_k64:
s_branch .Lqa_k65
.Lqa_e57:
v_subrev_u32 v16, 18432, v16
s_mov_b32 s73, 3
s_add_u32 s90, s90, .Lqa_e58-.Lqa_e57
s_addc_u32 s91, s91, 0
s_branch .Lqa_hr49
.Lqa_k65:
s_branch .Lqa_k66
.Lqa_e58:
v_add_u32 v45, s51, v45
s_mov_b32 s73, 2
s_add_u32 s90, s90, .Lqa_e59-.Lqa_e58
s_addc_u32 s91, s91, 0
s_branch .Lqa_hr49
.Lqa_k66:
s_branch .Lqa_k67
.Lqa_e59:
v_add_u32 v44, s51, v44
v_add_u32 v46, -32, v46
s_add_u32 s46, s46, s50
s_addc_u32 s47, s47, 0
s_mov_b32 s73, 0
s_add_u32 s90, s90, .Lqa_e60-.Lqa_e59
s_addc_u32 s91, s91, 0
s_branch .Lqa_hr49
.Lqa_k67:
s_branch .Lqa_bk68
.Lqa_e60:
v_or_b32 v32, s52, v32
v_or_b32 v33, s52, v33
v_or_b32 v34, s52, v34
v_or_b32 v35, s52, v35
s_mov_b32 s73, 999
s_nop 0
s_addc_u32 s91, s91, 0
s_branch .Lqa_hr49
.Lqa_hk73:
s_setpc_b64 s[90:91]
.Lqa_k82:
s_branch .Lqa_k83
.Lqa_e75:
s_add_u32 s42, s42, s50
s_addc_u32 s43, s43, 0
s_mov_b64 s[76:77], -1
s_mov_b64 s[46:47], s[44:45]
s_mov_b32 s73, 17
s_add_u32 s90, s90, .Lqa_e76-.Lqa_e75
s_addc_u32 s91, s91, 0
s_branch .Lqa_hr74
.Lqa_k83:
s_branch .Lqa_k84
.Lqa_e76:
s_mov_b32 s46, -18432
s_mov_b32 s47, -1
v_or_b32 v206, s52, v206
v_or_b32 v207, s52, v207
v_or_b32 v208, s52, v208
v_or_b32 v209, s52, v209
s_mov_b32 s73, 16
s_add_u32 s90, s90, .Lqa_e77-.Lqa_e76
s_addc_u32 s91, s91, 0
s_branch .Lqa_hr74
.Lqa_k84:
s_branch .Lqa_k85
.Lqa_e77:
v_mov_b32 v206, v32
v_mov_b32 v207, v33
v_mov_b32 v208, v34
v_mov_b32 v209, v35
s_mov_b32 s73, 15
s_add_u32 s90, s90, .Lqa_e78-.Lqa_e77
s_addc_u32 s91, s91, 0
s_branch .Lqa_hr74
.Lqa_k85:
s_branch .Lqa_k86
.Lqa_e78:
v_add_u32 v48, -1, v48
v_add_lshl_u32 v252, v48, 0, 7
v_bfi_b32 v252, s69, v252, v47
s_mov_b32 s73, 3
s_add_u32 s90, s90, .Lqa_e79-.Lqa_e78
s_addc_u32 s91, s91, 0
s_branch .Lqa_hr74
.Lqa_k86:
s_branch .Lqa_k87
.Lqa_e79:
s_mov_b32 s73, 2
s_add_u32 s90, s90, .Lqa_e80-.Lqa_e79
s_addc_u32 s91, s91, 0
s_branch .Lqa_hr74
.Lqa_k87:
s_branch .Lqa_k88
.Lqa_e80:
s_add_u32 s46, s46, s50
s_addc_u32 s47, s47, 0
s_mov_b32 s73, 0
s_add_u32 s90, s90, .Lqa_e81-.Lqa_e80
s_addc_u32 s91, s91, 0
s_branch .Lqa_hr74
.Lqa_k88:
s_branch .Lqa_bk89
.Lqa_e81:
v_or_b32 v32, s52, v32
v_or_b32 v33, s52, v33
v_or_b32 v34, s52, v34
v_or_b32 v35, s52, v35
s_mov_b32 s73, 999
s_nop 0
s_addc_u32 s91, s91, 0
s_branch .Lqa_hr74
.Lqa_sp94:
s_sleep 1
v_add_lshl_u32 v253, v50, 0, 7
v_bfi_b32 v253, s69, v253, v49
global_load_dword v53, v253, s[38:39] sc1
s_add_u32 s71, s71, 1
s_waitcnt vmcnt(0)
v_sub_u32 v53, v53, v50
v_cmp_lt_u32 vcc, 0x7f, v53
s_cbranch_vccz .Lqa_rt95
s_cmp_lt_u32 s71, 3000000
s_cbranch_scc1 .Lqa_sp94
s_branch .Lqa_rt95
.Lqa_sp96:
s_sleep 1
v_add_lshl_u32 v253, v50, 0, 7
v_bfi_b32 v253, s69, v253, v49
global_load_dword v53, v253, s[38:39] sc1
s_add_u32 s71, s71, 1
s_waitcnt vmcnt(0)
v_sub_u32 v53, v53, v50
v_cmp_lt_u32 vcc, 0x7f, v53
s_cbranch_vccz .Lqa_rt97
s_cmp_lt_u32 s71, 3000000
s_cbranch_scc1 .Lqa_sp96
s_branch .Lqa_rt97
.Lqa_sp98:
s_sleep 1
v_add_lshl_u32 v253, v50, 0, 7
v_bfi_b32 v253, s69, v253, v49
global_load_dword v53, v253, s[38:39] sc1
s_add_u32 s71, s71, 1
s_waitcnt vmcnt(0)
v_sub_u32 v53, v53, v50
v_cmp_lt_u32 vcc, 0x7f, v53
s_cbranch_vccz .Lqa_rt99
s_cmp_lt_u32 s71, 3000000
s_cbranch_scc1 .Lqa_sp98
s_branch .Lqa_rt99
.Lqa_sp100:
s_sleep 1
v_add_lshl_u32 v253, v50, 0, 7
v_bfi_b32 v253, s69, v253, v49
global_load_dword v53, v253, s[38:39] sc1
s_add_u32 s71, s71, 1
s_waitcnt vmcnt(0)
v_sub_u32 v53, v53, v50
v_cmp_lt_u32 vcc, 0x7f, v53
s_cbranch_vccz .Lqa_rt101
s_cmp_lt_u32 s71, 3000000
s_cbranch_scc1 .Lqa_sp100
s_branch .Lqa_rt101
.Le0:
)"
    :: "{v[0:31]}"(va), "{v[32:63]}"(vb), "{s[36:51]}"(sa), "{s[52:67]}"(sb) : "v255", "a255", "s99", "vcc", "memory");
    return;
  }
  if (wave == 2) {
    const unsigned o = min((lane & 7) * 1048576u, 5308412u);
    touch(wg + o); touch(wu + o); touch(wd + o); touch(out + min(o, 2359292u));
    touch(ws + min(lane * 2097152u, WS_END - 4));
    if (blockIdx.x - 64 < 464 * FL / 8192) touch(ws + WS_FL + ((blockIdx.x - 64) * 64 + lane) * 128);
  }
  if (ev) touch(wo + min((jj - 8) * 216 + threadIdx.x, 5183u) * 128);
  if (!ev && wave != 2) touch(x0 + min((xh * 32 + jj) * 144 + min(wave, 2u) * 64 + lane, 18431u) * 128);
  if (ev && jj < 10) {
    const unsigned wo_rho = xc >> 1, wo_cw = jj & 1;
    const unsigned r = lane & 15, g = lane >> 4, q = r >> 2, p = lane & 3;
    const unsigned rho = wo_rho, cw = wo_cw, row0 = rho * RP + PAD, pc0 = wave * 5;
    auto wv = [&](unsigned j) { const unsigned b = j * 1024 + lane * 16, rw = b / 576; return rw * 576 + b + 4096 - (j & 7) * 1024; };
    u32x32 va; u32x16 sa;
    va[0] = wv(0);
    va[1] = wv(1);
    va[2] = wv(2);
    va[3] = wv(3);
    va[4] = wv(4);
    va[5] = wv(5);
    va[6] = wv(6);
    va[7] = wv(7);
    va[8] = wv(8);
    va[9] = (8 * g + q) * 576 + cw * 64 + wave * 128 + p * 16;
    va[10] = (8 * g + q) * 576 + (cw ? 0 : 512) + p * 16 + (wave >> 1) * 8;
    va[11] = WS_O + (row0 - 2) * XT + pc0 * 1024 + 2048 + lane * 16;
    va[12] = WS_O + (row0 + 63) * XT + pc0 * 1024 + 2048 + lane * 16;
    va[13] = WS_X1 + (row0 - 5) * X1P + (cw * 10 + wave * 2) * 1024 + lane * 16;
    va[14] = WS_X1 + (row0 - 5) * X1P + (8 + cw) * 1024 + lane * 16;
    va[15] = WS_X1 + (row0 - 7) * X1P + XT + cw * 128 + r * 8;
    va[16] = ((rho * 32 + 31) * 16 + 0 + (lane >> 3)) * 1152 + (cw * 5 + wave) * 128 + (((lane ^ (lane >> 3)) & 7) << 4) - 0;
    va[17] = ((rho * 32 + 31) * 16 + 8 + (lane >> 3)) * 1152 + (cw * 5 + wave) * 128 + (((lane ^ (lane >> 3)) & 7) << 4) - 1024;
    va[18] = ((rho * 32 + 31) * 16 + r) * 1152 + (8 + cw) * 64 + g * 16;
    va[19] = 47104 + wave * 2048 + r * 128 + (((0 + g) ^ (r & 7)) << 4);
    va[20] = 47104 + wave * 2048 + r * 128 + (((4 + g) ^ (r & 7)) << 4);
    va[21] = WS_FL + rho * FR + FO + (lane & 7) * FL;
    va[22] = 0u - 2;
    va[23] = WS_FL + rho * FR + FX1 + cw * FL;
    va[24] = 0u - 6;
    va[25] = lane * 16;
    va[26] = lane * 16;
    va[27] = lane * 8;
    sa[0] = 4 + wave; sa[2] = (unsigned)(unsigned long long)ws; sa[3] = (unsigned)((unsigned long long)ws >> 32);
    sa[4] = uni((unsigned)(unsigned long long)(wo + wave * 16 * 1152 + cw * 576));
    sa[5] = uni((unsigned)((unsigned long long)(wo + wave * 16 * 1152 + cw * 576) >> 32));
    sa[6] = uni((unsigned)(unsigned long long)(x0));
    sa[7] = uni((unsigned)((unsigned long long)(x0) >> 32));
    sa[8] = uni((unsigned)(unsigned long long)(x0));
    sa[9] = uni((unsigned)((unsigned long long)(x0) >> 32));
    sa[10] = uni((unsigned)(unsigned long long)(ws + WS_X2));
    sa[11] = uni((unsigned)((unsigned long long)(ws + WS_X2) >> 32));
    sa[12] = uni(wave * 9216);
    sa[13] = uni(pc0 * 1024 + 2048);
    asm volatile(
R"(.Lb1:
s_movk_i32 s69, 0x380
s_mov_b32 s71, 0
s_mov_b32 s74, 0
s_load_dword s37, s[38:39], 0x0
s_nop 1
s_and_b32 s75, s36, 3
s_cmp_eq_u32 s75, 1
s_cbranch_scc1 .Lwo_w2
s_cmp_eq_u32 s75, 2
s_cbranch_scc1 .Lwo_w3
s_cmp_eq_u32 s75, 3
s_cbranch_scc1 .Lwo_w4
.Lwo_w1:
s_add_u32 m0, s48, 4096
s_nop 0
global_load_lds_dwordx4 v0, s[40:41] offset:-4096
global_load_lds_dwordx4 v1, s[40:41] offset:-3072
global_load_lds_dwordx4 v2, s[40:41] offset:-2048
global_load_lds_dwordx4 v3, s[40:41] offset:-1024
global_load_lds_dwordx4 v4, s[40:41] offset:0
global_load_lds_dwordx4 v5, s[40:41] offset:1024
global_load_lds_dwordx4 v6, s[40:41] offset:2048
global_load_lds_dwordx4 v7, s[40:41] offset:3072
s_add_u32 m0, s48, 12288
s_nop 0
global_load_lds_dwordx4 v8, s[40:41] offset:-4096
s_add_u32 s40, s40, 73728
s_addc_u32 s41, s41, 0
s_add_u32 m0, s48, 40960
s_nop 0
global_load_lds_dwordx4 v0, s[40:41] offset:-4096
global_load_lds_dwordx4 v1, s[40:41] offset:-3072
global_load_lds_dwordx4 v2, s[40:41] offset:-2048
global_load_lds_dwordx4 v3, s[40:41] offset:-1024
global_load_lds_dwordx4 v4, s[40:41] offset:0
global_load_lds_dwordx4 v5, s[40:41] offset:1024
global_load_lds_dwordx4 v6, s[40:41] offset:2048
global_load_lds_dwordx4 v7, s[40:41] offset:3072
s_add_u32 m0, s48, 49152
s_nop 0
global_load_lds_dwordx4 v8, s[40:41] offset:-4096
s_add_u32 s40, s40, 73728
s_addc_u32 s41, s41, 0
s_add_u32 m0, s48, 77824
s_nop 0
global_load_lds_dwordx4 v0, s[40:41] offset:-4096
global_load_lds_dwordx4 v1, s[40:41] offset:-3072
global_load_lds_dwordx4 v2, s[40:41] offset:-2048
global_load_lds_dwordx4 v3, s[40:41] offset:-1024
global_load_lds_dwordx4 v4, s[40:41] offset:0
global_load_lds_dwordx4 v5, s[40:41] offset:1024
global_load_lds_dwordx4 v6, s[40:41] offset:2048
global_load_lds_dwordx4 v7, s[40:41] offset:3072
s_add_u32 m0, s48, 86016
s_nop 0
global_load_lds_dwordx4 v8, s[40:41] offset:-4096
s_add_u32 s40, s40, 73728
s_addc_u32 s41, s41, 0
v_add_u32 v204, 0x10000, v9
v_add_u32 v205, 0x20000, v9
v_add_u32 v206, 0x10000, v10
v_add_u32 v207, 0x20000, v10
s_waitcnt lgkmcnt(0)
s_mul_i32 s37, s37, 0x9E3779B1
s_add_u32 s37, s37, 0x7F4A7C15
v_add_u32 v22, s37, v22
v_add_u32 v24, s37, v24
s_waitcnt vmcnt(18)
s_barrier
s_add_u32 m0, s48, 114688
ds_read_b64_tr_b16 a[0:1], v9 offset:0
ds_read_b64_tr_b16 a[2:3], v9 offset:2304
global_load_lds_dwordx4 v0, s[40:41] offset:-4096
ds_read_b64_tr_b16 a[4:5], v9 offset:8
ds_read_b64_tr_b16 a[6:7], v9 offset:2312
global_load_lds_dwordx4 v1, s[40:41] offset:-3072
ds_read_b64_tr_b16 a[8:9], v9 offset:64
global_load_lds_dwordx4 v2, s[40:41] offset:-2048
ds_read_b64_tr_b16 a[10:11], v9 offset:2368
ds_read_b64_tr_b16 a[12:13], v9 offset:72
global_load_lds_dwordx4 v3, s[40:41] offset:-1024
ds_read_b64_tr_b16 a[14:15], v9 offset:2376
global_load_lds_dwordx4 v4, s[40:41] offset:0
ds_read_b64_tr_b16 v[60:61], v10 offset:0
ds_read_b64_tr_b16 v[62:63], v10 offset:2304
global_load_lds_dwordx4 v5, s[40:41] offset:1024
ds_read_b64_tr_b16 a[16:17], v9 offset:18432
global_load_lds_dwordx4 v6, s[40:41] offset:2048
ds_read_b64_tr_b16 a[18:19], v9 offset:20736
ds_read_b64_tr_b16 a[20:21], v9 offset:18440
global_load_lds_dwordx4 v7, s[40:41] offset:3072
ds_read_b64_tr_b16 a[22:23], v9 offset:20744
s_add_u32 m0, s48, 122880
ds_read_b64_tr_b16 a[24:25], v9 offset:18496
ds_read_b64_tr_b16 a[26:27], v9 offset:20800
global_load_lds_dwordx4 v8, s[40:41] offset:-4096
ds_read_b64_tr_b16 a[28:29], v9 offset:18504
s_add_u32 s40, s40, 73728
ds_read_b64_tr_b16 a[30:31], v9 offset:20808
ds_read_b64_tr_b16 v[64:65], v10 offset:18432
s_addc_u32 s41, s41, 0
ds_read_b64_tr_b16 v[66:67], v10 offset:20736
s_waitcnt vmcnt(18) lgkmcnt(0)
s_barrier
s_add_u32 m0, s48, 4096
ds_read_b64_tr_b16 a[32:33], v9 offset:36864
ds_read_b64_tr_b16 a[34:35], v9 offset:39168
global_load_lds_dwordx4 v0, s[40:41] offset:-4096
ds_read_b64_tr_b16 a[36:37], v9 offset:36872
ds_read_b64_tr_b16 a[38:39], v9 offset:39176
global_load_lds_dwordx4 v1, s[40:41] offset:-3072
ds_read_b64_tr_b16 a[40:41], v9 offset:36928
global_load_lds_dwordx4 v2, s[40:41] offset:-2048
ds_read_b64_tr_b16 a[42:43], v9 offset:39232
ds_read_b64_tr_b16 a[44:45], v9 offset:36936
global_load_lds_dwordx4 v3, s[40:41] offset:-1024
ds_read_b64_tr_b16 a[46:47], v9 offset:39240
global_load_lds_dwordx4 v4, s[40:41] offset:0
ds_read_b64_tr_b16 v[68:69], v10 offset:36864
ds_read_b64_tr_b16 v[70:71], v10 offset:39168
global_load_lds_dwordx4 v5, s[40:41] offset:1024
ds_read_b64_tr_b16 a[48:49], v9 offset:55296
global_load_lds_dwordx4 v6, s[40:41] offset:2048
ds_read_b64_tr_b16 a[50:51], v9 offset:57600
ds_read_b64_tr_b16 a[52:53], v9 offset:55304
global_load_lds_dwordx4 v7, s[40:41] offset:3072
ds_read_b64_tr_b16 a[54:55], v9 offset:57608
s_add_u32 m0, s48, 12288
ds_read_b64_tr_b16 a[56:57], v9 offset:55360
ds_read_b64_tr_b16 a[58:59], v9 offset:57664
global_load_lds_dwordx4 v8, s[40:41] offset:-4096
ds_read_b64_tr_b16 a[60:61], v9 offset:55368
s_add_u32 s40, s40, 73728
ds_read_b64_tr_b16 a[62:63], v9 offset:57672
ds_read_b64_tr_b16 v[72:73], v10 offset:55296
s_addc_u32 s41, s41, 0
ds_read_b64_tr_b16 v[74:75], v10 offset:57600
s_waitcnt vmcnt(18) lgkmcnt(0)
s_barrier
s_add_u32 m0, s48, 40960
ds_read_b64_tr_b16 a[64:65], v204 offset:8192
ds_read_b64_tr_b16 a[66:67], v204 offset:10496
global_load_lds_dwordx4 v0, s[40:41] offset:-4096
ds_read_b64_tr_b16 a[68:69], v204 offset:8200
ds_read_b64_tr_b16 a[70:71], v204 offset:10504
global_load_lds_dwordx4 v1, s[40:41] offset:-3072
ds_read_b64_tr_b16 a[72:73], v204 offset:8256
global_load_lds_dwordx4 v2, s[40:41] offset:-2048
ds_read_b64_tr_b16 a[74:75], v204 offset:10560
ds_read_b64_tr_b16 a[76:77], v204 offset:8264
global_load_lds_dwordx4 v3, s[40:41] offset:-1024
ds_read_b64_tr_b16 a[78:79], v204 offset:10568
global_load_lds_dwordx4 v4, s[40:41] offset:0
ds_read_b64_tr_b16 v[76:77], v206 offset:8192
ds_read_b64_tr_b16 v[78:79], v206 offset:10496
global_load_lds_dwordx4 v5, s[40:41] offset:1024
ds_read_b64_tr_b16 a[80:81], v204 offset:26624
global_load_lds_dwordx4 v6, s[40:41] offset:2048
ds_read_b64_tr_b16 a[82:83], v204 offset:28928
ds_read_b64_tr_b16 a[84:85], v204 offset:26632
global_load_lds_dwordx4 v7, s[40:41] offset:3072
ds_read_b64_tr_b16 a[86:87], v204 offset:28936
s_add_u32 m0, s48, 49152
ds_read_b64_tr_b16 a[88:89], v204 offset:26688
ds_read_b64_tr_b16 a[90:91], v204 offset:28992
global_load_lds_dwordx4 v8, s[40:41] offset:-4096
ds_read_b64_tr_b16 a[92:93], v204 offset:26696
s_add_u32 s40, s40, 73728
ds_read_b64_tr_b16 a[94:95], v204 offset:29000
ds_read_b64_tr_b16 v[80:81], v206 offset:26624
s_addc_u32 s41, s41, 0
ds_read_b64_tr_b16 v[82:83], v206 offset:28928
s_waitcnt vmcnt(18) lgkmcnt(0)
s_barrier
s_add_u32 m0, s48, 77824
ds_read_b64_tr_b16 a[96:97], v204 offset:45056
ds_read_b64_tr_b16 a[98:99], v204 offset:47360
global_load_lds_dwordx4 v0, s[40:41] offset:-4096
ds_read_b64_tr_b16 a[100:101], v204 offset:45064
ds_read_b64_tr_b16 a[102:103], v204 offset:47368
global_load_lds_dwordx4 v1, s[40:41] offset:-3072
ds_read_b64_tr_b16 a[104:105], v204 offset:45120
global_load_lds_dwordx4 v2, s[40:41] offset:-2048
ds_read_b64_tr_b16 a[106:107], v204 offset:47424
ds_read_b64_tr_b16 a[108:109], v204 offset:45128
global_load_lds_dwordx4 v3, s[40:41] offset:-1024
ds_read_b64_tr_b16 a[110:111], v204 offset:47432
global_load_lds_dwordx4 v4, s[40:41] offset:0
ds_read_b64_tr_b16 v[84:85], v206 offset:45056
ds_read_b64_tr_b16 v[86:87], v206 offset:47360
global_load_lds_dwordx4 v5, s[40:41] offset:1024
ds_read_b64_tr_b16 a[112:113], v204 offset:63488
global_load_lds_dwordx4 v6, s[40:41] offset:2048
ds_read_b64_tr_b16 a[114:115], v205 offset:256
ds_read_b64_tr_b16 a[116:117], v204 offset:63496
global_load_lds_dwordx4 v7, s[40:41] offset:3072
ds_read_b64_tr_b16 a[118:119], v205 offset:264
s_add_u32 m0, s48, 86016
ds_read_b64_tr_b16 a[120:121], v204 offset:63552
ds_read_b64_tr_b16 a[122:123], v205 offset:320
global_load_lds_dwordx4 v8, s[40:41] offset:-4096
ds_read_b64_tr_b16 a[124:125], v204 offset:63560
s_add_u32 s40, s40, 73728
ds_read_b64_tr_b16 a[126:127], v205 offset:328
ds_read_b64_tr_b16 v[88:89], v206 offset:63488
s_addc_u32 s41, s41, 0
ds_read_b64_tr_b16 v[90:91], v207 offset:256
s_waitcnt vmcnt(18) lgkmcnt(0)
s_barrier
s_add_u32 m0, s48, 114688
ds_read_b64_tr_b16 a[128:129], v9 offset:0
ds_read_b64_tr_b16 a[130:131], v9 offset:2304
global_load_lds_dwordx4 v0, s[40:41] offset:-4096
ds_read_b64_tr_b16 a[132:133], v9 offset:8
global_load_lds_dwordx4 v1, s[40:41] offset:-3072
ds_read_b64_tr_b16 a[134:135], v9 offset:2312
ds_read_b64_tr_b16 a[136:137], v9 offset:64
global_load_lds_dwordx4 v2, s[40:41] offset:-2048
ds_read_b64_tr_b16 a[138:139], v9 offset:2368
global_load_lds_dwordx4 v3, s[40:41] offset:-1024
ds_read_b64_tr_b16 a[140:141], v9 offset:72
global_load_lds_dwordx4 v4, s[40:41] offset:0
ds_read_b64_tr_b16 a[142:143], v9 offset:2376
ds_read_b64_tr_b16 v[92:93], v10 offset:0
global_load_lds_dwordx4 v5, s[40:41] offset:1024
ds_read_b64_tr_b16 v[94:95], v10 offset:2304
global_load_lds_dwordx4 v6, s[40:41] offset:2048
ds_read_b64_tr_b16 a[144:145], v9 offset:18432
ds_read_b64_tr_b16 a[146:147], v9 offset:20736
global_load_lds_dwordx4 v7, s[40:41] offset:3072
ds_read_b64_tr_b16 a[148:149], v9 offset:18440
s_add_u32 m0, s48, 122880
ds_read_b64_tr_b16 a[150:151], v9 offset:20744
global_load_lds_dwordx4 v8, s[40:41] offset:-4096
ds_read_b64_tr_b16 a[152:153], v9 offset:18496
ds_read_b64_tr_b16 a[154:155], v9 offset:20800
s_add_u32 s40, s40, 73728
ds_read_b64_tr_b16 a[156:157], v9 offset:18504
s_addc_u32 s41, s41, 0
ds_read_b64_tr_b16 a[158:159], v9 offset:20808
s_waitcnt vmcnt(18) lgkmcnt(0)
s_barrier
s_add_u32 m0, s48, 4096
ds_read_b64_tr_b16 a[160:161], v9 offset:36864
ds_read_b64_tr_b16 a[162:163], v9 offset:39168
global_load_lds_dwordx4 v0, s[40:41] offset:-4096
ds_read_b64_tr_b16 a[164:165], v9 offset:36872
global_load_lds_dwordx4 v1, s[40:41] offset:-3072
ds_read_b64_tr_b16 a[166:167], v9 offset:39176
global_load_lds_dwordx4 v2, s[40:41] offset:-2048
ds_read_b64_tr_b16 a[168:169], v9 offset:36928
global_load_lds_dwordx4 v3, s[40:41] offset:-1024
ds_read_b64_tr_b16 a[170:171], v9 offset:39232
ds_read_b64_tr_b16 a[172:173], v9 offset:36936
global_load_lds_dwordx4 v4, s[40:41] offset:0
ds_read_b64_tr_b16 a[174:175], v9 offset:39240
global_load_lds_dwordx4 v5, s[40:41] offset:1024
ds_read_b64_tr_b16 a[176:177], v9 offset:55296
global_load_lds_dwordx4 v6, s[40:41] offset:2048
ds_read_b64_tr_b16 a[178:179], v9 offset:57600
global_load_lds_dwordx4 v7, s[40:41] offset:3072
ds_read_b64_tr_b16 a[180:181], v9 offset:55304
ds_read_b64_tr_b16 a[182:183], v9 offset:57608
s_add_u32 m0, s48, 12288
ds_read_b64_tr_b16 a[184:185], v9 offset:55360
global_load_lds_dwordx4 v8, s[40:41] offset:-4096
ds_read_b64_tr_b16 a[186:187], v9 offset:57664
s_sub_u32 s40, s40, 589824
ds_read_b64_tr_b16 a[188:189], v9 offset:55368
s_subb_u32 s41, s41, 0
ds_read_b64_tr_b16 a[190:191], v9 offset:57672
s_waitcnt vmcnt(18) lgkmcnt(0)
s_barrier
ds_read_b64_tr_b16 a[192:193], v204 offset:8192
ds_read_b64_tr_b16 a[194:195], v204 offset:10496
ds_read_b64_tr_b16 a[196:197], v204 offset:8200
ds_read_b64_tr_b16 a[198:199], v204 offset:10504
ds_read_b64_tr_b16 a[200:201], v204 offset:8256
ds_read_b64_tr_b16 a[202:203], v204 offset:10560
ds_read_b64_tr_b16 a[204:205], v204 offset:8264
ds_read_b64_tr_b16 a[206:207], v204 offset:10568
ds_read_b64_tr_b16 a[208:209], v204 offset:26624
ds_read_b64_tr_b16 a[210:211], v204 offset:28928
ds_read_b64_tr_b16 a[212:213], v204 offset:26632
ds_read_b64_tr_b16 a[214:215], v204 offset:28936
ds_read_b64_tr_b16 a[216:217], v204 offset:26688
ds_read_b64_tr_b16 a[218:219], v204 offset:28992
ds_read_b64_tr_b16 a[220:221], v204 offset:26696
ds_read_b64_tr_b16 a[222:223], v204 offset:29000
s_waitcnt vmcnt(9) lgkmcnt(0)
s_barrier
ds_read_b64_tr_b16 a[224:225], v204 offset:45056
ds_read_b64_tr_b16 a[226:227], v204 offset:47360
ds_read_b64_tr_b16 a[228:229], v204 offset:45064
ds_read_b64_tr_b16 a[230:231], v204 offset:47368
ds_read_b64_tr_b16 a[232:233], v204 offset:45120
ds_read_b64_tr_b16 a[234:235], v204 offset:47424
ds_read_b64_tr_b16 a[236:237], v204 offset:45128
ds_read_b64_tr_b16 a[238:239], v204 offset:47432
ds_read_b64_tr_b16 a[240:241], v204 offset:63488
ds_read_b64_tr_b16 a[242:243], v205 offset:256
ds_read_b64_tr_b16 a[244:245], v204 offset:63496
ds_read_b64_tr_b16 a[246:247], v205 offset:264
ds_read_b64_tr_b16 a[248:249], v204 offset:63552
ds_read_b64_tr_b16 a[250:251], v205 offset:320
ds_read_b64_tr_b16 a[252:253], v204 offset:63560
ds_read_b64_tr_b16 a[254:255], v205 offset:328
s_waitcnt vmcnt(0) lgkmcnt(0)
s_barrier
ds_read_b64_tr_b16 v[28:29], v9 offset:0
ds_read_b64_tr_b16 v[30:31], v9 offset:2304
ds_read_b64_tr_b16 v[32:33], v9 offset:8
ds_read_b64_tr_b16 v[34:35], v9 offset:2312
ds_read_b64_tr_b16 v[36:37], v9 offset:64
ds_read_b64_tr_b16 v[38:39], v9 offset:2368
ds_read_b64_tr_b16 v[40:41], v9 offset:72
ds_read_b64_tr_b16 v[42:43], v9 offset:2376
ds_read_b64_tr_b16 v[44:45], v9 offset:18432
ds_read_b64_tr_b16 v[46:47], v9 offset:20736
ds_read_b64_tr_b16 v[48:49], v9 offset:18440
ds_read_b64_tr_b16 v[50:51], v9 offset:20744
ds_read_b64_tr_b16 v[52:53], v9 offset:18496
ds_read_b64_tr_b16 v[54:55], v9 offset:20800
ds_read_b64_tr_b16 v[56:57], v9 offset:18504
ds_read_b64_tr_b16 v[58:59], v9 offset:20808
s_waitcnt lgkmcnt(0)
s_barrier
v_add_lshl_u32 v252, v24, 0, 7
v_bfi_b32 v252, s69, v252, v23
v_add_lshl_u32 v253, v22, 0, 7
v_bfi_b32 v253, s69, v253, v21
s_mov_b32 s92, 0x01010101
s_mov_b32 s93, 0x01010101
s_mov_b64 s[76:77], 0
s_add_u32 m0, s49, 18432
v_add_u32 v24, 1, v24
v_add_lshl_u32 v252, v24, 0, 7
v_bfi_b32 v252, s69, v252, v23
v_add_u32 v13, 18688, v13
s_xor_b32 s49, s49, 0x10000
s_mov_b32 m0, 47104
s_add_u32 m0, s49, 0
v_add_u32 v11, 18432, v11
v_add_u32 v24, 1, v24
v_add_lshl_u32 v252, v24, 0, 7
v_bfi_b32 v252, s69, v252, v23
s_mov_b32 m0, 55296
v_add_u32 v13, 18688, v13
s_add_u32 m0, s49, 18432
s_mov_b64 s[76:77], -1
s_mov_b32 s70, 33
s_waitcnt vmcnt(0) lgkmcnt(0)
s_nop 7
s_branch .Lwo_in5
.p2align 6
.Lwo_top6:
s_waitcnt lgkmcnt(2)
v_mfma_f32_16x16x32_bf16 v[96:99], a[0:3], v[132:135], v[96:99]
ds_read_b128 v[144:147], v25 offset:3072
v_mfma_f32_16x16x32_bf16 v[100:103], a[4:7], v[132:135], v[100:103]
v_mfma_f32_16x16x32_bf16 v[104:107], a[8:11], v[132:135], v[104:107]
s_waitcnt vmcnt(7)
s_barrier
v_mfma_f32_16x16x32_bf16 v[108:111], a[12:15], v[132:135], v[108:111]
v_cvt_pk_bf16_f32 v164, v112, v113
v_cvt_pk_bf16_f32 v165, v114, v115
s_mov_b64 exec, 1
global_store_dword v252, v24, s[38:39] sc1
s_mov_b64 exec, -1
v_mfma_f32_16x16x32_bf16 v[128:131], v[60:63], v[132:135], 0
s_waitcnt lgkmcnt(2)
v_cvt_pk_bf16_f32 v166, v116, v117
v_cvt_pk_bf16_f32 v167, v118, v119
v_add_u32 v24, 1, v24
v_add_lshl_u32 v252, v24, 0, 7
v_mfma_f32_16x16x32_bf16 v[96:99], a[16:19], v[136:139], v[96:99]
ds_read_b128 v[132:135], v25 offset:4096
v_bfi_b32 v252, s69, v252, v23
v_mfma_f32_16x16x32_bf16 v[100:103], a[20:23], v[136:139], v[100:103]
global_store_dwordx4 v13, v[164:167], s[38:39] offset:0 sc1
v_mfma_f32_16x16x32_bf16 v[104:107], a[24:27], v[136:139], v[104:107]
v_cvt_pk_bf16_f32 v168, v120, v121
v_cvt_pk_bf16_f32 v169, v122, v123
v_mfma_f32_16x16x32_bf16 v[108:111], a[28:31], v[136:139], v[108:111]
v_cvt_pk_bf16_f32 v170, v124, v125
v_cvt_pk_bf16_f32 v171, v126, v127
v_mfma_f32_16x16x32_bf16 v[128:131], v[64:67], v[136:139], v[128:131]
s_waitcnt lgkmcnt(2)
global_store_dwordx4 v13, v[168:171], s[38:39] offset:1024 sc1
v_mfma_f32_16x16x32_bf16 v[96:99], a[32:35], v[140:143], v[96:99]
s_cmp_eq_u32 s70, 32
ds_read_b128 v[136:139], v25 offset:5120
v_add_u32 v13, 18688, v13
v_mfma_f32_16x16x32_bf16 v[100:103], a[36:39], v[140:143], v[100:103]
s_cselect_b64 s[42:43], s[44:45], s[42:43]
v_add_f32 v254, v112, v113
v_mul_f32 v255, v112, v112
v_mfma_f32_16x16x32_bf16 v[104:107], a[40:43], v[140:143], v[104:107]
s_cmp_eq_u32 s70, 16
)"
R"(v_fmac_f32 v255, v113, v113
v_add_f32 v254, v254, v114
v_mfma_f32_16x16x32_bf16 v[108:111], a[44:47], v[140:143], v[108:111]
s_cselect_b64 s[42:43], s[46:47], s[42:43]
v_fmac_f32 v255, v114, v114
v_add_f32 v254, v254, v115
v_mfma_f32_16x16x32_bf16 v[128:131], v[68:71], v[140:143], v[128:131]
s_waitcnt lgkmcnt(2)
v_fmac_f32 v255, v115, v115
v_add_f32 v254, v254, v116
v_mfma_f32_16x16x32_bf16 v[96:99], a[48:51], v[144:147], v[96:99]
s_xor_b32 s49, s49, 0x10000
ds_read_b128 v[140:143], v25 offset:6144
v_fmac_f32 v255, v116, v116
v_mfma_f32_16x16x32_bf16 v[100:103], a[52:55], v[144:147], v[100:103]
s_mov_b32 m0, 47104
v_add_f32 v254, v254, v117
v_fmac_f32 v255, v117, v117
v_mfma_f32_16x16x32_bf16 v[104:107], a[56:59], v[144:147], v[104:107]
v_add_f32 v254, v254, v118
v_fmac_f32 v255, v118, v118
v_mfma_f32_16x16x32_bf16 v[108:111], a[60:63], v[144:147], v[108:111]
global_load_lds_dwordx4 v16, s[42:43] offset:0 sc1
v_add_f32 v254, v254, v119
v_mfma_f32_16x16x32_bf16 v[128:131], v[72:75], v[144:147], v[128:131]
s_waitcnt lgkmcnt(2)
v_fmac_f32 v255, v119, v119
v_add_f32 v254, v254, v120
v_mfma_f32_16x16x32_bf16 v[96:99], a[64:67], v[132:135], v[96:99]
ds_read_b128 v[144:147], v25 offset:7168
v_fmac_f32 v255, v120, v120
v_mfma_f32_16x16x32_bf16 v[100:103], a[68:71], v[132:135], v[100:103]
v_add_f32 v254, v254, v121
v_fmac_f32 v255, v121, v121
v_mfma_f32_16x16x32_bf16 v[104:107], a[72:75], v[132:135], v[104:107]
v_add_f32 v254, v254, v122
v_fmac_f32 v255, v122, v122
v_mfma_f32_16x16x32_bf16 v[108:111], a[76:79], v[132:135], v[108:111]
v_add_f32 v254, v254, v123
v_fmac_f32 v255, v123, v123
v_mfma_f32_16x16x32_bf16 v[128:131], v[76:79], v[132:135], v[128:131]
s_waitcnt vmcnt(9)
ds_read_b128 v[148:151], v19 offset:8192
ds_read_b128 v[152:155], v20 offset:8192
s_waitcnt lgkmcnt(4)
v_mfma_f32_16x16x32_bf16 v[96:99], a[80:83], v[136:139], v[96:99]
ds_read_b128 v[132:135], v25 offset:8192
global_load_lds_dwordx4 v17, s[42:43] offset:1024 sc1
v_mfma_f32_16x16x32_bf16 v[100:103], a[84:87], v[136:139], v[100:103]
v_add_f32 v254, v254, v124
v_fmac_f32 v255, v124, v124
v_mfma_f32_16x16x32_bf16 v[104:107], a[88:91], v[136:139], v[104:107]
v_add_f32 v254, v254, v125
v_fmac_f32 v255, v125, v125
v_mfma_f32_16x16x32_bf16 v[108:111], a[92:95], v[136:139], v[108:111]
v_add_f32 v254, v254, v126
v_fmac_f32 v255, v126, v126
v_mfma_f32_16x16x32_bf16 v[128:131], v[80:83], v[136:139], v[128:131]
s_waitcnt lgkmcnt(4)
v_add_f32 v254, v254, v127
v_fmac_f32 v255, v127, v127
v_mfma_f32_16x16x32_bf16 v[96:99], a[96:99], v[140:143], v[96:99]
s_add_u32 m0, s49, 0
ds_read_b128 v[136:139], v25 offset:9216
v_mfma_f32_16x16x32_bf16 v[100:103], a[100:103], v[140:143], v[100:103]
ds_write_b64 v27, v[254:255] offset:45056
v_mfma_f32_16x16x32_bf16 v[104:107], a[104:107], v[140:143], v[104:107]
s_waitcnt lgkmcnt(4)
v_lshlrev_b32 v112, 16, v148
v_and_b32 v113, 0xffff0000, v148
v_mfma_f32_16x16x32_bf16 v[108:111], a[108:111], v[140:143], v[108:111]
s_sub_u32 s42, s42, 18432
v_lshlrev_b32 v114, 16, v149
v_and_b32 v115, 0xffff0000, v149
v_mfma_f32_16x16x32_bf16 v[128:131], v[84:87], v[140:143], v[128:131]
v_lshlrev_b32 v116, 16, v150
v_and_b32 v117, 0xffff0000, v150
v_mfma_f32_16x16x32_bf16 v[96:99], a[112:115], v[144:147], v[96:99]
s_subb_u32 s43, s43, 0
ds_read_b128 v[140:143], v25 offset:10240
v_lshlrev_b32 v118, 16, v151
v_mfma_f32_16x16x32_bf16 v[100:103], a[116:119], v[144:147], v[100:103]
s_waitcnt lgkmcnt(4)
v_and_b32 v119, 0xffff0000, v151
v_lshlrev_b32 v120, 16, v152
v_mfma_f32_16x16x32_bf16 v[104:107], a[120:123], v[144:147], v[104:107]
v_and_b32 v121, 0xffff0000, v152
v_lshlrev_b32 v122, 16, v153
v_mfma_f32_16x16x32_bf16 v[108:111], a[124:127], v[144:147], v[108:111]
v_and_b32 v123, 0xffff0000, v153
v_lshlrev_b32 v124, 16, v154
v_mfma_f32_16x16x32_bf16 v[128:131], v[88:91], v[144:147], v[128:131]
s_waitcnt lgkmcnt(3)
v_and_b32 v125, 0xffff0000, v154
v_lshlrev_b32 v126, 16, v155
v_mfma_f32_16x16x32_bf16 v[96:99], a[128:131], v[132:135], v[96:99]
ds_read_b128 v[144:147], v25 offset:11264
v_and_b32 v127, 0xffff0000, v155
v_mfma_f32_16x16x32_bf16 v[100:103], a[132:135], v[132:135], v[100:103]
v_mfma_f32_16x16x32_bf16 v[104:107], a[136:139], v[132:135], v[104:107]
v_mfma_f32_16x16x32_bf16 v[108:111], a[140:143], v[132:135], v[108:111]
v_mfma_f32_16x16x32_bf16 v[128:131], v[92:95], v[132:135], v[128:131]
s_waitcnt lgkmcnt(3)
v_mfma_f32_16x16x32_bf16 v[96:99], a[144:147], v[136:139], v[96:99]
ds_read_b128 v[132:135], v25 offset:12288
v_mfma_f32_16x16x32_bf16 v[100:103], a[148:151], v[136:139], v[100:103]
v_mfma_f32_16x16x32_bf16 v[104:107], a[152:155], v[136:139], v[104:107]
v_mfma_f32_16x16x32_bf16 v[108:111], a[156:159], v[136:139], v[108:111]
s_waitcnt lgkmcnt(2)
v_mfma_f32_16x16x32_bf16 v[96:99], a[160:163], v[140:143], v[96:99]
ds_read_b128 v[136:139], v25 offset:13312
v_mfma_f32_16x16x32_bf16 v[100:103], a[164:167], v[140:143], v[100:103]
v_mfma_f32_16x16x32_bf16 v[104:107], a[168:171], v[140:143], v[104:107]
ds_write_b128 v26, v[128:131] offset:36864
v_mfma_f32_16x16x32_bf16 v[108:111], a[172:175], v[140:143], v[108:111]
s_waitcnt lgkmcnt(3)
v_mfma_f32_16x16x32_bf16 v[96:99], a[176:179], v[144:147], v[96:99]
ds_read_b128 v[140:143], v25 offset:14336
v_mfma_f32_16x16x32_bf16 v[100:103], a[180:183], v[144:147], v[100:103]
v_mfma_f32_16x16x32_bf16 v[104:107], a[184:187], v[144:147], v[104:107]
s_barrier
v_mfma_f32_16x16x32_bf16 v[108:111], a[188:191], v[144:147], v[108:111]
s_waitcnt lgkmcnt(3)
global_load_lds_dwordx4 v11, s[38:39] offset:-2048 sc1
v_mfma_f32_16x16x32_bf16 v[96:99], a[192:195], v[132:135], v[96:99]
ds_read_b128 v[144:147], v25 offset:15360
v_mfma_f32_16x16x32_bf16 v[100:103], a[196:199], v[132:135], v[100:103]
v_mfma_f32_16x16x32_bf16 v[104:107], a[200:203], v[132:135], v[104:107]
global_load_lds_dwordx4 v11, s[38:39] offset:-1024 sc1
v_mfma_f32_16x16x32_bf16 v[108:111], a[204:207], v[132:135], v[108:111]
s_waitcnt lgkmcnt(3)
v_mfma_f32_16x16x32_bf16 v[96:99], a[208:211], v[136:139], v[96:99]
ds_read_b128 v[132:135], v25 offset:16384
v_mfma_f32_16x16x32_bf16 v[100:103], a[212:215], v[136:139], v[100:103]
global_load_lds_dwordx4 v11, s[38:39] offset:0 sc1
v_mfma_f32_16x16x32_bf16 v[104:107], a[216:219], v[136:139], v[104:107]
v_mfma_f32_16x16x32_bf16 v[108:111], a[220:223], v[136:139], v[108:111]
s_waitcnt lgkmcnt(2)
v_mfma_f32_16x16x32_bf16 v[96:99], a[224:227], v[140:143], v[96:99]
ds_read_b128 v[136:139], v25 offset:17408
global_load_lds_dwordx4 v11, s[38:39] offset:1024 sc1
v_mfma_f32_16x16x32_bf16 v[100:103], a[228:231], v[140:143], v[100:103]
v_mfma_f32_16x16x32_bf16 v[104:107], a[232:235], v[140:143], v[104:107]
v_mfma_f32_16x16x32_bf16 v[108:111], a[236:239], v[140:143], v[108:111]
s_waitcnt lgkmcnt(2)
global_load_lds_dwordx4 v11, s[38:39] offset:2048 sc1
v_add_u32 v11, 18432, v11
v_mfma_f32_16x16x32_bf16 v[96:99], a[240:243], v[144:147], v[96:99]
v_min_u32 v11, v11, v12
v_mfma_f32_16x16x32_bf16 v[100:103], a[244:247], v[144:147], v[100:103]
v_mfma_f32_16x16x32_bf16 v[104:107], a[248:251], v[144:147], v[104:107]
s_waitcnt vmcnt(10)
s_barrier
v_mfma_f32_16x16x32_bf16 v[108:111], a[252:255], v[144:147], v[108:111]
s_waitcnt lgkmcnt(1)
ds_read_b128 v[140:143], v25 offset:18432
v_mfma_f32_16x16x32_bf16 v[96:99], v[28:31], v[132:135], v[96:99]
ds_read_b128 v[144:147], v25 offset:19456
v_mfma_f32_16x16x32_bf16 v[100:103], v[32:35], v[132:135], v[100:103]
v_mfma_f32_16x16x32_bf16 v[104:107], v[36:39], v[132:135], v[104:107]
v_mfma_f32_16x16x32_bf16 v[108:111], v[40:43], v[132:135], v[108:111]
s_waitcnt lgkmcnt(2)
v_mfma_f32_16x16x32_bf16 v[96:99], v[44:47], v[136:139], v[96:99]
ds_read_b128 v[132:135], v25 offset:20480
v_mfma_f32_16x16x32_bf16 v[100:103], v[48:51], v[136:139], v[100:103]
v_mfma_f32_16x16x32_bf16 v[104:107], v[52:55], v[136:139], v[104:107]
v_mfma_f32_16x16x32_bf16 v[108:111], v[56:59], v[136:139], v[108:111]
s_waitcnt lgkmcnt(2)
v_mfma_f32_16x16x32_bf16 v[112:115], a[0:3], v[140:143], v[112:115]
ds_read_b128 v[136:139], v25 offset:21504
v_mfma_f32_16x16x32_bf16 v[116:119], a[4:7], v[140:143], v[116:119]
v_mfma_f32_16x16x32_bf16 v[120:123], a[8:11], v[140:143], v[120:123]
s_waitcnt vmcnt(7)
s_barrier
v_mfma_f32_16x16x32_bf16 v[124:127], a[12:15], v[140:143], v[124:127]
s_mov_b64 exec, 1
global_store_dword v252, v24, s[38:39] sc1
s_mov_b64 exec, -1
v_add_u32 v24, 1, v24
v_add_lshl_u32 v252, v24, 0, 7
v_mfma_f32_16x16x32_bf16 v[128:131], v[60:63], v[140:143], 0
s_waitcnt lgkmcnt(2)
v_bfi_b32 v252, s69, v252, v23
v_cvt_pk_bf16_f32 v164, v96, v97
v_mfma_f32_16x16x32_bf16 v[112:115], a[16:19], v[144:147], v[112:115]
ds_read_b128 v[140:143], v25 offset:22528
v_cvt_pk_bf16_f32 v165, v98, v99
v_mfma_f32_16x16x32_bf16 v[116:119], a[20:23], v[144:147], v[116:119]
v_cvt_pk_bf16_f32 v166, v100, v101
v_cvt_pk_bf16_f32 v167, v102, v103
v_mfma_f32_16x16x32_bf16 v[120:123], a[24:27], v[144:147], v[120:123]
global_store_dwordx4 v13, v[164:167], s[38:39] offset:0 sc1
v_mfma_f32_16x16x32_bf16 v[124:127], a[28:31], v[144:147], v[124:127]
v_cvt_pk_bf16_f32 v168, v104, v105
v_cvt_pk_bf16_f32 v169, v106, v107
v_mfma_f32_16x16x32_bf16 v[128:131], v[64:67], v[144:147], v[128:131]
s_waitcnt lgkmcnt(2)
v_cvt_pk_bf16_f32 v170, v108, v109
v_cvt_pk_bf16_f32 v171, v110, v111
v_mfma_f32_16x16x32_bf16 v[112:115], a[32:35], v[132:135], v[112:115]
s_mov_b32 m0, 55296
ds_read_b128 v[144:147], v25 offset:23552
v_add_f32 v254, v96, v97
v_mfma_f32_16x16x32_bf16 v[116:119], a[36:39], v[132:135], v[116:119]
global_store_dwordx4 v13, v[168:171], s[38:39] offset:1024 sc1
v_mfma_f32_16x16x32_bf16 v[120:123], a[40:43], v[132:135], v[120:123]
v_add_u32 v13, 18688, v13
v_mul_f32 v255, v96, v96
v_mfma_f32_16x16x32_bf16 v[124:127], a[44:47], v[132:135], v[124:127]
v_fmac_f32 v255, v97, v97
v_add_f32 v254, v254, v98
v_mfma_f32_16x16x32_bf16 v[128:131], v[68:71], v[132:135], v[128:131]
s_waitcnt lgkmcnt(2)
v_fmac_f32 v255, v98, v98
v_add_f32 v254, v254, v99
v_mfma_f32_16x16x32_bf16 v[112:115], a[48:51], v[136:139], v[112:115]
ds_read_b128 v[132:135], v25 offset:24576
v_fmac_f32 v255, v99, v99
v_mfma_f32_16x16x32_bf16 v[116:119], a[52:55], v[136:139], v[116:119]
v_add_f32 v254, v254, v100
v_fmac_f32 v255, v100, v100
v_mfma_f32_16x16x32_bf16 v[120:123], a[56:59], v[136:139], v[120:123]
v_add_f32 v254, v254, v101
v_fmac_f32 v255, v101, v101
v_mfma_f32_16x16x32_bf16 v[124:127], a[60:63], v[136:139], v[124:127]
global_load_lds_dwordx4 v16, s[42:43] offset:0 sc1
v_add_f32 v254, v254, v102
v_mfma_f32_16x16x32_bf16 v[128:131], v[72:75], v[136:139], v[128:131]
s_waitcnt lgkmcnt(2)
v_fmac_f32 v255, v102, v102
v_add_f32 v254, v254, v103
v_mfma_f32_16x16x32_bf16 v[112:115], a[64:67], v[140:143], v[112:115]
ds_read_b128 v[136:139], v25 offset:25600
v_fmac_f32 v255, v103, v103
v_mfma_f32_16x16x32_bf16 v[116:119], a[68:71], v[140:143], v[116:119]
v_add_f32 v254, v254, v104
v_fmac_f32 v255, v104, v104
v_mfma_f32_16x16x32_bf16 v[120:123], a[72:75], v[140:143], v[120:123]
v_add_f32 v254, v254, v105
v_fmac_f32 v255, v105, v105
v_mfma_f32_16x16x32_bf16 v[124:127], a[76:79], v[140:143], v[124:127]
v_add_f32 v254, v254, v106
v_fmac_f32 v255, v106, v106
v_mfma_f32_16x16x32_bf16 v[128:131], v[76:79], v[140:143], v[128:131]
s_waitcnt vmcnt(9)
ds_read_b128 v[148:151], v19 offset:0
ds_read_b128 v[152:155], v20 offset:0
s_waitcnt lgkmcnt(4)
v_mfma_f32_16x16x32_bf16 v[112:115], a[80:83], v[144:147], v[112:115]
ds_read_b128 v[140:143], v25 offset:26624
global_load_lds_dwordx4 v17, s[42:43] offset:1024 sc1
v_mfma_f32_16x16x32_bf16 v[116:119], a[84:87], v[144:147], v[116:119]
v_add_f32 v254, v254, v107
v_fmac_f32 v255, v107, v107
v_mfma_f32_16x16x32_bf16 v[120:123], a[88:91], v[144:147], v[120:123]
v_add_f32 v254, v254, v108
v_fmac_f32 v255, v108, v108
v_mfma_f32_16x16x32_bf16 v[124:127], a[92:95], v[144:147], v[124:127]
v_add_f32 v254, v254, v109
v_fmac_f32 v255, v109, v109
v_mfma_f32_16x16x32_bf16 v[128:131], v[80:83], v[144:147], v[128:131]
s_waitcnt lgkmcnt(4)
v_add_f32 v254, v254, v110
v_fmac_f32 v255, v110, v110
v_mfma_f32_16x16x32_bf16 v[112:115], a[96:99], v[132:135], v[112:115]
s_add_u32 m0, s49, 18432
ds_read_b128 v[144:147], v25 offset:27648
v_add_f32 v254, v254, v111
v_mfma_f32_16x16x32_bf16 v[116:119], a[100:103], v[132:135], v[116:119]
v_fmac_f32 v255, v111, v111
v_mfma_f32_16x16x32_bf16 v[120:123], a[104:107], v[132:135], v[120:123]
ds_write_b64 v27, v[254:255] offset:43008
v_mfma_f32_16x16x32_bf16 v[124:127], a[108:111], v[132:135], v[124:127]
s_sub_u32 s42, s42, 18432
s_waitcnt lgkmcnt(4)
v_lshlrev_b32 v96, 16, v148
v_and_b32 v97, 0xffff0000, v148
v_mfma_f32_16x16x32_bf16 v[128:131], v[84:87], v[132:135], v[128:131]
v_lshlrev_b32 v98, 16, v149
v_and_b32 v99, 0xffff0000, v149
v_mfma_f32_16x16x32_bf16 v[112:115], a[112:115], v[136:139], v[112:115]
s_subb_u32 s43, s43, 0
ds_read_b128 v[132:135], v25 offset:28672
v_lshlrev_b32 v100, 16, v150
v_mfma_f32_16x16x32_bf16 v[116:119], a[116:119], v[136:139], v[116:119]
v_and_b32 v101, 0xffff0000, v150
v_lshlrev_b32 v102, 16, v151
v_mfma_f32_16x16x32_bf16 v[120:123], a[120:123], v[136:139], v[120:123]
s_waitcnt lgkmcnt(4)
v_and_b32 v103, 0xffff0000, v151
v_lshlrev_b32 v104, 16, v152
v_mfma_f32_16x16x32_bf16 v[124:127], a[124:127], v[136:139], v[124:127]
v_and_b32 v105, 0xffff0000, v152
v_lshlrev_b32 v106, 16, v153
v_mfma_f32_16x16x32_bf16 v[128:131], v[88:91], v[136:139], v[128:131]
s_waitcnt lgkmcnt(3)
v_and_b32 v107, 0xffff0000, v153
v_lshlrev_b32 v108, 16, v154
v_mfma_f32_16x16x32_bf16 v[112:115], a[128:131], v[140:143], v[112:115]
ds_read_b128 v[136:139], v25 offset:29696
v_and_b32 v109, 0xffff0000, v154
v_mfma_f32_16x16x32_bf16 v[116:119], a[132:135], v[140:143], v[116:119]
v_lshlrev_b32 v110, 16, v155
v_and_b32 v111, 0xffff0000, v155
v_mfma_f32_16x16x32_bf16 v[120:123], a[136:139], v[140:143], v[120:123]
v_mfma_f32_16x16x32_bf16 v[124:127], a[140:143], v[140:143], v[124:127]
v_mfma_f32_16x16x32_bf16 v[128:131], v[92:95], v[140:143], v[128:131]
s_waitcnt lgkmcnt(3)
v_mfma_f32_16x16x32_bf16 v[112:115], a[144:147], v[144:147], v[112:115]
ds_read_b128 v[140:143], v25 offset:30720
v_mfma_f32_16x16x32_bf16 v[116:119], a[148:151], v[144:147], v[116:119]
v_mfma_f32_16x16x32_bf16 v[120:123], a[152:155], v[144:147], v[120:123]
v_mfma_f32_16x16x32_bf16 v[124:127], a[156:159], v[144:147], v[124:127]
s_waitcnt lgkmcnt(2)
v_mfma_f32_16x16x32_bf16 v[112:115], a[160:163], v[132:135], v[112:115]
ds_read_b128 v[144:147], v25 offset:31744
v_mfma_f32_16x16x32_bf16 v[116:119], a[164:167], v[132:135], v[116:119]
v_mfma_f32_16x16x32_bf16 v[120:123], a[168:171], v[132:135], v[120:123]
ds_write_b128 v26, v[128:131] offset:39936
v_mfma_f32_16x16x32_bf16 v[124:127], a[172:175], v[132:135], v[124:127]
s_waitcnt lgkmcnt(3)
v_mfma_f32_16x16x32_bf16 v[112:115], a[176:179], v[136:139], v[112:115]
ds_read_b128 v[132:135], v25 offset:32768
v_mfma_f32_16x16x32_bf16 v[116:119], a[180:183], v[136:139], v[116:119]
v_mfma_f32_16x16x32_bf16 v[120:123], a[184:187], v[136:139], v[120:123]
s_barrier
v_mfma_f32_16x16x32_bf16 v[124:127], a[188:191], v[136:139], v[124:127]
s_waitcnt lgkmcnt(3)
global_load_lds_dwordx4 v11, s[38:39] offset:-2048 sc1
v_mfma_f32_16x16x32_bf16 v[112:115], a[192:195], v[140:143], v[112:115]
s_mov_b64 s[76:77], -1
ds_read_b128 v[136:139], v25 offset:33792
v_mfma_f32_16x16x32_bf16 v[116:119], a[196:199], v[140:143], v[116:119]
v_mfma_f32_16x16x32_bf16 v[120:123], a[200:203], v[140:143], v[120:123]
global_load_lds_dwordx4 v11, s[38:39] offset:-1024 sc1
.Lwo_in5:
v_mfma_f32_16x16x32_bf16 v[124:127], a[204:207], v[140:143], v[124:127]
s_waitcnt lgkmcnt(3)
v_mfma_f32_16x16x32_bf16 v[112:115], a[208:211], v[144:147], v[112:115]
ds_read_b128 v[140:143], v25 offset:34816
v_mfma_f32_16x16x32_bf16 v[116:119], a[212:215], v[144:147], v[116:119]
global_load_lds_dwordx4 v11, s[38:39] offset:0 sc1
v_mfma_f32_16x16x32_bf16 v[120:123], a[216:219], v[144:147], v[120:123]
v_mfma_f32_16x16x32_bf16 v[124:127], a[220:223], v[144:147], v[124:127]
s_waitcnt lgkmcnt(2)
v_mfma_f32_16x16x32_bf16 v[112:115], a[224:227], v[132:135], v[112:115]
ds_read_b128 v[144:147], v25 offset:35840
global_load_lds_dwordx4 v11, s[38:39] offset:1024 sc1
v_mfma_f32_16x16x32_bf16 v[116:119], a[228:231], v[132:135], v[116:119]
v_mfma_f32_16x16x32_bf16 v[120:123], a[232:235], v[132:135], v[120:123]
v_mfma_f32_16x16x32_bf16 v[124:127], a[236:239], v[132:135], v[124:127]
s_waitcnt lgkmcnt(2)
v_xor_b32 v25, 0x10000, v25
global_load_lds_dwordx4 v11, s[38:39] offset:2048 sc1
v_mfma_f32_16x16x32_bf16 v[112:115], a[240:243], v[136:139], v[112:115]
v_add_u32 v11, 18432, v11
v_min_u32 v11, v11, v12
v_mfma_f32_16x16x32_bf16 v[116:119], a[244:247], v[136:139], v[116:119]
v_mfma_f32_16x16x32_bf16 v[120:123], a[248:251], v[136:139], v[120:123]
s_waitcnt vmcnt(10)
s_barrier
v_mfma_f32_16x16x32_bf16 v[124:127], a[252:255], v[136:139], v[124:127]
s_waitcnt lgkmcnt(1)
ds_read_b128 v[132:135], v25 offset:0
v_mfma_f32_16x16x32_bf16 v[112:115], v[28:31], v[140:143], v[112:115]
ds_read_b128 v[136:139], v25 offset:1024
v_mfma_f32_16x16x32_bf16 v[116:119], v[32:35], v[140:143], v[116:119]
v_mfma_f32_16x16x32_bf16 v[120:123], v[36:39], v[140:143], v[120:123]
v_mfma_f32_16x16x32_bf16 v[124:127], v[40:43], v[140:143], v[124:127]
s_waitcnt lgkmcnt(2)
v_mfma_f32_16x16x32_bf16 v[112:115], v[44:47], v[144:147], v[112:115]
ds_read_b128 v[140:143], v25 offset:2048
v_mfma_f32_16x16x32_bf16 v[116:119], v[48:51], v[144:147], v[116:119]
v_mfma_f32_16x16x32_bf16 v[120:123], v[52:55], v[144:147], v[120:123]
v_mfma_f32_16x16x32_bf16 v[124:127], v[56:59], v[144:147], v[124:127]
s_sub_u32 s70, s70, 1
s_cbranch_scc0 .Lwo_top6
s_waitcnt vmcnt(7)
s_barrier
s_mov_b64 exec, 1
global_store_dword v252, v24, s[38:39] sc1
s_mov_b64 exec, -1
v_add_u32 v24, 1, v24
v_add_lshl_u32 v252, v24, 0, 7
v_bfi_b32 v252, s69, v252, v23
v_cvt_pk_bf16_f32 v164, v112, v113
v_cvt_pk_bf16_f32 v165, v114, v115
v_cvt_pk_bf16_f32 v166, v116, v117
v_cvt_pk_bf16_f32 v167, v118, v119
global_store_dwordx4 v13, v[164:167], s[38:39] offset:0 sc1
v_cvt_pk_bf16_f32 v168, v120, v121
v_cvt_pk_bf16_f32 v169, v122, v123
v_cvt_pk_bf16_f32 v170, v124, v125
v_cvt_pk_bf16_f32 v171, v126, v127
global_store_dwordx4 v13, v[168:171], s[38:39] offset:1024 sc1
v_add_f32 v254, v112, v113
v_mul_f32 v255, v112, v112
v_fmac_f32 v255, v113, v113
v_add_f32 v254, v254, v114
v_fmac_f32 v255, v114, v114
)"
R"(v_add_f32 v254, v254, v115
v_fmac_f32 v255, v115, v115
v_add_f32 v254, v254, v116
v_fmac_f32 v255, v116, v116
v_add_f32 v254, v254, v117
v_fmac_f32 v255, v117, v117
v_add_f32 v254, v254, v118
v_fmac_f32 v255, v118, v118
v_add_f32 v254, v254, v119
v_fmac_f32 v255, v119, v119
v_add_f32 v254, v254, v120
v_fmac_f32 v255, v120, v120
v_add_f32 v254, v254, v121
v_fmac_f32 v255, v121, v121
v_add_f32 v254, v254, v122
v_fmac_f32 v255, v122, v122
v_add_f32 v254, v254, v123
v_fmac_f32 v255, v123, v123
v_add_f32 v254, v254, v124
v_fmac_f32 v255, v124, v124
v_add_f32 v254, v254, v125
v_fmac_f32 v255, v125, v125
v_add_f32 v254, v254, v126
v_fmac_f32 v255, v126, v126
v_add_f32 v254, v254, v127
v_fmac_f32 v255, v127, v127
ds_write_b64 v27, v[254:255] offset:45056
s_waitcnt vmcnt(0) lgkmcnt(0)
s_barrier
s_mov_b64 exec, 1
global_store_dword v252, v24, s[38:39] sc1
s_mov_b64 exec, -1
s_waitcnt vmcnt(0)
s_barrier
v_add_u32 v24, 8, v24
v_mbcnt_lo_u32_b32 v252, -1, 0
v_mbcnt_hi_u32_b32 v252, -1, v252
v_add_lshl_u32 v252, v252, 0, 7
v_bfi_b32 v252, s69, v252, v23
s_mov_b64 exec, 0xff
global_store_dword v252, v24, s[38:39] sc1
s_mov_b64 exec, -1
s_endpgm
.Lwo_w2:
s_add_u32 m0, s48, 4096
s_nop 0
global_load_lds_dwordx4 v0, s[40:41] offset:-4096
global_load_lds_dwordx4 v1, s[40:41] offset:-3072
global_load_lds_dwordx4 v2, s[40:41] offset:-2048
global_load_lds_dwordx4 v3, s[40:41] offset:-1024
global_load_lds_dwordx4 v4, s[40:41] offset:0
global_load_lds_dwordx4 v5, s[40:41] offset:1024
global_load_lds_dwordx4 v6, s[40:41] offset:2048
global_load_lds_dwordx4 v7, s[40:41] offset:3072
s_add_u32 m0, s48, 12288
s_nop 0
global_load_lds_dwordx4 v8, s[40:41] offset:-4096
s_add_u32 s40, s40, 73728
s_addc_u32 s41, s41, 0
s_add_u32 m0, s48, 40960
s_nop 0
global_load_lds_dwordx4 v0, s[40:41] offset:-4096
global_load_lds_dwordx4 v1, s[40:41] offset:-3072
global_load_lds_dwordx4 v2, s[40:41] offset:-2048
global_load_lds_dwordx4 v3, s[40:41] offset:-1024
global_load_lds_dwordx4 v4, s[40:41] offset:0
global_load_lds_dwordx4 v5, s[40:41] offset:1024
global_load_lds_dwordx4 v6, s[40:41] offset:2048
global_load_lds_dwordx4 v7, s[40:41] offset:3072
s_add_u32 m0, s48, 49152
s_nop 0
global_load_lds_dwordx4 v8, s[40:41] offset:-4096
s_add_u32 s40, s40, 73728
s_addc_u32 s41, s41, 0
s_add_u32 m0, s48, 77824
s_nop 0
global_load_lds_dwordx4 v0, s[40:41] offset:-4096
global_load_lds_dwordx4 v1, s[40:41] offset:-3072
global_load_lds_dwordx4 v2, s[40:41] offset:-2048
global_load_lds_dwordx4 v3, s[40:41] offset:-1024
global_load_lds_dwordx4 v4, s[40:41] offset:0
global_load_lds_dwordx4 v5, s[40:41] offset:1024
global_load_lds_dwordx4 v6, s[40:41] offset:2048
global_load_lds_dwordx4 v7, s[40:41] offset:3072
s_add_u32 m0, s48, 86016
s_nop 0
global_load_lds_dwordx4 v8, s[40:41] offset:-4096
s_add_u32 s40, s40, 73728
s_addc_u32 s41, s41, 0
v_add_u32 v204, 0x10000, v9
v_add_u32 v205, 0x20000, v9
v_add_u32 v206, 0x10000, v10
v_add_u32 v207, 0x20000, v10
s_waitcnt lgkmcnt(0)
s_mul_i32 s37, s37, 0x9E3779B1
s_add_u32 s37, s37, 0x7F4A7C15
v_add_u32 v22, s37, v22
v_add_u32 v24, s37, v24
s_waitcnt vmcnt(18)
s_barrier
s_add_u32 m0, s48, 114688
ds_read_b64_tr_b16 a[0:1], v9 offset:0
ds_read_b64_tr_b16 a[2:3], v9 offset:2304
global_load_lds_dwordx4 v0, s[40:41] offset:-4096
ds_read_b64_tr_b16 a[4:5], v9 offset:8
global_load_lds_dwordx4 v1, s[40:41] offset:-3072
ds_read_b64_tr_b16 a[6:7], v9 offset:2312
global_load_lds_dwordx4 v2, s[40:41] offset:-2048
ds_read_b64_tr_b16 a[8:9], v9 offset:64
global_load_lds_dwordx4 v3, s[40:41] offset:-1024
ds_read_b64_tr_b16 a[10:11], v9 offset:2368
ds_read_b64_tr_b16 a[12:13], v9 offset:72
global_load_lds_dwordx4 v4, s[40:41] offset:0
ds_read_b64_tr_b16 a[14:15], v9 offset:2376
global_load_lds_dwordx4 v5, s[40:41] offset:1024
ds_read_b64_tr_b16 a[16:17], v9 offset:18432
global_load_lds_dwordx4 v6, s[40:41] offset:2048
ds_read_b64_tr_b16 a[18:19], v9 offset:20736
global_load_lds_dwordx4 v7, s[40:41] offset:3072
ds_read_b64_tr_b16 a[20:21], v9 offset:18440
ds_read_b64_tr_b16 a[22:23], v9 offset:20744
s_add_u32 m0, s48, 122880
ds_read_b64_tr_b16 a[24:25], v9 offset:18496
global_load_lds_dwordx4 v8, s[40:41] offset:-4096
ds_read_b64_tr_b16 a[26:27], v9 offset:20800
s_add_u32 s40, s40, 73728
ds_read_b64_tr_b16 a[28:29], v9 offset:18504
s_addc_u32 s41, s41, 0
ds_read_b64_tr_b16 a[30:31], v9 offset:20808
s_waitcnt vmcnt(18) lgkmcnt(0)
s_barrier
s_add_u32 m0, s48, 4096
ds_read_b64_tr_b16 a[32:33], v9 offset:36864
ds_read_b64_tr_b16 a[34:35], v9 offset:39168
global_load_lds_dwordx4 v0, s[40:41] offset:-4096
ds_read_b64_tr_b16 a[36:37], v9 offset:36872
global_load_lds_dwordx4 v1, s[40:41] offset:-3072
ds_read_b64_tr_b16 a[38:39], v9 offset:39176
global_load_lds_dwordx4 v2, s[40:41] offset:-2048
ds_read_b64_tr_b16 a[40:41], v9 offset:36928
global_load_lds_dwordx4 v3, s[40:41] offset:-1024
ds_read_b64_tr_b16 a[42:43], v9 offset:39232
ds_read_b64_tr_b16 a[44:45], v9 offset:36936
global_load_lds_dwordx4 v4, s[40:41] offset:0
ds_read_b64_tr_b16 a[46:47], v9 offset:39240
global_load_lds_dwordx4 v5, s[40:41] offset:1024
ds_read_b64_tr_b16 a[48:49], v9 offset:55296
global_load_lds_dwordx4 v6, s[40:41] offset:2048
ds_read_b64_tr_b16 a[50:51], v9 offset:57600
global_load_lds_dwordx4 v7, s[40:41] offset:3072
ds_read_b64_tr_b16 a[52:53], v9 offset:55304
ds_read_b64_tr_b16 a[54:55], v9 offset:57608
s_add_u32 m0, s48, 12288
ds_read_b64_tr_b16 a[56:57], v9 offset:55360
global_load_lds_dwordx4 v8, s[40:41] offset:-4096
ds_read_b64_tr_b16 a[58:59], v9 offset:57664
s_add_u32 s40, s40, 73728
ds_read_b64_tr_b16 a[60:61], v9 offset:55368
s_addc_u32 s41, s41, 0
ds_read_b64_tr_b16 a[62:63], v9 offset:57672
s_waitcnt vmcnt(18) lgkmcnt(0)
s_barrier
s_add_u32 m0, s48, 40960
ds_read_b64_tr_b16 a[64:65], v204 offset:8192
ds_read_b64_tr_b16 a[66:67], v204 offset:10496
global_load_lds_dwordx4 v0, s[40:41] offset:-4096
ds_read_b64_tr_b16 a[68:69], v204 offset:8200
global_load_lds_dwordx4 v1, s[40:41] offset:-3072
ds_read_b64_tr_b16 a[70:71], v204 offset:10504
global_load_lds_dwordx4 v2, s[40:41] offset:-2048
ds_read_b64_tr_b16 a[72:73], v204 offset:8256
global_load_lds_dwordx4 v3, s[40:41] offset:-1024
ds_read_b64_tr_b16 a[74:75], v204 offset:10560
ds_read_b64_tr_b16 a[76:77], v204 offset:8264
global_load_lds_dwordx4 v4, s[40:41] offset:0
ds_read_b64_tr_b16 a[78:79], v204 offset:10568
global_load_lds_dwordx4 v5, s[40:41] offset:1024
ds_read_b64_tr_b16 a[80:81], v204 offset:26624
global_load_lds_dwordx4 v6, s[40:41] offset:2048
ds_read_b64_tr_b16 a[82:83], v204 offset:28928
global_load_lds_dwordx4 v7, s[40:41] offset:3072
ds_read_b64_tr_b16 a[84:85], v204 offset:26632
ds_read_b64_tr_b16 a[86:87], v204 offset:28936
s_add_u32 m0, s48, 49152
ds_read_b64_tr_b16 a[88:89], v204 offset:26688
global_load_lds_dwordx4 v8, s[40:41] offset:-4096
ds_read_b64_tr_b16 a[90:91], v204 offset:28992
s_add_u32 s40, s40, 73728
ds_read_b64_tr_b16 a[92:93], v204 offset:26696
s_addc_u32 s41, s41, 0
ds_read_b64_tr_b16 a[94:95], v204 offset:29000
s_waitcnt vmcnt(18) lgkmcnt(0)
s_barrier
s_add_u32 m0, s48, 77824
ds_read_b64_tr_b16 a[96:97], v204 offset:45056
ds_read_b64_tr_b16 a[98:99], v204 offset:47360
global_load_lds_dwordx4 v0, s[40:41] offset:-4096
ds_read_b64_tr_b16 a[100:101], v204 offset:45064
global_load_lds_dwordx4 v1, s[40:41] offset:-3072
ds_read_b64_tr_b16 a[102:103], v204 offset:47368
global_load_lds_dwordx4 v2, s[40:41] offset:-2048
ds_read_b64_tr_b16 a[104:105], v204 offset:45120
global_load_lds_dwordx4 v3, s[40:41] offset:-1024
ds_read_b64_tr_b16 a[106:107], v204 offset:47424
ds_read_b64_tr_b16 a[108:109], v204 offset:45128
global_load_lds_dwordx4 v4, s[40:41] offset:0
ds_read_b64_tr_b16 a[110:111], v204 offset:47432
global_load_lds_dwordx4 v5, s[40:41] offset:1024
ds_read_b64_tr_b16 a[112:113], v204 offset:63488
global_load_lds_dwordx4 v6, s[40:41] offset:2048
ds_read_b64_tr_b16 a[114:115], v205 offset:256
global_load_lds_dwordx4 v7, s[40:41] offset:3072
ds_read_b64_tr_b16 a[116:117], v204 offset:63496
ds_read_b64_tr_b16 a[118:119], v205 offset:264
s_add_u32 m0, s48, 86016
ds_read_b64_tr_b16 a[120:121], v204 offset:63552
global_load_lds_dwordx4 v8, s[40:41] offset:-4096
ds_read_b64_tr_b16 a[122:123], v205 offset:320
s_add_u32 s40, s40, 73728
ds_read_b64_tr_b16 a[124:125], v204 offset:63560
s_addc_u32 s41, s41, 0
ds_read_b64_tr_b16 a[126:127], v205 offset:328
s_waitcnt vmcnt(18) lgkmcnt(0)
s_barrier
s_add_u32 m0, s48, 114688
ds_read_b64_tr_b16 a[128:129], v9 offset:0
ds_read_b64_tr_b16 a[130:131], v9 offset:2304
global_load_lds_dwordx4 v0, s[40:41] offset:-4096
ds_read_b64_tr_b16 a[132:133], v9 offset:8
global_load_lds_dwordx4 v1, s[40:41] offset:-3072
ds_read_b64_tr_b16 a[134:135], v9 offset:2312
ds_read_b64_tr_b16 a[136:137], v9 offset:64
global_load_lds_dwordx4 v2, s[40:41] offset:-2048
ds_read_b64_tr_b16 a[138:139], v9 offset:2368
global_load_lds_dwordx4 v3, s[40:41] offset:-1024
ds_read_b64_tr_b16 a[140:141], v9 offset:72
global_load_lds_dwordx4 v4, s[40:41] offset:0
ds_read_b64_tr_b16 a[142:143], v9 offset:2376
ds_read_b64_tr_b16 a[144:145], v9 offset:18432
global_load_lds_dwordx4 v5, s[40:41] offset:1024
ds_read_b64_tr_b16 a[146:147], v9 offset:20736
global_load_lds_dwordx4 v6, s[40:41] offset:2048
ds_read_b64_tr_b16 a[148:149], v9 offset:18440
ds_read_b64_tr_b16 a[150:151], v9 offset:20744
global_load_lds_dwordx4 v7, s[40:41] offset:3072
ds_read_b64_tr_b16 a[152:153], v9 offset:18496
s_add_u32 m0, s48, 122880
ds_read_b64_tr_b16 a[154:155], v9 offset:20800
global_load_lds_dwordx4 v8, s[40:41] offset:-4096
ds_read_b64_tr_b16 a[156:157], v9 offset:18504
ds_read_b64_tr_b16 a[158:159], v9 offset:20808
s_add_u32 s40, s40, 73728
ds_read_b64_tr_b16 v[60:61], v10 offset:18432
s_addc_u32 s41, s41, 0
ds_read_b64_tr_b16 v[62:63], v10 offset:20736
s_waitcnt vmcnt(18) lgkmcnt(0)
s_barrier
s_add_u32 m0, s48, 4096
ds_read_b64_tr_b16 a[160:161], v9 offset:36864
ds_read_b64_tr_b16 a[162:163], v9 offset:39168
global_load_lds_dwordx4 v0, s[40:41] offset:-4096
ds_read_b64_tr_b16 a[164:165], v9 offset:36872
ds_read_b64_tr_b16 a[166:167], v9 offset:39176
global_load_lds_dwordx4 v1, s[40:41] offset:-3072
ds_read_b64_tr_b16 a[168:169], v9 offset:36928
global_load_lds_dwordx4 v2, s[40:41] offset:-2048
ds_read_b64_tr_b16 a[170:171], v9 offset:39232
ds_read_b64_tr_b16 a[172:173], v9 offset:36936
global_load_lds_dwordx4 v3, s[40:41] offset:-1024
ds_read_b64_tr_b16 a[174:175], v9 offset:39240
global_load_lds_dwordx4 v4, s[40:41] offset:0
ds_read_b64_tr_b16 v[64:65], v10 offset:36864
ds_read_b64_tr_b16 v[66:67], v10 offset:39168
global_load_lds_dwordx4 v5, s[40:41] offset:1024
ds_read_b64_tr_b16 a[176:177], v9 offset:55296
global_load_lds_dwordx4 v6, s[40:41] offset:2048
ds_read_b64_tr_b16 a[178:179], v9 offset:57600
ds_read_b64_tr_b16 a[180:181], v9 offset:55304
global_load_lds_dwordx4 v7, s[40:41] offset:3072
ds_read_b64_tr_b16 a[182:183], v9 offset:57608
s_add_u32 m0, s48, 12288
ds_read_b64_tr_b16 a[184:185], v9 offset:55360
ds_read_b64_tr_b16 a[186:187], v9 offset:57664
global_load_lds_dwordx4 v8, s[40:41] offset:-4096
ds_read_b64_tr_b16 a[188:189], v9 offset:55368
s_sub_u32 s40, s40, 589824
ds_read_b64_tr_b16 a[190:191], v9 offset:57672
ds_read_b64_tr_b16 v[68:69], v10 offset:55296
s_subb_u32 s41, s41, 0
ds_read_b64_tr_b16 v[70:71], v10 offset:57600
s_waitcnt vmcnt(18) lgkmcnt(0)
s_barrier
ds_read_b64_tr_b16 a[192:193], v204 offset:8192
ds_read_b64_tr_b16 a[194:195], v204 offset:10496
ds_read_b64_tr_b16 a[196:197], v204 offset:8200
ds_read_b64_tr_b16 a[198:199], v204 offset:10504
ds_read_b64_tr_b16 a[200:201], v204 offset:8256
ds_read_b64_tr_b16 a[202:203], v204 offset:10560
ds_read_b64_tr_b16 a[204:205], v204 offset:8264
ds_read_b64_tr_b16 a[206:207], v204 offset:10568
ds_read_b64_tr_b16 v[72:73], v206 offset:8192
ds_read_b64_tr_b16 v[74:75], v206 offset:10496
ds_read_b64_tr_b16 a[208:209], v204 offset:26624
ds_read_b64_tr_b16 a[210:211], v204 offset:28928
ds_read_b64_tr_b16 a[212:213], v204 offset:26632
ds_read_b64_tr_b16 a[214:215], v204 offset:28936
ds_read_b64_tr_b16 a[216:217], v204 offset:26688
ds_read_b64_tr_b16 a[218:219], v204 offset:28992
ds_read_b64_tr_b16 a[220:221], v204 offset:26696
ds_read_b64_tr_b16 a[222:223], v204 offset:29000
ds_read_b64_tr_b16 v[76:77], v206 offset:26624
ds_read_b64_tr_b16 v[78:79], v206 offset:28928
s_waitcnt vmcnt(9) lgkmcnt(0)
s_barrier
ds_read_b64_tr_b16 a[224:225], v204 offset:45056
ds_read_b64_tr_b16 a[226:227], v204 offset:47360
ds_read_b64_tr_b16 a[228:229], v204 offset:45064
ds_read_b64_tr_b16 a[230:231], v204 offset:47368
ds_read_b64_tr_b16 a[232:233], v204 offset:45120
ds_read_b64_tr_b16 a[234:235], v204 offset:47424
ds_read_b64_tr_b16 a[236:237], v204 offset:45128
ds_read_b64_tr_b16 a[238:239], v204 offset:47432
ds_read_b64_tr_b16 v[80:81], v206 offset:45056
ds_read_b64_tr_b16 v[82:83], v206 offset:47360
ds_read_b64_tr_b16 a[240:241], v204 offset:63488
ds_read_b64_tr_b16 a[242:243], v205 offset:256
ds_read_b64_tr_b16 a[244:245], v204 offset:63496
ds_read_b64_tr_b16 a[246:247], v205 offset:264
ds_read_b64_tr_b16 a[248:249], v204 offset:63552
ds_read_b64_tr_b16 a[250:251], v205 offset:320
ds_read_b64_tr_b16 a[252:253], v204 offset:63560
ds_read_b64_tr_b16 a[254:255], v205 offset:328
ds_read_b64_tr_b16 v[84:85], v206 offset:63488
ds_read_b64_tr_b16 v[86:87], v207 offset:256
s_waitcnt vmcnt(0) lgkmcnt(0)
s_barrier
ds_read_b64_tr_b16 v[28:29], v9 offset:0
ds_read_b64_tr_b16 v[30:31], v9 offset:2304
ds_read_b64_tr_b16 v[32:33], v9 offset:8
ds_read_b64_tr_b16 v[34:35], v9 offset:2312
ds_read_b64_tr_b16 v[36:37], v9 offset:64
ds_read_b64_tr_b16 v[38:39], v9 offset:2368
ds_read_b64_tr_b16 v[40:41], v9 offset:72
ds_read_b64_tr_b16 v[42:43], v9 offset:2376
ds_read_b64_tr_b16 v[88:89], v10 offset:0
ds_read_b64_tr_b16 v[90:91], v10 offset:2304
ds_read_b64_tr_b16 v[44:45], v9 offset:18432
ds_read_b64_tr_b16 v[46:47], v9 offset:20736
ds_read_b64_tr_b16 v[48:49], v9 offset:18440
ds_read_b64_tr_b16 v[50:51], v9 offset:20744
ds_read_b64_tr_b16 v[52:53], v9 offset:18496
ds_read_b64_tr_b16 v[54:55], v9 offset:20800
ds_read_b64_tr_b16 v[56:57], v9 offset:18504
ds_read_b64_tr_b16 v[58:59], v9 offset:20808
ds_read_b64_tr_b16 v[92:93], v10 offset:18432
ds_read_b64_tr_b16 v[94:95], v10 offset:20736
s_waitcnt lgkmcnt(0)
s_barrier
v_add_lshl_u32 v252, v24, 0, 7
v_bfi_b32 v252, s69, v252, v23
v_add_lshl_u32 v253, v22, 0, 7
v_bfi_b32 v253, s69, v253, v21
s_mov_b32 s92, 0x01010101
s_mov_b32 s93, 0x01010101
s_mov_b64 s[76:77], 0
s_add_u32 m0, s49, 18432
v_add_u32 v13, 18688, v13
s_xor_b32 s49, s49, 0x10000
s_mov_b32 m0, 49152
s_add_u32 m0, s49, 0
v_add_u32 v22, 1, v22
v_add_lshl_u32 v253, v22, 0, 7
v_bfi_b32 v253, s69, v253, v21
v_add_u32 v11, 18432, v11
s_mov_b32 m0, 57344
v_add_u32 v13, 18688, v13
s_add_u32 m0, s49, 18432
s_mov_b64 s[76:77], -1
v_add_u32 v22, 1, v22
v_add_lshl_u32 v253, v22, 0, 7
v_bfi_b32 v253, s69, v253, v21
s_mov_b32 s70, 33
s_waitcnt vmcnt(0) lgkmcnt(0)
s_nop 7
s_branch .Lwo_in12
.p2align 6
.Lwo_top13:
s_waitcnt lgkmcnt(2)
v_mfma_f32_16x16x32_bf16 v[96:99], a[144:147], v[132:135], v[96:99]
ds_read_b128 v[144:147], v25 offset:12288
v_mfma_f32_16x16x32_bf16 v[100:103], a[148:151], v[132:135], v[100:103]
v_mfma_f32_16x16x32_bf16 v[104:107], a[152:155], v[132:135], v[104:107]
s_waitcnt vmcnt(8)
s_barrier
v_mfma_f32_16x16x32_bf16 v[108:111], a[156:159], v[132:135], v[108:111]
v_cvt_pk_bf16_f32 v164, v112, v113
v_cvt_pk_bf16_f32 v165, v114, v115
v_mfma_f32_16x16x32_bf16 v[128:131], v[60:63], v[132:135], 0
s_waitcnt lgkmcnt(2)
v_cvt_pk_bf16_f32 v166, v116, v117
)"
R"(v_cvt_pk_bf16_f32 v167, v118, v119
v_mfma_f32_16x16x32_bf16 v[96:99], a[160:163], v[136:139], v[96:99]
ds_read_b128 v[132:135], v25 offset:13312
v_add_f32 v254, v112, v113
v_mfma_f32_16x16x32_bf16 v[100:103], a[164:167], v[136:139], v[100:103]
global_store_dwordx4 v13, v[164:167], s[38:39] offset:0 sc1
v_mfma_f32_16x16x32_bf16 v[104:107], a[168:171], v[136:139], v[104:107]
v_cvt_pk_bf16_f32 v168, v120, v121
v_cvt_pk_bf16_f32 v169, v122, v123
v_mfma_f32_16x16x32_bf16 v[108:111], a[172:175], v[136:139], v[108:111]
v_cvt_pk_bf16_f32 v170, v124, v125
v_cvt_pk_bf16_f32 v171, v126, v127
v_mfma_f32_16x16x32_bf16 v[128:131], v[64:67], v[136:139], v[128:131]
s_waitcnt lgkmcnt(2)
v_mul_f32 v255, v112, v112
v_fmac_f32 v255, v113, v113
v_mfma_f32_16x16x32_bf16 v[96:99], a[176:179], v[140:143], v[96:99]
s_cmp_eq_u32 s70, 32
ds_read_b128 v[136:139], v25 offset:14336
v_add_f32 v254, v254, v114
v_mfma_f32_16x16x32_bf16 v[100:103], a[180:183], v[140:143], v[100:103]
s_cselect_b64 s[42:43], s[44:45], s[42:43]
global_store_dwordx4 v13, v[168:171], s[38:39] offset:1024 sc1
v_mfma_f32_16x16x32_bf16 v[104:107], a[184:187], v[140:143], v[104:107]
s_cmp_eq_u32 s70, 16
v_add_u32 v13, 18688, v13
v_fmac_f32 v255, v114, v114
v_mfma_f32_16x16x32_bf16 v[108:111], a[188:191], v[140:143], v[108:111]
s_cselect_b64 s[42:43], s[46:47], s[42:43]
v_add_f32 v254, v254, v115
v_fmac_f32 v255, v115, v115
v_mfma_f32_16x16x32_bf16 v[128:131], v[68:71], v[140:143], v[128:131]
s_waitcnt lgkmcnt(2)
v_add_f32 v254, v254, v116
v_fmac_f32 v255, v116, v116
v_mfma_f32_16x16x32_bf16 v[96:99], a[192:195], v[144:147], v[96:99]
s_xor_b32 s49, s49, 0x10000
ds_read_b128 v[140:143], v25 offset:15360
v_add_f32 v254, v254, v117
v_mfma_f32_16x16x32_bf16 v[100:103], a[196:199], v[144:147], v[100:103]
s_mov_b32 m0, 49152
v_fmac_f32 v255, v117, v117
v_add_f32 v254, v254, v118
v_mfma_f32_16x16x32_bf16 v[104:107], a[200:203], v[144:147], v[104:107]
v_fmac_f32 v255, v118, v118
v_add_f32 v254, v254, v119
v_mfma_f32_16x16x32_bf16 v[108:111], a[204:207], v[144:147], v[108:111]
v_fmac_f32 v255, v119, v119
v_add_f32 v254, v254, v120
v_mfma_f32_16x16x32_bf16 v[128:131], v[72:75], v[144:147], v[128:131]
s_waitcnt lgkmcnt(2)
v_fmac_f32 v255, v120, v120
v_add_f32 v254, v254, v121
v_mfma_f32_16x16x32_bf16 v[96:99], a[208:211], v[132:135], v[96:99]
ds_read_b128 v[144:147], v25 offset:16384
global_load_lds_dwordx4 v16, s[42:43] offset:0 sc1
v_mfma_f32_16x16x32_bf16 v[100:103], a[212:215], v[132:135], v[100:103]
v_fmac_f32 v255, v121, v121
v_add_f32 v254, v254, v122
v_mfma_f32_16x16x32_bf16 v[104:107], a[216:219], v[132:135], v[104:107]
v_fmac_f32 v255, v122, v122
v_add_f32 v254, v254, v123
v_mfma_f32_16x16x32_bf16 v[108:111], a[220:223], v[132:135], v[108:111]
v_fmac_f32 v255, v123, v123
v_add_f32 v254, v254, v124
v_mfma_f32_16x16x32_bf16 v[128:131], v[76:79], v[132:135], v[128:131]
s_waitcnt vmcnt(9)
ds_read_b128 v[148:151], v19 offset:8192
ds_read_b128 v[152:155], v20 offset:8192
s_waitcnt lgkmcnt(4)
v_mfma_f32_16x16x32_bf16 v[96:99], a[224:227], v[136:139], v[96:99]
ds_read_b128 v[132:135], v25 offset:17408
v_fmac_f32 v255, v124, v124
v_mfma_f32_16x16x32_bf16 v[100:103], a[228:231], v[136:139], v[100:103]
v_add_f32 v254, v254, v125
v_fmac_f32 v255, v125, v125
v_mfma_f32_16x16x32_bf16 v[104:107], a[232:235], v[136:139], v[104:107]
global_load_lds_dwordx4 v17, s[42:43] offset:1024 sc1
v_add_f32 v254, v254, v126
v_mfma_f32_16x16x32_bf16 v[108:111], a[236:239], v[136:139], v[108:111]
v_fmac_f32 v255, v126, v126
v_add_f32 v254, v254, v127
v_mfma_f32_16x16x32_bf16 v[128:131], v[80:83], v[136:139], v[128:131]
s_waitcnt lgkmcnt(4)
v_fmac_f32 v255, v127, v127
v_mfma_f32_16x16x32_bf16 v[96:99], a[240:243], v[140:143], v[96:99]
ds_read_b128 v[136:139], v25 offset:0
v_mfma_f32_16x16x32_bf16 v[100:103], a[244:247], v[140:143], v[100:103]
ds_write_b64 v27, v[254:255] offset:45568
v_mfma_f32_16x16x32_bf16 v[104:107], a[248:251], v[140:143], v[104:107]
s_add_u32 m0, s49, 0
s_waitcnt lgkmcnt(4)
v_lshlrev_b32 v112, 16, v148
v_and_b32 v113, 0xffff0000, v148
v_mfma_f32_16x16x32_bf16 v[108:111], a[252:255], v[140:143], v[108:111]
v_lshlrev_b32 v114, 16, v149
v_and_b32 v115, 0xffff0000, v149
v_mfma_f32_16x16x32_bf16 v[128:131], v[84:87], v[140:143], v[128:131]
v_lshlrev_b32 v116, 16, v150
v_and_b32 v117, 0xffff0000, v150
v_mfma_f32_16x16x32_bf16 v[96:99], v[28:31], v[144:147], v[96:99]
s_sub_u32 s42, s42, 18432
ds_read_b128 v[140:143], v25 offset:1024
v_lshlrev_b32 v118, 16, v151
v_mfma_f32_16x16x32_bf16 v[100:103], v[32:35], v[144:147], v[100:103]
s_subb_u32 s43, s43, 0
v_and_b32 v119, 0xffff0000, v151
s_waitcnt lgkmcnt(4)
v_lshlrev_b32 v120, 16, v152
v_mfma_f32_16x16x32_bf16 v[104:107], v[36:39], v[144:147], v[104:107]
v_and_b32 v121, 0xffff0000, v152
v_lshlrev_b32 v122, 16, v153
v_mfma_f32_16x16x32_bf16 v[108:111], v[40:43], v[144:147], v[108:111]
v_and_b32 v123, 0xffff0000, v153
v_lshlrev_b32 v124, 16, v154
v_mfma_f32_16x16x32_bf16 v[128:131], v[88:91], v[144:147], v[128:131]
s_waitcnt lgkmcnt(3)
v_and_b32 v125, 0xffff0000, v154
v_lshlrev_b32 v126, 16, v155
v_mfma_f32_16x16x32_bf16 v[96:99], v[44:47], v[132:135], v[96:99]
ds_read_b128 v[144:147], v25 offset:2048
v_and_b32 v127, 0xffff0000, v155
v_mfma_f32_16x16x32_bf16 v[100:103], v[48:51], v[132:135], v[100:103]
v_mfma_f32_16x16x32_bf16 v[104:107], v[52:55], v[132:135], v[104:107]
v_mfma_f32_16x16x32_bf16 v[108:111], v[56:59], v[132:135], v[108:111]
v_mfma_f32_16x16x32_bf16 v[128:131], v[92:95], v[132:135], v[128:131]
s_waitcnt lgkmcnt(3)
v_mfma_f32_16x16x32_bf16 v[96:99], a[0:3], v[136:139], v[96:99]
ds_read_b128 v[132:135], v25 offset:3072
v_mfma_f32_16x16x32_bf16 v[100:103], a[4:7], v[136:139], v[100:103]
v_mfma_f32_16x16x32_bf16 v[104:107], a[8:11], v[136:139], v[104:107]
v_mfma_f32_16x16x32_bf16 v[108:111], a[12:15], v[136:139], v[108:111]
s_waitcnt lgkmcnt(2)
v_mfma_f32_16x16x32_bf16 v[96:99], a[16:19], v[140:143], v[96:99]
ds_read_b128 v[136:139], v25 offset:4096
v_mfma_f32_16x16x32_bf16 v[100:103], a[20:23], v[140:143], v[100:103]
s_waitcnt vmcnt(6)
v_sub_u32 v200, v200, v22
v_cmp_lt_u32 vcc, 0x7f, v200
v_mfma_f32_16x16x32_bf16 v[104:107], a[24:27], v[140:143], v[104:107]
v_mfma_f32_16x16x32_bf16 v[108:111], a[28:31], v[140:143], v[108:111]
s_waitcnt lgkmcnt(2)
v_mfma_f32_16x16x32_bf16 v[96:99], a[32:35], v[144:147], v[96:99]
s_and_b64 vcc, vcc, s[76:77]
s_cbranch_vccnz .Lwo_sp8
.Lwo_rt9:
ds_read_b128 v[140:143], v25 offset:5120
v_mfma_f32_16x16x32_bf16 v[100:103], a[36:39], v[144:147], v[100:103]
v_mfma_f32_16x16x32_bf16 v[104:107], a[40:43], v[144:147], v[104:107]
s_barrier
v_mfma_f32_16x16x32_bf16 v[108:111], a[44:47], v[144:147], v[108:111]
s_waitcnt lgkmcnt(2)
ds_write_b128 v26, v[128:131] offset:37888
v_mfma_f32_16x16x32_bf16 v[96:99], a[48:51], v[132:135], v[96:99]
ds_read_b128 v[144:147], v25 offset:6144
v_add_u32 v22, 1, v22
v_mfma_f32_16x16x32_bf16 v[100:103], a[52:55], v[132:135], v[100:103]
v_add_lshl_u32 v253, v22, 0, 7
v_bfi_b32 v253, s69, v253, v21
v_mfma_f32_16x16x32_bf16 v[104:107], a[56:59], v[132:135], v[104:107]
global_load_lds_dwordx4 v11, s[38:39] offset:-2048 sc1
v_mfma_f32_16x16x32_bf16 v[108:111], a[60:63], v[132:135], v[108:111]
s_waitcnt lgkmcnt(3)
v_mfma_f32_16x16x32_bf16 v[96:99], a[64:67], v[136:139], v[96:99]
ds_read_b128 v[132:135], v25 offset:7168
v_mfma_f32_16x16x32_bf16 v[100:103], a[68:71], v[136:139], v[100:103]
global_load_lds_dwordx4 v11, s[38:39] offset:-1024 sc1
v_mfma_f32_16x16x32_bf16 v[104:107], a[72:75], v[136:139], v[104:107]
v_mfma_f32_16x16x32_bf16 v[108:111], a[76:79], v[136:139], v[108:111]
s_waitcnt lgkmcnt(3)
v_mfma_f32_16x16x32_bf16 v[96:99], a[80:83], v[140:143], v[96:99]
ds_read_b128 v[136:139], v25 offset:8192
global_load_lds_dwordx4 v11, s[38:39] offset:0 sc1
v_mfma_f32_16x16x32_bf16 v[100:103], a[84:87], v[140:143], v[100:103]
v_mfma_f32_16x16x32_bf16 v[104:107], a[88:91], v[140:143], v[104:107]
v_mfma_f32_16x16x32_bf16 v[108:111], a[92:95], v[140:143], v[108:111]
s_waitcnt lgkmcnt(2)
global_load_dword v201, v253, s[38:39] sc1
global_load_lds_dwordx4 v11, s[38:39] offset:1024 sc1
v_mfma_f32_16x16x32_bf16 v[96:99], a[96:99], v[144:147], v[96:99]
v_mfma_f32_16x16x32_bf16 v[100:103], a[100:103], v[144:147], v[100:103]
v_mfma_f32_16x16x32_bf16 v[104:107], a[104:107], v[144:147], v[104:107]
s_waitcnt vmcnt(9)
s_barrier
v_mfma_f32_16x16x32_bf16 v[108:111], a[108:111], v[144:147], v[108:111]
s_waitcnt lgkmcnt(1)
ds_read_b128 v[140:143], v25 offset:27648
global_load_lds_dwordx4 v11, s[38:39] offset:2048 sc1
v_mfma_f32_16x16x32_bf16 v[96:99], a[112:115], v[132:135], v[96:99]
ds_read_b128 v[144:147], v25 offset:28672
v_add_u32 v11, 18432, v11
v_mfma_f32_16x16x32_bf16 v[100:103], a[116:119], v[132:135], v[100:103]
v_min_u32 v11, v11, v12
v_mfma_f32_16x16x32_bf16 v[104:107], a[120:123], v[132:135], v[104:107]
v_mfma_f32_16x16x32_bf16 v[108:111], a[124:127], v[132:135], v[108:111]
s_waitcnt lgkmcnt(2)
v_mfma_f32_16x16x32_bf16 v[96:99], a[128:131], v[136:139], v[96:99]
ds_read_b128 v[132:135], v25 offset:29696
v_mfma_f32_16x16x32_bf16 v[100:103], a[132:135], v[136:139], v[100:103]
v_mfma_f32_16x16x32_bf16 v[104:107], a[136:139], v[136:139], v[104:107]
v_mfma_f32_16x16x32_bf16 v[108:111], a[140:143], v[136:139], v[108:111]
s_waitcnt lgkmcnt(2)
v_mfma_f32_16x16x32_bf16 v[112:115], a[144:147], v[140:143], v[112:115]
ds_read_b128 v[136:139], v25 offset:30720
v_mfma_f32_16x16x32_bf16 v[116:119], a[148:151], v[140:143], v[116:119]
v_mfma_f32_16x16x32_bf16 v[120:123], a[152:155], v[140:143], v[120:123]
s_waitcnt vmcnt(8)
s_barrier
v_mfma_f32_16x16x32_bf16 v[124:127], a[156:159], v[140:143], v[124:127]
v_cvt_pk_bf16_f32 v164, v96, v97
v_cvt_pk_bf16_f32 v165, v98, v99
v_mfma_f32_16x16x32_bf16 v[128:131], v[60:63], v[140:143], 0
s_waitcnt lgkmcnt(2)
v_cvt_pk_bf16_f32 v166, v100, v101
v_cvt_pk_bf16_f32 v167, v102, v103
v_mfma_f32_16x16x32_bf16 v[112:115], a[160:163], v[144:147], v[112:115]
ds_read_b128 v[140:143], v25 offset:31744
v_add_f32 v254, v96, v97
v_mfma_f32_16x16x32_bf16 v[116:119], a[164:167], v[144:147], v[116:119]
global_store_dwordx4 v13, v[164:167], s[38:39] offset:0 sc1
v_mfma_f32_16x16x32_bf16 v[120:123], a[168:171], v[144:147], v[120:123]
v_cvt_pk_bf16_f32 v168, v104, v105
v_cvt_pk_bf16_f32 v169, v106, v107
v_mfma_f32_16x16x32_bf16 v[124:127], a[172:175], v[144:147], v[124:127]
v_cvt_pk_bf16_f32 v170, v108, v109
v_cvt_pk_bf16_f32 v171, v110, v111
v_mfma_f32_16x16x32_bf16 v[128:131], v[64:67], v[144:147], v[128:131]
s_waitcnt lgkmcnt(2)
v_mul_f32 v255, v96, v96
v_fmac_f32 v255, v97, v97
v_mfma_f32_16x16x32_bf16 v[112:115], a[176:179], v[132:135], v[112:115]
s_mov_b32 m0, 57344
ds_read_b128 v[144:147], v25 offset:32768
v_add_f32 v254, v254, v98
v_mfma_f32_16x16x32_bf16 v[116:119], a[180:183], v[132:135], v[116:119]
global_store_dwordx4 v13, v[168:171], s[38:39] offset:1024 sc1
v_mfma_f32_16x16x32_bf16 v[120:123], a[184:187], v[132:135], v[120:123]
v_add_u32 v13, 18688, v13
v_fmac_f32 v255, v98, v98
v_mfma_f32_16x16x32_bf16 v[124:127], a[188:191], v[132:135], v[124:127]
v_add_f32 v254, v254, v99
v_fmac_f32 v255, v99, v99
v_mfma_f32_16x16x32_bf16 v[128:131], v[68:71], v[132:135], v[128:131]
s_waitcnt lgkmcnt(2)
v_add_f32 v254, v254, v100
v_fmac_f32 v255, v100, v100
v_mfma_f32_16x16x32_bf16 v[112:115], a[192:195], v[136:139], v[112:115]
ds_read_b128 v[132:135], v25 offset:33792
v_add_f32 v254, v254, v101
v_mfma_f32_16x16x32_bf16 v[116:119], a[196:199], v[136:139], v[116:119]
v_fmac_f32 v255, v101, v101
v_add_f32 v254, v254, v102
v_mfma_f32_16x16x32_bf16 v[120:123], a[200:203], v[136:139], v[120:123]
v_fmac_f32 v255, v102, v102
v_add_f32 v254, v254, v103
v_mfma_f32_16x16x32_bf16 v[124:127], a[204:207], v[136:139], v[124:127]
v_fmac_f32 v255, v103, v103
v_add_f32 v254, v254, v104
v_mfma_f32_16x16x32_bf16 v[128:131], v[72:75], v[136:139], v[128:131]
s_waitcnt lgkmcnt(2)
v_fmac_f32 v255, v104, v104
v_add_f32 v254, v254, v105
v_mfma_f32_16x16x32_bf16 v[112:115], a[208:211], v[140:143], v[112:115]
ds_read_b128 v[136:139], v25 offset:34816
global_load_lds_dwordx4 v16, s[42:43] offset:0 sc1
v_mfma_f32_16x16x32_bf16 v[116:119], a[212:215], v[140:143], v[116:119]
v_fmac_f32 v255, v105, v105
v_add_f32 v254, v254, v106
v_mfma_f32_16x16x32_bf16 v[120:123], a[216:219], v[140:143], v[120:123]
v_fmac_f32 v255, v106, v106
v_add_f32 v254, v254, v107
v_mfma_f32_16x16x32_bf16 v[124:127], a[220:223], v[140:143], v[124:127]
v_fmac_f32 v255, v107, v107
v_add_f32 v254, v254, v108
v_mfma_f32_16x16x32_bf16 v[128:131], v[76:79], v[140:143], v[128:131]
s_waitcnt vmcnt(9)
ds_read_b128 v[148:151], v19 offset:0
ds_read_b128 v[152:155], v20 offset:0
s_waitcnt lgkmcnt(4)
v_mfma_f32_16x16x32_bf16 v[112:115], a[224:227], v[144:147], v[112:115]
ds_read_b128 v[140:143], v25 offset:35840
v_fmac_f32 v255, v108, v108
v_mfma_f32_16x16x32_bf16 v[116:119], a[228:231], v[144:147], v[116:119]
v_add_f32 v254, v254, v109
v_fmac_f32 v255, v109, v109
v_mfma_f32_16x16x32_bf16 v[120:123], a[232:235], v[144:147], v[120:123]
global_load_lds_dwordx4 v17, s[42:43] offset:1024 sc1
v_add_f32 v254, v254, v110
v_mfma_f32_16x16x32_bf16 v[124:127], a[236:239], v[144:147], v[124:127]
v_fmac_f32 v255, v110, v110
v_add_f32 v254, v254, v111
v_mfma_f32_16x16x32_bf16 v[128:131], v[80:83], v[144:147], v[128:131]
s_waitcnt lgkmcnt(4)
v_fmac_f32 v255, v111, v111
v_mfma_f32_16x16x32_bf16 v[112:115], a[240:243], v[132:135], v[112:115]
ds_read_b128 v[144:147], v25 offset:18432
v_mfma_f32_16x16x32_bf16 v[116:119], a[244:247], v[132:135], v[116:119]
ds_write_b64 v27, v[254:255] offset:43520
v_mfma_f32_16x16x32_bf16 v[120:123], a[248:251], v[132:135], v[120:123]
s_add_u32 m0, s49, 18432
s_waitcnt lgkmcnt(4)
v_lshlrev_b32 v96, 16, v148
v_and_b32 v97, 0xffff0000, v148
v_mfma_f32_16x16x32_bf16 v[124:127], a[252:255], v[132:135], v[124:127]
v_lshlrev_b32 v98, 16, v149
v_and_b32 v99, 0xffff0000, v149
v_mfma_f32_16x16x32_bf16 v[128:131], v[84:87], v[132:135], v[128:131]
v_lshlrev_b32 v100, 16, v150
v_and_b32 v101, 0xffff0000, v150
v_mfma_f32_16x16x32_bf16 v[112:115], v[28:31], v[136:139], v[112:115]
s_sub_u32 s42, s42, 18432
ds_read_b128 v[132:135], v25 offset:19456
v_lshlrev_b32 v102, 16, v151
v_mfma_f32_16x16x32_bf16 v[116:119], v[32:35], v[136:139], v[116:119]
s_subb_u32 s43, s43, 0
v_and_b32 v103, 0xffff0000, v151
s_waitcnt lgkmcnt(4)
v_lshlrev_b32 v104, 16, v152
v_mfma_f32_16x16x32_bf16 v[120:123], v[36:39], v[136:139], v[120:123]
v_and_b32 v105, 0xffff0000, v152
v_lshlrev_b32 v106, 16, v153
v_mfma_f32_16x16x32_bf16 v[124:127], v[40:43], v[136:139], v[124:127]
v_and_b32 v107, 0xffff0000, v153
v_lshlrev_b32 v108, 16, v154
v_mfma_f32_16x16x32_bf16 v[128:131], v[88:91], v[136:139], v[128:131]
s_waitcnt lgkmcnt(3)
v_and_b32 v109, 0xffff0000, v154
v_lshlrev_b32 v110, 16, v155
v_mfma_f32_16x16x32_bf16 v[112:115], v[44:47], v[140:143], v[112:115]
ds_read_b128 v[136:139], v25 offset:20480
v_and_b32 v111, 0xffff0000, v155
v_mfma_f32_16x16x32_bf16 v[116:119], v[48:51], v[140:143], v[116:119]
v_mfma_f32_16x16x32_bf16 v[120:123], v[52:55], v[140:143], v[120:123]
v_mfma_f32_16x16x32_bf16 v[124:127], v[56:59], v[140:143], v[124:127]
v_mfma_f32_16x16x32_bf16 v[128:131], v[92:95], v[140:143], v[128:131]
s_waitcnt lgkmcnt(3)
v_mfma_f32_16x16x32_bf16 v[112:115], a[0:3], v[144:147], v[112:115]
ds_read_b128 v[140:143], v25 offset:21504
v_mfma_f32_16x16x32_bf16 v[116:119], a[4:7], v[144:147], v[116:119]
v_mfma_f32_16x16x32_bf16 v[120:123], a[8:11], v[144:147], v[120:123]
v_mfma_f32_16x16x32_bf16 v[124:127], a[12:15], v[144:147], v[124:127]
s_waitcnt lgkmcnt(2)
v_mfma_f32_16x16x32_bf16 v[112:115], a[16:19], v[132:135], v[112:115]
ds_read_b128 v[144:147], v25 offset:22528
v_mfma_f32_16x16x32_bf16 v[116:119], a[20:23], v[132:135], v[116:119]
s_waitcnt vmcnt(6)
v_sub_u32 v201, v201, v22
v_cmp_lt_u32 vcc, 0x7f, v201
v_mfma_f32_16x16x32_bf16 v[120:123], a[24:27], v[132:135], v[120:123]
v_mfma_f32_16x16x32_bf16 v[124:127], a[28:31], v[132:135], v[124:127]
s_waitcnt lgkmcnt(2)
v_mfma_f32_16x16x32_bf16 v[112:115], a[32:35], v[136:139], v[112:115]
s_and_b64 vcc, vcc, s[76:77]
s_cbranch_vccnz .Lwo_sp10
.Lwo_rt11:
ds_read_b128 v[132:135], v25 offset:23552
v_mfma_f32_16x16x32_bf16 v[116:119], a[36:39], v[136:139], v[116:119]
v_mfma_f32_16x16x32_bf16 v[120:123], a[40:43], v[136:139], v[120:123]
s_barrier
v_mfma_f32_16x16x32_bf16 v[124:127], a[44:47], v[136:139], v[124:127]
s_waitcnt lgkmcnt(2)
ds_write_b128 v26, v[128:131] offset:40960
v_mfma_f32_16x16x32_bf16 v[112:115], a[48:51], v[140:143], v[112:115]
s_mov_b64 s[76:77], -1
ds_read_b128 v[136:139], v25 offset:24576
v_add_u32 v22, 1, v22
v_mfma_f32_16x16x32_bf16 v[116:119], a[52:55], v[140:143], v[116:119]
v_add_lshl_u32 v253, v22, 0, 7
v_bfi_b32 v253, s69, v253, v21
v_mfma_f32_16x16x32_bf16 v[120:123], a[56:59], v[140:143], v[120:123]
global_load_lds_dwordx4 v11, s[38:39] offset:-2048 sc1
.Lwo_in12:
v_mfma_f32_16x16x32_bf16 v[124:127], a[60:63], v[140:143], v[124:127]
s_waitcnt lgkmcnt(3)
v_mfma_f32_16x16x32_bf16 v[112:115], a[64:67], v[144:147], v[112:115]
ds_read_b128 v[140:143], v25 offset:25600
v_mfma_f32_16x16x32_bf16 v[116:119], a[68:71], v[144:147], v[116:119]
global_load_lds_dwordx4 v11, s[38:39] offset:-1024 sc1
v_mfma_f32_16x16x32_bf16 v[120:123], a[72:75], v[144:147], v[120:123]
v_mfma_f32_16x16x32_bf16 v[124:127], a[76:79], v[144:147], v[124:127]
s_waitcnt lgkmcnt(3)
v_mfma_f32_16x16x32_bf16 v[112:115], a[80:83], v[132:135], v[112:115]
ds_read_b128 v[144:147], v25 offset:26624
global_load_lds_dwordx4 v11, s[38:39] offset:0 sc1
v_mfma_f32_16x16x32_bf16 v[116:119], a[84:87], v[132:135], v[116:119]
v_mfma_f32_16x16x32_bf16 v[120:123], a[88:91], v[132:135], v[120:123]
v_mfma_f32_16x16x32_bf16 v[124:127], a[92:95], v[132:135], v[124:127]
s_waitcnt lgkmcnt(2)
v_xor_b32 v25, 0x10000, v25
global_load_dword v200, v253, s[38:39] sc1
v_mfma_f32_16x16x32_bf16 v[112:115], a[96:99], v[136:139], v[112:115]
global_load_lds_dwordx4 v11, s[38:39] offset:1024 sc1
v_mfma_f32_16x16x32_bf16 v[116:119], a[100:103], v[136:139], v[116:119]
v_mfma_f32_16x16x32_bf16 v[120:123], a[104:107], v[136:139], v[120:123]
s_waitcnt vmcnt(9)
s_barrier
v_mfma_f32_16x16x32_bf16 v[124:127], a[108:111], v[136:139], v[124:127]
s_waitcnt lgkmcnt(1)
ds_read_b128 v[132:135], v25 offset:9216
global_load_lds_dwordx4 v11, s[38:39] offset:2048 sc1
)"
R"(v_mfma_f32_16x16x32_bf16 v[112:115], a[112:115], v[140:143], v[112:115]
ds_read_b128 v[136:139], v25 offset:10240
v_add_u32 v11, 18432, v11
v_mfma_f32_16x16x32_bf16 v[116:119], a[116:119], v[140:143], v[116:119]
v_min_u32 v11, v11, v12
v_mfma_f32_16x16x32_bf16 v[120:123], a[120:123], v[140:143], v[120:123]
v_mfma_f32_16x16x32_bf16 v[124:127], a[124:127], v[140:143], v[124:127]
s_waitcnt lgkmcnt(2)
v_mfma_f32_16x16x32_bf16 v[112:115], a[128:131], v[144:147], v[112:115]
ds_read_b128 v[140:143], v25 offset:11264
v_mfma_f32_16x16x32_bf16 v[116:119], a[132:135], v[144:147], v[116:119]
v_mfma_f32_16x16x32_bf16 v[120:123], a[136:139], v[144:147], v[120:123]
v_mfma_f32_16x16x32_bf16 v[124:127], a[140:143], v[144:147], v[124:127]
s_sub_u32 s70, s70, 1
s_cbranch_scc0 .Lwo_top13
s_waitcnt vmcnt(8)
s_barrier
v_cvt_pk_bf16_f32 v164, v112, v113
v_cvt_pk_bf16_f32 v165, v114, v115
v_cvt_pk_bf16_f32 v166, v116, v117
v_cvt_pk_bf16_f32 v167, v118, v119
global_store_dwordx4 v13, v[164:167], s[38:39] offset:0 sc1
v_cvt_pk_bf16_f32 v168, v120, v121
v_cvt_pk_bf16_f32 v169, v122, v123
v_cvt_pk_bf16_f32 v170, v124, v125
v_cvt_pk_bf16_f32 v171, v126, v127
global_store_dwordx4 v13, v[168:171], s[38:39] offset:1024 sc1
v_add_f32 v254, v112, v113
v_mul_f32 v255, v112, v112
v_fmac_f32 v255, v113, v113
v_add_f32 v254, v254, v114
v_fmac_f32 v255, v114, v114
v_add_f32 v254, v254, v115
v_fmac_f32 v255, v115, v115
v_add_f32 v254, v254, v116
v_fmac_f32 v255, v116, v116
v_add_f32 v254, v254, v117
v_fmac_f32 v255, v117, v117
v_add_f32 v254, v254, v118
v_fmac_f32 v255, v118, v118
v_add_f32 v254, v254, v119
v_fmac_f32 v255, v119, v119
v_add_f32 v254, v254, v120
v_fmac_f32 v255, v120, v120
v_add_f32 v254, v254, v121
v_fmac_f32 v255, v121, v121
v_add_f32 v254, v254, v122
v_fmac_f32 v255, v122, v122
v_add_f32 v254, v254, v123
v_fmac_f32 v255, v123, v123
v_add_f32 v254, v254, v124
v_fmac_f32 v255, v124, v124
v_add_f32 v254, v254, v125
v_fmac_f32 v255, v125, v125
v_add_f32 v254, v254, v126
v_fmac_f32 v255, v126, v126
v_add_f32 v254, v254, v127
v_fmac_f32 v255, v127, v127
ds_write_b64 v27, v[254:255] offset:45568
s_waitcnt vmcnt(0) lgkmcnt(0)
s_barrier
s_barrier
s_endpgm
.Lwo_w3:
s_add_u32 m0, s48, 4096
s_nop 0
global_load_lds_dwordx4 v0, s[40:41] offset:-4096
global_load_lds_dwordx4 v1, s[40:41] offset:-3072
global_load_lds_dwordx4 v2, s[40:41] offset:-2048
global_load_lds_dwordx4 v3, s[40:41] offset:-1024
global_load_lds_dwordx4 v4, s[40:41] offset:0
global_load_lds_dwordx4 v5, s[40:41] offset:1024
global_load_lds_dwordx4 v6, s[40:41] offset:2048
global_load_lds_dwordx4 v7, s[40:41] offset:3072
s_add_u32 m0, s48, 12288
s_nop 0
global_load_lds_dwordx4 v8, s[40:41] offset:-4096
s_add_u32 s40, s40, 73728
s_addc_u32 s41, s41, 0
s_add_u32 m0, s48, 40960
s_nop 0
global_load_lds_dwordx4 v0, s[40:41] offset:-4096
global_load_lds_dwordx4 v1, s[40:41] offset:-3072
global_load_lds_dwordx4 v2, s[40:41] offset:-2048
global_load_lds_dwordx4 v3, s[40:41] offset:-1024
global_load_lds_dwordx4 v4, s[40:41] offset:0
global_load_lds_dwordx4 v5, s[40:41] offset:1024
global_load_lds_dwordx4 v6, s[40:41] offset:2048
global_load_lds_dwordx4 v7, s[40:41] offset:3072
s_add_u32 m0, s48, 49152
s_nop 0
global_load_lds_dwordx4 v8, s[40:41] offset:-4096
s_add_u32 s40, s40, 73728
s_addc_u32 s41, s41, 0
s_add_u32 m0, s48, 77824
s_nop 0
global_load_lds_dwordx4 v0, s[40:41] offset:-4096
global_load_lds_dwordx4 v1, s[40:41] offset:-3072
global_load_lds_dwordx4 v2, s[40:41] offset:-2048
global_load_lds_dwordx4 v3, s[40:41] offset:-1024
global_load_lds_dwordx4 v4, s[40:41] offset:0
global_load_lds_dwordx4 v5, s[40:41] offset:1024
global_load_lds_dwordx4 v6, s[40:41] offset:2048
global_load_lds_dwordx4 v7, s[40:41] offset:3072
s_add_u32 m0, s48, 86016
s_nop 0
global_load_lds_dwordx4 v8, s[40:41] offset:-4096
s_add_u32 s40, s40, 73728
s_addc_u32 s41, s41, 0
v_add_u32 v204, 0x10000, v9
v_add_u32 v205, 0x20000, v9
v_add_u32 v206, 0x10000, v10
v_add_u32 v207, 0x20000, v10
s_waitcnt lgkmcnt(0)
s_mul_i32 s37, s37, 0x9E3779B1
s_add_u32 s37, s37, 0x7F4A7C15
v_add_u32 v22, s37, v22
v_add_u32 v24, s37, v24
s_waitcnt vmcnt(18)
s_barrier
s_add_u32 m0, s48, 114688
ds_read_b64_tr_b16 a[0:1], v9 offset:0
ds_read_b64_tr_b16 a[2:3], v9 offset:2304
global_load_lds_dwordx4 v0, s[40:41] offset:-4096
ds_read_b64_tr_b16 a[4:5], v9 offset:8
ds_read_b64_tr_b16 a[6:7], v9 offset:2312
global_load_lds_dwordx4 v1, s[40:41] offset:-3072
ds_read_b64_tr_b16 a[8:9], v9 offset:64
global_load_lds_dwordx4 v2, s[40:41] offset:-2048
ds_read_b64_tr_b16 a[10:11], v9 offset:2368
ds_read_b64_tr_b16 a[12:13], v9 offset:72
global_load_lds_dwordx4 v3, s[40:41] offset:-1024
ds_read_b64_tr_b16 a[14:15], v9 offset:2376
global_load_lds_dwordx4 v4, s[40:41] offset:0
ds_read_b64_tr_b16 v[60:61], v10 offset:0
ds_read_b64_tr_b16 v[62:63], v10 offset:2304
global_load_lds_dwordx4 v5, s[40:41] offset:1024
ds_read_b64_tr_b16 a[16:17], v9 offset:18432
global_load_lds_dwordx4 v6, s[40:41] offset:2048
ds_read_b64_tr_b16 a[18:19], v9 offset:20736
ds_read_b64_tr_b16 a[20:21], v9 offset:18440
global_load_lds_dwordx4 v7, s[40:41] offset:3072
ds_read_b64_tr_b16 a[22:23], v9 offset:20744
s_add_u32 m0, s48, 122880
ds_read_b64_tr_b16 a[24:25], v9 offset:18496
ds_read_b64_tr_b16 a[26:27], v9 offset:20800
global_load_lds_dwordx4 v8, s[40:41] offset:-4096
ds_read_b64_tr_b16 a[28:29], v9 offset:18504
s_add_u32 s40, s40, 73728
ds_read_b64_tr_b16 a[30:31], v9 offset:20808
ds_read_b64_tr_b16 v[64:65], v10 offset:18432
s_addc_u32 s41, s41, 0
ds_read_b64_tr_b16 v[66:67], v10 offset:20736
s_waitcnt vmcnt(18) lgkmcnt(0)
s_barrier
s_add_u32 m0, s48, 4096
ds_read_b64_tr_b16 a[32:33], v9 offset:36864
ds_read_b64_tr_b16 a[34:35], v9 offset:39168
global_load_lds_dwordx4 v0, s[40:41] offset:-4096
ds_read_b64_tr_b16 a[36:37], v9 offset:36872
ds_read_b64_tr_b16 a[38:39], v9 offset:39176
global_load_lds_dwordx4 v1, s[40:41] offset:-3072
ds_read_b64_tr_b16 a[40:41], v9 offset:36928
global_load_lds_dwordx4 v2, s[40:41] offset:-2048
ds_read_b64_tr_b16 a[42:43], v9 offset:39232
ds_read_b64_tr_b16 a[44:45], v9 offset:36936
global_load_lds_dwordx4 v3, s[40:41] offset:-1024
ds_read_b64_tr_b16 a[46:47], v9 offset:39240
global_load_lds_dwordx4 v4, s[40:41] offset:0
ds_read_b64_tr_b16 v[68:69], v10 offset:36864
ds_read_b64_tr_b16 v[70:71], v10 offset:39168
global_load_lds_dwordx4 v5, s[40:41] offset:1024
ds_read_b64_tr_b16 a[48:49], v9 offset:55296
global_load_lds_dwordx4 v6, s[40:41] offset:2048
ds_read_b64_tr_b16 a[50:51], v9 offset:57600
ds_read_b64_tr_b16 a[52:53], v9 offset:55304
global_load_lds_dwordx4 v7, s[40:41] offset:3072
ds_read_b64_tr_b16 a[54:55], v9 offset:57608
s_add_u32 m0, s48, 12288
ds_read_b64_tr_b16 a[56:57], v9 offset:55360
ds_read_b64_tr_b16 a[58:59], v9 offset:57664
global_load_lds_dwordx4 v8, s[40:41] offset:-4096
ds_read_b64_tr_b16 a[60:61], v9 offset:55368
s_add_u32 s40, s40, 73728
ds_read_b64_tr_b16 a[62:63], v9 offset:57672
ds_read_b64_tr_b16 v[72:73], v10 offset:55296
s_addc_u32 s41, s41, 0
ds_read_b64_tr_b16 v[74:75], v10 offset:57600
s_waitcnt vmcnt(18) lgkmcnt(0)
s_barrier
s_add_u32 m0, s48, 40960
ds_read_b64_tr_b16 a[64:65], v204 offset:8192
ds_read_b64_tr_b16 a[66:67], v204 offset:10496
global_load_lds_dwordx4 v0, s[40:41] offset:-4096
ds_read_b64_tr_b16 a[68:69], v204 offset:8200
ds_read_b64_tr_b16 a[70:71], v204 offset:10504
global_load_lds_dwordx4 v1, s[40:41] offset:-3072
ds_read_b64_tr_b16 a[72:73], v204 offset:8256
global_load_lds_dwordx4 v2, s[40:41] offset:-2048
ds_read_b64_tr_b16 a[74:75], v204 offset:10560
ds_read_b64_tr_b16 a[76:77], v204 offset:8264
global_load_lds_dwordx4 v3, s[40:41] offset:-1024
ds_read_b64_tr_b16 a[78:79], v204 offset:10568
global_load_lds_dwordx4 v4, s[40:41] offset:0
ds_read_b64_tr_b16 v[76:77], v206 offset:8192
ds_read_b64_tr_b16 v[78:79], v206 offset:10496
global_load_lds_dwordx4 v5, s[40:41] offset:1024
ds_read_b64_tr_b16 a[80:81], v204 offset:26624
global_load_lds_dwordx4 v6, s[40:41] offset:2048
ds_read_b64_tr_b16 a[82:83], v204 offset:28928
ds_read_b64_tr_b16 a[84:85], v204 offset:26632
global_load_lds_dwordx4 v7, s[40:41] offset:3072
ds_read_b64_tr_b16 a[86:87], v204 offset:28936
s_add_u32 m0, s48, 49152
ds_read_b64_tr_b16 a[88:89], v204 offset:26688
ds_read_b64_tr_b16 a[90:91], v204 offset:28992
global_load_lds_dwordx4 v8, s[40:41] offset:-4096
ds_read_b64_tr_b16 a[92:93], v204 offset:26696
s_add_u32 s40, s40, 73728
ds_read_b64_tr_b16 a[94:95], v204 offset:29000
ds_read_b64_tr_b16 v[80:81], v206 offset:26624
s_addc_u32 s41, s41, 0
ds_read_b64_tr_b16 v[82:83], v206 offset:28928
s_waitcnt vmcnt(18) lgkmcnt(0)
s_barrier
s_add_u32 m0, s48, 77824
ds_read_b64_tr_b16 a[96:97], v204 offset:45056
ds_read_b64_tr_b16 a[98:99], v204 offset:47360
global_load_lds_dwordx4 v0, s[40:41] offset:-4096
ds_read_b64_tr_b16 a[100:101], v204 offset:45064
ds_read_b64_tr_b16 a[102:103], v204 offset:47368
global_load_lds_dwordx4 v1, s[40:41] offset:-3072
ds_read_b64_tr_b16 a[104:105], v204 offset:45120
global_load_lds_dwordx4 v2, s[40:41] offset:-2048
ds_read_b64_tr_b16 a[106:107], v204 offset:47424
ds_read_b64_tr_b16 a[108:109], v204 offset:45128
global_load_lds_dwordx4 v3, s[40:41] offset:-1024
ds_read_b64_tr_b16 a[110:111], v204 offset:47432
global_load_lds_dwordx4 v4, s[40:41] offset:0
ds_read_b64_tr_b16 v[84:85], v206 offset:45056
ds_read_b64_tr_b16 v[86:87], v206 offset:47360
global_load_lds_dwordx4 v5, s[40:41] offset:1024
ds_read_b64_tr_b16 a[112:113], v204 offset:63488
global_load_lds_dwordx4 v6, s[40:41] offset:2048
ds_read_b64_tr_b16 a[114:115], v205 offset:256
ds_read_b64_tr_b16 a[116:117], v204 offset:63496
global_load_lds_dwordx4 v7, s[40:41] offset:3072
ds_read_b64_tr_b16 a[118:119], v205 offset:264
s_add_u32 m0, s48, 86016
ds_read_b64_tr_b16 a[120:121], v204 offset:63552
ds_read_b64_tr_b16 a[122:123], v205 offset:320
global_load_lds_dwordx4 v8, s[40:41] offset:-4096
ds_read_b64_tr_b16 a[124:125], v204 offset:63560
s_add_u32 s40, s40, 73728
ds_read_b64_tr_b16 a[126:127], v205 offset:328
ds_read_b64_tr_b16 v[88:89], v206 offset:63488
s_addc_u32 s41, s41, 0
ds_read_b64_tr_b16 v[90:91], v207 offset:256
s_waitcnt vmcnt(18) lgkmcnt(0)
s_barrier
s_add_u32 m0, s48, 114688
ds_read_b64_tr_b16 a[128:129], v9 offset:0
ds_read_b64_tr_b16 a[130:131], v9 offset:2304
global_load_lds_dwordx4 v0, s[40:41] offset:-4096
ds_read_b64_tr_b16 a[132:133], v9 offset:8
global_load_lds_dwordx4 v1, s[40:41] offset:-3072
ds_read_b64_tr_b16 a[134:135], v9 offset:2312
ds_read_b64_tr_b16 a[136:137], v9 offset:64
global_load_lds_dwordx4 v2, s[40:41] offset:-2048
ds_read_b64_tr_b16 a[138:139], v9 offset:2368
global_load_lds_dwordx4 v3, s[40:41] offset:-1024
ds_read_b64_tr_b16 a[140:141], v9 offset:72
global_load_lds_dwordx4 v4, s[40:41] offset:0
ds_read_b64_tr_b16 a[142:143], v9 offset:2376
ds_read_b64_tr_b16 v[92:93], v10 offset:0
global_load_lds_dwordx4 v5, s[40:41] offset:1024
ds_read_b64_tr_b16 v[94:95], v10 offset:2304
global_load_lds_dwordx4 v6, s[40:41] offset:2048
ds_read_b64_tr_b16 a[144:145], v9 offset:18432
ds_read_b64_tr_b16 a[146:147], v9 offset:20736
global_load_lds_dwordx4 v7, s[40:41] offset:3072
ds_read_b64_tr_b16 a[148:149], v9 offset:18440
s_add_u32 m0, s48, 122880
ds_read_b64_tr_b16 a[150:151], v9 offset:20744
global_load_lds_dwordx4 v8, s[40:41] offset:-4096
ds_read_b64_tr_b16 a[152:153], v9 offset:18496
ds_read_b64_tr_b16 a[154:155], v9 offset:20800
s_add_u32 s40, s40, 73728
ds_read_b64_tr_b16 a[156:157], v9 offset:18504
s_addc_u32 s41, s41, 0
ds_read_b64_tr_b16 a[158:159], v9 offset:20808
s_waitcnt vmcnt(18) lgkmcnt(0)
s_barrier
s_add_u32 m0, s48, 4096
ds_read_b64_tr_b16 a[160:161], v9 offset:36864
ds_read_b64_tr_b16 a[162:163], v9 offset:39168
global_load_lds_dwordx4 v0, s[40:41] offset:-4096
ds_read_b64_tr_b16 a[164:165], v9 offset:36872
global_load_lds_dwordx4 v1, s[40:41] offset:-3072
ds_read_b64_tr_b16 a[166:167], v9 offset:39176
global_load_lds_dwordx4 v2, s[40:41] offset:-2048
ds_read_b64_tr_b16 a[168:169], v9 offset:36928
global_load_lds_dwordx4 v3, s[40:41] offset:-1024
ds_read_b64_tr_b16 a[170:171], v9 offset:39232
ds_read_b64_tr_b16 a[172:173], v9 offset:36936
global_load_lds_dwordx4 v4, s[40:41] offset:0
ds_read_b64_tr_b16 a[174:175], v9 offset:39240
global_load_lds_dwordx4 v5, s[40:41] offset:1024
ds_read_b64_tr_b16 a[176:177], v9 offset:55296
global_load_lds_dwordx4 v6, s[40:41] offset:2048
ds_read_b64_tr_b16 a[178:179], v9 offset:57600
global_load_lds_dwordx4 v7, s[40:41] offset:3072
ds_read_b64_tr_b16 a[180:181], v9 offset:55304
ds_read_b64_tr_b16 a[182:183], v9 offset:57608
s_add_u32 m0, s48, 12288
ds_read_b64_tr_b16 a[184:185], v9 offset:55360
global_load_lds_dwordx4 v8, s[40:41] offset:-4096
ds_read_b64_tr_b16 a[186:187], v9 offset:57664
s_sub_u32 s40, s40, 589824
ds_read_b64_tr_b16 a[188:189], v9 offset:55368
s_subb_u32 s41, s41, 0
ds_read_b64_tr_b16 a[190:191], v9 offset:57672
s_waitcnt vmcnt(18) lgkmcnt(0)
s_barrier
ds_read_b64_tr_b16 a[192:193], v204 offset:8192
ds_read_b64_tr_b16 a[194:195], v204 offset:10496
ds_read_b64_tr_b16 a[196:197], v204 offset:8200
ds_read_b64_tr_b16 a[198:199], v204 offset:10504
ds_read_b64_tr_b16 a[200:201], v204 offset:8256
ds_read_b64_tr_b16 a[202:203], v204 offset:10560
ds_read_b64_tr_b16 a[204:205], v204 offset:8264
ds_read_b64_tr_b16 a[206:207], v204 offset:10568
ds_read_b64_tr_b16 a[208:209], v204 offset:26624
ds_read_b64_tr_b16 a[210:211], v204 offset:28928
ds_read_b64_tr_b16 a[212:213], v204 offset:26632
ds_read_b64_tr_b16 a[214:215], v204 offset:28936
ds_read_b64_tr_b16 a[216:217], v204 offset:26688
ds_read_b64_tr_b16 a[218:219], v204 offset:28992
ds_read_b64_tr_b16 a[220:221], v204 offset:26696
ds_read_b64_tr_b16 a[222:223], v204 offset:29000
s_waitcnt vmcnt(9) lgkmcnt(0)
s_barrier
ds_read_b64_tr_b16 a[224:225], v204 offset:45056
ds_read_b64_tr_b16 a[226:227], v204 offset:47360
ds_read_b64_tr_b16 a[228:229], v204 offset:45064
ds_read_b64_tr_b16 a[230:231], v204 offset:47368
ds_read_b64_tr_b16 a[232:233], v204 offset:45120
ds_read_b64_tr_b16 a[234:235], v204 offset:47424
ds_read_b64_tr_b16 a[236:237], v204 offset:45128
ds_read_b64_tr_b16 a[238:239], v204 offset:47432
ds_read_b64_tr_b16 a[240:241], v204 offset:63488
ds_read_b64_tr_b16 a[242:243], v205 offset:256
ds_read_b64_tr_b16 a[244:245], v204 offset:63496
ds_read_b64_tr_b16 a[246:247], v205 offset:264
ds_read_b64_tr_b16 a[248:249], v204 offset:63552
ds_read_b64_tr_b16 a[250:251], v205 offset:320
ds_read_b64_tr_b16 a[252:253], v204 offset:63560
ds_read_b64_tr_b16 a[254:255], v205 offset:328
s_waitcnt vmcnt(0) lgkmcnt(0)
s_barrier
ds_read_b64_tr_b16 v[28:29], v9 offset:0
ds_read_b64_tr_b16 v[30:31], v9 offset:2304
ds_read_b64_tr_b16 v[32:33], v9 offset:8
ds_read_b64_tr_b16 v[34:35], v9 offset:2312
ds_read_b64_tr_b16 v[36:37], v9 offset:64
ds_read_b64_tr_b16 v[38:39], v9 offset:2368
ds_read_b64_tr_b16 v[40:41], v9 offset:72
ds_read_b64_tr_b16 v[42:43], v9 offset:2376
ds_read_b64_tr_b16 v[44:45], v9 offset:18432
ds_read_b64_tr_b16 v[46:47], v9 offset:20736
ds_read_b64_tr_b16 v[48:49], v9 offset:18440
ds_read_b64_tr_b16 v[50:51], v9 offset:20744
ds_read_b64_tr_b16 v[52:53], v9 offset:18496
ds_read_b64_tr_b16 v[54:55], v9 offset:20800
ds_read_b64_tr_b16 v[56:57], v9 offset:18504
ds_read_b64_tr_b16 v[58:59], v9 offset:20808
s_waitcnt lgkmcnt(0)
s_barrier
v_add_lshl_u32 v252, v24, 0, 7
v_bfi_b32 v252, s69, v252, v23
v_add_lshl_u32 v253, v22, 0, 7
v_bfi_b32 v253, s69, v253, v21
s_mov_b32 s92, 0x01010101
s_mov_b32 s93, 0x01010101
s_mov_b64 s[76:77], 0
s_add_u32 m0, s49, 18432
v_add_u32 v15, 18688, v15
v_add_u32 v13, 18688, v13
s_xor_b32 s49, s49, 0x10000
s_mov_b32 m0, 51200
s_add_u32 m0, s49, 0
v_add_u32 v11, 18432, v11
v_add_u32 v15, 18688, v15
s_mov_b32 m0, 59392
v_add_u32 v13, 18688, v13
s_add_u32 m0, s49, 18432
s_mov_b64 s[76:77], -1
s_mov_b32 s70, 33
s_waitcnt vmcnt(0) lgkmcnt(0)
s_nop 7
)"
R"(s_branch .Lwo_in15
.p2align 6
.Lwo_top16:
s_waitcnt lgkmcnt(2)
v_mfma_f32_16x16x32_bf16 v[96:99], a[0:3], v[132:135], v[96:99]
ds_read_b128 v[144:147], v25 offset:3072
v_mfma_f32_16x16x32_bf16 v[100:103], a[4:7], v[132:135], v[100:103]
v_mfma_f32_16x16x32_bf16 v[104:107], a[8:11], v[132:135], v[104:107]
s_waitcnt vmcnt(7)
s_barrier
v_mfma_f32_16x16x32_bf16 v[108:111], a[12:15], v[132:135], v[108:111]
v_cvt_pk_bf16_f32 v164, v112, v113
v_cvt_pk_bf16_f32 v165, v114, v115
v_mfma_f32_16x16x32_bf16 v[128:131], v[60:63], v[132:135], 0
s_waitcnt lgkmcnt(2)
v_cvt_pk_bf16_f32 v166, v116, v117
v_cvt_pk_bf16_f32 v167, v118, v119
s_mov_b64 exec, 65535
global_store_dwordx2 v15, v[176:177], s[38:39] sc1
s_mov_b64 exec, -1
v_mfma_f32_16x16x32_bf16 v[96:99], a[16:19], v[136:139], v[96:99]
ds_read_b128 v[132:135], v25 offset:4096
v_add_f32 v254, v112, v113
v_add_u32 v15, 18688, v15
v_mfma_f32_16x16x32_bf16 v[100:103], a[20:23], v[136:139], v[100:103]
v_mul_f32 v255, v112, v112
v_fmac_f32 v255, v113, v113
v_mfma_f32_16x16x32_bf16 v[104:107], a[24:27], v[136:139], v[104:107]
global_store_dwordx4 v13, v[164:167], s[38:39] offset:0 sc1
v_mfma_f32_16x16x32_bf16 v[108:111], a[28:31], v[136:139], v[108:111]
v_cvt_pk_bf16_f32 v168, v120, v121
v_cvt_pk_bf16_f32 v169, v122, v123
v_mfma_f32_16x16x32_bf16 v[128:131], v[64:67], v[136:139], v[128:131]
s_waitcnt lgkmcnt(2)
v_cvt_pk_bf16_f32 v170, v124, v125
v_cvt_pk_bf16_f32 v171, v126, v127
v_mfma_f32_16x16x32_bf16 v[96:99], a[32:35], v[140:143], v[96:99]
s_cmp_eq_u32 s70, 32
ds_read_b128 v[136:139], v25 offset:5120
v_add_f32 v254, v254, v114
v_mfma_f32_16x16x32_bf16 v[100:103], a[36:39], v[140:143], v[100:103]
s_cselect_b64 s[42:43], s[44:45], s[42:43]
global_store_dwordx4 v13, v[168:171], s[38:39] offset:1024 sc1
v_mfma_f32_16x16x32_bf16 v[104:107], a[40:43], v[140:143], v[104:107]
s_cmp_eq_u32 s70, 16
v_add_u32 v13, 18688, v13
v_fmac_f32 v255, v114, v114
v_mfma_f32_16x16x32_bf16 v[108:111], a[44:47], v[140:143], v[108:111]
s_cselect_b64 s[42:43], s[46:47], s[42:43]
v_add_f32 v254, v254, v115
v_fmac_f32 v255, v115, v115
v_mfma_f32_16x16x32_bf16 v[128:131], v[68:71], v[140:143], v[128:131]
s_waitcnt lgkmcnt(2)
v_add_f32 v254, v254, v116
v_fmac_f32 v255, v116, v116
v_mfma_f32_16x16x32_bf16 v[96:99], a[48:51], v[144:147], v[96:99]
s_xor_b32 s49, s49, 0x10000
ds_read_b128 v[140:143], v25 offset:6144
v_add_f32 v254, v254, v117
v_mfma_f32_16x16x32_bf16 v[100:103], a[52:55], v[144:147], v[100:103]
s_mov_b32 m0, 51200
v_fmac_f32 v255, v117, v117
v_add_f32 v254, v254, v118
v_mfma_f32_16x16x32_bf16 v[104:107], a[56:59], v[144:147], v[104:107]
v_fmac_f32 v255, v118, v118
v_add_f32 v254, v254, v119
v_mfma_f32_16x16x32_bf16 v[108:111], a[60:63], v[144:147], v[108:111]
v_fmac_f32 v255, v119, v119
v_add_f32 v254, v254, v120
v_mfma_f32_16x16x32_bf16 v[128:131], v[72:75], v[144:147], v[128:131]
s_waitcnt lgkmcnt(2)
v_fmac_f32 v255, v120, v120
v_add_f32 v254, v254, v121
v_mfma_f32_16x16x32_bf16 v[96:99], a[64:67], v[132:135], v[96:99]
ds_read_b128 v[144:147], v25 offset:7168
v_fmac_f32 v255, v121, v121
v_mfma_f32_16x16x32_bf16 v[100:103], a[68:71], v[132:135], v[100:103]
v_add_f32 v254, v254, v122
v_fmac_f32 v255, v122, v122
v_mfma_f32_16x16x32_bf16 v[104:107], a[72:75], v[132:135], v[104:107]
global_load_lds_dwordx4 v16, s[42:43] offset:0 sc1
v_add_f32 v254, v254, v123
v_mfma_f32_16x16x32_bf16 v[108:111], a[76:79], v[132:135], v[108:111]
v_fmac_f32 v255, v123, v123
v_add_f32 v254, v254, v124
v_mfma_f32_16x16x32_bf16 v[128:131], v[76:79], v[132:135], v[128:131]
s_waitcnt vmcnt(9)
ds_read_b128 v[148:151], v19 offset:8192
ds_read_b128 v[152:155], v20 offset:8192
s_waitcnt lgkmcnt(4)
v_mfma_f32_16x16x32_bf16 v[96:99], a[80:83], v[136:139], v[96:99]
ds_read_b128 v[132:135], v25 offset:8192
v_fmac_f32 v255, v124, v124
v_mfma_f32_16x16x32_bf16 v[100:103], a[84:87], v[136:139], v[100:103]
v_add_f32 v254, v254, v125
v_fmac_f32 v255, v125, v125
v_mfma_f32_16x16x32_bf16 v[104:107], a[88:91], v[136:139], v[104:107]
v_add_f32 v254, v254, v126
v_fmac_f32 v255, v126, v126
v_mfma_f32_16x16x32_bf16 v[108:111], a[92:95], v[136:139], v[108:111]
v_add_f32 v254, v254, v127
v_fmac_f32 v255, v127, v127
v_mfma_f32_16x16x32_bf16 v[128:131], v[80:83], v[136:139], v[128:131]
s_waitcnt lgkmcnt(4)
global_load_lds_dwordx4 v17, s[42:43] offset:1024 sc1
v_mfma_f32_16x16x32_bf16 v[96:99], a[96:99], v[140:143], v[96:99]
ds_read_b128 v[136:139], v25 offset:9216
v_mfma_f32_16x16x32_bf16 v[100:103], a[100:103], v[140:143], v[100:103]
ds_write_b64 v27, v[254:255] offset:46080
v_mfma_f32_16x16x32_bf16 v[104:107], a[104:107], v[140:143], v[104:107]
s_waitcnt lgkmcnt(4)
v_lshlrev_b32 v112, 16, v148
v_and_b32 v113, 0xffff0000, v148
v_mfma_f32_16x16x32_bf16 v[108:111], a[108:111], v[140:143], v[108:111]
v_lshlrev_b32 v114, 16, v149
v_and_b32 v115, 0xffff0000, v149
v_mfma_f32_16x16x32_bf16 v[128:131], v[84:87], v[140:143], v[128:131]
v_lshlrev_b32 v116, 16, v150
v_and_b32 v117, 0xffff0000, v150
v_mfma_f32_16x16x32_bf16 v[96:99], a[112:115], v[144:147], v[96:99]
s_add_u32 m0, s49, 0
ds_read_b128 v[140:143], v25 offset:10240
v_lshlrev_b32 v118, 16, v151
v_mfma_f32_16x16x32_bf16 v[100:103], a[116:119], v[144:147], v[100:103]
s_waitcnt lgkmcnt(4)
v_and_b32 v119, 0xffff0000, v151
v_lshlrev_b32 v120, 16, v152
v_mfma_f32_16x16x32_bf16 v[104:107], a[120:123], v[144:147], v[104:107]
s_sub_u32 s42, s42, 18432
v_and_b32 v121, 0xffff0000, v152
v_lshlrev_b32 v122, 16, v153
v_mfma_f32_16x16x32_bf16 v[108:111], a[124:127], v[144:147], v[108:111]
s_subb_u32 s43, s43, 0
v_and_b32 v123, 0xffff0000, v153
v_lshlrev_b32 v124, 16, v154
v_mfma_f32_16x16x32_bf16 v[128:131], v[88:91], v[144:147], v[128:131]
s_waitcnt lgkmcnt(3)
v_and_b32 v125, 0xffff0000, v154
v_lshlrev_b32 v126, 16, v155
v_mfma_f32_16x16x32_bf16 v[96:99], a[128:131], v[132:135], v[96:99]
ds_read_b128 v[144:147], v25 offset:11264
v_and_b32 v127, 0xffff0000, v155
v_mfma_f32_16x16x32_bf16 v[100:103], a[132:135], v[132:135], v[100:103]
v_mfma_f32_16x16x32_bf16 v[104:107], a[136:139], v[132:135], v[104:107]
v_mfma_f32_16x16x32_bf16 v[108:111], a[140:143], v[132:135], v[108:111]
v_mfma_f32_16x16x32_bf16 v[128:131], v[92:95], v[132:135], v[128:131]
s_waitcnt lgkmcnt(3)
ds_read_b64 v[176:177], v27 offset:43008
ds_read_b64 v[180:181], v27 offset:43520
v_mfma_f32_16x16x32_bf16 v[96:99], a[144:147], v[136:139], v[96:99]
ds_read_b128 v[132:135], v25 offset:12288
ds_read_b64 v[184:185], v27 offset:44032
v_mfma_f32_16x16x32_bf16 v[100:103], a[148:151], v[136:139], v[100:103]
ds_read_b64 v[188:189], v27 offset:44544
v_mfma_f32_16x16x32_bf16 v[104:107], a[152:155], v[136:139], v[104:107]
v_mfma_f32_16x16x32_bf16 v[108:111], a[156:159], v[136:139], v[108:111]
s_waitcnt lgkmcnt(6)
v_mfma_f32_16x16x32_bf16 v[96:99], a[160:163], v[140:143], v[96:99]
ds_read_b128 v[136:139], v25 offset:13312
v_mfma_f32_16x16x32_bf16 v[100:103], a[164:167], v[140:143], v[100:103]
v_mfma_f32_16x16x32_bf16 v[104:107], a[168:171], v[140:143], v[104:107]
v_mfma_f32_16x16x32_bf16 v[108:111], a[172:175], v[140:143], v[108:111]
s_waitcnt lgkmcnt(4)
v_add_f32 v176, v176, v180
v_add_f32 v177, v177, v181
v_mfma_f32_16x16x32_bf16 v[96:99], a[176:179], v[144:147], v[96:99]
s_waitcnt lgkmcnt(1)
ds_read_b128 v[140:143], v25 offset:14336
v_add_f32 v184, v184, v188
v_mfma_f32_16x16x32_bf16 v[100:103], a[180:183], v[144:147], v[100:103]
v_mfma_f32_16x16x32_bf16 v[104:107], a[184:187], v[144:147], v[104:107]
s_barrier
v_mfma_f32_16x16x32_bf16 v[108:111], a[188:191], v[144:147], v[108:111]
global_load_lds_dwordx4 v11, s[38:39] offset:-2048 sc1
v_add_f32 v185, v185, v189
v_mfma_f32_16x16x32_bf16 v[96:99], a[192:195], v[132:135], v[96:99]
ds_read_b128 v[144:147], v25 offset:15360
v_add_f32 v176, v176, v184
v_mfma_f32_16x16x32_bf16 v[100:103], a[196:199], v[132:135], v[100:103]
v_add_f32 v177, v177, v185
v_mov_b32 v202, v176
v_mfma_f32_16x16x32_bf16 v[104:107], a[200:203], v[132:135], v[104:107]
ds_write_b128 v26, v[128:131] offset:38912
v_mfma_f32_16x16x32_bf16 v[108:111], a[204:207], v[132:135], v[108:111]
s_waitcnt lgkmcnt(3)
global_load_lds_dwordx4 v11, s[38:39] offset:-1024 sc1
v_mov_b32 v203, v177
v_mfma_f32_16x16x32_bf16 v[96:99], a[208:211], v[136:139], v[96:99]
ds_read_b128 v[132:135], v25 offset:16384
v_mfma_f32_16x16x32_bf16 v[100:103], a[212:215], v[136:139], v[100:103]
v_permlane16_swap_b32 v176, v202
v_mfma_f32_16x16x32_bf16 v[104:107], a[216:219], v[136:139], v[104:107]
global_load_lds_dwordx4 v11, s[38:39] offset:0 sc1
v_mfma_f32_16x16x32_bf16 v[108:111], a[220:223], v[136:139], v[108:111]
s_waitcnt lgkmcnt(3)
v_permlane16_swap_b32 v177, v203
v_mfma_f32_16x16x32_bf16 v[96:99], a[224:227], v[140:143], v[96:99]
ds_read_b128 v[136:139], v25 offset:17408
v_add_f32 v176, v176, v202
v_mfma_f32_16x16x32_bf16 v[100:103], a[228:231], v[140:143], v[100:103]
global_load_lds_dwordx4 v11, s[38:39] offset:1024 sc1
v_add_f32 v177, v177, v203
v_mfma_f32_16x16x32_bf16 v[104:107], a[232:235], v[140:143], v[104:107]
v_mov_b32 v202, v176
v_mov_b32 v203, v177
v_mfma_f32_16x16x32_bf16 v[108:111], a[236:239], v[140:143], v[108:111]
s_waitcnt lgkmcnt(3)
v_permlane32_swap_b32 v176, v202
v_mfma_f32_16x16x32_bf16 v[96:99], a[240:243], v[144:147], v[96:99]
global_load_lds_dwordx4 v11, s[38:39] offset:2048 sc1
v_add_u32 v11, 18432, v11
v_mfma_f32_16x16x32_bf16 v[100:103], a[244:247], v[144:147], v[100:103]
v_mfma_f32_16x16x32_bf16 v[104:107], a[248:251], v[144:147], v[104:107]
s_waitcnt vmcnt(10) lgkmcnt(2)
s_barrier
v_mfma_f32_16x16x32_bf16 v[108:111], a[252:255], v[144:147], v[108:111]
s_waitcnt lgkmcnt(1)
ds_read_b128 v[140:143], v25 offset:18432
v_min_u32 v11, v11, v12
v_mfma_f32_16x16x32_bf16 v[96:99], v[28:31], v[132:135], v[96:99]
ds_read_b128 v[144:147], v25 offset:19456
v_mfma_f32_16x16x32_bf16 v[100:103], v[32:35], v[132:135], v[100:103]
v_permlane32_swap_b32 v177, v203
v_mfma_f32_16x16x32_bf16 v[104:107], v[36:39], v[132:135], v[104:107]
v_add_f32 v176, v176, v202
v_add_f32 v177, v177, v203
v_mfma_f32_16x16x32_bf16 v[108:111], v[40:43], v[132:135], v[108:111]
s_waitcnt lgkmcnt(2)
v_mfma_f32_16x16x32_bf16 v[96:99], v[44:47], v[136:139], v[96:99]
ds_read_b128 v[132:135], v25 offset:20480
v_mfma_f32_16x16x32_bf16 v[100:103], v[48:51], v[136:139], v[100:103]
v_mfma_f32_16x16x32_bf16 v[104:107], v[52:55], v[136:139], v[104:107]
v_mfma_f32_16x16x32_bf16 v[108:111], v[56:59], v[136:139], v[108:111]
s_waitcnt lgkmcnt(2)
v_mfma_f32_16x16x32_bf16 v[112:115], a[0:3], v[140:143], v[112:115]
ds_read_b128 v[136:139], v25 offset:21504
v_mfma_f32_16x16x32_bf16 v[116:119], a[4:7], v[140:143], v[116:119]
v_mfma_f32_16x16x32_bf16 v[120:123], a[8:11], v[140:143], v[120:123]
s_waitcnt vmcnt(7)
s_barrier
v_mfma_f32_16x16x32_bf16 v[124:127], a[12:15], v[140:143], v[124:127]
v_cvt_pk_bf16_f32 v164, v96, v97
v_cvt_pk_bf16_f32 v165, v98, v99
v_mfma_f32_16x16x32_bf16 v[128:131], v[60:63], v[140:143], 0
s_waitcnt lgkmcnt(2)
v_cvt_pk_bf16_f32 v166, v100, v101
v_cvt_pk_bf16_f32 v167, v102, v103
s_mov_b64 exec, 65535
global_store_dwordx2 v15, v[176:177], s[38:39] sc1
s_mov_b64 exec, -1
v_mfma_f32_16x16x32_bf16 v[112:115], a[16:19], v[144:147], v[112:115]
ds_read_b128 v[140:143], v25 offset:22528
v_add_u32 v15, 18688, v15
v_mfma_f32_16x16x32_bf16 v[116:119], a[20:23], v[144:147], v[116:119]
v_add_f32 v254, v96, v97
v_mul_f32 v255, v96, v96
v_mfma_f32_16x16x32_bf16 v[120:123], a[24:27], v[144:147], v[120:123]
global_store_dwordx4 v13, v[164:167], s[38:39] offset:0 sc1
v_mfma_f32_16x16x32_bf16 v[124:127], a[28:31], v[144:147], v[124:127]
v_cvt_pk_bf16_f32 v168, v104, v105
v_cvt_pk_bf16_f32 v169, v106, v107
v_mfma_f32_16x16x32_bf16 v[128:131], v[64:67], v[144:147], v[128:131]
s_waitcnt lgkmcnt(2)
v_cvt_pk_bf16_f32 v170, v108, v109
v_cvt_pk_bf16_f32 v171, v110, v111
v_mfma_f32_16x16x32_bf16 v[112:115], a[32:35], v[132:135], v[112:115]
s_mov_b32 m0, 59392
ds_read_b128 v[144:147], v25 offset:23552
v_fmac_f32 v255, v97, v97
v_mfma_f32_16x16x32_bf16 v[116:119], a[36:39], v[132:135], v[116:119]
global_store_dwordx4 v13, v[168:171], s[38:39] offset:1024 sc1
v_mfma_f32_16x16x32_bf16 v[120:123], a[40:43], v[132:135], v[120:123]
v_add_u32 v13, 18688, v13
v_add_f32 v254, v254, v98
v_mfma_f32_16x16x32_bf16 v[124:127], a[44:47], v[132:135], v[124:127]
v_fmac_f32 v255, v98, v98
v_add_f32 v254, v254, v99
v_mfma_f32_16x16x32_bf16 v[128:131], v[68:71], v[132:135], v[128:131]
s_waitcnt lgkmcnt(2)
v_fmac_f32 v255, v99, v99
v_add_f32 v254, v254, v100
v_mfma_f32_16x16x32_bf16 v[112:115], a[48:51], v[136:139], v[112:115]
ds_read_b128 v[132:135], v25 offset:24576
v_fmac_f32 v255, v100, v100
v_mfma_f32_16x16x32_bf16 v[116:119], a[52:55], v[136:139], v[116:119]
v_add_f32 v254, v254, v101
v_fmac_f32 v255, v101, v101
v_mfma_f32_16x16x32_bf16 v[120:123], a[56:59], v[136:139], v[120:123]
v_add_f32 v254, v254, v102
v_fmac_f32 v255, v102, v102
v_mfma_f32_16x16x32_bf16 v[124:127], a[60:63], v[136:139], v[124:127]
v_add_f32 v254, v254, v103
v_fmac_f32 v255, v103, v103
v_mfma_f32_16x16x32_bf16 v[128:131], v[72:75], v[136:139], v[128:131]
s_waitcnt lgkmcnt(2)
v_add_f32 v254, v254, v104
v_fmac_f32 v255, v104, v104
v_mfma_f32_16x16x32_bf16 v[112:115], a[64:67], v[140:143], v[112:115]
ds_read_b128 v[136:139], v25 offset:25600
v_add_f32 v254, v254, v105
v_mfma_f32_16x16x32_bf16 v[116:119], a[68:71], v[140:143], v[116:119]
v_fmac_f32 v255, v105, v105
v_add_f32 v254, v254, v106
v_mfma_f32_16x16x32_bf16 v[120:123], a[72:75], v[140:143], v[120:123]
global_load_lds_dwordx4 v16, s[42:43] offset:0 sc1
v_fmac_f32 v255, v106, v106
v_mfma_f32_16x16x32_bf16 v[124:127], a[76:79], v[140:143], v[124:127]
v_add_f32 v254, v254, v107
v_fmac_f32 v255, v107, v107
v_mfma_f32_16x16x32_bf16 v[128:131], v[76:79], v[140:143], v[128:131]
s_waitcnt vmcnt(9)
ds_read_b128 v[148:151], v19 offset:0
ds_read_b128 v[152:155], v20 offset:0
s_waitcnt lgkmcnt(4)
v_mfma_f32_16x16x32_bf16 v[112:115], a[80:83], v[144:147], v[112:115]
ds_read_b128 v[140:143], v25 offset:26624
v_add_f32 v254, v254, v108
v_mfma_f32_16x16x32_bf16 v[116:119], a[84:87], v[144:147], v[116:119]
v_fmac_f32 v255, v108, v108
v_add_f32 v254, v254, v109
v_mfma_f32_16x16x32_bf16 v[120:123], a[88:91], v[144:147], v[120:123]
v_fmac_f32 v255, v109, v109
v_add_f32 v254, v254, v110
v_mfma_f32_16x16x32_bf16 v[124:127], a[92:95], v[144:147], v[124:127]
v_fmac_f32 v255, v110, v110
v_add_f32 v254, v254, v111
v_mfma_f32_16x16x32_bf16 v[128:131], v[80:83], v[144:147], v[128:131]
s_waitcnt lgkmcnt(4)
global_load_lds_dwordx4 v17, s[42:43] offset:1024 sc1
v_fmac_f32 v255, v111, v111
v_mfma_f32_16x16x32_bf16 v[112:115], a[96:99], v[132:135], v[112:115]
ds_read_b128 v[144:147], v25 offset:27648
v_mfma_f32_16x16x32_bf16 v[116:119], a[100:103], v[132:135], v[116:119]
ds_write_b64 v27, v[254:255] offset:44032
v_mfma_f32_16x16x32_bf16 v[120:123], a[104:107], v[132:135], v[120:123]
s_waitcnt lgkmcnt(4)
v_lshlrev_b32 v96, 16, v148
v_and_b32 v97, 0xffff0000, v148
v_mfma_f32_16x16x32_bf16 v[124:127], a[108:111], v[132:135], v[124:127]
v_lshlrev_b32 v98, 16, v149
v_and_b32 v99, 0xffff0000, v149
v_mfma_f32_16x16x32_bf16 v[128:131], v[84:87], v[132:135], v[128:131]
v_lshlrev_b32 v100, 16, v150
v_and_b32 v101, 0xffff0000, v150
v_mfma_f32_16x16x32_bf16 v[112:115], a[112:115], v[136:139], v[112:115]
s_add_u32 m0, s49, 18432
ds_read_b128 v[132:135], v25 offset:28672
v_lshlrev_b32 v102, 16, v151
v_mfma_f32_16x16x32_bf16 v[116:119], a[116:119], v[136:139], v[116:119]
s_waitcnt lgkmcnt(4)
v_and_b32 v103, 0xffff0000, v151
v_lshlrev_b32 v104, 16, v152
v_mfma_f32_16x16x32_bf16 v[120:123], a[120:123], v[136:139], v[120:123]
s_sub_u32 s42, s42, 18432
v_and_b32 v105, 0xffff0000, v152
v_lshlrev_b32 v106, 16, v153
v_mfma_f32_16x16x32_bf16 v[124:127], a[124:127], v[136:139], v[124:127]
s_subb_u32 s43, s43, 0
v_and_b32 v107, 0xffff0000, v153
v_lshlrev_b32 v108, 16, v154
v_mfma_f32_16x16x32_bf16 v[128:131], v[88:91], v[136:139], v[128:131]
s_waitcnt lgkmcnt(3)
v_and_b32 v109, 0xffff0000, v154
v_lshlrev_b32 v110, 16, v155
v_mfma_f32_16x16x32_bf16 v[112:115], a[128:131], v[140:143], v[112:115]
ds_read_b128 v[136:139], v25 offset:29696
v_and_b32 v111, 0xffff0000, v155
v_mfma_f32_16x16x32_bf16 v[116:119], a[132:135], v[140:143], v[116:119]
v_mfma_f32_16x16x32_bf16 v[120:123], a[136:139], v[140:143], v[120:123]
v_mfma_f32_16x16x32_bf16 v[124:127], a[140:143], v[140:143], v[124:127]
v_mfma_f32_16x16x32_bf16 v[128:131], v[92:95], v[140:143], v[128:131]
s_waitcnt lgkmcnt(3)
ds_read_b64 v[176:177], v27 offset:45056
ds_read_b64 v[180:181], v27 offset:45568
v_mfma_f32_16x16x32_bf16 v[112:115], a[144:147], v[144:147], v[112:115]
ds_read_b128 v[140:143], v25 offset:30720
ds_read_b64 v[184:185], v27 offset:46080
v_mfma_f32_16x16x32_bf16 v[116:119], a[148:151], v[144:147], v[116:119]
ds_read_b64 v[188:189], v27 offset:46592
v_mfma_f32_16x16x32_bf16 v[120:123], a[152:155], v[144:147], v[120:123]
v_mfma_f32_16x16x32_bf16 v[124:127], a[156:159], v[144:147], v[124:127]
s_waitcnt lgkmcnt(6)
v_mfma_f32_16x16x32_bf16 v[112:115], a[160:163], v[132:135], v[112:115]
ds_read_b128 v[144:147], v25 offset:31744
v_mfma_f32_16x16x32_bf16 v[116:119], a[164:167], v[132:135], v[116:119]
v_mfma_f32_16x16x32_bf16 v[120:123], a[168:171], v[132:135], v[120:123]
v_mfma_f32_16x16x32_bf16 v[124:127], a[172:175], v[132:135], v[124:127]
s_waitcnt lgkmcnt(4)
v_add_f32 v176, v176, v180
v_add_f32 v177, v177, v181
v_mfma_f32_16x16x32_bf16 v[112:115], a[176:179], v[136:139], v[112:115]
s_waitcnt lgkmcnt(1)
ds_read_b128 v[132:135], v25 offset:32768
v_add_f32 v184, v184, v188
v_mfma_f32_16x16x32_bf16 v[116:119], a[180:183], v[136:139], v[116:119]
v_mfma_f32_16x16x32_bf16 v[120:123], a[184:187], v[136:139], v[120:123]
s_barrier
v_mfma_f32_16x16x32_bf16 v[124:127], a[188:191], v[136:139], v[124:127]
global_load_lds_dwordx4 v11, s[38:39] offset:-2048 sc1
v_add_f32 v185, v185, v189
)"
R"(v_mfma_f32_16x16x32_bf16 v[112:115], a[192:195], v[140:143], v[112:115]
s_mov_b64 s[76:77], -1
ds_read_b128 v[136:139], v25 offset:33792
v_add_f32 v176, v176, v184
v_mfma_f32_16x16x32_bf16 v[116:119], a[196:199], v[140:143], v[116:119]
v_add_f32 v177, v177, v185
v_mov_b32 v202, v176
v_mfma_f32_16x16x32_bf16 v[120:123], a[200:203], v[140:143], v[120:123]
ds_write_b128 v26, v[128:131] offset:41984
.Lwo_in15:
v_mfma_f32_16x16x32_bf16 v[124:127], a[204:207], v[140:143], v[124:127]
s_waitcnt lgkmcnt(3)
global_load_lds_dwordx4 v11, s[38:39] offset:-1024 sc1
v_mov_b32 v203, v177
v_mfma_f32_16x16x32_bf16 v[112:115], a[208:211], v[144:147], v[112:115]
ds_read_b128 v[140:143], v25 offset:34816
v_mfma_f32_16x16x32_bf16 v[116:119], a[212:215], v[144:147], v[116:119]
v_permlane16_swap_b32 v176, v202
v_mfma_f32_16x16x32_bf16 v[120:123], a[216:219], v[144:147], v[120:123]
global_load_lds_dwordx4 v11, s[38:39] offset:0 sc1
v_mfma_f32_16x16x32_bf16 v[124:127], a[220:223], v[144:147], v[124:127]
s_waitcnt lgkmcnt(3)
v_permlane16_swap_b32 v177, v203
v_mfma_f32_16x16x32_bf16 v[112:115], a[224:227], v[132:135], v[112:115]
ds_read_b128 v[144:147], v25 offset:35840
v_add_f32 v176, v176, v202
v_mfma_f32_16x16x32_bf16 v[116:119], a[228:231], v[132:135], v[116:119]
global_load_lds_dwordx4 v11, s[38:39] offset:1024 sc1
v_add_f32 v177, v177, v203
v_mfma_f32_16x16x32_bf16 v[120:123], a[232:235], v[132:135], v[120:123]
v_mov_b32 v202, v176
v_mov_b32 v203, v177
v_mfma_f32_16x16x32_bf16 v[124:127], a[236:239], v[132:135], v[124:127]
s_waitcnt lgkmcnt(3)
v_xor_b32 v25, 0x10000, v25
v_mfma_f32_16x16x32_bf16 v[112:115], a[240:243], v[136:139], v[112:115]
global_load_lds_dwordx4 v11, s[38:39] offset:2048 sc1
v_add_u32 v11, 18432, v11
v_mfma_f32_16x16x32_bf16 v[116:119], a[244:247], v[136:139], v[116:119]
v_mfma_f32_16x16x32_bf16 v[120:123], a[248:251], v[136:139], v[120:123]
s_waitcnt vmcnt(10) lgkmcnt(2)
s_barrier
v_mfma_f32_16x16x32_bf16 v[124:127], a[252:255], v[136:139], v[124:127]
s_waitcnt lgkmcnt(1)
ds_read_b128 v[132:135], v25 offset:0
v_min_u32 v11, v11, v12
v_mfma_f32_16x16x32_bf16 v[112:115], v[28:31], v[140:143], v[112:115]
ds_read_b128 v[136:139], v25 offset:1024
v_mfma_f32_16x16x32_bf16 v[116:119], v[32:35], v[140:143], v[116:119]
v_permlane32_swap_b32 v176, v202
v_mfma_f32_16x16x32_bf16 v[120:123], v[36:39], v[140:143], v[120:123]
v_permlane32_swap_b32 v177, v203
v_mfma_f32_16x16x32_bf16 v[124:127], v[40:43], v[140:143], v[124:127]
s_waitcnt lgkmcnt(2)
v_add_f32 v176, v176, v202
v_add_f32 v177, v177, v203
v_mfma_f32_16x16x32_bf16 v[112:115], v[44:47], v[144:147], v[112:115]
ds_read_b128 v[140:143], v25 offset:2048
v_mfma_f32_16x16x32_bf16 v[116:119], v[48:51], v[144:147], v[116:119]
v_mfma_f32_16x16x32_bf16 v[120:123], v[52:55], v[144:147], v[120:123]
v_mfma_f32_16x16x32_bf16 v[124:127], v[56:59], v[144:147], v[124:127]
s_sub_u32 s70, s70, 1
s_cbranch_scc0 .Lwo_top16
s_waitcnt vmcnt(7)
s_barrier
s_mov_b64 exec, 65535
global_store_dwordx2 v15, v[176:177], s[38:39] sc1
s_mov_b64 exec, -1
v_add_u32 v15, 18688, v15
v_cvt_pk_bf16_f32 v164, v112, v113
v_cvt_pk_bf16_f32 v165, v114, v115
v_cvt_pk_bf16_f32 v166, v116, v117
v_cvt_pk_bf16_f32 v167, v118, v119
global_store_dwordx4 v13, v[164:167], s[38:39] offset:0 sc1
v_cvt_pk_bf16_f32 v168, v120, v121
v_cvt_pk_bf16_f32 v169, v122, v123
v_cvt_pk_bf16_f32 v170, v124, v125
v_cvt_pk_bf16_f32 v171, v126, v127
global_store_dwordx4 v13, v[168:171], s[38:39] offset:1024 sc1
v_add_f32 v254, v112, v113
v_mul_f32 v255, v112, v112
v_fmac_f32 v255, v113, v113
v_add_f32 v254, v254, v114
v_fmac_f32 v255, v114, v114
v_add_f32 v254, v254, v115
v_fmac_f32 v255, v115, v115
v_add_f32 v254, v254, v116
v_fmac_f32 v255, v116, v116
v_add_f32 v254, v254, v117
v_fmac_f32 v255, v117, v117
v_add_f32 v254, v254, v118
v_fmac_f32 v255, v118, v118
v_add_f32 v254, v254, v119
v_fmac_f32 v255, v119, v119
v_add_f32 v254, v254, v120
v_fmac_f32 v255, v120, v120
v_add_f32 v254, v254, v121
v_fmac_f32 v255, v121, v121
v_add_f32 v254, v254, v122
v_fmac_f32 v255, v122, v122
v_add_f32 v254, v254, v123
v_fmac_f32 v255, v123, v123
v_add_f32 v254, v254, v124
v_fmac_f32 v255, v124, v124
v_add_f32 v254, v254, v125
v_fmac_f32 v255, v125, v125
v_add_f32 v254, v254, v126
v_fmac_f32 v255, v126, v126
v_add_f32 v254, v254, v127
v_fmac_f32 v255, v127, v127
ds_write_b64 v27, v[254:255] offset:46080
s_waitcnt vmcnt(0) lgkmcnt(0)
s_barrier
ds_read_b64 v[176:177], v27 offset:43008
ds_read_b64 v[180:181], v27 offset:43520
ds_read_b64 v[184:185], v27 offset:44032
ds_read_b64 v[188:189], v27 offset:44544
s_waitcnt lgkmcnt(2)
v_add_f32 v176, v176, v180
v_add_f32 v177, v177, v181
s_waitcnt lgkmcnt(0)
v_add_f32 v184, v184, v188
v_add_f32 v185, v185, v189
v_add_f32 v176, v176, v184
v_add_f32 v177, v177, v185
v_mov_b32 v202, v176
v_mov_b32 v203, v177
s_nop 0
v_permlane16_swap_b32 v176, v202
v_permlane16_swap_b32 v177, v203
v_add_f32 v176, v176, v202
v_add_f32 v177, v177, v203
v_mov_b32 v202, v176
v_mov_b32 v203, v177
s_nop 0
v_permlane32_swap_b32 v176, v202
v_permlane32_swap_b32 v177, v203
v_add_f32 v176, v176, v202
v_add_f32 v177, v177, v203
s_mov_b64 exec, 65535
global_store_dwordx2 v15, v[176:177], s[38:39] sc1
s_mov_b64 exec, -1
v_add_u32 v15, 18688, v15
ds_read_b64 v[176:177], v27 offset:45056
ds_read_b64 v[180:181], v27 offset:45568
ds_read_b64 v[184:185], v27 offset:46080
ds_read_b64 v[188:189], v27 offset:46592
s_waitcnt lgkmcnt(2)
v_add_f32 v176, v176, v180
v_add_f32 v177, v177, v181
s_waitcnt lgkmcnt(0)
v_add_f32 v184, v184, v188
v_add_f32 v185, v185, v189
v_add_f32 v176, v176, v184
v_add_f32 v177, v177, v185
v_mov_b32 v202, v176
v_mov_b32 v203, v177
s_nop 0
v_permlane16_swap_b32 v176, v202
v_permlane16_swap_b32 v177, v203
v_add_f32 v176, v176, v202
v_add_f32 v177, v177, v203
v_mov_b32 v202, v176
v_mov_b32 v203, v177
s_nop 0
v_permlane32_swap_b32 v176, v202
v_permlane32_swap_b32 v177, v203
v_add_f32 v176, v176, v202
v_add_f32 v177, v177, v203
s_mov_b64 exec, 65535
global_store_dwordx2 v15, v[176:177], s[38:39] sc1
s_mov_b64 exec, -1
v_add_u32 v15, 18688, v15
s_waitcnt vmcnt(0)
s_barrier
s_endpgm
.Lwo_w4:
s_add_u32 m0, s48, 4096
s_nop 0
global_load_lds_dwordx4 v0, s[40:41] offset:-4096
global_load_lds_dwordx4 v1, s[40:41] offset:-3072
global_load_lds_dwordx4 v2, s[40:41] offset:-2048
global_load_lds_dwordx4 v3, s[40:41] offset:-1024
global_load_lds_dwordx4 v4, s[40:41] offset:0
global_load_lds_dwordx4 v5, s[40:41] offset:1024
global_load_lds_dwordx4 v6, s[40:41] offset:2048
global_load_lds_dwordx4 v7, s[40:41] offset:3072
s_add_u32 m0, s48, 12288
s_nop 0
global_load_lds_dwordx4 v8, s[40:41] offset:-4096
s_add_u32 s40, s40, 73728
s_addc_u32 s41, s41, 0
s_add_u32 m0, s48, 40960
s_nop 0
global_load_lds_dwordx4 v0, s[40:41] offset:-4096
global_load_lds_dwordx4 v1, s[40:41] offset:-3072
global_load_lds_dwordx4 v2, s[40:41] offset:-2048
global_load_lds_dwordx4 v3, s[40:41] offset:-1024
global_load_lds_dwordx4 v4, s[40:41] offset:0
global_load_lds_dwordx4 v5, s[40:41] offset:1024
global_load_lds_dwordx4 v6, s[40:41] offset:2048
global_load_lds_dwordx4 v7, s[40:41] offset:3072
s_add_u32 m0, s48, 49152
s_nop 0
global_load_lds_dwordx4 v8, s[40:41] offset:-4096
s_add_u32 s40, s40, 73728
s_addc_u32 s41, s41, 0
s_add_u32 m0, s48, 77824
s_nop 0
global_load_lds_dwordx4 v0, s[40:41] offset:-4096
global_load_lds_dwordx4 v1, s[40:41] offset:-3072
global_load_lds_dwordx4 v2, s[40:41] offset:-2048
global_load_lds_dwordx4 v3, s[40:41] offset:-1024
global_load_lds_dwordx4 v4, s[40:41] offset:0
global_load_lds_dwordx4 v5, s[40:41] offset:1024
global_load_lds_dwordx4 v6, s[40:41] offset:2048
global_load_lds_dwordx4 v7, s[40:41] offset:3072
s_add_u32 m0, s48, 86016
s_nop 0
global_load_lds_dwordx4 v8, s[40:41] offset:-4096
s_add_u32 s40, s40, 73728
s_addc_u32 s41, s41, 0
v_add_u32 v204, 0x10000, v9
v_add_u32 v205, 0x20000, v9
v_add_u32 v206, 0x10000, v10
v_add_u32 v207, 0x20000, v10
s_waitcnt lgkmcnt(0)
s_mul_i32 s37, s37, 0x9E3779B1
s_add_u32 s37, s37, 0x7F4A7C15
v_add_u32 v22, s37, v22
v_add_u32 v24, s37, v24
s_waitcnt vmcnt(18)
s_barrier
s_add_u32 m0, s48, 114688
ds_read_b64_tr_b16 a[0:1], v9 offset:0
ds_read_b64_tr_b16 a[2:3], v9 offset:2304
global_load_lds_dwordx4 v0, s[40:41] offset:-4096
ds_read_b64_tr_b16 a[4:5], v9 offset:8
global_load_lds_dwordx4 v1, s[40:41] offset:-3072
ds_read_b64_tr_b16 a[6:7], v9 offset:2312
global_load_lds_dwordx4 v2, s[40:41] offset:-2048
ds_read_b64_tr_b16 a[8:9], v9 offset:64
global_load_lds_dwordx4 v3, s[40:41] offset:-1024
ds_read_b64_tr_b16 a[10:11], v9 offset:2368
ds_read_b64_tr_b16 a[12:13], v9 offset:72
global_load_lds_dwordx4 v4, s[40:41] offset:0
ds_read_b64_tr_b16 a[14:15], v9 offset:2376
global_load_lds_dwordx4 v5, s[40:41] offset:1024
ds_read_b64_tr_b16 a[16:17], v9 offset:18432
global_load_lds_dwordx4 v6, s[40:41] offset:2048
ds_read_b64_tr_b16 a[18:19], v9 offset:20736
global_load_lds_dwordx4 v7, s[40:41] offset:3072
ds_read_b64_tr_b16 a[20:21], v9 offset:18440
ds_read_b64_tr_b16 a[22:23], v9 offset:20744
s_add_u32 m0, s48, 122880
ds_read_b64_tr_b16 a[24:25], v9 offset:18496
global_load_lds_dwordx4 v8, s[40:41] offset:-4096
ds_read_b64_tr_b16 a[26:27], v9 offset:20800
s_add_u32 s40, s40, 73728
ds_read_b64_tr_b16 a[28:29], v9 offset:18504
s_addc_u32 s41, s41, 0
ds_read_b64_tr_b16 a[30:31], v9 offset:20808
s_waitcnt vmcnt(18) lgkmcnt(0)
s_barrier
s_add_u32 m0, s48, 4096
ds_read_b64_tr_b16 a[32:33], v9 offset:36864
ds_read_b64_tr_b16 a[34:35], v9 offset:39168
global_load_lds_dwordx4 v0, s[40:41] offset:-4096
ds_read_b64_tr_b16 a[36:37], v9 offset:36872
global_load_lds_dwordx4 v1, s[40:41] offset:-3072
ds_read_b64_tr_b16 a[38:39], v9 offset:39176
global_load_lds_dwordx4 v2, s[40:41] offset:-2048
ds_read_b64_tr_b16 a[40:41], v9 offset:36928
global_load_lds_dwordx4 v3, s[40:41] offset:-1024
ds_read_b64_tr_b16 a[42:43], v9 offset:39232
ds_read_b64_tr_b16 a[44:45], v9 offset:36936
global_load_lds_dwordx4 v4, s[40:41] offset:0
ds_read_b64_tr_b16 a[46:47], v9 offset:39240
global_load_lds_dwordx4 v5, s[40:41] offset:1024
ds_read_b64_tr_b16 a[48:49], v9 offset:55296
global_load_lds_dwordx4 v6, s[40:41] offset:2048
ds_read_b64_tr_b16 a[50:51], v9 offset:57600
global_load_lds_dwordx4 v7, s[40:41] offset:3072
ds_read_b64_tr_b16 a[52:53], v9 offset:55304
ds_read_b64_tr_b16 a[54:55], v9 offset:57608
s_add_u32 m0, s48, 12288
ds_read_b64_tr_b16 a[56:57], v9 offset:55360
global_load_lds_dwordx4 v8, s[40:41] offset:-4096
ds_read_b64_tr_b16 a[58:59], v9 offset:57664
s_add_u32 s40, s40, 73728
ds_read_b64_tr_b16 a[60:61], v9 offset:55368
s_addc_u32 s41, s41, 0
ds_read_b64_tr_b16 a[62:63], v9 offset:57672
s_waitcnt vmcnt(18) lgkmcnt(0)
s_barrier
s_add_u32 m0, s48, 40960
ds_read_b64_tr_b16 a[64:65], v204 offset:8192
ds_read_b64_tr_b16 a[66:67], v204 offset:10496
global_load_lds_dwordx4 v0, s[40:41] offset:-4096
ds_read_b64_tr_b16 a[68:69], v204 offset:8200
global_load_lds_dwordx4 v1, s[40:41] offset:-3072
ds_read_b64_tr_b16 a[70:71], v204 offset:10504
global_load_lds_dwordx4 v2, s[40:41] offset:-2048
ds_read_b64_tr_b16 a[72:73], v204 offset:8256
global_load_lds_dwordx4 v3, s[40:41] offset:-1024
ds_read_b64_tr_b16 a[74:75], v204 offset:10560
ds_read_b64_tr_b16 a[76:77], v204 offset:8264
global_load_lds_dwordx4 v4, s[40:41] offset:0
ds_read_b64_tr_b16 a[78:79], v204 offset:10568
global_load_lds_dwordx4 v5, s[40:41] offset:1024
ds_read_b64_tr_b16 a[80:81], v204 offset:26624
global_load_lds_dwordx4 v6, s[40:41] offset:2048
ds_read_b64_tr_b16 a[82:83], v204 offset:28928
global_load_lds_dwordx4 v7, s[40:41] offset:3072
ds_read_b64_tr_b16 a[84:85], v204 offset:26632
ds_read_b64_tr_b16 a[86:87], v204 offset:28936
s_add_u32 m0, s48, 49152
ds_read_b64_tr_b16 a[88:89], v204 offset:26688
global_load_lds_dwordx4 v8, s[40:41] offset:-4096
ds_read_b64_tr_b16 a[90:91], v204 offset:28992
s_add_u32 s40, s40, 73728
ds_read_b64_tr_b16 a[92:93], v204 offset:26696
s_addc_u32 s41, s41, 0
ds_read_b64_tr_b16 a[94:95], v204 offset:29000
s_waitcnt vmcnt(18) lgkmcnt(0)
s_barrier
s_add_u32 m0, s48, 77824
ds_read_b64_tr_b16 a[96:97], v204 offset:45056
ds_read_b64_tr_b16 a[98:99], v204 offset:47360
global_load_lds_dwordx4 v0, s[40:41] offset:-4096
ds_read_b64_tr_b16 a[100:101], v204 offset:45064
global_load_lds_dwordx4 v1, s[40:41] offset:-3072
ds_read_b64_tr_b16 a[102:103], v204 offset:47368
global_load_lds_dwordx4 v2, s[40:41] offset:-2048
ds_read_b64_tr_b16 a[104:105], v204 offset:45120
global_load_lds_dwordx4 v3, s[40:41] offset:-1024
ds_read_b64_tr_b16 a[106:107], v204 offset:47424
ds_read_b64_tr_b16 a[108:109], v204 offset:45128
global_load_lds_dwordx4 v4, s[40:41] offset:0
ds_read_b64_tr_b16 a[110:111], v204 offset:47432
global_load_lds_dwordx4 v5, s[40:41] offset:1024
ds_read_b64_tr_b16 a[112:113], v204 offset:63488
global_load_lds_dwordx4 v6, s[40:41] offset:2048
ds_read_b64_tr_b16 a[114:115], v205 offset:256
global_load_lds_dwordx4 v7, s[40:41] offset:3072
ds_read_b64_tr_b16 a[116:117], v204 offset:63496
ds_read_b64_tr_b16 a[118:119], v205 offset:264
s_add_u32 m0, s48, 86016
ds_read_b64_tr_b16 a[120:121], v204 offset:63552
global_load_lds_dwordx4 v8, s[40:41] offset:-4096
ds_read_b64_tr_b16 a[122:123], v205 offset:320
s_add_u32 s40, s40, 73728
ds_read_b64_tr_b16 a[124:125], v204 offset:63560
s_addc_u32 s41, s41, 0
ds_read_b64_tr_b16 a[126:127], v205 offset:328
s_waitcnt vmcnt(18) lgkmcnt(0)
s_barrier
s_add_u32 m0, s48, 114688
ds_read_b64_tr_b16 a[128:129], v9 offset:0
ds_read_b64_tr_b16 a[130:131], v9 offset:2304
global_load_lds_dwordx4 v0, s[40:41] offset:-4096
ds_read_b64_tr_b16 a[132:133], v9 offset:8
global_load_lds_dwordx4 v1, s[40:41] offset:-3072
ds_read_b64_tr_b16 a[134:135], v9 offset:2312
ds_read_b64_tr_b16 a[136:137], v9 offset:64
global_load_lds_dwordx4 v2, s[40:41] offset:-2048
ds_read_b64_tr_b16 a[138:139], v9 offset:2368
global_load_lds_dwordx4 v3, s[40:41] offset:-1024
ds_read_b64_tr_b16 a[140:141], v9 offset:72
global_load_lds_dwordx4 v4, s[40:41] offset:0
ds_read_b64_tr_b16 a[142:143], v9 offset:2376
ds_read_b64_tr_b16 a[144:145], v9 offset:18432
global_load_lds_dwordx4 v5, s[40:41] offset:1024
ds_read_b64_tr_b16 a[146:147], v9 offset:20736
global_load_lds_dwordx4 v6, s[40:41] offset:2048
ds_read_b64_tr_b16 a[148:149], v9 offset:18440
ds_read_b64_tr_b16 a[150:151], v9 offset:20744
global_load_lds_dwordx4 v7, s[40:41] offset:3072
ds_read_b64_tr_b16 a[152:153], v9 offset:18496
s_add_u32 m0, s48, 122880
ds_read_b64_tr_b16 a[154:155], v9 offset:20800
global_load_lds_dwordx4 v8, s[40:41] offset:-4096
ds_read_b64_tr_b16 a[156:157], v9 offset:18504
ds_read_b64_tr_b16 a[158:159], v9 offset:20808
s_add_u32 s40, s40, 73728
ds_read_b64_tr_b16 v[60:61], v10 offset:18432
s_addc_u32 s41, s41, 0
ds_read_b64_tr_b16 v[62:63], v10 offset:20736
s_waitcnt vmcnt(18) lgkmcnt(0)
s_barrier
s_add_u32 m0, s48, 4096
ds_read_b64_tr_b16 a[160:161], v9 offset:36864
ds_read_b64_tr_b16 a[162:163], v9 offset:39168
global_load_lds_dwordx4 v0, s[40:41] offset:-4096
ds_read_b64_tr_b16 a[164:165], v9 offset:36872
ds_read_b64_tr_b16 a[166:167], v9 offset:39176
global_load_lds_dwordx4 v1, s[40:41] offset:-3072
ds_read_b64_tr_b16 a[168:169], v9 offset:36928
global_load_lds_dwordx4 v2, s[40:41] offset:-2048
ds_read_b64_tr_b16 a[170:171], v9 offset:39232
)"
R"(ds_read_b64_tr_b16 a[172:173], v9 offset:36936
global_load_lds_dwordx4 v3, s[40:41] offset:-1024
ds_read_b64_tr_b16 a[174:175], v9 offset:39240
global_load_lds_dwordx4 v4, s[40:41] offset:0
ds_read_b64_tr_b16 v[64:65], v10 offset:36864
ds_read_b64_tr_b16 v[66:67], v10 offset:39168
global_load_lds_dwordx4 v5, s[40:41] offset:1024
ds_read_b64_tr_b16 a[176:177], v9 offset:55296
global_load_lds_dwordx4 v6, s[40:41] offset:2048
ds_read_b64_tr_b16 a[178:179], v9 offset:57600
ds_read_b64_tr_b16 a[180:181], v9 offset:55304
global_load_lds_dwordx4 v7, s[40:41] offset:3072
ds_read_b64_tr_b16 a[182:183], v9 offset:57608
s_add_u32 m0, s48, 12288
ds_read_b64_tr_b16 a[184:185], v9 offset:55360
ds_read_b64_tr_b16 a[186:187], v9 offset:57664
global_load_lds_dwordx4 v8, s[40:41] offset:-4096
ds_read_b64_tr_b16 a[188:189], v9 offset:55368
s_sub_u32 s40, s40, 589824
ds_read_b64_tr_b16 a[190:191], v9 offset:57672
ds_read_b64_tr_b16 v[68:69], v10 offset:55296
s_subb_u32 s41, s41, 0
ds_read_b64_tr_b16 v[70:71], v10 offset:57600
s_waitcnt vmcnt(18) lgkmcnt(0)
s_barrier
ds_read_b64_tr_b16 a[192:193], v204 offset:8192
ds_read_b64_tr_b16 a[194:195], v204 offset:10496
ds_read_b64_tr_b16 a[196:197], v204 offset:8200
ds_read_b64_tr_b16 a[198:199], v204 offset:10504
ds_read_b64_tr_b16 a[200:201], v204 offset:8256
ds_read_b64_tr_b16 a[202:203], v204 offset:10560
ds_read_b64_tr_b16 a[204:205], v204 offset:8264
ds_read_b64_tr_b16 a[206:207], v204 offset:10568
ds_read_b64_tr_b16 v[72:73], v206 offset:8192
ds_read_b64_tr_b16 v[74:75], v206 offset:10496
ds_read_b64_tr_b16 a[208:209], v204 offset:26624
ds_read_b64_tr_b16 a[210:211], v204 offset:28928
ds_read_b64_tr_b16 a[212:213], v204 offset:26632
ds_read_b64_tr_b16 a[214:215], v204 offset:28936
ds_read_b64_tr_b16 a[216:217], v204 offset:26688
ds_read_b64_tr_b16 a[218:219], v204 offset:28992
ds_read_b64_tr_b16 a[220:221], v204 offset:26696
ds_read_b64_tr_b16 a[222:223], v204 offset:29000
ds_read_b64_tr_b16 v[76:77], v206 offset:26624
ds_read_b64_tr_b16 v[78:79], v206 offset:28928
s_waitcnt vmcnt(9) lgkmcnt(0)
s_barrier
ds_read_b64_tr_b16 a[224:225], v204 offset:45056
ds_read_b64_tr_b16 a[226:227], v204 offset:47360
ds_read_b64_tr_b16 a[228:229], v204 offset:45064
ds_read_b64_tr_b16 a[230:231], v204 offset:47368
ds_read_b64_tr_b16 a[232:233], v204 offset:45120
ds_read_b64_tr_b16 a[234:235], v204 offset:47424
ds_read_b64_tr_b16 a[236:237], v204 offset:45128
ds_read_b64_tr_b16 a[238:239], v204 offset:47432
ds_read_b64_tr_b16 v[80:81], v206 offset:45056
ds_read_b64_tr_b16 v[82:83], v206 offset:47360
ds_read_b64_tr_b16 a[240:241], v204 offset:63488
ds_read_b64_tr_b16 a[242:243], v205 offset:256
ds_read_b64_tr_b16 a[244:245], v204 offset:63496
ds_read_b64_tr_b16 a[246:247], v205 offset:264
ds_read_b64_tr_b16 a[248:249], v204 offset:63552
ds_read_b64_tr_b16 a[250:251], v205 offset:320
ds_read_b64_tr_b16 a[252:253], v204 offset:63560
ds_read_b64_tr_b16 a[254:255], v205 offset:328
ds_read_b64_tr_b16 v[84:85], v206 offset:63488
ds_read_b64_tr_b16 v[86:87], v207 offset:256
s_waitcnt vmcnt(0) lgkmcnt(0)
s_barrier
ds_read_b64_tr_b16 v[28:29], v9 offset:0
ds_read_b64_tr_b16 v[30:31], v9 offset:2304
ds_read_b64_tr_b16 v[32:33], v9 offset:8
ds_read_b64_tr_b16 v[34:35], v9 offset:2312
ds_read_b64_tr_b16 v[36:37], v9 offset:64
ds_read_b64_tr_b16 v[38:39], v9 offset:2368
ds_read_b64_tr_b16 v[40:41], v9 offset:72
ds_read_b64_tr_b16 v[42:43], v9 offset:2376
ds_read_b64_tr_b16 v[88:89], v10 offset:0
ds_read_b64_tr_b16 v[90:91], v10 offset:2304
ds_read_b64_tr_b16 v[44:45], v9 offset:18432
ds_read_b64_tr_b16 v[46:47], v9 offset:20736
ds_read_b64_tr_b16 v[48:49], v9 offset:18440
ds_read_b64_tr_b16 v[50:51], v9 offset:20744
ds_read_b64_tr_b16 v[52:53], v9 offset:18496
ds_read_b64_tr_b16 v[54:55], v9 offset:20800
ds_read_b64_tr_b16 v[56:57], v9 offset:18504
ds_read_b64_tr_b16 v[58:59], v9 offset:20808
ds_read_b64_tr_b16 v[92:93], v10 offset:18432
ds_read_b64_tr_b16 v[94:95], v10 offset:20736
s_waitcnt lgkmcnt(0)
s_barrier
v_add_lshl_u32 v252, v24, 0, 7
v_bfi_b32 v252, s69, v252, v23
v_add_lshl_u32 v253, v22, 0, 7
v_bfi_b32 v253, s69, v253, v21
s_mov_b32 s92, 0x01010101
s_mov_b32 s93, 0x01010101
s_mov_b64 s[76:77], 0
s_add_u32 m0, s49, 18432
v_add_u32 v14, 18688, v14
s_xor_b32 s49, s49, 0x10000
s_mov_b32 m0, 53248
v_add_u32 v13, 18688, v13
s_add_u32 m0, s49, 0
v_add_u32 v11, 18432, v11
s_mov_b32 m0, 61440
v_add_u32 v14, 18688, v14
v_add_u32 v13, 18688, v13
s_add_u32 m0, s49, 18432
s_mov_b64 s[76:77], -1
s_mov_b32 s70, 33
s_waitcnt vmcnt(0) lgkmcnt(0)
s_nop 7
s_branch .Lwo_in18
.p2align 6
.Lwo_top19:
s_waitcnt lgkmcnt(5)
v_mfma_f32_16x16x32_bf16 v[96:99], a[144:147], v[132:135], v[96:99]
s_waitcnt lgkmcnt(4)
ds_read_b128 v[144:147], v25 offset:12288
v_add_f32 v176, v176, v184
v_mfma_f32_16x16x32_bf16 v[100:103], a[148:151], v[132:135], v[100:103]
v_mfma_f32_16x16x32_bf16 v[104:107], a[152:155], v[132:135], v[104:107]
s_waitcnt vmcnt(6)
s_barrier
v_mfma_f32_16x16x32_bf16 v[108:111], a[156:159], v[132:135], v[108:111]
v_add_f32 v177, v177, v185
v_add_f32 v178, v178, v186
v_mfma_f32_16x16x32_bf16 v[128:131], v[60:63], v[132:135], 0
s_waitcnt lgkmcnt(3)
v_add_f32 v179, v179, v187
v_add_f32 v176, v176, v188
v_mfma_f32_16x16x32_bf16 v[96:99], a[160:163], v[136:139], v[96:99]
ds_read_b128 v[132:135], v25 offset:13312
v_add_f32 v177, v177, v189
v_mfma_f32_16x16x32_bf16 v[100:103], a[164:167], v[136:139], v[100:103]
v_add_f32 v178, v178, v190
v_add_f32 v179, v179, v191
v_mfma_f32_16x16x32_bf16 v[104:107], a[168:171], v[136:139], v[104:107]
s_waitcnt lgkmcnt(3)
v_add_f32 v180, v180, v192
v_add_f32 v181, v181, v193
v_mfma_f32_16x16x32_bf16 v[108:111], a[172:175], v[136:139], v[108:111]
v_add_f32 v182, v182, v194
v_add_f32 v183, v183, v195
v_mfma_f32_16x16x32_bf16 v[128:131], v[64:67], v[136:139], v[128:131]
s_waitcnt lgkmcnt(2)
v_cvt_pk_bf16_f32 v172, v176, v177
v_cvt_pk_bf16_f32 v173, v178, v179
v_mfma_f32_16x16x32_bf16 v[96:99], a[176:179], v[140:143], v[96:99]
s_cmp_eq_u32 s70, 32
ds_read_b128 v[136:139], v25 offset:14336
v_cvt_pk_bf16_f32 v174, v180, v181
v_mfma_f32_16x16x32_bf16 v[100:103], a[180:183], v[140:143], v[100:103]
s_cselect_b64 s[42:43], s[44:45], s[42:43]
v_cvt_pk_bf16_f32 v175, v182, v183
v_cvt_pk_bf16_f32 v164, v112, v113
v_mfma_f32_16x16x32_bf16 v[104:107], a[184:187], v[140:143], v[104:107]
s_cmp_eq_u32 s70, 16
global_store_dwordx4 v14, v[172:175], s[38:39] sc1
v_mfma_f32_16x16x32_bf16 v[108:111], a[188:191], v[140:143], v[108:111]
s_cselect_b64 s[42:43], s[46:47], s[42:43]
v_add_u32 v14, 18688, v14
v_cvt_pk_bf16_f32 v165, v114, v115
v_mfma_f32_16x16x32_bf16 v[128:131], v[68:71], v[140:143], v[128:131]
s_waitcnt lgkmcnt(2)
v_cvt_pk_bf16_f32 v166, v116, v117
v_cvt_pk_bf16_f32 v167, v118, v119
v_mfma_f32_16x16x32_bf16 v[96:99], a[192:195], v[144:147], v[96:99]
s_xor_b32 s49, s49, 0x10000
ds_read_b128 v[140:143], v25 offset:15360
v_add_f32 v254, v112, v113
v_mfma_f32_16x16x32_bf16 v[100:103], a[196:199], v[144:147], v[100:103]
s_mov_b32 m0, 53248
global_store_dwordx4 v13, v[164:167], s[38:39] offset:0 sc1
v_mfma_f32_16x16x32_bf16 v[104:107], a[200:203], v[144:147], v[104:107]
v_cvt_pk_bf16_f32 v168, v120, v121
v_cvt_pk_bf16_f32 v169, v122, v123
v_mfma_f32_16x16x32_bf16 v[108:111], a[204:207], v[144:147], v[108:111]
v_cvt_pk_bf16_f32 v170, v124, v125
v_cvt_pk_bf16_f32 v171, v126, v127
v_mfma_f32_16x16x32_bf16 v[128:131], v[72:75], v[144:147], v[128:131]
s_waitcnt lgkmcnt(2)
global_store_dwordx4 v13, v[168:171], s[38:39] offset:1024 sc1
v_mfma_f32_16x16x32_bf16 v[96:99], a[208:211], v[132:135], v[96:99]
ds_read_b128 v[144:147], v25 offset:16384
v_add_u32 v13, 18688, v13
v_mfma_f32_16x16x32_bf16 v[100:103], a[212:215], v[132:135], v[100:103]
v_mul_f32 v255, v112, v112
v_fmac_f32 v255, v113, v113
v_mfma_f32_16x16x32_bf16 v[104:107], a[216:219], v[132:135], v[104:107]
v_add_f32 v254, v254, v114
v_fmac_f32 v255, v114, v114
v_mfma_f32_16x16x32_bf16 v[108:111], a[220:223], v[132:135], v[108:111]
v_add_f32 v254, v254, v115
v_fmac_f32 v255, v115, v115
v_mfma_f32_16x16x32_bf16 v[128:131], v[76:79], v[132:135], v[128:131]
s_waitcnt vmcnt(7)
global_load_lds_dwordx4 v16, s[42:43] offset:0 sc1
ds_read_b128 v[148:151], v19 offset:8192
s_waitcnt lgkmcnt(3)
v_mfma_f32_16x16x32_bf16 v[96:99], a[224:227], v[136:139], v[96:99]
ds_read_b128 v[132:135], v25 offset:17408
ds_read_b128 v[152:155], v20 offset:8192
v_mfma_f32_16x16x32_bf16 v[100:103], a[228:231], v[136:139], v[100:103]
v_add_f32 v254, v254, v116
v_fmac_f32 v255, v116, v116
v_mfma_f32_16x16x32_bf16 v[104:107], a[232:235], v[136:139], v[104:107]
v_add_f32 v254, v254, v117
v_fmac_f32 v255, v117, v117
v_mfma_f32_16x16x32_bf16 v[108:111], a[236:239], v[136:139], v[108:111]
v_add_f32 v254, v254, v118
v_fmac_f32 v255, v118, v118
v_mfma_f32_16x16x32_bf16 v[128:131], v[80:83], v[136:139], v[128:131]
s_waitcnt lgkmcnt(4)
v_add_f32 v254, v254, v119
v_fmac_f32 v255, v119, v119
v_mfma_f32_16x16x32_bf16 v[96:99], a[240:243], v[140:143], v[96:99]
ds_read_b128 v[136:139], v25 offset:0
v_add_f32 v254, v254, v120
v_mfma_f32_16x16x32_bf16 v[100:103], a[244:247], v[140:143], v[100:103]
global_load_lds_dwordx4 v17, s[42:43] offset:1024 sc1
v_fmac_f32 v255, v120, v120
v_mfma_f32_16x16x32_bf16 v[104:107], a[248:251], v[140:143], v[104:107]
v_add_f32 v254, v254, v121
v_fmac_f32 v255, v121, v121
v_mfma_f32_16x16x32_bf16 v[108:111], a[252:255], v[140:143], v[108:111]
v_add_f32 v254, v254, v122
v_fmac_f32 v255, v122, v122
v_mfma_f32_16x16x32_bf16 v[128:131], v[84:87], v[140:143], v[128:131]
s_waitcnt lgkmcnt(4)
v_add_f32 v254, v254, v123
v_fmac_f32 v255, v123, v123
v_mfma_f32_16x16x32_bf16 v[96:99], v[28:31], v[144:147], v[96:99]
ds_read_b128 v[140:143], v25 offset:1024
v_add_f32 v254, v254, v124
v_mfma_f32_16x16x32_bf16 v[100:103], v[32:35], v[144:147], v[100:103]
s_add_u32 m0, s49, 0
v_fmac_f32 v255, v124, v124
v_add_f32 v254, v254, v125
v_mfma_f32_16x16x32_bf16 v[104:107], v[36:39], v[144:147], v[104:107]
v_fmac_f32 v255, v125, v125
v_add_f32 v254, v254, v126
v_mfma_f32_16x16x32_bf16 v[108:111], v[40:43], v[144:147], v[108:111]
v_fmac_f32 v255, v126, v126
v_add_f32 v254, v254, v127
v_mfma_f32_16x16x32_bf16 v[128:131], v[88:91], v[144:147], v[128:131]
s_waitcnt lgkmcnt(3)
v_fmac_f32 v255, v127, v127
v_add_f32 v254, v254, v176
v_mfma_f32_16x16x32_bf16 v[96:99], v[44:47], v[132:135], v[96:99]
ds_read_b128 v[144:147], v25 offset:2048
v_fmac_f32 v255, v176, v176
v_mfma_f32_16x16x32_bf16 v[100:103], v[48:51], v[132:135], v[100:103]
v_add_f32 v254, v254, v177
v_fmac_f32 v255, v177, v177
v_mfma_f32_16x16x32_bf16 v[104:107], v[52:55], v[132:135], v[104:107]
v_add_f32 v254, v254, v178
v_fmac_f32 v255, v178, v178
v_mfma_f32_16x16x32_bf16 v[108:111], v[56:59], v[132:135], v[108:111]
v_add_f32 v254, v254, v179
v_fmac_f32 v255, v179, v179
v_mfma_f32_16x16x32_bf16 v[128:131], v[92:95], v[132:135], v[128:131]
s_waitcnt lgkmcnt(2)
v_add_f32 v254, v254, v180
v_fmac_f32 v255, v180, v180
v_mfma_f32_16x16x32_bf16 v[96:99], a[0:3], v[136:139], v[96:99]
ds_read_b128 v[132:135], v25 offset:3072
v_add_f32 v254, v254, v181
v_mfma_f32_16x16x32_bf16 v[100:103], a[4:7], v[136:139], v[100:103]
v_fmac_f32 v255, v181, v181
v_add_f32 v254, v254, v182
v_mfma_f32_16x16x32_bf16 v[104:107], a[8:11], v[136:139], v[104:107]
v_fmac_f32 v255, v182, v182
v_add_f32 v254, v254, v183
v_mfma_f32_16x16x32_bf16 v[108:111], a[12:15], v[136:139], v[108:111]
s_waitcnt lgkmcnt(2)
v_fmac_f32 v255, v183, v183
v_mfma_f32_16x16x32_bf16 v[96:99], a[16:19], v[140:143], v[96:99]
ds_read_b128 v[136:139], v25 offset:4096
v_mfma_f32_16x16x32_bf16 v[100:103], a[20:23], v[140:143], v[100:103]
ds_write_b64 v27, v[254:255] offset:46592
v_mfma_f32_16x16x32_bf16 v[104:107], a[24:27], v[140:143], v[104:107]
v_lshlrev_b32 v176, 16, v156
v_and_b32 v177, 0xffff0000, v156
v_mfma_f32_16x16x32_bf16 v[108:111], a[28:31], v[140:143], v[108:111]
s_waitcnt lgkmcnt(3)
v_lshlrev_b32 v178, 16, v157
v_and_b32 v179, 0xffff0000, v157
v_mfma_f32_16x16x32_bf16 v[96:99], a[32:35], v[144:147], v[96:99]
ds_read_b128 v[140:143], v25 offset:5120
v_lshlrev_b32 v180, 16, v158
v_mfma_f32_16x16x32_bf16 v[100:103], a[36:39], v[144:147], v[100:103]
v_mfma_f32_16x16x32_bf16 v[104:107], a[40:43], v[144:147], v[104:107]
s_barrier
v_mfma_f32_16x16x32_bf16 v[108:111], a[44:47], v[144:147], v[108:111]
s_waitcnt lgkmcnt(3)
v_and_b32 v181, 0xffff0000, v158
v_lshlrev_b32 v182, 16, v159
v_mfma_f32_16x16x32_bf16 v[96:99], a[48:51], v[132:135], v[96:99]
ds_read_b128 v[144:147], v25 offset:6144
global_load_lds_dwordx4 v11, s[38:39] offset:-2048 sc1
v_mfma_f32_16x16x32_bf16 v[100:103], a[52:55], v[132:135], v[100:103]
v_and_b32 v183, 0xffff0000, v159
v_add_f32 v180, v180, v128
v_mfma_f32_16x16x32_bf16 v[104:107], a[56:59], v[132:135], v[104:107]
v_add_f32 v181, v181, v129
v_add_f32 v182, v182, v130
v_mfma_f32_16x16x32_bf16 v[108:111], a[60:63], v[132:135], v[108:111]
s_waitcnt lgkmcnt(3)
global_load_lds_dwordx4 v11, s[38:39] offset:-1024 sc1
v_add_f32 v183, v183, v131
v_mfma_f32_16x16x32_bf16 v[96:99], a[64:67], v[136:139], v[96:99]
ds_read_b128 v[132:135], v25 offset:7168
global_load_dwordx4 v[156:159], v18, s[42:43] sc1
v_mfma_f32_16x16x32_bf16 v[100:103], a[68:71], v[136:139], v[100:103]
s_sub_u32 s42, s42, 18432
v_lshlrev_b32 v112, 16, v148
v_and_b32 v113, 0xffff0000, v148
v_mfma_f32_16x16x32_bf16 v[104:107], a[72:75], v[136:139], v[104:107]
s_subb_u32 s43, s43, 0
global_load_lds_dwordx4 v11, s[38:39] offset:0 sc1
v_add_u32 v11, 18432, v11
v_mfma_f32_16x16x32_bf16 v[108:111], a[76:79], v[136:139], v[108:111]
s_waitcnt lgkmcnt(2)
v_min_u32 v11, v11, v12
v_lshlrev_b32 v114, 16, v149
v_mfma_f32_16x16x32_bf16 v[96:99], a[80:83], v[140:143], v[96:99]
ds_read_b128 v[136:139], v25 offset:8192
v_and_b32 v115, 0xffff0000, v149
v_mfma_f32_16x16x32_bf16 v[100:103], a[84:87], v[140:143], v[100:103]
v_lshlrev_b32 v116, 16, v150
v_and_b32 v117, 0xffff0000, v150
v_mfma_f32_16x16x32_bf16 v[104:107], a[88:91], v[140:143], v[104:107]
v_lshlrev_b32 v118, 16, v151
v_and_b32 v119, 0xffff0000, v151
v_mfma_f32_16x16x32_bf16 v[108:111], a[92:95], v[140:143], v[108:111]
s_waitcnt lgkmcnt(2)
v_lshlrev_b32 v120, 16, v152
v_and_b32 v121, 0xffff0000, v152
v_mfma_f32_16x16x32_bf16 v[96:99], a[96:99], v[144:147], v[96:99]
v_lshlrev_b32 v122, 16, v153
v_and_b32 v123, 0xffff0000, v153
v_mfma_f32_16x16x32_bf16 v[100:103], a[100:103], v[144:147], v[100:103]
v_mfma_f32_16x16x32_bf16 v[104:107], a[104:107], v[144:147], v[104:107]
s_waitcnt vmcnt(9)
s_barrier
v_mfma_f32_16x16x32_bf16 v[108:111], a[108:111], v[144:147], v[108:111]
s_waitcnt lgkmcnt(1)
ds_read_b128 v[140:143], v25 offset:27648
ds_read_b128 v[184:187], v26 offset:36864
v_mfma_f32_16x16x32_bf16 v[96:99], a[112:115], v[132:135], v[96:99]
ds_read_b128 v[144:147], v25 offset:28672
ds_read_b128 v[188:191], v26 offset:37888
v_mfma_f32_16x16x32_bf16 v[100:103], a[116:119], v[132:135], v[100:103]
ds_read_b128 v[192:195], v26 offset:38912
v_lshlrev_b32 v124, 16, v154
v_mfma_f32_16x16x32_bf16 v[104:107], a[120:123], v[132:135], v[104:107]
v_and_b32 v125, 0xffff0000, v154
v_lshlrev_b32 v126, 16, v155
v_mfma_f32_16x16x32_bf16 v[108:111], a[124:127], v[132:135], v[108:111]
s_waitcnt lgkmcnt(5)
v_and_b32 v127, 0xffff0000, v155
v_mfma_f32_16x16x32_bf16 v[96:99], a[128:131], v[136:139], v[96:99]
ds_read_b128 v[132:135], v25 offset:29696
v_mfma_f32_16x16x32_bf16 v[100:103], a[132:135], v[136:139], v[100:103]
v_mfma_f32_16x16x32_bf16 v[104:107], a[136:139], v[136:139], v[104:107]
s_waitcnt lgkmcnt(4)
v_add_f32 v176, v176, v184
v_add_f32 v177, v177, v185
v_mfma_f32_16x16x32_bf16 v[108:111], a[140:143], v[136:139], v[108:111]
v_add_f32 v178, v178, v186
v_add_f32 v179, v179, v187
v_mfma_f32_16x16x32_bf16 v[112:115], a[144:147], v[140:143], v[112:115]
s_waitcnt lgkmcnt(2)
ds_read_b128 v[136:139], v25 offset:30720
v_add_f32 v176, v176, v188
v_mfma_f32_16x16x32_bf16 v[116:119], a[148:151], v[140:143], v[116:119]
v_mfma_f32_16x16x32_bf16 v[120:123], a[152:155], v[140:143], v[120:123]
s_waitcnt vmcnt(6)
s_barrier
v_mfma_f32_16x16x32_bf16 v[124:127], a[156:159], v[140:143], v[124:127]
v_add_f32 v177, v177, v189
v_add_f32 v178, v178, v190
v_mfma_f32_16x16x32_bf16 v[128:131], v[60:63], v[140:143], 0
s_waitcnt lgkmcnt(2)
v_add_f32 v179, v179, v191
v_add_f32 v180, v180, v192
v_mfma_f32_16x16x32_bf16 v[112:115], a[160:163], v[144:147], v[112:115]
ds_read_b128 v[140:143], v25 offset:31744
v_add_f32 v181, v181, v193
v_mfma_f32_16x16x32_bf16 v[116:119], a[164:167], v[144:147], v[116:119]
v_add_f32 v182, v182, v194
v_add_f32 v183, v183, v195
v_mfma_f32_16x16x32_bf16 v[120:123], a[168:171], v[144:147], v[120:123]
v_cvt_pk_bf16_f32 v172, v176, v177
v_cvt_pk_bf16_f32 v173, v178, v179
v_mfma_f32_16x16x32_bf16 v[124:127], a[172:175], v[144:147], v[124:127]
v_cvt_pk_bf16_f32 v174, v180, v181
v_cvt_pk_bf16_f32 v175, v182, v183
)"
R"(v_mfma_f32_16x16x32_bf16 v[128:131], v[64:67], v[144:147], v[128:131]
s_waitcnt lgkmcnt(2)
global_store_dwordx4 v14, v[172:175], s[38:39] sc1
v_mfma_f32_16x16x32_bf16 v[112:115], a[176:179], v[132:135], v[112:115]
s_mov_b32 m0, 61440
ds_read_b128 v[144:147], v25 offset:32768
v_add_u32 v14, 18688, v14
v_mfma_f32_16x16x32_bf16 v[116:119], a[180:183], v[132:135], v[116:119]
v_cvt_pk_bf16_f32 v164, v96, v97
v_cvt_pk_bf16_f32 v165, v98, v99
v_mfma_f32_16x16x32_bf16 v[120:123], a[184:187], v[132:135], v[120:123]
v_cvt_pk_bf16_f32 v166, v100, v101
v_cvt_pk_bf16_f32 v167, v102, v103
v_mfma_f32_16x16x32_bf16 v[124:127], a[188:191], v[132:135], v[124:127]
global_store_dwordx4 v13, v[164:167], s[38:39] offset:0 sc1
v_mfma_f32_16x16x32_bf16 v[128:131], v[68:71], v[132:135], v[128:131]
s_waitcnt lgkmcnt(2)
v_cvt_pk_bf16_f32 v168, v104, v105
v_cvt_pk_bf16_f32 v169, v106, v107
v_mfma_f32_16x16x32_bf16 v[112:115], a[192:195], v[136:139], v[112:115]
ds_read_b128 v[132:135], v25 offset:33792
v_cvt_pk_bf16_f32 v170, v108, v109
v_mfma_f32_16x16x32_bf16 v[116:119], a[196:199], v[136:139], v[116:119]
v_cvt_pk_bf16_f32 v171, v110, v111
v_add_f32 v254, v96, v97
v_mfma_f32_16x16x32_bf16 v[120:123], a[200:203], v[136:139], v[120:123]
global_store_dwordx4 v13, v[168:171], s[38:39] offset:1024 sc1
v_mfma_f32_16x16x32_bf16 v[124:127], a[204:207], v[136:139], v[124:127]
v_add_u32 v13, 18688, v13
v_mul_f32 v255, v96, v96
v_mfma_f32_16x16x32_bf16 v[128:131], v[72:75], v[136:139], v[128:131]
s_waitcnt lgkmcnt(2)
v_fmac_f32 v255, v97, v97
v_add_f32 v254, v254, v98
v_mfma_f32_16x16x32_bf16 v[112:115], a[208:211], v[140:143], v[112:115]
ds_read_b128 v[136:139], v25 offset:34816
v_fmac_f32 v255, v98, v98
v_mfma_f32_16x16x32_bf16 v[116:119], a[212:215], v[140:143], v[116:119]
v_add_f32 v254, v254, v99
v_fmac_f32 v255, v99, v99
v_mfma_f32_16x16x32_bf16 v[120:123], a[216:219], v[140:143], v[120:123]
v_add_f32 v254, v254, v100
v_fmac_f32 v255, v100, v100
v_mfma_f32_16x16x32_bf16 v[124:127], a[220:223], v[140:143], v[124:127]
v_add_f32 v254, v254, v101
v_fmac_f32 v255, v101, v101
v_mfma_f32_16x16x32_bf16 v[128:131], v[76:79], v[140:143], v[128:131]
s_waitcnt vmcnt(7)
global_load_lds_dwordx4 v16, s[42:43] offset:0 sc1
ds_read_b128 v[148:151], v19 offset:0
s_waitcnt lgkmcnt(3)
v_mfma_f32_16x16x32_bf16 v[112:115], a[224:227], v[144:147], v[112:115]
ds_read_b128 v[140:143], v25 offset:35840
ds_read_b128 v[152:155], v20 offset:0
v_mfma_f32_16x16x32_bf16 v[116:119], a[228:231], v[144:147], v[116:119]
v_add_f32 v254, v254, v102
v_fmac_f32 v255, v102, v102
v_mfma_f32_16x16x32_bf16 v[120:123], a[232:235], v[144:147], v[120:123]
v_add_f32 v254, v254, v103
v_fmac_f32 v255, v103, v103
v_mfma_f32_16x16x32_bf16 v[124:127], a[236:239], v[144:147], v[124:127]
v_add_f32 v254, v254, v104
v_fmac_f32 v255, v104, v104
v_mfma_f32_16x16x32_bf16 v[128:131], v[80:83], v[144:147], v[128:131]
s_waitcnt lgkmcnt(4)
v_add_f32 v254, v254, v105
v_fmac_f32 v255, v105, v105
v_mfma_f32_16x16x32_bf16 v[112:115], a[240:243], v[132:135], v[112:115]
ds_read_b128 v[144:147], v25 offset:18432
v_add_f32 v254, v254, v106
v_mfma_f32_16x16x32_bf16 v[116:119], a[244:247], v[132:135], v[116:119]
global_load_lds_dwordx4 v17, s[42:43] offset:1024 sc1
v_fmac_f32 v255, v106, v106
v_mfma_f32_16x16x32_bf16 v[120:123], a[248:251], v[132:135], v[120:123]
v_add_f32 v254, v254, v107
v_fmac_f32 v255, v107, v107
v_mfma_f32_16x16x32_bf16 v[124:127], a[252:255], v[132:135], v[124:127]
v_add_f32 v254, v254, v108
v_fmac_f32 v255, v108, v108
v_mfma_f32_16x16x32_bf16 v[128:131], v[84:87], v[132:135], v[128:131]
s_waitcnt lgkmcnt(4)
v_add_f32 v254, v254, v109
v_fmac_f32 v255, v109, v109
v_mfma_f32_16x16x32_bf16 v[112:115], v[28:31], v[136:139], v[112:115]
ds_read_b128 v[132:135], v25 offset:19456
v_add_f32 v254, v254, v110
v_mfma_f32_16x16x32_bf16 v[116:119], v[32:35], v[136:139], v[116:119]
s_add_u32 m0, s49, 18432
v_fmac_f32 v255, v110, v110
v_add_f32 v254, v254, v111
v_mfma_f32_16x16x32_bf16 v[120:123], v[36:39], v[136:139], v[120:123]
v_fmac_f32 v255, v111, v111
v_add_f32 v254, v254, v176
v_mfma_f32_16x16x32_bf16 v[124:127], v[40:43], v[136:139], v[124:127]
v_fmac_f32 v255, v176, v176
v_add_f32 v254, v254, v177
v_mfma_f32_16x16x32_bf16 v[128:131], v[88:91], v[136:139], v[128:131]
s_waitcnt lgkmcnt(3)
v_fmac_f32 v255, v177, v177
v_add_f32 v254, v254, v178
v_mfma_f32_16x16x32_bf16 v[112:115], v[44:47], v[140:143], v[112:115]
ds_read_b128 v[136:139], v25 offset:20480
v_fmac_f32 v255, v178, v178
v_mfma_f32_16x16x32_bf16 v[116:119], v[48:51], v[140:143], v[116:119]
v_add_f32 v254, v254, v179
v_fmac_f32 v255, v179, v179
v_mfma_f32_16x16x32_bf16 v[120:123], v[52:55], v[140:143], v[120:123]
v_add_f32 v254, v254, v180
v_fmac_f32 v255, v180, v180
v_mfma_f32_16x16x32_bf16 v[124:127], v[56:59], v[140:143], v[124:127]
v_add_f32 v254, v254, v181
v_fmac_f32 v255, v181, v181
v_mfma_f32_16x16x32_bf16 v[128:131], v[92:95], v[140:143], v[128:131]
s_waitcnt lgkmcnt(2)
v_add_f32 v254, v254, v182
v_fmac_f32 v255, v182, v182
v_mfma_f32_16x16x32_bf16 v[112:115], a[0:3], v[144:147], v[112:115]
ds_read_b128 v[140:143], v25 offset:21504
v_add_f32 v254, v254, v183
v_mfma_f32_16x16x32_bf16 v[116:119], a[4:7], v[144:147], v[116:119]
v_fmac_f32 v255, v183, v183
v_mfma_f32_16x16x32_bf16 v[120:123], a[8:11], v[144:147], v[120:123]
ds_write_b64 v27, v[254:255] offset:44544
v_mfma_f32_16x16x32_bf16 v[124:127], a[12:15], v[144:147], v[124:127]
s_waitcnt lgkmcnt(3)
v_lshlrev_b32 v176, 16, v160
v_and_b32 v177, 0xffff0000, v160
v_mfma_f32_16x16x32_bf16 v[112:115], a[16:19], v[132:135], v[112:115]
ds_read_b128 v[144:147], v25 offset:22528
v_lshlrev_b32 v178, 16, v161
v_mfma_f32_16x16x32_bf16 v[116:119], a[20:23], v[132:135], v[116:119]
v_and_b32 v179, 0xffff0000, v161
v_lshlrev_b32 v180, 16, v162
v_mfma_f32_16x16x32_bf16 v[120:123], a[24:27], v[132:135], v[120:123]
v_and_b32 v181, 0xffff0000, v162
v_lshlrev_b32 v182, 16, v163
v_mfma_f32_16x16x32_bf16 v[124:127], a[28:31], v[132:135], v[124:127]
s_waitcnt lgkmcnt(3)
v_and_b32 v183, 0xffff0000, v163
v_add_f32 v180, v180, v128
v_mfma_f32_16x16x32_bf16 v[112:115], a[32:35], v[136:139], v[112:115]
ds_read_b128 v[132:135], v25 offset:23552
v_add_f32 v181, v181, v129
v_mfma_f32_16x16x32_bf16 v[116:119], a[36:39], v[136:139], v[116:119]
v_mfma_f32_16x16x32_bf16 v[120:123], a[40:43], v[136:139], v[120:123]
s_barrier
v_mfma_f32_16x16x32_bf16 v[124:127], a[44:47], v[136:139], v[124:127]
s_waitcnt lgkmcnt(3)
v_add_f32 v182, v182, v130
v_add_f32 v183, v183, v131
v_mfma_f32_16x16x32_bf16 v[112:115], a[48:51], v[140:143], v[112:115]
s_mov_b64 s[76:77], -1
ds_read_b128 v[136:139], v25 offset:24576
global_load_lds_dwordx4 v11, s[38:39] offset:-2048 sc1
v_mfma_f32_16x16x32_bf16 v[116:119], a[52:55], v[140:143], v[116:119]
global_load_dwordx4 v[160:163], v18, s[42:43] sc1
v_lshlrev_b32 v96, 16, v148
v_mfma_f32_16x16x32_bf16 v[120:123], a[56:59], v[140:143], v[120:123]
s_sub_u32 s42, s42, 18432
v_and_b32 v97, 0xffff0000, v148
v_lshlrev_b32 v98, 16, v149
.Lwo_in18:
v_mfma_f32_16x16x32_bf16 v[124:127], a[60:63], v[140:143], v[124:127]
s_waitcnt lgkmcnt(2)
global_load_lds_dwordx4 v11, s[38:39] offset:-1024 sc1
v_and_b32 v99, 0xffff0000, v149
v_mfma_f32_16x16x32_bf16 v[112:115], a[64:67], v[144:147], v[112:115]
s_subb_u32 s43, s43, 0
ds_read_b128 v[140:143], v25 offset:25600
v_lshlrev_b32 v100, 16, v150
v_mfma_f32_16x16x32_bf16 v[116:119], a[68:71], v[144:147], v[116:119]
v_and_b32 v101, 0xffff0000, v150
v_lshlrev_b32 v102, 16, v151
v_mfma_f32_16x16x32_bf16 v[120:123], a[72:75], v[144:147], v[120:123]
global_load_lds_dwordx4 v11, s[38:39] offset:0 sc1
v_add_u32 v11, 18432, v11
v_mfma_f32_16x16x32_bf16 v[124:127], a[76:79], v[144:147], v[124:127]
s_waitcnt lgkmcnt(2)
v_min_u32 v11, v11, v12
v_and_b32 v103, 0xffff0000, v151
v_mfma_f32_16x16x32_bf16 v[112:115], a[80:83], v[132:135], v[112:115]
ds_read_b128 v[144:147], v25 offset:26624
v_lshlrev_b32 v104, 16, v152
v_mfma_f32_16x16x32_bf16 v[116:119], a[84:87], v[132:135], v[116:119]
v_and_b32 v105, 0xffff0000, v152
v_lshlrev_b32 v106, 16, v153
v_mfma_f32_16x16x32_bf16 v[120:123], a[88:91], v[132:135], v[120:123]
v_and_b32 v107, 0xffff0000, v153
v_lshlrev_b32 v108, 16, v154
v_mfma_f32_16x16x32_bf16 v[124:127], a[92:95], v[132:135], v[124:127]
s_waitcnt lgkmcnt(2)
v_xor_b32 v25, 0x10000, v25
v_and_b32 v109, 0xffff0000, v154
v_mfma_f32_16x16x32_bf16 v[112:115], a[96:99], v[136:139], v[112:115]
v_lshlrev_b32 v110, 16, v155
v_and_b32 v111, 0xffff0000, v155
v_mfma_f32_16x16x32_bf16 v[116:119], a[100:103], v[136:139], v[116:119]
v_mfma_f32_16x16x32_bf16 v[120:123], a[104:107], v[136:139], v[120:123]
s_waitcnt vmcnt(9)
s_barrier
v_mfma_f32_16x16x32_bf16 v[124:127], a[108:111], v[136:139], v[124:127]
s_waitcnt lgkmcnt(1)
ds_read_b128 v[132:135], v25 offset:9216
ds_read_b128 v[184:187], v26 offset:39936
v_mfma_f32_16x16x32_bf16 v[112:115], a[112:115], v[140:143], v[112:115]
ds_read_b128 v[136:139], v25 offset:10240
ds_read_b128 v[188:191], v26 offset:40960
v_mfma_f32_16x16x32_bf16 v[116:119], a[116:119], v[140:143], v[116:119]
ds_read_b128 v[192:195], v26 offset:41984
v_mfma_f32_16x16x32_bf16 v[120:123], a[120:123], v[140:143], v[120:123]
v_mfma_f32_16x16x32_bf16 v[124:127], a[124:127], v[140:143], v[124:127]
s_waitcnt lgkmcnt(5)
v_mfma_f32_16x16x32_bf16 v[112:115], a[128:131], v[144:147], v[112:115]
ds_read_b128 v[140:143], v25 offset:11264
v_mfma_f32_16x16x32_bf16 v[116:119], a[132:135], v[144:147], v[116:119]
v_mfma_f32_16x16x32_bf16 v[120:123], a[136:139], v[144:147], v[120:123]
v_mfma_f32_16x16x32_bf16 v[124:127], a[140:143], v[144:147], v[124:127]
s_sub_u32 s70, s70, 1
s_cbranch_scc0 .Lwo_top19
s_waitcnt vmcnt(6)
s_barrier
s_waitcnt lgkmcnt(4)
v_add_f32 v176, v176, v184
v_add_f32 v177, v177, v185
v_add_f32 v178, v178, v186
v_add_f32 v179, v179, v187
s_waitcnt lgkmcnt(2)
v_add_f32 v176, v176, v188
v_add_f32 v177, v177, v189
v_add_f32 v178, v178, v190
v_add_f32 v179, v179, v191
s_waitcnt lgkmcnt(1)
v_add_f32 v180, v180, v192
v_add_f32 v181, v181, v193
v_add_f32 v182, v182, v194
v_add_f32 v183, v183, v195
v_cvt_pk_bf16_f32 v172, v176, v177
v_cvt_pk_bf16_f32 v173, v178, v179
v_cvt_pk_bf16_f32 v174, v180, v181
v_cvt_pk_bf16_f32 v175, v182, v183
v_cvt_pk_bf16_f32 v164, v112, v113
global_store_dwordx4 v14, v[172:175], s[38:39] sc1
v_cvt_pk_bf16_f32 v165, v114, v115
v_cvt_pk_bf16_f32 v166, v116, v117
v_cvt_pk_bf16_f32 v167, v118, v119
global_store_dwordx4 v13, v[164:167], s[38:39] offset:0 sc1
v_cvt_pk_bf16_f32 v168, v120, v121
v_cvt_pk_bf16_f32 v169, v122, v123
v_cvt_pk_bf16_f32 v170, v124, v125
v_cvt_pk_bf16_f32 v171, v126, v127
global_store_dwordx4 v13, v[168:171], s[38:39] offset:1024 sc1
v_add_f32 v254, v112, v113
v_mul_f32 v255, v112, v112
v_fmac_f32 v255, v113, v113
v_add_f32 v254, v254, v114
v_fmac_f32 v255, v114, v114
v_add_f32 v254, v254, v115
v_fmac_f32 v255, v115, v115
v_add_f32 v254, v254, v116
v_fmac_f32 v255, v116, v116
v_add_f32 v254, v254, v117
v_fmac_f32 v255, v117, v117
v_add_f32 v254, v254, v118
v_fmac_f32 v255, v118, v118
v_add_f32 v254, v254, v119
v_fmac_f32 v255, v119, v119
v_add_f32 v254, v254, v120
v_fmac_f32 v255, v120, v120
v_add_f32 v254, v254, v121
v_fmac_f32 v255, v121, v121
v_add_f32 v254, v254, v122
v_fmac_f32 v255, v122, v122
v_add_f32 v254, v254, v123
v_fmac_f32 v255, v123, v123
v_add_f32 v254, v254, v124
v_fmac_f32 v255, v124, v124
v_add_f32 v254, v254, v125
v_fmac_f32 v255, v125, v125
v_add_f32 v254, v254, v126
v_fmac_f32 v255, v126, v126
v_add_f32 v254, v254, v127
v_fmac_f32 v255, v127, v127
v_add_f32 v254, v254, v176
v_fmac_f32 v255, v176, v176
v_add_f32 v254, v254, v177
v_fmac_f32 v255, v177, v177
v_add_f32 v254, v254, v178
v_fmac_f32 v255, v178, v178
v_add_f32 v254, v254, v179
v_fmac_f32 v255, v179, v179
v_add_f32 v254, v254, v180
v_fmac_f32 v255, v180, v180
v_add_f32 v254, v254, v181
v_fmac_f32 v255, v181, v181
v_add_f32 v254, v254, v182
v_fmac_f32 v255, v182, v182
v_add_f32 v254, v254, v183
v_fmac_f32 v255, v183, v183
ds_write_b64 v27, v[254:255] offset:46592
s_waitcnt vmcnt(0) lgkmcnt(0)
s_barrier
s_barrier
s_endpgm
.Lwo_sp8:
s_sleep 1
v_add_lshl_u32 v253, v22, 0, 7
v_bfi_b32 v253, s69, v253, v21
global_load_dword v200, v253, s[38:39] sc1
s_add_u32 s71, s71, 1
s_waitcnt vmcnt(0)
v_sub_u32 v200, v200, v22
v_cmp_lt_u32 vcc, 0x7f, v200
s_and_b64 vcc, vcc, s[76:77]
s_cbranch_vccz .Lwo_rt9
s_cmp_lt_u32 s71, 3000000
s_cbranch_scc1 .Lwo_sp8
s_branch .Lwo_rt9
.Lwo_sp10:
s_sleep 1
v_add_lshl_u32 v253, v22, 0, 7
v_bfi_b32 v253, s69, v253, v21
global_load_dword v201, v253, s[38:39] sc1
s_add_u32 s71, s71, 1
s_waitcnt vmcnt(0)
v_sub_u32 v201, v201, v22
v_cmp_lt_u32 vcc, 0x7f, v201
s_and_b64 vcc, vcc, s[76:77]
s_cbranch_vccz .Lwo_rt11
s_cmp_lt_u32 s71, 3000000
s_cbranch_scc1 .Lwo_sp10
s_branch .Lwo_rt11
.Le1:
)"
    :: "{v[0:31]}"(va), "{s[36:51]}"(sa) : "v255", "a255", "s99", "vcc", "memory");
  } else if (jj < (ev ? 16 : 12)) {
    const unsigned dn_i = ev ? 48 + xh * 6 + jj - 10 : xh * 12 + jj, pf_g = (xh & 1) * 18 + (ev ? 10 : 0), pf_n = ev ? 24 : 48, pf_w = (ev ? jj - 10 : jj) * 4 + wave, pf_m = ev ? 8 : 10;
    const unsigned r = lane & 15, g = lane >> 4, q = r >> 2, p = lane & 3;
    const unsigned tri = dn_i / 3, nu = dn_i % 3, kap = tri >> 2, rho = tri & 3, row0 = rho * RP + PAD;
    auto pf = [&](unsigned k) { const unsigned u = k * pf_n + pf_w, b = u / pf_m, pg = pf_g + u - b * pf_m; return b < 16 ? (64 + b * 32 + (lane >> 1)) * 9216 + pg / 6 * 1536 + (pg % 6) * 128 + (lane & 1) * 768 : 0u; };
    auto wv = [&](unsigned j) { const unsigned b = j * 1024 + lane * 16, rw = b / 384; return rw * (1152 - 384) + b + 4096 - j * 1024; };
    u32x32 va; u32x16 sa;
    va[0] = wv(0);
    va[1] = wv(1);
    va[2] = wv(2);
    va[3] = wv(3);
    va[4] = wv(4);
    va[5] = wv(5);
    va[6] = (8 * g + q) * 384 + wave * 64 + p * 16 + (kap & 1) * 8;
    va[7] = (8 * g + q) * 384 + wave * 64 + p * 16 + 8 - (kap & 1) * 8;
    va[8] = (8 * g + q) * 384 + 256 + wave * 32 + (p ^ (kap & 1)) * 8;
    va[9] = WS_H + row0 * HT + (kap * 24 + wave * 6 + 2) * 1024 + lane * 16;
    va[10] = WS_H + (row0 + 63) * HT + (kap * 24 + wave * 6 + 2) * 1024 + lane * 16;
    va[11] = WS_P + (row0 - 1) * PT + (((nu * 3 + (wave >> 1)) * 4 + q) * 6 + kap) * 512 + (wave & 1) * 256 + g * 64 + p * 16;
    va[12] = WS_P + (row0 - 1) * PT + (((nu * 3 + 2) * 4 + q) * 6 + kap) * 512 + wave * 128 + (g >> 1) * 64 + p * 16;
    va[13] = WS_FL + rho * FR + FH + (kap * 6 + min(lane, 5u)) * FL;
    va[14] = 0u;
    va[15] = WS_FL + rho * FR + FP + (nu * 6 + kap) * FL;
    va[16] = 0u - 2;
    va[17] = lane * 16;
    va[18] = pf(0);
    va[19] = pf(1);
    va[20] = pf(2);
    va[21] = pf(3);
    va[22] = pf(4);
    va[23] = pf(5);
    sa[0] = 8 + wave; sa[2] = (unsigned)(unsigned long long)ws; sa[3] = (unsigned)((unsigned long long)ws >> 32);
    sa[4] = uni((unsigned)(unsigned long long)(wu));
    sa[5] = uni((unsigned)((unsigned long long)(wu) >> 32));
    sa[6] = uni((unsigned)(unsigned long long)(wd + (kap * 768 + wave * 16) * 1152 + nu * 384));
    sa[7] = uni((unsigned)((unsigned long long)(wd + (kap * 768 + wave * 16) * 1152 + nu * 384) >> 32));
    sa[8] = uni(wave * 6144);
    sa[9] = uni(wave * 6144 + 2048);
    asm volatile(
R"(.Lb2:
s_movk_i32 s69, 0x380
s_mov_b32 s71, 0
s_mov_b32 s74, 0
s_load_dword s37, s[38:39], 0x0
s_nop 1
s_sleep 70
s_mov_b32 m0, 0x27f00
s_nop 0
global_load_lds_dword v18, s[40:41]
global_load_lds_dword v19, s[40:41]
global_load_lds_dword v20, s[40:41]
global_load_lds_dword v21, s[40:41]
global_load_lds_dword v22, s[40:41]
global_load_lds_dword v23, s[40:41]
s_waitcnt vmcnt(0)
s_add_u32 m0, s44, 4096
s_nop 0
global_load_lds_dwordx4 v0, s[42:43] offset:-4096 nt
global_load_lds_dwordx4 v1, s[42:43] offset:-3072 nt
global_load_lds_dwordx4 v2, s[42:43] offset:-2048 nt
global_load_lds_dwordx4 v3, s[42:43] offset:-1024 nt
global_load_lds_dwordx4 v4, s[42:43] offset:0 nt
global_load_lds_dwordx4 v5, s[42:43] offset:1024 nt
s_add_u32 s42, s42, 73728
s_addc_u32 s43, s43, 0
s_add_u32 m0, s44, 28672
s_nop 0
global_load_lds_dwordx4 v0, s[42:43] offset:-4096 nt
global_load_lds_dwordx4 v1, s[42:43] offset:-3072 nt
global_load_lds_dwordx4 v2, s[42:43] offset:-2048 nt
global_load_lds_dwordx4 v3, s[42:43] offset:-1024 nt
global_load_lds_dwordx4 v4, s[42:43] offset:0 nt
global_load_lds_dwordx4 v5, s[42:43] offset:1024 nt
s_add_u32 s42, s42, 73728
s_addc_u32 s43, s43, 0
s_add_u32 m0, s44, 53248
s_nop 0
global_load_lds_dwordx4 v0, s[42:43] offset:-4096 nt
global_load_lds_dwordx4 v1, s[42:43] offset:-3072 nt
global_load_lds_dwordx4 v2, s[42:43] offset:-2048 nt
global_load_lds_dwordx4 v3, s[42:43] offset:-1024 nt
global_load_lds_dwordx4 v4, s[42:43] offset:0 nt
global_load_lds_dwordx4 v5, s[42:43] offset:1024 nt
s_add_u32 s42, s42, 73728
s_addc_u32 s43, s43, 0
s_add_u32 m0, s44, 77824
s_nop 0
global_load_lds_dwordx4 v0, s[42:43] offset:-4096 nt
global_load_lds_dwordx4 v1, s[42:43] offset:-3072 nt
global_load_lds_dwordx4 v2, s[42:43] offset:-2048 nt
global_load_lds_dwordx4 v3, s[42:43] offset:-1024 nt
global_load_lds_dwordx4 v4, s[42:43] offset:0 nt
global_load_lds_dwordx4 v5, s[42:43] offset:1024 nt
s_add_u32 s42, s42, 73728
s_addc_u32 s43, s43, 0
s_add_u32 m0, s44, 102400
s_nop 0
global_load_lds_dwordx4 v0, s[42:43] offset:-4096 nt
global_load_lds_dwordx4 v1, s[42:43] offset:-3072 nt
global_load_lds_dwordx4 v2, s[42:43] offset:-2048 nt
global_load_lds_dwordx4 v3, s[42:43] offset:-1024 nt
global_load_lds_dwordx4 v4, s[42:43] offset:0 nt
global_load_lds_dwordx4 v5, s[42:43] offset:1024 nt
s_add_u32 s42, s42, 73728
s_addc_u32 s43, s43, 0
v_add_u32 v136, 0x10000, v6
v_add_u32 v137, 0x20000, v6
v_add_u32 v138, 0x10000, v7
v_add_u32 v139, 0x20000, v7
v_add_u32 v140, 0x10000, v8
v_add_u32 v141, 0x20000, v8
s_waitcnt lgkmcnt(0)
s_mul_i32 s37, s37, 0x9E3779B1
s_add_u32 s37, s37, 0x7F4A7C15
v_add_u32 v14, s37, v14
v_add_u32 v16, s37, v16
s_waitcnt vmcnt(24)
s_barrier
s_add_u32 m0, s44, 126976
ds_read_b64_tr_b16 a[0:1], v6 offset:0
ds_read_b64_tr_b16 a[2:3], v6 offset:1536
global_load_lds_dwordx4 v0, s[42:43] offset:-4096 nt
ds_read_b64_tr_b16 a[4:5], v7 offset:0
global_load_lds_dwordx4 v1, s[42:43] offset:-3072 nt
ds_read_b64_tr_b16 a[6:7], v7 offset:1536
global_load_lds_dwordx4 v2, s[42:43] offset:-2048 nt
ds_read_b64_tr_b16 a[8:9], v8 offset:0
ds_read_b64_tr_b16 a[10:11], v8 offset:1536
global_load_lds_dwordx4 v3, s[42:43] offset:-1024 nt
ds_read_b64_tr_b16 a[12:13], v6 offset:12288
global_load_lds_dwordx4 v4, s[42:43] offset:0 nt
ds_read_b64_tr_b16 a[14:15], v6 offset:13824
global_load_lds_dwordx4 v5, s[42:43] offset:1024 nt
ds_read_b64_tr_b16 a[16:17], v7 offset:12288
ds_read_b64_tr_b16 a[18:19], v7 offset:13824
s_add_u32 s42, s42, 73728
ds_read_b64_tr_b16 a[20:21], v8 offset:12288
s_addc_u32 s43, s43, 0
ds_read_b64_tr_b16 a[22:23], v8 offset:13824
s_waitcnt vmcnt(24) lgkmcnt(0)
s_barrier
s_add_u32 m0, s44, 4096
ds_read_b64_tr_b16 a[24:25], v6 offset:24576
ds_read_b64_tr_b16 a[26:27], v6 offset:26112
global_load_lds_dwordx4 v0, s[42:43] offset:-4096 nt
ds_read_b64_tr_b16 a[28:29], v7 offset:24576
global_load_lds_dwordx4 v1, s[42:43] offset:-3072 nt
ds_read_b64_tr_b16 a[30:31], v7 offset:26112
global_load_lds_dwordx4 v2, s[42:43] offset:-2048 nt
ds_read_b64_tr_b16 a[32:33], v8 offset:24576
ds_read_b64_tr_b16 a[34:35], v8 offset:26112
global_load_lds_dwordx4 v3, s[42:43] offset:-1024 nt
ds_read_b64_tr_b16 a[36:37], v6 offset:36864
global_load_lds_dwordx4 v4, s[42:43] offset:0 nt
ds_read_b64_tr_b16 a[38:39], v6 offset:38400
global_load_lds_dwordx4 v5, s[42:43] offset:1024 nt
ds_read_b64_tr_b16 a[40:41], v7 offset:36864
ds_read_b64_tr_b16 a[42:43], v7 offset:38400
s_add_u32 s42, s42, 73728
ds_read_b64_tr_b16 a[44:45], v8 offset:36864
s_addc_u32 s43, s43, 0
ds_read_b64_tr_b16 a[46:47], v8 offset:38400
s_waitcnt vmcnt(24) lgkmcnt(0)
s_barrier
s_add_u32 m0, s44, 28672
ds_read_b64_tr_b16 a[48:49], v6 offset:49152
ds_read_b64_tr_b16 a[50:51], v6 offset:50688
global_load_lds_dwordx4 v0, s[42:43] offset:-4096 nt
ds_read_b64_tr_b16 a[52:53], v7 offset:49152
global_load_lds_dwordx4 v1, s[42:43] offset:-3072 nt
ds_read_b64_tr_b16 a[54:55], v7 offset:50688
global_load_lds_dwordx4 v2, s[42:43] offset:-2048 nt
ds_read_b64_tr_b16 a[56:57], v8 offset:49152
ds_read_b64_tr_b16 a[58:59], v8 offset:50688
global_load_lds_dwordx4 v3, s[42:43] offset:-1024 nt
ds_read_b64_tr_b16 a[60:61], v6 offset:61440
global_load_lds_dwordx4 v4, s[42:43] offset:0 nt
ds_read_b64_tr_b16 a[62:63], v6 offset:62976
global_load_lds_dwordx4 v5, s[42:43] offset:1024 nt
ds_read_b64_tr_b16 a[64:65], v7 offset:61440
ds_read_b64_tr_b16 a[66:67], v7 offset:62976
s_add_u32 s42, s42, 73728
ds_read_b64_tr_b16 a[68:69], v8 offset:61440
s_addc_u32 s43, s43, 0
ds_read_b64_tr_b16 a[70:71], v8 offset:62976
s_waitcnt vmcnt(24) lgkmcnt(0)
s_barrier
s_add_u32 m0, s44, 53248
ds_read_b64_tr_b16 a[72:73], v136 offset:8192
ds_read_b64_tr_b16 a[74:75], v136 offset:9728
global_load_lds_dwordx4 v0, s[42:43] offset:-4096 nt
ds_read_b64_tr_b16 a[76:77], v138 offset:8192
global_load_lds_dwordx4 v1, s[42:43] offset:-3072 nt
ds_read_b64_tr_b16 a[78:79], v138 offset:9728
global_load_lds_dwordx4 v2, s[42:43] offset:-2048 nt
ds_read_b64_tr_b16 a[80:81], v140 offset:8192
ds_read_b64_tr_b16 a[82:83], v140 offset:9728
global_load_lds_dwordx4 v3, s[42:43] offset:-1024 nt
ds_read_b64_tr_b16 a[84:85], v136 offset:20480
global_load_lds_dwordx4 v4, s[42:43] offset:0 nt
ds_read_b64_tr_b16 a[86:87], v136 offset:22016
global_load_lds_dwordx4 v5, s[42:43] offset:1024 nt
ds_read_b64_tr_b16 a[88:89], v138 offset:20480
ds_read_b64_tr_b16 a[90:91], v138 offset:22016
s_add_u32 s42, s42, 73728
ds_read_b64_tr_b16 a[92:93], v140 offset:20480
s_addc_u32 s43, s43, 0
ds_read_b64_tr_b16 a[94:95], v140 offset:22016
s_waitcnt vmcnt(24) lgkmcnt(0)
s_barrier
s_add_u32 m0, s44, 77824
ds_read_b64_tr_b16 a[96:97], v136 offset:32768
ds_read_b64_tr_b16 a[98:99], v136 offset:34304
global_load_lds_dwordx4 v0, s[42:43] offset:-4096 nt
ds_read_b64_tr_b16 a[100:101], v138 offset:32768
global_load_lds_dwordx4 v1, s[42:43] offset:-3072 nt
ds_read_b64_tr_b16 a[102:103], v138 offset:34304
global_load_lds_dwordx4 v2, s[42:43] offset:-2048 nt
ds_read_b64_tr_b16 a[104:105], v140 offset:32768
ds_read_b64_tr_b16 a[106:107], v140 offset:34304
global_load_lds_dwordx4 v3, s[42:43] offset:-1024 nt
ds_read_b64_tr_b16 a[108:109], v136 offset:45056
global_load_lds_dwordx4 v4, s[42:43] offset:0 nt
ds_read_b64_tr_b16 a[110:111], v136 offset:46592
global_load_lds_dwordx4 v5, s[42:43] offset:1024 nt
ds_read_b64_tr_b16 a[112:113], v138 offset:45056
ds_read_b64_tr_b16 a[114:115], v138 offset:46592
s_add_u32 s42, s42, 73728
ds_read_b64_tr_b16 a[116:117], v140 offset:45056
s_addc_u32 s43, s43, 0
ds_read_b64_tr_b16 a[118:119], v140 offset:46592
s_waitcnt vmcnt(24) lgkmcnt(0)
s_barrier
s_add_u32 m0, s44, 102400
ds_read_b64_tr_b16 a[120:121], v136 offset:57344
ds_read_b64_tr_b16 a[122:123], v136 offset:58880
global_load_lds_dwordx4 v0, s[42:43] offset:-4096 nt
ds_read_b64_tr_b16 a[124:125], v138 offset:57344
global_load_lds_dwordx4 v1, s[42:43] offset:-3072 nt
ds_read_b64_tr_b16 a[126:127], v138 offset:58880
global_load_lds_dwordx4 v2, s[42:43] offset:-2048 nt
ds_read_b64_tr_b16 a[128:129], v140 offset:57344
ds_read_b64_tr_b16 a[130:131], v140 offset:58880
global_load_lds_dwordx4 v3, s[42:43] offset:-1024 nt
ds_read_b64_tr_b16 a[132:133], v137 offset:4096
global_load_lds_dwordx4 v4, s[42:43] offset:0 nt
ds_read_b64_tr_b16 a[134:135], v137 offset:5632
global_load_lds_dwordx4 v5, s[42:43] offset:1024 nt
ds_read_b64_tr_b16 a[136:137], v139 offset:4096
ds_read_b64_tr_b16 a[138:139], v139 offset:5632
s_add_u32 s42, s42, 73728
ds_read_b64_tr_b16 a[140:141], v141 offset:4096
s_addc_u32 s43, s43, 0
ds_read_b64_tr_b16 a[142:143], v141 offset:5632
s_waitcnt vmcnt(24) lgkmcnt(0)
s_barrier
s_add_u32 m0, s44, 126976
ds_read_b64_tr_b16 a[144:145], v6 offset:0
ds_read_b64_tr_b16 a[146:147], v6 offset:1536
global_load_lds_dwordx4 v0, s[42:43] offset:-4096 nt
ds_read_b64_tr_b16 a[148:149], v7 offset:0
global_load_lds_dwordx4 v1, s[42:43] offset:-3072 nt
ds_read_b64_tr_b16 a[150:151], v7 offset:1536
global_load_lds_dwordx4 v2, s[42:43] offset:-2048 nt
ds_read_b64_tr_b16 a[152:153], v8 offset:0
ds_read_b64_tr_b16 a[154:155], v8 offset:1536
global_load_lds_dwordx4 v3, s[42:43] offset:-1024 nt
ds_read_b64_tr_b16 a[156:157], v6 offset:12288
global_load_lds_dwordx4 v4, s[42:43] offset:0 nt
ds_read_b64_tr_b16 a[158:159], v6 offset:13824
global_load_lds_dwordx4 v5, s[42:43] offset:1024 nt
ds_read_b64_tr_b16 a[160:161], v7 offset:12288
ds_read_b64_tr_b16 a[162:163], v7 offset:13824
s_sub_u32 s42, s42, 811008
ds_read_b64_tr_b16 a[164:165], v8 offset:12288
s_subb_u32 s43, s43, 0
ds_read_b64_tr_b16 a[166:167], v8 offset:13824
s_waitcnt vmcnt(24) lgkmcnt(0)
s_barrier
ds_read_b64_tr_b16 a[168:169], v6 offset:24576
ds_read_b64_tr_b16 a[170:171], v6 offset:26112
ds_read_b64_tr_b16 a[172:173], v7 offset:24576
ds_read_b64_tr_b16 a[174:175], v7 offset:26112
ds_read_b64_tr_b16 a[176:177], v8 offset:24576
ds_read_b64_tr_b16 a[178:179], v8 offset:26112
ds_read_b64_tr_b16 a[180:181], v6 offset:36864
ds_read_b64_tr_b16 a[182:183], v6 offset:38400
ds_read_b64_tr_b16 a[184:185], v7 offset:36864
ds_read_b64_tr_b16 a[186:187], v7 offset:38400
ds_read_b64_tr_b16 a[188:189], v8 offset:36864
ds_read_b64_tr_b16 a[190:191], v8 offset:38400
s_waitcnt vmcnt(18) lgkmcnt(0)
s_barrier
ds_read_b64_tr_b16 a[192:193], v6 offset:49152
ds_read_b64_tr_b16 a[194:195], v6 offset:50688
ds_read_b64_tr_b16 a[196:197], v7 offset:49152
ds_read_b64_tr_b16 a[198:199], v7 offset:50688
ds_read_b64_tr_b16 a[200:201], v8 offset:49152
ds_read_b64_tr_b16 a[202:203], v8 offset:50688
ds_read_b64_tr_b16 a[204:205], v6 offset:61440
ds_read_b64_tr_b16 a[206:207], v6 offset:62976
ds_read_b64_tr_b16 a[208:209], v7 offset:61440
ds_read_b64_tr_b16 a[210:211], v7 offset:62976
ds_read_b64_tr_b16 a[212:213], v8 offset:61440
ds_read_b64_tr_b16 a[214:215], v8 offset:62976
s_waitcnt vmcnt(12) lgkmcnt(0)
s_barrier
ds_read_b64_tr_b16 a[216:217], v136 offset:8192
ds_read_b64_tr_b16 a[218:219], v136 offset:9728
ds_read_b64_tr_b16 a[220:221], v138 offset:8192
ds_read_b64_tr_b16 a[222:223], v138 offset:9728
ds_read_b64_tr_b16 a[224:225], v140 offset:8192
ds_read_b64_tr_b16 a[226:227], v140 offset:9728
ds_read_b64_tr_b16 a[228:229], v136 offset:20480
ds_read_b64_tr_b16 a[230:231], v136 offset:22016
ds_read_b64_tr_b16 a[232:233], v138 offset:20480
ds_read_b64_tr_b16 a[234:235], v138 offset:22016
ds_read_b64_tr_b16 a[236:237], v140 offset:20480
ds_read_b64_tr_b16 a[238:239], v140 offset:22016
s_waitcnt vmcnt(6) lgkmcnt(0)
s_barrier
ds_read_b64_tr_b16 a[240:241], v136 offset:32768
ds_read_b64_tr_b16 a[242:243], v136 offset:34304
ds_read_b64_tr_b16 a[244:245], v138 offset:32768
ds_read_b64_tr_b16 a[246:247], v138 offset:34304
ds_read_b64_tr_b16 a[248:249], v140 offset:32768
ds_read_b64_tr_b16 a[250:251], v140 offset:34304
ds_read_b64_tr_b16 a[252:253], v136 offset:45056
ds_read_b64_tr_b16 a[254:255], v136 offset:46592
ds_read_b64_tr_b16 v[24:25], v138 offset:45056
ds_read_b64_tr_b16 v[26:27], v138 offset:46592
ds_read_b64_tr_b16 v[28:29], v140 offset:45056
ds_read_b64_tr_b16 v[30:31], v140 offset:46592
s_waitcnt vmcnt(0) lgkmcnt(0)
s_barrier
ds_read_b64_tr_b16 v[32:33], v136 offset:57344
ds_read_b64_tr_b16 v[34:35], v136 offset:58880
ds_read_b64_tr_b16 v[36:37], v138 offset:57344
ds_read_b64_tr_b16 v[38:39], v138 offset:58880
ds_read_b64_tr_b16 v[40:41], v140 offset:57344
ds_read_b64_tr_b16 v[42:43], v140 offset:58880
ds_read_b64_tr_b16 v[44:45], v137 offset:4096
ds_read_b64_tr_b16 v[46:47], v137 offset:5632
ds_read_b64_tr_b16 v[48:49], v139 offset:4096
ds_read_b64_tr_b16 v[50:51], v139 offset:5632
ds_read_b64_tr_b16 v[52:53], v141 offset:4096
ds_read_b64_tr_b16 v[54:55], v141 offset:5632
s_waitcnt lgkmcnt(0)
s_barrier
s_and_b32 s75, s36, 3
s_cmp_eq_u32 s75, 1
s_cbranch_scc1 .Ldn_w2
s_cmp_eq_u32 s75, 2
s_cbranch_scc1 .Ldn_w3
s_cmp_eq_u32 s75, 3
s_cbranch_scc1 .Ldn_w4
.Ldn_w1:
v_add_u32 v254, -1, v14
v_add_u32 v255, -1, v14
v_add_lshl_u32 v252, v16, 0, 7
v_bfi_b32 v252, s69, v252, v15
v_add_lshl_u32 v253, v14, 0, 7
v_bfi_b32 v253, s69, v253, v13
s_mov_b32 s76, 0xffff
s_mov_b32 s77, 0xffff
s_add_u32 m0, s45, 24576
s_xor_b32 s45, s45, 0x10000
s_mov_b32 m0, s45
s_barrier
global_load_lds_dwordx4 v9, s[38:39] offset:-2048 sc1
global_load_lds_dwordx4 v9, s[38:39] offset:-1024 sc1
global_load_lds_dwordx4 v9, s[38:39] offset:0 sc1
global_load_lds_dwordx4 v9, s[38:39] offset:1024 sc1
global_load_lds_dwordx4 v9, s[38:39] offset:2048 sc1
global_load_lds_dwordx4 v9, s[38:39] offset:3072 sc1
v_add_u32 v9, 147456, v9
s_mov_b32 s70, 32
s_waitcnt vmcnt(0) lgkmcnt(0)
s_nop 7
s_branch .Ldn_in5
.p2align 6
.Ldn_top6:
s_waitcnt lgkmcnt(5)
v_mfma_f32_16x16x32_bf16 v[56:59], a[0:3], v[80:83], 0
s_waitcnt lgkmcnt(4)
v_mfma_f32_16x16x32_bf16 v[56:59], a[12:15], v[84:87], v[56:59]
s_waitcnt lgkmcnt(3)
v_mfma_f32_16x16x32_bf16 v[56:59], a[24:27], v[88:91], v[56:59]
s_waitcnt lgkmcnt(2)
v_mfma_f32_16x16x32_bf16 v[56:59], a[36:39], v[92:95], v[56:59]
s_waitcnt lgkmcnt(1)
ds_read_b128 v[104:107], v17 offset:6144
v_cvt_pk_bf16_f32 v132, v76, v77
v_mfma_f32_16x16x32_bf16 v[56:59], a[48:51], v[96:99], v[56:59]
s_waitcnt lgkmcnt(1)
v_mfma_f32_16x16x32_bf16 v[56:59], a[60:63], v[100:103], v[56:59]
s_waitcnt vmcnt(6)
s_barrier
v_mfma_f32_16x16x32_bf16 v[60:63], a[4:7], v[80:83], 0
ds_read_b128 v[108:111], v17 offset:7168
v_cvt_pk_bf16_f32 v134, v76, v77
v_mfma_f32_16x16x32_bf16 v[60:63], a[16:19], v[84:87], v[60:63]
global_store_dwordx4 v11, v[128:131], s[38:39] sc1
v_mfma_f32_16x16x32_bf16 v[60:63], a[28:31], v[88:91], v[60:63]
ds_read_b128 v[112:115], v17 offset:8192
v_add_u32 v11, 110592, v11
v_mfma_f32_16x16x32_bf16 v[60:63], a[40:43], v[92:95], v[60:63]
v_cvt_pk_bf16_f32 v133, v78, v79
v_cvt_pk_bf16_f32 v135, v78, v79
v_mfma_f32_16x16x32_bf16 v[60:63], a[52:55], v[96:99], v[60:63]
ds_read_b128 v[116:119], v17 offset:9216
v_mfma_f32_16x16x32_bf16 v[60:63], a[64:67], v[100:103], v[60:63]
v_permlane16_swap_b32 v132, v134
v_mfma_f32_16x16x32_bf16 v[64:67], a[8:11], v[80:83], 0
ds_read_b128 v[120:123], v17 offset:10240
v_mfma_f32_16x16x32_bf16 v[64:67], a[20:23], v[84:87], v[64:67]
v_permlane16_swap_b32 v133, v135
v_mfma_f32_16x16x32_bf16 v[64:67], a[32:35], v[88:91], v[64:67]
ds_read_b128 v[124:127], v17 offset:11264
v_mfma_f32_16x16x32_bf16 v[64:67], a[44:47], v[92:95], v[64:67]
s_mov_b64 exec, s[76:77]
global_store_dwordx4 v12, v[132:135], s[38:39] sc1
s_mov_b64 exec, -1
v_mfma_f32_16x16x32_bf16 v[64:67], a[56:59], v[96:99], v[64:67]
v_add_u32 v12, 110592, v12
v_mfma_f32_16x16x32_bf16 v[64:67], a[68:71], v[100:103], v[64:67]
s_waitcnt lgkmcnt(5)
v_mfma_f32_16x16x32_bf16 v[56:59], a[72:75], v[104:107], v[56:59]
s_waitcnt lgkmcnt(4)
v_mfma_f32_16x16x32_bf16 v[56:59], a[84:87], v[108:111], v[56:59]
s_waitcnt lgkmcnt(3)
v_mfma_f32_16x16x32_bf16 v[56:59], a[96:99], v[112:115], v[56:59]
s_waitcnt lgkmcnt(2)
v_mfma_f32_16x16x32_bf16 v[56:59], a[108:111], v[116:119], v[56:59]
s_waitcnt lgkmcnt(1)
v_mfma_f32_16x16x32_bf16 v[56:59], a[120:123], v[120:123], v[56:59]
s_waitcnt lgkmcnt(0)
ds_read_b128 v[80:83], v17 offset:12288
v_mfma_f32_16x16x32_bf16 v[56:59], a[132:135], v[124:127], v[56:59]
)"
R"(v_mfma_f32_16x16x32_bf16 v[60:63], a[76:79], v[104:107], v[60:63]
ds_read_b128 v[84:87], v17 offset:13312
v_mfma_f32_16x16x32_bf16 v[60:63], a[88:91], v[108:111], v[60:63]
v_mfma_f32_16x16x32_bf16 v[60:63], a[100:103], v[112:115], v[60:63]
ds_read_b128 v[88:91], v17 offset:14336
v_mfma_f32_16x16x32_bf16 v[60:63], a[112:115], v[116:119], v[60:63]
v_mfma_f32_16x16x32_bf16 v[60:63], a[124:127], v[120:123], v[60:63]
ds_read_b128 v[92:95], v17 offset:15360
v_mfma_f32_16x16x32_bf16 v[60:63], a[136:139], v[124:127], v[60:63]
v_mfma_f32_16x16x32_bf16 v[64:67], a[80:83], v[104:107], v[64:67]
ds_read_b128 v[96:99], v17 offset:16384
v_mfma_f32_16x16x32_bf16 v[64:67], a[92:95], v[108:111], v[64:67]
v_mfma_f32_16x16x32_bf16 v[64:67], a[104:107], v[112:115], v[64:67]
ds_read_b128 v[100:103], v17 offset:17408
v_mfma_f32_16x16x32_bf16 v[64:67], a[116:119], v[116:119], v[64:67]
v_mfma_f32_16x16x32_bf16 v[64:67], a[128:131], v[120:123], v[64:67]
v_mfma_f32_16x16x32_bf16 v[64:67], a[140:143], v[124:127], v[64:67]
s_waitcnt lgkmcnt(5)
v_mfma_f32_16x16x32_bf16 v[56:59], a[144:147], v[80:83], v[56:59]
s_waitcnt lgkmcnt(4)
v_mfma_f32_16x16x32_bf16 v[56:59], a[156:159], v[84:87], v[56:59]
s_waitcnt lgkmcnt(3)
v_mfma_f32_16x16x32_bf16 v[56:59], a[168:171], v[88:91], v[56:59]
s_waitcnt lgkmcnt(2)
v_mfma_f32_16x16x32_bf16 v[56:59], a[180:183], v[92:95], v[56:59]
s_waitcnt lgkmcnt(1)
v_mfma_f32_16x16x32_bf16 v[56:59], a[192:195], v[96:99], v[56:59]
s_xor_b32 s45, s45, 0x10000
ds_read_b128 v[104:107], v17 offset:18432
s_waitcnt lgkmcnt(1)
v_mfma_f32_16x16x32_bf16 v[56:59], a[204:207], v[100:103], v[56:59]
s_add_u32 m0, s45, 0
v_mfma_f32_16x16x32_bf16 v[60:63], a[148:151], v[80:83], v[60:63]
ds_read_b128 v[108:111], v17 offset:19456
ds_read_b128 v[112:115], v17 offset:20480
v_mfma_f32_16x16x32_bf16 v[60:63], a[160:163], v[84:87], v[60:63]
v_mfma_f32_16x16x32_bf16 v[60:63], a[172:175], v[88:91], v[60:63]
s_barrier
v_mfma_f32_16x16x32_bf16 v[60:63], a[184:187], v[92:95], v[60:63]
global_load_lds_dwordx4 v9, s[38:39] offset:-2048 sc1
v_mfma_f32_16x16x32_bf16 v[60:63], a[196:199], v[96:99], v[60:63]
ds_read_b128 v[116:119], v17 offset:21504
v_mfma_f32_16x16x32_bf16 v[60:63], a[208:211], v[100:103], v[60:63]
v_mfma_f32_16x16x32_bf16 v[64:67], a[152:155], v[80:83], v[64:67]
ds_read_b128 v[120:123], v17 offset:22528
global_load_lds_dwordx4 v9, s[38:39] offset:-1024 sc1
v_mfma_f32_16x16x32_bf16 v[64:67], a[164:167], v[84:87], v[64:67]
v_mfma_f32_16x16x32_bf16 v[64:67], a[176:179], v[88:91], v[64:67]
ds_read_b128 v[124:127], v17 offset:23552
v_mfma_f32_16x16x32_bf16 v[64:67], a[188:191], v[92:95], v[64:67]
global_load_lds_dwordx4 v9, s[38:39] offset:0 sc1
v_mfma_f32_16x16x32_bf16 v[64:67], a[200:203], v[96:99], v[64:67]
v_mfma_f32_16x16x32_bf16 v[64:67], a[212:215], v[100:103], v[64:67]
s_waitcnt lgkmcnt(5)
v_mfma_f32_16x16x32_bf16 v[56:59], a[216:219], v[104:107], v[56:59]
s_waitcnt lgkmcnt(4)
global_load_lds_dwordx4 v9, s[38:39] offset:1024 sc1
v_mfma_f32_16x16x32_bf16 v[56:59], a[228:231], v[108:111], v[56:59]
s_waitcnt lgkmcnt(3)
v_mfma_f32_16x16x32_bf16 v[56:59], a[240:243], v[112:115], v[56:59]
s_waitcnt lgkmcnt(2)
v_mfma_f32_16x16x32_bf16 v[56:59], a[252:255], v[116:119], v[56:59]
s_waitcnt lgkmcnt(1)
global_load_lds_dwordx4 v9, s[38:39] offset:2048 sc1
v_mfma_f32_16x16x32_bf16 v[56:59], v[32:35], v[120:123], v[56:59]
s_waitcnt lgkmcnt(0)
v_mfma_f32_16x16x32_bf16 v[56:59], v[44:47], v[124:127], v[56:59]
v_mfma_f32_16x16x32_bf16 v[60:63], a[220:223], v[104:107], v[60:63]
global_load_lds_dwordx4 v9, s[38:39] offset:3072 sc1
v_add_u32 v9, 147456, v9
v_mfma_f32_16x16x32_bf16 v[60:63], a[232:235], v[108:111], v[60:63]
v_mfma_f32_16x16x32_bf16 v[60:63], a[244:247], v[112:115], v[60:63]
s_waitcnt vmcnt(8)
s_barrier
v_mfma_f32_16x16x32_bf16 v[60:63], v[24:27], v[116:119], v[60:63]
ds_read_b128 v[80:83], v17 offset:24576
v_min_u32 v9, v9, v10
v_mfma_f32_16x16x32_bf16 v[60:63], v[36:39], v[120:123], v[60:63]
ds_read_b128 v[84:87], v17 offset:25600
v_cvt_pk_bf16_f32 v128, v56, v57
v_mfma_f32_16x16x32_bf16 v[60:63], v[48:51], v[124:127], v[60:63]
ds_read_b128 v[88:91], v17 offset:26624
v_cvt_pk_bf16_f32 v129, v58, v59
v_mfma_f32_16x16x32_bf16 v[64:67], a[224:227], v[104:107], v[64:67]
ds_read_b128 v[92:95], v17 offset:27648
v_mfma_f32_16x16x32_bf16 v[64:67], a[236:239], v[108:111], v[64:67]
ds_read_b128 v[96:99], v17 offset:28672
v_mfma_f32_16x16x32_bf16 v[64:67], a[248:251], v[112:115], v[64:67]
ds_read_b128 v[100:103], v17 offset:29696
v_mfma_f32_16x16x32_bf16 v[64:67], v[28:31], v[116:119], v[64:67]
v_cvt_pk_bf16_f32 v130, v60, v61
v_cvt_pk_bf16_f32 v131, v62, v63
v_mfma_f32_16x16x32_bf16 v[64:67], v[40:43], v[120:123], v[64:67]
v_min_u32 v128, -2, v128
v_min_u32 v129, -2, v129
v_mfma_f32_16x16x32_bf16 v[64:67], v[52:55], v[124:127], v[64:67]
s_waitcnt lgkmcnt(5)
v_min_u32 v130, -2, v130
v_min_u32 v131, -2, v131
v_mfma_f32_16x16x32_bf16 v[68:71], a[0:3], v[80:83], 0
s_waitcnt lgkmcnt(4)
v_mfma_f32_16x16x32_bf16 v[68:71], a[12:15], v[84:87], v[68:71]
s_waitcnt lgkmcnt(3)
v_mfma_f32_16x16x32_bf16 v[68:71], a[24:27], v[88:91], v[68:71]
s_waitcnt lgkmcnt(2)
v_mfma_f32_16x16x32_bf16 v[68:71], a[36:39], v[92:95], v[68:71]
s_waitcnt lgkmcnt(1)
ds_read_b128 v[104:107], v17 offset:30720
v_cvt_pk_bf16_f32 v132, v64, v65
v_mfma_f32_16x16x32_bf16 v[68:71], a[48:51], v[96:99], v[68:71]
s_waitcnt lgkmcnt(1)
v_mfma_f32_16x16x32_bf16 v[68:71], a[60:63], v[100:103], v[68:71]
s_waitcnt vmcnt(6)
s_barrier
v_mfma_f32_16x16x32_bf16 v[72:75], a[4:7], v[80:83], 0
ds_read_b128 v[108:111], v17 offset:31744
v_cvt_pk_bf16_f32 v134, v64, v65
v_mfma_f32_16x16x32_bf16 v[72:75], a[16:19], v[84:87], v[72:75]
global_store_dwordx4 v11, v[128:131], s[38:39] sc1
v_mfma_f32_16x16x32_bf16 v[72:75], a[28:31], v[88:91], v[72:75]
ds_read_b128 v[112:115], v17 offset:32768
v_add_u32 v11, 110592, v11
v_mfma_f32_16x16x32_bf16 v[72:75], a[40:43], v[92:95], v[72:75]
v_cvt_pk_bf16_f32 v133, v66, v67
v_cvt_pk_bf16_f32 v135, v66, v67
v_mfma_f32_16x16x32_bf16 v[72:75], a[52:55], v[96:99], v[72:75]
ds_read_b128 v[116:119], v17 offset:33792
v_mfma_f32_16x16x32_bf16 v[72:75], a[64:67], v[100:103], v[72:75]
v_permlane16_swap_b32 v132, v134
v_mfma_f32_16x16x32_bf16 v[76:79], a[8:11], v[80:83], 0
ds_read_b128 v[120:123], v17 offset:34816
v_mfma_f32_16x16x32_bf16 v[76:79], a[20:23], v[84:87], v[76:79]
v_permlane16_swap_b32 v133, v135
v_mfma_f32_16x16x32_bf16 v[76:79], a[32:35], v[88:91], v[76:79]
ds_read_b128 v[124:127], v17 offset:35840
v_min_u32 v132, -2, v132
v_mfma_f32_16x16x32_bf16 v[76:79], a[44:47], v[92:95], v[76:79]
v_min_u32 v133, -2, v133
v_min_u32 v134, -2, v134
v_mfma_f32_16x16x32_bf16 v[76:79], a[56:59], v[96:99], v[76:79]
v_min_u32 v135, -2, v135
v_mfma_f32_16x16x32_bf16 v[76:79], a[68:71], v[100:103], v[76:79]
s_waitcnt lgkmcnt(5)
s_mov_b64 exec, s[76:77]
global_store_dwordx4 v12, v[132:135], s[38:39] sc1
s_mov_b64 exec, -1
v_mfma_f32_16x16x32_bf16 v[68:71], a[72:75], v[104:107], v[68:71]
s_waitcnt lgkmcnt(4)
v_add_u32 v12, 110592, v12
v_mfma_f32_16x16x32_bf16 v[68:71], a[84:87], v[108:111], v[68:71]
s_waitcnt lgkmcnt(3)
v_mfma_f32_16x16x32_bf16 v[68:71], a[96:99], v[112:115], v[68:71]
s_waitcnt lgkmcnt(2)
v_mfma_f32_16x16x32_bf16 v[68:71], a[108:111], v[116:119], v[68:71]
s_waitcnt lgkmcnt(1)
v_mfma_f32_16x16x32_bf16 v[68:71], a[120:123], v[120:123], v[68:71]
s_waitcnt lgkmcnt(0)
ds_read_b128 v[80:83], v17 offset:36864
v_mfma_f32_16x16x32_bf16 v[68:71], a[132:135], v[124:127], v[68:71]
v_mfma_f32_16x16x32_bf16 v[72:75], a[76:79], v[104:107], v[72:75]
ds_read_b128 v[84:87], v17 offset:37888
v_mfma_f32_16x16x32_bf16 v[72:75], a[88:91], v[108:111], v[72:75]
v_mfma_f32_16x16x32_bf16 v[72:75], a[100:103], v[112:115], v[72:75]
ds_read_b128 v[88:91], v17 offset:38912
v_mfma_f32_16x16x32_bf16 v[72:75], a[112:115], v[116:119], v[72:75]
v_mfma_f32_16x16x32_bf16 v[72:75], a[124:127], v[120:123], v[72:75]
ds_read_b128 v[92:95], v17 offset:39936
v_mfma_f32_16x16x32_bf16 v[72:75], a[136:139], v[124:127], v[72:75]
v_mfma_f32_16x16x32_bf16 v[76:79], a[80:83], v[104:107], v[76:79]
ds_read_b128 v[96:99], v17 offset:40960
v_mfma_f32_16x16x32_bf16 v[76:79], a[92:95], v[108:111], v[76:79]
v_mfma_f32_16x16x32_bf16 v[76:79], a[104:107], v[112:115], v[76:79]
ds_read_b128 v[100:103], v17 offset:41984
v_mfma_f32_16x16x32_bf16 v[76:79], a[116:119], v[116:119], v[76:79]
v_mfma_f32_16x16x32_bf16 v[76:79], a[128:131], v[120:123], v[76:79]
v_mfma_f32_16x16x32_bf16 v[76:79], a[140:143], v[124:127], v[76:79]
s_waitcnt lgkmcnt(5)
v_mfma_f32_16x16x32_bf16 v[68:71], a[144:147], v[80:83], v[68:71]
s_waitcnt lgkmcnt(4)
v_mfma_f32_16x16x32_bf16 v[68:71], a[156:159], v[84:87], v[68:71]
s_waitcnt lgkmcnt(3)
v_mfma_f32_16x16x32_bf16 v[68:71], a[168:171], v[88:91], v[68:71]
.Ldn_in5:
s_waitcnt lgkmcnt(2)
v_mfma_f32_16x16x32_bf16 v[68:71], a[180:183], v[92:95], v[68:71]
s_waitcnt lgkmcnt(1)
v_mfma_f32_16x16x32_bf16 v[68:71], a[192:195], v[96:99], v[68:71]
s_waitcnt lgkmcnt(0)
ds_read_b128 v[104:107], v17 offset:43008
v_mfma_f32_16x16x32_bf16 v[68:71], a[204:207], v[100:103], v[68:71]
s_add_u32 m0, s45, 24576
v_mfma_f32_16x16x32_bf16 v[72:75], a[148:151], v[80:83], v[72:75]
ds_read_b128 v[108:111], v17 offset:44032
ds_read_b128 v[112:115], v17 offset:45056
v_mfma_f32_16x16x32_bf16 v[72:75], a[160:163], v[84:87], v[72:75]
v_mfma_f32_16x16x32_bf16 v[72:75], a[172:175], v[88:91], v[72:75]
s_barrier
v_mfma_f32_16x16x32_bf16 v[72:75], a[184:187], v[92:95], v[72:75]
global_load_lds_dwordx4 v9, s[38:39] offset:-2048 sc1
v_mfma_f32_16x16x32_bf16 v[72:75], a[196:199], v[96:99], v[72:75]
ds_read_b128 v[116:119], v17 offset:46080
v_mfma_f32_16x16x32_bf16 v[72:75], a[208:211], v[100:103], v[72:75]
v_mfma_f32_16x16x32_bf16 v[76:79], a[152:155], v[80:83], v[76:79]
ds_read_b128 v[120:123], v17 offset:47104
global_load_lds_dwordx4 v9, s[38:39] offset:-1024 sc1
v_mfma_f32_16x16x32_bf16 v[76:79], a[164:167], v[84:87], v[76:79]
v_mfma_f32_16x16x32_bf16 v[76:79], a[176:179], v[88:91], v[76:79]
ds_read_b128 v[124:127], v17 offset:48128
v_mfma_f32_16x16x32_bf16 v[76:79], a[188:191], v[92:95], v[76:79]
global_load_lds_dwordx4 v9, s[38:39] offset:0 sc1
v_mfma_f32_16x16x32_bf16 v[76:79], a[200:203], v[96:99], v[76:79]
v_mfma_f32_16x16x32_bf16 v[76:79], a[212:215], v[100:103], v[76:79]
s_waitcnt lgkmcnt(5)
v_mfma_f32_16x16x32_bf16 v[68:71], a[216:219], v[104:107], v[68:71]
s_waitcnt lgkmcnt(4)
global_load_lds_dwordx4 v9, s[38:39] offset:1024 sc1
v_mfma_f32_16x16x32_bf16 v[68:71], a[228:231], v[108:111], v[68:71]
s_waitcnt lgkmcnt(3)
v_mfma_f32_16x16x32_bf16 v[68:71], a[240:243], v[112:115], v[68:71]
s_waitcnt lgkmcnt(2)
v_mfma_f32_16x16x32_bf16 v[68:71], a[252:255], v[116:119], v[68:71]
s_waitcnt lgkmcnt(1)
global_load_lds_dwordx4 v9, s[38:39] offset:2048 sc1
v_mfma_f32_16x16x32_bf16 v[68:71], v[32:35], v[120:123], v[68:71]
s_waitcnt lgkmcnt(0)
v_mfma_f32_16x16x32_bf16 v[68:71], v[44:47], v[124:127], v[68:71]
v_xor_b32 v17, 0x10000, v17
v_mfma_f32_16x16x32_bf16 v[72:75], a[220:223], v[104:107], v[72:75]
global_load_lds_dwordx4 v9, s[38:39] offset:3072 sc1
v_add_u32 v9, 147456, v9
v_mfma_f32_16x16x32_bf16 v[72:75], a[232:235], v[108:111], v[72:75]
v_mfma_f32_16x16x32_bf16 v[72:75], a[244:247], v[112:115], v[72:75]
s_waitcnt vmcnt(8)
s_barrier
v_mfma_f32_16x16x32_bf16 v[72:75], v[24:27], v[116:119], v[72:75]
ds_read_b128 v[80:83], v17 offset:0
v_min_u32 v9, v9, v10
v_mfma_f32_16x16x32_bf16 v[72:75], v[36:39], v[120:123], v[72:75]
ds_read_b128 v[84:87], v17 offset:1024
v_cvt_pk_bf16_f32 v128, v68, v69
v_mfma_f32_16x16x32_bf16 v[72:75], v[48:51], v[124:127], v[72:75]
ds_read_b128 v[88:91], v17 offset:2048
v_cvt_pk_bf16_f32 v129, v70, v71
v_mfma_f32_16x16x32_bf16 v[76:79], a[224:227], v[104:107], v[76:79]
ds_read_b128 v[92:95], v17 offset:3072
v_mfma_f32_16x16x32_bf16 v[76:79], a[236:239], v[108:111], v[76:79]
ds_read_b128 v[96:99], v17 offset:4096
v_mfma_f32_16x16x32_bf16 v[76:79], a[248:251], v[112:115], v[76:79]
ds_read_b128 v[100:103], v17 offset:5120
v_mfma_f32_16x16x32_bf16 v[76:79], v[28:31], v[116:119], v[76:79]
v_cvt_pk_bf16_f32 v130, v72, v73
v_cvt_pk_bf16_f32 v131, v74, v75
v_mfma_f32_16x16x32_bf16 v[76:79], v[40:43], v[120:123], v[76:79]
v_mfma_f32_16x16x32_bf16 v[76:79], v[52:55], v[124:127], v[76:79]
s_sub_u32 s70, s70, 1
s_cbranch_scc0 .Ldn_top6
v_cvt_pk_bf16_f32 v128, v68, v69
v_cvt_pk_bf16_f32 v129, v70, v71
v_cvt_pk_bf16_f32 v130, v72, v73
v_cvt_pk_bf16_f32 v131, v74, v75
v_min_u32 v128, -2, v128
v_min_u32 v129, -2, v129
v_min_u32 v130, -2, v130
v_min_u32 v131, -2, v131
global_store_dwordx4 v11, v[128:131], s[38:39] sc1
v_add_u32 v11, 110592, v11
v_cvt_pk_bf16_f32 v132, v76, v77
v_cvt_pk_bf16_f32 v134, v76, v77
v_cvt_pk_bf16_f32 v133, v78, v79
v_cvt_pk_bf16_f32 v135, v78, v79
v_permlane16_swap_b32 v132, v134
s_nop 0
v_permlane16_swap_b32 v133, v135
v_min_u32 v132, -2, v132
v_min_u32 v133, -2, v133
v_min_u32 v134, -2, v134
v_min_u32 v135, -2, v135
s_mov_b64 exec, s[76:77]
global_store_dwordx4 v12, v[132:135], s[38:39] sc1
s_mov_b64 exec, -1
v_add_u32 v12, 110592, v12
s_endpgm
.Ldn_w2:
v_add_u32 v254, -1, v14
v_add_u32 v255, -1, v14
v_add_lshl_u32 v252, v16, 0, 7
v_bfi_b32 v252, s69, v252, v15
v_add_lshl_u32 v253, v14, 0, 7
v_bfi_b32 v253, s69, v253, v13
s_mov_b32 s76, 0xffff
s_mov_b32 s77, 0xffff
s_add_u32 m0, s45, 24576
s_xor_b32 s45, s45, 0x10000
s_mov_b32 m0, s45
s_barrier
global_load_lds_dwordx4 v9, s[38:39] offset:-2048 sc1
global_load_lds_dwordx4 v9, s[38:39] offset:-1024 sc1
global_load_lds_dwordx4 v9, s[38:39] offset:0 sc1
global_load_lds_dwordx4 v9, s[38:39] offset:1024 sc1
global_load_lds_dwordx4 v9, s[38:39] offset:2048 sc1
global_load_lds_dwordx4 v9, s[38:39] offset:3072 sc1
v_add_u32 v9, 147456, v9
s_mov_b32 s70, 32
s_waitcnt vmcnt(0) lgkmcnt(0)
s_nop 7
s_branch .Ldn_in8
.p2align 6
.Ldn_top9:
s_waitcnt lgkmcnt(5)
v_mfma_f32_16x16x32_bf16 v[56:59], a[0:3], v[80:83], 0
s_waitcnt lgkmcnt(4)
v_mfma_f32_16x16x32_bf16 v[56:59], a[12:15], v[84:87], v[56:59]
s_waitcnt lgkmcnt(3)
v_mfma_f32_16x16x32_bf16 v[56:59], a[24:27], v[88:91], v[56:59]
s_waitcnt lgkmcnt(2)
v_mfma_f32_16x16x32_bf16 v[56:59], a[36:39], v[92:95], v[56:59]
s_waitcnt lgkmcnt(1)
ds_read_b128 v[104:107], v17 offset:6144
v_cvt_pk_bf16_f32 v132, v76, v77
v_mfma_f32_16x16x32_bf16 v[56:59], a[48:51], v[96:99], v[56:59]
s_waitcnt lgkmcnt(1)
v_mfma_f32_16x16x32_bf16 v[56:59], a[60:63], v[100:103], v[56:59]
s_waitcnt vmcnt(6)
s_barrier
v_mfma_f32_16x16x32_bf16 v[60:63], a[4:7], v[80:83], 0
ds_read_b128 v[108:111], v17 offset:7168
v_cvt_pk_bf16_f32 v134, v76, v77
v_mfma_f32_16x16x32_bf16 v[60:63], a[16:19], v[84:87], v[60:63]
v_cvt_pk_bf16_f32 v133, v78, v79
v_cvt_pk_bf16_f32 v135, v78, v79
v_mfma_f32_16x16x32_bf16 v[60:63], a[28:31], v[88:91], v[60:63]
ds_read_b128 v[112:115], v17 offset:8192
v_mfma_f32_16x16x32_bf16 v[60:63], a[40:43], v[92:95], v[60:63]
global_store_dwordx4 v11, v[128:131], s[38:39] sc1
v_mfma_f32_16x16x32_bf16 v[60:63], a[52:55], v[96:99], v[60:63]
ds_read_b128 v[116:119], v17 offset:9216
v_add_u32 v11, 110592, v11
v_mfma_f32_16x16x32_bf16 v[60:63], a[64:67], v[100:103], v[60:63]
v_permlane16_swap_b32 v132, v134
v_mfma_f32_16x16x32_bf16 v[64:67], a[8:11], v[80:83], 0
ds_read_b128 v[120:123], v17 offset:10240
v_mfma_f32_16x16x32_bf16 v[64:67], a[20:23], v[84:87], v[64:67]
v_permlane16_swap_b32 v133, v135
v_mfma_f32_16x16x32_bf16 v[64:67], a[32:35], v[88:91], v[64:67]
ds_read_b128 v[124:127], v17 offset:11264
v_mfma_f32_16x16x32_bf16 v[64:67], a[44:47], v[92:95], v[64:67]
s_mov_b64 exec, s[76:77]
global_store_dwordx4 v12, v[132:135], s[38:39] sc1
s_mov_b64 exec, -1
v_mfma_f32_16x16x32_bf16 v[64:67], a[56:59], v[96:99], v[64:67]
v_add_u32 v12, 110592, v12
v_mfma_f32_16x16x32_bf16 v[64:67], a[68:71], v[100:103], v[64:67]
s_waitcnt lgkmcnt(5)
v_mfma_f32_16x16x32_bf16 v[56:59], a[72:75], v[104:107], v[56:59]
s_waitcnt lgkmcnt(4)
v_mfma_f32_16x16x32_bf16 v[56:59], a[84:87], v[108:111], v[56:59]
s_waitcnt lgkmcnt(3)
v_mfma_f32_16x16x32_bf16 v[56:59], a[96:99], v[112:115], v[56:59]
s_waitcnt lgkmcnt(2)
v_mfma_f32_16x16x32_bf16 v[56:59], a[108:111], v[116:119], v[56:59]
s_waitcnt lgkmcnt(1)
v_mfma_f32_16x16x32_bf16 v[56:59], a[120:123], v[120:123], v[56:59]
s_waitcnt lgkmcnt(0)
ds_read_b128 v[80:83], v17 offset:12288
v_mfma_f32_16x16x32_bf16 v[56:59], a[132:135], v[124:127], v[56:59]
v_mfma_f32_16x16x32_bf16 v[60:63], a[76:79], v[104:107], v[60:63]
ds_read_b128 v[84:87], v17 offset:13312
v_mfma_f32_16x16x32_bf16 v[60:63], a[88:91], v[108:111], v[60:63]
v_mfma_f32_16x16x32_bf16 v[60:63], a[100:103], v[112:115], v[60:63]
ds_read_b128 v[88:91], v17 offset:14336
v_mfma_f32_16x16x32_bf16 v[60:63], a[112:115], v[116:119], v[60:63]
v_mfma_f32_16x16x32_bf16 v[60:63], a[124:127], v[120:123], v[60:63]
ds_read_b128 v[92:95], v17 offset:15360
v_mfma_f32_16x16x32_bf16 v[60:63], a[136:139], v[124:127], v[60:63]
v_mfma_f32_16x16x32_bf16 v[64:67], a[80:83], v[104:107], v[64:67]
ds_read_b128 v[96:99], v17 offset:16384
v_mfma_f32_16x16x32_bf16 v[64:67], a[92:95], v[108:111], v[64:67]
v_mfma_f32_16x16x32_bf16 v[64:67], a[104:107], v[112:115], v[64:67]
ds_read_b128 v[100:103], v17 offset:17408
v_mfma_f32_16x16x32_bf16 v[64:67], a[116:119], v[116:119], v[64:67]
v_mfma_f32_16x16x32_bf16 v[64:67], a[128:131], v[120:123], v[64:67]
v_mfma_f32_16x16x32_bf16 v[64:67], a[140:143], v[124:127], v[64:67]
s_waitcnt lgkmcnt(5)
v_mfma_f32_16x16x32_bf16 v[56:59], a[144:147], v[80:83], v[56:59]
s_waitcnt lgkmcnt(4)
v_mfma_f32_16x16x32_bf16 v[56:59], a[156:159], v[84:87], v[56:59]
s_waitcnt lgkmcnt(3)
v_mfma_f32_16x16x32_bf16 v[56:59], a[168:171], v[88:91], v[56:59]
s_waitcnt lgkmcnt(2)
v_mfma_f32_16x16x32_bf16 v[56:59], a[180:183], v[92:95], v[56:59]
s_waitcnt lgkmcnt(1)
v_mfma_f32_16x16x32_bf16 v[56:59], a[192:195], v[96:99], v[56:59]
s_xor_b32 s45, s45, 0x10000
ds_read_b128 v[104:107], v17 offset:18432
s_waitcnt lgkmcnt(1)
v_mfma_f32_16x16x32_bf16 v[56:59], a[204:207], v[100:103], v[56:59]
s_add_u32 m0, s45, 0
v_mfma_f32_16x16x32_bf16 v[60:63], a[148:151], v[80:83], v[60:63]
ds_read_b128 v[108:111], v17 offset:19456
ds_read_b128 v[112:115], v17 offset:20480
)"
R"(v_mfma_f32_16x16x32_bf16 v[60:63], a[160:163], v[84:87], v[60:63]
v_mfma_f32_16x16x32_bf16 v[60:63], a[172:175], v[88:91], v[60:63]
s_barrier
v_mfma_f32_16x16x32_bf16 v[60:63], a[184:187], v[92:95], v[60:63]
v_mfma_f32_16x16x32_bf16 v[60:63], a[196:199], v[96:99], v[60:63]
ds_read_b128 v[116:119], v17 offset:21504
global_load_lds_dwordx4 v9, s[38:39] offset:-2048 sc1
v_mfma_f32_16x16x32_bf16 v[60:63], a[208:211], v[100:103], v[60:63]
v_mfma_f32_16x16x32_bf16 v[64:67], a[152:155], v[80:83], v[64:67]
ds_read_b128 v[120:123], v17 offset:22528
v_mfma_f32_16x16x32_bf16 v[64:67], a[164:167], v[84:87], v[64:67]
global_load_lds_dwordx4 v9, s[38:39] offset:-1024 sc1
v_mfma_f32_16x16x32_bf16 v[64:67], a[176:179], v[88:91], v[64:67]
ds_read_b128 v[124:127], v17 offset:23552
v_mfma_f32_16x16x32_bf16 v[64:67], a[188:191], v[92:95], v[64:67]
v_mfma_f32_16x16x32_bf16 v[64:67], a[200:203], v[96:99], v[64:67]
global_load_lds_dwordx4 v9, s[38:39] offset:0 sc1
v_mfma_f32_16x16x32_bf16 v[64:67], a[212:215], v[100:103], v[64:67]
s_waitcnt lgkmcnt(5)
v_mfma_f32_16x16x32_bf16 v[56:59], a[216:219], v[104:107], v[56:59]
s_waitcnt lgkmcnt(4)
v_mfma_f32_16x16x32_bf16 v[56:59], a[228:231], v[108:111], v[56:59]
s_waitcnt lgkmcnt(3)
global_load_lds_dwordx4 v9, s[38:39] offset:1024 sc1
v_mfma_f32_16x16x32_bf16 v[56:59], a[240:243], v[112:115], v[56:59]
s_waitcnt lgkmcnt(2)
v_mfma_f32_16x16x32_bf16 v[56:59], a[252:255], v[116:119], v[56:59]
s_waitcnt lgkmcnt(1)
v_mfma_f32_16x16x32_bf16 v[56:59], v[32:35], v[120:123], v[56:59]
s_waitcnt lgkmcnt(0)
global_load_lds_dwordx4 v9, s[38:39] offset:2048 sc1
v_mfma_f32_16x16x32_bf16 v[56:59], v[44:47], v[124:127], v[56:59]
v_mfma_f32_16x16x32_bf16 v[60:63], a[220:223], v[104:107], v[60:63]
v_mfma_f32_16x16x32_bf16 v[60:63], a[232:235], v[108:111], v[60:63]
v_mfma_f32_16x16x32_bf16 v[60:63], a[244:247], v[112:115], v[60:63]
s_waitcnt vmcnt(7)
s_barrier
v_mfma_f32_16x16x32_bf16 v[60:63], v[24:27], v[116:119], v[60:63]
ds_read_b128 v[80:83], v17 offset:24576
global_load_lds_dwordx4 v9, s[38:39] offset:3072 sc1
v_mfma_f32_16x16x32_bf16 v[60:63], v[36:39], v[120:123], v[60:63]
ds_read_b128 v[84:87], v17 offset:25600
v_add_u32 v9, 147456, v9
v_mfma_f32_16x16x32_bf16 v[60:63], v[48:51], v[124:127], v[60:63]
ds_read_b128 v[88:91], v17 offset:26624
v_min_u32 v9, v9, v10
v_mfma_f32_16x16x32_bf16 v[64:67], a[224:227], v[104:107], v[64:67]
ds_read_b128 v[92:95], v17 offset:27648
v_cvt_pk_bf16_f32 v128, v56, v57
v_mfma_f32_16x16x32_bf16 v[64:67], a[236:239], v[108:111], v[64:67]
ds_read_b128 v[96:99], v17 offset:28672
v_cvt_pk_bf16_f32 v129, v58, v59
v_mfma_f32_16x16x32_bf16 v[64:67], a[248:251], v[112:115], v[64:67]
ds_read_b128 v[100:103], v17 offset:29696
v_mfma_f32_16x16x32_bf16 v[64:67], v[28:31], v[116:119], v[64:67]
v_cvt_pk_bf16_f32 v130, v60, v61
v_cvt_pk_bf16_f32 v131, v62, v63
v_mfma_f32_16x16x32_bf16 v[64:67], v[40:43], v[120:123], v[64:67]
v_min_u32 v128, -2, v128
v_min_u32 v129, -2, v129
v_mfma_f32_16x16x32_bf16 v[64:67], v[52:55], v[124:127], v[64:67]
s_waitcnt lgkmcnt(5)
v_min_u32 v130, -2, v130
v_min_u32 v131, -2, v131
v_mfma_f32_16x16x32_bf16 v[68:71], a[0:3], v[80:83], 0
s_waitcnt lgkmcnt(4)
v_mfma_f32_16x16x32_bf16 v[68:71], a[12:15], v[84:87], v[68:71]
s_waitcnt lgkmcnt(3)
v_mfma_f32_16x16x32_bf16 v[68:71], a[24:27], v[88:91], v[68:71]
s_waitcnt lgkmcnt(2)
v_mfma_f32_16x16x32_bf16 v[68:71], a[36:39], v[92:95], v[68:71]
s_waitcnt lgkmcnt(1)
ds_read_b128 v[104:107], v17 offset:30720
v_cvt_pk_bf16_f32 v132, v64, v65
v_mfma_f32_16x16x32_bf16 v[68:71], a[48:51], v[96:99], v[68:71]
s_waitcnt lgkmcnt(1)
v_mfma_f32_16x16x32_bf16 v[68:71], a[60:63], v[100:103], v[68:71]
s_waitcnt vmcnt(6)
s_barrier
v_mfma_f32_16x16x32_bf16 v[72:75], a[4:7], v[80:83], 0
ds_read_b128 v[108:111], v17 offset:31744
v_cvt_pk_bf16_f32 v134, v64, v65
v_mfma_f32_16x16x32_bf16 v[72:75], a[16:19], v[84:87], v[72:75]
v_cvt_pk_bf16_f32 v133, v66, v67
v_cvt_pk_bf16_f32 v135, v66, v67
v_mfma_f32_16x16x32_bf16 v[72:75], a[28:31], v[88:91], v[72:75]
ds_read_b128 v[112:115], v17 offset:32768
v_mfma_f32_16x16x32_bf16 v[72:75], a[40:43], v[92:95], v[72:75]
global_store_dwordx4 v11, v[128:131], s[38:39] sc1
v_mfma_f32_16x16x32_bf16 v[72:75], a[52:55], v[96:99], v[72:75]
ds_read_b128 v[116:119], v17 offset:33792
v_add_u32 v11, 110592, v11
v_mfma_f32_16x16x32_bf16 v[72:75], a[64:67], v[100:103], v[72:75]
v_permlane16_swap_b32 v132, v134
v_mfma_f32_16x16x32_bf16 v[76:79], a[8:11], v[80:83], 0
ds_read_b128 v[120:123], v17 offset:34816
v_mfma_f32_16x16x32_bf16 v[76:79], a[20:23], v[84:87], v[76:79]
v_permlane16_swap_b32 v133, v135
v_mfma_f32_16x16x32_bf16 v[76:79], a[32:35], v[88:91], v[76:79]
ds_read_b128 v[124:127], v17 offset:35840
v_min_u32 v132, -2, v132
v_mfma_f32_16x16x32_bf16 v[76:79], a[44:47], v[92:95], v[76:79]
v_min_u32 v133, -2, v133
v_min_u32 v134, -2, v134
v_mfma_f32_16x16x32_bf16 v[76:79], a[56:59], v[96:99], v[76:79]
v_min_u32 v135, -2, v135
v_mfma_f32_16x16x32_bf16 v[76:79], a[68:71], v[100:103], v[76:79]
s_waitcnt lgkmcnt(5)
s_mov_b64 exec, s[76:77]
global_store_dwordx4 v12, v[132:135], s[38:39] sc1
s_mov_b64 exec, -1
v_mfma_f32_16x16x32_bf16 v[68:71], a[72:75], v[104:107], v[68:71]
s_waitcnt lgkmcnt(4)
v_add_u32 v12, 110592, v12
v_mfma_f32_16x16x32_bf16 v[68:71], a[84:87], v[108:111], v[68:71]
s_waitcnt lgkmcnt(3)
v_mfma_f32_16x16x32_bf16 v[68:71], a[96:99], v[112:115], v[68:71]
s_waitcnt lgkmcnt(2)
v_mfma_f32_16x16x32_bf16 v[68:71], a[108:111], v[116:119], v[68:71]
s_waitcnt lgkmcnt(1)
v_mfma_f32_16x16x32_bf16 v[68:71], a[120:123], v[120:123], v[68:71]
s_waitcnt lgkmcnt(0)
ds_read_b128 v[80:83], v17 offset:36864
v_mfma_f32_16x16x32_bf16 v[68:71], a[132:135], v[124:127], v[68:71]
v_mfma_f32_16x16x32_bf16 v[72:75], a[76:79], v[104:107], v[72:75]
ds_read_b128 v[84:87], v17 offset:37888
v_mfma_f32_16x16x32_bf16 v[72:75], a[88:91], v[108:111], v[72:75]
v_mfma_f32_16x16x32_bf16 v[72:75], a[100:103], v[112:115], v[72:75]
ds_read_b128 v[88:91], v17 offset:38912
v_mfma_f32_16x16x32_bf16 v[72:75], a[112:115], v[116:119], v[72:75]
v_mfma_f32_16x16x32_bf16 v[72:75], a[124:127], v[120:123], v[72:75]
ds_read_b128 v[92:95], v17 offset:39936
v_mfma_f32_16x16x32_bf16 v[72:75], a[136:139], v[124:127], v[72:75]
v_mfma_f32_16x16x32_bf16 v[76:79], a[80:83], v[104:107], v[76:79]
ds_read_b128 v[96:99], v17 offset:40960
v_mfma_f32_16x16x32_bf16 v[76:79], a[92:95], v[108:111], v[76:79]
v_mfma_f32_16x16x32_bf16 v[76:79], a[104:107], v[112:115], v[76:79]
ds_read_b128 v[100:103], v17 offset:41984
v_mfma_f32_16x16x32_bf16 v[76:79], a[116:119], v[116:119], v[76:79]
v_mfma_f32_16x16x32_bf16 v[76:79], a[128:131], v[120:123], v[76:79]
v_mfma_f32_16x16x32_bf16 v[76:79], a[140:143], v[124:127], v[76:79]
s_waitcnt lgkmcnt(5)
v_mfma_f32_16x16x32_bf16 v[68:71], a[144:147], v[80:83], v[68:71]
s_waitcnt lgkmcnt(4)
v_mfma_f32_16x16x32_bf16 v[68:71], a[156:159], v[84:87], v[68:71]
s_waitcnt lgkmcnt(3)
v_mfma_f32_16x16x32_bf16 v[68:71], a[168:171], v[88:91], v[68:71]
.Ldn_in8:
s_waitcnt lgkmcnt(2)
v_mfma_f32_16x16x32_bf16 v[68:71], a[180:183], v[92:95], v[68:71]
s_waitcnt lgkmcnt(1)
v_mfma_f32_16x16x32_bf16 v[68:71], a[192:195], v[96:99], v[68:71]
s_waitcnt lgkmcnt(0)
ds_read_b128 v[104:107], v17 offset:43008
v_mfma_f32_16x16x32_bf16 v[68:71], a[204:207], v[100:103], v[68:71]
s_add_u32 m0, s45, 24576
v_mfma_f32_16x16x32_bf16 v[72:75], a[148:151], v[80:83], v[72:75]
ds_read_b128 v[108:111], v17 offset:44032
ds_read_b128 v[112:115], v17 offset:45056
v_mfma_f32_16x16x32_bf16 v[72:75], a[160:163], v[84:87], v[72:75]
v_mfma_f32_16x16x32_bf16 v[72:75], a[172:175], v[88:91], v[72:75]
s_barrier
v_mfma_f32_16x16x32_bf16 v[72:75], a[184:187], v[92:95], v[72:75]
v_mfma_f32_16x16x32_bf16 v[72:75], a[196:199], v[96:99], v[72:75]
ds_read_b128 v[116:119], v17 offset:46080
global_load_lds_dwordx4 v9, s[38:39] offset:-2048 sc1
v_mfma_f32_16x16x32_bf16 v[72:75], a[208:211], v[100:103], v[72:75]
v_mfma_f32_16x16x32_bf16 v[76:79], a[152:155], v[80:83], v[76:79]
ds_read_b128 v[120:123], v17 offset:47104
v_mfma_f32_16x16x32_bf16 v[76:79], a[164:167], v[84:87], v[76:79]
global_load_lds_dwordx4 v9, s[38:39] offset:-1024 sc1
v_mfma_f32_16x16x32_bf16 v[76:79], a[176:179], v[88:91], v[76:79]
ds_read_b128 v[124:127], v17 offset:48128
v_mfma_f32_16x16x32_bf16 v[76:79], a[188:191], v[92:95], v[76:79]
v_mfma_f32_16x16x32_bf16 v[76:79], a[200:203], v[96:99], v[76:79]
global_load_lds_dwordx4 v9, s[38:39] offset:0 sc1
v_mfma_f32_16x16x32_bf16 v[76:79], a[212:215], v[100:103], v[76:79]
s_waitcnt lgkmcnt(5)
v_mfma_f32_16x16x32_bf16 v[68:71], a[216:219], v[104:107], v[68:71]
s_waitcnt lgkmcnt(4)
v_mfma_f32_16x16x32_bf16 v[68:71], a[228:231], v[108:111], v[68:71]
s_waitcnt lgkmcnt(3)
global_load_lds_dwordx4 v9, s[38:39] offset:1024 sc1
v_mfma_f32_16x16x32_bf16 v[68:71], a[240:243], v[112:115], v[68:71]
s_waitcnt lgkmcnt(2)
v_mfma_f32_16x16x32_bf16 v[68:71], a[252:255], v[116:119], v[68:71]
s_waitcnt lgkmcnt(1)
v_mfma_f32_16x16x32_bf16 v[68:71], v[32:35], v[120:123], v[68:71]
s_waitcnt lgkmcnt(0)
global_load_lds_dwordx4 v9, s[38:39] offset:2048 sc1
v_mfma_f32_16x16x32_bf16 v[68:71], v[44:47], v[124:127], v[68:71]
v_xor_b32 v17, 0x10000, v17
v_mfma_f32_16x16x32_bf16 v[72:75], a[220:223], v[104:107], v[72:75]
v_mfma_f32_16x16x32_bf16 v[72:75], a[232:235], v[108:111], v[72:75]
v_mfma_f32_16x16x32_bf16 v[72:75], a[244:247], v[112:115], v[72:75]
s_waitcnt vmcnt(7)
s_barrier
v_mfma_f32_16x16x32_bf16 v[72:75], v[24:27], v[116:119], v[72:75]
ds_read_b128 v[80:83], v17 offset:0
global_load_lds_dwordx4 v9, s[38:39] offset:3072 sc1
v_mfma_f32_16x16x32_bf16 v[72:75], v[36:39], v[120:123], v[72:75]
ds_read_b128 v[84:87], v17 offset:1024
v_add_u32 v9, 147456, v9
v_mfma_f32_16x16x32_bf16 v[72:75], v[48:51], v[124:127], v[72:75]
ds_read_b128 v[88:91], v17 offset:2048
v_min_u32 v9, v9, v10
v_mfma_f32_16x16x32_bf16 v[76:79], a[224:227], v[104:107], v[76:79]
ds_read_b128 v[92:95], v17 offset:3072
v_cvt_pk_bf16_f32 v128, v68, v69
v_mfma_f32_16x16x32_bf16 v[76:79], a[236:239], v[108:111], v[76:79]
ds_read_b128 v[96:99], v17 offset:4096
v_cvt_pk_bf16_f32 v129, v70, v71
v_mfma_f32_16x16x32_bf16 v[76:79], a[248:251], v[112:115], v[76:79]
ds_read_b128 v[100:103], v17 offset:5120
v_mfma_f32_16x16x32_bf16 v[76:79], v[28:31], v[116:119], v[76:79]
v_cvt_pk_bf16_f32 v130, v72, v73
v_cvt_pk_bf16_f32 v131, v74, v75
v_mfma_f32_16x16x32_bf16 v[76:79], v[40:43], v[120:123], v[76:79]
v_mfma_f32_16x16x32_bf16 v[76:79], v[52:55], v[124:127], v[76:79]
s_sub_u32 s70, s70, 1
s_cbranch_scc0 .Ldn_top9
v_cvt_pk_bf16_f32 v128, v68, v69
v_cvt_pk_bf16_f32 v129, v70, v71
v_cvt_pk_bf16_f32 v130, v72, v73
v_cvt_pk_bf16_f32 v131, v74, v75
v_min_u32 v128, -2, v128
v_min_u32 v129, -2, v129
v_min_u32 v130, -2, v130
v_min_u32 v131, -2, v131
global_store_dwordx4 v11, v[128:131], s[38:39] sc1
v_add_u32 v11, 110592, v11
v_cvt_pk_bf16_f32 v132, v76, v77
v_cvt_pk_bf16_f32 v134, v76, v77
v_cvt_pk_bf16_f32 v133, v78, v79
v_cvt_pk_bf16_f32 v135, v78, v79
v_permlane16_swap_b32 v132, v134
s_nop 0
v_permlane16_swap_b32 v133, v135
v_min_u32 v132, -2, v132
v_min_u32 v133, -2, v133
v_min_u32 v134, -2, v134
v_min_u32 v135, -2, v135
s_mov_b64 exec, s[76:77]
global_store_dwordx4 v12, v[132:135], s[38:39] sc1
s_mov_b64 exec, -1
v_add_u32 v12, 110592, v12
s_endpgm
.Ldn_w3:
v_add_u32 v254, -1, v14
v_add_u32 v255, -1, v14
v_add_lshl_u32 v252, v16, 0, 7
v_bfi_b32 v252, s69, v252, v15
v_add_lshl_u32 v253, v14, 0, 7
v_bfi_b32 v253, s69, v253, v13
s_mov_b32 s76, 0xffff
s_mov_b32 s77, 0xffff
s_add_u32 m0, s45, 24576
s_xor_b32 s45, s45, 0x10000
s_mov_b32 m0, s45
s_barrier
global_load_lds_dwordx4 v9, s[38:39] offset:-2048 sc1
global_load_lds_dwordx4 v9, s[38:39] offset:-1024 sc1
global_load_lds_dwordx4 v9, s[38:39] offset:0 sc1
global_load_lds_dwordx4 v9, s[38:39] offset:1024 sc1
global_load_lds_dwordx4 v9, s[38:39] offset:2048 sc1
global_load_lds_dwordx4 v9, s[38:39] offset:3072 sc1
v_add_u32 v9, 147456, v9
s_mov_b32 s70, 32
s_waitcnt vmcnt(0) lgkmcnt(0)
s_nop 7
s_branch .Ldn_in11
.p2align 6
.Ldn_top12:
s_waitcnt lgkmcnt(5)
v_mfma_f32_16x16x32_bf16 v[56:59], a[0:3], v[80:83], 0
s_waitcnt lgkmcnt(4)
v_mfma_f32_16x16x32_bf16 v[56:59], a[12:15], v[84:87], v[56:59]
s_waitcnt lgkmcnt(3)
v_mfma_f32_16x16x32_bf16 v[56:59], a[24:27], v[88:91], v[56:59]
s_waitcnt lgkmcnt(2)
v_mfma_f32_16x16x32_bf16 v[56:59], a[36:39], v[92:95], v[56:59]
s_waitcnt lgkmcnt(1)
ds_read_b128 v[104:107], v17 offset:6144
v_cvt_pk_bf16_f32 v132, v76, v77
v_mfma_f32_16x16x32_bf16 v[56:59], a[48:51], v[96:99], v[56:59]
s_waitcnt lgkmcnt(1)
v_mfma_f32_16x16x32_bf16 v[56:59], a[60:63], v[100:103], v[56:59]
s_waitcnt vmcnt(6)
s_barrier
v_mfma_f32_16x16x32_bf16 v[60:63], a[4:7], v[80:83], 0
ds_read_b128 v[108:111], v17 offset:7168
s_mov_b64 exec, 1
global_store_dword v252, v16, s[38:39] sc1
s_mov_b64 exec, -1
v_mfma_f32_16x16x32_bf16 v[60:63], a[16:19], v[84:87], v[60:63]
v_add_u32 v16, 1, v16
v_add_lshl_u32 v252, v16, 0, 7
v_bfi_b32 v252, s69, v252, v15
v_mfma_f32_16x16x32_bf16 v[60:63], a[28:31], v[88:91], v[60:63]
ds_read_b128 v[112:115], v17 offset:8192
v_cvt_pk_bf16_f32 v134, v76, v77
v_mfma_f32_16x16x32_bf16 v[60:63], a[40:43], v[92:95], v[60:63]
global_store_dwordx4 v11, v[128:131], s[38:39] sc1
v_mfma_f32_16x16x32_bf16 v[60:63], a[52:55], v[96:99], v[60:63]
ds_read_b128 v[116:119], v17 offset:9216
v_add_u32 v11, 110592, v11
v_mfma_f32_16x16x32_bf16 v[60:63], a[64:67], v[100:103], v[60:63]
v_cvt_pk_bf16_f32 v133, v78, v79
v_cvt_pk_bf16_f32 v135, v78, v79
v_mfma_f32_16x16x32_bf16 v[64:67], a[8:11], v[80:83], 0
ds_read_b128 v[120:123], v17 offset:10240
v_mfma_f32_16x16x32_bf16 v[64:67], a[20:23], v[84:87], v[64:67]
v_permlane16_swap_b32 v132, v134
v_mfma_f32_16x16x32_bf16 v[64:67], a[32:35], v[88:91], v[64:67]
ds_read_b128 v[124:127], v17 offset:11264
v_mfma_f32_16x16x32_bf16 v[64:67], a[44:47], v[92:95], v[64:67]
v_permlane16_swap_b32 v133, v135
v_mfma_f32_16x16x32_bf16 v[64:67], a[56:59], v[96:99], v[64:67]
s_mov_b64 exec, s[76:77]
global_store_dwordx4 v12, v[132:135], s[38:39] sc1
s_mov_b64 exec, -1
v_mfma_f32_16x16x32_bf16 v[64:67], a[68:71], v[100:103], v[64:67]
s_waitcnt lgkmcnt(5)
v_add_u32 v12, 110592, v12
v_mfma_f32_16x16x32_bf16 v[56:59], a[72:75], v[104:107], v[56:59]
s_waitcnt lgkmcnt(4)
v_mfma_f32_16x16x32_bf16 v[56:59], a[84:87], v[108:111], v[56:59]
s_waitcnt lgkmcnt(3)
v_mfma_f32_16x16x32_bf16 v[56:59], a[96:99], v[112:115], v[56:59]
s_waitcnt lgkmcnt(2)
v_mfma_f32_16x16x32_bf16 v[56:59], a[108:111], v[116:119], v[56:59]
s_waitcnt lgkmcnt(1)
v_mfma_f32_16x16x32_bf16 v[56:59], a[120:123], v[120:123], v[56:59]
s_waitcnt lgkmcnt(0)
ds_read_b128 v[80:83], v17 offset:12288
v_mfma_f32_16x16x32_bf16 v[56:59], a[132:135], v[124:127], v[56:59]
v_mfma_f32_16x16x32_bf16 v[60:63], a[76:79], v[104:107], v[60:63]
ds_read_b128 v[84:87], v17 offset:13312
v_mfma_f32_16x16x32_bf16 v[60:63], a[88:91], v[108:111], v[60:63]
v_mfma_f32_16x16x32_bf16 v[60:63], a[100:103], v[112:115], v[60:63]
ds_read_b128 v[88:91], v17 offset:14336
v_mfma_f32_16x16x32_bf16 v[60:63], a[112:115], v[116:119], v[60:63]
v_mfma_f32_16x16x32_bf16 v[60:63], a[124:127], v[120:123], v[60:63]
ds_read_b128 v[92:95], v17 offset:15360
v_mfma_f32_16x16x32_bf16 v[60:63], a[136:139], v[124:127], v[60:63]
v_mfma_f32_16x16x32_bf16 v[64:67], a[80:83], v[104:107], v[64:67]
ds_read_b128 v[96:99], v17 offset:16384
v_mfma_f32_16x16x32_bf16 v[64:67], a[92:95], v[108:111], v[64:67]
v_mfma_f32_16x16x32_bf16 v[64:67], a[104:107], v[112:115], v[64:67]
ds_read_b128 v[100:103], v17 offset:17408
v_mfma_f32_16x16x32_bf16 v[64:67], a[116:119], v[116:119], v[64:67]
v_mfma_f32_16x16x32_bf16 v[64:67], a[128:131], v[120:123], v[64:67]
v_mfma_f32_16x16x32_bf16 v[64:67], a[140:143], v[124:127], v[64:67]
s_waitcnt lgkmcnt(5)
v_mfma_f32_16x16x32_bf16 v[56:59], a[144:147], v[80:83], v[56:59]
s_waitcnt lgkmcnt(4)
v_mfma_f32_16x16x32_bf16 v[56:59], a[156:159], v[84:87], v[56:59]
s_waitcnt lgkmcnt(3)
v_mfma_f32_16x16x32_bf16 v[56:59], a[168:171], v[88:91], v[56:59]
s_waitcnt lgkmcnt(2)
v_mfma_f32_16x16x32_bf16 v[56:59], a[180:183], v[92:95], v[56:59]
s_waitcnt lgkmcnt(1)
v_mfma_f32_16x16x32_bf16 v[56:59], a[192:195], v[96:99], v[56:59]
s_xor_b32 s45, s45, 0x10000
ds_read_b128 v[104:107], v17 offset:18432
s_waitcnt lgkmcnt(1)
v_mfma_f32_16x16x32_bf16 v[56:59], a[204:207], v[100:103], v[56:59]
s_add_u32 m0, s45, 0
v_mfma_f32_16x16x32_bf16 v[60:63], a[148:151], v[80:83], v[60:63]
ds_read_b128 v[108:111], v17 offset:19456
ds_read_b128 v[112:115], v17 offset:20480
v_mfma_f32_16x16x32_bf16 v[60:63], a[160:163], v[84:87], v[60:63]
v_mfma_f32_16x16x32_bf16 v[60:63], a[172:175], v[88:91], v[60:63]
s_barrier
v_mfma_f32_16x16x32_bf16 v[60:63], a[184:187], v[92:95], v[60:63]
v_mfma_f32_16x16x32_bf16 v[60:63], a[196:199], v[96:99], v[60:63]
ds_read_b128 v[116:119], v17 offset:21504
v_mfma_f32_16x16x32_bf16 v[60:63], a[208:211], v[100:103], v[60:63]
global_load_lds_dwordx4 v9, s[38:39] offset:-2048 sc1
v_mfma_f32_16x16x32_bf16 v[64:67], a[152:155], v[80:83], v[64:67]
ds_read_b128 v[120:123], v17 offset:22528
v_mfma_f32_16x16x32_bf16 v[64:67], a[164:167], v[84:87], v[64:67]
v_mfma_f32_16x16x32_bf16 v[64:67], a[176:179], v[88:91], v[64:67]
ds_read_b128 v[124:127], v17 offset:23552
global_load_lds_dwordx4 v9, s[38:39] offset:-1024 sc1
v_mfma_f32_16x16x32_bf16 v[64:67], a[188:191], v[92:95], v[64:67]
v_mfma_f32_16x16x32_bf16 v[64:67], a[200:203], v[96:99], v[64:67]
v_mfma_f32_16x16x32_bf16 v[64:67], a[212:215], v[100:103], v[64:67]
s_waitcnt lgkmcnt(5)
global_load_lds_dwordx4 v9, s[38:39] offset:0 sc1
v_mfma_f32_16x16x32_bf16 v[56:59], a[216:219], v[104:107], v[56:59]
s_waitcnt lgkmcnt(4)
v_mfma_f32_16x16x32_bf16 v[56:59], a[228:231], v[108:111], v[56:59]
s_waitcnt lgkmcnt(3)
v_mfma_f32_16x16x32_bf16 v[56:59], a[240:243], v[112:115], v[56:59]
s_waitcnt lgkmcnt(2)
global_load_lds_dwordx4 v9, s[38:39] offset:1024 sc1
v_mfma_f32_16x16x32_bf16 v[56:59], a[252:255], v[116:119], v[56:59]
s_waitcnt lgkmcnt(1)
v_mfma_f32_16x16x32_bf16 v[56:59], v[32:35], v[120:123], v[56:59]
)"
R"(s_waitcnt lgkmcnt(0)
v_mfma_f32_16x16x32_bf16 v[56:59], v[44:47], v[124:127], v[56:59]
global_load_lds_dwordx4 v9, s[38:39] offset:2048 sc1
v_mfma_f32_16x16x32_bf16 v[60:63], a[220:223], v[104:107], v[60:63]
v_mfma_f32_16x16x32_bf16 v[60:63], a[232:235], v[108:111], v[60:63]
v_mfma_f32_16x16x32_bf16 v[60:63], a[244:247], v[112:115], v[60:63]
s_waitcnt vmcnt(8)
s_barrier
v_mfma_f32_16x16x32_bf16 v[60:63], v[24:27], v[116:119], v[60:63]
ds_read_b128 v[80:83], v17 offset:24576
global_load_lds_dwordx4 v9, s[38:39] offset:3072 sc1
v_mfma_f32_16x16x32_bf16 v[60:63], v[36:39], v[120:123], v[60:63]
ds_read_b128 v[84:87], v17 offset:25600
v_add_u32 v9, 147456, v9
v_mfma_f32_16x16x32_bf16 v[60:63], v[48:51], v[124:127], v[60:63]
ds_read_b128 v[88:91], v17 offset:26624
v_min_u32 v9, v9, v10
v_mfma_f32_16x16x32_bf16 v[64:67], a[224:227], v[104:107], v[64:67]
ds_read_b128 v[92:95], v17 offset:27648
v_cvt_pk_bf16_f32 v128, v56, v57
v_mfma_f32_16x16x32_bf16 v[64:67], a[236:239], v[108:111], v[64:67]
ds_read_b128 v[96:99], v17 offset:28672
v_cvt_pk_bf16_f32 v129, v58, v59
v_mfma_f32_16x16x32_bf16 v[64:67], a[248:251], v[112:115], v[64:67]
ds_read_b128 v[100:103], v17 offset:29696
v_mfma_f32_16x16x32_bf16 v[64:67], v[28:31], v[116:119], v[64:67]
v_cvt_pk_bf16_f32 v130, v60, v61
v_cvt_pk_bf16_f32 v131, v62, v63
v_mfma_f32_16x16x32_bf16 v[64:67], v[40:43], v[120:123], v[64:67]
v_min_u32 v128, -2, v128
v_min_u32 v129, -2, v129
v_mfma_f32_16x16x32_bf16 v[64:67], v[52:55], v[124:127], v[64:67]
s_waitcnt lgkmcnt(5)
v_min_u32 v130, -2, v130
v_min_u32 v131, -2, v131
v_mfma_f32_16x16x32_bf16 v[68:71], a[0:3], v[80:83], 0
s_waitcnt lgkmcnt(4)
v_mfma_f32_16x16x32_bf16 v[68:71], a[12:15], v[84:87], v[68:71]
s_waitcnt lgkmcnt(3)
v_mfma_f32_16x16x32_bf16 v[68:71], a[24:27], v[88:91], v[68:71]
s_waitcnt lgkmcnt(2)
v_mfma_f32_16x16x32_bf16 v[68:71], a[36:39], v[92:95], v[68:71]
s_waitcnt lgkmcnt(1)
ds_read_b128 v[104:107], v17 offset:30720
v_cvt_pk_bf16_f32 v132, v64, v65
v_mfma_f32_16x16x32_bf16 v[68:71], a[48:51], v[96:99], v[68:71]
s_waitcnt lgkmcnt(1)
v_mfma_f32_16x16x32_bf16 v[68:71], a[60:63], v[100:103], v[68:71]
s_waitcnt vmcnt(6)
s_barrier
v_mfma_f32_16x16x32_bf16 v[72:75], a[4:7], v[80:83], 0
ds_read_b128 v[108:111], v17 offset:31744
v_cvt_pk_bf16_f32 v134, v64, v65
v_mfma_f32_16x16x32_bf16 v[72:75], a[16:19], v[84:87], v[72:75]
v_cvt_pk_bf16_f32 v133, v66, v67
v_cvt_pk_bf16_f32 v135, v66, v67
v_mfma_f32_16x16x32_bf16 v[72:75], a[28:31], v[88:91], v[72:75]
ds_read_b128 v[112:115], v17 offset:32768
s_mov_b64 exec, 1
global_store_dword v252, v16, s[38:39] sc1
s_mov_b64 exec, -1
v_mfma_f32_16x16x32_bf16 v[72:75], a[40:43], v[92:95], v[72:75]
global_store_dwordx4 v11, v[128:131], s[38:39] sc1
v_add_u32 v16, 1, v16
v_mfma_f32_16x16x32_bf16 v[72:75], a[52:55], v[96:99], v[72:75]
ds_read_b128 v[116:119], v17 offset:33792
v_add_u32 v11, 110592, v11
v_add_lshl_u32 v252, v16, 0, 7
v_mfma_f32_16x16x32_bf16 v[72:75], a[64:67], v[100:103], v[72:75]
v_permlane16_swap_b32 v132, v134
v_bfi_b32 v252, s69, v252, v15
v_mfma_f32_16x16x32_bf16 v[76:79], a[8:11], v[80:83], 0
ds_read_b128 v[120:123], v17 offset:34816
v_mfma_f32_16x16x32_bf16 v[76:79], a[20:23], v[84:87], v[76:79]
v_permlane16_swap_b32 v133, v135
v_mfma_f32_16x16x32_bf16 v[76:79], a[32:35], v[88:91], v[76:79]
ds_read_b128 v[124:127], v17 offset:35840
v_min_u32 v132, -2, v132
v_mfma_f32_16x16x32_bf16 v[76:79], a[44:47], v[92:95], v[76:79]
v_min_u32 v133, -2, v133
v_min_u32 v134, -2, v134
v_mfma_f32_16x16x32_bf16 v[76:79], a[56:59], v[96:99], v[76:79]
v_min_u32 v135, -2, v135
v_mfma_f32_16x16x32_bf16 v[76:79], a[68:71], v[100:103], v[76:79]
s_waitcnt lgkmcnt(5)
s_mov_b64 exec, s[76:77]
global_store_dwordx4 v12, v[132:135], s[38:39] sc1
s_mov_b64 exec, -1
v_mfma_f32_16x16x32_bf16 v[68:71], a[72:75], v[104:107], v[68:71]
s_waitcnt lgkmcnt(4)
v_add_u32 v12, 110592, v12
v_mfma_f32_16x16x32_bf16 v[68:71], a[84:87], v[108:111], v[68:71]
s_waitcnt lgkmcnt(3)
v_mfma_f32_16x16x32_bf16 v[68:71], a[96:99], v[112:115], v[68:71]
s_waitcnt lgkmcnt(2)
v_mfma_f32_16x16x32_bf16 v[68:71], a[108:111], v[116:119], v[68:71]
s_waitcnt lgkmcnt(1)
v_mfma_f32_16x16x32_bf16 v[68:71], a[120:123], v[120:123], v[68:71]
s_waitcnt lgkmcnt(0)
ds_read_b128 v[80:83], v17 offset:36864
v_mfma_f32_16x16x32_bf16 v[68:71], a[132:135], v[124:127], v[68:71]
v_mfma_f32_16x16x32_bf16 v[72:75], a[76:79], v[104:107], v[72:75]
ds_read_b128 v[84:87], v17 offset:37888
v_mfma_f32_16x16x32_bf16 v[72:75], a[88:91], v[108:111], v[72:75]
v_mfma_f32_16x16x32_bf16 v[72:75], a[100:103], v[112:115], v[72:75]
ds_read_b128 v[88:91], v17 offset:38912
v_mfma_f32_16x16x32_bf16 v[72:75], a[112:115], v[116:119], v[72:75]
v_mfma_f32_16x16x32_bf16 v[72:75], a[124:127], v[120:123], v[72:75]
ds_read_b128 v[92:95], v17 offset:39936
v_mfma_f32_16x16x32_bf16 v[72:75], a[136:139], v[124:127], v[72:75]
v_mfma_f32_16x16x32_bf16 v[76:79], a[80:83], v[104:107], v[76:79]
ds_read_b128 v[96:99], v17 offset:40960
v_mfma_f32_16x16x32_bf16 v[76:79], a[92:95], v[108:111], v[76:79]
v_mfma_f32_16x16x32_bf16 v[76:79], a[104:107], v[112:115], v[76:79]
ds_read_b128 v[100:103], v17 offset:41984
v_mfma_f32_16x16x32_bf16 v[76:79], a[116:119], v[116:119], v[76:79]
v_mfma_f32_16x16x32_bf16 v[76:79], a[128:131], v[120:123], v[76:79]
v_mfma_f32_16x16x32_bf16 v[76:79], a[140:143], v[124:127], v[76:79]
s_waitcnt lgkmcnt(5)
v_mfma_f32_16x16x32_bf16 v[68:71], a[144:147], v[80:83], v[68:71]
s_waitcnt lgkmcnt(4)
v_mfma_f32_16x16x32_bf16 v[68:71], a[156:159], v[84:87], v[68:71]
s_waitcnt lgkmcnt(3)
v_mfma_f32_16x16x32_bf16 v[68:71], a[168:171], v[88:91], v[68:71]
.Ldn_in11:
s_waitcnt lgkmcnt(2)
v_mfma_f32_16x16x32_bf16 v[68:71], a[180:183], v[92:95], v[68:71]
s_waitcnt lgkmcnt(1)
v_mfma_f32_16x16x32_bf16 v[68:71], a[192:195], v[96:99], v[68:71]
s_waitcnt lgkmcnt(0)
ds_read_b128 v[104:107], v17 offset:43008
v_mfma_f32_16x16x32_bf16 v[68:71], a[204:207], v[100:103], v[68:71]
s_add_u32 m0, s45, 24576
v_mfma_f32_16x16x32_bf16 v[72:75], a[148:151], v[80:83], v[72:75]
ds_read_b128 v[108:111], v17 offset:44032
ds_read_b128 v[112:115], v17 offset:45056
v_mfma_f32_16x16x32_bf16 v[72:75], a[160:163], v[84:87], v[72:75]
v_mfma_f32_16x16x32_bf16 v[72:75], a[172:175], v[88:91], v[72:75]
s_barrier
v_mfma_f32_16x16x32_bf16 v[72:75], a[184:187], v[92:95], v[72:75]
v_mfma_f32_16x16x32_bf16 v[72:75], a[196:199], v[96:99], v[72:75]
ds_read_b128 v[116:119], v17 offset:46080
v_mfma_f32_16x16x32_bf16 v[72:75], a[208:211], v[100:103], v[72:75]
global_load_lds_dwordx4 v9, s[38:39] offset:-2048 sc1
v_mfma_f32_16x16x32_bf16 v[76:79], a[152:155], v[80:83], v[76:79]
ds_read_b128 v[120:123], v17 offset:47104
v_mfma_f32_16x16x32_bf16 v[76:79], a[164:167], v[84:87], v[76:79]
v_mfma_f32_16x16x32_bf16 v[76:79], a[176:179], v[88:91], v[76:79]
ds_read_b128 v[124:127], v17 offset:48128
global_load_lds_dwordx4 v9, s[38:39] offset:-1024 sc1
v_mfma_f32_16x16x32_bf16 v[76:79], a[188:191], v[92:95], v[76:79]
v_mfma_f32_16x16x32_bf16 v[76:79], a[200:203], v[96:99], v[76:79]
v_mfma_f32_16x16x32_bf16 v[76:79], a[212:215], v[100:103], v[76:79]
s_waitcnt lgkmcnt(5)
global_load_lds_dwordx4 v9, s[38:39] offset:0 sc1
v_mfma_f32_16x16x32_bf16 v[68:71], a[216:219], v[104:107], v[68:71]
s_waitcnt lgkmcnt(4)
v_mfma_f32_16x16x32_bf16 v[68:71], a[228:231], v[108:111], v[68:71]
s_waitcnt lgkmcnt(3)
v_mfma_f32_16x16x32_bf16 v[68:71], a[240:243], v[112:115], v[68:71]
s_waitcnt lgkmcnt(2)
global_load_lds_dwordx4 v9, s[38:39] offset:1024 sc1
v_mfma_f32_16x16x32_bf16 v[68:71], a[252:255], v[116:119], v[68:71]
s_waitcnt lgkmcnt(1)
v_mfma_f32_16x16x32_bf16 v[68:71], v[32:35], v[120:123], v[68:71]
s_waitcnt lgkmcnt(0)
v_mfma_f32_16x16x32_bf16 v[68:71], v[44:47], v[124:127], v[68:71]
v_xor_b32 v17, 0x10000, v17
global_load_lds_dwordx4 v9, s[38:39] offset:2048 sc1
v_mfma_f32_16x16x32_bf16 v[72:75], a[220:223], v[104:107], v[72:75]
v_mfma_f32_16x16x32_bf16 v[72:75], a[232:235], v[108:111], v[72:75]
v_mfma_f32_16x16x32_bf16 v[72:75], a[244:247], v[112:115], v[72:75]
s_waitcnt vmcnt(8)
s_barrier
v_mfma_f32_16x16x32_bf16 v[72:75], v[24:27], v[116:119], v[72:75]
ds_read_b128 v[80:83], v17 offset:0
global_load_lds_dwordx4 v9, s[38:39] offset:3072 sc1
v_mfma_f32_16x16x32_bf16 v[72:75], v[36:39], v[120:123], v[72:75]
ds_read_b128 v[84:87], v17 offset:1024
v_add_u32 v9, 147456, v9
v_mfma_f32_16x16x32_bf16 v[72:75], v[48:51], v[124:127], v[72:75]
ds_read_b128 v[88:91], v17 offset:2048
v_min_u32 v9, v9, v10
v_mfma_f32_16x16x32_bf16 v[76:79], a[224:227], v[104:107], v[76:79]
ds_read_b128 v[92:95], v17 offset:3072
v_cvt_pk_bf16_f32 v128, v68, v69
v_mfma_f32_16x16x32_bf16 v[76:79], a[236:239], v[108:111], v[76:79]
ds_read_b128 v[96:99], v17 offset:4096
v_cvt_pk_bf16_f32 v129, v70, v71
v_mfma_f32_16x16x32_bf16 v[76:79], a[248:251], v[112:115], v[76:79]
ds_read_b128 v[100:103], v17 offset:5120
v_mfma_f32_16x16x32_bf16 v[76:79], v[28:31], v[116:119], v[76:79]
v_cvt_pk_bf16_f32 v130, v72, v73
v_cvt_pk_bf16_f32 v131, v74, v75
v_mfma_f32_16x16x32_bf16 v[76:79], v[40:43], v[120:123], v[76:79]
v_mfma_f32_16x16x32_bf16 v[76:79], v[52:55], v[124:127], v[76:79]
s_sub_u32 s70, s70, 1
s_cbranch_scc0 .Ldn_top12
v_cvt_pk_bf16_f32 v128, v68, v69
v_cvt_pk_bf16_f32 v129, v70, v71
v_cvt_pk_bf16_f32 v130, v72, v73
v_cvt_pk_bf16_f32 v131, v74, v75
v_min_u32 v128, -2, v128
v_min_u32 v129, -2, v129
v_min_u32 v130, -2, v130
v_min_u32 v131, -2, v131
global_store_dwordx4 v11, v[128:131], s[38:39] sc1
v_add_u32 v11, 110592, v11
v_cvt_pk_bf16_f32 v132, v76, v77
v_cvt_pk_bf16_f32 v134, v76, v77
v_cvt_pk_bf16_f32 v133, v78, v79
v_cvt_pk_bf16_f32 v135, v78, v79
v_permlane16_swap_b32 v132, v134
s_nop 0
v_permlane16_swap_b32 v133, v135
v_min_u32 v132, -2, v132
v_min_u32 v133, -2, v133
v_min_u32 v134, -2, v134
v_min_u32 v135, -2, v135
s_mov_b64 exec, s[76:77]
global_store_dwordx4 v12, v[132:135], s[38:39] sc1
s_mov_b64 exec, -1
v_add_u32 v12, 110592, v12
s_endpgm
.Ldn_w4:
v_add_u32 v254, -1, v14
v_add_u32 v255, -1, v14
v_add_lshl_u32 v252, v16, 0, 7
v_bfi_b32 v252, s69, v252, v15
v_add_lshl_u32 v253, v14, 0, 7
v_bfi_b32 v253, s69, v253, v13
s_mov_b32 s76, 0xffff
s_mov_b32 s77, 0xffff
s_add_u32 m0, s45, 24576
v_add_lshl_u32 v253, v14, 0, 7
v_bfi_b32 v253, s69, v253, v13
global_load_dword v254, v253, s[38:39] sc1
s_waitcnt vmcnt(0)
v_sub_u32 v254, v254, v14
v_cmp_lt_u32 vcc, 0x7f, v254
s_cbranch_vccnz .Ldn_sp19
.Ldn_rt20:
v_add_u32 v14, 1, v14
s_xor_b32 s45, s45, 0x10000
s_mov_b32 m0, s45
s_barrier
global_load_lds_dwordx4 v9, s[38:39] offset:-2048 sc1
global_load_lds_dwordx4 v9, s[38:39] offset:-1024 sc1
global_load_lds_dwordx4 v9, s[38:39] offset:0 sc1
global_load_lds_dwordx4 v9, s[38:39] offset:1024 sc1
global_load_lds_dwordx4 v9, s[38:39] offset:2048 sc1
global_load_lds_dwordx4 v9, s[38:39] offset:3072 sc1
v_add_u32 v9, 147456, v9
s_mov_b32 s70, 32
s_waitcnt vmcnt(0) lgkmcnt(0)
s_nop 7
s_branch .Ldn_in18
.p2align 6
.Ldn_top21:
s_waitcnt lgkmcnt(5)
v_mfma_f32_16x16x32_bf16 v[56:59], a[0:3], v[80:83], 0
s_waitcnt lgkmcnt(4)
v_mfma_f32_16x16x32_bf16 v[56:59], a[12:15], v[84:87], v[56:59]
s_waitcnt lgkmcnt(3)
global_load_dword v254, v253, s[38:39] sc1
v_mfma_f32_16x16x32_bf16 v[56:59], a[24:27], v[88:91], v[56:59]
s_waitcnt lgkmcnt(2)
v_mfma_f32_16x16x32_bf16 v[56:59], a[36:39], v[92:95], v[56:59]
s_waitcnt lgkmcnt(1)
ds_read_b128 v[104:107], v17 offset:6144
v_cvt_pk_bf16_f32 v132, v76, v77
v_mfma_f32_16x16x32_bf16 v[56:59], a[48:51], v[96:99], v[56:59]
s_waitcnt lgkmcnt(1)
v_mfma_f32_16x16x32_bf16 v[56:59], a[60:63], v[100:103], v[56:59]
s_waitcnt vmcnt(7)
s_barrier
v_mfma_f32_16x16x32_bf16 v[60:63], a[4:7], v[80:83], 0
ds_read_b128 v[108:111], v17 offset:7168
v_cvt_pk_bf16_f32 v134, v76, v77
v_mfma_f32_16x16x32_bf16 v[60:63], a[16:19], v[84:87], v[60:63]
v_cvt_pk_bf16_f32 v133, v78, v79
v_cvt_pk_bf16_f32 v135, v78, v79
v_mfma_f32_16x16x32_bf16 v[60:63], a[28:31], v[88:91], v[60:63]
ds_read_b128 v[112:115], v17 offset:8192
v_mfma_f32_16x16x32_bf16 v[60:63], a[40:43], v[92:95], v[60:63]
v_permlane16_swap_b32 v132, v134
v_mfma_f32_16x16x32_bf16 v[60:63], a[52:55], v[96:99], v[60:63]
ds_read_b128 v[116:119], v17 offset:9216
v_mfma_f32_16x16x32_bf16 v[60:63], a[64:67], v[100:103], v[60:63]
global_store_dwordx4 v11, v[128:131], s[38:39] sc1
v_mfma_f32_16x16x32_bf16 v[64:67], a[8:11], v[80:83], 0
ds_read_b128 v[120:123], v17 offset:10240
v_add_u32 v11, 110592, v11
v_mfma_f32_16x16x32_bf16 v[64:67], a[20:23], v[84:87], v[64:67]
v_permlane16_swap_b32 v133, v135
v_mfma_f32_16x16x32_bf16 v[64:67], a[32:35], v[88:91], v[64:67]
ds_read_b128 v[124:127], v17 offset:11264
v_mfma_f32_16x16x32_bf16 v[64:67], a[44:47], v[92:95], v[64:67]
s_mov_b64 exec, s[76:77]
global_store_dwordx4 v12, v[132:135], s[38:39] sc1
s_mov_b64 exec, -1
v_mfma_f32_16x16x32_bf16 v[64:67], a[56:59], v[96:99], v[64:67]
v_add_u32 v12, 110592, v12
v_mfma_f32_16x16x32_bf16 v[64:67], a[68:71], v[100:103], v[64:67]
s_waitcnt lgkmcnt(5)
v_mfma_f32_16x16x32_bf16 v[56:59], a[72:75], v[104:107], v[56:59]
s_waitcnt lgkmcnt(4)
v_mfma_f32_16x16x32_bf16 v[56:59], a[84:87], v[108:111], v[56:59]
s_waitcnt lgkmcnt(3)
v_mfma_f32_16x16x32_bf16 v[56:59], a[96:99], v[112:115], v[56:59]
s_waitcnt lgkmcnt(2)
v_mfma_f32_16x16x32_bf16 v[56:59], a[108:111], v[116:119], v[56:59]
s_waitcnt lgkmcnt(1)
v_mfma_f32_16x16x32_bf16 v[56:59], a[120:123], v[120:123], v[56:59]
s_waitcnt lgkmcnt(0)
ds_read_b128 v[80:83], v17 offset:12288
v_mfma_f32_16x16x32_bf16 v[56:59], a[132:135], v[124:127], v[56:59]
v_mfma_f32_16x16x32_bf16 v[60:63], a[76:79], v[104:107], v[60:63]
ds_read_b128 v[84:87], v17 offset:13312
v_mfma_f32_16x16x32_bf16 v[60:63], a[88:91], v[108:111], v[60:63]
v_mfma_f32_16x16x32_bf16 v[60:63], a[100:103], v[112:115], v[60:63]
ds_read_b128 v[88:91], v17 offset:14336
v_mfma_f32_16x16x32_bf16 v[60:63], a[112:115], v[116:119], v[60:63]
v_mfma_f32_16x16x32_bf16 v[60:63], a[124:127], v[120:123], v[60:63]
ds_read_b128 v[92:95], v17 offset:15360
v_mfma_f32_16x16x32_bf16 v[60:63], a[136:139], v[124:127], v[60:63]
v_mfma_f32_16x16x32_bf16 v[64:67], a[80:83], v[104:107], v[64:67]
ds_read_b128 v[96:99], v17 offset:16384
v_mfma_f32_16x16x32_bf16 v[64:67], a[92:95], v[108:111], v[64:67]
v_mfma_f32_16x16x32_bf16 v[64:67], a[104:107], v[112:115], v[64:67]
ds_read_b128 v[100:103], v17 offset:17408
v_mfma_f32_16x16x32_bf16 v[64:67], a[116:119], v[116:119], v[64:67]
v_mfma_f32_16x16x32_bf16 v[64:67], a[128:131], v[120:123], v[64:67]
v_mfma_f32_16x16x32_bf16 v[64:67], a[140:143], v[124:127], v[64:67]
s_waitcnt lgkmcnt(5)
v_mfma_f32_16x16x32_bf16 v[56:59], a[144:147], v[80:83], v[56:59]
s_waitcnt lgkmcnt(4)
v_mfma_f32_16x16x32_bf16 v[56:59], a[156:159], v[84:87], v[56:59]
s_waitcnt lgkmcnt(3)
v_mfma_f32_16x16x32_bf16 v[56:59], a[168:171], v[88:91], v[56:59]
s_waitcnt lgkmcnt(2)
v_mfma_f32_16x16x32_bf16 v[56:59], a[180:183], v[92:95], v[56:59]
s_waitcnt vmcnt(2)
v_sub_u32 v254, v254, v14
v_cmp_lt_u32 vcc, 0x7f, v254
s_waitcnt lgkmcnt(1)
v_mfma_f32_16x16x32_bf16 v[56:59], a[192:195], v[96:99], v[56:59]
s_xor_b32 s45, s45, 0x10000
ds_read_b128 v[104:107], v17 offset:18432
s_waitcnt lgkmcnt(1)
v_mfma_f32_16x16x32_bf16 v[56:59], a[204:207], v[100:103], v[56:59]
s_add_u32 m0, s45, 0
v_mfma_f32_16x16x32_bf16 v[60:63], a[148:151], v[80:83], v[60:63]
s_cbranch_vccnz .Ldn_sp14
.Ldn_rt15:
ds_read_b128 v[108:111], v17 offset:19456
ds_read_b128 v[112:115], v17 offset:20480
v_mfma_f32_16x16x32_bf16 v[60:63], a[160:163], v[84:87], v[60:63]
v_mfma_f32_16x16x32_bf16 v[60:63], a[172:175], v[88:91], v[60:63]
s_barrier
v_mfma_f32_16x16x32_bf16 v[60:63], a[184:187], v[92:95], v[60:63]
v_add_u32 v14, 1, v14
v_add_lshl_u32 v253, v14, 0, 7
v_mfma_f32_16x16x32_bf16 v[60:63], a[196:199], v[96:99], v[60:63]
ds_read_b128 v[116:119], v17 offset:21504
v_bfi_b32 v253, s69, v253, v13
v_mfma_f32_16x16x32_bf16 v[60:63], a[208:211], v[100:103], v[60:63]
v_mfma_f32_16x16x32_bf16 v[64:67], a[152:155], v[80:83], v[64:67]
ds_read_b128 v[120:123], v17 offset:22528
global_load_lds_dwordx4 v9, s[38:39] offset:-2048 sc1
v_mfma_f32_16x16x32_bf16 v[64:67], a[164:167], v[84:87], v[64:67]
v_mfma_f32_16x16x32_bf16 v[64:67], a[176:179], v[88:91], v[64:67]
ds_read_b128 v[124:127], v17 offset:23552
v_mfma_f32_16x16x32_bf16 v[64:67], a[188:191], v[92:95], v[64:67]
global_load_lds_dwordx4 v9, s[38:39] offset:-1024 sc1
v_mfma_f32_16x16x32_bf16 v[64:67], a[200:203], v[96:99], v[64:67]
v_mfma_f32_16x16x32_bf16 v[64:67], a[212:215], v[100:103], v[64:67]
s_waitcnt lgkmcnt(5)
v_mfma_f32_16x16x32_bf16 v[56:59], a[216:219], v[104:107], v[56:59]
s_waitcnt lgkmcnt(4)
global_load_lds_dwordx4 v9, s[38:39] offset:0 sc1
v_mfma_f32_16x16x32_bf16 v[56:59], a[228:231], v[108:111], v[56:59]
s_waitcnt lgkmcnt(3)
v_mfma_f32_16x16x32_bf16 v[56:59], a[240:243], v[112:115], v[56:59]
s_waitcnt lgkmcnt(2)
v_mfma_f32_16x16x32_bf16 v[56:59], a[252:255], v[116:119], v[56:59]
s_waitcnt lgkmcnt(1)
global_load_lds_dwordx4 v9, s[38:39] offset:1024 sc1
v_mfma_f32_16x16x32_bf16 v[56:59], v[32:35], v[120:123], v[56:59]
s_waitcnt lgkmcnt(0)
v_mfma_f32_16x16x32_bf16 v[56:59], v[44:47], v[124:127], v[56:59]
v_mfma_f32_16x16x32_bf16 v[60:63], a[220:223], v[104:107], v[60:63]
global_load_lds_dwordx4 v9, s[38:39] offset:2048 sc1
v_mfma_f32_16x16x32_bf16 v[60:63], a[232:235], v[108:111], v[60:63]
v_mfma_f32_16x16x32_bf16 v[60:63], a[244:247], v[112:115], v[60:63]
s_barrier
v_mfma_f32_16x16x32_bf16 v[60:63], v[24:27], v[116:119], v[60:63]
ds_read_b128 v[80:83], v17 offset:24576
global_load_lds_dwordx4 v9, s[38:39] offset:3072 sc1
v_mfma_f32_16x16x32_bf16 v[60:63], v[36:39], v[120:123], v[60:63]
)"
R"(ds_read_b128 v[84:87], v17 offset:25600
v_add_u32 v9, 147456, v9
v_mfma_f32_16x16x32_bf16 v[60:63], v[48:51], v[124:127], v[60:63]
ds_read_b128 v[88:91], v17 offset:26624
v_min_u32 v9, v9, v10
v_mfma_f32_16x16x32_bf16 v[64:67], a[224:227], v[104:107], v[64:67]
ds_read_b128 v[92:95], v17 offset:27648
v_cvt_pk_bf16_f32 v128, v56, v57
v_mfma_f32_16x16x32_bf16 v[64:67], a[236:239], v[108:111], v[64:67]
ds_read_b128 v[96:99], v17 offset:28672
v_cvt_pk_bf16_f32 v129, v58, v59
v_mfma_f32_16x16x32_bf16 v[64:67], a[248:251], v[112:115], v[64:67]
ds_read_b128 v[100:103], v17 offset:29696
v_mfma_f32_16x16x32_bf16 v[64:67], v[28:31], v[116:119], v[64:67]
v_cvt_pk_bf16_f32 v130, v60, v61
v_cvt_pk_bf16_f32 v131, v62, v63
v_mfma_f32_16x16x32_bf16 v[64:67], v[40:43], v[120:123], v[64:67]
v_min_u32 v128, -2, v128
v_min_u32 v129, -2, v129
v_mfma_f32_16x16x32_bf16 v[64:67], v[52:55], v[124:127], v[64:67]
s_waitcnt lgkmcnt(5)
v_min_u32 v130, -2, v130
v_min_u32 v131, -2, v131
v_mfma_f32_16x16x32_bf16 v[68:71], a[0:3], v[80:83], 0
s_waitcnt lgkmcnt(4)
v_mfma_f32_16x16x32_bf16 v[68:71], a[12:15], v[84:87], v[68:71]
s_waitcnt lgkmcnt(3)
global_load_dword v255, v253, s[38:39] sc1
v_mfma_f32_16x16x32_bf16 v[68:71], a[24:27], v[88:91], v[68:71]
s_waitcnt lgkmcnt(2)
v_mfma_f32_16x16x32_bf16 v[68:71], a[36:39], v[92:95], v[68:71]
s_waitcnt lgkmcnt(1)
ds_read_b128 v[104:107], v17 offset:30720
v_cvt_pk_bf16_f32 v132, v64, v65
v_mfma_f32_16x16x32_bf16 v[68:71], a[48:51], v[96:99], v[68:71]
s_waitcnt lgkmcnt(1)
v_mfma_f32_16x16x32_bf16 v[68:71], a[60:63], v[100:103], v[68:71]
s_waitcnt vmcnt(7)
s_barrier
v_mfma_f32_16x16x32_bf16 v[72:75], a[4:7], v[80:83], 0
ds_read_b128 v[108:111], v17 offset:31744
v_cvt_pk_bf16_f32 v134, v64, v65
v_mfma_f32_16x16x32_bf16 v[72:75], a[16:19], v[84:87], v[72:75]
v_cvt_pk_bf16_f32 v133, v66, v67
v_cvt_pk_bf16_f32 v135, v66, v67
v_mfma_f32_16x16x32_bf16 v[72:75], a[28:31], v[88:91], v[72:75]
ds_read_b128 v[112:115], v17 offset:32768
v_mfma_f32_16x16x32_bf16 v[72:75], a[40:43], v[92:95], v[72:75]
v_permlane16_swap_b32 v132, v134
v_mfma_f32_16x16x32_bf16 v[72:75], a[52:55], v[96:99], v[72:75]
ds_read_b128 v[116:119], v17 offset:33792
v_mfma_f32_16x16x32_bf16 v[72:75], a[64:67], v[100:103], v[72:75]
global_store_dwordx4 v11, v[128:131], s[38:39] sc1
v_mfma_f32_16x16x32_bf16 v[76:79], a[8:11], v[80:83], 0
ds_read_b128 v[120:123], v17 offset:34816
v_add_u32 v11, 110592, v11
v_mfma_f32_16x16x32_bf16 v[76:79], a[20:23], v[84:87], v[76:79]
v_permlane16_swap_b32 v133, v135
v_mfma_f32_16x16x32_bf16 v[76:79], a[32:35], v[88:91], v[76:79]
ds_read_b128 v[124:127], v17 offset:35840
v_min_u32 v132, -2, v132
v_mfma_f32_16x16x32_bf16 v[76:79], a[44:47], v[92:95], v[76:79]
v_min_u32 v133, -2, v133
v_min_u32 v134, -2, v134
v_mfma_f32_16x16x32_bf16 v[76:79], a[56:59], v[96:99], v[76:79]
v_min_u32 v135, -2, v135
v_mfma_f32_16x16x32_bf16 v[76:79], a[68:71], v[100:103], v[76:79]
s_waitcnt lgkmcnt(5)
s_mov_b64 exec, s[76:77]
global_store_dwordx4 v12, v[132:135], s[38:39] sc1
s_mov_b64 exec, -1
v_mfma_f32_16x16x32_bf16 v[68:71], a[72:75], v[104:107], v[68:71]
s_waitcnt lgkmcnt(4)
v_add_u32 v12, 110592, v12
v_mfma_f32_16x16x32_bf16 v[68:71], a[84:87], v[108:111], v[68:71]
s_waitcnt lgkmcnt(3)
v_mfma_f32_16x16x32_bf16 v[68:71], a[96:99], v[112:115], v[68:71]
s_waitcnt lgkmcnt(2)
v_mfma_f32_16x16x32_bf16 v[68:71], a[108:111], v[116:119], v[68:71]
s_waitcnt lgkmcnt(1)
v_mfma_f32_16x16x32_bf16 v[68:71], a[120:123], v[120:123], v[68:71]
s_waitcnt lgkmcnt(0)
ds_read_b128 v[80:83], v17 offset:36864
v_mfma_f32_16x16x32_bf16 v[68:71], a[132:135], v[124:127], v[68:71]
v_mfma_f32_16x16x32_bf16 v[72:75], a[76:79], v[104:107], v[72:75]
ds_read_b128 v[84:87], v17 offset:37888
v_mfma_f32_16x16x32_bf16 v[72:75], a[88:91], v[108:111], v[72:75]
v_mfma_f32_16x16x32_bf16 v[72:75], a[100:103], v[112:115], v[72:75]
ds_read_b128 v[88:91], v17 offset:38912
v_mfma_f32_16x16x32_bf16 v[72:75], a[112:115], v[116:119], v[72:75]
v_mfma_f32_16x16x32_bf16 v[72:75], a[124:127], v[120:123], v[72:75]
ds_read_b128 v[92:95], v17 offset:39936
v_mfma_f32_16x16x32_bf16 v[72:75], a[136:139], v[124:127], v[72:75]
v_mfma_f32_16x16x32_bf16 v[76:79], a[80:83], v[104:107], v[76:79]
ds_read_b128 v[96:99], v17 offset:40960
v_mfma_f32_16x16x32_bf16 v[76:79], a[92:95], v[108:111], v[76:79]
v_mfma_f32_16x16x32_bf16 v[76:79], a[104:107], v[112:115], v[76:79]
ds_read_b128 v[100:103], v17 offset:41984
v_mfma_f32_16x16x32_bf16 v[76:79], a[116:119], v[116:119], v[76:79]
v_mfma_f32_16x16x32_bf16 v[76:79], a[128:131], v[120:123], v[76:79]
v_mfma_f32_16x16x32_bf16 v[76:79], a[140:143], v[124:127], v[76:79]
s_waitcnt lgkmcnt(5)
v_mfma_f32_16x16x32_bf16 v[68:71], a[144:147], v[80:83], v[68:71]
s_waitcnt lgkmcnt(4)
v_mfma_f32_16x16x32_bf16 v[68:71], a[156:159], v[84:87], v[68:71]
s_waitcnt lgkmcnt(3)
v_mfma_f32_16x16x32_bf16 v[68:71], a[168:171], v[88:91], v[68:71]
.Ldn_in18:
s_waitcnt lgkmcnt(2)
v_mfma_f32_16x16x32_bf16 v[68:71], a[180:183], v[92:95], v[68:71]
s_waitcnt vmcnt(2)
v_sub_u32 v255, v255, v14
v_cmp_lt_u32 vcc, 0x7f, v255
s_waitcnt lgkmcnt(1)
v_mfma_f32_16x16x32_bf16 v[68:71], a[192:195], v[96:99], v[68:71]
s_waitcnt lgkmcnt(0)
ds_read_b128 v[104:107], v17 offset:43008
v_mfma_f32_16x16x32_bf16 v[68:71], a[204:207], v[100:103], v[68:71]
s_add_u32 m0, s45, 24576
v_mfma_f32_16x16x32_bf16 v[72:75], a[148:151], v[80:83], v[72:75]
s_cbranch_vccnz .Ldn_sp16
.Ldn_rt17:
ds_read_b128 v[108:111], v17 offset:44032
ds_read_b128 v[112:115], v17 offset:45056
v_mfma_f32_16x16x32_bf16 v[72:75], a[160:163], v[84:87], v[72:75]
v_mfma_f32_16x16x32_bf16 v[72:75], a[172:175], v[88:91], v[72:75]
s_barrier
v_mfma_f32_16x16x32_bf16 v[72:75], a[184:187], v[92:95], v[72:75]
v_add_u32 v14, 1, v14
v_add_lshl_u32 v253, v14, 0, 7
v_mfma_f32_16x16x32_bf16 v[72:75], a[196:199], v[96:99], v[72:75]
ds_read_b128 v[116:119], v17 offset:46080
v_bfi_b32 v253, s69, v253, v13
v_mfma_f32_16x16x32_bf16 v[72:75], a[208:211], v[100:103], v[72:75]
v_mfma_f32_16x16x32_bf16 v[76:79], a[152:155], v[80:83], v[76:79]
ds_read_b128 v[120:123], v17 offset:47104
global_load_lds_dwordx4 v9, s[38:39] offset:-2048 sc1
v_mfma_f32_16x16x32_bf16 v[76:79], a[164:167], v[84:87], v[76:79]
v_mfma_f32_16x16x32_bf16 v[76:79], a[176:179], v[88:91], v[76:79]
ds_read_b128 v[124:127], v17 offset:48128
v_mfma_f32_16x16x32_bf16 v[76:79], a[188:191], v[92:95], v[76:79]
global_load_lds_dwordx4 v9, s[38:39] offset:-1024 sc1
v_mfma_f32_16x16x32_bf16 v[76:79], a[200:203], v[96:99], v[76:79]
v_mfma_f32_16x16x32_bf16 v[76:79], a[212:215], v[100:103], v[76:79]
s_waitcnt lgkmcnt(5)
v_mfma_f32_16x16x32_bf16 v[68:71], a[216:219], v[104:107], v[68:71]
s_waitcnt lgkmcnt(4)
global_load_lds_dwordx4 v9, s[38:39] offset:0 sc1
v_mfma_f32_16x16x32_bf16 v[68:71], a[228:231], v[108:111], v[68:71]
s_waitcnt lgkmcnt(3)
v_mfma_f32_16x16x32_bf16 v[68:71], a[240:243], v[112:115], v[68:71]
s_waitcnt lgkmcnt(2)
v_mfma_f32_16x16x32_bf16 v[68:71], a[252:255], v[116:119], v[68:71]
s_waitcnt lgkmcnt(1)
global_load_lds_dwordx4 v9, s[38:39] offset:1024 sc1
v_mfma_f32_16x16x32_bf16 v[68:71], v[32:35], v[120:123], v[68:71]
s_waitcnt lgkmcnt(0)
v_mfma_f32_16x16x32_bf16 v[68:71], v[44:47], v[124:127], v[68:71]
v_xor_b32 v17, 0x10000, v17
v_mfma_f32_16x16x32_bf16 v[72:75], a[220:223], v[104:107], v[72:75]
global_load_lds_dwordx4 v9, s[38:39] offset:2048 sc1
v_mfma_f32_16x16x32_bf16 v[72:75], a[232:235], v[108:111], v[72:75]
v_mfma_f32_16x16x32_bf16 v[72:75], a[244:247], v[112:115], v[72:75]
s_barrier
v_mfma_f32_16x16x32_bf16 v[72:75], v[24:27], v[116:119], v[72:75]
ds_read_b128 v[80:83], v17 offset:0
global_load_lds_dwordx4 v9, s[38:39] offset:3072 sc1
v_mfma_f32_16x16x32_bf16 v[72:75], v[36:39], v[120:123], v[72:75]
ds_read_b128 v[84:87], v17 offset:1024
v_add_u32 v9, 147456, v9
v_mfma_f32_16x16x32_bf16 v[72:75], v[48:51], v[124:127], v[72:75]
ds_read_b128 v[88:91], v17 offset:2048
v_min_u32 v9, v9, v10
v_mfma_f32_16x16x32_bf16 v[76:79], a[224:227], v[104:107], v[76:79]
ds_read_b128 v[92:95], v17 offset:3072
v_cvt_pk_bf16_f32 v128, v68, v69
v_mfma_f32_16x16x32_bf16 v[76:79], a[236:239], v[108:111], v[76:79]
ds_read_b128 v[96:99], v17 offset:4096
v_cvt_pk_bf16_f32 v129, v70, v71
v_mfma_f32_16x16x32_bf16 v[76:79], a[248:251], v[112:115], v[76:79]
ds_read_b128 v[100:103], v17 offset:5120
v_mfma_f32_16x16x32_bf16 v[76:79], v[28:31], v[116:119], v[76:79]
v_cvt_pk_bf16_f32 v130, v72, v73
v_cvt_pk_bf16_f32 v131, v74, v75
v_mfma_f32_16x16x32_bf16 v[76:79], v[40:43], v[120:123], v[76:79]
v_mfma_f32_16x16x32_bf16 v[76:79], v[52:55], v[124:127], v[76:79]
s_sub_u32 s70, s70, 1
s_cbranch_scc0 .Ldn_top21
v_cvt_pk_bf16_f32 v128, v68, v69
v_cvt_pk_bf16_f32 v129, v70, v71
v_cvt_pk_bf16_f32 v130, v72, v73
v_cvt_pk_bf16_f32 v131, v74, v75
v_min_u32 v128, -2, v128
v_min_u32 v129, -2, v129
v_min_u32 v130, -2, v130
v_min_u32 v131, -2, v131
global_store_dwordx4 v11, v[128:131], s[38:39] sc1
v_add_u32 v11, 110592, v11
v_cvt_pk_bf16_f32 v132, v76, v77
v_cvt_pk_bf16_f32 v134, v76, v77
v_cvt_pk_bf16_f32 v133, v78, v79
v_cvt_pk_bf16_f32 v135, v78, v79
v_permlane16_swap_b32 v132, v134
s_nop 0
v_permlane16_swap_b32 v133, v135
v_min_u32 v132, -2, v132
v_min_u32 v133, -2, v133
v_min_u32 v134, -2, v134
v_min_u32 v135, -2, v135
s_mov_b64 exec, s[76:77]
global_store_dwordx4 v12, v[132:135], s[38:39] sc1
s_mov_b64 exec, -1
v_add_u32 v12, 110592, v12
s_endpgm
.Ldn_sp14:
s_sleep 1
v_add_lshl_u32 v253, v14, 0, 7
v_bfi_b32 v253, s69, v253, v13
global_load_dword v254, v253, s[38:39] sc1
s_add_u32 s71, s71, 1
s_waitcnt vmcnt(0)
v_sub_u32 v254, v254, v14
v_cmp_lt_u32 vcc, 0x7f, v254
s_cbranch_vccz .Ldn_rt15
s_cmp_lt_u32 s71, 3000000
s_cbranch_scc1 .Ldn_sp14
s_branch .Ldn_rt15
.Ldn_sp16:
s_sleep 1
v_add_lshl_u32 v253, v14, 0, 7
v_bfi_b32 v253, s69, v253, v13
global_load_dword v255, v253, s[38:39] sc1
s_add_u32 s71, s71, 1
s_waitcnt vmcnt(0)
v_sub_u32 v255, v255, v14
v_cmp_lt_u32 vcc, 0x7f, v255
s_cbranch_vccz .Ldn_rt17
s_cmp_lt_u32 s71, 3000000
s_cbranch_scc1 .Ldn_sp16
s_branch .Ldn_rt17
.Ldn_sp19:
s_sleep 1
v_add_lshl_u32 v253, v14, 0, 7
v_bfi_b32 v253, s69, v253, v13
global_load_dword v254, v253, s[38:39] sc1
s_add_u32 s71, s71, 1
s_waitcnt vmcnt(0)
v_sub_u32 v254, v254, v14
v_cmp_lt_u32 vcc, 0x7f, v254
s_cbranch_vccz .Ldn_rt20
s_cmp_lt_u32 s71, 3000000
s_cbranch_scc1 .Ldn_sp19
s_branch .Ldn_rt20
.Le2:
)"
    :: "{v[0:31]}"(va), "{s[36:51]}"(sa) : "v255", "a255", "s99", "vcc", "memory");
  } else {
    const unsigned gu_i = xh * 36 + (ev ? jj + 4 : jj - 12);
    const unsigned r = lane & 15, g = lane >> 4, q = r >> 2, p = lane & 3;
    const unsigned ii = gu_i, gam = (ii % 72) >> 1, rho = ii / 72 * 2 + (ii & 1), row0 = rho * RP + PAD, pc0 = wave * 6;
    const unsigned bk = gam >> 2, qd = gam & 3, od = (lane >> 3) & 1, un = g * 8 + (lane & 7), jr = qd * 4 + p, jc = (un >> 2) * 8 + od * 4;
    u32x32 va; u32x16 sa, sb;
    va[0] = g * 9216 + ((r ^ (g << 2)) << 4) + 4096 + (((r ^ (g << 2)) >> 3) & 1) * 640;
    va[1] = (8 * g + q) * 256 + (((4 * wave + p) ^ (q << 2)) << 4);
    va[2] = WS_X1 + row0 * X1P + pc0 * 1024 + 2048 + lane * 16;
    va[3] = WS_H + (row0 - 1) * HT + (gam / 6 * 24 + (gam % 6) * 2 + (wave >> 1) * 12 + (wave & 1)) * 1024 + lane * 16;
    va[4] = WS_FL + rho * FR + FX1 + (lane & 1) * FL;
    va[5] = 0u;
    va[6] = WS_FL + rho * FR + FH + gam * FL;
    va[7] = 0u - 2;
    va[8] = lane * 16;
    va[9] = WS_X1 + (row0 + 63) * X1P + pc0 * 1024 + 2048 + lane * 16;
    va[10] = WS_P + row0 * PT + gam * 3072 + od * 512 + un * 16;
    va[11] = WS_X1 + row0 * X1P + bk * 2048 + (un >> 2) * 256 + jr * 16 + od * 8;
    va[12] = WS_X1 + row0 * X1P + XT + jr * 8;
    va[13] = ((rho * 32 + 31) * 16 + qd * 4 + g) * 1152 + bk * 128 + r * 8;
    va[14] = 49152 + wave * 1024 + p * 144 + jc * 2;
    va[15] = 49152 + wave * 1024 + g * 144 + r * 8;
    va[16] = WS_FL + rho * FR + FP + (bk / 3 * 6 + min(lane, 5u)) * FL;
    va[17] = 0u;
    va[18] = WS_FL + rho * FR + FX2 + gam * FL;
    va[19] = 0u;
    va[20] = 0u;
    va[21] = 0u;
    va[22] = (g == q && p < 2) ? (0x3F80u << (16 * p)) : 0u;
    va[23] = (g == q && p >= 2) ? (0x3F80u << (16 * (p - 2))) : 0u;
    va[24] = 0u;
    va[25] = 0u;
    sa[0] = 12 + wave; sa[2] = (unsigned)(unsigned long long)ws; sa[3] = (unsigned)((unsigned long long)ws >> 32);
    sa[4] = uni((unsigned)(unsigned long long)(wg + gam / 6 * 1536 + (gam % 6) * 128 + wave * (16 * 9216)));
    sa[5] = uni((unsigned)((unsigned long long)(wg + gam / 6 * 1536 + (gam % 6) * 128 + wave * (16 * 9216)) >> 32));
    sa[6] = uni((unsigned)(unsigned long long)(wu + gam / 6 * 1536 + (gam % 6) * 128 + wave * (16 * 9216)));
    sa[7] = uni((unsigned)((unsigned long long)(wu + gam / 6 * 1536 + (gam % 6) * 128 + wave * (16 * 9216)) >> 32));
    sa[8] = uni(wave * 4096);
    sa[9] = uni(pc0 * 1024 + 2048);
    sa[10] = uni((unsigned)(unsigned long long)(ws + WS_X2));
    sa[11] = uni((unsigned)((unsigned long long)(ws + WS_X2) >> 32));
    sa[12] = uni((unsigned)(unsigned long long)(out + 32 * XT));
    sa[13] = uni((unsigned)((unsigned long long)(out + 32 * XT) >> 32));
    sa[14] = uni((unsigned)(unsigned long long)(ws + WS_XD));
    sa[15] = uni((unsigned)((unsigned long long)(ws + WS_XD) >> 32));
    sb[0] = uni((unsigned)(unsigned long long)(ws - PT));
    sb[1] = uni((unsigned)((unsigned long long)(ws - PT) >> 32));
    sb[2] = uni((unsigned)(unsigned long long)(ws - X1P));
    sb[3] = uni((unsigned)((unsigned long long)(ws - X1P) >> 32));
    sb[4] = uni((unsigned)(unsigned long long)(ws + WS_X2));
    sb[5] = uni((unsigned)((unsigned long long)(ws + WS_X2) >> 32));
    sb[6] = uni((unsigned)(unsigned long long)(ws + WS_XD));
    sb[7] = uni((unsigned)((unsigned long long)(ws + WS_XD) >> 32));
    sb[8] = uni((unsigned)(unsigned long long)(ws + WS_XD));
    sb[9] = uni((unsigned)((unsigned long long)(ws + WS_XD) >> 32));
    sb[10] = uni(0u);
    asm volatile(
R"(.Lb3:
s_movk_i32 s69, 0x380
s_mov_b32 s71, 0
s_mov_b32 s74, 0
s_load_dword s37, s[38:39], 0x0
s_nop 1
v_add_u32 v166, 35840, v0
v_add_u32 v167, 71680, v0
v_add_u32 v168, 107520, v0
s_sleep 20
s_add_u32 m0, s44, 4096
s_nop 0
global_load_lds_dwordx4 v0, s[40:41] offset:-4096 nt
global_load_lds_dwordx4 v166, s[40:41] offset:-3072 nt
global_load_lds_dwordx4 v167, s[40:41] offset:-2048 nt
global_load_lds_dwordx4 v168, s[40:41] offset:-1024 nt
s_add_u32 s40, s40, 589824
s_addc_u32 s41, s41, 0
s_add_u32 m0, s44, 20480
s_nop 0
global_load_lds_dwordx4 v0, s[40:41] offset:-4096 nt
global_load_lds_dwordx4 v166, s[40:41] offset:-3072 nt
global_load_lds_dwordx4 v167, s[40:41] offset:-2048 nt
global_load_lds_dwordx4 v168, s[40:41] offset:-1024 nt
s_add_u32 s40, s40, 589824
s_addc_u32 s41, s41, 0
s_add_u32 m0, s44, 36864
s_nop 0
global_load_lds_dwordx4 v0, s[40:41] offset:-4096 nt
global_load_lds_dwordx4 v166, s[40:41] offset:-3072 nt
global_load_lds_dwordx4 v167, s[40:41] offset:-2048 nt
global_load_lds_dwordx4 v168, s[40:41] offset:-1024 nt
s_add_u32 s40, s40, 589824
s_addc_u32 s41, s41, 0
v_add_u32 v169, 0x10000, v1
v_mov_b32 v186, 0x3F803F80
v_mov_b32 v187, 0x3F803F80
v_mov_b32 v188, 0x3F803F80
v_mov_b32 v189, 0x3F803F80
s_waitcnt lgkmcnt(0)
s_mul_i32 s37, s37, 0x9E3779B1
s_add_u32 s37, s37, 0x7F4A7C15
v_add_u32 v5, s37, v5
v_add_u32 v7, s37, v7
v_add_u32 v17, s37, v17
s_waitcnt vmcnt(8)
s_barrier
s_add_u32 m0, s44, 53248
ds_read_b64_tr_b16 a[0:1], v1 offset:0
ds_read_b64_tr_b16 a[2:3], v1 offset:1024
global_load_lds_dwordx4 v0, s[40:41] offset:-4096 nt
ds_read_b64_tr_b16 a[4:5], v1 offset:8
global_load_lds_dwordx4 v166, s[40:41] offset:-3072 nt
ds_read_b64_tr_b16 a[6:7], v1 offset:1032
global_load_lds_dwordx4 v167, s[40:41] offset:-2048 nt
ds_read_b64_tr_b16 a[8:9], v1 offset:8192
global_load_lds_dwordx4 v168, s[40:41] offset:-1024 nt
ds_read_b64_tr_b16 a[10:11], v1 offset:9216
s_add_u32 s40, s40, 589824
ds_read_b64_tr_b16 a[12:13], v1 offset:8200
s_addc_u32 s41, s41, 0
ds_read_b64_tr_b16 a[14:15], v1 offset:9224
s_waitcnt vmcnt(8) lgkmcnt(0)
s_barrier
s_add_u32 m0, s44, 4096
ds_read_b64_tr_b16 a[16:17], v1 offset:16384
ds_read_b64_tr_b16 a[18:19], v1 offset:17408
global_load_lds_dwordx4 v0, s[40:41] offset:-4096 nt
ds_read_b64_tr_b16 a[20:21], v1 offset:16392
ds_read_b64_tr_b16 a[22:23], v1 offset:17416
global_load_lds_dwordx4 v166, s[40:41] offset:-3072 nt
ds_read_b64_tr_b16 a[24:25], v1 offset:24576
ds_read_b64_tr_b16 a[26:27], v1 offset:25600
global_load_lds_dwordx4 v167, s[40:41] offset:-2048 nt
ds_read_b64_tr_b16 a[28:29], v1 offset:24584
global_load_lds_dwordx4 v168, s[40:41] offset:-1024 nt
ds_read_b64_tr_b16 a[30:31], v1 offset:25608
v_mfma_f32_16x16x32_bf16 v[170:173], v[186:189], a[0:3], 0
s_add_u32 s40, s40, 589824
v_mfma_f32_16x16x32_bf16 v[174:177], v[186:189], a[4:7], 0
v_mfma_f32_16x16x32_bf16 v[170:173], v[186:189], a[8:11], v[170:173]
s_addc_u32 s41, s41, 0
v_mfma_f32_16x16x32_bf16 v[174:177], v[186:189], a[12:15], v[174:177]
s_waitcnt vmcnt(8) lgkmcnt(0)
s_barrier
s_add_u32 m0, s44, 20480
ds_read_b64_tr_b16 a[32:33], v1 offset:32768
ds_read_b64_tr_b16 a[34:35], v1 offset:33792
global_load_lds_dwordx4 v0, s[40:41] offset:-4096 nt
ds_read_b64_tr_b16 a[36:37], v1 offset:32776
ds_read_b64_tr_b16 a[38:39], v1 offset:33800
global_load_lds_dwordx4 v166, s[40:41] offset:-3072 nt
ds_read_b64_tr_b16 a[40:41], v1 offset:40960
ds_read_b64_tr_b16 a[42:43], v1 offset:41984
global_load_lds_dwordx4 v167, s[40:41] offset:-2048 nt
ds_read_b64_tr_b16 a[44:45], v1 offset:40968
global_load_lds_dwordx4 v168, s[40:41] offset:-1024 nt
ds_read_b64_tr_b16 a[46:47], v1 offset:41992
v_mfma_f32_16x16x32_bf16 v[170:173], v[186:189], a[16:19], v[170:173]
s_add_u32 s40, s40, 589824
v_mfma_f32_16x16x32_bf16 v[174:177], v[186:189], a[20:23], v[174:177]
v_mfma_f32_16x16x32_bf16 v[170:173], v[186:189], a[24:27], v[170:173]
s_addc_u32 s41, s41, 0
v_mfma_f32_16x16x32_bf16 v[174:177], v[186:189], a[28:31], v[174:177]
s_waitcnt vmcnt(8) lgkmcnt(0)
s_barrier
s_add_u32 m0, s44, 36864
ds_read_b64_tr_b16 a[48:49], v1 offset:49152
ds_read_b64_tr_b16 a[50:51], v1 offset:50176
global_load_lds_dwordx4 v0, s[40:41] offset:-4096 nt
ds_read_b64_tr_b16 a[52:53], v1 offset:49160
ds_read_b64_tr_b16 a[54:55], v1 offset:50184
global_load_lds_dwordx4 v166, s[40:41] offset:-3072 nt
ds_read_b64_tr_b16 a[56:57], v1 offset:57344
ds_read_b64_tr_b16 a[58:59], v1 offset:58368
global_load_lds_dwordx4 v167, s[40:41] offset:-2048 nt
ds_read_b64_tr_b16 a[60:61], v1 offset:57352
global_load_lds_dwordx4 v168, s[40:41] offset:-1024 nt
ds_read_b64_tr_b16 a[62:63], v1 offset:58376
v_mfma_f32_16x16x32_bf16 v[170:173], v[186:189], a[32:35], v[170:173]
s_add_u32 s40, s40, 589824
v_mfma_f32_16x16x32_bf16 v[174:177], v[186:189], a[36:39], v[174:177]
v_mfma_f32_16x16x32_bf16 v[170:173], v[186:189], a[40:43], v[170:173]
s_addc_u32 s41, s41, 0
v_mfma_f32_16x16x32_bf16 v[174:177], v[186:189], a[44:47], v[174:177]
s_waitcnt vmcnt(8) lgkmcnt(0)
s_barrier
s_add_u32 m0, s44, 53248
ds_read_b64_tr_b16 a[64:65], v1 offset:0
ds_read_b64_tr_b16 a[66:67], v1 offset:1024
global_load_lds_dwordx4 v0, s[40:41] offset:-4096 nt
ds_read_b64_tr_b16 a[68:69], v1 offset:8
ds_read_b64_tr_b16 a[70:71], v1 offset:1032
global_load_lds_dwordx4 v166, s[40:41] offset:-3072 nt
ds_read_b64_tr_b16 a[72:73], v1 offset:8192
ds_read_b64_tr_b16 a[74:75], v1 offset:9216
global_load_lds_dwordx4 v167, s[40:41] offset:-2048 nt
ds_read_b64_tr_b16 a[76:77], v1 offset:8200
global_load_lds_dwordx4 v168, s[40:41] offset:-1024 nt
ds_read_b64_tr_b16 a[78:79], v1 offset:9224
v_mfma_f32_16x16x32_bf16 v[170:173], v[186:189], a[48:51], v[170:173]
s_add_u32 s40, s40, 589824
v_mfma_f32_16x16x32_bf16 v[174:177], v[186:189], a[52:55], v[174:177]
v_mfma_f32_16x16x32_bf16 v[170:173], v[186:189], a[56:59], v[170:173]
s_addc_u32 s41, s41, 0
v_mfma_f32_16x16x32_bf16 v[174:177], v[186:189], a[60:63], v[174:177]
s_waitcnt vmcnt(8) lgkmcnt(0)
s_barrier
s_add_u32 m0, s44, 4096
ds_read_b64_tr_b16 a[80:81], v1 offset:16384
ds_read_b64_tr_b16 a[82:83], v1 offset:17408
global_load_lds_dwordx4 v0, s[40:41] offset:-4096 nt
ds_read_b64_tr_b16 a[84:85], v1 offset:16392
ds_read_b64_tr_b16 a[86:87], v1 offset:17416
global_load_lds_dwordx4 v166, s[40:41] offset:-3072 nt
ds_read_b64_tr_b16 a[88:89], v1 offset:24576
ds_read_b64_tr_b16 a[90:91], v1 offset:25600
global_load_lds_dwordx4 v167, s[40:41] offset:-2048 nt
ds_read_b64_tr_b16 a[92:93], v1 offset:24584
ds_read_b64_tr_b16 a[94:95], v1 offset:25608
global_load_lds_dwordx4 v168, s[40:41] offset:-1024 nt
v_mfma_f32_16x16x32_bf16 v[170:173], v[186:189], a[64:67], v[170:173]
v_mfma_f32_16x16x32_bf16 v[174:177], v[186:189], a[68:71], v[174:177]
s_mov_b64 s[40:41], s[42:43]
v_mfma_f32_16x16x32_bf16 v[170:173], v[186:189], a[72:75], v[170:173]
v_mfma_f32_16x16x32_bf16 v[174:177], v[186:189], a[76:79], v[174:177]
s_waitcnt vmcnt(8) lgkmcnt(0)
s_barrier
s_add_u32 m0, s44, 20480
ds_read_b64_tr_b16 a[96:97], v1 offset:32768
ds_read_b64_tr_b16 a[98:99], v1 offset:33792
global_load_lds_dwordx4 v0, s[40:41] offset:-4096 nt
ds_read_b64_tr_b16 a[100:101], v1 offset:32776
ds_read_b64_tr_b16 a[102:103], v1 offset:33800
global_load_lds_dwordx4 v166, s[40:41] offset:-3072 nt
ds_read_b64_tr_b16 a[104:105], v1 offset:40960
ds_read_b64_tr_b16 a[106:107], v1 offset:41984
global_load_lds_dwordx4 v167, s[40:41] offset:-2048 nt
ds_read_b64_tr_b16 a[108:109], v1 offset:40968
global_load_lds_dwordx4 v168, s[40:41] offset:-1024 nt
ds_read_b64_tr_b16 a[110:111], v1 offset:41992
v_mfma_f32_16x16x32_bf16 v[170:173], v[186:189], a[80:83], v[170:173]
s_add_u32 s40, s40, 589824
v_mfma_f32_16x16x32_bf16 v[174:177], v[186:189], a[84:87], v[174:177]
v_mfma_f32_16x16x32_bf16 v[170:173], v[186:189], a[88:91], v[170:173]
s_addc_u32 s41, s41, 0
v_mfma_f32_16x16x32_bf16 v[174:177], v[186:189], a[92:95], v[174:177]
s_waitcnt vmcnt(8) lgkmcnt(0)
s_barrier
s_add_u32 m0, s44, 36864
ds_read_b64_tr_b16 a[112:113], v1 offset:49152
ds_read_b64_tr_b16 a[114:115], v1 offset:50176
global_load_lds_dwordx4 v0, s[40:41] offset:-4096 nt
ds_read_b64_tr_b16 a[116:117], v1 offset:49160
ds_read_b64_tr_b16 a[118:119], v1 offset:50184
global_load_lds_dwordx4 v166, s[40:41] offset:-3072 nt
ds_read_b64_tr_b16 a[120:121], v1 offset:57344
ds_read_b64_tr_b16 a[122:123], v1 offset:58368
global_load_lds_dwordx4 v167, s[40:41] offset:-2048 nt
ds_read_b64_tr_b16 a[124:125], v1 offset:57352
global_load_lds_dwordx4 v168, s[40:41] offset:-1024 nt
ds_read_b64_tr_b16 a[126:127], v1 offset:58376
v_mfma_f32_16x16x32_bf16 v[170:173], v[186:189], a[96:99], v[170:173]
s_add_u32 s40, s40, 589824
v_mfma_f32_16x16x32_bf16 v[174:177], v[186:189], a[100:103], v[174:177]
v_mfma_f32_16x16x32_bf16 v[170:173], v[186:189], a[104:107], v[170:173]
s_addc_u32 s41, s41, 0
v_mfma_f32_16x16x32_bf16 v[174:177], v[186:189], a[108:111], v[174:177]
s_waitcnt vmcnt(8) lgkmcnt(0)
s_barrier
s_add_u32 m0, s44, 53248
ds_read_b64_tr_b16 a[128:129], v1 offset:0
ds_read_b64_tr_b16 a[130:131], v1 offset:1024
global_load_lds_dwordx4 v0, s[40:41] offset:-4096 nt
ds_read_b64_tr_b16 a[132:133], v1 offset:8
ds_read_b64_tr_b16 a[134:135], v1 offset:1032
global_load_lds_dwordx4 v166, s[40:41] offset:-3072 nt
ds_read_b64_tr_b16 a[136:137], v1 offset:8192
ds_read_b64_tr_b16 a[138:139], v1 offset:9216
global_load_lds_dwordx4 v167, s[40:41] offset:-2048 nt
ds_read_b64_tr_b16 a[140:141], v1 offset:8200
global_load_lds_dwordx4 v168, s[40:41] offset:-1024 nt
ds_read_b64_tr_b16 a[142:143], v1 offset:9224
v_mfma_f32_16x16x32_bf16 v[170:173], v[186:189], a[112:115], v[170:173]
s_add_u32 s40, s40, 589824
v_mfma_f32_16x16x32_bf16 v[174:177], v[186:189], a[116:119], v[174:177]
v_mfma_f32_16x16x32_bf16 v[170:173], v[186:189], a[120:123], v[170:173]
s_addc_u32 s41, s41, 0
v_mfma_f32_16x16x32_bf16 v[174:177], v[186:189], a[124:127], v[174:177]
s_waitcnt vmcnt(8) lgkmcnt(0)
s_barrier
s_add_u32 m0, s44, 4096
ds_read_b64_tr_b16 a[144:145], v1 offset:16384
ds_read_b64_tr_b16 a[146:147], v1 offset:17408
global_load_lds_dwordx4 v0, s[40:41] offset:-4096 nt
ds_read_b64_tr_b16 a[148:149], v1 offset:16392
ds_read_b64_tr_b16 a[150:151], v1 offset:17416
global_load_lds_dwordx4 v166, s[40:41] offset:-3072 nt
ds_read_b64_tr_b16 a[152:153], v1 offset:24576
ds_read_b64_tr_b16 a[154:155], v1 offset:25600
global_load_lds_dwordx4 v167, s[40:41] offset:-2048 nt
ds_read_b64_tr_b16 a[156:157], v1 offset:24584
global_load_lds_dwordx4 v168, s[40:41] offset:-1024 nt
ds_read_b64_tr_b16 a[158:159], v1 offset:25608
v_mfma_f32_16x16x32_bf16 v[170:173], v[186:189], a[128:131], v[170:173]
s_add_u32 s40, s40, 589824
v_mfma_f32_16x16x32_bf16 v[174:177], v[186:189], a[132:135], v[174:177]
v_mfma_f32_16x16x32_bf16 v[170:173], v[186:189], a[136:139], v[170:173]
s_addc_u32 s41, s41, 0
v_mfma_f32_16x16x32_bf16 v[174:177], v[186:189], a[140:143], v[174:177]
s_waitcnt vmcnt(8) lgkmcnt(0)
s_barrier
s_add_u32 m0, s44, 20480
ds_read_b64_tr_b16 a[160:161], v1 offset:32768
v_mfma_f32_16x16x32_bf16 v[178:181], v[186:189], a[144:147], 0
v_mfma_f32_16x16x32_bf16 v[182:185], v[186:189], a[148:151], 0
v_mfma_f32_16x16x32_bf16 v[178:181], v[186:189], a[152:155], v[178:181]
v_mfma_f32_16x16x32_bf16 v[182:185], v[186:189], a[156:159], v[182:185]
v_mul_f32 v170, 0xbae38e39, v170
v_mul_f32 v171, 0xbae38e39, v171
ds_read_b64_tr_b16 a[162:163], v1 offset:33792
v_mul_f32 v172, 0xbae38e39, v172
global_load_lds_dwordx4 v0, s[40:41] offset:-4096 nt
v_mul_f32 v173, 0xbae38e39, v173
v_mul_f32 v174, 0xbae38e39, v174
v_mul_f32 v175, 0xbae38e39, v175
v_mul_f32 v176, 0xbae38e39, v176
v_mul_f32 v177, 0xbae38e39, v177
ds_read_b64_tr_b16 a[164:165], v1 offset:32776
v_mfma_f32_16x16x32_bf16 v[190:193], v[22:25], a[0:3], v[170:173]
v_mfma_f32_16x16x32_bf16 v[194:197], v[20:23], a[0:3], v[170:173]
global_load_lds_dwordx4 v166, s[40:41] offset:-3072 nt
v_mfma_f32_16x16x32_bf16 v[198:201], v[22:25], a[4:7], v[174:177]
v_mfma_f32_16x16x32_bf16 v[202:205], v[20:23], a[4:7], v[174:177]
v_mfma_f32_16x16x32_bf16 v[206:209], v[22:25], a[8:11], v[170:173]
s_nop 2
v_cvt_pk_bf16_f32 v222, v190, v191
ds_read_b64_tr_b16 a[166:167], v1 offset:33800
v_cvt_pk_bf16_f32 v223, v192, v193
v_cvt_pk_bf16_f32 v224, v194, v195
v_cvt_pk_bf16_f32 v225, v196, v197
global_load_lds_dwordx4 v167, s[40:41] offset:-2048 nt
v_mfma_f32_16x16x32_bf16 v[210:213], v[20:23], a[8:11], v[170:173]
v_accvgpr_write_b32 a0, v222
ds_read_b64_tr_b16 a[168:169], v1 offset:40960
v_accvgpr_write_b32 a1, v223
v_accvgpr_write_b32 a2, v224
v_accvgpr_write_b32 a3, v225
v_mfma_f32_16x16x32_bf16 v[214:217], v[22:25], a[12:15], v[174:177]
v_cvt_pk_bf16_f32 v226, v198, v199
v_cvt_pk_bf16_f32 v227, v200, v201
global_load_lds_dwordx4 v168, s[40:41] offset:-1024 nt
ds_read_b64_tr_b16 a[170:171], v1 offset:41984
v_cvt_pk_bf16_f32 v228, v202, v203
v_cvt_pk_bf16_f32 v229, v204, v205
v_mfma_f32_16x16x32_bf16 v[218:221], v[20:23], a[12:15], v[174:177]
s_add_u32 s40, s40, 589824
v_accvgpr_write_b32 a4, v226
v_accvgpr_write_b32 a5, v227
v_accvgpr_write_b32 a6, v228
ds_read_b64_tr_b16 a[172:173], v1 offset:40968
v_accvgpr_write_b32 a7, v229
v_mfma_f32_16x16x32_bf16 v[190:193], v[22:25], a[16:19], v[170:173]
v_cvt_pk_bf16_f32 v222, v206, v207
v_cvt_pk_bf16_f32 v223, v208, v209
v_cvt_pk_bf16_f32 v224, v210, v211
v_cvt_pk_bf16_f32 v225, v212, v213
ds_read_b64_tr_b16 a[174:175], v1 offset:41992
v_mfma_f32_16x16x32_bf16 v[194:197], v[20:23], a[16:19], v[170:173]
s_addc_u32 s41, s41, 0
v_accvgpr_write_b32 a8, v222
v_accvgpr_write_b32 a9, v223
v_accvgpr_write_b32 a10, v224
v_accvgpr_write_b32 a11, v225
v_mfma_f32_16x16x32_bf16 v[198:201], v[22:25], a[20:23], v[174:177]
s_waitcnt vmcnt(8) lgkmcnt(0)
v_cvt_pk_bf16_f32 v226, v214, v215
v_cvt_pk_bf16_f32 v227, v216, v217
v_cvt_pk_bf16_f32 v228, v218, v219
s_barrier
s_add_u32 m0, s44, 36864
ds_read_b64_tr_b16 a[176:177], v1 offset:49152
v_mfma_f32_16x16x32_bf16 v[178:181], v[186:189], a[160:163], v[178:181]
v_mfma_f32_16x16x32_bf16 v[182:185], v[186:189], a[164:167], v[182:185]
v_mfma_f32_16x16x32_bf16 v[178:181], v[186:189], a[168:171], v[178:181]
v_mfma_f32_16x16x32_bf16 v[182:185], v[186:189], a[172:175], v[182:185]
v_cvt_pk_bf16_f32 v229, v220, v221
v_mfma_f32_16x16x32_bf16 v[202:205], v[20:23], a[20:23], v[174:177]
ds_read_b64_tr_b16 a[178:179], v1 offset:50176
v_accvgpr_write_b32 a12, v226
global_load_lds_dwordx4 v0, s[40:41] offset:-4096 nt
v_accvgpr_write_b32 a13, v227
v_accvgpr_write_b32 a14, v228
v_accvgpr_write_b32 a15, v229
v_mfma_f32_16x16x32_bf16 v[206:209], v[22:25], a[24:27], v[170:173]
v_cvt_pk_bf16_f32 v222, v190, v191
ds_read_b64_tr_b16 a[180:181], v1 offset:49160
v_cvt_pk_bf16_f32 v223, v192, v193
v_cvt_pk_bf16_f32 v224, v194, v195
global_load_lds_dwordx4 v166, s[40:41] offset:-3072 nt
v_cvt_pk_bf16_f32 v225, v196, v197
v_mfma_f32_16x16x32_bf16 v[210:213], v[20:23], a[24:27], v[170:173]
v_accvgpr_write_b32 a16, v222
v_accvgpr_write_b32 a17, v223
ds_read_b64_tr_b16 a[182:183], v1 offset:50184
v_accvgpr_write_b32 a18, v224
v_accvgpr_write_b32 a19, v225
v_mfma_f32_16x16x32_bf16 v[214:217], v[22:25], a[28:31], v[174:177]
global_load_lds_dwordx4 v167, s[40:41] offset:-2048 nt
v_cvt_pk_bf16_f32 v226, v198, v199
v_cvt_pk_bf16_f32 v227, v200, v201
ds_read_b64_tr_b16 a[184:185], v1 offset:57344
v_cvt_pk_bf16_f32 v228, v202, v203
v_cvt_pk_bf16_f32 v229, v204, v205
v_mfma_f32_16x16x32_bf16 v[218:221], v[20:23], a[28:31], v[174:177]
v_accvgpr_write_b32 a20, v226
v_accvgpr_write_b32 a21, v227
v_accvgpr_write_b32 a22, v228
global_load_lds_dwordx4 v168, s[40:41] offset:-1024 nt
ds_read_b64_tr_b16 a[186:187], v1 offset:58368
v_accvgpr_write_b32 a23, v229
v_mfma_f32_16x16x32_bf16 v[190:193], v[22:25], a[32:35], v[170:173]
s_add_u32 s40, s40, 589824
v_cvt_pk_bf16_f32 v222, v206, v207
v_cvt_pk_bf16_f32 v223, v208, v209
v_cvt_pk_bf16_f32 v224, v210, v211
v_cvt_pk_bf16_f32 v225, v212, v213
ds_read_b64_tr_b16 a[188:189], v1 offset:57352
v_mfma_f32_16x16x32_bf16 v[194:197], v[20:23], a[32:35], v[170:173]
v_accvgpr_write_b32 a24, v222
v_accvgpr_write_b32 a25, v223
v_accvgpr_write_b32 a26, v224
v_accvgpr_write_b32 a27, v225
v_mfma_f32_16x16x32_bf16 v[198:201], v[22:25], a[36:39], v[174:177]
s_addc_u32 s41, s41, 0
ds_read_b64_tr_b16 a[190:191], v1 offset:58376
v_cvt_pk_bf16_f32 v226, v214, v215
v_cvt_pk_bf16_f32 v227, v216, v217
v_cvt_pk_bf16_f32 v228, v218, v219
v_cvt_pk_bf16_f32 v229, v220, v221
v_mfma_f32_16x16x32_bf16 v[202:205], v[20:23], a[36:39], v[174:177]
s_waitcnt vmcnt(8) lgkmcnt(0)
v_accvgpr_write_b32 a28, v226
v_accvgpr_write_b32 a29, v227
v_accvgpr_write_b32 a30, v228
v_accvgpr_write_b32 a31, v229
s_barrier
s_add_u32 m0, s44, 53248
ds_read_b64_tr_b16 a[192:193], v1 offset:0
v_mfma_f32_16x16x32_bf16 v[178:181], v[186:189], a[176:179], v[178:181]
v_mfma_f32_16x16x32_bf16 v[182:185], v[186:189], a[180:183], v[182:185]
v_mfma_f32_16x16x32_bf16 v[178:181], v[186:189], a[184:187], v[178:181]
v_mfma_f32_16x16x32_bf16 v[182:185], v[186:189], a[188:191], v[182:185]
v_mfma_f32_16x16x32_bf16 v[206:209], v[22:25], a[40:43], v[170:173]
v_cvt_pk_bf16_f32 v222, v190, v191
ds_read_b64_tr_b16 a[194:195], v1 offset:1024
v_cvt_pk_bf16_f32 v223, v192, v193
global_load_lds_dwordx4 v0, s[40:41] offset:-4096 nt
v_cvt_pk_bf16_f32 v224, v194, v195
v_cvt_pk_bf16_f32 v225, v196, v197
)"
R"(v_mfma_f32_16x16x32_bf16 v[210:213], v[20:23], a[40:43], v[170:173]
v_accvgpr_write_b32 a32, v222
v_accvgpr_write_b32 a33, v223
ds_read_b64_tr_b16 a[196:197], v1 offset:8
v_accvgpr_write_b32 a34, v224
v_accvgpr_write_b32 a35, v225
global_load_lds_dwordx4 v166, s[40:41] offset:-3072 nt
v_mfma_f32_16x16x32_bf16 v[214:217], v[22:25], a[44:47], v[174:177]
v_cvt_pk_bf16_f32 v226, v198, v199
v_cvt_pk_bf16_f32 v227, v200, v201
v_cvt_pk_bf16_f32 v228, v202, v203
ds_read_b64_tr_b16 a[198:199], v1 offset:1032
v_cvt_pk_bf16_f32 v229, v204, v205
v_mfma_f32_16x16x32_bf16 v[218:221], v[20:23], a[44:47], v[174:177]
v_accvgpr_write_b32 a36, v226
global_load_lds_dwordx4 v167, s[40:41] offset:-2048 nt
v_accvgpr_write_b32 a37, v227
v_accvgpr_write_b32 a38, v228
ds_read_b64_tr_b16 a[200:201], v1 offset:8192
v_accvgpr_write_b32 a39, v229
v_mfma_f32_16x16x32_bf16 v[190:193], v[22:25], a[48:51], v[170:173]
v_cvt_pk_bf16_f32 v222, v206, v207
v_cvt_pk_bf16_f32 v223, v208, v209
v_cvt_pk_bf16_f32 v224, v210, v211
v_cvt_pk_bf16_f32 v225, v212, v213
global_load_lds_dwordx4 v168, s[40:41] offset:-1024 nt
ds_read_b64_tr_b16 a[202:203], v1 offset:9216
v_mfma_f32_16x16x32_bf16 v[194:197], v[20:23], a[48:51], v[170:173]
v_accvgpr_write_b32 a40, v222
v_accvgpr_write_b32 a41, v223
v_accvgpr_write_b32 a42, v224
v_accvgpr_write_b32 a43, v225
v_mfma_f32_16x16x32_bf16 v[198:201], v[22:25], a[52:55], v[174:177]
s_add_u32 s40, s40, 589824
ds_read_b64_tr_b16 a[204:205], v1 offset:8200
v_cvt_pk_bf16_f32 v226, v214, v215
v_cvt_pk_bf16_f32 v227, v216, v217
v_cvt_pk_bf16_f32 v228, v218, v219
v_cvt_pk_bf16_f32 v229, v220, v221
v_mfma_f32_16x16x32_bf16 v[202:205], v[20:23], a[52:55], v[174:177]
s_addc_u32 s41, s41, 0
v_accvgpr_write_b32 a44, v226
ds_read_b64_tr_b16 a[206:207], v1 offset:9224
v_accvgpr_write_b32 a45, v227
v_accvgpr_write_b32 a46, v228
v_accvgpr_write_b32 a47, v229
v_mfma_f32_16x16x32_bf16 v[206:209], v[22:25], a[56:59], v[170:173]
v_cvt_pk_bf16_f32 v222, v190, v191
v_cvt_pk_bf16_f32 v223, v192, v193
v_cvt_pk_bf16_f32 v224, v194, v195
v_cvt_pk_bf16_f32 v225, v196, v197
v_mfma_f32_16x16x32_bf16 v[210:213], v[20:23], a[56:59], v[170:173]
s_waitcnt vmcnt(8) lgkmcnt(0)
s_barrier
s_add_u32 m0, s44, 4096
ds_read_b64_tr_b16 a[208:209], v1 offset:16384
v_mfma_f32_16x16x32_bf16 v[178:181], v[186:189], a[192:195], v[178:181]
v_mfma_f32_16x16x32_bf16 v[182:185], v[186:189], a[196:199], v[182:185]
v_mfma_f32_16x16x32_bf16 v[178:181], v[186:189], a[200:203], v[178:181]
v_mfma_f32_16x16x32_bf16 v[182:185], v[186:189], a[204:207], v[182:185]
v_accvgpr_write_b32 a48, v222
v_accvgpr_write_b32 a49, v223
ds_read_b64_tr_b16 a[210:211], v1 offset:17408
v_accvgpr_write_b32 a50, v224
global_load_lds_dwordx4 v0, s[40:41] offset:-4096 nt
v_accvgpr_write_b32 a51, v225
v_mfma_f32_16x16x32_bf16 v[214:217], v[22:25], a[60:63], v[174:177]
v_cvt_pk_bf16_f32 v226, v198, v199
v_cvt_pk_bf16_f32 v227, v200, v201
v_cvt_pk_bf16_f32 v228, v202, v203
ds_read_b64_tr_b16 a[212:213], v1 offset:16392
v_cvt_pk_bf16_f32 v229, v204, v205
v_mfma_f32_16x16x32_bf16 v[218:221], v[20:23], a[60:63], v[174:177]
global_load_lds_dwordx4 v166, s[40:41] offset:-3072 nt
v_accvgpr_write_b32 a52, v226
v_accvgpr_write_b32 a53, v227
v_accvgpr_write_b32 a54, v228
v_accvgpr_write_b32 a55, v229
ds_read_b64_tr_b16 a[214:215], v1 offset:17416
v_mfma_f32_16x16x32_bf16 v[190:193], v[22:25], a[64:67], v[170:173]
v_cvt_pk_bf16_f32 v222, v206, v207
v_cvt_pk_bf16_f32 v223, v208, v209
global_load_lds_dwordx4 v167, s[40:41] offset:-2048 nt
v_cvt_pk_bf16_f32 v224, v210, v211
v_cvt_pk_bf16_f32 v225, v212, v213
ds_read_b64_tr_b16 a[216:217], v1 offset:24576
v_mfma_f32_16x16x32_bf16 v[194:197], v[20:23], a[64:67], v[170:173]
v_accvgpr_write_b32 a56, v222
v_accvgpr_write_b32 a57, v223
v_accvgpr_write_b32 a58, v224
v_accvgpr_write_b32 a59, v225
v_mfma_f32_16x16x32_bf16 v[198:201], v[22:25], a[68:71], v[174:177]
global_load_lds_dwordx4 v168, s[40:41] offset:-1024 nt
ds_read_b64_tr_b16 a[218:219], v1 offset:25600
v_cvt_pk_bf16_f32 v226, v214, v215
v_cvt_pk_bf16_f32 v227, v216, v217
v_cvt_pk_bf16_f32 v228, v218, v219
v_cvt_pk_bf16_f32 v229, v220, v221
v_mfma_f32_16x16x32_bf16 v[202:205], v[20:23], a[68:71], v[174:177]
s_add_u32 s40, s40, 589824
v_accvgpr_write_b32 a60, v226
ds_read_b64_tr_b16 a[220:221], v1 offset:24584
v_accvgpr_write_b32 a61, v227
v_accvgpr_write_b32 a62, v228
v_accvgpr_write_b32 a63, v229
v_mfma_f32_16x16x32_bf16 v[206:209], v[22:25], a[72:75], v[170:173]
s_addc_u32 s41, s41, 0
v_cvt_pk_bf16_f32 v222, v190, v191
v_cvt_pk_bf16_f32 v223, v192, v193
ds_read_b64_tr_b16 a[222:223], v1 offset:25608
v_cvt_pk_bf16_f32 v224, v194, v195
v_cvt_pk_bf16_f32 v225, v196, v197
v_mfma_f32_16x16x32_bf16 v[210:213], v[20:23], a[72:75], v[170:173]
v_accvgpr_write_b32 a64, v222
v_accvgpr_write_b32 a65, v223
v_accvgpr_write_b32 a66, v224
v_accvgpr_write_b32 a67, v225
v_mfma_f32_16x16x32_bf16 v[214:217], v[22:25], a[76:79], v[174:177]
s_waitcnt vmcnt(8) lgkmcnt(0)
v_cvt_pk_bf16_f32 v226, v198, v199
s_barrier
s_add_u32 m0, s44, 20480
ds_read_b64_tr_b16 a[224:225], v1 offset:32768
v_mfma_f32_16x16x32_bf16 v[178:181], v[186:189], a[208:211], v[178:181]
v_mfma_f32_16x16x32_bf16 v[182:185], v[186:189], a[212:215], v[182:185]
v_mfma_f32_16x16x32_bf16 v[178:181], v[186:189], a[216:219], v[178:181]
v_mfma_f32_16x16x32_bf16 v[182:185], v[186:189], a[220:223], v[182:185]
v_cvt_pk_bf16_f32 v227, v200, v201
v_cvt_pk_bf16_f32 v228, v202, v203
ds_read_b64_tr_b16 a[226:227], v1 offset:33792
v_cvt_pk_bf16_f32 v229, v204, v205
global_load_lds_dwordx4 v0, s[40:41] offset:-4096 nt
v_mfma_f32_16x16x32_bf16 v[218:221], v[20:23], a[76:79], v[174:177]
v_accvgpr_write_b32 a68, v226
v_accvgpr_write_b32 a69, v227
v_accvgpr_write_b32 a70, v228
v_accvgpr_write_b32 a71, v229
ds_read_b64_tr_b16 a[228:229], v1 offset:32776
v_mfma_f32_16x16x32_bf16 v[190:193], v[22:25], a[80:83], v[170:173]
v_cvt_pk_bf16_f32 v222, v206, v207
global_load_lds_dwordx4 v166, s[40:41] offset:-3072 nt
v_cvt_pk_bf16_f32 v223, v208, v209
v_cvt_pk_bf16_f32 v224, v210, v211
v_cvt_pk_bf16_f32 v225, v212, v213
v_mfma_f32_16x16x32_bf16 v[194:197], v[20:23], a[80:83], v[170:173]
ds_read_b64_tr_b16 a[230:231], v1 offset:33800
v_accvgpr_write_b32 a72, v222
v_accvgpr_write_b32 a73, v223
v_accvgpr_write_b32 a74, v224
global_load_lds_dwordx4 v167, s[40:41] offset:-2048 nt
v_accvgpr_write_b32 a75, v225
v_mfma_f32_16x16x32_bf16 v[198:201], v[22:25], a[84:87], v[174:177]
ds_read_b64_tr_b16 a[232:233], v1 offset:40960
v_cvt_pk_bf16_f32 v226, v214, v215
v_cvt_pk_bf16_f32 v227, v216, v217
v_cvt_pk_bf16_f32 v228, v218, v219
v_cvt_pk_bf16_f32 v229, v220, v221
v_mfma_f32_16x16x32_bf16 v[202:205], v[20:23], a[84:87], v[174:177]
v_accvgpr_write_b32 a76, v226
global_load_lds_dwordx4 v168, s[40:41] offset:-1024 nt
ds_read_b64_tr_b16 a[234:235], v1 offset:41984
v_accvgpr_write_b32 a77, v227
v_accvgpr_write_b32 a78, v228
v_accvgpr_write_b32 a79, v229
v_mfma_f32_16x16x32_bf16 v[206:209], v[22:25], a[88:91], v[170:173]
s_sub_u32 s40, s40, 10027008
v_cvt_pk_bf16_f32 v222, v190, v191
v_cvt_pk_bf16_f32 v223, v192, v193
ds_read_b64_tr_b16 a[236:237], v1 offset:40968
v_cvt_pk_bf16_f32 v224, v194, v195
v_cvt_pk_bf16_f32 v225, v196, v197
v_mfma_f32_16x16x32_bf16 v[210:213], v[20:23], a[88:91], v[170:173]
s_subb_u32 s41, s41, 0
v_accvgpr_write_b32 a80, v222
v_accvgpr_write_b32 a81, v223
v_accvgpr_write_b32 a82, v224
ds_read_b64_tr_b16 a[238:239], v1 offset:41992
v_accvgpr_write_b32 a83, v225
v_mfma_f32_16x16x32_bf16 v[214:217], v[22:25], a[92:95], v[174:177]
v_cvt_pk_bf16_f32 v226, v198, v199
v_cvt_pk_bf16_f32 v227, v200, v201
v_cvt_pk_bf16_f32 v228, v202, v203
v_cvt_pk_bf16_f32 v229, v204, v205
v_mfma_f32_16x16x32_bf16 v[218:221], v[20:23], a[92:95], v[174:177]
s_waitcnt vmcnt(8) lgkmcnt(0)
v_accvgpr_write_b32 a84, v226
v_accvgpr_write_b32 a85, v227
s_barrier
ds_read_b64_tr_b16 v[26:27], v1 offset:49152
v_mfma_f32_16x16x32_bf16 v[178:181], v[186:189], a[224:227], v[178:181]
v_mfma_f32_16x16x32_bf16 v[182:185], v[186:189], a[228:231], v[182:185]
v_mfma_f32_16x16x32_bf16 v[178:181], v[186:189], a[232:235], v[178:181]
v_mfma_f32_16x16x32_bf16 v[182:185], v[186:189], a[236:239], v[182:185]
v_accvgpr_write_b32 a86, v228
v_accvgpr_write_b32 a87, v229
ds_read_b64_tr_b16 v[28:29], v1 offset:50176
v_mfma_f32_16x16x32_bf16 v[190:193], v[22:25], a[96:99], v[170:173]
v_cvt_pk_bf16_f32 v222, v206, v207
v_cvt_pk_bf16_f32 v223, v208, v209
v_cvt_pk_bf16_f32 v224, v210, v211
v_cvt_pk_bf16_f32 v225, v212, v213
v_mfma_f32_16x16x32_bf16 v[194:197], v[20:23], a[96:99], v[170:173]
ds_read_b64_tr_b16 v[30:31], v1 offset:49160
v_accvgpr_write_b32 a88, v222
v_accvgpr_write_b32 a89, v223
v_accvgpr_write_b32 a90, v224
v_accvgpr_write_b32 a91, v225
v_mfma_f32_16x16x32_bf16 v[198:201], v[22:25], a[100:103], v[174:177]
v_cvt_pk_bf16_f32 v226, v214, v215
ds_read_b64_tr_b16 v[32:33], v1 offset:50184
v_cvt_pk_bf16_f32 v227, v216, v217
v_cvt_pk_bf16_f32 v228, v218, v219
v_cvt_pk_bf16_f32 v229, v220, v221
v_mfma_f32_16x16x32_bf16 v[202:205], v[20:23], a[100:103], v[174:177]
v_accvgpr_write_b32 a92, v226
ds_read_b64_tr_b16 v[34:35], v1 offset:57344
v_accvgpr_write_b32 a93, v227
v_accvgpr_write_b32 a94, v228
v_accvgpr_write_b32 a95, v229
v_mfma_f32_16x16x32_bf16 v[206:209], v[22:25], a[104:107], v[170:173]
v_cvt_pk_bf16_f32 v222, v190, v191
v_cvt_pk_bf16_f32 v223, v192, v193
ds_read_b64_tr_b16 v[36:37], v1 offset:58368
v_cvt_pk_bf16_f32 v224, v194, v195
v_cvt_pk_bf16_f32 v225, v196, v197
v_mfma_f32_16x16x32_bf16 v[210:213], v[20:23], a[104:107], v[170:173]
v_accvgpr_write_b32 a96, v222
v_accvgpr_write_b32 a97, v223
v_accvgpr_write_b32 a98, v224
ds_read_b64_tr_b16 v[38:39], v1 offset:57352
v_accvgpr_write_b32 a99, v225
v_mfma_f32_16x16x32_bf16 v[214:217], v[22:25], a[108:111], v[174:177]
v_cvt_pk_bf16_f32 v226, v198, v199
v_cvt_pk_bf16_f32 v227, v200, v201
v_cvt_pk_bf16_f32 v228, v202, v203
v_cvt_pk_bf16_f32 v229, v204, v205
ds_read_b64_tr_b16 v[40:41], v1 offset:58376
v_mfma_f32_16x16x32_bf16 v[218:221], v[20:23], a[108:111], v[174:177]
v_accvgpr_write_b32 a100, v226
v_accvgpr_write_b32 a101, v227
v_accvgpr_write_b32 a102, v228
v_accvgpr_write_b32 a103, v229
v_mfma_f32_16x16x32_bf16 v[190:193], v[22:25], a[112:115], v[170:173]
s_waitcnt vmcnt(4) lgkmcnt(0)
v_cvt_pk_bf16_f32 v222, v206, v207
v_cvt_pk_bf16_f32 v223, v208, v209
v_cvt_pk_bf16_f32 v224, v210, v211
s_barrier
ds_read_b64_tr_b16 v[42:43], v1 offset:0
v_mfma_f32_16x16x32_bf16 v[178:181], v[186:189], v[26:29], v[178:181]
v_mfma_f32_16x16x32_bf16 v[182:185], v[186:189], v[30:33], v[182:185]
v_mfma_f32_16x16x32_bf16 v[178:181], v[186:189], v[34:37], v[178:181]
v_mfma_f32_16x16x32_bf16 v[182:185], v[186:189], v[38:41], v[182:185]
v_cvt_pk_bf16_f32 v225, v212, v213
v_mfma_f32_16x16x32_bf16 v[194:197], v[20:23], a[112:115], v[170:173]
ds_read_b64_tr_b16 v[44:45], v1 offset:1024
v_accvgpr_write_b32 a104, v222
v_accvgpr_write_b32 a105, v223
v_accvgpr_write_b32 a106, v224
v_accvgpr_write_b32 a107, v225
v_mfma_f32_16x16x32_bf16 v[198:201], v[22:25], a[116:119], v[174:177]
v_cvt_pk_bf16_f32 v226, v214, v215
ds_read_b64_tr_b16 v[46:47], v1 offset:8
v_cvt_pk_bf16_f32 v227, v216, v217
v_cvt_pk_bf16_f32 v228, v218, v219
v_cvt_pk_bf16_f32 v229, v220, v221
v_mfma_f32_16x16x32_bf16 v[202:205], v[20:23], a[116:119], v[174:177]
v_accvgpr_write_b32 a108, v226
v_accvgpr_write_b32 a109, v227
ds_read_b64_tr_b16 v[48:49], v1 offset:1032
v_accvgpr_write_b32 a110, v228
v_accvgpr_write_b32 a111, v229
v_mfma_f32_16x16x32_bf16 v[206:209], v[22:25], a[120:123], v[170:173]
v_cvt_pk_bf16_f32 v222, v190, v191
v_cvt_pk_bf16_f32 v223, v192, v193
ds_read_b64_tr_b16 v[50:51], v1 offset:8192
v_cvt_pk_bf16_f32 v224, v194, v195
v_cvt_pk_bf16_f32 v225, v196, v197
v_mfma_f32_16x16x32_bf16 v[210:213], v[20:23], a[120:123], v[170:173]
v_accvgpr_write_b32 a112, v222
v_accvgpr_write_b32 a113, v223
v_accvgpr_write_b32 a114, v224
ds_read_b64_tr_b16 v[52:53], v1 offset:9216
v_accvgpr_write_b32 a115, v225
v_mfma_f32_16x16x32_bf16 v[214:217], v[22:25], a[124:127], v[174:177]
v_cvt_pk_bf16_f32 v226, v198, v199
v_cvt_pk_bf16_f32 v227, v200, v201
v_cvt_pk_bf16_f32 v228, v202, v203
v_cvt_pk_bf16_f32 v229, v204, v205
ds_read_b64_tr_b16 v[54:55], v1 offset:8200
v_mfma_f32_16x16x32_bf16 v[218:221], v[20:23], a[124:127], v[174:177]
v_accvgpr_write_b32 a116, v226
v_accvgpr_write_b32 a117, v227
v_accvgpr_write_b32 a118, v228
v_accvgpr_write_b32 a119, v229
v_mfma_f32_16x16x32_bf16 v[190:193], v[22:25], a[128:131], v[170:173]
ds_read_b64_tr_b16 v[56:57], v1 offset:9224
v_cvt_pk_bf16_f32 v222, v206, v207
v_cvt_pk_bf16_f32 v223, v208, v209
v_cvt_pk_bf16_f32 v224, v210, v211
v_cvt_pk_bf16_f32 v225, v212, v213
v_mfma_f32_16x16x32_bf16 v[194:197], v[20:23], a[128:131], v[170:173]
s_waitcnt vmcnt(0) lgkmcnt(0)
v_accvgpr_write_b32 a120, v222
v_accvgpr_write_b32 a121, v223
v_accvgpr_write_b32 a122, v224
v_accvgpr_write_b32 a123, v225
s_barrier
ds_read_b64_tr_b16 v[58:59], v1 offset:16384
v_mfma_f32_16x16x32_bf16 v[178:181], v[186:189], v[42:45], v[178:181]
v_mfma_f32_16x16x32_bf16 v[182:185], v[186:189], v[46:49], v[182:185]
v_mfma_f32_16x16x32_bf16 v[178:181], v[186:189], v[50:53], v[178:181]
v_mfma_f32_16x16x32_bf16 v[182:185], v[186:189], v[54:57], v[182:185]
v_mfma_f32_16x16x32_bf16 v[198:201], v[22:25], a[132:135], v[174:177]
v_cvt_pk_bf16_f32 v226, v214, v215
ds_read_b64_tr_b16 v[60:61], v1 offset:17408
v_cvt_pk_bf16_f32 v227, v216, v217
v_cvt_pk_bf16_f32 v228, v218, v219
v_cvt_pk_bf16_f32 v229, v220, v221
v_mfma_f32_16x16x32_bf16 v[202:205], v[20:23], a[132:135], v[174:177]
v_accvgpr_write_b32 a124, v226
v_accvgpr_write_b32 a125, v227
ds_read_b64_tr_b16 v[62:63], v1 offset:16392
v_accvgpr_write_b32 a126, v228
v_accvgpr_write_b32 a127, v229
v_mfma_f32_16x16x32_bf16 v[206:209], v[22:25], a[136:139], v[170:173]
v_cvt_pk_bf16_f32 v222, v190, v191
v_cvt_pk_bf16_f32 v223, v192, v193
v_cvt_pk_bf16_f32 v224, v194, v195
ds_read_b64_tr_b16 v[64:65], v1 offset:17416
v_cvt_pk_bf16_f32 v225, v196, v197
v_mfma_f32_16x16x32_bf16 v[210:213], v[20:23], a[136:139], v[170:173]
v_accvgpr_write_b32 a128, v222
v_accvgpr_write_b32 a129, v223
v_accvgpr_write_b32 a130, v224
ds_read_b64_tr_b16 v[66:67], v1 offset:24576
v_accvgpr_write_b32 a131, v225
v_mfma_f32_16x16x32_bf16 v[214:217], v[22:25], a[140:143], v[174:177]
v_cvt_pk_bf16_f32 v226, v198, v199
v_cvt_pk_bf16_f32 v227, v200, v201
v_cvt_pk_bf16_f32 v228, v202, v203
v_cvt_pk_bf16_f32 v229, v204, v205
ds_read_b64_tr_b16 v[68:69], v1 offset:25600
v_mfma_f32_16x16x32_bf16 v[218:221], v[20:23], a[140:143], v[174:177]
s_waitcnt lgkmcnt(4)
v_accvgpr_write_b32 a132, v226
v_accvgpr_write_b32 a133, v227
v_accvgpr_write_b32 a134, v228
v_accvgpr_write_b32 a135, v229
v_cvt_pk_bf16_f32 v222, v206, v207
ds_read_b64_tr_b16 v[70:71], v1 offset:24584
v_cvt_pk_bf16_f32 v223, v208, v209
v_cvt_pk_bf16_f32 v224, v210, v211
v_cvt_pk_bf16_f32 v225, v212, v213
v_accvgpr_write_b32 a136, v222
v_accvgpr_write_b32 a137, v223
v_accvgpr_write_b32 a138, v224
ds_read_b64_tr_b16 v[72:73], v1 offset:25608
v_accvgpr_write_b32 a139, v225
v_cvt_pk_bf16_f32 v226, v214, v215
v_cvt_pk_bf16_f32 v227, v216, v217
v_cvt_pk_bf16_f32 v228, v218, v219
v_cvt_pk_bf16_f32 v229, v220, v221
v_accvgpr_write_b32 a140, v226
v_accvgpr_write_b32 a141, v227
v_accvgpr_write_b32 a142, v228
v_accvgpr_write_b32 a143, v229
v_mfma_f32_16x16x32_bf16 v[178:181], v[186:189], v[58:61], v[178:181]
s_waitcnt lgkmcnt(4)
v_mfma_f32_16x16x32_bf16 v[182:185], v[186:189], v[62:65], v[182:185]
s_waitcnt lgkmcnt(2)
v_mfma_f32_16x16x32_bf16 v[178:181], v[186:189], v[66:69], v[178:181]
s_waitcnt lgkmcnt(0)
v_mfma_f32_16x16x32_bf16 v[182:185], v[186:189], v[70:73], v[182:185]
s_nop 5
v_mul_f32 v178, 0xbae38e39, v178
v_mul_f32 v179, 0xbae38e39, v179
v_mul_f32 v180, 0xbae38e39, v180
v_mul_f32 v181, 0xbae38e39, v181
v_mul_f32 v182, 0xbae38e39, v182
v_mul_f32 v183, 0xbae38e39, v183
v_mul_f32 v184, 0xbae38e39, v184
v_mul_f32 v185, 0xbae38e39, v185
v_mfma_f32_16x16x32_bf16 v[190:193], v[22:25], a[144:147], v[178:181]
v_mfma_f32_16x16x32_bf16 v[194:197], v[20:23], a[144:147], v[178:181]
v_mfma_f32_16x16x32_bf16 v[198:201], v[22:25], a[148:151], v[182:185]
v_mfma_f32_16x16x32_bf16 v[202:205], v[20:23], a[148:151], v[182:185]
v_mfma_f32_16x16x32_bf16 v[206:209], v[22:25], a[152:155], v[178:181]
s_nop 3
v_cvt_pk_bf16_f32 v222, v190, v191
v_cvt_pk_bf16_f32 v223, v192, v193
v_cvt_pk_bf16_f32 v224, v194, v195
v_cvt_pk_bf16_f32 v225, v196, v197
v_mfma_f32_16x16x32_bf16 v[210:213], v[20:23], a[152:155], v[178:181]
v_accvgpr_write_b32 a144, v222
v_accvgpr_write_b32 a145, v223
v_accvgpr_write_b32 a146, v224
v_accvgpr_write_b32 a147, v225
v_mfma_f32_16x16x32_bf16 v[214:217], v[22:25], a[156:159], v[182:185]
v_cvt_pk_bf16_f32 v226, v198, v199
)"
R"(v_cvt_pk_bf16_f32 v227, v200, v201
v_cvt_pk_bf16_f32 v228, v202, v203
v_cvt_pk_bf16_f32 v229, v204, v205
v_mfma_f32_16x16x32_bf16 v[218:221], v[20:23], a[156:159], v[182:185]
v_accvgpr_write_b32 a148, v226
v_accvgpr_write_b32 a149, v227
v_accvgpr_write_b32 a150, v228
v_accvgpr_write_b32 a151, v229
v_mfma_f32_16x16x32_bf16 v[190:193], v[22:25], a[160:163], v[178:181]
v_cvt_pk_bf16_f32 v222, v206, v207
v_cvt_pk_bf16_f32 v223, v208, v209
v_cvt_pk_bf16_f32 v224, v210, v211
v_cvt_pk_bf16_f32 v225, v212, v213
v_mfma_f32_16x16x32_bf16 v[194:197], v[20:23], a[160:163], v[178:181]
v_accvgpr_write_b32 a152, v222
v_accvgpr_write_b32 a153, v223
v_accvgpr_write_b32 a154, v224
v_accvgpr_write_b32 a155, v225
v_mfma_f32_16x16x32_bf16 v[198:201], v[22:25], a[164:167], v[182:185]
v_cvt_pk_bf16_f32 v226, v214, v215
v_cvt_pk_bf16_f32 v227, v216, v217
v_cvt_pk_bf16_f32 v228, v218, v219
v_cvt_pk_bf16_f32 v229, v220, v221
v_mfma_f32_16x16x32_bf16 v[202:205], v[20:23], a[164:167], v[182:185]
v_accvgpr_write_b32 a156, v226
v_accvgpr_write_b32 a157, v227
v_accvgpr_write_b32 a158, v228
v_accvgpr_write_b32 a159, v229
v_mfma_f32_16x16x32_bf16 v[206:209], v[22:25], a[168:171], v[178:181]
v_cvt_pk_bf16_f32 v222, v190, v191
v_cvt_pk_bf16_f32 v223, v192, v193
v_cvt_pk_bf16_f32 v224, v194, v195
v_cvt_pk_bf16_f32 v225, v196, v197
v_mfma_f32_16x16x32_bf16 v[210:213], v[20:23], a[168:171], v[178:181]
v_accvgpr_write_b32 a160, v222
v_accvgpr_write_b32 a161, v223
v_accvgpr_write_b32 a162, v224
v_accvgpr_write_b32 a163, v225
v_mfma_f32_16x16x32_bf16 v[214:217], v[22:25], a[172:175], v[182:185]
v_cvt_pk_bf16_f32 v226, v198, v199
v_cvt_pk_bf16_f32 v227, v200, v201
v_cvt_pk_bf16_f32 v228, v202, v203
v_cvt_pk_bf16_f32 v229, v204, v205
v_mfma_f32_16x16x32_bf16 v[218:221], v[20:23], a[172:175], v[182:185]
v_accvgpr_write_b32 a164, v226
v_accvgpr_write_b32 a165, v227
v_accvgpr_write_b32 a166, v228
v_accvgpr_write_b32 a167, v229
v_mfma_f32_16x16x32_bf16 v[190:193], v[22:25], a[176:179], v[178:181]
v_cvt_pk_bf16_f32 v222, v206, v207
v_cvt_pk_bf16_f32 v223, v208, v209
v_cvt_pk_bf16_f32 v224, v210, v211
v_cvt_pk_bf16_f32 v225, v212, v213
v_mfma_f32_16x16x32_bf16 v[194:197], v[20:23], a[176:179], v[178:181]
v_accvgpr_write_b32 a168, v222
v_accvgpr_write_b32 a169, v223
v_accvgpr_write_b32 a170, v224
v_accvgpr_write_b32 a171, v225
v_mfma_f32_16x16x32_bf16 v[198:201], v[22:25], a[180:183], v[182:185]
v_cvt_pk_bf16_f32 v226, v214, v215
v_cvt_pk_bf16_f32 v227, v216, v217
v_cvt_pk_bf16_f32 v228, v218, v219
v_cvt_pk_bf16_f32 v229, v220, v221
v_mfma_f32_16x16x32_bf16 v[202:205], v[20:23], a[180:183], v[182:185]
v_accvgpr_write_b32 a172, v226
v_accvgpr_write_b32 a173, v227
v_accvgpr_write_b32 a174, v228
v_accvgpr_write_b32 a175, v229
v_mfma_f32_16x16x32_bf16 v[206:209], v[22:25], a[184:187], v[178:181]
v_cvt_pk_bf16_f32 v222, v190, v191
v_cvt_pk_bf16_f32 v223, v192, v193
v_cvt_pk_bf16_f32 v224, v194, v195
v_cvt_pk_bf16_f32 v225, v196, v197
v_mfma_f32_16x16x32_bf16 v[210:213], v[20:23], a[184:187], v[178:181]
v_accvgpr_write_b32 a176, v222
v_accvgpr_write_b32 a177, v223
v_accvgpr_write_b32 a178, v224
v_accvgpr_write_b32 a179, v225
v_mfma_f32_16x16x32_bf16 v[214:217], v[22:25], a[188:191], v[182:185]
v_cvt_pk_bf16_f32 v226, v198, v199
v_cvt_pk_bf16_f32 v227, v200, v201
v_cvt_pk_bf16_f32 v228, v202, v203
v_cvt_pk_bf16_f32 v229, v204, v205
v_mfma_f32_16x16x32_bf16 v[218:221], v[20:23], a[188:191], v[182:185]
v_accvgpr_write_b32 a180, v226
v_accvgpr_write_b32 a181, v227
v_accvgpr_write_b32 a182, v228
v_accvgpr_write_b32 a183, v229
v_mfma_f32_16x16x32_bf16 v[190:193], v[22:25], a[192:195], v[178:181]
v_cvt_pk_bf16_f32 v222, v206, v207
v_cvt_pk_bf16_f32 v223, v208, v209
v_cvt_pk_bf16_f32 v224, v210, v211
v_cvt_pk_bf16_f32 v225, v212, v213
v_mfma_f32_16x16x32_bf16 v[194:197], v[20:23], a[192:195], v[178:181]
v_accvgpr_write_b32 a184, v222
v_accvgpr_write_b32 a185, v223
v_accvgpr_write_b32 a186, v224
v_accvgpr_write_b32 a187, v225
v_mfma_f32_16x16x32_bf16 v[198:201], v[22:25], a[196:199], v[182:185]
v_cvt_pk_bf16_f32 v226, v214, v215
v_cvt_pk_bf16_f32 v227, v216, v217
v_cvt_pk_bf16_f32 v228, v218, v219
v_cvt_pk_bf16_f32 v229, v220, v221
v_mfma_f32_16x16x32_bf16 v[202:205], v[20:23], a[196:199], v[182:185]
v_accvgpr_write_b32 a188, v226
v_accvgpr_write_b32 a189, v227
v_accvgpr_write_b32 a190, v228
v_accvgpr_write_b32 a191, v229
v_mfma_f32_16x16x32_bf16 v[206:209], v[22:25], a[200:203], v[178:181]
v_cvt_pk_bf16_f32 v222, v190, v191
v_cvt_pk_bf16_f32 v223, v192, v193
v_cvt_pk_bf16_f32 v224, v194, v195
v_cvt_pk_bf16_f32 v225, v196, v197
v_mfma_f32_16x16x32_bf16 v[210:213], v[20:23], a[200:203], v[178:181]
v_accvgpr_write_b32 a192, v222
v_accvgpr_write_b32 a193, v223
v_accvgpr_write_b32 a194, v224
v_accvgpr_write_b32 a195, v225
v_mfma_f32_16x16x32_bf16 v[214:217], v[22:25], a[204:207], v[182:185]
v_cvt_pk_bf16_f32 v226, v198, v199
v_cvt_pk_bf16_f32 v227, v200, v201
v_cvt_pk_bf16_f32 v228, v202, v203
v_cvt_pk_bf16_f32 v229, v204, v205
v_mfma_f32_16x16x32_bf16 v[218:221], v[20:23], a[204:207], v[182:185]
v_accvgpr_write_b32 a196, v226
v_accvgpr_write_b32 a197, v227
v_accvgpr_write_b32 a198, v228
v_accvgpr_write_b32 a199, v229
v_mfma_f32_16x16x32_bf16 v[190:193], v[22:25], a[208:211], v[178:181]
v_cvt_pk_bf16_f32 v222, v206, v207
v_cvt_pk_bf16_f32 v223, v208, v209
v_cvt_pk_bf16_f32 v224, v210, v211
v_cvt_pk_bf16_f32 v225, v212, v213
v_mfma_f32_16x16x32_bf16 v[194:197], v[20:23], a[208:211], v[178:181]
v_accvgpr_write_b32 a200, v222
v_accvgpr_write_b32 a201, v223
v_accvgpr_write_b32 a202, v224
v_accvgpr_write_b32 a203, v225
v_mfma_f32_16x16x32_bf16 v[198:201], v[22:25], a[212:215], v[182:185]
v_cvt_pk_bf16_f32 v226, v214, v215
v_cvt_pk_bf16_f32 v227, v216, v217
v_cvt_pk_bf16_f32 v228, v218, v219
v_cvt_pk_bf16_f32 v229, v220, v221
v_mfma_f32_16x16x32_bf16 v[202:205], v[20:23], a[212:215], v[182:185]
v_accvgpr_write_b32 a204, v226
v_accvgpr_write_b32 a205, v227
v_accvgpr_write_b32 a206, v228
v_accvgpr_write_b32 a207, v229
v_mfma_f32_16x16x32_bf16 v[206:209], v[22:25], a[216:219], v[178:181]
v_cvt_pk_bf16_f32 v222, v190, v191
v_cvt_pk_bf16_f32 v223, v192, v193
v_cvt_pk_bf16_f32 v224, v194, v195
v_cvt_pk_bf16_f32 v225, v196, v197
v_mfma_f32_16x16x32_bf16 v[210:213], v[20:23], a[216:219], v[178:181]
v_accvgpr_write_b32 a208, v222
v_accvgpr_write_b32 a209, v223
v_accvgpr_write_b32 a210, v224
v_accvgpr_write_b32 a211, v225
v_mfma_f32_16x16x32_bf16 v[214:217], v[22:25], a[220:223], v[182:185]
v_cvt_pk_bf16_f32 v226, v198, v199
v_cvt_pk_bf16_f32 v227, v200, v201
v_cvt_pk_bf16_f32 v228, v202, v203
v_cvt_pk_bf16_f32 v229, v204, v205
v_mfma_f32_16x16x32_bf16 v[218:221], v[20:23], a[220:223], v[182:185]
v_accvgpr_write_b32 a212, v226
v_accvgpr_write_b32 a213, v227
v_accvgpr_write_b32 a214, v228
v_accvgpr_write_b32 a215, v229
v_mfma_f32_16x16x32_bf16 v[190:193], v[22:25], a[224:227], v[178:181]
v_cvt_pk_bf16_f32 v222, v206, v207
v_cvt_pk_bf16_f32 v223, v208, v209
v_cvt_pk_bf16_f32 v224, v210, v211
v_cvt_pk_bf16_f32 v225, v212, v213
v_mfma_f32_16x16x32_bf16 v[194:197], v[20:23], a[224:227], v[178:181]
v_accvgpr_write_b32 a216, v222
v_accvgpr_write_b32 a217, v223
v_accvgpr_write_b32 a218, v224
v_accvgpr_write_b32 a219, v225
v_mfma_f32_16x16x32_bf16 v[198:201], v[22:25], a[228:231], v[182:185]
v_cvt_pk_bf16_f32 v226, v214, v215
v_cvt_pk_bf16_f32 v227, v216, v217
v_cvt_pk_bf16_f32 v228, v218, v219
v_cvt_pk_bf16_f32 v229, v220, v221
v_mfma_f32_16x16x32_bf16 v[202:205], v[20:23], a[228:231], v[182:185]
v_accvgpr_write_b32 a220, v226
v_accvgpr_write_b32 a221, v227
v_accvgpr_write_b32 a222, v228
v_accvgpr_write_b32 a223, v229
v_mfma_f32_16x16x32_bf16 v[206:209], v[22:25], a[232:235], v[178:181]
v_cvt_pk_bf16_f32 v222, v190, v191
v_cvt_pk_bf16_f32 v223, v192, v193
v_cvt_pk_bf16_f32 v224, v194, v195
v_cvt_pk_bf16_f32 v225, v196, v197
v_mfma_f32_16x16x32_bf16 v[210:213], v[20:23], a[232:235], v[178:181]
v_accvgpr_write_b32 a224, v222
v_accvgpr_write_b32 a225, v223
v_accvgpr_write_b32 a226, v224
v_accvgpr_write_b32 a227, v225
v_mfma_f32_16x16x32_bf16 v[214:217], v[22:25], a[236:239], v[182:185]
v_cvt_pk_bf16_f32 v226, v198, v199
v_cvt_pk_bf16_f32 v227, v200, v201
v_cvt_pk_bf16_f32 v228, v202, v203
v_cvt_pk_bf16_f32 v229, v204, v205
v_mfma_f32_16x16x32_bf16 v[218:221], v[20:23], a[236:239], v[182:185]
v_accvgpr_write_b32 a228, v226
v_accvgpr_write_b32 a229, v227
v_accvgpr_write_b32 a230, v228
v_accvgpr_write_b32 a231, v229
v_mfma_f32_16x16x32_bf16 v[190:193], v[22:25], v[26:29], v[178:181]
v_cvt_pk_bf16_f32 v222, v206, v207
v_cvt_pk_bf16_f32 v223, v208, v209
v_cvt_pk_bf16_f32 v224, v210, v211
v_cvt_pk_bf16_f32 v225, v212, v213
v_mfma_f32_16x16x32_bf16 v[194:197], v[20:23], v[26:29], v[178:181]
v_accvgpr_write_b32 a232, v222
v_accvgpr_write_b32 a233, v223
v_accvgpr_write_b32 a234, v224
v_accvgpr_write_b32 a235, v225
v_mfma_f32_16x16x32_bf16 v[198:201], v[22:25], v[30:33], v[182:185]
v_cvt_pk_bf16_f32 v226, v214, v215
v_cvt_pk_bf16_f32 v227, v216, v217
v_cvt_pk_bf16_f32 v228, v218, v219
v_cvt_pk_bf16_f32 v229, v220, v221
v_mfma_f32_16x16x32_bf16 v[202:205], v[20:23], v[30:33], v[182:185]
v_accvgpr_write_b32 a236, v226
v_accvgpr_write_b32 a237, v227
v_accvgpr_write_b32 a238, v228
v_accvgpr_write_b32 a239, v229
v_mfma_f32_16x16x32_bf16 v[206:209], v[22:25], v[34:37], v[178:181]
v_cvt_pk_bf16_f32 v26, v190, v191
v_cvt_pk_bf16_f32 v27, v192, v193
v_cvt_pk_bf16_f32 v28, v194, v195
v_cvt_pk_bf16_f32 v29, v196, v197
v_mfma_f32_16x16x32_bf16 v[210:213], v[20:23], v[34:37], v[178:181]
v_mfma_f32_16x16x32_bf16 v[214:217], v[22:25], v[38:41], v[182:185]
v_cvt_pk_bf16_f32 v30, v198, v199
v_cvt_pk_bf16_f32 v31, v200, v201
v_cvt_pk_bf16_f32 v32, v202, v203
v_cvt_pk_bf16_f32 v33, v204, v205
v_mfma_f32_16x16x32_bf16 v[218:221], v[20:23], v[38:41], v[182:185]
v_mfma_f32_16x16x32_bf16 v[190:193], v[22:25], v[42:45], v[178:181]
v_cvt_pk_bf16_f32 v34, v206, v207
v_cvt_pk_bf16_f32 v35, v208, v209
v_cvt_pk_bf16_f32 v36, v210, v211
v_cvt_pk_bf16_f32 v37, v212, v213
v_mfma_f32_16x16x32_bf16 v[194:197], v[20:23], v[42:45], v[178:181]
v_mfma_f32_16x16x32_bf16 v[198:201], v[22:25], v[46:49], v[182:185]
v_cvt_pk_bf16_f32 v38, v214, v215
v_cvt_pk_bf16_f32 v39, v216, v217
v_cvt_pk_bf16_f32 v40, v218, v219
v_cvt_pk_bf16_f32 v41, v220, v221
v_mfma_f32_16x16x32_bf16 v[202:205], v[20:23], v[46:49], v[182:185]
v_mfma_f32_16x16x32_bf16 v[206:209], v[22:25], v[50:53], v[178:181]
v_cvt_pk_bf16_f32 v42, v190, v191
v_cvt_pk_bf16_f32 v43, v192, v193
v_cvt_pk_bf16_f32 v44, v194, v195
v_cvt_pk_bf16_f32 v45, v196, v197
v_mfma_f32_16x16x32_bf16 v[210:213], v[20:23], v[50:53], v[178:181]
v_mfma_f32_16x16x32_bf16 v[214:217], v[22:25], v[54:57], v[182:185]
v_cvt_pk_bf16_f32 v46, v198, v199
v_cvt_pk_bf16_f32 v47, v200, v201
v_cvt_pk_bf16_f32 v48, v202, v203
v_cvt_pk_bf16_f32 v49, v204, v205
v_mfma_f32_16x16x32_bf16 v[218:221], v[20:23], v[54:57], v[182:185]
v_mfma_f32_16x16x32_bf16 v[190:193], v[22:25], v[58:61], v[178:181]
v_cvt_pk_bf16_f32 v50, v206, v207
v_cvt_pk_bf16_f32 v51, v208, v209
v_cvt_pk_bf16_f32 v52, v210, v211
v_cvt_pk_bf16_f32 v53, v212, v213
v_mfma_f32_16x16x32_bf16 v[194:197], v[20:23], v[58:61], v[178:181]
v_mfma_f32_16x16x32_bf16 v[198:201], v[22:25], v[62:65], v[182:185]
v_cvt_pk_bf16_f32 v54, v214, v215
v_cvt_pk_bf16_f32 v55, v216, v217
v_cvt_pk_bf16_f32 v56, v218, v219
v_cvt_pk_bf16_f32 v57, v220, v221
v_mfma_f32_16x16x32_bf16 v[202:205], v[20:23], v[62:65], v[182:185]
v_mfma_f32_16x16x32_bf16 v[206:209], v[22:25], v[66:69], v[178:181]
v_cvt_pk_bf16_f32 v58, v190, v191
v_cvt_pk_bf16_f32 v59, v192, v193
v_cvt_pk_bf16_f32 v60, v194, v195
v_cvt_pk_bf16_f32 v61, v196, v197
v_mfma_f32_16x16x32_bf16 v[210:213], v[20:23], v[66:69], v[178:181]
v_mfma_f32_16x16x32_bf16 v[214:217], v[22:25], v[70:73], v[182:185]
v_cvt_pk_bf16_f32 v62, v198, v199
v_cvt_pk_bf16_f32 v63, v200, v201
v_cvt_pk_bf16_f32 v64, v202, v203
v_cvt_pk_bf16_f32 v65, v204, v205
v_mfma_f32_16x16x32_bf16 v[218:221], v[20:23], v[70:73], v[182:185]
v_cvt_pk_bf16_f32 v66, v206, v207
v_cvt_pk_bf16_f32 v67, v208, v209
v_cvt_pk_bf16_f32 v68, v210, v211
v_cvt_pk_bf16_f32 v69, v212, v213
v_cvt_pk_bf16_f32 v70, v214, v215
v_cvt_pk_bf16_f32 v71, v216, v217
s_nop 1
v_cvt_pk_bf16_f32 v72, v218, v219
v_cvt_pk_bf16_f32 v73, v220, v221
s_barrier
s_and_b32 s75, s36, 3
s_cmp_eq_u32 s75, 1
s_cbranch_scc1 .Lgu_w2
s_cmp_eq_u32 s75, 2
s_cbranch_scc1 .Lgu_w3
s_cmp_eq_u32 s75, 3
s_cbranch_scc1 .Lgu_w4
.Lgu_w1:
v_add_u32 v254, -1, v5
v_add_u32 v255, -1, v5
v_add_lshl_u32 v252, v7, 0, 7
v_bfi_b32 v252, s69, v252, v6
v_add_lshl_u32 v253, v5, 0, 7
v_bfi_b32 v253, s69, v253, v4
s_add_u32 m0, s45, 18432
s_xor_b32 s45, s45, 0x10000
s_mov_b32 m0, s45
s_barrier
global_load_lds_dwordx4 v2, s[38:39] offset:-2048 sc1
global_load_lds_dwordx4 v2, s[38:39] offset:-1024 sc1
global_load_lds_dwordx4 v2, s[38:39] offset:0 sc1
global_load_lds_dwordx4 v2, s[38:39] offset:1024 sc1
global_load_lds_dwordx4 v2, s[38:39] offset:2048 sc1
global_load_lds_dwordx4 v2, s[38:39] offset:3072 sc1
v_add_u32 v2, 18688, v2
s_mov_b32 s70, 32
s_waitcnt vmcnt(0) lgkmcnt(0)
s_nop 7
s_branch .Lgu_in5
.p2align 6
.Lgu_top6:
s_waitcnt lgkmcnt(5)
v_mfma_f32_16x16x32_bf16 v[74:77], a[0:3], v[106:109], 0
s_waitcnt lgkmcnt(4)
v_cvt_pk_bf16_f32 v162, v154, v155
v_cvt_pk_bf16_f32 v163, v156, v157
v_mfma_f32_16x16x32_bf16 v[74:77], a[8:11], v[110:113], v[74:77]
s_waitcnt lgkmcnt(3)
v_mfma_f32_16x16x32_bf16 v[74:77], a[16:19], v[114:117], v[74:77]
s_waitcnt lgkmcnt(2)
v_mfma_f32_16x16x32_bf16 v[74:77], a[24:27], v[118:121], v[74:77]
s_waitcnt lgkmcnt(1)
v_mul_f32 v158, v94, v102
v_mul_f32 v159, v95, v103
v_mfma_f32_16x16x32_bf16 v[74:77], a[32:35], v[122:125], v[74:77]
s_waitcnt lgkmcnt(0)
v_mul_f32 v160, v96, v104
v_mul_f32 v161, v97, v105
v_mfma_f32_16x16x32_bf16 v[74:77], a[40:43], v[126:129], v[74:77]
ds_read_b128 v[130:133], v8 offset:6144
v_cvt_pk_bf16_f32 v164, v158, v159
v_mfma_f32_16x16x32_bf16 v[82:85], a[144:147], v[106:109], 0
ds_read_b128 v[134:137], v8 offset:7168
v_cvt_pk_bf16_f32 v165, v160, v161
v_mfma_f32_16x16x32_bf16 v[82:85], a[152:155], v[110:113], v[82:85]
v_mfma_f32_16x16x32_bf16 v[82:85], a[160:163], v[114:117], v[82:85]
s_waitcnt vmcnt(6)
s_barrier
v_mfma_f32_16x16x32_bf16 v[82:85], a[168:171], v[118:121], v[82:85]
v_mfma_f32_16x16x32_bf16 v[82:85], a[176:179], v[122:125], v[82:85]
global_store_dwordx4 v3, v[162:165], s[38:39] sc1
v_mfma_f32_16x16x32_bf16 v[82:85], a[184:187], v[126:129], v[82:85]
ds_read_b128 v[138:141], v8 offset:8192
v_add_u32 v3, 147456, v3
v_mfma_f32_16x16x32_bf16 v[78:81], a[4:7], v[106:109], 0
v_mfma_f32_16x16x32_bf16 v[78:81], a[12:15], v[110:113], v[78:81]
v_mfma_f32_16x16x32_bf16 v[78:81], a[20:23], v[114:117], v[78:81]
ds_read_b128 v[142:145], v8 offset:9216
v_mfma_f32_16x16x32_bf16 v[78:81], a[28:31], v[118:121], v[78:81]
v_mfma_f32_16x16x32_bf16 v[78:81], a[36:39], v[122:125], v[78:81]
v_mfma_f32_16x16x32_bf16 v[78:81], a[44:47], v[126:129], v[78:81]
ds_read_b128 v[146:149], v8 offset:10240
v_mfma_f32_16x16x32_bf16 v[86:89], a[148:151], v[106:109], 0
v_mfma_f32_16x16x32_bf16 v[86:89], a[156:159], v[110:113], v[86:89]
v_mfma_f32_16x16x32_bf16 v[86:89], a[164:167], v[114:117], v[86:89]
ds_read_b128 v[150:153], v8 offset:11264
v_mfma_f32_16x16x32_bf16 v[86:89], a[172:175], v[118:121], v[86:89]
v_mfma_f32_16x16x32_bf16 v[86:89], a[180:183], v[122:125], v[86:89]
v_mfma_f32_16x16x32_bf16 v[86:89], a[188:191], v[126:129], v[86:89]
s_waitcnt lgkmcnt(5)
v_mfma_f32_16x16x32_bf16 v[74:77], a[48:51], v[130:133], v[74:77]
s_waitcnt lgkmcnt(4)
v_mfma_f32_16x16x32_bf16 v[74:77], a[56:59], v[134:137], v[74:77]
s_waitcnt lgkmcnt(3)
v_mfma_f32_16x16x32_bf16 v[74:77], a[64:67], v[138:141], v[74:77]
s_waitcnt lgkmcnt(2)
v_mfma_f32_16x16x32_bf16 v[74:77], a[72:75], v[142:145], v[74:77]
s_waitcnt lgkmcnt(1)
v_mfma_f32_16x16x32_bf16 v[74:77], a[80:83], v[146:149], v[74:77]
s_waitcnt lgkmcnt(0)
v_mfma_f32_16x16x32_bf16 v[74:77], a[88:91], v[150:153], v[74:77]
ds_read_b128 v[106:109], v8 offset:12288
v_mfma_f32_16x16x32_bf16 v[82:85], a[192:195], v[130:133], v[82:85]
v_mfma_f32_16x16x32_bf16 v[82:85], a[200:203], v[134:137], v[82:85]
v_mfma_f32_16x16x32_bf16 v[82:85], a[208:211], v[138:141], v[82:85]
ds_read_b128 v[110:113], v8 offset:13312
v_mfma_f32_16x16x32_bf16 v[82:85], a[216:219], v[142:145], v[82:85]
v_mfma_f32_16x16x32_bf16 v[82:85], a[224:227], v[146:149], v[82:85]
v_mfma_f32_16x16x32_bf16 v[82:85], a[232:235], v[150:153], v[82:85]
ds_read_b128 v[114:117], v8 offset:14336
v_mfma_f32_16x16x32_bf16 v[78:81], a[52:55], v[130:133], v[78:81]
v_mfma_f32_16x16x32_bf16 v[78:81], a[60:63], v[134:137], v[78:81]
)"
R"(v_mfma_f32_16x16x32_bf16 v[78:81], a[68:71], v[138:141], v[78:81]
ds_read_b128 v[118:121], v8 offset:15360
v_mfma_f32_16x16x32_bf16 v[78:81], a[76:79], v[142:145], v[78:81]
v_mfma_f32_16x16x32_bf16 v[78:81], a[84:87], v[146:149], v[78:81]
v_mfma_f32_16x16x32_bf16 v[78:81], a[92:95], v[150:153], v[78:81]
ds_read_b128 v[122:125], v8 offset:16384
v_mfma_f32_16x16x32_bf16 v[86:89], a[196:199], v[130:133], v[86:89]
v_mfma_f32_16x16x32_bf16 v[86:89], a[204:207], v[134:137], v[86:89]
v_mfma_f32_16x16x32_bf16 v[86:89], a[212:215], v[138:141], v[86:89]
ds_read_b128 v[126:129], v8 offset:17408
v_mfma_f32_16x16x32_bf16 v[86:89], a[220:223], v[142:145], v[86:89]
s_xor_b32 s45, s45, 0x10000
v_mfma_f32_16x16x32_bf16 v[86:89], a[228:231], v[146:149], v[86:89]
s_add_u32 m0, s45, 0
v_mfma_f32_16x16x32_bf16 v[86:89], a[236:239], v[150:153], v[86:89]
s_waitcnt lgkmcnt(5)
v_mfma_f32_16x16x32_bf16 v[74:77], a[96:99], v[106:109], v[74:77]
s_barrier
s_waitcnt lgkmcnt(4)
v_mfma_f32_16x16x32_bf16 v[74:77], a[104:107], v[110:113], v[74:77]
s_waitcnt lgkmcnt(3)
global_load_lds_dwordx4 v2, s[38:39] offset:-2048 sc1
v_mfma_f32_16x16x32_bf16 v[74:77], a[112:115], v[114:117], v[74:77]
s_waitcnt lgkmcnt(2)
v_mfma_f32_16x16x32_bf16 v[74:77], a[120:123], v[118:121], v[74:77]
s_waitcnt lgkmcnt(1)
v_mfma_f32_16x16x32_bf16 v[74:77], a[128:131], v[122:125], v[74:77]
s_waitcnt lgkmcnt(0)
global_load_lds_dwordx4 v2, s[38:39] offset:-1024 sc1
v_mfma_f32_16x16x32_bf16 v[74:77], a[136:139], v[126:129], v[74:77]
v_mfma_f32_16x16x32_bf16 v[82:85], v[26:29], v[106:109], v[82:85]
v_mfma_f32_16x16x32_bf16 v[82:85], v[34:37], v[110:113], v[82:85]
global_load_lds_dwordx4 v2, s[38:39] offset:0 sc1
v_mfma_f32_16x16x32_bf16 v[82:85], v[42:45], v[114:117], v[82:85]
v_mfma_f32_16x16x32_bf16 v[82:85], v[50:53], v[118:121], v[82:85]
v_mfma_f32_16x16x32_bf16 v[82:85], v[58:61], v[122:125], v[82:85]
global_load_lds_dwordx4 v2, s[38:39] offset:1024 sc1
v_mfma_f32_16x16x32_bf16 v[82:85], v[66:69], v[126:129], v[82:85]
v_mfma_f32_16x16x32_bf16 v[78:81], a[100:103], v[106:109], v[78:81]
s_waitcnt vmcnt(5)
s_barrier
v_mfma_f32_16x16x32_bf16 v[78:81], a[108:111], v[110:113], v[78:81]
global_load_lds_dwordx4 v2, s[38:39] offset:2048 sc1
v_mfma_f32_16x16x32_bf16 v[78:81], a[116:119], v[114:117], v[78:81]
v_mfma_f32_16x16x32_bf16 v[78:81], a[124:127], v[118:121], v[78:81]
ds_read_b128 v[130:133], v8 offset:18432
v_mfma_f32_16x16x32_bf16 v[78:81], a[132:135], v[122:125], v[78:81]
ds_read_b128 v[134:137], v8 offset:19456
global_load_lds_dwordx4 v2, s[38:39] offset:3072 sc1
v_mfma_f32_16x16x32_bf16 v[78:81], a[140:143], v[126:129], v[78:81]
ds_read_b128 v[138:141], v8 offset:20480
v_add_u32 v2, 18688, v2
v_mfma_f32_16x16x32_bf16 v[86:89], v[30:33], v[106:109], v[86:89]
ds_read_b128 v[142:145], v8 offset:21504
v_min_u32 v2, v2, v9
v_mfma_f32_16x16x32_bf16 v[86:89], v[38:41], v[110:113], v[86:89]
ds_read_b128 v[146:149], v8 offset:22528
v_mul_f32 v154, v74, v82
v_mfma_f32_16x16x32_bf16 v[86:89], v[46:49], v[114:117], v[86:89]
ds_read_b128 v[150:153], v8 offset:23552
v_mul_f32 v155, v75, v83
v_mfma_f32_16x16x32_bf16 v[86:89], v[54:57], v[118:121], v[86:89]
v_mul_f32 v156, v76, v84
v_mul_f32 v157, v77, v85
v_mfma_f32_16x16x32_bf16 v[86:89], v[62:65], v[122:125], v[86:89]
v_cvt_pk_bf16_f32 v162, v154, v155
v_cvt_pk_bf16_f32 v163, v156, v157
v_mfma_f32_16x16x32_bf16 v[86:89], v[70:73], v[126:129], v[86:89]
s_waitcnt lgkmcnt(5)
v_mfma_f32_16x16x32_bf16 v[90:93], a[0:3], v[130:133], 0
s_waitcnt lgkmcnt(4)
v_mfma_f32_16x16x32_bf16 v[90:93], a[8:11], v[134:137], v[90:93]
s_waitcnt lgkmcnt(3)
v_mfma_f32_16x16x32_bf16 v[90:93], a[16:19], v[138:141], v[90:93]
s_waitcnt lgkmcnt(2)
v_mfma_f32_16x16x32_bf16 v[90:93], a[24:27], v[142:145], v[90:93]
s_waitcnt lgkmcnt(1)
v_mul_f32 v158, v78, v86
v_mul_f32 v159, v79, v87
v_mfma_f32_16x16x32_bf16 v[90:93], a[32:35], v[146:149], v[90:93]
s_waitcnt lgkmcnt(0)
v_mul_f32 v160, v80, v88
v_mul_f32 v161, v81, v89
v_mfma_f32_16x16x32_bf16 v[90:93], a[40:43], v[150:153], v[90:93]
ds_read_b128 v[106:109], v8 offset:24576
v_cvt_pk_bf16_f32 v164, v158, v159
v_mfma_f32_16x16x32_bf16 v[98:101], a[144:147], v[130:133], 0
ds_read_b128 v[110:113], v8 offset:25600
v_cvt_pk_bf16_f32 v165, v160, v161
v_mfma_f32_16x16x32_bf16 v[98:101], a[152:155], v[134:137], v[98:101]
v_mfma_f32_16x16x32_bf16 v[98:101], a[160:163], v[138:141], v[98:101]
s_waitcnt vmcnt(6)
s_barrier
v_mfma_f32_16x16x32_bf16 v[98:101], a[168:171], v[142:145], v[98:101]
v_mfma_f32_16x16x32_bf16 v[98:101], a[176:179], v[146:149], v[98:101]
global_store_dwordx4 v3, v[162:165], s[38:39] sc1
v_mfma_f32_16x16x32_bf16 v[98:101], a[184:187], v[150:153], v[98:101]
ds_read_b128 v[114:117], v8 offset:26624
v_add_u32 v3, 147456, v3
v_mfma_f32_16x16x32_bf16 v[94:97], a[4:7], v[130:133], 0
v_mfma_f32_16x16x32_bf16 v[94:97], a[12:15], v[134:137], v[94:97]
v_mfma_f32_16x16x32_bf16 v[94:97], a[20:23], v[138:141], v[94:97]
ds_read_b128 v[118:121], v8 offset:27648
v_mfma_f32_16x16x32_bf16 v[94:97], a[28:31], v[142:145], v[94:97]
v_mfma_f32_16x16x32_bf16 v[94:97], a[36:39], v[146:149], v[94:97]
v_mfma_f32_16x16x32_bf16 v[94:97], a[44:47], v[150:153], v[94:97]
ds_read_b128 v[122:125], v8 offset:28672
v_mfma_f32_16x16x32_bf16 v[102:105], a[148:151], v[130:133], 0
v_mfma_f32_16x16x32_bf16 v[102:105], a[156:159], v[134:137], v[102:105]
v_mfma_f32_16x16x32_bf16 v[102:105], a[164:167], v[138:141], v[102:105]
ds_read_b128 v[126:129], v8 offset:29696
v_mfma_f32_16x16x32_bf16 v[102:105], a[172:175], v[142:145], v[102:105]
v_mfma_f32_16x16x32_bf16 v[102:105], a[180:183], v[146:149], v[102:105]
v_mfma_f32_16x16x32_bf16 v[102:105], a[188:191], v[150:153], v[102:105]
s_waitcnt lgkmcnt(5)
v_mfma_f32_16x16x32_bf16 v[90:93], a[48:51], v[106:109], v[90:93]
s_waitcnt lgkmcnt(4)
v_mfma_f32_16x16x32_bf16 v[90:93], a[56:59], v[110:113], v[90:93]
s_waitcnt lgkmcnt(3)
v_mfma_f32_16x16x32_bf16 v[90:93], a[64:67], v[114:117], v[90:93]
s_waitcnt lgkmcnt(2)
v_mfma_f32_16x16x32_bf16 v[90:93], a[72:75], v[118:121], v[90:93]
s_waitcnt lgkmcnt(1)
v_mfma_f32_16x16x32_bf16 v[90:93], a[80:83], v[122:125], v[90:93]
s_waitcnt lgkmcnt(0)
v_mfma_f32_16x16x32_bf16 v[90:93], a[88:91], v[126:129], v[90:93]
ds_read_b128 v[130:133], v8 offset:30720
v_mfma_f32_16x16x32_bf16 v[98:101], a[192:195], v[106:109], v[98:101]
v_mfma_f32_16x16x32_bf16 v[98:101], a[200:203], v[110:113], v[98:101]
v_mfma_f32_16x16x32_bf16 v[98:101], a[208:211], v[114:117], v[98:101]
ds_read_b128 v[134:137], v8 offset:31744
v_mfma_f32_16x16x32_bf16 v[98:101], a[216:219], v[118:121], v[98:101]
v_mfma_f32_16x16x32_bf16 v[98:101], a[224:227], v[122:125], v[98:101]
v_mfma_f32_16x16x32_bf16 v[98:101], a[232:235], v[126:129], v[98:101]
ds_read_b128 v[138:141], v8 offset:32768
v_mfma_f32_16x16x32_bf16 v[94:97], a[52:55], v[106:109], v[94:97]
v_mfma_f32_16x16x32_bf16 v[94:97], a[60:63], v[110:113], v[94:97]
v_mfma_f32_16x16x32_bf16 v[94:97], a[68:71], v[114:117], v[94:97]
ds_read_b128 v[142:145], v8 offset:33792
v_mfma_f32_16x16x32_bf16 v[94:97], a[76:79], v[118:121], v[94:97]
v_mfma_f32_16x16x32_bf16 v[94:97], a[84:87], v[122:125], v[94:97]
v_mfma_f32_16x16x32_bf16 v[94:97], a[92:95], v[126:129], v[94:97]
ds_read_b128 v[146:149], v8 offset:34816
v_mfma_f32_16x16x32_bf16 v[102:105], a[196:199], v[106:109], v[102:105]
.Lgu_in5:
v_mfma_f32_16x16x32_bf16 v[102:105], a[204:207], v[110:113], v[102:105]
v_mfma_f32_16x16x32_bf16 v[102:105], a[212:215], v[114:117], v[102:105]
ds_read_b128 v[150:153], v8 offset:35840
v_mfma_f32_16x16x32_bf16 v[102:105], a[220:223], v[118:121], v[102:105]
v_mfma_f32_16x16x32_bf16 v[102:105], a[228:231], v[122:125], v[102:105]
s_add_u32 m0, s45, 18432
v_mfma_f32_16x16x32_bf16 v[102:105], a[236:239], v[126:129], v[102:105]
s_waitcnt lgkmcnt(5)
v_mfma_f32_16x16x32_bf16 v[90:93], a[96:99], v[130:133], v[90:93]
s_barrier
s_waitcnt lgkmcnt(4)
v_mfma_f32_16x16x32_bf16 v[90:93], a[104:107], v[134:137], v[90:93]
s_waitcnt lgkmcnt(3)
global_load_lds_dwordx4 v2, s[38:39] offset:-2048 sc1
v_mfma_f32_16x16x32_bf16 v[90:93], a[112:115], v[138:141], v[90:93]
s_waitcnt lgkmcnt(2)
v_mfma_f32_16x16x32_bf16 v[90:93], a[120:123], v[142:145], v[90:93]
s_waitcnt lgkmcnt(1)
v_mfma_f32_16x16x32_bf16 v[90:93], a[128:131], v[146:149], v[90:93]
s_waitcnt lgkmcnt(0)
global_load_lds_dwordx4 v2, s[38:39] offset:-1024 sc1
v_mfma_f32_16x16x32_bf16 v[90:93], a[136:139], v[150:153], v[90:93]
v_mfma_f32_16x16x32_bf16 v[98:101], v[26:29], v[130:133], v[98:101]
v_mfma_f32_16x16x32_bf16 v[98:101], v[34:37], v[134:137], v[98:101]
global_load_lds_dwordx4 v2, s[38:39] offset:0 sc1
v_mfma_f32_16x16x32_bf16 v[98:101], v[42:45], v[138:141], v[98:101]
v_mfma_f32_16x16x32_bf16 v[98:101], v[50:53], v[142:145], v[98:101]
v_xor_b32 v8, 0x10000, v8
v_mfma_f32_16x16x32_bf16 v[98:101], v[58:61], v[146:149], v[98:101]
global_load_lds_dwordx4 v2, s[38:39] offset:1024 sc1
v_mfma_f32_16x16x32_bf16 v[98:101], v[66:69], v[150:153], v[98:101]
v_mfma_f32_16x16x32_bf16 v[94:97], a[100:103], v[130:133], v[94:97]
s_waitcnt vmcnt(5)
s_barrier
v_mfma_f32_16x16x32_bf16 v[94:97], a[108:111], v[134:137], v[94:97]
global_load_lds_dwordx4 v2, s[38:39] offset:2048 sc1
v_mfma_f32_16x16x32_bf16 v[94:97], a[116:119], v[138:141], v[94:97]
v_mfma_f32_16x16x32_bf16 v[94:97], a[124:127], v[142:145], v[94:97]
ds_read_b128 v[106:109], v8 offset:0
v_mfma_f32_16x16x32_bf16 v[94:97], a[132:135], v[146:149], v[94:97]
ds_read_b128 v[110:113], v8 offset:1024
global_load_lds_dwordx4 v2, s[38:39] offset:3072 sc1
v_mfma_f32_16x16x32_bf16 v[94:97], a[140:143], v[150:153], v[94:97]
ds_read_b128 v[114:117], v8 offset:2048
v_add_u32 v2, 18688, v2
v_mfma_f32_16x16x32_bf16 v[102:105], v[30:33], v[130:133], v[102:105]
ds_read_b128 v[118:121], v8 offset:3072
v_min_u32 v2, v2, v9
v_mfma_f32_16x16x32_bf16 v[102:105], v[38:41], v[134:137], v[102:105]
ds_read_b128 v[122:125], v8 offset:4096
v_mul_f32 v154, v90, v98
v_mfma_f32_16x16x32_bf16 v[102:105], v[46:49], v[138:141], v[102:105]
ds_read_b128 v[126:129], v8 offset:5120
v_mul_f32 v155, v91, v99
v_mfma_f32_16x16x32_bf16 v[102:105], v[54:57], v[142:145], v[102:105]
v_mul_f32 v156, v92, v100
v_mul_f32 v157, v93, v101
v_mfma_f32_16x16x32_bf16 v[102:105], v[62:65], v[146:149], v[102:105]
v_mfma_f32_16x16x32_bf16 v[102:105], v[70:73], v[150:153], v[102:105]
s_sub_u32 s70, s70, 1
s_cbranch_scc0 .Lgu_top6
s_waitcnt vmcnt(6) lgkmcnt(0)
s_barrier
v_mul_f32 v154, v90, v98
v_mul_f32 v155, v91, v99
v_mul_f32 v156, v92, v100
v_mul_f32 v157, v93, v101
v_cvt_pk_bf16_f32 v162, v154, v155
v_cvt_pk_bf16_f32 v163, v156, v157
v_mul_f32 v158, v94, v102
v_mul_f32 v159, v95, v103
v_mul_f32 v160, v96, v104
v_mul_f32 v161, v97, v105
v_cvt_pk_bf16_f32 v164, v158, v159
v_cvt_pk_bf16_f32 v165, v160, v161
global_store_dwordx4 v3, v[162:165], s[38:39] sc1
v_mov_b32 v231, 192
ds_read_b32 v231, v231 offset:36864
s_waitcnt vmcnt(0)
s_barrier
s_waitcnt lgkmcnt(0)
v_readfirstlane_b32 s62, v231
s_nop 3
s_max_i32 s62, s62, 2
s_min_i32 s62, s62, 66
s_add_u32 s62, s62, -2
s_branch .Lgu_fin
.Lgu_w2:
v_add_u32 v254, -1, v5
v_add_u32 v255, -1, v5
v_add_lshl_u32 v252, v7, 0, 7
v_bfi_b32 v252, s69, v252, v6
v_add_lshl_u32 v253, v5, 0, 7
v_bfi_b32 v253, s69, v253, v4
s_add_u32 m0, s45, 18432
s_xor_b32 s45, s45, 0x10000
s_mov_b32 m0, s45
s_barrier
global_load_lds_dwordx4 v2, s[38:39] offset:-2048 sc1
global_load_lds_dwordx4 v2, s[38:39] offset:-1024 sc1
global_load_lds_dwordx4 v2, s[38:39] offset:0 sc1
global_load_lds_dwordx4 v2, s[38:39] offset:1024 sc1
global_load_lds_dwordx4 v2, s[38:39] offset:2048 sc1
global_load_lds_dwordx4 v2, s[38:39] offset:3072 sc1
v_add_u32 v2, 18688, v2
s_mov_b32 s70, 32
s_waitcnt vmcnt(0) lgkmcnt(0)
s_nop 7
s_branch .Lgu_in8
.p2align 6
.Lgu_top9:
s_waitcnt lgkmcnt(5)
v_mfma_f32_16x16x32_bf16 v[74:77], a[0:3], v[106:109], 0
s_waitcnt lgkmcnt(4)
v_mul_f32 v157, v93, v101
v_cvt_pk_bf16_f32 v162, v154, v155
v_mfma_f32_16x16x32_bf16 v[74:77], a[8:11], v[110:113], v[74:77]
s_waitcnt lgkmcnt(3)
v_cvt_pk_bf16_f32 v163, v156, v157
v_mfma_f32_16x16x32_bf16 v[74:77], a[16:19], v[114:117], v[74:77]
s_waitcnt lgkmcnt(2)
v_mfma_f32_16x16x32_bf16 v[74:77], a[24:27], v[118:121], v[74:77]
s_waitcnt lgkmcnt(1)
v_mul_f32 v158, v94, v102
v_mul_f32 v159, v95, v103
v_mfma_f32_16x16x32_bf16 v[74:77], a[32:35], v[122:125], v[74:77]
s_waitcnt lgkmcnt(0)
v_mul_f32 v160, v96, v104
v_mul_f32 v161, v97, v105
v_mfma_f32_16x16x32_bf16 v[74:77], a[40:43], v[126:129], v[74:77]
ds_read_b128 v[130:133], v8 offset:6144
v_cvt_pk_bf16_f32 v164, v158, v159
v_mfma_f32_16x16x32_bf16 v[82:85], a[144:147], v[106:109], 0
ds_read_b128 v[134:137], v8 offset:7168
v_cvt_pk_bf16_f32 v165, v160, v161
v_mfma_f32_16x16x32_bf16 v[82:85], a[152:155], v[110:113], v[82:85]
v_mfma_f32_16x16x32_bf16 v[82:85], a[160:163], v[114:117], v[82:85]
s_waitcnt vmcnt(6)
s_barrier
v_mfma_f32_16x16x32_bf16 v[82:85], a[168:171], v[118:121], v[82:85]
s_mov_b64 exec, 1
global_store_dword v252, v7, s[38:39] sc1
s_mov_b64 exec, -1
v_mfma_f32_16x16x32_bf16 v[82:85], a[176:179], v[122:125], v[82:85]
v_add_u32 v7, 1, v7
v_add_lshl_u32 v252, v7, 0, 7
v_mfma_f32_16x16x32_bf16 v[82:85], a[184:187], v[126:129], v[82:85]
ds_read_b128 v[138:141], v8 offset:8192
v_bfi_b32 v252, s69, v252, v6
v_mfma_f32_16x16x32_bf16 v[78:81], a[4:7], v[106:109], 0
global_store_dwordx4 v3, v[162:165], s[38:39] sc1
v_mfma_f32_16x16x32_bf16 v[78:81], a[12:15], v[110:113], v[78:81]
v_add_u32 v3, 147456, v3
v_mfma_f32_16x16x32_bf16 v[78:81], a[20:23], v[114:117], v[78:81]
ds_read_b128 v[142:145], v8 offset:9216
v_mfma_f32_16x16x32_bf16 v[78:81], a[28:31], v[118:121], v[78:81]
v_mfma_f32_16x16x32_bf16 v[78:81], a[36:39], v[122:125], v[78:81]
v_mfma_f32_16x16x32_bf16 v[78:81], a[44:47], v[126:129], v[78:81]
ds_read_b128 v[146:149], v8 offset:10240
v_mfma_f32_16x16x32_bf16 v[86:89], a[148:151], v[106:109], 0
v_mfma_f32_16x16x32_bf16 v[86:89], a[156:159], v[110:113], v[86:89]
v_mfma_f32_16x16x32_bf16 v[86:89], a[164:167], v[114:117], v[86:89]
ds_read_b128 v[150:153], v8 offset:11264
v_mfma_f32_16x16x32_bf16 v[86:89], a[172:175], v[118:121], v[86:89]
v_mfma_f32_16x16x32_bf16 v[86:89], a[180:183], v[122:125], v[86:89]
v_mfma_f32_16x16x32_bf16 v[86:89], a[188:191], v[126:129], v[86:89]
s_waitcnt lgkmcnt(5)
v_mfma_f32_16x16x32_bf16 v[74:77], a[48:51], v[130:133], v[74:77]
s_waitcnt lgkmcnt(4)
v_mfma_f32_16x16x32_bf16 v[74:77], a[56:59], v[134:137], v[74:77]
s_waitcnt lgkmcnt(3)
v_mfma_f32_16x16x32_bf16 v[74:77], a[64:67], v[138:141], v[74:77]
s_waitcnt lgkmcnt(2)
v_mfma_f32_16x16x32_bf16 v[74:77], a[72:75], v[142:145], v[74:77]
s_waitcnt lgkmcnt(1)
v_mfma_f32_16x16x32_bf16 v[74:77], a[80:83], v[146:149], v[74:77]
s_waitcnt lgkmcnt(0)
v_mfma_f32_16x16x32_bf16 v[74:77], a[88:91], v[150:153], v[74:77]
ds_read_b128 v[106:109], v8 offset:12288
v_mfma_f32_16x16x32_bf16 v[82:85], a[192:195], v[130:133], v[82:85]
v_mfma_f32_16x16x32_bf16 v[82:85], a[200:203], v[134:137], v[82:85]
v_mfma_f32_16x16x32_bf16 v[82:85], a[208:211], v[138:141], v[82:85]
ds_read_b128 v[110:113], v8 offset:13312
v_mfma_f32_16x16x32_bf16 v[82:85], a[216:219], v[142:145], v[82:85]
v_mfma_f32_16x16x32_bf16 v[82:85], a[224:227], v[146:149], v[82:85]
v_mfma_f32_16x16x32_bf16 v[82:85], a[232:235], v[150:153], v[82:85]
ds_read_b128 v[114:117], v8 offset:14336
v_mfma_f32_16x16x32_bf16 v[78:81], a[52:55], v[130:133], v[78:81]
v_mfma_f32_16x16x32_bf16 v[78:81], a[60:63], v[134:137], v[78:81]
v_mfma_f32_16x16x32_bf16 v[78:81], a[68:71], v[138:141], v[78:81]
ds_read_b128 v[118:121], v8 offset:15360
v_mfma_f32_16x16x32_bf16 v[78:81], a[76:79], v[142:145], v[78:81]
v_mfma_f32_16x16x32_bf16 v[78:81], a[84:87], v[146:149], v[78:81]
v_mfma_f32_16x16x32_bf16 v[78:81], a[92:95], v[150:153], v[78:81]
ds_read_b128 v[122:125], v8 offset:16384
v_mfma_f32_16x16x32_bf16 v[86:89], a[196:199], v[130:133], v[86:89]
v_mfma_f32_16x16x32_bf16 v[86:89], a[204:207], v[134:137], v[86:89]
v_mfma_f32_16x16x32_bf16 v[86:89], a[212:215], v[138:141], v[86:89]
ds_read_b128 v[126:129], v8 offset:17408
v_mfma_f32_16x16x32_bf16 v[86:89], a[220:223], v[142:145], v[86:89]
s_xor_b32 s45, s45, 0x10000
v_mfma_f32_16x16x32_bf16 v[86:89], a[228:231], v[146:149], v[86:89]
s_add_u32 m0, s45, 0
v_mfma_f32_16x16x32_bf16 v[86:89], a[236:239], v[150:153], v[86:89]
s_waitcnt lgkmcnt(5)
v_mfma_f32_16x16x32_bf16 v[74:77], a[96:99], v[106:109], v[74:77]
s_barrier
s_waitcnt lgkmcnt(4)
v_mfma_f32_16x16x32_bf16 v[74:77], a[104:107], v[110:113], v[74:77]
s_waitcnt lgkmcnt(3)
v_mfma_f32_16x16x32_bf16 v[74:77], a[112:115], v[114:117], v[74:77]
s_waitcnt lgkmcnt(2)
global_load_lds_dwordx4 v2, s[38:39] offset:-2048 sc1
v_mfma_f32_16x16x32_bf16 v[74:77], a[120:123], v[118:121], v[74:77]
s_waitcnt lgkmcnt(1)
v_mfma_f32_16x16x32_bf16 v[74:77], a[128:131], v[122:125], v[74:77]
s_waitcnt lgkmcnt(0)
v_mfma_f32_16x16x32_bf16 v[74:77], a[136:139], v[126:129], v[74:77]
global_load_lds_dwordx4 v2, s[38:39] offset:-1024 sc1
v_mfma_f32_16x16x32_bf16 v[82:85], v[26:29], v[106:109], v[82:85]
v_mfma_f32_16x16x32_bf16 v[82:85], v[34:37], v[110:113], v[82:85]
v_mfma_f32_16x16x32_bf16 v[82:85], v[42:45], v[114:117], v[82:85]
global_load_lds_dwordx4 v2, s[38:39] offset:0 sc1
v_mfma_f32_16x16x32_bf16 v[82:85], v[50:53], v[118:121], v[82:85]
v_mfma_f32_16x16x32_bf16 v[82:85], v[58:61], v[122:125], v[82:85]
v_mfma_f32_16x16x32_bf16 v[82:85], v[66:69], v[126:129], v[82:85]
v_mfma_f32_16x16x32_bf16 v[78:81], a[100:103], v[106:109], v[78:81]
s_waitcnt vmcnt(5)
s_barrier
v_mfma_f32_16x16x32_bf16 v[78:81], a[108:111], v[110:113], v[78:81]
global_load_lds_dwordx4 v2, s[38:39] offset:1024 sc1
v_mfma_f32_16x16x32_bf16 v[78:81], a[116:119], v[114:117], v[78:81]
v_mfma_f32_16x16x32_bf16 v[78:81], a[124:127], v[118:121], v[78:81]
ds_read_b128 v[130:133], v8 offset:18432
v_mfma_f32_16x16x32_bf16 v[78:81], a[132:135], v[122:125], v[78:81]
ds_read_b128 v[134:137], v8 offset:19456
global_load_lds_dwordx4 v2, s[38:39] offset:2048 sc1
v_mfma_f32_16x16x32_bf16 v[78:81], a[140:143], v[126:129], v[78:81]
ds_read_b128 v[138:141], v8 offset:20480
v_mul_f32 v154, v74, v82
v_mfma_f32_16x16x32_bf16 v[86:89], v[30:33], v[106:109], v[86:89]
ds_read_b128 v[142:145], v8 offset:21504
v_mul_f32 v155, v75, v83
v_mfma_f32_16x16x32_bf16 v[86:89], v[38:41], v[110:113], v[86:89]
ds_read_b128 v[146:149], v8 offset:22528
global_load_lds_dwordx4 v2, s[38:39] offset:3072 sc1
v_mfma_f32_16x16x32_bf16 v[86:89], v[46:49], v[114:117], v[86:89]
ds_read_b128 v[150:153], v8 offset:23552
v_add_u32 v2, 18688, v2
v_mfma_f32_16x16x32_bf16 v[86:89], v[54:57], v[118:121], v[86:89]
v_min_u32 v2, v2, v9
v_mul_f32 v156, v76, v84
v_mfma_f32_16x16x32_bf16 v[86:89], v[62:65], v[122:125], v[86:89]
v_mul_f32 v157, v77, v85
v_cvt_pk_bf16_f32 v162, v154, v155
)"
R"(v_mfma_f32_16x16x32_bf16 v[86:89], v[70:73], v[126:129], v[86:89]
s_waitcnt lgkmcnt(5)
v_cvt_pk_bf16_f32 v163, v156, v157
v_mfma_f32_16x16x32_bf16 v[90:93], a[0:3], v[130:133], 0
s_waitcnt lgkmcnt(4)
v_mfma_f32_16x16x32_bf16 v[90:93], a[8:11], v[134:137], v[90:93]
s_waitcnt lgkmcnt(3)
v_mfma_f32_16x16x32_bf16 v[90:93], a[16:19], v[138:141], v[90:93]
s_waitcnt lgkmcnt(2)
v_mfma_f32_16x16x32_bf16 v[90:93], a[24:27], v[142:145], v[90:93]
s_waitcnt lgkmcnt(1)
v_mul_f32 v158, v78, v86
v_mul_f32 v159, v79, v87
v_mfma_f32_16x16x32_bf16 v[90:93], a[32:35], v[146:149], v[90:93]
s_waitcnt lgkmcnt(0)
v_mul_f32 v160, v80, v88
v_mul_f32 v161, v81, v89
v_mfma_f32_16x16x32_bf16 v[90:93], a[40:43], v[150:153], v[90:93]
ds_read_b128 v[106:109], v8 offset:24576
v_cvt_pk_bf16_f32 v164, v158, v159
v_mfma_f32_16x16x32_bf16 v[98:101], a[144:147], v[130:133], 0
ds_read_b128 v[110:113], v8 offset:25600
v_cvt_pk_bf16_f32 v165, v160, v161
v_mfma_f32_16x16x32_bf16 v[98:101], a[152:155], v[134:137], v[98:101]
v_mfma_f32_16x16x32_bf16 v[98:101], a[160:163], v[138:141], v[98:101]
s_waitcnt vmcnt(6)
s_barrier
v_mfma_f32_16x16x32_bf16 v[98:101], a[168:171], v[142:145], v[98:101]
s_mov_b64 exec, 1
global_store_dword v252, v7, s[38:39] sc1
s_mov_b64 exec, -1
v_mfma_f32_16x16x32_bf16 v[98:101], a[176:179], v[146:149], v[98:101]
v_add_u32 v7, 1, v7
v_add_lshl_u32 v252, v7, 0, 7
v_mfma_f32_16x16x32_bf16 v[98:101], a[184:187], v[150:153], v[98:101]
ds_read_b128 v[114:117], v8 offset:26624
v_bfi_b32 v252, s69, v252, v6
v_mfma_f32_16x16x32_bf16 v[94:97], a[4:7], v[130:133], 0
global_store_dwordx4 v3, v[162:165], s[38:39] sc1
v_mfma_f32_16x16x32_bf16 v[94:97], a[12:15], v[134:137], v[94:97]
v_add_u32 v3, 147456, v3
v_mfma_f32_16x16x32_bf16 v[94:97], a[20:23], v[138:141], v[94:97]
ds_read_b128 v[118:121], v8 offset:27648
v_mfma_f32_16x16x32_bf16 v[94:97], a[28:31], v[142:145], v[94:97]
v_mfma_f32_16x16x32_bf16 v[94:97], a[36:39], v[146:149], v[94:97]
v_mfma_f32_16x16x32_bf16 v[94:97], a[44:47], v[150:153], v[94:97]
ds_read_b128 v[122:125], v8 offset:28672
v_mfma_f32_16x16x32_bf16 v[102:105], a[148:151], v[130:133], 0
v_mfma_f32_16x16x32_bf16 v[102:105], a[156:159], v[134:137], v[102:105]
v_mfma_f32_16x16x32_bf16 v[102:105], a[164:167], v[138:141], v[102:105]
ds_read_b128 v[126:129], v8 offset:29696
v_mfma_f32_16x16x32_bf16 v[102:105], a[172:175], v[142:145], v[102:105]
v_mfma_f32_16x16x32_bf16 v[102:105], a[180:183], v[146:149], v[102:105]
v_mfma_f32_16x16x32_bf16 v[102:105], a[188:191], v[150:153], v[102:105]
s_waitcnt lgkmcnt(5)
v_mfma_f32_16x16x32_bf16 v[90:93], a[48:51], v[106:109], v[90:93]
s_waitcnt lgkmcnt(4)
v_mfma_f32_16x16x32_bf16 v[90:93], a[56:59], v[110:113], v[90:93]
s_waitcnt lgkmcnt(3)
v_mfma_f32_16x16x32_bf16 v[90:93], a[64:67], v[114:117], v[90:93]
s_waitcnt lgkmcnt(2)
v_mfma_f32_16x16x32_bf16 v[90:93], a[72:75], v[118:121], v[90:93]
s_waitcnt lgkmcnt(1)
v_mfma_f32_16x16x32_bf16 v[90:93], a[80:83], v[122:125], v[90:93]
s_waitcnt lgkmcnt(0)
v_mfma_f32_16x16x32_bf16 v[90:93], a[88:91], v[126:129], v[90:93]
ds_read_b128 v[130:133], v8 offset:30720
v_mfma_f32_16x16x32_bf16 v[98:101], a[192:195], v[106:109], v[98:101]
v_mfma_f32_16x16x32_bf16 v[98:101], a[200:203], v[110:113], v[98:101]
v_mfma_f32_16x16x32_bf16 v[98:101], a[208:211], v[114:117], v[98:101]
ds_read_b128 v[134:137], v8 offset:31744
v_mfma_f32_16x16x32_bf16 v[98:101], a[216:219], v[118:121], v[98:101]
v_mfma_f32_16x16x32_bf16 v[98:101], a[224:227], v[122:125], v[98:101]
v_mfma_f32_16x16x32_bf16 v[98:101], a[232:235], v[126:129], v[98:101]
ds_read_b128 v[138:141], v8 offset:32768
v_mfma_f32_16x16x32_bf16 v[94:97], a[52:55], v[106:109], v[94:97]
v_mfma_f32_16x16x32_bf16 v[94:97], a[60:63], v[110:113], v[94:97]
v_mfma_f32_16x16x32_bf16 v[94:97], a[68:71], v[114:117], v[94:97]
ds_read_b128 v[142:145], v8 offset:33792
v_mfma_f32_16x16x32_bf16 v[94:97], a[76:79], v[118:121], v[94:97]
v_mfma_f32_16x16x32_bf16 v[94:97], a[84:87], v[122:125], v[94:97]
v_mfma_f32_16x16x32_bf16 v[94:97], a[92:95], v[126:129], v[94:97]
ds_read_b128 v[146:149], v8 offset:34816
v_mfma_f32_16x16x32_bf16 v[102:105], a[196:199], v[106:109], v[102:105]
.Lgu_in8:
v_mfma_f32_16x16x32_bf16 v[102:105], a[204:207], v[110:113], v[102:105]
v_mfma_f32_16x16x32_bf16 v[102:105], a[212:215], v[114:117], v[102:105]
ds_read_b128 v[150:153], v8 offset:35840
v_mfma_f32_16x16x32_bf16 v[102:105], a[220:223], v[118:121], v[102:105]
v_mfma_f32_16x16x32_bf16 v[102:105], a[228:231], v[122:125], v[102:105]
s_add_u32 m0, s45, 18432
v_mfma_f32_16x16x32_bf16 v[102:105], a[236:239], v[126:129], v[102:105]
s_waitcnt lgkmcnt(5)
v_mfma_f32_16x16x32_bf16 v[90:93], a[96:99], v[130:133], v[90:93]
s_barrier
s_waitcnt lgkmcnt(4)
v_mfma_f32_16x16x32_bf16 v[90:93], a[104:107], v[134:137], v[90:93]
s_waitcnt lgkmcnt(3)
v_mfma_f32_16x16x32_bf16 v[90:93], a[112:115], v[138:141], v[90:93]
s_waitcnt lgkmcnt(2)
global_load_lds_dwordx4 v2, s[38:39] offset:-2048 sc1
v_mfma_f32_16x16x32_bf16 v[90:93], a[120:123], v[142:145], v[90:93]
s_waitcnt lgkmcnt(1)
v_mfma_f32_16x16x32_bf16 v[90:93], a[128:131], v[146:149], v[90:93]
s_waitcnt lgkmcnt(0)
v_mfma_f32_16x16x32_bf16 v[90:93], a[136:139], v[150:153], v[90:93]
global_load_lds_dwordx4 v2, s[38:39] offset:-1024 sc1
v_mfma_f32_16x16x32_bf16 v[98:101], v[26:29], v[130:133], v[98:101]
v_mfma_f32_16x16x32_bf16 v[98:101], v[34:37], v[134:137], v[98:101]
v_mfma_f32_16x16x32_bf16 v[98:101], v[42:45], v[138:141], v[98:101]
global_load_lds_dwordx4 v2, s[38:39] offset:0 sc1
v_mfma_f32_16x16x32_bf16 v[98:101], v[50:53], v[142:145], v[98:101]
v_xor_b32 v8, 0x10000, v8
v_mfma_f32_16x16x32_bf16 v[98:101], v[58:61], v[146:149], v[98:101]
v_mfma_f32_16x16x32_bf16 v[98:101], v[66:69], v[150:153], v[98:101]
v_mfma_f32_16x16x32_bf16 v[94:97], a[100:103], v[130:133], v[94:97]
s_waitcnt vmcnt(5)
s_barrier
v_mfma_f32_16x16x32_bf16 v[94:97], a[108:111], v[134:137], v[94:97]
global_load_lds_dwordx4 v2, s[38:39] offset:1024 sc1
v_mfma_f32_16x16x32_bf16 v[94:97], a[116:119], v[138:141], v[94:97]
v_mfma_f32_16x16x32_bf16 v[94:97], a[124:127], v[142:145], v[94:97]
ds_read_b128 v[106:109], v8 offset:0
v_mfma_f32_16x16x32_bf16 v[94:97], a[132:135], v[146:149], v[94:97]
ds_read_b128 v[110:113], v8 offset:1024
global_load_lds_dwordx4 v2, s[38:39] offset:2048 sc1
v_mfma_f32_16x16x32_bf16 v[94:97], a[140:143], v[150:153], v[94:97]
ds_read_b128 v[114:117], v8 offset:2048
v_mul_f32 v154, v90, v98
v_mfma_f32_16x16x32_bf16 v[102:105], v[30:33], v[130:133], v[102:105]
ds_read_b128 v[118:121], v8 offset:3072
v_mul_f32 v155, v91, v99
v_mfma_f32_16x16x32_bf16 v[102:105], v[38:41], v[134:137], v[102:105]
ds_read_b128 v[122:125], v8 offset:4096
global_load_lds_dwordx4 v2, s[38:39] offset:3072 sc1
v_mfma_f32_16x16x32_bf16 v[102:105], v[46:49], v[138:141], v[102:105]
ds_read_b128 v[126:129], v8 offset:5120
v_add_u32 v2, 18688, v2
v_mfma_f32_16x16x32_bf16 v[102:105], v[54:57], v[142:145], v[102:105]
v_min_u32 v2, v2, v9
v_mul_f32 v156, v92, v100
v_mfma_f32_16x16x32_bf16 v[102:105], v[62:65], v[146:149], v[102:105]
v_mfma_f32_16x16x32_bf16 v[102:105], v[70:73], v[150:153], v[102:105]
s_sub_u32 s70, s70, 1
s_cbranch_scc0 .Lgu_top9
s_waitcnt vmcnt(6) lgkmcnt(0)
s_barrier
s_mov_b64 exec, 1
global_store_dword v252, v7, s[38:39] sc1
s_mov_b64 exec, -1
v_mul_f32 v154, v90, v98
v_mul_f32 v155, v91, v99
v_mul_f32 v156, v92, v100
v_mul_f32 v157, v93, v101
v_cvt_pk_bf16_f32 v162, v154, v155
v_cvt_pk_bf16_f32 v163, v156, v157
v_mul_f32 v158, v94, v102
v_mul_f32 v159, v95, v103
v_mul_f32 v160, v96, v104
v_mul_f32 v161, v97, v105
v_cvt_pk_bf16_f32 v164, v158, v159
v_cvt_pk_bf16_f32 v165, v160, v161
global_store_dwordx4 v3, v[162:165], s[38:39] sc1
v_mov_b32 v231, 192
ds_read_b32 v231, v231 offset:36864
s_waitcnt vmcnt(0)
s_barrier
v_add_u32 v7, 8, v7
v_mbcnt_lo_u32_b32 v252, -1, 0
v_mbcnt_hi_u32_b32 v252, -1, v252
v_add_lshl_u32 v252, v252, 0, 7
v_bfi_b32 v252, s69, v252, v6
s_mov_b64 exec, 0xff
global_store_dword v252, v7, s[38:39] sc1
s_mov_b64 exec, -1
s_waitcnt lgkmcnt(0)
v_readfirstlane_b32 s62, v231
s_nop 3
s_max_i32 s62, s62, 2
s_min_i32 s62, s62, 66
s_add_u32 s62, s62, -1
s_branch .Lgu_fin
.Lgu_w3:
v_add_u32 v254, -1, v5
v_add_u32 v255, -1, v5
v_add_lshl_u32 v252, v7, 0, 7
v_bfi_b32 v252, s69, v252, v6
v_add_lshl_u32 v253, v5, 0, 7
v_bfi_b32 v253, s69, v253, v4
s_add_u32 m0, s45, 18432
v_add_lshl_u32 v253, v5, 0, 7
v_bfi_b32 v253, s69, v253, v4
global_load_dword v254, v253, s[38:39] sc1
s_waitcnt vmcnt(0)
v_sub_u32 v254, v254, v5
v_cmp_lt_u32 vcc, 0x7f, v254
s_cbranch_vccnz .Lgu_sp16
.Lgu_rt17:
v_add_u32 v5, 1, v5
s_xor_b32 s45, s45, 0x10000
s_mov_b32 m0, s45
s_barrier
global_load_lds_dwordx4 v2, s[38:39] offset:-2048 sc1
global_load_lds_dwordx4 v2, s[38:39] offset:-1024 sc1
global_load_lds_dwordx4 v2, s[38:39] offset:0 sc1
global_load_lds_dwordx4 v2, s[38:39] offset:1024 sc1
global_load_lds_dwordx4 v2, s[38:39] offset:2048 sc1
global_load_lds_dwordx4 v2, s[38:39] offset:3072 sc1
v_add_u32 v2, 18688, v2
s_mov_b32 s70, 32
s_waitcnt vmcnt(0) lgkmcnt(0)
s_nop 7
s_branch .Lgu_in15
.p2align 6
.Lgu_top18:
s_waitcnt lgkmcnt(5)
v_mfma_f32_16x16x32_bf16 v[74:77], a[0:3], v[106:109], 0
s_waitcnt lgkmcnt(4)
v_mul_f32 v157, v93, v101
v_cvt_pk_bf16_f32 v162, v154, v155
v_mfma_f32_16x16x32_bf16 v[74:77], a[8:11], v[110:113], v[74:77]
s_waitcnt lgkmcnt(3)
v_cvt_pk_bf16_f32 v163, v156, v157
v_mfma_f32_16x16x32_bf16 v[74:77], a[16:19], v[114:117], v[74:77]
s_waitcnt lgkmcnt(2)
global_load_dword v254, v253, s[38:39] sc1
v_mfma_f32_16x16x32_bf16 v[74:77], a[24:27], v[118:121], v[74:77]
s_waitcnt lgkmcnt(1)
v_mul_f32 v158, v94, v102
v_mul_f32 v159, v95, v103
v_mfma_f32_16x16x32_bf16 v[74:77], a[32:35], v[122:125], v[74:77]
s_waitcnt lgkmcnt(0)
v_mul_f32 v160, v96, v104
v_mul_f32 v161, v97, v105
v_mfma_f32_16x16x32_bf16 v[74:77], a[40:43], v[126:129], v[74:77]
ds_read_b128 v[130:133], v8 offset:6144
v_cvt_pk_bf16_f32 v164, v158, v159
v_mfma_f32_16x16x32_bf16 v[82:85], a[144:147], v[106:109], 0
ds_read_b128 v[134:137], v8 offset:7168
v_cvt_pk_bf16_f32 v165, v160, v161
v_mfma_f32_16x16x32_bf16 v[82:85], a[152:155], v[110:113], v[82:85]
v_mfma_f32_16x16x32_bf16 v[82:85], a[160:163], v[114:117], v[82:85]
s_waitcnt vmcnt(7)
s_barrier
v_mfma_f32_16x16x32_bf16 v[82:85], a[168:171], v[118:121], v[82:85]
v_mfma_f32_16x16x32_bf16 v[82:85], a[176:179], v[122:125], v[82:85]
v_mfma_f32_16x16x32_bf16 v[82:85], a[184:187], v[126:129], v[82:85]
ds_read_b128 v[138:141], v8 offset:8192
v_mfma_f32_16x16x32_bf16 v[78:81], a[4:7], v[106:109], 0
global_store_dwordx4 v3, v[162:165], s[38:39] sc1
v_mfma_f32_16x16x32_bf16 v[78:81], a[12:15], v[110:113], v[78:81]
v_add_u32 v3, 147456, v3
v_mfma_f32_16x16x32_bf16 v[78:81], a[20:23], v[114:117], v[78:81]
ds_read_b128 v[142:145], v8 offset:9216
v_mfma_f32_16x16x32_bf16 v[78:81], a[28:31], v[118:121], v[78:81]
v_mfma_f32_16x16x32_bf16 v[78:81], a[36:39], v[122:125], v[78:81]
v_mfma_f32_16x16x32_bf16 v[78:81], a[44:47], v[126:129], v[78:81]
ds_read_b128 v[146:149], v8 offset:10240
v_mfma_f32_16x16x32_bf16 v[86:89], a[148:151], v[106:109], 0
v_mfma_f32_16x16x32_bf16 v[86:89], a[156:159], v[110:113], v[86:89]
v_mfma_f32_16x16x32_bf16 v[86:89], a[164:167], v[114:117], v[86:89]
ds_read_b128 v[150:153], v8 offset:11264
v_mfma_f32_16x16x32_bf16 v[86:89], a[172:175], v[118:121], v[86:89]
v_mfma_f32_16x16x32_bf16 v[86:89], a[180:183], v[122:125], v[86:89]
v_mfma_f32_16x16x32_bf16 v[86:89], a[188:191], v[126:129], v[86:89]
s_waitcnt lgkmcnt(5)
v_mfma_f32_16x16x32_bf16 v[74:77], a[48:51], v[130:133], v[74:77]
s_waitcnt lgkmcnt(4)
v_mfma_f32_16x16x32_bf16 v[74:77], a[56:59], v[134:137], v[74:77]
s_waitcnt lgkmcnt(3)
v_mfma_f32_16x16x32_bf16 v[74:77], a[64:67], v[138:141], v[74:77]
s_waitcnt lgkmcnt(2)
v_mfma_f32_16x16x32_bf16 v[74:77], a[72:75], v[142:145], v[74:77]
s_waitcnt lgkmcnt(1)
v_mfma_f32_16x16x32_bf16 v[74:77], a[80:83], v[146:149], v[74:77]
s_waitcnt lgkmcnt(0)
v_mfma_f32_16x16x32_bf16 v[74:77], a[88:91], v[150:153], v[74:77]
ds_read_b128 v[106:109], v8 offset:12288
v_mfma_f32_16x16x32_bf16 v[82:85], a[192:195], v[130:133], v[82:85]
v_mfma_f32_16x16x32_bf16 v[82:85], a[200:203], v[134:137], v[82:85]
v_mfma_f32_16x16x32_bf16 v[82:85], a[208:211], v[138:141], v[82:85]
ds_read_b128 v[110:113], v8 offset:13312
v_mfma_f32_16x16x32_bf16 v[82:85], a[216:219], v[142:145], v[82:85]
v_mfma_f32_16x16x32_bf16 v[82:85], a[224:227], v[146:149], v[82:85]
v_mfma_f32_16x16x32_bf16 v[82:85], a[232:235], v[150:153], v[82:85]
ds_read_b128 v[114:117], v8 offset:14336
v_mfma_f32_16x16x32_bf16 v[78:81], a[52:55], v[130:133], v[78:81]
v_mfma_f32_16x16x32_bf16 v[78:81], a[60:63], v[134:137], v[78:81]
v_mfma_f32_16x16x32_bf16 v[78:81], a[68:71], v[138:141], v[78:81]
ds_read_b128 v[118:121], v8 offset:15360
v_mfma_f32_16x16x32_bf16 v[78:81], a[76:79], v[142:145], v[78:81]
v_mfma_f32_16x16x32_bf16 v[78:81], a[84:87], v[146:149], v[78:81]
v_mfma_f32_16x16x32_bf16 v[78:81], a[92:95], v[150:153], v[78:81]
ds_read_b128 v[122:125], v8 offset:16384
v_mfma_f32_16x16x32_bf16 v[86:89], a[196:199], v[130:133], v[86:89]
v_mfma_f32_16x16x32_bf16 v[86:89], a[204:207], v[134:137], v[86:89]
s_waitcnt vmcnt(1)
v_sub_u32 v254, v254, v5
v_cmp_lt_u32 vcc, 0x7f, v254
v_mfma_f32_16x16x32_bf16 v[86:89], a[212:215], v[138:141], v[86:89]
ds_read_b128 v[126:129], v8 offset:17408
v_mfma_f32_16x16x32_bf16 v[86:89], a[220:223], v[142:145], v[86:89]
s_xor_b32 s45, s45, 0x10000
v_mfma_f32_16x16x32_bf16 v[86:89], a[228:231], v[146:149], v[86:89]
s_cbranch_vccnz .Lgu_sp11
.Lgu_rt12:
s_add_u32 m0, s45, 0
v_mfma_f32_16x16x32_bf16 v[86:89], a[236:239], v[150:153], v[86:89]
s_waitcnt lgkmcnt(5)
v_mfma_f32_16x16x32_bf16 v[74:77], a[96:99], v[106:109], v[74:77]
s_barrier
s_waitcnt lgkmcnt(4)
v_mfma_f32_16x16x32_bf16 v[74:77], a[104:107], v[110:113], v[74:77]
s_waitcnt lgkmcnt(3)
v_add_u32 v5, 1, v5
v_add_lshl_u32 v253, v5, 0, 7
v_mfma_f32_16x16x32_bf16 v[74:77], a[112:115], v[114:117], v[74:77]
s_waitcnt lgkmcnt(2)
v_bfi_b32 v253, s69, v253, v4
v_mfma_f32_16x16x32_bf16 v[74:77], a[120:123], v[118:121], v[74:77]
s_waitcnt lgkmcnt(1)
global_load_lds_dwordx4 v2, s[38:39] offset:-2048 sc1
v_mfma_f32_16x16x32_bf16 v[74:77], a[128:131], v[122:125], v[74:77]
s_waitcnt lgkmcnt(0)
v_mfma_f32_16x16x32_bf16 v[74:77], a[136:139], v[126:129], v[74:77]
v_mfma_f32_16x16x32_bf16 v[82:85], v[26:29], v[106:109], v[82:85]
global_load_lds_dwordx4 v2, s[38:39] offset:-1024 sc1
v_mfma_f32_16x16x32_bf16 v[82:85], v[34:37], v[110:113], v[82:85]
v_mfma_f32_16x16x32_bf16 v[82:85], v[42:45], v[114:117], v[82:85]
v_mfma_f32_16x16x32_bf16 v[82:85], v[50:53], v[118:121], v[82:85]
global_load_lds_dwordx4 v2, s[38:39] offset:0 sc1
v_mfma_f32_16x16x32_bf16 v[82:85], v[58:61], v[122:125], v[82:85]
v_mfma_f32_16x16x32_bf16 v[82:85], v[66:69], v[126:129], v[82:85]
v_mfma_f32_16x16x32_bf16 v[78:81], a[100:103], v[106:109], v[78:81]
s_barrier
v_mfma_f32_16x16x32_bf16 v[78:81], a[108:111], v[110:113], v[78:81]
global_load_lds_dwordx4 v2, s[38:39] offset:1024 sc1
v_mfma_f32_16x16x32_bf16 v[78:81], a[116:119], v[114:117], v[78:81]
v_mfma_f32_16x16x32_bf16 v[78:81], a[124:127], v[118:121], v[78:81]
ds_read_b128 v[130:133], v8 offset:18432
v_mfma_f32_16x16x32_bf16 v[78:81], a[132:135], v[122:125], v[78:81]
ds_read_b128 v[134:137], v8 offset:19456
global_load_lds_dwordx4 v2, s[38:39] offset:2048 sc1
v_mfma_f32_16x16x32_bf16 v[78:81], a[140:143], v[126:129], v[78:81]
ds_read_b128 v[138:141], v8 offset:20480
v_mul_f32 v154, v74, v82
v_mfma_f32_16x16x32_bf16 v[86:89], v[30:33], v[106:109], v[86:89]
ds_read_b128 v[142:145], v8 offset:21504
v_mul_f32 v155, v75, v83
v_mfma_f32_16x16x32_bf16 v[86:89], v[38:41], v[110:113], v[86:89]
ds_read_b128 v[146:149], v8 offset:22528
global_load_lds_dwordx4 v2, s[38:39] offset:3072 sc1
v_mfma_f32_16x16x32_bf16 v[86:89], v[46:49], v[114:117], v[86:89]
ds_read_b128 v[150:153], v8 offset:23552
v_add_u32 v2, 18688, v2
v_mfma_f32_16x16x32_bf16 v[86:89], v[54:57], v[118:121], v[86:89]
v_min_u32 v2, v2, v9
v_mul_f32 v156, v76, v84
v_mfma_f32_16x16x32_bf16 v[86:89], v[62:65], v[122:125], v[86:89]
v_mul_f32 v157, v77, v85
v_cvt_pk_bf16_f32 v162, v154, v155
v_mfma_f32_16x16x32_bf16 v[86:89], v[70:73], v[126:129], v[86:89]
s_waitcnt lgkmcnt(5)
v_cvt_pk_bf16_f32 v163, v156, v157
v_mfma_f32_16x16x32_bf16 v[90:93], a[0:3], v[130:133], 0
s_waitcnt lgkmcnt(4)
v_mfma_f32_16x16x32_bf16 v[90:93], a[8:11], v[134:137], v[90:93]
s_waitcnt lgkmcnt(3)
v_mfma_f32_16x16x32_bf16 v[90:93], a[16:19], v[138:141], v[90:93]
s_waitcnt lgkmcnt(2)
global_load_dword v255, v253, s[38:39] sc1
v_mfma_f32_16x16x32_bf16 v[90:93], a[24:27], v[142:145], v[90:93]
s_waitcnt lgkmcnt(1)
v_mul_f32 v158, v78, v86
v_mul_f32 v159, v79, v87
v_mfma_f32_16x16x32_bf16 v[90:93], a[32:35], v[146:149], v[90:93]
s_waitcnt lgkmcnt(0)
v_mul_f32 v160, v80, v88
v_mul_f32 v161, v81, v89
v_mfma_f32_16x16x32_bf16 v[90:93], a[40:43], v[150:153], v[90:93]
ds_read_b128 v[106:109], v8 offset:24576
v_cvt_pk_bf16_f32 v164, v158, v159
v_mfma_f32_16x16x32_bf16 v[98:101], a[144:147], v[130:133], 0
ds_read_b128 v[110:113], v8 offset:25600
v_cvt_pk_bf16_f32 v165, v160, v161
v_mfma_f32_16x16x32_bf16 v[98:101], a[152:155], v[134:137], v[98:101]
v_mfma_f32_16x16x32_bf16 v[98:101], a[160:163], v[138:141], v[98:101]
s_waitcnt vmcnt(7)
s_barrier
v_mfma_f32_16x16x32_bf16 v[98:101], a[168:171], v[142:145], v[98:101]
v_mfma_f32_16x16x32_bf16 v[98:101], a[176:179], v[146:149], v[98:101]
v_mfma_f32_16x16x32_bf16 v[98:101], a[184:187], v[150:153], v[98:101]
ds_read_b128 v[114:117], v8 offset:26624
v_mfma_f32_16x16x32_bf16 v[94:97], a[4:7], v[130:133], 0
global_store_dwordx4 v3, v[162:165], s[38:39] sc1
v_mfma_f32_16x16x32_bf16 v[94:97], a[12:15], v[134:137], v[94:97]
v_add_u32 v3, 147456, v3
v_mfma_f32_16x16x32_bf16 v[94:97], a[20:23], v[138:141], v[94:97]
ds_read_b128 v[118:121], v8 offset:27648
v_mfma_f32_16x16x32_bf16 v[94:97], a[28:31], v[142:145], v[94:97]
)"
R"(v_mfma_f32_16x16x32_bf16 v[94:97], a[36:39], v[146:149], v[94:97]
v_mfma_f32_16x16x32_bf16 v[94:97], a[44:47], v[150:153], v[94:97]
ds_read_b128 v[122:125], v8 offset:28672
v_mfma_f32_16x16x32_bf16 v[102:105], a[148:151], v[130:133], 0
v_mfma_f32_16x16x32_bf16 v[102:105], a[156:159], v[134:137], v[102:105]
v_mfma_f32_16x16x32_bf16 v[102:105], a[164:167], v[138:141], v[102:105]
ds_read_b128 v[126:129], v8 offset:29696
v_mfma_f32_16x16x32_bf16 v[102:105], a[172:175], v[142:145], v[102:105]
v_mfma_f32_16x16x32_bf16 v[102:105], a[180:183], v[146:149], v[102:105]
v_mfma_f32_16x16x32_bf16 v[102:105], a[188:191], v[150:153], v[102:105]
s_waitcnt lgkmcnt(5)
v_mfma_f32_16x16x32_bf16 v[90:93], a[48:51], v[106:109], v[90:93]
s_waitcnt lgkmcnt(4)
v_mfma_f32_16x16x32_bf16 v[90:93], a[56:59], v[110:113], v[90:93]
s_waitcnt lgkmcnt(3)
v_mfma_f32_16x16x32_bf16 v[90:93], a[64:67], v[114:117], v[90:93]
s_waitcnt lgkmcnt(2)
v_mfma_f32_16x16x32_bf16 v[90:93], a[72:75], v[118:121], v[90:93]
s_waitcnt lgkmcnt(1)
v_mfma_f32_16x16x32_bf16 v[90:93], a[80:83], v[122:125], v[90:93]
s_waitcnt lgkmcnt(0)
v_mfma_f32_16x16x32_bf16 v[90:93], a[88:91], v[126:129], v[90:93]
ds_read_b128 v[130:133], v8 offset:30720
v_mfma_f32_16x16x32_bf16 v[98:101], a[192:195], v[106:109], v[98:101]
v_mfma_f32_16x16x32_bf16 v[98:101], a[200:203], v[110:113], v[98:101]
v_mfma_f32_16x16x32_bf16 v[98:101], a[208:211], v[114:117], v[98:101]
ds_read_b128 v[134:137], v8 offset:31744
v_mfma_f32_16x16x32_bf16 v[98:101], a[216:219], v[118:121], v[98:101]
v_mfma_f32_16x16x32_bf16 v[98:101], a[224:227], v[122:125], v[98:101]
v_mfma_f32_16x16x32_bf16 v[98:101], a[232:235], v[126:129], v[98:101]
ds_read_b128 v[138:141], v8 offset:32768
v_mfma_f32_16x16x32_bf16 v[94:97], a[52:55], v[106:109], v[94:97]
v_mfma_f32_16x16x32_bf16 v[94:97], a[60:63], v[110:113], v[94:97]
v_mfma_f32_16x16x32_bf16 v[94:97], a[68:71], v[114:117], v[94:97]
ds_read_b128 v[142:145], v8 offset:33792
v_mfma_f32_16x16x32_bf16 v[94:97], a[76:79], v[118:121], v[94:97]
v_mfma_f32_16x16x32_bf16 v[94:97], a[84:87], v[122:125], v[94:97]
v_mfma_f32_16x16x32_bf16 v[94:97], a[92:95], v[126:129], v[94:97]
ds_read_b128 v[146:149], v8 offset:34816
v_mfma_f32_16x16x32_bf16 v[102:105], a[196:199], v[106:109], v[102:105]
.Lgu_in15:
v_mfma_f32_16x16x32_bf16 v[102:105], a[204:207], v[110:113], v[102:105]
s_waitcnt vmcnt(1)
v_sub_u32 v255, v255, v5
v_cmp_lt_u32 vcc, 0x7f, v255
v_mfma_f32_16x16x32_bf16 v[102:105], a[212:215], v[114:117], v[102:105]
ds_read_b128 v[150:153], v8 offset:35840
v_mfma_f32_16x16x32_bf16 v[102:105], a[220:223], v[118:121], v[102:105]
v_mfma_f32_16x16x32_bf16 v[102:105], a[228:231], v[122:125], v[102:105]
s_cbranch_vccnz .Lgu_sp13
.Lgu_rt14:
s_add_u32 m0, s45, 18432
v_mfma_f32_16x16x32_bf16 v[102:105], a[236:239], v[126:129], v[102:105]
s_waitcnt lgkmcnt(5)
v_mfma_f32_16x16x32_bf16 v[90:93], a[96:99], v[130:133], v[90:93]
s_barrier
s_waitcnt lgkmcnt(4)
v_mfma_f32_16x16x32_bf16 v[90:93], a[104:107], v[134:137], v[90:93]
s_waitcnt lgkmcnt(3)
v_add_u32 v5, 1, v5
v_add_lshl_u32 v253, v5, 0, 7
v_mfma_f32_16x16x32_bf16 v[90:93], a[112:115], v[138:141], v[90:93]
s_waitcnt lgkmcnt(2)
v_bfi_b32 v253, s69, v253, v4
v_mfma_f32_16x16x32_bf16 v[90:93], a[120:123], v[142:145], v[90:93]
s_waitcnt lgkmcnt(1)
global_load_lds_dwordx4 v2, s[38:39] offset:-2048 sc1
v_mfma_f32_16x16x32_bf16 v[90:93], a[128:131], v[146:149], v[90:93]
s_waitcnt lgkmcnt(0)
v_mfma_f32_16x16x32_bf16 v[90:93], a[136:139], v[150:153], v[90:93]
v_mfma_f32_16x16x32_bf16 v[98:101], v[26:29], v[130:133], v[98:101]
global_load_lds_dwordx4 v2, s[38:39] offset:-1024 sc1
v_mfma_f32_16x16x32_bf16 v[98:101], v[34:37], v[134:137], v[98:101]
v_mfma_f32_16x16x32_bf16 v[98:101], v[42:45], v[138:141], v[98:101]
v_mfma_f32_16x16x32_bf16 v[98:101], v[50:53], v[142:145], v[98:101]
v_xor_b32 v8, 0x10000, v8
global_load_lds_dwordx4 v2, s[38:39] offset:0 sc1
v_mfma_f32_16x16x32_bf16 v[98:101], v[58:61], v[146:149], v[98:101]
v_mfma_f32_16x16x32_bf16 v[98:101], v[66:69], v[150:153], v[98:101]
v_mfma_f32_16x16x32_bf16 v[94:97], a[100:103], v[130:133], v[94:97]
s_barrier
v_mfma_f32_16x16x32_bf16 v[94:97], a[108:111], v[134:137], v[94:97]
global_load_lds_dwordx4 v2, s[38:39] offset:1024 sc1
v_mfma_f32_16x16x32_bf16 v[94:97], a[116:119], v[138:141], v[94:97]
v_mfma_f32_16x16x32_bf16 v[94:97], a[124:127], v[142:145], v[94:97]
ds_read_b128 v[106:109], v8 offset:0
v_mfma_f32_16x16x32_bf16 v[94:97], a[132:135], v[146:149], v[94:97]
ds_read_b128 v[110:113], v8 offset:1024
global_load_lds_dwordx4 v2, s[38:39] offset:2048 sc1
v_mfma_f32_16x16x32_bf16 v[94:97], a[140:143], v[150:153], v[94:97]
ds_read_b128 v[114:117], v8 offset:2048
v_mul_f32 v154, v90, v98
v_mfma_f32_16x16x32_bf16 v[102:105], v[30:33], v[130:133], v[102:105]
ds_read_b128 v[118:121], v8 offset:3072
v_mul_f32 v155, v91, v99
v_mfma_f32_16x16x32_bf16 v[102:105], v[38:41], v[134:137], v[102:105]
ds_read_b128 v[122:125], v8 offset:4096
global_load_lds_dwordx4 v2, s[38:39] offset:3072 sc1
v_mfma_f32_16x16x32_bf16 v[102:105], v[46:49], v[138:141], v[102:105]
ds_read_b128 v[126:129], v8 offset:5120
v_add_u32 v2, 18688, v2
v_mfma_f32_16x16x32_bf16 v[102:105], v[54:57], v[142:145], v[102:105]
v_min_u32 v2, v2, v9
v_mul_f32 v156, v92, v100
v_mfma_f32_16x16x32_bf16 v[102:105], v[62:65], v[146:149], v[102:105]
v_mfma_f32_16x16x32_bf16 v[102:105], v[70:73], v[150:153], v[102:105]
s_sub_u32 s70, s70, 1
s_cbranch_scc0 .Lgu_top18
s_waitcnt vmcnt(6) lgkmcnt(0)
s_barrier
v_mul_f32 v154, v90, v98
v_mul_f32 v155, v91, v99
v_mul_f32 v156, v92, v100
v_mul_f32 v157, v93, v101
v_cvt_pk_bf16_f32 v162, v154, v155
v_cvt_pk_bf16_f32 v163, v156, v157
v_mul_f32 v158, v94, v102
v_mul_f32 v159, v95, v103
v_mul_f32 v160, v96, v104
v_mul_f32 v161, v97, v105
v_cvt_pk_bf16_f32 v164, v158, v159
v_cvt_pk_bf16_f32 v165, v160, v161
global_store_dwordx4 v3, v[162:165], s[38:39] sc1
v_mov_b32 v231, 192
ds_read_b32 v231, v231 offset:36864
s_waitcnt vmcnt(0)
s_barrier
s_waitcnt lgkmcnt(0)
v_readfirstlane_b32 s62, v231
s_nop 3
s_max_i32 s62, s62, 2
s_min_i32 s62, s62, 66
s_add_u32 s62, s62, 0
s_branch .Lgu_fin
.Lgu_w4:
v_mov_b32 v206, -1
v_mov_b32 v207, -1
v_mov_b32 v208, -1
v_mov_b32 v209, -1
v_add_u32 v232, 6856704, v10
global_store_dwordx4 v232, v[206:209], s[38:39] offset:0 sc1
global_store_dwordx4 v232, v[206:209], s[38:39] offset:1024 sc1
global_store_dwordx4 v232, v[206:209], s[38:39] offset:2048 sc1
v_add_u32 v232, 6967296, v10
global_store_dwordx4 v232, v[206:209], s[38:39] offset:0 sc1
global_store_dwordx4 v232, v[206:209], s[38:39] offset:1024 sc1
global_store_dwordx4 v232, v[206:209], s[38:39] offset:2048 sc1
s_nop 1
s_add_u32 s84, s54, 18688
s_addc_u32 s85, s55, 0
s_add_u32 s86, s52, 110592
s_addc_u32 s87, s53, 0
s_mov_b32 s80, 0
s_mov_b64 s[76:77], 0
s_mov_b32 s72, -14
v_add_u32 v230, -1, v17
v_add_u32 v226, -1, s37
v_add_u32 v227, -1, s37
v_add_u32 v228, -1, s37
v_add_u32 v229, -1, s37
v_add_lshl_u32 v252, v228, 0, 7
v_bfi_b32 v252, s69, v252, v18
v_add_lshl_u32 v253, v17, 0, 7
v_bfi_b32 v253, s69, v253, v16
v_add_u32 v254, -1, v5
v_add_u32 v255, -1, v5
s_add_u32 m0, s45, 18432
s_xor_b32 s45, s45, 0x10000
s_mov_b32 m0, s45
s_barrier
v_add_u32 v2, 18688, v2
s_mov_b32 s70, 32
s_waitcnt vmcnt(0) lgkmcnt(0)
s_nop 7
s_branch .Lgu_in24
.p2align 6
.Lgu_top25:
s_waitcnt lgkmcnt(5)
v_mfma_f32_16x16x32_bf16 v[74:77], a[0:3], v[106:109], 0
s_waitcnt lgkmcnt(4)
global_load_dword v230, v253, s[38:39] sc1
v_lshlrev_b32 v216, 16, v187
v_mfma_f32_16x16x32_bf16 v[74:77], a[8:11], v[110:113], v[74:77]
s_waitcnt lgkmcnt(3)
v_and_b32 v217, 0xffff0000, v187
v_add_f32_dpp v206, v210, v206 row_ror:8 row_mask:0xf bank_mask:0xf
v_mfma_f32_16x16x32_bf16 v[74:77], a[16:19], v[114:117], v[74:77]
s_waitcnt lgkmcnt(2)
v_add_f32_dpp v207, v211, v207 row_ror:8 row_mask:0xf bank_mask:0xf
v_add_f32_dpp v208, v212, v208 row_ror:8 row_mask:0xf bank_mask:0xf
v_mfma_f32_16x16x32_bf16 v[74:77], a[24:27], v[118:121], v[74:77]
s_waitcnt lgkmcnt(1)
v_mul_f32 v158, v94, v102
v_mul_f32 v159, v95, v103
v_mfma_f32_16x16x32_bf16 v[74:77], a[32:35], v[122:125], v[74:77]
s_waitcnt lgkmcnt(0)
v_mul_f32 v160, v96, v104
v_mul_f32 v161, v97, v105
v_mfma_f32_16x16x32_bf16 v[74:77], a[40:43], v[126:129], v[74:77]
ds_read_b128 v[130:133], v8 offset:6144
v_cvt_pk_bf16_f32 v164, v158, v159
v_mfma_f32_16x16x32_bf16 v[82:85], a[144:147], v[106:109], 0
ds_read_b128 v[134:137], v8 offset:7168
v_cvt_pk_bf16_f32 v165, v160, v161
v_mfma_f32_16x16x32_bf16 v[82:85], a[152:155], v[110:113], v[82:85]
v_mfma_f32_16x16x32_bf16 v[82:85], a[160:163], v[114:117], v[82:85]
s_waitcnt vmcnt(8)
s_barrier
v_mfma_f32_16x16x32_bf16 v[82:85], a[168:171], v[118:121], v[82:85]
v_add_f32_dpp v209, v213, v209 row_ror:8 row_mask:0xf bank_mask:0xf
v_fma_f32 v206, v206, v231, v214
v_mfma_f32_16x16x32_bf16 v[82:85], a[176:179], v[122:125], v[82:85]
v_fma_f32 v207, v207, v231, v215
v_fma_f32 v208, v208, v231, v216
v_mfma_f32_16x16x32_bf16 v[82:85], a[184:187], v[126:129], v[82:85]
ds_read_b128 v[138:141], v8 offset:8192
v_fma_f32 v209, v209, v231, v217
v_mfma_f32_16x16x32_bf16 v[78:81], a[4:7], v[106:109], 0
v_cvt_pk_bf16_f32 v222, v206, v207
v_cvt_pk_bf16_f32 v223, v208, v209
v_mfma_f32_16x16x32_bf16 v[78:81], a[12:15], v[110:113], v[78:81]
global_store_dwordx4 v3, v[162:165], s[38:39] sc1
v_mfma_f32_16x16x32_bf16 v[78:81], a[20:23], v[114:117], v[78:81]
ds_read_b128 v[142:145], v8 offset:9216
v_add_u32 v3, 147456, v3
v_mfma_f32_16x16x32_bf16 v[78:81], a[28:31], v[118:121], v[78:81]
ds_write_b64 v14, v[222:223]
v_mfma_f32_16x16x32_bf16 v[78:81], a[36:39], v[122:125], v[78:81]
s_waitcnt vmcnt(8)
ds_read_b64 v[224:225], v15
s_mov_b64 exec, 1
global_store_dword v252, v229, s[38:39] sc1
s_mov_b64 exec, -1
v_mfma_f32_16x16x32_bf16 v[78:81], a[44:47], v[126:129], v[78:81]
ds_read_b128 v[146:149], v8 offset:10240
v_mfma_f32_16x16x32_bf16 v[86:89], a[148:151], v[106:109], 0
v_mfma_f32_16x16x32_bf16 v[86:89], a[156:159], v[110:113], v[86:89]
v_mfma_f32_16x16x32_bf16 v[86:89], a[164:167], v[114:117], v[86:89]
ds_read_b128 v[150:153], v8 offset:11264
v_mfma_f32_16x16x32_bf16 v[86:89], a[172:175], v[118:121], v[86:89]
v_mfma_f32_16x16x32_bf16 v[86:89], a[180:183], v[122:125], v[86:89]
v_mfma_f32_16x16x32_bf16 v[86:89], a[188:191], v[126:129], v[86:89]
s_waitcnt lgkmcnt(2)
global_store_dwordx2 v13, v[224:225], s[58:59] sc1
v_mov_b32 v228, v226
v_mfma_f32_16x16x32_bf16 v[74:77], a[48:51], v[130:133], v[74:77]
v_mfma_f32_16x16x32_bf16 v[74:77], a[56:59], v[134:137], v[74:77]
v_mfma_f32_16x16x32_bf16 v[74:77], a[64:67], v[138:141], v[74:77]
v_mfma_f32_16x16x32_bf16 v[74:77], a[72:75], v[142:145], v[74:77]
s_waitcnt vmcnt(8)
v_add_f32 v200, v200, v202
s_waitcnt vmcnt(7)
v_lshlrev_b32 v206, 16, v188
v_add_f32 v201, v201, v203
s_waitcnt lgkmcnt(1)
v_mfma_f32_16x16x32_bf16 v[74:77], a[80:83], v[146:149], v[74:77]
s_waitcnt lgkmcnt(0)
v_and_b32 v207, 0xffff0000, v188
v_mul_f32 v200, 0x3ae38e39, v200
v_lshlrev_b32 v208, 16, v189
v_mfma_f32_16x16x32_bf16 v[74:77], a[88:91], v[150:153], v[74:77]
ds_read_b128 v[106:109], v8 offset:12288
v_mul_f32 v201, 0x3ae38e39, v201
v_and_b32 v209, 0xffff0000, v189
v_mfma_f32_16x16x32_bf16 v[82:85], a[192:195], v[130:133], v[82:85]
v_fma_f32 v232, -v200, v200, v201
v_lshlrev_b32 v210, 16, v190
v_max_f32 v232, 0, v232
v_mfma_f32_16x16x32_bf16 v[82:85], a[200:203], v[134:137], v[82:85]
v_and_b32 v211, 0xffff0000, v190
v_add_f32 v232, 0x3727c5ac, v232
v_mfma_f32_16x16x32_bf16 v[82:85], a[208:211], v[138:141], v[82:85]
ds_read_b128 v[110:113], v8 offset:13312
v_lshlrev_b32 v212, 16, v191
v_mfma_f32_16x16x32_bf16 v[82:85], a[216:219], v[142:145], v[82:85]
s_waitcnt vmcnt(3)
v_sub_u32 v230, v230, v17
v_cmp_lt_u32 vcc, 0x7f, v230
v_mfma_f32_16x16x32_bf16 v[82:85], a[224:227], v[146:149], v[82:85]
v_rcp_f32 v231, v232
v_mfma_f32_16x16x32_bf16 v[82:85], a[232:235], v[150:153], v[82:85]
ds_read_b128 v[114:117], v8 offset:14336
v_and_b32 v213, 0xffff0000, v191
v_mfma_f32_16x16x32_bf16 v[78:81], a[52:55], v[130:133], v[78:81]
s_and_b64 s[88:89], vcc, s[76:77]
s_cbranch_scc1 .Lgu_sp20
.Lgu_rt21:
v_mfma_f32_16x16x32_bf16 v[78:81], a[60:63], v[134:137], v[78:81]
s_cmp_eq_u64 vcc, 0
v_lshlrev_b32 v214, 16, v192
v_and_b32 v215, 0xffff0000, v192
v_mfma_f32_16x16x32_bf16 v[78:81], a[68:71], v[138:141], v[78:81]
s_cselect_b64 s[54:55], s[84:85], s[54:55]
ds_read_b128 v[118:121], v8 offset:15360
v_lshlrev_b32 v216, 16, v193
v_mfma_f32_16x16x32_bf16 v[78:81], a[76:79], v[142:145], v[78:81]
s_cselect_b64 s[52:53], s[86:87], s[52:53]
global_load_dwordx2 v[182:183], v12, s[54:55] offset:0 sc1
v_and_b32 v217, 0xffff0000, v193
v_mfma_f32_16x16x32_bf16 v[78:81], a[84:87], v[146:149], v[78:81]
s_cselect_b64 s[58:59], s[56:57], s[50:51]
global_load_dwordx2 v[184:185], v12, s[54:55] offset:128 sc1
v_lshlrev_b32 v218, 16, v194
v_mfma_f32_16x16x32_bf16 v[78:81], a[92:95], v[150:153], v[78:81]
s_cselect_b32 s82, 1, 0
ds_read_b128 v[122:125], v8 offset:16384
global_load_dwordx4 v[170:173], v10, s[52:53] offset:0 sc1
v_mfma_f32_16x16x32_bf16 v[86:89], a[196:199], v[130:133], v[86:89]
s_add_u32 s62, s62, s82
global_load_dwordx4 v[174:177], v10, s[52:53] offset:1024 sc1
v_and_b32 v219, 0xffff0000, v194
v_mfma_f32_16x16x32_bf16 v[86:89], a[204:207], v[134:137], v[86:89]
s_add_u32 s83, s62, s37
global_load_dwordx4 v[178:181], v10, s[52:53] offset:2048 sc1
v_add_u32 v17, s82, v17
v_add_u32 v226, -1, s83
v_mfma_f32_16x16x32_bf16 v[86:89], a[212:215], v[138:141], v[86:89]
s_cmp_lt_i32 s62, 32
ds_read_b128 v[126:129], v8 offset:17408
global_load_dwordx2 v[186:187], v11, s[54:55] sc1
v_add_lshl_u32 v253, v17, 0, 7
v_mfma_f32_16x16x32_bf16 v[86:89], a[220:223], v[142:145], v[86:89]
s_cselect_b64 s[56:57], s[46:47], s[48:49]
v_bfi_b32 v253, s69, v253, v16
v_lshlrev_b32 v220, 16, v195
v_mfma_f32_16x16x32_bf16 v[86:89], a[228:231], v[146:149], v[86:89]
s_mul_i32 s80, s62, 18432
v_and_b32 v221, 0xffff0000, v195
v_add_f32 v206, v206, v214
v_mfma_f32_16x16x32_bf16 v[86:89], a[236:239], v[150:153], v[86:89]
s_waitcnt lgkmcnt(5)
v_mfma_f32_16x16x32_bf16 v[74:77], a[96:99], v[106:109], v[74:77]
s_barrier
s_waitcnt lgkmcnt(4)
v_mfma_f32_16x16x32_bf16 v[74:77], a[104:107], v[110:113], v[74:77]
s_waitcnt lgkmcnt(3)
v_add_f32 v207, v207, v215
v_add_f32 v208, v208, v216
v_mfma_f32_16x16x32_bf16 v[74:77], a[112:115], v[114:117], v[74:77]
s_waitcnt lgkmcnt(2)
v_add_f32 v209, v209, v217
v_add_f32 v210, v210, v218
v_mfma_f32_16x16x32_bf16 v[74:77], a[120:123], v[118:121], v[74:77]
s_waitcnt lgkmcnt(1)
v_add_f32 v211, v211, v219
v_add_f32 v212, v212, v220
v_mfma_f32_16x16x32_bf16 v[74:77], a[128:131], v[122:125], v[74:77]
s_waitcnt lgkmcnt(0)
v_add_f32 v213, v213, v221
v_lshlrev_b32 v214, 16, v196
v_mfma_f32_16x16x32_bf16 v[74:77], a[136:139], v[126:129], v[74:77]
s_sub_u32 s56, s56, s80
v_and_b32 v215, 0xffff0000, v196
v_lshlrev_b32 v216, 16, v197
v_mfma_f32_16x16x32_bf16 v[82:85], v[26:29], v[106:109], v[82:85]
s_subb_u32 s57, s57, 0
v_and_b32 v217, 0xffff0000, v197
v_lshlrev_b32 v218, 16, v198
v_mfma_f32_16x16x32_bf16 v[82:85], v[34:37], v[110:113], v[82:85]
s_add_u32 s84, s54, 18688
v_and_b32 v219, 0xffff0000, v198
v_lshlrev_b32 v220, 16, v199
v_mfma_f32_16x16x32_bf16 v[82:85], v[42:45], v[114:117], v[82:85]
s_addc_u32 s85, s55, 0
v_and_b32 v221, 0xffff0000, v199
v_add_f32 v206, v206, v214
v_mfma_f32_16x16x32_bf16 v[82:85], v[50:53], v[118:121], v[82:85]
s_add_u32 s86, s52, 110592
v_add_f32 v207, v207, v215
v_add_f32 v208, v208, v216
v_mfma_f32_16x16x32_bf16 v[82:85], v[58:61], v[122:125], v[82:85]
s_addc_u32 s87, s53, 0
v_add_f32 v209, v209, v217
v_add_f32 v210, v210, v218
v_mfma_f32_16x16x32_bf16 v[82:85], v[66:69], v[126:129], v[82:85]
v_mfma_f32_16x16x32_bf16 v[78:81], a[100:103], v[106:109], v[78:81]
s_barrier
v_add_lshl_u32 v252, v228, 0, 7
v_mfma_f32_16x16x32_bf16 v[78:81], a[108:111], v[110:113], v[78:81]
s_add_u32 s72, s72, 1
v_bfi_b32 v252, s69, v252, v18
v_add_f32 v211, v211, v219
v_mfma_f32_16x16x32_bf16 v[78:81], a[116:119], v[114:117], v[78:81]
s_cmp_le_i32 s62, s72
v_add_f32 v212, v212, v220
v_add_f32 v213, v213, v221
v_mfma_f32_16x16x32_bf16 v[78:81], a[124:127], v[118:121], v[78:81]
s_cselect_b64 s[76:77], -1, 0
ds_read_b128 v[130:133], v8 offset:18432
v_lshlrev_b32 v214, 16, v204
v_mfma_f32_16x16x32_bf16 v[78:81], a[132:135], v[122:125], v[78:81]
ds_read_b128 v[134:137], v8 offset:19456
v_mul_f32 v154, v74, v82
v_mfma_f32_16x16x32_bf16 v[78:81], a[140:143], v[126:129], v[78:81]
ds_read_b128 v[138:141], v8 offset:20480
v_mul_f32 v155, v75, v83
v_mfma_f32_16x16x32_bf16 v[86:89], v[30:33], v[106:109], v[86:89]
ds_read_b128 v[142:145], v8 offset:21504
v_mul_f32 v156, v76, v84
v_mfma_f32_16x16x32_bf16 v[86:89], v[38:41], v[110:113], v[86:89]
)"
R"(ds_read_b128 v[146:149], v8 offset:22528
v_mul_f32 v157, v77, v85
v_mfma_f32_16x16x32_bf16 v[86:89], v[46:49], v[114:117], v[86:89]
ds_read_b128 v[150:153], v8 offset:23552
v_cvt_pk_bf16_f32 v162, v154, v155
v_mfma_f32_16x16x32_bf16 v[86:89], v[54:57], v[118:121], v[86:89]
v_cvt_pk_bf16_f32 v163, v156, v157
v_and_b32 v215, 0xffff0000, v204
v_mfma_f32_16x16x32_bf16 v[86:89], v[62:65], v[122:125], v[86:89]
v_lshlrev_b32 v216, 16, v205
v_and_b32 v217, 0xffff0000, v205
v_mfma_f32_16x16x32_bf16 v[86:89], v[70:73], v[126:129], v[86:89]
s_waitcnt lgkmcnt(5)
global_load_dword v230, v253, s[38:39] sc1
v_add_f32_dpp v206, v210, v206 row_ror:8 row_mask:0xf bank_mask:0xf
v_mfma_f32_16x16x32_bf16 v[90:93], a[0:3], v[130:133], 0
s_waitcnt lgkmcnt(4)
v_add_f32_dpp v207, v211, v207 row_ror:8 row_mask:0xf bank_mask:0xf
v_add_f32_dpp v208, v212, v208 row_ror:8 row_mask:0xf bank_mask:0xf
v_mfma_f32_16x16x32_bf16 v[90:93], a[8:11], v[134:137], v[90:93]
s_waitcnt lgkmcnt(3)
v_add_f32_dpp v209, v213, v209 row_ror:8 row_mask:0xf bank_mask:0xf
v_fma_f32 v206, v206, v231, v214
v_mfma_f32_16x16x32_bf16 v[90:93], a[16:19], v[138:141], v[90:93]
s_waitcnt lgkmcnt(2)
v_fma_f32 v207, v207, v231, v215
v_fma_f32 v208, v208, v231, v216
v_mfma_f32_16x16x32_bf16 v[90:93], a[24:27], v[142:145], v[90:93]
s_waitcnt lgkmcnt(1)
v_mul_f32 v158, v78, v86
v_mul_f32 v159, v79, v87
v_mfma_f32_16x16x32_bf16 v[90:93], a[32:35], v[146:149], v[90:93]
s_waitcnt lgkmcnt(0)
v_mul_f32 v160, v80, v88
v_mul_f32 v161, v81, v89
v_mfma_f32_16x16x32_bf16 v[90:93], a[40:43], v[150:153], v[90:93]
ds_read_b128 v[106:109], v8 offset:24576
v_cvt_pk_bf16_f32 v164, v158, v159
v_mfma_f32_16x16x32_bf16 v[98:101], a[144:147], v[130:133], 0
ds_read_b128 v[110:113], v8 offset:25600
v_cvt_pk_bf16_f32 v165, v160, v161
v_mfma_f32_16x16x32_bf16 v[98:101], a[152:155], v[134:137], v[98:101]
v_mfma_f32_16x16x32_bf16 v[98:101], a[160:163], v[138:141], v[98:101]
s_waitcnt vmcnt(9)
s_barrier
v_mfma_f32_16x16x32_bf16 v[98:101], a[168:171], v[142:145], v[98:101]
s_waitcnt vmcnt(7)
s_mov_b64 exec, 1
global_store_dword v252, v228, s[38:39] sc1
s_mov_b64 exec, -1
v_mfma_f32_16x16x32_bf16 v[98:101], a[176:179], v[146:149], v[98:101]
v_fma_f32 v209, v209, v231, v217
v_cvt_pk_bf16_f32 v222, v206, v207
v_mfma_f32_16x16x32_bf16 v[98:101], a[184:187], v[150:153], v[98:101]
ds_read_b128 v[114:117], v8 offset:26624
v_cvt_pk_bf16_f32 v223, v208, v209
v_mfma_f32_16x16x32_bf16 v[94:97], a[4:7], v[130:133], 0
ds_write_b64 v14, v[222:223]
v_mfma_f32_16x16x32_bf16 v[94:97], a[12:15], v[134:137], v[94:97]
global_store_dwordx4 v3, v[162:165], s[38:39] sc1
v_mfma_f32_16x16x32_bf16 v[94:97], a[20:23], v[138:141], v[94:97]
ds_read_b128 v[118:121], v8 offset:27648
v_add_u32 v3, 147456, v3
v_mfma_f32_16x16x32_bf16 v[94:97], a[28:31], v[142:145], v[94:97]
ds_read_b64 v[224:225], v15
v_mfma_f32_16x16x32_bf16 v[94:97], a[36:39], v[146:149], v[94:97]
v_mfma_f32_16x16x32_bf16 v[94:97], a[44:47], v[150:153], v[94:97]
ds_read_b128 v[122:125], v8 offset:28672
v_mfma_f32_16x16x32_bf16 v[102:105], a[148:151], v[130:133], 0
v_mfma_f32_16x16x32_bf16 v[102:105], a[156:159], v[134:137], v[102:105]
v_mfma_f32_16x16x32_bf16 v[102:105], a[164:167], v[138:141], v[102:105]
ds_read_b128 v[126:129], v8 offset:29696
v_mfma_f32_16x16x32_bf16 v[102:105], a[172:175], v[142:145], v[102:105]
v_mfma_f32_16x16x32_bf16 v[102:105], a[180:183], v[146:149], v[102:105]
s_waitcnt lgkmcnt(2)
global_store_dwordx2 v13, v[224:225], s[60:61] sc1
v_mov_b32 v229, v227
v_mfma_f32_16x16x32_bf16 v[102:105], a[188:191], v[150:153], v[102:105]
v_mfma_f32_16x16x32_bf16 v[90:93], a[48:51], v[106:109], v[90:93]
v_mfma_f32_16x16x32_bf16 v[90:93], a[56:59], v[110:113], v[90:93]
v_mfma_f32_16x16x32_bf16 v[90:93], a[64:67], v[114:117], v[90:93]
v_mfma_f32_16x16x32_bf16 v[90:93], a[72:75], v[118:121], v[90:93]
s_waitcnt vmcnt(8)
v_add_f32 v182, v182, v184
s_waitcnt vmcnt(7)
v_lshlrev_b32 v206, 16, v170
v_add_f32 v183, v183, v185
s_waitcnt lgkmcnt(1)
v_mfma_f32_16x16x32_bf16 v[90:93], a[80:83], v[122:125], v[90:93]
s_waitcnt lgkmcnt(0)
v_and_b32 v207, 0xffff0000, v170
v_mul_f32 v182, 0x3ae38e39, v182
v_lshlrev_b32 v208, 16, v171
v_mfma_f32_16x16x32_bf16 v[90:93], a[88:91], v[126:129], v[90:93]
ds_read_b128 v[130:133], v8 offset:30720
v_mul_f32 v183, 0x3ae38e39, v183
v_and_b32 v209, 0xffff0000, v171
v_mfma_f32_16x16x32_bf16 v[98:101], a[192:195], v[106:109], v[98:101]
v_fma_f32 v232, -v182, v182, v183
v_lshlrev_b32 v210, 16, v172
v_max_f32 v232, 0, v232
v_mfma_f32_16x16x32_bf16 v[98:101], a[200:203], v[110:113], v[98:101]
v_and_b32 v211, 0xffff0000, v172
v_add_f32 v232, 0x3727c5ac, v232
v_mfma_f32_16x16x32_bf16 v[98:101], a[208:211], v[114:117], v[98:101]
ds_read_b128 v[134:137], v8 offset:31744
v_lshlrev_b32 v212, 16, v173
.Lgu_in24:
v_mfma_f32_16x16x32_bf16 v[98:101], a[216:219], v[118:121], v[98:101]
s_waitcnt vmcnt(3)
v_sub_u32 v230, v230, v17
v_cmp_lt_u32 vcc, 0x7f, v230
v_mfma_f32_16x16x32_bf16 v[98:101], a[224:227], v[122:125], v[98:101]
v_rcp_f32 v231, v232
v_mfma_f32_16x16x32_bf16 v[98:101], a[232:235], v[126:129], v[98:101]
ds_read_b128 v[138:141], v8 offset:32768
v_and_b32 v213, 0xffff0000, v173
v_mfma_f32_16x16x32_bf16 v[94:97], a[52:55], v[106:109], v[94:97]
s_and_b64 s[88:89], vcc, s[76:77]
s_cbranch_scc1 .Lgu_sp22
.Lgu_rt23:
v_mfma_f32_16x16x32_bf16 v[94:97], a[60:63], v[110:113], v[94:97]
s_cmp_eq_u64 vcc, 0
v_lshlrev_b32 v214, 16, v174
v_and_b32 v215, 0xffff0000, v174
v_mfma_f32_16x16x32_bf16 v[94:97], a[68:71], v[114:117], v[94:97]
s_cselect_b64 s[54:55], s[84:85], s[54:55]
ds_read_b128 v[142:145], v8 offset:33792
v_lshlrev_b32 v216, 16, v175
v_mfma_f32_16x16x32_bf16 v[94:97], a[76:79], v[118:121], v[94:97]
s_cselect_b64 s[52:53], s[86:87], s[52:53]
global_load_dwordx2 v[200:201], v12, s[54:55] offset:0 sc1
v_and_b32 v217, 0xffff0000, v175
v_mfma_f32_16x16x32_bf16 v[94:97], a[84:87], v[122:125], v[94:97]
s_cselect_b64 s[60:61], s[56:57], s[50:51]
global_load_dwordx2 v[202:203], v12, s[54:55] offset:128 sc1
v_lshlrev_b32 v218, 16, v176
v_mfma_f32_16x16x32_bf16 v[94:97], a[92:95], v[126:129], v[94:97]
s_cselect_b32 s82, 1, 0
ds_read_b128 v[146:149], v8 offset:34816
global_load_dwordx4 v[188:191], v10, s[52:53] offset:0 sc1
v_mfma_f32_16x16x32_bf16 v[102:105], a[196:199], v[106:109], v[102:105]
s_add_u32 s62, s62, s82
global_load_dwordx4 v[192:195], v10, s[52:53] offset:1024 sc1
v_and_b32 v219, 0xffff0000, v176
v_mfma_f32_16x16x32_bf16 v[102:105], a[204:207], v[110:113], v[102:105]
s_add_u32 s83, s62, s37
global_load_dwordx4 v[196:199], v10, s[52:53] offset:2048 sc1
v_add_u32 v17, s82, v17
v_add_u32 v227, -1, s83
v_mfma_f32_16x16x32_bf16 v[102:105], a[212:215], v[114:117], v[102:105]
s_cmp_lt_i32 s62, 32
ds_read_b128 v[150:153], v8 offset:35840
global_load_dwordx2 v[204:205], v11, s[54:55] sc1
v_add_lshl_u32 v253, v17, 0, 7
v_mfma_f32_16x16x32_bf16 v[102:105], a[220:223], v[118:121], v[102:105]
s_cselect_b64 s[56:57], s[46:47], s[48:49]
v_bfi_b32 v253, s69, v253, v16
v_lshlrev_b32 v220, 16, v177
v_mfma_f32_16x16x32_bf16 v[102:105], a[228:231], v[122:125], v[102:105]
s_mul_i32 s80, s62, 18432
v_and_b32 v221, 0xffff0000, v177
v_add_f32 v206, v206, v214
v_mfma_f32_16x16x32_bf16 v[102:105], a[236:239], v[126:129], v[102:105]
s_waitcnt lgkmcnt(5)
v_mfma_f32_16x16x32_bf16 v[90:93], a[96:99], v[130:133], v[90:93]
s_barrier
s_waitcnt lgkmcnt(4)
v_mfma_f32_16x16x32_bf16 v[90:93], a[104:107], v[134:137], v[90:93]
s_waitcnt lgkmcnt(3)
v_add_f32 v207, v207, v215
v_add_f32 v208, v208, v216
v_mfma_f32_16x16x32_bf16 v[90:93], a[112:115], v[138:141], v[90:93]
s_waitcnt lgkmcnt(2)
v_add_f32 v209, v209, v217
v_add_f32 v210, v210, v218
v_mfma_f32_16x16x32_bf16 v[90:93], a[120:123], v[142:145], v[90:93]
s_waitcnt lgkmcnt(1)
v_add_f32 v211, v211, v219
v_add_f32 v212, v212, v220
v_mfma_f32_16x16x32_bf16 v[90:93], a[128:131], v[146:149], v[90:93]
s_waitcnt lgkmcnt(0)
v_add_f32 v213, v213, v221
v_lshlrev_b32 v214, 16, v178
v_mfma_f32_16x16x32_bf16 v[90:93], a[136:139], v[150:153], v[90:93]
s_sub_u32 s56, s56, s80
v_and_b32 v215, 0xffff0000, v178
v_lshlrev_b32 v216, 16, v179
v_mfma_f32_16x16x32_bf16 v[98:101], v[26:29], v[130:133], v[98:101]
s_subb_u32 s57, s57, 0
v_and_b32 v217, 0xffff0000, v179
v_lshlrev_b32 v218, 16, v180
v_mfma_f32_16x16x32_bf16 v[98:101], v[34:37], v[134:137], v[98:101]
s_add_u32 s84, s54, 18688
v_and_b32 v219, 0xffff0000, v180
v_lshlrev_b32 v220, 16, v181
v_mfma_f32_16x16x32_bf16 v[98:101], v[42:45], v[138:141], v[98:101]
s_addc_u32 s85, s55, 0
v_and_b32 v221, 0xffff0000, v181
v_add_f32 v206, v206, v214
v_mfma_f32_16x16x32_bf16 v[98:101], v[50:53], v[142:145], v[98:101]
s_add_u32 s86, s52, 110592
v_xor_b32 v8, 0x10000, v8
v_add_f32 v207, v207, v215
v_mfma_f32_16x16x32_bf16 v[98:101], v[58:61], v[146:149], v[98:101]
s_addc_u32 s87, s53, 0
v_add_f32 v208, v208, v216
v_add_f32 v209, v209, v217
v_mfma_f32_16x16x32_bf16 v[98:101], v[66:69], v[150:153], v[98:101]
v_mfma_f32_16x16x32_bf16 v[94:97], a[100:103], v[130:133], v[94:97]
s_barrier
v_add_lshl_u32 v252, v229, 0, 7
v_mfma_f32_16x16x32_bf16 v[94:97], a[108:111], v[134:137], v[94:97]
s_add_u32 s72, s72, 1
v_add_f32 v210, v210, v218
v_add_f32 v211, v211, v219
v_bfi_b32 v252, s69, v252, v18
v_mfma_f32_16x16x32_bf16 v[94:97], a[116:119], v[138:141], v[94:97]
s_cmp_le_i32 s62, s72
v_add_f32 v212, v212, v220
v_add_f32 v213, v213, v221
v_mfma_f32_16x16x32_bf16 v[94:97], a[124:127], v[142:145], v[94:97]
s_cselect_b64 s[76:77], -1, 0
ds_read_b128 v[106:109], v8 offset:0
v_lshlrev_b32 v214, 16, v186
v_mfma_f32_16x16x32_bf16 v[94:97], a[132:135], v[146:149], v[94:97]
ds_read_b128 v[110:113], v8 offset:1024
v_mul_f32 v154, v90, v98
v_mfma_f32_16x16x32_bf16 v[94:97], a[140:143], v[150:153], v[94:97]
ds_read_b128 v[114:117], v8 offset:2048
v_mul_f32 v155, v91, v99
v_mfma_f32_16x16x32_bf16 v[102:105], v[30:33], v[130:133], v[102:105]
ds_read_b128 v[118:121], v8 offset:3072
v_mul_f32 v156, v92, v100
v_mfma_f32_16x16x32_bf16 v[102:105], v[38:41], v[134:137], v[102:105]
ds_read_b128 v[122:125], v8 offset:4096
v_mul_f32 v157, v93, v101
v_mfma_f32_16x16x32_bf16 v[102:105], v[46:49], v[138:141], v[102:105]
ds_read_b128 v[126:129], v8 offset:5120
v_cvt_pk_bf16_f32 v162, v154, v155
v_mfma_f32_16x16x32_bf16 v[102:105], v[54:57], v[142:145], v[102:105]
v_cvt_pk_bf16_f32 v163, v156, v157
v_and_b32 v215, 0xffff0000, v186
v_mfma_f32_16x16x32_bf16 v[102:105], v[62:65], v[146:149], v[102:105]
v_mfma_f32_16x16x32_bf16 v[102:105], v[70:73], v[150:153], v[102:105]
s_sub_u32 s70, s70, 1
s_cbranch_scc0 .Lgu_top25
v_mov_b32 v232, s62
ds_write_b32 v1, v232 offset:36864
v_subrev_u32 v17, s62, v17
s_waitcnt vmcnt(7) lgkmcnt(0)
s_barrier
v_mul_f32 v154, v90, v98
v_mul_f32 v155, v91, v99
v_mul_f32 v156, v92, v100
v_mul_f32 v157, v93, v101
v_cvt_pk_bf16_f32 v162, v154, v155
v_cvt_pk_bf16_f32 v163, v156, v157
v_mul_f32 v158, v94, v102
v_mul_f32 v159, v95, v103
v_mul_f32 v160, v96, v104
v_mul_f32 v161, v97, v105
v_cvt_pk_bf16_f32 v164, v158, v159
v_cvt_pk_bf16_f32 v165, v160, v161
global_store_dwordx4 v3, v[162:165], s[38:39] sc1
v_mov_b32 v231, 192
ds_read_b32 v231, v231 offset:36864
s_waitcnt vmcnt(0)
s_barrier
s_waitcnt lgkmcnt(0)
v_readfirstlane_b32 s62, v231
s_nop 3
s_max_i32 s62, s62, 2
s_min_i32 s62, s62, 66
s_add_u32 s62, s62, 1
s_branch .Lgu_fin
.Lgu_fin:
s_cmp_gt_i32 s62, 63
s_cbranch_scc1 .Lgu_end
s_cmp_gt_i32 s62, 61
s_cselect_b32 s82, -2, 0
s_add_u32 s82, s82, s62
v_add_u32 v233, s82, v17
v_add_lshl_u32 v253, v233, 0, 7
v_bfi_b32 v253, s69, v253, v16
global_load_dword v230, v253, s[38:39] sc1
s_cmp_lt_i32 s62, 32
s_cselect_b64 s[56:57], s[46:47], s[48:49]
s_mul_i32 s80, s62, 18432
s_sub_u32 s56, s56, s80
s_subb_u32 s57, s57, 0
s_mov_b64 s[58:59], s[56:57]
s_mul_i32 s80, s62, 110592
s_mul_i32 s81, s62, 18688
s_mov_b64 s[52:53], s[38:39]
s_mov_b64 s[54:55], s[38:39]
s_add_u32 s52, s52, s80
s_addc_u32 s53, s53, 0
s_add_u32 s54, s54, s81
s_addc_u32 s55, s55, 0
s_waitcnt vmcnt(0)
v_sub_u32 v230, v230, v233
v_cmp_lt_u32 vcc, 0x7f, v230
s_cbranch_vccnz .Lgu_sp27
.Lgu_rt28:
.Lgu_re:
global_load_dwordx2 v[182:183], v12, s[54:55] offset:0 sc1
global_load_dwordx2 v[184:185], v12, s[54:55] offset:128 sc1
global_load_dwordx4 v[170:173], v10, s[52:53] offset:0 sc1
global_load_dwordx4 v[174:177], v10, s[52:53] offset:1024 sc1
global_load_dwordx4 v[178:181], v10, s[52:53] offset:2048 sc1
global_load_dwordx2 v[186:187], v11, s[54:55] sc1
s_waitcnt vmcnt(3)
v_max3_u32 v232, v170, v171, v172
s_waitcnt vmcnt(2)
v_max3_u32 v232, v232, v173, v174
v_max3_u32 v232, v232, v175, v176
s_waitcnt vmcnt(1)
v_max3_u32 v232, v232, v177, v178
v_max3_u32 v232, v232, v179, v180
v_max3_u32 v232, v232, v181, v181
v_cmp_eq_u32 vcc, -1, v232
s_waitcnt vmcnt(0)
s_cbranch_vccz .Lgu_ok
s_cmp_lt_i32 s62, 62
s_cbranch_scc1 .Lgu_ok
s_add_u32 s71, s71, 1
s_cmp_lt_u32 s71, 3000000
s_cbranch_scc1 .Lgu_re
.Lgu_ok:
v_add_f32 v182, v182, v184
v_lshlrev_b32 v206, 16, v170
v_add_f32 v183, v183, v185
v_and_b32 v207, 0xffff0000, v170
v_mul_f32 v182, 0x3ae38e39, v182
v_lshlrev_b32 v208, 16, v171
v_mul_f32 v183, 0x3ae38e39, v183
v_and_b32 v209, 0xffff0000, v171
v_fma_f32 v232, -v182, v182, v183
v_lshlrev_b32 v210, 16, v172
v_max_f32 v232, 0, v232
v_and_b32 v211, 0xffff0000, v172
v_add_f32 v232, 0x3727c5ac, v232
v_lshlrev_b32 v212, 16, v173
v_rcp_f32 v231, v232
v_and_b32 v213, 0xffff0000, v173
v_lshlrev_b32 v214, 16, v174
v_and_b32 v215, 0xffff0000, v174
v_lshlrev_b32 v216, 16, v175
v_and_b32 v217, 0xffff0000, v175
v_lshlrev_b32 v218, 16, v176
v_and_b32 v219, 0xffff0000, v176
v_lshlrev_b32 v220, 16, v177
v_and_b32 v221, 0xffff0000, v177
v_add_f32 v206, v206, v214
v_add_f32 v207, v207, v215
v_add_f32 v208, v208, v216
v_add_f32 v209, v209, v217
v_add_f32 v210, v210, v218
v_add_f32 v211, v211, v219
v_add_f32 v212, v212, v220
v_add_f32 v213, v213, v221
v_lshlrev_b32 v214, 16, v178
v_and_b32 v215, 0xffff0000, v178
v_lshlrev_b32 v216, 16, v179
v_and_b32 v217, 0xffff0000, v179
v_lshlrev_b32 v218, 16, v180
v_and_b32 v219, 0xffff0000, v180
v_lshlrev_b32 v220, 16, v181
v_and_b32 v221, 0xffff0000, v181
v_add_f32 v206, v206, v214
v_add_f32 v207, v207, v215
v_add_f32 v208, v208, v216
v_add_f32 v209, v209, v217
v_add_f32 v210, v210, v218
v_add_f32 v211, v211, v219
v_add_f32 v212, v212, v220
v_add_f32 v213, v213, v221
v_lshlrev_b32 v214, 16, v186
v_and_b32 v215, 0xffff0000, v186
v_lshlrev_b32 v216, 16, v187
v_and_b32 v217, 0xffff0000, v187
v_add_f32_dpp v206, v210, v206 row_ror:8 row_mask:0xf bank_mask:0xf
v_add_f32_dpp v207, v211, v207 row_ror:8 row_mask:0xf bank_mask:0xf
v_add_f32_dpp v208, v212, v208 row_ror:8 row_mask:0xf bank_mask:0xf
v_add_f32_dpp v209, v213, v209 row_ror:8 row_mask:0xf bank_mask:0xf
v_fma_f32 v206, v206, v231, v214
v_fma_f32 v207, v207, v231, v215
v_fma_f32 v208, v208, v231, v216
v_fma_f32 v209, v209, v231, v217
v_cvt_pk_bf16_f32 v222, v206, v207
v_cvt_pk_bf16_f32 v223, v208, v209
ds_write_b64 v14, v[222:223]
ds_read_b64 v[224:225], v15
s_waitcnt lgkmcnt(0)
global_store_dwordx2 v13, v[224:225], s[58:59] sc1
s_add_u32 s62, s62, 4
s_branch .Lgu_fin
.Lgu_end:
s_endpgm
)"
R"(.Lgu_sp11:
s_sleep 1
v_add_lshl_u32 v253, v5, 0, 7
v_bfi_b32 v253, s69, v253, v4
global_load_dword v254, v253, s[38:39] sc1
s_add_u32 s71, s71, 1
s_waitcnt vmcnt(0)
v_sub_u32 v254, v254, v5
v_cmp_lt_u32 vcc, 0x7f, v254
s_cbranch_vccz .Lgu_rt12
s_cmp_lt_u32 s71, 3000000
s_cbranch_scc1 .Lgu_sp11
s_branch .Lgu_rt12
.Lgu_sp13:
s_sleep 1
v_add_lshl_u32 v253, v5, 0, 7
v_bfi_b32 v253, s69, v253, v4
global_load_dword v255, v253, s[38:39] sc1
s_add_u32 s71, s71, 1
s_waitcnt vmcnt(0)
v_sub_u32 v255, v255, v5
v_cmp_lt_u32 vcc, 0x7f, v255
s_cbranch_vccz .Lgu_rt14
s_cmp_lt_u32 s71, 3000000
s_cbranch_scc1 .Lgu_sp13
s_branch .Lgu_rt14
.Lgu_sp16:
s_sleep 1
v_add_lshl_u32 v253, v5, 0, 7
v_bfi_b32 v253, s69, v253, v4
global_load_dword v254, v253, s[38:39] sc1
s_add_u32 s71, s71, 1
s_waitcnt vmcnt(0)
v_sub_u32 v254, v254, v5
v_cmp_lt_u32 vcc, 0x7f, v254
s_cbranch_vccz .Lgu_rt17
s_cmp_lt_u32 s71, 3000000
s_cbranch_scc1 .Lgu_sp16
s_branch .Lgu_rt17
.Lgu_sp20:
s_sleep 1
v_add_lshl_u32 v253, v17, 0, 7
v_bfi_b32 v253, s69, v253, v16
global_load_dword v230, v253, s[38:39] sc1
s_add_u32 s71, s71, 1
s_waitcnt vmcnt(0)
v_sub_u32 v230, v230, v17
v_cmp_lt_u32 vcc, 0x7f, v230
s_and_b64 vcc, vcc, s[76:77]
s_cbranch_vccz .Lgu_rt21
s_cmp_lt_u32 s71, 3000000
s_cbranch_scc1 .Lgu_sp20
s_branch .Lgu_rt21
.Lgu_sp22:
s_sleep 1
v_add_lshl_u32 v253, v17, 0, 7
v_bfi_b32 v253, s69, v253, v16
global_load_dword v230, v253, s[38:39] sc1
s_add_u32 s71, s71, 1
s_waitcnt vmcnt(0)
v_sub_u32 v230, v230, v17
v_cmp_lt_u32 vcc, 0x7f, v230
s_and_b64 vcc, vcc, s[76:77]
s_cbranch_vccz .Lgu_rt23
s_cmp_lt_u32 s71, 3000000
s_cbranch_scc1 .Lgu_sp22
s_branch .Lgu_rt23
.Lgu_sp27:
s_sleep 1
v_add_lshl_u32 v253, v233, 0, 7
v_bfi_b32 v253, s69, v253, v16
global_load_dword v230, v253, s[38:39] sc1
s_add_u32 s71, s71, 1
s_waitcnt vmcnt(0)
v_sub_u32 v230, v230, v233
v_cmp_lt_u32 vcc, 0x7f, v230
s_cbranch_vccz .Lgu_rt28
s_cmp_lt_u32 s71, 3000000
s_cbranch_scc1 .Lgu_sp27
s_branch .Lgu_rt28
.Le3:
)"
    :: "{v[0:31]}"(va), "{s[36:51]}"(sa), "{s[52:67]}"(sb) : "v255", "a255", "s99", "vcc", "memory");
  }
}

extern "C" void kernel_launch(void* const* d_in, const int* in_sizes, int n_in,
                              void* d_out, int out_size, void* d_ws, size_t ws_size,
                              hipStream_t stream) {
  k_main<<<256, 256, 0, stream>>>((const char*)d_in[0], (const char*)d_in[1], (const char*)d_in[2], (const char*)d_in[4],
                                  (const char*)d_in[5], (const char*)d_in[6], (char*)d_ws, (char*)d_out);
}
```

```cpp
#include <hip/hip_runtime.h>
#include <hip/hip_bf16.h>
#include <stdint.h>
typedef __attribute__((ext_vector_type(32))) unsigned u32x32;
typedef __attribute__((ext_vector_type(16))) unsigned u32x16;
constexpr unsigned RP = 80u;
constexpr unsigned PAD = 12u;
constexpr unsigned XT = 18432u;
constexpr unsigned X1P = 18688u;
constexpr unsigned HT = 147456u;
constexpr unsigned PT = 110592u;
constexpr unsigned FL = 1024u;
constexpr unsigned FR = 118784u;
constexpr unsigned WS_FL = 16384u;
constexpr unsigned WS_ST = 491520u;
constexpr unsigned WS_O = 1048576u;
constexpr unsigned WS_X1 = 6946816u;
constexpr unsigned WS_X2 = 13000704u;
constexpr unsigned WS_XD = 15396864u;
constexpr unsigned WS_P = 17977344u;
constexpr unsigned WS_H = 53366784u;
constexpr unsigned WS_END = 100552704u;
constexpr unsigned FO = 0u;
constexpr unsigned FX1 = 8192u;
constexpr unsigned FX2 = 65536u;
constexpr unsigned FP = 10240u;
constexpr unsigned FH = 28672u;
__device__ __forceinline__ void touch(const void* p) {
  asm volatile("s_mov_b32 m0, 0x27f00\n\ts_nop 0\n\tglobal_load_lds_dword %0, off" :: "v"(p) : "memory");
}
__device__ __forceinline__ unsigned uni(unsigned v) { return __builtin_amdgcn_readfirstlane(v); }
#define TOUCH_CODE(K, B, N) do { unsigned long long pc; unsigned len; int rel;                   \
    asm volatile("s_getpc_b64 %0\n.Lpc" #K ":\n\ts_mov_b32 %1, .Le" #K "-(" B ")-4\n\ts_mov_b32 %2, (" B ")-.Lpc" #K : "=s"(pc), "=s"(len), "=s"(rel)); \
    touch((const char*)pc + rel + min(((N) * 64 + lane) * 128, len)); } while (0)
__global__ __launch_bounds__(256, 1) void k_main(const char* x0, const char* wqkv, const char* wo, const char* wg, const char* wu, const char* wd,
                                                 char* ws, char* out) {
  __shared__ __attribute__((aligned(1024))) char smem[163840];
  asm volatile("" :: "s"((unsigned)(size_t)(__attribute__((address_space(3))) const char*)smem) : "memory");
  unsigned lane = threadIdx.x & 63;
  const unsigned wave = uni(threadIdx.x >> 6), xc = blockIdx.x & 7, jj = blockIdx.x >> 3;
  if (wave == 3) { if (jj < 8) TOUCH_CODE(0, ".Lpc0", jj); else if (jj < 16) TOUCH_CODE(1, ".Lb1-2048", jj - 8); else if (jj < 24) TOUCH_CODE(2, ".Lb2-2048", jj - 16); else TOUCH_CODE(3, ".Lb3-2048", jj - 24); };
  const bool ev = !(xc & 1); const unsigned xh = xc >> 1;
  if (ev && jj >= 8) { const unsigned u = min((jj - 8) * 216 + threadIdx.x, 5183u), sg = u / 3; touch(wqkv + sg * 1152 + xh * 288 + min((u - 3 * sg) * 128, 284u)); }
  if (__builtin_expect(ev && jj < 8, 1)) {
    const unsigned qa_hg = xc + (jj >> 2), qa_rho = jj & 3, pq0 = wave < 2 ? wave * 5 : 2 + wave * 4;
    unsigned qx[5];
#pragma unroll
    for (unsigned j = 0; j < 5; ++j)
      qx[j] = ((qa_rho * 32 + 31) * 16 + ((pq0 + j) & 1) * 8 + (lane >> 3)) * 1152 + ((pq0 + j) >> 1) * 128 + (((lane ^ (lane >> 3)) & 7) << 4) + 4096 - j * 1024;
    const unsigned long long xb = (unsigned long long)(x0 - 2048);
    asm volatile("s_add_u32 m0, %6, 0\n\ts_sub_u32 vcc_lo, %7, 0\n\ts_subb_u32 vcc_hi, %8, 0\n\tglobal_load_lds_dwordx4 %1, vcc offset:-2048 sc1\n\tglobal_load_lds_dwordx4 %2, vcc offset:-1024 sc1\n\tglobal_load_lds_dwordx4 %3, vcc offset:0 sc1\n\tglobal_load_lds_dwordx4 %4, vcc offset:1024 sc1\n\ts_cmp_gt_u32 %9, 1\n\ts_cbranch_scc1 .Lqx0\n\tglobal_load_lds_dwordx4 %5, vcc offset:2048 sc1\n.Lqx0:\n\ts_add_u32 m0, %6, 18432\n\ts_sub_u32 vcc_lo, %7, 18432\n\ts_subb_u32 vcc_hi, %8, 0\n\tglobal_load_lds_dwordx4 %1, vcc offset:-2048 sc1\n\tglobal_load_lds_dwordx4 %2, vcc offset:-1024 sc1\n\tglobal_load_lds_dwordx4 %3, vcc offset:0 sc1\n\tglobal_load_lds_dwordx4 %4, vcc offset:1024 sc1\n\ts_cmp_gt_u32 %9, 1\n\ts_cbranch_scc1 .Lqx1\n\tglobal_load_lds_dwordx4 %5, vcc offset:2048 sc1\n.Lqx1:\n\t" : "+v"(lane) : "v"(qx[0]), "v"(qx[1]), "v"(qx[2]), "v"(qx[3]), "v"(qx[4]), "s"(pq0 * 1024 + 2048),
                 "s"((unsigned)xb), "s"((unsigned)(xb >> 32)), "s"(wave) : "vcc", "scc", "memory");
    const unsigned r = lane & 15, g = lane >> 4, q = r >> 2, p = lane & 3;
    const unsigned hg = qa_hg, rho = qa_rho, row0 = rho * RP + PAD, pc0 = wave < 2 ? wave * 5 : 2 + wave * 4, hb = wave * 36 + (wave & 1) * 4;
    auto oa = [&](unsigned b) { return 43520 + (b >> 4) * 256 + r * 16 + (b & 15); };
    const unsigned m0 = 4 * g + (r & 1), m1 = m0 + 2, hm = (r & 1) ? 0xFFFF0000u : 0xFFFFu, ninf = 0xFF800000u;
    u32x32 va, vb; u32x16 sa, sb;
    va[0] = (((0 + lane) * 5462u) >> 16) * (1152 - 192) + lane * 16 + 4096;
    va[1] = (((64 + lane) * 5462u) >> 16) * (1152 - 192) + lane * 16 + 4096;
    va[2] = (((128 + lane) * 5462u) >> 16) * (1152 - 192) + lane * 16 + 4096;
    va[3] = (((192 + lane) * 5462u) >> 16) * (1152 - 192) + lane * 16 + 4096;
    va[4] = (((256 + lane) * 5462u) >> 16) * (1152 - 192) + lane * 16 + 4096;
    va[5] = (((320 + lane) * 5462u) >> 16) * (1152 - 192) + lane * 16 + 4096;
    va[6] = (((384 + lane) * 5462u) >> 16) * (1152 - 192) + lane * 16 + 4096;
    va[7] = (((448 + lane) * 5462u) >> 16) * (1152 - 192) + lane * 16 + 4096;
    va[8] = (((512 + lane) * 5462u) >> 16) * (1152 - 192) + lane * 16 + 12288;
    va[9] = (8 * g + q) * 576 + (hg & 3) * 16 + hb + p * 8;
    va[10] = (8 * g + q) * 576 + (hg & 3) * 16 + 32 + (p & 1) * 72 + (wave == 0 ? (p >> 1) * 192 : 384);
    va[11] = qx[0];
    va[12] = qx[1];
    va[13] = qx[2];
    va[14] = qx[3];
    va[15] = qx[4];
    va[16] = WS_O + (row0 - 1) * XT + hg * 2304 + wave * 1024 + (wave < 2 ? lane : r) * 16;
    va[17] = 43520 + wave * 1024 + (wave < 2 ? lane : r) * 16;
    va[18] = oa(hb + g * 8);
    va[19] = ((g & 1) == (wave >> 1)) ? oa(32 + (wave & 1) * 4 + (wave >> 1) * 72) : 46336 + lane * 4;
    va[20] = g == 0 ? 40960 + ((wave >> 1) * 16 + r) * 8 + (wave & 1) * 4 : 46080;
    va[21] = 41216 + ((wave >> 1) * 16 + r) * 8 + (wave & 1) * 4;
    va[22] = lane * 8;
    va[23] = lane * 16;
    va[24] = 43008 + r * 4;
    va[25] = 43008 + g * 16;
    va[26] = 41984 + (q * 16 + r) * 16 + p * 4;
    va[27] = 43136 + (((m0 > r) ? 0 : 1) * 16 + m0) * 4;
    va[28] = 43136 + (((m1 > r) ? 0 : 1) * 16 + m1) * 4;
    va[29] = 43136 + (((m0 > r) ? 1 : 0) * 16 + m0) * 4;
    va[30] = 43136 + (((m1 > r) ? 1 : 0) * 16 + m1) * 4;
    va[31] = 0u;
    vb[0] = (4 * g + 0 > r && !((r ^ 0) & 1)) ? 0u : ninf;
    vb[1] = (4 * g + 1 > r && !((r ^ 1) & 1)) ? 0u : ninf;
    vb[2] = (4 * g + 2 > r && !((r ^ 2) & 1)) ? 0u : ninf;
    vb[3] = (4 * g + 3 > r && !((r ^ 3) & 1)) ? 0u : ninf;
    vb[4] = (4 * g + 0 <= r && !((r ^ 0) & 1)) ? 0u : ninf;
    vb[5] = (4 * g + 1 <= r && !((r ^ 1) & 1)) ? 0u : ninf;
    vb[6] = (4 * g + 2 <= r && !((r ^ 2) & 1)) ? 0u : ninf;
    vb[7] = (4 * g + 3 <= r && !((r ^ 3) & 1)) ? 0u : ninf;
    vb[8] = m0 > r ? hm : 0u;
    vb[9] = m1 > r ? hm : 0u;
    vb[10] = m0 > r ? 0u : hm;
    vb[11] = m1 > r ? 0u : hm;
    vb[12] = WS_FL + rho * FR + FX2 + min(lane, 35u) * FL;
    vb[13] = WS_FL + rho * FR + FX2 + min(lane, 35u) * FL;
    vb[14] = 0u - 29;
    vb[15] = WS_FL + rho * FR + FO + hg * FL;
    vb[16] = 0u - 2;
    vb[17] = WS_FL + lane * FL;
    vb[18] = 0u;
    vb[19] = r * 128 + (((0 + g) ^ (r & 7)) << 4);
    vb[20] = r * 128 + (((4 + g) ^ (r & 7)) << 4);
    sa[0] = 0 + wave; sa[2] = (unsigned)(unsigned long long)ws; sa[3] = (unsigned)((unsigned long long)ws >> 32);
    sa[4] = uni((unsigned)(unsigned long long)(wqkv + ((hg * 144) & ~63u) + wave * (16 * 3456) + 0 * 64 * 3456));
    sa[5] = uni((unsigned)((unsigned long long)(wqkv + ((hg * 144) & ~63u) + wave * (16 * 3456) + 0 * 64 * 3456) >> 32));
    sa[6] = uni((unsigned)(unsigned long long)(x0 - 2048 - 2 * XT));
    sa[7] = uni((unsigned)((unsigned long long)(x0 - 2048 - 2 * XT) >> 32));
    sa[8] = uni((unsigned)(unsigned long long)((ws + WS_X2) - x0 + (rho ? 32 : 31) * (long long)XT));
    sa[9] = uni((unsigned)((unsigned long long)((ws + WS_X2) - x0 + (rho ? 32 : 31) * (long long)XT) >> 32));
    sa[10] = uni((unsigned)(unsigned long long)(-(long long)XT));
    sa[11] = uni((unsigned)((unsigned long long)(-(long long)XT) >> 32));
    sa[12] = uni(47104 + wave * 9216);
    sa[13] = uni(pc0 * 1024 + 2048 + 0x10000);
    sa[14] = uni(rho ? 0u : XT);
    sa[15] = uni(rho ? 0u - FR : 0u);
    sb[0] = uni(rho ? 0u : ninf);
    sb[1] = uni(hg * 4 + rho);
    asm volatile(
R"(.Lb0:
s_movk_i32 s69, 0x380
s_mov_b32 s71, 0
s_mov_b32 s74, 0
s_load_dword s37, s[38:39], 0x0
s_nop 1
s_and_b32 s75, s36, 3
s_cmp_eq_u32 s75, 1
s_cbranch_scc1 .Lqa_w2
s_cmp_eq_u32 s75, 2
s_cbranch_scc1 .Lqa_w3
s_cmp_eq_u32 s75, 3
s_cbranch_scc1 .Lqa_w4
.Lqa_w1:
s_getpc_b64 s[90:91]
.Lqa_pc25:
s_add_u32 s90, s90, .Lqa_e8-.Lqa_pc25
s_addc_u32 s91, s91, 0
s_branch .Lqa_k16
.Lqa_bk24:
v_add_u32 v255, 0x10000, v9
v_add_u32 v162, 0x20000, v9
v_add_u32 v163, 0x10000, v10
v_add_u32 v164, 0x20000, v10
v_mov_b32 v106, 0x3F803F80
v_mov_b32 v107, 0x3F803F80
v_mov_b32 v108, 0x3F803F80
v_mov_b32 v109, 0x3F803F80
v_mov_b32 v141, 0
v_mov_b32 v145, 0
v_mov_b32 v149, 0
v_mov_b32 v153, 0
v_mov_b32 v165, 46080
s_waitcnt lgkmcnt(0)
s_mul_i32 s37, s37, 0x9E3779B1
s_add_u32 s37, s37, 0x7F4A7C15
v_add_u32 v46, s37, v46
v_add_u32 v48, s37, v48
v_add_u32 v50, s37, v50
ds_write_b32 v165, v141
s_mov_b64 s[76:77], 0
s_mov_b32 s73, 18
s_add_u32 m0, s48, 4096
s_nop 0
global_load_lds_dwordx4 v0, s[40:41] offset:-4096
global_load_lds_dwordx4 v1, s[40:41] offset:-3072
global_load_lds_dwordx4 v2, s[40:41] offset:-2048
global_load_lds_dwordx4 v3, s[40:41] offset:-1024
global_load_lds_dwordx4 v4, s[40:41] offset:0
global_load_lds_dwordx4 v5, s[40:41] offset:1024
global_load_lds_dwordx4 v6, s[40:41] offset:2048
global_load_lds_dwordx4 v7, s[40:41] offset:3072
s_add_u32 m0, s48, 12288
s_nop 0
global_load_lds_dwordx4 v8, s[40:41] offset:-4096
s_add_u32 s40, s40, 221184
s_addc_u32 s41, s41, 0
s_add_u32 m0, s48, 40960
s_nop 0
global_load_lds_dwordx4 v0, s[40:41] offset:-4096
global_load_lds_dwordx4 v1, s[40:41] offset:-3072
global_load_lds_dwordx4 v2, s[40:41] offset:-2048
global_load_lds_dwordx4 v3, s[40:41] offset:-1024
global_load_lds_dwordx4 v4, s[40:41] offset:0
global_load_lds_dwordx4 v5, s[40:41] offset:1024
global_load_lds_dwordx4 v6, s[40:41] offset:2048
global_load_lds_dwordx4 v7, s[40:41] offset:3072
s_add_u32 m0, s48, 49152
s_nop 0
global_load_lds_dwordx4 v8, s[40:41] offset:-4096
s_add_u32 s40, s40, 221184
s_addc_u32 s41, s41, 0
s_waitcnt vmcnt(9) lgkmcnt(0)
s_barrier
s_add_u32 m0, s48, 77824
ds_read_b64_tr_b16 a[0:1], v9 offset:47104
ds_read_b64_tr_b16 a[2:3], v9 offset:49408
global_load_lds_dwordx4 v0, s[40:41] offset:-4096
ds_read_b64_tr_b16 a[4:5], v9 offset:47296
ds_read_b64_tr_b16 a[6:7], v9 offset:49600
global_load_lds_dwordx4 v1, s[40:41] offset:-3072
ds_read_b64_tr_b16 a[8:9], v9 offset:47488
global_load_lds_dwordx4 v2, s[40:41] offset:-2048
ds_read_b64_tr_b16 a[10:11], v9 offset:49792
ds_read_b64_tr_b16 a[216:217], v10 offset:47104
global_load_lds_dwordx4 v3, s[40:41] offset:-1024
ds_read_b64_tr_b16 a[218:219], v10 offset:49408
global_load_lds_dwordx4 v4, s[40:41] offset:0
ds_read_b128 v[166:169], v51 offset:0
ds_read_b128 v[174:177], v51 offset:18432
global_load_lds_dwordx4 v5, s[40:41] offset:1024
ds_read_b64_tr_b16 a[12:13], v255 offset:0
global_load_lds_dwordx4 v6, s[40:41] offset:2048
ds_read_b64_tr_b16 a[14:15], v255 offset:2304
ds_read_b64_tr_b16 a[16:17], v255 offset:192
global_load_lds_dwordx4 v7, s[40:41] offset:3072
ds_read_b64_tr_b16 a[18:19], v255 offset:2496
s_add_u32 m0, s48, 86016
ds_read_b64_tr_b16 a[20:21], v255 offset:384
ds_read_b64_tr_b16 a[22:23], v255 offset:2688
global_load_lds_dwordx4 v8, s[40:41] offset:-4096
ds_read_b64_tr_b16 a[220:221], v163 offset:0
s_add_u32 s40, s40, 221184
ds_read_b64_tr_b16 a[222:223], v163 offset:2304
ds_read_b128 v[170:173], v52 offset:0
s_addc_u32 s41, s41, 0
ds_read_b128 v[178:181], v52 offset:18432
s_waitcnt vmcnt(9) lgkmcnt(0)
s_barrier
s_add_u32 m0, s48, 4096
ds_read_b64_tr_b16 a[24:25], v255 offset:18432
v_mfma_f32_16x16x32_bf16 v[86:89], a[0:3], v[106:109], 0
ds_read_b64_tr_b16 a[26:27], v255 offset:20736
v_mfma_f32_16x16x32_bf16 v[90:93], a[4:7], v[106:109], 0
global_load_lds_dwordx4 v0, s[40:41] offset:-4096
ds_read_b64_tr_b16 a[28:29], v255 offset:18624
v_mfma_f32_16x16x32_bf16 v[94:97], v[106:109], a[8:11], 0
ds_read_b64_tr_b16 a[30:31], v255 offset:20928
global_load_lds_dwordx4 v1, s[40:41] offset:-3072
v_mfma_f32_16x16x32_bf16 v[98:101], a[216:219], v[106:109], 0
ds_read_b64_tr_b16 a[32:33], v255 offset:18816
v_mfma_f32_16x16x32_bf16 v[110:113], a[0:3], v[166:169], 0
ds_read_b64_tr_b16 a[34:35], v255 offset:21120
global_load_lds_dwordx4 v2, s[40:41] offset:-2048
v_mfma_f32_16x16x32_bf16 v[114:117], a[4:7], v[166:169], 0
ds_read_b64_tr_b16 a[224:225], v163 offset:18432
v_mfma_f32_16x16x32_bf16 v[118:121], v[166:169], a[8:11], 0
global_load_lds_dwordx4 v3, s[40:41] offset:-1024
ds_read_b64_tr_b16 a[226:227], v163 offset:20736
v_mfma_f32_16x16x32_bf16 v[134:137], a[216:219], v[166:169], 0
ds_read_b128 v[182:185], v51 offset:2048
global_load_lds_dwordx4 v4, s[40:41] offset:0
v_mfma_f32_16x16x32_bf16 v[122:125], a[0:3], v[174:177], 0
ds_read_b128 v[190:193], v51 offset:20480
v_mfma_f32_16x16x32_bf16 v[126:129], a[4:7], v[174:177], 0
ds_read_b64_tr_b16 a[36:37], v255 offset:36864
global_load_lds_dwordx4 v5, s[40:41] offset:1024
v_mfma_f32_16x16x32_bf16 v[130:133], v[174:177], a[8:11], 0
ds_read_b64_tr_b16 a[38:39], v255 offset:39168
v_mfma_f32_16x16x32_bf16 v[158:161], a[216:219], v[174:177], 0
global_load_lds_dwordx4 v6, s[40:41] offset:2048
ds_read_b64_tr_b16 a[40:41], v255 offset:37056
v_mfma_f32_16x16x32_bf16 v[86:89], a[12:15], v[106:109], v[86:89]
ds_read_b64_tr_b16 a[42:43], v255 offset:39360
v_mfma_f32_16x16x32_bf16 v[90:93], a[16:19], v[106:109], v[90:93]
global_load_lds_dwordx4 v7, s[40:41] offset:3072
ds_read_b64_tr_b16 a[44:45], v255 offset:37248
v_mfma_f32_16x16x32_bf16 v[94:97], v[106:109], a[20:23], v[94:97]
s_add_u32 m0, s48, 12288
ds_read_b64_tr_b16 a[46:47], v255 offset:39552
v_mfma_f32_16x16x32_bf16 v[98:101], a[220:223], v[106:109], v[98:101]
ds_read_b64_tr_b16 a[228:229], v163 offset:36864
v_mfma_f32_16x16x32_bf16 v[110:113], a[12:15], v[170:173], v[110:113]
global_load_lds_dwordx4 v8, s[40:41] offset:-4096
ds_read_b64_tr_b16 a[230:231], v163 offset:39168
v_mfma_f32_16x16x32_bf16 v[114:117], a[16:19], v[170:173], v[114:117]
ds_read_b128 v[186:189], v52 offset:2048
v_mfma_f32_16x16x32_bf16 v[118:121], v[170:173], a[20:23], v[118:121]
s_add_u32 s40, s40, 221184
ds_read_b128 v[194:197], v52 offset:20480
v_mfma_f32_16x16x32_bf16 v[134:137], a[220:223], v[170:173], v[134:137]
v_mfma_f32_16x16x32_bf16 v[122:125], a[12:15], v[178:181], v[122:125]
s_addc_u32 s41, s41, 0
v_mfma_f32_16x16x32_bf16 v[126:129], a[16:19], v[178:181], v[126:129]
v_mfma_f32_16x16x32_bf16 v[130:133], v[178:181], a[20:23], v[130:133]
v_mfma_f32_16x16x32_bf16 v[158:161], a[220:223], v[178:181], v[158:161]
s_waitcnt vmcnt(9) lgkmcnt(0)
s_barrier
s_add_u32 m0, s48, 40960
ds_read_b64_tr_b16 a[48:49], v255 offset:55296
v_mfma_f32_16x16x32_bf16 v[86:89], a[24:27], v[106:109], v[86:89]
ds_read_b64_tr_b16 a[50:51], v255 offset:57600
v_mfma_f32_16x16x32_bf16 v[90:93], a[28:31], v[106:109], v[90:93]
global_load_lds_dwordx4 v0, s[40:41] offset:-4096
ds_read_b64_tr_b16 a[52:53], v255 offset:55488
v_mfma_f32_16x16x32_bf16 v[94:97], v[106:109], a[32:35], v[94:97]
ds_read_b64_tr_b16 a[54:55], v255 offset:57792
global_load_lds_dwordx4 v1, s[40:41] offset:-3072
v_mfma_f32_16x16x32_bf16 v[98:101], a[224:227], v[106:109], v[98:101]
ds_read_b64_tr_b16 a[56:57], v255 offset:55680
v_mfma_f32_16x16x32_bf16 v[110:113], a[24:27], v[182:185], v[110:113]
ds_read_b64_tr_b16 a[58:59], v255 offset:57984
global_load_lds_dwordx4 v2, s[40:41] offset:-2048
v_mfma_f32_16x16x32_bf16 v[114:117], a[28:31], v[182:185], v[114:117]
ds_read_b64_tr_b16 a[232:233], v163 offset:55296
v_mfma_f32_16x16x32_bf16 v[118:121], v[182:185], a[32:35], v[118:121]
global_load_lds_dwordx4 v3, s[40:41] offset:-1024
ds_read_b64_tr_b16 a[234:235], v163 offset:57600
v_mfma_f32_16x16x32_bf16 v[134:137], a[224:227], v[182:185], v[134:137]
ds_read_b128 v[166:169], v51 offset:4096
global_load_lds_dwordx4 v4, s[40:41] offset:0
v_mfma_f32_16x16x32_bf16 v[122:125], a[24:27], v[190:193], v[122:125]
ds_read_b128 v[174:177], v51 offset:22528
v_mfma_f32_16x16x32_bf16 v[126:129], a[28:31], v[190:193], v[126:129]
ds_read_b64_tr_b16 a[60:61], v162 offset:8192
global_load_lds_dwordx4 v5, s[40:41] offset:1024
v_mfma_f32_16x16x32_bf16 v[130:133], v[190:193], a[32:35], v[130:133]
ds_read_b64_tr_b16 a[62:63], v162 offset:10496
v_mfma_f32_16x16x32_bf16 v[158:161], a[224:227], v[190:193], v[158:161]
global_load_lds_dwordx4 v6, s[40:41] offset:2048
ds_read_b64_tr_b16 a[64:65], v162 offset:8384
v_mfma_f32_16x16x32_bf16 v[86:89], a[36:39], v[106:109], v[86:89]
ds_read_b64_tr_b16 a[66:67], v162 offset:10688
v_mfma_f32_16x16x32_bf16 v[90:93], a[40:43], v[106:109], v[90:93]
global_load_lds_dwordx4 v7, s[40:41] offset:3072
ds_read_b64_tr_b16 a[68:69], v162 offset:8576
v_mfma_f32_16x16x32_bf16 v[94:97], v[106:109], a[44:47], v[94:97]
s_add_u32 m0, s48, 49152
ds_read_b64_tr_b16 a[70:71], v162 offset:10880
v_mfma_f32_16x16x32_bf16 v[98:101], a[228:231], v[106:109], v[98:101]
ds_read_b64_tr_b16 a[236:237], v164 offset:8192
v_mfma_f32_16x16x32_bf16 v[110:113], a[36:39], v[186:189], v[110:113]
global_load_lds_dwordx4 v8, s[40:41] offset:-4096
ds_read_b64_tr_b16 a[238:239], v164 offset:10496
v_mfma_f32_16x16x32_bf16 v[114:117], a[40:43], v[186:189], v[114:117]
ds_read_b128 v[170:173], v52 offset:4096
v_mfma_f32_16x16x32_bf16 v[118:121], v[186:189], a[44:47], v[118:121]
s_add_u32 s40, s40, 221184
ds_read_b128 v[178:181], v52 offset:22528
v_mfma_f32_16x16x32_bf16 v[134:137], a[228:231], v[186:189], v[134:137]
v_mfma_f32_16x16x32_bf16 v[122:125], a[36:39], v[194:197], v[122:125]
s_addc_u32 s41, s41, 0
v_mfma_f32_16x16x32_bf16 v[126:129], a[40:43], v[194:197], v[126:129]
v_mfma_f32_16x16x32_bf16 v[130:133], v[194:197], a[44:47], v[130:133]
v_mfma_f32_16x16x32_bf16 v[158:161], a[228:231], v[194:197], v[158:161]
s_waitcnt vmcnt(9) lgkmcnt(0)
s_barrier
s_add_u32 m0, s48, 77824
ds_read_b64_tr_b16 a[72:73], v9 offset:47104
v_mfma_f32_16x16x32_bf16 v[86:89], a[48:51], v[106:109], v[86:89]
ds_read_b64_tr_b16 a[74:75], v9 offset:49408
v_mfma_f32_16x16x32_bf16 v[90:93], a[52:55], v[106:109], v[90:93]
global_load_lds_dwordx4 v0, s[40:41] offset:-4096
ds_read_b64_tr_b16 a[76:77], v9 offset:47296
v_mfma_f32_16x16x32_bf16 v[94:97], v[106:109], a[56:59], v[94:97]
ds_read_b64_tr_b16 a[78:79], v9 offset:49600
global_load_lds_dwordx4 v1, s[40:41] offset:-3072
v_mfma_f32_16x16x32_bf16 v[98:101], a[232:235], v[106:109], v[98:101]
ds_read_b64_tr_b16 a[80:81], v9 offset:47488
v_mfma_f32_16x16x32_bf16 v[110:113], a[48:51], v[166:169], v[110:113]
ds_read_b64_tr_b16 a[82:83], v9 offset:49792
global_load_lds_dwordx4 v2, s[40:41] offset:-2048
v_mfma_f32_16x16x32_bf16 v[114:117], a[52:55], v[166:169], v[114:117]
ds_read_b64_tr_b16 a[240:241], v10 offset:47104
v_mfma_f32_16x16x32_bf16 v[118:121], v[166:169], a[56:59], v[118:121]
global_load_lds_dwordx4 v3, s[40:41] offset:-1024
ds_read_b64_tr_b16 a[242:243], v10 offset:49408
v_mfma_f32_16x16x32_bf16 v[134:137], a[232:235], v[166:169], v[134:137]
ds_read_b128 v[182:185], v51 offset:6144
global_load_lds_dwordx4 v4, s[40:41] offset:0
v_mfma_f32_16x16x32_bf16 v[122:125], a[48:51], v[174:177], v[122:125]
ds_read_b128 v[190:193], v51 offset:24576
v_mfma_f32_16x16x32_bf16 v[126:129], a[52:55], v[174:177], v[126:129]
ds_read_b64_tr_b16 a[84:85], v255 offset:0
global_load_lds_dwordx4 v5, s[40:41] offset:1024
v_mfma_f32_16x16x32_bf16 v[130:133], v[174:177], a[56:59], v[130:133]
ds_read_b64_tr_b16 a[86:87], v255 offset:2304
v_mfma_f32_16x16x32_bf16 v[158:161], a[232:235], v[174:177], v[158:161]
global_load_lds_dwordx4 v6, s[40:41] offset:2048
ds_read_b64_tr_b16 a[88:89], v255 offset:192
v_mfma_f32_16x16x32_bf16 v[86:89], a[60:63], v[106:109], v[86:89]
ds_read_b64_tr_b16 a[90:91], v255 offset:2496
v_mfma_f32_16x16x32_bf16 v[90:93], a[64:67], v[106:109], v[90:93]
global_load_lds_dwordx4 v7, s[40:41] offset:3072
ds_read_b64_tr_b16 a[92:93], v255 offset:384
v_mfma_f32_16x16x32_bf16 v[94:97], v[106:109], a[68:71], v[94:97]
s_add_u32 m0, s48, 86016
ds_read_b64_tr_b16 a[94:95], v255 offset:2688
v_mfma_f32_16x16x32_bf16 v[98:101], a[236:239], v[106:109], v[98:101]
ds_read_b64_tr_b16 a[244:245], v163 offset:0
v_mfma_f32_16x16x32_bf16 v[110:113], a[60:63], v[170:173], v[110:113]
global_load_lds_dwordx4 v8, s[40:41] offset:-4096
ds_read_b64_tr_b16 a[246:247], v163 offset:2304
v_mfma_f32_16x16x32_bf16 v[114:117], a[64:67], v[170:173], v[114:117]
ds_read_b128 v[186:189], v52 offset:6144
v_mfma_f32_16x16x32_bf16 v[118:121], v[170:173], a[68:71], v[118:121]
s_add_u32 s40, s40, 221184
ds_read_b128 v[194:197], v52 offset:24576
v_mfma_f32_16x16x32_bf16 v[134:137], a[236:239], v[170:173], v[134:137]
v_mfma_f32_16x16x32_bf16 v[122:125], a[60:63], v[178:181], v[122:125]
s_addc_u32 s41, s41, 0
v_mfma_f32_16x16x32_bf16 v[126:129], a[64:67], v[178:181], v[126:129]
v_mfma_f32_16x16x32_bf16 v[130:133], v[178:181], a[68:71], v[130:133]
v_mfma_f32_16x16x32_bf16 v[158:161], a[236:239], v[178:181], v[158:161]
s_waitcnt vmcnt(9) lgkmcnt(0)
s_barrier
s_add_u32 m0, s48, 4096
ds_read_b64_tr_b16 a[96:97], v255 offset:18432
v_mfma_f32_16x16x32_bf16 v[86:89], a[72:75], v[106:109], v[86:89]
ds_read_b64_tr_b16 a[98:99], v255 offset:20736
v_mfma_f32_16x16x32_bf16 v[90:93], a[76:79], v[106:109], v[90:93]
global_load_lds_dwordx4 v0, s[40:41] offset:-4096
ds_read_b64_tr_b16 a[100:101], v255 offset:18624
v_mfma_f32_16x16x32_bf16 v[94:97], v[106:109], a[80:83], v[94:97]
ds_read_b64_tr_b16 a[102:103], v255 offset:20928
global_load_lds_dwordx4 v1, s[40:41] offset:-3072
v_mfma_f32_16x16x32_bf16 v[98:101], a[240:243], v[106:109], v[98:101]
ds_read_b64_tr_b16 a[104:105], v255 offset:18816
v_mfma_f32_16x16x32_bf16 v[110:113], a[72:75], v[182:185], v[110:113]
ds_read_b64_tr_b16 a[106:107], v255 offset:21120
global_load_lds_dwordx4 v2, s[40:41] offset:-2048
v_mfma_f32_16x16x32_bf16 v[114:117], a[76:79], v[182:185], v[114:117]
ds_read_b64_tr_b16 a[248:249], v163 offset:18432
v_mfma_f32_16x16x32_bf16 v[118:121], v[182:185], a[80:83], v[118:121]
global_load_lds_dwordx4 v3, s[40:41] offset:-1024
ds_read_b64_tr_b16 a[250:251], v163 offset:20736
v_mfma_f32_16x16x32_bf16 v[134:137], a[240:243], v[182:185], v[134:137]
ds_read_b128 v[166:169], v51 offset:8192
global_load_lds_dwordx4 v4, s[40:41] offset:0
v_mfma_f32_16x16x32_bf16 v[122:125], a[72:75], v[190:193], v[122:125]
ds_read_b128 v[174:177], v51 offset:26624
v_mfma_f32_16x16x32_bf16 v[126:129], a[76:79], v[190:193], v[126:129]
ds_read_b64_tr_b16 a[108:109], v255 offset:36864
global_load_lds_dwordx4 v5, s[40:41] offset:1024
v_mfma_f32_16x16x32_bf16 v[130:133], v[190:193], a[80:83], v[130:133]
ds_read_b64_tr_b16 a[110:111], v255 offset:39168
v_mfma_f32_16x16x32_bf16 v[158:161], a[240:243], v[190:193], v[158:161]
global_load_lds_dwordx4 v6, s[40:41] offset:2048
ds_read_b64_tr_b16 a[112:113], v255 offset:37056
v_mfma_f32_16x16x32_bf16 v[86:89], a[84:87], v[106:109], v[86:89]
ds_read_b64_tr_b16 a[114:115], v255 offset:39360
v_mfma_f32_16x16x32_bf16 v[90:93], a[88:91], v[106:109], v[90:93]
global_load_lds_dwordx4 v7, s[40:41] offset:3072
ds_read_b64_tr_b16 a[116:117], v255 offset:37248
v_mfma_f32_16x16x32_bf16 v[94:97], v[106:109], a[92:95], v[94:97]
s_add_u32 m0, s48, 12288
ds_read_b64_tr_b16 a[118:119], v255 offset:39552
v_mfma_f32_16x16x32_bf16 v[98:101], a[244:247], v[106:109], v[98:101]
ds_read_b64_tr_b16 a[252:253], v163 offset:36864
v_mfma_f32_16x16x32_bf16 v[110:113], a[84:87], v[186:189], v[110:113]
global_load_lds_dwordx4 v8, s[40:41] offset:-4096
ds_read_b64_tr_b16 a[254:255], v163 offset:39168
v_mfma_f32_16x16x32_bf16 v[114:117], a[88:91], v[186:189], v[114:117]
ds_read_b128 v[170:173], v52 offset:8192
v_mfma_f32_16x16x32_bf16 v[118:121], v[186:189], a[92:95], v[118:121]
s_add_u32 s40, s40, 221184
ds_read_b128 v[178:181], v52 offset:26624
v_mfma_f32_16x16x32_bf16 v[134:137], a[244:247], v[186:189], v[134:137]
v_mfma_f32_16x16x32_bf16 v[122:125], a[84:87], v[194:197], v[122:125]
s_addc_u32 s41, s41, 0
v_mfma_f32_16x16x32_bf16 v[126:129], a[88:91], v[194:197], v[126:129]
v_mfma_f32_16x16x32_bf16 v[130:133], v[194:197], a[92:95], v[130:133]
v_mfma_f32_16x16x32_bf16 v[158:161], a[244:247], v[194:197], v[158:161]
s_waitcnt vmcnt(9) lgkmcnt(0)
s_barrier
s_add_u32 m0, s48, 40960
ds_read_b64_tr_b16 a[120:121], v255 offset:55296
v_mfma_f32_16x16x32_bf16 v[86:89], a[96:99], v[106:109], v[86:89]
ds_read_b64_tr_b16 a[122:123], v255 offset:57600
v_mfma_f32_16x16x32_bf16 v[90:93], a[100:103], v[106:109], v[90:93]
global_load_lds_dwordx4 v0, s[40:41] offset:-4096
ds_read_b64_tr_b16 a[124:125], v255 offset:55488
v_mfma_f32_16x16x32_bf16 v[94:97], v[106:109], a[104:107], v[94:97]
ds_read_b64_tr_b16 a[126:127], v255 offset:57792
global_load_lds_dwordx4 v1, s[40:41] offset:-3072
v_mfma_f32_16x16x32_bf16 v[98:101], a[248:251], v[106:109], v[98:101]
ds_read_b64_tr_b16 a[128:129], v255 offset:55680
v_mfma_f32_16x16x32_bf16 v[110:113], a[96:99], v[166:169], v[110:113]
ds_read_b64_tr_b16 a[130:131], v255 offset:57984
global_load_lds_dwordx4 v2, s[40:41] offset:-2048
v_mfma_f32_16x16x32_bf16 v[114:117], a[100:103], v[166:169], v[114:117]
ds_read_b64_tr_b16 v[54:55], v163 offset:55296
v_mfma_f32_16x16x32_bf16 v[118:121], v[166:169], a[104:107], v[118:121]
global_load_lds_dwordx4 v3, s[40:41] offset:-1024
ds_read_b64_tr_b16 v[56:57], v163 offset:57600
v_mfma_f32_16x16x32_bf16 v[134:137], a[248:251], v[166:169], v[134:137]
ds_read_b128 v[182:185], v51 offset:10240
global_load_lds_dwordx4 v4, s[40:41] offset:0
v_mfma_f32_16x16x32_bf16 v[122:125], a[96:99], v[174:177], v[122:125]
ds_read_b128 v[190:193], v51 offset:28672
v_mfma_f32_16x16x32_bf16 v[126:129], a[100:103], v[174:177], v[126:129]
ds_read_b64_tr_b16 a[132:133], v162 offset:8192
global_load_lds_dwordx4 v5, s[40:41] offset:1024
v_mfma_f32_16x16x32_bf16 v[130:133], v[174:177], a[104:107], v[130:133]
ds_read_b64_tr_b16 a[134:135], v162 offset:10496
v_mfma_f32_16x16x32_bf16 v[158:161], a[248:251], v[174:177], v[158:161]
global_load_lds_dwordx4 v6, s[40:41] offset:2048
ds_read_b64_tr_b16 a[136:137], v162 offset:8384
v_mfma_f32_16x16x32_bf16 v[86:89], a[108:111], v[106:109], v[86:89]
ds_read_b64_tr_b16 a[138:139], v162 offset:10688
v_mfma_f32_16x16x32_bf16 v[90:93], a[112:115], v[106:109], v[90:93]
global_load_lds_dwordx4 v7, s[40:41] offset:3072
ds_read_b64_tr_b16 a[140:141], v162 offset:8576
v_mfma_f32_16x16x32_bf16 v[94:97], v[106:109], a[116:119], v[94:97]
s_add_u32 m0, s48, 49152
ds_read_b64_tr_b16 a[142:143], v162 offset:10880
v_mfma_f32_16x16x32_bf16 v[98:101], a[252:255], v[106:109], v[98:101]
ds_read_b64_tr_b16 v[58:59], v164 offset:8192
v_mfma_f32_16x16x32_bf16 v[110:113], a[108:111], v[170:173], v[110:113]
global_load_lds_dwordx4 v8, s[40:41] offset:-4096
ds_read_b64_tr_b16 v[60:61], v164 offset:10496
v_mfma_f32_16x16x32_bf16 v[114:117], a[112:115], v[170:173], v[114:117]
ds_read_b128 v[186:189], v52 offset:10240
v_mfma_f32_16x16x32_bf16 v[118:121], v[170:173], a[116:119], v[118:121]
s_add_u32 s40, s40, 221184
ds_read_b128 v[194:197], v52 offset:28672
v_mfma_f32_16x16x32_bf16 v[134:137], a[252:255], v[170:173], v[134:137]
v_mfma_f32_16x16x32_bf16 v[122:125], a[108:111], v[178:181], v[122:125]
s_addc_u32 s41, s41, 0
v_mfma_f32_16x16x32_bf16 v[126:129], a[112:115], v[178:181], v[126:129]
)"
R"(v_mfma_f32_16x16x32_bf16 v[130:133], v[178:181], a[116:119], v[130:133]
v_mfma_f32_16x16x32_bf16 v[158:161], a[252:255], v[178:181], v[158:161]
s_waitcnt vmcnt(9) lgkmcnt(0)
s_barrier
s_add_u32 m0, s48, 77824
ds_read_b64_tr_b16 a[144:145], v9 offset:47104
v_mfma_f32_16x16x32_bf16 v[86:89], a[120:123], v[106:109], v[86:89]
ds_read_b64_tr_b16 a[146:147], v9 offset:49408
v_mfma_f32_16x16x32_bf16 v[90:93], a[124:127], v[106:109], v[90:93]
global_load_lds_dwordx4 v0, s[40:41] offset:-4096
ds_read_b64_tr_b16 a[148:149], v9 offset:47296
v_mfma_f32_16x16x32_bf16 v[94:97], v[106:109], a[128:131], v[94:97]
ds_read_b64_tr_b16 a[150:151], v9 offset:49600
global_load_lds_dwordx4 v1, s[40:41] offset:-3072
v_mfma_f32_16x16x32_bf16 v[98:101], v[54:57], v[106:109], v[98:101]
ds_read_b64_tr_b16 a[152:153], v9 offset:47488
v_mfma_f32_16x16x32_bf16 v[110:113], a[120:123], v[182:185], v[110:113]
ds_read_b64_tr_b16 a[154:155], v9 offset:49792
global_load_lds_dwordx4 v2, s[40:41] offset:-2048
v_mfma_f32_16x16x32_bf16 v[114:117], a[124:127], v[182:185], v[114:117]
ds_read_b64_tr_b16 v[62:63], v10 offset:47104
v_mfma_f32_16x16x32_bf16 v[118:121], v[182:185], a[128:131], v[118:121]
global_load_lds_dwordx4 v3, s[40:41] offset:-1024
ds_read_b64_tr_b16 v[64:65], v10 offset:49408
v_mfma_f32_16x16x32_bf16 v[134:137], v[54:57], v[182:185], v[134:137]
ds_read_b128 v[166:169], v51 offset:12288
global_load_lds_dwordx4 v4, s[40:41] offset:0
v_mfma_f32_16x16x32_bf16 v[122:125], a[120:123], v[190:193], v[122:125]
ds_read_b128 v[174:177], v51 offset:30720
v_mfma_f32_16x16x32_bf16 v[126:129], a[124:127], v[190:193], v[126:129]
ds_read_b64_tr_b16 a[156:157], v255 offset:0
global_load_lds_dwordx4 v5, s[40:41] offset:1024
v_mfma_f32_16x16x32_bf16 v[130:133], v[190:193], a[128:131], v[130:133]
ds_read_b64_tr_b16 a[158:159], v255 offset:2304
v_mfma_f32_16x16x32_bf16 v[158:161], v[54:57], v[190:193], v[158:161]
global_load_lds_dwordx4 v6, s[40:41] offset:2048
ds_read_b64_tr_b16 a[160:161], v255 offset:192
v_mfma_f32_16x16x32_bf16 v[86:89], a[132:135], v[106:109], v[86:89]
ds_read_b64_tr_b16 a[162:163], v255 offset:2496
v_mfma_f32_16x16x32_bf16 v[90:93], a[136:139], v[106:109], v[90:93]
global_load_lds_dwordx4 v7, s[40:41] offset:3072
ds_read_b64_tr_b16 a[164:165], v255 offset:384
v_mfma_f32_16x16x32_bf16 v[94:97], v[106:109], a[140:143], v[94:97]
s_add_u32 m0, s48, 86016
ds_read_b64_tr_b16 a[166:167], v255 offset:2688
v_mfma_f32_16x16x32_bf16 v[98:101], v[58:61], v[106:109], v[98:101]
ds_read_b64_tr_b16 v[66:67], v163 offset:0
v_mfma_f32_16x16x32_bf16 v[110:113], a[132:135], v[186:189], v[110:113]
global_load_lds_dwordx4 v8, s[40:41] offset:-4096
ds_read_b64_tr_b16 v[68:69], v163 offset:2304
v_mfma_f32_16x16x32_bf16 v[114:117], a[136:139], v[186:189], v[114:117]
ds_read_b128 v[170:173], v52 offset:12288
v_mfma_f32_16x16x32_bf16 v[118:121], v[186:189], a[140:143], v[118:121]
s_sub_u32 s40, s40, 1769472
ds_read_b128 v[178:181], v52 offset:30720
v_mfma_f32_16x16x32_bf16 v[134:137], v[58:61], v[186:189], v[134:137]
v_mfma_f32_16x16x32_bf16 v[122:125], a[132:135], v[194:197], v[122:125]
s_subb_u32 s41, s41, 0
v_mfma_f32_16x16x32_bf16 v[126:129], a[136:139], v[194:197], v[126:129]
v_mfma_f32_16x16x32_bf16 v[130:133], v[194:197], a[140:143], v[130:133]
v_mfma_f32_16x16x32_bf16 v[158:161], v[58:61], v[194:197], v[158:161]
s_waitcnt vmcnt(9) lgkmcnt(0)
s_barrier
s_add_u32 m0, s49, 0
ds_read_b64_tr_b16 a[168:169], v255 offset:18432
v_mfma_f32_16x16x32_bf16 v[86:89], a[144:147], v[106:109], v[86:89]
ds_read_b64_tr_b16 a[170:171], v255 offset:20736
v_mfma_f32_16x16x32_bf16 v[90:93], a[148:151], v[106:109], v[90:93]
ds_read_b64_tr_b16 a[172:173], v255 offset:18624
v_mfma_f32_16x16x32_bf16 v[94:97], v[106:109], a[152:155], v[94:97]
ds_read_b64_tr_b16 a[174:175], v255 offset:20928
global_load_lds_dwordx4 v11, s[42:43] offset:-2048 sc1
v_mfma_f32_16x16x32_bf16 v[98:101], v[62:65], v[106:109], v[98:101]
ds_read_b64_tr_b16 a[176:177], v255 offset:18816
v_mfma_f32_16x16x32_bf16 v[110:113], a[144:147], v[166:169], v[110:113]
ds_read_b64_tr_b16 a[178:179], v255 offset:21120
v_mfma_f32_16x16x32_bf16 v[114:117], a[148:151], v[166:169], v[114:117]
ds_read_b64_tr_b16 v[70:71], v163 offset:18432
global_load_lds_dwordx4 v12, s[42:43] offset:-1024 sc1
v_mfma_f32_16x16x32_bf16 v[118:121], v[166:169], a[152:155], v[118:121]
ds_read_b64_tr_b16 v[72:73], v163 offset:20736
v_mfma_f32_16x16x32_bf16 v[134:137], v[62:65], v[166:169], v[134:137]
ds_read_b128 v[182:185], v51 offset:14336
v_mfma_f32_16x16x32_bf16 v[122:125], a[144:147], v[174:177], v[122:125]
ds_read_b128 v[190:193], v51 offset:32768
global_load_lds_dwordx4 v13, s[42:43] offset:0 sc1
v_mfma_f32_16x16x32_bf16 v[126:129], a[148:151], v[174:177], v[126:129]
ds_read_b64_tr_b16 a[180:181], v255 offset:36864
v_mfma_f32_16x16x32_bf16 v[130:133], v[174:177], a[152:155], v[130:133]
ds_read_b64_tr_b16 a[182:183], v255 offset:39168
v_mfma_f32_16x16x32_bf16 v[158:161], v[62:65], v[174:177], v[158:161]
ds_read_b64_tr_b16 a[184:185], v255 offset:37056
v_mfma_f32_16x16x32_bf16 v[86:89], a[156:159], v[106:109], v[86:89]
global_load_lds_dwordx4 v14, s[42:43] offset:1024 sc1
ds_read_b64_tr_b16 a[186:187], v255 offset:39360
v_mfma_f32_16x16x32_bf16 v[90:93], a[160:163], v[106:109], v[90:93]
ds_read_b64_tr_b16 a[188:189], v255 offset:37248
v_mfma_f32_16x16x32_bf16 v[94:97], v[106:109], a[164:167], v[94:97]
ds_read_b64_tr_b16 a[190:191], v255 offset:39552
v_mfma_f32_16x16x32_bf16 v[98:101], v[66:69], v[106:109], v[98:101]
global_load_lds_dwordx4 v15, s[42:43] offset:2048 sc1
ds_read_b64_tr_b16 v[74:75], v163 offset:36864
v_mfma_f32_16x16x32_bf16 v[110:113], a[156:159], v[170:173], v[110:113]
ds_read_b64_tr_b16 v[76:77], v163 offset:39168
v_mfma_f32_16x16x32_bf16 v[114:117], a[160:163], v[170:173], v[114:117]
ds_read_b128 v[186:189], v52 offset:14336
v_mfma_f32_16x16x32_bf16 v[118:121], v[170:173], a[164:167], v[118:121]
s_sub_u32 s42, s42, 18432
s_subb_u32 s43, s43, 0
ds_read_b128 v[194:197], v52 offset:32768
v_mfma_f32_16x16x32_bf16 v[134:137], v[66:69], v[170:173], v[134:137]
v_mfma_f32_16x16x32_bf16 v[122:125], a[156:159], v[178:181], v[122:125]
v_mfma_f32_16x16x32_bf16 v[126:129], a[160:163], v[178:181], v[126:129]
v_mfma_f32_16x16x32_bf16 v[130:133], v[178:181], a[164:167], v[130:133]
v_mfma_f32_16x16x32_bf16 v[158:161], v[66:69], v[178:181], v[158:161]
s_waitcnt vmcnt(5) lgkmcnt(0)
s_barrier
s_add_u32 m0, s49, 18432
ds_read_b64_tr_b16 a[192:193], v255 offset:55296
v_mfma_f32_16x16x32_bf16 v[86:89], a[168:171], v[106:109], v[86:89]
ds_read_b64_tr_b16 a[194:195], v255 offset:57600
v_mfma_f32_16x16x32_bf16 v[90:93], a[172:175], v[106:109], v[90:93]
ds_read_b64_tr_b16 a[196:197], v255 offset:55488
v_mfma_f32_16x16x32_bf16 v[94:97], v[106:109], a[176:179], v[94:97]
ds_read_b64_tr_b16 a[198:199], v255 offset:57792
global_load_lds_dwordx4 v11, s[42:43] offset:-2048 sc1
v_mfma_f32_16x16x32_bf16 v[98:101], v[70:73], v[106:109], v[98:101]
ds_read_b64_tr_b16 a[200:201], v255 offset:55680
v_mfma_f32_16x16x32_bf16 v[110:113], a[168:171], v[182:185], v[110:113]
ds_read_b64_tr_b16 a[202:203], v255 offset:57984
v_mfma_f32_16x16x32_bf16 v[114:117], a[172:175], v[182:185], v[114:117]
ds_read_b64_tr_b16 v[78:79], v163 offset:55296
global_load_lds_dwordx4 v12, s[42:43] offset:-1024 sc1
v_mfma_f32_16x16x32_bf16 v[118:121], v[182:185], a[176:179], v[118:121]
ds_read_b64_tr_b16 v[80:81], v163 offset:57600
v_mfma_f32_16x16x32_bf16 v[134:137], v[70:73], v[182:185], v[134:137]
ds_read_b128 v[166:169], v51 offset:16384
v_mfma_f32_16x16x32_bf16 v[122:125], a[168:171], v[190:193], v[122:125]
ds_read_b128 v[174:177], v51 offset:34816
global_load_lds_dwordx4 v13, s[42:43] offset:0 sc1
v_mfma_f32_16x16x32_bf16 v[126:129], a[172:175], v[190:193], v[126:129]
ds_read_b64_tr_b16 a[204:205], v162 offset:8192
v_mfma_f32_16x16x32_bf16 v[130:133], v[190:193], a[176:179], v[130:133]
ds_read_b64_tr_b16 a[206:207], v162 offset:10496
v_mfma_f32_16x16x32_bf16 v[158:161], v[70:73], v[190:193], v[158:161]
ds_read_b64_tr_b16 a[208:209], v162 offset:8384
global_load_lds_dwordx4 v14, s[42:43] offset:1024 sc1
v_mfma_f32_16x16x32_bf16 v[86:89], a[180:183], v[106:109], v[86:89]
ds_read_b64_tr_b16 a[210:211], v162 offset:10688
v_mfma_f32_16x16x32_bf16 v[90:93], a[184:187], v[106:109], v[90:93]
ds_read_b64_tr_b16 a[212:213], v162 offset:8576
v_mfma_f32_16x16x32_bf16 v[94:97], v[106:109], a[188:191], v[94:97]
ds_read_b64_tr_b16 a[214:215], v162 offset:10880
global_load_lds_dwordx4 v15, s[42:43] offset:2048 sc1
v_mfma_f32_16x16x32_bf16 v[98:101], v[74:77], v[106:109], v[98:101]
ds_read_b64_tr_b16 v[82:83], v164 offset:8192
v_mfma_f32_16x16x32_bf16 v[110:113], a[180:183], v[186:189], v[110:113]
ds_read_b64_tr_b16 v[84:85], v164 offset:10496
v_mfma_f32_16x16x32_bf16 v[114:117], a[184:187], v[186:189], v[114:117]
ds_read_b128 v[170:173], v52 offset:16384
s_sub_u32 s42, s42, 18432
s_subb_u32 s43, s43, 0
v_mfma_f32_16x16x32_bf16 v[118:121], v[186:189], a[188:191], v[118:121]
ds_read_b128 v[178:181], v52 offset:34816
v_mfma_f32_16x16x32_bf16 v[134:137], v[74:77], v[186:189], v[134:137]
v_mfma_f32_16x16x32_bf16 v[122:125], a[180:183], v[194:197], v[122:125]
v_mfma_f32_16x16x32_bf16 v[126:129], a[184:187], v[194:197], v[126:129]
v_mfma_f32_16x16x32_bf16 v[158:161], v[74:77], v[194:197], v[158:161]
s_waitcnt lgkmcnt(15)
v_mfma_f32_16x16x32_bf16 v[86:89], a[192:195], v[106:109], v[86:89]
v_mfma_f32_16x16x32_bf16 v[90:93], a[196:199], v[106:109], v[90:93]
s_waitcnt lgkmcnt(14)
v_mfma_f32_16x16x32_bf16 v[94:97], v[106:109], a[200:203], v[94:97]
s_waitcnt lgkmcnt(12)
v_mfma_f32_16x16x32_bf16 v[98:101], v[78:81], v[106:109], v[98:101]
s_waitcnt lgkmcnt(11)
v_mfma_f32_16x16x32_bf16 v[110:113], a[192:195], v[166:169], v[110:113]
v_mfma_f32_16x16x32_bf16 v[114:117], a[196:199], v[166:169], v[114:117]
v_mfma_f32_16x16x32_bf16 v[118:121], v[166:169], a[200:203], v[118:121]
v_mfma_f32_16x16x32_bf16 v[134:137], v[78:81], v[166:169], v[134:137]
s_waitcnt lgkmcnt(10)
v_mfma_f32_16x16x32_bf16 v[158:161], v[78:81], v[174:177], v[158:161]
s_waitcnt lgkmcnt(8)
v_mfma_f32_16x16x32_bf16 v[86:89], a[204:207], v[106:109], v[86:89]
s_waitcnt lgkmcnt(6)
v_mfma_f32_16x16x32_bf16 v[90:93], a[208:211], v[106:109], v[90:93]
s_waitcnt lgkmcnt(4)
v_mfma_f32_16x16x32_bf16 v[94:97], v[106:109], a[212:215], v[94:97]
s_waitcnt lgkmcnt(2)
v_mfma_f32_16x16x32_bf16 v[98:101], v[82:85], v[106:109], v[98:101]
s_waitcnt lgkmcnt(1)
v_mfma_f32_16x16x32_bf16 v[110:113], a[204:207], v[170:173], v[110:113]
v_mfma_f32_16x16x32_bf16 v[114:117], a[208:211], v[170:173], v[114:117]
v_mfma_f32_16x16x32_bf16 v[118:121], v[170:173], a[212:215], v[118:121]
v_mfma_f32_16x16x32_bf16 v[134:137], v[82:85], v[170:173], v[134:137]
s_waitcnt lgkmcnt(0)
v_mfma_f32_16x16x32_bf16 v[158:161], v[82:85], v[178:181], v[158:161]
s_barrier
ds_read_b32 v242, v24 offset:0
s_waitcnt lgkmcnt(0)
s_nop 2
v_fma_f32 v230, v98, v242, v134
v_fma_f32 v231, v99, v242, v135
v_fma_f32 v232, v100, v242, v136
v_fma_f32 v233, v101, v242, v137
v_cvt_pk_bf16_f32 v230, v230, v231
v_cvt_pk_bf16_f32 v231, v232, v233
ds_write_b64 v22, v[230:231] offset:40960
ds_read_b128 v[202:205], v52 offset:32768
ds_read_b128 v[206:209], v51 offset:34816
ds_read_b128 v[210:213], v52 offset:34816
v_mov_b32 v234, v32
v_mov_b32 v235, v33
v_mov_b32 v236, v34
v_mov_b32 v237, v35
v_mov_b32 v134, v158
v_mov_b32 v135, v159
v_mov_b32 v136, v160
v_mov_b32 v137, v161
s_waitcnt lgkmcnt(3)
s_barrier
ds_read_b32 v23, v24 offset:0
ds_read_b32 v148, v21
ds_read_b32 v26, v24 offset:192
ds_read_b32 v53, v27
ds_read_b32 v8, v28
ds_read_b128 v[226:229], v25 offset:0
ds_read_b64 v[158:159], v22 offset:41472
ds_read_b32 v140, v20
v_xor_b32 v51, 0x10000, v51
v_xor_b32 v52, 0x10000, v52
s_waitcnt lgkmcnt(7)
v_fma_f32 v238, v90, v23, v114
v_fma_f32 v239, v91, v23, v115
v_fma_f32 v240, v92, v23, v116
v_fma_f32 v241, v93, v23, v117
v_cvt_pk_bf16_f32 v146, v238, v239
v_cvt_pk_bf16_f32 v147, v240, v241
s_waitcnt lgkmcnt(0)
s_barrier
ds_read_b32 v242, v24 offset:64
v_fma_f32 v238, v94, v226, v118
v_fma_f32 v239, v94, v227, v119
v_fma_f32 v240, v94, v228, v120
v_fma_f32 v241, v94, v229, v121
v_cvt_pk_bf16_f32 v154, v238, v239
v_cvt_pk_bf16_f32 v155, v240, v241
v_fma_f32 v238, v86, v23, v110
v_fma_f32 v239, v87, v23, v111
v_fma_f32 v240, v88, v23, v112
v_fma_f32 v241, v89, v23, v113
v_cvt_pk_bf16_f32 v138, v238, v239
v_cvt_pk_bf16_f32 v139, v240, v241
s_waitcnt lgkmcnt(0)
v_fma_f32 v230, v98, v242, v134
v_fma_f32 v231, v99, v242, v135
v_fma_f32 v232, v100, v242, v136
v_fma_f32 v233, v101, v242, v137
v_cvt_pk_bf16_f32 v230, v230, v231
v_cvt_pk_bf16_f32 v231, v232, v233
ds_write_b64 v22, v[230:231] offset:40960
s_waitcnt vmcnt(5) lgkmcnt(0)
s_barrier
s_mov_b32 s70, 32
s_waitcnt lgkmcnt(0)
s_nop 1
s_branch .Lqa_in5
.p2align 6
.Lqa_top26:
s_waitcnt lgkmcnt(9)
v_mfma_f32_16x16x32_bf16 v[134:137], a[216:219], v[44:47], 0
s_waitcnt lgkmcnt(8)
ds_read_b128 v[162:165], v52 offset:4096
ds_read_b32 v8, v30
v_mfma_f32_16x16x32_bf16 v[134:137], a[220:223], v[102:105], v[134:137]
s_waitcnt lgkmcnt(9)
global_store_dwordx4 v16, v[230:233], s[38:39] sc1
v_mfma_f32_16x16x32_bf16 v[110:113], a[0:3], v[44:47], 0
s_waitcnt lgkmcnt(6)
ds_read_b128 v[166:169], v51 offset:6144
v_add_u32 v16, 18432, v16
v_mfma_f32_16x16x32_bf16 v[134:137], a[224:227], v[106:109], v[134:137]
s_waitcnt lgkmcnt(5)
ds_read_b128 v[170:173], v52 offset:6144
ds_read_b128 v[226:229], v25 offset:64
v_mfma_f32_16x16x32_bf16 v[134:137], a[228:231], v[0:3], v[134:137]
ds_read_b64 v[160:161], v22 offset:41472
ds_read_b32 v144, v20
v_mfma_f32_16x16x32_bf16 v[114:117], a[4:7], v[44:47], 0
s_waitcnt lgkmcnt(7)
ds_read_b128 v[174:177], v51 offset:8192
v_fma_f32 v238, v90, v23, v126
v_mfma_f32_16x16x32_bf16 v[134:137], a[232:235], v[4:7], v[134:137]
s_waitcnt lgkmcnt(7)
ds_read_b128 v[178:181], v52 offset:8192
v_fma_f32 v239, v91, v23, v127
v_mfma_f32_16x16x32_bf16 v[134:137], a[236:239], v[162:165], v[134:137]
v_fma_f32 v240, v92, v23, v128
v_fma_f32 v241, v93, v23, v129
v_mfma_f32_16x16x32_bf16 v[118:121], v[44:47], a[8:11], 0
s_waitcnt lgkmcnt(6)
ds_read_b128 v[182:185], v51 offset:10240
v_cvt_pk_bf16_f32 v150, v238, v239
v_mfma_f32_16x16x32_bf16 v[134:137], a[240:243], v[166:169], v[134:137]
s_waitcnt lgkmcnt(6)
ds_read_b128 v[186:189], v52 offset:10240
v_cvt_pk_bf16_f32 v151, v240, v241
v_mfma_f32_16x16x32_bf16 v[134:137], a[244:247], v[170:173], v[134:137]
s_nop 0
v_mfma_f32_16x16x32_bf16 v[214:217], v[150:153], v[138:141], v[32:35]
v_max_f32 v48, v218, v219
v_max_f32 v49, v220, v221
v_mfma_f32_16x16x32_bf16 v[110:113], a[12:15], v[102:105], v[110:113]
s_waitcnt lgkmcnt(6)
ds_read_b128 v[190:193], v51 offset:12288
v_fma_f32 v238, v94, v226, v130
s_waitcnt lgkmcnt(4)
v_mfma_f32_16x16x32_bf16 v[134:137], a[248:251], v[174:177], v[134:137]
s_xor_b32 s49, s49, 0x10000
ds_read_b128 v[194:197], v52 offset:12288
s_waitcnt lgkmcnt(4)
v_mfma_f32_16x16x32_bf16 v[134:137], a[252:255], v[178:181], v[134:137]
s_add_u32 m0, s49, 0
v_mul_f32 v26, 0x3eae1a93, v26
v_mul_f32 v9, v53, v26
v_mfma_f32_16x16x32_bf16 v[114:117], a[16:19], v[102:105], v[114:117]
s_waitcnt lgkmcnt(3)
ds_read_b128 v[198:201], v51 offset:14336
global_load_lds_dwordx4 v11, s[42:43] offset:-2048 sc1
v_mfma_f32_16x16x32_bf16 v[134:137], v[54:57], v[182:185], v[134:137]
s_waitcnt lgkmcnt(3)
ds_read_b128 v[202:205], v52 offset:14336
v_mul_f32 v10, v8, v26
v_mfma_f32_16x16x32_bf16 v[134:137], v[58:61], v[186:189], v[134:137]
v_max3_f32 v48, v214, v215, v48
v_max3_f32 v49, v216, v217, v49
v_mfma_f32_16x16x32_bf16 v[118:121], v[102:105], a[20:23], v[118:121]
s_waitcnt lgkmcnt(3)
ds_read_b128 v[206:209], v51 offset:16384
v_mul_f32 v48, v48, v9
v_mfma_f32_16x16x32_bf16 v[134:137], v[62:65], v[190:193], v[134:137]
s_waitcnt lgkmcnt(3)
ds_read_b128 v[210:213], v52 offset:16384
v_mul_f32 v49, v49, v10
v_mfma_f32_16x16x32_bf16 v[134:137], v[66:69], v[194:197], v[134:137]
v_max_f32 v50, v48, v49
v_mov_b32 v95, v50
v_mfma_f32_16x16x32_bf16 v[110:113], a[24:27], v[106:109], v[110:113]
s_waitcnt lgkmcnt(3)
v_permlane16_swap_b32 v50, v95
v_mfma_f32_16x16x32_bf16 v[134:137], v[70:73], v[198:201], v[134:137]
s_waitcnt lgkmcnt(2)
v_max_f32 v50, v50, v95
v_mov_b32 v95, v50
v_mfma_f32_16x16x32_bf16 v[134:137], v[74:77], v[202:205], v[134:137]
global_load_lds_dwordx4 v12, s[42:43] offset:-1024 sc1
v_fma_f32 v239, v94, v227, v131
v_mfma_f32_16x16x32_bf16 v[114:117], a[28:31], v[106:109], v[114:117]
s_waitcnt lgkmcnt(1)
v_permlane32_swap_b32 v50, v95
v_mfma_f32_16x16x32_bf16 v[134:137], v[78:81], v[206:209], v[134:137]
s_waitcnt lgkmcnt(0)
v_max_f32 v50, v50, v95
v_sub_f32 v48, v48, v50
v_mfma_f32_16x16x32_bf16 v[134:137], v[82:85], v[210:213], v[134:137]
v_sub_f32 v49, v49, v50
v_fma_f32 v240, v94, v228, v132
v_mfma_f32_16x16x32_bf16 v[118:121], v[106:109], a[32:35], v[118:121]
v_exp_f32 v96, v48
v_mfma_f32_16x16x32_bf16 v[110:113], a[36:39], v[0:3], v[110:113]
v_exp_f32 v97, v49
v_mfma_f32_16x16x32_bf16 v[114:117], a[40:43], v[0:3], v[114:117]
v_add_f32 v50, v96, v97
v_mov_b32 v95, v50
v_mfma_f32_16x16x32_bf16 v[118:121], v[0:3], a[44:47], v[118:121]
v_mul_f32 v96, v96, v53
v_fma_f32 v241, v94, v229, v133
v_mfma_f32_16x16x32_bf16 v[110:113], a[48:51], v[4:7], v[110:113]
global_load_lds_dwordx4 v13, s[42:43] offset:0 sc1
v_cvt_pk_bf16_f32 v156, v238, v239
v_mfma_f32_16x16x32_bf16 v[114:117], a[52:55], v[4:7], v[114:117]
v_permlane16_swap_b32 v50, v95
v_mfma_f32_16x16x32_bf16 v[118:121], v[4:7], a[56:59], v[118:121]
v_add_f32 v50, v50, v95
v_mov_b32 v95, v50
v_mfma_f32_16x16x32_bf16 v[110:113], a[60:63], v[162:165], v[110:113]
v_mul_f32 v97, v97, v8
v_cvt_pk_bf16_f32 v157, v240, v241
v_mfma_f32_16x16x32_bf16 v[114:117], a[64:67], v[162:165], v[114:117]
v_permlane32_swap_b32 v50, v95
v_mfma_f32_16x16x32_bf16 v[118:121], v[162:165], a[68:71], v[118:121]
v_mfma_f32_16x16x32_bf16 v[110:113], a[72:75], v[166:169], v[110:113]
s_barrier
v_mfma_f32_16x16x32_bf16 v[114:117], a[76:79], v[166:169], v[114:117]
ds_read_b32 v242, v24 offset:0
v_add_f32 v50, v50, v95
v_mfma_f32_16x16x32_bf16 v[118:121], v[166:169], a[80:83], v[118:121]
global_load_lds_dwordx4 v14, s[42:43] offset:1024 sc1
v_fma_f32 v238, v86, v23, v122
v_mfma_f32_16x16x32_bf16 v[110:113], a[84:87], v[170:173], v[110:113]
)"
R"(v_rcp_f32 v50, v50
v_mfma_f32_16x16x32_bf16 v[114:117], a[88:91], v[170:173], v[114:117]
v_mul_f32 v96, v96, v50
v_mul_f32 v97, v97, v50
v_mfma_f32_16x16x32_bf16 v[118:121], v[170:173], a[92:95], v[118:121]
v_cvt_pk_bf16_f32 v96, v96, v96
v_cvt_pk_bf16_f32 v97, v97, v97
v_mfma_f32_16x16x32_bf16 v[110:113], a[96:99], v[174:177], v[110:113]
v_and_b32 v224, v96, v40
v_and_b32 v225, v97, v41
v_mfma_f32_16x16x32_bf16 v[114:117], a[100:103], v[174:177], v[114:117]
v_and_b32 v222, v96, v42
v_and_b32 v223, v97, v43
v_mfma_f32_16x16x32_bf16 v[118:121], v[174:177], a[104:107], v[118:121]
s_nop 0
v_mfma_f32_16x16x32_bf16 v[218:221], v[154:157], v[222:225], 0
v_mfma_f32_16x16x32_bf16 v[214:217], v[158:161], v[222:225], 0
s_waitcnt lgkmcnt(0)
v_fma_f32 v230, v98, v242, v134
v_fma_f32 v231, v99, v242, v135
v_mfma_f32_16x16x32_bf16 v[110:113], a[108:111], v[178:181], v[110:113]
v_fma_f32 v232, v100, v242, v136
v_fma_f32 v233, v101, v242, v137
v_mfma_f32_16x16x32_bf16 v[114:117], a[112:115], v[178:181], v[114:117]
global_load_lds_dwordx4 v15, s[42:43] offset:2048 sc1
s_add_u32 s42, s42, s46
s_addc_u32 s43, s43, s47
v_mfma_f32_16x16x32_bf16 v[118:121], v[178:181], a[116:119], v[118:121]
v_cvt_pk_bf16_f32 v230, v230, v231
v_cvt_pk_bf16_f32 v231, v232, v233
v_mfma_f32_16x16x32_bf16 v[110:113], a[120:123], v[182:185], v[110:113]
v_cvt_pk_bf16_f32 v254, v218, v219
v_cvt_pk_bf16_f32 v255, v220, v221
v_mfma_f32_16x16x32_bf16 v[114:117], a[124:127], v[182:185], v[114:117]
ds_write_b64 v18, v[254:255]
v_mfma_f32_16x16x32_bf16 v[118:121], v[182:185], a[128:131], v[118:121]
v_cvt_pk_bf16_f32 v96, v214, v215
v_fma_f32 v239, v87, v23, v123
v_mfma_f32_16x16x32_bf16 v[110:113], a[132:135], v[186:189], v[110:113]
ds_write_b32 v19, v96
v_mfma_f32_16x16x32_bf16 v[114:117], a[136:139], v[186:189], v[114:117]
ds_write_b64 v22, v[230:231] offset:40960
v_mfma_f32_16x16x32_bf16 v[118:121], v[186:189], a[140:143], v[118:121]
v_fma_f32 v240, v88, v23, v124
v_fma_f32 v241, v89, v23, v125
v_mfma_f32_16x16x32_bf16 v[110:113], a[144:147], v[190:193], v[110:113]
v_cvt_pk_bf16_f32 v142, v238, v239
v_cvt_pk_bf16_f32 v143, v240, v241
v_mfma_f32_16x16x32_bf16 v[114:117], a[148:151], v[190:193], v[114:117]
v_mfma_f32_16x16x32_bf16 v[118:121], v[190:193], a[152:155], v[118:121]
v_mfma_f32_16x16x32_bf16 v[110:113], a[156:159], v[194:197], v[110:113]
v_mfma_f32_16x16x32_bf16 v[114:117], a[160:163], v[194:197], v[114:117]
v_mfma_f32_16x16x32_bf16 v[118:121], v[194:197], a[164:167], v[118:121]
v_mfma_f32_16x16x32_bf16 v[110:113], a[168:171], v[198:201], v[110:113]
v_mfma_f32_16x16x32_bf16 v[114:117], a[172:175], v[198:201], v[114:117]
v_mfma_f32_16x16x32_bf16 v[118:121], v[198:201], a[176:179], v[118:121]
v_mfma_f32_16x16x32_bf16 v[110:113], a[180:183], v[202:205], v[110:113]
v_mfma_f32_16x16x32_bf16 v[114:117], a[184:187], v[202:205], v[114:117]
s_waitcnt vmcnt(5) lgkmcnt(0)
s_barrier
v_mfma_f32_16x16x32_bf16 v[118:121], v[202:205], a[188:191], v[118:121]
ds_read_b128 v[44:47], v51 offset:18432
v_mfma_f32_16x16x32_bf16 v[110:113], a[192:195], v[206:209], v[110:113]
ds_read_b128 v[102:105], v52 offset:18432
ds_read_b128 v[230:233], v17 offset:0
v_mfma_f32_16x16x32_bf16 v[114:117], a[196:199], v[206:209], v[114:117]
ds_read_b32 v23, v24 offset:0
ds_read_b32 v148, v21
v_mfma_f32_16x16x32_bf16 v[218:221], v[150:153], v[142:145], v[36:39]
v_mfma_f32_16x16x32_bf16 v[118:121], v[206:209], a[200:203], v[118:121]
ds_read_b128 v[106:109], v51 offset:20480
ds_read_b32 v26, v24 offset:192
v_mfma_f32_16x16x32_bf16 v[110:113], a[204:207], v[210:213], v[110:113]
ds_read_b128 v[0:3], v52 offset:20480
ds_read_b32 v53, v27
v_mfma_f32_16x16x32_bf16 v[114:117], a[208:211], v[210:213], v[114:117]
ds_read_b32 v8, v28
ds_read_b128 v[226:229], v25 offset:0
v_mfma_f32_16x16x32_bf16 v[118:121], v[210:213], a[212:215], v[118:121]
s_waitcnt lgkmcnt(10)
ds_read_b128 v[4:7], v51 offset:22528
ds_read_b64 v[158:159], v22 offset:41472
v_mfma_f32_16x16x32_bf16 v[134:137], a[216:219], v[44:47], 0
s_waitcnt lgkmcnt(11)
ds_read_b128 v[162:165], v52 offset:22528
ds_read_b32 v140, v20
v_mfma_f32_16x16x32_bf16 v[134:137], a[220:223], v[102:105], v[134:137]
s_waitcnt lgkmcnt(12)
global_store_dwordx4 v16, v[230:233], s[38:39] sc1
v_mfma_f32_16x16x32_bf16 v[122:125], a[0:3], v[44:47], 0
s_waitcnt lgkmcnt(9)
ds_read_b128 v[166:169], v51 offset:24576
v_add_u32 v16, 18432, v16
v_mfma_f32_16x16x32_bf16 v[134:137], a[224:227], v[106:109], v[134:137]
s_waitcnt lgkmcnt(8)
ds_read_b128 v[170:173], v52 offset:24576
v_fma_f32 v238, v90, v23, v114
v_mfma_f32_16x16x32_bf16 v[134:137], a[228:231], v[0:3], v[134:137]
v_fma_f32 v239, v91, v23, v115
v_fma_f32 v240, v92, v23, v116
v_mfma_f32_16x16x32_bf16 v[126:129], a[4:7], v[44:47], 0
s_waitcnt lgkmcnt(5)
ds_read_b128 v[174:177], v51 offset:26624
v_fma_f32 v241, v93, v23, v117
v_mfma_f32_16x16x32_bf16 v[134:137], a[232:235], v[4:7], v[134:137]
s_waitcnt lgkmcnt(4)
ds_read_b128 v[178:181], v52 offset:26624
v_cvt_pk_bf16_f32 v146, v238, v239
v_mfma_f32_16x16x32_bf16 v[134:137], a[236:239], v[162:165], v[134:137]
v_cvt_pk_bf16_f32 v147, v240, v241
v_max_f32 v48, v218, v219
v_mfma_f32_16x16x32_bf16 v[130:133], v[44:47], a[8:11], 0
ds_read_b128 v[182:185], v51 offset:28672
v_mfma_f32_16x16x32_bf16 v[214:217], v[146:149], v[142:145], v[234:237]
s_waitcnt lgkmcnt(4)
v_max_f32 v49, v220, v221
v_fma_f32 v238, v94, v226, v118
v_mfma_f32_16x16x32_bf16 v[134:137], a[240:243], v[166:169], v[134:137]
s_waitcnt lgkmcnt(3)
ds_read_b128 v[186:189], v52 offset:28672
v_fma_f32 v239, v94, v227, v119
v_mfma_f32_16x16x32_bf16 v[134:137], a[244:247], v[170:173], v[134:137]
v_mul_f32 v26, 0x3eae1a93, v26
v_mul_f32 v9, v53, v26
v_mfma_f32_16x16x32_bf16 v[122:125], a[12:15], v[102:105], v[122:125]
s_waitcnt lgkmcnt(3)
ds_read_b128 v[190:193], v51 offset:30720
v_mul_f32 v10, v8, v26
v_mfma_f32_16x16x32_bf16 v[134:137], a[248:251], v[174:177], v[134:137]
s_add_u32 m0, s49, 18432
ds_read_b128 v[194:197], v52 offset:30720
s_waitcnt lgkmcnt(4)
v_mfma_f32_16x16x32_bf16 v[134:137], a[252:255], v[178:181], v[134:137]
global_load_lds_dwordx4 v11, s[42:43] offset:-2048 sc1
v_max3_f32 v48, v214, v215, v48
v_mfma_f32_16x16x32_bf16 v[126:129], a[16:19], v[102:105], v[126:129]
s_waitcnt lgkmcnt(3)
ds_read_b128 v[198:201], v51 offset:32768
v_max3_f32 v49, v216, v217, v49
v_mfma_f32_16x16x32_bf16 v[134:137], v[54:57], v[182:185], v[134:137]
s_waitcnt lgkmcnt(3)
ds_read_b128 v[202:205], v52 offset:32768
v_mul_f32 v48, v48, v9
v_mfma_f32_16x16x32_bf16 v[134:137], v[58:61], v[186:189], v[134:137]
v_mul_f32 v49, v49, v10
v_max_f32 v50, v48, v49
v_mfma_f32_16x16x32_bf16 v[130:133], v[102:105], a[20:23], v[130:133]
s_waitcnt lgkmcnt(3)
ds_read_b128 v[206:209], v51 offset:34816
v_mov_b32 v95, v50
v_mfma_f32_16x16x32_bf16 v[134:137], v[62:65], v[190:193], v[134:137]
s_waitcnt lgkmcnt(3)
ds_read_b128 v[210:213], v52 offset:34816
v_fma_f32 v240, v94, v228, v120
v_mfma_f32_16x16x32_bf16 v[134:137], v[66:69], v[194:197], v[134:137]
v_permlane16_swap_b32 v50, v95
v_mfma_f32_16x16x32_bf16 v[122:125], a[24:27], v[106:109], v[122:125]
s_waitcnt lgkmcnt(3)
v_max_f32 v50, v50, v95
v_mov_b32 v95, v50
v_mfma_f32_16x16x32_bf16 v[134:137], v[70:73], v[198:201], v[134:137]
s_waitcnt lgkmcnt(2)
global_load_lds_dwordx4 v12, s[42:43] offset:-1024 sc1
v_fma_f32 v241, v94, v229, v121
v_mfma_f32_16x16x32_bf16 v[134:137], v[74:77], v[202:205], v[134:137]
v_permlane32_swap_b32 v50, v95
v_mfma_f32_16x16x32_bf16 v[126:129], a[28:31], v[106:109], v[126:129]
s_waitcnt lgkmcnt(1)
v_max_f32 v50, v50, v95
v_sub_f32 v48, v48, v50
v_mfma_f32_16x16x32_bf16 v[134:137], v[78:81], v[206:209], v[134:137]
s_waitcnt lgkmcnt(0)
v_sub_f32 v49, v49, v50
v_cvt_pk_bf16_f32 v154, v238, v239
v_mfma_f32_16x16x32_bf16 v[134:137], v[82:85], v[210:213], v[134:137]
v_exp_f32 v96, v48
v_mfma_f32_16x16x32_bf16 v[130:133], v[106:109], a[32:35], v[130:133]
v_exp_f32 v97, v49
v_mfma_f32_16x16x32_bf16 v[122:125], a[36:39], v[0:3], v[122:125]
v_add_f32 v50, v96, v97
v_mov_b32 v95, v50
v_mfma_f32_16x16x32_bf16 v[126:129], a[40:43], v[0:3], v[126:129]
v_mul_f32 v96, v96, v53
v_cvt_pk_bf16_f32 v155, v240, v241
v_mfma_f32_16x16x32_bf16 v[130:133], v[0:3], a[44:47], v[130:133]
global_load_lds_dwordx4 v13, s[42:43] offset:0 sc1
v_fma_f32 v238, v86, v23, v110
v_mfma_f32_16x16x32_bf16 v[122:125], a[48:51], v[4:7], v[122:125]
v_permlane16_swap_b32 v50, v95
v_mfma_f32_16x16x32_bf16 v[126:129], a[52:55], v[4:7], v[126:129]
v_add_f32 v50, v50, v95
v_mov_b32 v95, v50
v_mfma_f32_16x16x32_bf16 v[130:133], v[4:7], a[56:59], v[130:133]
v_mul_f32 v97, v97, v8
v_fma_f32 v239, v87, v23, v111
v_mfma_f32_16x16x32_bf16 v[122:125], a[60:63], v[162:165], v[122:125]
v_permlane32_swap_b32 v50, v95
v_mfma_f32_16x16x32_bf16 v[126:129], a[64:67], v[162:165], v[126:129]
v_add_f32 v50, v50, v95
v_fma_f32 v240, v88, v23, v112
v_mfma_f32_16x16x32_bf16 v[130:133], v[162:165], a[68:71], v[130:133]
v_mfma_f32_16x16x32_bf16 v[122:125], a[72:75], v[166:169], v[122:125]
s_barrier
v_mfma_f32_16x16x32_bf16 v[126:129], a[76:79], v[166:169], v[126:129]
global_load_lds_dwordx4 v14, s[42:43] offset:1024 sc1
ds_read_b32 v242, v24 offset:64
v_mfma_f32_16x16x32_bf16 v[130:133], v[166:169], a[80:83], v[130:133]
v_rcp_f32 v50, v50
v_mfma_f32_16x16x32_bf16 v[122:125], a[84:87], v[170:173], v[122:125]
v_mul_f32 v96, v96, v50
v_mul_f32 v97, v97, v50
v_mfma_f32_16x16x32_bf16 v[126:129], a[88:91], v[170:173], v[126:129]
v_cvt_pk_bf16_f32 v96, v96, v96
v_cvt_pk_bf16_f32 v97, v97, v97
v_mfma_f32_16x16x32_bf16 v[130:133], v[170:173], a[92:95], v[130:133]
v_and_b32 v222, v96, v40
v_and_b32 v223, v97, v41
v_mfma_f32_16x16x32_bf16 v[122:125], a[96:99], v[174:177], v[122:125]
v_and_b32 v224, v96, v42
v_and_b32 v225, v97, v43
v_mfma_f32_16x16x32_bf16 v[126:129], a[100:103], v[174:177], v[126:129]
s_nop 0
v_mfma_f32_16x16x32_bf16 v[218:221], v[154:157], v[222:225], 0
v_mfma_f32_16x16x32_bf16 v[214:217], v[158:161], v[222:225], 0
s_waitcnt lgkmcnt(0)
v_fma_f32 v230, v98, v242, v134
v_fma_f32 v231, v99, v242, v135
v_mfma_f32_16x16x32_bf16 v[130:133], v[174:177], a[104:107], v[130:133]
v_fma_f32 v232, v100, v242, v136
v_fma_f32 v233, v101, v242, v137
v_mfma_f32_16x16x32_bf16 v[122:125], a[108:111], v[178:181], v[122:125]
global_load_lds_dwordx4 v15, s[42:43] offset:2048 sc1
s_sub_u32 s42, s42, 18432
s_subb_u32 s43, s43, 0
v_mfma_f32_16x16x32_bf16 v[126:129], a[112:115], v[178:181], v[126:129]
v_cvt_pk_bf16_f32 v230, v230, v231
v_cvt_pk_bf16_f32 v231, v232, v233
v_mfma_f32_16x16x32_bf16 v[130:133], v[178:181], a[116:119], v[130:133]
v_cvt_pk_bf16_f32 v254, v218, v219
v_cvt_pk_bf16_f32 v255, v220, v221
v_mfma_f32_16x16x32_bf16 v[122:125], a[120:123], v[182:185], v[122:125]
ds_write_b64 v18, v[254:255]
v_mfma_f32_16x16x32_bf16 v[126:129], a[124:127], v[182:185], v[126:129]
v_cvt_pk_bf16_f32 v96, v214, v215
v_fma_f32 v241, v89, v23, v113
v_mfma_f32_16x16x32_bf16 v[130:133], v[182:185], a[128:131], v[130:133]
ds_write_b32 v19, v96
v_mfma_f32_16x16x32_bf16 v[122:125], a[132:135], v[186:189], v[122:125]
ds_write_b64 v22, v[230:231] offset:40960
v_mfma_f32_16x16x32_bf16 v[126:129], a[136:139], v[186:189], v[126:129]
v_cvt_pk_bf16_f32 v138, v238, v239
v_cvt_pk_bf16_f32 v139, v240, v241
v_mfma_f32_16x16x32_bf16 v[130:133], v[186:189], a[140:143], v[130:133]
v_mfma_f32_16x16x32_bf16 v[122:125], a[144:147], v[190:193], v[122:125]
v_mfma_f32_16x16x32_bf16 v[126:129], a[148:151], v[190:193], v[126:129]
v_mfma_f32_16x16x32_bf16 v[130:133], v[190:193], a[152:155], v[130:133]
v_mfma_f32_16x16x32_bf16 v[122:125], a[156:159], v[194:197], v[122:125]
v_mfma_f32_16x16x32_bf16 v[126:129], a[160:163], v[194:197], v[126:129]
v_mfma_f32_16x16x32_bf16 v[130:133], v[194:197], a[164:167], v[130:133]
v_mfma_f32_16x16x32_bf16 v[122:125], a[168:171], v[198:201], v[122:125]
v_mfma_f32_16x16x32_bf16 v[126:129], a[172:175], v[198:201], v[126:129]
v_xor_b32 v51, 0x10000, v51
v_xor_b32 v52, 0x10000, v52
v_mfma_f32_16x16x32_bf16 v[130:133], v[198:201], a[176:179], v[130:133]
v_mfma_f32_16x16x32_bf16 v[122:125], a[180:183], v[202:205], v[122:125]
v_mfma_f32_16x16x32_bf16 v[126:129], a[184:187], v[202:205], v[126:129]
s_waitcnt vmcnt(5) lgkmcnt(0)
s_barrier
.Lqa_in5:
v_mfma_f32_16x16x32_bf16 v[130:133], v[202:205], a[188:191], v[130:133]
s_cmp_eq_u32 s70, s73
s_cbranch_scc1 .Lqa_hk6
.Lqa_hr7:
ds_read_b128 v[44:47], v51 offset:0
v_mfma_f32_16x16x32_bf16 v[122:125], a[192:195], v[206:209], v[122:125]
ds_read_b128 v[102:105], v52 offset:0
ds_read_b128 v[230:233], v17 offset:0
v_mfma_f32_16x16x32_bf16 v[126:129], a[196:199], v[206:209], v[126:129]
ds_read_b32 v23, v24 offset:64
ds_read_b32 v152, v21
v_mfma_f32_16x16x32_bf16 v[218:221], v[146:149], v[138:141], v[36:39]
v_mfma_f32_16x16x32_bf16 v[130:133], v[206:209], a[200:203], v[130:133]
ds_read_b128 v[106:109], v51 offset:2048
ds_read_b32 v26, v24 offset:128
v_mfma_f32_16x16x32_bf16 v[122:125], a[204:207], v[210:213], v[122:125]
ds_read_b128 v[0:3], v52 offset:2048
ds_read_b32 v53, v29
v_mfma_f32_16x16x32_bf16 v[126:129], a[208:211], v[210:213], v[126:129]
v_mfma_f32_16x16x32_bf16 v[130:133], v[210:213], a[212:215], v[130:133]
s_sub_u32 s70, s70, 1
ds_read_b128 v[4:7], v51 offset:4096
s_cbranch_scc0 .Lqa_top26
s_waitcnt lgkmcnt(0)
ds_read_b32 v8, v30
global_store_dwordx4 v16, v[230:233], s[38:39] sc1
v_add_u32 v16, 18432, v16
ds_read_b128 v[226:229], v25 offset:64
ds_read_b64 v[160:161], v22 offset:41472
ds_read_b32 v144, v20
v_fma_f32 v238, v90, v23, v126
v_fma_f32 v239, v91, v23, v127
v_fma_f32 v240, v92, v23, v128
v_fma_f32 v241, v93, v23, v129
v_cvt_pk_bf16_f32 v150, v238, v239
v_cvt_pk_bf16_f32 v151, v240, v241
s_nop 1
v_mfma_f32_16x16x32_bf16 v[214:217], v[150:153], v[138:141], v[32:35]
s_waitcnt lgkmcnt(2)
v_max_f32 v48, v218, v219
v_max_f32 v49, v220, v221
v_fma_f32 v238, v94, v226, v130
v_mul_f32 v26, 0x3eae1a93, v26
v_mul_f32 v9, v53, v26
v_mul_f32 v10, v8, v26
s_nop 0
v_max3_f32 v48, v214, v215, v48
v_max3_f32 v49, v216, v217, v49
v_mul_f32 v48, v48, v9
v_mul_f32 v49, v49, v10
v_max_f32 v50, v48, v49
v_mov_b32 v95, v50
s_nop 1
v_permlane16_swap_b32 v50, v95
v_max_f32 v50, v50, v95
v_mov_b32 v95, v50
v_fma_f32 v239, v94, v227, v131
s_nop 0
v_permlane32_swap_b32 v50, v95
v_max_f32 v50, v50, v95
v_sub_f32 v48, v48, v50
v_sub_f32 v49, v49, v50
v_fma_f32 v240, v94, v228, v132
v_exp_f32 v96, v48
v_exp_f32 v97, v49
s_nop 0
v_add_f32 v50, v96, v97
v_mov_b32 v95, v50
v_mul_f32 v96, v96, v53
v_fma_f32 v241, v94, v229, v133
v_cvt_pk_bf16_f32 v156, v238, v239
v_permlane16_swap_b32 v50, v95
v_add_f32 v50, v50, v95
v_mov_b32 v95, v50
v_mul_f32 v97, v97, v8
v_cvt_pk_bf16_f32 v157, v240, v241
v_permlane32_swap_b32 v50, v95
s_waitcnt lgkmcnt(0)
s_barrier
v_add_f32 v50, v50, v95
v_fma_f32 v238, v86, v23, v122
v_rcp_f32 v50, v50
s_nop 0
v_mul_f32 v96, v96, v50
v_mul_f32 v97, v97, v50
v_cvt_pk_bf16_f32 v96, v96, v96
v_cvt_pk_bf16_f32 v97, v97, v97
v_and_b32 v224, v96, v40
v_and_b32 v225, v97, v41
v_and_b32 v222, v96, v42
v_and_b32 v223, v97, v43
s_nop 1
v_mfma_f32_16x16x32_bf16 v[218:221], v[154:157], v[222:225], 0
v_mfma_f32_16x16x32_bf16 v[214:217], v[158:161], v[222:225], 0
s_nop 6
v_cvt_pk_bf16_f32 v254, v218, v219
v_cvt_pk_bf16_f32 v255, v220, v221
ds_write_b64 v18, v[254:255]
v_cvt_pk_bf16_f32 v96, v214, v215
v_fma_f32 v239, v87, v23, v123
ds_write_b32 v19, v96
v_fma_f32 v240, v88, v23, v124
v_fma_f32 v241, v89, v23, v125
v_cvt_pk_bf16_f32 v142, v238, v239
v_cvt_pk_bf16_f32 v143, v240, v241
s_waitcnt vmcnt(0)
s_waitcnt lgkmcnt(0)
s_barrier
ds_read_b128 v[230:233], v17 offset:0
s_waitcnt lgkmcnt(0)
global_store_dwordx4 v16, v[230:233], s[38:39] sc1
s_waitcnt vmcnt(0)
s_barrier
s_endpgm
.Lqa_w2:
s_getpc_b64 s[90:91]
.Lqa_pc48:
s_add_u32 s90, s90, .Lqa_e31-.Lqa_pc48
s_addc_u32 s91, s91, 0
s_branch .Lqa_k39
.Lqa_bk47:
v_add_u32 v255, 0x10000, v9
v_add_u32 v162, 0x20000, v9
v_add_u32 v163, 0x10000, v10
v_add_u32 v164, 0x20000, v10
v_mov_b32 v106, 0x3F803F80
v_mov_b32 v107, 0x3F803F80
v_mov_b32 v108, 0x3F803F80
v_mov_b32 v109, 0x3F803F80
v_mov_b32 v141, 0
v_mov_b32 v145, 0
)"
R"(v_mov_b32 v149, 0
v_mov_b32 v153, 0
v_mov_b32 v165, 46080
s_waitcnt lgkmcnt(0)
s_mul_i32 s37, s37, 0x9E3779B1
s_add_u32 s37, s37, 0x7F4A7C15
v_add_u32 v46, s37, v46
v_add_u32 v48, s37, v48
v_add_u32 v50, s37, v50
ds_write_b32 v165, v141
s_mov_b64 s[76:77], 0
s_mov_b32 s73, 18
s_add_u32 m0, s48, 4096
s_nop 0
global_load_lds_dwordx4 v0, s[40:41] offset:-4096
global_load_lds_dwordx4 v1, s[40:41] offset:-3072
global_load_lds_dwordx4 v2, s[40:41] offset:-2048
global_load_lds_dwordx4 v3, s[40:41] offset:-1024
global_load_lds_dwordx4 v4, s[40:41] offset:0
global_load_lds_dwordx4 v5, s[40:41] offset:1024
global_load_lds_dwordx4 v6, s[40:41] offset:2048
global_load_lds_dwordx4 v7, s[40:41] offset:3072
s_add_u32 m0, s48, 12288
s_nop 0
global_load_lds_dwordx4 v8, s[40:41] offset:-4096
s_add_u32 s40, s40, 221184
s_addc_u32 s41, s41, 0
s_add_u32 m0, s48, 40960
s_nop 0
global_load_lds_dwordx4 v0, s[40:41] offset:-4096
global_load_lds_dwordx4 v1, s[40:41] offset:-3072
global_load_lds_dwordx4 v2, s[40:41] offset:-2048
global_load_lds_dwordx4 v3, s[40:41] offset:-1024
global_load_lds_dwordx4 v4, s[40:41] offset:0
global_load_lds_dwordx4 v5, s[40:41] offset:1024
global_load_lds_dwordx4 v6, s[40:41] offset:2048
global_load_lds_dwordx4 v7, s[40:41] offset:3072
s_add_u32 m0, s48, 49152
s_nop 0
global_load_lds_dwordx4 v8, s[40:41] offset:-4096
s_add_u32 s40, s40, 221184
s_addc_u32 s41, s41, 0
s_waitcnt vmcnt(9) lgkmcnt(0)
s_barrier
s_add_u32 m0, s48, 77824
ds_read_b64_tr_b16 a[0:1], v9 offset:47104
ds_read_b64_tr_b16 a[2:3], v9 offset:49408
global_load_lds_dwordx4 v0, s[40:41] offset:-4096
ds_read_b64_tr_b16 a[4:5], v9 offset:47296
ds_read_b64_tr_b16 a[6:7], v9 offset:49600
global_load_lds_dwordx4 v1, s[40:41] offset:-3072
ds_read_b64_tr_b16 a[8:9], v9 offset:47488
global_load_lds_dwordx4 v2, s[40:41] offset:-2048
ds_read_b64_tr_b16 a[10:11], v9 offset:49792
ds_read_b64_tr_b16 a[216:217], v10 offset:47104
global_load_lds_dwordx4 v3, s[40:41] offset:-1024
ds_read_b64_tr_b16 a[218:219], v10 offset:49408
global_load_lds_dwordx4 v4, s[40:41] offset:0
ds_read_b128 v[166:169], v51 offset:0
ds_read_b128 v[174:177], v51 offset:18432
global_load_lds_dwordx4 v5, s[40:41] offset:1024
ds_read_b64_tr_b16 a[12:13], v255 offset:0
global_load_lds_dwordx4 v6, s[40:41] offset:2048
ds_read_b64_tr_b16 a[14:15], v255 offset:2304
ds_read_b64_tr_b16 a[16:17], v255 offset:192
global_load_lds_dwordx4 v7, s[40:41] offset:3072
ds_read_b64_tr_b16 a[18:19], v255 offset:2496
s_add_u32 m0, s48, 86016
ds_read_b64_tr_b16 a[20:21], v255 offset:384
ds_read_b64_tr_b16 a[22:23], v255 offset:2688
global_load_lds_dwordx4 v8, s[40:41] offset:-4096
ds_read_b64_tr_b16 a[220:221], v163 offset:0
s_add_u32 s40, s40, 221184
ds_read_b64_tr_b16 a[222:223], v163 offset:2304
ds_read_b128 v[170:173], v52 offset:0
s_addc_u32 s41, s41, 0
ds_read_b128 v[178:181], v52 offset:18432
s_waitcnt vmcnt(9) lgkmcnt(0)
s_barrier
s_add_u32 m0, s48, 4096
ds_read_b64_tr_b16 a[24:25], v255 offset:18432
v_mfma_f32_16x16x32_bf16 v[86:89], a[0:3], v[106:109], 0
ds_read_b64_tr_b16 a[26:27], v255 offset:20736
v_mfma_f32_16x16x32_bf16 v[90:93], a[4:7], v[106:109], 0
global_load_lds_dwordx4 v0, s[40:41] offset:-4096
ds_read_b64_tr_b16 a[28:29], v255 offset:18624
v_mfma_f32_16x16x32_bf16 v[94:97], v[106:109], a[8:11], 0
ds_read_b64_tr_b16 a[30:31], v255 offset:20928
global_load_lds_dwordx4 v1, s[40:41] offset:-3072
v_mfma_f32_16x16x32_bf16 v[102:105], v[106:109], a[216:219], 0
ds_read_b64_tr_b16 a[32:33], v255 offset:18816
v_mfma_f32_16x16x32_bf16 v[110:113], a[0:3], v[166:169], 0
ds_read_b64_tr_b16 a[34:35], v255 offset:21120
global_load_lds_dwordx4 v2, s[40:41] offset:-2048
v_mfma_f32_16x16x32_bf16 v[114:117], a[4:7], v[166:169], 0
ds_read_b64_tr_b16 a[224:225], v163 offset:18432
v_mfma_f32_16x16x32_bf16 v[118:121], v[166:169], a[8:11], 0
global_load_lds_dwordx4 v3, s[40:41] offset:-1024
ds_read_b64_tr_b16 a[226:227], v163 offset:20736
v_mfma_f32_16x16x32_bf16 v[134:137], v[166:169], a[216:219], 0
ds_read_b128 v[182:185], v51 offset:2048
global_load_lds_dwordx4 v4, s[40:41] offset:0
v_mfma_f32_16x16x32_bf16 v[122:125], a[0:3], v[174:177], 0
ds_read_b128 v[190:193], v51 offset:20480
v_mfma_f32_16x16x32_bf16 v[126:129], a[4:7], v[174:177], 0
ds_read_b64_tr_b16 a[36:37], v255 offset:36864
global_load_lds_dwordx4 v5, s[40:41] offset:1024
v_mfma_f32_16x16x32_bf16 v[130:133], v[174:177], a[8:11], 0
ds_read_b64_tr_b16 a[38:39], v255 offset:39168
v_mfma_f32_16x16x32_bf16 v[158:161], v[174:177], a[216:219], 0
global_load_lds_dwordx4 v6, s[40:41] offset:2048
ds_read_b64_tr_b16 a[40:41], v255 offset:37056
v_mfma_f32_16x16x32_bf16 v[86:89], a[12:15], v[106:109], v[86:89]
ds_read_b64_tr_b16 a[42:43], v255 offset:39360
v_mfma_f32_16x16x32_bf16 v[90:93], a[16:19], v[106:109], v[90:93]
global_load_lds_dwordx4 v7, s[40:41] offset:3072
ds_read_b64_tr_b16 a[44:45], v255 offset:37248
v_mfma_f32_16x16x32_bf16 v[94:97], v[106:109], a[20:23], v[94:97]
s_add_u32 m0, s48, 12288
ds_read_b64_tr_b16 a[46:47], v255 offset:39552
v_mfma_f32_16x16x32_bf16 v[102:105], v[106:109], a[220:223], v[102:105]
ds_read_b64_tr_b16 a[228:229], v163 offset:36864
v_mfma_f32_16x16x32_bf16 v[110:113], a[12:15], v[170:173], v[110:113]
global_load_lds_dwordx4 v8, s[40:41] offset:-4096
ds_read_b64_tr_b16 a[230:231], v163 offset:39168
v_mfma_f32_16x16x32_bf16 v[114:117], a[16:19], v[170:173], v[114:117]
ds_read_b128 v[186:189], v52 offset:2048
v_mfma_f32_16x16x32_bf16 v[118:121], v[170:173], a[20:23], v[118:121]
s_add_u32 s40, s40, 221184
ds_read_b128 v[194:197], v52 offset:20480
v_mfma_f32_16x16x32_bf16 v[134:137], v[170:173], a[220:223], v[134:137]
v_mfma_f32_16x16x32_bf16 v[122:125], a[12:15], v[178:181], v[122:125]
s_addc_u32 s41, s41, 0
v_mfma_f32_16x16x32_bf16 v[126:129], a[16:19], v[178:181], v[126:129]
v_mfma_f32_16x16x32_bf16 v[130:133], v[178:181], a[20:23], v[130:133]
v_mfma_f32_16x16x32_bf16 v[158:161], v[178:181], a[220:223], v[158:161]
s_waitcnt vmcnt(9) lgkmcnt(0)
s_barrier
s_add_u32 m0, s48, 40960
ds_read_b64_tr_b16 a[48:49], v255 offset:55296
v_mfma_f32_16x16x32_bf16 v[86:89], a[24:27], v[106:109], v[86:89]
ds_read_b64_tr_b16 a[50:51], v255 offset:57600
v_mfma_f32_16x16x32_bf16 v[90:93], a[28:31], v[106:109], v[90:93]
global_load_lds_dwordx4 v0, s[40:41] offset:-4096
ds_read_b64_tr_b16 a[52:53], v255 offset:55488
v_mfma_f32_16x16x32_bf16 v[94:97], v[106:109], a[32:35], v[94:97]
ds_read_b64_tr_b16 a[54:55], v255 offset:57792
global_load_lds_dwordx4 v1, s[40:41] offset:-3072
v_mfma_f32_16x16x32_bf16 v[102:105], v[106:109], a[224:227], v[102:105]
ds_read_b64_tr_b16 a[56:57], v255 offset:55680
v_mfma_f32_16x16x32_bf16 v[110:113], a[24:27], v[182:185], v[110:113]
ds_read_b64_tr_b16 a[58:59], v255 offset:57984
global_load_lds_dwordx4 v2, s[40:41] offset:-2048
v_mfma_f32_16x16x32_bf16 v[114:117], a[28:31], v[182:185], v[114:117]
ds_read_b64_tr_b16 a[232:233], v163 offset:55296
v_mfma_f32_16x16x32_bf16 v[118:121], v[182:185], a[32:35], v[118:121]
global_load_lds_dwordx4 v3, s[40:41] offset:-1024
ds_read_b64_tr_b16 a[234:235], v163 offset:57600
v_mfma_f32_16x16x32_bf16 v[134:137], v[182:185], a[224:227], v[134:137]
ds_read_b128 v[166:169], v51 offset:4096
global_load_lds_dwordx4 v4, s[40:41] offset:0
v_mfma_f32_16x16x32_bf16 v[122:125], a[24:27], v[190:193], v[122:125]
ds_read_b128 v[174:177], v51 offset:22528
v_mfma_f32_16x16x32_bf16 v[126:129], a[28:31], v[190:193], v[126:129]
ds_read_b64_tr_b16 a[60:61], v162 offset:8192
global_load_lds_dwordx4 v5, s[40:41] offset:1024
v_mfma_f32_16x16x32_bf16 v[130:133], v[190:193], a[32:35], v[130:133]
ds_read_b64_tr_b16 a[62:63], v162 offset:10496
v_mfma_f32_16x16x32_bf16 v[158:161], v[190:193], a[224:227], v[158:161]
global_load_lds_dwordx4 v6, s[40:41] offset:2048
ds_read_b64_tr_b16 a[64:65], v162 offset:8384
v_mfma_f32_16x16x32_bf16 v[86:89], a[36:39], v[106:109], v[86:89]
ds_read_b64_tr_b16 a[66:67], v162 offset:10688
v_mfma_f32_16x16x32_bf16 v[90:93], a[40:43], v[106:109], v[90:93]
global_load_lds_dwordx4 v7, s[40:41] offset:3072
ds_read_b64_tr_b16 a[68:69], v162 offset:8576
v_mfma_f32_16x16x32_bf16 v[94:97], v[106:109], a[44:47], v[94:97]
s_add_u32 m0, s48, 49152
ds_read_b64_tr_b16 a[70:71], v162 offset:10880
v_mfma_f32_16x16x32_bf16 v[102:105], v[106:109], a[228:231], v[102:105]
ds_read_b64_tr_b16 a[236:237], v164 offset:8192
v_mfma_f32_16x16x32_bf16 v[110:113], a[36:39], v[186:189], v[110:113]
global_load_lds_dwordx4 v8, s[40:41] offset:-4096
ds_read_b64_tr_b16 a[238:239], v164 offset:10496
v_mfma_f32_16x16x32_bf16 v[114:117], a[40:43], v[186:189], v[114:117]
ds_read_b128 v[170:173], v52 offset:4096
v_mfma_f32_16x16x32_bf16 v[118:121], v[186:189], a[44:47], v[118:121]
s_add_u32 s40, s40, 221184
ds_read_b128 v[178:181], v52 offset:22528
v_mfma_f32_16x16x32_bf16 v[134:137], v[186:189], a[228:231], v[134:137]
v_mfma_f32_16x16x32_bf16 v[122:125], a[36:39], v[194:197], v[122:125]
s_addc_u32 s41, s41, 0
v_mfma_f32_16x16x32_bf16 v[126:129], a[40:43], v[194:197], v[126:129]
v_mfma_f32_16x16x32_bf16 v[130:133], v[194:197], a[44:47], v[130:133]
v_mfma_f32_16x16x32_bf16 v[158:161], v[194:197], a[228:231], v[158:161]
s_waitcnt vmcnt(9) lgkmcnt(0)
s_barrier
s_add_u32 m0, s48, 77824
ds_read_b64_tr_b16 a[72:73], v9 offset:47104
v_mfma_f32_16x16x32_bf16 v[86:89], a[48:51], v[106:109], v[86:89]
ds_read_b64_tr_b16 a[74:75], v9 offset:49408
v_mfma_f32_16x16x32_bf16 v[90:93], a[52:55], v[106:109], v[90:93]
global_load_lds_dwordx4 v0, s[40:41] offset:-4096
ds_read_b64_tr_b16 a[76:77], v9 offset:47296
v_mfma_f32_16x16x32_bf16 v[94:97], v[106:109], a[56:59], v[94:97]
ds_read_b64_tr_b16 a[78:79], v9 offset:49600
global_load_lds_dwordx4 v1, s[40:41] offset:-3072
v_mfma_f32_16x16x32_bf16 v[102:105], v[106:109], a[232:235], v[102:105]
ds_read_b64_tr_b16 a[80:81], v9 offset:47488
v_mfma_f32_16x16x32_bf16 v[110:113], a[48:51], v[166:169], v[110:113]
ds_read_b64_tr_b16 a[82:83], v9 offset:49792
global_load_lds_dwordx4 v2, s[40:41] offset:-2048
v_mfma_f32_16x16x32_bf16 v[114:117], a[52:55], v[166:169], v[114:117]
ds_read_b64_tr_b16 a[240:241], v10 offset:47104
v_mfma_f32_16x16x32_bf16 v[118:121], v[166:169], a[56:59], v[118:121]
global_load_lds_dwordx4 v3, s[40:41] offset:-1024
ds_read_b64_tr_b16 a[242:243], v10 offset:49408
v_mfma_f32_16x16x32_bf16 v[134:137], v[166:169], a[232:235], v[134:137]
ds_read_b128 v[182:185], v51 offset:6144
global_load_lds_dwordx4 v4, s[40:41] offset:0
v_mfma_f32_16x16x32_bf16 v[122:125], a[48:51], v[174:177], v[122:125]
ds_read_b128 v[190:193], v51 offset:24576
v_mfma_f32_16x16x32_bf16 v[126:129], a[52:55], v[174:177], v[126:129]
ds_read_b64_tr_b16 a[84:85], v255 offset:0
global_load_lds_dwordx4 v5, s[40:41] offset:1024
v_mfma_f32_16x16x32_bf16 v[130:133], v[174:177], a[56:59], v[130:133]
ds_read_b64_tr_b16 a[86:87], v255 offset:2304
v_mfma_f32_16x16x32_bf16 v[158:161], v[174:177], a[232:235], v[158:161]
global_load_lds_dwordx4 v6, s[40:41] offset:2048
ds_read_b64_tr_b16 a[88:89], v255 offset:192
v_mfma_f32_16x16x32_bf16 v[86:89], a[60:63], v[106:109], v[86:89]
ds_read_b64_tr_b16 a[90:91], v255 offset:2496
v_mfma_f32_16x16x32_bf16 v[90:93], a[64:67], v[106:109], v[90:93]
global_load_lds_dwordx4 v7, s[40:41] offset:3072
ds_read_b64_tr_b16 a[92:93], v255 offset:384
v_mfma_f32_16x16x32_bf16 v[94:97], v[106:109], a[68:71], v[94:97]
s_add_u32 m0, s48, 86016
ds_read_b64_tr_b16 a[94:95], v255 offset:2688
v_mfma_f32_16x16x32_bf16 v[102:105], v[106:109], a[236:239], v[102:105]
ds_read_b64_tr_b16 a[244:245], v163 offset:0
v_mfma_f32_16x16x32_bf16 v[110:113], a[60:63], v[170:173], v[110:113]
global_load_lds_dwordx4 v8, s[40:41] offset:-4096
ds_read_b64_tr_b16 a[246:247], v163 offset:2304
v_mfma_f32_16x16x32_bf16 v[114:117], a[64:67], v[170:173], v[114:117]
ds_read_b128 v[186:189], v52 offset:6144
v_mfma_f32_16x16x32_bf16 v[118:121], v[170:173], a[68:71], v[118:121]
s_add_u32 s40, s40, 221184
ds_read_b128 v[194:197], v52 offset:24576
v_mfma_f32_16x16x32_bf16 v[134:137], v[170:173], a[236:239], v[134:137]
v_mfma_f32_16x16x32_bf16 v[122:125], a[60:63], v[178:181], v[122:125]
s_addc_u32 s41, s41, 0
v_mfma_f32_16x16x32_bf16 v[126:129], a[64:67], v[178:181], v[126:129]
v_mfma_f32_16x16x32_bf16 v[130:133], v[178:181], a[68:71], v[130:133]
v_mfma_f32_16x16x32_bf16 v[158:161], v[178:181], a[236:239], v[158:161]
s_waitcnt vmcnt(9) lgkmcnt(0)
s_barrier
s_add_u32 m0, s48, 4096
ds_read_b64_tr_b16 a[96:97], v255 offset:18432
v_mfma_f32_16x16x32_bf16 v[86:89], a[72:75], v[106:109], v[86:89]
ds_read_b64_tr_b16 a[98:99], v255 offset:20736
v_mfma_f32_16x16x32_bf16 v[90:93], a[76:79], v[106:109], v[90:93]
global_load_lds_dwordx4 v0, s[40:41] offset:-4096
ds_read_b64_tr_b16 a[100:101], v255 offset:18624
v_mfma_f32_16x16x32_bf16 v[94:97], v[106:109], a[80:83], v[94:97]
ds_read_b64_tr_b16 a[102:103], v255 offset:20928
global_load_lds_dwordx4 v1, s[40:41] offset:-3072
v_mfma_f32_16x16x32_bf16 v[102:105], v[106:109], a[240:243], v[102:105]
ds_read_b64_tr_b16 a[104:105], v255 offset:18816
v_mfma_f32_16x16x32_bf16 v[110:113], a[72:75], v[182:185], v[110:113]
ds_read_b64_tr_b16 a[106:107], v255 offset:21120
global_load_lds_dwordx4 v2, s[40:41] offset:-2048
v_mfma_f32_16x16x32_bf16 v[114:117], a[76:79], v[182:185], v[114:117]
ds_read_b64_tr_b16 a[248:249], v163 offset:18432
v_mfma_f32_16x16x32_bf16 v[118:121], v[182:185], a[80:83], v[118:121]
global_load_lds_dwordx4 v3, s[40:41] offset:-1024
ds_read_b64_tr_b16 a[250:251], v163 offset:20736
v_mfma_f32_16x16x32_bf16 v[134:137], v[182:185], a[240:243], v[134:137]
ds_read_b128 v[166:169], v51 offset:8192
global_load_lds_dwordx4 v4, s[40:41] offset:0
v_mfma_f32_16x16x32_bf16 v[122:125], a[72:75], v[190:193], v[122:125]
ds_read_b128 v[174:177], v51 offset:26624
v_mfma_f32_16x16x32_bf16 v[126:129], a[76:79], v[190:193], v[126:129]
ds_read_b64_tr_b16 a[108:109], v255 offset:36864
global_load_lds_dwordx4 v5, s[40:41] offset:1024
v_mfma_f32_16x16x32_bf16 v[130:133], v[190:193], a[80:83], v[130:133]
ds_read_b64_tr_b16 a[110:111], v255 offset:39168
v_mfma_f32_16x16x32_bf16 v[158:161], v[190:193], a[240:243], v[158:161]
global_load_lds_dwordx4 v6, s[40:41] offset:2048
ds_read_b64_tr_b16 a[112:113], v255 offset:37056
v_mfma_f32_16x16x32_bf16 v[86:89], a[84:87], v[106:109], v[86:89]
ds_read_b64_tr_b16 a[114:115], v255 offset:39360
v_mfma_f32_16x16x32_bf16 v[90:93], a[88:91], v[106:109], v[90:93]
global_load_lds_dwordx4 v7, s[40:41] offset:3072
ds_read_b64_tr_b16 a[116:117], v255 offset:37248
v_mfma_f32_16x16x32_bf16 v[94:97], v[106:109], a[92:95], v[94:97]
s_add_u32 m0, s48, 12288
ds_read_b64_tr_b16 a[118:119], v255 offset:39552
v_mfma_f32_16x16x32_bf16 v[102:105], v[106:109], a[244:247], v[102:105]
ds_read_b64_tr_b16 a[252:253], v163 offset:36864
v_mfma_f32_16x16x32_bf16 v[110:113], a[84:87], v[186:189], v[110:113]
global_load_lds_dwordx4 v8, s[40:41] offset:-4096
ds_read_b64_tr_b16 a[254:255], v163 offset:39168
v_mfma_f32_16x16x32_bf16 v[114:117], a[88:91], v[186:189], v[114:117]
ds_read_b128 v[170:173], v52 offset:8192
v_mfma_f32_16x16x32_bf16 v[118:121], v[186:189], a[92:95], v[118:121]
s_add_u32 s40, s40, 221184
ds_read_b128 v[178:181], v52 offset:26624
v_mfma_f32_16x16x32_bf16 v[134:137], v[186:189], a[244:247], v[134:137]
v_mfma_f32_16x16x32_bf16 v[122:125], a[84:87], v[194:197], v[122:125]
s_addc_u32 s41, s41, 0
v_mfma_f32_16x16x32_bf16 v[126:129], a[88:91], v[194:197], v[126:129]
v_mfma_f32_16x16x32_bf16 v[130:133], v[194:197], a[92:95], v[130:133]
v_mfma_f32_16x16x32_bf16 v[158:161], v[194:197], a[244:247], v[158:161]
s_waitcnt vmcnt(9) lgkmcnt(0)
s_barrier
s_add_u32 m0, s48, 40960
ds_read_b64_tr_b16 a[120:121], v255 offset:55296
v_mfma_f32_16x16x32_bf16 v[86:89], a[96:99], v[106:109], v[86:89]
ds_read_b64_tr_b16 a[122:123], v255 offset:57600
v_mfma_f32_16x16x32_bf16 v[90:93], a[100:103], v[106:109], v[90:93]
global_load_lds_dwordx4 v0, s[40:41] offset:-4096
ds_read_b64_tr_b16 a[124:125], v255 offset:55488
v_mfma_f32_16x16x32_bf16 v[94:97], v[106:109], a[104:107], v[94:97]
ds_read_b64_tr_b16 a[126:127], v255 offset:57792
global_load_lds_dwordx4 v1, s[40:41] offset:-3072
v_mfma_f32_16x16x32_bf16 v[102:105], v[106:109], a[248:251], v[102:105]
ds_read_b64_tr_b16 a[128:129], v255 offset:55680
v_mfma_f32_16x16x32_bf16 v[110:113], a[96:99], v[166:169], v[110:113]
ds_read_b64_tr_b16 a[130:131], v255 offset:57984
global_load_lds_dwordx4 v2, s[40:41] offset:-2048
v_mfma_f32_16x16x32_bf16 v[114:117], a[100:103], v[166:169], v[114:117]
ds_read_b64_tr_b16 v[54:55], v163 offset:55296
v_mfma_f32_16x16x32_bf16 v[118:121], v[166:169], a[104:107], v[118:121]
global_load_lds_dwordx4 v3, s[40:41] offset:-1024
ds_read_b64_tr_b16 v[56:57], v163 offset:57600
v_mfma_f32_16x16x32_bf16 v[134:137], v[166:169], a[248:251], v[134:137]
ds_read_b128 v[182:185], v51 offset:10240
global_load_lds_dwordx4 v4, s[40:41] offset:0
v_mfma_f32_16x16x32_bf16 v[122:125], a[96:99], v[174:177], v[122:125]
ds_read_b128 v[190:193], v51 offset:28672
v_mfma_f32_16x16x32_bf16 v[126:129], a[100:103], v[174:177], v[126:129]
ds_read_b64_tr_b16 a[132:133], v162 offset:8192
global_load_lds_dwordx4 v5, s[40:41] offset:1024
v_mfma_f32_16x16x32_bf16 v[130:133], v[174:177], a[104:107], v[130:133]
ds_read_b64_tr_b16 a[134:135], v162 offset:10496
v_mfma_f32_16x16x32_bf16 v[158:161], v[174:177], a[248:251], v[158:161]
global_load_lds_dwordx4 v6, s[40:41] offset:2048
ds_read_b64_tr_b16 a[136:137], v162 offset:8384
v_mfma_f32_16x16x32_bf16 v[86:89], a[108:111], v[106:109], v[86:89]
ds_read_b64_tr_b16 a[138:139], v162 offset:10688
v_mfma_f32_16x16x32_bf16 v[90:93], a[112:115], v[106:109], v[90:93]
global_load_lds_dwordx4 v7, s[40:41] offset:3072
ds_read_b64_tr_b16 a[140:141], v162 offset:8576
v_mfma_f32_16x16x32_bf16 v[94:97], v[106:109], a[116:119], v[94:97]
s_add_u32 m0, s48, 49152
ds_read_b64_tr_b16 a[142:143], v162 offset:10880
v_mfma_f32_16x16x32_bf16 v[102:105], v[106:109], a[252:255], v[102:105]
ds_read_b64_tr_b16 v[58:59], v164 offset:8192
v_mfma_f32_16x16x32_bf16 v[110:113], a[108:111], v[170:173], v[110:113]
global_load_lds_dwordx4 v8, s[40:41] offset:-4096
ds_read_b64_tr_b16 v[60:61], v164 offset:10496
v_mfma_f32_16x16x32_bf16 v[114:117], a[112:115], v[170:173], v[114:117]
ds_read_b128 v[186:189], v52 offset:10240
v_mfma_f32_16x16x32_bf16 v[118:121], v[170:173], a[116:119], v[118:121]
s_add_u32 s40, s40, 221184
ds_read_b128 v[194:197], v52 offset:28672
v_mfma_f32_16x16x32_bf16 v[134:137], v[170:173], a[252:255], v[134:137]
v_mfma_f32_16x16x32_bf16 v[122:125], a[108:111], v[178:181], v[122:125]
s_addc_u32 s41, s41, 0
v_mfma_f32_16x16x32_bf16 v[126:129], a[112:115], v[178:181], v[126:129]
v_mfma_f32_16x16x32_bf16 v[130:133], v[178:181], a[116:119], v[130:133]
v_mfma_f32_16x16x32_bf16 v[158:161], v[178:181], a[252:255], v[158:161]
s_waitcnt vmcnt(9) lgkmcnt(0)
s_barrier
s_add_u32 m0, s48, 77824
ds_read_b64_tr_b16 a[144:145], v9 offset:47104
v_mfma_f32_16x16x32_bf16 v[86:89], a[120:123], v[106:109], v[86:89]
ds_read_b64_tr_b16 a[146:147], v9 offset:49408
v_mfma_f32_16x16x32_bf16 v[90:93], a[124:127], v[106:109], v[90:93]
global_load_lds_dwordx4 v0, s[40:41] offset:-4096
ds_read_b64_tr_b16 a[148:149], v9 offset:47296
v_mfma_f32_16x16x32_bf16 v[94:97], v[106:109], a[128:131], v[94:97]
ds_read_b64_tr_b16 a[150:151], v9 offset:49600
global_load_lds_dwordx4 v1, s[40:41] offset:-3072
v_mfma_f32_16x16x32_bf16 v[102:105], v[106:109], v[54:57], v[102:105]
ds_read_b64_tr_b16 a[152:153], v9 offset:47488
v_mfma_f32_16x16x32_bf16 v[110:113], a[120:123], v[182:185], v[110:113]
ds_read_b64_tr_b16 a[154:155], v9 offset:49792
global_load_lds_dwordx4 v2, s[40:41] offset:-2048
v_mfma_f32_16x16x32_bf16 v[114:117], a[124:127], v[182:185], v[114:117]
ds_read_b64_tr_b16 v[62:63], v10 offset:47104
v_mfma_f32_16x16x32_bf16 v[118:121], v[182:185], a[128:131], v[118:121]
global_load_lds_dwordx4 v3, s[40:41] offset:-1024
ds_read_b64_tr_b16 v[64:65], v10 offset:49408
v_mfma_f32_16x16x32_bf16 v[134:137], v[182:185], v[54:57], v[134:137]
ds_read_b128 v[166:169], v51 offset:12288
global_load_lds_dwordx4 v4, s[40:41] offset:0
v_mfma_f32_16x16x32_bf16 v[122:125], a[120:123], v[190:193], v[122:125]
ds_read_b128 v[174:177], v51 offset:30720
v_mfma_f32_16x16x32_bf16 v[126:129], a[124:127], v[190:193], v[126:129]
)"
R"(ds_read_b64_tr_b16 a[156:157], v255 offset:0
global_load_lds_dwordx4 v5, s[40:41] offset:1024
v_mfma_f32_16x16x32_bf16 v[130:133], v[190:193], a[128:131], v[130:133]
ds_read_b64_tr_b16 a[158:159], v255 offset:2304
v_mfma_f32_16x16x32_bf16 v[158:161], v[190:193], v[54:57], v[158:161]
global_load_lds_dwordx4 v6, s[40:41] offset:2048
ds_read_b64_tr_b16 a[160:161], v255 offset:192
v_mfma_f32_16x16x32_bf16 v[86:89], a[132:135], v[106:109], v[86:89]
ds_read_b64_tr_b16 a[162:163], v255 offset:2496
v_mfma_f32_16x16x32_bf16 v[90:93], a[136:139], v[106:109], v[90:93]
global_load_lds_dwordx4 v7, s[40:41] offset:3072
ds_read_b64_tr_b16 a[164:165], v255 offset:384
v_mfma_f32_16x16x32_bf16 v[94:97], v[106:109], a[140:143], v[94:97]
s_add_u32 m0, s48, 86016
ds_read_b64_tr_b16 a[166:167], v255 offset:2688
v_mfma_f32_16x16x32_bf16 v[102:105], v[106:109], v[58:61], v[102:105]
ds_read_b64_tr_b16 v[66:67], v163 offset:0
v_mfma_f32_16x16x32_bf16 v[110:113], a[132:135], v[186:189], v[110:113]
global_load_lds_dwordx4 v8, s[40:41] offset:-4096
ds_read_b64_tr_b16 v[68:69], v163 offset:2304
v_mfma_f32_16x16x32_bf16 v[114:117], a[136:139], v[186:189], v[114:117]
ds_read_b128 v[170:173], v52 offset:12288
v_mfma_f32_16x16x32_bf16 v[118:121], v[186:189], a[140:143], v[118:121]
s_sub_u32 s40, s40, 1769472
ds_read_b128 v[178:181], v52 offset:30720
v_mfma_f32_16x16x32_bf16 v[134:137], v[186:189], v[58:61], v[134:137]
v_mfma_f32_16x16x32_bf16 v[122:125], a[132:135], v[194:197], v[122:125]
s_subb_u32 s41, s41, 0
v_mfma_f32_16x16x32_bf16 v[126:129], a[136:139], v[194:197], v[126:129]
v_mfma_f32_16x16x32_bf16 v[130:133], v[194:197], a[140:143], v[130:133]
v_mfma_f32_16x16x32_bf16 v[158:161], v[194:197], v[58:61], v[158:161]
s_waitcnt vmcnt(9) lgkmcnt(0)
s_barrier
s_add_u32 m0, s49, 0
ds_read_b64_tr_b16 a[168:169], v255 offset:18432
v_mfma_f32_16x16x32_bf16 v[86:89], a[144:147], v[106:109], v[86:89]
ds_read_b64_tr_b16 a[170:171], v255 offset:20736
v_mfma_f32_16x16x32_bf16 v[90:93], a[148:151], v[106:109], v[90:93]
ds_read_b64_tr_b16 a[172:173], v255 offset:18624
v_mfma_f32_16x16x32_bf16 v[94:97], v[106:109], a[152:155], v[94:97]
ds_read_b64_tr_b16 a[174:175], v255 offset:20928
global_load_lds_dwordx4 v11, s[42:43] offset:-2048 sc1
v_mfma_f32_16x16x32_bf16 v[102:105], v[106:109], v[62:65], v[102:105]
ds_read_b64_tr_b16 a[176:177], v255 offset:18816
v_mfma_f32_16x16x32_bf16 v[110:113], a[144:147], v[166:169], v[110:113]
ds_read_b64_tr_b16 a[178:179], v255 offset:21120
v_mfma_f32_16x16x32_bf16 v[114:117], a[148:151], v[166:169], v[114:117]
ds_read_b64_tr_b16 v[70:71], v163 offset:18432
global_load_lds_dwordx4 v12, s[42:43] offset:-1024 sc1
v_mfma_f32_16x16x32_bf16 v[118:121], v[166:169], a[152:155], v[118:121]
ds_read_b64_tr_b16 v[72:73], v163 offset:20736
v_mfma_f32_16x16x32_bf16 v[134:137], v[166:169], v[62:65], v[134:137]
ds_read_b128 v[182:185], v51 offset:14336
v_mfma_f32_16x16x32_bf16 v[122:125], a[144:147], v[174:177], v[122:125]
ds_read_b128 v[190:193], v51 offset:32768
global_load_lds_dwordx4 v13, s[42:43] offset:0 sc1
v_mfma_f32_16x16x32_bf16 v[126:129], a[148:151], v[174:177], v[126:129]
ds_read_b64_tr_b16 a[180:181], v255 offset:36864
v_mfma_f32_16x16x32_bf16 v[130:133], v[174:177], a[152:155], v[130:133]
ds_read_b64_tr_b16 a[182:183], v255 offset:39168
v_mfma_f32_16x16x32_bf16 v[158:161], v[174:177], v[62:65], v[158:161]
ds_read_b64_tr_b16 a[184:185], v255 offset:37056
v_mfma_f32_16x16x32_bf16 v[86:89], a[156:159], v[106:109], v[86:89]
global_load_lds_dwordx4 v14, s[42:43] offset:1024 sc1
ds_read_b64_tr_b16 a[186:187], v255 offset:39360
v_mfma_f32_16x16x32_bf16 v[90:93], a[160:163], v[106:109], v[90:93]
ds_read_b64_tr_b16 a[188:189], v255 offset:37248
v_mfma_f32_16x16x32_bf16 v[94:97], v[106:109], a[164:167], v[94:97]
ds_read_b64_tr_b16 a[190:191], v255 offset:39552
v_mfma_f32_16x16x32_bf16 v[102:105], v[106:109], v[66:69], v[102:105]
global_load_lds_dwordx4 v15, s[42:43] offset:2048 sc1
ds_read_b64_tr_b16 v[74:75], v163 offset:36864
v_mfma_f32_16x16x32_bf16 v[110:113], a[156:159], v[170:173], v[110:113]
ds_read_b64_tr_b16 v[76:77], v163 offset:39168
v_mfma_f32_16x16x32_bf16 v[114:117], a[160:163], v[170:173], v[114:117]
ds_read_b128 v[186:189], v52 offset:14336
v_mfma_f32_16x16x32_bf16 v[118:121], v[170:173], a[164:167], v[118:121]
s_sub_u32 s42, s42, 18432
s_subb_u32 s43, s43, 0
ds_read_b128 v[194:197], v52 offset:32768
v_mfma_f32_16x16x32_bf16 v[134:137], v[170:173], v[66:69], v[134:137]
v_mfma_f32_16x16x32_bf16 v[122:125], a[156:159], v[178:181], v[122:125]
v_mfma_f32_16x16x32_bf16 v[126:129], a[160:163], v[178:181], v[126:129]
v_mfma_f32_16x16x32_bf16 v[130:133], v[178:181], a[164:167], v[130:133]
v_mfma_f32_16x16x32_bf16 v[158:161], v[178:181], v[66:69], v[158:161]
s_waitcnt vmcnt(5) lgkmcnt(0)
s_barrier
s_add_u32 m0, s49, 18432
ds_read_b64_tr_b16 a[192:193], v255 offset:55296
v_mfma_f32_16x16x32_bf16 v[86:89], a[168:171], v[106:109], v[86:89]
ds_read_b64_tr_b16 a[194:195], v255 offset:57600
v_mfma_f32_16x16x32_bf16 v[90:93], a[172:175], v[106:109], v[90:93]
ds_read_b64_tr_b16 a[196:197], v255 offset:55488
v_mfma_f32_16x16x32_bf16 v[94:97], v[106:109], a[176:179], v[94:97]
ds_read_b64_tr_b16 a[198:199], v255 offset:57792
global_load_lds_dwordx4 v11, s[42:43] offset:-2048 sc1
v_mfma_f32_16x16x32_bf16 v[102:105], v[106:109], v[70:73], v[102:105]
ds_read_b64_tr_b16 a[200:201], v255 offset:55680
v_mfma_f32_16x16x32_bf16 v[110:113], a[168:171], v[182:185], v[110:113]
ds_read_b64_tr_b16 a[202:203], v255 offset:57984
v_mfma_f32_16x16x32_bf16 v[114:117], a[172:175], v[182:185], v[114:117]
ds_read_b64_tr_b16 v[78:79], v163 offset:55296
global_load_lds_dwordx4 v12, s[42:43] offset:-1024 sc1
v_mfma_f32_16x16x32_bf16 v[118:121], v[182:185], a[176:179], v[118:121]
ds_read_b64_tr_b16 v[80:81], v163 offset:57600
v_mfma_f32_16x16x32_bf16 v[134:137], v[182:185], v[70:73], v[134:137]
ds_read_b128 v[166:169], v51 offset:16384
v_mfma_f32_16x16x32_bf16 v[122:125], a[168:171], v[190:193], v[122:125]
ds_read_b128 v[174:177], v51 offset:34816
global_load_lds_dwordx4 v13, s[42:43] offset:0 sc1
v_mfma_f32_16x16x32_bf16 v[126:129], a[172:175], v[190:193], v[126:129]
ds_read_b64_tr_b16 a[204:205], v162 offset:8192
v_mfma_f32_16x16x32_bf16 v[130:133], v[190:193], a[176:179], v[130:133]
ds_read_b64_tr_b16 a[206:207], v162 offset:10496
v_mfma_f32_16x16x32_bf16 v[158:161], v[190:193], v[70:73], v[158:161]
ds_read_b64_tr_b16 a[208:209], v162 offset:8384
global_load_lds_dwordx4 v14, s[42:43] offset:1024 sc1
v_mfma_f32_16x16x32_bf16 v[86:89], a[180:183], v[106:109], v[86:89]
ds_read_b64_tr_b16 a[210:211], v162 offset:10688
v_mfma_f32_16x16x32_bf16 v[90:93], a[184:187], v[106:109], v[90:93]
ds_read_b64_tr_b16 a[212:213], v162 offset:8576
v_mfma_f32_16x16x32_bf16 v[94:97], v[106:109], a[188:191], v[94:97]
ds_read_b64_tr_b16 a[214:215], v162 offset:10880
global_load_lds_dwordx4 v15, s[42:43] offset:2048 sc1
v_mfma_f32_16x16x32_bf16 v[102:105], v[106:109], v[74:77], v[102:105]
ds_read_b64_tr_b16 v[82:83], v164 offset:8192
v_mfma_f32_16x16x32_bf16 v[110:113], a[180:183], v[186:189], v[110:113]
ds_read_b64_tr_b16 v[84:85], v164 offset:10496
v_mfma_f32_16x16x32_bf16 v[114:117], a[184:187], v[186:189], v[114:117]
ds_read_b128 v[170:173], v52 offset:16384
s_sub_u32 s42, s42, 18432
s_subb_u32 s43, s43, 0
v_mfma_f32_16x16x32_bf16 v[118:121], v[186:189], a[188:191], v[118:121]
ds_read_b128 v[178:181], v52 offset:34816
v_mfma_f32_16x16x32_bf16 v[134:137], v[186:189], v[74:77], v[134:137]
v_mfma_f32_16x16x32_bf16 v[122:125], a[180:183], v[194:197], v[122:125]
v_mfma_f32_16x16x32_bf16 v[126:129], a[184:187], v[194:197], v[126:129]
v_mfma_f32_16x16x32_bf16 v[158:161], v[194:197], v[74:77], v[158:161]
s_waitcnt lgkmcnt(15)
v_mfma_f32_16x16x32_bf16 v[86:89], a[192:195], v[106:109], v[86:89]
v_mfma_f32_16x16x32_bf16 v[90:93], a[196:199], v[106:109], v[90:93]
s_waitcnt lgkmcnt(14)
v_mfma_f32_16x16x32_bf16 v[94:97], v[106:109], a[200:203], v[94:97]
s_waitcnt lgkmcnt(12)
v_mfma_f32_16x16x32_bf16 v[102:105], v[106:109], v[78:81], v[102:105]
s_waitcnt lgkmcnt(11)
v_mfma_f32_16x16x32_bf16 v[110:113], a[192:195], v[166:169], v[110:113]
v_mfma_f32_16x16x32_bf16 v[114:117], a[196:199], v[166:169], v[114:117]
v_mfma_f32_16x16x32_bf16 v[118:121], v[166:169], a[200:203], v[118:121]
v_mfma_f32_16x16x32_bf16 v[134:137], v[166:169], v[78:81], v[134:137]
s_waitcnt lgkmcnt(10)
v_mfma_f32_16x16x32_bf16 v[158:161], v[174:177], v[78:81], v[158:161]
s_waitcnt lgkmcnt(8)
v_mfma_f32_16x16x32_bf16 v[86:89], a[204:207], v[106:109], v[86:89]
s_waitcnt lgkmcnt(6)
v_mfma_f32_16x16x32_bf16 v[90:93], a[208:211], v[106:109], v[90:93]
s_waitcnt lgkmcnt(4)
v_mfma_f32_16x16x32_bf16 v[94:97], v[106:109], a[212:215], v[94:97]
s_waitcnt lgkmcnt(2)
v_mfma_f32_16x16x32_bf16 v[102:105], v[106:109], v[82:85], v[102:105]
s_waitcnt lgkmcnt(1)
v_mfma_f32_16x16x32_bf16 v[110:113], a[204:207], v[170:173], v[110:113]
v_mfma_f32_16x16x32_bf16 v[114:117], a[208:211], v[170:173], v[114:117]
v_mfma_f32_16x16x32_bf16 v[118:121], v[170:173], a[212:215], v[118:121]
v_mfma_f32_16x16x32_bf16 v[134:137], v[170:173], v[82:85], v[134:137]
s_waitcnt lgkmcnt(0)
v_mfma_f32_16x16x32_bf16 v[158:161], v[178:181], v[82:85], v[158:161]
s_barrier
ds_read_b128 v[242:245], v25 offset:0
s_waitcnt lgkmcnt(0)
s_nop 2
v_fma_f32 v230, v102, v242, v134
v_fma_f32 v231, v102, v243, v135
v_fma_f32 v232, v102, v244, v136
v_fma_f32 v233, v102, v245, v137
v_cvt_pk_bf16_f32 v230, v230, v231
v_cvt_pk_bf16_f32 v231, v232, v233
ds_write_b64 v22, v[230:231] offset:41472
ds_read_b128 v[202:205], v52 offset:32768
ds_read_b128 v[206:209], v51 offset:34816
ds_read_b128 v[210:213], v52 offset:34816
v_mov_b32 v234, v32
v_mov_b32 v235, v33
v_mov_b32 v236, v34
v_mov_b32 v237, v35
v_mov_b32 v134, v158
v_mov_b32 v135, v159
v_mov_b32 v136, v160
v_mov_b32 v137, v161
s_waitcnt lgkmcnt(3)
s_barrier
ds_read_b32 v23, v24 offset:0
ds_read_b32 v148, v21
ds_read_b32 v26, v24 offset:192
ds_read_b32 v53, v27
ds_read_b32 v95, v28
ds_read_b128 v[226:229], v25 offset:0
ds_read_b64 v[158:159], v22 offset:41472
ds_read_b32 v140, v20
v_xor_b32 v51, 0x10000, v51
v_xor_b32 v52, 0x10000, v52
s_waitcnt lgkmcnt(7)
v_fma_f32 v238, v90, v23, v114
v_fma_f32 v239, v91, v23, v115
v_fma_f32 v240, v92, v23, v116
v_fma_f32 v241, v93, v23, v117
v_cvt_pk_bf16_f32 v146, v238, v239
v_cvt_pk_bf16_f32 v147, v240, v241
s_waitcnt lgkmcnt(0)
s_barrier
ds_read_b128 v[242:245], v25 offset:64
v_fma_f32 v238, v94, v226, v118
v_fma_f32 v239, v94, v227, v119
v_fma_f32 v240, v94, v228, v120
v_fma_f32 v241, v94, v229, v121
v_cvt_pk_bf16_f32 v154, v238, v239
v_cvt_pk_bf16_f32 v155, v240, v241
v_fma_f32 v238, v86, v23, v110
v_fma_f32 v239, v87, v23, v111
v_fma_f32 v240, v88, v23, v112
v_fma_f32 v241, v89, v23, v113
v_cvt_pk_bf16_f32 v138, v238, v239
v_cvt_pk_bf16_f32 v139, v240, v241
s_waitcnt lgkmcnt(0)
v_fma_f32 v230, v102, v242, v134
v_fma_f32 v231, v102, v243, v135
v_fma_f32 v232, v102, v244, v136
v_fma_f32 v233, v102, v245, v137
v_cvt_pk_bf16_f32 v230, v230, v231
v_cvt_pk_bf16_f32 v231, v232, v233
ds_write_b64 v22, v[230:231] offset:41472
s_waitcnt vmcnt(5) lgkmcnt(0)
s_barrier
s_mov_b32 s70, 32
s_waitcnt lgkmcnt(0)
s_nop 1
s_branch .Lqa_in28
.p2align 6
.Lqa_top49:
s_waitcnt lgkmcnt(9)
v_mfma_f32_16x16x32_bf16 v[134:137], v[44:47], a[216:219], 0
s_waitcnt lgkmcnt(8)
ds_read_b128 v[162:165], v52 offset:4096
ds_read_b32 v95, v30
v_mfma_f32_16x16x32_bf16 v[134:137], v[96:99], a[220:223], v[134:137]
ds_read_b128 v[226:229], v25 offset:64
ds_read_b64 v[160:161], v22 offset:41472
v_mfma_f32_16x16x32_bf16 v[110:113], a[0:3], v[44:47], 0
s_waitcnt lgkmcnt(8)
ds_read_b128 v[166:169], v51 offset:6144
ds_read_b32 v144, v20
v_mfma_f32_16x16x32_bf16 v[134:137], v[104:107], a[224:227], v[134:137]
s_waitcnt lgkmcnt(8)
ds_read_b128 v[170:173], v52 offset:6144
v_fma_f32 v238, v90, v23, v126
v_mfma_f32_16x16x32_bf16 v[134:137], v[0:3], a[228:231], v[134:137]
global_store_dwordx4 v16, v[230:233], s[38:39] sc1
v_mfma_f32_16x16x32_bf16 v[114:117], a[4:7], v[44:47], 0
s_waitcnt lgkmcnt(7)
ds_read_b128 v[174:177], v51 offset:8192
v_add_u32 v16, 18432, v16
v_mfma_f32_16x16x32_bf16 v[134:137], v[4:7], a[232:235], v[134:137]
s_waitcnt lgkmcnt(7)
ds_read_b128 v[178:181], v52 offset:8192
v_fma_f32 v239, v91, v23, v127
v_mfma_f32_16x16x32_bf16 v[134:137], v[162:165], a[236:239], v[134:137]
v_fma_f32 v240, v92, v23, v128
v_fma_f32 v241, v93, v23, v129
v_mfma_f32_16x16x32_bf16 v[118:121], v[44:47], a[8:11], 0
s_waitcnt lgkmcnt(4)
ds_read_b128 v[182:185], v51 offset:10240
v_cvt_pk_bf16_f32 v150, v238, v239
v_mfma_f32_16x16x32_bf16 v[134:137], v[166:169], a[240:243], v[134:137]
s_waitcnt lgkmcnt(3)
ds_read_b128 v[186:189], v52 offset:10240
v_cvt_pk_bf16_f32 v151, v240, v241
v_mfma_f32_16x16x32_bf16 v[134:137], v[170:173], a[244:247], v[134:137]
s_nop 0
v_mfma_f32_16x16x32_bf16 v[214:217], v[150:153], v[138:141], v[32:35]
v_max_f32 v109, v218, v219
v_max_f32 v254, v220, v221
v_mfma_f32_16x16x32_bf16 v[110:113], a[12:15], v[96:99], v[110:113]
s_waitcnt lgkmcnt(3)
ds_read_b128 v[190:193], v51 offset:12288
v_fma_f32 v238, v94, v226, v130
v_mfma_f32_16x16x32_bf16 v[134:137], v[174:177], a[248:251], v[134:137]
s_xor_b32 s49, s49, 0x10000
ds_read_b128 v[194:197], v52 offset:12288
s_waitcnt lgkmcnt(4)
v_mfma_f32_16x16x32_bf16 v[134:137], v[178:181], a[252:255], v[134:137]
s_add_u32 m0, s49, 0
v_mul_f32 v26, 0x3eae1a93, v26
v_mul_f32 v103, v53, v26
v_mfma_f32_16x16x32_bf16 v[114:117], a[16:19], v[96:99], v[114:117]
s_waitcnt lgkmcnt(3)
ds_read_b128 v[198:201], v51 offset:14336
v_mul_f32 v108, v95, v26
v_mfma_f32_16x16x32_bf16 v[134:137], v[182:185], v[54:57], v[134:137]
s_waitcnt lgkmcnt(3)
ds_read_b128 v[202:205], v52 offset:14336
v_max3_f32 v109, v214, v215, v109
v_mfma_f32_16x16x32_bf16 v[134:137], v[186:189], v[58:61], v[134:137]
global_load_lds_dwordx4 v11, s[42:43] offset:-2048 sc1
v_max3_f32 v254, v216, v217, v254
v_mfma_f32_16x16x32_bf16 v[118:121], v[96:99], a[20:23], v[118:121]
s_waitcnt lgkmcnt(3)
ds_read_b128 v[206:209], v51 offset:16384
v_mul_f32 v109, v109, v103
v_mfma_f32_16x16x32_bf16 v[134:137], v[190:193], v[62:65], v[134:137]
s_waitcnt lgkmcnt(3)
ds_read_b128 v[210:213], v52 offset:16384
v_mul_f32 v254, v254, v108
v_mfma_f32_16x16x32_bf16 v[134:137], v[194:197], v[66:69], v[134:137]
v_max_f32 v255, v109, v254
v_mov_b32 v8, v255
v_mfma_f32_16x16x32_bf16 v[110:113], a[24:27], v[104:107], v[110:113]
s_waitcnt lgkmcnt(3)
v_permlane16_swap_b32 v255, v8
v_mfma_f32_16x16x32_bf16 v[134:137], v[198:201], v[70:73], v[134:137]
s_waitcnt lgkmcnt(2)
v_max_f32 v255, v255, v8
v_mov_b32 v8, v255
v_mfma_f32_16x16x32_bf16 v[134:137], v[202:205], v[74:77], v[134:137]
s_nop 0
v_permlane32_swap_b32 v255, v8
v_mfma_f32_16x16x32_bf16 v[114:117], a[28:31], v[104:107], v[114:117]
s_waitcnt lgkmcnt(1)
v_max_f32 v255, v255, v8
v_sub_f32 v109, v109, v255
v_mfma_f32_16x16x32_bf16 v[134:137], v[206:209], v[78:81], v[134:137]
s_waitcnt lgkmcnt(0)
global_load_lds_dwordx4 v12, s[42:43] offset:-1024 sc1
v_sub_f32 v254, v254, v255
v_mfma_f32_16x16x32_bf16 v[134:137], v[210:213], v[82:85], v[134:137]
v_exp_f32 v9, v109
v_mfma_f32_16x16x32_bf16 v[118:121], v[104:107], a[32:35], v[118:121]
v_exp_f32 v10, v254
v_mfma_f32_16x16x32_bf16 v[110:113], a[36:39], v[0:3], v[110:113]
v_add_f32 v255, v9, v10
v_mov_b32 v8, v255
v_mfma_f32_16x16x32_bf16 v[114:117], a[40:43], v[0:3], v[114:117]
v_mul_f32 v9, v9, v53
v_fma_f32 v239, v94, v227, v131
v_mfma_f32_16x16x32_bf16 v[118:121], v[0:3], a[44:47], v[118:121]
v_permlane16_swap_b32 v255, v8
v_mfma_f32_16x16x32_bf16 v[110:113], a[48:51], v[4:7], v[110:113]
v_add_f32 v255, v255, v8
v_mov_b32 v8, v255
v_mfma_f32_16x16x32_bf16 v[114:117], a[52:55], v[4:7], v[114:117]
v_mul_f32 v10, v10, v95
v_fma_f32 v240, v94, v228, v132
v_mfma_f32_16x16x32_bf16 v[118:121], v[4:7], a[56:59], v[118:121]
global_load_lds_dwordx4 v13, s[42:43] offset:0 sc1
v_fma_f32 v241, v94, v229, v133
v_mfma_f32_16x16x32_bf16 v[110:113], a[60:63], v[162:165], v[110:113]
v_permlane32_swap_b32 v255, v8
v_mfma_f32_16x16x32_bf16 v[114:117], a[64:67], v[162:165], v[114:117]
v_add_f32 v255, v255, v8
v_cvt_pk_bf16_f32 v156, v238, v239
v_mfma_f32_16x16x32_bf16 v[118:121], v[162:165], a[68:71], v[118:121]
v_mfma_f32_16x16x32_bf16 v[110:113], a[72:75], v[166:169], v[110:113]
s_barrier
v_mfma_f32_16x16x32_bf16 v[114:117], a[76:79], v[166:169], v[114:117]
ds_read_b128 v[242:245], v25 offset:0
v_cvt_pk_bf16_f32 v157, v240, v241
v_mfma_f32_16x16x32_bf16 v[118:121], v[166:169], a[80:83], v[118:121]
v_rcp_f32 v255, v255
v_mfma_f32_16x16x32_bf16 v[110:113], a[84:87], v[170:173], v[110:113]
v_mul_f32 v9, v9, v255
v_mul_f32 v10, v10, v255
v_mfma_f32_16x16x32_bf16 v[114:117], a[88:91], v[170:173], v[114:117]
global_load_lds_dwordx4 v14, s[42:43] offset:1024 sc1
v_cvt_pk_bf16_f32 v9, v9, v9
v_mfma_f32_16x16x32_bf16 v[118:121], v[170:173], a[92:95], v[118:121]
v_cvt_pk_bf16_f32 v10, v10, v10
v_and_b32 v224, v9, v40
v_mfma_f32_16x16x32_bf16 v[110:113], a[96:99], v[174:177], v[110:113]
v_and_b32 v225, v10, v41
v_and_b32 v222, v9, v42
v_mfma_f32_16x16x32_bf16 v[114:117], a[100:103], v[174:177], v[114:117]
s_waitcnt lgkmcnt(0)
v_and_b32 v223, v10, v43
v_fma_f32 v230, v102, v242, v134
v_mfma_f32_16x16x32_bf16 v[118:121], v[174:177], a[104:107], v[118:121]
v_mfma_f32_16x16x32_bf16 v[218:221], v[154:157], v[222:225], 0
v_mfma_f32_16x16x32_bf16 v[214:217], v[158:161], v[222:225], 0
v_fma_f32 v231, v102, v243, v135
v_fma_f32 v232, v102, v244, v136
v_mfma_f32_16x16x32_bf16 v[110:113], a[108:111], v[178:181], v[110:113]
v_fma_f32 v233, v102, v245, v137
v_cvt_pk_bf16_f32 v230, v230, v231
v_mfma_f32_16x16x32_bf16 v[114:117], a[112:115], v[178:181], v[114:117]
v_cvt_pk_bf16_f32 v231, v232, v233
v_fma_f32 v238, v86, v23, v122
v_mfma_f32_16x16x32_bf16 v[118:121], v[178:181], a[116:119], v[118:121]
ds_write_b64 v22, v[230:231] offset:41472
v_mfma_f32_16x16x32_bf16 v[110:113], a[120:123], v[182:185], v[110:113]
global_load_lds_dwordx4 v15, s[42:43] offset:2048 sc1
s_add_u32 s42, s42, s46
s_addc_u32 s43, s43, s47
)"
R"(v_mfma_f32_16x16x32_bf16 v[114:117], a[124:127], v[182:185], v[114:117]
v_cvt_pk_bf16_f32 v100, v218, v219
v_cvt_pk_bf16_f32 v101, v220, v221
v_mfma_f32_16x16x32_bf16 v[118:121], v[182:185], a[128:131], v[118:121]
ds_write_b64 v18, v[100:101]
v_mfma_f32_16x16x32_bf16 v[110:113], a[132:135], v[186:189], v[110:113]
v_cvt_pk_bf16_f32 v9, v216, v217
v_fma_f32 v239, v87, v23, v123
v_mfma_f32_16x16x32_bf16 v[114:117], a[136:139], v[186:189], v[114:117]
ds_write_b32 v19, v9
v_mfma_f32_16x16x32_bf16 v[118:121], v[186:189], a[140:143], v[118:121]
v_fma_f32 v240, v88, v23, v124
v_fma_f32 v241, v89, v23, v125
v_mfma_f32_16x16x32_bf16 v[110:113], a[144:147], v[190:193], v[110:113]
v_cvt_pk_bf16_f32 v142, v238, v239
v_cvt_pk_bf16_f32 v143, v240, v241
v_mfma_f32_16x16x32_bf16 v[114:117], a[148:151], v[190:193], v[114:117]
v_mfma_f32_16x16x32_bf16 v[118:121], v[190:193], a[152:155], v[118:121]
v_mfma_f32_16x16x32_bf16 v[110:113], a[156:159], v[194:197], v[110:113]
v_mfma_f32_16x16x32_bf16 v[114:117], a[160:163], v[194:197], v[114:117]
v_mfma_f32_16x16x32_bf16 v[118:121], v[194:197], a[164:167], v[118:121]
v_mfma_f32_16x16x32_bf16 v[110:113], a[168:171], v[198:201], v[110:113]
v_mfma_f32_16x16x32_bf16 v[114:117], a[172:175], v[198:201], v[114:117]
v_mfma_f32_16x16x32_bf16 v[118:121], v[198:201], a[176:179], v[118:121]
v_mfma_f32_16x16x32_bf16 v[110:113], a[180:183], v[202:205], v[110:113]
v_mfma_f32_16x16x32_bf16 v[114:117], a[184:187], v[202:205], v[114:117]
s_waitcnt vmcnt(5) lgkmcnt(0)
s_barrier
v_mfma_f32_16x16x32_bf16 v[118:121], v[202:205], a[188:191], v[118:121]
ds_read_b128 v[44:47], v51 offset:18432
v_mfma_f32_16x16x32_bf16 v[110:113], a[192:195], v[206:209], v[110:113]
ds_read_b128 v[96:99], v52 offset:18432
ds_read_b128 v[230:233], v17 offset:0
v_mfma_f32_16x16x32_bf16 v[114:117], a[196:199], v[206:209], v[114:117]
ds_read_b32 v23, v24 offset:0
ds_read_b32 v148, v21
v_mfma_f32_16x16x32_bf16 v[218:221], v[150:153], v[142:145], v[36:39]
v_mfma_f32_16x16x32_bf16 v[118:121], v[206:209], a[200:203], v[118:121]
ds_read_b128 v[104:107], v51 offset:20480
ds_read_b32 v26, v24 offset:192
v_mfma_f32_16x16x32_bf16 v[110:113], a[204:207], v[210:213], v[110:113]
ds_read_b128 v[0:3], v52 offset:20480
ds_read_b32 v53, v27
v_mfma_f32_16x16x32_bf16 v[114:117], a[208:211], v[210:213], v[114:117]
ds_read_b32 v95, v28
ds_read_b128 v[226:229], v25 offset:0
v_mfma_f32_16x16x32_bf16 v[118:121], v[210:213], a[212:215], v[118:121]
s_waitcnt lgkmcnt(10)
ds_read_b128 v[4:7], v51 offset:22528
ds_read_b64 v[158:159], v22 offset:41472
v_mfma_f32_16x16x32_bf16 v[134:137], v[44:47], a[216:219], 0
s_waitcnt lgkmcnt(11)
ds_read_b128 v[162:165], v52 offset:22528
ds_read_b32 v140, v20
v_mfma_f32_16x16x32_bf16 v[134:137], v[96:99], a[220:223], v[134:137]
v_max_f32 v109, v218, v219
v_max_f32 v254, v220, v221
v_mfma_f32_16x16x32_bf16 v[122:125], a[0:3], v[44:47], 0
s_waitcnt lgkmcnt(11)
ds_read_b128 v[166:169], v51 offset:24576
v_fma_f32 v238, v90, v23, v114
s_waitcnt lgkmcnt(10)
v_mfma_f32_16x16x32_bf16 v[134:137], v[104:107], a[224:227], v[134:137]
s_waitcnt lgkmcnt(8)
ds_read_b128 v[170:173], v52 offset:24576
v_fma_f32 v239, v91, v23, v115
v_mfma_f32_16x16x32_bf16 v[134:137], v[0:3], a[228:231], v[134:137]
global_store_dwordx4 v16, v[230:233], s[38:39] sc1
v_mfma_f32_16x16x32_bf16 v[126:129], a[4:7], v[44:47], 0
s_waitcnt lgkmcnt(5)
ds_read_b128 v[174:177], v51 offset:26624
v_add_u32 v16, 18432, v16
v_mfma_f32_16x16x32_bf16 v[134:137], v[4:7], a[232:235], v[134:137]
s_waitcnt lgkmcnt(4)
ds_read_b128 v[178:181], v52 offset:26624
v_fma_f32 v240, v92, v23, v116
v_mfma_f32_16x16x32_bf16 v[134:137], v[162:165], a[236:239], v[134:137]
v_fma_f32 v241, v93, v23, v117
v_cvt_pk_bf16_f32 v146, v238, v239
v_mfma_f32_16x16x32_bf16 v[130:133], v[44:47], a[8:11], 0
s_waitcnt lgkmcnt(3)
ds_read_b128 v[182:185], v51 offset:28672
v_cvt_pk_bf16_f32 v147, v240, v241
v_mfma_f32_16x16x32_bf16 v[134:137], v[166:169], a[240:243], v[134:137]
ds_read_b128 v[186:189], v52 offset:28672
v_mfma_f32_16x16x32_bf16 v[214:217], v[146:149], v[142:145], v[234:237]
s_waitcnt lgkmcnt(4)
v_mul_f32 v26, 0x3eae1a93, v26
v_mul_f32 v103, v53, v26
v_mfma_f32_16x16x32_bf16 v[134:137], v[170:173], a[244:247], v[134:137]
v_mul_f32 v108, v95, v26
v_fma_f32 v238, v94, v226, v118
v_mfma_f32_16x16x32_bf16 v[122:125], a[12:15], v[96:99], v[122:125]
s_waitcnt lgkmcnt(3)
ds_read_b128 v[190:193], v51 offset:30720
v_fma_f32 v239, v94, v227, v119
v_mfma_f32_16x16x32_bf16 v[134:137], v[174:177], a[248:251], v[134:137]
s_add_u32 m0, s49, 18432
ds_read_b128 v[194:197], v52 offset:30720
s_waitcnt lgkmcnt(4)
v_mfma_f32_16x16x32_bf16 v[134:137], v[178:181], a[252:255], v[134:137]
v_max3_f32 v109, v214, v215, v109
v_max3_f32 v254, v216, v217, v254
v_mfma_f32_16x16x32_bf16 v[126:129], a[16:19], v[96:99], v[126:129]
s_waitcnt lgkmcnt(3)
ds_read_b128 v[198:201], v51 offset:32768
v_mul_f32 v109, v109, v103
v_mfma_f32_16x16x32_bf16 v[134:137], v[182:185], v[54:57], v[134:137]
s_waitcnt lgkmcnt(3)
ds_read_b128 v[202:205], v52 offset:32768
global_load_lds_dwordx4 v11, s[42:43] offset:-2048 sc1
v_mfma_f32_16x16x32_bf16 v[134:137], v[186:189], v[58:61], v[134:137]
v_mul_f32 v254, v254, v108
v_max_f32 v255, v109, v254
v_mfma_f32_16x16x32_bf16 v[130:133], v[96:99], a[20:23], v[130:133]
s_waitcnt lgkmcnt(3)
ds_read_b128 v[206:209], v51 offset:34816
v_mov_b32 v8, v255
v_mfma_f32_16x16x32_bf16 v[134:137], v[190:193], v[62:65], v[134:137]
s_waitcnt lgkmcnt(3)
ds_read_b128 v[210:213], v52 offset:34816
v_fma_f32 v240, v94, v228, v120
v_mfma_f32_16x16x32_bf16 v[134:137], v[194:197], v[66:69], v[134:137]
v_permlane16_swap_b32 v255, v8
v_mfma_f32_16x16x32_bf16 v[122:125], a[24:27], v[104:107], v[122:125]
s_waitcnt lgkmcnt(3)
v_max_f32 v255, v255, v8
v_mov_b32 v8, v255
v_mfma_f32_16x16x32_bf16 v[134:137], v[198:201], v[70:73], v[134:137]
s_waitcnt lgkmcnt(2)
v_permlane32_swap_b32 v255, v8
v_mfma_f32_16x16x32_bf16 v[134:137], v[202:205], v[74:77], v[134:137]
v_max_f32 v255, v255, v8
v_sub_f32 v109, v109, v255
v_mfma_f32_16x16x32_bf16 v[126:129], a[28:31], v[104:107], v[126:129]
s_waitcnt lgkmcnt(1)
global_load_lds_dwordx4 v12, s[42:43] offset:-1024 sc1
v_sub_f32 v254, v254, v255
v_mfma_f32_16x16x32_bf16 v[134:137], v[206:209], v[78:81], v[134:137]
s_waitcnt lgkmcnt(0)
v_exp_f32 v9, v109
v_mfma_f32_16x16x32_bf16 v[134:137], v[210:213], v[82:85], v[134:137]
v_exp_f32 v10, v254
v_mfma_f32_16x16x32_bf16 v[130:133], v[104:107], a[32:35], v[130:133]
v_add_f32 v255, v9, v10
v_mov_b32 v8, v255
v_mfma_f32_16x16x32_bf16 v[122:125], a[36:39], v[0:3], v[122:125]
v_mul_f32 v9, v9, v53
v_fma_f32 v241, v94, v229, v121
v_mfma_f32_16x16x32_bf16 v[126:129], a[40:43], v[0:3], v[126:129]
v_permlane16_swap_b32 v255, v8
v_mfma_f32_16x16x32_bf16 v[130:133], v[0:3], a[44:47], v[130:133]
v_add_f32 v255, v255, v8
v_mov_b32 v8, v255
v_mfma_f32_16x16x32_bf16 v[122:125], a[48:51], v[4:7], v[122:125]
v_mul_f32 v10, v10, v95
v_cvt_pk_bf16_f32 v154, v238, v239
v_mfma_f32_16x16x32_bf16 v[126:129], a[52:55], v[4:7], v[126:129]
global_load_lds_dwordx4 v13, s[42:43] offset:0 sc1
v_cvt_pk_bf16_f32 v155, v240, v241
v_mfma_f32_16x16x32_bf16 v[130:133], v[4:7], a[56:59], v[130:133]
v_permlane32_swap_b32 v255, v8
v_mfma_f32_16x16x32_bf16 v[122:125], a[60:63], v[162:165], v[122:125]
v_add_f32 v255, v255, v8
v_fma_f32 v238, v86, v23, v110
v_mfma_f32_16x16x32_bf16 v[126:129], a[64:67], v[162:165], v[126:129]
v_rcp_f32 v255, v255
v_mfma_f32_16x16x32_bf16 v[130:133], v[162:165], a[68:71], v[130:133]
v_mfma_f32_16x16x32_bf16 v[122:125], a[72:75], v[166:169], v[122:125]
s_barrier
v_mfma_f32_16x16x32_bf16 v[126:129], a[76:79], v[166:169], v[126:129]
ds_read_b128 v[242:245], v25 offset:64
v_mul_f32 v9, v9, v255
v_mfma_f32_16x16x32_bf16 v[130:133], v[166:169], a[80:83], v[130:133]
v_mul_f32 v10, v10, v255
v_cvt_pk_bf16_f32 v9, v9, v9
v_mfma_f32_16x16x32_bf16 v[122:125], a[84:87], v[170:173], v[122:125]
global_load_lds_dwordx4 v14, s[42:43] offset:1024 sc1
v_cvt_pk_bf16_f32 v10, v10, v10
v_mfma_f32_16x16x32_bf16 v[126:129], a[88:91], v[170:173], v[126:129]
v_and_b32 v222, v9, v40
v_and_b32 v223, v10, v41
v_mfma_f32_16x16x32_bf16 v[130:133], v[170:173], a[92:95], v[130:133]
v_and_b32 v224, v9, v42
v_and_b32 v225, v10, v43
v_mfma_f32_16x16x32_bf16 v[122:125], a[96:99], v[174:177], v[122:125]
s_nop 0
v_mfma_f32_16x16x32_bf16 v[218:221], v[154:157], v[222:225], 0
v_mfma_f32_16x16x32_bf16 v[214:217], v[158:161], v[222:225], 0
v_fma_f32 v239, v87, v23, v111
v_fma_f32 v240, v88, v23, v112
v_mfma_f32_16x16x32_bf16 v[126:129], a[100:103], v[174:177], v[126:129]
s_waitcnt lgkmcnt(0)
v_fma_f32 v230, v102, v242, v134
v_fma_f32 v231, v102, v243, v135
v_mfma_f32_16x16x32_bf16 v[130:133], v[174:177], a[104:107], v[130:133]
v_fma_f32 v232, v102, v244, v136
v_fma_f32 v233, v102, v245, v137
v_mfma_f32_16x16x32_bf16 v[122:125], a[108:111], v[178:181], v[122:125]
v_cvt_pk_bf16_f32 v230, v230, v231
v_cvt_pk_bf16_f32 v231, v232, v233
v_mfma_f32_16x16x32_bf16 v[126:129], a[112:115], v[178:181], v[126:129]
v_cvt_pk_bf16_f32 v100, v218, v219
v_cvt_pk_bf16_f32 v101, v220, v221
v_mfma_f32_16x16x32_bf16 v[130:133], v[178:181], a[116:119], v[130:133]
global_load_lds_dwordx4 v15, s[42:43] offset:2048 sc1
s_sub_u32 s42, s42, 18432
s_subb_u32 s43, s43, 0
v_mfma_f32_16x16x32_bf16 v[122:125], a[120:123], v[182:185], v[122:125]
ds_write_b64 v18, v[100:101]
v_mfma_f32_16x16x32_bf16 v[126:129], a[124:127], v[182:185], v[126:129]
v_cvt_pk_bf16_f32 v9, v216, v217
v_fma_f32 v241, v89, v23, v113
v_mfma_f32_16x16x32_bf16 v[130:133], v[182:185], a[128:131], v[130:133]
ds_write_b32 v19, v9
v_mfma_f32_16x16x32_bf16 v[122:125], a[132:135], v[186:189], v[122:125]
ds_write_b64 v22, v[230:231] offset:41472
v_mfma_f32_16x16x32_bf16 v[126:129], a[136:139], v[186:189], v[126:129]
v_cvt_pk_bf16_f32 v138, v238, v239
v_cvt_pk_bf16_f32 v139, v240, v241
v_mfma_f32_16x16x32_bf16 v[130:133], v[186:189], a[140:143], v[130:133]
v_mfma_f32_16x16x32_bf16 v[122:125], a[144:147], v[190:193], v[122:125]
v_mfma_f32_16x16x32_bf16 v[126:129], a[148:151], v[190:193], v[126:129]
v_mfma_f32_16x16x32_bf16 v[130:133], v[190:193], a[152:155], v[130:133]
v_mfma_f32_16x16x32_bf16 v[122:125], a[156:159], v[194:197], v[122:125]
v_mfma_f32_16x16x32_bf16 v[126:129], a[160:163], v[194:197], v[126:129]
v_mfma_f32_16x16x32_bf16 v[130:133], v[194:197], a[164:167], v[130:133]
v_mfma_f32_16x16x32_bf16 v[122:125], a[168:171], v[198:201], v[122:125]
v_mfma_f32_16x16x32_bf16 v[126:129], a[172:175], v[198:201], v[126:129]
v_xor_b32 v51, 0x10000, v51
v_xor_b32 v52, 0x10000, v52
v_mfma_f32_16x16x32_bf16 v[130:133], v[198:201], a[176:179], v[130:133]
v_mfma_f32_16x16x32_bf16 v[122:125], a[180:183], v[202:205], v[122:125]
v_mfma_f32_16x16x32_bf16 v[126:129], a[184:187], v[202:205], v[126:129]
s_waitcnt vmcnt(5) lgkmcnt(0)
s_barrier
.Lqa_in28:
v_mfma_f32_16x16x32_bf16 v[130:133], v[202:205], a[188:191], v[130:133]
s_cmp_eq_u32 s70, s73
s_cbranch_scc1 .Lqa_hk29
.Lqa_hr30:
ds_read_b128 v[44:47], v51 offset:0
v_mfma_f32_16x16x32_bf16 v[122:125], a[192:195], v[206:209], v[122:125]
ds_read_b128 v[96:99], v52 offset:0
ds_read_b128 v[230:233], v17 offset:0
v_mfma_f32_16x16x32_bf16 v[126:129], a[196:199], v[206:209], v[126:129]
ds_read_b32 v23, v24 offset:64
ds_read_b32 v152, v21
v_mfma_f32_16x16x32_bf16 v[218:221], v[146:149], v[138:141], v[36:39]
v_mfma_f32_16x16x32_bf16 v[130:133], v[206:209], a[200:203], v[130:133]
ds_read_b128 v[104:107], v51 offset:2048
ds_read_b32 v26, v24 offset:128
v_mfma_f32_16x16x32_bf16 v[122:125], a[204:207], v[210:213], v[122:125]
ds_read_b128 v[0:3], v52 offset:2048
ds_read_b32 v53, v29
v_mfma_f32_16x16x32_bf16 v[126:129], a[208:211], v[210:213], v[126:129]
v_mfma_f32_16x16x32_bf16 v[130:133], v[210:213], a[212:215], v[130:133]
s_sub_u32 s70, s70, 1
ds_read_b128 v[4:7], v51 offset:4096
s_cbranch_scc0 .Lqa_top49
s_waitcnt lgkmcnt(0)
ds_read_b32 v95, v30
ds_read_b128 v[226:229], v25 offset:64
ds_read_b64 v[160:161], v22 offset:41472
ds_read_b32 v144, v20
v_fma_f32 v238, v90, v23, v126
global_store_dwordx4 v16, v[230:233], s[38:39] sc1
v_add_u32 v16, 18432, v16
v_fma_f32 v239, v91, v23, v127
v_fma_f32 v240, v92, v23, v128
v_fma_f32 v241, v93, v23, v129
v_cvt_pk_bf16_f32 v150, v238, v239
v_cvt_pk_bf16_f32 v151, v240, v241
s_nop 1
v_mfma_f32_16x16x32_bf16 v[214:217], v[150:153], v[138:141], v[32:35]
s_waitcnt lgkmcnt(2)
v_max_f32 v109, v218, v219
v_max_f32 v254, v220, v221
v_fma_f32 v238, v94, v226, v130
v_mul_f32 v26, 0x3eae1a93, v26
v_mul_f32 v103, v53, v26
v_mul_f32 v108, v95, v26
s_nop 0
v_max3_f32 v109, v214, v215, v109
v_max3_f32 v254, v216, v217, v254
v_mul_f32 v109, v109, v103
v_mul_f32 v254, v254, v108
v_max_f32 v255, v109, v254
v_mov_b32 v8, v255
s_nop 1
v_permlane16_swap_b32 v255, v8
v_max_f32 v255, v255, v8
v_mov_b32 v8, v255
s_nop 1
v_permlane32_swap_b32 v255, v8
v_max_f32 v255, v255, v8
v_sub_f32 v109, v109, v255
v_sub_f32 v254, v254, v255
v_exp_f32 v9, v109
v_exp_f32 v10, v254
s_nop 0
v_add_f32 v255, v9, v10
v_mov_b32 v8, v255
v_mul_f32 v9, v9, v53
v_fma_f32 v239, v94, v227, v131
v_permlane16_swap_b32 v255, v8
v_add_f32 v255, v255, v8
v_mov_b32 v8, v255
v_mul_f32 v10, v10, v95
v_fma_f32 v240, v94, v228, v132
v_fma_f32 v241, v94, v229, v133
v_permlane32_swap_b32 v255, v8
v_add_f32 v255, v255, v8
v_cvt_pk_bf16_f32 v156, v238, v239
s_waitcnt lgkmcnt(0)
s_barrier
v_cvt_pk_bf16_f32 v157, v240, v241
v_rcp_f32 v255, v255
s_nop 0
v_mul_f32 v9, v9, v255
v_mul_f32 v10, v10, v255
v_cvt_pk_bf16_f32 v9, v9, v9
v_cvt_pk_bf16_f32 v10, v10, v10
v_and_b32 v224, v9, v40
v_and_b32 v225, v10, v41
v_and_b32 v222, v9, v42
v_and_b32 v223, v10, v43
s_nop 1
v_mfma_f32_16x16x32_bf16 v[218:221], v[154:157], v[222:225], 0
v_mfma_f32_16x16x32_bf16 v[214:217], v[158:161], v[222:225], 0
v_fma_f32 v238, v86, v23, v122
s_nop 5
v_cvt_pk_bf16_f32 v100, v218, v219
v_cvt_pk_bf16_f32 v101, v220, v221
ds_write_b64 v18, v[100:101]
v_cvt_pk_bf16_f32 v9, v216, v217
v_fma_f32 v239, v87, v23, v123
ds_write_b32 v19, v9
v_fma_f32 v240, v88, v23, v124
v_fma_f32 v241, v89, v23, v125
v_cvt_pk_bf16_f32 v142, v238, v239
v_cvt_pk_bf16_f32 v143, v240, v241
s_waitcnt vmcnt(0)
s_waitcnt lgkmcnt(0)
s_barrier
ds_read_b128 v[230:233], v17 offset:0
s_waitcnt lgkmcnt(0)
global_store_dwordx4 v16, v[230:233], s[38:39] sc1
s_waitcnt vmcnt(0)
s_barrier
s_endpgm
.Lqa_w3:
s_getpc_b64 s[90:91]
.Lqa_pc75:
s_add_u32 s90, s90, .Lqa_e58-.Lqa_pc75
s_addc_u32 s91, s91, 0
s_branch .Lqa_k66
.Lqa_bk74:
v_add_u32 v255, 0x10000, v9
v_add_u32 v162, 0x20000, v9
v_add_u32 v163, 0x10000, v10
v_add_u32 v164, 0x20000, v10
v_mov_b32 v106, 0x3F803F80
v_mov_b32 v107, 0x3F803F80
v_mov_b32 v108, 0x3F803F80
v_mov_b32 v109, 0x3F803F80
v_mov_b32 v141, 0
v_mov_b32 v145, 0
v_mov_b32 v149, 0
v_mov_b32 v153, 0
v_mov_b32 v165, 46080
s_waitcnt lgkmcnt(0)
s_mul_i32 s37, s37, 0x9E3779B1
s_add_u32 s37, s37, 0x7F4A7C15
v_add_u32 v46, s37, v46
v_add_u32 v48, s37, v48
v_add_u32 v50, s37, v50
ds_write_b32 v165, v141
s_mov_b64 s[76:77], 0
s_mov_b32 s73, 18
s_add_u32 m0, s48, 4096
s_nop 0
global_load_lds_dwordx4 v0, s[40:41] offset:-4096
global_load_lds_dwordx4 v1, s[40:41] offset:-3072
global_load_lds_dwordx4 v2, s[40:41] offset:-2048
global_load_lds_dwordx4 v3, s[40:41] offset:-1024
global_load_lds_dwordx4 v4, s[40:41] offset:0
global_load_lds_dwordx4 v5, s[40:41] offset:1024
global_load_lds_dwordx4 v6, s[40:41] offset:2048
global_load_lds_dwordx4 v7, s[40:41] offset:3072
s_add_u32 m0, s48, 12288
s_nop 0
global_load_lds_dwordx4 v8, s[40:41] offset:-4096
s_add_u32 s40, s40, 221184
s_addc_u32 s41, s41, 0
s_add_u32 m0, s48, 40960
s_nop 0
global_load_lds_dwordx4 v0, s[40:41] offset:-4096
global_load_lds_dwordx4 v1, s[40:41] offset:-3072
global_load_lds_dwordx4 v2, s[40:41] offset:-2048
)"
R"(global_load_lds_dwordx4 v3, s[40:41] offset:-1024
global_load_lds_dwordx4 v4, s[40:41] offset:0
global_load_lds_dwordx4 v5, s[40:41] offset:1024
global_load_lds_dwordx4 v6, s[40:41] offset:2048
global_load_lds_dwordx4 v7, s[40:41] offset:3072
s_add_u32 m0, s48, 49152
s_nop 0
global_load_lds_dwordx4 v8, s[40:41] offset:-4096
s_add_u32 s40, s40, 221184
s_addc_u32 s41, s41, 0
s_waitcnt vmcnt(9) lgkmcnt(0)
s_barrier
s_add_u32 m0, s48, 77824
ds_read_b64_tr_b16 a[0:1], v9 offset:47104
ds_read_b64_tr_b16 a[2:3], v9 offset:49408
global_load_lds_dwordx4 v0, s[40:41] offset:-4096
ds_read_b64_tr_b16 a[4:5], v9 offset:47296
global_load_lds_dwordx4 v1, s[40:41] offset:-3072
ds_read_b64_tr_b16 a[6:7], v9 offset:49600
global_load_lds_dwordx4 v2, s[40:41] offset:-2048
ds_read_b64_tr_b16 a[8:9], v9 offset:47488
global_load_lds_dwordx4 v3, s[40:41] offset:-1024
ds_read_b64_tr_b16 a[10:11], v9 offset:49792
ds_read_b128 v[166:169], v51 offset:0
global_load_lds_dwordx4 v4, s[40:41] offset:0
ds_read_b128 v[174:177], v51 offset:18432
global_load_lds_dwordx4 v5, s[40:41] offset:1024
ds_read_b64_tr_b16 a[12:13], v255 offset:0
global_load_lds_dwordx4 v6, s[40:41] offset:2048
ds_read_b64_tr_b16 a[14:15], v255 offset:2304
global_load_lds_dwordx4 v7, s[40:41] offset:3072
ds_read_b64_tr_b16 a[16:17], v255 offset:192
ds_read_b64_tr_b16 a[18:19], v255 offset:2496
s_add_u32 m0, s48, 86016
ds_read_b64_tr_b16 a[20:21], v255 offset:384
global_load_lds_dwordx4 v8, s[40:41] offset:-4096
ds_read_b64_tr_b16 a[22:23], v255 offset:2688
s_add_u32 s40, s40, 221184
ds_read_b128 v[170:173], v52 offset:0
s_addc_u32 s41, s41, 0
ds_read_b128 v[178:181], v52 offset:18432
s_waitcnt vmcnt(9) lgkmcnt(0)
s_barrier
s_add_u32 m0, s48, 4096
ds_read_b64_tr_b16 a[24:25], v255 offset:18432
v_mfma_f32_16x16x32_bf16 v[86:89], a[0:3], v[106:109], 0
v_mfma_f32_16x16x32_bf16 v[90:93], a[4:7], v[106:109], 0
global_load_lds_dwordx4 v0, s[40:41] offset:-4096
ds_read_b64_tr_b16 a[26:27], v255 offset:20736
v_mfma_f32_16x16x32_bf16 v[94:97], v[106:109], a[8:11], 0
ds_read_b64_tr_b16 a[28:29], v255 offset:18624
global_load_lds_dwordx4 v1, s[40:41] offset:-3072
v_mfma_f32_16x16x32_bf16 v[110:113], a[0:3], v[166:169], 0
ds_read_b64_tr_b16 a[30:31], v255 offset:20928
v_mfma_f32_16x16x32_bf16 v[114:117], a[4:7], v[166:169], 0
global_load_lds_dwordx4 v2, s[40:41] offset:-2048
ds_read_b64_tr_b16 a[32:33], v255 offset:18816
v_mfma_f32_16x16x32_bf16 v[118:121], v[166:169], a[8:11], 0
ds_read_b64_tr_b16 a[34:35], v255 offset:21120
global_load_lds_dwordx4 v3, s[40:41] offset:-1024
v_mfma_f32_16x16x32_bf16 v[134:137], v[166:169], v[166:169], 0
ds_read_b128 v[182:185], v51 offset:2048
v_mfma_f32_16x16x32_bf16 v[122:125], a[0:3], v[174:177], 0
global_load_lds_dwordx4 v4, s[40:41] offset:0
ds_read_b128 v[190:193], v51 offset:20480
v_mfma_f32_16x16x32_bf16 v[126:129], a[4:7], v[174:177], 0
ds_read_b64_tr_b16 a[36:37], v255 offset:36864
global_load_lds_dwordx4 v5, s[40:41] offset:1024
v_mfma_f32_16x16x32_bf16 v[130:133], v[174:177], a[8:11], 0
v_mfma_f32_16x16x32_bf16 v[158:161], v[174:177], v[174:177], 0
ds_read_b64_tr_b16 a[38:39], v255 offset:39168
global_load_lds_dwordx4 v6, s[40:41] offset:2048
v_mfma_f32_16x16x32_bf16 v[86:89], a[12:15], v[106:109], v[86:89]
ds_read_b64_tr_b16 a[40:41], v255 offset:37056
v_mfma_f32_16x16x32_bf16 v[90:93], a[16:19], v[106:109], v[90:93]
global_load_lds_dwordx4 v7, s[40:41] offset:3072
ds_read_b64_tr_b16 a[42:43], v255 offset:39360
v_mfma_f32_16x16x32_bf16 v[94:97], v[106:109], a[20:23], v[94:97]
s_add_u32 m0, s48, 12288
ds_read_b64_tr_b16 a[44:45], v255 offset:37248
v_mfma_f32_16x16x32_bf16 v[110:113], a[12:15], v[170:173], v[110:113]
ds_read_b64_tr_b16 a[46:47], v255 offset:39552
v_mfma_f32_16x16x32_bf16 v[114:117], a[16:19], v[170:173], v[114:117]
global_load_lds_dwordx4 v8, s[40:41] offset:-4096
ds_read_b128 v[186:189], v52 offset:2048
v_mfma_f32_16x16x32_bf16 v[118:121], v[170:173], a[20:23], v[118:121]
s_add_u32 s40, s40, 221184
ds_read_b128 v[194:197], v52 offset:20480
v_mfma_f32_16x16x32_bf16 v[134:137], v[170:173], v[170:173], v[134:137]
v_mfma_f32_16x16x32_bf16 v[122:125], a[12:15], v[178:181], v[122:125]
v_mfma_f32_16x16x32_bf16 v[126:129], a[16:19], v[178:181], v[126:129]
s_addc_u32 s41, s41, 0
v_mfma_f32_16x16x32_bf16 v[130:133], v[178:181], a[20:23], v[130:133]
v_mfma_f32_16x16x32_bf16 v[158:161], v[178:181], v[178:181], v[158:161]
s_waitcnt vmcnt(9) lgkmcnt(0)
s_barrier
s_add_u32 m0, s48, 40960
ds_read_b64_tr_b16 a[48:49], v255 offset:55296
v_mfma_f32_16x16x32_bf16 v[86:89], a[24:27], v[106:109], v[86:89]
v_mfma_f32_16x16x32_bf16 v[90:93], a[28:31], v[106:109], v[90:93]
global_load_lds_dwordx4 v0, s[40:41] offset:-4096
ds_read_b64_tr_b16 a[50:51], v255 offset:57600
v_mfma_f32_16x16x32_bf16 v[94:97], v[106:109], a[32:35], v[94:97]
ds_read_b64_tr_b16 a[52:53], v255 offset:55488
global_load_lds_dwordx4 v1, s[40:41] offset:-3072
v_mfma_f32_16x16x32_bf16 v[110:113], a[24:27], v[182:185], v[110:113]
ds_read_b64_tr_b16 a[54:55], v255 offset:57792
v_mfma_f32_16x16x32_bf16 v[114:117], a[28:31], v[182:185], v[114:117]
global_load_lds_dwordx4 v2, s[40:41] offset:-2048
ds_read_b64_tr_b16 a[56:57], v255 offset:55680
v_mfma_f32_16x16x32_bf16 v[118:121], v[182:185], a[32:35], v[118:121]
ds_read_b64_tr_b16 a[58:59], v255 offset:57984
global_load_lds_dwordx4 v3, s[40:41] offset:-1024
v_mfma_f32_16x16x32_bf16 v[134:137], v[182:185], v[182:185], v[134:137]
ds_read_b128 v[166:169], v51 offset:4096
v_mfma_f32_16x16x32_bf16 v[122:125], a[24:27], v[190:193], v[122:125]
global_load_lds_dwordx4 v4, s[40:41] offset:0
ds_read_b128 v[174:177], v51 offset:22528
v_mfma_f32_16x16x32_bf16 v[126:129], a[28:31], v[190:193], v[126:129]
ds_read_b64_tr_b16 a[60:61], v162 offset:8192
global_load_lds_dwordx4 v5, s[40:41] offset:1024
v_mfma_f32_16x16x32_bf16 v[130:133], v[190:193], a[32:35], v[130:133]
v_mfma_f32_16x16x32_bf16 v[158:161], v[190:193], v[190:193], v[158:161]
ds_read_b64_tr_b16 a[62:63], v162 offset:10496
global_load_lds_dwordx4 v6, s[40:41] offset:2048
v_mfma_f32_16x16x32_bf16 v[86:89], a[36:39], v[106:109], v[86:89]
ds_read_b64_tr_b16 a[64:65], v162 offset:8384
v_mfma_f32_16x16x32_bf16 v[90:93], a[40:43], v[106:109], v[90:93]
global_load_lds_dwordx4 v7, s[40:41] offset:3072
ds_read_b64_tr_b16 a[66:67], v162 offset:10688
v_mfma_f32_16x16x32_bf16 v[94:97], v[106:109], a[44:47], v[94:97]
s_add_u32 m0, s48, 49152
ds_read_b64_tr_b16 a[68:69], v162 offset:8576
v_mfma_f32_16x16x32_bf16 v[110:113], a[36:39], v[186:189], v[110:113]
ds_read_b64_tr_b16 a[70:71], v162 offset:10880
v_mfma_f32_16x16x32_bf16 v[114:117], a[40:43], v[186:189], v[114:117]
global_load_lds_dwordx4 v8, s[40:41] offset:-4096
ds_read_b128 v[170:173], v52 offset:4096
v_mfma_f32_16x16x32_bf16 v[118:121], v[186:189], a[44:47], v[118:121]
s_add_u32 s40, s40, 221184
ds_read_b128 v[178:181], v52 offset:22528
v_mfma_f32_16x16x32_bf16 v[134:137], v[186:189], v[186:189], v[134:137]
v_mfma_f32_16x16x32_bf16 v[122:125], a[36:39], v[194:197], v[122:125]
v_mfma_f32_16x16x32_bf16 v[126:129], a[40:43], v[194:197], v[126:129]
s_addc_u32 s41, s41, 0
v_mfma_f32_16x16x32_bf16 v[130:133], v[194:197], a[44:47], v[130:133]
v_mfma_f32_16x16x32_bf16 v[158:161], v[194:197], v[194:197], v[158:161]
s_waitcnt vmcnt(9) lgkmcnt(0)
s_barrier
s_add_u32 m0, s48, 77824
ds_read_b64_tr_b16 a[72:73], v9 offset:47104
v_mfma_f32_16x16x32_bf16 v[86:89], a[48:51], v[106:109], v[86:89]
v_mfma_f32_16x16x32_bf16 v[90:93], a[52:55], v[106:109], v[90:93]
global_load_lds_dwordx4 v0, s[40:41] offset:-4096
ds_read_b64_tr_b16 a[74:75], v9 offset:49408
v_mfma_f32_16x16x32_bf16 v[94:97], v[106:109], a[56:59], v[94:97]
ds_read_b64_tr_b16 a[76:77], v9 offset:47296
global_load_lds_dwordx4 v1, s[40:41] offset:-3072
v_mfma_f32_16x16x32_bf16 v[110:113], a[48:51], v[166:169], v[110:113]
ds_read_b64_tr_b16 a[78:79], v9 offset:49600
v_mfma_f32_16x16x32_bf16 v[114:117], a[52:55], v[166:169], v[114:117]
global_load_lds_dwordx4 v2, s[40:41] offset:-2048
ds_read_b64_tr_b16 a[80:81], v9 offset:47488
v_mfma_f32_16x16x32_bf16 v[118:121], v[166:169], a[56:59], v[118:121]
ds_read_b64_tr_b16 a[82:83], v9 offset:49792
global_load_lds_dwordx4 v3, s[40:41] offset:-1024
v_mfma_f32_16x16x32_bf16 v[134:137], v[166:169], v[166:169], v[134:137]
ds_read_b128 v[182:185], v51 offset:6144
v_mfma_f32_16x16x32_bf16 v[122:125], a[48:51], v[174:177], v[122:125]
global_load_lds_dwordx4 v4, s[40:41] offset:0
ds_read_b128 v[190:193], v51 offset:24576
v_mfma_f32_16x16x32_bf16 v[126:129], a[52:55], v[174:177], v[126:129]
ds_read_b64_tr_b16 a[84:85], v255 offset:0
global_load_lds_dwordx4 v5, s[40:41] offset:1024
v_mfma_f32_16x16x32_bf16 v[130:133], v[174:177], a[56:59], v[130:133]
v_mfma_f32_16x16x32_bf16 v[158:161], v[174:177], v[174:177], v[158:161]
ds_read_b64_tr_b16 a[86:87], v255 offset:2304
global_load_lds_dwordx4 v6, s[40:41] offset:2048
v_mfma_f32_16x16x32_bf16 v[86:89], a[60:63], v[106:109], v[86:89]
ds_read_b64_tr_b16 a[88:89], v255 offset:192
v_mfma_f32_16x16x32_bf16 v[90:93], a[64:67], v[106:109], v[90:93]
global_load_lds_dwordx4 v7, s[40:41] offset:3072
ds_read_b64_tr_b16 a[90:91], v255 offset:2496
v_mfma_f32_16x16x32_bf16 v[94:97], v[106:109], a[68:71], v[94:97]
s_add_u32 m0, s48, 86016
ds_read_b64_tr_b16 a[92:93], v255 offset:384
v_mfma_f32_16x16x32_bf16 v[110:113], a[60:63], v[170:173], v[110:113]
ds_read_b64_tr_b16 a[94:95], v255 offset:2688
v_mfma_f32_16x16x32_bf16 v[114:117], a[64:67], v[170:173], v[114:117]
global_load_lds_dwordx4 v8, s[40:41] offset:-4096
ds_read_b128 v[186:189], v52 offset:6144
v_mfma_f32_16x16x32_bf16 v[118:121], v[170:173], a[68:71], v[118:121]
s_add_u32 s40, s40, 221184
ds_read_b128 v[194:197], v52 offset:24576
v_mfma_f32_16x16x32_bf16 v[134:137], v[170:173], v[170:173], v[134:137]
v_mfma_f32_16x16x32_bf16 v[122:125], a[60:63], v[178:181], v[122:125]
v_mfma_f32_16x16x32_bf16 v[126:129], a[64:67], v[178:181], v[126:129]
s_addc_u32 s41, s41, 0
v_mfma_f32_16x16x32_bf16 v[130:133], v[178:181], a[68:71], v[130:133]
v_mfma_f32_16x16x32_bf16 v[158:161], v[178:181], v[178:181], v[158:161]
s_waitcnt vmcnt(9) lgkmcnt(0)
s_barrier
s_add_u32 m0, s48, 4096
ds_read_b64_tr_b16 a[96:97], v255 offset:18432
v_mfma_f32_16x16x32_bf16 v[86:89], a[72:75], v[106:109], v[86:89]
v_mfma_f32_16x16x32_bf16 v[90:93], a[76:79], v[106:109], v[90:93]
global_load_lds_dwordx4 v0, s[40:41] offset:-4096
ds_read_b64_tr_b16 a[98:99], v255 offset:20736
v_mfma_f32_16x16x32_bf16 v[94:97], v[106:109], a[80:83], v[94:97]
ds_read_b64_tr_b16 a[100:101], v255 offset:18624
global_load_lds_dwordx4 v1, s[40:41] offset:-3072
v_mfma_f32_16x16x32_bf16 v[110:113], a[72:75], v[182:185], v[110:113]
ds_read_b64_tr_b16 a[102:103], v255 offset:20928
v_mfma_f32_16x16x32_bf16 v[114:117], a[76:79], v[182:185], v[114:117]
global_load_lds_dwordx4 v2, s[40:41] offset:-2048
ds_read_b64_tr_b16 a[104:105], v255 offset:18816
v_mfma_f32_16x16x32_bf16 v[118:121], v[182:185], a[80:83], v[118:121]
ds_read_b64_tr_b16 a[106:107], v255 offset:21120
global_load_lds_dwordx4 v3, s[40:41] offset:-1024
v_mfma_f32_16x16x32_bf16 v[134:137], v[182:185], v[182:185], v[134:137]
ds_read_b128 v[166:169], v51 offset:8192
v_mfma_f32_16x16x32_bf16 v[122:125], a[72:75], v[190:193], v[122:125]
global_load_lds_dwordx4 v4, s[40:41] offset:0
ds_read_b128 v[174:177], v51 offset:26624
v_mfma_f32_16x16x32_bf16 v[126:129], a[76:79], v[190:193], v[126:129]
ds_read_b64_tr_b16 a[108:109], v255 offset:36864
global_load_lds_dwordx4 v5, s[40:41] offset:1024
v_mfma_f32_16x16x32_bf16 v[130:133], v[190:193], a[80:83], v[130:133]
v_mfma_f32_16x16x32_bf16 v[158:161], v[190:193], v[190:193], v[158:161]
ds_read_b64_tr_b16 a[110:111], v255 offset:39168
global_load_lds_dwordx4 v6, s[40:41] offset:2048
v_mfma_f32_16x16x32_bf16 v[86:89], a[84:87], v[106:109], v[86:89]
ds_read_b64_tr_b16 a[112:113], v255 offset:37056
v_mfma_f32_16x16x32_bf16 v[90:93], a[88:91], v[106:109], v[90:93]
global_load_lds_dwordx4 v7, s[40:41] offset:3072
ds_read_b64_tr_b16 a[114:115], v255 offset:39360
v_mfma_f32_16x16x32_bf16 v[94:97], v[106:109], a[92:95], v[94:97]
s_add_u32 m0, s48, 12288
ds_read_b64_tr_b16 a[116:117], v255 offset:37248
v_mfma_f32_16x16x32_bf16 v[110:113], a[84:87], v[186:189], v[110:113]
ds_read_b64_tr_b16 a[118:119], v255 offset:39552
v_mfma_f32_16x16x32_bf16 v[114:117], a[88:91], v[186:189], v[114:117]
global_load_lds_dwordx4 v8, s[40:41] offset:-4096
ds_read_b128 v[170:173], v52 offset:8192
v_mfma_f32_16x16x32_bf16 v[118:121], v[186:189], a[92:95], v[118:121]
s_add_u32 s40, s40, 221184
ds_read_b128 v[178:181], v52 offset:26624
v_mfma_f32_16x16x32_bf16 v[134:137], v[186:189], v[186:189], v[134:137]
v_mfma_f32_16x16x32_bf16 v[122:125], a[84:87], v[194:197], v[122:125]
v_mfma_f32_16x16x32_bf16 v[126:129], a[88:91], v[194:197], v[126:129]
s_addc_u32 s41, s41, 0
v_mfma_f32_16x16x32_bf16 v[130:133], v[194:197], a[92:95], v[130:133]
v_mfma_f32_16x16x32_bf16 v[158:161], v[194:197], v[194:197], v[158:161]
s_waitcnt vmcnt(9) lgkmcnt(0)
s_barrier
s_add_u32 m0, s48, 40960
ds_read_b64_tr_b16 a[120:121], v255 offset:55296
v_mfma_f32_16x16x32_bf16 v[86:89], a[96:99], v[106:109], v[86:89]
v_mfma_f32_16x16x32_bf16 v[90:93], a[100:103], v[106:109], v[90:93]
global_load_lds_dwordx4 v0, s[40:41] offset:-4096
ds_read_b64_tr_b16 a[122:123], v255 offset:57600
v_mfma_f32_16x16x32_bf16 v[94:97], v[106:109], a[104:107], v[94:97]
ds_read_b64_tr_b16 a[124:125], v255 offset:55488
global_load_lds_dwordx4 v1, s[40:41] offset:-3072
v_mfma_f32_16x16x32_bf16 v[110:113], a[96:99], v[166:169], v[110:113]
ds_read_b64_tr_b16 a[126:127], v255 offset:57792
v_mfma_f32_16x16x32_bf16 v[114:117], a[100:103], v[166:169], v[114:117]
global_load_lds_dwordx4 v2, s[40:41] offset:-2048
ds_read_b64_tr_b16 a[128:129], v255 offset:55680
v_mfma_f32_16x16x32_bf16 v[118:121], v[166:169], a[104:107], v[118:121]
ds_read_b64_tr_b16 a[130:131], v255 offset:57984
global_load_lds_dwordx4 v3, s[40:41] offset:-1024
v_mfma_f32_16x16x32_bf16 v[134:137], v[166:169], v[166:169], v[134:137]
ds_read_b128 v[182:185], v51 offset:10240
v_mfma_f32_16x16x32_bf16 v[122:125], a[96:99], v[174:177], v[122:125]
global_load_lds_dwordx4 v4, s[40:41] offset:0
ds_read_b128 v[190:193], v51 offset:28672
v_mfma_f32_16x16x32_bf16 v[126:129], a[100:103], v[174:177], v[126:129]
ds_read_b64_tr_b16 a[132:133], v162 offset:8192
global_load_lds_dwordx4 v5, s[40:41] offset:1024
v_mfma_f32_16x16x32_bf16 v[130:133], v[174:177], a[104:107], v[130:133]
v_mfma_f32_16x16x32_bf16 v[158:161], v[174:177], v[174:177], v[158:161]
ds_read_b64_tr_b16 a[134:135], v162 offset:10496
global_load_lds_dwordx4 v6, s[40:41] offset:2048
v_mfma_f32_16x16x32_bf16 v[86:89], a[108:111], v[106:109], v[86:89]
ds_read_b64_tr_b16 a[136:137], v162 offset:8384
v_mfma_f32_16x16x32_bf16 v[90:93], a[112:115], v[106:109], v[90:93]
global_load_lds_dwordx4 v7, s[40:41] offset:3072
ds_read_b64_tr_b16 a[138:139], v162 offset:10688
v_mfma_f32_16x16x32_bf16 v[94:97], v[106:109], a[116:119], v[94:97]
s_add_u32 m0, s48, 49152
ds_read_b64_tr_b16 a[140:141], v162 offset:8576
v_mfma_f32_16x16x32_bf16 v[110:113], a[108:111], v[170:173], v[110:113]
ds_read_b64_tr_b16 a[142:143], v162 offset:10880
v_mfma_f32_16x16x32_bf16 v[114:117], a[112:115], v[170:173], v[114:117]
global_load_lds_dwordx4 v8, s[40:41] offset:-4096
ds_read_b128 v[186:189], v52 offset:10240
v_mfma_f32_16x16x32_bf16 v[118:121], v[170:173], a[116:119], v[118:121]
s_add_u32 s40, s40, 221184
ds_read_b128 v[194:197], v52 offset:28672
v_mfma_f32_16x16x32_bf16 v[134:137], v[170:173], v[170:173], v[134:137]
v_mfma_f32_16x16x32_bf16 v[122:125], a[108:111], v[178:181], v[122:125]
v_mfma_f32_16x16x32_bf16 v[126:129], a[112:115], v[178:181], v[126:129]
s_addc_u32 s41, s41, 0
v_mfma_f32_16x16x32_bf16 v[130:133], v[178:181], a[116:119], v[130:133]
v_mfma_f32_16x16x32_bf16 v[158:161], v[178:181], v[178:181], v[158:161]
s_waitcnt vmcnt(9) lgkmcnt(0)
s_barrier
s_add_u32 m0, s48, 77824
ds_read_b64_tr_b16 a[144:145], v9 offset:47104
v_mfma_f32_16x16x32_bf16 v[86:89], a[120:123], v[106:109], v[86:89]
v_mfma_f32_16x16x32_bf16 v[90:93], a[124:127], v[106:109], v[90:93]
global_load_lds_dwordx4 v0, s[40:41] offset:-4096
ds_read_b64_tr_b16 a[146:147], v9 offset:49408
v_mfma_f32_16x16x32_bf16 v[94:97], v[106:109], a[128:131], v[94:97]
ds_read_b64_tr_b16 a[148:149], v9 offset:47296
global_load_lds_dwordx4 v1, s[40:41] offset:-3072
v_mfma_f32_16x16x32_bf16 v[110:113], a[120:123], v[182:185], v[110:113]
ds_read_b64_tr_b16 a[150:151], v9 offset:49600
v_mfma_f32_16x16x32_bf16 v[114:117], a[124:127], v[182:185], v[114:117]
global_load_lds_dwordx4 v2, s[40:41] offset:-2048
ds_read_b64_tr_b16 a[152:153], v9 offset:47488
v_mfma_f32_16x16x32_bf16 v[118:121], v[182:185], a[128:131], v[118:121]
ds_read_b64_tr_b16 a[154:155], v9 offset:49792
global_load_lds_dwordx4 v3, s[40:41] offset:-1024
v_mfma_f32_16x16x32_bf16 v[134:137], v[182:185], v[182:185], v[134:137]
ds_read_b128 v[166:169], v51 offset:12288
v_mfma_f32_16x16x32_bf16 v[122:125], a[120:123], v[190:193], v[122:125]
global_load_lds_dwordx4 v4, s[40:41] offset:0
ds_read_b128 v[174:177], v51 offset:30720
v_mfma_f32_16x16x32_bf16 v[126:129], a[124:127], v[190:193], v[126:129]
ds_read_b64_tr_b16 a[156:157], v255 offset:0
global_load_lds_dwordx4 v5, s[40:41] offset:1024
v_mfma_f32_16x16x32_bf16 v[130:133], v[190:193], a[128:131], v[130:133]
v_mfma_f32_16x16x32_bf16 v[158:161], v[190:193], v[190:193], v[158:161]
ds_read_b64_tr_b16 a[158:159], v255 offset:2304
global_load_lds_dwordx4 v6, s[40:41] offset:2048
v_mfma_f32_16x16x32_bf16 v[86:89], a[132:135], v[106:109], v[86:89]
ds_read_b64_tr_b16 a[160:161], v255 offset:192
v_mfma_f32_16x16x32_bf16 v[90:93], a[136:139], v[106:109], v[90:93]
global_load_lds_dwordx4 v7, s[40:41] offset:3072
ds_read_b64_tr_b16 a[162:163], v255 offset:2496
v_mfma_f32_16x16x32_bf16 v[94:97], v[106:109], a[140:143], v[94:97]
s_add_u32 m0, s48, 86016
ds_read_b64_tr_b16 a[164:165], v255 offset:384
v_mfma_f32_16x16x32_bf16 v[110:113], a[132:135], v[186:189], v[110:113]
ds_read_b64_tr_b16 a[166:167], v255 offset:2688
v_mfma_f32_16x16x32_bf16 v[114:117], a[136:139], v[186:189], v[114:117]
global_load_lds_dwordx4 v8, s[40:41] offset:-4096
ds_read_b128 v[170:173], v52 offset:12288
v_mfma_f32_16x16x32_bf16 v[118:121], v[186:189], a[140:143], v[118:121]
s_sub_u32 s40, s40, 1769472
ds_read_b128 v[178:181], v52 offset:30720
v_mfma_f32_16x16x32_bf16 v[134:137], v[186:189], v[186:189], v[134:137]
v_mfma_f32_16x16x32_bf16 v[122:125], a[132:135], v[194:197], v[122:125]
v_mfma_f32_16x16x32_bf16 v[126:129], a[136:139], v[194:197], v[126:129]
s_subb_u32 s41, s41, 0
v_mfma_f32_16x16x32_bf16 v[130:133], v[194:197], a[140:143], v[130:133]
v_mfma_f32_16x16x32_bf16 v[158:161], v[194:197], v[194:197], v[158:161]
s_waitcnt vmcnt(9) lgkmcnt(0)
s_barrier
s_add_u32 m0, s49, 0
ds_read_b64_tr_b16 a[168:169], v255 offset:18432
v_mfma_f32_16x16x32_bf16 v[86:89], a[144:147], v[106:109], v[86:89]
v_mfma_f32_16x16x32_bf16 v[90:93], a[148:151], v[106:109], v[90:93]
ds_read_b64_tr_b16 a[170:171], v255 offset:20736
v_mfma_f32_16x16x32_bf16 v[94:97], v[106:109], a[152:155], v[94:97]
ds_read_b64_tr_b16 a[172:173], v255 offset:18624
v_mfma_f32_16x16x32_bf16 v[110:113], a[144:147], v[166:169], v[110:113]
global_load_lds_dwordx4 v11, s[42:43] offset:-2048 sc1
ds_read_b64_tr_b16 a[174:175], v255 offset:20928
v_mfma_f32_16x16x32_bf16 v[114:117], a[148:151], v[166:169], v[114:117]
ds_read_b64_tr_b16 a[176:177], v255 offset:18816
v_mfma_f32_16x16x32_bf16 v[118:121], v[166:169], a[152:155], v[118:121]
ds_read_b64_tr_b16 a[178:179], v255 offset:21120
v_mfma_f32_16x16x32_bf16 v[134:137], v[166:169], v[166:169], v[134:137]
global_load_lds_dwordx4 v12, s[42:43] offset:-1024 sc1
ds_read_b128 v[182:185], v51 offset:14336
v_mfma_f32_16x16x32_bf16 v[122:125], a[144:147], v[174:177], v[122:125]
ds_read_b128 v[190:193], v51 offset:32768
v_mfma_f32_16x16x32_bf16 v[126:129], a[148:151], v[174:177], v[126:129]
ds_read_b64_tr_b16 a[180:181], v255 offset:36864
v_mfma_f32_16x16x32_bf16 v[130:133], v[174:177], a[152:155], v[130:133]
global_load_lds_dwordx4 v13, s[42:43] offset:0 sc1
v_mfma_f32_16x16x32_bf16 v[158:161], v[174:177], v[174:177], v[158:161]
ds_read_b64_tr_b16 a[182:183], v255 offset:39168
v_mfma_f32_16x16x32_bf16 v[86:89], a[156:159], v[106:109], v[86:89]
ds_read_b64_tr_b16 a[184:185], v255 offset:37056
v_mfma_f32_16x16x32_bf16 v[90:93], a[160:163], v[106:109], v[90:93]
ds_read_b64_tr_b16 a[186:187], v255 offset:39360
v_mfma_f32_16x16x32_bf16 v[94:97], v[106:109], a[164:167], v[94:97]
global_load_lds_dwordx4 v14, s[42:43] offset:1024 sc1
ds_read_b64_tr_b16 a[188:189], v255 offset:37248
v_mfma_f32_16x16x32_bf16 v[110:113], a[156:159], v[170:173], v[110:113]
ds_read_b64_tr_b16 a[190:191], v255 offset:39552
v_mfma_f32_16x16x32_bf16 v[114:117], a[160:163], v[170:173], v[114:117]
ds_read_b128 v[186:189], v52 offset:14336
v_mfma_f32_16x16x32_bf16 v[118:121], v[170:173], a[164:167], v[118:121]
s_sub_u32 s42, s42, 18432
s_subb_u32 s43, s43, 0
)"
R"(ds_read_b128 v[194:197], v52 offset:32768
v_mfma_f32_16x16x32_bf16 v[134:137], v[170:173], v[170:173], v[134:137]
v_mfma_f32_16x16x32_bf16 v[122:125], a[156:159], v[178:181], v[122:125]
v_mfma_f32_16x16x32_bf16 v[126:129], a[160:163], v[178:181], v[126:129]
v_mfma_f32_16x16x32_bf16 v[130:133], v[178:181], a[164:167], v[130:133]
v_mfma_f32_16x16x32_bf16 v[158:161], v[178:181], v[178:181], v[158:161]
s_waitcnt vmcnt(4) lgkmcnt(0)
s_barrier
s_add_u32 m0, s49, 18432
ds_read_b64_tr_b16 a[192:193], v255 offset:55296
v_mfma_f32_16x16x32_bf16 v[86:89], a[168:171], v[106:109], v[86:89]
v_mfma_f32_16x16x32_bf16 v[90:93], a[172:175], v[106:109], v[90:93]
ds_read_b64_tr_b16 a[194:195], v255 offset:57600
v_mfma_f32_16x16x32_bf16 v[94:97], v[106:109], a[176:179], v[94:97]
ds_read_b64_tr_b16 a[196:197], v255 offset:55488
v_mfma_f32_16x16x32_bf16 v[110:113], a[168:171], v[182:185], v[110:113]
global_load_lds_dwordx4 v11, s[42:43] offset:-2048 sc1
ds_read_b64_tr_b16 a[198:199], v255 offset:57792
v_mfma_f32_16x16x32_bf16 v[114:117], a[172:175], v[182:185], v[114:117]
ds_read_b64_tr_b16 a[200:201], v255 offset:55680
v_mfma_f32_16x16x32_bf16 v[118:121], v[182:185], a[176:179], v[118:121]
ds_read_b64_tr_b16 a[202:203], v255 offset:57984
v_mfma_f32_16x16x32_bf16 v[134:137], v[182:185], v[182:185], v[134:137]
global_load_lds_dwordx4 v12, s[42:43] offset:-1024 sc1
ds_read_b128 v[166:169], v51 offset:16384
v_mfma_f32_16x16x32_bf16 v[122:125], a[168:171], v[190:193], v[122:125]
ds_read_b128 v[174:177], v51 offset:34816
v_mfma_f32_16x16x32_bf16 v[126:129], a[172:175], v[190:193], v[126:129]
ds_read_b64_tr_b16 a[204:205], v162 offset:8192
v_mfma_f32_16x16x32_bf16 v[130:133], v[190:193], a[176:179], v[130:133]
global_load_lds_dwordx4 v13, s[42:43] offset:0 sc1
ds_read_b64_tr_b16 a[206:207], v162 offset:10496
v_mfma_f32_16x16x32_bf16 v[158:161], v[190:193], v[190:193], v[158:161]
ds_read_b64_tr_b16 a[208:209], v162 offset:8384
v_mfma_f32_16x16x32_bf16 v[86:89], a[180:183], v[106:109], v[86:89]
ds_read_b64_tr_b16 a[210:211], v162 offset:10688
v_mfma_f32_16x16x32_bf16 v[90:93], a[184:187], v[106:109], v[90:93]
global_load_lds_dwordx4 v14, s[42:43] offset:1024 sc1
ds_read_b64_tr_b16 a[212:213], v162 offset:8576
v_mfma_f32_16x16x32_bf16 v[94:97], v[106:109], a[188:191], v[94:97]
ds_read_b64_tr_b16 a[214:215], v162 offset:10880
v_mfma_f32_16x16x32_bf16 v[110:113], a[180:183], v[186:189], v[110:113]
ds_read_b128 v[170:173], v52 offset:16384
v_mfma_f32_16x16x32_bf16 v[114:117], a[184:187], v[186:189], v[114:117]
s_sub_u32 s42, s42, 18432
s_subb_u32 s43, s43, 0
ds_read_b128 v[178:181], v52 offset:34816
v_mfma_f32_16x16x32_bf16 v[118:121], v[186:189], a[188:191], v[118:121]
v_mfma_f32_16x16x32_bf16 v[134:137], v[186:189], v[186:189], v[134:137]
v_mfma_f32_16x16x32_bf16 v[122:125], a[180:183], v[194:197], v[122:125]
v_mfma_f32_16x16x32_bf16 v[126:129], a[184:187], v[194:197], v[126:129]
v_mfma_f32_16x16x32_bf16 v[158:161], v[194:197], v[194:197], v[158:161]
s_waitcnt lgkmcnt(14)
v_mfma_f32_16x16x32_bf16 v[86:89], a[192:195], v[106:109], v[86:89]
s_waitcnt lgkmcnt(12)
v_mfma_f32_16x16x32_bf16 v[90:93], a[196:199], v[106:109], v[90:93]
s_waitcnt lgkmcnt(10)
v_mfma_f32_16x16x32_bf16 v[94:97], v[106:109], a[200:203], v[94:97]
s_waitcnt lgkmcnt(9)
v_mfma_f32_16x16x32_bf16 v[110:113], a[192:195], v[166:169], v[110:113]
v_mfma_f32_16x16x32_bf16 v[114:117], a[196:199], v[166:169], v[114:117]
v_mfma_f32_16x16x32_bf16 v[118:121], v[166:169], a[200:203], v[118:121]
v_mfma_f32_16x16x32_bf16 v[134:137], v[166:169], v[166:169], v[134:137]
s_waitcnt lgkmcnt(8)
v_mfma_f32_16x16x32_bf16 v[158:161], v[174:177], v[174:177], v[158:161]
s_waitcnt lgkmcnt(6)
v_mfma_f32_16x16x32_bf16 v[86:89], a[204:207], v[106:109], v[86:89]
s_waitcnt lgkmcnt(4)
v_mfma_f32_16x16x32_bf16 v[90:93], a[208:211], v[106:109], v[90:93]
s_waitcnt lgkmcnt(2)
v_mfma_f32_16x16x32_bf16 v[94:97], v[106:109], a[212:215], v[94:97]
s_waitcnt lgkmcnt(1)
v_mfma_f32_16x16x32_bf16 v[110:113], a[204:207], v[170:173], v[110:113]
v_mfma_f32_16x16x32_bf16 v[114:117], a[208:211], v[170:173], v[114:117]
v_mfma_f32_16x16x32_bf16 v[118:121], v[170:173], a[212:215], v[118:121]
v_mfma_f32_16x16x32_bf16 v[134:137], v[170:173], v[170:173], v[134:137]
s_waitcnt lgkmcnt(0)
v_mfma_f32_16x16x32_bf16 v[158:161], v[178:181], v[178:181], v[158:161]
s_nop 5
ds_write_b128 v23, v[134:137] offset:41984
s_waitcnt lgkmcnt(0)
s_barrier
ds_read_b128 v[170:173], v52 offset:32768
ds_read_b128 v[174:177], v51 offset:34816
ds_read_b128 v[178:181], v52 offset:34816
v_mov_b32 v202, v32
v_mov_b32 v203, v33
v_mov_b32 v204, v34
v_mov_b32 v205, v35
v_mov_b32 v134, v158
v_mov_b32 v135, v159
v_mov_b32 v136, v160
v_mov_b32 v137, v161
s_barrier
ds_read_b32 v26, v24 offset:0
ds_read_b32 v148, v21
ds_read_b32 v95, v24 offset:192
ds_read_b32 v255, v27
ds_read_b32 v8, v28
ds_read_b128 v[194:197], v25 offset:0
ds_read_b64 v[158:159], v22 offset:41472
ds_read_b32 v140, v20
v_xor_b32 v51, 0x10000, v51
v_xor_b32 v52, 0x10000, v52
ds_write_b128 v23, v[134:137] offset:41984
s_waitcnt lgkmcnt(8)
v_fma_f32 v206, v90, v26, v114
v_fma_f32 v207, v91, v26, v115
v_fma_f32 v208, v92, v26, v116
v_fma_f32 v209, v93, v26, v117
v_cvt_pk_bf16_f32 v146, v206, v207
v_cvt_pk_bf16_f32 v147, v208, v209
s_waitcnt lgkmcnt(0)
s_barrier
v_fma_f32 v206, v94, v194, v118
v_fma_f32 v207, v94, v195, v119
v_fma_f32 v208, v94, v196, v120
v_fma_f32 v209, v94, v197, v121
v_cvt_pk_bf16_f32 v154, v206, v207
v_cvt_pk_bf16_f32 v155, v208, v209
v_fma_f32 v206, v86, v26, v110
v_fma_f32 v207, v87, v26, v111
v_fma_f32 v208, v88, v26, v112
v_fma_f32 v209, v89, v26, v113
v_cvt_pk_bf16_f32 v138, v206, v207
v_cvt_pk_bf16_f32 v139, v208, v209
s_waitcnt vmcnt(4) lgkmcnt(0)
s_barrier
s_mov_b32 s70, 32
s_waitcnt lgkmcnt(0)
s_nop 1
s_branch .Lqa_in51
.p2align 6
.Lqa_top76:
s_waitcnt lgkmcnt(9)
v_mfma_f32_16x16x32_bf16 v[134:137], v[0:3], v[0:3], 0
s_waitcnt lgkmcnt(8)
ds_read_b128 v[54:57], v52 offset:4096
ds_read_b32 v8, v30
v_mfma_f32_16x16x32_bf16 v[134:137], v[4:7], v[4:7], v[134:137]
ds_read_b128 v[194:197], v25 offset:64
ds_read_b64 v[160:161], v22 offset:41472
v_mfma_f32_16x16x32_bf16 v[110:113], a[0:3], v[0:3], 0
s_waitcnt lgkmcnt(8)
ds_read_b128 v[58:61], v51 offset:6144
ds_read_b32 v144, v20
v_mfma_f32_16x16x32_bf16 v[134:137], v[96:99], v[96:99], v[134:137]
s_waitcnt lgkmcnt(8)
ds_read_b128 v[62:65], v52 offset:6144
v_add_u32 v46, 1, v46
v_mfma_f32_16x16x32_bf16 v[134:137], v[100:103], v[100:103], v[134:137]
s_mov_b64 exec, 65535
global_store_dwordx4 v16, v[198:201], s[38:39] sc1
s_mov_b64 exec, -1
v_mfma_f32_16x16x32_bf16 v[114:117], a[4:7], v[0:3], 0
s_waitcnt lgkmcnt(7)
ds_read_b128 v[66:69], v51 offset:8192
v_add_u32 v16, 18432, v16
v_mfma_f32_16x16x32_bf16 v[134:137], v[104:107], v[104:107], v[134:137]
s_waitcnt lgkmcnt(7)
ds_read_b128 v[70:73], v52 offset:8192
v_fma_f32 v206, v90, v26, v126
v_mfma_f32_16x16x32_bf16 v[134:137], v[54:57], v[54:57], v[134:137]
v_fma_f32 v207, v91, v26, v127
v_fma_f32 v208, v92, v26, v128
v_mfma_f32_16x16x32_bf16 v[118:121], v[0:3], a[8:11], 0
s_waitcnt lgkmcnt(4)
ds_read_b128 v[74:77], v51 offset:10240
v_fma_f32 v209, v93, v26, v129
v_mfma_f32_16x16x32_bf16 v[134:137], v[58:61], v[58:61], v[134:137]
s_waitcnt lgkmcnt(3)
ds_read_b128 v[78:81], v52 offset:10240
v_cvt_pk_bf16_f32 v150, v206, v207
v_mfma_f32_16x16x32_bf16 v[134:137], v[62:65], v[62:65], v[134:137]
v_cvt_pk_bf16_f32 v151, v208, v209
v_max_f32 v47, v186, v187
v_mfma_f32_16x16x32_bf16 v[110:113], a[12:15], v[4:7], v[110:113]
ds_read_b128 v[82:85], v51 offset:12288
v_mfma_f32_16x16x32_bf16 v[182:185], v[150:153], v[138:141], v[32:35]
s_waitcnt lgkmcnt(4)
v_max_f32 v48, v188, v189
v_fma_f32 v206, v94, v194, v130
v_mfma_f32_16x16x32_bf16 v[134:137], v[66:69], v[66:69], v[134:137]
s_xor_b32 s49, s49, 0x10000
ds_read_b128 v[162:165], v52 offset:12288
s_waitcnt lgkmcnt(4)
v_mfma_f32_16x16x32_bf16 v[134:137], v[70:73], v[70:73], v[134:137]
s_add_u32 m0, s49, 0
v_mul_f32 v95, 0x3eae1a93, v95
v_mul_f32 v9, v255, v95
v_mfma_f32_16x16x32_bf16 v[114:117], a[16:19], v[4:7], v[114:117]
s_waitcnt lgkmcnt(3)
ds_read_b128 v[166:169], v51 offset:14336
v_mul_f32 v10, v8, v95
v_mfma_f32_16x16x32_bf16 v[134:137], v[74:77], v[74:77], v[134:137]
s_waitcnt lgkmcnt(3)
ds_read_b128 v[170:173], v52 offset:14336
v_max3_f32 v47, v182, v183, v47
v_mfma_f32_16x16x32_bf16 v[134:137], v[78:81], v[78:81], v[134:137]
v_max3_f32 v48, v184, v185, v48
v_mul_f32 v47, v47, v9
v_mfma_f32_16x16x32_bf16 v[118:121], v[4:7], a[20:23], v[118:121]
s_waitcnt lgkmcnt(3)
ds_read_b128 v[174:177], v51 offset:16384
v_mul_f32 v48, v48, v10
v_mfma_f32_16x16x32_bf16 v[134:137], v[82:85], v[82:85], v[134:137]
s_waitcnt lgkmcnt(3)
ds_read_b128 v[178:181], v52 offset:16384
global_load_lds_dwordx4 v11, s[42:43] offset:-2048 sc1
v_mfma_f32_16x16x32_bf16 v[134:137], v[162:165], v[162:165], v[134:137]
v_max_f32 v49, v47, v48
v_mov_b32 v50, v49
v_mfma_f32_16x16x32_bf16 v[110:113], a[24:27], v[96:99], v[110:113]
s_waitcnt lgkmcnt(3)
v_permlane16_swap_b32 v49, v50
v_mfma_f32_16x16x32_bf16 v[134:137], v[166:169], v[166:169], v[134:137]
s_waitcnt lgkmcnt(2)
v_max_f32 v49, v49, v50
v_mov_b32 v50, v49
v_mfma_f32_16x16x32_bf16 v[134:137], v[170:173], v[170:173], v[134:137]
s_nop 0
v_permlane32_swap_b32 v49, v50
v_mfma_f32_16x16x32_bf16 v[114:117], a[28:31], v[96:99], v[114:117]
s_waitcnt lgkmcnt(1)
v_max_f32 v49, v49, v50
v_sub_f32 v47, v47, v49
v_mfma_f32_16x16x32_bf16 v[134:137], v[174:177], v[174:177], v[134:137]
s_waitcnt lgkmcnt(0)
v_sub_f32 v48, v48, v49
v_fma_f32 v207, v94, v195, v131
v_mfma_f32_16x16x32_bf16 v[134:137], v[178:181], v[178:181], v[134:137]
v_exp_f32 v210, v47
v_mfma_f32_16x16x32_bf16 v[118:121], v[96:99], a[32:35], v[118:121]
global_load_lds_dwordx4 v12, s[42:43] offset:-1024 sc1
v_fma_f32 v208, v94, v196, v132
v_mfma_f32_16x16x32_bf16 v[110:113], a[36:39], v[100:103], v[110:113]
v_exp_f32 v211, v48
v_mfma_f32_16x16x32_bf16 v[114:117], a[40:43], v[100:103], v[114:117]
v_add_f32 v49, v210, v211
v_mov_b32 v50, v49
v_mfma_f32_16x16x32_bf16 v[118:121], v[100:103], a[44:47], v[118:121]
ds_write_b128 v23, v[134:137] offset:41984
v_mfma_f32_16x16x32_bf16 v[110:113], a[48:51], v[104:107], v[110:113]
v_mul_f32 v210, v210, v255
v_fma_f32 v209, v94, v197, v133
v_mfma_f32_16x16x32_bf16 v[114:117], a[52:55], v[104:107], v[114:117]
v_permlane16_swap_b32 v49, v50
v_mfma_f32_16x16x32_bf16 v[118:121], v[104:107], a[56:59], v[118:121]
v_add_f32 v49, v49, v50
v_mov_b32 v50, v49
v_mfma_f32_16x16x32_bf16 v[110:113], a[60:63], v[54:57], v[110:113]
v_mul_f32 v211, v211, v8
v_cvt_pk_bf16_f32 v156, v206, v207
v_mfma_f32_16x16x32_bf16 v[114:117], a[64:67], v[54:57], v[114:117]
global_load_lds_dwordx4 v13, s[42:43] offset:0 sc1
v_cvt_pk_bf16_f32 v157, v208, v209
v_mfma_f32_16x16x32_bf16 v[118:121], v[54:57], a[68:71], v[118:121]
v_mfma_f32_16x16x32_bf16 v[110:113], a[72:75], v[58:61], v[110:113]
s_waitcnt lgkmcnt(0)
s_barrier
v_mfma_f32_16x16x32_bf16 v[114:117], a[76:79], v[58:61], v[114:117]
v_permlane32_swap_b32 v49, v50
v_mfma_f32_16x16x32_bf16 v[118:121], v[58:61], a[80:83], v[118:121]
v_add_f32 v49, v49, v50
v_fma_f32 v206, v86, v26, v122
v_mfma_f32_16x16x32_bf16 v[110:113], a[84:87], v[62:65], v[110:113]
v_rcp_f32 v49, v49
v_mfma_f32_16x16x32_bf16 v[114:117], a[88:91], v[62:65], v[114:117]
v_mul_f32 v210, v210, v49
v_mul_f32 v211, v211, v49
v_mfma_f32_16x16x32_bf16 v[118:121], v[62:65], a[92:95], v[118:121]
v_cvt_pk_bf16_f32 v210, v210, v210
v_cvt_pk_bf16_f32 v211, v211, v211
v_mfma_f32_16x16x32_bf16 v[110:113], a[96:99], v[66:69], v[110:113]
global_load_lds_dwordx4 v14, s[42:43] offset:1024 sc1
s_add_u32 s42, s42, s46
s_addc_u32 s43, s43, s47
v_mfma_f32_16x16x32_bf16 v[114:117], a[100:103], v[66:69], v[114:117]
v_and_b32 v192, v210, v40
v_and_b32 v193, v211, v41
v_mfma_f32_16x16x32_bf16 v[118:121], v[66:69], a[104:107], v[118:121]
v_and_b32 v190, v210, v42
v_and_b32 v191, v211, v43
v_mfma_f32_16x16x32_bf16 v[110:113], a[108:111], v[70:73], v[110:113]
s_nop 0
v_mfma_f32_16x16x32_bf16 v[186:189], v[154:157], v[190:193], 0
v_mfma_f32_16x16x32_bf16 v[182:185], v[158:161], v[190:193], 0
v_fma_f32 v207, v87, v26, v123
v_fma_f32 v208, v88, v26, v124
v_mfma_f32_16x16x32_bf16 v[114:117], a[112:115], v[70:73], v[114:117]
v_fma_f32 v209, v89, v26, v125
v_cvt_pk_bf16_f32 v142, v206, v207
v_mfma_f32_16x16x32_bf16 v[118:121], v[70:73], a[116:119], v[118:121]
v_cvt_pk_bf16_f32 v143, v208, v209
v_mfma_f32_16x16x32_bf16 v[110:113], a[120:123], v[74:77], v[110:113]
v_mfma_f32_16x16x32_bf16 v[114:117], a[124:127], v[74:77], v[114:117]
v_cvt_pk_bf16_f32 v108, v186, v187
v_cvt_pk_bf16_f32 v109, v188, v189
v_mfma_f32_16x16x32_bf16 v[118:121], v[74:77], a[128:131], v[118:121]
ds_write_b64 v18, v[108:109]
v_mfma_f32_16x16x32_bf16 v[110:113], a[132:135], v[78:81], v[110:113]
v_cvt_pk_bf16_f32 v210, v182, v183
v_mfma_f32_16x16x32_bf16 v[114:117], a[136:139], v[78:81], v[114:117]
ds_write_b32 v19, v210
v_mfma_f32_16x16x32_bf16 v[118:121], v[78:81], a[140:143], v[118:121]
v_mfma_f32_16x16x32_bf16 v[110:113], a[144:147], v[82:85], v[110:113]
v_mfma_f32_16x16x32_bf16 v[114:117], a[148:151], v[82:85], v[114:117]
v_mfma_f32_16x16x32_bf16 v[118:121], v[82:85], a[152:155], v[118:121]
v_mfma_f32_16x16x32_bf16 v[110:113], a[156:159], v[162:165], v[110:113]
v_add_lshl_u32 v253, v46, 1, 7
v_bfi_b32 v253, s69, v253, v44
v_mfma_f32_16x16x32_bf16 v[114:117], a[160:163], v[162:165], v[114:117]
global_load_dword v254, v253, s[38:39] sc1
v_mfma_f32_16x16x32_bf16 v[118:121], v[162:165], a[164:167], v[118:121]
s_waitcnt vmcnt(6)
v_sub_u32 v53, v53, v46
v_cmp_lt_u32 vcc, 0x7f, v53
v_mfma_f32_16x16x32_bf16 v[110:113], a[168:171], v[166:169], v[110:113]
v_mfma_f32_16x16x32_bf16 v[114:117], a[172:175], v[166:169], v[114:117]
v_mfma_f32_16x16x32_bf16 v[118:121], v[166:169], a[176:179], v[118:121]
s_and_b64 vcc, vcc, s[76:77]
s_cbranch_vccnz .Lqa_sp54
.Lqa_rt55:
v_mfma_f32_16x16x32_bf16 v[110:113], a[180:183], v[170:173], v[110:113]
v_mfma_f32_16x16x32_bf16 v[114:117], a[184:187], v[170:173], v[114:117]
s_waitcnt vmcnt(5) lgkmcnt(0)
s_barrier
v_mfma_f32_16x16x32_bf16 v[118:121], v[170:173], a[188:191], v[118:121]
ds_read_b128 v[0:3], v51 offset:18432
v_mfma_f32_16x16x32_bf16 v[110:113], a[192:195], v[174:177], v[110:113]
ds_read_b128 v[4:7], v52 offset:18432
ds_read_b128 v[198:201], v17 offset:0
v_mfma_f32_16x16x32_bf16 v[114:117], a[196:199], v[174:177], v[114:117]
ds_read_b32 v26, v24 offset:0
ds_read_b32 v148, v21
v_mfma_f32_16x16x32_bf16 v[186:189], v[150:153], v[142:145], v[36:39]
v_mfma_f32_16x16x32_bf16 v[118:121], v[174:177], a[200:203], v[118:121]
ds_read_b128 v[96:99], v51 offset:20480
ds_read_b32 v95, v24 offset:192
v_mfma_f32_16x16x32_bf16 v[110:113], a[204:207], v[178:181], v[110:113]
ds_read_b128 v[100:103], v52 offset:20480
ds_read_b32 v255, v27
v_mfma_f32_16x16x32_bf16 v[114:117], a[208:211], v[178:181], v[114:117]
ds_read_b32 v8, v28
ds_read_b128 v[194:197], v25 offset:0
v_mfma_f32_16x16x32_bf16 v[118:121], v[178:181], a[212:215], v[118:121]
s_waitcnt lgkmcnt(10)
ds_read_b128 v[104:107], v51 offset:22528
ds_read_b64 v[158:159], v22 offset:41472
v_mfma_f32_16x16x32_bf16 v[134:137], v[0:3], v[0:3], 0
s_waitcnt lgkmcnt(11)
ds_read_b128 v[54:57], v52 offset:22528
ds_read_b32 v140, v20
v_mfma_f32_16x16x32_bf16 v[134:137], v[4:7], v[4:7], v[134:137]
v_add_u32 v46, 1, v46
v_max_f32 v47, v186, v187
v_mfma_f32_16x16x32_bf16 v[122:125], a[0:3], v[0:3], 0
s_waitcnt lgkmcnt(11)
ds_read_b128 v[58:61], v51 offset:24576
v_fma_f32 v206, v90, v26, v114
s_waitcnt lgkmcnt(10)
v_mfma_f32_16x16x32_bf16 v[134:137], v[96:99], v[96:99], v[134:137]
s_waitcnt lgkmcnt(8)
ds_read_b128 v[62:65], v52 offset:24576
v_fma_f32 v207, v91, v26, v115
v_mfma_f32_16x16x32_bf16 v[134:137], v[100:103], v[100:103], v[134:137]
s_mov_b64 exec, 65535
global_store_dwordx4 v16, v[198:201], s[38:39] sc1
s_mov_b64 exec, -1
v_mfma_f32_16x16x32_bf16 v[126:129], a[4:7], v[0:3], 0
s_waitcnt lgkmcnt(5)
ds_read_b128 v[66:69], v51 offset:26624
v_add_u32 v16, 18432, v16
v_mfma_f32_16x16x32_bf16 v[134:137], v[104:107], v[104:107], v[134:137]
s_waitcnt lgkmcnt(4)
ds_read_b128 v[70:73], v52 offset:26624
v_fma_f32 v208, v92, v26, v116
v_mfma_f32_16x16x32_bf16 v[134:137], v[54:57], v[54:57], v[134:137]
v_fma_f32 v209, v93, v26, v117
v_cvt_pk_bf16_f32 v146, v206, v207
v_mfma_f32_16x16x32_bf16 v[130:133], v[0:3], a[8:11], 0
s_waitcnt lgkmcnt(3)
ds_read_b128 v[74:77], v51 offset:28672
v_cvt_pk_bf16_f32 v147, v208, v209
v_mfma_f32_16x16x32_bf16 v[134:137], v[58:61], v[58:61], v[134:137]
ds_read_b128 v[78:81], v52 offset:28672
v_mfma_f32_16x16x32_bf16 v[182:185], v[146:149], v[142:145], v[202:205]
s_waitcnt lgkmcnt(4)
v_max_f32 v48, v188, v189
v_fma_f32 v206, v94, v194, v118
v_mfma_f32_16x16x32_bf16 v[134:137], v[62:65], v[62:65], v[134:137]
v_fma_f32 v207, v94, v195, v119
v_fma_f32 v208, v94, v196, v120
v_mfma_f32_16x16x32_bf16 v[122:125], a[12:15], v[4:7], v[122:125]
s_waitcnt lgkmcnt(3)
ds_read_b128 v[82:85], v51 offset:30720
v_mul_f32 v95, 0x3eae1a93, v95
v_mfma_f32_16x16x32_bf16 v[134:137], v[66:69], v[66:69], v[134:137]
s_add_u32 m0, s49, 18432
ds_read_b128 v[162:165], v52 offset:30720
)"
R"(s_waitcnt lgkmcnt(4)
v_mfma_f32_16x16x32_bf16 v[134:137], v[70:73], v[70:73], v[134:137]
v_mul_f32 v9, v255, v95
v_mul_f32 v10, v8, v95
v_mfma_f32_16x16x32_bf16 v[126:129], a[16:19], v[4:7], v[126:129]
s_waitcnt lgkmcnt(3)
ds_read_b128 v[166:169], v51 offset:32768
v_max3_f32 v47, v182, v183, v47
v_mfma_f32_16x16x32_bf16 v[134:137], v[74:77], v[74:77], v[134:137]
s_waitcnt lgkmcnt(3)
ds_read_b128 v[170:173], v52 offset:32768
v_max3_f32 v48, v184, v185, v48
v_mfma_f32_16x16x32_bf16 v[134:137], v[78:81], v[78:81], v[134:137]
v_mul_f32 v47, v47, v9
v_mul_f32 v48, v48, v10
v_mfma_f32_16x16x32_bf16 v[130:133], v[4:7], a[20:23], v[130:133]
s_waitcnt lgkmcnt(3)
ds_read_b128 v[174:177], v51 offset:34816
global_load_lds_dwordx4 v11, s[42:43] offset:-2048 sc1
v_mfma_f32_16x16x32_bf16 v[134:137], v[82:85], v[82:85], v[134:137]
s_waitcnt lgkmcnt(3)
ds_read_b128 v[178:181], v52 offset:34816
v_max_f32 v49, v47, v48
v_mfma_f32_16x16x32_bf16 v[134:137], v[162:165], v[162:165], v[134:137]
v_mov_b32 v50, v49
v_fma_f32 v209, v94, v197, v121
v_mfma_f32_16x16x32_bf16 v[122:125], a[24:27], v[96:99], v[122:125]
s_waitcnt lgkmcnt(3)
v_permlane16_swap_b32 v49, v50
v_mfma_f32_16x16x32_bf16 v[134:137], v[166:169], v[166:169], v[134:137]
s_waitcnt lgkmcnt(2)
v_max_f32 v49, v49, v50
v_mov_b32 v50, v49
v_mfma_f32_16x16x32_bf16 v[134:137], v[170:173], v[170:173], v[134:137]
s_nop 0
v_permlane32_swap_b32 v49, v50
v_mfma_f32_16x16x32_bf16 v[126:129], a[28:31], v[96:99], v[126:129]
s_waitcnt lgkmcnt(1)
v_max_f32 v49, v49, v50
v_sub_f32 v47, v47, v49
v_mfma_f32_16x16x32_bf16 v[134:137], v[174:177], v[174:177], v[134:137]
s_waitcnt lgkmcnt(0)
v_sub_f32 v48, v48, v49
v_cvt_pk_bf16_f32 v154, v206, v207
v_mfma_f32_16x16x32_bf16 v[134:137], v[178:181], v[178:181], v[134:137]
global_load_lds_dwordx4 v12, s[42:43] offset:-1024 sc1
v_cvt_pk_bf16_f32 v155, v208, v209
v_mfma_f32_16x16x32_bf16 v[130:133], v[96:99], a[32:35], v[130:133]
v_exp_f32 v210, v47
v_mfma_f32_16x16x32_bf16 v[122:125], a[36:39], v[100:103], v[122:125]
v_exp_f32 v211, v48
v_mfma_f32_16x16x32_bf16 v[126:129], a[40:43], v[100:103], v[126:129]
v_add_f32 v49, v210, v211
v_mov_b32 v50, v49
v_mfma_f32_16x16x32_bf16 v[130:133], v[100:103], a[44:47], v[130:133]
ds_write_b128 v23, v[134:137] offset:41984
v_mfma_f32_16x16x32_bf16 v[122:125], a[48:51], v[104:107], v[122:125]
v_mul_f32 v210, v210, v255
v_fma_f32 v206, v86, v26, v110
v_mfma_f32_16x16x32_bf16 v[126:129], a[52:55], v[104:107], v[126:129]
v_permlane16_swap_b32 v49, v50
v_mfma_f32_16x16x32_bf16 v[130:133], v[104:107], a[56:59], v[130:133]
v_add_f32 v49, v49, v50
v_mov_b32 v50, v49
v_mfma_f32_16x16x32_bf16 v[122:125], a[60:63], v[54:57], v[122:125]
global_load_lds_dwordx4 v13, s[42:43] offset:0 sc1
v_mul_f32 v211, v211, v8
v_mfma_f32_16x16x32_bf16 v[126:129], a[64:67], v[54:57], v[126:129]
v_permlane32_swap_b32 v49, v50
v_mfma_f32_16x16x32_bf16 v[130:133], v[54:57], a[68:71], v[130:133]
v_mfma_f32_16x16x32_bf16 v[122:125], a[72:75], v[58:61], v[122:125]
s_waitcnt lgkmcnt(0)
s_barrier
v_mfma_f32_16x16x32_bf16 v[126:129], a[76:79], v[58:61], v[126:129]
v_add_f32 v49, v49, v50
v_fma_f32 v207, v87, v26, v111
v_mfma_f32_16x16x32_bf16 v[130:133], v[58:61], a[80:83], v[130:133]
v_rcp_f32 v49, v49
v_mfma_f32_16x16x32_bf16 v[122:125], a[84:87], v[62:65], v[122:125]
v_mul_f32 v210, v210, v49
v_mul_f32 v211, v211, v49
v_mfma_f32_16x16x32_bf16 v[126:129], a[88:91], v[62:65], v[126:129]
v_cvt_pk_bf16_f32 v210, v210, v210
v_cvt_pk_bf16_f32 v211, v211, v211
v_mfma_f32_16x16x32_bf16 v[130:133], v[62:65], a[92:95], v[130:133]
global_load_lds_dwordx4 v14, s[42:43] offset:1024 sc1
s_sub_u32 s42, s42, 18432
s_subb_u32 s43, s43, 0
v_mfma_f32_16x16x32_bf16 v[122:125], a[96:99], v[66:69], v[122:125]
v_and_b32 v190, v210, v40
v_and_b32 v191, v211, v41
v_mfma_f32_16x16x32_bf16 v[126:129], a[100:103], v[66:69], v[126:129]
v_and_b32 v192, v210, v42
v_and_b32 v193, v211, v43
v_mfma_f32_16x16x32_bf16 v[130:133], v[66:69], a[104:107], v[130:133]
s_nop 0
v_mfma_f32_16x16x32_bf16 v[186:189], v[154:157], v[190:193], 0
v_mfma_f32_16x16x32_bf16 v[182:185], v[158:161], v[190:193], 0
v_fma_f32 v208, v88, v26, v112
v_fma_f32 v209, v89, v26, v113
v_mfma_f32_16x16x32_bf16 v[122:125], a[108:111], v[70:73], v[122:125]
v_cvt_pk_bf16_f32 v138, v206, v207
v_cvt_pk_bf16_f32 v139, v208, v209
v_mfma_f32_16x16x32_bf16 v[126:129], a[112:115], v[70:73], v[126:129]
v_mfma_f32_16x16x32_bf16 v[130:133], v[70:73], a[116:119], v[130:133]
v_mfma_f32_16x16x32_bf16 v[122:125], a[120:123], v[74:77], v[122:125]
v_cvt_pk_bf16_f32 v108, v186, v187
v_cvt_pk_bf16_f32 v109, v188, v189
v_mfma_f32_16x16x32_bf16 v[126:129], a[124:127], v[74:77], v[126:129]
ds_write_b64 v18, v[108:109]
v_mfma_f32_16x16x32_bf16 v[130:133], v[74:77], a[128:131], v[130:133]
v_cvt_pk_bf16_f32 v210, v182, v183
v_mfma_f32_16x16x32_bf16 v[122:125], a[132:135], v[78:81], v[122:125]
ds_write_b32 v19, v210
v_mfma_f32_16x16x32_bf16 v[126:129], a[136:139], v[78:81], v[126:129]
v_mfma_f32_16x16x32_bf16 v[130:133], v[78:81], a[140:143], v[130:133]
v_mfma_f32_16x16x32_bf16 v[122:125], a[144:147], v[82:85], v[122:125]
v_mfma_f32_16x16x32_bf16 v[126:129], a[148:151], v[82:85], v[126:129]
v_mfma_f32_16x16x32_bf16 v[130:133], v[82:85], a[152:155], v[130:133]
v_mfma_f32_16x16x32_bf16 v[122:125], a[156:159], v[162:165], v[122:125]
v_add_lshl_u32 v253, v46, 1, 7
v_bfi_b32 v253, s69, v253, v45
v_mfma_f32_16x16x32_bf16 v[126:129], a[160:163], v[162:165], v[126:129]
global_load_dword v53, v253, s[38:39] sc1
v_mfma_f32_16x16x32_bf16 v[130:133], v[162:165], a[164:167], v[130:133]
s_waitcnt vmcnt(6)
v_sub_u32 v254, v254, v46
v_cmp_lt_u32 vcc, 0x7f, v254
v_mfma_f32_16x16x32_bf16 v[122:125], a[168:171], v[166:169], v[122:125]
v_mfma_f32_16x16x32_bf16 v[126:129], a[172:175], v[166:169], v[126:129]
v_xor_b32 v51, 0x10000, v51
v_xor_b32 v52, 0x10000, v52
v_mfma_f32_16x16x32_bf16 v[130:133], v[166:169], a[176:179], v[130:133]
s_and_b64 vcc, vcc, s[76:77]
s_cbranch_vccnz .Lqa_sp56
.Lqa_rt57:
v_mfma_f32_16x16x32_bf16 v[122:125], a[180:183], v[170:173], v[122:125]
v_mfma_f32_16x16x32_bf16 v[126:129], a[184:187], v[170:173], v[126:129]
s_waitcnt vmcnt(5) lgkmcnt(0)
s_barrier
.Lqa_in51:
v_mfma_f32_16x16x32_bf16 v[130:133], v[170:173], a[188:191], v[130:133]
s_cmp_eq_u32 s70, s73
s_cbranch_scc1 .Lqa_hk52
.Lqa_hr53:
ds_read_b128 v[0:3], v51 offset:0
v_mfma_f32_16x16x32_bf16 v[122:125], a[192:195], v[174:177], v[122:125]
ds_read_b128 v[4:7], v52 offset:0
ds_read_b128 v[198:201], v17 offset:0
v_mfma_f32_16x16x32_bf16 v[126:129], a[196:199], v[174:177], v[126:129]
ds_read_b32 v26, v24 offset:64
ds_read_b32 v152, v21
v_mfma_f32_16x16x32_bf16 v[186:189], v[146:149], v[138:141], v[36:39]
v_mfma_f32_16x16x32_bf16 v[130:133], v[174:177], a[200:203], v[130:133]
ds_read_b128 v[96:99], v51 offset:2048
ds_read_b32 v95, v24 offset:128
v_mfma_f32_16x16x32_bf16 v[122:125], a[204:207], v[178:181], v[122:125]
ds_read_b128 v[100:103], v52 offset:2048
ds_read_b32 v255, v29
v_mfma_f32_16x16x32_bf16 v[126:129], a[208:211], v[178:181], v[126:129]
v_mfma_f32_16x16x32_bf16 v[130:133], v[178:181], a[212:215], v[130:133]
s_sub_u32 s70, s70, 1
ds_read_b128 v[104:107], v51 offset:4096
s_cbranch_scc0 .Lqa_top76
s_waitcnt lgkmcnt(0)
ds_read_b32 v8, v30
ds_read_b128 v[194:197], v25 offset:64
ds_read_b64 v[160:161], v22 offset:41472
ds_read_b32 v144, v20
s_mov_b64 exec, 65535
global_store_dwordx4 v16, v[198:201], s[38:39] sc1
s_mov_b64 exec, -1
v_add_u32 v16, 18432, v16
v_fma_f32 v206, v90, v26, v126
v_fma_f32 v207, v91, v26, v127
v_fma_f32 v208, v92, v26, v128
v_fma_f32 v209, v93, v26, v129
v_cvt_pk_bf16_f32 v150, v206, v207
v_cvt_pk_bf16_f32 v151, v208, v209
v_max_f32 v47, v186, v187
s_nop 0
v_mfma_f32_16x16x32_bf16 v[182:185], v[150:153], v[138:141], v[32:35]
s_waitcnt lgkmcnt(2)
v_max_f32 v48, v188, v189
v_fma_f32 v206, v94, v194, v130
v_mul_f32 v95, 0x3eae1a93, v95
v_mul_f32 v9, v255, v95
v_mul_f32 v10, v8, v95
s_nop 1
v_max3_f32 v47, v182, v183, v47
v_max3_f32 v48, v184, v185, v48
v_mul_f32 v47, v47, v9
v_mul_f32 v48, v48, v10
v_max_f32 v49, v47, v48
v_mov_b32 v50, v49
s_nop 1
v_permlane16_swap_b32 v49, v50
v_max_f32 v49, v49, v50
v_mov_b32 v50, v49
s_nop 1
v_permlane32_swap_b32 v49, v50
v_max_f32 v49, v49, v50
v_sub_f32 v47, v47, v49
v_sub_f32 v48, v48, v49
v_fma_f32 v207, v94, v195, v131
v_exp_f32 v210, v47
v_fma_f32 v208, v94, v196, v132
v_exp_f32 v211, v48
s_nop 0
v_add_f32 v49, v210, v211
v_mov_b32 v50, v49
v_mul_f32 v210, v210, v255
v_fma_f32 v209, v94, v197, v133
v_permlane16_swap_b32 v49, v50
v_add_f32 v49, v49, v50
v_mov_b32 v50, v49
v_mul_f32 v211, v211, v8
v_cvt_pk_bf16_f32 v156, v206, v207
v_cvt_pk_bf16_f32 v157, v208, v209
s_waitcnt lgkmcnt(0)
s_barrier
v_permlane32_swap_b32 v49, v50
v_add_f32 v49, v49, v50
v_fma_f32 v206, v86, v26, v122
v_rcp_f32 v49, v49
s_nop 0
v_mul_f32 v210, v210, v49
v_mul_f32 v211, v211, v49
v_cvt_pk_bf16_f32 v210, v210, v210
v_cvt_pk_bf16_f32 v211, v211, v211
v_and_b32 v192, v210, v40
v_and_b32 v193, v211, v41
v_and_b32 v190, v210, v42
v_and_b32 v191, v211, v43
s_nop 1
v_mfma_f32_16x16x32_bf16 v[186:189], v[154:157], v[190:193], 0
v_mfma_f32_16x16x32_bf16 v[182:185], v[158:161], v[190:193], 0
v_fma_f32 v207, v87, v26, v123
v_fma_f32 v208, v88, v26, v124
v_fma_f32 v209, v89, v26, v125
v_cvt_pk_bf16_f32 v142, v206, v207
v_cvt_pk_bf16_f32 v143, v208, v209
s_nop 1
v_cvt_pk_bf16_f32 v108, v186, v187
v_cvt_pk_bf16_f32 v109, v188, v189
ds_write_b64 v18, v[108:109]
v_cvt_pk_bf16_f32 v210, v182, v183
ds_write_b32 v19, v210
s_waitcnt vmcnt(0)
s_waitcnt lgkmcnt(0)
s_barrier
ds_read_b128 v[198:201], v17 offset:0
s_waitcnt lgkmcnt(0)
global_store_dwordx4 v16, v[198:201], s[38:39] sc1
s_waitcnt vmcnt(0)
s_barrier
s_endpgm
.Lqa_w4:
s_getpc_b64 s[90:91]
.Lqa_pc98:
s_add_u32 s90, s90, .Lqa_e81-.Lqa_pc98
s_addc_u32 s91, s91, 0
s_branch .Lqa_k89
.Lqa_bk97:
v_add_u32 v255, 0x10000, v9
v_add_u32 v162, 0x20000, v9
v_add_u32 v163, 0x10000, v10
v_add_u32 v164, 0x20000, v10
v_mov_b32 v106, 0x3F803F80
v_mov_b32 v107, 0x3F803F80
v_mov_b32 v108, 0x3F803F80
v_mov_b32 v109, 0x3F803F80
v_mov_b32 v141, 0
v_mov_b32 v145, 0
v_mov_b32 v149, 0
v_mov_b32 v153, 0
v_mov_b32 v165, 46080
s_waitcnt lgkmcnt(0)
s_mul_i32 s37, s37, 0x9E3779B1
s_add_u32 s37, s37, 0x7F4A7C15
v_add_u32 v46, s37, v46
v_add_u32 v48, s37, v48
v_add_u32 v50, s37, v50
ds_write_b32 v165, v141
s_mov_b64 s[76:77], 0
s_mov_b32 s73, 18
s_add_u32 m0, s48, 4096
s_nop 0
global_load_lds_dwordx4 v0, s[40:41] offset:-4096
global_load_lds_dwordx4 v1, s[40:41] offset:-3072
global_load_lds_dwordx4 v2, s[40:41] offset:-2048
global_load_lds_dwordx4 v3, s[40:41] offset:-1024
global_load_lds_dwordx4 v4, s[40:41] offset:0
global_load_lds_dwordx4 v5, s[40:41] offset:1024
global_load_lds_dwordx4 v6, s[40:41] offset:2048
global_load_lds_dwordx4 v7, s[40:41] offset:3072
s_add_u32 m0, s48, 12288
s_nop 0
global_load_lds_dwordx4 v8, s[40:41] offset:-4096
s_add_u32 s40, s40, 221184
s_addc_u32 s41, s41, 0
s_add_u32 m0, s48, 40960
s_nop 0
global_load_lds_dwordx4 v0, s[40:41] offset:-4096
global_load_lds_dwordx4 v1, s[40:41] offset:-3072
global_load_lds_dwordx4 v2, s[40:41] offset:-2048
global_load_lds_dwordx4 v3, s[40:41] offset:-1024
global_load_lds_dwordx4 v4, s[40:41] offset:0
global_load_lds_dwordx4 v5, s[40:41] offset:1024
global_load_lds_dwordx4 v6, s[40:41] offset:2048
global_load_lds_dwordx4 v7, s[40:41] offset:3072
s_add_u32 m0, s48, 49152
s_nop 0
global_load_lds_dwordx4 v8, s[40:41] offset:-4096
s_add_u32 s40, s40, 221184
s_addc_u32 s41, s41, 0
s_waitcnt vmcnt(9) lgkmcnt(0)
s_barrier
s_add_u32 m0, s48, 77824
ds_read_b64_tr_b16 a[0:1], v9 offset:47104
ds_read_b64_tr_b16 a[2:3], v9 offset:49408
global_load_lds_dwordx4 v0, s[40:41] offset:-4096
ds_read_b64_tr_b16 a[4:5], v9 offset:47296
global_load_lds_dwordx4 v1, s[40:41] offset:-3072
ds_read_b64_tr_b16 a[6:7], v9 offset:49600
global_load_lds_dwordx4 v2, s[40:41] offset:-2048
ds_read_b64_tr_b16 a[8:9], v9 offset:47488
global_load_lds_dwordx4 v3, s[40:41] offset:-1024
ds_read_b64_tr_b16 a[10:11], v9 offset:49792
ds_read_b128 v[166:169], v51 offset:0
global_load_lds_dwordx4 v4, s[40:41] offset:0
ds_read_b128 v[174:177], v51 offset:18432
global_load_lds_dwordx4 v5, s[40:41] offset:1024
ds_read_b64_tr_b16 a[12:13], v255 offset:0
global_load_lds_dwordx4 v6, s[40:41] offset:2048
ds_read_b64_tr_b16 a[14:15], v255 offset:2304
global_load_lds_dwordx4 v7, s[40:41] offset:3072
ds_read_b64_tr_b16 a[16:17], v255 offset:192
ds_read_b64_tr_b16 a[18:19], v255 offset:2496
s_add_u32 m0, s48, 86016
ds_read_b64_tr_b16 a[20:21], v255 offset:384
global_load_lds_dwordx4 v8, s[40:41] offset:-4096
ds_read_b64_tr_b16 a[22:23], v255 offset:2688
s_add_u32 s40, s40, 221184
ds_read_b128 v[170:173], v52 offset:0
s_addc_u32 s41, s41, 0
ds_read_b128 v[178:181], v52 offset:18432
s_waitcnt vmcnt(9) lgkmcnt(0)
s_barrier
s_add_u32 m0, s48, 4096
ds_read_b64_tr_b16 a[24:25], v255 offset:18432
v_mfma_f32_16x16x32_bf16 v[86:89], a[0:3], v[106:109], 0
v_mfma_f32_16x16x32_bf16 v[90:93], a[4:7], v[106:109], 0
global_load_lds_dwordx4 v0, s[40:41] offset:-4096
ds_read_b64_tr_b16 a[26:27], v255 offset:20736
v_mfma_f32_16x16x32_bf16 v[94:97], v[106:109], a[8:11], 0
ds_read_b64_tr_b16 a[28:29], v255 offset:18624
global_load_lds_dwordx4 v1, s[40:41] offset:-3072
v_mfma_f32_16x16x32_bf16 v[110:113], a[0:3], v[166:169], 0
ds_read_b64_tr_b16 a[30:31], v255 offset:20928
v_mfma_f32_16x16x32_bf16 v[114:117], a[4:7], v[166:169], 0
global_load_lds_dwordx4 v2, s[40:41] offset:-2048
ds_read_b64_tr_b16 a[32:33], v255 offset:18816
v_mfma_f32_16x16x32_bf16 v[118:121], v[166:169], a[8:11], 0
ds_read_b64_tr_b16 a[34:35], v255 offset:21120
global_load_lds_dwordx4 v3, s[40:41] offset:-1024
v_mfma_f32_16x16x32_bf16 v[134:137], v[106:109], v[166:169], 0
ds_read_b128 v[182:185], v51 offset:2048
v_mfma_f32_16x16x32_bf16 v[122:125], a[0:3], v[174:177], 0
global_load_lds_dwordx4 v4, s[40:41] offset:0
ds_read_b128 v[190:193], v51 offset:20480
v_mfma_f32_16x16x32_bf16 v[126:129], a[4:7], v[174:177], 0
ds_read_b64_tr_b16 a[36:37], v255 offset:36864
global_load_lds_dwordx4 v5, s[40:41] offset:1024
v_mfma_f32_16x16x32_bf16 v[130:133], v[174:177], a[8:11], 0
v_mfma_f32_16x16x32_bf16 v[158:161], v[106:109], v[174:177], 0
ds_read_b64_tr_b16 a[38:39], v255 offset:39168
global_load_lds_dwordx4 v6, s[40:41] offset:2048
v_mfma_f32_16x16x32_bf16 v[86:89], a[12:15], v[106:109], v[86:89]
ds_read_b64_tr_b16 a[40:41], v255 offset:37056
v_mfma_f32_16x16x32_bf16 v[90:93], a[16:19], v[106:109], v[90:93]
global_load_lds_dwordx4 v7, s[40:41] offset:3072
ds_read_b64_tr_b16 a[42:43], v255 offset:39360
v_mfma_f32_16x16x32_bf16 v[94:97], v[106:109], a[20:23], v[94:97]
s_add_u32 m0, s48, 12288
ds_read_b64_tr_b16 a[44:45], v255 offset:37248
v_mfma_f32_16x16x32_bf16 v[110:113], a[12:15], v[170:173], v[110:113]
ds_read_b64_tr_b16 a[46:47], v255 offset:39552
v_mfma_f32_16x16x32_bf16 v[114:117], a[16:19], v[170:173], v[114:117]
global_load_lds_dwordx4 v8, s[40:41] offset:-4096
ds_read_b128 v[186:189], v52 offset:2048
v_mfma_f32_16x16x32_bf16 v[118:121], v[170:173], a[20:23], v[118:121]
s_add_u32 s40, s40, 221184
ds_read_b128 v[194:197], v52 offset:20480
v_mfma_f32_16x16x32_bf16 v[134:137], v[106:109], v[170:173], v[134:137]
v_mfma_f32_16x16x32_bf16 v[122:125], a[12:15], v[178:181], v[122:125]
v_mfma_f32_16x16x32_bf16 v[126:129], a[16:19], v[178:181], v[126:129]
s_addc_u32 s41, s41, 0
v_mfma_f32_16x16x32_bf16 v[130:133], v[178:181], a[20:23], v[130:133]
v_mfma_f32_16x16x32_bf16 v[158:161], v[106:109], v[178:181], v[158:161]
)"
R"(s_waitcnt vmcnt(9) lgkmcnt(0)
s_barrier
s_add_u32 m0, s48, 40960
ds_read_b64_tr_b16 a[48:49], v255 offset:55296
v_mfma_f32_16x16x32_bf16 v[86:89], a[24:27], v[106:109], v[86:89]
v_mfma_f32_16x16x32_bf16 v[90:93], a[28:31], v[106:109], v[90:93]
global_load_lds_dwordx4 v0, s[40:41] offset:-4096
ds_read_b64_tr_b16 a[50:51], v255 offset:57600
v_mfma_f32_16x16x32_bf16 v[94:97], v[106:109], a[32:35], v[94:97]
ds_read_b64_tr_b16 a[52:53], v255 offset:55488
global_load_lds_dwordx4 v1, s[40:41] offset:-3072
v_mfma_f32_16x16x32_bf16 v[110:113], a[24:27], v[182:185], v[110:113]
ds_read_b64_tr_b16 a[54:55], v255 offset:57792
v_mfma_f32_16x16x32_bf16 v[114:117], a[28:31], v[182:185], v[114:117]
global_load_lds_dwordx4 v2, s[40:41] offset:-2048
ds_read_b64_tr_b16 a[56:57], v255 offset:55680
v_mfma_f32_16x16x32_bf16 v[118:121], v[182:185], a[32:35], v[118:121]
ds_read_b64_tr_b16 a[58:59], v255 offset:57984
global_load_lds_dwordx4 v3, s[40:41] offset:-1024
v_mfma_f32_16x16x32_bf16 v[134:137], v[106:109], v[182:185], v[134:137]
ds_read_b128 v[166:169], v51 offset:4096
v_mfma_f32_16x16x32_bf16 v[122:125], a[24:27], v[190:193], v[122:125]
global_load_lds_dwordx4 v4, s[40:41] offset:0
ds_read_b128 v[174:177], v51 offset:22528
v_mfma_f32_16x16x32_bf16 v[126:129], a[28:31], v[190:193], v[126:129]
ds_read_b64_tr_b16 a[60:61], v162 offset:8192
global_load_lds_dwordx4 v5, s[40:41] offset:1024
v_mfma_f32_16x16x32_bf16 v[130:133], v[190:193], a[32:35], v[130:133]
v_mfma_f32_16x16x32_bf16 v[158:161], v[106:109], v[190:193], v[158:161]
ds_read_b64_tr_b16 a[62:63], v162 offset:10496
global_load_lds_dwordx4 v6, s[40:41] offset:2048
v_mfma_f32_16x16x32_bf16 v[86:89], a[36:39], v[106:109], v[86:89]
ds_read_b64_tr_b16 a[64:65], v162 offset:8384
v_mfma_f32_16x16x32_bf16 v[90:93], a[40:43], v[106:109], v[90:93]
global_load_lds_dwordx4 v7, s[40:41] offset:3072
ds_read_b64_tr_b16 a[66:67], v162 offset:10688
v_mfma_f32_16x16x32_bf16 v[94:97], v[106:109], a[44:47], v[94:97]
s_add_u32 m0, s48, 49152
ds_read_b64_tr_b16 a[68:69], v162 offset:8576
v_mfma_f32_16x16x32_bf16 v[110:113], a[36:39], v[186:189], v[110:113]
ds_read_b64_tr_b16 a[70:71], v162 offset:10880
v_mfma_f32_16x16x32_bf16 v[114:117], a[40:43], v[186:189], v[114:117]
global_load_lds_dwordx4 v8, s[40:41] offset:-4096
ds_read_b128 v[170:173], v52 offset:4096
v_mfma_f32_16x16x32_bf16 v[118:121], v[186:189], a[44:47], v[118:121]
s_add_u32 s40, s40, 221184
ds_read_b128 v[178:181], v52 offset:22528
v_mfma_f32_16x16x32_bf16 v[134:137], v[106:109], v[186:189], v[134:137]
v_mfma_f32_16x16x32_bf16 v[122:125], a[36:39], v[194:197], v[122:125]
v_mfma_f32_16x16x32_bf16 v[126:129], a[40:43], v[194:197], v[126:129]
s_addc_u32 s41, s41, 0
v_mfma_f32_16x16x32_bf16 v[130:133], v[194:197], a[44:47], v[130:133]
v_mfma_f32_16x16x32_bf16 v[158:161], v[106:109], v[194:197], v[158:161]
s_waitcnt vmcnt(9) lgkmcnt(0)
s_barrier
s_add_u32 m0, s48, 77824
ds_read_b64_tr_b16 a[72:73], v9 offset:47104
v_mfma_f32_16x16x32_bf16 v[86:89], a[48:51], v[106:109], v[86:89]
v_mfma_f32_16x16x32_bf16 v[90:93], a[52:55], v[106:109], v[90:93]
global_load_lds_dwordx4 v0, s[40:41] offset:-4096
ds_read_b64_tr_b16 a[74:75], v9 offset:49408
v_mfma_f32_16x16x32_bf16 v[94:97], v[106:109], a[56:59], v[94:97]
ds_read_b64_tr_b16 a[76:77], v9 offset:47296
global_load_lds_dwordx4 v1, s[40:41] offset:-3072
v_mfma_f32_16x16x32_bf16 v[110:113], a[48:51], v[166:169], v[110:113]
ds_read_b64_tr_b16 a[78:79], v9 offset:49600
v_mfma_f32_16x16x32_bf16 v[114:117], a[52:55], v[166:169], v[114:117]
global_load_lds_dwordx4 v2, s[40:41] offset:-2048
ds_read_b64_tr_b16 a[80:81], v9 offset:47488
v_mfma_f32_16x16x32_bf16 v[118:121], v[166:169], a[56:59], v[118:121]
ds_read_b64_tr_b16 a[82:83], v9 offset:49792
global_load_lds_dwordx4 v3, s[40:41] offset:-1024
v_mfma_f32_16x16x32_bf16 v[134:137], v[106:109], v[166:169], v[134:137]
ds_read_b128 v[182:185], v51 offset:6144
v_mfma_f32_16x16x32_bf16 v[122:125], a[48:51], v[174:177], v[122:125]
global_load_lds_dwordx4 v4, s[40:41] offset:0
ds_read_b128 v[190:193], v51 offset:24576
v_mfma_f32_16x16x32_bf16 v[126:129], a[52:55], v[174:177], v[126:129]
ds_read_b64_tr_b16 a[84:85], v255 offset:0
global_load_lds_dwordx4 v5, s[40:41] offset:1024
v_mfma_f32_16x16x32_bf16 v[130:133], v[174:177], a[56:59], v[130:133]
v_mfma_f32_16x16x32_bf16 v[158:161], v[106:109], v[174:177], v[158:161]
ds_read_b64_tr_b16 a[86:87], v255 offset:2304
global_load_lds_dwordx4 v6, s[40:41] offset:2048
v_mfma_f32_16x16x32_bf16 v[86:89], a[60:63], v[106:109], v[86:89]
ds_read_b64_tr_b16 a[88:89], v255 offset:192
v_mfma_f32_16x16x32_bf16 v[90:93], a[64:67], v[106:109], v[90:93]
global_load_lds_dwordx4 v7, s[40:41] offset:3072
ds_read_b64_tr_b16 a[90:91], v255 offset:2496
v_mfma_f32_16x16x32_bf16 v[94:97], v[106:109], a[68:71], v[94:97]
s_add_u32 m0, s48, 86016
ds_read_b64_tr_b16 a[92:93], v255 offset:384
v_mfma_f32_16x16x32_bf16 v[110:113], a[60:63], v[170:173], v[110:113]
ds_read_b64_tr_b16 a[94:95], v255 offset:2688
v_mfma_f32_16x16x32_bf16 v[114:117], a[64:67], v[170:173], v[114:117]
global_load_lds_dwordx4 v8, s[40:41] offset:-4096
ds_read_b128 v[186:189], v52 offset:6144
v_mfma_f32_16x16x32_bf16 v[118:121], v[170:173], a[68:71], v[118:121]
s_add_u32 s40, s40, 221184
ds_read_b128 v[194:197], v52 offset:24576
v_mfma_f32_16x16x32_bf16 v[134:137], v[106:109], v[170:173], v[134:137]
v_mfma_f32_16x16x32_bf16 v[122:125], a[60:63], v[178:181], v[122:125]
v_mfma_f32_16x16x32_bf16 v[126:129], a[64:67], v[178:181], v[126:129]
s_addc_u32 s41, s41, 0
v_mfma_f32_16x16x32_bf16 v[130:133], v[178:181], a[68:71], v[130:133]
v_mfma_f32_16x16x32_bf16 v[158:161], v[106:109], v[178:181], v[158:161]
s_waitcnt vmcnt(9) lgkmcnt(0)
s_barrier
s_add_u32 m0, s48, 4096
ds_read_b64_tr_b16 a[96:97], v255 offset:18432
v_mfma_f32_16x16x32_bf16 v[86:89], a[72:75], v[106:109], v[86:89]
v_mfma_f32_16x16x32_bf16 v[90:93], a[76:79], v[106:109], v[90:93]
global_load_lds_dwordx4 v0, s[40:41] offset:-4096
ds_read_b64_tr_b16 a[98:99], v255 offset:20736
v_mfma_f32_16x16x32_bf16 v[94:97], v[106:109], a[80:83], v[94:97]
ds_read_b64_tr_b16 a[100:101], v255 offset:18624
global_load_lds_dwordx4 v1, s[40:41] offset:-3072
v_mfma_f32_16x16x32_bf16 v[110:113], a[72:75], v[182:185], v[110:113]
ds_read_b64_tr_b16 a[102:103], v255 offset:20928
v_mfma_f32_16x16x32_bf16 v[114:117], a[76:79], v[182:185], v[114:117]
global_load_lds_dwordx4 v2, s[40:41] offset:-2048
ds_read_b64_tr_b16 a[104:105], v255 offset:18816
v_mfma_f32_16x16x32_bf16 v[118:121], v[182:185], a[80:83], v[118:121]
ds_read_b64_tr_b16 a[106:107], v255 offset:21120
global_load_lds_dwordx4 v3, s[40:41] offset:-1024
v_mfma_f32_16x16x32_bf16 v[134:137], v[106:109], v[182:185], v[134:137]
ds_read_b128 v[166:169], v51 offset:8192
v_mfma_f32_16x16x32_bf16 v[122:125], a[72:75], v[190:193], v[122:125]
global_load_lds_dwordx4 v4, s[40:41] offset:0
ds_read_b128 v[174:177], v51 offset:26624
v_mfma_f32_16x16x32_bf16 v[126:129], a[76:79], v[190:193], v[126:129]
ds_read_b64_tr_b16 a[108:109], v255 offset:36864
global_load_lds_dwordx4 v5, s[40:41] offset:1024
v_mfma_f32_16x16x32_bf16 v[130:133], v[190:193], a[80:83], v[130:133]
v_mfma_f32_16x16x32_bf16 v[158:161], v[106:109], v[190:193], v[158:161]
ds_read_b64_tr_b16 a[110:111], v255 offset:39168
global_load_lds_dwordx4 v6, s[40:41] offset:2048
v_mfma_f32_16x16x32_bf16 v[86:89], a[84:87], v[106:109], v[86:89]
ds_read_b64_tr_b16 a[112:113], v255 offset:37056
v_mfma_f32_16x16x32_bf16 v[90:93], a[88:91], v[106:109], v[90:93]
global_load_lds_dwordx4 v7, s[40:41] offset:3072
ds_read_b64_tr_b16 a[114:115], v255 offset:39360
v_mfma_f32_16x16x32_bf16 v[94:97], v[106:109], a[92:95], v[94:97]
s_add_u32 m0, s48, 12288
ds_read_b64_tr_b16 a[116:117], v255 offset:37248
v_mfma_f32_16x16x32_bf16 v[110:113], a[84:87], v[186:189], v[110:113]
ds_read_b64_tr_b16 a[118:119], v255 offset:39552
v_mfma_f32_16x16x32_bf16 v[114:117], a[88:91], v[186:189], v[114:117]
global_load_lds_dwordx4 v8, s[40:41] offset:-4096
ds_read_b128 v[170:173], v52 offset:8192
v_mfma_f32_16x16x32_bf16 v[118:121], v[186:189], a[92:95], v[118:121]
s_add_u32 s40, s40, 221184
ds_read_b128 v[178:181], v52 offset:26624
v_mfma_f32_16x16x32_bf16 v[134:137], v[106:109], v[186:189], v[134:137]
v_mfma_f32_16x16x32_bf16 v[122:125], a[84:87], v[194:197], v[122:125]
v_mfma_f32_16x16x32_bf16 v[126:129], a[88:91], v[194:197], v[126:129]
s_addc_u32 s41, s41, 0
v_mfma_f32_16x16x32_bf16 v[130:133], v[194:197], a[92:95], v[130:133]
v_mfma_f32_16x16x32_bf16 v[158:161], v[106:109], v[194:197], v[158:161]
s_waitcnt vmcnt(9) lgkmcnt(0)
s_barrier
s_add_u32 m0, s48, 40960
ds_read_b64_tr_b16 a[120:121], v255 offset:55296
v_mfma_f32_16x16x32_bf16 v[86:89], a[96:99], v[106:109], v[86:89]
v_mfma_f32_16x16x32_bf16 v[90:93], a[100:103], v[106:109], v[90:93]
global_load_lds_dwordx4 v0, s[40:41] offset:-4096
ds_read_b64_tr_b16 a[122:123], v255 offset:57600
v_mfma_f32_16x16x32_bf16 v[94:97], v[106:109], a[104:107], v[94:97]
ds_read_b64_tr_b16 a[124:125], v255 offset:55488
global_load_lds_dwordx4 v1, s[40:41] offset:-3072
v_mfma_f32_16x16x32_bf16 v[110:113], a[96:99], v[166:169], v[110:113]
ds_read_b64_tr_b16 a[126:127], v255 offset:57792
v_mfma_f32_16x16x32_bf16 v[114:117], a[100:103], v[166:169], v[114:117]
global_load_lds_dwordx4 v2, s[40:41] offset:-2048
ds_read_b64_tr_b16 a[128:129], v255 offset:55680
v_mfma_f32_16x16x32_bf16 v[118:121], v[166:169], a[104:107], v[118:121]
ds_read_b64_tr_b16 a[130:131], v255 offset:57984
global_load_lds_dwordx4 v3, s[40:41] offset:-1024
v_mfma_f32_16x16x32_bf16 v[134:137], v[106:109], v[166:169], v[134:137]
ds_read_b128 v[182:185], v51 offset:10240
v_mfma_f32_16x16x32_bf16 v[122:125], a[96:99], v[174:177], v[122:125]
global_load_lds_dwordx4 v4, s[40:41] offset:0
ds_read_b128 v[190:193], v51 offset:28672
v_mfma_f32_16x16x32_bf16 v[126:129], a[100:103], v[174:177], v[126:129]
ds_read_b64_tr_b16 a[132:133], v162 offset:8192
global_load_lds_dwordx4 v5, s[40:41] offset:1024
v_mfma_f32_16x16x32_bf16 v[130:133], v[174:177], a[104:107], v[130:133]
v_mfma_f32_16x16x32_bf16 v[158:161], v[106:109], v[174:177], v[158:161]
ds_read_b64_tr_b16 a[134:135], v162 offset:10496
global_load_lds_dwordx4 v6, s[40:41] offset:2048
v_mfma_f32_16x16x32_bf16 v[86:89], a[108:111], v[106:109], v[86:89]
ds_read_b64_tr_b16 a[136:137], v162 offset:8384
v_mfma_f32_16x16x32_bf16 v[90:93], a[112:115], v[106:109], v[90:93]
global_load_lds_dwordx4 v7, s[40:41] offset:3072
ds_read_b64_tr_b16 a[138:139], v162 offset:10688
v_mfma_f32_16x16x32_bf16 v[94:97], v[106:109], a[116:119], v[94:97]
s_add_u32 m0, s48, 49152
ds_read_b64_tr_b16 a[140:141], v162 offset:8576
v_mfma_f32_16x16x32_bf16 v[110:113], a[108:111], v[170:173], v[110:113]
ds_read_b64_tr_b16 a[142:143], v162 offset:10880
v_mfma_f32_16x16x32_bf16 v[114:117], a[112:115], v[170:173], v[114:117]
global_load_lds_dwordx4 v8, s[40:41] offset:-4096
ds_read_b128 v[186:189], v52 offset:10240
v_mfma_f32_16x16x32_bf16 v[118:121], v[170:173], a[116:119], v[118:121]
s_add_u32 s40, s40, 221184
ds_read_b128 v[194:197], v52 offset:28672
v_mfma_f32_16x16x32_bf16 v[134:137], v[106:109], v[170:173], v[134:137]
v_mfma_f32_16x16x32_bf16 v[122:125], a[108:111], v[178:181], v[122:125]
v_mfma_f32_16x16x32_bf16 v[126:129], a[112:115], v[178:181], v[126:129]
s_addc_u32 s41, s41, 0
v_mfma_f32_16x16x32_bf16 v[130:133], v[178:181], a[116:119], v[130:133]
v_mfma_f32_16x16x32_bf16 v[158:161], v[106:109], v[178:181], v[158:161]
s_waitcnt vmcnt(9) lgkmcnt(0)
s_barrier
s_add_u32 m0, s48, 77824
ds_read_b64_tr_b16 a[144:145], v9 offset:47104
v_mfma_f32_16x16x32_bf16 v[86:89], a[120:123], v[106:109], v[86:89]
v_mfma_f32_16x16x32_bf16 v[90:93], a[124:127], v[106:109], v[90:93]
global_load_lds_dwordx4 v0, s[40:41] offset:-4096
ds_read_b64_tr_b16 a[146:147], v9 offset:49408
v_mfma_f32_16x16x32_bf16 v[94:97], v[106:109], a[128:131], v[94:97]
ds_read_b64_tr_b16 a[148:149], v9 offset:47296
global_load_lds_dwordx4 v1, s[40:41] offset:-3072
v_mfma_f32_16x16x32_bf16 v[110:113], a[120:123], v[182:185], v[110:113]
ds_read_b64_tr_b16 a[150:151], v9 offset:49600
v_mfma_f32_16x16x32_bf16 v[114:117], a[124:127], v[182:185], v[114:117]
global_load_lds_dwordx4 v2, s[40:41] offset:-2048
ds_read_b64_tr_b16 a[152:153], v9 offset:47488
v_mfma_f32_16x16x32_bf16 v[118:121], v[182:185], a[128:131], v[118:121]
ds_read_b64_tr_b16 a[154:155], v9 offset:49792
global_load_lds_dwordx4 v3, s[40:41] offset:-1024
v_mfma_f32_16x16x32_bf16 v[134:137], v[106:109], v[182:185], v[134:137]
ds_read_b128 v[166:169], v51 offset:12288
v_mfma_f32_16x16x32_bf16 v[122:125], a[120:123], v[190:193], v[122:125]
global_load_lds_dwordx4 v4, s[40:41] offset:0
ds_read_b128 v[174:177], v51 offset:30720
v_mfma_f32_16x16x32_bf16 v[126:129], a[124:127], v[190:193], v[126:129]
ds_read_b64_tr_b16 a[156:157], v255 offset:0
global_load_lds_dwordx4 v5, s[40:41] offset:1024
v_mfma_f32_16x16x32_bf16 v[130:133], v[190:193], a[128:131], v[130:133]
v_mfma_f32_16x16x32_bf16 v[158:161], v[106:109], v[190:193], v[158:161]
ds_read_b64_tr_b16 a[158:159], v255 offset:2304
global_load_lds_dwordx4 v6, s[40:41] offset:2048
v_mfma_f32_16x16x32_bf16 v[86:89], a[132:135], v[106:109], v[86:89]
ds_read_b64_tr_b16 a[160:161], v255 offset:192
v_mfma_f32_16x16x32_bf16 v[90:93], a[136:139], v[106:109], v[90:93]
global_load_lds_dwordx4 v7, s[40:41] offset:3072
ds_read_b64_tr_b16 a[162:163], v255 offset:2496
v_mfma_f32_16x16x32_bf16 v[94:97], v[106:109], a[140:143], v[94:97]
s_add_u32 m0, s48, 86016
ds_read_b64_tr_b16 a[164:165], v255 offset:384
v_mfma_f32_16x16x32_bf16 v[110:113], a[132:135], v[186:189], v[110:113]
ds_read_b64_tr_b16 a[166:167], v255 offset:2688
v_mfma_f32_16x16x32_bf16 v[114:117], a[136:139], v[186:189], v[114:117]
global_load_lds_dwordx4 v8, s[40:41] offset:-4096
ds_read_b128 v[170:173], v52 offset:12288
v_mfma_f32_16x16x32_bf16 v[118:121], v[186:189], a[140:143], v[118:121]
s_sub_u32 s40, s40, 1769472
ds_read_b128 v[178:181], v52 offset:30720
v_mfma_f32_16x16x32_bf16 v[134:137], v[106:109], v[186:189], v[134:137]
v_mfma_f32_16x16x32_bf16 v[122:125], a[132:135], v[194:197], v[122:125]
v_mfma_f32_16x16x32_bf16 v[126:129], a[136:139], v[194:197], v[126:129]
s_subb_u32 s41, s41, 0
v_mfma_f32_16x16x32_bf16 v[130:133], v[194:197], a[140:143], v[130:133]
v_mfma_f32_16x16x32_bf16 v[158:161], v[106:109], v[194:197], v[158:161]
s_waitcnt vmcnt(9) lgkmcnt(0)
s_barrier
s_add_u32 m0, s49, 0
ds_read_b64_tr_b16 a[168:169], v255 offset:18432
v_mfma_f32_16x16x32_bf16 v[86:89], a[144:147], v[106:109], v[86:89]
v_mfma_f32_16x16x32_bf16 v[90:93], a[148:151], v[106:109], v[90:93]
ds_read_b64_tr_b16 a[170:171], v255 offset:20736
v_mfma_f32_16x16x32_bf16 v[94:97], v[106:109], a[152:155], v[94:97]
ds_read_b64_tr_b16 a[172:173], v255 offset:18624
v_mfma_f32_16x16x32_bf16 v[110:113], a[144:147], v[166:169], v[110:113]
global_load_lds_dwordx4 v11, s[42:43] offset:-2048 sc1
ds_read_b64_tr_b16 a[174:175], v255 offset:20928
v_mfma_f32_16x16x32_bf16 v[114:117], a[148:151], v[166:169], v[114:117]
ds_read_b64_tr_b16 a[176:177], v255 offset:18816
v_mfma_f32_16x16x32_bf16 v[118:121], v[166:169], a[152:155], v[118:121]
ds_read_b64_tr_b16 a[178:179], v255 offset:21120
v_mfma_f32_16x16x32_bf16 v[134:137], v[106:109], v[166:169], v[134:137]
global_load_lds_dwordx4 v12, s[42:43] offset:-1024 sc1
ds_read_b128 v[182:185], v51 offset:14336
v_mfma_f32_16x16x32_bf16 v[122:125], a[144:147], v[174:177], v[122:125]
ds_read_b128 v[190:193], v51 offset:32768
v_mfma_f32_16x16x32_bf16 v[126:129], a[148:151], v[174:177], v[126:129]
ds_read_b64_tr_b16 a[180:181], v255 offset:36864
v_mfma_f32_16x16x32_bf16 v[130:133], v[174:177], a[152:155], v[130:133]
global_load_lds_dwordx4 v13, s[42:43] offset:0 sc1
v_mfma_f32_16x16x32_bf16 v[158:161], v[106:109], v[174:177], v[158:161]
ds_read_b64_tr_b16 a[182:183], v255 offset:39168
v_mfma_f32_16x16x32_bf16 v[86:89], a[156:159], v[106:109], v[86:89]
ds_read_b64_tr_b16 a[184:185], v255 offset:37056
v_mfma_f32_16x16x32_bf16 v[90:93], a[160:163], v[106:109], v[90:93]
ds_read_b64_tr_b16 a[186:187], v255 offset:39360
v_mfma_f32_16x16x32_bf16 v[94:97], v[106:109], a[164:167], v[94:97]
global_load_lds_dwordx4 v14, s[42:43] offset:1024 sc1
ds_read_b64_tr_b16 a[188:189], v255 offset:37248
v_mfma_f32_16x16x32_bf16 v[110:113], a[156:159], v[170:173], v[110:113]
ds_read_b64_tr_b16 a[190:191], v255 offset:39552
v_mfma_f32_16x16x32_bf16 v[114:117], a[160:163], v[170:173], v[114:117]
ds_read_b128 v[186:189], v52 offset:14336
v_mfma_f32_16x16x32_bf16 v[118:121], v[170:173], a[164:167], v[118:121]
s_sub_u32 s42, s42, 18432
s_subb_u32 s43, s43, 0
ds_read_b128 v[194:197], v52 offset:32768
v_mfma_f32_16x16x32_bf16 v[134:137], v[106:109], v[170:173], v[134:137]
v_mfma_f32_16x16x32_bf16 v[122:125], a[156:159], v[178:181], v[122:125]
v_mfma_f32_16x16x32_bf16 v[126:129], a[160:163], v[178:181], v[126:129]
v_mfma_f32_16x16x32_bf16 v[130:133], v[178:181], a[164:167], v[130:133]
v_mfma_f32_16x16x32_bf16 v[158:161], v[106:109], v[178:181], v[158:161]
s_waitcnt vmcnt(4) lgkmcnt(0)
s_barrier
s_add_u32 m0, s49, 18432
ds_read_b64_tr_b16 a[192:193], v255 offset:55296
v_mfma_f32_16x16x32_bf16 v[86:89], a[168:171], v[106:109], v[86:89]
v_mfma_f32_16x16x32_bf16 v[90:93], a[172:175], v[106:109], v[90:93]
ds_read_b64_tr_b16 a[194:195], v255 offset:57600
v_mfma_f32_16x16x32_bf16 v[94:97], v[106:109], a[176:179], v[94:97]
ds_read_b64_tr_b16 a[196:197], v255 offset:55488
v_mfma_f32_16x16x32_bf16 v[110:113], a[168:171], v[182:185], v[110:113]
global_load_lds_dwordx4 v11, s[42:43] offset:-2048 sc1
ds_read_b64_tr_b16 a[198:199], v255 offset:57792
v_mfma_f32_16x16x32_bf16 v[114:117], a[172:175], v[182:185], v[114:117]
ds_read_b64_tr_b16 a[200:201], v255 offset:55680
v_mfma_f32_16x16x32_bf16 v[118:121], v[182:185], a[176:179], v[118:121]
ds_read_b64_tr_b16 a[202:203], v255 offset:57984
v_mfma_f32_16x16x32_bf16 v[134:137], v[106:109], v[182:185], v[134:137]
global_load_lds_dwordx4 v12, s[42:43] offset:-1024 sc1
ds_read_b128 v[166:169], v51 offset:16384
v_mfma_f32_16x16x32_bf16 v[122:125], a[168:171], v[190:193], v[122:125]
ds_read_b128 v[174:177], v51 offset:34816
v_mfma_f32_16x16x32_bf16 v[126:129], a[172:175], v[190:193], v[126:129]
ds_read_b64_tr_b16 a[204:205], v162 offset:8192
v_mfma_f32_16x16x32_bf16 v[130:133], v[190:193], a[176:179], v[130:133]
global_load_lds_dwordx4 v13, s[42:43] offset:0 sc1
ds_read_b64_tr_b16 a[206:207], v162 offset:10496
v_mfma_f32_16x16x32_bf16 v[158:161], v[106:109], v[190:193], v[158:161]
ds_read_b64_tr_b16 a[208:209], v162 offset:8384
v_mfma_f32_16x16x32_bf16 v[86:89], a[180:183], v[106:109], v[86:89]
ds_read_b64_tr_b16 a[210:211], v162 offset:10688
v_mfma_f32_16x16x32_bf16 v[90:93], a[184:187], v[106:109], v[90:93]
global_load_lds_dwordx4 v14, s[42:43] offset:1024 sc1
ds_read_b64_tr_b16 a[212:213], v162 offset:8576
v_mfma_f32_16x16x32_bf16 v[94:97], v[106:109], a[188:191], v[94:97]
ds_read_b64_tr_b16 a[214:215], v162 offset:10880
v_mfma_f32_16x16x32_bf16 v[110:113], a[180:183], v[186:189], v[110:113]
ds_read_b128 v[170:173], v52 offset:16384
v_mfma_f32_16x16x32_bf16 v[114:117], a[184:187], v[186:189], v[114:117]
s_sub_u32 s42, s42, 18432
s_subb_u32 s43, s43, 0
ds_read_b128 v[178:181], v52 offset:34816
v_mfma_f32_16x16x32_bf16 v[118:121], v[186:189], a[188:191], v[118:121]
v_mfma_f32_16x16x32_bf16 v[134:137], v[106:109], v[186:189], v[134:137]
v_mfma_f32_16x16x32_bf16 v[122:125], a[180:183], v[194:197], v[122:125]
v_mfma_f32_16x16x32_bf16 v[126:129], a[184:187], v[194:197], v[126:129]
v_mfma_f32_16x16x32_bf16 v[158:161], v[106:109], v[194:197], v[158:161]
s_waitcnt lgkmcnt(14)
v_mfma_f32_16x16x32_bf16 v[86:89], a[192:195], v[106:109], v[86:89]
s_waitcnt lgkmcnt(12)
v_mfma_f32_16x16x32_bf16 v[90:93], a[196:199], v[106:109], v[90:93]
s_waitcnt lgkmcnt(10)
v_mfma_f32_16x16x32_bf16 v[94:97], v[106:109], a[200:203], v[94:97]
s_waitcnt lgkmcnt(9)
v_mfma_f32_16x16x32_bf16 v[110:113], a[192:195], v[166:169], v[110:113]
v_mfma_f32_16x16x32_bf16 v[114:117], a[196:199], v[166:169], v[114:117]
v_mfma_f32_16x16x32_bf16 v[118:121], v[166:169], a[200:203], v[118:121]
v_mfma_f32_16x16x32_bf16 v[134:137], v[106:109], v[166:169], v[134:137]
s_waitcnt lgkmcnt(8)
v_mfma_f32_16x16x32_bf16 v[158:161], v[106:109], v[174:177], v[158:161]
s_waitcnt lgkmcnt(6)
v_mfma_f32_16x16x32_bf16 v[86:89], a[204:207], v[106:109], v[86:89]
s_waitcnt lgkmcnt(4)
v_mfma_f32_16x16x32_bf16 v[90:93], a[208:211], v[106:109], v[90:93]
s_waitcnt lgkmcnt(2)
v_mfma_f32_16x16x32_bf16 v[94:97], v[106:109], a[212:215], v[94:97]
s_waitcnt lgkmcnt(1)
v_mfma_f32_16x16x32_bf16 v[110:113], a[204:207], v[170:173], v[110:113]
v_mfma_f32_16x16x32_bf16 v[114:117], a[208:211], v[170:173], v[114:117]
v_mfma_f32_16x16x32_bf16 v[118:121], v[170:173], a[212:215], v[118:121]
v_mfma_f32_16x16x32_bf16 v[134:137], v[106:109], v[170:173], v[134:137]
s_waitcnt lgkmcnt(0)
v_mfma_f32_16x16x32_bf16 v[158:161], v[106:109], v[178:181], v[158:161]
s_nop 5
v_mul_f32 v202, 0x3ae38e39, v134
v_cvt_pk_bf16_f32 v216, -v202, -v202
v_lshlrev_b32 v216, 16, v216
ds_write_b32 v24, v216 offset:0
s_waitcnt lgkmcnt(0)
s_barrier
ds_read_b32 v216, v26
s_waitcnt lgkmcnt(0)
v_mul_f32 v216, 0x3ae38e39, v216
v_fma_f32 v203, -v202, v202, v216
v_max_f32 v203, 0, v203
v_cvt_pk_bf16_f32 v203, v203, v203
v_lshlrev_b32 v203, 16, v203
v_add_f32 v203, 0x3727c5ac, v203
v_cvt_pk_bf16_f32 v203, v203, v203
)"
R"(v_lshlrev_b32 v203, 16, v203
v_sqrt_f32 v203, v203
s_nop 0
v_cvt_pk_bf16_f32 v203, v203, v203
v_lshlrev_b32 v203, 16, v203
v_rcp_f32 v216, v203
s_nop 0
ds_write_b32 v24, v216 offset:128
ds_read_b128 v[174:177], v52 offset:32768
ds_read_b128 v[178:181], v51 offset:34816
ds_read_b128 v[182:185], v52 offset:34816
v_mov_b32 v206, v32
v_mov_b32 v207, v33
v_mov_b32 v208, v34
v_mov_b32 v209, v35
v_mov_b32 v134, v158
v_mov_b32 v135, v159
v_mov_b32 v136, v160
v_mov_b32 v137, v161
s_waitcnt lgkmcnt(3)
s_barrier
ds_read_b32 v23, v24 offset:0
ds_read_b32 v148, v21
ds_read_b32 v95, v24 offset:192
ds_read_b32 v254, v27
ds_read_b32 v255, v28
ds_read_b128 v[198:201], v25 offset:0
ds_read_b64 v[158:159], v22 offset:41472
ds_read_b32 v140, v20
v_xor_b32 v51, 0x10000, v51
v_xor_b32 v52, 0x10000, v52
v_mul_f32 v202, 0x3ae38e39, v134
v_cvt_pk_bf16_f32 v216, -v202, -v202
v_lshlrev_b32 v216, 16, v216
ds_write_b32 v24, v216 offset:64
s_waitcnt lgkmcnt(8)
v_fma_f32 v210, v90, v23, v114
v_fma_f32 v211, v91, v23, v115
v_fma_f32 v212, v92, v23, v116
v_fma_f32 v213, v93, v23, v117
v_cvt_pk_bf16_f32 v146, v210, v211
v_cvt_pk_bf16_f32 v147, v212, v213
s_waitcnt lgkmcnt(0)
s_barrier
ds_read_b32 v216, v26
v_fma_f32 v210, v94, v198, v118
v_fma_f32 v211, v94, v199, v119
v_fma_f32 v212, v94, v200, v120
v_fma_f32 v213, v94, v201, v121
v_cvt_pk_bf16_f32 v154, v210, v211
v_cvt_pk_bf16_f32 v155, v212, v213
v_fma_f32 v210, v86, v23, v110
v_fma_f32 v211, v87, v23, v111
v_fma_f32 v212, v88, v23, v112
v_fma_f32 v213, v89, v23, v113
v_cvt_pk_bf16_f32 v138, v210, v211
v_cvt_pk_bf16_f32 v139, v212, v213
s_waitcnt lgkmcnt(0)
v_mul_f32 v216, 0x3ae38e39, v216
v_fma_f32 v203, -v202, v202, v216
v_max_f32 v203, 0, v203
v_cvt_pk_bf16_f32 v203, v203, v203
v_lshlrev_b32 v203, 16, v203
v_add_f32 v203, 0x3727c5ac, v203
v_cvt_pk_bf16_f32 v203, v203, v203
v_lshlrev_b32 v203, 16, v203
v_sqrt_f32 v203, v203
s_nop 0
v_cvt_pk_bf16_f32 v203, v203, v203
v_lshlrev_b32 v203, 16, v203
v_rcp_f32 v216, v203
s_nop 0
ds_write_b32 v24, v216 offset:192
v_add_lshl_u32 v252, v48, 0, 7
v_bfi_b32 v252, s69, v252, v47
s_waitcnt vmcnt(4) lgkmcnt(0)
s_barrier
s_mov_b32 s70, 32
s_waitcnt lgkmcnt(0)
s_nop 1
s_branch .Lqa_in78
.p2align 6
.Lqa_top99:
s_waitcnt lgkmcnt(5)
v_mfma_f32_16x16x32_bf16 v[134:137], v[106:109], v[0:3], 0
s_waitcnt lgkmcnt(4)
ds_read_b128 v[58:61], v52 offset:4096
v_bfi_b32 v252, s69, v252, v47
v_mfma_f32_16x16x32_bf16 v[134:137], v[106:109], v[4:7], v[134:137]
ds_read_b32 v152, v21
ds_read_b32 v95, v24 offset:128
v_mfma_f32_16x16x32_bf16 v[110:113], a[0:3], v[0:3], 0
s_waitcnt lgkmcnt(5)
ds_read_b128 v[62:65], v51 offset:6144
ds_read_b32 v254, v29
v_mfma_f32_16x16x32_bf16 v[134:137], v[106:109], v[96:99], v[134:137]
s_waitcnt lgkmcnt(6)
ds_read_b128 v[66:69], v52 offset:6144
ds_read_b32 v255, v30
v_mfma_f32_16x16x32_bf16 v[134:137], v[106:109], v[100:103], v[134:137]
ds_read_b128 v[198:201], v25 offset:64
ds_read_b64 v[160:161], v22 offset:41472
v_mfma_f32_16x16x32_bf16 v[114:117], a[4:7], v[0:3], 0
s_waitcnt lgkmcnt(9)
ds_read_b128 v[70:73], v51 offset:8192
ds_read_b32 v144, v20
v_mfma_f32_16x16x32_bf16 v[134:137], v[106:109], v[54:57], v[134:137]
s_waitcnt lgkmcnt(10)
ds_read_b128 v[74:77], v52 offset:8192
v_fma_f32 v210, v90, v23, v126
v_mfma_f32_16x16x32_bf16 v[134:137], v[106:109], v[58:61], v[134:137]
v_fma_f32 v211, v91, v23, v127
v_fma_f32 v212, v92, v23, v128
v_mfma_f32_16x16x32_bf16 v[118:121], v[0:3], a[8:11], 0
s_waitcnt lgkmcnt(8)
ds_read_b128 v[78:81], v51 offset:10240
v_fma_f32 v213, v93, v23, v129
v_mfma_f32_16x16x32_bf16 v[134:137], v[106:109], v[62:65], v[134:137]
s_waitcnt lgkmcnt(7)
ds_read_b128 v[82:85], v52 offset:10240
v_cvt_pk_bf16_f32 v150, v210, v211
v_mfma_f32_16x16x32_bf16 v[134:137], v[106:109], v[66:69], v[134:137]
v_cvt_pk_bf16_f32 v151, v212, v213
v_max_f32 v10, v190, v191
v_mfma_f32_16x16x32_bf16 v[110:113], a[12:15], v[4:7], v[110:113]
ds_read_b128 v[162:165], v51 offset:12288
v_mfma_f32_16x16x32_bf16 v[186:189], v[150:153], v[138:141], v[32:35]
s_waitcnt lgkmcnt(7)
v_max_f32 v44, v192, v193
v_fma_f32 v210, v94, v198, v130
s_waitcnt lgkmcnt(5)
v_mfma_f32_16x16x32_bf16 v[134:137], v[106:109], v[70:73], v[134:137]
s_xor_b32 s49, s49, 0x10000
ds_read_b128 v[166:169], v52 offset:12288
s_waitcnt lgkmcnt(4)
v_mfma_f32_16x16x32_bf16 v[134:137], v[106:109], v[74:77], v[134:137]
s_add_u32 m0, s49, 0
v_mul_f32 v95, 0x3eae1a93, v95
v_mul_f32 v8, v254, v95
v_mfma_f32_16x16x32_bf16 v[114:117], a[16:19], v[4:7], v[114:117]
s_waitcnt lgkmcnt(3)
ds_read_b128 v[170:173], v51 offset:14336
v_mul_f32 v9, v255, v95
v_mfma_f32_16x16x32_bf16 v[134:137], v[106:109], v[78:81], v[134:137]
s_waitcnt lgkmcnt(3)
ds_read_b128 v[174:177], v52 offset:14336
v_max3_f32 v10, v186, v187, v10
v_mfma_f32_16x16x32_bf16 v[134:137], v[106:109], v[82:85], v[134:137]
v_max3_f32 v44, v188, v189, v44
v_mul_f32 v10, v10, v8
v_mfma_f32_16x16x32_bf16 v[118:121], v[4:7], a[20:23], v[118:121]
s_waitcnt lgkmcnt(3)
ds_read_b128 v[178:181], v51 offset:16384
v_mul_f32 v44, v44, v9
v_mfma_f32_16x16x32_bf16 v[134:137], v[106:109], v[162:165], v[134:137]
s_waitcnt lgkmcnt(3)
ds_read_b128 v[182:185], v52 offset:16384
v_max_f32 v45, v10, v44
v_mfma_f32_16x16x32_bf16 v[134:137], v[106:109], v[166:169], v[134:137]
v_mov_b32 v46, v45
v_fma_f32 v211, v94, v199, v131
v_mfma_f32_16x16x32_bf16 v[110:113], a[24:27], v[96:99], v[110:113]
s_waitcnt lgkmcnt(3)
global_load_lds_dwordx4 v11, s[42:43] offset:-2048 sc1
v_fma_f32 v212, v94, v200, v132
v_mfma_f32_16x16x32_bf16 v[134:137], v[106:109], v[170:173], v[134:137]
s_waitcnt lgkmcnt(2)
v_permlane16_swap_b32 v45, v46
v_mfma_f32_16x16x32_bf16 v[134:137], v[106:109], v[174:177], v[134:137]
v_max_f32 v45, v45, v46
v_mov_b32 v46, v45
v_mfma_f32_16x16x32_bf16 v[114:117], a[28:31], v[96:99], v[114:117]
s_waitcnt lgkmcnt(1)
v_permlane32_swap_b32 v45, v46
v_mfma_f32_16x16x32_bf16 v[134:137], v[106:109], v[178:181], v[134:137]
s_waitcnt lgkmcnt(0)
v_max_f32 v45, v45, v46
v_sub_f32 v10, v10, v45
v_mfma_f32_16x16x32_bf16 v[134:137], v[106:109], v[182:185], v[134:137]
v_sub_f32 v44, v44, v45
v_fma_f32 v213, v94, v201, v133
v_mfma_f32_16x16x32_bf16 v[118:121], v[96:99], a[32:35], v[118:121]
v_exp_f32 v214, v10
v_mfma_f32_16x16x32_bf16 v[110:113], a[36:39], v[100:103], v[110:113]
v_exp_f32 v215, v44
v_mfma_f32_16x16x32_bf16 v[114:117], a[40:43], v[100:103], v[114:117]
s_nop 0
v_mul_f32 v202, 0x3ae38e39, v134
v_cvt_pk_bf16_f32 v216, -v202, -v202
v_mfma_f32_16x16x32_bf16 v[118:121], v[100:103], a[44:47], v[118:121]
v_lshlrev_b32 v216, 16, v216
global_load_lds_dwordx4 v12, s[42:43] offset:-1024 sc1
v_mfma_f32_16x16x32_bf16 v[110:113], a[48:51], v[54:57], v[110:113]
ds_write_b32 v24, v216 offset:0
v_mfma_f32_16x16x32_bf16 v[114:117], a[52:55], v[54:57], v[114:117]
v_add_f32 v45, v214, v215
v_mov_b32 v46, v45
v_mfma_f32_16x16x32_bf16 v[118:121], v[54:57], a[56:59], v[118:121]
v_mul_f32 v214, v214, v254
v_cvt_pk_bf16_f32 v156, v210, v211
v_mfma_f32_16x16x32_bf16 v[110:113], a[60:63], v[58:61], v[110:113]
v_permlane16_swap_b32 v45, v46
v_mfma_f32_16x16x32_bf16 v[114:117], a[64:67], v[58:61], v[114:117]
v_add_f32 v45, v45, v46
v_mov_b32 v46, v45
v_mfma_f32_16x16x32_bf16 v[118:121], v[58:61], a[68:71], v[118:121]
v_mfma_f32_16x16x32_bf16 v[110:113], a[72:75], v[62:65], v[110:113]
s_waitcnt lgkmcnt(0)
s_barrier
v_mfma_f32_16x16x32_bf16 v[114:117], a[76:79], v[62:65], v[114:117]
global_load_lds_dwordx4 v13, s[42:43] offset:0 sc1
ds_read_b32 v216, v26
v_mfma_f32_16x16x32_bf16 v[118:121], v[62:65], a[80:83], v[118:121]
v_mul_f32 v215, v215, v255
v_cvt_pk_bf16_f32 v157, v212, v213
v_mfma_f32_16x16x32_bf16 v[110:113], a[84:87], v[66:69], v[110:113]
v_permlane32_swap_b32 v45, v46
v_mfma_f32_16x16x32_bf16 v[114:117], a[88:91], v[66:69], v[114:117]
v_add_f32 v45, v45, v46
v_fma_f32 v210, v86, v23, v122
v_mfma_f32_16x16x32_bf16 v[118:121], v[66:69], a[92:95], v[118:121]
v_rcp_f32 v45, v45
v_mfma_f32_16x16x32_bf16 v[110:113], a[96:99], v[70:73], v[110:113]
v_mul_f32 v214, v214, v45
v_mul_f32 v215, v215, v45
v_mfma_f32_16x16x32_bf16 v[114:117], a[100:103], v[70:73], v[114:117]
v_cvt_pk_bf16_f32 v214, v214, v214
v_cvt_pk_bf16_f32 v215, v215, v215
v_mfma_f32_16x16x32_bf16 v[118:121], v[70:73], a[104:107], v[118:121]
v_and_b32 v196, v214, v40
v_and_b32 v197, v215, v41
v_mfma_f32_16x16x32_bf16 v[110:113], a[108:111], v[74:77], v[110:113]
global_load_lds_dwordx4 v14, s[42:43] offset:1024 sc1
s_add_u32 s42, s42, s46
s_addc_u32 s43, s43, s47
v_mfma_f32_16x16x32_bf16 v[114:117], a[112:115], v[74:77], v[114:117]
v_and_b32 v194, v214, v42
v_and_b32 v195, v215, v43
v_mfma_f32_16x16x32_bf16 v[118:121], v[74:77], a[116:119], v[118:121]
s_nop 0
v_mfma_f32_16x16x32_bf16 v[190:193], v[154:157], v[194:197], 0
v_mfma_f32_16x16x32_bf16 v[186:189], v[158:161], v[194:197], 0
s_waitcnt lgkmcnt(0)
v_mul_f32 v216, 0x3ae38e39, v216
v_fma_f32 v203, -v202, v202, v216
v_mfma_f32_16x16x32_bf16 v[110:113], a[120:123], v[78:81], v[110:113]
v_max_f32 v203, 0, v203
v_cvt_pk_bf16_f32 v203, v203, v203
v_mfma_f32_16x16x32_bf16 v[114:117], a[124:127], v[78:81], v[114:117]
v_lshlrev_b32 v203, 16, v203
v_add_f32 v203, 0x3727c5ac, v203
v_mfma_f32_16x16x32_bf16 v[118:121], v[78:81], a[128:131], v[118:121]
v_cvt_pk_bf16_f32 v203, v203, v203
v_lshlrev_b32 v203, 16, v203
v_mfma_f32_16x16x32_bf16 v[110:113], a[132:135], v[82:85], v[110:113]
v_cvt_pk_bf16_f32 v104, v190, v191
v_cvt_pk_bf16_f32 v105, v192, v193
v_mfma_f32_16x16x32_bf16 v[114:117], a[136:139], v[82:85], v[114:117]
ds_write_b64 v18, v[104:105]
v_mfma_f32_16x16x32_bf16 v[118:121], v[82:85], a[140:143], v[118:121]
v_cvt_pk_bf16_f32 v214, v188, v189
v_fma_f32 v211, v87, v23, v123
v_mfma_f32_16x16x32_bf16 v[110:113], a[144:147], v[162:165], v[110:113]
ds_write_b32 v19, v214
v_mfma_f32_16x16x32_bf16 v[114:117], a[148:151], v[162:165], v[114:117]
v_sqrt_f32 v203, v203
v_mfma_f32_16x16x32_bf16 v[118:121], v[162:165], a[152:155], v[118:121]
v_cvt_pk_bf16_f32 v203, v203, v203
v_lshlrev_b32 v203, 16, v203
v_mfma_f32_16x16x32_bf16 v[110:113], a[156:159], v[166:169], v[110:113]
v_rcp_f32 v216, v203
v_mfma_f32_16x16x32_bf16 v[114:117], a[160:163], v[166:169], v[114:117]
ds_write_b32 v24, v216 offset:128
v_mfma_f32_16x16x32_bf16 v[118:121], v[166:169], a[164:167], v[118:121]
v_fma_f32 v212, v88, v23, v124
v_fma_f32 v213, v89, v23, v125
v_mfma_f32_16x16x32_bf16 v[110:113], a[168:171], v[170:173], v[110:113]
v_cvt_pk_bf16_f32 v142, v210, v211
v_cvt_pk_bf16_f32 v143, v212, v213
v_mfma_f32_16x16x32_bf16 v[114:117], a[172:175], v[170:173], v[114:117]
v_mfma_f32_16x16x32_bf16 v[118:121], v[170:173], a[176:179], v[118:121]
v_mfma_f32_16x16x32_bf16 v[110:113], a[180:183], v[174:177], v[110:113]
v_mfma_f32_16x16x32_bf16 v[114:117], a[184:187], v[174:177], v[114:117]
s_waitcnt vmcnt(5) lgkmcnt(0)
s_barrier
v_mfma_f32_16x16x32_bf16 v[118:121], v[174:177], a[188:191], v[118:121]
ds_read_b128 v[0:3], v51 offset:18432
v_mfma_f32_16x16x32_bf16 v[110:113], a[192:195], v[178:181], v[110:113]
ds_read_b128 v[4:7], v52 offset:18432
ds_read_b32 v23, v24 offset:0
v_mfma_f32_16x16x32_bf16 v[114:117], a[196:199], v[178:181], v[114:117]
s_mov_b64 exec, 1
global_store_dword v252, v48, s[38:39] sc1
s_mov_b64 exec, -1
v_mfma_f32_16x16x32_bf16 v[190:193], v[150:153], v[142:145], v[36:39]
v_mfma_f32_16x16x32_bf16 v[118:121], v[178:181], a[200:203], v[118:121]
ds_read_b128 v[96:99], v51 offset:20480
v_add_u32 v48, 1, v48
v_mfma_f32_16x16x32_bf16 v[110:113], a[204:207], v[182:185], v[110:113]
ds_read_b128 v[100:103], v52 offset:20480
v_add_lshl_u32 v252, v48, 0, 7
v_mfma_f32_16x16x32_bf16 v[114:117], a[208:211], v[182:185], v[114:117]
v_bfi_b32 v252, s69, v252, v47
ds_read_b32 v148, v21
v_mfma_f32_16x16x32_bf16 v[118:121], v[182:185], a[212:215], v[118:121]
s_waitcnt lgkmcnt(5)
ds_read_b128 v[54:57], v51 offset:22528
ds_read_b32 v95, v24 offset:192
v_mfma_f32_16x16x32_bf16 v[134:137], v[106:109], v[0:3], 0
s_waitcnt lgkmcnt(6)
ds_read_b128 v[58:61], v52 offset:22528
ds_read_b32 v254, v27
v_mfma_f32_16x16x32_bf16 v[134:137], v[106:109], v[4:7], v[134:137]
ds_read_b32 v255, v28
ds_read_b128 v[198:201], v25 offset:0
v_mfma_f32_16x16x32_bf16 v[122:125], a[0:3], v[0:3], 0
s_waitcnt lgkmcnt(8)
ds_read_b128 v[62:65], v51 offset:24576
ds_read_b64 v[158:159], v22 offset:41472
v_mfma_f32_16x16x32_bf16 v[134:137], v[106:109], v[96:99], v[134:137]
s_waitcnt lgkmcnt(9)
ds_read_b128 v[66:69], v52 offset:24576
ds_read_b32 v140, v20
v_mfma_f32_16x16x32_bf16 v[134:137], v[106:109], v[100:103], v[134:137]
v_fma_f32 v210, v90, v23, v114
v_fma_f32 v211, v91, v23, v115
v_mfma_f32_16x16x32_bf16 v[126:129], a[4:7], v[0:3], 0
s_waitcnt lgkmcnt(9)
ds_read_b128 v[70:73], v51 offset:26624
v_fma_f32 v212, v92, v23, v116
v_mfma_f32_16x16x32_bf16 v[134:137], v[106:109], v[54:57], v[134:137]
s_waitcnt lgkmcnt(8)
ds_read_b128 v[74:77], v52 offset:26624
v_fma_f32 v213, v93, v23, v117
v_mfma_f32_16x16x32_bf16 v[134:137], v[106:109], v[58:61], v[134:137]
v_cvt_pk_bf16_f32 v146, v210, v211
v_cvt_pk_bf16_f32 v147, v212, v213
v_mfma_f32_16x16x32_bf16 v[130:133], v[0:3], a[8:11], 0
ds_read_b128 v[78:81], v51 offset:28672
v_mfma_f32_16x16x32_bf16 v[186:189], v[146:149], v[142:145], v[206:209]
s_waitcnt lgkmcnt(6)
v_max_f32 v10, v190, v191
v_max_f32 v44, v192, v193
v_mfma_f32_16x16x32_bf16 v[134:137], v[106:109], v[62:65], v[134:137]
s_waitcnt lgkmcnt(4)
ds_read_b128 v[82:85], v52 offset:28672
v_fma_f32 v210, v94, v198, v118
v_mfma_f32_16x16x32_bf16 v[134:137], v[106:109], v[66:69], v[134:137]
v_mul_f32 v95, 0x3eae1a93, v95
v_mul_f32 v8, v254, v95
v_mfma_f32_16x16x32_bf16 v[122:125], a[12:15], v[4:7], v[122:125]
s_waitcnt lgkmcnt(3)
ds_read_b128 v[162:165], v51 offset:30720
v_mul_f32 v9, v255, v95
v_mfma_f32_16x16x32_bf16 v[134:137], v[106:109], v[70:73], v[134:137]
s_add_u32 m0, s49, 18432
ds_read_b128 v[166:169], v52 offset:30720
s_waitcnt lgkmcnt(4)
v_mfma_f32_16x16x32_bf16 v[134:137], v[106:109], v[74:77], v[134:137]
v_max3_f32 v10, v186, v187, v10
v_max3_f32 v44, v188, v189, v44
v_mfma_f32_16x16x32_bf16 v[126:129], a[16:19], v[4:7], v[126:129]
s_waitcnt lgkmcnt(3)
ds_read_b128 v[170:173], v51 offset:32768
v_mul_f32 v10, v10, v8
v_mfma_f32_16x16x32_bf16 v[134:137], v[106:109], v[78:81], v[134:137]
s_waitcnt lgkmcnt(3)
ds_read_b128 v[174:177], v52 offset:32768
v_mul_f32 v44, v44, v9
v_mfma_f32_16x16x32_bf16 v[134:137], v[106:109], v[82:85], v[134:137]
v_max_f32 v45, v10, v44
v_mov_b32 v46, v45
v_mfma_f32_16x16x32_bf16 v[130:133], v[4:7], a[20:23], v[130:133]
s_waitcnt lgkmcnt(3)
ds_read_b128 v[178:181], v51 offset:34816
v_fma_f32 v211, v94, v199, v119
v_mfma_f32_16x16x32_bf16 v[134:137], v[106:109], v[162:165], v[134:137]
s_waitcnt lgkmcnt(3)
ds_read_b128 v[182:185], v52 offset:34816
v_fma_f32 v212, v94, v200, v120
v_mfma_f32_16x16x32_bf16 v[134:137], v[106:109], v[166:169], v[134:137]
global_load_lds_dwordx4 v11, s[42:43] offset:-2048 sc1
v_fma_f32 v213, v94, v201, v121
v_mfma_f32_16x16x32_bf16 v[122:125], a[24:27], v[96:99], v[122:125]
s_waitcnt lgkmcnt(3)
v_permlane16_swap_b32 v45, v46
v_mfma_f32_16x16x32_bf16 v[134:137], v[106:109], v[170:173], v[134:137]
s_waitcnt lgkmcnt(2)
v_max_f32 v45, v45, v46
v_mov_b32 v46, v45
v_mfma_f32_16x16x32_bf16 v[134:137], v[106:109], v[174:177], v[134:137]
s_nop 0
v_permlane32_swap_b32 v45, v46
v_mfma_f32_16x16x32_bf16 v[126:129], a[28:31], v[96:99], v[126:129]
s_waitcnt lgkmcnt(1)
v_max_f32 v45, v45, v46
v_sub_f32 v10, v10, v45
v_mfma_f32_16x16x32_bf16 v[134:137], v[106:109], v[178:181], v[134:137]
s_waitcnt lgkmcnt(0)
v_sub_f32 v44, v44, v45
)"
R"(v_cvt_pk_bf16_f32 v154, v210, v211
v_mfma_f32_16x16x32_bf16 v[134:137], v[106:109], v[182:185], v[134:137]
v_exp_f32 v214, v10
v_mfma_f32_16x16x32_bf16 v[130:133], v[96:99], a[32:35], v[130:133]
v_exp_f32 v215, v44
v_mfma_f32_16x16x32_bf16 v[122:125], a[36:39], v[100:103], v[122:125]
global_load_lds_dwordx4 v12, s[42:43] offset:-1024 sc1
v_add_f32 v45, v214, v215
v_mfma_f32_16x16x32_bf16 v[126:129], a[40:43], v[100:103], v[126:129]
s_nop 0
v_mul_f32 v202, 0x3ae38e39, v134
v_cvt_pk_bf16_f32 v216, -v202, -v202
v_mfma_f32_16x16x32_bf16 v[130:133], v[100:103], a[44:47], v[130:133]
v_lshlrev_b32 v216, 16, v216
v_mov_b32 v46, v45
v_mfma_f32_16x16x32_bf16 v[122:125], a[48:51], v[54:57], v[122:125]
ds_write_b32 v24, v216 offset:64
v_mfma_f32_16x16x32_bf16 v[126:129], a[52:55], v[54:57], v[126:129]
v_mul_f32 v214, v214, v254
v_cvt_pk_bf16_f32 v155, v212, v213
v_mfma_f32_16x16x32_bf16 v[130:133], v[54:57], a[56:59], v[130:133]
v_permlane16_swap_b32 v45, v46
v_mfma_f32_16x16x32_bf16 v[122:125], a[60:63], v[58:61], v[122:125]
v_add_f32 v45, v45, v46
v_mov_b32 v46, v45
v_mfma_f32_16x16x32_bf16 v[126:129], a[64:67], v[58:61], v[126:129]
v_mul_f32 v215, v215, v255
v_fma_f32 v210, v86, v23, v110
v_mfma_f32_16x16x32_bf16 v[130:133], v[58:61], a[68:71], v[130:133]
v_mfma_f32_16x16x32_bf16 v[122:125], a[72:75], v[62:65], v[122:125]
s_waitcnt lgkmcnt(0)
s_barrier
v_mfma_f32_16x16x32_bf16 v[126:129], a[76:79], v[62:65], v[126:129]
global_load_lds_dwordx4 v13, s[42:43] offset:0 sc1
ds_read_b32 v216, v26
v_mfma_f32_16x16x32_bf16 v[130:133], v[62:65], a[80:83], v[130:133]
v_permlane32_swap_b32 v45, v46
v_mfma_f32_16x16x32_bf16 v[122:125], a[84:87], v[66:69], v[122:125]
v_add_f32 v45, v45, v46
v_fma_f32 v211, v87, v23, v111
v_mfma_f32_16x16x32_bf16 v[126:129], a[88:91], v[66:69], v[126:129]
v_rcp_f32 v45, v45
v_mfma_f32_16x16x32_bf16 v[130:133], v[66:69], a[92:95], v[130:133]
v_mul_f32 v214, v214, v45
v_mul_f32 v215, v215, v45
v_mfma_f32_16x16x32_bf16 v[122:125], a[96:99], v[70:73], v[122:125]
v_cvt_pk_bf16_f32 v214, v214, v214
v_cvt_pk_bf16_f32 v215, v215, v215
v_mfma_f32_16x16x32_bf16 v[126:129], a[100:103], v[70:73], v[126:129]
v_and_b32 v194, v214, v40
v_and_b32 v195, v215, v41
v_mfma_f32_16x16x32_bf16 v[130:133], v[70:73], a[104:107], v[130:133]
v_and_b32 v196, v214, v42
v_and_b32 v197, v215, v43
v_mfma_f32_16x16x32_bf16 v[122:125], a[108:111], v[74:77], v[122:125]
global_load_lds_dwordx4 v14, s[42:43] offset:1024 sc1
s_sub_u32 s42, s42, 18432
s_subb_u32 s43, s43, 0
v_mfma_f32_16x16x32_bf16 v[190:193], v[154:157], v[194:197], 0
v_mfma_f32_16x16x32_bf16 v[186:189], v[158:161], v[194:197], 0
s_waitcnt lgkmcnt(0)
v_mul_f32 v216, 0x3ae38e39, v216
v_fma_f32 v203, -v202, v202, v216
v_mfma_f32_16x16x32_bf16 v[126:129], a[112:115], v[74:77], v[126:129]
v_max_f32 v203, 0, v203
v_cvt_pk_bf16_f32 v203, v203, v203
v_mfma_f32_16x16x32_bf16 v[130:133], v[74:77], a[116:119], v[130:133]
v_lshlrev_b32 v203, 16, v203
v_add_f32 v203, 0x3727c5ac, v203
v_mfma_f32_16x16x32_bf16 v[122:125], a[120:123], v[78:81], v[122:125]
v_cvt_pk_bf16_f32 v203, v203, v203
v_lshlrev_b32 v203, 16, v203
v_mfma_f32_16x16x32_bf16 v[126:129], a[124:127], v[78:81], v[126:129]
v_cvt_pk_bf16_f32 v104, v190, v191
v_cvt_pk_bf16_f32 v105, v192, v193
v_mfma_f32_16x16x32_bf16 v[130:133], v[78:81], a[128:131], v[130:133]
ds_write_b64 v18, v[104:105]
v_mfma_f32_16x16x32_bf16 v[122:125], a[132:135], v[82:85], v[122:125]
v_cvt_pk_bf16_f32 v214, v188, v189
v_fma_f32 v212, v88, v23, v112
v_mfma_f32_16x16x32_bf16 v[126:129], a[136:139], v[82:85], v[126:129]
ds_write_b32 v19, v214
v_mfma_f32_16x16x32_bf16 v[130:133], v[82:85], a[140:143], v[130:133]
v_sqrt_f32 v203, v203
v_mfma_f32_16x16x32_bf16 v[122:125], a[144:147], v[162:165], v[122:125]
v_cvt_pk_bf16_f32 v203, v203, v203
v_lshlrev_b32 v203, 16, v203
v_mfma_f32_16x16x32_bf16 v[126:129], a[148:151], v[162:165], v[126:129]
v_rcp_f32 v216, v203
v_mfma_f32_16x16x32_bf16 v[130:133], v[162:165], a[152:155], v[130:133]
ds_write_b32 v24, v216 offset:192
v_mfma_f32_16x16x32_bf16 v[122:125], a[156:159], v[166:169], v[122:125]
v_fma_f32 v213, v89, v23, v113
v_cvt_pk_bf16_f32 v138, v210, v211
v_mfma_f32_16x16x32_bf16 v[126:129], a[160:163], v[166:169], v[126:129]
v_cvt_pk_bf16_f32 v139, v212, v213
v_mfma_f32_16x16x32_bf16 v[130:133], v[166:169], a[164:167], v[130:133]
v_mfma_f32_16x16x32_bf16 v[122:125], a[168:171], v[170:173], v[122:125]
v_mfma_f32_16x16x32_bf16 v[126:129], a[172:175], v[170:173], v[126:129]
v_xor_b32 v51, 0x10000, v51
v_xor_b32 v52, 0x10000, v52
v_mfma_f32_16x16x32_bf16 v[130:133], v[170:173], a[176:179], v[130:133]
v_mfma_f32_16x16x32_bf16 v[122:125], a[180:183], v[174:177], v[122:125]
v_mfma_f32_16x16x32_bf16 v[126:129], a[184:187], v[174:177], v[126:129]
s_waitcnt vmcnt(5) lgkmcnt(0)
s_barrier
.Lqa_in78:
v_mfma_f32_16x16x32_bf16 v[130:133], v[174:177], a[188:191], v[130:133]
s_cmp_eq_u32 s70, s73
s_cbranch_scc1 .Lqa_hk79
.Lqa_hr80:
ds_read_b128 v[0:3], v51 offset:0
v_mfma_f32_16x16x32_bf16 v[122:125], a[192:195], v[178:181], v[122:125]
ds_read_b128 v[4:7], v52 offset:0
ds_read_b32 v23, v24 offset:64
v_mfma_f32_16x16x32_bf16 v[126:129], a[196:199], v[178:181], v[126:129]
s_mov_b64 exec, 1
global_store_dword v252, v48, s[38:39] sc1
s_mov_b64 exec, -1
v_mfma_f32_16x16x32_bf16 v[190:193], v[146:149], v[138:141], v[36:39]
v_mfma_f32_16x16x32_bf16 v[130:133], v[178:181], a[200:203], v[130:133]
ds_read_b128 v[96:99], v51 offset:2048
v_add_u32 v48, 1, v48
v_mfma_f32_16x16x32_bf16 v[122:125], a[204:207], v[182:185], v[122:125]
ds_read_b128 v[100:103], v52 offset:2048
v_add_lshl_u32 v252, v48, 0, 7
v_mfma_f32_16x16x32_bf16 v[126:129], a[208:211], v[182:185], v[126:129]
v_mfma_f32_16x16x32_bf16 v[130:133], v[182:185], a[212:215], v[130:133]
s_sub_u32 s70, s70, 1
ds_read_b128 v[54:57], v51 offset:4096
s_cbranch_scc0 .Lqa_top99
s_waitcnt lgkmcnt(0)
v_bfi_b32 v252, s69, v252, v47
ds_read_b32 v152, v21
ds_read_b32 v95, v24 offset:128
ds_read_b32 v254, v29
ds_read_b32 v255, v30
ds_read_b128 v[198:201], v25 offset:64
ds_read_b64 v[160:161], v22 offset:41472
ds_read_b32 v144, v20
v_fma_f32 v210, v90, v23, v126
v_fma_f32 v211, v91, v23, v127
v_fma_f32 v212, v92, v23, v128
v_fma_f32 v213, v93, v23, v129
v_cvt_pk_bf16_f32 v150, v210, v211
v_cvt_pk_bf16_f32 v151, v212, v213
v_max_f32 v10, v190, v191
s_waitcnt lgkmcnt(6)
v_mfma_f32_16x16x32_bf16 v[186:189], v[150:153], v[138:141], v[32:35]
s_waitcnt lgkmcnt(2)
v_max_f32 v44, v192, v193
v_fma_f32 v210, v94, v198, v130
v_mul_f32 v95, 0x3eae1a93, v95
v_mul_f32 v8, v254, v95
v_mul_f32 v9, v255, v95
s_nop 1
v_max3_f32 v10, v186, v187, v10
v_max3_f32 v44, v188, v189, v44
v_mul_f32 v10, v10, v8
v_mul_f32 v44, v44, v9
v_max_f32 v45, v10, v44
v_mov_b32 v46, v45
v_fma_f32 v211, v94, v199, v131
v_fma_f32 v212, v94, v200, v132
v_permlane16_swap_b32 v45, v46
v_max_f32 v45, v45, v46
v_mov_b32 v46, v45
s_nop 1
v_permlane32_swap_b32 v45, v46
v_max_f32 v45, v45, v46
v_sub_f32 v10, v10, v45
v_sub_f32 v44, v44, v45
v_fma_f32 v213, v94, v201, v133
v_exp_f32 v214, v10
v_exp_f32 v215, v44
s_nop 0
v_add_f32 v45, v214, v215
v_mov_b32 v46, v45
v_mul_f32 v214, v214, v254
v_cvt_pk_bf16_f32 v156, v210, v211
v_permlane16_swap_b32 v45, v46
v_add_f32 v45, v45, v46
v_mov_b32 v46, v45
s_waitcnt lgkmcnt(0)
s_barrier
v_mul_f32 v215, v215, v255
v_cvt_pk_bf16_f32 v157, v212, v213
v_permlane32_swap_b32 v45, v46
v_add_f32 v45, v45, v46
v_fma_f32 v210, v86, v23, v122
v_rcp_f32 v45, v45
s_nop 0
v_mul_f32 v214, v214, v45
v_mul_f32 v215, v215, v45
v_cvt_pk_bf16_f32 v214, v214, v214
v_cvt_pk_bf16_f32 v215, v215, v215
v_and_b32 v196, v214, v40
v_and_b32 v197, v215, v41
v_and_b32 v194, v214, v42
v_and_b32 v195, v215, v43
s_nop 1
v_mfma_f32_16x16x32_bf16 v[190:193], v[154:157], v[194:197], 0
v_mfma_f32_16x16x32_bf16 v[186:189], v[158:161], v[194:197], 0
s_nop 6
v_cvt_pk_bf16_f32 v104, v190, v191
v_cvt_pk_bf16_f32 v105, v192, v193
ds_write_b64 v18, v[104:105]
v_cvt_pk_bf16_f32 v214, v188, v189
v_fma_f32 v211, v87, v23, v123
ds_write_b32 v19, v214
v_fma_f32 v212, v88, v23, v124
v_fma_f32 v213, v89, v23, v125
v_cvt_pk_bf16_f32 v142, v210, v211
v_cvt_pk_bf16_f32 v143, v212, v213
s_waitcnt vmcnt(0)
s_waitcnt lgkmcnt(0)
s_barrier
s_mov_b64 exec, 1
global_store_dword v252, v48, s[38:39] sc1
s_mov_b64 exec, -1
s_waitcnt vmcnt(1)
s_barrier
v_add_u32 v48, 8, v48
v_mbcnt_lo_u32_b32 v252, -1, 0
v_mbcnt_hi_u32_b32 v252, -1, v252
v_add_lshl_u32 v252, v252, 0, 7
v_bfi_b32 v252, s69, v252, v47
s_mov_b64 exec, 0xff
global_store_dword v252, v48, s[38:39] sc1
s_mov_b64 exec, -1
s_cmp_lg_u32 s53, 0
s_cbranch_scc1 .Lqa_n101
v_add_lshl_u32 v253, v50, 0, 7
v_bfi_b32 v253, s69, v253, v49
global_load_dword v53, v253, s[38:39] sc1
s_waitcnt vmcnt(0)
v_sub_u32 v53, v53, v50
v_cmp_lt_u32 vcc, 0x7f, v53
s_cbranch_vccnz .Lqa_sp102
.Lqa_rt103:
v_add_u32 v49, 118784, v49
v_add_lshl_u32 v253, v50, 0, 7
v_bfi_b32 v253, s69, v253, v49
global_load_dword v53, v253, s[38:39] sc1
s_waitcnt vmcnt(0)
v_sub_u32 v53, v53, v50
v_cmp_lt_u32 vcc, 0x7f, v53
s_cbranch_vccnz .Lqa_sp104
.Lqa_rt105:
v_add_u32 v49, 118784, v49
v_add_lshl_u32 v253, v50, 0, 7
v_bfi_b32 v253, s69, v253, v49
global_load_dword v53, v253, s[38:39] sc1
s_waitcnt vmcnt(0)
v_sub_u32 v53, v53, v50
v_cmp_lt_u32 vcc, 0x7f, v53
s_cbranch_vccnz .Lqa_sp106
.Lqa_rt107:
v_add_u32 v49, 118784, v49
v_add_lshl_u32 v253, v50, 0, 7
v_bfi_b32 v253, s69, v253, v49
global_load_dword v53, v253, s[38:39] sc1
s_waitcnt vmcnt(0)
v_sub_u32 v53, v53, v50
v_cmp_lt_u32 vcc, 0x7f, v53
s_cbranch_vccnz .Lqa_sp108
.Lqa_rt109:
v_add_u32 v49, 118784, v49
s_load_dword s75, s[38:39], 0x0
s_waitcnt vmcnt(0) lgkmcnt(0)
s_add_u32 s75, s75, 1
v_mov_b32 v53, s75
v_mov_b32 v49, 0
global_store_dword v49, v53, s[38:39] sc1
.Lqa_n101:
s_endpgm
.Lqa_hk6:
s_setpc_b64 s[90:91]
.Lqa_k16:
s_branch .Lqa_k17
.Lqa_e8:
s_add_u32 s42, s42, s50
s_addc_u32 s43, s43, 0
s_mov_b64 s[76:77], -1
s_mov_b64 s[46:47], s[44:45]
s_mov_b32 s73, 17
s_add_u32 s90, s90, .Lqa_e9-.Lqa_e8
s_addc_u32 s91, s91, 0
s_branch .Lqa_hr7
.Lqa_k17:
s_branch .Lqa_k18
.Lqa_e9:
s_mov_b32 s46, -18432
s_mov_b32 s47, -1
v_or_b32 v234, s52, v234
v_or_b32 v235, s52, v235
v_or_b32 v236, s52, v236
v_or_b32 v237, s52, v237
s_mov_b32 s73, 16
s_add_u32 s90, s90, .Lqa_e10-.Lqa_e9
s_addc_u32 s91, s91, 0
s_branch .Lqa_hr7
.Lqa_k18:
s_branch .Lqa_k19
.Lqa_e10:
v_mov_b32 v234, v32
v_mov_b32 v235, v33
v_mov_b32 v236, v34
v_mov_b32 v237, v35
s_mov_b32 s73, 15
s_add_u32 s90, s90, .Lqa_e11-.Lqa_e10
s_addc_u32 s91, s91, 0
s_branch .Lqa_hr7
.Lqa_k19:
s_branch .Lqa_k20
.Lqa_e11:
v_subrev_u32 v16, 18432, v16
s_mov_b32 s73, 3
s_add_u32 s90, s90, .Lqa_e12-.Lqa_e11
s_addc_u32 s91, s91, 0
s_branch .Lqa_hr7
.Lqa_k20:
s_branch .Lqa_k21
.Lqa_e12:
s_mov_b32 s73, 2
s_add_u32 s90, s90, .Lqa_e13-.Lqa_e12
s_addc_u32 s91, s91, 0
s_branch .Lqa_hr7
.Lqa_k21:
s_branch .Lqa_k22
.Lqa_e13:
s_add_u32 s46, s46, s50
s_addc_u32 s47, s47, 0
s_mov_b32 s73, 1
s_add_u32 s90, s90, .Lqa_e14-.Lqa_e13
s_addc_u32 s91, s91, 0
s_branch .Lqa_hr7
.Lqa_k22:
s_branch .Lqa_k23
.Lqa_e14:
s_add_u32 s42, s42, 73728
s_addc_u32 s43, s43, 0
s_mov_b32 s73, 0
s_add_u32 s90, s90, .Lqa_e15-.Lqa_e14
s_addc_u32 s91, s91, 0
s_branch .Lqa_hr7
.Lqa_k23:
s_branch .Lqa_bk24
.Lqa_e15:
v_or_b32 v32, s52, v32
v_or_b32 v33, s52, v33
v_or_b32 v34, s52, v34
v_or_b32 v35, s52, v35
s_mov_b32 s73, 999
s_nop 0
s_addc_u32 s91, s91, 0
s_branch .Lqa_hr7
.Lqa_hk29:
s_setpc_b64 s[90:91]
.Lqa_k39:
s_branch .Lqa_k40
.Lqa_e31:
s_add_u32 s42, s42, s50
s_addc_u32 s43, s43, 0
s_mov_b64 s[76:77], -1
s_mov_b64 s[46:47], s[44:45]
s_mov_b32 s73, 17
s_add_u32 s90, s90, .Lqa_e32-.Lqa_e31
s_addc_u32 s91, s91, 0
s_branch .Lqa_hr30
.Lqa_k40:
s_branch .Lqa_k41
.Lqa_e32:
s_mov_b32 s46, -18432
s_mov_b32 s47, -1
v_or_b32 v234, s52, v234
v_or_b32 v235, s52, v235
v_or_b32 v236, s52, v236
v_or_b32 v237, s52, v237
s_mov_b32 s73, 16
s_add_u32 s90, s90, .Lqa_e33-.Lqa_e32
s_addc_u32 s91, s91, 0
s_branch .Lqa_hr30
.Lqa_k41:
s_branch .Lqa_k42
.Lqa_e33:
v_mov_b32 v234, v32
v_mov_b32 v235, v33
v_mov_b32 v236, v34
v_mov_b32 v237, v35
s_mov_b32 s73, 15
s_add_u32 s90, s90, .Lqa_e34-.Lqa_e33
s_addc_u32 s91, s91, 0
s_branch .Lqa_hr30
.Lqa_k42:
s_branch .Lqa_k43
.Lqa_e34:
v_subrev_u32 v16, 18432, v16
s_mov_b32 s73, 3
s_add_u32 s90, s90, .Lqa_e35-.Lqa_e34
)"
R"(s_addc_u32 s91, s91, 0
s_branch .Lqa_hr30
.Lqa_k43:
s_branch .Lqa_k44
.Lqa_e35:
s_mov_b32 s73, 2
s_add_u32 s90, s90, .Lqa_e36-.Lqa_e35
s_addc_u32 s91, s91, 0
s_branch .Lqa_hr30
.Lqa_k44:
s_branch .Lqa_k45
.Lqa_e36:
s_add_u32 s46, s46, s50
s_addc_u32 s47, s47, 0
s_mov_b32 s73, 1
s_add_u32 s90, s90, .Lqa_e37-.Lqa_e36
s_addc_u32 s91, s91, 0
s_branch .Lqa_hr30
.Lqa_k45:
s_branch .Lqa_k46
.Lqa_e37:
s_add_u32 s42, s42, 73728
s_addc_u32 s43, s43, 0
s_mov_b32 s73, 0
s_add_u32 s90, s90, .Lqa_e38-.Lqa_e37
s_addc_u32 s91, s91, 0
s_branch .Lqa_hr30
.Lqa_k46:
s_branch .Lqa_bk47
.Lqa_e38:
v_or_b32 v32, s52, v32
v_or_b32 v33, s52, v33
v_or_b32 v34, s52, v34
v_or_b32 v35, s52, v35
s_mov_b32 s73, 999
s_nop 0
s_addc_u32 s91, s91, 0
s_branch .Lqa_hr30
.Lqa_sp54:
s_sleep 1
v_add_lshl_u32 v253, v46, 0, 7
v_bfi_b32 v253, s69, v253, v44
global_load_dword v53, v253, s[38:39] sc1
s_add_u32 s71, s71, 1
s_waitcnt vmcnt(0)
v_sub_u32 v53, v53, v46
v_cmp_lt_u32 vcc, 0x7f, v53
s_and_b64 vcc, vcc, s[76:77]
s_cbranch_vccz .Lqa_rt55
s_cmp_lt_u32 s71, 3000000
s_cbranch_scc1 .Lqa_sp54
s_branch .Lqa_rt55
.Lqa_sp56:
s_sleep 1
v_add_lshl_u32 v253, v46, 0, 7
v_bfi_b32 v253, s69, v253, v44
global_load_dword v254, v253, s[38:39] sc1
s_add_u32 s71, s71, 1
s_waitcnt vmcnt(0)
v_sub_u32 v254, v254, v46
v_cmp_lt_u32 vcc, 0x7f, v254
s_and_b64 vcc, vcc, s[76:77]
s_cbranch_vccz .Lqa_rt57
s_cmp_lt_u32 s71, 3000000
s_cbranch_scc1 .Lqa_sp56
s_branch .Lqa_rt57
.Lqa_hk52:
s_setpc_b64 s[90:91]
.Lqa_k66:
s_branch .Lqa_k67
.Lqa_e58:
s_add_u32 s42, s42, s50
s_addc_u32 s43, s43, 0
s_mov_b64 s[76:77], -1
s_mov_b64 s[46:47], s[44:45]
s_mov_b32 s73, 17
s_add_u32 s90, s90, .Lqa_e59-.Lqa_e58
s_addc_u32 s91, s91, 0
s_branch .Lqa_hr53
.Lqa_k67:
s_branch .Lqa_k68
.Lqa_e59:
s_mov_b32 s46, -18432
s_mov_b32 s47, -1
v_or_b32 v202, s52, v202
v_or_b32 v203, s52, v203
v_or_b32 v204, s52, v204
v_or_b32 v205, s52, v205
s_mov_b32 s73, 16
s_add_u32 s90, s90, .Lqa_e60-.Lqa_e59
s_addc_u32 s91, s91, 0
s_branch .Lqa_hr53
.Lqa_k68:
s_branch .Lqa_k69
.Lqa_e60:
v_mov_b32 v202, v32
v_mov_b32 v203, v33
v_mov_b32 v204, v34
v_mov_b32 v205, v35
s_mov_b32 s73, 15
s_add_u32 s90, s90, .Lqa_e61-.Lqa_e60
s_addc_u32 s91, s91, 0
s_branch .Lqa_hr53
.Lqa_k69:
s_branch .Lqa_k70
.Lqa_e61:
v_subrev_u32 v16, 18432, v16
s_mov_b32 s73, 3
s_add_u32 s90, s90, .Lqa_e62-.Lqa_e61
s_addc_u32 s91, s91, 0
s_branch .Lqa_hr53
.Lqa_k70:
s_branch .Lqa_k71
.Lqa_e62:
v_add_u32 v45, s51, v45
s_mov_b32 s73, 2
s_add_u32 s90, s90, .Lqa_e63-.Lqa_e62
s_addc_u32 s91, s91, 0
s_branch .Lqa_hr53
.Lqa_k71:
s_branch .Lqa_k72
.Lqa_e63:
v_add_u32 v44, s51, v44
v_add_u32 v46, -32, v46
s_add_u32 s46, s46, s50
s_addc_u32 s47, s47, 0
s_mov_b32 s73, 1
s_add_u32 s90, s90, .Lqa_e64-.Lqa_e63
s_addc_u32 s91, s91, 0
s_branch .Lqa_hr53
.Lqa_k72:
s_branch .Lqa_k73
.Lqa_e64:
s_add_u32 s42, s42, 73728
s_addc_u32 s43, s43, 0
s_mov_b32 s73, 0
s_add_u32 s90, s90, .Lqa_e65-.Lqa_e64
s_addc_u32 s91, s91, 0
s_branch .Lqa_hr53
.Lqa_k73:
s_branch .Lqa_bk74
.Lqa_e65:
v_or_b32 v32, s52, v32
v_or_b32 v33, s52, v33
v_or_b32 v34, s52, v34
v_or_b32 v35, s52, v35
s_mov_b32 s73, 999
s_nop 0
s_addc_u32 s91, s91, 0
s_branch .Lqa_hr53
.Lqa_hk79:
s_setpc_b64 s[90:91]
.Lqa_k89:
s_branch .Lqa_k90
.Lqa_e81:
s_add_u32 s42, s42, s50
s_addc_u32 s43, s43, 0
s_mov_b64 s[76:77], -1
s_mov_b64 s[46:47], s[44:45]
s_mov_b32 s73, 17
s_add_u32 s90, s90, .Lqa_e82-.Lqa_e81
s_addc_u32 s91, s91, 0
s_branch .Lqa_hr80
.Lqa_k90:
s_branch .Lqa_k91
.Lqa_e82:
s_mov_b32 s46, -18432
s_mov_b32 s47, -1
v_or_b32 v206, s52, v206
v_or_b32 v207, s52, v207
v_or_b32 v208, s52, v208
v_or_b32 v209, s52, v209
s_mov_b32 s73, 16
s_add_u32 s90, s90, .Lqa_e83-.Lqa_e82
s_addc_u32 s91, s91, 0
s_branch .Lqa_hr80
.Lqa_k91:
s_branch .Lqa_k92
.Lqa_e83:
v_mov_b32 v206, v32
v_mov_b32 v207, v33
v_mov_b32 v208, v34
v_mov_b32 v209, v35
s_mov_b32 s73, 15
s_add_u32 s90, s90, .Lqa_e84-.Lqa_e83
s_addc_u32 s91, s91, 0
s_branch .Lqa_hr80
.Lqa_k92:
s_branch .Lqa_k93
.Lqa_e84:
v_add_u32 v48, -1, v48
v_add_lshl_u32 v252, v48, 0, 7
v_bfi_b32 v252, s69, v252, v47
s_mov_b32 s73, 3
s_add_u32 s90, s90, .Lqa_e85-.Lqa_e84
s_addc_u32 s91, s91, 0
s_branch .Lqa_hr80
.Lqa_k93:
s_branch .Lqa_k94
.Lqa_e85:
s_mov_b32 s73, 2
s_add_u32 s90, s90, .Lqa_e86-.Lqa_e85
s_addc_u32 s91, s91, 0
s_branch .Lqa_hr80
.Lqa_k94:
s_branch .Lqa_k95
.Lqa_e86:
s_add_u32 s46, s46, s50
s_addc_u32 s47, s47, 0
s_mov_b32 s73, 1
s_add_u32 s90, s90, .Lqa_e87-.Lqa_e86
s_addc_u32 s91, s91, 0
s_branch .Lqa_hr80
.Lqa_k95:
s_branch .Lqa_k96
.Lqa_e87:
s_add_u32 s42, s42, 73728
s_addc_u32 s43, s43, 0
s_mov_b32 s73, 0
s_add_u32 s90, s90, .Lqa_e88-.Lqa_e87
s_addc_u32 s91, s91, 0
s_branch .Lqa_hr80
.Lqa_k96:
s_branch .Lqa_bk97
.Lqa_e88:
v_or_b32 v32, s52, v32
v_or_b32 v33, s52, v33
v_or_b32 v34, s52, v34
v_or_b32 v35, s52, v35
s_mov_b32 s73, 999
s_nop 0
s_addc_u32 s91, s91, 0
s_branch .Lqa_hr80
.Lqa_sp102:
s_sleep 1
v_add_lshl_u32 v253, v50, 0, 7
v_bfi_b32 v253, s69, v253, v49
global_load_dword v53, v253, s[38:39] sc1
s_add_u32 s71, s71, 1
s_waitcnt vmcnt(0)
v_sub_u32 v53, v53, v50
v_cmp_lt_u32 vcc, 0x7f, v53
s_cbranch_vccz .Lqa_rt103
s_cmp_lt_u32 s71, 3000000
s_cbranch_scc1 .Lqa_sp102
s_branch .Lqa_rt103
.Lqa_sp104:
s_sleep 1
v_add_lshl_u32 v253, v50, 0, 7
v_bfi_b32 v253, s69, v253, v49
global_load_dword v53, v253, s[38:39] sc1
s_add_u32 s71, s71, 1
s_waitcnt vmcnt(0)
v_sub_u32 v53, v53, v50
v_cmp_lt_u32 vcc, 0x7f, v53
s_cbranch_vccz .Lqa_rt105
s_cmp_lt_u32 s71, 3000000
s_cbranch_scc1 .Lqa_sp104
s_branch .Lqa_rt105
.Lqa_sp106:
s_sleep 1
v_add_lshl_u32 v253, v50, 0, 7
v_bfi_b32 v253, s69, v253, v49
global_load_dword v53, v253, s[38:39] sc1
s_add_u32 s71, s71, 1
s_waitcnt vmcnt(0)
v_sub_u32 v53, v53, v50
v_cmp_lt_u32 vcc, 0x7f, v53
s_cbranch_vccz .Lqa_rt107
s_cmp_lt_u32 s71, 3000000
s_cbranch_scc1 .Lqa_sp106
s_branch .Lqa_rt107
.Lqa_sp108:
s_sleep 1
v_add_lshl_u32 v253, v50, 0, 7
v_bfi_b32 v253, s69, v253, v49
global_load_dword v53, v253, s[38:39] sc1
s_add_u32 s71, s71, 1
s_waitcnt vmcnt(0)
v_sub_u32 v53, v53, v50
v_cmp_lt_u32 vcc, 0x7f, v53
s_cbranch_vccz .Lqa_rt109
s_cmp_lt_u32 s71, 3000000
s_cbranch_scc1 .Lqa_sp108
s_branch .Lqa_rt109
.Le0:
)"
    :: "{v[0:31]}"(va), "{v[32:63]}"(vb), "{s[36:51]}"(sa), "{s[52:67]}"(sb) : "v255", "a255", "s99", "vcc", "memory");
    return;
  }
  if (wave == 2) {
    const unsigned o = min((lane & 7) * 1048576u, 5308412u);
    touch(wg + o); touch(wu + o); touch(wd + o); touch(out + min(o, 2359292u));
    touch(ws + min(lane * 2097152u, WS_END - 4));
    if (blockIdx.x - 64 < 464 * FL / 8192) touch(ws + WS_FL + ((blockIdx.x - 64) * 64 + lane) * 128);
  }
  if (ev) touch(wo + min((jj - 8) * 216 + threadIdx.x, 5183u) * 128);
  if (!ev && wave != 2) touch(x0 + min((xh * 32 + jj) * 144 + min(wave, 2u) * 64 + lane, 18431u) * 128);
  if (ev && jj < 10) {
    const unsigned wo_rho = xc >> 1, wo_cw = jj & 1;
    const unsigned r = lane & 15, g = lane >> 4, q = r >> 2, p = lane & 3;
    const unsigned rho = wo_rho, cw = wo_cw, row0 = rho * RP + PAD, pc0 = wave * 5;
    auto wv = [&](unsigned j) { const unsigned b = j * 1024 + lane * 16, rw = b / 576; return rw * 576 + b + 4096 - (j & 7) * 1024; };
    u32x32 va; u32x16 sa;
    va[0] = wv(0);
    va[1] = wv(1);
    va[2] = wv(2);
    va[3] = wv(3);
    va[4] = wv(4);
    va[5] = wv(5);
    va[6] = wv(6);
    va[7] = wv(7);
    va[8] = wv(8);
    va[9] = (8 * g + q) * 576 + cw * 64 + wave * 128 + p * 16;
    va[10] = (8 * g + q) * 576 + (cw ? 0 : 512) + p * 16 + (wave >> 1) * 8;
    va[11] = WS_O + (row0 - 2) * XT + pc0 * 1024 + 2048 + lane * 16;
    va[12] = WS_O + (row0 + 63) * XT + pc0 * 1024 + 2048 + lane * 16;
    va[13] = WS_X1 + (row0 - 5) * X1P + (cw * 10 + wave * 2) * 1024 + lane * 16;
    va[14] = WS_X1 + (row0 - 5) * X1P + (8 + cw) * 1024 + lane * 16;
    va[15] = WS_X1 + (row0 - 7) * X1P + XT + cw * 128 + r * 8;
    va[16] = ((rho * 32 + 31) * 16 + 0 + (lane >> 3)) * 1152 + (cw * 5 + wave) * 128 + (((lane ^ (lane >> 3)) & 7) << 4) - 0;
    va[17] = ((rho * 32 + 31) * 16 + 8 + (lane >> 3)) * 1152 + (cw * 5 + wave) * 128 + (((lane ^ (lane >> 3)) & 7) << 4) - 1024;
    va[18] = ((rho * 32 + 31) * 16 + r) * 1152 + (8 + cw) * 64 + g * 16;
    va[19] = 47104 + wave * 2048 + r * 128 + (((0 + g) ^ (r & 7)) << 4);
    va[20] = 47104 + wave * 2048 + r * 128 + (((4 + g) ^ (r & 7)) << 4);
    va[21] = WS_FL + rho * FR + FO + (lane & 7) * FL;
    va[22] = 0u - 2;
    va[23] = WS_FL + rho * FR + FX1 + cw * FL;
    va[24] = 0u - 6;
    va[25] = lane * 16;
    va[26] = lane * 16;
    va[27] = lane * 8;
    sa[0] = 4 + wave; sa[2] = (unsigned)(unsigned long long)ws; sa[3] = (unsigned)((unsigned long long)ws >> 32);
    sa[4] = uni((unsigned)(unsigned long long)(wo + wave * 16 * 1152 + cw * 576));
    sa[5] = uni((unsigned)((unsigned long long)(wo + wave * 16 * 1152 + cw * 576) >> 32));
    sa[6] = uni((unsigned)(unsigned long long)(x0));
    sa[7] = uni((unsigned)((unsigned long long)(x0) >> 32));
    sa[8] = uni((unsigned)(unsigned long long)(x0));
    sa[9] = uni((unsigned)((unsigned long long)(x0) >> 32));
    sa[10] = uni((unsigned)(unsigned long long)(ws + WS_X2));
    sa[11] = uni((unsigned)((unsigned long long)(ws + WS_X2) >> 32));
    sa[12] = uni(wave * 9216);
    sa[13] = uni(pc0 * 1024 + 2048);
    asm volatile(
R"(.Lb1:
s_movk_i32 s69, 0x380
s_mov_b32 s71, 0
s_mov_b32 s74, 0
s_load_dword s37, s[38:39], 0x0
s_nop 1
s_and_b32 s75, s36, 3
s_cmp_eq_u32 s75, 1
s_cbranch_scc1 .Lwo_w2
s_cmp_eq_u32 s75, 2
s_cbranch_scc1 .Lwo_w3
s_cmp_eq_u32 s75, 3
s_cbranch_scc1 .Lwo_w4
.Lwo_w1:
s_add_u32 m0, s48, 4096
s_nop 0
global_load_lds_dwordx4 v0, s[40:41] offset:-4096
global_load_lds_dwordx4 v1, s[40:41] offset:-3072
global_load_lds_dwordx4 v2, s[40:41] offset:-2048
global_load_lds_dwordx4 v3, s[40:41] offset:-1024
global_load_lds_dwordx4 v4, s[40:41] offset:0
global_load_lds_dwordx4 v5, s[40:41] offset:1024
global_load_lds_dwordx4 v6, s[40:41] offset:2048
global_load_lds_dwordx4 v7, s[40:41] offset:3072
s_add_u32 m0, s48, 12288
s_nop 0
global_load_lds_dwordx4 v8, s[40:41] offset:-4096
s_add_u32 s40, s40, 73728
s_addc_u32 s41, s41, 0
s_add_u32 m0, s48, 40960
s_nop 0
global_load_lds_dwordx4 v0, s[40:41] offset:-4096
global_load_lds_dwordx4 v1, s[40:41] offset:-3072
global_load_lds_dwordx4 v2, s[40:41] offset:-2048
global_load_lds_dwordx4 v3, s[40:41] offset:-1024
global_load_lds_dwordx4 v4, s[40:41] offset:0
global_load_lds_dwordx4 v5, s[40:41] offset:1024
global_load_lds_dwordx4 v6, s[40:41] offset:2048
global_load_lds_dwordx4 v7, s[40:41] offset:3072
s_add_u32 m0, s48, 49152
s_nop 0
global_load_lds_dwordx4 v8, s[40:41] offset:-4096
s_add_u32 s40, s40, 73728
s_addc_u32 s41, s41, 0
s_add_u32 m0, s48, 77824
s_nop 0
global_load_lds_dwordx4 v0, s[40:41] offset:-4096
global_load_lds_dwordx4 v1, s[40:41] offset:-3072
global_load_lds_dwordx4 v2, s[40:41] offset:-2048
global_load_lds_dwordx4 v3, s[40:41] offset:-1024
global_load_lds_dwordx4 v4, s[40:41] offset:0
global_load_lds_dwordx4 v5, s[40:41] offset:1024
global_load_lds_dwordx4 v6, s[40:41] offset:2048
global_load_lds_dwordx4 v7, s[40:41] offset:3072
s_add_u32 m0, s48, 86016
s_nop 0
global_load_lds_dwordx4 v8, s[40:41] offset:-4096
s_add_u32 s40, s40, 73728
s_addc_u32 s41, s41, 0
v_add_u32 v204, 0x10000, v9
v_add_u32 v205, 0x20000, v9
v_add_u32 v206, 0x10000, v10
v_add_u32 v207, 0x20000, v10
s_waitcnt lgkmcnt(0)
s_mul_i32 s37, s37, 0x9E3779B1
s_add_u32 s37, s37, 0x7F4A7C15
v_add_u32 v22, s37, v22
v_add_u32 v24, s37, v24
s_waitcnt vmcnt(18)
s_barrier
s_add_u32 m0, s48, 114688
ds_read_b64_tr_b16 a[0:1], v9 offset:0
ds_read_b64_tr_b16 a[2:3], v9 offset:2304
global_load_lds_dwordx4 v0, s[40:41] offset:-4096
ds_read_b64_tr_b16 a[4:5], v9 offset:8
ds_read_b64_tr_b16 a[6:7], v9 offset:2312
global_load_lds_dwordx4 v1, s[40:41] offset:-3072
ds_read_b64_tr_b16 a[8:9], v9 offset:64
global_load_lds_dwordx4 v2, s[40:41] offset:-2048
ds_read_b64_tr_b16 a[10:11], v9 offset:2368
ds_read_b64_tr_b16 a[12:13], v9 offset:72
global_load_lds_dwordx4 v3, s[40:41] offset:-1024
ds_read_b64_tr_b16 a[14:15], v9 offset:2376
global_load_lds_dwordx4 v4, s[40:41] offset:0
ds_read_b64_tr_b16 v[60:61], v10 offset:0
ds_read_b64_tr_b16 v[62:63], v10 offset:2304
global_load_lds_dwordx4 v5, s[40:41] offset:1024
ds_read_b64_tr_b16 a[16:17], v9 offset:18432
global_load_lds_dwordx4 v6, s[40:41] offset:2048
ds_read_b64_tr_b16 a[18:19], v9 offset:20736
ds_read_b64_tr_b16 a[20:21], v9 offset:18440
global_load_lds_dwordx4 v7, s[40:41] offset:3072
ds_read_b64_tr_b16 a[22:23], v9 offset:20744
s_add_u32 m0, s48, 122880
ds_read_b64_tr_b16 a[24:25], v9 offset:18496
ds_read_b64_tr_b16 a[26:27], v9 offset:20800
global_load_lds_dwordx4 v8, s[40:41] offset:-4096
ds_read_b64_tr_b16 a[28:29], v9 offset:18504
s_add_u32 s40, s40, 73728
ds_read_b64_tr_b16 a[30:31], v9 offset:20808
ds_read_b64_tr_b16 v[64:65], v10 offset:18432
s_addc_u32 s41, s41, 0
ds_read_b64_tr_b16 v[66:67], v10 offset:20736
s_waitcnt vmcnt(18) lgkmcnt(0)
s_barrier
s_add_u32 m0, s48, 4096
ds_read_b64_tr_b16 a[32:33], v9 offset:36864
ds_read_b64_tr_b16 a[34:35], v9 offset:39168
global_load_lds_dwordx4 v0, s[40:41] offset:-4096
ds_read_b64_tr_b16 a[36:37], v9 offset:36872
ds_read_b64_tr_b16 a[38:39], v9 offset:39176
global_load_lds_dwordx4 v1, s[40:41] offset:-3072
ds_read_b64_tr_b16 a[40:41], v9 offset:36928
global_load_lds_dwordx4 v2, s[40:41] offset:-2048
ds_read_b64_tr_b16 a[42:43], v9 offset:39232
ds_read_b64_tr_b16 a[44:45], v9 offset:36936
global_load_lds_dwordx4 v3, s[40:41] offset:-1024
ds_read_b64_tr_b16 a[46:47], v9 offset:39240
global_load_lds_dwordx4 v4, s[40:41] offset:0
ds_read_b64_tr_b16 v[68:69], v10 offset:36864
ds_read_b64_tr_b16 v[70:71], v10 offset:39168
global_load_lds_dwordx4 v5, s[40:41] offset:1024
ds_read_b64_tr_b16 a[48:49], v9 offset:55296
global_load_lds_dwordx4 v6, s[40:41] offset:2048
ds_read_b64_tr_b16 a[50:51], v9 offset:57600
ds_read_b64_tr_b16 a[52:53], v9 offset:55304
global_load_lds_dwordx4 v7, s[40:41] offset:3072
ds_read_b64_tr_b16 a[54:55], v9 offset:57608
s_add_u32 m0, s48, 12288
ds_read_b64_tr_b16 a[56:57], v9 offset:55360
ds_read_b64_tr_b16 a[58:59], v9 offset:57664
global_load_lds_dwordx4 v8, s[40:41] offset:-4096
ds_read_b64_tr_b16 a[60:61], v9 offset:55368
s_add_u32 s40, s40, 73728
ds_read_b64_tr_b16 a[62:63], v9 offset:57672
ds_read_b64_tr_b16 v[72:73], v10 offset:55296
s_addc_u32 s41, s41, 0
ds_read_b64_tr_b16 v[74:75], v10 offset:57600
s_waitcnt vmcnt(18) lgkmcnt(0)
s_barrier
s_add_u32 m0, s48, 40960
ds_read_b64_tr_b16 a[64:65], v204 offset:8192
ds_read_b64_tr_b16 a[66:67], v204 offset:10496
global_load_lds_dwordx4 v0, s[40:41] offset:-4096
ds_read_b64_tr_b16 a[68:69], v204 offset:8200
ds_read_b64_tr_b16 a[70:71], v204 offset:10504
global_load_lds_dwordx4 v1, s[40:41] offset:-3072
ds_read_b64_tr_b16 a[72:73], v204 offset:8256
global_load_lds_dwordx4 v2, s[40:41] offset:-2048
ds_read_b64_tr_b16 a[74:75], v204 offset:10560
ds_read_b64_tr_b16 a[76:77], v204 offset:8264
global_load_lds_dwordx4 v3, s[40:41] offset:-1024
ds_read_b64_tr_b16 a[78:79], v204 offset:10568
global_load_lds_dwordx4 v4, s[40:41] offset:0
ds_read_b64_tr_b16 v[76:77], v206 offset:8192
ds_read_b64_tr_b16 v[78:79], v206 offset:10496
global_load_lds_dwordx4 v5, s[40:41] offset:1024
ds_read_b64_tr_b16 a[80:81], v204 offset:26624
global_load_lds_dwordx4 v6, s[40:41] offset:2048
ds_read_b64_tr_b16 a[82:83], v204 offset:28928
ds_read_b64_tr_b16 a[84:85], v204 offset:26632
global_load_lds_dwordx4 v7, s[40:41] offset:3072
ds_read_b64_tr_b16 a[86:87], v204 offset:28936
s_add_u32 m0, s48, 49152
ds_read_b64_tr_b16 a[88:89], v204 offset:26688
ds_read_b64_tr_b16 a[90:91], v204 offset:28992
global_load_lds_dwordx4 v8, s[40:41] offset:-4096
ds_read_b64_tr_b16 a[92:93], v204 offset:26696
s_add_u32 s40, s40, 73728
ds_read_b64_tr_b16 a[94:95], v204 offset:29000
ds_read_b64_tr_b16 v[80:81], v206 offset:26624
s_addc_u32 s41, s41, 0
ds_read_b64_tr_b16 v[82:83], v206 offset:28928
s_waitcnt vmcnt(18) lgkmcnt(0)
s_barrier
s_add_u32 m0, s48, 77824
ds_read_b64_tr_b16 a[96:97], v204 offset:45056
ds_read_b64_tr_b16 a[98:99], v204 offset:47360
global_load_lds_dwordx4 v0, s[40:41] offset:-4096
ds_read_b64_tr_b16 a[100:101], v204 offset:45064
ds_read_b64_tr_b16 a[102:103], v204 offset:47368
global_load_lds_dwordx4 v1, s[40:41] offset:-3072
ds_read_b64_tr_b16 a[104:105], v204 offset:45120
global_load_lds_dwordx4 v2, s[40:41] offset:-2048
ds_read_b64_tr_b16 a[106:107], v204 offset:47424
ds_read_b64_tr_b16 a[108:109], v204 offset:45128
global_load_lds_dwordx4 v3, s[40:41] offset:-1024
ds_read_b64_tr_b16 a[110:111], v204 offset:47432
global_load_lds_dwordx4 v4, s[40:41] offset:0
ds_read_b64_tr_b16 v[84:85], v206 offset:45056
ds_read_b64_tr_b16 v[86:87], v206 offset:47360
global_load_lds_dwordx4 v5, s[40:41] offset:1024
ds_read_b64_tr_b16 a[112:113], v204 offset:63488
global_load_lds_dwordx4 v6, s[40:41] offset:2048
ds_read_b64_tr_b16 a[114:115], v205 offset:256
ds_read_b64_tr_b16 a[116:117], v204 offset:63496
global_load_lds_dwordx4 v7, s[40:41] offset:3072
ds_read_b64_tr_b16 a[118:119], v205 offset:264
s_add_u32 m0, s48, 86016
ds_read_b64_tr_b16 a[120:121], v204 offset:63552
ds_read_b64_tr_b16 a[122:123], v205 offset:320
global_load_lds_dwordx4 v8, s[40:41] offset:-4096
ds_read_b64_tr_b16 a[124:125], v204 offset:63560
s_add_u32 s40, s40, 73728
ds_read_b64_tr_b16 a[126:127], v205 offset:328
ds_read_b64_tr_b16 v[88:89], v206 offset:63488
s_addc_u32 s41, s41, 0
ds_read_b64_tr_b16 v[90:91], v207 offset:256
s_waitcnt vmcnt(18) lgkmcnt(0)
s_barrier
s_add_u32 m0, s48, 114688
ds_read_b64_tr_b16 a[128:129], v9 offset:0
ds_read_b64_tr_b16 a[130:131], v9 offset:2304
global_load_lds_dwordx4 v0, s[40:41] offset:-4096
ds_read_b64_tr_b16 a[132:133], v9 offset:8
global_load_lds_dwordx4 v1, s[40:41] offset:-3072
ds_read_b64_tr_b16 a[134:135], v9 offset:2312
ds_read_b64_tr_b16 a[136:137], v9 offset:64
global_load_lds_dwordx4 v2, s[40:41] offset:-2048
ds_read_b64_tr_b16 a[138:139], v9 offset:2368
global_load_lds_dwordx4 v3, s[40:41] offset:-1024
ds_read_b64_tr_b16 a[140:141], v9 offset:72
global_load_lds_dwordx4 v4, s[40:41] offset:0
ds_read_b64_tr_b16 a[142:143], v9 offset:2376
ds_read_b64_tr_b16 v[92:93], v10 offset:0
global_load_lds_dwordx4 v5, s[40:41] offset:1024
ds_read_b64_tr_b16 v[94:95], v10 offset:2304
global_load_lds_dwordx4 v6, s[40:41] offset:2048
ds_read_b64_tr_b16 a[144:145], v9 offset:18432
ds_read_b64_tr_b16 a[146:147], v9 offset:20736
global_load_lds_dwordx4 v7, s[40:41] offset:3072
ds_read_b64_tr_b16 a[148:149], v9 offset:18440
s_add_u32 m0, s48, 122880
ds_read_b64_tr_b16 a[150:151], v9 offset:20744
global_load_lds_dwordx4 v8, s[40:41] offset:-4096
ds_read_b64_tr_b16 a[152:153], v9 offset:18496
ds_read_b64_tr_b16 a[154:155], v9 offset:20800
s_add_u32 s40, s40, 73728
ds_read_b64_tr_b16 a[156:157], v9 offset:18504
s_addc_u32 s41, s41, 0
ds_read_b64_tr_b16 a[158:159], v9 offset:20808
s_waitcnt vmcnt(18) lgkmcnt(0)
s_barrier
s_add_u32 m0, s48, 4096
ds_read_b64_tr_b16 a[160:161], v9 offset:36864
ds_read_b64_tr_b16 a[162:163], v9 offset:39168
global_load_lds_dwordx4 v0, s[40:41] offset:-4096
ds_read_b64_tr_b16 a[164:165], v9 offset:36872
global_load_lds_dwordx4 v1, s[40:41] offset:-3072
ds_read_b64_tr_b16 a[166:167], v9 offset:39176
global_load_lds_dwordx4 v2, s[40:41] offset:-2048
ds_read_b64_tr_b16 a[168:169], v9 offset:36928
global_load_lds_dwordx4 v3, s[40:41] offset:-1024
ds_read_b64_tr_b16 a[170:171], v9 offset:39232
ds_read_b64_tr_b16 a[172:173], v9 offset:36936
global_load_lds_dwordx4 v4, s[40:41] offset:0
ds_read_b64_tr_b16 a[174:175], v9 offset:39240
global_load_lds_dwordx4 v5, s[40:41] offset:1024
ds_read_b64_tr_b16 a[176:177], v9 offset:55296
global_load_lds_dwordx4 v6, s[40:41] offset:2048
ds_read_b64_tr_b16 a[178:179], v9 offset:57600
global_load_lds_dwordx4 v7, s[40:41] offset:3072
ds_read_b64_tr_b16 a[180:181], v9 offset:55304
ds_read_b64_tr_b16 a[182:183], v9 offset:57608
s_add_u32 m0, s48, 12288
ds_read_b64_tr_b16 a[184:185], v9 offset:55360
global_load_lds_dwordx4 v8, s[40:41] offset:-4096
ds_read_b64_tr_b16 a[186:187], v9 offset:57664
s_sub_u32 s40, s40, 589824
ds_read_b64_tr_b16 a[188:189], v9 offset:55368
s_subb_u32 s41, s41, 0
ds_read_b64_tr_b16 a[190:191], v9 offset:57672
s_waitcnt vmcnt(18) lgkmcnt(0)
s_barrier
ds_read_b64_tr_b16 a[192:193], v204 offset:8192
ds_read_b64_tr_b16 a[194:195], v204 offset:10496
ds_read_b64_tr_b16 a[196:197], v204 offset:8200
ds_read_b64_tr_b16 a[198:199], v204 offset:10504
ds_read_b64_tr_b16 a[200:201], v204 offset:8256
ds_read_b64_tr_b16 a[202:203], v204 offset:10560
ds_read_b64_tr_b16 a[204:205], v204 offset:8264
ds_read_b64_tr_b16 a[206:207], v204 offset:10568
ds_read_b64_tr_b16 a[208:209], v204 offset:26624
ds_read_b64_tr_b16 a[210:211], v204 offset:28928
ds_read_b64_tr_b16 a[212:213], v204 offset:26632
ds_read_b64_tr_b16 a[214:215], v204 offset:28936
ds_read_b64_tr_b16 a[216:217], v204 offset:26688
ds_read_b64_tr_b16 a[218:219], v204 offset:28992
ds_read_b64_tr_b16 a[220:221], v204 offset:26696
ds_read_b64_tr_b16 a[222:223], v204 offset:29000
s_waitcnt vmcnt(9) lgkmcnt(0)
s_barrier
ds_read_b64_tr_b16 a[224:225], v204 offset:45056
ds_read_b64_tr_b16 a[226:227], v204 offset:47360
ds_read_b64_tr_b16 a[228:229], v204 offset:45064
ds_read_b64_tr_b16 a[230:231], v204 offset:47368
ds_read_b64_tr_b16 a[232:233], v204 offset:45120
ds_read_b64_tr_b16 a[234:235], v204 offset:47424
ds_read_b64_tr_b16 a[236:237], v204 offset:45128
ds_read_b64_tr_b16 a[238:239], v204 offset:47432
ds_read_b64_tr_b16 a[240:241], v204 offset:63488
ds_read_b64_tr_b16 a[242:243], v205 offset:256
ds_read_b64_tr_b16 a[244:245], v204 offset:63496
ds_read_b64_tr_b16 a[246:247], v205 offset:264
ds_read_b64_tr_b16 a[248:249], v204 offset:63552
ds_read_b64_tr_b16 a[250:251], v205 offset:320
ds_read_b64_tr_b16 a[252:253], v204 offset:63560
ds_read_b64_tr_b16 a[254:255], v205 offset:328
s_waitcnt vmcnt(0) lgkmcnt(0)
s_barrier
ds_read_b64_tr_b16 v[28:29], v9 offset:0
ds_read_b64_tr_b16 v[30:31], v9 offset:2304
ds_read_b64_tr_b16 v[32:33], v9 offset:8
ds_read_b64_tr_b16 v[34:35], v9 offset:2312
ds_read_b64_tr_b16 v[36:37], v9 offset:64
ds_read_b64_tr_b16 v[38:39], v9 offset:2368
ds_read_b64_tr_b16 v[40:41], v9 offset:72
ds_read_b64_tr_b16 v[42:43], v9 offset:2376
ds_read_b64_tr_b16 v[44:45], v9 offset:18432
ds_read_b64_tr_b16 v[46:47], v9 offset:20736
ds_read_b64_tr_b16 v[48:49], v9 offset:18440
ds_read_b64_tr_b16 v[50:51], v9 offset:20744
ds_read_b64_tr_b16 v[52:53], v9 offset:18496
ds_read_b64_tr_b16 v[54:55], v9 offset:20800
ds_read_b64_tr_b16 v[56:57], v9 offset:18504
ds_read_b64_tr_b16 v[58:59], v9 offset:20808
s_waitcnt lgkmcnt(0)
s_barrier
v_add_lshl_u32 v252, v24, 0, 7
v_bfi_b32 v252, s69, v252, v23
v_add_lshl_u32 v253, v22, 0, 7
v_bfi_b32 v253, s69, v253, v21
s_mov_b32 s92, 0x01010101
s_mov_b32 s93, 0x01010101
s_mov_b64 s[76:77], 0
s_add_u32 m0, s49, 18432
v_add_u32 v24, 1, v24
v_add_lshl_u32 v252, v24, 0, 7
v_bfi_b32 v252, s69, v252, v23
v_add_u32 v13, 18688, v13
s_xor_b32 s49, s49, 0x10000
s_mov_b32 m0, 47104
s_add_u32 m0, s49, 0
v_add_u32 v11, 18432, v11
v_add_u32 v24, 1, v24
v_add_lshl_u32 v252, v24, 0, 7
v_bfi_b32 v252, s69, v252, v23
s_mov_b32 m0, 55296
v_add_u32 v13, 18688, v13
s_add_u32 m0, s49, 18432
s_mov_b64 s[76:77], -1
s_mov_b32 s70, 33
s_waitcnt vmcnt(0) lgkmcnt(0)
s_nop 7
s_branch .Lwo_in5
.p2align 6
.Lwo_top6:
s_waitcnt lgkmcnt(2)
v_mfma_f32_16x16x32_bf16 v[96:99], a[0:3], v[132:135], v[96:99]
ds_read_b128 v[144:147], v25 offset:3072
v_mfma_f32_16x16x32_bf16 v[100:103], a[4:7], v[132:135], v[100:103]
v_mfma_f32_16x16x32_bf16 v[104:107], a[8:11], v[132:135], v[104:107]
s_waitcnt vmcnt(7)
s_barrier
v_mfma_f32_16x16x32_bf16 v[108:111], a[12:15], v[132:135], v[108:111]
v_cvt_pk_bf16_f32 v164, v112, v113
v_cvt_pk_bf16_f32 v165, v114, v115
s_mov_b64 exec, 1
global_store_dword v252, v24, s[38:39] sc1
s_mov_b64 exec, -1
v_mfma_f32_16x16x32_bf16 v[128:131], v[60:63], v[132:135], 0
s_waitcnt lgkmcnt(2)
v_cvt_pk_bf16_f32 v166, v116, v117
v_cvt_pk_bf16_f32 v167, v118, v119
v_add_u32 v24, 1, v24
v_add_lshl_u32 v252, v24, 0, 7
v_mfma_f32_16x16x32_bf16 v[96:99], a[16:19], v[136:139], v[96:99]
ds_read_b128 v[132:135], v25 offset:4096
v_bfi_b32 v252, s69, v252, v23
v_mfma_f32_16x16x32_bf16 v[100:103], a[20:23], v[136:139], v[100:103]
global_store_dwordx4 v13, v[164:167], s[38:39] offset:0 sc1
v_mfma_f32_16x16x32_bf16 v[104:107], a[24:27], v[136:139], v[104:107]
v_cvt_pk_bf16_f32 v168, v120, v121
v_cvt_pk_bf16_f32 v169, v122, v123
v_mfma_f32_16x16x32_bf16 v[108:111], a[28:31], v[136:139], v[108:111]
v_cvt_pk_bf16_f32 v170, v124, v125
v_cvt_pk_bf16_f32 v171, v126, v127
v_mfma_f32_16x16x32_bf16 v[128:131], v[64:67], v[136:139], v[128:131]
s_waitcnt lgkmcnt(2)
global_store_dwordx4 v13, v[168:171], s[38:39] offset:1024 sc1
v_mfma_f32_16x16x32_bf16 v[96:99], a[32:35], v[140:143], v[96:99]
s_and_b32 s75, s70, 15
ds_read_b128 v[136:139], v25 offset:5120
v_add_u32 v13, 18688, v13
v_mfma_f32_16x16x32_bf16 v[100:103], a[36:39], v[140:143], v[100:103]
s_cselect_b64 s[42:43], s[42:43], s[46:47]
v_add_f32 v254, v112, v113
v_mul_f32 v255, v112, v112
v_mfma_f32_16x16x32_bf16 v[104:107], a[40:43], v[140:143], v[104:107]
s_cmp_eq_u32 s70, 32
)"
R"(v_fmac_f32 v255, v113, v113
v_add_f32 v254, v254, v114
v_mfma_f32_16x16x32_bf16 v[108:111], a[44:47], v[140:143], v[108:111]
s_cselect_b64 s[42:43], s[44:45], s[42:43]
v_fmac_f32 v255, v114, v114
v_add_f32 v254, v254, v115
v_mfma_f32_16x16x32_bf16 v[128:131], v[68:71], v[140:143], v[128:131]
s_waitcnt lgkmcnt(2)
v_fmac_f32 v255, v115, v115
v_add_f32 v254, v254, v116
v_mfma_f32_16x16x32_bf16 v[96:99], a[48:51], v[144:147], v[96:99]
s_xor_b32 s49, s49, 0x10000
ds_read_b128 v[140:143], v25 offset:6144
v_fmac_f32 v255, v116, v116
v_mfma_f32_16x16x32_bf16 v[100:103], a[52:55], v[144:147], v[100:103]
s_mov_b32 m0, 47104
v_add_f32 v254, v254, v117
v_fmac_f32 v255, v117, v117
v_mfma_f32_16x16x32_bf16 v[104:107], a[56:59], v[144:147], v[104:107]
v_add_f32 v254, v254, v118
v_fmac_f32 v255, v118, v118
v_mfma_f32_16x16x32_bf16 v[108:111], a[60:63], v[144:147], v[108:111]
global_load_lds_dwordx4 v16, s[42:43] offset:0 sc1
v_add_f32 v254, v254, v119
v_mfma_f32_16x16x32_bf16 v[128:131], v[72:75], v[144:147], v[128:131]
s_waitcnt lgkmcnt(2)
v_fmac_f32 v255, v119, v119
v_add_f32 v254, v254, v120
v_mfma_f32_16x16x32_bf16 v[96:99], a[64:67], v[132:135], v[96:99]
ds_read_b128 v[144:147], v25 offset:7168
v_fmac_f32 v255, v120, v120
v_mfma_f32_16x16x32_bf16 v[100:103], a[68:71], v[132:135], v[100:103]
v_add_f32 v254, v254, v121
v_fmac_f32 v255, v121, v121
v_mfma_f32_16x16x32_bf16 v[104:107], a[72:75], v[132:135], v[104:107]
v_add_f32 v254, v254, v122
v_fmac_f32 v255, v122, v122
v_mfma_f32_16x16x32_bf16 v[108:111], a[76:79], v[132:135], v[108:111]
v_add_f32 v254, v254, v123
v_fmac_f32 v255, v123, v123
v_mfma_f32_16x16x32_bf16 v[128:131], v[76:79], v[132:135], v[128:131]
s_waitcnt vmcnt(9)
ds_read_b128 v[148:151], v19 offset:8192
ds_read_b128 v[152:155], v20 offset:8192
s_waitcnt lgkmcnt(4)
v_mfma_f32_16x16x32_bf16 v[96:99], a[80:83], v[136:139], v[96:99]
ds_read_b128 v[132:135], v25 offset:8192
global_load_lds_dwordx4 v17, s[42:43] offset:1024 sc1
v_mfma_f32_16x16x32_bf16 v[100:103], a[84:87], v[136:139], v[100:103]
v_add_f32 v254, v254, v124
v_fmac_f32 v255, v124, v124
v_mfma_f32_16x16x32_bf16 v[104:107], a[88:91], v[136:139], v[104:107]
v_add_f32 v254, v254, v125
v_fmac_f32 v255, v125, v125
v_mfma_f32_16x16x32_bf16 v[108:111], a[92:95], v[136:139], v[108:111]
v_add_f32 v254, v254, v126
v_fmac_f32 v255, v126, v126
v_mfma_f32_16x16x32_bf16 v[128:131], v[80:83], v[136:139], v[128:131]
s_waitcnt lgkmcnt(4)
v_add_f32 v254, v254, v127
v_fmac_f32 v255, v127, v127
v_mfma_f32_16x16x32_bf16 v[96:99], a[96:99], v[140:143], v[96:99]
s_add_u32 m0, s49, 0
ds_read_b128 v[136:139], v25 offset:9216
v_mfma_f32_16x16x32_bf16 v[100:103], a[100:103], v[140:143], v[100:103]
ds_write_b64 v27, v[254:255] offset:45056
v_mfma_f32_16x16x32_bf16 v[104:107], a[104:107], v[140:143], v[104:107]
s_waitcnt lgkmcnt(4)
v_lshlrev_b32 v112, 16, v148
v_and_b32 v113, 0xffff0000, v148
v_mfma_f32_16x16x32_bf16 v[108:111], a[108:111], v[140:143], v[108:111]
s_sub_u32 s42, s42, 18432
v_lshlrev_b32 v114, 16, v149
v_and_b32 v115, 0xffff0000, v149
v_mfma_f32_16x16x32_bf16 v[128:131], v[84:87], v[140:143], v[128:131]
v_lshlrev_b32 v116, 16, v150
v_and_b32 v117, 0xffff0000, v150
v_mfma_f32_16x16x32_bf16 v[96:99], a[112:115], v[144:147], v[96:99]
s_subb_u32 s43, s43, 0
ds_read_b128 v[140:143], v25 offset:10240
v_lshlrev_b32 v118, 16, v151
v_mfma_f32_16x16x32_bf16 v[100:103], a[116:119], v[144:147], v[100:103]
s_waitcnt lgkmcnt(4)
v_and_b32 v119, 0xffff0000, v151
v_lshlrev_b32 v120, 16, v152
v_mfma_f32_16x16x32_bf16 v[104:107], a[120:123], v[144:147], v[104:107]
v_and_b32 v121, 0xffff0000, v152
v_lshlrev_b32 v122, 16, v153
v_mfma_f32_16x16x32_bf16 v[108:111], a[124:127], v[144:147], v[108:111]
v_and_b32 v123, 0xffff0000, v153
v_lshlrev_b32 v124, 16, v154
v_mfma_f32_16x16x32_bf16 v[128:131], v[88:91], v[144:147], v[128:131]
s_waitcnt lgkmcnt(3)
v_and_b32 v125, 0xffff0000, v154
v_lshlrev_b32 v126, 16, v155
v_mfma_f32_16x16x32_bf16 v[96:99], a[128:131], v[132:135], v[96:99]
ds_read_b128 v[144:147], v25 offset:11264
v_and_b32 v127, 0xffff0000, v155
v_mfma_f32_16x16x32_bf16 v[100:103], a[132:135], v[132:135], v[100:103]
v_mfma_f32_16x16x32_bf16 v[104:107], a[136:139], v[132:135], v[104:107]
v_mfma_f32_16x16x32_bf16 v[108:111], a[140:143], v[132:135], v[108:111]
v_mfma_f32_16x16x32_bf16 v[128:131], v[92:95], v[132:135], v[128:131]
s_waitcnt lgkmcnt(3)
v_mfma_f32_16x16x32_bf16 v[96:99], a[144:147], v[136:139], v[96:99]
ds_read_b128 v[132:135], v25 offset:12288
v_mfma_f32_16x16x32_bf16 v[100:103], a[148:151], v[136:139], v[100:103]
v_mfma_f32_16x16x32_bf16 v[104:107], a[152:155], v[136:139], v[104:107]
v_mfma_f32_16x16x32_bf16 v[108:111], a[156:159], v[136:139], v[108:111]
s_waitcnt lgkmcnt(2)
v_mfma_f32_16x16x32_bf16 v[96:99], a[160:163], v[140:143], v[96:99]
ds_read_b128 v[136:139], v25 offset:13312
v_mfma_f32_16x16x32_bf16 v[100:103], a[164:167], v[140:143], v[100:103]
v_mfma_f32_16x16x32_bf16 v[104:107], a[168:171], v[140:143], v[104:107]
ds_write_b128 v26, v[128:131] offset:36864
v_mfma_f32_16x16x32_bf16 v[108:111], a[172:175], v[140:143], v[108:111]
s_waitcnt lgkmcnt(3)
v_mfma_f32_16x16x32_bf16 v[96:99], a[176:179], v[144:147], v[96:99]
ds_read_b128 v[140:143], v25 offset:14336
v_mfma_f32_16x16x32_bf16 v[100:103], a[180:183], v[144:147], v[100:103]
v_mfma_f32_16x16x32_bf16 v[104:107], a[184:187], v[144:147], v[104:107]
s_barrier
v_mfma_f32_16x16x32_bf16 v[108:111], a[188:191], v[144:147], v[108:111]
s_waitcnt lgkmcnt(3)
global_load_lds_dwordx4 v11, s[38:39] offset:-2048 sc1
v_mfma_f32_16x16x32_bf16 v[96:99], a[192:195], v[132:135], v[96:99]
ds_read_b128 v[144:147], v25 offset:15360
v_mfma_f32_16x16x32_bf16 v[100:103], a[196:199], v[132:135], v[100:103]
v_mfma_f32_16x16x32_bf16 v[104:107], a[200:203], v[132:135], v[104:107]
global_load_lds_dwordx4 v11, s[38:39] offset:-1024 sc1
v_mfma_f32_16x16x32_bf16 v[108:111], a[204:207], v[132:135], v[108:111]
s_waitcnt lgkmcnt(3)
v_mfma_f32_16x16x32_bf16 v[96:99], a[208:211], v[136:139], v[96:99]
ds_read_b128 v[132:135], v25 offset:16384
v_mfma_f32_16x16x32_bf16 v[100:103], a[212:215], v[136:139], v[100:103]
global_load_lds_dwordx4 v11, s[38:39] offset:0 sc1
v_mfma_f32_16x16x32_bf16 v[104:107], a[216:219], v[136:139], v[104:107]
v_mfma_f32_16x16x32_bf16 v[108:111], a[220:223], v[136:139], v[108:111]
s_waitcnt lgkmcnt(2)
v_mfma_f32_16x16x32_bf16 v[96:99], a[224:227], v[140:143], v[96:99]
ds_read_b128 v[136:139], v25 offset:17408
global_load_lds_dwordx4 v11, s[38:39] offset:1024 sc1
v_mfma_f32_16x16x32_bf16 v[100:103], a[228:231], v[140:143], v[100:103]
v_mfma_f32_16x16x32_bf16 v[104:107], a[232:235], v[140:143], v[104:107]
v_mfma_f32_16x16x32_bf16 v[108:111], a[236:239], v[140:143], v[108:111]
s_waitcnt lgkmcnt(2)
global_load_lds_dwordx4 v11, s[38:39] offset:2048 sc1
v_add_u32 v11, 18432, v11
v_mfma_f32_16x16x32_bf16 v[96:99], a[240:243], v[144:147], v[96:99]
v_min_u32 v11, v11, v12
v_mfma_f32_16x16x32_bf16 v[100:103], a[244:247], v[144:147], v[100:103]
v_mfma_f32_16x16x32_bf16 v[104:107], a[248:251], v[144:147], v[104:107]
s_waitcnt vmcnt(10)
s_barrier
v_mfma_f32_16x16x32_bf16 v[108:111], a[252:255], v[144:147], v[108:111]
s_waitcnt lgkmcnt(1)
ds_read_b128 v[140:143], v25 offset:18432
v_mfma_f32_16x16x32_bf16 v[96:99], v[28:31], v[132:135], v[96:99]
ds_read_b128 v[144:147], v25 offset:19456
v_mfma_f32_16x16x32_bf16 v[100:103], v[32:35], v[132:135], v[100:103]
v_mfma_f32_16x16x32_bf16 v[104:107], v[36:39], v[132:135], v[104:107]
v_mfma_f32_16x16x32_bf16 v[108:111], v[40:43], v[132:135], v[108:111]
s_waitcnt lgkmcnt(2)
v_mfma_f32_16x16x32_bf16 v[96:99], v[44:47], v[136:139], v[96:99]
ds_read_b128 v[132:135], v25 offset:20480
v_mfma_f32_16x16x32_bf16 v[100:103], v[48:51], v[136:139], v[100:103]
v_mfma_f32_16x16x32_bf16 v[104:107], v[52:55], v[136:139], v[104:107]
v_mfma_f32_16x16x32_bf16 v[108:111], v[56:59], v[136:139], v[108:111]
s_waitcnt lgkmcnt(2)
v_mfma_f32_16x16x32_bf16 v[112:115], a[0:3], v[140:143], v[112:115]
ds_read_b128 v[136:139], v25 offset:21504
v_mfma_f32_16x16x32_bf16 v[116:119], a[4:7], v[140:143], v[116:119]
v_mfma_f32_16x16x32_bf16 v[120:123], a[8:11], v[140:143], v[120:123]
s_waitcnt vmcnt(7)
s_barrier
v_mfma_f32_16x16x32_bf16 v[124:127], a[12:15], v[140:143], v[124:127]
s_mov_b64 exec, 1
global_store_dword v252, v24, s[38:39] sc1
s_mov_b64 exec, -1
v_add_u32 v24, 1, v24
v_add_lshl_u32 v252, v24, 0, 7
v_mfma_f32_16x16x32_bf16 v[128:131], v[60:63], v[140:143], 0
s_waitcnt lgkmcnt(2)
v_bfi_b32 v252, s69, v252, v23
v_cvt_pk_bf16_f32 v164, v96, v97
v_mfma_f32_16x16x32_bf16 v[112:115], a[16:19], v[144:147], v[112:115]
ds_read_b128 v[140:143], v25 offset:22528
v_cvt_pk_bf16_f32 v165, v98, v99
v_mfma_f32_16x16x32_bf16 v[116:119], a[20:23], v[144:147], v[116:119]
v_cvt_pk_bf16_f32 v166, v100, v101
v_cvt_pk_bf16_f32 v167, v102, v103
v_mfma_f32_16x16x32_bf16 v[120:123], a[24:27], v[144:147], v[120:123]
global_store_dwordx4 v13, v[164:167], s[38:39] offset:0 sc1
v_mfma_f32_16x16x32_bf16 v[124:127], a[28:31], v[144:147], v[124:127]
v_cvt_pk_bf16_f32 v168, v104, v105
v_cvt_pk_bf16_f32 v169, v106, v107
v_mfma_f32_16x16x32_bf16 v[128:131], v[64:67], v[144:147], v[128:131]
s_waitcnt lgkmcnt(2)
v_cvt_pk_bf16_f32 v170, v108, v109
v_cvt_pk_bf16_f32 v171, v110, v111
v_mfma_f32_16x16x32_bf16 v[112:115], a[32:35], v[132:135], v[112:115]
s_mov_b32 m0, 55296
ds_read_b128 v[144:147], v25 offset:23552
v_add_f32 v254, v96, v97
v_mfma_f32_16x16x32_bf16 v[116:119], a[36:39], v[132:135], v[116:119]
global_store_dwordx4 v13, v[168:171], s[38:39] offset:1024 sc1
v_mfma_f32_16x16x32_bf16 v[120:123], a[40:43], v[132:135], v[120:123]
v_add_u32 v13, 18688, v13
v_mul_f32 v255, v96, v96
v_mfma_f32_16x16x32_bf16 v[124:127], a[44:47], v[132:135], v[124:127]
v_fmac_f32 v255, v97, v97
v_add_f32 v254, v254, v98
v_mfma_f32_16x16x32_bf16 v[128:131], v[68:71], v[132:135], v[128:131]
s_waitcnt lgkmcnt(2)
v_fmac_f32 v255, v98, v98
v_add_f32 v254, v254, v99
v_mfma_f32_16x16x32_bf16 v[112:115], a[48:51], v[136:139], v[112:115]
ds_read_b128 v[132:135], v25 offset:24576
v_fmac_f32 v255, v99, v99
v_mfma_f32_16x16x32_bf16 v[116:119], a[52:55], v[136:139], v[116:119]
v_add_f32 v254, v254, v100
v_fmac_f32 v255, v100, v100
v_mfma_f32_16x16x32_bf16 v[120:123], a[56:59], v[136:139], v[120:123]
v_add_f32 v254, v254, v101
v_fmac_f32 v255, v101, v101
v_mfma_f32_16x16x32_bf16 v[124:127], a[60:63], v[136:139], v[124:127]
global_load_lds_dwordx4 v16, s[42:43] offset:0 sc1
v_add_f32 v254, v254, v102
v_mfma_f32_16x16x32_bf16 v[128:131], v[72:75], v[136:139], v[128:131]
s_waitcnt lgkmcnt(2)
v_fmac_f32 v255, v102, v102
v_add_f32 v254, v254, v103
v_mfma_f32_16x16x32_bf16 v[112:115], a[64:67], v[140:143], v[112:115]
ds_read_b128 v[136:139], v25 offset:25600
v_fmac_f32 v255, v103, v103
v_mfma_f32_16x16x32_bf16 v[116:119], a[68:71], v[140:143], v[116:119]
v_add_f32 v254, v254, v104
v_fmac_f32 v255, v104, v104
v_mfma_f32_16x16x32_bf16 v[120:123], a[72:75], v[140:143], v[120:123]
v_add_f32 v254, v254, v105
v_fmac_f32 v255, v105, v105
v_mfma_f32_16x16x32_bf16 v[124:127], a[76:79], v[140:143], v[124:127]
v_add_f32 v254, v254, v106
v_fmac_f32 v255, v106, v106
v_mfma_f32_16x16x32_bf16 v[128:131], v[76:79], v[140:143], v[128:131]
s_waitcnt vmcnt(9)
ds_read_b128 v[148:151], v19 offset:0
ds_read_b128 v[152:155], v20 offset:0
s_waitcnt lgkmcnt(4)
v_mfma_f32_16x16x32_bf16 v[112:115], a[80:83], v[144:147], v[112:115]
ds_read_b128 v[140:143], v25 offset:26624
global_load_lds_dwordx4 v17, s[42:43] offset:1024 sc1
v_mfma_f32_16x16x32_bf16 v[116:119], a[84:87], v[144:147], v[116:119]
v_add_f32 v254, v254, v107
v_fmac_f32 v255, v107, v107
v_mfma_f32_16x16x32_bf16 v[120:123], a[88:91], v[144:147], v[120:123]
v_add_f32 v254, v254, v108
v_fmac_f32 v255, v108, v108
v_mfma_f32_16x16x32_bf16 v[124:127], a[92:95], v[144:147], v[124:127]
v_add_f32 v254, v254, v109
v_fmac_f32 v255, v109, v109
v_mfma_f32_16x16x32_bf16 v[128:131], v[80:83], v[144:147], v[128:131]
s_waitcnt lgkmcnt(4)
v_add_f32 v254, v254, v110
v_fmac_f32 v255, v110, v110
v_mfma_f32_16x16x32_bf16 v[112:115], a[96:99], v[132:135], v[112:115]
s_add_u32 m0, s49, 18432
ds_read_b128 v[144:147], v25 offset:27648
v_add_f32 v254, v254, v111
v_mfma_f32_16x16x32_bf16 v[116:119], a[100:103], v[132:135], v[116:119]
v_fmac_f32 v255, v111, v111
v_mfma_f32_16x16x32_bf16 v[120:123], a[104:107], v[132:135], v[120:123]
ds_write_b64 v27, v[254:255] offset:43008
v_mfma_f32_16x16x32_bf16 v[124:127], a[108:111], v[132:135], v[124:127]
s_sub_u32 s42, s42, 18432
s_waitcnt lgkmcnt(4)
v_lshlrev_b32 v96, 16, v148
v_and_b32 v97, 0xffff0000, v148
v_mfma_f32_16x16x32_bf16 v[128:131], v[84:87], v[132:135], v[128:131]
v_lshlrev_b32 v98, 16, v149
v_and_b32 v99, 0xffff0000, v149
v_mfma_f32_16x16x32_bf16 v[112:115], a[112:115], v[136:139], v[112:115]
s_subb_u32 s43, s43, 0
ds_read_b128 v[132:135], v25 offset:28672
v_lshlrev_b32 v100, 16, v150
v_mfma_f32_16x16x32_bf16 v[116:119], a[116:119], v[136:139], v[116:119]
v_and_b32 v101, 0xffff0000, v150
v_lshlrev_b32 v102, 16, v151
v_mfma_f32_16x16x32_bf16 v[120:123], a[120:123], v[136:139], v[120:123]
s_waitcnt lgkmcnt(4)
v_and_b32 v103, 0xffff0000, v151
v_lshlrev_b32 v104, 16, v152
v_mfma_f32_16x16x32_bf16 v[124:127], a[124:127], v[136:139], v[124:127]
v_and_b32 v105, 0xffff0000, v152
v_lshlrev_b32 v106, 16, v153
v_mfma_f32_16x16x32_bf16 v[128:131], v[88:91], v[136:139], v[128:131]
s_waitcnt lgkmcnt(3)
v_and_b32 v107, 0xffff0000, v153
v_lshlrev_b32 v108, 16, v154
v_mfma_f32_16x16x32_bf16 v[112:115], a[128:131], v[140:143], v[112:115]
ds_read_b128 v[136:139], v25 offset:29696
v_and_b32 v109, 0xffff0000, v154
v_mfma_f32_16x16x32_bf16 v[116:119], a[132:135], v[140:143], v[116:119]
v_lshlrev_b32 v110, 16, v155
v_and_b32 v111, 0xffff0000, v155
v_mfma_f32_16x16x32_bf16 v[120:123], a[136:139], v[140:143], v[120:123]
v_mfma_f32_16x16x32_bf16 v[124:127], a[140:143], v[140:143], v[124:127]
v_mfma_f32_16x16x32_bf16 v[128:131], v[92:95], v[140:143], v[128:131]
s_waitcnt lgkmcnt(3)
v_mfma_f32_16x16x32_bf16 v[112:115], a[144:147], v[144:147], v[112:115]
ds_read_b128 v[140:143], v25 offset:30720
v_mfma_f32_16x16x32_bf16 v[116:119], a[148:151], v[144:147], v[116:119]
v_mfma_f32_16x16x32_bf16 v[120:123], a[152:155], v[144:147], v[120:123]
v_mfma_f32_16x16x32_bf16 v[124:127], a[156:159], v[144:147], v[124:127]
s_waitcnt lgkmcnt(2)
v_mfma_f32_16x16x32_bf16 v[112:115], a[160:163], v[132:135], v[112:115]
ds_read_b128 v[144:147], v25 offset:31744
v_mfma_f32_16x16x32_bf16 v[116:119], a[164:167], v[132:135], v[116:119]
v_mfma_f32_16x16x32_bf16 v[120:123], a[168:171], v[132:135], v[120:123]
ds_write_b128 v26, v[128:131] offset:39936
v_mfma_f32_16x16x32_bf16 v[124:127], a[172:175], v[132:135], v[124:127]
s_waitcnt lgkmcnt(3)
v_mfma_f32_16x16x32_bf16 v[112:115], a[176:179], v[136:139], v[112:115]
ds_read_b128 v[132:135], v25 offset:32768
v_mfma_f32_16x16x32_bf16 v[116:119], a[180:183], v[136:139], v[116:119]
v_mfma_f32_16x16x32_bf16 v[120:123], a[184:187], v[136:139], v[120:123]
s_barrier
v_mfma_f32_16x16x32_bf16 v[124:127], a[188:191], v[136:139], v[124:127]
s_waitcnt lgkmcnt(3)
global_load_lds_dwordx4 v11, s[38:39] offset:-2048 sc1
v_mfma_f32_16x16x32_bf16 v[112:115], a[192:195], v[140:143], v[112:115]
s_mov_b64 s[76:77], -1
ds_read_b128 v[136:139], v25 offset:33792
v_mfma_f32_16x16x32_bf16 v[116:119], a[196:199], v[140:143], v[116:119]
v_mfma_f32_16x16x32_bf16 v[120:123], a[200:203], v[140:143], v[120:123]
global_load_lds_dwordx4 v11, s[38:39] offset:-1024 sc1
.Lwo_in5:
v_mfma_f32_16x16x32_bf16 v[124:127], a[204:207], v[140:143], v[124:127]
s_waitcnt lgkmcnt(3)
v_mfma_f32_16x16x32_bf16 v[112:115], a[208:211], v[144:147], v[112:115]
ds_read_b128 v[140:143], v25 offset:34816
v_mfma_f32_16x16x32_bf16 v[116:119], a[212:215], v[144:147], v[116:119]
global_load_lds_dwordx4 v11, s[38:39] offset:0 sc1
v_mfma_f32_16x16x32_bf16 v[120:123], a[216:219], v[144:147], v[120:123]
v_mfma_f32_16x16x32_bf16 v[124:127], a[220:223], v[144:147], v[124:127]
s_waitcnt lgkmcnt(2)
v_mfma_f32_16x16x32_bf16 v[112:115], a[224:227], v[132:135], v[112:115]
ds_read_b128 v[144:147], v25 offset:35840
global_load_lds_dwordx4 v11, s[38:39] offset:1024 sc1
v_mfma_f32_16x16x32_bf16 v[116:119], a[228:231], v[132:135], v[116:119]
v_mfma_f32_16x16x32_bf16 v[120:123], a[232:235], v[132:135], v[120:123]
v_mfma_f32_16x16x32_bf16 v[124:127], a[236:239], v[132:135], v[124:127]
s_waitcnt lgkmcnt(2)
v_xor_b32 v25, 0x10000, v25
global_load_lds_dwordx4 v11, s[38:39] offset:2048 sc1
v_mfma_f32_16x16x32_bf16 v[112:115], a[240:243], v[136:139], v[112:115]
v_add_u32 v11, 18432, v11
v_min_u32 v11, v11, v12
v_mfma_f32_16x16x32_bf16 v[116:119], a[244:247], v[136:139], v[116:119]
v_mfma_f32_16x16x32_bf16 v[120:123], a[248:251], v[136:139], v[120:123]
s_waitcnt vmcnt(10)
s_barrier
v_mfma_f32_16x16x32_bf16 v[124:127], a[252:255], v[136:139], v[124:127]
s_waitcnt lgkmcnt(1)
ds_read_b128 v[132:135], v25 offset:0
v_mfma_f32_16x16x32_bf16 v[112:115], v[28:31], v[140:143], v[112:115]
ds_read_b128 v[136:139], v25 offset:1024
v_mfma_f32_16x16x32_bf16 v[116:119], v[32:35], v[140:143], v[116:119]
v_mfma_f32_16x16x32_bf16 v[120:123], v[36:39], v[140:143], v[120:123]
v_mfma_f32_16x16x32_bf16 v[124:127], v[40:43], v[140:143], v[124:127]
s_waitcnt lgkmcnt(2)
v_mfma_f32_16x16x32_bf16 v[112:115], v[44:47], v[144:147], v[112:115]
ds_read_b128 v[140:143], v25 offset:2048
v_mfma_f32_16x16x32_bf16 v[116:119], v[48:51], v[144:147], v[116:119]
v_mfma_f32_16x16x32_bf16 v[120:123], v[52:55], v[144:147], v[120:123]
v_mfma_f32_16x16x32_bf16 v[124:127], v[56:59], v[144:147], v[124:127]
s_sub_u32 s70, s70, 1
s_cbranch_scc0 .Lwo_top6
s_waitcnt vmcnt(7)
s_barrier
s_mov_b64 exec, 1
global_store_dword v252, v24, s[38:39] sc1
s_mov_b64 exec, -1
v_add_u32 v24, 1, v24
v_add_lshl_u32 v252, v24, 0, 7
v_bfi_b32 v252, s69, v252, v23
v_cvt_pk_bf16_f32 v164, v112, v113
v_cvt_pk_bf16_f32 v165, v114, v115
v_cvt_pk_bf16_f32 v166, v116, v117
v_cvt_pk_bf16_f32 v167, v118, v119
global_store_dwordx4 v13, v[164:167], s[38:39] offset:0 sc1
v_cvt_pk_bf16_f32 v168, v120, v121
v_cvt_pk_bf16_f32 v169, v122, v123
v_cvt_pk_bf16_f32 v170, v124, v125
v_cvt_pk_bf16_f32 v171, v126, v127
global_store_dwordx4 v13, v[168:171], s[38:39] offset:1024 sc1
v_add_f32 v254, v112, v113
v_mul_f32 v255, v112, v112
v_fmac_f32 v255, v113, v113
v_add_f32 v254, v254, v114
v_fmac_f32 v255, v114, v114
)"
R"(v_add_f32 v254, v254, v115
v_fmac_f32 v255, v115, v115
v_add_f32 v254, v254, v116
v_fmac_f32 v255, v116, v116
v_add_f32 v254, v254, v117
v_fmac_f32 v255, v117, v117
v_add_f32 v254, v254, v118
v_fmac_f32 v255, v118, v118
v_add_f32 v254, v254, v119
v_fmac_f32 v255, v119, v119
v_add_f32 v254, v254, v120
v_fmac_f32 v255, v120, v120
v_add_f32 v254, v254, v121
v_fmac_f32 v255, v121, v121
v_add_f32 v254, v254, v122
v_fmac_f32 v255, v122, v122
v_add_f32 v254, v254, v123
v_fmac_f32 v255, v123, v123
v_add_f32 v254, v254, v124
v_fmac_f32 v255, v124, v124
v_add_f32 v254, v254, v125
v_fmac_f32 v255, v125, v125
v_add_f32 v254, v254, v126
v_fmac_f32 v255, v126, v126
v_add_f32 v254, v254, v127
v_fmac_f32 v255, v127, v127
ds_write_b64 v27, v[254:255] offset:45056
s_waitcnt vmcnt(0) lgkmcnt(0)
s_barrier
s_mov_b64 exec, 1
global_store_dword v252, v24, s[38:39] sc1
s_mov_b64 exec, -1
s_waitcnt vmcnt(0)
s_barrier
v_add_u32 v24, 8, v24
v_mbcnt_lo_u32_b32 v252, -1, 0
v_mbcnt_hi_u32_b32 v252, -1, v252
v_add_lshl_u32 v252, v252, 0, 7
v_bfi_b32 v252, s69, v252, v23
s_mov_b64 exec, 0xff
global_store_dword v252, v24, s[38:39] sc1
s_mov_b64 exec, -1
s_endpgm
.Lwo_w2:
s_add_u32 m0, s48, 4096
s_nop 0
global_load_lds_dwordx4 v0, s[40:41] offset:-4096
global_load_lds_dwordx4 v1, s[40:41] offset:-3072
global_load_lds_dwordx4 v2, s[40:41] offset:-2048
global_load_lds_dwordx4 v3, s[40:41] offset:-1024
global_load_lds_dwordx4 v4, s[40:41] offset:0
global_load_lds_dwordx4 v5, s[40:41] offset:1024
global_load_lds_dwordx4 v6, s[40:41] offset:2048
global_load_lds_dwordx4 v7, s[40:41] offset:3072
s_add_u32 m0, s48, 12288
s_nop 0
global_load_lds_dwordx4 v8, s[40:41] offset:-4096
s_add_u32 s40, s40, 73728
s_addc_u32 s41, s41, 0
s_add_u32 m0, s48, 40960
s_nop 0
global_load_lds_dwordx4 v0, s[40:41] offset:-4096
global_load_lds_dwordx4 v1, s[40:41] offset:-3072
global_load_lds_dwordx4 v2, s[40:41] offset:-2048
global_load_lds_dwordx4 v3, s[40:41] offset:-1024
global_load_lds_dwordx4 v4, s[40:41] offset:0
global_load_lds_dwordx4 v5, s[40:41] offset:1024
global_load_lds_dwordx4 v6, s[40:41] offset:2048
global_load_lds_dwordx4 v7, s[40:41] offset:3072
s_add_u32 m0, s48, 49152
s_nop 0
global_load_lds_dwordx4 v8, s[40:41] offset:-4096
s_add_u32 s40, s40, 73728
s_addc_u32 s41, s41, 0
s_add_u32 m0, s48, 77824
s_nop 0
global_load_lds_dwordx4 v0, s[40:41] offset:-4096
global_load_lds_dwordx4 v1, s[40:41] offset:-3072
global_load_lds_dwordx4 v2, s[40:41] offset:-2048
global_load_lds_dwordx4 v3, s[40:41] offset:-1024
global_load_lds_dwordx4 v4, s[40:41] offset:0
global_load_lds_dwordx4 v5, s[40:41] offset:1024
global_load_lds_dwordx4 v6, s[40:41] offset:2048
global_load_lds_dwordx4 v7, s[40:41] offset:3072
s_add_u32 m0, s48, 86016
s_nop 0
global_load_lds_dwordx4 v8, s[40:41] offset:-4096
s_add_u32 s40, s40, 73728
s_addc_u32 s41, s41, 0
v_add_u32 v204, 0x10000, v9
v_add_u32 v205, 0x20000, v9
v_add_u32 v206, 0x10000, v10
v_add_u32 v207, 0x20000, v10
s_waitcnt lgkmcnt(0)
s_mul_i32 s37, s37, 0x9E3779B1
s_add_u32 s37, s37, 0x7F4A7C15
v_add_u32 v22, s37, v22
v_add_u32 v24, s37, v24
s_waitcnt vmcnt(18)
s_barrier
s_add_u32 m0, s48, 114688
ds_read_b64_tr_b16 a[0:1], v9 offset:0
ds_read_b64_tr_b16 a[2:3], v9 offset:2304
global_load_lds_dwordx4 v0, s[40:41] offset:-4096
ds_read_b64_tr_b16 a[4:5], v9 offset:8
global_load_lds_dwordx4 v1, s[40:41] offset:-3072
ds_read_b64_tr_b16 a[6:7], v9 offset:2312
global_load_lds_dwordx4 v2, s[40:41] offset:-2048
ds_read_b64_tr_b16 a[8:9], v9 offset:64
global_load_lds_dwordx4 v3, s[40:41] offset:-1024
ds_read_b64_tr_b16 a[10:11], v9 offset:2368
ds_read_b64_tr_b16 a[12:13], v9 offset:72
global_load_lds_dwordx4 v4, s[40:41] offset:0
ds_read_b64_tr_b16 a[14:15], v9 offset:2376
global_load_lds_dwordx4 v5, s[40:41] offset:1024
ds_read_b64_tr_b16 a[16:17], v9 offset:18432
global_load_lds_dwordx4 v6, s[40:41] offset:2048
ds_read_b64_tr_b16 a[18:19], v9 offset:20736
global_load_lds_dwordx4 v7, s[40:41] offset:3072
ds_read_b64_tr_b16 a[20:21], v9 offset:18440
ds_read_b64_tr_b16 a[22:23], v9 offset:20744
s_add_u32 m0, s48, 122880
ds_read_b64_tr_b16 a[24:25], v9 offset:18496
global_load_lds_dwordx4 v8, s[40:41] offset:-4096
ds_read_b64_tr_b16 a[26:27], v9 offset:20800
s_add_u32 s40, s40, 73728
ds_read_b64_tr_b16 a[28:29], v9 offset:18504
s_addc_u32 s41, s41, 0
ds_read_b64_tr_b16 a[30:31], v9 offset:20808
s_waitcnt vmcnt(18) lgkmcnt(0)
s_barrier
s_add_u32 m0, s48, 4096
ds_read_b64_tr_b16 a[32:33], v9 offset:36864
ds_read_b64_tr_b16 a[34:35], v9 offset:39168
global_load_lds_dwordx4 v0, s[40:41] offset:-4096
ds_read_b64_tr_b16 a[36:37], v9 offset:36872
global_load_lds_dwordx4 v1, s[40:41] offset:-3072
ds_read_b64_tr_b16 a[38:39], v9 offset:39176
global_load_lds_dwordx4 v2, s[40:41] offset:-2048
ds_read_b64_tr_b16 a[40:41], v9 offset:36928
global_load_lds_dwordx4 v3, s[40:41] offset:-1024
ds_read_b64_tr_b16 a[42:43], v9 offset:39232
ds_read_b64_tr_b16 a[44:45], v9 offset:36936
global_load_lds_dwordx4 v4, s[40:41] offset:0
ds_read_b64_tr_b16 a[46:47], v9 offset:39240
global_load_lds_dwordx4 v5, s[40:41] offset:1024
ds_read_b64_tr_b16 a[48:49], v9 offset:55296
global_load_lds_dwordx4 v6, s[40:41] offset:2048
ds_read_b64_tr_b16 a[50:51], v9 offset:57600
global_load_lds_dwordx4 v7, s[40:41] offset:3072
ds_read_b64_tr_b16 a[52:53], v9 offset:55304
ds_read_b64_tr_b16 a[54:55], v9 offset:57608
s_add_u32 m0, s48, 12288
ds_read_b64_tr_b16 a[56:57], v9 offset:55360
global_load_lds_dwordx4 v8, s[40:41] offset:-4096
ds_read_b64_tr_b16 a[58:59], v9 offset:57664
s_add_u32 s40, s40, 73728
ds_read_b64_tr_b16 a[60:61], v9 offset:55368
s_addc_u32 s41, s41, 0
ds_read_b64_tr_b16 a[62:63], v9 offset:57672
s_waitcnt vmcnt(18) lgkmcnt(0)
s_barrier
s_add_u32 m0, s48, 40960
ds_read_b64_tr_b16 a[64:65], v204 offset:8192
ds_read_b64_tr_b16 a[66:67], v204 offset:10496
global_load_lds_dwordx4 v0, s[40:41] offset:-4096
ds_read_b64_tr_b16 a[68:69], v204 offset:8200
global_load_lds_dwordx4 v1, s[40:41] offset:-3072
ds_read_b64_tr_b16 a[70:71], v204 offset:10504
global_load_lds_dwordx4 v2, s[40:41] offset:-2048
ds_read_b64_tr_b16 a[72:73], v204 offset:8256
global_load_lds_dwordx4 v3, s[40:41] offset:-1024
ds_read_b64_tr_b16 a[74:75], v204 offset:10560
ds_read_b64_tr_b16 a[76:77], v204 offset:8264
global_load_lds_dwordx4 v4, s[40:41] offset:0
ds_read_b64_tr_b16 a[78:79], v204 offset:10568
global_load_lds_dwordx4 v5, s[40:41] offset:1024
ds_read_b64_tr_b16 a[80:81], v204 offset:26624
global_load_lds_dwordx4 v6, s[40:41] offset:2048
ds_read_b64_tr_b16 a[82:83], v204 offset:28928
global_load_lds_dwordx4 v7, s[40:41] offset:3072
ds_read_b64_tr_b16 a[84:85], v204 offset:26632
ds_read_b64_tr_b16 a[86:87], v204 offset:28936
s_add_u32 m0, s48, 49152
ds_read_b64_tr_b16 a[88:89], v204 offset:26688
global_load_lds_dwordx4 v8, s[40:41] offset:-4096
ds_read_b64_tr_b16 a[90:91], v204 offset:28992
s_add_u32 s40, s40, 73728
ds_read_b64_tr_b16 a[92:93], v204 offset:26696
s_addc_u32 s41, s41, 0
ds_read_b64_tr_b16 a[94:95], v204 offset:29000
s_waitcnt vmcnt(18) lgkmcnt(0)
s_barrier
s_add_u32 m0, s48, 77824
ds_read_b64_tr_b16 a[96:97], v204 offset:45056
ds_read_b64_tr_b16 a[98:99], v204 offset:47360
global_load_lds_dwordx4 v0, s[40:41] offset:-4096
ds_read_b64_tr_b16 a[100:101], v204 offset:45064
global_load_lds_dwordx4 v1, s[40:41] offset:-3072
ds_read_b64_tr_b16 a[102:103], v204 offset:47368
global_load_lds_dwordx4 v2, s[40:41] offset:-2048
ds_read_b64_tr_b16 a[104:105], v204 offset:45120
global_load_lds_dwordx4 v3, s[40:41] offset:-1024
ds_read_b64_tr_b16 a[106:107], v204 offset:47424
ds_read_b64_tr_b16 a[108:109], v204 offset:45128
global_load_lds_dwordx4 v4, s[40:41] offset:0
ds_read_b64_tr_b16 a[110:111], v204 offset:47432
global_load_lds_dwordx4 v5, s[40:41] offset:1024
ds_read_b64_tr_b16 a[112:113], v204 offset:63488
global_load_lds_dwordx4 v6, s[40:41] offset:2048
ds_read_b64_tr_b16 a[114:115], v205 offset:256
global_load_lds_dwordx4 v7, s[40:41] offset:3072
ds_read_b64_tr_b16 a[116:117], v204 offset:63496
ds_read_b64_tr_b16 a[118:119], v205 offset:264
s_add_u32 m0, s48, 86016
ds_read_b64_tr_b16 a[120:121], v204 offset:63552
global_load_lds_dwordx4 v8, s[40:41] offset:-4096
ds_read_b64_tr_b16 a[122:123], v205 offset:320
s_add_u32 s40, s40, 73728
ds_read_b64_tr_b16 a[124:125], v204 offset:63560
s_addc_u32 s41, s41, 0
ds_read_b64_tr_b16 a[126:127], v205 offset:328
s_waitcnt vmcnt(18) lgkmcnt(0)
s_barrier
s_add_u32 m0, s48, 114688
ds_read_b64_tr_b16 a[128:129], v9 offset:0
ds_read_b64_tr_b16 a[130:131], v9 offset:2304
global_load_lds_dwordx4 v0, s[40:41] offset:-4096
ds_read_b64_tr_b16 a[132:133], v9 offset:8
global_load_lds_dwordx4 v1, s[40:41] offset:-3072
ds_read_b64_tr_b16 a[134:135], v9 offset:2312
ds_read_b64_tr_b16 a[136:137], v9 offset:64
global_load_lds_dwordx4 v2, s[40:41] offset:-2048
ds_read_b64_tr_b16 a[138:139], v9 offset:2368
global_load_lds_dwordx4 v3, s[40:41] offset:-1024
ds_read_b64_tr_b16 a[140:141], v9 offset:72
global_load_lds_dwordx4 v4, s[40:41] offset:0
ds_read_b64_tr_b16 a[142:143], v9 offset:2376
ds_read_b64_tr_b16 a[144:145], v9 offset:18432
global_load_lds_dwordx4 v5, s[40:41] offset:1024
ds_read_b64_tr_b16 a[146:147], v9 offset:20736
global_load_lds_dwordx4 v6, s[40:41] offset:2048
ds_read_b64_tr_b16 a[148:149], v9 offset:18440
ds_read_b64_tr_b16 a[150:151], v9 offset:20744
global_load_lds_dwordx4 v7, s[40:41] offset:3072
ds_read_b64_tr_b16 a[152:153], v9 offset:18496
s_add_u32 m0, s48, 122880
ds_read_b64_tr_b16 a[154:155], v9 offset:20800
global_load_lds_dwordx4 v8, s[40:41] offset:-4096
ds_read_b64_tr_b16 a[156:157], v9 offset:18504
ds_read_b64_tr_b16 a[158:159], v9 offset:20808
s_add_u32 s40, s40, 73728
ds_read_b64_tr_b16 v[60:61], v10 offset:18432
s_addc_u32 s41, s41, 0
ds_read_b64_tr_b16 v[62:63], v10 offset:20736
s_waitcnt vmcnt(18) lgkmcnt(0)
s_barrier
s_add_u32 m0, s48, 4096
ds_read_b64_tr_b16 a[160:161], v9 offset:36864
ds_read_b64_tr_b16 a[162:163], v9 offset:39168
global_load_lds_dwordx4 v0, s[40:41] offset:-4096
ds_read_b64_tr_b16 a[164:165], v9 offset:36872
ds_read_b64_tr_b16 a[166:167], v9 offset:39176
global_load_lds_dwordx4 v1, s[40:41] offset:-3072
ds_read_b64_tr_b16 a[168:169], v9 offset:36928
global_load_lds_dwordx4 v2, s[40:41] offset:-2048
ds_read_b64_tr_b16 a[170:171], v9 offset:39232
ds_read_b64_tr_b16 a[172:173], v9 offset:36936
global_load_lds_dwordx4 v3, s[40:41] offset:-1024
ds_read_b64_tr_b16 a[174:175], v9 offset:39240
global_load_lds_dwordx4 v4, s[40:41] offset:0
ds_read_b64_tr_b16 v[64:65], v10 offset:36864
ds_read_b64_tr_b16 v[66:67], v10 offset:39168
global_load_lds_dwordx4 v5, s[40:41] offset:1024
ds_read_b64_tr_b16 a[176:177], v9 offset:55296
global_load_lds_dwordx4 v6, s[40:41] offset:2048
ds_read_b64_tr_b16 a[178:179], v9 offset:57600
ds_read_b64_tr_b16 a[180:181], v9 offset:55304
global_load_lds_dwordx4 v7, s[40:41] offset:3072
ds_read_b64_tr_b16 a[182:183], v9 offset:57608
s_add_u32 m0, s48, 12288
ds_read_b64_tr_b16 a[184:185], v9 offset:55360
ds_read_b64_tr_b16 a[186:187], v9 offset:57664
global_load_lds_dwordx4 v8, s[40:41] offset:-4096
ds_read_b64_tr_b16 a[188:189], v9 offset:55368
s_sub_u32 s40, s40, 589824
ds_read_b64_tr_b16 a[190:191], v9 offset:57672
ds_read_b64_tr_b16 v[68:69], v10 offset:55296
s_subb_u32 s41, s41, 0
ds_read_b64_tr_b16 v[70:71], v10 offset:57600
s_waitcnt vmcnt(18) lgkmcnt(0)
s_barrier
ds_read_b64_tr_b16 a[192:193], v204 offset:8192
ds_read_b64_tr_b16 a[194:195], v204 offset:10496
ds_read_b64_tr_b16 a[196:197], v204 offset:8200
ds_read_b64_tr_b16 a[198:199], v204 offset:10504
ds_read_b64_tr_b16 a[200:201], v204 offset:8256
ds_read_b64_tr_b16 a[202:203], v204 offset:10560
ds_read_b64_tr_b16 a[204:205], v204 offset:8264
ds_read_b64_tr_b16 a[206:207], v204 offset:10568
ds_read_b64_tr_b16 v[72:73], v206 offset:8192
ds_read_b64_tr_b16 v[74:75], v206 offset:10496
ds_read_b64_tr_b16 a[208:209], v204 offset:26624
ds_read_b64_tr_b16 a[210:211], v204 offset:28928
ds_read_b64_tr_b16 a[212:213], v204 offset:26632
ds_read_b64_tr_b16 a[214:215], v204 offset:28936
ds_read_b64_tr_b16 a[216:217], v204 offset:26688
ds_read_b64_tr_b16 a[218:219], v204 offset:28992
ds_read_b64_tr_b16 a[220:221], v204 offset:26696
ds_read_b64_tr_b16 a[222:223], v204 offset:29000
ds_read_b64_tr_b16 v[76:77], v206 offset:26624
ds_read_b64_tr_b16 v[78:79], v206 offset:28928
s_waitcnt vmcnt(9) lgkmcnt(0)
s_barrier
ds_read_b64_tr_b16 a[224:225], v204 offset:45056
ds_read_b64_tr_b16 a[226:227], v204 offset:47360
ds_read_b64_tr_b16 a[228:229], v204 offset:45064
ds_read_b64_tr_b16 a[230:231], v204 offset:47368
ds_read_b64_tr_b16 a[232:233], v204 offset:45120
ds_read_b64_tr_b16 a[234:235], v204 offset:47424
ds_read_b64_tr_b16 a[236:237], v204 offset:45128
ds_read_b64_tr_b16 a[238:239], v204 offset:47432
ds_read_b64_tr_b16 v[80:81], v206 offset:45056
ds_read_b64_tr_b16 v[82:83], v206 offset:47360
ds_read_b64_tr_b16 a[240:241], v204 offset:63488
ds_read_b64_tr_b16 a[242:243], v205 offset:256
ds_read_b64_tr_b16 a[244:245], v204 offset:63496
ds_read_b64_tr_b16 a[246:247], v205 offset:264
ds_read_b64_tr_b16 a[248:249], v204 offset:63552
ds_read_b64_tr_b16 a[250:251], v205 offset:320
ds_read_b64_tr_b16 a[252:253], v204 offset:63560
ds_read_b64_tr_b16 a[254:255], v205 offset:328
ds_read_b64_tr_b16 v[84:85], v206 offset:63488
ds_read_b64_tr_b16 v[86:87], v207 offset:256
s_waitcnt vmcnt(0) lgkmcnt(0)
s_barrier
ds_read_b64_tr_b16 v[28:29], v9 offset:0
ds_read_b64_tr_b16 v[30:31], v9 offset:2304
ds_read_b64_tr_b16 v[32:33], v9 offset:8
ds_read_b64_tr_b16 v[34:35], v9 offset:2312
ds_read_b64_tr_b16 v[36:37], v9 offset:64
ds_read_b64_tr_b16 v[38:39], v9 offset:2368
ds_read_b64_tr_b16 v[40:41], v9 offset:72
ds_read_b64_tr_b16 v[42:43], v9 offset:2376
ds_read_b64_tr_b16 v[88:89], v10 offset:0
ds_read_b64_tr_b16 v[90:91], v10 offset:2304
ds_read_b64_tr_b16 v[44:45], v9 offset:18432
ds_read_b64_tr_b16 v[46:47], v9 offset:20736
ds_read_b64_tr_b16 v[48:49], v9 offset:18440
ds_read_b64_tr_b16 v[50:51], v9 offset:20744
ds_read_b64_tr_b16 v[52:53], v9 offset:18496
ds_read_b64_tr_b16 v[54:55], v9 offset:20800
ds_read_b64_tr_b16 v[56:57], v9 offset:18504
ds_read_b64_tr_b16 v[58:59], v9 offset:20808
ds_read_b64_tr_b16 v[92:93], v10 offset:18432
ds_read_b64_tr_b16 v[94:95], v10 offset:20736
s_waitcnt lgkmcnt(0)
s_barrier
v_add_lshl_u32 v252, v24, 0, 7
v_bfi_b32 v252, s69, v252, v23
v_add_lshl_u32 v253, v22, 0, 7
v_bfi_b32 v253, s69, v253, v21
s_mov_b32 s92, 0x01010101
s_mov_b32 s93, 0x01010101
s_mov_b64 s[76:77], 0
s_add_u32 m0, s49, 18432
v_add_u32 v13, 18688, v13
s_xor_b32 s49, s49, 0x10000
s_mov_b32 m0, 49152
s_add_u32 m0, s49, 0
v_add_u32 v22, 1, v22
v_add_lshl_u32 v253, v22, 0, 7
v_bfi_b32 v253, s69, v253, v21
v_add_u32 v11, 18432, v11
s_mov_b32 m0, 57344
v_add_u32 v13, 18688, v13
s_add_u32 m0, s49, 18432
s_mov_b64 s[76:77], -1
v_add_u32 v22, 1, v22
v_add_lshl_u32 v253, v22, 0, 7
v_bfi_b32 v253, s69, v253, v21
s_mov_b32 s70, 33
s_waitcnt vmcnt(0) lgkmcnt(0)
s_nop 7
s_branch .Lwo_in12
.p2align 6
.Lwo_top13:
s_waitcnt lgkmcnt(2)
v_mfma_f32_16x16x32_bf16 v[96:99], a[144:147], v[132:135], v[96:99]
ds_read_b128 v[144:147], v25 offset:12288
v_mfma_f32_16x16x32_bf16 v[100:103], a[148:151], v[132:135], v[100:103]
v_mfma_f32_16x16x32_bf16 v[104:107], a[152:155], v[132:135], v[104:107]
s_waitcnt vmcnt(8)
s_barrier
v_mfma_f32_16x16x32_bf16 v[108:111], a[156:159], v[132:135], v[108:111]
v_cvt_pk_bf16_f32 v164, v112, v113
v_cvt_pk_bf16_f32 v165, v114, v115
v_mfma_f32_16x16x32_bf16 v[128:131], v[60:63], v[132:135], 0
s_waitcnt lgkmcnt(2)
v_cvt_pk_bf16_f32 v166, v116, v117
)"
R"(v_cvt_pk_bf16_f32 v167, v118, v119
v_mfma_f32_16x16x32_bf16 v[96:99], a[160:163], v[136:139], v[96:99]
ds_read_b128 v[132:135], v25 offset:13312
v_add_f32 v254, v112, v113
v_mfma_f32_16x16x32_bf16 v[100:103], a[164:167], v[136:139], v[100:103]
global_store_dwordx4 v13, v[164:167], s[38:39] offset:0 sc1
v_mfma_f32_16x16x32_bf16 v[104:107], a[168:171], v[136:139], v[104:107]
v_cvt_pk_bf16_f32 v168, v120, v121
v_cvt_pk_bf16_f32 v169, v122, v123
v_mfma_f32_16x16x32_bf16 v[108:111], a[172:175], v[136:139], v[108:111]
v_cvt_pk_bf16_f32 v170, v124, v125
v_cvt_pk_bf16_f32 v171, v126, v127
v_mfma_f32_16x16x32_bf16 v[128:131], v[64:67], v[136:139], v[128:131]
s_waitcnt lgkmcnt(2)
v_mul_f32 v255, v112, v112
v_fmac_f32 v255, v113, v113
v_mfma_f32_16x16x32_bf16 v[96:99], a[176:179], v[140:143], v[96:99]
s_and_b32 s75, s70, 15
ds_read_b128 v[136:139], v25 offset:14336
v_add_f32 v254, v254, v114
v_mfma_f32_16x16x32_bf16 v[100:103], a[180:183], v[140:143], v[100:103]
s_cselect_b64 s[42:43], s[42:43], s[46:47]
global_store_dwordx4 v13, v[168:171], s[38:39] offset:1024 sc1
v_mfma_f32_16x16x32_bf16 v[104:107], a[184:187], v[140:143], v[104:107]
s_cmp_eq_u32 s70, 32
v_add_u32 v13, 18688, v13
v_fmac_f32 v255, v114, v114
v_mfma_f32_16x16x32_bf16 v[108:111], a[188:191], v[140:143], v[108:111]
s_cselect_b64 s[42:43], s[44:45], s[42:43]
v_add_f32 v254, v254, v115
v_fmac_f32 v255, v115, v115
v_mfma_f32_16x16x32_bf16 v[128:131], v[68:71], v[140:143], v[128:131]
s_waitcnt lgkmcnt(2)
v_add_f32 v254, v254, v116
v_fmac_f32 v255, v116, v116
v_mfma_f32_16x16x32_bf16 v[96:99], a[192:195], v[144:147], v[96:99]
s_xor_b32 s49, s49, 0x10000
ds_read_b128 v[140:143], v25 offset:15360
v_add_f32 v254, v254, v117
v_mfma_f32_16x16x32_bf16 v[100:103], a[196:199], v[144:147], v[100:103]
s_mov_b32 m0, 49152
v_fmac_f32 v255, v117, v117
v_add_f32 v254, v254, v118
v_mfma_f32_16x16x32_bf16 v[104:107], a[200:203], v[144:147], v[104:107]
v_fmac_f32 v255, v118, v118
v_add_f32 v254, v254, v119
v_mfma_f32_16x16x32_bf16 v[108:111], a[204:207], v[144:147], v[108:111]
v_fmac_f32 v255, v119, v119
v_add_f32 v254, v254, v120
v_mfma_f32_16x16x32_bf16 v[128:131], v[72:75], v[144:147], v[128:131]
s_waitcnt lgkmcnt(2)
v_fmac_f32 v255, v120, v120
v_add_f32 v254, v254, v121
v_mfma_f32_16x16x32_bf16 v[96:99], a[208:211], v[132:135], v[96:99]
ds_read_b128 v[144:147], v25 offset:16384
global_load_lds_dwordx4 v16, s[42:43] offset:0 sc1
v_mfma_f32_16x16x32_bf16 v[100:103], a[212:215], v[132:135], v[100:103]
v_fmac_f32 v255, v121, v121
v_add_f32 v254, v254, v122
v_mfma_f32_16x16x32_bf16 v[104:107], a[216:219], v[132:135], v[104:107]
v_fmac_f32 v255, v122, v122
v_add_f32 v254, v254, v123
v_mfma_f32_16x16x32_bf16 v[108:111], a[220:223], v[132:135], v[108:111]
v_fmac_f32 v255, v123, v123
v_add_f32 v254, v254, v124
v_mfma_f32_16x16x32_bf16 v[128:131], v[76:79], v[132:135], v[128:131]
s_waitcnt vmcnt(9)
ds_read_b128 v[148:151], v19 offset:8192
ds_read_b128 v[152:155], v20 offset:8192
s_waitcnt lgkmcnt(4)
v_mfma_f32_16x16x32_bf16 v[96:99], a[224:227], v[136:139], v[96:99]
ds_read_b128 v[132:135], v25 offset:17408
v_fmac_f32 v255, v124, v124
v_mfma_f32_16x16x32_bf16 v[100:103], a[228:231], v[136:139], v[100:103]
v_add_f32 v254, v254, v125
v_fmac_f32 v255, v125, v125
v_mfma_f32_16x16x32_bf16 v[104:107], a[232:235], v[136:139], v[104:107]
global_load_lds_dwordx4 v17, s[42:43] offset:1024 sc1
v_add_f32 v254, v254, v126
v_mfma_f32_16x16x32_bf16 v[108:111], a[236:239], v[136:139], v[108:111]
v_fmac_f32 v255, v126, v126
v_add_f32 v254, v254, v127
v_mfma_f32_16x16x32_bf16 v[128:131], v[80:83], v[136:139], v[128:131]
s_waitcnt lgkmcnt(4)
v_fmac_f32 v255, v127, v127
v_mfma_f32_16x16x32_bf16 v[96:99], a[240:243], v[140:143], v[96:99]
ds_read_b128 v[136:139], v25 offset:0
v_mfma_f32_16x16x32_bf16 v[100:103], a[244:247], v[140:143], v[100:103]
ds_write_b64 v27, v[254:255] offset:45568
v_mfma_f32_16x16x32_bf16 v[104:107], a[248:251], v[140:143], v[104:107]
s_add_u32 m0, s49, 0
s_waitcnt lgkmcnt(4)
v_lshlrev_b32 v112, 16, v148
v_and_b32 v113, 0xffff0000, v148
v_mfma_f32_16x16x32_bf16 v[108:111], a[252:255], v[140:143], v[108:111]
v_lshlrev_b32 v114, 16, v149
v_and_b32 v115, 0xffff0000, v149
v_mfma_f32_16x16x32_bf16 v[128:131], v[84:87], v[140:143], v[128:131]
v_lshlrev_b32 v116, 16, v150
v_and_b32 v117, 0xffff0000, v150
v_mfma_f32_16x16x32_bf16 v[96:99], v[28:31], v[144:147], v[96:99]
s_sub_u32 s42, s42, 18432
ds_read_b128 v[140:143], v25 offset:1024
v_lshlrev_b32 v118, 16, v151
v_mfma_f32_16x16x32_bf16 v[100:103], v[32:35], v[144:147], v[100:103]
s_subb_u32 s43, s43, 0
v_and_b32 v119, 0xffff0000, v151
s_waitcnt lgkmcnt(4)
v_lshlrev_b32 v120, 16, v152
v_mfma_f32_16x16x32_bf16 v[104:107], v[36:39], v[144:147], v[104:107]
v_and_b32 v121, 0xffff0000, v152
v_lshlrev_b32 v122, 16, v153
v_mfma_f32_16x16x32_bf16 v[108:111], v[40:43], v[144:147], v[108:111]
v_and_b32 v123, 0xffff0000, v153
v_lshlrev_b32 v124, 16, v154
v_mfma_f32_16x16x32_bf16 v[128:131], v[88:91], v[144:147], v[128:131]
s_waitcnt lgkmcnt(3)
v_and_b32 v125, 0xffff0000, v154
v_lshlrev_b32 v126, 16, v155
v_mfma_f32_16x16x32_bf16 v[96:99], v[44:47], v[132:135], v[96:99]
ds_read_b128 v[144:147], v25 offset:2048
v_and_b32 v127, 0xffff0000, v155
v_mfma_f32_16x16x32_bf16 v[100:103], v[48:51], v[132:135], v[100:103]
v_mfma_f32_16x16x32_bf16 v[104:107], v[52:55], v[132:135], v[104:107]
v_mfma_f32_16x16x32_bf16 v[108:111], v[56:59], v[132:135], v[108:111]
v_mfma_f32_16x16x32_bf16 v[128:131], v[92:95], v[132:135], v[128:131]
s_waitcnt lgkmcnt(3)
v_mfma_f32_16x16x32_bf16 v[96:99], a[0:3], v[136:139], v[96:99]
ds_read_b128 v[132:135], v25 offset:3072
v_mfma_f32_16x16x32_bf16 v[100:103], a[4:7], v[136:139], v[100:103]
v_mfma_f32_16x16x32_bf16 v[104:107], a[8:11], v[136:139], v[104:107]
v_mfma_f32_16x16x32_bf16 v[108:111], a[12:15], v[136:139], v[108:111]
s_waitcnt lgkmcnt(2)
v_mfma_f32_16x16x32_bf16 v[96:99], a[16:19], v[140:143], v[96:99]
ds_read_b128 v[136:139], v25 offset:4096
v_mfma_f32_16x16x32_bf16 v[100:103], a[20:23], v[140:143], v[100:103]
s_waitcnt vmcnt(6)
v_sub_u32 v200, v200, v22
v_cmp_lt_u32 vcc, 0x7f, v200
v_mfma_f32_16x16x32_bf16 v[104:107], a[24:27], v[140:143], v[104:107]
v_mfma_f32_16x16x32_bf16 v[108:111], a[28:31], v[140:143], v[108:111]
s_waitcnt lgkmcnt(2)
v_mfma_f32_16x16x32_bf16 v[96:99], a[32:35], v[144:147], v[96:99]
s_and_b64 vcc, vcc, s[76:77]
s_cbranch_vccnz .Lwo_sp8
.Lwo_rt9:
ds_read_b128 v[140:143], v25 offset:5120
v_mfma_f32_16x16x32_bf16 v[100:103], a[36:39], v[144:147], v[100:103]
v_mfma_f32_16x16x32_bf16 v[104:107], a[40:43], v[144:147], v[104:107]
s_barrier
v_mfma_f32_16x16x32_bf16 v[108:111], a[44:47], v[144:147], v[108:111]
s_waitcnt lgkmcnt(2)
ds_write_b128 v26, v[128:131] offset:37888
v_mfma_f32_16x16x32_bf16 v[96:99], a[48:51], v[132:135], v[96:99]
ds_read_b128 v[144:147], v25 offset:6144
v_add_u32 v22, 1, v22
v_mfma_f32_16x16x32_bf16 v[100:103], a[52:55], v[132:135], v[100:103]
v_add_lshl_u32 v253, v22, 0, 7
v_bfi_b32 v253, s69, v253, v21
v_mfma_f32_16x16x32_bf16 v[104:107], a[56:59], v[132:135], v[104:107]
global_load_lds_dwordx4 v11, s[38:39] offset:-2048 sc1
v_mfma_f32_16x16x32_bf16 v[108:111], a[60:63], v[132:135], v[108:111]
s_waitcnt lgkmcnt(3)
v_mfma_f32_16x16x32_bf16 v[96:99], a[64:67], v[136:139], v[96:99]
ds_read_b128 v[132:135], v25 offset:7168
v_mfma_f32_16x16x32_bf16 v[100:103], a[68:71], v[136:139], v[100:103]
global_load_lds_dwordx4 v11, s[38:39] offset:-1024 sc1
v_mfma_f32_16x16x32_bf16 v[104:107], a[72:75], v[136:139], v[104:107]
v_mfma_f32_16x16x32_bf16 v[108:111], a[76:79], v[136:139], v[108:111]
s_waitcnt lgkmcnt(3)
v_mfma_f32_16x16x32_bf16 v[96:99], a[80:83], v[140:143], v[96:99]
ds_read_b128 v[136:139], v25 offset:8192
global_load_lds_dwordx4 v11, s[38:39] offset:0 sc1
v_mfma_f32_16x16x32_bf16 v[100:103], a[84:87], v[140:143], v[100:103]
v_mfma_f32_16x16x32_bf16 v[104:107], a[88:91], v[140:143], v[104:107]
v_mfma_f32_16x16x32_bf16 v[108:111], a[92:95], v[140:143], v[108:111]
s_waitcnt lgkmcnt(2)
global_load_dword v201, v253, s[38:39] sc1
global_load_lds_dwordx4 v11, s[38:39] offset:1024 sc1
v_mfma_f32_16x16x32_bf16 v[96:99], a[96:99], v[144:147], v[96:99]
v_mfma_f32_16x16x32_bf16 v[100:103], a[100:103], v[144:147], v[100:103]
v_mfma_f32_16x16x32_bf16 v[104:107], a[104:107], v[144:147], v[104:107]
s_waitcnt vmcnt(9)
s_barrier
v_mfma_f32_16x16x32_bf16 v[108:111], a[108:111], v[144:147], v[108:111]
s_waitcnt lgkmcnt(1)
ds_read_b128 v[140:143], v25 offset:27648
global_load_lds_dwordx4 v11, s[38:39] offset:2048 sc1
v_mfma_f32_16x16x32_bf16 v[96:99], a[112:115], v[132:135], v[96:99]
ds_read_b128 v[144:147], v25 offset:28672
v_add_u32 v11, 18432, v11
v_mfma_f32_16x16x32_bf16 v[100:103], a[116:119], v[132:135], v[100:103]
v_min_u32 v11, v11, v12
v_mfma_f32_16x16x32_bf16 v[104:107], a[120:123], v[132:135], v[104:107]
v_mfma_f32_16x16x32_bf16 v[108:111], a[124:127], v[132:135], v[108:111]
s_waitcnt lgkmcnt(2)
v_mfma_f32_16x16x32_bf16 v[96:99], a[128:131], v[136:139], v[96:99]
ds_read_b128 v[132:135], v25 offset:29696
v_mfma_f32_16x16x32_bf16 v[100:103], a[132:135], v[136:139], v[100:103]
v_mfma_f32_16x16x32_bf16 v[104:107], a[136:139], v[136:139], v[104:107]
v_mfma_f32_16x16x32_bf16 v[108:111], a[140:143], v[136:139], v[108:111]
s_waitcnt lgkmcnt(2)
v_mfma_f32_16x16x32_bf16 v[112:115], a[144:147], v[140:143], v[112:115]
ds_read_b128 v[136:139], v25 offset:30720
v_mfma_f32_16x16x32_bf16 v[116:119], a[148:151], v[140:143], v[116:119]
v_mfma_f32_16x16x32_bf16 v[120:123], a[152:155], v[140:143], v[120:123]
s_waitcnt vmcnt(8)
s_barrier
v_mfma_f32_16x16x32_bf16 v[124:127], a[156:159], v[140:143], v[124:127]
v_cvt_pk_bf16_f32 v164, v96, v97
v_cvt_pk_bf16_f32 v165, v98, v99
v_mfma_f32_16x16x32_bf16 v[128:131], v[60:63], v[140:143], 0
s_waitcnt lgkmcnt(2)
v_cvt_pk_bf16_f32 v166, v100, v101
v_cvt_pk_bf16_f32 v167, v102, v103
v_mfma_f32_16x16x32_bf16 v[112:115], a[160:163], v[144:147], v[112:115]
ds_read_b128 v[140:143], v25 offset:31744
v_add_f32 v254, v96, v97
v_mfma_f32_16x16x32_bf16 v[116:119], a[164:167], v[144:147], v[116:119]
global_store_dwordx4 v13, v[164:167], s[38:39] offset:0 sc1
v_mfma_f32_16x16x32_bf16 v[120:123], a[168:171], v[144:147], v[120:123]
v_cvt_pk_bf16_f32 v168, v104, v105
v_cvt_pk_bf16_f32 v169, v106, v107
v_mfma_f32_16x16x32_bf16 v[124:127], a[172:175], v[144:147], v[124:127]
v_cvt_pk_bf16_f32 v170, v108, v109
v_cvt_pk_bf16_f32 v171, v110, v111
v_mfma_f32_16x16x32_bf16 v[128:131], v[64:67], v[144:147], v[128:131]
s_waitcnt lgkmcnt(2)
v_mul_f32 v255, v96, v96
v_fmac_f32 v255, v97, v97
v_mfma_f32_16x16x32_bf16 v[112:115], a[176:179], v[132:135], v[112:115]
s_mov_b32 m0, 57344
ds_read_b128 v[144:147], v25 offset:32768
v_add_f32 v254, v254, v98
v_mfma_f32_16x16x32_bf16 v[116:119], a[180:183], v[132:135], v[116:119]
global_store_dwordx4 v13, v[168:171], s[38:39] offset:1024 sc1
v_mfma_f32_16x16x32_bf16 v[120:123], a[184:187], v[132:135], v[120:123]
v_add_u32 v13, 18688, v13
v_fmac_f32 v255, v98, v98
v_mfma_f32_16x16x32_bf16 v[124:127], a[188:191], v[132:135], v[124:127]
v_add_f32 v254, v254, v99
v_fmac_f32 v255, v99, v99
v_mfma_f32_16x16x32_bf16 v[128:131], v[68:71], v[132:135], v[128:131]
s_waitcnt lgkmcnt(2)
v_add_f32 v254, v254, v100
v_fmac_f32 v255, v100, v100
v_mfma_f32_16x16x32_bf16 v[112:115], a[192:195], v[136:139], v[112:115]
ds_read_b128 v[132:135], v25 offset:33792
v_add_f32 v254, v254, v101
v_mfma_f32_16x16x32_bf16 v[116:119], a[196:199], v[136:139], v[116:119]
v_fmac_f32 v255, v101, v101
v_add_f32 v254, v254, v102
v_mfma_f32_16x16x32_bf16 v[120:123], a[200:203], v[136:139], v[120:123]
v_fmac_f32 v255, v102, v102
v_add_f32 v254, v254, v103
v_mfma_f32_16x16x32_bf16 v[124:127], a[204:207], v[136:139], v[124:127]
v_fmac_f32 v255, v103, v103
v_add_f32 v254, v254, v104
v_mfma_f32_16x16x32_bf16 v[128:131], v[72:75], v[136:139], v[128:131]
s_waitcnt lgkmcnt(2)
v_fmac_f32 v255, v104, v104
v_add_f32 v254, v254, v105
v_mfma_f32_16x16x32_bf16 v[112:115], a[208:211], v[140:143], v[112:115]
ds_read_b128 v[136:139], v25 offset:34816
global_load_lds_dwordx4 v16, s[42:43] offset:0 sc1
v_mfma_f32_16x16x32_bf16 v[116:119], a[212:215], v[140:143], v[116:119]
v_fmac_f32 v255, v105, v105
v_add_f32 v254, v254, v106
v_mfma_f32_16x16x32_bf16 v[120:123], a[216:219], v[140:143], v[120:123]
v_fmac_f32 v255, v106, v106
v_add_f32 v254, v254, v107
v_mfma_f32_16x16x32_bf16 v[124:127], a[220:223], v[140:143], v[124:127]
v_fmac_f32 v255, v107, v107
v_add_f32 v254, v254, v108
v_mfma_f32_16x16x32_bf16 v[128:131], v[76:79], v[140:143], v[128:131]
s_waitcnt vmcnt(9)
ds_read_b128 v[148:151], v19 offset:0
ds_read_b128 v[152:155], v20 offset:0
s_waitcnt lgkmcnt(4)
v_mfma_f32_16x16x32_bf16 v[112:115], a[224:227], v[144:147], v[112:115]
ds_read_b128 v[140:143], v25 offset:35840
v_fmac_f32 v255, v108, v108
v_mfma_f32_16x16x32_bf16 v[116:119], a[228:231], v[144:147], v[116:119]
v_add_f32 v254, v254, v109
v_fmac_f32 v255, v109, v109
v_mfma_f32_16x16x32_bf16 v[120:123], a[232:235], v[144:147], v[120:123]
global_load_lds_dwordx4 v17, s[42:43] offset:1024 sc1
v_add_f32 v254, v254, v110
v_mfma_f32_16x16x32_bf16 v[124:127], a[236:239], v[144:147], v[124:127]
v_fmac_f32 v255, v110, v110
v_add_f32 v254, v254, v111
v_mfma_f32_16x16x32_bf16 v[128:131], v[80:83], v[144:147], v[128:131]
s_waitcnt lgkmcnt(4)
v_fmac_f32 v255, v111, v111
v_mfma_f32_16x16x32_bf16 v[112:115], a[240:243], v[132:135], v[112:115]
ds_read_b128 v[144:147], v25 offset:18432
v_mfma_f32_16x16x32_bf16 v[116:119], a[244:247], v[132:135], v[116:119]
ds_write_b64 v27, v[254:255] offset:43520
v_mfma_f32_16x16x32_bf16 v[120:123], a[248:251], v[132:135], v[120:123]
s_add_u32 m0, s49, 18432
s_waitcnt lgkmcnt(4)
v_lshlrev_b32 v96, 16, v148
v_and_b32 v97, 0xffff0000, v148
v_mfma_f32_16x16x32_bf16 v[124:127], a[252:255], v[132:135], v[124:127]
v_lshlrev_b32 v98, 16, v149
v_and_b32 v99, 0xffff0000, v149
v_mfma_f32_16x16x32_bf16 v[128:131], v[84:87], v[132:135], v[128:131]
v_lshlrev_b32 v100, 16, v150
v_and_b32 v101, 0xffff0000, v150
v_mfma_f32_16x16x32_bf16 v[112:115], v[28:31], v[136:139], v[112:115]
s_sub_u32 s42, s42, 18432
ds_read_b128 v[132:135], v25 offset:19456
v_lshlrev_b32 v102, 16, v151
v_mfma_f32_16x16x32_bf16 v[116:119], v[32:35], v[136:139], v[116:119]
s_subb_u32 s43, s43, 0
v_and_b32 v103, 0xffff0000, v151
s_waitcnt lgkmcnt(4)
v_lshlrev_b32 v104, 16, v152
v_mfma_f32_16x16x32_bf16 v[120:123], v[36:39], v[136:139], v[120:123]
v_and_b32 v105, 0xffff0000, v152
v_lshlrev_b32 v106, 16, v153
v_mfma_f32_16x16x32_bf16 v[124:127], v[40:43], v[136:139], v[124:127]
v_and_b32 v107, 0xffff0000, v153
v_lshlrev_b32 v108, 16, v154
v_mfma_f32_16x16x32_bf16 v[128:131], v[88:91], v[136:139], v[128:131]
s_waitcnt lgkmcnt(3)
v_and_b32 v109, 0xffff0000, v154
v_lshlrev_b32 v110, 16, v155
v_mfma_f32_16x16x32_bf16 v[112:115], v[44:47], v[140:143], v[112:115]
ds_read_b128 v[136:139], v25 offset:20480
v_and_b32 v111, 0xffff0000, v155
v_mfma_f32_16x16x32_bf16 v[116:119], v[48:51], v[140:143], v[116:119]
v_mfma_f32_16x16x32_bf16 v[120:123], v[52:55], v[140:143], v[120:123]
v_mfma_f32_16x16x32_bf16 v[124:127], v[56:59], v[140:143], v[124:127]
v_mfma_f32_16x16x32_bf16 v[128:131], v[92:95], v[140:143], v[128:131]
s_waitcnt lgkmcnt(3)
v_mfma_f32_16x16x32_bf16 v[112:115], a[0:3], v[144:147], v[112:115]
ds_read_b128 v[140:143], v25 offset:21504
v_mfma_f32_16x16x32_bf16 v[116:119], a[4:7], v[144:147], v[116:119]
v_mfma_f32_16x16x32_bf16 v[120:123], a[8:11], v[144:147], v[120:123]
v_mfma_f32_16x16x32_bf16 v[124:127], a[12:15], v[144:147], v[124:127]
s_waitcnt lgkmcnt(2)
v_mfma_f32_16x16x32_bf16 v[112:115], a[16:19], v[132:135], v[112:115]
ds_read_b128 v[144:147], v25 offset:22528
v_mfma_f32_16x16x32_bf16 v[116:119], a[20:23], v[132:135], v[116:119]
s_waitcnt vmcnt(6)
v_sub_u32 v201, v201, v22
v_cmp_lt_u32 vcc, 0x7f, v201
v_mfma_f32_16x16x32_bf16 v[120:123], a[24:27], v[132:135], v[120:123]
v_mfma_f32_16x16x32_bf16 v[124:127], a[28:31], v[132:135], v[124:127]
s_waitcnt lgkmcnt(2)
v_mfma_f32_16x16x32_bf16 v[112:115], a[32:35], v[136:139], v[112:115]
s_and_b64 vcc, vcc, s[76:77]
s_cbranch_vccnz .Lwo_sp10
.Lwo_rt11:
ds_read_b128 v[132:135], v25 offset:23552
v_mfma_f32_16x16x32_bf16 v[116:119], a[36:39], v[136:139], v[116:119]
v_mfma_f32_16x16x32_bf16 v[120:123], a[40:43], v[136:139], v[120:123]
s_barrier
v_mfma_f32_16x16x32_bf16 v[124:127], a[44:47], v[136:139], v[124:127]
s_waitcnt lgkmcnt(2)
ds_write_b128 v26, v[128:131] offset:40960
v_mfma_f32_16x16x32_bf16 v[112:115], a[48:51], v[140:143], v[112:115]
s_mov_b64 s[76:77], -1
ds_read_b128 v[136:139], v25 offset:24576
v_add_u32 v22, 1, v22
v_mfma_f32_16x16x32_bf16 v[116:119], a[52:55], v[140:143], v[116:119]
v_add_lshl_u32 v253, v22, 0, 7
v_bfi_b32 v253, s69, v253, v21
v_mfma_f32_16x16x32_bf16 v[120:123], a[56:59], v[140:143], v[120:123]
global_load_lds_dwordx4 v11, s[38:39] offset:-2048 sc1
.Lwo_in12:
v_mfma_f32_16x16x32_bf16 v[124:127], a[60:63], v[140:143], v[124:127]
s_waitcnt lgkmcnt(3)
v_mfma_f32_16x16x32_bf16 v[112:115], a[64:67], v[144:147], v[112:115]
ds_read_b128 v[140:143], v25 offset:25600
v_mfma_f32_16x16x32_bf16 v[116:119], a[68:71], v[144:147], v[116:119]
global_load_lds_dwordx4 v11, s[38:39] offset:-1024 sc1
v_mfma_f32_16x16x32_bf16 v[120:123], a[72:75], v[144:147], v[120:123]
v_mfma_f32_16x16x32_bf16 v[124:127], a[76:79], v[144:147], v[124:127]
s_waitcnt lgkmcnt(3)
v_mfma_f32_16x16x32_bf16 v[112:115], a[80:83], v[132:135], v[112:115]
ds_read_b128 v[144:147], v25 offset:26624
global_load_lds_dwordx4 v11, s[38:39] offset:0 sc1
v_mfma_f32_16x16x32_bf16 v[116:119], a[84:87], v[132:135], v[116:119]
v_mfma_f32_16x16x32_bf16 v[120:123], a[88:91], v[132:135], v[120:123]
v_mfma_f32_16x16x32_bf16 v[124:127], a[92:95], v[132:135], v[124:127]
s_waitcnt lgkmcnt(2)
v_xor_b32 v25, 0x10000, v25
global_load_dword v200, v253, s[38:39] sc1
v_mfma_f32_16x16x32_bf16 v[112:115], a[96:99], v[136:139], v[112:115]
global_load_lds_dwordx4 v11, s[38:39] offset:1024 sc1
v_mfma_f32_16x16x32_bf16 v[116:119], a[100:103], v[136:139], v[116:119]
v_mfma_f32_16x16x32_bf16 v[120:123], a[104:107], v[136:139], v[120:123]
s_waitcnt vmcnt(9)
s_barrier
v_mfma_f32_16x16x32_bf16 v[124:127], a[108:111], v[136:139], v[124:127]
s_waitcnt lgkmcnt(1)
ds_read_b128 v[132:135], v25 offset:9216
global_load_lds_dwordx4 v11, s[38:39] offset:2048 sc1
)"
R"(v_mfma_f32_16x16x32_bf16 v[112:115], a[112:115], v[140:143], v[112:115]
ds_read_b128 v[136:139], v25 offset:10240
v_add_u32 v11, 18432, v11
v_mfma_f32_16x16x32_bf16 v[116:119], a[116:119], v[140:143], v[116:119]
v_min_u32 v11, v11, v12
v_mfma_f32_16x16x32_bf16 v[120:123], a[120:123], v[140:143], v[120:123]
v_mfma_f32_16x16x32_bf16 v[124:127], a[124:127], v[140:143], v[124:127]
s_waitcnt lgkmcnt(2)
v_mfma_f32_16x16x32_bf16 v[112:115], a[128:131], v[144:147], v[112:115]
ds_read_b128 v[140:143], v25 offset:11264
v_mfma_f32_16x16x32_bf16 v[116:119], a[132:135], v[144:147], v[116:119]
v_mfma_f32_16x16x32_bf16 v[120:123], a[136:139], v[144:147], v[120:123]
v_mfma_f32_16x16x32_bf16 v[124:127], a[140:143], v[144:147], v[124:127]
s_sub_u32 s70, s70, 1
s_cbranch_scc0 .Lwo_top13
s_waitcnt vmcnt(8)
s_barrier
v_cvt_pk_bf16_f32 v164, v112, v113
v_cvt_pk_bf16_f32 v165, v114, v115
v_cvt_pk_bf16_f32 v166, v116, v117
v_cvt_pk_bf16_f32 v167, v118, v119
global_store_dwordx4 v13, v[164:167], s[38:39] offset:0 sc1
v_cvt_pk_bf16_f32 v168, v120, v121
v_cvt_pk_bf16_f32 v169, v122, v123
v_cvt_pk_bf16_f32 v170, v124, v125
v_cvt_pk_bf16_f32 v171, v126, v127
global_store_dwordx4 v13, v[168:171], s[38:39] offset:1024 sc1
v_add_f32 v254, v112, v113
v_mul_f32 v255, v112, v112
v_fmac_f32 v255, v113, v113
v_add_f32 v254, v254, v114
v_fmac_f32 v255, v114, v114
v_add_f32 v254, v254, v115
v_fmac_f32 v255, v115, v115
v_add_f32 v254, v254, v116
v_fmac_f32 v255, v116, v116
v_add_f32 v254, v254, v117
v_fmac_f32 v255, v117, v117
v_add_f32 v254, v254, v118
v_fmac_f32 v255, v118, v118
v_add_f32 v254, v254, v119
v_fmac_f32 v255, v119, v119
v_add_f32 v254, v254, v120
v_fmac_f32 v255, v120, v120
v_add_f32 v254, v254, v121
v_fmac_f32 v255, v121, v121
v_add_f32 v254, v254, v122
v_fmac_f32 v255, v122, v122
v_add_f32 v254, v254, v123
v_fmac_f32 v255, v123, v123
v_add_f32 v254, v254, v124
v_fmac_f32 v255, v124, v124
v_add_f32 v254, v254, v125
v_fmac_f32 v255, v125, v125
v_add_f32 v254, v254, v126
v_fmac_f32 v255, v126, v126
v_add_f32 v254, v254, v127
v_fmac_f32 v255, v127, v127
ds_write_b64 v27, v[254:255] offset:45568
s_waitcnt vmcnt(0) lgkmcnt(0)
s_barrier
s_barrier
s_endpgm
.Lwo_w3:
s_add_u32 m0, s48, 4096
s_nop 0
global_load_lds_dwordx4 v0, s[40:41] offset:-4096
global_load_lds_dwordx4 v1, s[40:41] offset:-3072
global_load_lds_dwordx4 v2, s[40:41] offset:-2048
global_load_lds_dwordx4 v3, s[40:41] offset:-1024
global_load_lds_dwordx4 v4, s[40:41] offset:0
global_load_lds_dwordx4 v5, s[40:41] offset:1024
global_load_lds_dwordx4 v6, s[40:41] offset:2048
global_load_lds_dwordx4 v7, s[40:41] offset:3072
s_add_u32 m0, s48, 12288
s_nop 0
global_load_lds_dwordx4 v8, s[40:41] offset:-4096
s_add_u32 s40, s40, 73728
s_addc_u32 s41, s41, 0
s_add_u32 m0, s48, 40960
s_nop 0
global_load_lds_dwordx4 v0, s[40:41] offset:-4096
global_load_lds_dwordx4 v1, s[40:41] offset:-3072
global_load_lds_dwordx4 v2, s[40:41] offset:-2048
global_load_lds_dwordx4 v3, s[40:41] offset:-1024
global_load_lds_dwordx4 v4, s[40:41] offset:0
global_load_lds_dwordx4 v5, s[40:41] offset:1024
global_load_lds_dwordx4 v6, s[40:41] offset:2048
global_load_lds_dwordx4 v7, s[40:41] offset:3072
s_add_u32 m0, s48, 49152
s_nop 0
global_load_lds_dwordx4 v8, s[40:41] offset:-4096
s_add_u32 s40, s40, 73728
s_addc_u32 s41, s41, 0
s_add_u32 m0, s48, 77824
s_nop 0
global_load_lds_dwordx4 v0, s[40:41] offset:-4096
global_load_lds_dwordx4 v1, s[40:41] offset:-3072
global_load_lds_dwordx4 v2, s[40:41] offset:-2048
global_load_lds_dwordx4 v3, s[40:41] offset:-1024
global_load_lds_dwordx4 v4, s[40:41] offset:0
global_load_lds_dwordx4 v5, s[40:41] offset:1024
global_load_lds_dwordx4 v6, s[40:41] offset:2048
global_load_lds_dwordx4 v7, s[40:41] offset:3072
s_add_u32 m0, s48, 86016
s_nop 0
global_load_lds_dwordx4 v8, s[40:41] offset:-4096
s_add_u32 s40, s40, 73728
s_addc_u32 s41, s41, 0
v_add_u32 v204, 0x10000, v9
v_add_u32 v205, 0x20000, v9
v_add_u32 v206, 0x10000, v10
v_add_u32 v207, 0x20000, v10
s_waitcnt lgkmcnt(0)
s_mul_i32 s37, s37, 0x9E3779B1
s_add_u32 s37, s37, 0x7F4A7C15
v_add_u32 v22, s37, v22
v_add_u32 v24, s37, v24
s_waitcnt vmcnt(18)
s_barrier
s_add_u32 m0, s48, 114688
ds_read_b64_tr_b16 a[0:1], v9 offset:0
ds_read_b64_tr_b16 a[2:3], v9 offset:2304
global_load_lds_dwordx4 v0, s[40:41] offset:-4096
ds_read_b64_tr_b16 a[4:5], v9 offset:8
ds_read_b64_tr_b16 a[6:7], v9 offset:2312
global_load_lds_dwordx4 v1, s[40:41] offset:-3072
ds_read_b64_tr_b16 a[8:9], v9 offset:64
global_load_lds_dwordx4 v2, s[40:41] offset:-2048
ds_read_b64_tr_b16 a[10:11], v9 offset:2368
ds_read_b64_tr_b16 a[12:13], v9 offset:72
global_load_lds_dwordx4 v3, s[40:41] offset:-1024
ds_read_b64_tr_b16 a[14:15], v9 offset:2376
global_load_lds_dwordx4 v4, s[40:41] offset:0
ds_read_b64_tr_b16 v[60:61], v10 offset:0
ds_read_b64_tr_b16 v[62:63], v10 offset:2304
global_load_lds_dwordx4 v5, s[40:41] offset:1024
ds_read_b64_tr_b16 a[16:17], v9 offset:18432
global_load_lds_dwordx4 v6, s[40:41] offset:2048
ds_read_b64_tr_b16 a[18:19], v9 offset:20736
ds_read_b64_tr_b16 a[20:21], v9 offset:18440
global_load_lds_dwordx4 v7, s[40:41] offset:3072
ds_read_b64_tr_b16 a[22:23], v9 offset:20744
s_add_u32 m0, s48, 122880
ds_read_b64_tr_b16 a[24:25], v9 offset:18496
ds_read_b64_tr_b16 a[26:27], v9 offset:20800
global_load_lds_dwordx4 v8, s[40:41] offset:-4096
ds_read_b64_tr_b16 a[28:29], v9 offset:18504
s_add_u32 s40, s40, 73728
ds_read_b64_tr_b16 a[30:31], v9 offset:20808
ds_read_b64_tr_b16 v[64:65], v10 offset:18432
s_addc_u32 s41, s41, 0
ds_read_b64_tr_b16 v[66:67], v10 offset:20736
s_waitcnt vmcnt(18) lgkmcnt(0)
s_barrier
s_add_u32 m0, s48, 4096
ds_read_b64_tr_b16 a[32:33], v9 offset:36864
ds_read_b64_tr_b16 a[34:35], v9 offset:39168
global_load_lds_dwordx4 v0, s[40:41] offset:-4096
ds_read_b64_tr_b16 a[36:37], v9 offset:36872
ds_read_b64_tr_b16 a[38:39], v9 offset:39176
global_load_lds_dwordx4 v1, s[40:41] offset:-3072
ds_read_b64_tr_b16 a[40:41], v9 offset:36928
global_load_lds_dwordx4 v2, s[40:41] offset:-2048
ds_read_b64_tr_b16 a[42:43], v9 offset:39232
ds_read_b64_tr_b16 a[44:45], v9 offset:36936
global_load_lds_dwordx4 v3, s[40:41] offset:-1024
ds_read_b64_tr_b16 a[46:47], v9 offset:39240
global_load_lds_dwordx4 v4, s[40:41] offset:0
ds_read_b64_tr_b16 v[68:69], v10 offset:36864
ds_read_b64_tr_b16 v[70:71], v10 offset:39168
global_load_lds_dwordx4 v5, s[40:41] offset:1024
ds_read_b64_tr_b16 a[48:49], v9 offset:55296
global_load_lds_dwordx4 v6, s[40:41] offset:2048
ds_read_b64_tr_b16 a[50:51], v9 offset:57600
ds_read_b64_tr_b16 a[52:53], v9 offset:55304
global_load_lds_dwordx4 v7, s[40:41] offset:3072
ds_read_b64_tr_b16 a[54:55], v9 offset:57608
s_add_u32 m0, s48, 12288
ds_read_b64_tr_b16 a[56:57], v9 offset:55360
ds_read_b64_tr_b16 a[58:59], v9 offset:57664
global_load_lds_dwordx4 v8, s[40:41] offset:-4096
ds_read_b64_tr_b16 a[60:61], v9 offset:55368
s_add_u32 s40, s40, 73728
ds_read_b64_tr_b16 a[62:63], v9 offset:57672
ds_read_b64_tr_b16 v[72:73], v10 offset:55296
s_addc_u32 s41, s41, 0
ds_read_b64_tr_b16 v[74:75], v10 offset:57600
s_waitcnt vmcnt(18) lgkmcnt(0)
s_barrier
s_add_u32 m0, s48, 40960
ds_read_b64_tr_b16 a[64:65], v204 offset:8192
ds_read_b64_tr_b16 a[66:67], v204 offset:10496
global_load_lds_dwordx4 v0, s[40:41] offset:-4096
ds_read_b64_tr_b16 a[68:69], v204 offset:8200
ds_read_b64_tr_b16 a[70:71], v204 offset:10504
global_load_lds_dwordx4 v1, s[40:41] offset:-3072
ds_read_b64_tr_b16 a[72:73], v204 offset:8256
global_load_lds_dwordx4 v2, s[40:41] offset:-2048
ds_read_b64_tr_b16 a[74:75], v204 offset:10560
ds_read_b64_tr_b16 a[76:77], v204 offset:8264
global_load_lds_dwordx4 v3, s[40:41] offset:-1024
ds_read_b64_tr_b16 a[78:79], v204 offset:10568
global_load_lds_dwordx4 v4, s[40:41] offset:0
ds_read_b64_tr_b16 v[76:77], v206 offset:8192
ds_read_b64_tr_b16 v[78:79], v206 offset:10496
global_load_lds_dwordx4 v5, s[40:41] offset:1024
ds_read_b64_tr_b16 a[80:81], v204 offset:26624
global_load_lds_dwordx4 v6, s[40:41] offset:2048
ds_read_b64_tr_b16 a[82:83], v204 offset:28928
ds_read_b64_tr_b16 a[84:85], v204 offset:26632
global_load_lds_dwordx4 v7, s[40:41] offset:3072
ds_read_b64_tr_b16 a[86:87], v204 offset:28936
s_add_u32 m0, s48, 49152
ds_read_b64_tr_b16 a[88:89], v204 offset:26688
ds_read_b64_tr_b16 a[90:91], v204 offset:28992
global_load_lds_dwordx4 v8, s[40:41] offset:-4096
ds_read_b64_tr_b16 a[92:93], v204 offset:26696
s_add_u32 s40, s40, 73728
ds_read_b64_tr_b16 a[94:95], v204 offset:29000
ds_read_b64_tr_b16 v[80:81], v206 offset:26624
s_addc_u32 s41, s41, 0
ds_read_b64_tr_b16 v[82:83], v206 offset:28928
s_waitcnt vmcnt(18) lgkmcnt(0)
s_barrier
s_add_u32 m0, s48, 77824
ds_read_b64_tr_b16 a[96:97], v204 offset:45056
ds_read_b64_tr_b16 a[98:99], v204 offset:47360
global_load_lds_dwordx4 v0, s[40:41] offset:-4096
ds_read_b64_tr_b16 a[100:101], v204 offset:45064
ds_read_b64_tr_b16 a[102:103], v204 offset:47368
global_load_lds_dwordx4 v1, s[40:41] offset:-3072
ds_read_b64_tr_b16 a[104:105], v204 offset:45120
global_load_lds_dwordx4 v2, s[40:41] offset:-2048
ds_read_b64_tr_b16 a[106:107], v204 offset:47424
ds_read_b64_tr_b16 a[108:109], v204 offset:45128
global_load_lds_dwordx4 v3, s[40:41] offset:-1024
ds_read_b64_tr_b16 a[110:111], v204 offset:47432
global_load_lds_dwordx4 v4, s[40:41] offset:0
ds_read_b64_tr_b16 v[84:85], v206 offset:45056
ds_read_b64_tr_b16 v[86:87], v206 offset:47360
global_load_lds_dwordx4 v5, s[40:41] offset:1024
ds_read_b64_tr_b16 a[112:113], v204 offset:63488
global_load_lds_dwordx4 v6, s[40:41] offset:2048
ds_read_b64_tr_b16 a[114:115], v205 offset:256
ds_read_b64_tr_b16 a[116:117], v204 offset:63496
global_load_lds_dwordx4 v7, s[40:41] offset:3072
ds_read_b64_tr_b16 a[118:119], v205 offset:264
s_add_u32 m0, s48, 86016
ds_read_b64_tr_b16 a[120:121], v204 offset:63552
ds_read_b64_tr_b16 a[122:123], v205 offset:320
global_load_lds_dwordx4 v8, s[40:41] offset:-4096
ds_read_b64_tr_b16 a[124:125], v204 offset:63560
s_add_u32 s40, s40, 73728
ds_read_b64_tr_b16 a[126:127], v205 offset:328
ds_read_b64_tr_b16 v[88:89], v206 offset:63488
s_addc_u32 s41, s41, 0
ds_read_b64_tr_b16 v[90:91], v207 offset:256
s_waitcnt vmcnt(18) lgkmcnt(0)
s_barrier
s_add_u32 m0, s48, 114688
ds_read_b64_tr_b16 a[128:129], v9 offset:0
ds_read_b64_tr_b16 a[130:131], v9 offset:2304
global_load_lds_dwordx4 v0, s[40:41] offset:-4096
ds_read_b64_tr_b16 a[132:133], v9 offset:8
global_load_lds_dwordx4 v1, s[40:41] offset:-3072
ds_read_b64_tr_b16 a[134:135], v9 offset:2312
ds_read_b64_tr_b16 a[136:137], v9 offset:64
global_load_lds_dwordx4 v2, s[40:41] offset:-2048
ds_read_b64_tr_b16 a[138:139], v9 offset:2368
global_load_lds_dwordx4 v3, s[40:41] offset:-1024
ds_read_b64_tr_b16 a[140:141], v9 offset:72
global_load_lds_dwordx4 v4, s[40:41] offset:0
ds_read_b64_tr_b16 a[142:143], v9 offset:2376
ds_read_b64_tr_b16 v[92:93], v10 offset:0
global_load_lds_dwordx4 v5, s[40:41] offset:1024
ds_read_b64_tr_b16 v[94:95], v10 offset:2304
global_load_lds_dwordx4 v6, s[40:41] offset:2048
ds_read_b64_tr_b16 a[144:145], v9 offset:18432
ds_read_b64_tr_b16 a[146:147], v9 offset:20736
global_load_lds_dwordx4 v7, s[40:41] offset:3072
ds_read_b64_tr_b16 a[148:149], v9 offset:18440
s_add_u32 m0, s48, 122880
ds_read_b64_tr_b16 a[150:151], v9 offset:20744
global_load_lds_dwordx4 v8, s[40:41] offset:-4096
ds_read_b64_tr_b16 a[152:153], v9 offset:18496
ds_read_b64_tr_b16 a[154:155], v9 offset:20800
s_add_u32 s40, s40, 73728
ds_read_b64_tr_b16 a[156:157], v9 offset:18504
s_addc_u32 s41, s41, 0
ds_read_b64_tr_b16 a[158:159], v9 offset:20808
s_waitcnt vmcnt(18) lgkmcnt(0)
s_barrier
s_add_u32 m0, s48, 4096
ds_read_b64_tr_b16 a[160:161], v9 offset:36864
ds_read_b64_tr_b16 a[162:163], v9 offset:39168
global_load_lds_dwordx4 v0, s[40:41] offset:-4096
ds_read_b64_tr_b16 a[164:165], v9 offset:36872
global_load_lds_dwordx4 v1, s[40:41] offset:-3072
ds_read_b64_tr_b16 a[166:167], v9 offset:39176
global_load_lds_dwordx4 v2, s[40:41] offset:-2048
ds_read_b64_tr_b16 a[168:169], v9 offset:36928
global_load_lds_dwordx4 v3, s[40:41] offset:-1024
ds_read_b64_tr_b16 a[170:171], v9 offset:39232
ds_read_b64_tr_b16 a[172:173], v9 offset:36936
global_load_lds_dwordx4 v4, s[40:41] offset:0
ds_read_b64_tr_b16 a[174:175], v9 offset:39240
global_load_lds_dwordx4 v5, s[40:41] offset:1024
ds_read_b64_tr_b16 a[176:177], v9 offset:55296
global_load_lds_dwordx4 v6, s[40:41] offset:2048
ds_read_b64_tr_b16 a[178:179], v9 offset:57600
global_load_lds_dwordx4 v7, s[40:41] offset:3072
ds_read_b64_tr_b16 a[180:181], v9 offset:55304
ds_read_b64_tr_b16 a[182:183], v9 offset:57608
s_add_u32 m0, s48, 12288
ds_read_b64_tr_b16 a[184:185], v9 offset:55360
global_load_lds_dwordx4 v8, s[40:41] offset:-4096
ds_read_b64_tr_b16 a[186:187], v9 offset:57664
s_sub_u32 s40, s40, 589824
ds_read_b64_tr_b16 a[188:189], v9 offset:55368
s_subb_u32 s41, s41, 0
ds_read_b64_tr_b16 a[190:191], v9 offset:57672
s_waitcnt vmcnt(18) lgkmcnt(0)
s_barrier
ds_read_b64_tr_b16 a[192:193], v204 offset:8192
ds_read_b64_tr_b16 a[194:195], v204 offset:10496
ds_read_b64_tr_b16 a[196:197], v204 offset:8200
ds_read_b64_tr_b16 a[198:199], v204 offset:10504
ds_read_b64_tr_b16 a[200:201], v204 offset:8256
ds_read_b64_tr_b16 a[202:203], v204 offset:10560
ds_read_b64_tr_b16 a[204:205], v204 offset:8264
ds_read_b64_tr_b16 a[206:207], v204 offset:10568
ds_read_b64_tr_b16 a[208:209], v204 offset:26624
ds_read_b64_tr_b16 a[210:211], v204 offset:28928
ds_read_b64_tr_b16 a[212:213], v204 offset:26632
ds_read_b64_tr_b16 a[214:215], v204 offset:28936
ds_read_b64_tr_b16 a[216:217], v204 offset:26688
ds_read_b64_tr_b16 a[218:219], v204 offset:28992
ds_read_b64_tr_b16 a[220:221], v204 offset:26696
ds_read_b64_tr_b16 a[222:223], v204 offset:29000
s_waitcnt vmcnt(9) lgkmcnt(0)
s_barrier
ds_read_b64_tr_b16 a[224:225], v204 offset:45056
ds_read_b64_tr_b16 a[226:227], v204 offset:47360
ds_read_b64_tr_b16 a[228:229], v204 offset:45064
ds_read_b64_tr_b16 a[230:231], v204 offset:47368
ds_read_b64_tr_b16 a[232:233], v204 offset:45120
ds_read_b64_tr_b16 a[234:235], v204 offset:47424
ds_read_b64_tr_b16 a[236:237], v204 offset:45128
ds_read_b64_tr_b16 a[238:239], v204 offset:47432
ds_read_b64_tr_b16 a[240:241], v204 offset:63488
ds_read_b64_tr_b16 a[242:243], v205 offset:256
ds_read_b64_tr_b16 a[244:245], v204 offset:63496
ds_read_b64_tr_b16 a[246:247], v205 offset:264
ds_read_b64_tr_b16 a[248:249], v204 offset:63552
ds_read_b64_tr_b16 a[250:251], v205 offset:320
ds_read_b64_tr_b16 a[252:253], v204 offset:63560
ds_read_b64_tr_b16 a[254:255], v205 offset:328
s_waitcnt vmcnt(0) lgkmcnt(0)
s_barrier
ds_read_b64_tr_b16 v[28:29], v9 offset:0
ds_read_b64_tr_b16 v[30:31], v9 offset:2304
ds_read_b64_tr_b16 v[32:33], v9 offset:8
ds_read_b64_tr_b16 v[34:35], v9 offset:2312
ds_read_b64_tr_b16 v[36:37], v9 offset:64
ds_read_b64_tr_b16 v[38:39], v9 offset:2368
ds_read_b64_tr_b16 v[40:41], v9 offset:72
ds_read_b64_tr_b16 v[42:43], v9 offset:2376
ds_read_b64_tr_b16 v[44:45], v9 offset:18432
ds_read_b64_tr_b16 v[46:47], v9 offset:20736
ds_read_b64_tr_b16 v[48:49], v9 offset:18440
ds_read_b64_tr_b16 v[50:51], v9 offset:20744
ds_read_b64_tr_b16 v[52:53], v9 offset:18496
ds_read_b64_tr_b16 v[54:55], v9 offset:20800
ds_read_b64_tr_b16 v[56:57], v9 offset:18504
ds_read_b64_tr_b16 v[58:59], v9 offset:20808
s_waitcnt lgkmcnt(0)
s_barrier
v_add_lshl_u32 v252, v24, 0, 7
v_bfi_b32 v252, s69, v252, v23
v_add_lshl_u32 v253, v22, 0, 7
v_bfi_b32 v253, s69, v253, v21
s_mov_b32 s92, 0x01010101
s_mov_b32 s93, 0x01010101
s_mov_b64 s[76:77], 0
s_add_u32 m0, s49, 18432
v_add_u32 v15, 18688, v15
v_add_u32 v13, 18688, v13
s_xor_b32 s49, s49, 0x10000
s_mov_b32 m0, 51200
s_add_u32 m0, s49, 0
v_add_u32 v11, 18432, v11
v_add_u32 v15, 18688, v15
s_mov_b32 m0, 59392
v_add_u32 v13, 18688, v13
s_add_u32 m0, s49, 18432
s_mov_b64 s[76:77], -1
s_mov_b32 s70, 33
s_waitcnt vmcnt(0) lgkmcnt(0)
s_nop 7
)"
R"(s_branch .Lwo_in15
.p2align 6
.Lwo_top16:
s_waitcnt lgkmcnt(2)
v_mfma_f32_16x16x32_bf16 v[96:99], a[0:3], v[132:135], v[96:99]
ds_read_b128 v[144:147], v25 offset:3072
v_mfma_f32_16x16x32_bf16 v[100:103], a[4:7], v[132:135], v[100:103]
v_mfma_f32_16x16x32_bf16 v[104:107], a[8:11], v[132:135], v[104:107]
s_waitcnt vmcnt(7)
s_barrier
v_mfma_f32_16x16x32_bf16 v[108:111], a[12:15], v[132:135], v[108:111]
v_cvt_pk_bf16_f32 v164, v112, v113
v_cvt_pk_bf16_f32 v165, v114, v115
v_mfma_f32_16x16x32_bf16 v[128:131], v[60:63], v[132:135], 0
s_waitcnt lgkmcnt(2)
v_cvt_pk_bf16_f32 v166, v116, v117
v_cvt_pk_bf16_f32 v167, v118, v119
s_mov_b64 exec, 65535
global_store_dwordx2 v15, v[176:177], s[38:39] sc1
s_mov_b64 exec, -1
v_mfma_f32_16x16x32_bf16 v[96:99], a[16:19], v[136:139], v[96:99]
ds_read_b128 v[132:135], v25 offset:4096
v_add_f32 v254, v112, v113
v_add_u32 v15, 18688, v15
v_mfma_f32_16x16x32_bf16 v[100:103], a[20:23], v[136:139], v[100:103]
v_mul_f32 v255, v112, v112
v_fmac_f32 v255, v113, v113
v_mfma_f32_16x16x32_bf16 v[104:107], a[24:27], v[136:139], v[104:107]
global_store_dwordx4 v13, v[164:167], s[38:39] offset:0 sc1
v_mfma_f32_16x16x32_bf16 v[108:111], a[28:31], v[136:139], v[108:111]
v_cvt_pk_bf16_f32 v168, v120, v121
v_cvt_pk_bf16_f32 v169, v122, v123
v_mfma_f32_16x16x32_bf16 v[128:131], v[64:67], v[136:139], v[128:131]
s_waitcnt lgkmcnt(2)
v_cvt_pk_bf16_f32 v170, v124, v125
v_cvt_pk_bf16_f32 v171, v126, v127
v_mfma_f32_16x16x32_bf16 v[96:99], a[32:35], v[140:143], v[96:99]
s_and_b32 s75, s70, 15
ds_read_b128 v[136:139], v25 offset:5120
v_add_f32 v254, v254, v114
v_mfma_f32_16x16x32_bf16 v[100:103], a[36:39], v[140:143], v[100:103]
s_cselect_b64 s[42:43], s[42:43], s[46:47]
global_store_dwordx4 v13, v[168:171], s[38:39] offset:1024 sc1
v_mfma_f32_16x16x32_bf16 v[104:107], a[40:43], v[140:143], v[104:107]
s_cmp_eq_u32 s70, 32
v_add_u32 v13, 18688, v13
v_fmac_f32 v255, v114, v114
v_mfma_f32_16x16x32_bf16 v[108:111], a[44:47], v[140:143], v[108:111]
s_cselect_b64 s[42:43], s[44:45], s[42:43]
v_add_f32 v254, v254, v115
v_fmac_f32 v255, v115, v115
v_mfma_f32_16x16x32_bf16 v[128:131], v[68:71], v[140:143], v[128:131]
s_waitcnt lgkmcnt(2)
v_add_f32 v254, v254, v116
v_fmac_f32 v255, v116, v116
v_mfma_f32_16x16x32_bf16 v[96:99], a[48:51], v[144:147], v[96:99]
s_xor_b32 s49, s49, 0x10000
ds_read_b128 v[140:143], v25 offset:6144
v_add_f32 v254, v254, v117
v_mfma_f32_16x16x32_bf16 v[100:103], a[52:55], v[144:147], v[100:103]
s_mov_b32 m0, 51200
v_fmac_f32 v255, v117, v117
v_add_f32 v254, v254, v118
v_mfma_f32_16x16x32_bf16 v[104:107], a[56:59], v[144:147], v[104:107]
v_fmac_f32 v255, v118, v118
v_add_f32 v254, v254, v119
v_mfma_f32_16x16x32_bf16 v[108:111], a[60:63], v[144:147], v[108:111]
v_fmac_f32 v255, v119, v119
v_add_f32 v254, v254, v120
v_mfma_f32_16x16x32_bf16 v[128:131], v[72:75], v[144:147], v[128:131]
s_waitcnt lgkmcnt(2)
v_fmac_f32 v255, v120, v120
v_add_f32 v254, v254, v121
v_mfma_f32_16x16x32_bf16 v[96:99], a[64:67], v[132:135], v[96:99]
ds_read_b128 v[144:147], v25 offset:7168
v_fmac_f32 v255, v121, v121
v_mfma_f32_16x16x32_bf16 v[100:103], a[68:71], v[132:135], v[100:103]
v_add_f32 v254, v254, v122
v_fmac_f32 v255, v122, v122
v_mfma_f32_16x16x32_bf16 v[104:107], a[72:75], v[132:135], v[104:107]
global_load_lds_dwordx4 v16, s[42:43] offset:0 sc1
v_add_f32 v254, v254, v123
v_mfma_f32_16x16x32_bf16 v[108:111], a[76:79], v[132:135], v[108:111]
v_fmac_f32 v255, v123, v123
v_add_f32 v254, v254, v124
v_mfma_f32_16x16x32_bf16 v[128:131], v[76:79], v[132:135], v[128:131]
s_waitcnt vmcnt(9)
ds_read_b128 v[148:151], v19 offset:8192
ds_read_b128 v[152:155], v20 offset:8192
s_waitcnt lgkmcnt(4)
v_mfma_f32_16x16x32_bf16 v[96:99], a[80:83], v[136:139], v[96:99]
ds_read_b128 v[132:135], v25 offset:8192
v_fmac_f32 v255, v124, v124
v_mfma_f32_16x16x32_bf16 v[100:103], a[84:87], v[136:139], v[100:103]
v_add_f32 v254, v254, v125
v_fmac_f32 v255, v125, v125
v_mfma_f32_16x16x32_bf16 v[104:107], a[88:91], v[136:139], v[104:107]
v_add_f32 v254, v254, v126
v_fmac_f32 v255, v126, v126
v_mfma_f32_16x16x32_bf16 v[108:111], a[92:95], v[136:139], v[108:111]
v_add_f32 v254, v254, v127
v_fmac_f32 v255, v127, v127
v_mfma_f32_16x16x32_bf16 v[128:131], v[80:83], v[136:139], v[128:131]
s_waitcnt lgkmcnt(4)
global_load_lds_dwordx4 v17, s[42:43] offset:1024 sc1
v_mfma_f32_16x16x32_bf16 v[96:99], a[96:99], v[140:143], v[96:99]
ds_read_b128 v[136:139], v25 offset:9216
v_mfma_f32_16x16x32_bf16 v[100:103], a[100:103], v[140:143], v[100:103]
ds_write_b64 v27, v[254:255] offset:46080
v_mfma_f32_16x16x32_bf16 v[104:107], a[104:107], v[140:143], v[104:107]
s_waitcnt lgkmcnt(4)
v_lshlrev_b32 v112, 16, v148
v_and_b32 v113, 0xffff0000, v148
v_mfma_f32_16x16x32_bf16 v[108:111], a[108:111], v[140:143], v[108:111]
v_lshlrev_b32 v114, 16, v149
v_and_b32 v115, 0xffff0000, v149
v_mfma_f32_16x16x32_bf16 v[128:131], v[84:87], v[140:143], v[128:131]
v_lshlrev_b32 v116, 16, v150
v_and_b32 v117, 0xffff0000, v150
v_mfma_f32_16x16x32_bf16 v[96:99], a[112:115], v[144:147], v[96:99]
s_add_u32 m0, s49, 0
ds_read_b128 v[140:143], v25 offset:10240
v_lshlrev_b32 v118, 16, v151
v_mfma_f32_16x16x32_bf16 v[100:103], a[116:119], v[144:147], v[100:103]
s_waitcnt lgkmcnt(4)
v_and_b32 v119, 0xffff0000, v151
v_lshlrev_b32 v120, 16, v152
v_mfma_f32_16x16x32_bf16 v[104:107], a[120:123], v[144:147], v[104:107]
s_sub_u32 s42, s42, 18432
v_and_b32 v121, 0xffff0000, v152
v_lshlrev_b32 v122, 16, v153
v_mfma_f32_16x16x32_bf16 v[108:111], a[124:127], v[144:147], v[108:111]
s_subb_u32 s43, s43, 0
v_and_b32 v123, 0xffff0000, v153
v_lshlrev_b32 v124, 16, v154
v_mfma_f32_16x16x32_bf16 v[128:131], v[88:91], v[144:147], v[128:131]
s_waitcnt lgkmcnt(3)
v_and_b32 v125, 0xffff0000, v154
v_lshlrev_b32 v126, 16, v155
v_mfma_f32_16x16x32_bf16 v[96:99], a[128:131], v[132:135], v[96:99]
ds_read_b128 v[144:147], v25 offset:11264
v_and_b32 v127, 0xffff0000, v155
v_mfma_f32_16x16x32_bf16 v[100:103], a[132:135], v[132:135], v[100:103]
v_mfma_f32_16x16x32_bf16 v[104:107], a[136:139], v[132:135], v[104:107]
v_mfma_f32_16x16x32_bf16 v[108:111], a[140:143], v[132:135], v[108:111]
v_mfma_f32_16x16x32_bf16 v[128:131], v[92:95], v[132:135], v[128:131]
s_waitcnt lgkmcnt(3)
ds_read_b64 v[176:177], v27 offset:43008
ds_read_b64 v[180:181], v27 offset:43520
v_mfma_f32_16x16x32_bf16 v[96:99], a[144:147], v[136:139], v[96:99]
ds_read_b128 v[132:135], v25 offset:12288
ds_read_b64 v[184:185], v27 offset:44032
v_mfma_f32_16x16x32_bf16 v[100:103], a[148:151], v[136:139], v[100:103]
ds_read_b64 v[188:189], v27 offset:44544
v_mfma_f32_16x16x32_bf16 v[104:107], a[152:155], v[136:139], v[104:107]
v_mfma_f32_16x16x32_bf16 v[108:111], a[156:159], v[136:139], v[108:111]
s_waitcnt lgkmcnt(6)
v_mfma_f32_16x16x32_bf16 v[96:99], a[160:163], v[140:143], v[96:99]
ds_read_b128 v[136:139], v25 offset:13312
v_mfma_f32_16x16x32_bf16 v[100:103], a[164:167], v[140:143], v[100:103]
v_mfma_f32_16x16x32_bf16 v[104:107], a[168:171], v[140:143], v[104:107]
v_mfma_f32_16x16x32_bf16 v[108:111], a[172:175], v[140:143], v[108:111]
s_waitcnt lgkmcnt(4)
v_add_f32 v176, v176, v180
v_add_f32 v177, v177, v181
v_mfma_f32_16x16x32_bf16 v[96:99], a[176:179], v[144:147], v[96:99]
s_waitcnt lgkmcnt(1)
ds_read_b128 v[140:143], v25 offset:14336
v_add_f32 v184, v184, v188
v_mfma_f32_16x16x32_bf16 v[100:103], a[180:183], v[144:147], v[100:103]
v_mfma_f32_16x16x32_bf16 v[104:107], a[184:187], v[144:147], v[104:107]
s_barrier
v_mfma_f32_16x16x32_bf16 v[108:111], a[188:191], v[144:147], v[108:111]
global_load_lds_dwordx4 v11, s[38:39] offset:-2048 sc1
v_add_f32 v185, v185, v189
v_mfma_f32_16x16x32_bf16 v[96:99], a[192:195], v[132:135], v[96:99]
ds_read_b128 v[144:147], v25 offset:15360
v_add_f32 v176, v176, v184
v_mfma_f32_16x16x32_bf16 v[100:103], a[196:199], v[132:135], v[100:103]
v_add_f32 v177, v177, v185
v_mov_b32 v202, v176
v_mfma_f32_16x16x32_bf16 v[104:107], a[200:203], v[132:135], v[104:107]
ds_write_b128 v26, v[128:131] offset:38912
v_mfma_f32_16x16x32_bf16 v[108:111], a[204:207], v[132:135], v[108:111]
s_waitcnt lgkmcnt(3)
global_load_lds_dwordx4 v11, s[38:39] offset:-1024 sc1
v_mov_b32 v203, v177
v_mfma_f32_16x16x32_bf16 v[96:99], a[208:211], v[136:139], v[96:99]
ds_read_b128 v[132:135], v25 offset:16384
v_mfma_f32_16x16x32_bf16 v[100:103], a[212:215], v[136:139], v[100:103]
v_permlane16_swap_b32 v176, v202
v_mfma_f32_16x16x32_bf16 v[104:107], a[216:219], v[136:139], v[104:107]
global_load_lds_dwordx4 v11, s[38:39] offset:0 sc1
v_mfma_f32_16x16x32_bf16 v[108:111], a[220:223], v[136:139], v[108:111]
s_waitcnt lgkmcnt(3)
v_permlane16_swap_b32 v177, v203
v_mfma_f32_16x16x32_bf16 v[96:99], a[224:227], v[140:143], v[96:99]
ds_read_b128 v[136:139], v25 offset:17408
v_add_f32 v176, v176, v202
v_mfma_f32_16x16x32_bf16 v[100:103], a[228:231], v[140:143], v[100:103]
global_load_lds_dwordx4 v11, s[38:39] offset:1024 sc1
v_add_f32 v177, v177, v203
v_mfma_f32_16x16x32_bf16 v[104:107], a[232:235], v[140:143], v[104:107]
v_mov_b32 v202, v176
v_mov_b32 v203, v177
v_mfma_f32_16x16x32_bf16 v[108:111], a[236:239], v[140:143], v[108:111]
s_waitcnt lgkmcnt(3)
v_permlane32_swap_b32 v176, v202
v_mfma_f32_16x16x32_bf16 v[96:99], a[240:243], v[144:147], v[96:99]
global_load_lds_dwordx4 v11, s[38:39] offset:2048 sc1
v_add_u32 v11, 18432, v11
v_mfma_f32_16x16x32_bf16 v[100:103], a[244:247], v[144:147], v[100:103]
v_mfma_f32_16x16x32_bf16 v[104:107], a[248:251], v[144:147], v[104:107]
s_waitcnt vmcnt(10) lgkmcnt(2)
s_barrier
v_mfma_f32_16x16x32_bf16 v[108:111], a[252:255], v[144:147], v[108:111]
s_waitcnt lgkmcnt(1)
ds_read_b128 v[140:143], v25 offset:18432
v_min_u32 v11, v11, v12
v_mfma_f32_16x16x32_bf16 v[96:99], v[28:31], v[132:135], v[96:99]
ds_read_b128 v[144:147], v25 offset:19456
v_mfma_f32_16x16x32_bf16 v[100:103], v[32:35], v[132:135], v[100:103]
v_permlane32_swap_b32 v177, v203
v_mfma_f32_16x16x32_bf16 v[104:107], v[36:39], v[132:135], v[104:107]
v_add_f32 v176, v176, v202
v_add_f32 v177, v177, v203
v_mfma_f32_16x16x32_bf16 v[108:111], v[40:43], v[132:135], v[108:111]
s_waitcnt lgkmcnt(2)
v_mfma_f32_16x16x32_bf16 v[96:99], v[44:47], v[136:139], v[96:99]
ds_read_b128 v[132:135], v25 offset:20480
v_mfma_f32_16x16x32_bf16 v[100:103], v[48:51], v[136:139], v[100:103]
v_mfma_f32_16x16x32_bf16 v[104:107], v[52:55], v[136:139], v[104:107]
v_mfma_f32_16x16x32_bf16 v[108:111], v[56:59], v[136:139], v[108:111]
s_waitcnt lgkmcnt(2)
v_mfma_f32_16x16x32_bf16 v[112:115], a[0:3], v[140:143], v[112:115]
ds_read_b128 v[136:139], v25 offset:21504
v_mfma_f32_16x16x32_bf16 v[116:119], a[4:7], v[140:143], v[116:119]
v_mfma_f32_16x16x32_bf16 v[120:123], a[8:11], v[140:143], v[120:123]
s_waitcnt vmcnt(7)
s_barrier
v_mfma_f32_16x16x32_bf16 v[124:127], a[12:15], v[140:143], v[124:127]
v_cvt_pk_bf16_f32 v164, v96, v97
v_cvt_pk_bf16_f32 v165, v98, v99
v_mfma_f32_16x16x32_bf16 v[128:131], v[60:63], v[140:143], 0
s_waitcnt lgkmcnt(2)
v_cvt_pk_bf16_f32 v166, v100, v101
v_cvt_pk_bf16_f32 v167, v102, v103
s_mov_b64 exec, 65535
global_store_dwordx2 v15, v[176:177], s[38:39] sc1
s_mov_b64 exec, -1
v_mfma_f32_16x16x32_bf16 v[112:115], a[16:19], v[144:147], v[112:115]
ds_read_b128 v[140:143], v25 offset:22528
v_add_u32 v15, 18688, v15
v_mfma_f32_16x16x32_bf16 v[116:119], a[20:23], v[144:147], v[116:119]
v_add_f32 v254, v96, v97
v_mul_f32 v255, v96, v96
v_mfma_f32_16x16x32_bf16 v[120:123], a[24:27], v[144:147], v[120:123]
global_store_dwordx4 v13, v[164:167], s[38:39] offset:0 sc1
v_mfma_f32_16x16x32_bf16 v[124:127], a[28:31], v[144:147], v[124:127]
v_cvt_pk_bf16_f32 v168, v104, v105
v_cvt_pk_bf16_f32 v169, v106, v107
v_mfma_f32_16x16x32_bf16 v[128:131], v[64:67], v[144:147], v[128:131]
s_waitcnt lgkmcnt(2)
v_cvt_pk_bf16_f32 v170, v108, v109
v_cvt_pk_bf16_f32 v171, v110, v111
v_mfma_f32_16x16x32_bf16 v[112:115], a[32:35], v[132:135], v[112:115]
s_mov_b32 m0, 59392
ds_read_b128 v[144:147], v25 offset:23552
v_fmac_f32 v255, v97, v97
v_mfma_f32_16x16x32_bf16 v[116:119], a[36:39], v[132:135], v[116:119]
global_store_dwordx4 v13, v[168:171], s[38:39] offset:1024 sc1
v_mfma_f32_16x16x32_bf16 v[120:123], a[40:43], v[132:135], v[120:123]
v_add_u32 v13, 18688, v13
v_add_f32 v254, v254, v98
v_mfma_f32_16x16x32_bf16 v[124:127], a[44:47], v[132:135], v[124:127]
v_fmac_f32 v255, v98, v98
v_add_f32 v254, v254, v99
v_mfma_f32_16x16x32_bf16 v[128:131], v[68:71], v[132:135], v[128:131]
s_waitcnt lgkmcnt(2)
v_fmac_f32 v255, v99, v99
v_add_f32 v254, v254, v100
v_mfma_f32_16x16x32_bf16 v[112:115], a[48:51], v[136:139], v[112:115]
ds_read_b128 v[132:135], v25 offset:24576
v_fmac_f32 v255, v100, v100
v_mfma_f32_16x16x32_bf16 v[116:119], a[52:55], v[136:139], v[116:119]
v_add_f32 v254, v254, v101
v_fmac_f32 v255, v101, v101
v_mfma_f32_16x16x32_bf16 v[120:123], a[56:59], v[136:139], v[120:123]
v_add_f32 v254, v254, v102
v_fmac_f32 v255, v102, v102
v_mfma_f32_16x16x32_bf16 v[124:127], a[60:63], v[136:139], v[124:127]
v_add_f32 v254, v254, v103
v_fmac_f32 v255, v103, v103
v_mfma_f32_16x16x32_bf16 v[128:131], v[72:75], v[136:139], v[128:131]
s_waitcnt lgkmcnt(2)
v_add_f32 v254, v254, v104
v_fmac_f32 v255, v104, v104
v_mfma_f32_16x16x32_bf16 v[112:115], a[64:67], v[140:143], v[112:115]
ds_read_b128 v[136:139], v25 offset:25600
v_add_f32 v254, v254, v105
v_mfma_f32_16x16x32_bf16 v[116:119], a[68:71], v[140:143], v[116:119]
v_fmac_f32 v255, v105, v105
v_add_f32 v254, v254, v106
v_mfma_f32_16x16x32_bf16 v[120:123], a[72:75], v[140:143], v[120:123]
global_load_lds_dwordx4 v16, s[42:43] offset:0 sc1
v_fmac_f32 v255, v106, v106
v_mfma_f32_16x16x32_bf16 v[124:127], a[76:79], v[140:143], v[124:127]
v_add_f32 v254, v254, v107
v_fmac_f32 v255, v107, v107
v_mfma_f32_16x16x32_bf16 v[128:131], v[76:79], v[140:143], v[128:131]
s_waitcnt vmcnt(9)
ds_read_b128 v[148:151], v19 offset:0
ds_read_b128 v[152:155], v20 offset:0
s_waitcnt lgkmcnt(4)
v_mfma_f32_16x16x32_bf16 v[112:115], a[80:83], v[144:147], v[112:115]
ds_read_b128 v[140:143], v25 offset:26624
v_add_f32 v254, v254, v108
v_mfma_f32_16x16x32_bf16 v[116:119], a[84:87], v[144:147], v[116:119]
v_fmac_f32 v255, v108, v108
v_add_f32 v254, v254, v109
v_mfma_f32_16x16x32_bf16 v[120:123], a[88:91], v[144:147], v[120:123]
v_fmac_f32 v255, v109, v109
v_add_f32 v254, v254, v110
v_mfma_f32_16x16x32_bf16 v[124:127], a[92:95], v[144:147], v[124:127]
v_fmac_f32 v255, v110, v110
v_add_f32 v254, v254, v111
v_mfma_f32_16x16x32_bf16 v[128:131], v[80:83], v[144:147], v[128:131]
s_waitcnt lgkmcnt(4)
global_load_lds_dwordx4 v17, s[42:43] offset:1024 sc1
v_fmac_f32 v255, v111, v111
v_mfma_f32_16x16x32_bf16 v[112:115], a[96:99], v[132:135], v[112:115]
ds_read_b128 v[144:147], v25 offset:27648
v_mfma_f32_16x16x32_bf16 v[116:119], a[100:103], v[132:135], v[116:119]
ds_write_b64 v27, v[254:255] offset:44032
v_mfma_f32_16x16x32_bf16 v[120:123], a[104:107], v[132:135], v[120:123]
s_waitcnt lgkmcnt(4)
v_lshlrev_b32 v96, 16, v148
v_and_b32 v97, 0xffff0000, v148
v_mfma_f32_16x16x32_bf16 v[124:127], a[108:111], v[132:135], v[124:127]
v_lshlrev_b32 v98, 16, v149
v_and_b32 v99, 0xffff0000, v149
v_mfma_f32_16x16x32_bf16 v[128:131], v[84:87], v[132:135], v[128:131]
v_lshlrev_b32 v100, 16, v150
v_and_b32 v101, 0xffff0000, v150
v_mfma_f32_16x16x32_bf16 v[112:115], a[112:115], v[136:139], v[112:115]
s_add_u32 m0, s49, 18432
ds_read_b128 v[132:135], v25 offset:28672
v_lshlrev_b32 v102, 16, v151
v_mfma_f32_16x16x32_bf16 v[116:119], a[116:119], v[136:139], v[116:119]
s_waitcnt lgkmcnt(4)
v_and_b32 v103, 0xffff0000, v151
v_lshlrev_b32 v104, 16, v152
v_mfma_f32_16x16x32_bf16 v[120:123], a[120:123], v[136:139], v[120:123]
s_sub_u32 s42, s42, 18432
v_and_b32 v105, 0xffff0000, v152
v_lshlrev_b32 v106, 16, v153
v_mfma_f32_16x16x32_bf16 v[124:127], a[124:127], v[136:139], v[124:127]
s_subb_u32 s43, s43, 0
v_and_b32 v107, 0xffff0000, v153
v_lshlrev_b32 v108, 16, v154
v_mfma_f32_16x16x32_bf16 v[128:131], v[88:91], v[136:139], v[128:131]
s_waitcnt lgkmcnt(3)
v_and_b32 v109, 0xffff0000, v154
v_lshlrev_b32 v110, 16, v155
v_mfma_f32_16x16x32_bf16 v[112:115], a[128:131], v[140:143], v[112:115]
ds_read_b128 v[136:139], v25 offset:29696
v_and_b32 v111, 0xffff0000, v155
v_mfma_f32_16x16x32_bf16 v[116:119], a[132:135], v[140:143], v[116:119]
v_mfma_f32_16x16x32_bf16 v[120:123], a[136:139], v[140:143], v[120:123]
v_mfma_f32_16x16x32_bf16 v[124:127], a[140:143], v[140:143], v[124:127]
v_mfma_f32_16x16x32_bf16 v[128:131], v[92:95], v[140:143], v[128:131]
s_waitcnt lgkmcnt(3)
ds_read_b64 v[176:177], v27 offset:45056
ds_read_b64 v[180:181], v27 offset:45568
v_mfma_f32_16x16x32_bf16 v[112:115], a[144:147], v[144:147], v[112:115]
ds_read_b128 v[140:143], v25 offset:30720
ds_read_b64 v[184:185], v27 offset:46080
v_mfma_f32_16x16x32_bf16 v[116:119], a[148:151], v[144:147], v[116:119]
ds_read_b64 v[188:189], v27 offset:46592
v_mfma_f32_16x16x32_bf16 v[120:123], a[152:155], v[144:147], v[120:123]
v_mfma_f32_16x16x32_bf16 v[124:127], a[156:159], v[144:147], v[124:127]
s_waitcnt lgkmcnt(6)
v_mfma_f32_16x16x32_bf16 v[112:115], a[160:163], v[132:135], v[112:115]
ds_read_b128 v[144:147], v25 offset:31744
v_mfma_f32_16x16x32_bf16 v[116:119], a[164:167], v[132:135], v[116:119]
v_mfma_f32_16x16x32_bf16 v[120:123], a[168:171], v[132:135], v[120:123]
v_mfma_f32_16x16x32_bf16 v[124:127], a[172:175], v[132:135], v[124:127]
s_waitcnt lgkmcnt(4)
v_add_f32 v176, v176, v180
v_add_f32 v177, v177, v181
v_mfma_f32_16x16x32_bf16 v[112:115], a[176:179], v[136:139], v[112:115]
s_waitcnt lgkmcnt(1)
ds_read_b128 v[132:135], v25 offset:32768
v_add_f32 v184, v184, v188
v_mfma_f32_16x16x32_bf16 v[116:119], a[180:183], v[136:139], v[116:119]
v_mfma_f32_16x16x32_bf16 v[120:123], a[184:187], v[136:139], v[120:123]
s_barrier
v_mfma_f32_16x16x32_bf16 v[124:127], a[188:191], v[136:139], v[124:127]
global_load_lds_dwordx4 v11, s[38:39] offset:-2048 sc1
v_add_f32 v185, v185, v189
)"
R"(v_mfma_f32_16x16x32_bf16 v[112:115], a[192:195], v[140:143], v[112:115]
s_mov_b64 s[76:77], -1
ds_read_b128 v[136:139], v25 offset:33792
v_add_f32 v176, v176, v184
v_mfma_f32_16x16x32_bf16 v[116:119], a[196:199], v[140:143], v[116:119]
v_add_f32 v177, v177, v185
v_mov_b32 v202, v176
v_mfma_f32_16x16x32_bf16 v[120:123], a[200:203], v[140:143], v[120:123]
ds_write_b128 v26, v[128:131] offset:41984
.Lwo_in15:
v_mfma_f32_16x16x32_bf16 v[124:127], a[204:207], v[140:143], v[124:127]
s_waitcnt lgkmcnt(3)
global_load_lds_dwordx4 v11, s[38:39] offset:-1024 sc1
v_mov_b32 v203, v177
v_mfma_f32_16x16x32_bf16 v[112:115], a[208:211], v[144:147], v[112:115]
ds_read_b128 v[140:143], v25 offset:34816
v_mfma_f32_16x16x32_bf16 v[116:119], a[212:215], v[144:147], v[116:119]
v_permlane16_swap_b32 v176, v202
v_mfma_f32_16x16x32_bf16 v[120:123], a[216:219], v[144:147], v[120:123]
global_load_lds_dwordx4 v11, s[38:39] offset:0 sc1
v_mfma_f32_16x16x32_bf16 v[124:127], a[220:223], v[144:147], v[124:127]
s_waitcnt lgkmcnt(3)
v_permlane16_swap_b32 v177, v203
v_mfma_f32_16x16x32_bf16 v[112:115], a[224:227], v[132:135], v[112:115]
ds_read_b128 v[144:147], v25 offset:35840
v_add_f32 v176, v176, v202
v_mfma_f32_16x16x32_bf16 v[116:119], a[228:231], v[132:135], v[116:119]
global_load_lds_dwordx4 v11, s[38:39] offset:1024 sc1
v_add_f32 v177, v177, v203
v_mfma_f32_16x16x32_bf16 v[120:123], a[232:235], v[132:135], v[120:123]
v_mov_b32 v202, v176
v_mov_b32 v203, v177
v_mfma_f32_16x16x32_bf16 v[124:127], a[236:239], v[132:135], v[124:127]
s_waitcnt lgkmcnt(3)
v_xor_b32 v25, 0x10000, v25
v_mfma_f32_16x16x32_bf16 v[112:115], a[240:243], v[136:139], v[112:115]
global_load_lds_dwordx4 v11, s[38:39] offset:2048 sc1
v_add_u32 v11, 18432, v11
v_mfma_f32_16x16x32_bf16 v[116:119], a[244:247], v[136:139], v[116:119]
v_mfma_f32_16x16x32_bf16 v[120:123], a[248:251], v[136:139], v[120:123]
s_waitcnt vmcnt(10) lgkmcnt(2)
s_barrier
v_mfma_f32_16x16x32_bf16 v[124:127], a[252:255], v[136:139], v[124:127]
s_waitcnt lgkmcnt(1)
ds_read_b128 v[132:135], v25 offset:0
v_min_u32 v11, v11, v12
v_mfma_f32_16x16x32_bf16 v[112:115], v[28:31], v[140:143], v[112:115]
ds_read_b128 v[136:139], v25 offset:1024
v_mfma_f32_16x16x32_bf16 v[116:119], v[32:35], v[140:143], v[116:119]
v_permlane32_swap_b32 v176, v202
v_mfma_f32_16x16x32_bf16 v[120:123], v[36:39], v[140:143], v[120:123]
v_permlane32_swap_b32 v177, v203
v_mfma_f32_16x16x32_bf16 v[124:127], v[40:43], v[140:143], v[124:127]
s_waitcnt lgkmcnt(2)
v_add_f32 v176, v176, v202
v_add_f32 v177, v177, v203
v_mfma_f32_16x16x32_bf16 v[112:115], v[44:47], v[144:147], v[112:115]
ds_read_b128 v[140:143], v25 offset:2048
v_mfma_f32_16x16x32_bf16 v[116:119], v[48:51], v[144:147], v[116:119]
v_mfma_f32_16x16x32_bf16 v[120:123], v[52:55], v[144:147], v[120:123]
v_mfma_f32_16x16x32_bf16 v[124:127], v[56:59], v[144:147], v[124:127]
s_sub_u32 s70, s70, 1
s_cbranch_scc0 .Lwo_top16
s_waitcnt vmcnt(7)
s_barrier
s_mov_b64 exec, 65535
global_store_dwordx2 v15, v[176:177], s[38:39] sc1
s_mov_b64 exec, -1
v_add_u32 v15, 18688, v15
v_cvt_pk_bf16_f32 v164, v112, v113
v_cvt_pk_bf16_f32 v165, v114, v115
v_cvt_pk_bf16_f32 v166, v116, v117
v_cvt_pk_bf16_f32 v167, v118, v119
global_store_dwordx4 v13, v[164:167], s[38:39] offset:0 sc1
v_cvt_pk_bf16_f32 v168, v120, v121
v_cvt_pk_bf16_f32 v169, v122, v123
v_cvt_pk_bf16_f32 v170, v124, v125
v_cvt_pk_bf16_f32 v171, v126, v127
global_store_dwordx4 v13, v[168:171], s[38:39] offset:1024 sc1
v_add_f32 v254, v112, v113
v_mul_f32 v255, v112, v112
v_fmac_f32 v255, v113, v113
v_add_f32 v254, v254, v114
v_fmac_f32 v255, v114, v114
v_add_f32 v254, v254, v115
v_fmac_f32 v255, v115, v115
v_add_f32 v254, v254, v116
v_fmac_f32 v255, v116, v116
v_add_f32 v254, v254, v117
v_fmac_f32 v255, v117, v117
v_add_f32 v254, v254, v118
v_fmac_f32 v255, v118, v118
v_add_f32 v254, v254, v119
v_fmac_f32 v255, v119, v119
v_add_f32 v254, v254, v120
v_fmac_f32 v255, v120, v120
v_add_f32 v254, v254, v121
v_fmac_f32 v255, v121, v121
v_add_f32 v254, v254, v122
v_fmac_f32 v255, v122, v122
v_add_f32 v254, v254, v123
v_fmac_f32 v255, v123, v123
v_add_f32 v254, v254, v124
v_fmac_f32 v255, v124, v124
v_add_f32 v254, v254, v125
v_fmac_f32 v255, v125, v125
v_add_f32 v254, v254, v126
v_fmac_f32 v255, v126, v126
v_add_f32 v254, v254, v127
v_fmac_f32 v255, v127, v127
ds_write_b64 v27, v[254:255] offset:46080
s_waitcnt vmcnt(0) lgkmcnt(0)
s_barrier
ds_read_b64 v[176:177], v27 offset:43008
ds_read_b64 v[180:181], v27 offset:43520
ds_read_b64 v[184:185], v27 offset:44032
ds_read_b64 v[188:189], v27 offset:44544
s_waitcnt lgkmcnt(2)
v_add_f32 v176, v176, v180
v_add_f32 v177, v177, v181
s_waitcnt lgkmcnt(0)
v_add_f32 v184, v184, v188
v_add_f32 v185, v185, v189
v_add_f32 v176, v176, v184
v_add_f32 v177, v177, v185
v_mov_b32 v202, v176
v_mov_b32 v203, v177
s_nop 0
v_permlane16_swap_b32 v176, v202
v_permlane16_swap_b32 v177, v203
v_add_f32 v176, v176, v202
v_add_f32 v177, v177, v203
v_mov_b32 v202, v176
v_mov_b32 v203, v177
s_nop 0
v_permlane32_swap_b32 v176, v202
v_permlane32_swap_b32 v177, v203
v_add_f32 v176, v176, v202
v_add_f32 v177, v177, v203
s_mov_b64 exec, 65535
global_store_dwordx2 v15, v[176:177], s[38:39] sc1
s_mov_b64 exec, -1
v_add_u32 v15, 18688, v15
ds_read_b64 v[176:177], v27 offset:45056
ds_read_b64 v[180:181], v27 offset:45568
ds_read_b64 v[184:185], v27 offset:46080
ds_read_b64 v[188:189], v27 offset:46592
s_waitcnt lgkmcnt(2)
v_add_f32 v176, v176, v180
v_add_f32 v177, v177, v181
s_waitcnt lgkmcnt(0)
v_add_f32 v184, v184, v188
v_add_f32 v185, v185, v189
v_add_f32 v176, v176, v184
v_add_f32 v177, v177, v185
v_mov_b32 v202, v176
v_mov_b32 v203, v177
s_nop 0
v_permlane16_swap_b32 v176, v202
v_permlane16_swap_b32 v177, v203
v_add_f32 v176, v176, v202
v_add_f32 v177, v177, v203
v_mov_b32 v202, v176
v_mov_b32 v203, v177
s_nop 0
v_permlane32_swap_b32 v176, v202
v_permlane32_swap_b32 v177, v203
v_add_f32 v176, v176, v202
v_add_f32 v177, v177, v203
s_mov_b64 exec, 65535
global_store_dwordx2 v15, v[176:177], s[38:39] sc1
s_mov_b64 exec, -1
v_add_u32 v15, 18688, v15
s_waitcnt vmcnt(0)
s_barrier
s_endpgm
.Lwo_w4:
s_add_u32 m0, s48, 4096
s_nop 0
global_load_lds_dwordx4 v0, s[40:41] offset:-4096
global_load_lds_dwordx4 v1, s[40:41] offset:-3072
global_load_lds_dwordx4 v2, s[40:41] offset:-2048
global_load_lds_dwordx4 v3, s[40:41] offset:-1024
global_load_lds_dwordx4 v4, s[40:41] offset:0
global_load_lds_dwordx4 v5, s[40:41] offset:1024
global_load_lds_dwordx4 v6, s[40:41] offset:2048
global_load_lds_dwordx4 v7, s[40:41] offset:3072
s_add_u32 m0, s48, 12288
s_nop 0
global_load_lds_dwordx4 v8, s[40:41] offset:-4096
s_add_u32 s40, s40, 73728
s_addc_u32 s41, s41, 0
s_add_u32 m0, s48, 40960
s_nop 0
global_load_lds_dwordx4 v0, s[40:41] offset:-4096
global_load_lds_dwordx4 v1, s[40:41] offset:-3072
global_load_lds_dwordx4 v2, s[40:41] offset:-2048
global_load_lds_dwordx4 v3, s[40:41] offset:-1024
global_load_lds_dwordx4 v4, s[40:41] offset:0
global_load_lds_dwordx4 v5, s[40:41] offset:1024
global_load_lds_dwordx4 v6, s[40:41] offset:2048
global_load_lds_dwordx4 v7, s[40:41] offset:3072
s_add_u32 m0, s48, 49152
s_nop 0
global_load_lds_dwordx4 v8, s[40:41] offset:-4096
s_add_u32 s40, s40, 73728
s_addc_u32 s41, s41, 0
s_add_u32 m0, s48, 77824
s_nop 0
global_load_lds_dwordx4 v0, s[40:41] offset:-4096
global_load_lds_dwordx4 v1, s[40:41] offset:-3072
global_load_lds_dwordx4 v2, s[40:41] offset:-2048
global_load_lds_dwordx4 v3, s[40:41] offset:-1024
global_load_lds_dwordx4 v4, s[40:41] offset:0
global_load_lds_dwordx4 v5, s[40:41] offset:1024
global_load_lds_dwordx4 v6, s[40:41] offset:2048
global_load_lds_dwordx4 v7, s[40:41] offset:3072
s_add_u32 m0, s48, 86016
s_nop 0
global_load_lds_dwordx4 v8, s[40:41] offset:-4096
s_add_u32 s40, s40, 73728
s_addc_u32 s41, s41, 0
v_add_u32 v204, 0x10000, v9
v_add_u32 v205, 0x20000, v9
v_add_u32 v206, 0x10000, v10
v_add_u32 v207, 0x20000, v10
s_waitcnt lgkmcnt(0)
s_mul_i32 s37, s37, 0x9E3779B1
s_add_u32 s37, s37, 0x7F4A7C15
v_add_u32 v22, s37, v22
v_add_u32 v24, s37, v24
s_waitcnt vmcnt(18)
s_barrier
s_add_u32 m0, s48, 114688
ds_read_b64_tr_b16 a[0:1], v9 offset:0
ds_read_b64_tr_b16 a[2:3], v9 offset:2304
global_load_lds_dwordx4 v0, s[40:41] offset:-4096
ds_read_b64_tr_b16 a[4:5], v9 offset:8
global_load_lds_dwordx4 v1, s[40:41] offset:-3072
ds_read_b64_tr_b16 a[6:7], v9 offset:2312
global_load_lds_dwordx4 v2, s[40:41] offset:-2048
ds_read_b64_tr_b16 a[8:9], v9 offset:64
global_load_lds_dwordx4 v3, s[40:41] offset:-1024
ds_read_b64_tr_b16 a[10:11], v9 offset:2368
ds_read_b64_tr_b16 a[12:13], v9 offset:72
global_load_lds_dwordx4 v4, s[40:41] offset:0
ds_read_b64_tr_b16 a[14:15], v9 offset:2376
global_load_lds_dwordx4 v5, s[40:41] offset:1024
ds_read_b64_tr_b16 a[16:17], v9 offset:18432
global_load_lds_dwordx4 v6, s[40:41] offset:2048
ds_read_b64_tr_b16 a[18:19], v9 offset:20736
global_load_lds_dwordx4 v7, s[40:41] offset:3072
ds_read_b64_tr_b16 a[20:21], v9 offset:18440
ds_read_b64_tr_b16 a[22:23], v9 offset:20744
s_add_u32 m0, s48, 122880
ds_read_b64_tr_b16 a[24:25], v9 offset:18496
global_load_lds_dwordx4 v8, s[40:41] offset:-4096
ds_read_b64_tr_b16 a[26:27], v9 offset:20800
s_add_u32 s40, s40, 73728
ds_read_b64_tr_b16 a[28:29], v9 offset:18504
s_addc_u32 s41, s41, 0
ds_read_b64_tr_b16 a[30:31], v9 offset:20808
s_waitcnt vmcnt(18) lgkmcnt(0)
s_barrier
s_add_u32 m0, s48, 4096
ds_read_b64_tr_b16 a[32:33], v9 offset:36864
ds_read_b64_tr_b16 a[34:35], v9 offset:39168
global_load_lds_dwordx4 v0, s[40:41] offset:-4096
ds_read_b64_tr_b16 a[36:37], v9 offset:36872
global_load_lds_dwordx4 v1, s[40:41] offset:-3072
ds_read_b64_tr_b16 a[38:39], v9 offset:39176
global_load_lds_dwordx4 v2, s[40:41] offset:-2048
ds_read_b64_tr_b16 a[40:41], v9 offset:36928
global_load_lds_dwordx4 v3, s[40:41] offset:-1024
ds_read_b64_tr_b16 a[42:43], v9 offset:39232
ds_read_b64_tr_b16 a[44:45], v9 offset:36936
global_load_lds_dwordx4 v4, s[40:41] offset:0
ds_read_b64_tr_b16 a[46:47], v9 offset:39240
global_load_lds_dwordx4 v5, s[40:41] offset:1024
ds_read_b64_tr_b16 a[48:49], v9 offset:55296
global_load_lds_dwordx4 v6, s[40:41] offset:2048
ds_read_b64_tr_b16 a[50:51], v9 offset:57600
global_load_lds_dwordx4 v7, s[40:41] offset:3072
ds_read_b64_tr_b16 a[52:53], v9 offset:55304
ds_read_b64_tr_b16 a[54:55], v9 offset:57608
s_add_u32 m0, s48, 12288
ds_read_b64_tr_b16 a[56:57], v9 offset:55360
global_load_lds_dwordx4 v8, s[40:41] offset:-4096
ds_read_b64_tr_b16 a[58:59], v9 offset:57664
s_add_u32 s40, s40, 73728
ds_read_b64_tr_b16 a[60:61], v9 offset:55368
s_addc_u32 s41, s41, 0
ds_read_b64_tr_b16 a[62:63], v9 offset:57672
s_waitcnt vmcnt(18) lgkmcnt(0)
s_barrier
s_add_u32 m0, s48, 40960
ds_read_b64_tr_b16 a[64:65], v204 offset:8192
ds_read_b64_tr_b16 a[66:67], v204 offset:10496
global_load_lds_dwordx4 v0, s[40:41] offset:-4096
ds_read_b64_tr_b16 a[68:69], v204 offset:8200
global_load_lds_dwordx4 v1, s[40:41] offset:-3072
ds_read_b64_tr_b16 a[70:71], v204 offset:10504
global_load_lds_dwordx4 v2, s[40:41] offset:-2048
ds_read_b64_tr_b16 a[72:73], v204 offset:8256
global_load_lds_dwordx4 v3, s[40:41] offset:-1024
ds_read_b64_tr_b16 a[74:75], v204 offset:10560
ds_read_b64_tr_b16 a[76:77], v204 offset:8264
global_load_lds_dwordx4 v4, s[40:41] offset:0
ds_read_b64_tr_b16 a[78:79], v204 offset:10568
global_load_lds_dwordx4 v5, s[40:41] offset:1024
ds_read_b64_tr_b16 a[80:81], v204 offset:26624
global_load_lds_dwordx4 v6, s[40:41] offset:2048
ds_read_b64_tr_b16 a[82:83], v204 offset:28928
global_load_lds_dwordx4 v7, s[40:41] offset:3072
ds_read_b64_tr_b16 a[84:85], v204 offset:26632
ds_read_b64_tr_b16 a[86:87], v204 offset:28936
s_add_u32 m0, s48, 49152
ds_read_b64_tr_b16 a[88:89], v204 offset:26688
global_load_lds_dwordx4 v8, s[40:41] offset:-4096
ds_read_b64_tr_b16 a[90:91], v204 offset:28992
s_add_u32 s40, s40, 73728
ds_read_b64_tr_b16 a[92:93], v204 offset:26696
s_addc_u32 s41, s41, 0
ds_read_b64_tr_b16 a[94:95], v204 offset:29000
s_waitcnt vmcnt(18) lgkmcnt(0)
s_barrier
s_add_u32 m0, s48, 77824
ds_read_b64_tr_b16 a[96:97], v204 offset:45056
ds_read_b64_tr_b16 a[98:99], v204 offset:47360
global_load_lds_dwordx4 v0, s[40:41] offset:-4096
ds_read_b64_tr_b16 a[100:101], v204 offset:45064
global_load_lds_dwordx4 v1, s[40:41] offset:-3072
ds_read_b64_tr_b16 a[102:103], v204 offset:47368
global_load_lds_dwordx4 v2, s[40:41] offset:-2048
ds_read_b64_tr_b16 a[104:105], v204 offset:45120
global_load_lds_dwordx4 v3, s[40:41] offset:-1024
ds_read_b64_tr_b16 a[106:107], v204 offset:47424
ds_read_b64_tr_b16 a[108:109], v204 offset:45128
global_load_lds_dwordx4 v4, s[40:41] offset:0
ds_read_b64_tr_b16 a[110:111], v204 offset:47432
global_load_lds_dwordx4 v5, s[40:41] offset:1024
ds_read_b64_tr_b16 a[112:113], v204 offset:63488
global_load_lds_dwordx4 v6, s[40:41] offset:2048
ds_read_b64_tr_b16 a[114:115], v205 offset:256
global_load_lds_dwordx4 v7, s[40:41] offset:3072
ds_read_b64_tr_b16 a[116:117], v204 offset:63496
ds_read_b64_tr_b16 a[118:119], v205 offset:264
s_add_u32 m0, s48, 86016
ds_read_b64_tr_b16 a[120:121], v204 offset:63552
global_load_lds_dwordx4 v8, s[40:41] offset:-4096
ds_read_b64_tr_b16 a[122:123], v205 offset:320
s_add_u32 s40, s40, 73728
ds_read_b64_tr_b16 a[124:125], v204 offset:63560
s_addc_u32 s41, s41, 0
ds_read_b64_tr_b16 a[126:127], v205 offset:328
s_waitcnt vmcnt(18) lgkmcnt(0)
s_barrier
s_add_u32 m0, s48, 114688
ds_read_b64_tr_b16 a[128:129], v9 offset:0
ds_read_b64_tr_b16 a[130:131], v9 offset:2304
global_load_lds_dwordx4 v0, s[40:41] offset:-4096
ds_read_b64_tr_b16 a[132:133], v9 offset:8
global_load_lds_dwordx4 v1, s[40:41] offset:-3072
ds_read_b64_tr_b16 a[134:135], v9 offset:2312
ds_read_b64_tr_b16 a[136:137], v9 offset:64
global_load_lds_dwordx4 v2, s[40:41] offset:-2048
ds_read_b64_tr_b16 a[138:139], v9 offset:2368
global_load_lds_dwordx4 v3, s[40:41] offset:-1024
ds_read_b64_tr_b16 a[140:141], v9 offset:72
global_load_lds_dwordx4 v4, s[40:41] offset:0
ds_read_b64_tr_b16 a[142:143], v9 offset:2376
ds_read_b64_tr_b16 a[144:145], v9 offset:18432
global_load_lds_dwordx4 v5, s[40:41] offset:1024
ds_read_b64_tr_b16 a[146:147], v9 offset:20736
global_load_lds_dwordx4 v6, s[40:41] offset:2048
ds_read_b64_tr_b16 a[148:149], v9 offset:18440
ds_read_b64_tr_b16 a[150:151], v9 offset:20744
global_load_lds_dwordx4 v7, s[40:41] offset:3072
ds_read_b64_tr_b16 a[152:153], v9 offset:18496
s_add_u32 m0, s48, 122880
ds_read_b64_tr_b16 a[154:155], v9 offset:20800
global_load_lds_dwordx4 v8, s[40:41] offset:-4096
ds_read_b64_tr_b16 a[156:157], v9 offset:18504
ds_read_b64_tr_b16 a[158:159], v9 offset:20808
s_add_u32 s40, s40, 73728
ds_read_b64_tr_b16 v[60:61], v10 offset:18432
s_addc_u32 s41, s41, 0
ds_read_b64_tr_b16 v[62:63], v10 offset:20736
s_waitcnt vmcnt(18) lgkmcnt(0)
s_barrier
s_add_u32 m0, s48, 4096
ds_read_b64_tr_b16 a[160:161], v9 offset:36864
ds_read_b64_tr_b16 a[162:163], v9 offset:39168
global_load_lds_dwordx4 v0, s[40:41] offset:-4096
ds_read_b64_tr_b16 a[164:165], v9 offset:36872
ds_read_b64_tr_b16 a[166:167], v9 offset:39176
global_load_lds_dwordx4 v1, s[40:41] offset:-3072
ds_read_b64_tr_b16 a[168:169], v9 offset:36928
global_load_lds_dwordx4 v2, s[40:41] offset:-2048
ds_read_b64_tr_b16 a[170:171], v9 offset:39232
)"
R"(ds_read_b64_tr_b16 a[172:173], v9 offset:36936
global_load_lds_dwordx4 v3, s[40:41] offset:-1024
ds_read_b64_tr_b16 a[174:175], v9 offset:39240
global_load_lds_dwordx4 v4, s[40:41] offset:0
ds_read_b64_tr_b16 v[64:65], v10 offset:36864
ds_read_b64_tr_b16 v[66:67], v10 offset:39168
global_load_lds_dwordx4 v5, s[40:41] offset:1024
ds_read_b64_tr_b16 a[176:177], v9 offset:55296
global_load_lds_dwordx4 v6, s[40:41] offset:2048
ds_read_b64_tr_b16 a[178:179], v9 offset:57600
ds_read_b64_tr_b16 a[180:181], v9 offset:55304
global_load_lds_dwordx4 v7, s[40:41] offset:3072
ds_read_b64_tr_b16 a[182:183], v9 offset:57608
s_add_u32 m0, s48, 12288
ds_read_b64_tr_b16 a[184:185], v9 offset:55360
ds_read_b64_tr_b16 a[186:187], v9 offset:57664
global_load_lds_dwordx4 v8, s[40:41] offset:-4096
ds_read_b64_tr_b16 a[188:189], v9 offset:55368
s_sub_u32 s40, s40, 589824
ds_read_b64_tr_b16 a[190:191], v9 offset:57672
ds_read_b64_tr_b16 v[68:69], v10 offset:55296
s_subb_u32 s41, s41, 0
ds_read_b64_tr_b16 v[70:71], v10 offset:57600
s_waitcnt vmcnt(18) lgkmcnt(0)
s_barrier
ds_read_b64_tr_b16 a[192:193], v204 offset:8192
ds_read_b64_tr_b16 a[194:195], v204 offset:10496
ds_read_b64_tr_b16 a[196:197], v204 offset:8200
ds_read_b64_tr_b16 a[198:199], v204 offset:10504
ds_read_b64_tr_b16 a[200:201], v204 offset:8256
ds_read_b64_tr_b16 a[202:203], v204 offset:10560
ds_read_b64_tr_b16 a[204:205], v204 offset:8264
ds_read_b64_tr_b16 a[206:207], v204 offset:10568
ds_read_b64_tr_b16 v[72:73], v206 offset:8192
ds_read_b64_tr_b16 v[74:75], v206 offset:10496
ds_read_b64_tr_b16 a[208:209], v204 offset:26624
ds_read_b64_tr_b16 a[210:211], v204 offset:28928
ds_read_b64_tr_b16 a[212:213], v204 offset:26632
ds_read_b64_tr_b16 a[214:215], v204 offset:28936
ds_read_b64_tr_b16 a[216:217], v204 offset:26688
ds_read_b64_tr_b16 a[218:219], v204 offset:28992
ds_read_b64_tr_b16 a[220:221], v204 offset:26696
ds_read_b64_tr_b16 a[222:223], v204 offset:29000
ds_read_b64_tr_b16 v[76:77], v206 offset:26624
ds_read_b64_tr_b16 v[78:79], v206 offset:28928
s_waitcnt vmcnt(9) lgkmcnt(0)
s_barrier
ds_read_b64_tr_b16 a[224:225], v204 offset:45056
ds_read_b64_tr_b16 a[226:227], v204 offset:47360
ds_read_b64_tr_b16 a[228:229], v204 offset:45064
ds_read_b64_tr_b16 a[230:231], v204 offset:47368
ds_read_b64_tr_b16 a[232:233], v204 offset:45120
ds_read_b64_tr_b16 a[234:235], v204 offset:47424
ds_read_b64_tr_b16 a[236:237], v204 offset:45128
ds_read_b64_tr_b16 a[238:239], v204 offset:47432
ds_read_b64_tr_b16 v[80:81], v206 offset:45056
ds_read_b64_tr_b16 v[82:83], v206 offset:47360
ds_read_b64_tr_b16 a[240:241], v204 offset:63488
ds_read_b64_tr_b16 a[242:243], v205 offset:256
ds_read_b64_tr_b16 a[244:245], v204 offset:63496
ds_read_b64_tr_b16 a[246:247], v205 offset:264
ds_read_b64_tr_b16 a[248:249], v204 offset:63552
ds_read_b64_tr_b16 a[250:251], v205 offset:320
ds_read_b64_tr_b16 a[252:253], v204 offset:63560
ds_read_b64_tr_b16 a[254:255], v205 offset:328
ds_read_b64_tr_b16 v[84:85], v206 offset:63488
ds_read_b64_tr_b16 v[86:87], v207 offset:256
s_waitcnt vmcnt(0) lgkmcnt(0)
s_barrier
ds_read_b64_tr_b16 v[28:29], v9 offset:0
ds_read_b64_tr_b16 v[30:31], v9 offset:2304
ds_read_b64_tr_b16 v[32:33], v9 offset:8
ds_read_b64_tr_b16 v[34:35], v9 offset:2312
ds_read_b64_tr_b16 v[36:37], v9 offset:64
ds_read_b64_tr_b16 v[38:39], v9 offset:2368
ds_read_b64_tr_b16 v[40:41], v9 offset:72
ds_read_b64_tr_b16 v[42:43], v9 offset:2376
ds_read_b64_tr_b16 v[88:89], v10 offset:0
ds_read_b64_tr_b16 v[90:91], v10 offset:2304
ds_read_b64_tr_b16 v[44:45], v9 offset:18432
ds_read_b64_tr_b16 v[46:47], v9 offset:20736
ds_read_b64_tr_b16 v[48:49], v9 offset:18440
ds_read_b64_tr_b16 v[50:51], v9 offset:20744
ds_read_b64_tr_b16 v[52:53], v9 offset:18496
ds_read_b64_tr_b16 v[54:55], v9 offset:20800
ds_read_b64_tr_b16 v[56:57], v9 offset:18504
ds_read_b64_tr_b16 v[58:59], v9 offset:20808
ds_read_b64_tr_b16 v[92:93], v10 offset:18432
ds_read_b64_tr_b16 v[94:95], v10 offset:20736
s_waitcnt lgkmcnt(0)
s_barrier
v_add_lshl_u32 v252, v24, 0, 7
v_bfi_b32 v252, s69, v252, v23
v_add_lshl_u32 v253, v22, 0, 7
v_bfi_b32 v253, s69, v253, v21
s_mov_b32 s92, 0x01010101
s_mov_b32 s93, 0x01010101
s_mov_b64 s[76:77], 0
s_add_u32 m0, s49, 18432
v_add_u32 v14, 18688, v14
s_xor_b32 s49, s49, 0x10000
s_mov_b32 m0, 53248
v_add_u32 v13, 18688, v13
s_add_u32 m0, s49, 0
v_add_u32 v11, 18432, v11
s_mov_b32 m0, 61440
v_add_u32 v14, 18688, v14
v_add_u32 v13, 18688, v13
s_add_u32 m0, s49, 18432
s_mov_b64 s[76:77], -1
s_mov_b32 s70, 33
s_waitcnt vmcnt(0) lgkmcnt(0)
s_nop 7
s_branch .Lwo_in18
.p2align 6
.Lwo_top19:
s_waitcnt lgkmcnt(5)
v_mfma_f32_16x16x32_bf16 v[96:99], a[144:147], v[132:135], v[96:99]
s_waitcnt lgkmcnt(4)
ds_read_b128 v[144:147], v25 offset:12288
v_add_f32 v176, v176, v184
v_mfma_f32_16x16x32_bf16 v[100:103], a[148:151], v[132:135], v[100:103]
v_mfma_f32_16x16x32_bf16 v[104:107], a[152:155], v[132:135], v[104:107]
s_waitcnt vmcnt(6)
s_barrier
v_mfma_f32_16x16x32_bf16 v[108:111], a[156:159], v[132:135], v[108:111]
v_add_f32 v177, v177, v185
v_add_f32 v178, v178, v186
v_mfma_f32_16x16x32_bf16 v[128:131], v[60:63], v[132:135], 0
s_waitcnt lgkmcnt(3)
v_add_f32 v179, v179, v187
v_add_f32 v176, v176, v188
v_mfma_f32_16x16x32_bf16 v[96:99], a[160:163], v[136:139], v[96:99]
ds_read_b128 v[132:135], v25 offset:13312
v_add_f32 v177, v177, v189
v_mfma_f32_16x16x32_bf16 v[100:103], a[164:167], v[136:139], v[100:103]
v_add_f32 v178, v178, v190
v_add_f32 v179, v179, v191
v_mfma_f32_16x16x32_bf16 v[104:107], a[168:171], v[136:139], v[104:107]
s_waitcnt lgkmcnt(3)
v_add_f32 v180, v180, v192
v_add_f32 v181, v181, v193
v_mfma_f32_16x16x32_bf16 v[108:111], a[172:175], v[136:139], v[108:111]
v_add_f32 v182, v182, v194
v_add_f32 v183, v183, v195
v_mfma_f32_16x16x32_bf16 v[128:131], v[64:67], v[136:139], v[128:131]
s_waitcnt lgkmcnt(2)
v_cvt_pk_bf16_f32 v172, v176, v177
v_cvt_pk_bf16_f32 v173, v178, v179
v_mfma_f32_16x16x32_bf16 v[96:99], a[176:179], v[140:143], v[96:99]
s_and_b32 s75, s70, 15
ds_read_b128 v[136:139], v25 offset:14336
v_cvt_pk_bf16_f32 v174, v180, v181
v_mfma_f32_16x16x32_bf16 v[100:103], a[180:183], v[140:143], v[100:103]
s_cselect_b64 s[42:43], s[42:43], s[46:47]
v_cvt_pk_bf16_f32 v175, v182, v183
v_cvt_pk_bf16_f32 v164, v112, v113
v_mfma_f32_16x16x32_bf16 v[104:107], a[184:187], v[140:143], v[104:107]
s_cmp_eq_u32 s70, 32
global_store_dwordx4 v14, v[172:175], s[38:39] sc1
v_mfma_f32_16x16x32_bf16 v[108:111], a[188:191], v[140:143], v[108:111]
s_cselect_b64 s[42:43], s[44:45], s[42:43]
v_add_u32 v14, 18688, v14
v_cvt_pk_bf16_f32 v165, v114, v115
v_mfma_f32_16x16x32_bf16 v[128:131], v[68:71], v[140:143], v[128:131]
s_waitcnt lgkmcnt(2)
v_cvt_pk_bf16_f32 v166, v116, v117
v_cvt_pk_bf16_f32 v167, v118, v119
v_mfma_f32_16x16x32_bf16 v[96:99], a[192:195], v[144:147], v[96:99]
s_xor_b32 s49, s49, 0x10000
ds_read_b128 v[140:143], v25 offset:15360
v_add_f32 v254, v112, v113
v_mfma_f32_16x16x32_bf16 v[100:103], a[196:199], v[144:147], v[100:103]
s_mov_b32 m0, 53248
global_store_dwordx4 v13, v[164:167], s[38:39] offset:0 sc1
v_mfma_f32_16x16x32_bf16 v[104:107], a[200:203], v[144:147], v[104:107]
v_cvt_pk_bf16_f32 v168, v120, v121
v_cvt_pk_bf16_f32 v169, v122, v123
v_mfma_f32_16x16x32_bf16 v[108:111], a[204:207], v[144:147], v[108:111]
v_cvt_pk_bf16_f32 v170, v124, v125
v_cvt_pk_bf16_f32 v171, v126, v127
v_mfma_f32_16x16x32_bf16 v[128:131], v[72:75], v[144:147], v[128:131]
s_waitcnt lgkmcnt(2)
global_store_dwordx4 v13, v[168:171], s[38:39] offset:1024 sc1
v_mfma_f32_16x16x32_bf16 v[96:99], a[208:211], v[132:135], v[96:99]
ds_read_b128 v[144:147], v25 offset:16384
v_add_u32 v13, 18688, v13
v_mfma_f32_16x16x32_bf16 v[100:103], a[212:215], v[132:135], v[100:103]
v_mul_f32 v255, v112, v112
v_fmac_f32 v255, v113, v113
v_mfma_f32_16x16x32_bf16 v[104:107], a[216:219], v[132:135], v[104:107]
v_add_f32 v254, v254, v114
v_fmac_f32 v255, v114, v114
v_mfma_f32_16x16x32_bf16 v[108:111], a[220:223], v[132:135], v[108:111]
v_add_f32 v254, v254, v115
v_fmac_f32 v255, v115, v115
v_mfma_f32_16x16x32_bf16 v[128:131], v[76:79], v[132:135], v[128:131]
s_waitcnt vmcnt(7)
global_load_lds_dwordx4 v16, s[42:43] offset:0 sc1
ds_read_b128 v[148:151], v19 offset:8192
s_waitcnt lgkmcnt(3)
v_mfma_f32_16x16x32_bf16 v[96:99], a[224:227], v[136:139], v[96:99]
ds_read_b128 v[132:135], v25 offset:17408
ds_read_b128 v[152:155], v20 offset:8192
v_mfma_f32_16x16x32_bf16 v[100:103], a[228:231], v[136:139], v[100:103]
v_add_f32 v254, v254, v116
v_fmac_f32 v255, v116, v116
v_mfma_f32_16x16x32_bf16 v[104:107], a[232:235], v[136:139], v[104:107]
v_add_f32 v254, v254, v117
v_fmac_f32 v255, v117, v117
v_mfma_f32_16x16x32_bf16 v[108:111], a[236:239], v[136:139], v[108:111]
v_add_f32 v254, v254, v118
v_fmac_f32 v255, v118, v118
v_mfma_f32_16x16x32_bf16 v[128:131], v[80:83], v[136:139], v[128:131]
s_waitcnt lgkmcnt(4)
v_add_f32 v254, v254, v119
v_fmac_f32 v255, v119, v119
v_mfma_f32_16x16x32_bf16 v[96:99], a[240:243], v[140:143], v[96:99]
ds_read_b128 v[136:139], v25 offset:0
v_add_f32 v254, v254, v120
v_mfma_f32_16x16x32_bf16 v[100:103], a[244:247], v[140:143], v[100:103]
global_load_lds_dwordx4 v17, s[42:43] offset:1024 sc1
v_fmac_f32 v255, v120, v120
v_mfma_f32_16x16x32_bf16 v[104:107], a[248:251], v[140:143], v[104:107]
v_add_f32 v254, v254, v121
v_fmac_f32 v255, v121, v121
v_mfma_f32_16x16x32_bf16 v[108:111], a[252:255], v[140:143], v[108:111]
v_add_f32 v254, v254, v122
v_fmac_f32 v255, v122, v122
v_mfma_f32_16x16x32_bf16 v[128:131], v[84:87], v[140:143], v[128:131]
s_waitcnt lgkmcnt(4)
v_add_f32 v254, v254, v123
v_fmac_f32 v255, v123, v123
v_mfma_f32_16x16x32_bf16 v[96:99], v[28:31], v[144:147], v[96:99]
ds_read_b128 v[140:143], v25 offset:1024
v_add_f32 v254, v254, v124
v_mfma_f32_16x16x32_bf16 v[100:103], v[32:35], v[144:147], v[100:103]
s_add_u32 m0, s49, 0
v_fmac_f32 v255, v124, v124
v_add_f32 v254, v254, v125
v_mfma_f32_16x16x32_bf16 v[104:107], v[36:39], v[144:147], v[104:107]
v_fmac_f32 v255, v125, v125
v_add_f32 v254, v254, v126
v_mfma_f32_16x16x32_bf16 v[108:111], v[40:43], v[144:147], v[108:111]
v_fmac_f32 v255, v126, v126
v_add_f32 v254, v254, v127
v_mfma_f32_16x16x32_bf16 v[128:131], v[88:91], v[144:147], v[128:131]
s_waitcnt lgkmcnt(3)
v_fmac_f32 v255, v127, v127
v_add_f32 v254, v254, v176
v_mfma_f32_16x16x32_bf16 v[96:99], v[44:47], v[132:135], v[96:99]
ds_read_b128 v[144:147], v25 offset:2048
v_fmac_f32 v255, v176, v176
v_mfma_f32_16x16x32_bf16 v[100:103], v[48:51], v[132:135], v[100:103]
v_add_f32 v254, v254, v177
v_fmac_f32 v255, v177, v177
v_mfma_f32_16x16x32_bf16 v[104:107], v[52:55], v[132:135], v[104:107]
v_add_f32 v254, v254, v178
v_fmac_f32 v255, v178, v178
v_mfma_f32_16x16x32_bf16 v[108:111], v[56:59], v[132:135], v[108:111]
v_add_f32 v254, v254, v179
v_fmac_f32 v255, v179, v179
v_mfma_f32_16x16x32_bf16 v[128:131], v[92:95], v[132:135], v[128:131]
s_waitcnt lgkmcnt(2)
v_add_f32 v254, v254, v180
v_fmac_f32 v255, v180, v180
v_mfma_f32_16x16x32_bf16 v[96:99], a[0:3], v[136:139], v[96:99]
ds_read_b128 v[132:135], v25 offset:3072
v_add_f32 v254, v254, v181
v_mfma_f32_16x16x32_bf16 v[100:103], a[4:7], v[136:139], v[100:103]
v_fmac_f32 v255, v181, v181
v_add_f32 v254, v254, v182
v_mfma_f32_16x16x32_bf16 v[104:107], a[8:11], v[136:139], v[104:107]
v_fmac_f32 v255, v182, v182
v_add_f32 v254, v254, v183
v_mfma_f32_16x16x32_bf16 v[108:111], a[12:15], v[136:139], v[108:111]
s_waitcnt lgkmcnt(2)
v_fmac_f32 v255, v183, v183
v_mfma_f32_16x16x32_bf16 v[96:99], a[16:19], v[140:143], v[96:99]
ds_read_b128 v[136:139], v25 offset:4096
v_mfma_f32_16x16x32_bf16 v[100:103], a[20:23], v[140:143], v[100:103]
ds_write_b64 v27, v[254:255] offset:46592
v_mfma_f32_16x16x32_bf16 v[104:107], a[24:27], v[140:143], v[104:107]
v_lshlrev_b32 v176, 16, v156
v_and_b32 v177, 0xffff0000, v156
v_mfma_f32_16x16x32_bf16 v[108:111], a[28:31], v[140:143], v[108:111]
s_waitcnt lgkmcnt(3)
v_lshlrev_b32 v178, 16, v157
v_and_b32 v179, 0xffff0000, v157
v_mfma_f32_16x16x32_bf16 v[96:99], a[32:35], v[144:147], v[96:99]
ds_read_b128 v[140:143], v25 offset:5120
v_lshlrev_b32 v180, 16, v158
v_mfma_f32_16x16x32_bf16 v[100:103], a[36:39], v[144:147], v[100:103]
v_mfma_f32_16x16x32_bf16 v[104:107], a[40:43], v[144:147], v[104:107]
s_barrier
v_mfma_f32_16x16x32_bf16 v[108:111], a[44:47], v[144:147], v[108:111]
s_waitcnt lgkmcnt(3)
v_and_b32 v181, 0xffff0000, v158
v_lshlrev_b32 v182, 16, v159
v_mfma_f32_16x16x32_bf16 v[96:99], a[48:51], v[132:135], v[96:99]
ds_read_b128 v[144:147], v25 offset:6144
global_load_lds_dwordx4 v11, s[38:39] offset:-2048 sc1
v_mfma_f32_16x16x32_bf16 v[100:103], a[52:55], v[132:135], v[100:103]
v_and_b32 v183, 0xffff0000, v159
v_add_f32 v180, v180, v128
v_mfma_f32_16x16x32_bf16 v[104:107], a[56:59], v[132:135], v[104:107]
v_add_f32 v181, v181, v129
v_add_f32 v182, v182, v130
v_mfma_f32_16x16x32_bf16 v[108:111], a[60:63], v[132:135], v[108:111]
s_waitcnt lgkmcnt(3)
global_load_lds_dwordx4 v11, s[38:39] offset:-1024 sc1
v_add_f32 v183, v183, v131
v_mfma_f32_16x16x32_bf16 v[96:99], a[64:67], v[136:139], v[96:99]
ds_read_b128 v[132:135], v25 offset:7168
global_load_dwordx4 v[156:159], v18, s[42:43] sc1
v_mfma_f32_16x16x32_bf16 v[100:103], a[68:71], v[136:139], v[100:103]
s_sub_u32 s42, s42, 18432
v_lshlrev_b32 v112, 16, v148
v_and_b32 v113, 0xffff0000, v148
v_mfma_f32_16x16x32_bf16 v[104:107], a[72:75], v[136:139], v[104:107]
s_subb_u32 s43, s43, 0
global_load_lds_dwordx4 v11, s[38:39] offset:0 sc1
v_add_u32 v11, 18432, v11
v_mfma_f32_16x16x32_bf16 v[108:111], a[76:79], v[136:139], v[108:111]
s_waitcnt lgkmcnt(2)
v_min_u32 v11, v11, v12
v_lshlrev_b32 v114, 16, v149
v_mfma_f32_16x16x32_bf16 v[96:99], a[80:83], v[140:143], v[96:99]
ds_read_b128 v[136:139], v25 offset:8192
v_and_b32 v115, 0xffff0000, v149
v_mfma_f32_16x16x32_bf16 v[100:103], a[84:87], v[140:143], v[100:103]
v_lshlrev_b32 v116, 16, v150
v_and_b32 v117, 0xffff0000, v150
v_mfma_f32_16x16x32_bf16 v[104:107], a[88:91], v[140:143], v[104:107]
v_lshlrev_b32 v118, 16, v151
v_and_b32 v119, 0xffff0000, v151
v_mfma_f32_16x16x32_bf16 v[108:111], a[92:95], v[140:143], v[108:111]
s_waitcnt lgkmcnt(2)
v_lshlrev_b32 v120, 16, v152
v_and_b32 v121, 0xffff0000, v152
v_mfma_f32_16x16x32_bf16 v[96:99], a[96:99], v[144:147], v[96:99]
v_lshlrev_b32 v122, 16, v153
v_and_b32 v123, 0xffff0000, v153
v_mfma_f32_16x16x32_bf16 v[100:103], a[100:103], v[144:147], v[100:103]
v_mfma_f32_16x16x32_bf16 v[104:107], a[104:107], v[144:147], v[104:107]
s_waitcnt vmcnt(9)
s_barrier
v_mfma_f32_16x16x32_bf16 v[108:111], a[108:111], v[144:147], v[108:111]
s_waitcnt lgkmcnt(1)
ds_read_b128 v[140:143], v25 offset:27648
ds_read_b128 v[184:187], v26 offset:36864
v_mfma_f32_16x16x32_bf16 v[96:99], a[112:115], v[132:135], v[96:99]
ds_read_b128 v[144:147], v25 offset:28672
ds_read_b128 v[188:191], v26 offset:37888
v_mfma_f32_16x16x32_bf16 v[100:103], a[116:119], v[132:135], v[100:103]
ds_read_b128 v[192:195], v26 offset:38912
v_lshlrev_b32 v124, 16, v154
v_mfma_f32_16x16x32_bf16 v[104:107], a[120:123], v[132:135], v[104:107]
v_and_b32 v125, 0xffff0000, v154
v_lshlrev_b32 v126, 16, v155
v_mfma_f32_16x16x32_bf16 v[108:111], a[124:127], v[132:135], v[108:111]
s_waitcnt lgkmcnt(5)
v_and_b32 v127, 0xffff0000, v155
v_mfma_f32_16x16x32_bf16 v[96:99], a[128:131], v[136:139], v[96:99]
ds_read_b128 v[132:135], v25 offset:29696
v_mfma_f32_16x16x32_bf16 v[100:103], a[132:135], v[136:139], v[100:103]
v_mfma_f32_16x16x32_bf16 v[104:107], a[136:139], v[136:139], v[104:107]
s_waitcnt lgkmcnt(4)
v_add_f32 v176, v176, v184
v_add_f32 v177, v177, v185
v_mfma_f32_16x16x32_bf16 v[108:111], a[140:143], v[136:139], v[108:111]
v_add_f32 v178, v178, v186
v_add_f32 v179, v179, v187
v_mfma_f32_16x16x32_bf16 v[112:115], a[144:147], v[140:143], v[112:115]
s_waitcnt lgkmcnt(2)
ds_read_b128 v[136:139], v25 offset:30720
v_add_f32 v176, v176, v188
v_mfma_f32_16x16x32_bf16 v[116:119], a[148:151], v[140:143], v[116:119]
v_mfma_f32_16x16x32_bf16 v[120:123], a[152:155], v[140:143], v[120:123]
s_waitcnt vmcnt(6)
s_barrier
v_mfma_f32_16x16x32_bf16 v[124:127], a[156:159], v[140:143], v[124:127]
v_add_f32 v177, v177, v189
v_add_f32 v178, v178, v190
v_mfma_f32_16x16x32_bf16 v[128:131], v[60:63], v[140:143], 0
s_waitcnt lgkmcnt(2)
v_add_f32 v179, v179, v191
v_add_f32 v180, v180, v192
v_mfma_f32_16x16x32_bf16 v[112:115], a[160:163], v[144:147], v[112:115]
ds_read_b128 v[140:143], v25 offset:31744
v_add_f32 v181, v181, v193
v_mfma_f32_16x16x32_bf16 v[116:119], a[164:167], v[144:147], v[116:119]
v_add_f32 v182, v182, v194
v_add_f32 v183, v183, v195
v_mfma_f32_16x16x32_bf16 v[120:123], a[168:171], v[144:147], v[120:123]
v_cvt_pk_bf16_f32 v172, v176, v177
v_cvt_pk_bf16_f32 v173, v178, v179
v_mfma_f32_16x16x32_bf16 v[124:127], a[172:175], v[144:147], v[124:127]
v_cvt_pk_bf16_f32 v174, v180, v181
v_cvt_pk_bf16_f32 v175, v182, v183
)"
R"(v_mfma_f32_16x16x32_bf16 v[128:131], v[64:67], v[144:147], v[128:131]
s_waitcnt lgkmcnt(2)
global_store_dwordx4 v14, v[172:175], s[38:39] sc1
v_mfma_f32_16x16x32_bf16 v[112:115], a[176:179], v[132:135], v[112:115]
s_mov_b32 m0, 61440
ds_read_b128 v[144:147], v25 offset:32768
v_add_u32 v14, 18688, v14
v_mfma_f32_16x16x32_bf16 v[116:119], a[180:183], v[132:135], v[116:119]
v_cvt_pk_bf16_f32 v164, v96, v97
v_cvt_pk_bf16_f32 v165, v98, v99
v_mfma_f32_16x16x32_bf16 v[120:123], a[184:187], v[132:135], v[120:123]
v_cvt_pk_bf16_f32 v166, v100, v101
v_cvt_pk_bf16_f32 v167, v102, v103
v_mfma_f32_16x16x32_bf16 v[124:127], a[188:191], v[132:135], v[124:127]
global_store_dwordx4 v13, v[164:167], s[38:39] offset:0 sc1
v_mfma_f32_16x16x32_bf16 v[128:131], v[68:71], v[132:135], v[128:131]
s_waitcnt lgkmcnt(2)
v_cvt_pk_bf16_f32 v168, v104, v105
v_cvt_pk_bf16_f32 v169, v106, v107
v_mfma_f32_16x16x32_bf16 v[112:115], a[192:195], v[136:139], v[112:115]
ds_read_b128 v[132:135], v25 offset:33792
v_cvt_pk_bf16_f32 v170, v108, v109
v_mfma_f32_16x16x32_bf16 v[116:119], a[196:199], v[136:139], v[116:119]
v_cvt_pk_bf16_f32 v171, v110, v111
v_add_f32 v254, v96, v97
v_mfma_f32_16x16x32_bf16 v[120:123], a[200:203], v[136:139], v[120:123]
global_store_dwordx4 v13, v[168:171], s[38:39] offset:1024 sc1
v_mfma_f32_16x16x32_bf16 v[124:127], a[204:207], v[136:139], v[124:127]
v_add_u32 v13, 18688, v13
v_mul_f32 v255, v96, v96
v_mfma_f32_16x16x32_bf16 v[128:131], v[72:75], v[136:139], v[128:131]
s_waitcnt lgkmcnt(2)
v_fmac_f32 v255, v97, v97
v_add_f32 v254, v254, v98
v_mfma_f32_16x16x32_bf16 v[112:115], a[208:211], v[140:143], v[112:115]
ds_read_b128 v[136:139], v25 offset:34816
v_fmac_f32 v255, v98, v98
v_mfma_f32_16x16x32_bf16 v[116:119], a[212:215], v[140:143], v[116:119]
v_add_f32 v254, v254, v99
v_fmac_f32 v255, v99, v99
v_mfma_f32_16x16x32_bf16 v[120:123], a[216:219], v[140:143], v[120:123]
v_add_f32 v254, v254, v100
v_fmac_f32 v255, v100, v100
v_mfma_f32_16x16x32_bf16 v[124:127], a[220:223], v[140:143], v[124:127]
v_add_f32 v254, v254, v101
v_fmac_f32 v255, v101, v101
v_mfma_f32_16x16x32_bf16 v[128:131], v[76:79], v[140:143], v[128:131]
s_waitcnt vmcnt(7)
global_load_lds_dwordx4 v16, s[42:43] offset:0 sc1
ds_read_b128 v[148:151], v19 offset:0
s_waitcnt lgkmcnt(3)
v_mfma_f32_16x16x32_bf16 v[112:115], a[224:227], v[144:147], v[112:115]
ds_read_b128 v[140:143], v25 offset:35840
ds_read_b128 v[152:155], v20 offset:0
v_mfma_f32_16x16x32_bf16 v[116:119], a[228:231], v[144:147], v[116:119]
v_add_f32 v254, v254, v102
v_fmac_f32 v255, v102, v102
v_mfma_f32_16x16x32_bf16 v[120:123], a[232:235], v[144:147], v[120:123]
v_add_f32 v254, v254, v103
v_fmac_f32 v255, v103, v103
v_mfma_f32_16x16x32_bf16 v[124:127], a[236:239], v[144:147], v[124:127]
v_add_f32 v254, v254, v104
v_fmac_f32 v255, v104, v104
v_mfma_f32_16x16x32_bf16 v[128:131], v[80:83], v[144:147], v[128:131]
s_waitcnt lgkmcnt(4)
v_add_f32 v254, v254, v105
v_fmac_f32 v255, v105, v105
v_mfma_f32_16x16x32_bf16 v[112:115], a[240:243], v[132:135], v[112:115]
ds_read_b128 v[144:147], v25 offset:18432
v_add_f32 v254, v254, v106
v_mfma_f32_16x16x32_bf16 v[116:119], a[244:247], v[132:135], v[116:119]
global_load_lds_dwordx4 v17, s[42:43] offset:1024 sc1
v_fmac_f32 v255, v106, v106
v_mfma_f32_16x16x32_bf16 v[120:123], a[248:251], v[132:135], v[120:123]
v_add_f32 v254, v254, v107
v_fmac_f32 v255, v107, v107
v_mfma_f32_16x16x32_bf16 v[124:127], a[252:255], v[132:135], v[124:127]
v_add_f32 v254, v254, v108
v_fmac_f32 v255, v108, v108
v_mfma_f32_16x16x32_bf16 v[128:131], v[84:87], v[132:135], v[128:131]
s_waitcnt lgkmcnt(4)
v_add_f32 v254, v254, v109
v_fmac_f32 v255, v109, v109
v_mfma_f32_16x16x32_bf16 v[112:115], v[28:31], v[136:139], v[112:115]
ds_read_b128 v[132:135], v25 offset:19456
v_add_f32 v254, v254, v110
v_mfma_f32_16x16x32_bf16 v[116:119], v[32:35], v[136:139], v[116:119]
s_add_u32 m0, s49, 18432
v_fmac_f32 v255, v110, v110
v_add_f32 v254, v254, v111
v_mfma_f32_16x16x32_bf16 v[120:123], v[36:39], v[136:139], v[120:123]
v_fmac_f32 v255, v111, v111
v_add_f32 v254, v254, v176
v_mfma_f32_16x16x32_bf16 v[124:127], v[40:43], v[136:139], v[124:127]
v_fmac_f32 v255, v176, v176
v_add_f32 v254, v254, v177
v_mfma_f32_16x16x32_bf16 v[128:131], v[88:91], v[136:139], v[128:131]
s_waitcnt lgkmcnt(3)
v_fmac_f32 v255, v177, v177
v_add_f32 v254, v254, v178
v_mfma_f32_16x16x32_bf16 v[112:115], v[44:47], v[140:143], v[112:115]
ds_read_b128 v[136:139], v25 offset:20480
v_fmac_f32 v255, v178, v178
v_mfma_f32_16x16x32_bf16 v[116:119], v[48:51], v[140:143], v[116:119]
v_add_f32 v254, v254, v179
v_fmac_f32 v255, v179, v179
v_mfma_f32_16x16x32_bf16 v[120:123], v[52:55], v[140:143], v[120:123]
v_add_f32 v254, v254, v180
v_fmac_f32 v255, v180, v180
v_mfma_f32_16x16x32_bf16 v[124:127], v[56:59], v[140:143], v[124:127]
v_add_f32 v254, v254, v181
v_fmac_f32 v255, v181, v181
v_mfma_f32_16x16x32_bf16 v[128:131], v[92:95], v[140:143], v[128:131]
s_waitcnt lgkmcnt(2)
v_add_f32 v254, v254, v182
v_fmac_f32 v255, v182, v182
v_mfma_f32_16x16x32_bf16 v[112:115], a[0:3], v[144:147], v[112:115]
ds_read_b128 v[140:143], v25 offset:21504
v_add_f32 v254, v254, v183
v_mfma_f32_16x16x32_bf16 v[116:119], a[4:7], v[144:147], v[116:119]
v_fmac_f32 v255, v183, v183
v_mfma_f32_16x16x32_bf16 v[120:123], a[8:11], v[144:147], v[120:123]
ds_write_b64 v27, v[254:255] offset:44544
v_mfma_f32_16x16x32_bf16 v[124:127], a[12:15], v[144:147], v[124:127]
s_waitcnt lgkmcnt(3)
v_lshlrev_b32 v176, 16, v160
v_and_b32 v177, 0xffff0000, v160
v_mfma_f32_16x16x32_bf16 v[112:115], a[16:19], v[132:135], v[112:115]
ds_read_b128 v[144:147], v25 offset:22528
v_lshlrev_b32 v178, 16, v161
v_mfma_f32_16x16x32_bf16 v[116:119], a[20:23], v[132:135], v[116:119]
v_and_b32 v179, 0xffff0000, v161
v_lshlrev_b32 v180, 16, v162
v_mfma_f32_16x16x32_bf16 v[120:123], a[24:27], v[132:135], v[120:123]
v_and_b32 v181, 0xffff0000, v162
v_lshlrev_b32 v182, 16, v163
v_mfma_f32_16x16x32_bf16 v[124:127], a[28:31], v[132:135], v[124:127]
s_waitcnt lgkmcnt(3)
v_and_b32 v183, 0xffff0000, v163
v_add_f32 v180, v180, v128
v_mfma_f32_16x16x32_bf16 v[112:115], a[32:35], v[136:139], v[112:115]
ds_read_b128 v[132:135], v25 offset:23552
v_add_f32 v181, v181, v129
v_mfma_f32_16x16x32_bf16 v[116:119], a[36:39], v[136:139], v[116:119]
v_mfma_f32_16x16x32_bf16 v[120:123], a[40:43], v[136:139], v[120:123]
s_barrier
v_mfma_f32_16x16x32_bf16 v[124:127], a[44:47], v[136:139], v[124:127]
s_waitcnt lgkmcnt(3)
v_add_f32 v182, v182, v130
v_add_f32 v183, v183, v131
v_mfma_f32_16x16x32_bf16 v[112:115], a[48:51], v[140:143], v[112:115]
s_mov_b64 s[76:77], -1
ds_read_b128 v[136:139], v25 offset:24576
global_load_lds_dwordx4 v11, s[38:39] offset:-2048 sc1
v_mfma_f32_16x16x32_bf16 v[116:119], a[52:55], v[140:143], v[116:119]
global_load_dwordx4 v[160:163], v18, s[42:43] sc1
v_lshlrev_b32 v96, 16, v148
v_mfma_f32_16x16x32_bf16 v[120:123], a[56:59], v[140:143], v[120:123]
s_sub_u32 s42, s42, 18432
v_and_b32 v97, 0xffff0000, v148
v_lshlrev_b32 v98, 16, v149
.Lwo_in18:
v_mfma_f32_16x16x32_bf16 v[124:127], a[60:63], v[140:143], v[124:127]
s_waitcnt lgkmcnt(2)
global_load_lds_dwordx4 v11, s[38:39] offset:-1024 sc1
v_and_b32 v99, 0xffff0000, v149
v_mfma_f32_16x16x32_bf16 v[112:115], a[64:67], v[144:147], v[112:115]
s_subb_u32 s43, s43, 0
ds_read_b128 v[140:143], v25 offset:25600
v_lshlrev_b32 v100, 16, v150
v_mfma_f32_16x16x32_bf16 v[116:119], a[68:71], v[144:147], v[116:119]
v_and_b32 v101, 0xffff0000, v150
v_lshlrev_b32 v102, 16, v151
v_mfma_f32_16x16x32_bf16 v[120:123], a[72:75], v[144:147], v[120:123]
global_load_lds_dwordx4 v11, s[38:39] offset:0 sc1
v_add_u32 v11, 18432, v11
v_mfma_f32_16x16x32_bf16 v[124:127], a[76:79], v[144:147], v[124:127]
s_waitcnt lgkmcnt(2)
v_min_u32 v11, v11, v12
v_and_b32 v103, 0xffff0000, v151
v_mfma_f32_16x16x32_bf16 v[112:115], a[80:83], v[132:135], v[112:115]
ds_read_b128 v[144:147], v25 offset:26624
v_lshlrev_b32 v104, 16, v152
v_mfma_f32_16x16x32_bf16 v[116:119], a[84:87], v[132:135], v[116:119]
v_and_b32 v105, 0xffff0000, v152
v_lshlrev_b32 v106, 16, v153
v_mfma_f32_16x16x32_bf16 v[120:123], a[88:91], v[132:135], v[120:123]
v_and_b32 v107, 0xffff0000, v153
v_lshlrev_b32 v108, 16, v154
v_mfma_f32_16x16x32_bf16 v[124:127], a[92:95], v[132:135], v[124:127]
s_waitcnt lgkmcnt(2)
v_xor_b32 v25, 0x10000, v25
v_and_b32 v109, 0xffff0000, v154
v_mfma_f32_16x16x32_bf16 v[112:115], a[96:99], v[136:139], v[112:115]
v_lshlrev_b32 v110, 16, v155
v_and_b32 v111, 0xffff0000, v155
v_mfma_f32_16x16x32_bf16 v[116:119], a[100:103], v[136:139], v[116:119]
v_mfma_f32_16x16x32_bf16 v[120:123], a[104:107], v[136:139], v[120:123]
s_waitcnt vmcnt(9)
s_barrier
v_mfma_f32_16x16x32_bf16 v[124:127], a[108:111], v[136:139], v[124:127]
s_waitcnt lgkmcnt(1)
ds_read_b128 v[132:135], v25 offset:9216
ds_read_b128 v[184:187], v26 offset:39936
v_mfma_f32_16x16x32_bf16 v[112:115], a[112:115], v[140:143], v[112:115]
ds_read_b128 v[136:139], v25 offset:10240
ds_read_b128 v[188:191], v26 offset:40960
v_mfma_f32_16x16x32_bf16 v[116:119], a[116:119], v[140:143], v[116:119]
ds_read_b128 v[192:195], v26 offset:41984
v_mfma_f32_16x16x32_bf16 v[120:123], a[120:123], v[140:143], v[120:123]
v_mfma_f32_16x16x32_bf16 v[124:127], a[124:127], v[140:143], v[124:127]
s_waitcnt lgkmcnt(5)
v_mfma_f32_16x16x32_bf16 v[112:115], a[128:131], v[144:147], v[112:115]
ds_read_b128 v[140:143], v25 offset:11264
v_mfma_f32_16x16x32_bf16 v[116:119], a[132:135], v[144:147], v[116:119]
v_mfma_f32_16x16x32_bf16 v[120:123], a[136:139], v[144:147], v[120:123]
v_mfma_f32_16x16x32_bf16 v[124:127], a[140:143], v[144:147], v[124:127]
s_sub_u32 s70, s70, 1
s_cbranch_scc0 .Lwo_top19
s_waitcnt vmcnt(6)
s_barrier
s_waitcnt lgkmcnt(4)
v_add_f32 v176, v176, v184
v_add_f32 v177, v177, v185
v_add_f32 v178, v178, v186
v_add_f32 v179, v179, v187
s_waitcnt lgkmcnt(2)
v_add_f32 v176, v176, v188
v_add_f32 v177, v177, v189
v_add_f32 v178, v178, v190
v_add_f32 v179, v179, v191
s_waitcnt lgkmcnt(1)
v_add_f32 v180, v180, v192
v_add_f32 v181, v181, v193
v_add_f32 v182, v182, v194
v_add_f32 v183, v183, v195
v_cvt_pk_bf16_f32 v172, v176, v177
v_cvt_pk_bf16_f32 v173, v178, v179
v_cvt_pk_bf16_f32 v174, v180, v181
v_cvt_pk_bf16_f32 v175, v182, v183
v_cvt_pk_bf16_f32 v164, v112, v113
global_store_dwordx4 v14, v[172:175], s[38:39] sc1
v_cvt_pk_bf16_f32 v165, v114, v115
v_cvt_pk_bf16_f32 v166, v116, v117
v_cvt_pk_bf16_f32 v167, v118, v119
global_store_dwordx4 v13, v[164:167], s[38:39] offset:0 sc1
v_cvt_pk_bf16_f32 v168, v120, v121
v_cvt_pk_bf16_f32 v169, v122, v123
v_cvt_pk_bf16_f32 v170, v124, v125
v_cvt_pk_bf16_f32 v171, v126, v127
global_store_dwordx4 v13, v[168:171], s[38:39] offset:1024 sc1
v_add_f32 v254, v112, v113
v_mul_f32 v255, v112, v112
v_fmac_f32 v255, v113, v113
v_add_f32 v254, v254, v114
v_fmac_f32 v255, v114, v114
v_add_f32 v254, v254, v115
v_fmac_f32 v255, v115, v115
v_add_f32 v254, v254, v116
v_fmac_f32 v255, v116, v116
v_add_f32 v254, v254, v117
v_fmac_f32 v255, v117, v117
v_add_f32 v254, v254, v118
v_fmac_f32 v255, v118, v118
v_add_f32 v254, v254, v119
v_fmac_f32 v255, v119, v119
v_add_f32 v254, v254, v120
v_fmac_f32 v255, v120, v120
v_add_f32 v254, v254, v121
v_fmac_f32 v255, v121, v121
v_add_f32 v254, v254, v122
v_fmac_f32 v255, v122, v122
v_add_f32 v254, v254, v123
v_fmac_f32 v255, v123, v123
v_add_f32 v254, v254, v124
v_fmac_f32 v255, v124, v124
v_add_f32 v254, v254, v125
v_fmac_f32 v255, v125, v125
v_add_f32 v254, v254, v126
v_fmac_f32 v255, v126, v126
v_add_f32 v254, v254, v127
v_fmac_f32 v255, v127, v127
v_add_f32 v254, v254, v176
v_fmac_f32 v255, v176, v176
v_add_f32 v254, v254, v177
v_fmac_f32 v255, v177, v177
v_add_f32 v254, v254, v178
v_fmac_f32 v255, v178, v178
v_add_f32 v254, v254, v179
v_fmac_f32 v255, v179, v179
v_add_f32 v254, v254, v180
v_fmac_f32 v255, v180, v180
v_add_f32 v254, v254, v181
v_fmac_f32 v255, v181, v181
v_add_f32 v254, v254, v182
v_fmac_f32 v255, v182, v182
v_add_f32 v254, v254, v183
v_fmac_f32 v255, v183, v183
ds_write_b64 v27, v[254:255] offset:46592
s_waitcnt vmcnt(0) lgkmcnt(0)
s_barrier
s_barrier
s_endpgm
.Lwo_sp8:
s_sleep 1
v_add_lshl_u32 v253, v22, 0, 7
v_bfi_b32 v253, s69, v253, v21
global_load_dword v200, v253, s[38:39] sc1
s_add_u32 s71, s71, 1
s_waitcnt vmcnt(0)
v_sub_u32 v200, v200, v22
v_cmp_lt_u32 vcc, 0x7f, v200
s_and_b64 vcc, vcc, s[76:77]
s_cbranch_vccz .Lwo_rt9
s_cmp_lt_u32 s71, 3000000
s_cbranch_scc1 .Lwo_sp8
s_branch .Lwo_rt9
.Lwo_sp10:
s_sleep 1
v_add_lshl_u32 v253, v22, 0, 7
v_bfi_b32 v253, s69, v253, v21
global_load_dword v201, v253, s[38:39] sc1
s_add_u32 s71, s71, 1
s_waitcnt vmcnt(0)
v_sub_u32 v201, v201, v22
v_cmp_lt_u32 vcc, 0x7f, v201
s_and_b64 vcc, vcc, s[76:77]
s_cbranch_vccz .Lwo_rt11
s_cmp_lt_u32 s71, 3000000
s_cbranch_scc1 .Lwo_sp10
s_branch .Lwo_rt11
.Le1:
)"
    :: "{v[0:31]}"(va), "{s[36:51]}"(sa) : "v255", "a255", "s99", "vcc", "memory");
  } else if (jj < (ev ? 16 : 12)) {
    const unsigned dn_i = ev ? 48 + xh * 6 + jj - 10 : xh * 12 + jj, pf_g = (xh & 1) * 18 + (ev ? 10 : 0), pf_n = ev ? 24 : 48, pf_w = (ev ? jj - 10 : jj) * 4 + wave, pf_m = ev ? 8 : 10;
    const unsigned r = lane & 15, g = lane >> 4, q = r >> 2, p = lane & 3;
    const unsigned tri = dn_i / 3, nu = dn_i % 3, kap = tri >> 2, rho = tri & 3, row0 = rho * RP + PAD;
    auto pf = [&](unsigned k) { const unsigned u = k * pf_n + pf_w, b = u / pf_m, pg = pf_g + u - b * pf_m; return b < 16 ? (64 + b * 32 + (lane >> 1)) * 9216 + pg / 6 * 1536 + (pg % 6) * 128 + (lane & 1) * 768 : 0u; };
    auto wv = [&](unsigned j) { const unsigned b = j * 1024 + lane * 16, rw = b / 384; return rw * (1152 - 384) + b + 4096 - j * 1024; };
    u32x32 va; u32x16 sa;
    va[0] = wv(0);
    va[1] = wv(1);
    va[2] = wv(2);
    va[3] = wv(3);
    va[4] = wv(4);
    va[5] = wv(5);
    va[6] = (8 * g + q) * 384 + wave * 64 + p * 16 + (kap & 1) * 8;
    va[7] = (8 * g + q) * 384 + wave * 64 + p * 16 + 8 - (kap & 1) * 8;
    va[8] = (8 * g + q) * 384 + 256 + wave * 32 + (p ^ (kap & 1)) * 8;
    va[9] = WS_H + row0 * HT + (kap * 24 + wave * 6 + 2) * 1024 + lane * 16;
    va[10] = WS_H + (row0 + 63) * HT + (kap * 24 + wave * 6 + 2) * 1024 + lane * 16;
    va[11] = WS_P + (row0 - 1) * PT + (((nu * 3 + (wave >> 1)) * 4 + q) * 6 + kap) * 512 + (wave & 1) * 256 + g * 64 + p * 16;
    va[12] = WS_P + (row0 - 1) * PT + (((nu * 3 + 2) * 4 + q) * 6 + kap) * 512 + wave * 128 + (g >> 1) * 64 + p * 16;
    va[13] = WS_FL + rho * FR + FH + (kap * 6 + min(lane, 5u)) * FL;
    va[14] = 0u;
    va[15] = WS_FL + rho * FR + FP + (nu * 6 + kap) * FL;
    va[16] = 0u - 2;
    va[17] = lane * 16;
    va[18] = pf(0);
    va[19] = pf(1);
    va[20] = pf(2);
    va[21] = pf(3);
    va[22] = pf(4);
    va[23] = pf(5);
    sa[0] = 8 + wave; sa[2] = (unsigned)(unsigned long long)ws; sa[3] = (unsigned)((unsigned long long)ws >> 32);
    sa[4] = uni((unsigned)(unsigned long long)(wu));
    sa[5] = uni((unsigned)((unsigned long long)(wu) >> 32));
    sa[6] = uni((unsigned)(unsigned long long)(wd + (kap * 768 + wave * 16) * 1152 + nu * 384));
    sa[7] = uni((unsigned)((unsigned long long)(wd + (kap * 768 + wave * 16) * 1152 + nu * 384) >> 32));
    sa[8] = uni(wave * 6144);
    sa[9] = uni(wave * 6144 + 2048);
    asm volatile(
R"(.Lb2:
s_movk_i32 s69, 0x380
s_mov_b32 s71, 0
s_mov_b32 s74, 0
s_load_dword s37, s[38:39], 0x0
s_nop 1
s_sleep 70
s_mov_b32 m0, 0x27f00
s_nop 0
global_load_lds_dword v18, s[40:41]
global_load_lds_dword v19, s[40:41]
global_load_lds_dword v20, s[40:41]
global_load_lds_dword v21, s[40:41]
global_load_lds_dword v22, s[40:41]
global_load_lds_dword v23, s[40:41]
s_waitcnt vmcnt(0)
s_add_u32 m0, s44, 4096
s_nop 0
global_load_lds_dwordx4 v0, s[42:43] offset:-4096 nt
global_load_lds_dwordx4 v1, s[42:43] offset:-3072 nt
global_load_lds_dwordx4 v2, s[42:43] offset:-2048 nt
global_load_lds_dwordx4 v3, s[42:43] offset:-1024 nt
global_load_lds_dwordx4 v4, s[42:43] offset:0 nt
global_load_lds_dwordx4 v5, s[42:43] offset:1024 nt
s_add_u32 s42, s42, 73728
s_addc_u32 s43, s43, 0
s_add_u32 m0, s44, 28672
s_nop 0
global_load_lds_dwordx4 v0, s[42:43] offset:-4096 nt
global_load_lds_dwordx4 v1, s[42:43] offset:-3072 nt
global_load_lds_dwordx4 v2, s[42:43] offset:-2048 nt
global_load_lds_dwordx4 v3, s[42:43] offset:-1024 nt
global_load_lds_dwordx4 v4, s[42:43] offset:0 nt
global_load_lds_dwordx4 v5, s[42:43] offset:1024 nt
s_add_u32 s42, s42, 73728
s_addc_u32 s43, s43, 0
s_add_u32 m0, s44, 53248
s_nop 0
global_load_lds_dwordx4 v0, s[42:43] offset:-4096 nt
global_load_lds_dwordx4 v1, s[42:43] offset:-3072 nt
global_load_lds_dwordx4 v2, s[42:43] offset:-2048 nt
global_load_lds_dwordx4 v3, s[42:43] offset:-1024 nt
global_load_lds_dwordx4 v4, s[42:43] offset:0 nt
global_load_lds_dwordx4 v5, s[42:43] offset:1024 nt
s_add_u32 s42, s42, 73728
s_addc_u32 s43, s43, 0
s_add_u32 m0, s44, 77824
s_nop 0
global_load_lds_dwordx4 v0, s[42:43] offset:-4096 nt
global_load_lds_dwordx4 v1, s[42:43] offset:-3072 nt
global_load_lds_dwordx4 v2, s[42:43] offset:-2048 nt
global_load_lds_dwordx4 v3, s[42:43] offset:-1024 nt
global_load_lds_dwordx4 v4, s[42:43] offset:0 nt
global_load_lds_dwordx4 v5, s[42:43] offset:1024 nt
s_add_u32 s42, s42, 73728
s_addc_u32 s43, s43, 0
s_add_u32 m0, s44, 102400
s_nop 0
global_load_lds_dwordx4 v0, s[42:43] offset:-4096 nt
global_load_lds_dwordx4 v1, s[42:43] offset:-3072 nt
global_load_lds_dwordx4 v2, s[42:43] offset:-2048 nt
global_load_lds_dwordx4 v3, s[42:43] offset:-1024 nt
global_load_lds_dwordx4 v4, s[42:43] offset:0 nt
global_load_lds_dwordx4 v5, s[42:43] offset:1024 nt
s_add_u32 s42, s42, 73728
s_addc_u32 s43, s43, 0
v_add_u32 v136, 0x10000, v6
v_add_u32 v137, 0x20000, v6
v_add_u32 v138, 0x10000, v7
v_add_u32 v139, 0x20000, v7
v_add_u32 v140, 0x10000, v8
v_add_u32 v141, 0x20000, v8
s_waitcnt lgkmcnt(0)
s_mul_i32 s37, s37, 0x9E3779B1
s_add_u32 s37, s37, 0x7F4A7C15
v_add_u32 v14, s37, v14
v_add_u32 v16, s37, v16
s_waitcnt vmcnt(24)
s_barrier
s_add_u32 m0, s44, 126976
ds_read_b64_tr_b16 a[0:1], v6 offset:0
ds_read_b64_tr_b16 a[2:3], v6 offset:1536
global_load_lds_dwordx4 v0, s[42:43] offset:-4096 nt
ds_read_b64_tr_b16 a[4:5], v7 offset:0
global_load_lds_dwordx4 v1, s[42:43] offset:-3072 nt
ds_read_b64_tr_b16 a[6:7], v7 offset:1536
global_load_lds_dwordx4 v2, s[42:43] offset:-2048 nt
ds_read_b64_tr_b16 a[8:9], v8 offset:0
ds_read_b64_tr_b16 a[10:11], v8 offset:1536
global_load_lds_dwordx4 v3, s[42:43] offset:-1024 nt
ds_read_b64_tr_b16 a[12:13], v6 offset:12288
global_load_lds_dwordx4 v4, s[42:43] offset:0 nt
ds_read_b64_tr_b16 a[14:15], v6 offset:13824
global_load_lds_dwordx4 v5, s[42:43] offset:1024 nt
ds_read_b64_tr_b16 a[16:17], v7 offset:12288
ds_read_b64_tr_b16 a[18:19], v7 offset:13824
s_add_u32 s42, s42, 73728
ds_read_b64_tr_b16 a[20:21], v8 offset:12288
s_addc_u32 s43, s43, 0
ds_read_b64_tr_b16 a[22:23], v8 offset:13824
s_waitcnt vmcnt(24) lgkmcnt(0)
s_barrier
s_add_u32 m0, s44, 4096
ds_read_b64_tr_b16 a[24:25], v6 offset:24576
ds_read_b64_tr_b16 a[26:27], v6 offset:26112
global_load_lds_dwordx4 v0, s[42:43] offset:-4096 nt
ds_read_b64_tr_b16 a[28:29], v7 offset:24576
global_load_lds_dwordx4 v1, s[42:43] offset:-3072 nt
ds_read_b64_tr_b16 a[30:31], v7 offset:26112
global_load_lds_dwordx4 v2, s[42:43] offset:-2048 nt
ds_read_b64_tr_b16 a[32:33], v8 offset:24576
ds_read_b64_tr_b16 a[34:35], v8 offset:26112
global_load_lds_dwordx4 v3, s[42:43] offset:-1024 nt
ds_read_b64_tr_b16 a[36:37], v6 offset:36864
global_load_lds_dwordx4 v4, s[42:43] offset:0 nt
ds_read_b64_tr_b16 a[38:39], v6 offset:38400
global_load_lds_dwordx4 v5, s[42:43] offset:1024 nt
ds_read_b64_tr_b16 a[40:41], v7 offset:36864
ds_read_b64_tr_b16 a[42:43], v7 offset:38400
s_add_u32 s42, s42, 73728
ds_read_b64_tr_b16 a[44:45], v8 offset:36864
s_addc_u32 s43, s43, 0
ds_read_b64_tr_b16 a[46:47], v8 offset:38400
s_waitcnt vmcnt(24) lgkmcnt(0)
s_barrier
s_add_u32 m0, s44, 28672
ds_read_b64_tr_b16 a[48:49], v6 offset:49152
ds_read_b64_tr_b16 a[50:51], v6 offset:50688
global_load_lds_dwordx4 v0, s[42:43] offset:-4096 nt
ds_read_b64_tr_b16 a[52:53], v7 offset:49152
global_load_lds_dwordx4 v1, s[42:43] offset:-3072 nt
ds_read_b64_tr_b16 a[54:55], v7 offset:50688
global_load_lds_dwordx4 v2, s[42:43] offset:-2048 nt
ds_read_b64_tr_b16 a[56:57], v8 offset:49152
ds_read_b64_tr_b16 a[58:59], v8 offset:50688
global_load_lds_dwordx4 v3, s[42:43] offset:-1024 nt
ds_read_b64_tr_b16 a[60:61], v6 offset:61440
global_load_lds_dwordx4 v4, s[42:43] offset:0 nt
ds_read_b64_tr_b16 a[62:63], v6 offset:62976
global_load_lds_dwordx4 v5, s[42:43] offset:1024 nt
ds_read_b64_tr_b16 a[64:65], v7 offset:61440
ds_read_b64_tr_b16 a[66:67], v7 offset:62976
s_add_u32 s42, s42, 73728
ds_read_b64_tr_b16 a[68:69], v8 offset:61440
s_addc_u32 s43, s43, 0
ds_read_b64_tr_b16 a[70:71], v8 offset:62976
s_waitcnt vmcnt(24) lgkmcnt(0)
s_barrier
s_add_u32 m0, s44, 53248
ds_read_b64_tr_b16 a[72:73], v136 offset:8192
ds_read_b64_tr_b16 a[74:75], v136 offset:9728
global_load_lds_dwordx4 v0, s[42:43] offset:-4096 nt
ds_read_b64_tr_b16 a[76:77], v138 offset:8192
global_load_lds_dwordx4 v1, s[42:43] offset:-3072 nt
ds_read_b64_tr_b16 a[78:79], v138 offset:9728
global_load_lds_dwordx4 v2, s[42:43] offset:-2048 nt
ds_read_b64_tr_b16 a[80:81], v140 offset:8192
ds_read_b64_tr_b16 a[82:83], v140 offset:9728
global_load_lds_dwordx4 v3, s[42:43] offset:-1024 nt
ds_read_b64_tr_b16 a[84:85], v136 offset:20480
global_load_lds_dwordx4 v4, s[42:43] offset:0 nt
ds_read_b64_tr_b16 a[86:87], v136 offset:22016
global_load_lds_dwordx4 v5, s[42:43] offset:1024 nt
ds_read_b64_tr_b16 a[88:89], v138 offset:20480
ds_read_b64_tr_b16 a[90:91], v138 offset:22016
s_add_u32 s42, s42, 73728
ds_read_b64_tr_b16 a[92:93], v140 offset:20480
s_addc_u32 s43, s43, 0
ds_read_b64_tr_b16 a[94:95], v140 offset:22016
s_waitcnt vmcnt(24) lgkmcnt(0)
s_barrier
s_add_u32 m0, s44, 77824
ds_read_b64_tr_b16 a[96:97], v136 offset:32768
ds_read_b64_tr_b16 a[98:99], v136 offset:34304
global_load_lds_dwordx4 v0, s[42:43] offset:-4096 nt
ds_read_b64_tr_b16 a[100:101], v138 offset:32768
global_load_lds_dwordx4 v1, s[42:43] offset:-3072 nt
ds_read_b64_tr_b16 a[102:103], v138 offset:34304
global_load_lds_dwordx4 v2, s[42:43] offset:-2048 nt
ds_read_b64_tr_b16 a[104:105], v140 offset:32768
ds_read_b64_tr_b16 a[106:107], v140 offset:34304
global_load_lds_dwordx4 v3, s[42:43] offset:-1024 nt
ds_read_b64_tr_b16 a[108:109], v136 offset:45056
global_load_lds_dwordx4 v4, s[42:43] offset:0 nt
ds_read_b64_tr_b16 a[110:111], v136 offset:46592
global_load_lds_dwordx4 v5, s[42:43] offset:1024 nt
ds_read_b64_tr_b16 a[112:113], v138 offset:45056
ds_read_b64_tr_b16 a[114:115], v138 offset:46592
s_add_u32 s42, s42, 73728
ds_read_b64_tr_b16 a[116:117], v140 offset:45056
s_addc_u32 s43, s43, 0
ds_read_b64_tr_b16 a[118:119], v140 offset:46592
s_waitcnt vmcnt(24) lgkmcnt(0)
s_barrier
s_add_u32 m0, s44, 102400
ds_read_b64_tr_b16 a[120:121], v136 offset:57344
ds_read_b64_tr_b16 a[122:123], v136 offset:58880
global_load_lds_dwordx4 v0, s[42:43] offset:-4096 nt
ds_read_b64_tr_b16 a[124:125], v138 offset:57344
global_load_lds_dwordx4 v1, s[42:43] offset:-3072 nt
ds_read_b64_tr_b16 a[126:127], v138 offset:58880
global_load_lds_dwordx4 v2, s[42:43] offset:-2048 nt
ds_read_b64_tr_b16 a[128:129], v140 offset:57344
ds_read_b64_tr_b16 a[130:131], v140 offset:58880
global_load_lds_dwordx4 v3, s[42:43] offset:-1024 nt
ds_read_b64_tr_b16 a[132:133], v137 offset:4096
global_load_lds_dwordx4 v4, s[42:43] offset:0 nt
ds_read_b64_tr_b16 a[134:135], v137 offset:5632
global_load_lds_dwordx4 v5, s[42:43] offset:1024 nt
ds_read_b64_tr_b16 a[136:137], v139 offset:4096
ds_read_b64_tr_b16 a[138:139], v139 offset:5632
s_add_u32 s42, s42, 73728
ds_read_b64_tr_b16 a[140:141], v141 offset:4096
s_addc_u32 s43, s43, 0
ds_read_b64_tr_b16 a[142:143], v141 offset:5632
s_waitcnt vmcnt(24) lgkmcnt(0)
s_barrier
s_add_u32 m0, s44, 126976
ds_read_b64_tr_b16 a[144:145], v6 offset:0
ds_read_b64_tr_b16 a[146:147], v6 offset:1536
global_load_lds_dwordx4 v0, s[42:43] offset:-4096 nt
ds_read_b64_tr_b16 a[148:149], v7 offset:0
global_load_lds_dwordx4 v1, s[42:43] offset:-3072 nt
ds_read_b64_tr_b16 a[150:151], v7 offset:1536
global_load_lds_dwordx4 v2, s[42:43] offset:-2048 nt
ds_read_b64_tr_b16 a[152:153], v8 offset:0
ds_read_b64_tr_b16 a[154:155], v8 offset:1536
global_load_lds_dwordx4 v3, s[42:43] offset:-1024 nt
ds_read_b64_tr_b16 a[156:157], v6 offset:12288
global_load_lds_dwordx4 v4, s[42:43] offset:0 nt
ds_read_b64_tr_b16 a[158:159], v6 offset:13824
global_load_lds_dwordx4 v5, s[42:43] offset:1024 nt
ds_read_b64_tr_b16 a[160:161], v7 offset:12288
ds_read_b64_tr_b16 a[162:163], v7 offset:13824
s_sub_u32 s42, s42, 811008
ds_read_b64_tr_b16 a[164:165], v8 offset:12288
s_subb_u32 s43, s43, 0
ds_read_b64_tr_b16 a[166:167], v8 offset:13824
s_waitcnt vmcnt(24) lgkmcnt(0)
s_barrier
ds_read_b64_tr_b16 a[168:169], v6 offset:24576
ds_read_b64_tr_b16 a[170:171], v6 offset:26112
ds_read_b64_tr_b16 a[172:173], v7 offset:24576
ds_read_b64_tr_b16 a[174:175], v7 offset:26112
ds_read_b64_tr_b16 a[176:177], v8 offset:24576
ds_read_b64_tr_b16 a[178:179], v8 offset:26112
ds_read_b64_tr_b16 a[180:181], v6 offset:36864
ds_read_b64_tr_b16 a[182:183], v6 offset:38400
ds_read_b64_tr_b16 a[184:185], v7 offset:36864
ds_read_b64_tr_b16 a[186:187], v7 offset:38400
ds_read_b64_tr_b16 a[188:189], v8 offset:36864
ds_read_b64_tr_b16 a[190:191], v8 offset:38400
s_waitcnt vmcnt(18) lgkmcnt(0)
s_barrier
ds_read_b64_tr_b16 a[192:193], v6 offset:49152
ds_read_b64_tr_b16 a[194:195], v6 offset:50688
ds_read_b64_tr_b16 a[196:197], v7 offset:49152
ds_read_b64_tr_b16 a[198:199], v7 offset:50688
ds_read_b64_tr_b16 a[200:201], v8 offset:49152
ds_read_b64_tr_b16 a[202:203], v8 offset:50688
ds_read_b64_tr_b16 a[204:205], v6 offset:61440
ds_read_b64_tr_b16 a[206:207], v6 offset:62976
ds_read_b64_tr_b16 a[208:209], v7 offset:61440
ds_read_b64_tr_b16 a[210:211], v7 offset:62976
ds_read_b64_tr_b16 a[212:213], v8 offset:61440
ds_read_b64_tr_b16 a[214:215], v8 offset:62976
s_waitcnt vmcnt(12) lgkmcnt(0)
s_barrier
ds_read_b64_tr_b16 a[216:217], v136 offset:8192
ds_read_b64_tr_b16 a[218:219], v136 offset:9728
ds_read_b64_tr_b16 a[220:221], v138 offset:8192
ds_read_b64_tr_b16 a[222:223], v138 offset:9728
ds_read_b64_tr_b16 a[224:225], v140 offset:8192
ds_read_b64_tr_b16 a[226:227], v140 offset:9728
ds_read_b64_tr_b16 a[228:229], v136 offset:20480
ds_read_b64_tr_b16 a[230:231], v136 offset:22016
ds_read_b64_tr_b16 a[232:233], v138 offset:20480
ds_read_b64_tr_b16 a[234:235], v138 offset:22016
ds_read_b64_tr_b16 a[236:237], v140 offset:20480
ds_read_b64_tr_b16 a[238:239], v140 offset:22016
s_waitcnt vmcnt(6) lgkmcnt(0)
s_barrier
ds_read_b64_tr_b16 a[240:241], v136 offset:32768
ds_read_b64_tr_b16 a[242:243], v136 offset:34304
ds_read_b64_tr_b16 a[244:245], v138 offset:32768
ds_read_b64_tr_b16 a[246:247], v138 offset:34304
ds_read_b64_tr_b16 a[248:249], v140 offset:32768
ds_read_b64_tr_b16 a[250:251], v140 offset:34304
ds_read_b64_tr_b16 a[252:253], v136 offset:45056
ds_read_b64_tr_b16 a[254:255], v136 offset:46592
ds_read_b64_tr_b16 v[24:25], v138 offset:45056
ds_read_b64_tr_b16 v[26:27], v138 offset:46592
ds_read_b64_tr_b16 v[28:29], v140 offset:45056
ds_read_b64_tr_b16 v[30:31], v140 offset:46592
s_waitcnt vmcnt(0) lgkmcnt(0)
s_barrier
ds_read_b64_tr_b16 v[32:33], v136 offset:57344
ds_read_b64_tr_b16 v[34:35], v136 offset:58880
ds_read_b64_tr_b16 v[36:37], v138 offset:57344
ds_read_b64_tr_b16 v[38:39], v138 offset:58880
ds_read_b64_tr_b16 v[40:41], v140 offset:57344
ds_read_b64_tr_b16 v[42:43], v140 offset:58880
ds_read_b64_tr_b16 v[44:45], v137 offset:4096
ds_read_b64_tr_b16 v[46:47], v137 offset:5632
ds_read_b64_tr_b16 v[48:49], v139 offset:4096
ds_read_b64_tr_b16 v[50:51], v139 offset:5632
ds_read_b64_tr_b16 v[52:53], v141 offset:4096
ds_read_b64_tr_b16 v[54:55], v141 offset:5632
s_waitcnt lgkmcnt(0)
s_barrier
s_and_b32 s75, s36, 3
s_cmp_eq_u32 s75, 1
s_cbranch_scc1 .Ldn_w2
s_cmp_eq_u32 s75, 2
s_cbranch_scc1 .Ldn_w3
s_cmp_eq_u32 s75, 3
s_cbranch_scc1 .Ldn_w4
.Ldn_w1:
v_add_u32 v254, -1, v14
v_add_u32 v255, -1, v14
v_add_lshl_u32 v252, v16, 0, 7
v_bfi_b32 v252, s69, v252, v15
v_add_lshl_u32 v253, v14, 0, 7
v_bfi_b32 v253, s69, v253, v13
s_mov_b32 s76, 0xffff
s_mov_b32 s77, 0xffff
s_add_u32 m0, s45, 24576
s_xor_b32 s45, s45, 0x10000
s_mov_b32 m0, s45
s_barrier
global_load_lds_dwordx4 v9, s[38:39] offset:-2048 sc1
global_load_lds_dwordx4 v9, s[38:39] offset:-1024 sc1
global_load_lds_dwordx4 v9, s[38:39] offset:0 sc1
global_load_lds_dwordx4 v9, s[38:39] offset:1024 sc1
global_load_lds_dwordx4 v9, s[38:39] offset:2048 sc1
global_load_lds_dwordx4 v9, s[38:39] offset:3072 sc1
v_add_u32 v9, 147456, v9
s_mov_b32 s70, 32
s_waitcnt vmcnt(0) lgkmcnt(0)
s_nop 7
s_branch .Ldn_in5
.p2align 6
.Ldn_top6:
s_waitcnt lgkmcnt(5)
v_mfma_f32_16x16x32_bf16 v[56:59], a[0:3], v[80:83], 0
s_waitcnt lgkmcnt(4)
v_mfma_f32_16x16x32_bf16 v[56:59], a[12:15], v[84:87], v[56:59]
s_waitcnt lgkmcnt(3)
v_mfma_f32_16x16x32_bf16 v[56:59], a[24:27], v[88:91], v[56:59]
s_waitcnt lgkmcnt(2)
v_mfma_f32_16x16x32_bf16 v[56:59], a[36:39], v[92:95], v[56:59]
s_waitcnt lgkmcnt(1)
ds_read_b128 v[104:107], v17 offset:6144
v_cvt_pk_bf16_f32 v132, v76, v77
v_mfma_f32_16x16x32_bf16 v[56:59], a[48:51], v[96:99], v[56:59]
s_waitcnt lgkmcnt(1)
v_mfma_f32_16x16x32_bf16 v[56:59], a[60:63], v[100:103], v[56:59]
s_waitcnt vmcnt(6)
s_barrier
v_mfma_f32_16x16x32_bf16 v[60:63], a[4:7], v[80:83], 0
ds_read_b128 v[108:111], v17 offset:7168
v_cvt_pk_bf16_f32 v134, v76, v77
v_mfma_f32_16x16x32_bf16 v[60:63], a[16:19], v[84:87], v[60:63]
global_store_dwordx4 v11, v[128:131], s[38:39] sc1
v_mfma_f32_16x16x32_bf16 v[60:63], a[28:31], v[88:91], v[60:63]
ds_read_b128 v[112:115], v17 offset:8192
v_add_u32 v11, 110592, v11
v_mfma_f32_16x16x32_bf16 v[60:63], a[40:43], v[92:95], v[60:63]
v_cvt_pk_bf16_f32 v133, v78, v79
v_cvt_pk_bf16_f32 v135, v78, v79
v_mfma_f32_16x16x32_bf16 v[60:63], a[52:55], v[96:99], v[60:63]
ds_read_b128 v[116:119], v17 offset:9216
v_mfma_f32_16x16x32_bf16 v[60:63], a[64:67], v[100:103], v[60:63]
v_permlane16_swap_b32 v132, v134
v_mfma_f32_16x16x32_bf16 v[64:67], a[8:11], v[80:83], 0
ds_read_b128 v[120:123], v17 offset:10240
v_mfma_f32_16x16x32_bf16 v[64:67], a[20:23], v[84:87], v[64:67]
v_permlane16_swap_b32 v133, v135
v_mfma_f32_16x16x32_bf16 v[64:67], a[32:35], v[88:91], v[64:67]
ds_read_b128 v[124:127], v17 offset:11264
v_mfma_f32_16x16x32_bf16 v[64:67], a[44:47], v[92:95], v[64:67]
s_mov_b64 exec, s[76:77]
global_store_dwordx4 v12, v[132:135], s[38:39] sc1
s_mov_b64 exec, -1
v_mfma_f32_16x16x32_bf16 v[64:67], a[56:59], v[96:99], v[64:67]
v_add_u32 v12, 110592, v12
v_mfma_f32_16x16x32_bf16 v[64:67], a[68:71], v[100:103], v[64:67]
s_waitcnt lgkmcnt(5)
v_mfma_f32_16x16x32_bf16 v[56:59], a[72:75], v[104:107], v[56:59]
s_waitcnt lgkmcnt(4)
v_mfma_f32_16x16x32_bf16 v[56:59], a[84:87], v[108:111], v[56:59]
s_waitcnt lgkmcnt(3)
v_mfma_f32_16x16x32_bf16 v[56:59], a[96:99], v[112:115], v[56:59]
s_waitcnt lgkmcnt(2)
v_mfma_f32_16x16x32_bf16 v[56:59], a[108:111], v[116:119], v[56:59]
s_waitcnt lgkmcnt(1)
v_mfma_f32_16x16x32_bf16 v[56:59], a[120:123], v[120:123], v[56:59]
s_waitcnt lgkmcnt(0)
ds_read_b128 v[80:83], v17 offset:12288
v_mfma_f32_16x16x32_bf16 v[56:59], a[132:135], v[124:127], v[56:59]
)"
R"(v_mfma_f32_16x16x32_bf16 v[60:63], a[76:79], v[104:107], v[60:63]
ds_read_b128 v[84:87], v17 offset:13312
v_mfma_f32_16x16x32_bf16 v[60:63], a[88:91], v[108:111], v[60:63]
v_mfma_f32_16x16x32_bf16 v[60:63], a[100:103], v[112:115], v[60:63]
ds_read_b128 v[88:91], v17 offset:14336
v_mfma_f32_16x16x32_bf16 v[60:63], a[112:115], v[116:119], v[60:63]
v_mfma_f32_16x16x32_bf16 v[60:63], a[124:127], v[120:123], v[60:63]
ds_read_b128 v[92:95], v17 offset:15360
v_mfma_f32_16x16x32_bf16 v[60:63], a[136:139], v[124:127], v[60:63]
v_mfma_f32_16x16x32_bf16 v[64:67], a[80:83], v[104:107], v[64:67]
ds_read_b128 v[96:99], v17 offset:16384
v_mfma_f32_16x16x32_bf16 v[64:67], a[92:95], v[108:111], v[64:67]
v_mfma_f32_16x16x32_bf16 v[64:67], a[104:107], v[112:115], v[64:67]
ds_read_b128 v[100:103], v17 offset:17408
v_mfma_f32_16x16x32_bf16 v[64:67], a[116:119], v[116:119], v[64:67]
v_mfma_f32_16x16x32_bf16 v[64:67], a[128:131], v[120:123], v[64:67]
v_mfma_f32_16x16x32_bf16 v[64:67], a[140:143], v[124:127], v[64:67]
s_waitcnt lgkmcnt(5)
v_mfma_f32_16x16x32_bf16 v[56:59], a[144:147], v[80:83], v[56:59]
s_waitcnt lgkmcnt(4)
v_mfma_f32_16x16x32_bf16 v[56:59], a[156:159], v[84:87], v[56:59]
s_waitcnt lgkmcnt(3)
v_mfma_f32_16x16x32_bf16 v[56:59], a[168:171], v[88:91], v[56:59]
s_waitcnt lgkmcnt(2)
v_mfma_f32_16x16x32_bf16 v[56:59], a[180:183], v[92:95], v[56:59]
s_waitcnt lgkmcnt(1)
v_mfma_f32_16x16x32_bf16 v[56:59], a[192:195], v[96:99], v[56:59]
s_xor_b32 s45, s45, 0x10000
ds_read_b128 v[104:107], v17 offset:18432
s_waitcnt lgkmcnt(1)
v_mfma_f32_16x16x32_bf16 v[56:59], a[204:207], v[100:103], v[56:59]
s_add_u32 m0, s45, 0
v_mfma_f32_16x16x32_bf16 v[60:63], a[148:151], v[80:83], v[60:63]
ds_read_b128 v[108:111], v17 offset:19456
ds_read_b128 v[112:115], v17 offset:20480
v_mfma_f32_16x16x32_bf16 v[60:63], a[160:163], v[84:87], v[60:63]
v_mfma_f32_16x16x32_bf16 v[60:63], a[172:175], v[88:91], v[60:63]
s_barrier
v_mfma_f32_16x16x32_bf16 v[60:63], a[184:187], v[92:95], v[60:63]
global_load_lds_dwordx4 v9, s[38:39] offset:-2048 sc1
v_mfma_f32_16x16x32_bf16 v[60:63], a[196:199], v[96:99], v[60:63]
ds_read_b128 v[116:119], v17 offset:21504
v_mfma_f32_16x16x32_bf16 v[60:63], a[208:211], v[100:103], v[60:63]
v_mfma_f32_16x16x32_bf16 v[64:67], a[152:155], v[80:83], v[64:67]
ds_read_b128 v[120:123], v17 offset:22528
global_load_lds_dwordx4 v9, s[38:39] offset:-1024 sc1
v_mfma_f32_16x16x32_bf16 v[64:67], a[164:167], v[84:87], v[64:67]
v_mfma_f32_16x16x32_bf16 v[64:67], a[176:179], v[88:91], v[64:67]
ds_read_b128 v[124:127], v17 offset:23552
v_mfma_f32_16x16x32_bf16 v[64:67], a[188:191], v[92:95], v[64:67]
global_load_lds_dwordx4 v9, s[38:39] offset:0 sc1
v_mfma_f32_16x16x32_bf16 v[64:67], a[200:203], v[96:99], v[64:67]
v_mfma_f32_16x16x32_bf16 v[64:67], a[212:215], v[100:103], v[64:67]
s_waitcnt lgkmcnt(5)
v_mfma_f32_16x16x32_bf16 v[56:59], a[216:219], v[104:107], v[56:59]
s_waitcnt lgkmcnt(4)
global_load_lds_dwordx4 v9, s[38:39] offset:1024 sc1
v_mfma_f32_16x16x32_bf16 v[56:59], a[228:231], v[108:111], v[56:59]
s_waitcnt lgkmcnt(3)
v_mfma_f32_16x16x32_bf16 v[56:59], a[240:243], v[112:115], v[56:59]
s_waitcnt lgkmcnt(2)
v_mfma_f32_16x16x32_bf16 v[56:59], a[252:255], v[116:119], v[56:59]
s_waitcnt lgkmcnt(1)
global_load_lds_dwordx4 v9, s[38:39] offset:2048 sc1
v_mfma_f32_16x16x32_bf16 v[56:59], v[32:35], v[120:123], v[56:59]
s_waitcnt lgkmcnt(0)
v_mfma_f32_16x16x32_bf16 v[56:59], v[44:47], v[124:127], v[56:59]
v_mfma_f32_16x16x32_bf16 v[60:63], a[220:223], v[104:107], v[60:63]
global_load_lds_dwordx4 v9, s[38:39] offset:3072 sc1
v_add_u32 v9, 147456, v9
v_mfma_f32_16x16x32_bf16 v[60:63], a[232:235], v[108:111], v[60:63]
v_mfma_f32_16x16x32_bf16 v[60:63], a[244:247], v[112:115], v[60:63]
s_waitcnt vmcnt(8)
s_barrier
v_mfma_f32_16x16x32_bf16 v[60:63], v[24:27], v[116:119], v[60:63]
ds_read_b128 v[80:83], v17 offset:24576
v_min_u32 v9, v9, v10
v_mfma_f32_16x16x32_bf16 v[60:63], v[36:39], v[120:123], v[60:63]
ds_read_b128 v[84:87], v17 offset:25600
v_cvt_pk_bf16_f32 v128, v56, v57
v_mfma_f32_16x16x32_bf16 v[60:63], v[48:51], v[124:127], v[60:63]
ds_read_b128 v[88:91], v17 offset:26624
v_cvt_pk_bf16_f32 v129, v58, v59
v_mfma_f32_16x16x32_bf16 v[64:67], a[224:227], v[104:107], v[64:67]
ds_read_b128 v[92:95], v17 offset:27648
v_mfma_f32_16x16x32_bf16 v[64:67], a[236:239], v[108:111], v[64:67]
ds_read_b128 v[96:99], v17 offset:28672
v_mfma_f32_16x16x32_bf16 v[64:67], a[248:251], v[112:115], v[64:67]
ds_read_b128 v[100:103], v17 offset:29696
v_mfma_f32_16x16x32_bf16 v[64:67], v[28:31], v[116:119], v[64:67]
v_cvt_pk_bf16_f32 v130, v60, v61
v_cvt_pk_bf16_f32 v131, v62, v63
v_mfma_f32_16x16x32_bf16 v[64:67], v[40:43], v[120:123], v[64:67]
v_min_u32 v128, -2, v128
v_min_u32 v129, -2, v129
v_mfma_f32_16x16x32_bf16 v[64:67], v[52:55], v[124:127], v[64:67]
s_waitcnt lgkmcnt(5)
v_min_u32 v130, -2, v130
v_min_u32 v131, -2, v131
v_mfma_f32_16x16x32_bf16 v[68:71], a[0:3], v[80:83], 0
s_waitcnt lgkmcnt(4)
v_mfma_f32_16x16x32_bf16 v[68:71], a[12:15], v[84:87], v[68:71]
s_waitcnt lgkmcnt(3)
v_mfma_f32_16x16x32_bf16 v[68:71], a[24:27], v[88:91], v[68:71]
s_waitcnt lgkmcnt(2)
v_mfma_f32_16x16x32_bf16 v[68:71], a[36:39], v[92:95], v[68:71]
s_waitcnt lgkmcnt(1)
ds_read_b128 v[104:107], v17 offset:30720
v_cvt_pk_bf16_f32 v132, v64, v65
v_mfma_f32_16x16x32_bf16 v[68:71], a[48:51], v[96:99], v[68:71]
s_waitcnt lgkmcnt(1)
v_mfma_f32_16x16x32_bf16 v[68:71], a[60:63], v[100:103], v[68:71]
s_waitcnt vmcnt(6)
s_barrier
v_mfma_f32_16x16x32_bf16 v[72:75], a[4:7], v[80:83], 0
ds_read_b128 v[108:111], v17 offset:31744
v_cvt_pk_bf16_f32 v134, v64, v65
v_mfma_f32_16x16x32_bf16 v[72:75], a[16:19], v[84:87], v[72:75]
global_store_dwordx4 v11, v[128:131], s[38:39] sc1
v_mfma_f32_16x16x32_bf16 v[72:75], a[28:31], v[88:91], v[72:75]
ds_read_b128 v[112:115], v17 offset:32768
v_add_u32 v11, 110592, v11
v_mfma_f32_16x16x32_bf16 v[72:75], a[40:43], v[92:95], v[72:75]
v_cvt_pk_bf16_f32 v133, v66, v67
v_cvt_pk_bf16_f32 v135, v66, v67
v_mfma_f32_16x16x32_bf16 v[72:75], a[52:55], v[96:99], v[72:75]
ds_read_b128 v[116:119], v17 offset:33792
v_mfma_f32_16x16x32_bf16 v[72:75], a[64:67], v[100:103], v[72:75]
v_permlane16_swap_b32 v132, v134
v_mfma_f32_16x16x32_bf16 v[76:79], a[8:11], v[80:83], 0
ds_read_b128 v[120:123], v17 offset:34816
v_mfma_f32_16x16x32_bf16 v[76:79], a[20:23], v[84:87], v[76:79]
v_permlane16_swap_b32 v133, v135
v_mfma_f32_16x16x32_bf16 v[76:79], a[32:35], v[88:91], v[76:79]
ds_read_b128 v[124:127], v17 offset:35840
v_min_u32 v132, -2, v132
v_mfma_f32_16x16x32_bf16 v[76:79], a[44:47], v[92:95], v[76:79]
v_min_u32 v133, -2, v133
v_min_u32 v134, -2, v134
v_mfma_f32_16x16x32_bf16 v[76:79], a[56:59], v[96:99], v[76:79]
v_min_u32 v135, -2, v135
v_mfma_f32_16x16x32_bf16 v[76:79], a[68:71], v[100:103], v[76:79]
s_waitcnt lgkmcnt(5)
s_mov_b64 exec, s[76:77]
global_store_dwordx4 v12, v[132:135], s[38:39] sc1
s_mov_b64 exec, -1
v_mfma_f32_16x16x32_bf16 v[68:71], a[72:75], v[104:107], v[68:71]
s_waitcnt lgkmcnt(4)
v_add_u32 v12, 110592, v12
v_mfma_f32_16x16x32_bf16 v[68:71], a[84:87], v[108:111], v[68:71]
s_waitcnt lgkmcnt(3)
v_mfma_f32_16x16x32_bf16 v[68:71], a[96:99], v[112:115], v[68:71]
s_waitcnt lgkmcnt(2)
v_mfma_f32_16x16x32_bf16 v[68:71], a[108:111], v[116:119], v[68:71]
s_waitcnt lgkmcnt(1)
v_mfma_f32_16x16x32_bf16 v[68:71], a[120:123], v[120:123], v[68:71]
s_waitcnt lgkmcnt(0)
ds_read_b128 v[80:83], v17 offset:36864
v_mfma_f32_16x16x32_bf16 v[68:71], a[132:135], v[124:127], v[68:71]
v_mfma_f32_16x16x32_bf16 v[72:75], a[76:79], v[104:107], v[72:75]
ds_read_b128 v[84:87], v17 offset:37888
v_mfma_f32_16x16x32_bf16 v[72:75], a[88:91], v[108:111], v[72:75]
v_mfma_f32_16x16x32_bf16 v[72:75], a[100:103], v[112:115], v[72:75]
ds_read_b128 v[88:91], v17 offset:38912
v_mfma_f32_16x16x32_bf16 v[72:75], a[112:115], v[116:119], v[72:75]
v_mfma_f32_16x16x32_bf16 v[72:75], a[124:127], v[120:123], v[72:75]
ds_read_b128 v[92:95], v17 offset:39936
v_mfma_f32_16x16x32_bf16 v[72:75], a[136:139], v[124:127], v[72:75]
v_mfma_f32_16x16x32_bf16 v[76:79], a[80:83], v[104:107], v[76:79]
ds_read_b128 v[96:99], v17 offset:40960
v_mfma_f32_16x16x32_bf16 v[76:79], a[92:95], v[108:111], v[76:79]
v_mfma_f32_16x16x32_bf16 v[76:79], a[104:107], v[112:115], v[76:79]
ds_read_b128 v[100:103], v17 offset:41984
v_mfma_f32_16x16x32_bf16 v[76:79], a[116:119], v[116:119], v[76:79]
v_mfma_f32_16x16x32_bf16 v[76:79], a[128:131], v[120:123], v[76:79]
v_mfma_f32_16x16x32_bf16 v[76:79], a[140:143], v[124:127], v[76:79]
s_waitcnt lgkmcnt(5)
v_mfma_f32_16x16x32_bf16 v[68:71], a[144:147], v[80:83], v[68:71]
s_waitcnt lgkmcnt(4)
v_mfma_f32_16x16x32_bf16 v[68:71], a[156:159], v[84:87], v[68:71]
s_waitcnt lgkmcnt(3)
v_mfma_f32_16x16x32_bf16 v[68:71], a[168:171], v[88:91], v[68:71]
.Ldn_in5:
s_waitcnt lgkmcnt(2)
v_mfma_f32_16x16x32_bf16 v[68:71], a[180:183], v[92:95], v[68:71]
s_waitcnt lgkmcnt(1)
v_mfma_f32_16x16x32_bf16 v[68:71], a[192:195], v[96:99], v[68:71]
s_waitcnt lgkmcnt(0)
ds_read_b128 v[104:107], v17 offset:43008
v_mfma_f32_16x16x32_bf16 v[68:71], a[204:207], v[100:103], v[68:71]
s_add_u32 m0, s45, 24576
v_mfma_f32_16x16x32_bf16 v[72:75], a[148:151], v[80:83], v[72:75]
ds_read_b128 v[108:111], v17 offset:44032
ds_read_b128 v[112:115], v17 offset:45056
v_mfma_f32_16x16x32_bf16 v[72:75], a[160:163], v[84:87], v[72:75]
v_mfma_f32_16x16x32_bf16 v[72:75], a[172:175], v[88:91], v[72:75]
s_barrier
v_mfma_f32_16x16x32_bf16 v[72:75], a[184:187], v[92:95], v[72:75]
global_load_lds_dwordx4 v9, s[38:39] offset:-2048 sc1
v_mfma_f32_16x16x32_bf16 v[72:75], a[196:199], v[96:99], v[72:75]
ds_read_b128 v[116:119], v17 offset:46080
v_mfma_f32_16x16x32_bf16 v[72:75], a[208:211], v[100:103], v[72:75]
v_mfma_f32_16x16x32_bf16 v[76:79], a[152:155], v[80:83], v[76:79]
ds_read_b128 v[120:123], v17 offset:47104
global_load_lds_dwordx4 v9, s[38:39] offset:-1024 sc1
v_mfma_f32_16x16x32_bf16 v[76:79], a[164:167], v[84:87], v[76:79]
v_mfma_f32_16x16x32_bf16 v[76:79], a[176:179], v[88:91], v[76:79]
ds_read_b128 v[124:127], v17 offset:48128
v_mfma_f32_16x16x32_bf16 v[76:79], a[188:191], v[92:95], v[76:79]
global_load_lds_dwordx4 v9, s[38:39] offset:0 sc1
v_mfma_f32_16x16x32_bf16 v[76:79], a[200:203], v[96:99], v[76:79]
v_mfma_f32_16x16x32_bf16 v[76:79], a[212:215], v[100:103], v[76:79]
s_waitcnt lgkmcnt(5)
v_mfma_f32_16x16x32_bf16 v[68:71], a[216:219], v[104:107], v[68:71]
s_waitcnt lgkmcnt(4)
global_load_lds_dwordx4 v9, s[38:39] offset:1024 sc1
v_mfma_f32_16x16x32_bf16 v[68:71], a[228:231], v[108:111], v[68:71]
s_waitcnt lgkmcnt(3)
v_mfma_f32_16x16x32_bf16 v[68:71], a[240:243], v[112:115], v[68:71]
s_waitcnt lgkmcnt(2)
v_mfma_f32_16x16x32_bf16 v[68:71], a[252:255], v[116:119], v[68:71]
s_waitcnt lgkmcnt(1)
global_load_lds_dwordx4 v9, s[38:39] offset:2048 sc1
v_mfma_f32_16x16x32_bf16 v[68:71], v[32:35], v[120:123], v[68:71]
s_waitcnt lgkmcnt(0)
v_mfma_f32_16x16x32_bf16 v[68:71], v[44:47], v[124:127], v[68:71]
v_xor_b32 v17, 0x10000, v17
v_mfma_f32_16x16x32_bf16 v[72:75], a[220:223], v[104:107], v[72:75]
global_load_lds_dwordx4 v9, s[38:39] offset:3072 sc1
v_add_u32 v9, 147456, v9
v_mfma_f32_16x16x32_bf16 v[72:75], a[232:235], v[108:111], v[72:75]
v_mfma_f32_16x16x32_bf16 v[72:75], a[244:247], v[112:115], v[72:75]
s_waitcnt vmcnt(8)
s_barrier
v_mfma_f32_16x16x32_bf16 v[72:75], v[24:27], v[116:119], v[72:75]
ds_read_b128 v[80:83], v17 offset:0
v_min_u32 v9, v9, v10
v_mfma_f32_16x16x32_bf16 v[72:75], v[36:39], v[120:123], v[72:75]
ds_read_b128 v[84:87], v17 offset:1024
v_cvt_pk_bf16_f32 v128, v68, v69
v_mfma_f32_16x16x32_bf16 v[72:75], v[48:51], v[124:127], v[72:75]
ds_read_b128 v[88:91], v17 offset:2048
v_cvt_pk_bf16_f32 v129, v70, v71
v_mfma_f32_16x16x32_bf16 v[76:79], a[224:227], v[104:107], v[76:79]
ds_read_b128 v[92:95], v17 offset:3072
v_mfma_f32_16x16x32_bf16 v[76:79], a[236:239], v[108:111], v[76:79]
ds_read_b128 v[96:99], v17 offset:4096
v_mfma_f32_16x16x32_bf16 v[76:79], a[248:251], v[112:115], v[76:79]
ds_read_b128 v[100:103], v17 offset:5120
v_mfma_f32_16x16x32_bf16 v[76:79], v[28:31], v[116:119], v[76:79]
v_cvt_pk_bf16_f32 v130, v72, v73
v_cvt_pk_bf16_f32 v131, v74, v75
v_mfma_f32_16x16x32_bf16 v[76:79], v[40:43], v[120:123], v[76:79]
v_mfma_f32_16x16x32_bf16 v[76:79], v[52:55], v[124:127], v[76:79]
s_sub_u32 s70, s70, 1
s_cbranch_scc0 .Ldn_top6
v_cvt_pk_bf16_f32 v128, v68, v69
v_cvt_pk_bf16_f32 v129, v70, v71
v_cvt_pk_bf16_f32 v130, v72, v73
v_cvt_pk_bf16_f32 v131, v74, v75
v_min_u32 v128, -2, v128
v_min_u32 v129, -2, v129
v_min_u32 v130, -2, v130
v_min_u32 v131, -2, v131
global_store_dwordx4 v11, v[128:131], s[38:39] sc1
v_add_u32 v11, 110592, v11
v_cvt_pk_bf16_f32 v132, v76, v77
v_cvt_pk_bf16_f32 v134, v76, v77
v_cvt_pk_bf16_f32 v133, v78, v79
v_cvt_pk_bf16_f32 v135, v78, v79
v_permlane16_swap_b32 v132, v134
s_nop 0
v_permlane16_swap_b32 v133, v135
v_min_u32 v132, -2, v132
v_min_u32 v133, -2, v133
v_min_u32 v134, -2, v134
v_min_u32 v135, -2, v135
s_mov_b64 exec, s[76:77]
global_store_dwordx4 v12, v[132:135], s[38:39] sc1
s_mov_b64 exec, -1
v_add_u32 v12, 110592, v12
s_endpgm
.Ldn_w2:
v_add_u32 v254, -1, v14
v_add_u32 v255, -1, v14
v_add_lshl_u32 v252, v16, 0, 7
v_bfi_b32 v252, s69, v252, v15
v_add_lshl_u32 v253, v14, 0, 7
v_bfi_b32 v253, s69, v253, v13
s_mov_b32 s76, 0xffff
s_mov_b32 s77, 0xffff
s_add_u32 m0, s45, 24576
s_xor_b32 s45, s45, 0x10000
s_mov_b32 m0, s45
s_barrier
global_load_lds_dwordx4 v9, s[38:39] offset:-2048 sc1
global_load_lds_dwordx4 v9, s[38:39] offset:-1024 sc1
global_load_lds_dwordx4 v9, s[38:39] offset:0 sc1
global_load_lds_dwordx4 v9, s[38:39] offset:1024 sc1
global_load_lds_dwordx4 v9, s[38:39] offset:2048 sc1
global_load_lds_dwordx4 v9, s[38:39] offset:3072 sc1
v_add_u32 v9, 147456, v9
s_mov_b32 s70, 32
s_waitcnt vmcnt(0) lgkmcnt(0)
s_nop 7
s_branch .Ldn_in8
.p2align 6
.Ldn_top9:
s_waitcnt lgkmcnt(5)
v_mfma_f32_16x16x32_bf16 v[56:59], a[0:3], v[80:83], 0
s_waitcnt lgkmcnt(4)
v_mfma_f32_16x16x32_bf16 v[56:59], a[12:15], v[84:87], v[56:59]
s_waitcnt lgkmcnt(3)
v_mfma_f32_16x16x32_bf16 v[56:59], a[24:27], v[88:91], v[56:59]
s_waitcnt lgkmcnt(2)
v_mfma_f32_16x16x32_bf16 v[56:59], a[36:39], v[92:95], v[56:59]
s_waitcnt lgkmcnt(1)
ds_read_b128 v[104:107], v17 offset:6144
v_cvt_pk_bf16_f32 v132, v76, v77
v_mfma_f32_16x16x32_bf16 v[56:59], a[48:51], v[96:99], v[56:59]
s_waitcnt lgkmcnt(1)
v_mfma_f32_16x16x32_bf16 v[56:59], a[60:63], v[100:103], v[56:59]
s_waitcnt vmcnt(6)
s_barrier
v_mfma_f32_16x16x32_bf16 v[60:63], a[4:7], v[80:83], 0
ds_read_b128 v[108:111], v17 offset:7168
v_cvt_pk_bf16_f32 v134, v76, v77
v_mfma_f32_16x16x32_bf16 v[60:63], a[16:19], v[84:87], v[60:63]
v_cvt_pk_bf16_f32 v133, v78, v79
v_cvt_pk_bf16_f32 v135, v78, v79
v_mfma_f32_16x16x32_bf16 v[60:63], a[28:31], v[88:91], v[60:63]
ds_read_b128 v[112:115], v17 offset:8192
v_mfma_f32_16x16x32_bf16 v[60:63], a[40:43], v[92:95], v[60:63]
global_store_dwordx4 v11, v[128:131], s[38:39] sc1
v_mfma_f32_16x16x32_bf16 v[60:63], a[52:55], v[96:99], v[60:63]
ds_read_b128 v[116:119], v17 offset:9216
v_add_u32 v11, 110592, v11
v_mfma_f32_16x16x32_bf16 v[60:63], a[64:67], v[100:103], v[60:63]
v_permlane16_swap_b32 v132, v134
v_mfma_f32_16x16x32_bf16 v[64:67], a[8:11], v[80:83], 0
ds_read_b128 v[120:123], v17 offset:10240
v_mfma_f32_16x16x32_bf16 v[64:67], a[20:23], v[84:87], v[64:67]
v_permlane16_swap_b32 v133, v135
v_mfma_f32_16x16x32_bf16 v[64:67], a[32:35], v[88:91], v[64:67]
ds_read_b128 v[124:127], v17 offset:11264
v_mfma_f32_16x16x32_bf16 v[64:67], a[44:47], v[92:95], v[64:67]
s_mov_b64 exec, s[76:77]
global_store_dwordx4 v12, v[132:135], s[38:39] sc1
s_mov_b64 exec, -1
v_mfma_f32_16x16x32_bf16 v[64:67], a[56:59], v[96:99], v[64:67]
v_add_u32 v12, 110592, v12
v_mfma_f32_16x16x32_bf16 v[64:67], a[68:71], v[100:103], v[64:67]
s_waitcnt lgkmcnt(5)
v_mfma_f32_16x16x32_bf16 v[56:59], a[72:75], v[104:107], v[56:59]
s_waitcnt lgkmcnt(4)
v_mfma_f32_16x16x32_bf16 v[56:59], a[84:87], v[108:111], v[56:59]
s_waitcnt lgkmcnt(3)
v_mfma_f32_16x16x32_bf16 v[56:59], a[96:99], v[112:115], v[56:59]
s_waitcnt lgkmcnt(2)
v_mfma_f32_16x16x32_bf16 v[56:59], a[108:111], v[116:119], v[56:59]
s_waitcnt lgkmcnt(1)
v_mfma_f32_16x16x32_bf16 v[56:59], a[120:123], v[120:123], v[56:59]
s_waitcnt lgkmcnt(0)
ds_read_b128 v[80:83], v17 offset:12288
v_mfma_f32_16x16x32_bf16 v[56:59], a[132:135], v[124:127], v[56:59]
v_mfma_f32_16x16x32_bf16 v[60:63], a[76:79], v[104:107], v[60:63]
ds_read_b128 v[84:87], v17 offset:13312
v_mfma_f32_16x16x32_bf16 v[60:63], a[88:91], v[108:111], v[60:63]
v_mfma_f32_16x16x32_bf16 v[60:63], a[100:103], v[112:115], v[60:63]
ds_read_b128 v[88:91], v17 offset:14336
v_mfma_f32_16x16x32_bf16 v[60:63], a[112:115], v[116:119], v[60:63]
v_mfma_f32_16x16x32_bf16 v[60:63], a[124:127], v[120:123], v[60:63]
ds_read_b128 v[92:95], v17 offset:15360
v_mfma_f32_16x16x32_bf16 v[60:63], a[136:139], v[124:127], v[60:63]
v_mfma_f32_16x16x32_bf16 v[64:67], a[80:83], v[104:107], v[64:67]
ds_read_b128 v[96:99], v17 offset:16384
v_mfma_f32_16x16x32_bf16 v[64:67], a[92:95], v[108:111], v[64:67]
v_mfma_f32_16x16x32_bf16 v[64:67], a[104:107], v[112:115], v[64:67]
ds_read_b128 v[100:103], v17 offset:17408
v_mfma_f32_16x16x32_bf16 v[64:67], a[116:119], v[116:119], v[64:67]
v_mfma_f32_16x16x32_bf16 v[64:67], a[128:131], v[120:123], v[64:67]
v_mfma_f32_16x16x32_bf16 v[64:67], a[140:143], v[124:127], v[64:67]
s_waitcnt lgkmcnt(5)
v_mfma_f32_16x16x32_bf16 v[56:59], a[144:147], v[80:83], v[56:59]
s_waitcnt lgkmcnt(4)
v_mfma_f32_16x16x32_bf16 v[56:59], a[156:159], v[84:87], v[56:59]
s_waitcnt lgkmcnt(3)
v_mfma_f32_16x16x32_bf16 v[56:59], a[168:171], v[88:91], v[56:59]
s_waitcnt lgkmcnt(2)
v_mfma_f32_16x16x32_bf16 v[56:59], a[180:183], v[92:95], v[56:59]
s_waitcnt lgkmcnt(1)
v_mfma_f32_16x16x32_bf16 v[56:59], a[192:195], v[96:99], v[56:59]
s_xor_b32 s45, s45, 0x10000
ds_read_b128 v[104:107], v17 offset:18432
s_waitcnt lgkmcnt(1)
v_mfma_f32_16x16x32_bf16 v[56:59], a[204:207], v[100:103], v[56:59]
s_add_u32 m0, s45, 0
v_mfma_f32_16x16x32_bf16 v[60:63], a[148:151], v[80:83], v[60:63]
ds_read_b128 v[108:111], v17 offset:19456
ds_read_b128 v[112:115], v17 offset:20480
)"
R"(v_mfma_f32_16x16x32_bf16 v[60:63], a[160:163], v[84:87], v[60:63]
v_mfma_f32_16x16x32_bf16 v[60:63], a[172:175], v[88:91], v[60:63]
s_barrier
v_mfma_f32_16x16x32_bf16 v[60:63], a[184:187], v[92:95], v[60:63]
v_mfma_f32_16x16x32_bf16 v[60:63], a[196:199], v[96:99], v[60:63]
ds_read_b128 v[116:119], v17 offset:21504
global_load_lds_dwordx4 v9, s[38:39] offset:-2048 sc1
v_mfma_f32_16x16x32_bf16 v[60:63], a[208:211], v[100:103], v[60:63]
v_mfma_f32_16x16x32_bf16 v[64:67], a[152:155], v[80:83], v[64:67]
ds_read_b128 v[120:123], v17 offset:22528
v_mfma_f32_16x16x32_bf16 v[64:67], a[164:167], v[84:87], v[64:67]
global_load_lds_dwordx4 v9, s[38:39] offset:-1024 sc1
v_mfma_f32_16x16x32_bf16 v[64:67], a[176:179], v[88:91], v[64:67]
ds_read_b128 v[124:127], v17 offset:23552
v_mfma_f32_16x16x32_bf16 v[64:67], a[188:191], v[92:95], v[64:67]
v_mfma_f32_16x16x32_bf16 v[64:67], a[200:203], v[96:99], v[64:67]
global_load_lds_dwordx4 v9, s[38:39] offset:0 sc1
v_mfma_f32_16x16x32_bf16 v[64:67], a[212:215], v[100:103], v[64:67]
s_waitcnt lgkmcnt(5)
v_mfma_f32_16x16x32_bf16 v[56:59], a[216:219], v[104:107], v[56:59]
s_waitcnt lgkmcnt(4)
v_mfma_f32_16x16x32_bf16 v[56:59], a[228:231], v[108:111], v[56:59]
s_waitcnt lgkmcnt(3)
global_load_lds_dwordx4 v9, s[38:39] offset:1024 sc1
v_mfma_f32_16x16x32_bf16 v[56:59], a[240:243], v[112:115], v[56:59]
s_waitcnt lgkmcnt(2)
v_mfma_f32_16x16x32_bf16 v[56:59], a[252:255], v[116:119], v[56:59]
s_waitcnt lgkmcnt(1)
v_mfma_f32_16x16x32_bf16 v[56:59], v[32:35], v[120:123], v[56:59]
s_waitcnt lgkmcnt(0)
global_load_lds_dwordx4 v9, s[38:39] offset:2048 sc1
v_mfma_f32_16x16x32_bf16 v[56:59], v[44:47], v[124:127], v[56:59]
v_mfma_f32_16x16x32_bf16 v[60:63], a[220:223], v[104:107], v[60:63]
v_mfma_f32_16x16x32_bf16 v[60:63], a[232:235], v[108:111], v[60:63]
v_mfma_f32_16x16x32_bf16 v[60:63], a[244:247], v[112:115], v[60:63]
s_waitcnt vmcnt(7)
s_barrier
v_mfma_f32_16x16x32_bf16 v[60:63], v[24:27], v[116:119], v[60:63]
ds_read_b128 v[80:83], v17 offset:24576
global_load_lds_dwordx4 v9, s[38:39] offset:3072 sc1
v_mfma_f32_16x16x32_bf16 v[60:63], v[36:39], v[120:123], v[60:63]
ds_read_b128 v[84:87], v17 offset:25600
v_add_u32 v9, 147456, v9
v_mfma_f32_16x16x32_bf16 v[60:63], v[48:51], v[124:127], v[60:63]
ds_read_b128 v[88:91], v17 offset:26624
v_min_u32 v9, v9, v10
v_mfma_f32_16x16x32_bf16 v[64:67], a[224:227], v[104:107], v[64:67]
ds_read_b128 v[92:95], v17 offset:27648
v_cvt_pk_bf16_f32 v128, v56, v57
v_mfma_f32_16x16x32_bf16 v[64:67], a[236:239], v[108:111], v[64:67]
ds_read_b128 v[96:99], v17 offset:28672
v_cvt_pk_bf16_f32 v129, v58, v59
v_mfma_f32_16x16x32_bf16 v[64:67], a[248:251], v[112:115], v[64:67]
ds_read_b128 v[100:103], v17 offset:29696
v_mfma_f32_16x16x32_bf16 v[64:67], v[28:31], v[116:119], v[64:67]
v_cvt_pk_bf16_f32 v130, v60, v61
v_cvt_pk_bf16_f32 v131, v62, v63
v_mfma_f32_16x16x32_bf16 v[64:67], v[40:43], v[120:123], v[64:67]
v_min_u32 v128, -2, v128
v_min_u32 v129, -2, v129
v_mfma_f32_16x16x32_bf16 v[64:67], v[52:55], v[124:127], v[64:67]
s_waitcnt lgkmcnt(5)
v_min_u32 v130, -2, v130
v_min_u32 v131, -2, v131
v_mfma_f32_16x16x32_bf16 v[68:71], a[0:3], v[80:83], 0
s_waitcnt lgkmcnt(4)
v_mfma_f32_16x16x32_bf16 v[68:71], a[12:15], v[84:87], v[68:71]
s_waitcnt lgkmcnt(3)
v_mfma_f32_16x16x32_bf16 v[68:71], a[24:27], v[88:91], v[68:71]
s_waitcnt lgkmcnt(2)
v_mfma_f32_16x16x32_bf16 v[68:71], a[36:39], v[92:95], v[68:71]
s_waitcnt lgkmcnt(1)
ds_read_b128 v[104:107], v17 offset:30720
v_cvt_pk_bf16_f32 v132, v64, v65
v_mfma_f32_16x16x32_bf16 v[68:71], a[48:51], v[96:99], v[68:71]
s_waitcnt lgkmcnt(1)
v_mfma_f32_16x16x32_bf16 v[68:71], a[60:63], v[100:103], v[68:71]
s_waitcnt vmcnt(6)
s_barrier
v_mfma_f32_16x16x32_bf16 v[72:75], a[4:7], v[80:83], 0
ds_read_b128 v[108:111], v17 offset:31744
v_cvt_pk_bf16_f32 v134, v64, v65
v_mfma_f32_16x16x32_bf16 v[72:75], a[16:19], v[84:87], v[72:75]
v_cvt_pk_bf16_f32 v133, v66, v67
v_cvt_pk_bf16_f32 v135, v66, v67
v_mfma_f32_16x16x32_bf16 v[72:75], a[28:31], v[88:91], v[72:75]
ds_read_b128 v[112:115], v17 offset:32768
v_mfma_f32_16x16x32_bf16 v[72:75], a[40:43], v[92:95], v[72:75]
global_store_dwordx4 v11, v[128:131], s[38:39] sc1
v_mfma_f32_16x16x32_bf16 v[72:75], a[52:55], v[96:99], v[72:75]
ds_read_b128 v[116:119], v17 offset:33792
v_add_u32 v11, 110592, v11
v_mfma_f32_16x16x32_bf16 v[72:75], a[64:67], v[100:103], v[72:75]
v_permlane16_swap_b32 v132, v134
v_mfma_f32_16x16x32_bf16 v[76:79], a[8:11], v[80:83], 0
ds_read_b128 v[120:123], v17 offset:34816
v_mfma_f32_16x16x32_bf16 v[76:79], a[20:23], v[84:87], v[76:79]
v_permlane16_swap_b32 v133, v135
v_mfma_f32_16x16x32_bf16 v[76:79], a[32:35], v[88:91], v[76:79]
ds_read_b128 v[124:127], v17 offset:35840
v_min_u32 v132, -2, v132
v_mfma_f32_16x16x32_bf16 v[76:79], a[44:47], v[92:95], v[76:79]
v_min_u32 v133, -2, v133
v_min_u32 v134, -2, v134
v_mfma_f32_16x16x32_bf16 v[76:79], a[56:59], v[96:99], v[76:79]
v_min_u32 v135, -2, v135
v_mfma_f32_16x16x32_bf16 v[76:79], a[68:71], v[100:103], v[76:79]
s_waitcnt lgkmcnt(5)
s_mov_b64 exec, s[76:77]
global_store_dwordx4 v12, v[132:135], s[38:39] sc1
s_mov_b64 exec, -1
v_mfma_f32_16x16x32_bf16 v[68:71], a[72:75], v[104:107], v[68:71]
s_waitcnt lgkmcnt(4)
v_add_u32 v12, 110592, v12
v_mfma_f32_16x16x32_bf16 v[68:71], a[84:87], v[108:111], v[68:71]
s_waitcnt lgkmcnt(3)
v_mfma_f32_16x16x32_bf16 v[68:71], a[96:99], v[112:115], v[68:71]
s_waitcnt lgkmcnt(2)
v_mfma_f32_16x16x32_bf16 v[68:71], a[108:111], v[116:119], v[68:71]
s_waitcnt lgkmcnt(1)
v_mfma_f32_16x16x32_bf16 v[68:71], a[120:123], v[120:123], v[68:71]
s_waitcnt lgkmcnt(0)
ds_read_b128 v[80:83], v17 offset:36864
v_mfma_f32_16x16x32_bf16 v[68:71], a[132:135], v[124:127], v[68:71]
v_mfma_f32_16x16x32_bf16 v[72:75], a[76:79], v[104:107], v[72:75]
ds_read_b128 v[84:87], v17 offset:37888
v_mfma_f32_16x16x32_bf16 v[72:75], a[88:91], v[108:111], v[72:75]
v_mfma_f32_16x16x32_bf16 v[72:75], a[100:103], v[112:115], v[72:75]
ds_read_b128 v[88:91], v17 offset:38912
v_mfma_f32_16x16x32_bf16 v[72:75], a[112:115], v[116:119], v[72:75]
v_mfma_f32_16x16x32_bf16 v[72:75], a[124:127], v[120:123], v[72:75]
ds_read_b128 v[92:95], v17 offset:39936
v_mfma_f32_16x16x32_bf16 v[72:75], a[136:139], v[124:127], v[72:75]
v_mfma_f32_16x16x32_bf16 v[76:79], a[80:83], v[104:107], v[76:79]
ds_read_b128 v[96:99], v17 offset:40960
v_mfma_f32_16x16x32_bf16 v[76:79], a[92:95], v[108:111], v[76:79]
v_mfma_f32_16x16x32_bf16 v[76:79], a[104:107], v[112:115], v[76:79]
ds_read_b128 v[100:103], v17 offset:41984
v_mfma_f32_16x16x32_bf16 v[76:79], a[116:119], v[116:119], v[76:79]
v_mfma_f32_16x16x32_bf16 v[76:79], a[128:131], v[120:123], v[76:79]
v_mfma_f32_16x16x32_bf16 v[76:79], a[140:143], v[124:127], v[76:79]
s_waitcnt lgkmcnt(5)
v_mfma_f32_16x16x32_bf16 v[68:71], a[144:147], v[80:83], v[68:71]
s_waitcnt lgkmcnt(4)
v_mfma_f32_16x16x32_bf16 v[68:71], a[156:159], v[84:87], v[68:71]
s_waitcnt lgkmcnt(3)
v_mfma_f32_16x16x32_bf16 v[68:71], a[168:171], v[88:91], v[68:71]
.Ldn_in8:
s_waitcnt lgkmcnt(2)
v_mfma_f32_16x16x32_bf16 v[68:71], a[180:183], v[92:95], v[68:71]
s_waitcnt lgkmcnt(1)
v_mfma_f32_16x16x32_bf16 v[68:71], a[192:195], v[96:99], v[68:71]
s_waitcnt lgkmcnt(0)
ds_read_b128 v[104:107], v17 offset:43008
v_mfma_f32_16x16x32_bf16 v[68:71], a[204:207], v[100:103], v[68:71]
s_add_u32 m0, s45, 24576
v_mfma_f32_16x16x32_bf16 v[72:75], a[148:151], v[80:83], v[72:75]
ds_read_b128 v[108:111], v17 offset:44032
ds_read_b128 v[112:115], v17 offset:45056
v_mfma_f32_16x16x32_bf16 v[72:75], a[160:163], v[84:87], v[72:75]
v_mfma_f32_16x16x32_bf16 v[72:75], a[172:175], v[88:91], v[72:75]
s_barrier
v_mfma_f32_16x16x32_bf16 v[72:75], a[184:187], v[92:95], v[72:75]
v_mfma_f32_16x16x32_bf16 v[72:75], a[196:199], v[96:99], v[72:75]
ds_read_b128 v[116:119], v17 offset:46080
global_load_lds_dwordx4 v9, s[38:39] offset:-2048 sc1
v_mfma_f32_16x16x32_bf16 v[72:75], a[208:211], v[100:103], v[72:75]
v_mfma_f32_16x16x32_bf16 v[76:79], a[152:155], v[80:83], v[76:79]
ds_read_b128 v[120:123], v17 offset:47104
v_mfma_f32_16x16x32_bf16 v[76:79], a[164:167], v[84:87], v[76:79]
global_load_lds_dwordx4 v9, s[38:39] offset:-1024 sc1
v_mfma_f32_16x16x32_bf16 v[76:79], a[176:179], v[88:91], v[76:79]
ds_read_b128 v[124:127], v17 offset:48128
v_mfma_f32_16x16x32_bf16 v[76:79], a[188:191], v[92:95], v[76:79]
v_mfma_f32_16x16x32_bf16 v[76:79], a[200:203], v[96:99], v[76:79]
global_load_lds_dwordx4 v9, s[38:39] offset:0 sc1
v_mfma_f32_16x16x32_bf16 v[76:79], a[212:215], v[100:103], v[76:79]
s_waitcnt lgkmcnt(5)
v_mfma_f32_16x16x32_bf16 v[68:71], a[216:219], v[104:107], v[68:71]
s_waitcnt lgkmcnt(4)
v_mfma_f32_16x16x32_bf16 v[68:71], a[228:231], v[108:111], v[68:71]
s_waitcnt lgkmcnt(3)
global_load_lds_dwordx4 v9, s[38:39] offset:1024 sc1
v_mfma_f32_16x16x32_bf16 v[68:71], a[240:243], v[112:115], v[68:71]
s_waitcnt lgkmcnt(2)
v_mfma_f32_16x16x32_bf16 v[68:71], a[252:255], v[116:119], v[68:71]
s_waitcnt lgkmcnt(1)
v_mfma_f32_16x16x32_bf16 v[68:71], v[32:35], v[120:123], v[68:71]
s_waitcnt lgkmcnt(0)
global_load_lds_dwordx4 v9, s[38:39] offset:2048 sc1
v_mfma_f32_16x16x32_bf16 v[68:71], v[44:47], v[124:127], v[68:71]
v_xor_b32 v17, 0x10000, v17
v_mfma_f32_16x16x32_bf16 v[72:75], a[220:223], v[104:107], v[72:75]
v_mfma_f32_16x16x32_bf16 v[72:75], a[232:235], v[108:111], v[72:75]
v_mfma_f32_16x16x32_bf16 v[72:75], a[244:247], v[112:115], v[72:75]
s_waitcnt vmcnt(7)
s_barrier
v_mfma_f32_16x16x32_bf16 v[72:75], v[24:27], v[116:119], v[72:75]
ds_read_b128 v[80:83], v17 offset:0
global_load_lds_dwordx4 v9, s[38:39] offset:3072 sc1
v_mfma_f32_16x16x32_bf16 v[72:75], v[36:39], v[120:123], v[72:75]
ds_read_b128 v[84:87], v17 offset:1024
v_add_u32 v9, 147456, v9
v_mfma_f32_16x16x32_bf16 v[72:75], v[48:51], v[124:127], v[72:75]
ds_read_b128 v[88:91], v17 offset:2048
v_min_u32 v9, v9, v10
v_mfma_f32_16x16x32_bf16 v[76:79], a[224:227], v[104:107], v[76:79]
ds_read_b128 v[92:95], v17 offset:3072
v_cvt_pk_bf16_f32 v128, v68, v69
v_mfma_f32_16x16x32_bf16 v[76:79], a[236:239], v[108:111], v[76:79]
ds_read_b128 v[96:99], v17 offset:4096
v_cvt_pk_bf16_f32 v129, v70, v71
v_mfma_f32_16x16x32_bf16 v[76:79], a[248:251], v[112:115], v[76:79]
ds_read_b128 v[100:103], v17 offset:5120
v_mfma_f32_16x16x32_bf16 v[76:79], v[28:31], v[116:119], v[76:79]
v_cvt_pk_bf16_f32 v130, v72, v73
v_cvt_pk_bf16_f32 v131, v74, v75
v_mfma_f32_16x16x32_bf16 v[76:79], v[40:43], v[120:123], v[76:79]
v_mfma_f32_16x16x32_bf16 v[76:79], v[52:55], v[124:127], v[76:79]
s_sub_u32 s70, s70, 1
s_cbranch_scc0 .Ldn_top9
v_cvt_pk_bf16_f32 v128, v68, v69
v_cvt_pk_bf16_f32 v129, v70, v71
v_cvt_pk_bf16_f32 v130, v72, v73
v_cvt_pk_bf16_f32 v131, v74, v75
v_min_u32 v128, -2, v128
v_min_u32 v129, -2, v129
v_min_u32 v130, -2, v130
v_min_u32 v131, -2, v131
global_store_dwordx4 v11, v[128:131], s[38:39] sc1
v_add_u32 v11, 110592, v11
v_cvt_pk_bf16_f32 v132, v76, v77
v_cvt_pk_bf16_f32 v134, v76, v77
v_cvt_pk_bf16_f32 v133, v78, v79
v_cvt_pk_bf16_f32 v135, v78, v79
v_permlane16_swap_b32 v132, v134
s_nop 0
v_permlane16_swap_b32 v133, v135
v_min_u32 v132, -2, v132
v_min_u32 v133, -2, v133
v_min_u32 v134, -2, v134
v_min_u32 v135, -2, v135
s_mov_b64 exec, s[76:77]
global_store_dwordx4 v12, v[132:135], s[38:39] sc1
s_mov_b64 exec, -1
v_add_u32 v12, 110592, v12
s_endpgm
.Ldn_w3:
v_add_u32 v254, -1, v14
v_add_u32 v255, -1, v14
v_add_lshl_u32 v252, v16, 0, 7
v_bfi_b32 v252, s69, v252, v15
v_add_lshl_u32 v253, v14, 0, 7
v_bfi_b32 v253, s69, v253, v13
s_mov_b32 s76, 0xffff
s_mov_b32 s77, 0xffff
s_add_u32 m0, s45, 24576
s_xor_b32 s45, s45, 0x10000
s_mov_b32 m0, s45
s_barrier
global_load_lds_dwordx4 v9, s[38:39] offset:-2048 sc1
global_load_lds_dwordx4 v9, s[38:39] offset:-1024 sc1
global_load_lds_dwordx4 v9, s[38:39] offset:0 sc1
global_load_lds_dwordx4 v9, s[38:39] offset:1024 sc1
global_load_lds_dwordx4 v9, s[38:39] offset:2048 sc1
global_load_lds_dwordx4 v9, s[38:39] offset:3072 sc1
v_add_u32 v9, 147456, v9
s_mov_b32 s70, 32
s_waitcnt vmcnt(0) lgkmcnt(0)
s_nop 7
s_branch .Ldn_in11
.p2align 6
.Ldn_top12:
s_waitcnt lgkmcnt(5)
v_mfma_f32_16x16x32_bf16 v[56:59], a[0:3], v[80:83], 0
s_waitcnt lgkmcnt(4)
v_mfma_f32_16x16x32_bf16 v[56:59], a[12:15], v[84:87], v[56:59]
s_waitcnt lgkmcnt(3)
v_mfma_f32_16x16x32_bf16 v[56:59], a[24:27], v[88:91], v[56:59]
s_waitcnt lgkmcnt(2)
v_mfma_f32_16x16x32_bf16 v[56:59], a[36:39], v[92:95], v[56:59]
s_waitcnt lgkmcnt(1)
ds_read_b128 v[104:107], v17 offset:6144
v_cvt_pk_bf16_f32 v132, v76, v77
v_mfma_f32_16x16x32_bf16 v[56:59], a[48:51], v[96:99], v[56:59]
s_waitcnt lgkmcnt(1)
v_mfma_f32_16x16x32_bf16 v[56:59], a[60:63], v[100:103], v[56:59]
s_waitcnt vmcnt(6)
s_barrier
v_mfma_f32_16x16x32_bf16 v[60:63], a[4:7], v[80:83], 0
ds_read_b128 v[108:111], v17 offset:7168
s_mov_b64 exec, 1
global_store_dword v252, v16, s[38:39] sc1
s_mov_b64 exec, -1
v_mfma_f32_16x16x32_bf16 v[60:63], a[16:19], v[84:87], v[60:63]
v_add_u32 v16, 1, v16
v_add_lshl_u32 v252, v16, 0, 7
v_bfi_b32 v252, s69, v252, v15
v_mfma_f32_16x16x32_bf16 v[60:63], a[28:31], v[88:91], v[60:63]
ds_read_b128 v[112:115], v17 offset:8192
v_cvt_pk_bf16_f32 v134, v76, v77
v_mfma_f32_16x16x32_bf16 v[60:63], a[40:43], v[92:95], v[60:63]
global_store_dwordx4 v11, v[128:131], s[38:39] sc1
v_mfma_f32_16x16x32_bf16 v[60:63], a[52:55], v[96:99], v[60:63]
ds_read_b128 v[116:119], v17 offset:9216
v_add_u32 v11, 110592, v11
v_mfma_f32_16x16x32_bf16 v[60:63], a[64:67], v[100:103], v[60:63]
v_cvt_pk_bf16_f32 v133, v78, v79
v_cvt_pk_bf16_f32 v135, v78, v79
v_mfma_f32_16x16x32_bf16 v[64:67], a[8:11], v[80:83], 0
ds_read_b128 v[120:123], v17 offset:10240
v_mfma_f32_16x16x32_bf16 v[64:67], a[20:23], v[84:87], v[64:67]
v_permlane16_swap_b32 v132, v134
v_mfma_f32_16x16x32_bf16 v[64:67], a[32:35], v[88:91], v[64:67]
ds_read_b128 v[124:127], v17 offset:11264
v_mfma_f32_16x16x32_bf16 v[64:67], a[44:47], v[92:95], v[64:67]
v_permlane16_swap_b32 v133, v135
v_mfma_f32_16x16x32_bf16 v[64:67], a[56:59], v[96:99], v[64:67]
s_mov_b64 exec, s[76:77]
global_store_dwordx4 v12, v[132:135], s[38:39] sc1
s_mov_b64 exec, -1
v_mfma_f32_16x16x32_bf16 v[64:67], a[68:71], v[100:103], v[64:67]
s_waitcnt lgkmcnt(5)
v_add_u32 v12, 110592, v12
v_mfma_f32_16x16x32_bf16 v[56:59], a[72:75], v[104:107], v[56:59]
s_waitcnt lgkmcnt(4)
v_mfma_f32_16x16x32_bf16 v[56:59], a[84:87], v[108:111], v[56:59]
s_waitcnt lgkmcnt(3)
v_mfma_f32_16x16x32_bf16 v[56:59], a[96:99], v[112:115], v[56:59]
s_waitcnt lgkmcnt(2)
v_mfma_f32_16x16x32_bf16 v[56:59], a[108:111], v[116:119], v[56:59]
s_waitcnt lgkmcnt(1)
v_mfma_f32_16x16x32_bf16 v[56:59], a[120:123], v[120:123], v[56:59]
s_waitcnt lgkmcnt(0)
ds_read_b128 v[80:83], v17 offset:12288
v_mfma_f32_16x16x32_bf16 v[56:59], a[132:135], v[124:127], v[56:59]
v_mfma_f32_16x16x32_bf16 v[60:63], a[76:79], v[104:107], v[60:63]
ds_read_b128 v[84:87], v17 offset:13312
v_mfma_f32_16x16x32_bf16 v[60:63], a[88:91], v[108:111], v[60:63]
v_mfma_f32_16x16x32_bf16 v[60:63], a[100:103], v[112:115], v[60:63]
ds_read_b128 v[88:91], v17 offset:14336
v_mfma_f32_16x16x32_bf16 v[60:63], a[112:115], v[116:119], v[60:63]
v_mfma_f32_16x16x32_bf16 v[60:63], a[124:127], v[120:123], v[60:63]
ds_read_b128 v[92:95], v17 offset:15360
v_mfma_f32_16x16x32_bf16 v[60:63], a[136:139], v[124:127], v[60:63]
v_mfma_f32_16x16x32_bf16 v[64:67], a[80:83], v[104:107], v[64:67]
ds_read_b128 v[96:99], v17 offset:16384
v_mfma_f32_16x16x32_bf16 v[64:67], a[92:95], v[108:111], v[64:67]
v_mfma_f32_16x16x32_bf16 v[64:67], a[104:107], v[112:115], v[64:67]
ds_read_b128 v[100:103], v17 offset:17408
v_mfma_f32_16x16x32_bf16 v[64:67], a[116:119], v[116:119], v[64:67]
v_mfma_f32_16x16x32_bf16 v[64:67], a[128:131], v[120:123], v[64:67]
v_mfma_f32_16x16x32_bf16 v[64:67], a[140:143], v[124:127], v[64:67]
s_waitcnt lgkmcnt(5)
v_mfma_f32_16x16x32_bf16 v[56:59], a[144:147], v[80:83], v[56:59]
s_waitcnt lgkmcnt(4)
v_mfma_f32_16x16x32_bf16 v[56:59], a[156:159], v[84:87], v[56:59]
s_waitcnt lgkmcnt(3)
v_mfma_f32_16x16x32_bf16 v[56:59], a[168:171], v[88:91], v[56:59]
s_waitcnt lgkmcnt(2)
v_mfma_f32_16x16x32_bf16 v[56:59], a[180:183], v[92:95], v[56:59]
s_waitcnt lgkmcnt(1)
v_mfma_f32_16x16x32_bf16 v[56:59], a[192:195], v[96:99], v[56:59]
s_xor_b32 s45, s45, 0x10000
ds_read_b128 v[104:107], v17 offset:18432
s_waitcnt lgkmcnt(1)
v_mfma_f32_16x16x32_bf16 v[56:59], a[204:207], v[100:103], v[56:59]
s_add_u32 m0, s45, 0
v_mfma_f32_16x16x32_bf16 v[60:63], a[148:151], v[80:83], v[60:63]
ds_read_b128 v[108:111], v17 offset:19456
ds_read_b128 v[112:115], v17 offset:20480
v_mfma_f32_16x16x32_bf16 v[60:63], a[160:163], v[84:87], v[60:63]
v_mfma_f32_16x16x32_bf16 v[60:63], a[172:175], v[88:91], v[60:63]
s_barrier
v_mfma_f32_16x16x32_bf16 v[60:63], a[184:187], v[92:95], v[60:63]
v_mfma_f32_16x16x32_bf16 v[60:63], a[196:199], v[96:99], v[60:63]
ds_read_b128 v[116:119], v17 offset:21504
v_mfma_f32_16x16x32_bf16 v[60:63], a[208:211], v[100:103], v[60:63]
global_load_lds_dwordx4 v9, s[38:39] offset:-2048 sc1
v_mfma_f32_16x16x32_bf16 v[64:67], a[152:155], v[80:83], v[64:67]
ds_read_b128 v[120:123], v17 offset:22528
v_mfma_f32_16x16x32_bf16 v[64:67], a[164:167], v[84:87], v[64:67]
v_mfma_f32_16x16x32_bf16 v[64:67], a[176:179], v[88:91], v[64:67]
ds_read_b128 v[124:127], v17 offset:23552
global_load_lds_dwordx4 v9, s[38:39] offset:-1024 sc1
v_mfma_f32_16x16x32_bf16 v[64:67], a[188:191], v[92:95], v[64:67]
v_mfma_f32_16x16x32_bf16 v[64:67], a[200:203], v[96:99], v[64:67]
v_mfma_f32_16x16x32_bf16 v[64:67], a[212:215], v[100:103], v[64:67]
s_waitcnt lgkmcnt(5)
global_load_lds_dwordx4 v9, s[38:39] offset:0 sc1
v_mfma_f32_16x16x32_bf16 v[56:59], a[216:219], v[104:107], v[56:59]
s_waitcnt lgkmcnt(4)
v_mfma_f32_16x16x32_bf16 v[56:59], a[228:231], v[108:111], v[56:59]
s_waitcnt lgkmcnt(3)
v_mfma_f32_16x16x32_bf16 v[56:59], a[240:243], v[112:115], v[56:59]
s_waitcnt lgkmcnt(2)
global_load_lds_dwordx4 v9, s[38:39] offset:1024 sc1
v_mfma_f32_16x16x32_bf16 v[56:59], a[252:255], v[116:119], v[56:59]
s_waitcnt lgkmcnt(1)
v_mfma_f32_16x16x32_bf16 v[56:59], v[32:35], v[120:123], v[56:59]
)"
R"(s_waitcnt lgkmcnt(0)
v_mfma_f32_16x16x32_bf16 v[56:59], v[44:47], v[124:127], v[56:59]
global_load_lds_dwordx4 v9, s[38:39] offset:2048 sc1
v_mfma_f32_16x16x32_bf16 v[60:63], a[220:223], v[104:107], v[60:63]
v_mfma_f32_16x16x32_bf16 v[60:63], a[232:235], v[108:111], v[60:63]
v_mfma_f32_16x16x32_bf16 v[60:63], a[244:247], v[112:115], v[60:63]
s_waitcnt vmcnt(8)
s_barrier
v_mfma_f32_16x16x32_bf16 v[60:63], v[24:27], v[116:119], v[60:63]
ds_read_b128 v[80:83], v17 offset:24576
global_load_lds_dwordx4 v9, s[38:39] offset:3072 sc1
v_mfma_f32_16x16x32_bf16 v[60:63], v[36:39], v[120:123], v[60:63]
ds_read_b128 v[84:87], v17 offset:25600
v_add_u32 v9, 147456, v9
v_mfma_f32_16x16x32_bf16 v[60:63], v[48:51], v[124:127], v[60:63]
ds_read_b128 v[88:91], v17 offset:26624
v_min_u32 v9, v9, v10
v_mfma_f32_16x16x32_bf16 v[64:67], a[224:227], v[104:107], v[64:67]
ds_read_b128 v[92:95], v17 offset:27648
v_cvt_pk_bf16_f32 v128, v56, v57
v_mfma_f32_16x16x32_bf16 v[64:67], a[236:239], v[108:111], v[64:67]
ds_read_b128 v[96:99], v17 offset:28672
v_cvt_pk_bf16_f32 v129, v58, v59
v_mfma_f32_16x16x32_bf16 v[64:67], a[248:251], v[112:115], v[64:67]
ds_read_b128 v[100:103], v17 offset:29696
v_mfma_f32_16x16x32_bf16 v[64:67], v[28:31], v[116:119], v[64:67]
v_cvt_pk_bf16_f32 v130, v60, v61
v_cvt_pk_bf16_f32 v131, v62, v63
v_mfma_f32_16x16x32_bf16 v[64:67], v[40:43], v[120:123], v[64:67]
v_min_u32 v128, -2, v128
v_min_u32 v129, -2, v129
v_mfma_f32_16x16x32_bf16 v[64:67], v[52:55], v[124:127], v[64:67]
s_waitcnt lgkmcnt(5)
v_min_u32 v130, -2, v130
v_min_u32 v131, -2, v131
v_mfma_f32_16x16x32_bf16 v[68:71], a[0:3], v[80:83], 0
s_waitcnt lgkmcnt(4)
v_mfma_f32_16x16x32_bf16 v[68:71], a[12:15], v[84:87], v[68:71]
s_waitcnt lgkmcnt(3)
v_mfma_f32_16x16x32_bf16 v[68:71], a[24:27], v[88:91], v[68:71]
s_waitcnt lgkmcnt(2)
v_mfma_f32_16x16x32_bf16 v[68:71], a[36:39], v[92:95], v[68:71]
s_waitcnt lgkmcnt(1)
ds_read_b128 v[104:107], v17 offset:30720
v_cvt_pk_bf16_f32 v132, v64, v65
v_mfma_f32_16x16x32_bf16 v[68:71], a[48:51], v[96:99], v[68:71]
s_waitcnt lgkmcnt(1)
v_mfma_f32_16x16x32_bf16 v[68:71], a[60:63], v[100:103], v[68:71]
s_waitcnt vmcnt(6)
s_barrier
v_mfma_f32_16x16x32_bf16 v[72:75], a[4:7], v[80:83], 0
ds_read_b128 v[108:111], v17 offset:31744
v_cvt_pk_bf16_f32 v134, v64, v65
v_mfma_f32_16x16x32_bf16 v[72:75], a[16:19], v[84:87], v[72:75]
v_cvt_pk_bf16_f32 v133, v66, v67
v_cvt_pk_bf16_f32 v135, v66, v67
v_mfma_f32_16x16x32_bf16 v[72:75], a[28:31], v[88:91], v[72:75]
ds_read_b128 v[112:115], v17 offset:32768
s_mov_b64 exec, 1
global_store_dword v252, v16, s[38:39] sc1
s_mov_b64 exec, -1
v_mfma_f32_16x16x32_bf16 v[72:75], a[40:43], v[92:95], v[72:75]
global_store_dwordx4 v11, v[128:131], s[38:39] sc1
v_add_u32 v16, 1, v16
v_mfma_f32_16x16x32_bf16 v[72:75], a[52:55], v[96:99], v[72:75]
ds_read_b128 v[116:119], v17 offset:33792
v_add_u32 v11, 110592, v11
v_add_lshl_u32 v252, v16, 0, 7
v_mfma_f32_16x16x32_bf16 v[72:75], a[64:67], v[100:103], v[72:75]
v_permlane16_swap_b32 v132, v134
v_bfi_b32 v252, s69, v252, v15
v_mfma_f32_16x16x32_bf16 v[76:79], a[8:11], v[80:83], 0
ds_read_b128 v[120:123], v17 offset:34816
v_mfma_f32_16x16x32_bf16 v[76:79], a[20:23], v[84:87], v[76:79]
v_permlane16_swap_b32 v133, v135
v_mfma_f32_16x16x32_bf16 v[76:79], a[32:35], v[88:91], v[76:79]
ds_read_b128 v[124:127], v17 offset:35840
v_min_u32 v132, -2, v132
v_mfma_f32_16x16x32_bf16 v[76:79], a[44:47], v[92:95], v[76:79]
v_min_u32 v133, -2, v133
v_min_u32 v134, -2, v134
v_mfma_f32_16x16x32_bf16 v[76:79], a[56:59], v[96:99], v[76:79]
v_min_u32 v135, -2, v135
v_mfma_f32_16x16x32_bf16 v[76:79], a[68:71], v[100:103], v[76:79]
s_waitcnt lgkmcnt(5)
s_mov_b64 exec, s[76:77]
global_store_dwordx4 v12, v[132:135], s[38:39] sc1
s_mov_b64 exec, -1
v_mfma_f32_16x16x32_bf16 v[68:71], a[72:75], v[104:107], v[68:71]
s_waitcnt lgkmcnt(4)
v_add_u32 v12, 110592, v12
v_mfma_f32_16x16x32_bf16 v[68:71], a[84:87], v[108:111], v[68:71]
s_waitcnt lgkmcnt(3)
v_mfma_f32_16x16x32_bf16 v[68:71], a[96:99], v[112:115], v[68:71]
s_waitcnt lgkmcnt(2)
v_mfma_f32_16x16x32_bf16 v[68:71], a[108:111], v[116:119], v[68:71]
s_waitcnt lgkmcnt(1)
v_mfma_f32_16x16x32_bf16 v[68:71], a[120:123], v[120:123], v[68:71]
s_waitcnt lgkmcnt(0)
ds_read_b128 v[80:83], v17 offset:36864
v_mfma_f32_16x16x32_bf16 v[68:71], a[132:135], v[124:127], v[68:71]
v_mfma_f32_16x16x32_bf16 v[72:75], a[76:79], v[104:107], v[72:75]
ds_read_b128 v[84:87], v17 offset:37888
v_mfma_f32_16x16x32_bf16 v[72:75], a[88:91], v[108:111], v[72:75]
v_mfma_f32_16x16x32_bf16 v[72:75], a[100:103], v[112:115], v[72:75]
ds_read_b128 v[88:91], v17 offset:38912
v_mfma_f32_16x16x32_bf16 v[72:75], a[112:115], v[116:119], v[72:75]
v_mfma_f32_16x16x32_bf16 v[72:75], a[124:127], v[120:123], v[72:75]
ds_read_b128 v[92:95], v17 offset:39936
v_mfma_f32_16x16x32_bf16 v[72:75], a[136:139], v[124:127], v[72:75]
v_mfma_f32_16x16x32_bf16 v[76:79], a[80:83], v[104:107], v[76:79]
ds_read_b128 v[96:99], v17 offset:40960
v_mfma_f32_16x16x32_bf16 v[76:79], a[92:95], v[108:111], v[76:79]
v_mfma_f32_16x16x32_bf16 v[76:79], a[104:107], v[112:115], v[76:79]
ds_read_b128 v[100:103], v17 offset:41984
v_mfma_f32_16x16x32_bf16 v[76:79], a[116:119], v[116:119], v[76:79]
v_mfma_f32_16x16x32_bf16 v[76:79], a[128:131], v[120:123], v[76:79]
v_mfma_f32_16x16x32_bf16 v[76:79], a[140:143], v[124:127], v[76:79]
s_waitcnt lgkmcnt(5)
v_mfma_f32_16x16x32_bf16 v[68:71], a[144:147], v[80:83], v[68:71]
s_waitcnt lgkmcnt(4)
v_mfma_f32_16x16x32_bf16 v[68:71], a[156:159], v[84:87], v[68:71]
s_waitcnt lgkmcnt(3)
v_mfma_f32_16x16x32_bf16 v[68:71], a[168:171], v[88:91], v[68:71]
.Ldn_in11:
s_waitcnt lgkmcnt(2)
v_mfma_f32_16x16x32_bf16 v[68:71], a[180:183], v[92:95], v[68:71]
s_waitcnt lgkmcnt(1)
v_mfma_f32_16x16x32_bf16 v[68:71], a[192:195], v[96:99], v[68:71]
s_waitcnt lgkmcnt(0)
ds_read_b128 v[104:107], v17 offset:43008
v_mfma_f32_16x16x32_bf16 v[68:71], a[204:207], v[100:103], v[68:71]
s_add_u32 m0, s45, 24576
v_mfma_f32_16x16x32_bf16 v[72:75], a[148:151], v[80:83], v[72:75]
ds_read_b128 v[108:111], v17 offset:44032
ds_read_b128 v[112:115], v17 offset:45056
v_mfma_f32_16x16x32_bf16 v[72:75], a[160:163], v[84:87], v[72:75]
v_mfma_f32_16x16x32_bf16 v[72:75], a[172:175], v[88:91], v[72:75]
s_barrier
v_mfma_f32_16x16x32_bf16 v[72:75], a[184:187], v[92:95], v[72:75]
v_mfma_f32_16x16x32_bf16 v[72:75], a[196:199], v[96:99], v[72:75]
ds_read_b128 v[116:119], v17 offset:46080
v_mfma_f32_16x16x32_bf16 v[72:75], a[208:211], v[100:103], v[72:75]
global_load_lds_dwordx4 v9, s[38:39] offset:-2048 sc1
v_mfma_f32_16x16x32_bf16 v[76:79], a[152:155], v[80:83], v[76:79]
ds_read_b128 v[120:123], v17 offset:47104
v_mfma_f32_16x16x32_bf16 v[76:79], a[164:167], v[84:87], v[76:79]
v_mfma_f32_16x16x32_bf16 v[76:79], a[176:179], v[88:91], v[76:79]
ds_read_b128 v[124:127], v17 offset:48128
global_load_lds_dwordx4 v9, s[38:39] offset:-1024 sc1
v_mfma_f32_16x16x32_bf16 v[76:79], a[188:191], v[92:95], v[76:79]
v_mfma_f32_16x16x32_bf16 v[76:79], a[200:203], v[96:99], v[76:79]
v_mfma_f32_16x16x32_bf16 v[76:79], a[212:215], v[100:103], v[76:79]
s_waitcnt lgkmcnt(5)
global_load_lds_dwordx4 v9, s[38:39] offset:0 sc1
v_mfma_f32_16x16x32_bf16 v[68:71], a[216:219], v[104:107], v[68:71]
s_waitcnt lgkmcnt(4)
v_mfma_f32_16x16x32_bf16 v[68:71], a[228:231], v[108:111], v[68:71]
s_waitcnt lgkmcnt(3)
v_mfma_f32_16x16x32_bf16 v[68:71], a[240:243], v[112:115], v[68:71]
s_waitcnt lgkmcnt(2)
global_load_lds_dwordx4 v9, s[38:39] offset:1024 sc1
v_mfma_f32_16x16x32_bf16 v[68:71], a[252:255], v[116:119], v[68:71]
s_waitcnt lgkmcnt(1)
v_mfma_f32_16x16x32_bf16 v[68:71], v[32:35], v[120:123], v[68:71]
s_waitcnt lgkmcnt(0)
v_mfma_f32_16x16x32_bf16 v[68:71], v[44:47], v[124:127], v[68:71]
v_xor_b32 v17, 0x10000, v17
global_load_lds_dwordx4 v9, s[38:39] offset:2048 sc1
v_mfma_f32_16x16x32_bf16 v[72:75], a[220:223], v[104:107], v[72:75]
v_mfma_f32_16x16x32_bf16 v[72:75], a[232:235], v[108:111], v[72:75]
v_mfma_f32_16x16x32_bf16 v[72:75], a[244:247], v[112:115], v[72:75]
s_waitcnt vmcnt(8)
s_barrier
v_mfma_f32_16x16x32_bf16 v[72:75], v[24:27], v[116:119], v[72:75]
ds_read_b128 v[80:83], v17 offset:0
global_load_lds_dwordx4 v9, s[38:39] offset:3072 sc1
v_mfma_f32_16x16x32_bf16 v[72:75], v[36:39], v[120:123], v[72:75]
ds_read_b128 v[84:87], v17 offset:1024
v_add_u32 v9, 147456, v9
v_mfma_f32_16x16x32_bf16 v[72:75], v[48:51], v[124:127], v[72:75]
ds_read_b128 v[88:91], v17 offset:2048
v_min_u32 v9, v9, v10
v_mfma_f32_16x16x32_bf16 v[76:79], a[224:227], v[104:107], v[76:79]
ds_read_b128 v[92:95], v17 offset:3072
v_cvt_pk_bf16_f32 v128, v68, v69
v_mfma_f32_16x16x32_bf16 v[76:79], a[236:239], v[108:111], v[76:79]
ds_read_b128 v[96:99], v17 offset:4096
v_cvt_pk_bf16_f32 v129, v70, v71
v_mfma_f32_16x16x32_bf16 v[76:79], a[248:251], v[112:115], v[76:79]
ds_read_b128 v[100:103], v17 offset:5120
v_mfma_f32_16x16x32_bf16 v[76:79], v[28:31], v[116:119], v[76:79]
v_cvt_pk_bf16_f32 v130, v72, v73
v_cvt_pk_bf16_f32 v131, v74, v75
v_mfma_f32_16x16x32_bf16 v[76:79], v[40:43], v[120:123], v[76:79]
v_mfma_f32_16x16x32_bf16 v[76:79], v[52:55], v[124:127], v[76:79]
s_sub_u32 s70, s70, 1
s_cbranch_scc0 .Ldn_top12
v_cvt_pk_bf16_f32 v128, v68, v69
v_cvt_pk_bf16_f32 v129, v70, v71
v_cvt_pk_bf16_f32 v130, v72, v73
v_cvt_pk_bf16_f32 v131, v74, v75
v_min_u32 v128, -2, v128
v_min_u32 v129, -2, v129
v_min_u32 v130, -2, v130
v_min_u32 v131, -2, v131
global_store_dwordx4 v11, v[128:131], s[38:39] sc1
v_add_u32 v11, 110592, v11
v_cvt_pk_bf16_f32 v132, v76, v77
v_cvt_pk_bf16_f32 v134, v76, v77
v_cvt_pk_bf16_f32 v133, v78, v79
v_cvt_pk_bf16_f32 v135, v78, v79
v_permlane16_swap_b32 v132, v134
s_nop 0
v_permlane16_swap_b32 v133, v135
v_min_u32 v132, -2, v132
v_min_u32 v133, -2, v133
v_min_u32 v134, -2, v134
v_min_u32 v135, -2, v135
s_mov_b64 exec, s[76:77]
global_store_dwordx4 v12, v[132:135], s[38:39] sc1
s_mov_b64 exec, -1
v_add_u32 v12, 110592, v12
s_endpgm
.Ldn_w4:
v_add_u32 v254, -1, v14
v_add_u32 v255, -1, v14
v_add_lshl_u32 v252, v16, 0, 7
v_bfi_b32 v252, s69, v252, v15
v_add_lshl_u32 v253, v14, 0, 7
v_bfi_b32 v253, s69, v253, v13
s_mov_b32 s76, 0xffff
s_mov_b32 s77, 0xffff
s_add_u32 m0, s45, 24576
v_add_lshl_u32 v253, v14, 0, 7
v_bfi_b32 v253, s69, v253, v13
global_load_dword v254, v253, s[38:39] sc1
s_waitcnt vmcnt(0)
v_sub_u32 v254, v254, v14
v_cmp_lt_u32 vcc, 0x7f, v254
s_cbranch_vccnz .Ldn_sp19
.Ldn_rt20:
v_add_u32 v14, 1, v14
s_xor_b32 s45, s45, 0x10000
s_mov_b32 m0, s45
s_barrier
global_load_lds_dwordx4 v9, s[38:39] offset:-2048 sc1
global_load_lds_dwordx4 v9, s[38:39] offset:-1024 sc1
global_load_lds_dwordx4 v9, s[38:39] offset:0 sc1
global_load_lds_dwordx4 v9, s[38:39] offset:1024 sc1
global_load_lds_dwordx4 v9, s[38:39] offset:2048 sc1
global_load_lds_dwordx4 v9, s[38:39] offset:3072 sc1
v_add_u32 v9, 147456, v9
s_mov_b32 s70, 32
s_waitcnt vmcnt(0) lgkmcnt(0)
s_nop 7
s_branch .Ldn_in18
.p2align 6
.Ldn_top21:
s_waitcnt lgkmcnt(5)
v_mfma_f32_16x16x32_bf16 v[56:59], a[0:3], v[80:83], 0
s_waitcnt lgkmcnt(4)
v_mfma_f32_16x16x32_bf16 v[56:59], a[12:15], v[84:87], v[56:59]
s_waitcnt lgkmcnt(3)
global_load_dword v254, v253, s[38:39] sc1
v_mfma_f32_16x16x32_bf16 v[56:59], a[24:27], v[88:91], v[56:59]
s_waitcnt lgkmcnt(2)
v_mfma_f32_16x16x32_bf16 v[56:59], a[36:39], v[92:95], v[56:59]
s_waitcnt lgkmcnt(1)
ds_read_b128 v[104:107], v17 offset:6144
v_cvt_pk_bf16_f32 v132, v76, v77
v_mfma_f32_16x16x32_bf16 v[56:59], a[48:51], v[96:99], v[56:59]
s_waitcnt lgkmcnt(1)
v_mfma_f32_16x16x32_bf16 v[56:59], a[60:63], v[100:103], v[56:59]
s_waitcnt vmcnt(7)
s_barrier
v_mfma_f32_16x16x32_bf16 v[60:63], a[4:7], v[80:83], 0
ds_read_b128 v[108:111], v17 offset:7168
v_cvt_pk_bf16_f32 v134, v76, v77
v_mfma_f32_16x16x32_bf16 v[60:63], a[16:19], v[84:87], v[60:63]
v_cvt_pk_bf16_f32 v133, v78, v79
v_cvt_pk_bf16_f32 v135, v78, v79
v_mfma_f32_16x16x32_bf16 v[60:63], a[28:31], v[88:91], v[60:63]
ds_read_b128 v[112:115], v17 offset:8192
v_mfma_f32_16x16x32_bf16 v[60:63], a[40:43], v[92:95], v[60:63]
v_permlane16_swap_b32 v132, v134
v_mfma_f32_16x16x32_bf16 v[60:63], a[52:55], v[96:99], v[60:63]
ds_read_b128 v[116:119], v17 offset:9216
v_mfma_f32_16x16x32_bf16 v[60:63], a[64:67], v[100:103], v[60:63]
global_store_dwordx4 v11, v[128:131], s[38:39] sc1
v_mfma_f32_16x16x32_bf16 v[64:67], a[8:11], v[80:83], 0
ds_read_b128 v[120:123], v17 offset:10240
v_add_u32 v11, 110592, v11
v_mfma_f32_16x16x32_bf16 v[64:67], a[20:23], v[84:87], v[64:67]
v_permlane16_swap_b32 v133, v135
v_mfma_f32_16x16x32_bf16 v[64:67], a[32:35], v[88:91], v[64:67]
ds_read_b128 v[124:127], v17 offset:11264
v_mfma_f32_16x16x32_bf16 v[64:67], a[44:47], v[92:95], v[64:67]
s_mov_b64 exec, s[76:77]
global_store_dwordx4 v12, v[132:135], s[38:39] sc1
s_mov_b64 exec, -1
v_mfma_f32_16x16x32_bf16 v[64:67], a[56:59], v[96:99], v[64:67]
v_add_u32 v12, 110592, v12
v_mfma_f32_16x16x32_bf16 v[64:67], a[68:71], v[100:103], v[64:67]
s_waitcnt lgkmcnt(5)
v_mfma_f32_16x16x32_bf16 v[56:59], a[72:75], v[104:107], v[56:59]
s_waitcnt lgkmcnt(4)
v_mfma_f32_16x16x32_bf16 v[56:59], a[84:87], v[108:111], v[56:59]
s_waitcnt lgkmcnt(3)
v_mfma_f32_16x16x32_bf16 v[56:59], a[96:99], v[112:115], v[56:59]
s_waitcnt lgkmcnt(2)
v_mfma_f32_16x16x32_bf16 v[56:59], a[108:111], v[116:119], v[56:59]
s_waitcnt lgkmcnt(1)
v_mfma_f32_16x16x32_bf16 v[56:59], a[120:123], v[120:123], v[56:59]
s_waitcnt lgkmcnt(0)
ds_read_b128 v[80:83], v17 offset:12288
v_mfma_f32_16x16x32_bf16 v[56:59], a[132:135], v[124:127], v[56:59]
v_mfma_f32_16x16x32_bf16 v[60:63], a[76:79], v[104:107], v[60:63]
ds_read_b128 v[84:87], v17 offset:13312
v_mfma_f32_16x16x32_bf16 v[60:63], a[88:91], v[108:111], v[60:63]
v_mfma_f32_16x16x32_bf16 v[60:63], a[100:103], v[112:115], v[60:63]
ds_read_b128 v[88:91], v17 offset:14336
v_mfma_f32_16x16x32_bf16 v[60:63], a[112:115], v[116:119], v[60:63]
v_mfma_f32_16x16x32_bf16 v[60:63], a[124:127], v[120:123], v[60:63]
ds_read_b128 v[92:95], v17 offset:15360
v_mfma_f32_16x16x32_bf16 v[60:63], a[136:139], v[124:127], v[60:63]
v_mfma_f32_16x16x32_bf16 v[64:67], a[80:83], v[104:107], v[64:67]
ds_read_b128 v[96:99], v17 offset:16384
v_mfma_f32_16x16x32_bf16 v[64:67], a[92:95], v[108:111], v[64:67]
v_mfma_f32_16x16x32_bf16 v[64:67], a[104:107], v[112:115], v[64:67]
ds_read_b128 v[100:103], v17 offset:17408
v_mfma_f32_16x16x32_bf16 v[64:67], a[116:119], v[116:119], v[64:67]
v_mfma_f32_16x16x32_bf16 v[64:67], a[128:131], v[120:123], v[64:67]
v_mfma_f32_16x16x32_bf16 v[64:67], a[140:143], v[124:127], v[64:67]
s_waitcnt lgkmcnt(5)
v_mfma_f32_16x16x32_bf16 v[56:59], a[144:147], v[80:83], v[56:59]
s_waitcnt lgkmcnt(4)
v_mfma_f32_16x16x32_bf16 v[56:59], a[156:159], v[84:87], v[56:59]
s_waitcnt lgkmcnt(3)
v_mfma_f32_16x16x32_bf16 v[56:59], a[168:171], v[88:91], v[56:59]
s_waitcnt lgkmcnt(2)
v_mfma_f32_16x16x32_bf16 v[56:59], a[180:183], v[92:95], v[56:59]
s_waitcnt vmcnt(2)
v_sub_u32 v254, v254, v14
v_cmp_lt_u32 vcc, 0x7f, v254
s_waitcnt lgkmcnt(1)
v_mfma_f32_16x16x32_bf16 v[56:59], a[192:195], v[96:99], v[56:59]
s_xor_b32 s45, s45, 0x10000
ds_read_b128 v[104:107], v17 offset:18432
s_waitcnt lgkmcnt(1)
v_mfma_f32_16x16x32_bf16 v[56:59], a[204:207], v[100:103], v[56:59]
s_add_u32 m0, s45, 0
v_mfma_f32_16x16x32_bf16 v[60:63], a[148:151], v[80:83], v[60:63]
s_cbranch_vccnz .Ldn_sp14
.Ldn_rt15:
ds_read_b128 v[108:111], v17 offset:19456
ds_read_b128 v[112:115], v17 offset:20480
v_mfma_f32_16x16x32_bf16 v[60:63], a[160:163], v[84:87], v[60:63]
v_mfma_f32_16x16x32_bf16 v[60:63], a[172:175], v[88:91], v[60:63]
s_barrier
v_mfma_f32_16x16x32_bf16 v[60:63], a[184:187], v[92:95], v[60:63]
v_add_u32 v14, 1, v14
v_add_lshl_u32 v253, v14, 0, 7
v_mfma_f32_16x16x32_bf16 v[60:63], a[196:199], v[96:99], v[60:63]
ds_read_b128 v[116:119], v17 offset:21504
v_bfi_b32 v253, s69, v253, v13
v_mfma_f32_16x16x32_bf16 v[60:63], a[208:211], v[100:103], v[60:63]
v_mfma_f32_16x16x32_bf16 v[64:67], a[152:155], v[80:83], v[64:67]
ds_read_b128 v[120:123], v17 offset:22528
global_load_lds_dwordx4 v9, s[38:39] offset:-2048 sc1
v_mfma_f32_16x16x32_bf16 v[64:67], a[164:167], v[84:87], v[64:67]
v_mfma_f32_16x16x32_bf16 v[64:67], a[176:179], v[88:91], v[64:67]
ds_read_b128 v[124:127], v17 offset:23552
v_mfma_f32_16x16x32_bf16 v[64:67], a[188:191], v[92:95], v[64:67]
global_load_lds_dwordx4 v9, s[38:39] offset:-1024 sc1
v_mfma_f32_16x16x32_bf16 v[64:67], a[200:203], v[96:99], v[64:67]
v_mfma_f32_16x16x32_bf16 v[64:67], a[212:215], v[100:103], v[64:67]
s_waitcnt lgkmcnt(5)
v_mfma_f32_16x16x32_bf16 v[56:59], a[216:219], v[104:107], v[56:59]
s_waitcnt lgkmcnt(4)
global_load_lds_dwordx4 v9, s[38:39] offset:0 sc1
v_mfma_f32_16x16x32_bf16 v[56:59], a[228:231], v[108:111], v[56:59]
s_waitcnt lgkmcnt(3)
v_mfma_f32_16x16x32_bf16 v[56:59], a[240:243], v[112:115], v[56:59]
s_waitcnt lgkmcnt(2)
v_mfma_f32_16x16x32_bf16 v[56:59], a[252:255], v[116:119], v[56:59]
s_waitcnt lgkmcnt(1)
global_load_lds_dwordx4 v9, s[38:39] offset:1024 sc1
v_mfma_f32_16x16x32_bf16 v[56:59], v[32:35], v[120:123], v[56:59]
s_waitcnt lgkmcnt(0)
v_mfma_f32_16x16x32_bf16 v[56:59], v[44:47], v[124:127], v[56:59]
v_mfma_f32_16x16x32_bf16 v[60:63], a[220:223], v[104:107], v[60:63]
global_load_lds_dwordx4 v9, s[38:39] offset:2048 sc1
v_mfma_f32_16x16x32_bf16 v[60:63], a[232:235], v[108:111], v[60:63]
v_mfma_f32_16x16x32_bf16 v[60:63], a[244:247], v[112:115], v[60:63]
s_barrier
v_mfma_f32_16x16x32_bf16 v[60:63], v[24:27], v[116:119], v[60:63]
ds_read_b128 v[80:83], v17 offset:24576
global_load_lds_dwordx4 v9, s[38:39] offset:3072 sc1
v_mfma_f32_16x16x32_bf16 v[60:63], v[36:39], v[120:123], v[60:63]
)"
R"(ds_read_b128 v[84:87], v17 offset:25600
v_add_u32 v9, 147456, v9
v_mfma_f32_16x16x32_bf16 v[60:63], v[48:51], v[124:127], v[60:63]
ds_read_b128 v[88:91], v17 offset:26624
v_min_u32 v9, v9, v10
v_mfma_f32_16x16x32_bf16 v[64:67], a[224:227], v[104:107], v[64:67]
ds_read_b128 v[92:95], v17 offset:27648
v_cvt_pk_bf16_f32 v128, v56, v57
v_mfma_f32_16x16x32_bf16 v[64:67], a[236:239], v[108:111], v[64:67]
ds_read_b128 v[96:99], v17 offset:28672
v_cvt_pk_bf16_f32 v129, v58, v59
v_mfma_f32_16x16x32_bf16 v[64:67], a[248:251], v[112:115], v[64:67]
ds_read_b128 v[100:103], v17 offset:29696
v_mfma_f32_16x16x32_bf16 v[64:67], v[28:31], v[116:119], v[64:67]
v_cvt_pk_bf16_f32 v130, v60, v61
v_cvt_pk_bf16_f32 v131, v62, v63
v_mfma_f32_16x16x32_bf16 v[64:67], v[40:43], v[120:123], v[64:67]
v_min_u32 v128, -2, v128
v_min_u32 v129, -2, v129
v_mfma_f32_16x16x32_bf16 v[64:67], v[52:55], v[124:127], v[64:67]
s_waitcnt lgkmcnt(5)
v_min_u32 v130, -2, v130
v_min_u32 v131, -2, v131
v_mfma_f32_16x16x32_bf16 v[68:71], a[0:3], v[80:83], 0
s_waitcnt lgkmcnt(4)
v_mfma_f32_16x16x32_bf16 v[68:71], a[12:15], v[84:87], v[68:71]
s_waitcnt lgkmcnt(3)
global_load_dword v255, v253, s[38:39] sc1
v_mfma_f32_16x16x32_bf16 v[68:71], a[24:27], v[88:91], v[68:71]
s_waitcnt lgkmcnt(2)
v_mfma_f32_16x16x32_bf16 v[68:71], a[36:39], v[92:95], v[68:71]
s_waitcnt lgkmcnt(1)
ds_read_b128 v[104:107], v17 offset:30720
v_cvt_pk_bf16_f32 v132, v64, v65
v_mfma_f32_16x16x32_bf16 v[68:71], a[48:51], v[96:99], v[68:71]
s_waitcnt lgkmcnt(1)
v_mfma_f32_16x16x32_bf16 v[68:71], a[60:63], v[100:103], v[68:71]
s_waitcnt vmcnt(7)
s_barrier
v_mfma_f32_16x16x32_bf16 v[72:75], a[4:7], v[80:83], 0
ds_read_b128 v[108:111], v17 offset:31744
v_cvt_pk_bf16_f32 v134, v64, v65
v_mfma_f32_16x16x32_bf16 v[72:75], a[16:19], v[84:87], v[72:75]
v_cvt_pk_bf16_f32 v133, v66, v67
v_cvt_pk_bf16_f32 v135, v66, v67
v_mfma_f32_16x16x32_bf16 v[72:75], a[28:31], v[88:91], v[72:75]
ds_read_b128 v[112:115], v17 offset:32768
v_mfma_f32_16x16x32_bf16 v[72:75], a[40:43], v[92:95], v[72:75]
v_permlane16_swap_b32 v132, v134
v_mfma_f32_16x16x32_bf16 v[72:75], a[52:55], v[96:99], v[72:75]
ds_read_b128 v[116:119], v17 offset:33792
v_mfma_f32_16x16x32_bf16 v[72:75], a[64:67], v[100:103], v[72:75]
global_store_dwordx4 v11, v[128:131], s[38:39] sc1
v_mfma_f32_16x16x32_bf16 v[76:79], a[8:11], v[80:83], 0
ds_read_b128 v[120:123], v17 offset:34816
v_add_u32 v11, 110592, v11
v_mfma_f32_16x16x32_bf16 v[76:79], a[20:23], v[84:87], v[76:79]
v_permlane16_swap_b32 v133, v135
v_mfma_f32_16x16x32_bf16 v[76:79], a[32:35], v[88:91], v[76:79]
ds_read_b128 v[124:127], v17 offset:35840
v_min_u32 v132, -2, v132
v_mfma_f32_16x16x32_bf16 v[76:79], a[44:47], v[92:95], v[76:79]
v_min_u32 v133, -2, v133
v_min_u32 v134, -2, v134
v_mfma_f32_16x16x32_bf16 v[76:79], a[56:59], v[96:99], v[76:79]
v_min_u32 v135, -2, v135
v_mfma_f32_16x16x32_bf16 v[76:79], a[68:71], v[100:103], v[76:79]
s_waitcnt lgkmcnt(5)
s_mov_b64 exec, s[76:77]
global_store_dwordx4 v12, v[132:135], s[38:39] sc1
s_mov_b64 exec, -1
v_mfma_f32_16x16x32_bf16 v[68:71], a[72:75], v[104:107], v[68:71]
s_waitcnt lgkmcnt(4)
v_add_u32 v12, 110592, v12
v_mfma_f32_16x16x32_bf16 v[68:71], a[84:87], v[108:111], v[68:71]
s_waitcnt lgkmcnt(3)
v_mfma_f32_16x16x32_bf16 v[68:71], a[96:99], v[112:115], v[68:71]
s_waitcnt lgkmcnt(2)
v_mfma_f32_16x16x32_bf16 v[68:71], a[108:111], v[116:119], v[68:71]
s_waitcnt lgkmcnt(1)
v_mfma_f32_16x16x32_bf16 v[68:71], a[120:123], v[120:123], v[68:71]
s_waitcnt lgkmcnt(0)
ds_read_b128 v[80:83], v17 offset:36864
v_mfma_f32_16x16x32_bf16 v[68:71], a[132:135], v[124:127], v[68:71]
v_mfma_f32_16x16x32_bf16 v[72:75], a[76:79], v[104:107], v[72:75]
ds_read_b128 v[84:87], v17 offset:37888
v_mfma_f32_16x16x32_bf16 v[72:75], a[88:91], v[108:111], v[72:75]
v_mfma_f32_16x16x32_bf16 v[72:75], a[100:103], v[112:115], v[72:75]
ds_read_b128 v[88:91], v17 offset:38912
v_mfma_f32_16x16x32_bf16 v[72:75], a[112:115], v[116:119], v[72:75]
v_mfma_f32_16x16x32_bf16 v[72:75], a[124:127], v[120:123], v[72:75]
ds_read_b128 v[92:95], v17 offset:39936
v_mfma_f32_16x16x32_bf16 v[72:75], a[136:139], v[124:127], v[72:75]
v_mfma_f32_16x16x32_bf16 v[76:79], a[80:83], v[104:107], v[76:79]
ds_read_b128 v[96:99], v17 offset:40960
v_mfma_f32_16x16x32_bf16 v[76:79], a[92:95], v[108:111], v[76:79]
v_mfma_f32_16x16x32_bf16 v[76:79], a[104:107], v[112:115], v[76:79]
ds_read_b128 v[100:103], v17 offset:41984
v_mfma_f32_16x16x32_bf16 v[76:79], a[116:119], v[116:119], v[76:79]
v_mfma_f32_16x16x32_bf16 v[76:79], a[128:131], v[120:123], v[76:79]
v_mfma_f32_16x16x32_bf16 v[76:79], a[140:143], v[124:127], v[76:79]
s_waitcnt lgkmcnt(5)
v_mfma_f32_16x16x32_bf16 v[68:71], a[144:147], v[80:83], v[68:71]
s_waitcnt lgkmcnt(4)
v_mfma_f32_16x16x32_bf16 v[68:71], a[156:159], v[84:87], v[68:71]
s_waitcnt lgkmcnt(3)
v_mfma_f32_16x16x32_bf16 v[68:71], a[168:171], v[88:91], v[68:71]
.Ldn_in18:
s_waitcnt lgkmcnt(2)
v_mfma_f32_16x16x32_bf16 v[68:71], a[180:183], v[92:95], v[68:71]
s_waitcnt vmcnt(2)
v_sub_u32 v255, v255, v14
v_cmp_lt_u32 vcc, 0x7f, v255
s_waitcnt lgkmcnt(1)
v_mfma_f32_16x16x32_bf16 v[68:71], a[192:195], v[96:99], v[68:71]
s_waitcnt lgkmcnt(0)
ds_read_b128 v[104:107], v17 offset:43008
v_mfma_f32_16x16x32_bf16 v[68:71], a[204:207], v[100:103], v[68:71]
s_add_u32 m0, s45, 24576
v_mfma_f32_16x16x32_bf16 v[72:75], a[148:151], v[80:83], v[72:75]
s_cbranch_vccnz .Ldn_sp16
.Ldn_rt17:
ds_read_b128 v[108:111], v17 offset:44032
ds_read_b128 v[112:115], v17 offset:45056
v_mfma_f32_16x16x32_bf16 v[72:75], a[160:163], v[84:87], v[72:75]
v_mfma_f32_16x16x32_bf16 v[72:75], a[172:175], v[88:91], v[72:75]
s_barrier
v_mfma_f32_16x16x32_bf16 v[72:75], a[184:187], v[92:95], v[72:75]
v_add_u32 v14, 1, v14
v_add_lshl_u32 v253, v14, 0, 7
v_mfma_f32_16x16x32_bf16 v[72:75], a[196:199], v[96:99], v[72:75]
ds_read_b128 v[116:119], v17 offset:46080
v_bfi_b32 v253, s69, v253, v13
v_mfma_f32_16x16x32_bf16 v[72:75], a[208:211], v[100:103], v[72:75]
v_mfma_f32_16x16x32_bf16 v[76:79], a[152:155], v[80:83], v[76:79]
ds_read_b128 v[120:123], v17 offset:47104
global_load_lds_dwordx4 v9, s[38:39] offset:-2048 sc1
v_mfma_f32_16x16x32_bf16 v[76:79], a[164:167], v[84:87], v[76:79]
v_mfma_f32_16x16x32_bf16 v[76:79], a[176:179], v[88:91], v[76:79]
ds_read_b128 v[124:127], v17 offset:48128
v_mfma_f32_16x16x32_bf16 v[76:79], a[188:191], v[92:95], v[76:79]
global_load_lds_dwordx4 v9, s[38:39] offset:-1024 sc1
v_mfma_f32_16x16x32_bf16 v[76:79], a[200:203], v[96:99], v[76:79]
v_mfma_f32_16x16x32_bf16 v[76:79], a[212:215], v[100:103], v[76:79]
s_waitcnt lgkmcnt(5)
v_mfma_f32_16x16x32_bf16 v[68:71], a[216:219], v[104:107], v[68:71]
s_waitcnt lgkmcnt(4)
global_load_lds_dwordx4 v9, s[38:39] offset:0 sc1
v_mfma_f32_16x16x32_bf16 v[68:71], a[228:231], v[108:111], v[68:71]
s_waitcnt lgkmcnt(3)
v_mfma_f32_16x16x32_bf16 v[68:71], a[240:243], v[112:115], v[68:71]
s_waitcnt lgkmcnt(2)
v_mfma_f32_16x16x32_bf16 v[68:71], a[252:255], v[116:119], v[68:71]
s_waitcnt lgkmcnt(1)
global_load_lds_dwordx4 v9, s[38:39] offset:1024 sc1
v_mfma_f32_16x16x32_bf16 v[68:71], v[32:35], v[120:123], v[68:71]
s_waitcnt lgkmcnt(0)
v_mfma_f32_16x16x32_bf16 v[68:71], v[44:47], v[124:127], v[68:71]
v_xor_b32 v17, 0x10000, v17
v_mfma_f32_16x16x32_bf16 v[72:75], a[220:223], v[104:107], v[72:75]
global_load_lds_dwordx4 v9, s[38:39] offset:2048 sc1
v_mfma_f32_16x16x32_bf16 v[72:75], a[232:235], v[108:111], v[72:75]
v_mfma_f32_16x16x32_bf16 v[72:75], a[244:247], v[112:115], v[72:75]
s_barrier
v_mfma_f32_16x16x32_bf16 v[72:75], v[24:27], v[116:119], v[72:75]
ds_read_b128 v[80:83], v17 offset:0
global_load_lds_dwordx4 v9, s[38:39] offset:3072 sc1
v_mfma_f32_16x16x32_bf16 v[72:75], v[36:39], v[120:123], v[72:75]
ds_read_b128 v[84:87], v17 offset:1024
v_add_u32 v9, 147456, v9
v_mfma_f32_16x16x32_bf16 v[72:75], v[48:51], v[124:127], v[72:75]
ds_read_b128 v[88:91], v17 offset:2048
v_min_u32 v9, v9, v10
v_mfma_f32_16x16x32_bf16 v[76:79], a[224:227], v[104:107], v[76:79]
ds_read_b128 v[92:95], v17 offset:3072
v_cvt_pk_bf16_f32 v128, v68, v69
v_mfma_f32_16x16x32_bf16 v[76:79], a[236:239], v[108:111], v[76:79]
ds_read_b128 v[96:99], v17 offset:4096
v_cvt_pk_bf16_f32 v129, v70, v71
v_mfma_f32_16x16x32_bf16 v[76:79], a[248:251], v[112:115], v[76:79]
ds_read_b128 v[100:103], v17 offset:5120
v_mfma_f32_16x16x32_bf16 v[76:79], v[28:31], v[116:119], v[76:79]
v_cvt_pk_bf16_f32 v130, v72, v73
v_cvt_pk_bf16_f32 v131, v74, v75
v_mfma_f32_16x16x32_bf16 v[76:79], v[40:43], v[120:123], v[76:79]
v_mfma_f32_16x16x32_bf16 v[76:79], v[52:55], v[124:127], v[76:79]
s_sub_u32 s70, s70, 1
s_cbranch_scc0 .Ldn_top21
v_cvt_pk_bf16_f32 v128, v68, v69
v_cvt_pk_bf16_f32 v129, v70, v71
v_cvt_pk_bf16_f32 v130, v72, v73
v_cvt_pk_bf16_f32 v131, v74, v75
v_min_u32 v128, -2, v128
v_min_u32 v129, -2, v129
v_min_u32 v130, -2, v130
v_min_u32 v131, -2, v131
global_store_dwordx4 v11, v[128:131], s[38:39] sc1
v_add_u32 v11, 110592, v11
v_cvt_pk_bf16_f32 v132, v76, v77
v_cvt_pk_bf16_f32 v134, v76, v77
v_cvt_pk_bf16_f32 v133, v78, v79
v_cvt_pk_bf16_f32 v135, v78, v79
v_permlane16_swap_b32 v132, v134
s_nop 0
v_permlane16_swap_b32 v133, v135
v_min_u32 v132, -2, v132
v_min_u32 v133, -2, v133
v_min_u32 v134, -2, v134
v_min_u32 v135, -2, v135
s_mov_b64 exec, s[76:77]
global_store_dwordx4 v12, v[132:135], s[38:39] sc1
s_mov_b64 exec, -1
v_add_u32 v12, 110592, v12
s_endpgm
.Ldn_sp14:
s_sleep 1
v_add_lshl_u32 v253, v14, 0, 7
v_bfi_b32 v253, s69, v253, v13
global_load_dword v254, v253, s[38:39] sc1
s_add_u32 s71, s71, 1
s_waitcnt vmcnt(0)
v_sub_u32 v254, v254, v14
v_cmp_lt_u32 vcc, 0x7f, v254
s_cbranch_vccz .Ldn_rt15
s_cmp_lt_u32 s71, 3000000
s_cbranch_scc1 .Ldn_sp14
s_branch .Ldn_rt15
.Ldn_sp16:
s_sleep 1
v_add_lshl_u32 v253, v14, 0, 7
v_bfi_b32 v253, s69, v253, v13
global_load_dword v255, v253, s[38:39] sc1
s_add_u32 s71, s71, 1
s_waitcnt vmcnt(0)
v_sub_u32 v255, v255, v14
v_cmp_lt_u32 vcc, 0x7f, v255
s_cbranch_vccz .Ldn_rt17
s_cmp_lt_u32 s71, 3000000
s_cbranch_scc1 .Ldn_sp16
s_branch .Ldn_rt17
.Ldn_sp19:
s_sleep 1
v_add_lshl_u32 v253, v14, 0, 7
v_bfi_b32 v253, s69, v253, v13
global_load_dword v254, v253, s[38:39] sc1
s_add_u32 s71, s71, 1
s_waitcnt vmcnt(0)
v_sub_u32 v254, v254, v14
v_cmp_lt_u32 vcc, 0x7f, v254
s_cbranch_vccz .Ldn_rt20
s_cmp_lt_u32 s71, 3000000
s_cbranch_scc1 .Ldn_sp19
s_branch .Ldn_rt20
.Le2:
)"
    :: "{v[0:31]}"(va), "{s[36:51]}"(sa) : "v255", "a255", "s99", "vcc", "memory");
  } else {
    const unsigned gu_i = xh * 36 + (ev ? jj + 4 : jj - 12);
    const unsigned r = lane & 15, g = lane >> 4, q = r >> 2, p = lane & 3;
    const unsigned ii = gu_i, gam = (ii % 72) >> 1, rho = ii / 72 * 2 + (ii & 1), row0 = rho * RP + PAD, pc0 = wave * 6;
    const unsigned bk = gam >> 2, qd = gam & 3, od = (lane >> 3) & 1, un = g * 8 + (lane & 7), jr = qd * 4 + p, jc = (un >> 2) * 8 + od * 4;
    u32x32 va; u32x16 sa, sb;
    va[0] = g * 9216 + ((r ^ (g << 2)) << 4) + 4096 + (((r ^ (g << 2)) >> 3) & 1) * 640;
    va[1] = (8 * g + q) * 256 + (((4 * wave + p) ^ (q << 2)) << 4);
    va[2] = WS_X1 + row0 * X1P + pc0 * 1024 + 2048 + lane * 16;
    va[3] = WS_H + (row0 - 1) * HT + (gam / 6 * 24 + (gam % 6) * 2 + (wave >> 1) * 12 + (wave & 1)) * 1024 + lane * 16;
    va[4] = WS_FL + rho * FR + FX1 + (lane & 1) * FL;
    va[5] = 0u;
    va[6] = WS_FL + rho * FR + FH + gam * FL;
    va[7] = 0u - 2;
    va[8] = lane * 16;
    va[9] = WS_X1 + (row0 + 63) * X1P + pc0 * 1024 + 2048 + lane * 16;
    va[10] = WS_P + row0 * PT + gam * 3072 + od * 512 + un * 16;
    va[11] = WS_X1 + row0 * X1P + bk * 2048 + (un >> 2) * 256 + jr * 16 + od * 8;
    va[12] = WS_X1 + row0 * X1P + XT + jr * 8;
    va[13] = ((rho * 32 + 31) * 16 + qd * 4 + g) * 1152 + bk * 128 + r * 8;
    va[14] = 49152 + wave * 1024 + p * 144 + jc * 2;
    va[15] = 49152 + wave * 1024 + g * 144 + r * 8;
    va[16] = WS_FL + rho * FR + FP + (bk / 3 * 6 + min(lane, 5u)) * FL;
    va[17] = 0u;
    va[18] = WS_FL + rho * FR + FX2 + gam * FL;
    va[19] = 0u;
    va[20] = 0u;
    va[21] = 0u;
    va[22] = (g == q && p < 2) ? (0x3F80u << (16 * p)) : 0u;
    va[23] = (g == q && p >= 2) ? (0x3F80u << (16 * (p - 2))) : 0u;
    va[24] = 0u;
    va[25] = 0u;
    sa[0] = 12 + wave; sa[2] = (unsigned)(unsigned long long)ws; sa[3] = (unsigned)((unsigned long long)ws >> 32);
    sa[4] = uni((unsigned)(unsigned long long)(wg + gam / 6 * 1536 + (gam % 6) * 128 + wave * (16 * 9216)));
    sa[5] = uni((unsigned)((unsigned long long)(wg + gam / 6 * 1536 + (gam % 6) * 128 + wave * (16 * 9216)) >> 32));
    sa[6] = uni((unsigned)(unsigned long long)(wu + gam / 6 * 1536 + (gam % 6) * 128 + wave * (16 * 9216)));
    sa[7] = uni((unsigned)((unsigned long long)(wu + gam / 6 * 1536 + (gam % 6) * 128 + wave * (16 * 9216)) >> 32));
    sa[8] = uni(wave * 4096);
    sa[9] = uni(pc0 * 1024 + 2048);
    sa[10] = uni((unsigned)(unsigned long long)(ws + WS_X2));
    sa[11] = uni((unsigned)((unsigned long long)(ws + WS_X2) >> 32));
    sa[12] = uni((unsigned)(unsigned long long)(out + 32 * XT));
    sa[13] = uni((unsigned)((unsigned long long)(out + 32 * XT) >> 32));
    sa[14] = uni((unsigned)(unsigned long long)(ws + WS_XD));
    sa[15] = uni((unsigned)((unsigned long long)(ws + WS_XD) >> 32));
    sb[0] = uni((unsigned)(unsigned long long)(ws - PT));
    sb[1] = uni((unsigned)((unsigned long long)(ws - PT) >> 32));
    sb[2] = uni((unsigned)(unsigned long long)(ws - X1P));
    sb[3] = uni((unsigned)((unsigned long long)(ws - X1P) >> 32));
    sb[4] = uni((unsigned)(unsigned long long)(ws + WS_X2));
    sb[5] = uni((unsigned)((unsigned long long)(ws + WS_X2) >> 32));
    sb[6] = uni((unsigned)(unsigned long long)(ws + WS_XD));
    sb[7] = uni((unsigned)((unsigned long long)(ws + WS_XD) >> 32));
    sb[8] = uni((unsigned)(unsigned long long)(ws + WS_XD));
    sb[9] = uni((unsigned)((unsigned long long)(ws + WS_XD) >> 32));
    sb[10] = uni(0u);
    asm volatile(
R"(.Lb3:
s_movk_i32 s69, 0x380
s_mov_b32 s71, 0
s_mov_b32 s74, 0
s_load_dword s37, s[38:39], 0x0
s_nop 1
v_add_u32 v166, 35840, v0
v_add_u32 v167, 71680, v0
v_add_u32 v168, 107520, v0
s_sleep 20
s_add_u32 m0, s44, 4096
s_nop 0
global_load_lds_dwordx4 v0, s[40:41] offset:-4096 nt
global_load_lds_dwordx4 v166, s[40:41] offset:-3072 nt
global_load_lds_dwordx4 v167, s[40:41] offset:-2048 nt
global_load_lds_dwordx4 v168, s[40:41] offset:-1024 nt
s_add_u32 s40, s40, 589824
s_addc_u32 s41, s41, 0
s_add_u32 m0, s44, 20480
s_nop 0
global_load_lds_dwordx4 v0, s[40:41] offset:-4096 nt
global_load_lds_dwordx4 v166, s[40:41] offset:-3072 nt
global_load_lds_dwordx4 v167, s[40:41] offset:-2048 nt
global_load_lds_dwordx4 v168, s[40:41] offset:-1024 nt
s_add_u32 s40, s40, 589824
s_addc_u32 s41, s41, 0
s_add_u32 m0, s44, 36864
s_nop 0
global_load_lds_dwordx4 v0, s[40:41] offset:-4096 nt
global_load_lds_dwordx4 v166, s[40:41] offset:-3072 nt
global_load_lds_dwordx4 v167, s[40:41] offset:-2048 nt
global_load_lds_dwordx4 v168, s[40:41] offset:-1024 nt
s_add_u32 s40, s40, 589824
s_addc_u32 s41, s41, 0
v_add_u32 v169, 0x10000, v1
v_mov_b32 v186, 0x3F803F80
v_mov_b32 v187, 0x3F803F80
v_mov_b32 v188, 0x3F803F80
v_mov_b32 v189, 0x3F803F80
s_waitcnt lgkmcnt(0)
s_mul_i32 s37, s37, 0x9E3779B1
s_add_u32 s37, s37, 0x7F4A7C15
v_add_u32 v5, s37, v5
v_add_u32 v7, s37, v7
v_add_u32 v17, s37, v17
s_waitcnt vmcnt(8)
s_barrier
s_add_u32 m0, s44, 53248
ds_read_b64_tr_b16 a[0:1], v1 offset:0
ds_read_b64_tr_b16 a[2:3], v1 offset:1024
global_load_lds_dwordx4 v0, s[40:41] offset:-4096 nt
ds_read_b64_tr_b16 a[4:5], v1 offset:8
global_load_lds_dwordx4 v166, s[40:41] offset:-3072 nt
ds_read_b64_tr_b16 a[6:7], v1 offset:1032
global_load_lds_dwordx4 v167, s[40:41] offset:-2048 nt
ds_read_b64_tr_b16 a[8:9], v1 offset:8192
global_load_lds_dwordx4 v168, s[40:41] offset:-1024 nt
ds_read_b64_tr_b16 a[10:11], v1 offset:9216
s_add_u32 s40, s40, 589824
ds_read_b64_tr_b16 a[12:13], v1 offset:8200
s_addc_u32 s41, s41, 0
ds_read_b64_tr_b16 a[14:15], v1 offset:9224
s_waitcnt vmcnt(8) lgkmcnt(0)
s_barrier
s_add_u32 m0, s44, 4096
ds_read_b64_tr_b16 a[16:17], v1 offset:16384
ds_read_b64_tr_b16 a[18:19], v1 offset:17408
global_load_lds_dwordx4 v0, s[40:41] offset:-4096 nt
ds_read_b64_tr_b16 a[20:21], v1 offset:16392
ds_read_b64_tr_b16 a[22:23], v1 offset:17416
global_load_lds_dwordx4 v166, s[40:41] offset:-3072 nt
ds_read_b64_tr_b16 a[24:25], v1 offset:24576
ds_read_b64_tr_b16 a[26:27], v1 offset:25600
global_load_lds_dwordx4 v167, s[40:41] offset:-2048 nt
ds_read_b64_tr_b16 a[28:29], v1 offset:24584
global_load_lds_dwordx4 v168, s[40:41] offset:-1024 nt
ds_read_b64_tr_b16 a[30:31], v1 offset:25608
v_mfma_f32_16x16x32_bf16 v[170:173], v[186:189], a[0:3], 0
s_add_u32 s40, s40, 589824
v_mfma_f32_16x16x32_bf16 v[174:177], v[186:189], a[4:7], 0
v_mfma_f32_16x16x32_bf16 v[170:173], v[186:189], a[8:11], v[170:173]
s_addc_u32 s41, s41, 0
v_mfma_f32_16x16x32_bf16 v[174:177], v[186:189], a[12:15], v[174:177]
s_waitcnt vmcnt(8) lgkmcnt(0)
s_barrier
s_add_u32 m0, s44, 20480
ds_read_b64_tr_b16 a[32:33], v1 offset:32768
ds_read_b64_tr_b16 a[34:35], v1 offset:33792
global_load_lds_dwordx4 v0, s[40:41] offset:-4096 nt
ds_read_b64_tr_b16 a[36:37], v1 offset:32776
ds_read_b64_tr_b16 a[38:39], v1 offset:33800
global_load_lds_dwordx4 v166, s[40:41] offset:-3072 nt
ds_read_b64_tr_b16 a[40:41], v1 offset:40960
ds_read_b64_tr_b16 a[42:43], v1 offset:41984
global_load_lds_dwordx4 v167, s[40:41] offset:-2048 nt
ds_read_b64_tr_b16 a[44:45], v1 offset:40968
global_load_lds_dwordx4 v168, s[40:41] offset:-1024 nt
ds_read_b64_tr_b16 a[46:47], v1 offset:41992
v_mfma_f32_16x16x32_bf16 v[170:173], v[186:189], a[16:19], v[170:173]
s_add_u32 s40, s40, 589824
v_mfma_f32_16x16x32_bf16 v[174:177], v[186:189], a[20:23], v[174:177]
v_mfma_f32_16x16x32_bf16 v[170:173], v[186:189], a[24:27], v[170:173]
s_addc_u32 s41, s41, 0
v_mfma_f32_16x16x32_bf16 v[174:177], v[186:189], a[28:31], v[174:177]
s_waitcnt vmcnt(8) lgkmcnt(0)
s_barrier
s_add_u32 m0, s44, 36864
ds_read_b64_tr_b16 a[48:49], v1 offset:49152
ds_read_b64_tr_b16 a[50:51], v1 offset:50176
global_load_lds_dwordx4 v0, s[40:41] offset:-4096 nt
ds_read_b64_tr_b16 a[52:53], v1 offset:49160
ds_read_b64_tr_b16 a[54:55], v1 offset:50184
global_load_lds_dwordx4 v166, s[40:41] offset:-3072 nt
ds_read_b64_tr_b16 a[56:57], v1 offset:57344
ds_read_b64_tr_b16 a[58:59], v1 offset:58368
global_load_lds_dwordx4 v167, s[40:41] offset:-2048 nt
ds_read_b64_tr_b16 a[60:61], v1 offset:57352
global_load_lds_dwordx4 v168, s[40:41] offset:-1024 nt
ds_read_b64_tr_b16 a[62:63], v1 offset:58376
v_mfma_f32_16x16x32_bf16 v[170:173], v[186:189], a[32:35], v[170:173]
s_add_u32 s40, s40, 589824
v_mfma_f32_16x16x32_bf16 v[174:177], v[186:189], a[36:39], v[174:177]
v_mfma_f32_16x16x32_bf16 v[170:173], v[186:189], a[40:43], v[170:173]
s_addc_u32 s41, s41, 0
v_mfma_f32_16x16x32_bf16 v[174:177], v[186:189], a[44:47], v[174:177]
s_waitcnt vmcnt(8) lgkmcnt(0)
s_barrier
s_add_u32 m0, s44, 53248
ds_read_b64_tr_b16 a[64:65], v1 offset:0
ds_read_b64_tr_b16 a[66:67], v1 offset:1024
global_load_lds_dwordx4 v0, s[40:41] offset:-4096 nt
ds_read_b64_tr_b16 a[68:69], v1 offset:8
ds_read_b64_tr_b16 a[70:71], v1 offset:1032
global_load_lds_dwordx4 v166, s[40:41] offset:-3072 nt
ds_read_b64_tr_b16 a[72:73], v1 offset:8192
ds_read_b64_tr_b16 a[74:75], v1 offset:9216
global_load_lds_dwordx4 v167, s[40:41] offset:-2048 nt
ds_read_b64_tr_b16 a[76:77], v1 offset:8200
global_load_lds_dwordx4 v168, s[40:41] offset:-1024 nt
ds_read_b64_tr_b16 a[78:79], v1 offset:9224
v_mfma_f32_16x16x32_bf16 v[170:173], v[186:189], a[48:51], v[170:173]
s_add_u32 s40, s40, 589824
v_mfma_f32_16x16x32_bf16 v[174:177], v[186:189], a[52:55], v[174:177]
v_mfma_f32_16x16x32_bf16 v[170:173], v[186:189], a[56:59], v[170:173]
s_addc_u32 s41, s41, 0
v_mfma_f32_16x16x32_bf16 v[174:177], v[186:189], a[60:63], v[174:177]
s_waitcnt vmcnt(8) lgkmcnt(0)
s_barrier
s_add_u32 m0, s44, 4096
ds_read_b64_tr_b16 a[80:81], v1 offset:16384
ds_read_b64_tr_b16 a[82:83], v1 offset:17408
global_load_lds_dwordx4 v0, s[40:41] offset:-4096 nt
ds_read_b64_tr_b16 a[84:85], v1 offset:16392
ds_read_b64_tr_b16 a[86:87], v1 offset:17416
global_load_lds_dwordx4 v166, s[40:41] offset:-3072 nt
ds_read_b64_tr_b16 a[88:89], v1 offset:24576
ds_read_b64_tr_b16 a[90:91], v1 offset:25600
global_load_lds_dwordx4 v167, s[40:41] offset:-2048 nt
ds_read_b64_tr_b16 a[92:93], v1 offset:24584
ds_read_b64_tr_b16 a[94:95], v1 offset:25608
global_load_lds_dwordx4 v168, s[40:41] offset:-1024 nt
v_mfma_f32_16x16x32_bf16 v[170:173], v[186:189], a[64:67], v[170:173]
v_mfma_f32_16x16x32_bf16 v[174:177], v[186:189], a[68:71], v[174:177]
s_mov_b64 s[40:41], s[42:43]
v_mfma_f32_16x16x32_bf16 v[170:173], v[186:189], a[72:75], v[170:173]
v_mfma_f32_16x16x32_bf16 v[174:177], v[186:189], a[76:79], v[174:177]
s_waitcnt vmcnt(8) lgkmcnt(0)
s_barrier
s_add_u32 m0, s44, 20480
ds_read_b64_tr_b16 a[96:97], v1 offset:32768
ds_read_b64_tr_b16 a[98:99], v1 offset:33792
global_load_lds_dwordx4 v0, s[40:41] offset:-4096 nt
ds_read_b64_tr_b16 a[100:101], v1 offset:32776
ds_read_b64_tr_b16 a[102:103], v1 offset:33800
global_load_lds_dwordx4 v166, s[40:41] offset:-3072 nt
ds_read_b64_tr_b16 a[104:105], v1 offset:40960
ds_read_b64_tr_b16 a[106:107], v1 offset:41984
global_load_lds_dwordx4 v167, s[40:41] offset:-2048 nt
ds_read_b64_tr_b16 a[108:109], v1 offset:40968
global_load_lds_dwordx4 v168, s[40:41] offset:-1024 nt
ds_read_b64_tr_b16 a[110:111], v1 offset:41992
v_mfma_f32_16x16x32_bf16 v[170:173], v[186:189], a[80:83], v[170:173]
s_add_u32 s40, s40, 589824
v_mfma_f32_16x16x32_bf16 v[174:177], v[186:189], a[84:87], v[174:177]
v_mfma_f32_16x16x32_bf16 v[170:173], v[186:189], a[88:91], v[170:173]
s_addc_u32 s41, s41, 0
v_mfma_f32_16x16x32_bf16 v[174:177], v[186:189], a[92:95], v[174:177]
s_waitcnt vmcnt(8) lgkmcnt(0)
s_barrier
s_add_u32 m0, s44, 36864
ds_read_b64_tr_b16 a[112:113], v1 offset:49152
ds_read_b64_tr_b16 a[114:115], v1 offset:50176
global_load_lds_dwordx4 v0, s[40:41] offset:-4096 nt
ds_read_b64_tr_b16 a[116:117], v1 offset:49160
ds_read_b64_tr_b16 a[118:119], v1 offset:50184
global_load_lds_dwordx4 v166, s[40:41] offset:-3072 nt
ds_read_b64_tr_b16 a[120:121], v1 offset:57344
ds_read_b64_tr_b16 a[122:123], v1 offset:58368
global_load_lds_dwordx4 v167, s[40:41] offset:-2048 nt
ds_read_b64_tr_b16 a[124:125], v1 offset:57352
global_load_lds_dwordx4 v168, s[40:41] offset:-1024 nt
ds_read_b64_tr_b16 a[126:127], v1 offset:58376
v_mfma_f32_16x16x32_bf16 v[170:173], v[186:189], a[96:99], v[170:173]
s_add_u32 s40, s40, 589824
v_mfma_f32_16x16x32_bf16 v[174:177], v[186:189], a[100:103], v[174:177]
v_mfma_f32_16x16x32_bf16 v[170:173], v[186:189], a[104:107], v[170:173]
s_addc_u32 s41, s41, 0
v_mfma_f32_16x16x32_bf16 v[174:177], v[186:189], a[108:111], v[174:177]
s_waitcnt vmcnt(8) lgkmcnt(0)
s_barrier
s_add_u32 m0, s44, 53248
ds_read_b64_tr_b16 a[128:129], v1 offset:0
ds_read_b64_tr_b16 a[130:131], v1 offset:1024
global_load_lds_dwordx4 v0, s[40:41] offset:-4096 nt
ds_read_b64_tr_b16 a[132:133], v1 offset:8
ds_read_b64_tr_b16 a[134:135], v1 offset:1032
global_load_lds_dwordx4 v166, s[40:41] offset:-3072 nt
ds_read_b64_tr_b16 a[136:137], v1 offset:8192
ds_read_b64_tr_b16 a[138:139], v1 offset:9216
global_load_lds_dwordx4 v167, s[40:41] offset:-2048 nt
ds_read_b64_tr_b16 a[140:141], v1 offset:8200
global_load_lds_dwordx4 v168, s[40:41] offset:-1024 nt
ds_read_b64_tr_b16 a[142:143], v1 offset:9224
v_mfma_f32_16x16x32_bf16 v[170:173], v[186:189], a[112:115], v[170:173]
s_add_u32 s40, s40, 589824
v_mfma_f32_16x16x32_bf16 v[174:177], v[186:189], a[116:119], v[174:177]
v_mfma_f32_16x16x32_bf16 v[170:173], v[186:189], a[120:123], v[170:173]
s_addc_u32 s41, s41, 0
v_mfma_f32_16x16x32_bf16 v[174:177], v[186:189], a[124:127], v[174:177]
s_waitcnt vmcnt(8) lgkmcnt(0)
s_barrier
s_add_u32 m0, s44, 4096
ds_read_b64_tr_b16 a[144:145], v1 offset:16384
ds_read_b64_tr_b16 a[146:147], v1 offset:17408
global_load_lds_dwordx4 v0, s[40:41] offset:-4096 nt
ds_read_b64_tr_b16 a[148:149], v1 offset:16392
ds_read_b64_tr_b16 a[150:151], v1 offset:17416
global_load_lds_dwordx4 v166, s[40:41] offset:-3072 nt
ds_read_b64_tr_b16 a[152:153], v1 offset:24576
ds_read_b64_tr_b16 a[154:155], v1 offset:25600
global_load_lds_dwordx4 v167, s[40:41] offset:-2048 nt
ds_read_b64_tr_b16 a[156:157], v1 offset:24584
global_load_lds_dwordx4 v168, s[40:41] offset:-1024 nt
ds_read_b64_tr_b16 a[158:159], v1 offset:25608
v_mfma_f32_16x16x32_bf16 v[170:173], v[186:189], a[128:131], v[170:173]
s_add_u32 s40, s40, 589824
v_mfma_f32_16x16x32_bf16 v[174:177], v[186:189], a[132:135], v[174:177]
v_mfma_f32_16x16x32_bf16 v[170:173], v[186:189], a[136:139], v[170:173]
s_addc_u32 s41, s41, 0
v_mfma_f32_16x16x32_bf16 v[174:177], v[186:189], a[140:143], v[174:177]
s_waitcnt vmcnt(8) lgkmcnt(0)
s_barrier
s_add_u32 m0, s44, 20480
ds_read_b64_tr_b16 a[160:161], v1 offset:32768
v_mfma_f32_16x16x32_bf16 v[178:181], v[186:189], a[144:147], 0
v_mfma_f32_16x16x32_bf16 v[182:185], v[186:189], a[148:151], 0
v_mfma_f32_16x16x32_bf16 v[178:181], v[186:189], a[152:155], v[178:181]
v_mfma_f32_16x16x32_bf16 v[182:185], v[186:189], a[156:159], v[182:185]
v_mul_f32 v170, 0xbae38e39, v170
v_mul_f32 v171, 0xbae38e39, v171
ds_read_b64_tr_b16 a[162:163], v1 offset:33792
v_mul_f32 v172, 0xbae38e39, v172
global_load_lds_dwordx4 v0, s[40:41] offset:-4096 nt
v_mul_f32 v173, 0xbae38e39, v173
v_mul_f32 v174, 0xbae38e39, v174
v_mul_f32 v175, 0xbae38e39, v175
v_mul_f32 v176, 0xbae38e39, v176
v_mul_f32 v177, 0xbae38e39, v177
ds_read_b64_tr_b16 a[164:165], v1 offset:32776
v_mfma_f32_16x16x32_bf16 v[190:193], v[22:25], a[0:3], v[170:173]
v_mfma_f32_16x16x32_bf16 v[194:197], v[20:23], a[0:3], v[170:173]
global_load_lds_dwordx4 v166, s[40:41] offset:-3072 nt
v_mfma_f32_16x16x32_bf16 v[198:201], v[22:25], a[4:7], v[174:177]
v_mfma_f32_16x16x32_bf16 v[202:205], v[20:23], a[4:7], v[174:177]
v_mfma_f32_16x16x32_bf16 v[206:209], v[22:25], a[8:11], v[170:173]
s_nop 2
v_cvt_pk_bf16_f32 v222, v190, v191
ds_read_b64_tr_b16 a[166:167], v1 offset:33800
v_cvt_pk_bf16_f32 v223, v192, v193
v_cvt_pk_bf16_f32 v224, v194, v195
v_cvt_pk_bf16_f32 v225, v196, v197
global_load_lds_dwordx4 v167, s[40:41] offset:-2048 nt
v_mfma_f32_16x16x32_bf16 v[210:213], v[20:23], a[8:11], v[170:173]
v_accvgpr_write_b32 a0, v222
ds_read_b64_tr_b16 a[168:169], v1 offset:40960
v_accvgpr_write_b32 a1, v223
v_accvgpr_write_b32 a2, v224
v_accvgpr_write_b32 a3, v225
v_mfma_f32_16x16x32_bf16 v[214:217], v[22:25], a[12:15], v[174:177]
v_cvt_pk_bf16_f32 v226, v198, v199
v_cvt_pk_bf16_f32 v227, v200, v201
global_load_lds_dwordx4 v168, s[40:41] offset:-1024 nt
ds_read_b64_tr_b16 a[170:171], v1 offset:41984
v_cvt_pk_bf16_f32 v228, v202, v203
v_cvt_pk_bf16_f32 v229, v204, v205
v_mfma_f32_16x16x32_bf16 v[218:221], v[20:23], a[12:15], v[174:177]
s_add_u32 s40, s40, 589824
v_accvgpr_write_b32 a4, v226
v_accvgpr_write_b32 a5, v227
v_accvgpr_write_b32 a6, v228
ds_read_b64_tr_b16 a[172:173], v1 offset:40968
v_accvgpr_write_b32 a7, v229
v_mfma_f32_16x16x32_bf16 v[190:193], v[22:25], a[16:19], v[170:173]
v_cvt_pk_bf16_f32 v222, v206, v207
v_cvt_pk_bf16_f32 v223, v208, v209
v_cvt_pk_bf16_f32 v224, v210, v211
v_cvt_pk_bf16_f32 v225, v212, v213
ds_read_b64_tr_b16 a[174:175], v1 offset:41992
v_mfma_f32_16x16x32_bf16 v[194:197], v[20:23], a[16:19], v[170:173]
s_addc_u32 s41, s41, 0
v_accvgpr_write_b32 a8, v222
v_accvgpr_write_b32 a9, v223
v_accvgpr_write_b32 a10, v224
v_accvgpr_write_b32 a11, v225
v_mfma_f32_16x16x32_bf16 v[198:201], v[22:25], a[20:23], v[174:177]
s_waitcnt vmcnt(8) lgkmcnt(0)
v_cvt_pk_bf16_f32 v226, v214, v215
v_cvt_pk_bf16_f32 v227, v216, v217
v_cvt_pk_bf16_f32 v228, v218, v219
s_barrier
s_add_u32 m0, s44, 36864
ds_read_b64_tr_b16 a[176:177], v1 offset:49152
v_mfma_f32_16x16x32_bf16 v[178:181], v[186:189], a[160:163], v[178:181]
v_mfma_f32_16x16x32_bf16 v[182:185], v[186:189], a[164:167], v[182:185]
v_mfma_f32_16x16x32_bf16 v[178:181], v[186:189], a[168:171], v[178:181]
v_mfma_f32_16x16x32_bf16 v[182:185], v[186:189], a[172:175], v[182:185]
v_cvt_pk_bf16_f32 v229, v220, v221
v_mfma_f32_16x16x32_bf16 v[202:205], v[20:23], a[20:23], v[174:177]
ds_read_b64_tr_b16 a[178:179], v1 offset:50176
v_accvgpr_write_b32 a12, v226
global_load_lds_dwordx4 v0, s[40:41] offset:-4096 nt
v_accvgpr_write_b32 a13, v227
v_accvgpr_write_b32 a14, v228
v_accvgpr_write_b32 a15, v229
v_mfma_f32_16x16x32_bf16 v[206:209], v[22:25], a[24:27], v[170:173]
v_cvt_pk_bf16_f32 v222, v190, v191
ds_read_b64_tr_b16 a[180:181], v1 offset:49160
v_cvt_pk_bf16_f32 v223, v192, v193
v_cvt_pk_bf16_f32 v224, v194, v195
global_load_lds_dwordx4 v166, s[40:41] offset:-3072 nt
v_cvt_pk_bf16_f32 v225, v196, v197
v_mfma_f32_16x16x32_bf16 v[210:213], v[20:23], a[24:27], v[170:173]
v_accvgpr_write_b32 a16, v222
v_accvgpr_write_b32 a17, v223
ds_read_b64_tr_b16 a[182:183], v1 offset:50184
v_accvgpr_write_b32 a18, v224
v_accvgpr_write_b32 a19, v225
v_mfma_f32_16x16x32_bf16 v[214:217], v[22:25], a[28:31], v[174:177]
global_load_lds_dwordx4 v167, s[40:41] offset:-2048 nt
v_cvt_pk_bf16_f32 v226, v198, v199
v_cvt_pk_bf16_f32 v227, v200, v201
ds_read_b64_tr_b16 a[184:185], v1 offset:57344
v_cvt_pk_bf16_f32 v228, v202, v203
v_cvt_pk_bf16_f32 v229, v204, v205
v_mfma_f32_16x16x32_bf16 v[218:221], v[20:23], a[28:31], v[174:177]
v_accvgpr_write_b32 a20, v226
v_accvgpr_write_b32 a21, v227
v_accvgpr_write_b32 a22, v228
global_load_lds_dwordx4 v168, s[40:41] offset:-1024 nt
ds_read_b64_tr_b16 a[186:187], v1 offset:58368
v_accvgpr_write_b32 a23, v229
v_mfma_f32_16x16x32_bf16 v[190:193], v[22:25], a[32:35], v[170:173]
s_add_u32 s40, s40, 589824
v_cvt_pk_bf16_f32 v222, v206, v207
v_cvt_pk_bf16_f32 v223, v208, v209
v_cvt_pk_bf16_f32 v224, v210, v211
v_cvt_pk_bf16_f32 v225, v212, v213
ds_read_b64_tr_b16 a[188:189], v1 offset:57352
v_mfma_f32_16x16x32_bf16 v[194:197], v[20:23], a[32:35], v[170:173]
v_accvgpr_write_b32 a24, v222
v_accvgpr_write_b32 a25, v223
v_accvgpr_write_b32 a26, v224
v_accvgpr_write_b32 a27, v225
v_mfma_f32_16x16x32_bf16 v[198:201], v[22:25], a[36:39], v[174:177]
s_addc_u32 s41, s41, 0
ds_read_b64_tr_b16 a[190:191], v1 offset:58376
v_cvt_pk_bf16_f32 v226, v214, v215
v_cvt_pk_bf16_f32 v227, v216, v217
v_cvt_pk_bf16_f32 v228, v218, v219
v_cvt_pk_bf16_f32 v229, v220, v221
v_mfma_f32_16x16x32_bf16 v[202:205], v[20:23], a[36:39], v[174:177]
s_waitcnt vmcnt(8) lgkmcnt(0)
v_accvgpr_write_b32 a28, v226
v_accvgpr_write_b32 a29, v227
v_accvgpr_write_b32 a30, v228
v_accvgpr_write_b32 a31, v229
s_barrier
s_add_u32 m0, s44, 53248
ds_read_b64_tr_b16 a[192:193], v1 offset:0
v_mfma_f32_16x16x32_bf16 v[178:181], v[186:189], a[176:179], v[178:181]
v_mfma_f32_16x16x32_bf16 v[182:185], v[186:189], a[180:183], v[182:185]
v_mfma_f32_16x16x32_bf16 v[178:181], v[186:189], a[184:187], v[178:181]
v_mfma_f32_16x16x32_bf16 v[182:185], v[186:189], a[188:191], v[182:185]
v_mfma_f32_16x16x32_bf16 v[206:209], v[22:25], a[40:43], v[170:173]
v_cvt_pk_bf16_f32 v222, v190, v191
ds_read_b64_tr_b16 a[194:195], v1 offset:1024
v_cvt_pk_bf16_f32 v223, v192, v193
global_load_lds_dwordx4 v0, s[40:41] offset:-4096 nt
v_cvt_pk_bf16_f32 v224, v194, v195
v_cvt_pk_bf16_f32 v225, v196, v197
)"
R"(v_mfma_f32_16x16x32_bf16 v[210:213], v[20:23], a[40:43], v[170:173]
v_accvgpr_write_b32 a32, v222
v_accvgpr_write_b32 a33, v223
ds_read_b64_tr_b16 a[196:197], v1 offset:8
v_accvgpr_write_b32 a34, v224
v_accvgpr_write_b32 a35, v225
global_load_lds_dwordx4 v166, s[40:41] offset:-3072 nt
v_mfma_f32_16x16x32_bf16 v[214:217], v[22:25], a[44:47], v[174:177]
v_cvt_pk_bf16_f32 v226, v198, v199
v_cvt_pk_bf16_f32 v227, v200, v201
v_cvt_pk_bf16_f32 v228, v202, v203
ds_read_b64_tr_b16 a[198:199], v1 offset:1032
v_cvt_pk_bf16_f32 v229, v204, v205
v_mfma_f32_16x16x32_bf16 v[218:221], v[20:23], a[44:47], v[174:177]
v_accvgpr_write_b32 a36, v226
global_load_lds_dwordx4 v167, s[40:41] offset:-2048 nt
v_accvgpr_write_b32 a37, v227
v_accvgpr_write_b32 a38, v228
ds_read_b64_tr_b16 a[200:201], v1 offset:8192
v_accvgpr_write_b32 a39, v229
v_mfma_f32_16x16x32_bf16 v[190:193], v[22:25], a[48:51], v[170:173]
v_cvt_pk_bf16_f32 v222, v206, v207
v_cvt_pk_bf16_f32 v223, v208, v209
v_cvt_pk_bf16_f32 v224, v210, v211
v_cvt_pk_bf16_f32 v225, v212, v213
global_load_lds_dwordx4 v168, s[40:41] offset:-1024 nt
ds_read_b64_tr_b16 a[202:203], v1 offset:9216
v_mfma_f32_16x16x32_bf16 v[194:197], v[20:23], a[48:51], v[170:173]
v_accvgpr_write_b32 a40, v222
v_accvgpr_write_b32 a41, v223
v_accvgpr_write_b32 a42, v224
v_accvgpr_write_b32 a43, v225
v_mfma_f32_16x16x32_bf16 v[198:201], v[22:25], a[52:55], v[174:177]
s_add_u32 s40, s40, 589824
ds_read_b64_tr_b16 a[204:205], v1 offset:8200
v_cvt_pk_bf16_f32 v226, v214, v215
v_cvt_pk_bf16_f32 v227, v216, v217
v_cvt_pk_bf16_f32 v228, v218, v219
v_cvt_pk_bf16_f32 v229, v220, v221
v_mfma_f32_16x16x32_bf16 v[202:205], v[20:23], a[52:55], v[174:177]
s_addc_u32 s41, s41, 0
v_accvgpr_write_b32 a44, v226
ds_read_b64_tr_b16 a[206:207], v1 offset:9224
v_accvgpr_write_b32 a45, v227
v_accvgpr_write_b32 a46, v228
v_accvgpr_write_b32 a47, v229
v_mfma_f32_16x16x32_bf16 v[206:209], v[22:25], a[56:59], v[170:173]
v_cvt_pk_bf16_f32 v222, v190, v191
v_cvt_pk_bf16_f32 v223, v192, v193
v_cvt_pk_bf16_f32 v224, v194, v195
v_cvt_pk_bf16_f32 v225, v196, v197
v_mfma_f32_16x16x32_bf16 v[210:213], v[20:23], a[56:59], v[170:173]
s_waitcnt vmcnt(8) lgkmcnt(0)
s_barrier
s_add_u32 m0, s44, 4096
ds_read_b64_tr_b16 a[208:209], v1 offset:16384
v_mfma_f32_16x16x32_bf16 v[178:181], v[186:189], a[192:195], v[178:181]
v_mfma_f32_16x16x32_bf16 v[182:185], v[186:189], a[196:199], v[182:185]
v_mfma_f32_16x16x32_bf16 v[178:181], v[186:189], a[200:203], v[178:181]
v_mfma_f32_16x16x32_bf16 v[182:185], v[186:189], a[204:207], v[182:185]
v_accvgpr_write_b32 a48, v222
v_accvgpr_write_b32 a49, v223
ds_read_b64_tr_b16 a[210:211], v1 offset:17408
v_accvgpr_write_b32 a50, v224
global_load_lds_dwordx4 v0, s[40:41] offset:-4096 nt
v_accvgpr_write_b32 a51, v225
v_mfma_f32_16x16x32_bf16 v[214:217], v[22:25], a[60:63], v[174:177]
v_cvt_pk_bf16_f32 v226, v198, v199
v_cvt_pk_bf16_f32 v227, v200, v201
v_cvt_pk_bf16_f32 v228, v202, v203
ds_read_b64_tr_b16 a[212:213], v1 offset:16392
v_cvt_pk_bf16_f32 v229, v204, v205
v_mfma_f32_16x16x32_bf16 v[218:221], v[20:23], a[60:63], v[174:177]
global_load_lds_dwordx4 v166, s[40:41] offset:-3072 nt
v_accvgpr_write_b32 a52, v226
v_accvgpr_write_b32 a53, v227
v_accvgpr_write_b32 a54, v228
v_accvgpr_write_b32 a55, v229
ds_read_b64_tr_b16 a[214:215], v1 offset:17416
v_mfma_f32_16x16x32_bf16 v[190:193], v[22:25], a[64:67], v[170:173]
v_cvt_pk_bf16_f32 v222, v206, v207
v_cvt_pk_bf16_f32 v223, v208, v209
global_load_lds_dwordx4 v167, s[40:41] offset:-2048 nt
v_cvt_pk_bf16_f32 v224, v210, v211
v_cvt_pk_bf16_f32 v225, v212, v213
ds_read_b64_tr_b16 a[216:217], v1 offset:24576
v_mfma_f32_16x16x32_bf16 v[194:197], v[20:23], a[64:67], v[170:173]
v_accvgpr_write_b32 a56, v222
v_accvgpr_write_b32 a57, v223
v_accvgpr_write_b32 a58, v224
v_accvgpr_write_b32 a59, v225
v_mfma_f32_16x16x32_bf16 v[198:201], v[22:25], a[68:71], v[174:177]
global_load_lds_dwordx4 v168, s[40:41] offset:-1024 nt
ds_read_b64_tr_b16 a[218:219], v1 offset:25600
v_cvt_pk_bf16_f32 v226, v214, v215
v_cvt_pk_bf16_f32 v227, v216, v217
v_cvt_pk_bf16_f32 v228, v218, v219
v_cvt_pk_bf16_f32 v229, v220, v221
v_mfma_f32_16x16x32_bf16 v[202:205], v[20:23], a[68:71], v[174:177]
s_add_u32 s40, s40, 589824
v_accvgpr_write_b32 a60, v226
ds_read_b64_tr_b16 a[220:221], v1 offset:24584
v_accvgpr_write_b32 a61, v227
v_accvgpr_write_b32 a62, v228
v_accvgpr_write_b32 a63, v229
v_mfma_f32_16x16x32_bf16 v[206:209], v[22:25], a[72:75], v[170:173]
s_addc_u32 s41, s41, 0
v_cvt_pk_bf16_f32 v222, v190, v191
v_cvt_pk_bf16_f32 v223, v192, v193
ds_read_b64_tr_b16 a[222:223], v1 offset:25608
v_cvt_pk_bf16_f32 v224, v194, v195
v_cvt_pk_bf16_f32 v225, v196, v197
v_mfma_f32_16x16x32_bf16 v[210:213], v[20:23], a[72:75], v[170:173]
v_accvgpr_write_b32 a64, v222
v_accvgpr_write_b32 a65, v223
v_accvgpr_write_b32 a66, v224
v_accvgpr_write_b32 a67, v225
v_mfma_f32_16x16x32_bf16 v[214:217], v[22:25], a[76:79], v[174:177]
s_waitcnt vmcnt(8) lgkmcnt(0)
v_cvt_pk_bf16_f32 v226, v198, v199
s_barrier
s_add_u32 m0, s44, 20480
ds_read_b64_tr_b16 a[224:225], v1 offset:32768
v_mfma_f32_16x16x32_bf16 v[178:181], v[186:189], a[208:211], v[178:181]
v_mfma_f32_16x16x32_bf16 v[182:185], v[186:189], a[212:215], v[182:185]
v_mfma_f32_16x16x32_bf16 v[178:181], v[186:189], a[216:219], v[178:181]
v_mfma_f32_16x16x32_bf16 v[182:185], v[186:189], a[220:223], v[182:185]
v_cvt_pk_bf16_f32 v227, v200, v201
v_cvt_pk_bf16_f32 v228, v202, v203
ds_read_b64_tr_b16 a[226:227], v1 offset:33792
v_cvt_pk_bf16_f32 v229, v204, v205
global_load_lds_dwordx4 v0, s[40:41] offset:-4096 nt
v_mfma_f32_16x16x32_bf16 v[218:221], v[20:23], a[76:79], v[174:177]
v_accvgpr_write_b32 a68, v226
v_accvgpr_write_b32 a69, v227
v_accvgpr_write_b32 a70, v228
v_accvgpr_write_b32 a71, v229
ds_read_b64_tr_b16 a[228:229], v1 offset:32776
v_mfma_f32_16x16x32_bf16 v[190:193], v[22:25], a[80:83], v[170:173]
v_cvt_pk_bf16_f32 v222, v206, v207
global_load_lds_dwordx4 v166, s[40:41] offset:-3072 nt
v_cvt_pk_bf16_f32 v223, v208, v209
v_cvt_pk_bf16_f32 v224, v210, v211
v_cvt_pk_bf16_f32 v225, v212, v213
v_mfma_f32_16x16x32_bf16 v[194:197], v[20:23], a[80:83], v[170:173]
ds_read_b64_tr_b16 a[230:231], v1 offset:33800
v_accvgpr_write_b32 a72, v222
v_accvgpr_write_b32 a73, v223
v_accvgpr_write_b32 a74, v224
global_load_lds_dwordx4 v167, s[40:41] offset:-2048 nt
v_accvgpr_write_b32 a75, v225
v_mfma_f32_16x16x32_bf16 v[198:201], v[22:25], a[84:87], v[174:177]
ds_read_b64_tr_b16 a[232:233], v1 offset:40960
v_cvt_pk_bf16_f32 v226, v214, v215
v_cvt_pk_bf16_f32 v227, v216, v217
v_cvt_pk_bf16_f32 v228, v218, v219
v_cvt_pk_bf16_f32 v229, v220, v221
v_mfma_f32_16x16x32_bf16 v[202:205], v[20:23], a[84:87], v[174:177]
v_accvgpr_write_b32 a76, v226
global_load_lds_dwordx4 v168, s[40:41] offset:-1024 nt
ds_read_b64_tr_b16 a[234:235], v1 offset:41984
v_accvgpr_write_b32 a77, v227
v_accvgpr_write_b32 a78, v228
v_accvgpr_write_b32 a79, v229
v_mfma_f32_16x16x32_bf16 v[206:209], v[22:25], a[88:91], v[170:173]
s_sub_u32 s40, s40, 10027008
v_cvt_pk_bf16_f32 v222, v190, v191
v_cvt_pk_bf16_f32 v223, v192, v193
ds_read_b64_tr_b16 a[236:237], v1 offset:40968
v_cvt_pk_bf16_f32 v224, v194, v195
v_cvt_pk_bf16_f32 v225, v196, v197
v_mfma_f32_16x16x32_bf16 v[210:213], v[20:23], a[88:91], v[170:173]
s_subb_u32 s41, s41, 0
v_accvgpr_write_b32 a80, v222
v_accvgpr_write_b32 a81, v223
v_accvgpr_write_b32 a82, v224
ds_read_b64_tr_b16 a[238:239], v1 offset:41992
v_accvgpr_write_b32 a83, v225
v_mfma_f32_16x16x32_bf16 v[214:217], v[22:25], a[92:95], v[174:177]
v_cvt_pk_bf16_f32 v226, v198, v199
v_cvt_pk_bf16_f32 v227, v200, v201
v_cvt_pk_bf16_f32 v228, v202, v203
v_cvt_pk_bf16_f32 v229, v204, v205
v_mfma_f32_16x16x32_bf16 v[218:221], v[20:23], a[92:95], v[174:177]
s_waitcnt vmcnt(8) lgkmcnt(0)
v_accvgpr_write_b32 a84, v226
v_accvgpr_write_b32 a85, v227
s_barrier
ds_read_b64_tr_b16 v[26:27], v1 offset:49152
v_mfma_f32_16x16x32_bf16 v[178:181], v[186:189], a[224:227], v[178:181]
v_mfma_f32_16x16x32_bf16 v[182:185], v[186:189], a[228:231], v[182:185]
v_mfma_f32_16x16x32_bf16 v[178:181], v[186:189], a[232:235], v[178:181]
v_mfma_f32_16x16x32_bf16 v[182:185], v[186:189], a[236:239], v[182:185]
v_accvgpr_write_b32 a86, v228
v_accvgpr_write_b32 a87, v229
ds_read_b64_tr_b16 v[28:29], v1 offset:50176
v_mfma_f32_16x16x32_bf16 v[190:193], v[22:25], a[96:99], v[170:173]
v_cvt_pk_bf16_f32 v222, v206, v207
v_cvt_pk_bf16_f32 v223, v208, v209
v_cvt_pk_bf16_f32 v224, v210, v211
v_cvt_pk_bf16_f32 v225, v212, v213
v_mfma_f32_16x16x32_bf16 v[194:197], v[20:23], a[96:99], v[170:173]
ds_read_b64_tr_b16 v[30:31], v1 offset:49160
v_accvgpr_write_b32 a88, v222
v_accvgpr_write_b32 a89, v223
v_accvgpr_write_b32 a90, v224
v_accvgpr_write_b32 a91, v225
v_mfma_f32_16x16x32_bf16 v[198:201], v[22:25], a[100:103], v[174:177]
v_cvt_pk_bf16_f32 v226, v214, v215
ds_read_b64_tr_b16 v[32:33], v1 offset:50184
v_cvt_pk_bf16_f32 v227, v216, v217
v_cvt_pk_bf16_f32 v228, v218, v219
v_cvt_pk_bf16_f32 v229, v220, v221
v_mfma_f32_16x16x32_bf16 v[202:205], v[20:23], a[100:103], v[174:177]
v_accvgpr_write_b32 a92, v226
ds_read_b64_tr_b16 v[34:35], v1 offset:57344
v_accvgpr_write_b32 a93, v227
v_accvgpr_write_b32 a94, v228
v_accvgpr_write_b32 a95, v229
v_mfma_f32_16x16x32_bf16 v[206:209], v[22:25], a[104:107], v[170:173]
v_cvt_pk_bf16_f32 v222, v190, v191
v_cvt_pk_bf16_f32 v223, v192, v193
ds_read_b64_tr_b16 v[36:37], v1 offset:58368
v_cvt_pk_bf16_f32 v224, v194, v195
v_cvt_pk_bf16_f32 v225, v196, v197
v_mfma_f32_16x16x32_bf16 v[210:213], v[20:23], a[104:107], v[170:173]
v_accvgpr_write_b32 a96, v222
v_accvgpr_write_b32 a97, v223
v_accvgpr_write_b32 a98, v224
ds_read_b64_tr_b16 v[38:39], v1 offset:57352
v_accvgpr_write_b32 a99, v225
v_mfma_f32_16x16x32_bf16 v[214:217], v[22:25], a[108:111], v[174:177]
v_cvt_pk_bf16_f32 v226, v198, v199
v_cvt_pk_bf16_f32 v227, v200, v201
v_cvt_pk_bf16_f32 v228, v202, v203
v_cvt_pk_bf16_f32 v229, v204, v205
ds_read_b64_tr_b16 v[40:41], v1 offset:58376
v_mfma_f32_16x16x32_bf16 v[218:221], v[20:23], a[108:111], v[174:177]
v_accvgpr_write_b32 a100, v226
v_accvgpr_write_b32 a101, v227
v_accvgpr_write_b32 a102, v228
v_accvgpr_write_b32 a103, v229
v_mfma_f32_16x16x32_bf16 v[190:193], v[22:25], a[112:115], v[170:173]
s_waitcnt vmcnt(4) lgkmcnt(0)
v_cvt_pk_bf16_f32 v222, v206, v207
v_cvt_pk_bf16_f32 v223, v208, v209
v_cvt_pk_bf16_f32 v224, v210, v211
s_barrier
ds_read_b64_tr_b16 v[42:43], v1 offset:0
v_mfma_f32_16x16x32_bf16 v[178:181], v[186:189], v[26:29], v[178:181]
v_mfma_f32_16x16x32_bf16 v[182:185], v[186:189], v[30:33], v[182:185]
v_mfma_f32_16x16x32_bf16 v[178:181], v[186:189], v[34:37], v[178:181]
v_mfma_f32_16x16x32_bf16 v[182:185], v[186:189], v[38:41], v[182:185]
v_cvt_pk_bf16_f32 v225, v212, v213
v_mfma_f32_16x16x32_bf16 v[194:197], v[20:23], a[112:115], v[170:173]
ds_read_b64_tr_b16 v[44:45], v1 offset:1024
v_accvgpr_write_b32 a104, v222
v_accvgpr_write_b32 a105, v223
v_accvgpr_write_b32 a106, v224
v_accvgpr_write_b32 a107, v225
v_mfma_f32_16x16x32_bf16 v[198:201], v[22:25], a[116:119], v[174:177]
v_cvt_pk_bf16_f32 v226, v214, v215
ds_read_b64_tr_b16 v[46:47], v1 offset:8
v_cvt_pk_bf16_f32 v227, v216, v217
v_cvt_pk_bf16_f32 v228, v218, v219
v_cvt_pk_bf16_f32 v229, v220, v221
v_mfma_f32_16x16x32_bf16 v[202:205], v[20:23], a[116:119], v[174:177]
v_accvgpr_write_b32 a108, v226
v_accvgpr_write_b32 a109, v227
ds_read_b64_tr_b16 v[48:49], v1 offset:1032
v_accvgpr_write_b32 a110, v228
v_accvgpr_write_b32 a111, v229
v_mfma_f32_16x16x32_bf16 v[206:209], v[22:25], a[120:123], v[170:173]
v_cvt_pk_bf16_f32 v222, v190, v191
v_cvt_pk_bf16_f32 v223, v192, v193
ds_read_b64_tr_b16 v[50:51], v1 offset:8192
v_cvt_pk_bf16_f32 v224, v194, v195
v_cvt_pk_bf16_f32 v225, v196, v197
v_mfma_f32_16x16x32_bf16 v[210:213], v[20:23], a[120:123], v[170:173]
v_accvgpr_write_b32 a112, v222
v_accvgpr_write_b32 a113, v223
v_accvgpr_write_b32 a114, v224
ds_read_b64_tr_b16 v[52:53], v1 offset:9216
v_accvgpr_write_b32 a115, v225
v_mfma_f32_16x16x32_bf16 v[214:217], v[22:25], a[124:127], v[174:177]
v_cvt_pk_bf16_f32 v226, v198, v199
v_cvt_pk_bf16_f32 v227, v200, v201
v_cvt_pk_bf16_f32 v228, v202, v203
v_cvt_pk_bf16_f32 v229, v204, v205
ds_read_b64_tr_b16 v[54:55], v1 offset:8200
v_mfma_f32_16x16x32_bf16 v[218:221], v[20:23], a[124:127], v[174:177]
v_accvgpr_write_b32 a116, v226
v_accvgpr_write_b32 a117, v227
v_accvgpr_write_b32 a118, v228
v_accvgpr_write_b32 a119, v229
v_mfma_f32_16x16x32_bf16 v[190:193], v[22:25], a[128:131], v[170:173]
ds_read_b64_tr_b16 v[56:57], v1 offset:9224
v_cvt_pk_bf16_f32 v222, v206, v207
v_cvt_pk_bf16_f32 v223, v208, v209
v_cvt_pk_bf16_f32 v224, v210, v211
v_cvt_pk_bf16_f32 v225, v212, v213
v_mfma_f32_16x16x32_bf16 v[194:197], v[20:23], a[128:131], v[170:173]
s_waitcnt vmcnt(0) lgkmcnt(0)
v_accvgpr_write_b32 a120, v222
v_accvgpr_write_b32 a121, v223
v_accvgpr_write_b32 a122, v224
v_accvgpr_write_b32 a123, v225
s_barrier
ds_read_b64_tr_b16 v[58:59], v1 offset:16384
v_mfma_f32_16x16x32_bf16 v[178:181], v[186:189], v[42:45], v[178:181]
v_mfma_f32_16x16x32_bf16 v[182:185], v[186:189], v[46:49], v[182:185]
v_mfma_f32_16x16x32_bf16 v[178:181], v[186:189], v[50:53], v[178:181]
v_mfma_f32_16x16x32_bf16 v[182:185], v[186:189], v[54:57], v[182:185]
v_mfma_f32_16x16x32_bf16 v[198:201], v[22:25], a[132:135], v[174:177]
v_cvt_pk_bf16_f32 v226, v214, v215
ds_read_b64_tr_b16 v[60:61], v1 offset:17408
v_cvt_pk_bf16_f32 v227, v216, v217
v_cvt_pk_bf16_f32 v228, v218, v219
v_cvt_pk_bf16_f32 v229, v220, v221
v_mfma_f32_16x16x32_bf16 v[202:205], v[20:23], a[132:135], v[174:177]
v_accvgpr_write_b32 a124, v226
v_accvgpr_write_b32 a125, v227
ds_read_b64_tr_b16 v[62:63], v1 offset:16392
v_accvgpr_write_b32 a126, v228
v_accvgpr_write_b32 a127, v229
v_mfma_f32_16x16x32_bf16 v[206:209], v[22:25], a[136:139], v[170:173]
v_cvt_pk_bf16_f32 v222, v190, v191
v_cvt_pk_bf16_f32 v223, v192, v193
v_cvt_pk_bf16_f32 v224, v194, v195
ds_read_b64_tr_b16 v[64:65], v1 offset:17416
v_cvt_pk_bf16_f32 v225, v196, v197
v_mfma_f32_16x16x32_bf16 v[210:213], v[20:23], a[136:139], v[170:173]
v_accvgpr_write_b32 a128, v222
v_accvgpr_write_b32 a129, v223
v_accvgpr_write_b32 a130, v224
ds_read_b64_tr_b16 v[66:67], v1 offset:24576
v_accvgpr_write_b32 a131, v225
v_mfma_f32_16x16x32_bf16 v[214:217], v[22:25], a[140:143], v[174:177]
v_cvt_pk_bf16_f32 v226, v198, v199
v_cvt_pk_bf16_f32 v227, v200, v201
v_cvt_pk_bf16_f32 v228, v202, v203
v_cvt_pk_bf16_f32 v229, v204, v205
ds_read_b64_tr_b16 v[68:69], v1 offset:25600
v_mfma_f32_16x16x32_bf16 v[218:221], v[20:23], a[140:143], v[174:177]
s_waitcnt lgkmcnt(4)
v_accvgpr_write_b32 a132, v226
v_accvgpr_write_b32 a133, v227
v_accvgpr_write_b32 a134, v228
v_accvgpr_write_b32 a135, v229
v_cvt_pk_bf16_f32 v222, v206, v207
ds_read_b64_tr_b16 v[70:71], v1 offset:24584
v_cvt_pk_bf16_f32 v223, v208, v209
v_cvt_pk_bf16_f32 v224, v210, v211
v_cvt_pk_bf16_f32 v225, v212, v213
v_accvgpr_write_b32 a136, v222
v_accvgpr_write_b32 a137, v223
v_accvgpr_write_b32 a138, v224
ds_read_b64_tr_b16 v[72:73], v1 offset:25608
v_accvgpr_write_b32 a139, v225
v_cvt_pk_bf16_f32 v226, v214, v215
v_cvt_pk_bf16_f32 v227, v216, v217
v_cvt_pk_bf16_f32 v228, v218, v219
v_cvt_pk_bf16_f32 v229, v220, v221
v_accvgpr_write_b32 a140, v226
v_accvgpr_write_b32 a141, v227
v_accvgpr_write_b32 a142, v228
v_accvgpr_write_b32 a143, v229
v_mfma_f32_16x16x32_bf16 v[178:181], v[186:189], v[58:61], v[178:181]
s_waitcnt lgkmcnt(4)
v_mfma_f32_16x16x32_bf16 v[182:185], v[186:189], v[62:65], v[182:185]
s_waitcnt lgkmcnt(2)
v_mfma_f32_16x16x32_bf16 v[178:181], v[186:189], v[66:69], v[178:181]
s_waitcnt lgkmcnt(0)
v_mfma_f32_16x16x32_bf16 v[182:185], v[186:189], v[70:73], v[182:185]
s_nop 5
v_mul_f32 v178, 0xbae38e39, v178
v_mul_f32 v179, 0xbae38e39, v179
v_mul_f32 v180, 0xbae38e39, v180
v_mul_f32 v181, 0xbae38e39, v181
v_mul_f32 v182, 0xbae38e39, v182
v_mul_f32 v183, 0xbae38e39, v183
v_mul_f32 v184, 0xbae38e39, v184
v_mul_f32 v185, 0xbae38e39, v185
v_mfma_f32_16x16x32_bf16 v[190:193], v[22:25], a[144:147], v[178:181]
v_mfma_f32_16x16x32_bf16 v[194:197], v[20:23], a[144:147], v[178:181]
v_mfma_f32_16x16x32_bf16 v[198:201], v[22:25], a[148:151], v[182:185]
v_mfma_f32_16x16x32_bf16 v[202:205], v[20:23], a[148:151], v[182:185]
v_mfma_f32_16x16x32_bf16 v[206:209], v[22:25], a[152:155], v[178:181]
s_nop 3
v_cvt_pk_bf16_f32 v222, v190, v191
v_cvt_pk_bf16_f32 v223, v192, v193
v_cvt_pk_bf16_f32 v224, v194, v195
v_cvt_pk_bf16_f32 v225, v196, v197
v_mfma_f32_16x16x32_bf16 v[210:213], v[20:23], a[152:155], v[178:181]
v_accvgpr_write_b32 a144, v222
v_accvgpr_write_b32 a145, v223
v_accvgpr_write_b32 a146, v224
v_accvgpr_write_b32 a147, v225
v_mfma_f32_16x16x32_bf16 v[214:217], v[22:25], a[156:159], v[182:185]
v_cvt_pk_bf16_f32 v226, v198, v199
)"
R"(v_cvt_pk_bf16_f32 v227, v200, v201
v_cvt_pk_bf16_f32 v228, v202, v203
v_cvt_pk_bf16_f32 v229, v204, v205
v_mfma_f32_16x16x32_bf16 v[218:221], v[20:23], a[156:159], v[182:185]
v_accvgpr_write_b32 a148, v226
v_accvgpr_write_b32 a149, v227
v_accvgpr_write_b32 a150, v228
v_accvgpr_write_b32 a151, v229
v_mfma_f32_16x16x32_bf16 v[190:193], v[22:25], a[160:163], v[178:181]
v_cvt_pk_bf16_f32 v222, v206, v207
v_cvt_pk_bf16_f32 v223, v208, v209
v_cvt_pk_bf16_f32 v224, v210, v211
v_cvt_pk_bf16_f32 v225, v212, v213
v_mfma_f32_16x16x32_bf16 v[194:197], v[20:23], a[160:163], v[178:181]
v_accvgpr_write_b32 a152, v222
v_accvgpr_write_b32 a153, v223
v_accvgpr_write_b32 a154, v224
v_accvgpr_write_b32 a155, v225
v_mfma_f32_16x16x32_bf16 v[198:201], v[22:25], a[164:167], v[182:185]
v_cvt_pk_bf16_f32 v226, v214, v215
v_cvt_pk_bf16_f32 v227, v216, v217
v_cvt_pk_bf16_f32 v228, v218, v219
v_cvt_pk_bf16_f32 v229, v220, v221
v_mfma_f32_16x16x32_bf16 v[202:205], v[20:23], a[164:167], v[182:185]
v_accvgpr_write_b32 a156, v226
v_accvgpr_write_b32 a157, v227
v_accvgpr_write_b32 a158, v228
v_accvgpr_write_b32 a159, v229
v_mfma_f32_16x16x32_bf16 v[206:209], v[22:25], a[168:171], v[178:181]
v_cvt_pk_bf16_f32 v222, v190, v191
v_cvt_pk_bf16_f32 v223, v192, v193
v_cvt_pk_bf16_f32 v224, v194, v195
v_cvt_pk_bf16_f32 v225, v196, v197
v_mfma_f32_16x16x32_bf16 v[210:213], v[20:23], a[168:171], v[178:181]
v_accvgpr_write_b32 a160, v222
v_accvgpr_write_b32 a161, v223
v_accvgpr_write_b32 a162, v224
v_accvgpr_write_b32 a163, v225
v_mfma_f32_16x16x32_bf16 v[214:217], v[22:25], a[172:175], v[182:185]
v_cvt_pk_bf16_f32 v226, v198, v199
v_cvt_pk_bf16_f32 v227, v200, v201
v_cvt_pk_bf16_f32 v228, v202, v203
v_cvt_pk_bf16_f32 v229, v204, v205
v_mfma_f32_16x16x32_bf16 v[218:221], v[20:23], a[172:175], v[182:185]
v_accvgpr_write_b32 a164, v226
v_accvgpr_write_b32 a165, v227
v_accvgpr_write_b32 a166, v228
v_accvgpr_write_b32 a167, v229
v_mfma_f32_16x16x32_bf16 v[190:193], v[22:25], a[176:179], v[178:181]
v_cvt_pk_bf16_f32 v222, v206, v207
v_cvt_pk_bf16_f32 v223, v208, v209
v_cvt_pk_bf16_f32 v224, v210, v211
v_cvt_pk_bf16_f32 v225, v212, v213
v_mfma_f32_16x16x32_bf16 v[194:197], v[20:23], a[176:179], v[178:181]
v_accvgpr_write_b32 a168, v222
v_accvgpr_write_b32 a169, v223
v_accvgpr_write_b32 a170, v224
v_accvgpr_write_b32 a171, v225
v_mfma_f32_16x16x32_bf16 v[198:201], v[22:25], a[180:183], v[182:185]
v_cvt_pk_bf16_f32 v226, v214, v215
v_cvt_pk_bf16_f32 v227, v216, v217
v_cvt_pk_bf16_f32 v228, v218, v219
v_cvt_pk_bf16_f32 v229, v220, v221
v_mfma_f32_16x16x32_bf16 v[202:205], v[20:23], a[180:183], v[182:185]
v_accvgpr_write_b32 a172, v226
v_accvgpr_write_b32 a173, v227
v_accvgpr_write_b32 a174, v228
v_accvgpr_write_b32 a175, v229
v_mfma_f32_16x16x32_bf16 v[206:209], v[22:25], a[184:187], v[178:181]
v_cvt_pk_bf16_f32 v222, v190, v191
v_cvt_pk_bf16_f32 v223, v192, v193
v_cvt_pk_bf16_f32 v224, v194, v195
v_cvt_pk_bf16_f32 v225, v196, v197
v_mfma_f32_16x16x32_bf16 v[210:213], v[20:23], a[184:187], v[178:181]
v_accvgpr_write_b32 a176, v222
v_accvgpr_write_b32 a177, v223
v_accvgpr_write_b32 a178, v224
v_accvgpr_write_b32 a179, v225
v_mfma_f32_16x16x32_bf16 v[214:217], v[22:25], a[188:191], v[182:185]
v_cvt_pk_bf16_f32 v226, v198, v199
v_cvt_pk_bf16_f32 v227, v200, v201
v_cvt_pk_bf16_f32 v228, v202, v203
v_cvt_pk_bf16_f32 v229, v204, v205
v_mfma_f32_16x16x32_bf16 v[218:221], v[20:23], a[188:191], v[182:185]
v_accvgpr_write_b32 a180, v226
v_accvgpr_write_b32 a181, v227
v_accvgpr_write_b32 a182, v228
v_accvgpr_write_b32 a183, v229
v_mfma_f32_16x16x32_bf16 v[190:193], v[22:25], a[192:195], v[178:181]
v_cvt_pk_bf16_f32 v222, v206, v207
v_cvt_pk_bf16_f32 v223, v208, v209
v_cvt_pk_bf16_f32 v224, v210, v211
v_cvt_pk_bf16_f32 v225, v212, v213
v_mfma_f32_16x16x32_bf16 v[194:197], v[20:23], a[192:195], v[178:181]
v_accvgpr_write_b32 a184, v222
v_accvgpr_write_b32 a185, v223
v_accvgpr_write_b32 a186, v224
v_accvgpr_write_b32 a187, v225
v_mfma_f32_16x16x32_bf16 v[198:201], v[22:25], a[196:199], v[182:185]
v_cvt_pk_bf16_f32 v226, v214, v215
v_cvt_pk_bf16_f32 v227, v216, v217
v_cvt_pk_bf16_f32 v228, v218, v219
v_cvt_pk_bf16_f32 v229, v220, v221
v_mfma_f32_16x16x32_bf16 v[202:205], v[20:23], a[196:199], v[182:185]
v_accvgpr_write_b32 a188, v226
v_accvgpr_write_b32 a189, v227
v_accvgpr_write_b32 a190, v228
v_accvgpr_write_b32 a191, v229
v_mfma_f32_16x16x32_bf16 v[206:209], v[22:25], a[200:203], v[178:181]
v_cvt_pk_bf16_f32 v222, v190, v191
v_cvt_pk_bf16_f32 v223, v192, v193
v_cvt_pk_bf16_f32 v224, v194, v195
v_cvt_pk_bf16_f32 v225, v196, v197
v_mfma_f32_16x16x32_bf16 v[210:213], v[20:23], a[200:203], v[178:181]
v_accvgpr_write_b32 a192, v222
v_accvgpr_write_b32 a193, v223
v_accvgpr_write_b32 a194, v224
v_accvgpr_write_b32 a195, v225
v_mfma_f32_16x16x32_bf16 v[214:217], v[22:25], a[204:207], v[182:185]
v_cvt_pk_bf16_f32 v226, v198, v199
v_cvt_pk_bf16_f32 v227, v200, v201
v_cvt_pk_bf16_f32 v228, v202, v203
v_cvt_pk_bf16_f32 v229, v204, v205
v_mfma_f32_16x16x32_bf16 v[218:221], v[20:23], a[204:207], v[182:185]
v_accvgpr_write_b32 a196, v226
v_accvgpr_write_b32 a197, v227
v_accvgpr_write_b32 a198, v228
v_accvgpr_write_b32 a199, v229
v_mfma_f32_16x16x32_bf16 v[190:193], v[22:25], a[208:211], v[178:181]
v_cvt_pk_bf16_f32 v222, v206, v207
v_cvt_pk_bf16_f32 v223, v208, v209
v_cvt_pk_bf16_f32 v224, v210, v211
v_cvt_pk_bf16_f32 v225, v212, v213
v_mfma_f32_16x16x32_bf16 v[194:197], v[20:23], a[208:211], v[178:181]
v_accvgpr_write_b32 a200, v222
v_accvgpr_write_b32 a201, v223
v_accvgpr_write_b32 a202, v224
v_accvgpr_write_b32 a203, v225
v_mfma_f32_16x16x32_bf16 v[198:201], v[22:25], a[212:215], v[182:185]
v_cvt_pk_bf16_f32 v226, v214, v215
v_cvt_pk_bf16_f32 v227, v216, v217
v_cvt_pk_bf16_f32 v228, v218, v219
v_cvt_pk_bf16_f32 v229, v220, v221
v_mfma_f32_16x16x32_bf16 v[202:205], v[20:23], a[212:215], v[182:185]
v_accvgpr_write_b32 a204, v226
v_accvgpr_write_b32 a205, v227
v_accvgpr_write_b32 a206, v228
v_accvgpr_write_b32 a207, v229
v_mfma_f32_16x16x32_bf16 v[206:209], v[22:25], a[216:219], v[178:181]
v_cvt_pk_bf16_f32 v222, v190, v191
v_cvt_pk_bf16_f32 v223, v192, v193
v_cvt_pk_bf16_f32 v224, v194, v195
v_cvt_pk_bf16_f32 v225, v196, v197
v_mfma_f32_16x16x32_bf16 v[210:213], v[20:23], a[216:219], v[178:181]
v_accvgpr_write_b32 a208, v222
v_accvgpr_write_b32 a209, v223
v_accvgpr_write_b32 a210, v224
v_accvgpr_write_b32 a211, v225
v_mfma_f32_16x16x32_bf16 v[214:217], v[22:25], a[220:223], v[182:185]
v_cvt_pk_bf16_f32 v226, v198, v199
v_cvt_pk_bf16_f32 v227, v200, v201
v_cvt_pk_bf16_f32 v228, v202, v203
v_cvt_pk_bf16_f32 v229, v204, v205
v_mfma_f32_16x16x32_bf16 v[218:221], v[20:23], a[220:223], v[182:185]
v_accvgpr_write_b32 a212, v226
v_accvgpr_write_b32 a213, v227
v_accvgpr_write_b32 a214, v228
v_accvgpr_write_b32 a215, v229
v_mfma_f32_16x16x32_bf16 v[190:193], v[22:25], a[224:227], v[178:181]
v_cvt_pk_bf16_f32 v222, v206, v207
v_cvt_pk_bf16_f32 v223, v208, v209
v_cvt_pk_bf16_f32 v224, v210, v211
v_cvt_pk_bf16_f32 v225, v212, v213
v_mfma_f32_16x16x32_bf16 v[194:197], v[20:23], a[224:227], v[178:181]
v_accvgpr_write_b32 a216, v222
v_accvgpr_write_b32 a217, v223
v_accvgpr_write_b32 a218, v224
v_accvgpr_write_b32 a219, v225
v_mfma_f32_16x16x32_bf16 v[198:201], v[22:25], a[228:231], v[182:185]
v_cvt_pk_bf16_f32 v226, v214, v215
v_cvt_pk_bf16_f32 v227, v216, v217
v_cvt_pk_bf16_f32 v228, v218, v219
v_cvt_pk_bf16_f32 v229, v220, v221
v_mfma_f32_16x16x32_bf16 v[202:205], v[20:23], a[228:231], v[182:185]
v_accvgpr_write_b32 a220, v226
v_accvgpr_write_b32 a221, v227
v_accvgpr_write_b32 a222, v228
v_accvgpr_write_b32 a223, v229
v_mfma_f32_16x16x32_bf16 v[206:209], v[22:25], a[232:235], v[178:181]
v_cvt_pk_bf16_f32 v222, v190, v191
v_cvt_pk_bf16_f32 v223, v192, v193
v_cvt_pk_bf16_f32 v224, v194, v195
v_cvt_pk_bf16_f32 v225, v196, v197
v_mfma_f32_16x16x32_bf16 v[210:213], v[20:23], a[232:235], v[178:181]
v_accvgpr_write_b32 a224, v222
v_accvgpr_write_b32 a225, v223
v_accvgpr_write_b32 a226, v224
v_accvgpr_write_b32 a227, v225
v_mfma_f32_16x16x32_bf16 v[214:217], v[22:25], a[236:239], v[182:185]
v_cvt_pk_bf16_f32 v226, v198, v199
v_cvt_pk_bf16_f32 v227, v200, v201
v_cvt_pk_bf16_f32 v228, v202, v203
v_cvt_pk_bf16_f32 v229, v204, v205
v_mfma_f32_16x16x32_bf16 v[218:221], v[20:23], a[236:239], v[182:185]
v_accvgpr_write_b32 a228, v226
v_accvgpr_write_b32 a229, v227
v_accvgpr_write_b32 a230, v228
v_accvgpr_write_b32 a231, v229
v_mfma_f32_16x16x32_bf16 v[190:193], v[22:25], v[26:29], v[178:181]
v_cvt_pk_bf16_f32 v222, v206, v207
v_cvt_pk_bf16_f32 v223, v208, v209
v_cvt_pk_bf16_f32 v224, v210, v211
v_cvt_pk_bf16_f32 v225, v212, v213
v_mfma_f32_16x16x32_bf16 v[194:197], v[20:23], v[26:29], v[178:181]
v_accvgpr_write_b32 a232, v222
v_accvgpr_write_b32 a233, v223
v_accvgpr_write_b32 a234, v224
v_accvgpr_write_b32 a235, v225
v_mfma_f32_16x16x32_bf16 v[198:201], v[22:25], v[30:33], v[182:185]
v_cvt_pk_bf16_f32 v226, v214, v215
v_cvt_pk_bf16_f32 v227, v216, v217
v_cvt_pk_bf16_f32 v228, v218, v219
v_cvt_pk_bf16_f32 v229, v220, v221
v_mfma_f32_16x16x32_bf16 v[202:205], v[20:23], v[30:33], v[182:185]
v_accvgpr_write_b32 a236, v226
v_accvgpr_write_b32 a237, v227
v_accvgpr_write_b32 a238, v228
v_accvgpr_write_b32 a239, v229
v_mfma_f32_16x16x32_bf16 v[206:209], v[22:25], v[34:37], v[178:181]
v_cvt_pk_bf16_f32 v26, v190, v191
v_cvt_pk_bf16_f32 v27, v192, v193
v_cvt_pk_bf16_f32 v28, v194, v195
v_cvt_pk_bf16_f32 v29, v196, v197
v_mfma_f32_16x16x32_bf16 v[210:213], v[20:23], v[34:37], v[178:181]
v_mfma_f32_16x16x32_bf16 v[214:217], v[22:25], v[38:41], v[182:185]
v_cvt_pk_bf16_f32 v30, v198, v199
v_cvt_pk_bf16_f32 v31, v200, v201
v_cvt_pk_bf16_f32 v32, v202, v203
v_cvt_pk_bf16_f32 v33, v204, v205
v_mfma_f32_16x16x32_bf16 v[218:221], v[20:23], v[38:41], v[182:185]
v_mfma_f32_16x16x32_bf16 v[190:193], v[22:25], v[42:45], v[178:181]
v_cvt_pk_bf16_f32 v34, v206, v207
v_cvt_pk_bf16_f32 v35, v208, v209
v_cvt_pk_bf16_f32 v36, v210, v211
v_cvt_pk_bf16_f32 v37, v212, v213
v_mfma_f32_16x16x32_bf16 v[194:197], v[20:23], v[42:45], v[178:181]
v_mfma_f32_16x16x32_bf16 v[198:201], v[22:25], v[46:49], v[182:185]
v_cvt_pk_bf16_f32 v38, v214, v215
v_cvt_pk_bf16_f32 v39, v216, v217
v_cvt_pk_bf16_f32 v40, v218, v219
v_cvt_pk_bf16_f32 v41, v220, v221
v_mfma_f32_16x16x32_bf16 v[202:205], v[20:23], v[46:49], v[182:185]
v_mfma_f32_16x16x32_bf16 v[206:209], v[22:25], v[50:53], v[178:181]
v_cvt_pk_bf16_f32 v42, v190, v191
v_cvt_pk_bf16_f32 v43, v192, v193
v_cvt_pk_bf16_f32 v44, v194, v195
v_cvt_pk_bf16_f32 v45, v196, v197
v_mfma_f32_16x16x32_bf16 v[210:213], v[20:23], v[50:53], v[178:181]
v_mfma_f32_16x16x32_bf16 v[214:217], v[22:25], v[54:57], v[182:185]
v_cvt_pk_bf16_f32 v46, v198, v199
v_cvt_pk_bf16_f32 v47, v200, v201
v_cvt_pk_bf16_f32 v48, v202, v203
v_cvt_pk_bf16_f32 v49, v204, v205
v_mfma_f32_16x16x32_bf16 v[218:221], v[20:23], v[54:57], v[182:185]
v_mfma_f32_16x16x32_bf16 v[190:193], v[22:25], v[58:61], v[178:181]
v_cvt_pk_bf16_f32 v50, v206, v207
v_cvt_pk_bf16_f32 v51, v208, v209
v_cvt_pk_bf16_f32 v52, v210, v211
v_cvt_pk_bf16_f32 v53, v212, v213
v_mfma_f32_16x16x32_bf16 v[194:197], v[20:23], v[58:61], v[178:181]
v_mfma_f32_16x16x32_bf16 v[198:201], v[22:25], v[62:65], v[182:185]
v_cvt_pk_bf16_f32 v54, v214, v215
v_cvt_pk_bf16_f32 v55, v216, v217
v_cvt_pk_bf16_f32 v56, v218, v219
v_cvt_pk_bf16_f32 v57, v220, v221
v_mfma_f32_16x16x32_bf16 v[202:205], v[20:23], v[62:65], v[182:185]
v_mfma_f32_16x16x32_bf16 v[206:209], v[22:25], v[66:69], v[178:181]
v_cvt_pk_bf16_f32 v58, v190, v191
v_cvt_pk_bf16_f32 v59, v192, v193
v_cvt_pk_bf16_f32 v60, v194, v195
v_cvt_pk_bf16_f32 v61, v196, v197
v_mfma_f32_16x16x32_bf16 v[210:213], v[20:23], v[66:69], v[178:181]
v_mfma_f32_16x16x32_bf16 v[214:217], v[22:25], v[70:73], v[182:185]
v_cvt_pk_bf16_f32 v62, v198, v199
v_cvt_pk_bf16_f32 v63, v200, v201
v_cvt_pk_bf16_f32 v64, v202, v203
v_cvt_pk_bf16_f32 v65, v204, v205
v_mfma_f32_16x16x32_bf16 v[218:221], v[20:23], v[70:73], v[182:185]
v_cvt_pk_bf16_f32 v66, v206, v207
v_cvt_pk_bf16_f32 v67, v208, v209
v_cvt_pk_bf16_f32 v68, v210, v211
v_cvt_pk_bf16_f32 v69, v212, v213
v_cvt_pk_bf16_f32 v70, v214, v215
v_cvt_pk_bf16_f32 v71, v216, v217
s_nop 1
v_cvt_pk_bf16_f32 v72, v218, v219
v_cvt_pk_bf16_f32 v73, v220, v221
s_barrier
s_and_b32 s75, s36, 3
s_cmp_eq_u32 s75, 1
s_cbranch_scc1 .Lgu_w2
s_cmp_eq_u32 s75, 2
s_cbranch_scc1 .Lgu_w3
s_cmp_eq_u32 s75, 3
s_cbranch_scc1 .Lgu_w4
.Lgu_w1:
v_add_u32 v254, -1, v5
v_add_u32 v255, -1, v5
v_add_lshl_u32 v252, v7, 0, 7
v_bfi_b32 v252, s69, v252, v6
v_add_lshl_u32 v253, v5, 0, 7
v_bfi_b32 v253, s69, v253, v4
s_add_u32 m0, s45, 18432
s_xor_b32 s45, s45, 0x10000
s_mov_b32 m0, s45
s_barrier
global_load_lds_dwordx4 v2, s[38:39] offset:-2048 sc1
global_load_lds_dwordx4 v2, s[38:39] offset:-1024 sc1
global_load_lds_dwordx4 v2, s[38:39] offset:0 sc1
global_load_lds_dwordx4 v2, s[38:39] offset:1024 sc1
global_load_lds_dwordx4 v2, s[38:39] offset:2048 sc1
global_load_lds_dwordx4 v2, s[38:39] offset:3072 sc1
v_add_u32 v2, 18688, v2
s_mov_b32 s70, 32
s_waitcnt vmcnt(0) lgkmcnt(0)
s_nop 7
s_branch .Lgu_in5
.p2align 6
.Lgu_top6:
s_waitcnt lgkmcnt(5)
v_mfma_f32_16x16x32_bf16 v[74:77], a[0:3], v[106:109], 0
s_waitcnt lgkmcnt(4)
v_cvt_pk_bf16_f32 v162, v154, v155
v_cvt_pk_bf16_f32 v163, v156, v157
v_mfma_f32_16x16x32_bf16 v[74:77], a[8:11], v[110:113], v[74:77]
s_waitcnt lgkmcnt(3)
v_mfma_f32_16x16x32_bf16 v[74:77], a[16:19], v[114:117], v[74:77]
s_waitcnt lgkmcnt(2)
v_mfma_f32_16x16x32_bf16 v[74:77], a[24:27], v[118:121], v[74:77]
s_waitcnt lgkmcnt(1)
v_mul_f32 v158, v94, v102
v_mul_f32 v159, v95, v103
v_mfma_f32_16x16x32_bf16 v[74:77], a[32:35], v[122:125], v[74:77]
s_waitcnt lgkmcnt(0)
v_mul_f32 v160, v96, v104
v_mul_f32 v161, v97, v105
v_mfma_f32_16x16x32_bf16 v[74:77], a[40:43], v[126:129], v[74:77]
ds_read_b128 v[130:133], v8 offset:6144
v_cvt_pk_bf16_f32 v164, v158, v159
v_mfma_f32_16x16x32_bf16 v[82:85], a[144:147], v[106:109], 0
ds_read_b128 v[134:137], v8 offset:7168
v_cvt_pk_bf16_f32 v165, v160, v161
v_mfma_f32_16x16x32_bf16 v[82:85], a[152:155], v[110:113], v[82:85]
v_mfma_f32_16x16x32_bf16 v[82:85], a[160:163], v[114:117], v[82:85]
s_waitcnt vmcnt(6)
s_barrier
v_mfma_f32_16x16x32_bf16 v[82:85], a[168:171], v[118:121], v[82:85]
v_mfma_f32_16x16x32_bf16 v[82:85], a[176:179], v[122:125], v[82:85]
global_store_dwordx4 v3, v[162:165], s[38:39] sc1
v_mfma_f32_16x16x32_bf16 v[82:85], a[184:187], v[126:129], v[82:85]
ds_read_b128 v[138:141], v8 offset:8192
v_add_u32 v3, 147456, v3
v_mfma_f32_16x16x32_bf16 v[78:81], a[4:7], v[106:109], 0
v_mfma_f32_16x16x32_bf16 v[78:81], a[12:15], v[110:113], v[78:81]
v_mfma_f32_16x16x32_bf16 v[78:81], a[20:23], v[114:117], v[78:81]
ds_read_b128 v[142:145], v8 offset:9216
v_mfma_f32_16x16x32_bf16 v[78:81], a[28:31], v[118:121], v[78:81]
v_mfma_f32_16x16x32_bf16 v[78:81], a[36:39], v[122:125], v[78:81]
v_mfma_f32_16x16x32_bf16 v[78:81], a[44:47], v[126:129], v[78:81]
ds_read_b128 v[146:149], v8 offset:10240
v_mfma_f32_16x16x32_bf16 v[86:89], a[148:151], v[106:109], 0
v_mfma_f32_16x16x32_bf16 v[86:89], a[156:159], v[110:113], v[86:89]
v_mfma_f32_16x16x32_bf16 v[86:89], a[164:167], v[114:117], v[86:89]
ds_read_b128 v[150:153], v8 offset:11264
v_mfma_f32_16x16x32_bf16 v[86:89], a[172:175], v[118:121], v[86:89]
v_mfma_f32_16x16x32_bf16 v[86:89], a[180:183], v[122:125], v[86:89]
v_mfma_f32_16x16x32_bf16 v[86:89], a[188:191], v[126:129], v[86:89]
s_waitcnt lgkmcnt(5)
v_mfma_f32_16x16x32_bf16 v[74:77], a[48:51], v[130:133], v[74:77]
s_waitcnt lgkmcnt(4)
v_mfma_f32_16x16x32_bf16 v[74:77], a[56:59], v[134:137], v[74:77]
s_waitcnt lgkmcnt(3)
v_mfma_f32_16x16x32_bf16 v[74:77], a[64:67], v[138:141], v[74:77]
s_waitcnt lgkmcnt(2)
v_mfma_f32_16x16x32_bf16 v[74:77], a[72:75], v[142:145], v[74:77]
s_waitcnt lgkmcnt(1)
v_mfma_f32_16x16x32_bf16 v[74:77], a[80:83], v[146:149], v[74:77]
s_waitcnt lgkmcnt(0)
v_mfma_f32_16x16x32_bf16 v[74:77], a[88:91], v[150:153], v[74:77]
ds_read_b128 v[106:109], v8 offset:12288
v_mfma_f32_16x16x32_bf16 v[82:85], a[192:195], v[130:133], v[82:85]
v_mfma_f32_16x16x32_bf16 v[82:85], a[200:203], v[134:137], v[82:85]
v_mfma_f32_16x16x32_bf16 v[82:85], a[208:211], v[138:141], v[82:85]
ds_read_b128 v[110:113], v8 offset:13312
v_mfma_f32_16x16x32_bf16 v[82:85], a[216:219], v[142:145], v[82:85]
v_mfma_f32_16x16x32_bf16 v[82:85], a[224:227], v[146:149], v[82:85]
v_mfma_f32_16x16x32_bf16 v[82:85], a[232:235], v[150:153], v[82:85]
ds_read_b128 v[114:117], v8 offset:14336
v_mfma_f32_16x16x32_bf16 v[78:81], a[52:55], v[130:133], v[78:81]
v_mfma_f32_16x16x32_bf16 v[78:81], a[60:63], v[134:137], v[78:81]
)"
R"(v_mfma_f32_16x16x32_bf16 v[78:81], a[68:71], v[138:141], v[78:81]
ds_read_b128 v[118:121], v8 offset:15360
v_mfma_f32_16x16x32_bf16 v[78:81], a[76:79], v[142:145], v[78:81]
v_mfma_f32_16x16x32_bf16 v[78:81], a[84:87], v[146:149], v[78:81]
v_mfma_f32_16x16x32_bf16 v[78:81], a[92:95], v[150:153], v[78:81]
ds_read_b128 v[122:125], v8 offset:16384
v_mfma_f32_16x16x32_bf16 v[86:89], a[196:199], v[130:133], v[86:89]
v_mfma_f32_16x16x32_bf16 v[86:89], a[204:207], v[134:137], v[86:89]
v_mfma_f32_16x16x32_bf16 v[86:89], a[212:215], v[138:141], v[86:89]
ds_read_b128 v[126:129], v8 offset:17408
v_mfma_f32_16x16x32_bf16 v[86:89], a[220:223], v[142:145], v[86:89]
s_xor_b32 s45, s45, 0x10000
v_mfma_f32_16x16x32_bf16 v[86:89], a[228:231], v[146:149], v[86:89]
s_add_u32 m0, s45, 0
v_mfma_f32_16x16x32_bf16 v[86:89], a[236:239], v[150:153], v[86:89]
s_waitcnt lgkmcnt(5)
v_mfma_f32_16x16x32_bf16 v[74:77], a[96:99], v[106:109], v[74:77]
s_barrier
s_waitcnt lgkmcnt(4)
v_mfma_f32_16x16x32_bf16 v[74:77], a[104:107], v[110:113], v[74:77]
s_waitcnt lgkmcnt(3)
global_load_lds_dwordx4 v2, s[38:39] offset:-2048 sc1
v_mfma_f32_16x16x32_bf16 v[74:77], a[112:115], v[114:117], v[74:77]
s_waitcnt lgkmcnt(2)
v_mfma_f32_16x16x32_bf16 v[74:77], a[120:123], v[118:121], v[74:77]
s_waitcnt lgkmcnt(1)
v_mfma_f32_16x16x32_bf16 v[74:77], a[128:131], v[122:125], v[74:77]
s_waitcnt lgkmcnt(0)
global_load_lds_dwordx4 v2, s[38:39] offset:-1024 sc1
v_mfma_f32_16x16x32_bf16 v[74:77], a[136:139], v[126:129], v[74:77]
v_mfma_f32_16x16x32_bf16 v[82:85], v[26:29], v[106:109], v[82:85]
v_mfma_f32_16x16x32_bf16 v[82:85], v[34:37], v[110:113], v[82:85]
global_load_lds_dwordx4 v2, s[38:39] offset:0 sc1
v_mfma_f32_16x16x32_bf16 v[82:85], v[42:45], v[114:117], v[82:85]
v_mfma_f32_16x16x32_bf16 v[82:85], v[50:53], v[118:121], v[82:85]
v_mfma_f32_16x16x32_bf16 v[82:85], v[58:61], v[122:125], v[82:85]
global_load_lds_dwordx4 v2, s[38:39] offset:1024 sc1
v_mfma_f32_16x16x32_bf16 v[82:85], v[66:69], v[126:129], v[82:85]
v_mfma_f32_16x16x32_bf16 v[78:81], a[100:103], v[106:109], v[78:81]
s_waitcnt vmcnt(5)
s_barrier
v_mfma_f32_16x16x32_bf16 v[78:81], a[108:111], v[110:113], v[78:81]
global_load_lds_dwordx4 v2, s[38:39] offset:2048 sc1
v_mfma_f32_16x16x32_bf16 v[78:81], a[116:119], v[114:117], v[78:81]
v_mfma_f32_16x16x32_bf16 v[78:81], a[124:127], v[118:121], v[78:81]
ds_read_b128 v[130:133], v8 offset:18432
v_mfma_f32_16x16x32_bf16 v[78:81], a[132:135], v[122:125], v[78:81]
ds_read_b128 v[134:137], v8 offset:19456
global_load_lds_dwordx4 v2, s[38:39] offset:3072 sc1
v_mfma_f32_16x16x32_bf16 v[78:81], a[140:143], v[126:129], v[78:81]
ds_read_b128 v[138:141], v8 offset:20480
v_add_u32 v2, 18688, v2
v_mfma_f32_16x16x32_bf16 v[86:89], v[30:33], v[106:109], v[86:89]
ds_read_b128 v[142:145], v8 offset:21504
v_min_u32 v2, v2, v9
v_mfma_f32_16x16x32_bf16 v[86:89], v[38:41], v[110:113], v[86:89]
ds_read_b128 v[146:149], v8 offset:22528
v_mul_f32 v154, v74, v82
v_mfma_f32_16x16x32_bf16 v[86:89], v[46:49], v[114:117], v[86:89]
ds_read_b128 v[150:153], v8 offset:23552
v_mul_f32 v155, v75, v83
v_mfma_f32_16x16x32_bf16 v[86:89], v[54:57], v[118:121], v[86:89]
v_mul_f32 v156, v76, v84
v_mul_f32 v157, v77, v85
v_mfma_f32_16x16x32_bf16 v[86:89], v[62:65], v[122:125], v[86:89]
v_cvt_pk_bf16_f32 v162, v154, v155
v_cvt_pk_bf16_f32 v163, v156, v157
v_mfma_f32_16x16x32_bf16 v[86:89], v[70:73], v[126:129], v[86:89]
s_waitcnt lgkmcnt(5)
v_mfma_f32_16x16x32_bf16 v[90:93], a[0:3], v[130:133], 0
s_waitcnt lgkmcnt(4)
v_mfma_f32_16x16x32_bf16 v[90:93], a[8:11], v[134:137], v[90:93]
s_waitcnt lgkmcnt(3)
v_mfma_f32_16x16x32_bf16 v[90:93], a[16:19], v[138:141], v[90:93]
s_waitcnt lgkmcnt(2)
v_mfma_f32_16x16x32_bf16 v[90:93], a[24:27], v[142:145], v[90:93]
s_waitcnt lgkmcnt(1)
v_mul_f32 v158, v78, v86
v_mul_f32 v159, v79, v87
v_mfma_f32_16x16x32_bf16 v[90:93], a[32:35], v[146:149], v[90:93]
s_waitcnt lgkmcnt(0)
v_mul_f32 v160, v80, v88
v_mul_f32 v161, v81, v89
v_mfma_f32_16x16x32_bf16 v[90:93], a[40:43], v[150:153], v[90:93]
ds_read_b128 v[106:109], v8 offset:24576
v_cvt_pk_bf16_f32 v164, v158, v159
v_mfma_f32_16x16x32_bf16 v[98:101], a[144:147], v[130:133], 0
ds_read_b128 v[110:113], v8 offset:25600
v_cvt_pk_bf16_f32 v165, v160, v161
v_mfma_f32_16x16x32_bf16 v[98:101], a[152:155], v[134:137], v[98:101]
v_mfma_f32_16x16x32_bf16 v[98:101], a[160:163], v[138:141], v[98:101]
s_waitcnt vmcnt(6)
s_barrier
v_mfma_f32_16x16x32_bf16 v[98:101], a[168:171], v[142:145], v[98:101]
v_mfma_f32_16x16x32_bf16 v[98:101], a[176:179], v[146:149], v[98:101]
global_store_dwordx4 v3, v[162:165], s[38:39] sc1
v_mfma_f32_16x16x32_bf16 v[98:101], a[184:187], v[150:153], v[98:101]
ds_read_b128 v[114:117], v8 offset:26624
v_add_u32 v3, 147456, v3
v_mfma_f32_16x16x32_bf16 v[94:97], a[4:7], v[130:133], 0
v_mfma_f32_16x16x32_bf16 v[94:97], a[12:15], v[134:137], v[94:97]
v_mfma_f32_16x16x32_bf16 v[94:97], a[20:23], v[138:141], v[94:97]
ds_read_b128 v[118:121], v8 offset:27648
v_mfma_f32_16x16x32_bf16 v[94:97], a[28:31], v[142:145], v[94:97]
v_mfma_f32_16x16x32_bf16 v[94:97], a[36:39], v[146:149], v[94:97]
v_mfma_f32_16x16x32_bf16 v[94:97], a[44:47], v[150:153], v[94:97]
ds_read_b128 v[122:125], v8 offset:28672
v_mfma_f32_16x16x32_bf16 v[102:105], a[148:151], v[130:133], 0
v_mfma_f32_16x16x32_bf16 v[102:105], a[156:159], v[134:137], v[102:105]
v_mfma_f32_16x16x32_bf16 v[102:105], a[164:167], v[138:141], v[102:105]
ds_read_b128 v[126:129], v8 offset:29696
v_mfma_f32_16x16x32_bf16 v[102:105], a[172:175], v[142:145], v[102:105]
v_mfma_f32_16x16x32_bf16 v[102:105], a[180:183], v[146:149], v[102:105]
v_mfma_f32_16x16x32_bf16 v[102:105], a[188:191], v[150:153], v[102:105]
s_waitcnt lgkmcnt(5)
v_mfma_f32_16x16x32_bf16 v[90:93], a[48:51], v[106:109], v[90:93]
s_waitcnt lgkmcnt(4)
v_mfma_f32_16x16x32_bf16 v[90:93], a[56:59], v[110:113], v[90:93]
s_waitcnt lgkmcnt(3)
v_mfma_f32_16x16x32_bf16 v[90:93], a[64:67], v[114:117], v[90:93]
s_waitcnt lgkmcnt(2)
v_mfma_f32_16x16x32_bf16 v[90:93], a[72:75], v[118:121], v[90:93]
s_waitcnt lgkmcnt(1)
v_mfma_f32_16x16x32_bf16 v[90:93], a[80:83], v[122:125], v[90:93]
s_waitcnt lgkmcnt(0)
v_mfma_f32_16x16x32_bf16 v[90:93], a[88:91], v[126:129], v[90:93]
ds_read_b128 v[130:133], v8 offset:30720
v_mfma_f32_16x16x32_bf16 v[98:101], a[192:195], v[106:109], v[98:101]
v_mfma_f32_16x16x32_bf16 v[98:101], a[200:203], v[110:113], v[98:101]
v_mfma_f32_16x16x32_bf16 v[98:101], a[208:211], v[114:117], v[98:101]
ds_read_b128 v[134:137], v8 offset:31744
v_mfma_f32_16x16x32_bf16 v[98:101], a[216:219], v[118:121], v[98:101]
v_mfma_f32_16x16x32_bf16 v[98:101], a[224:227], v[122:125], v[98:101]
v_mfma_f32_16x16x32_bf16 v[98:101], a[232:235], v[126:129], v[98:101]
ds_read_b128 v[138:141], v8 offset:32768
v_mfma_f32_16x16x32_bf16 v[94:97], a[52:55], v[106:109], v[94:97]
v_mfma_f32_16x16x32_bf16 v[94:97], a[60:63], v[110:113], v[94:97]
v_mfma_f32_16x16x32_bf16 v[94:97], a[68:71], v[114:117], v[94:97]
ds_read_b128 v[142:145], v8 offset:33792
v_mfma_f32_16x16x32_bf16 v[94:97], a[76:79], v[118:121], v[94:97]
v_mfma_f32_16x16x32_bf16 v[94:97], a[84:87], v[122:125], v[94:97]
v_mfma_f32_16x16x32_bf16 v[94:97], a[92:95], v[126:129], v[94:97]
ds_read_b128 v[146:149], v8 offset:34816
v_mfma_f32_16x16x32_bf16 v[102:105], a[196:199], v[106:109], v[102:105]
.Lgu_in5:
v_mfma_f32_16x16x32_bf16 v[102:105], a[204:207], v[110:113], v[102:105]
v_mfma_f32_16x16x32_bf16 v[102:105], a[212:215], v[114:117], v[102:105]
ds_read_b128 v[150:153], v8 offset:35840
v_mfma_f32_16x16x32_bf16 v[102:105], a[220:223], v[118:121], v[102:105]
v_mfma_f32_16x16x32_bf16 v[102:105], a[228:231], v[122:125], v[102:105]
s_add_u32 m0, s45, 18432
v_mfma_f32_16x16x32_bf16 v[102:105], a[236:239], v[126:129], v[102:105]
s_waitcnt lgkmcnt(5)
v_mfma_f32_16x16x32_bf16 v[90:93], a[96:99], v[130:133], v[90:93]
s_barrier
s_waitcnt lgkmcnt(4)
v_mfma_f32_16x16x32_bf16 v[90:93], a[104:107], v[134:137], v[90:93]
s_waitcnt lgkmcnt(3)
global_load_lds_dwordx4 v2, s[38:39] offset:-2048 sc1
v_mfma_f32_16x16x32_bf16 v[90:93], a[112:115], v[138:141], v[90:93]
s_waitcnt lgkmcnt(2)
v_mfma_f32_16x16x32_bf16 v[90:93], a[120:123], v[142:145], v[90:93]
s_waitcnt lgkmcnt(1)
v_mfma_f32_16x16x32_bf16 v[90:93], a[128:131], v[146:149], v[90:93]
s_waitcnt lgkmcnt(0)
global_load_lds_dwordx4 v2, s[38:39] offset:-1024 sc1
v_mfma_f32_16x16x32_bf16 v[90:93], a[136:139], v[150:153], v[90:93]
v_mfma_f32_16x16x32_bf16 v[98:101], v[26:29], v[130:133], v[98:101]
v_mfma_f32_16x16x32_bf16 v[98:101], v[34:37], v[134:137], v[98:101]
global_load_lds_dwordx4 v2, s[38:39] offset:0 sc1
v_mfma_f32_16x16x32_bf16 v[98:101], v[42:45], v[138:141], v[98:101]
v_mfma_f32_16x16x32_bf16 v[98:101], v[50:53], v[142:145], v[98:101]
v_xor_b32 v8, 0x10000, v8
v_mfma_f32_16x16x32_bf16 v[98:101], v[58:61], v[146:149], v[98:101]
global_load_lds_dwordx4 v2, s[38:39] offset:1024 sc1
v_mfma_f32_16x16x32_bf16 v[98:101], v[66:69], v[150:153], v[98:101]
v_mfma_f32_16x16x32_bf16 v[94:97], a[100:103], v[130:133], v[94:97]
s_waitcnt vmcnt(5)
s_barrier
v_mfma_f32_16x16x32_bf16 v[94:97], a[108:111], v[134:137], v[94:97]
global_load_lds_dwordx4 v2, s[38:39] offset:2048 sc1
v_mfma_f32_16x16x32_bf16 v[94:97], a[116:119], v[138:141], v[94:97]
v_mfma_f32_16x16x32_bf16 v[94:97], a[124:127], v[142:145], v[94:97]
ds_read_b128 v[106:109], v8 offset:0
v_mfma_f32_16x16x32_bf16 v[94:97], a[132:135], v[146:149], v[94:97]
ds_read_b128 v[110:113], v8 offset:1024
global_load_lds_dwordx4 v2, s[38:39] offset:3072 sc1
v_mfma_f32_16x16x32_bf16 v[94:97], a[140:143], v[150:153], v[94:97]
ds_read_b128 v[114:117], v8 offset:2048
v_add_u32 v2, 18688, v2
v_mfma_f32_16x16x32_bf16 v[102:105], v[30:33], v[130:133], v[102:105]
ds_read_b128 v[118:121], v8 offset:3072
v_min_u32 v2, v2, v9
v_mfma_f32_16x16x32_bf16 v[102:105], v[38:41], v[134:137], v[102:105]
ds_read_b128 v[122:125], v8 offset:4096
v_mul_f32 v154, v90, v98
v_mfma_f32_16x16x32_bf16 v[102:105], v[46:49], v[138:141], v[102:105]
ds_read_b128 v[126:129], v8 offset:5120
v_mul_f32 v155, v91, v99
v_mfma_f32_16x16x32_bf16 v[102:105], v[54:57], v[142:145], v[102:105]
v_mul_f32 v156, v92, v100
v_mul_f32 v157, v93, v101
v_mfma_f32_16x16x32_bf16 v[102:105], v[62:65], v[146:149], v[102:105]
v_mfma_f32_16x16x32_bf16 v[102:105], v[70:73], v[150:153], v[102:105]
s_sub_u32 s70, s70, 1
s_cbranch_scc0 .Lgu_top6
s_waitcnt vmcnt(6) lgkmcnt(0)
s_barrier
v_mul_f32 v154, v90, v98
v_mul_f32 v155, v91, v99
v_mul_f32 v156, v92, v100
v_mul_f32 v157, v93, v101
v_cvt_pk_bf16_f32 v162, v154, v155
v_cvt_pk_bf16_f32 v163, v156, v157
v_mul_f32 v158, v94, v102
v_mul_f32 v159, v95, v103
v_mul_f32 v160, v96, v104
v_mul_f32 v161, v97, v105
v_cvt_pk_bf16_f32 v164, v158, v159
v_cvt_pk_bf16_f32 v165, v160, v161
global_store_dwordx4 v3, v[162:165], s[38:39] sc1
v_mov_b32 v231, 192
ds_read_b32 v231, v231 offset:36864
s_waitcnt vmcnt(0)
s_barrier
s_waitcnt lgkmcnt(0)
v_readfirstlane_b32 s62, v231
s_nop 3
s_max_i32 s62, s62, 2
s_min_i32 s62, s62, 66
s_add_u32 s62, s62, -2
s_branch .Lgu_fin
.Lgu_w2:
v_add_u32 v254, -1, v5
v_add_u32 v255, -1, v5
v_add_lshl_u32 v252, v7, 0, 7
v_bfi_b32 v252, s69, v252, v6
v_add_lshl_u32 v253, v5, 0, 7
v_bfi_b32 v253, s69, v253, v4
s_add_u32 m0, s45, 18432
s_xor_b32 s45, s45, 0x10000
s_mov_b32 m0, s45
s_barrier
global_load_lds_dwordx4 v2, s[38:39] offset:-2048 sc1
global_load_lds_dwordx4 v2, s[38:39] offset:-1024 sc1
global_load_lds_dwordx4 v2, s[38:39] offset:0 sc1
global_load_lds_dwordx4 v2, s[38:39] offset:1024 sc1
global_load_lds_dwordx4 v2, s[38:39] offset:2048 sc1
global_load_lds_dwordx4 v2, s[38:39] offset:3072 sc1
v_add_u32 v2, 18688, v2
s_mov_b32 s70, 32
s_waitcnt vmcnt(0) lgkmcnt(0)
s_nop 7
s_branch .Lgu_in8
.p2align 6
.Lgu_top9:
s_waitcnt lgkmcnt(5)
v_mfma_f32_16x16x32_bf16 v[74:77], a[0:3], v[106:109], 0
s_waitcnt lgkmcnt(4)
v_mul_f32 v157, v93, v101
v_cvt_pk_bf16_f32 v162, v154, v155
v_mfma_f32_16x16x32_bf16 v[74:77], a[8:11], v[110:113], v[74:77]
s_waitcnt lgkmcnt(3)
v_cvt_pk_bf16_f32 v163, v156, v157
v_mfma_f32_16x16x32_bf16 v[74:77], a[16:19], v[114:117], v[74:77]
s_waitcnt lgkmcnt(2)
v_mfma_f32_16x16x32_bf16 v[74:77], a[24:27], v[118:121], v[74:77]
s_waitcnt lgkmcnt(1)
v_mul_f32 v158, v94, v102
v_mul_f32 v159, v95, v103
v_mfma_f32_16x16x32_bf16 v[74:77], a[32:35], v[122:125], v[74:77]
s_waitcnt lgkmcnt(0)
v_mul_f32 v160, v96, v104
v_mul_f32 v161, v97, v105
v_mfma_f32_16x16x32_bf16 v[74:77], a[40:43], v[126:129], v[74:77]
ds_read_b128 v[130:133], v8 offset:6144
v_cvt_pk_bf16_f32 v164, v158, v159
v_mfma_f32_16x16x32_bf16 v[82:85], a[144:147], v[106:109], 0
ds_read_b128 v[134:137], v8 offset:7168
v_cvt_pk_bf16_f32 v165, v160, v161
v_mfma_f32_16x16x32_bf16 v[82:85], a[152:155], v[110:113], v[82:85]
v_mfma_f32_16x16x32_bf16 v[82:85], a[160:163], v[114:117], v[82:85]
s_waitcnt vmcnt(6)
s_barrier
v_mfma_f32_16x16x32_bf16 v[82:85], a[168:171], v[118:121], v[82:85]
s_mov_b64 exec, 1
global_store_dword v252, v7, s[38:39] sc1
s_mov_b64 exec, -1
v_mfma_f32_16x16x32_bf16 v[82:85], a[176:179], v[122:125], v[82:85]
v_add_u32 v7, 1, v7
v_add_lshl_u32 v252, v7, 0, 7
v_mfma_f32_16x16x32_bf16 v[82:85], a[184:187], v[126:129], v[82:85]
ds_read_b128 v[138:141], v8 offset:8192
v_bfi_b32 v252, s69, v252, v6
v_mfma_f32_16x16x32_bf16 v[78:81], a[4:7], v[106:109], 0
global_store_dwordx4 v3, v[162:165], s[38:39] sc1
v_mfma_f32_16x16x32_bf16 v[78:81], a[12:15], v[110:113], v[78:81]
v_add_u32 v3, 147456, v3
v_mfma_f32_16x16x32_bf16 v[78:81], a[20:23], v[114:117], v[78:81]
ds_read_b128 v[142:145], v8 offset:9216
v_mfma_f32_16x16x32_bf16 v[78:81], a[28:31], v[118:121], v[78:81]
v_mfma_f32_16x16x32_bf16 v[78:81], a[36:39], v[122:125], v[78:81]
v_mfma_f32_16x16x32_bf16 v[78:81], a[44:47], v[126:129], v[78:81]
ds_read_b128 v[146:149], v8 offset:10240
v_mfma_f32_16x16x32_bf16 v[86:89], a[148:151], v[106:109], 0
v_mfma_f32_16x16x32_bf16 v[86:89], a[156:159], v[110:113], v[86:89]
v_mfma_f32_16x16x32_bf16 v[86:89], a[164:167], v[114:117], v[86:89]
ds_read_b128 v[150:153], v8 offset:11264
v_mfma_f32_16x16x32_bf16 v[86:89], a[172:175], v[118:121], v[86:89]
v_mfma_f32_16x16x32_bf16 v[86:89], a[180:183], v[122:125], v[86:89]
v_mfma_f32_16x16x32_bf16 v[86:89], a[188:191], v[126:129], v[86:89]
s_waitcnt lgkmcnt(5)
v_mfma_f32_16x16x32_bf16 v[74:77], a[48:51], v[130:133], v[74:77]
s_waitcnt lgkmcnt(4)
v_mfma_f32_16x16x32_bf16 v[74:77], a[56:59], v[134:137], v[74:77]
s_waitcnt lgkmcnt(3)
v_mfma_f32_16x16x32_bf16 v[74:77], a[64:67], v[138:141], v[74:77]
s_waitcnt lgkmcnt(2)
v_mfma_f32_16x16x32_bf16 v[74:77], a[72:75], v[142:145], v[74:77]
s_waitcnt lgkmcnt(1)
v_mfma_f32_16x16x32_bf16 v[74:77], a[80:83], v[146:149], v[74:77]
s_waitcnt lgkmcnt(0)
v_mfma_f32_16x16x32_bf16 v[74:77], a[88:91], v[150:153], v[74:77]
ds_read_b128 v[106:109], v8 offset:12288
v_mfma_f32_16x16x32_bf16 v[82:85], a[192:195], v[130:133], v[82:85]
v_mfma_f32_16x16x32_bf16 v[82:85], a[200:203], v[134:137], v[82:85]
v_mfma_f32_16x16x32_bf16 v[82:85], a[208:211], v[138:141], v[82:85]
ds_read_b128 v[110:113], v8 offset:13312
v_mfma_f32_16x16x32_bf16 v[82:85], a[216:219], v[142:145], v[82:85]
v_mfma_f32_16x16x32_bf16 v[82:85], a[224:227], v[146:149], v[82:85]
v_mfma_f32_16x16x32_bf16 v[82:85], a[232:235], v[150:153], v[82:85]
ds_read_b128 v[114:117], v8 offset:14336
v_mfma_f32_16x16x32_bf16 v[78:81], a[52:55], v[130:133], v[78:81]
v_mfma_f32_16x16x32_bf16 v[78:81], a[60:63], v[134:137], v[78:81]
v_mfma_f32_16x16x32_bf16 v[78:81], a[68:71], v[138:141], v[78:81]
ds_read_b128 v[118:121], v8 offset:15360
v_mfma_f32_16x16x32_bf16 v[78:81], a[76:79], v[142:145], v[78:81]
v_mfma_f32_16x16x32_bf16 v[78:81], a[84:87], v[146:149], v[78:81]
v_mfma_f32_16x16x32_bf16 v[78:81], a[92:95], v[150:153], v[78:81]
ds_read_b128 v[122:125], v8 offset:16384
v_mfma_f32_16x16x32_bf16 v[86:89], a[196:199], v[130:133], v[86:89]
v_mfma_f32_16x16x32_bf16 v[86:89], a[204:207], v[134:137], v[86:89]
v_mfma_f32_16x16x32_bf16 v[86:89], a[212:215], v[138:141], v[86:89]
ds_read_b128 v[126:129], v8 offset:17408
v_mfma_f32_16x16x32_bf16 v[86:89], a[220:223], v[142:145], v[86:89]
s_xor_b32 s45, s45, 0x10000
v_mfma_f32_16x16x32_bf16 v[86:89], a[228:231], v[146:149], v[86:89]
s_add_u32 m0, s45, 0
v_mfma_f32_16x16x32_bf16 v[86:89], a[236:239], v[150:153], v[86:89]
s_waitcnt lgkmcnt(5)
v_mfma_f32_16x16x32_bf16 v[74:77], a[96:99], v[106:109], v[74:77]
s_barrier
s_waitcnt lgkmcnt(4)
v_mfma_f32_16x16x32_bf16 v[74:77], a[104:107], v[110:113], v[74:77]
s_waitcnt lgkmcnt(3)
v_mfma_f32_16x16x32_bf16 v[74:77], a[112:115], v[114:117], v[74:77]
s_waitcnt lgkmcnt(2)
global_load_lds_dwordx4 v2, s[38:39] offset:-2048 sc1
v_mfma_f32_16x16x32_bf16 v[74:77], a[120:123], v[118:121], v[74:77]
s_waitcnt lgkmcnt(1)
v_mfma_f32_16x16x32_bf16 v[74:77], a[128:131], v[122:125], v[74:77]
s_waitcnt lgkmcnt(0)
v_mfma_f32_16x16x32_bf16 v[74:77], a[136:139], v[126:129], v[74:77]
global_load_lds_dwordx4 v2, s[38:39] offset:-1024 sc1
v_mfma_f32_16x16x32_bf16 v[82:85], v[26:29], v[106:109], v[82:85]
v_mfma_f32_16x16x32_bf16 v[82:85], v[34:37], v[110:113], v[82:85]
v_mfma_f32_16x16x32_bf16 v[82:85], v[42:45], v[114:117], v[82:85]
global_load_lds_dwordx4 v2, s[38:39] offset:0 sc1
v_mfma_f32_16x16x32_bf16 v[82:85], v[50:53], v[118:121], v[82:85]
v_mfma_f32_16x16x32_bf16 v[82:85], v[58:61], v[122:125], v[82:85]
v_mfma_f32_16x16x32_bf16 v[82:85], v[66:69], v[126:129], v[82:85]
v_mfma_f32_16x16x32_bf16 v[78:81], a[100:103], v[106:109], v[78:81]
s_waitcnt vmcnt(5)
s_barrier
v_mfma_f32_16x16x32_bf16 v[78:81], a[108:111], v[110:113], v[78:81]
global_load_lds_dwordx4 v2, s[38:39] offset:1024 sc1
v_mfma_f32_16x16x32_bf16 v[78:81], a[116:119], v[114:117], v[78:81]
v_mfma_f32_16x16x32_bf16 v[78:81], a[124:127], v[118:121], v[78:81]
ds_read_b128 v[130:133], v8 offset:18432
v_mfma_f32_16x16x32_bf16 v[78:81], a[132:135], v[122:125], v[78:81]
ds_read_b128 v[134:137], v8 offset:19456
global_load_lds_dwordx4 v2, s[38:39] offset:2048 sc1
v_mfma_f32_16x16x32_bf16 v[78:81], a[140:143], v[126:129], v[78:81]
ds_read_b128 v[138:141], v8 offset:20480
v_mul_f32 v154, v74, v82
v_mfma_f32_16x16x32_bf16 v[86:89], v[30:33], v[106:109], v[86:89]
ds_read_b128 v[142:145], v8 offset:21504
v_mul_f32 v155, v75, v83
v_mfma_f32_16x16x32_bf16 v[86:89], v[38:41], v[110:113], v[86:89]
ds_read_b128 v[146:149], v8 offset:22528
global_load_lds_dwordx4 v2, s[38:39] offset:3072 sc1
v_mfma_f32_16x16x32_bf16 v[86:89], v[46:49], v[114:117], v[86:89]
ds_read_b128 v[150:153], v8 offset:23552
v_add_u32 v2, 18688, v2
v_mfma_f32_16x16x32_bf16 v[86:89], v[54:57], v[118:121], v[86:89]
v_min_u32 v2, v2, v9
v_mul_f32 v156, v76, v84
v_mfma_f32_16x16x32_bf16 v[86:89], v[62:65], v[122:125], v[86:89]
v_mul_f32 v157, v77, v85
v_cvt_pk_bf16_f32 v162, v154, v155
)"
R"(v_mfma_f32_16x16x32_bf16 v[86:89], v[70:73], v[126:129], v[86:89]
s_waitcnt lgkmcnt(5)
v_cvt_pk_bf16_f32 v163, v156, v157
v_mfma_f32_16x16x32_bf16 v[90:93], a[0:3], v[130:133], 0
s_waitcnt lgkmcnt(4)
v_mfma_f32_16x16x32_bf16 v[90:93], a[8:11], v[134:137], v[90:93]
s_waitcnt lgkmcnt(3)
v_mfma_f32_16x16x32_bf16 v[90:93], a[16:19], v[138:141], v[90:93]
s_waitcnt lgkmcnt(2)
v_mfma_f32_16x16x32_bf16 v[90:93], a[24:27], v[142:145], v[90:93]
s_waitcnt lgkmcnt(1)
v_mul_f32 v158, v78, v86
v_mul_f32 v159, v79, v87
v_mfma_f32_16x16x32_bf16 v[90:93], a[32:35], v[146:149], v[90:93]
s_waitcnt lgkmcnt(0)
v_mul_f32 v160, v80, v88
v_mul_f32 v161, v81, v89
v_mfma_f32_16x16x32_bf16 v[90:93], a[40:43], v[150:153], v[90:93]
ds_read_b128 v[106:109], v8 offset:24576
v_cvt_pk_bf16_f32 v164, v158, v159
v_mfma_f32_16x16x32_bf16 v[98:101], a[144:147], v[130:133], 0
ds_read_b128 v[110:113], v8 offset:25600
v_cvt_pk_bf16_f32 v165, v160, v161
v_mfma_f32_16x16x32_bf16 v[98:101], a[152:155], v[134:137], v[98:101]
v_mfma_f32_16x16x32_bf16 v[98:101], a[160:163], v[138:141], v[98:101]
s_waitcnt vmcnt(6)
s_barrier
v_mfma_f32_16x16x32_bf16 v[98:101], a[168:171], v[142:145], v[98:101]
s_mov_b64 exec, 1
global_store_dword v252, v7, s[38:39] sc1
s_mov_b64 exec, -1
v_mfma_f32_16x16x32_bf16 v[98:101], a[176:179], v[146:149], v[98:101]
v_add_u32 v7, 1, v7
v_add_lshl_u32 v252, v7, 0, 7
v_mfma_f32_16x16x32_bf16 v[98:101], a[184:187], v[150:153], v[98:101]
ds_read_b128 v[114:117], v8 offset:26624
v_bfi_b32 v252, s69, v252, v6
v_mfma_f32_16x16x32_bf16 v[94:97], a[4:7], v[130:133], 0
global_store_dwordx4 v3, v[162:165], s[38:39] sc1
v_mfma_f32_16x16x32_bf16 v[94:97], a[12:15], v[134:137], v[94:97]
v_add_u32 v3, 147456, v3
v_mfma_f32_16x16x32_bf16 v[94:97], a[20:23], v[138:141], v[94:97]
ds_read_b128 v[118:121], v8 offset:27648
v_mfma_f32_16x16x32_bf16 v[94:97], a[28:31], v[142:145], v[94:97]
v_mfma_f32_16x16x32_bf16 v[94:97], a[36:39], v[146:149], v[94:97]
v_mfma_f32_16x16x32_bf16 v[94:97], a[44:47], v[150:153], v[94:97]
ds_read_b128 v[122:125], v8 offset:28672
v_mfma_f32_16x16x32_bf16 v[102:105], a[148:151], v[130:133], 0
v_mfma_f32_16x16x32_bf16 v[102:105], a[156:159], v[134:137], v[102:105]
v_mfma_f32_16x16x32_bf16 v[102:105], a[164:167], v[138:141], v[102:105]
ds_read_b128 v[126:129], v8 offset:29696
v_mfma_f32_16x16x32_bf16 v[102:105], a[172:175], v[142:145], v[102:105]
v_mfma_f32_16x16x32_bf16 v[102:105], a[180:183], v[146:149], v[102:105]
v_mfma_f32_16x16x32_bf16 v[102:105], a[188:191], v[150:153], v[102:105]
s_waitcnt lgkmcnt(5)
v_mfma_f32_16x16x32_bf16 v[90:93], a[48:51], v[106:109], v[90:93]
s_waitcnt lgkmcnt(4)
v_mfma_f32_16x16x32_bf16 v[90:93], a[56:59], v[110:113], v[90:93]
s_waitcnt lgkmcnt(3)
v_mfma_f32_16x16x32_bf16 v[90:93], a[64:67], v[114:117], v[90:93]
s_waitcnt lgkmcnt(2)
v_mfma_f32_16x16x32_bf16 v[90:93], a[72:75], v[118:121], v[90:93]
s_waitcnt lgkmcnt(1)
v_mfma_f32_16x16x32_bf16 v[90:93], a[80:83], v[122:125], v[90:93]
s_waitcnt lgkmcnt(0)
v_mfma_f32_16x16x32_bf16 v[90:93], a[88:91], v[126:129], v[90:93]
ds_read_b128 v[130:133], v8 offset:30720
v_mfma_f32_16x16x32_bf16 v[98:101], a[192:195], v[106:109], v[98:101]
v_mfma_f32_16x16x32_bf16 v[98:101], a[200:203], v[110:113], v[98:101]
v_mfma_f32_16x16x32_bf16 v[98:101], a[208:211], v[114:117], v[98:101]
ds_read_b128 v[134:137], v8 offset:31744
v_mfma_f32_16x16x32_bf16 v[98:101], a[216:219], v[118:121], v[98:101]
v_mfma_f32_16x16x32_bf16 v[98:101], a[224:227], v[122:125], v[98:101]
v_mfma_f32_16x16x32_bf16 v[98:101], a[232:235], v[126:129], v[98:101]
ds_read_b128 v[138:141], v8 offset:32768
v_mfma_f32_16x16x32_bf16 v[94:97], a[52:55], v[106:109], v[94:97]
v_mfma_f32_16x16x32_bf16 v[94:97], a[60:63], v[110:113], v[94:97]
v_mfma_f32_16x16x32_bf16 v[94:97], a[68:71], v[114:117], v[94:97]
ds_read_b128 v[142:145], v8 offset:33792
v_mfma_f32_16x16x32_bf16 v[94:97], a[76:79], v[118:121], v[94:97]
v_mfma_f32_16x16x32_bf16 v[94:97], a[84:87], v[122:125], v[94:97]
v_mfma_f32_16x16x32_bf16 v[94:97], a[92:95], v[126:129], v[94:97]
ds_read_b128 v[146:149], v8 offset:34816
v_mfma_f32_16x16x32_bf16 v[102:105], a[196:199], v[106:109], v[102:105]
.Lgu_in8:
v_mfma_f32_16x16x32_bf16 v[102:105], a[204:207], v[110:113], v[102:105]
v_mfma_f32_16x16x32_bf16 v[102:105], a[212:215], v[114:117], v[102:105]
ds_read_b128 v[150:153], v8 offset:35840
v_mfma_f32_16x16x32_bf16 v[102:105], a[220:223], v[118:121], v[102:105]
v_mfma_f32_16x16x32_bf16 v[102:105], a[228:231], v[122:125], v[102:105]
s_add_u32 m0, s45, 18432
v_mfma_f32_16x16x32_bf16 v[102:105], a[236:239], v[126:129], v[102:105]
s_waitcnt lgkmcnt(5)
v_mfma_f32_16x16x32_bf16 v[90:93], a[96:99], v[130:133], v[90:93]
s_barrier
s_waitcnt lgkmcnt(4)
v_mfma_f32_16x16x32_bf16 v[90:93], a[104:107], v[134:137], v[90:93]
s_waitcnt lgkmcnt(3)
v_mfma_f32_16x16x32_bf16 v[90:93], a[112:115], v[138:141], v[90:93]
s_waitcnt lgkmcnt(2)
global_load_lds_dwordx4 v2, s[38:39] offset:-2048 sc1
v_mfma_f32_16x16x32_bf16 v[90:93], a[120:123], v[142:145], v[90:93]
s_waitcnt lgkmcnt(1)
v_mfma_f32_16x16x32_bf16 v[90:93], a[128:131], v[146:149], v[90:93]
s_waitcnt lgkmcnt(0)
v_mfma_f32_16x16x32_bf16 v[90:93], a[136:139], v[150:153], v[90:93]
global_load_lds_dwordx4 v2, s[38:39] offset:-1024 sc1
v_mfma_f32_16x16x32_bf16 v[98:101], v[26:29], v[130:133], v[98:101]
v_mfma_f32_16x16x32_bf16 v[98:101], v[34:37], v[134:137], v[98:101]
v_mfma_f32_16x16x32_bf16 v[98:101], v[42:45], v[138:141], v[98:101]
global_load_lds_dwordx4 v2, s[38:39] offset:0 sc1
v_mfma_f32_16x16x32_bf16 v[98:101], v[50:53], v[142:145], v[98:101]
v_xor_b32 v8, 0x10000, v8
v_mfma_f32_16x16x32_bf16 v[98:101], v[58:61], v[146:149], v[98:101]
v_mfma_f32_16x16x32_bf16 v[98:101], v[66:69], v[150:153], v[98:101]
v_mfma_f32_16x16x32_bf16 v[94:97], a[100:103], v[130:133], v[94:97]
s_waitcnt vmcnt(5)
s_barrier
v_mfma_f32_16x16x32_bf16 v[94:97], a[108:111], v[134:137], v[94:97]
global_load_lds_dwordx4 v2, s[38:39] offset:1024 sc1
v_mfma_f32_16x16x32_bf16 v[94:97], a[116:119], v[138:141], v[94:97]
v_mfma_f32_16x16x32_bf16 v[94:97], a[124:127], v[142:145], v[94:97]
ds_read_b128 v[106:109], v8 offset:0
v_mfma_f32_16x16x32_bf16 v[94:97], a[132:135], v[146:149], v[94:97]
ds_read_b128 v[110:113], v8 offset:1024
global_load_lds_dwordx4 v2, s[38:39] offset:2048 sc1
v_mfma_f32_16x16x32_bf16 v[94:97], a[140:143], v[150:153], v[94:97]
ds_read_b128 v[114:117], v8 offset:2048
v_mul_f32 v154, v90, v98
v_mfma_f32_16x16x32_bf16 v[102:105], v[30:33], v[130:133], v[102:105]
ds_read_b128 v[118:121], v8 offset:3072
v_mul_f32 v155, v91, v99
v_mfma_f32_16x16x32_bf16 v[102:105], v[38:41], v[134:137], v[102:105]
ds_read_b128 v[122:125], v8 offset:4096
global_load_lds_dwordx4 v2, s[38:39] offset:3072 sc1
v_mfma_f32_16x16x32_bf16 v[102:105], v[46:49], v[138:141], v[102:105]
ds_read_b128 v[126:129], v8 offset:5120
v_add_u32 v2, 18688, v2
v_mfma_f32_16x16x32_bf16 v[102:105], v[54:57], v[142:145], v[102:105]
v_min_u32 v2, v2, v9
v_mul_f32 v156, v92, v100
v_mfma_f32_16x16x32_bf16 v[102:105], v[62:65], v[146:149], v[102:105]
v_mfma_f32_16x16x32_bf16 v[102:105], v[70:73], v[150:153], v[102:105]
s_sub_u32 s70, s70, 1
s_cbranch_scc0 .Lgu_top9
s_waitcnt vmcnt(6) lgkmcnt(0)
s_barrier
s_mov_b64 exec, 1
global_store_dword v252, v7, s[38:39] sc1
s_mov_b64 exec, -1
v_mul_f32 v154, v90, v98
v_mul_f32 v155, v91, v99
v_mul_f32 v156, v92, v100
v_mul_f32 v157, v93, v101
v_cvt_pk_bf16_f32 v162, v154, v155
v_cvt_pk_bf16_f32 v163, v156, v157
v_mul_f32 v158, v94, v102
v_mul_f32 v159, v95, v103
v_mul_f32 v160, v96, v104
v_mul_f32 v161, v97, v105
v_cvt_pk_bf16_f32 v164, v158, v159
v_cvt_pk_bf16_f32 v165, v160, v161
global_store_dwordx4 v3, v[162:165], s[38:39] sc1
v_mov_b32 v231, 192
ds_read_b32 v231, v231 offset:36864
s_waitcnt vmcnt(0)
s_barrier
v_add_u32 v7, 8, v7
v_mbcnt_lo_u32_b32 v252, -1, 0
v_mbcnt_hi_u32_b32 v252, -1, v252
v_add_lshl_u32 v252, v252, 0, 7
v_bfi_b32 v252, s69, v252, v6
s_mov_b64 exec, 0xff
global_store_dword v252, v7, s[38:39] sc1
s_mov_b64 exec, -1
s_waitcnt lgkmcnt(0)
v_readfirstlane_b32 s62, v231
s_nop 3
s_max_i32 s62, s62, 2
s_min_i32 s62, s62, 66
s_add_u32 s62, s62, -1
s_branch .Lgu_fin
.Lgu_w3:
v_add_u32 v254, -1, v5
v_add_u32 v255, -1, v5
v_add_lshl_u32 v252, v7, 0, 7
v_bfi_b32 v252, s69, v252, v6
v_add_lshl_u32 v253, v5, 0, 7
v_bfi_b32 v253, s69, v253, v4
s_add_u32 m0, s45, 18432
v_add_lshl_u32 v253, v5, 0, 7
v_bfi_b32 v253, s69, v253, v4
global_load_dword v254, v253, s[38:39] sc1
s_waitcnt vmcnt(0)
v_sub_u32 v254, v254, v5
v_cmp_lt_u32 vcc, 0x7f, v254
s_cbranch_vccnz .Lgu_sp16
.Lgu_rt17:
v_add_u32 v5, 1, v5
s_xor_b32 s45, s45, 0x10000
s_mov_b32 m0, s45
s_barrier
global_load_lds_dwordx4 v2, s[38:39] offset:-2048 sc1
global_load_lds_dwordx4 v2, s[38:39] offset:-1024 sc1
global_load_lds_dwordx4 v2, s[38:39] offset:0 sc1
global_load_lds_dwordx4 v2, s[38:39] offset:1024 sc1
global_load_lds_dwordx4 v2, s[38:39] offset:2048 sc1
global_load_lds_dwordx4 v2, s[38:39] offset:3072 sc1
v_add_u32 v2, 18688, v2
s_mov_b32 s70, 32
s_waitcnt vmcnt(0) lgkmcnt(0)
s_nop 7
s_branch .Lgu_in15
.p2align 6
.Lgu_top18:
s_waitcnt lgkmcnt(5)
v_mfma_f32_16x16x32_bf16 v[74:77], a[0:3], v[106:109], 0
s_waitcnt lgkmcnt(4)
v_mul_f32 v157, v93, v101
v_cvt_pk_bf16_f32 v162, v154, v155
v_mfma_f32_16x16x32_bf16 v[74:77], a[8:11], v[110:113], v[74:77]
s_waitcnt lgkmcnt(3)
v_cvt_pk_bf16_f32 v163, v156, v157
v_mfma_f32_16x16x32_bf16 v[74:77], a[16:19], v[114:117], v[74:77]
s_waitcnt lgkmcnt(2)
global_load_dword v254, v253, s[38:39] sc1
v_mfma_f32_16x16x32_bf16 v[74:77], a[24:27], v[118:121], v[74:77]
s_waitcnt lgkmcnt(1)
v_mul_f32 v158, v94, v102
v_mul_f32 v159, v95, v103
v_mfma_f32_16x16x32_bf16 v[74:77], a[32:35], v[122:125], v[74:77]
s_waitcnt lgkmcnt(0)
v_mul_f32 v160, v96, v104
v_mul_f32 v161, v97, v105
v_mfma_f32_16x16x32_bf16 v[74:77], a[40:43], v[126:129], v[74:77]
ds_read_b128 v[130:133], v8 offset:6144
v_cvt_pk_bf16_f32 v164, v158, v159
v_mfma_f32_16x16x32_bf16 v[82:85], a[144:147], v[106:109], 0
ds_read_b128 v[134:137], v8 offset:7168
v_cvt_pk_bf16_f32 v165, v160, v161
v_mfma_f32_16x16x32_bf16 v[82:85], a[152:155], v[110:113], v[82:85]
v_mfma_f32_16x16x32_bf16 v[82:85], a[160:163], v[114:117], v[82:85]
s_waitcnt vmcnt(7)
s_barrier
v_mfma_f32_16x16x32_bf16 v[82:85], a[168:171], v[118:121], v[82:85]
v_mfma_f32_16x16x32_bf16 v[82:85], a[176:179], v[122:125], v[82:85]
v_mfma_f32_16x16x32_bf16 v[82:85], a[184:187], v[126:129], v[82:85]
ds_read_b128 v[138:141], v8 offset:8192
v_mfma_f32_16x16x32_bf16 v[78:81], a[4:7], v[106:109], 0
global_store_dwordx4 v3, v[162:165], s[38:39] sc1
v_mfma_f32_16x16x32_bf16 v[78:81], a[12:15], v[110:113], v[78:81]
v_add_u32 v3, 147456, v3
v_mfma_f32_16x16x32_bf16 v[78:81], a[20:23], v[114:117], v[78:81]
ds_read_b128 v[142:145], v8 offset:9216
v_mfma_f32_16x16x32_bf16 v[78:81], a[28:31], v[118:121], v[78:81]
v_mfma_f32_16x16x32_bf16 v[78:81], a[36:39], v[122:125], v[78:81]
v_mfma_f32_16x16x32_bf16 v[78:81], a[44:47], v[126:129], v[78:81]
ds_read_b128 v[146:149], v8 offset:10240
v_mfma_f32_16x16x32_bf16 v[86:89], a[148:151], v[106:109], 0
v_mfma_f32_16x16x32_bf16 v[86:89], a[156:159], v[110:113], v[86:89]
v_mfma_f32_16x16x32_bf16 v[86:89], a[164:167], v[114:117], v[86:89]
ds_read_b128 v[150:153], v8 offset:11264
v_mfma_f32_16x16x32_bf16 v[86:89], a[172:175], v[118:121], v[86:89]
v_mfma_f32_16x16x32_bf16 v[86:89], a[180:183], v[122:125], v[86:89]
v_mfma_f32_16x16x32_bf16 v[86:89], a[188:191], v[126:129], v[86:89]
s_waitcnt lgkmcnt(5)
v_mfma_f32_16x16x32_bf16 v[74:77], a[48:51], v[130:133], v[74:77]
s_waitcnt lgkmcnt(4)
v_mfma_f32_16x16x32_bf16 v[74:77], a[56:59], v[134:137], v[74:77]
s_waitcnt lgkmcnt(3)
v_mfma_f32_16x16x32_bf16 v[74:77], a[64:67], v[138:141], v[74:77]
s_waitcnt lgkmcnt(2)
v_mfma_f32_16x16x32_bf16 v[74:77], a[72:75], v[142:145], v[74:77]
s_waitcnt lgkmcnt(1)
v_mfma_f32_16x16x32_bf16 v[74:77], a[80:83], v[146:149], v[74:77]
s_waitcnt lgkmcnt(0)
v_mfma_f32_16x16x32_bf16 v[74:77], a[88:91], v[150:153], v[74:77]
ds_read_b128 v[106:109], v8 offset:12288
v_mfma_f32_16x16x32_bf16 v[82:85], a[192:195], v[130:133], v[82:85]
v_mfma_f32_16x16x32_bf16 v[82:85], a[200:203], v[134:137], v[82:85]
v_mfma_f32_16x16x32_bf16 v[82:85], a[208:211], v[138:141], v[82:85]
ds_read_b128 v[110:113], v8 offset:13312
v_mfma_f32_16x16x32_bf16 v[82:85], a[216:219], v[142:145], v[82:85]
v_mfma_f32_16x16x32_bf16 v[82:85], a[224:227], v[146:149], v[82:85]
v_mfma_f32_16x16x32_bf16 v[82:85], a[232:235], v[150:153], v[82:85]
ds_read_b128 v[114:117], v8 offset:14336
v_mfma_f32_16x16x32_bf16 v[78:81], a[52:55], v[130:133], v[78:81]
v_mfma_f32_16x16x32_bf16 v[78:81], a[60:63], v[134:137], v[78:81]
v_mfma_f32_16x16x32_bf16 v[78:81], a[68:71], v[138:141], v[78:81]
ds_read_b128 v[118:121], v8 offset:15360
v_mfma_f32_16x16x32_bf16 v[78:81], a[76:79], v[142:145], v[78:81]
v_mfma_f32_16x16x32_bf16 v[78:81], a[84:87], v[146:149], v[78:81]
v_mfma_f32_16x16x32_bf16 v[78:81], a[92:95], v[150:153], v[78:81]
ds_read_b128 v[122:125], v8 offset:16384
v_mfma_f32_16x16x32_bf16 v[86:89], a[196:199], v[130:133], v[86:89]
v_mfma_f32_16x16x32_bf16 v[86:89], a[204:207], v[134:137], v[86:89]
s_waitcnt vmcnt(1)
v_sub_u32 v254, v254, v5
v_cmp_lt_u32 vcc, 0x7f, v254
v_mfma_f32_16x16x32_bf16 v[86:89], a[212:215], v[138:141], v[86:89]
ds_read_b128 v[126:129], v8 offset:17408
v_mfma_f32_16x16x32_bf16 v[86:89], a[220:223], v[142:145], v[86:89]
s_xor_b32 s45, s45, 0x10000
v_mfma_f32_16x16x32_bf16 v[86:89], a[228:231], v[146:149], v[86:89]
s_cbranch_vccnz .Lgu_sp11
.Lgu_rt12:
s_add_u32 m0, s45, 0
v_mfma_f32_16x16x32_bf16 v[86:89], a[236:239], v[150:153], v[86:89]
s_waitcnt lgkmcnt(5)
v_mfma_f32_16x16x32_bf16 v[74:77], a[96:99], v[106:109], v[74:77]
s_barrier
s_waitcnt lgkmcnt(4)
v_mfma_f32_16x16x32_bf16 v[74:77], a[104:107], v[110:113], v[74:77]
s_waitcnt lgkmcnt(3)
v_add_u32 v5, 1, v5
v_add_lshl_u32 v253, v5, 0, 7
v_mfma_f32_16x16x32_bf16 v[74:77], a[112:115], v[114:117], v[74:77]
s_waitcnt lgkmcnt(2)
v_bfi_b32 v253, s69, v253, v4
v_mfma_f32_16x16x32_bf16 v[74:77], a[120:123], v[118:121], v[74:77]
s_waitcnt lgkmcnt(1)
global_load_lds_dwordx4 v2, s[38:39] offset:-2048 sc1
v_mfma_f32_16x16x32_bf16 v[74:77], a[128:131], v[122:125], v[74:77]
s_waitcnt lgkmcnt(0)
v_mfma_f32_16x16x32_bf16 v[74:77], a[136:139], v[126:129], v[74:77]
v_mfma_f32_16x16x32_bf16 v[82:85], v[26:29], v[106:109], v[82:85]
global_load_lds_dwordx4 v2, s[38:39] offset:-1024 sc1
v_mfma_f32_16x16x32_bf16 v[82:85], v[34:37], v[110:113], v[82:85]
v_mfma_f32_16x16x32_bf16 v[82:85], v[42:45], v[114:117], v[82:85]
v_mfma_f32_16x16x32_bf16 v[82:85], v[50:53], v[118:121], v[82:85]
global_load_lds_dwordx4 v2, s[38:39] offset:0 sc1
v_mfma_f32_16x16x32_bf16 v[82:85], v[58:61], v[122:125], v[82:85]
v_mfma_f32_16x16x32_bf16 v[82:85], v[66:69], v[126:129], v[82:85]
v_mfma_f32_16x16x32_bf16 v[78:81], a[100:103], v[106:109], v[78:81]
s_barrier
v_mfma_f32_16x16x32_bf16 v[78:81], a[108:111], v[110:113], v[78:81]
global_load_lds_dwordx4 v2, s[38:39] offset:1024 sc1
v_mfma_f32_16x16x32_bf16 v[78:81], a[116:119], v[114:117], v[78:81]
v_mfma_f32_16x16x32_bf16 v[78:81], a[124:127], v[118:121], v[78:81]
ds_read_b128 v[130:133], v8 offset:18432
v_mfma_f32_16x16x32_bf16 v[78:81], a[132:135], v[122:125], v[78:81]
ds_read_b128 v[134:137], v8 offset:19456
global_load_lds_dwordx4 v2, s[38:39] offset:2048 sc1
v_mfma_f32_16x16x32_bf16 v[78:81], a[140:143], v[126:129], v[78:81]
ds_read_b128 v[138:141], v8 offset:20480
v_mul_f32 v154, v74, v82
v_mfma_f32_16x16x32_bf16 v[86:89], v[30:33], v[106:109], v[86:89]
ds_read_b128 v[142:145], v8 offset:21504
v_mul_f32 v155, v75, v83
v_mfma_f32_16x16x32_bf16 v[86:89], v[38:41], v[110:113], v[86:89]
ds_read_b128 v[146:149], v8 offset:22528
global_load_lds_dwordx4 v2, s[38:39] offset:3072 sc1
v_mfma_f32_16x16x32_bf16 v[86:89], v[46:49], v[114:117], v[86:89]
ds_read_b128 v[150:153], v8 offset:23552
v_add_u32 v2, 18688, v2
v_mfma_f32_16x16x32_bf16 v[86:89], v[54:57], v[118:121], v[86:89]
v_min_u32 v2, v2, v9
v_mul_f32 v156, v76, v84
v_mfma_f32_16x16x32_bf16 v[86:89], v[62:65], v[122:125], v[86:89]
v_mul_f32 v157, v77, v85
v_cvt_pk_bf16_f32 v162, v154, v155
v_mfma_f32_16x16x32_bf16 v[86:89], v[70:73], v[126:129], v[86:89]
s_waitcnt lgkmcnt(5)
v_cvt_pk_bf16_f32 v163, v156, v157
v_mfma_f32_16x16x32_bf16 v[90:93], a[0:3], v[130:133], 0
s_waitcnt lgkmcnt(4)
v_mfma_f32_16x16x32_bf16 v[90:93], a[8:11], v[134:137], v[90:93]
s_waitcnt lgkmcnt(3)
v_mfma_f32_16x16x32_bf16 v[90:93], a[16:19], v[138:141], v[90:93]
s_waitcnt lgkmcnt(2)
global_load_dword v255, v253, s[38:39] sc1
v_mfma_f32_16x16x32_bf16 v[90:93], a[24:27], v[142:145], v[90:93]
s_waitcnt lgkmcnt(1)
v_mul_f32 v158, v78, v86
v_mul_f32 v159, v79, v87
v_mfma_f32_16x16x32_bf16 v[90:93], a[32:35], v[146:149], v[90:93]
s_waitcnt lgkmcnt(0)
v_mul_f32 v160, v80, v88
v_mul_f32 v161, v81, v89
v_mfma_f32_16x16x32_bf16 v[90:93], a[40:43], v[150:153], v[90:93]
ds_read_b128 v[106:109], v8 offset:24576
v_cvt_pk_bf16_f32 v164, v158, v159
v_mfma_f32_16x16x32_bf16 v[98:101], a[144:147], v[130:133], 0
ds_read_b128 v[110:113], v8 offset:25600
v_cvt_pk_bf16_f32 v165, v160, v161
v_mfma_f32_16x16x32_bf16 v[98:101], a[152:155], v[134:137], v[98:101]
v_mfma_f32_16x16x32_bf16 v[98:101], a[160:163], v[138:141], v[98:101]
s_waitcnt vmcnt(7)
s_barrier
v_mfma_f32_16x16x32_bf16 v[98:101], a[168:171], v[142:145], v[98:101]
v_mfma_f32_16x16x32_bf16 v[98:101], a[176:179], v[146:149], v[98:101]
v_mfma_f32_16x16x32_bf16 v[98:101], a[184:187], v[150:153], v[98:101]
ds_read_b128 v[114:117], v8 offset:26624
v_mfma_f32_16x16x32_bf16 v[94:97], a[4:7], v[130:133], 0
global_store_dwordx4 v3, v[162:165], s[38:39] sc1
v_mfma_f32_16x16x32_bf16 v[94:97], a[12:15], v[134:137], v[94:97]
v_add_u32 v3, 147456, v3
v_mfma_f32_16x16x32_bf16 v[94:97], a[20:23], v[138:141], v[94:97]
ds_read_b128 v[118:121], v8 offset:27648
v_mfma_f32_16x16x32_bf16 v[94:97], a[28:31], v[142:145], v[94:97]
)"
R"(v_mfma_f32_16x16x32_bf16 v[94:97], a[36:39], v[146:149], v[94:97]
v_mfma_f32_16x16x32_bf16 v[94:97], a[44:47], v[150:153], v[94:97]
ds_read_b128 v[122:125], v8 offset:28672
v_mfma_f32_16x16x32_bf16 v[102:105], a[148:151], v[130:133], 0
v_mfma_f32_16x16x32_bf16 v[102:105], a[156:159], v[134:137], v[102:105]
v_mfma_f32_16x16x32_bf16 v[102:105], a[164:167], v[138:141], v[102:105]
ds_read_b128 v[126:129], v8 offset:29696
v_mfma_f32_16x16x32_bf16 v[102:105], a[172:175], v[142:145], v[102:105]
v_mfma_f32_16x16x32_bf16 v[102:105], a[180:183], v[146:149], v[102:105]
v_mfma_f32_16x16x32_bf16 v[102:105], a[188:191], v[150:153], v[102:105]
s_waitcnt lgkmcnt(5)
v_mfma_f32_16x16x32_bf16 v[90:93], a[48:51], v[106:109], v[90:93]
s_waitcnt lgkmcnt(4)
v_mfma_f32_16x16x32_bf16 v[90:93], a[56:59], v[110:113], v[90:93]
s_waitcnt lgkmcnt(3)
v_mfma_f32_16x16x32_bf16 v[90:93], a[64:67], v[114:117], v[90:93]
s_waitcnt lgkmcnt(2)
v_mfma_f32_16x16x32_bf16 v[90:93], a[72:75], v[118:121], v[90:93]
s_waitcnt lgkmcnt(1)
v_mfma_f32_16x16x32_bf16 v[90:93], a[80:83], v[122:125], v[90:93]
s_waitcnt lgkmcnt(0)
v_mfma_f32_16x16x32_bf16 v[90:93], a[88:91], v[126:129], v[90:93]
ds_read_b128 v[130:133], v8 offset:30720
v_mfma_f32_16x16x32_bf16 v[98:101], a[192:195], v[106:109], v[98:101]
v_mfma_f32_16x16x32_bf16 v[98:101], a[200:203], v[110:113], v[98:101]
v_mfma_f32_16x16x32_bf16 v[98:101], a[208:211], v[114:117], v[98:101]
ds_read_b128 v[134:137], v8 offset:31744
v_mfma_f32_16x16x32_bf16 v[98:101], a[216:219], v[118:121], v[98:101]
v_mfma_f32_16x16x32_bf16 v[98:101], a[224:227], v[122:125], v[98:101]
v_mfma_f32_16x16x32_bf16 v[98:101], a[232:235], v[126:129], v[98:101]
ds_read_b128 v[138:141], v8 offset:32768
v_mfma_f32_16x16x32_bf16 v[94:97], a[52:55], v[106:109], v[94:97]
v_mfma_f32_16x16x32_bf16 v[94:97], a[60:63], v[110:113], v[94:97]
v_mfma_f32_16x16x32_bf16 v[94:97], a[68:71], v[114:117], v[94:97]
ds_read_b128 v[142:145], v8 offset:33792
v_mfma_f32_16x16x32_bf16 v[94:97], a[76:79], v[118:121], v[94:97]
v_mfma_f32_16x16x32_bf16 v[94:97], a[84:87], v[122:125], v[94:97]
v_mfma_f32_16x16x32_bf16 v[94:97], a[92:95], v[126:129], v[94:97]
ds_read_b128 v[146:149], v8 offset:34816
v_mfma_f32_16x16x32_bf16 v[102:105], a[196:199], v[106:109], v[102:105]
.Lgu_in15:
v_mfma_f32_16x16x32_bf16 v[102:105], a[204:207], v[110:113], v[102:105]
s_waitcnt vmcnt(1)
v_sub_u32 v255, v255, v5
v_cmp_lt_u32 vcc, 0x7f, v255
v_mfma_f32_16x16x32_bf16 v[102:105], a[212:215], v[114:117], v[102:105]
ds_read_b128 v[150:153], v8 offset:35840
v_mfma_f32_16x16x32_bf16 v[102:105], a[220:223], v[118:121], v[102:105]
v_mfma_f32_16x16x32_bf16 v[102:105], a[228:231], v[122:125], v[102:105]
s_cbranch_vccnz .Lgu_sp13
.Lgu_rt14:
s_add_u32 m0, s45, 18432
v_mfma_f32_16x16x32_bf16 v[102:105], a[236:239], v[126:129], v[102:105]
s_waitcnt lgkmcnt(5)
v_mfma_f32_16x16x32_bf16 v[90:93], a[96:99], v[130:133], v[90:93]
s_barrier
s_waitcnt lgkmcnt(4)
v_mfma_f32_16x16x32_bf16 v[90:93], a[104:107], v[134:137], v[90:93]
s_waitcnt lgkmcnt(3)
v_add_u32 v5, 1, v5
v_add_lshl_u32 v253, v5, 0, 7
v_mfma_f32_16x16x32_bf16 v[90:93], a[112:115], v[138:141], v[90:93]
s_waitcnt lgkmcnt(2)
v_bfi_b32 v253, s69, v253, v4
v_mfma_f32_16x16x32_bf16 v[90:93], a[120:123], v[142:145], v[90:93]
s_waitcnt lgkmcnt(1)
global_load_lds_dwordx4 v2, s[38:39] offset:-2048 sc1
v_mfma_f32_16x16x32_bf16 v[90:93], a[128:131], v[146:149], v[90:93]
s_waitcnt lgkmcnt(0)
v_mfma_f32_16x16x32_bf16 v[90:93], a[136:139], v[150:153], v[90:93]
v_mfma_f32_16x16x32_bf16 v[98:101], v[26:29], v[130:133], v[98:101]
global_load_lds_dwordx4 v2, s[38:39] offset:-1024 sc1
v_mfma_f32_16x16x32_bf16 v[98:101], v[34:37], v[134:137], v[98:101]
v_mfma_f32_16x16x32_bf16 v[98:101], v[42:45], v[138:141], v[98:101]
v_mfma_f32_16x16x32_bf16 v[98:101], v[50:53], v[142:145], v[98:101]
v_xor_b32 v8, 0x10000, v8
global_load_lds_dwordx4 v2, s[38:39] offset:0 sc1
v_mfma_f32_16x16x32_bf16 v[98:101], v[58:61], v[146:149], v[98:101]
v_mfma_f32_16x16x32_bf16 v[98:101], v[66:69], v[150:153], v[98:101]
v_mfma_f32_16x16x32_bf16 v[94:97], a[100:103], v[130:133], v[94:97]
s_barrier
v_mfma_f32_16x16x32_bf16 v[94:97], a[108:111], v[134:137], v[94:97]
global_load_lds_dwordx4 v2, s[38:39] offset:1024 sc1
v_mfma_f32_16x16x32_bf16 v[94:97], a[116:119], v[138:141], v[94:97]
v_mfma_f32_16x16x32_bf16 v[94:97], a[124:127], v[142:145], v[94:97]
ds_read_b128 v[106:109], v8 offset:0
v_mfma_f32_16x16x32_bf16 v[94:97], a[132:135], v[146:149], v[94:97]
ds_read_b128 v[110:113], v8 offset:1024
global_load_lds_dwordx4 v2, s[38:39] offset:2048 sc1
v_mfma_f32_16x16x32_bf16 v[94:97], a[140:143], v[150:153], v[94:97]
ds_read_b128 v[114:117], v8 offset:2048
v_mul_f32 v154, v90, v98
v_mfma_f32_16x16x32_bf16 v[102:105], v[30:33], v[130:133], v[102:105]
ds_read_b128 v[118:121], v8 offset:3072
v_mul_f32 v155, v91, v99
v_mfma_f32_16x16x32_bf16 v[102:105], v[38:41], v[134:137], v[102:105]
ds_read_b128 v[122:125], v8 offset:4096
global_load_lds_dwordx4 v2, s[38:39] offset:3072 sc1
v_mfma_f32_16x16x32_bf16 v[102:105], v[46:49], v[138:141], v[102:105]
ds_read_b128 v[126:129], v8 offset:5120
v_add_u32 v2, 18688, v2
v_mfma_f32_16x16x32_bf16 v[102:105], v[54:57], v[142:145], v[102:105]
v_min_u32 v2, v2, v9
v_mul_f32 v156, v92, v100
v_mfma_f32_16x16x32_bf16 v[102:105], v[62:65], v[146:149], v[102:105]
v_mfma_f32_16x16x32_bf16 v[102:105], v[70:73], v[150:153], v[102:105]
s_sub_u32 s70, s70, 1
s_cbranch_scc0 .Lgu_top18
s_waitcnt vmcnt(6) lgkmcnt(0)
s_barrier
v_mul_f32 v154, v90, v98
v_mul_f32 v155, v91, v99
v_mul_f32 v156, v92, v100
v_mul_f32 v157, v93, v101
v_cvt_pk_bf16_f32 v162, v154, v155
v_cvt_pk_bf16_f32 v163, v156, v157
v_mul_f32 v158, v94, v102
v_mul_f32 v159, v95, v103
v_mul_f32 v160, v96, v104
v_mul_f32 v161, v97, v105
v_cvt_pk_bf16_f32 v164, v158, v159
v_cvt_pk_bf16_f32 v165, v160, v161
global_store_dwordx4 v3, v[162:165], s[38:39] sc1
v_mov_b32 v231, 192
ds_read_b32 v231, v231 offset:36864
s_waitcnt vmcnt(0)
s_barrier
s_waitcnt lgkmcnt(0)
v_readfirstlane_b32 s62, v231
s_nop 3
s_max_i32 s62, s62, 2
s_min_i32 s62, s62, 66
s_add_u32 s62, s62, 0
s_branch .Lgu_fin
.Lgu_w4:
v_mov_b32 v206, -1
v_mov_b32 v207, -1
v_mov_b32 v208, -1
v_mov_b32 v209, -1
v_add_u32 v232, 6856704, v10
global_store_dwordx4 v232, v[206:209], s[38:39] offset:0 sc1
global_store_dwordx4 v232, v[206:209], s[38:39] offset:1024 sc1
global_store_dwordx4 v232, v[206:209], s[38:39] offset:2048 sc1
v_add_u32 v232, 6967296, v10
global_store_dwordx4 v232, v[206:209], s[38:39] offset:0 sc1
global_store_dwordx4 v232, v[206:209], s[38:39] offset:1024 sc1
global_store_dwordx4 v232, v[206:209], s[38:39] offset:2048 sc1
s_nop 1
s_add_u32 s84, s54, 18688
s_addc_u32 s85, s55, 0
s_add_u32 s86, s52, 110592
s_addc_u32 s87, s53, 0
s_mov_b32 s80, 0
s_mov_b64 s[76:77], 0
s_mov_b32 s72, -14
v_add_u32 v230, -1, v17
v_add_u32 v226, -1, s37
v_add_u32 v227, -1, s37
v_add_u32 v228, -1, s37
v_add_u32 v229, -1, s37
v_add_lshl_u32 v252, v228, 0, 7
v_bfi_b32 v252, s69, v252, v18
v_add_lshl_u32 v253, v17, 0, 7
v_bfi_b32 v253, s69, v253, v16
v_add_u32 v254, -1, v5
v_add_u32 v255, -1, v5
s_add_u32 m0, s45, 18432
s_xor_b32 s45, s45, 0x10000
s_mov_b32 m0, s45
s_barrier
v_add_u32 v2, 18688, v2
s_mov_b32 s70, 32
s_waitcnt vmcnt(0) lgkmcnt(0)
s_nop 7
s_branch .Lgu_in24
.p2align 6
.Lgu_top25:
s_waitcnt lgkmcnt(5)
v_mfma_f32_16x16x32_bf16 v[74:77], a[0:3], v[106:109], 0
s_waitcnt lgkmcnt(4)
global_load_dword v230, v253, s[38:39] sc1
v_lshlrev_b32 v216, 16, v187
v_mfma_f32_16x16x32_bf16 v[74:77], a[8:11], v[110:113], v[74:77]
s_waitcnt lgkmcnt(3)
v_and_b32 v217, 0xffff0000, v187
v_add_f32_dpp v206, v210, v206 row_ror:8 row_mask:0xf bank_mask:0xf
v_mfma_f32_16x16x32_bf16 v[74:77], a[16:19], v[114:117], v[74:77]
s_waitcnt lgkmcnt(2)
v_add_f32_dpp v207, v211, v207 row_ror:8 row_mask:0xf bank_mask:0xf
v_add_f32_dpp v208, v212, v208 row_ror:8 row_mask:0xf bank_mask:0xf
v_mfma_f32_16x16x32_bf16 v[74:77], a[24:27], v[118:121], v[74:77]
s_waitcnt lgkmcnt(1)
v_mul_f32 v158, v94, v102
v_mul_f32 v159, v95, v103
v_mfma_f32_16x16x32_bf16 v[74:77], a[32:35], v[122:125], v[74:77]
s_waitcnt lgkmcnt(0)
v_mul_f32 v160, v96, v104
v_mul_f32 v161, v97, v105
v_mfma_f32_16x16x32_bf16 v[74:77], a[40:43], v[126:129], v[74:77]
ds_read_b128 v[130:133], v8 offset:6144
v_cvt_pk_bf16_f32 v164, v158, v159
v_mfma_f32_16x16x32_bf16 v[82:85], a[144:147], v[106:109], 0
ds_read_b128 v[134:137], v8 offset:7168
v_cvt_pk_bf16_f32 v165, v160, v161
v_mfma_f32_16x16x32_bf16 v[82:85], a[152:155], v[110:113], v[82:85]
v_mfma_f32_16x16x32_bf16 v[82:85], a[160:163], v[114:117], v[82:85]
s_waitcnt vmcnt(8)
s_barrier
v_mfma_f32_16x16x32_bf16 v[82:85], a[168:171], v[118:121], v[82:85]
v_add_f32_dpp v209, v213, v209 row_ror:8 row_mask:0xf bank_mask:0xf
v_fma_f32 v206, v206, v231, v214
v_mfma_f32_16x16x32_bf16 v[82:85], a[176:179], v[122:125], v[82:85]
v_fma_f32 v207, v207, v231, v215
v_fma_f32 v208, v208, v231, v216
v_mfma_f32_16x16x32_bf16 v[82:85], a[184:187], v[126:129], v[82:85]
ds_read_b128 v[138:141], v8 offset:8192
v_fma_f32 v209, v209, v231, v217
v_mfma_f32_16x16x32_bf16 v[78:81], a[4:7], v[106:109], 0
v_cvt_pk_bf16_f32 v222, v206, v207
v_cvt_pk_bf16_f32 v223, v208, v209
v_mfma_f32_16x16x32_bf16 v[78:81], a[12:15], v[110:113], v[78:81]
global_store_dwordx4 v3, v[162:165], s[38:39] sc1
v_mfma_f32_16x16x32_bf16 v[78:81], a[20:23], v[114:117], v[78:81]
ds_read_b128 v[142:145], v8 offset:9216
v_add_u32 v3, 147456, v3
v_mfma_f32_16x16x32_bf16 v[78:81], a[28:31], v[118:121], v[78:81]
ds_write_b64 v14, v[222:223]
v_mfma_f32_16x16x32_bf16 v[78:81], a[36:39], v[122:125], v[78:81]
s_waitcnt vmcnt(8)
ds_read_b64 v[224:225], v15
s_mov_b64 exec, 1
global_store_dword v252, v229, s[38:39] sc1
s_mov_b64 exec, -1
v_mfma_f32_16x16x32_bf16 v[78:81], a[44:47], v[126:129], v[78:81]
ds_read_b128 v[146:149], v8 offset:10240
v_mfma_f32_16x16x32_bf16 v[86:89], a[148:151], v[106:109], 0
v_mfma_f32_16x16x32_bf16 v[86:89], a[156:159], v[110:113], v[86:89]
v_mfma_f32_16x16x32_bf16 v[86:89], a[164:167], v[114:117], v[86:89]
ds_read_b128 v[150:153], v8 offset:11264
v_mfma_f32_16x16x32_bf16 v[86:89], a[172:175], v[118:121], v[86:89]
v_mfma_f32_16x16x32_bf16 v[86:89], a[180:183], v[122:125], v[86:89]
v_mfma_f32_16x16x32_bf16 v[86:89], a[188:191], v[126:129], v[86:89]
s_waitcnt lgkmcnt(2)
global_store_dwordx2 v13, v[224:225], s[58:59] sc1
v_mov_b32 v228, v226
v_mfma_f32_16x16x32_bf16 v[74:77], a[48:51], v[130:133], v[74:77]
v_mfma_f32_16x16x32_bf16 v[74:77], a[56:59], v[134:137], v[74:77]
v_mfma_f32_16x16x32_bf16 v[74:77], a[64:67], v[138:141], v[74:77]
v_mfma_f32_16x16x32_bf16 v[74:77], a[72:75], v[142:145], v[74:77]
s_waitcnt vmcnt(8)
v_add_f32 v200, v200, v202
s_waitcnt vmcnt(7)
v_lshlrev_b32 v206, 16, v188
v_add_f32 v201, v201, v203
s_waitcnt lgkmcnt(1)
v_mfma_f32_16x16x32_bf16 v[74:77], a[80:83], v[146:149], v[74:77]
s_waitcnt lgkmcnt(0)
v_and_b32 v207, 0xffff0000, v188
v_mul_f32 v200, 0x3ae38e39, v200
v_lshlrev_b32 v208, 16, v189
v_mfma_f32_16x16x32_bf16 v[74:77], a[88:91], v[150:153], v[74:77]
ds_read_b128 v[106:109], v8 offset:12288
v_mul_f32 v201, 0x3ae38e39, v201
v_and_b32 v209, 0xffff0000, v189
v_mfma_f32_16x16x32_bf16 v[82:85], a[192:195], v[130:133], v[82:85]
v_fma_f32 v232, -v200, v200, v201
v_lshlrev_b32 v210, 16, v190
v_max_f32 v232, 0, v232
v_mfma_f32_16x16x32_bf16 v[82:85], a[200:203], v[134:137], v[82:85]
v_and_b32 v211, 0xffff0000, v190
v_add_f32 v232, 0x3727c5ac, v232
v_mfma_f32_16x16x32_bf16 v[82:85], a[208:211], v[138:141], v[82:85]
ds_read_b128 v[110:113], v8 offset:13312
v_lshlrev_b32 v212, 16, v191
v_mfma_f32_16x16x32_bf16 v[82:85], a[216:219], v[142:145], v[82:85]
s_waitcnt vmcnt(3)
v_sub_u32 v230, v230, v17
v_cmp_lt_u32 vcc, 0x7f, v230
v_mfma_f32_16x16x32_bf16 v[82:85], a[224:227], v[146:149], v[82:85]
v_rcp_f32 v231, v232
v_mfma_f32_16x16x32_bf16 v[82:85], a[232:235], v[150:153], v[82:85]
ds_read_b128 v[114:117], v8 offset:14336
v_and_b32 v213, 0xffff0000, v191
v_mfma_f32_16x16x32_bf16 v[78:81], a[52:55], v[130:133], v[78:81]
s_and_b64 s[88:89], vcc, s[76:77]
s_cbranch_scc1 .Lgu_sp20
.Lgu_rt21:
v_mfma_f32_16x16x32_bf16 v[78:81], a[60:63], v[134:137], v[78:81]
s_cmp_eq_u64 vcc, 0
v_lshlrev_b32 v214, 16, v192
v_and_b32 v215, 0xffff0000, v192
v_mfma_f32_16x16x32_bf16 v[78:81], a[68:71], v[138:141], v[78:81]
s_cselect_b64 s[54:55], s[84:85], s[54:55]
ds_read_b128 v[118:121], v8 offset:15360
v_lshlrev_b32 v216, 16, v193
v_mfma_f32_16x16x32_bf16 v[78:81], a[76:79], v[142:145], v[78:81]
s_cselect_b64 s[52:53], s[86:87], s[52:53]
global_load_dwordx2 v[182:183], v12, s[54:55] offset:0 sc1
v_and_b32 v217, 0xffff0000, v193
v_mfma_f32_16x16x32_bf16 v[78:81], a[84:87], v[146:149], v[78:81]
s_cselect_b64 s[58:59], s[56:57], s[50:51]
global_load_dwordx2 v[184:185], v12, s[54:55] offset:128 sc1
v_lshlrev_b32 v218, 16, v194
v_mfma_f32_16x16x32_bf16 v[78:81], a[92:95], v[150:153], v[78:81]
s_cselect_b32 s82, 1, 0
ds_read_b128 v[122:125], v8 offset:16384
global_load_dwordx4 v[170:173], v10, s[52:53] offset:0 sc1
v_mfma_f32_16x16x32_bf16 v[86:89], a[196:199], v[130:133], v[86:89]
s_add_u32 s62, s62, s82
global_load_dwordx4 v[174:177], v10, s[52:53] offset:1024 sc1
v_and_b32 v219, 0xffff0000, v194
v_mfma_f32_16x16x32_bf16 v[86:89], a[204:207], v[134:137], v[86:89]
s_add_u32 s83, s62, s37
global_load_dwordx4 v[178:181], v10, s[52:53] offset:2048 sc1
v_add_u32 v17, s82, v17
v_add_u32 v226, -1, s83
v_mfma_f32_16x16x32_bf16 v[86:89], a[212:215], v[138:141], v[86:89]
s_cmp_lt_i32 s62, 32
ds_read_b128 v[126:129], v8 offset:17408
global_load_dwordx2 v[186:187], v11, s[54:55] sc1
v_add_lshl_u32 v253, v17, 0, 7
v_mfma_f32_16x16x32_bf16 v[86:89], a[220:223], v[142:145], v[86:89]
s_cselect_b64 s[56:57], s[46:47], s[48:49]
v_bfi_b32 v253, s69, v253, v16
v_lshlrev_b32 v220, 16, v195
v_mfma_f32_16x16x32_bf16 v[86:89], a[228:231], v[146:149], v[86:89]
s_mul_i32 s80, s62, 18432
v_and_b32 v221, 0xffff0000, v195
v_add_f32 v206, v206, v214
v_mfma_f32_16x16x32_bf16 v[86:89], a[236:239], v[150:153], v[86:89]
s_waitcnt lgkmcnt(5)
v_mfma_f32_16x16x32_bf16 v[74:77], a[96:99], v[106:109], v[74:77]
s_barrier
s_waitcnt lgkmcnt(4)
v_mfma_f32_16x16x32_bf16 v[74:77], a[104:107], v[110:113], v[74:77]
s_waitcnt lgkmcnt(3)
v_add_f32 v207, v207, v215
v_add_f32 v208, v208, v216
v_mfma_f32_16x16x32_bf16 v[74:77], a[112:115], v[114:117], v[74:77]
s_waitcnt lgkmcnt(2)
v_add_f32 v209, v209, v217
v_add_f32 v210, v210, v218
v_mfma_f32_16x16x32_bf16 v[74:77], a[120:123], v[118:121], v[74:77]
s_waitcnt lgkmcnt(1)
v_add_f32 v211, v211, v219
v_add_f32 v212, v212, v220
v_mfma_f32_16x16x32_bf16 v[74:77], a[128:131], v[122:125], v[74:77]
s_waitcnt lgkmcnt(0)
v_add_f32 v213, v213, v221
v_lshlrev_b32 v214, 16, v196
v_mfma_f32_16x16x32_bf16 v[74:77], a[136:139], v[126:129], v[74:77]
s_sub_u32 s56, s56, s80
v_and_b32 v215, 0xffff0000, v196
v_lshlrev_b32 v216, 16, v197
v_mfma_f32_16x16x32_bf16 v[82:85], v[26:29], v[106:109], v[82:85]
s_subb_u32 s57, s57, 0
v_and_b32 v217, 0xffff0000, v197
v_lshlrev_b32 v218, 16, v198
v_mfma_f32_16x16x32_bf16 v[82:85], v[34:37], v[110:113], v[82:85]
s_add_u32 s84, s54, 18688
v_and_b32 v219, 0xffff0000, v198
v_lshlrev_b32 v220, 16, v199
v_mfma_f32_16x16x32_bf16 v[82:85], v[42:45], v[114:117], v[82:85]
s_addc_u32 s85, s55, 0
v_and_b32 v221, 0xffff0000, v199
v_add_f32 v206, v206, v214
v_mfma_f32_16x16x32_bf16 v[82:85], v[50:53], v[118:121], v[82:85]
s_add_u32 s86, s52, 110592
v_add_f32 v207, v207, v215
v_add_f32 v208, v208, v216
v_mfma_f32_16x16x32_bf16 v[82:85], v[58:61], v[122:125], v[82:85]
s_addc_u32 s87, s53, 0
v_add_f32 v209, v209, v217
v_add_f32 v210, v210, v218
v_mfma_f32_16x16x32_bf16 v[82:85], v[66:69], v[126:129], v[82:85]
v_mfma_f32_16x16x32_bf16 v[78:81], a[100:103], v[106:109], v[78:81]
s_barrier
v_add_lshl_u32 v252, v228, 0, 7
v_mfma_f32_16x16x32_bf16 v[78:81], a[108:111], v[110:113], v[78:81]
s_add_u32 s72, s72, 1
v_bfi_b32 v252, s69, v252, v18
v_add_f32 v211, v211, v219
v_mfma_f32_16x16x32_bf16 v[78:81], a[116:119], v[114:117], v[78:81]
s_cmp_le_i32 s62, s72
v_add_f32 v212, v212, v220
v_add_f32 v213, v213, v221
v_mfma_f32_16x16x32_bf16 v[78:81], a[124:127], v[118:121], v[78:81]
s_cselect_b64 s[76:77], -1, 0
ds_read_b128 v[130:133], v8 offset:18432
v_lshlrev_b32 v214, 16, v204
v_mfma_f32_16x16x32_bf16 v[78:81], a[132:135], v[122:125], v[78:81]
ds_read_b128 v[134:137], v8 offset:19456
v_mul_f32 v154, v74, v82
v_mfma_f32_16x16x32_bf16 v[78:81], a[140:143], v[126:129], v[78:81]
ds_read_b128 v[138:141], v8 offset:20480
v_mul_f32 v155, v75, v83
v_mfma_f32_16x16x32_bf16 v[86:89], v[30:33], v[106:109], v[86:89]
ds_read_b128 v[142:145], v8 offset:21504
v_mul_f32 v156, v76, v84
v_mfma_f32_16x16x32_bf16 v[86:89], v[38:41], v[110:113], v[86:89]
)"
R"(ds_read_b128 v[146:149], v8 offset:22528
v_mul_f32 v157, v77, v85
v_mfma_f32_16x16x32_bf16 v[86:89], v[46:49], v[114:117], v[86:89]
ds_read_b128 v[150:153], v8 offset:23552
v_cvt_pk_bf16_f32 v162, v154, v155
v_mfma_f32_16x16x32_bf16 v[86:89], v[54:57], v[118:121], v[86:89]
v_cvt_pk_bf16_f32 v163, v156, v157
v_and_b32 v215, 0xffff0000, v204
v_mfma_f32_16x16x32_bf16 v[86:89], v[62:65], v[122:125], v[86:89]
v_lshlrev_b32 v216, 16, v205
v_and_b32 v217, 0xffff0000, v205
v_mfma_f32_16x16x32_bf16 v[86:89], v[70:73], v[126:129], v[86:89]
s_waitcnt lgkmcnt(5)
global_load_dword v230, v253, s[38:39] sc1
v_add_f32_dpp v206, v210, v206 row_ror:8 row_mask:0xf bank_mask:0xf
v_mfma_f32_16x16x32_bf16 v[90:93], a[0:3], v[130:133], 0
s_waitcnt lgkmcnt(4)
v_add_f32_dpp v207, v211, v207 row_ror:8 row_mask:0xf bank_mask:0xf
v_add_f32_dpp v208, v212, v208 row_ror:8 row_mask:0xf bank_mask:0xf
v_mfma_f32_16x16x32_bf16 v[90:93], a[8:11], v[134:137], v[90:93]
s_waitcnt lgkmcnt(3)
v_add_f32_dpp v209, v213, v209 row_ror:8 row_mask:0xf bank_mask:0xf
v_fma_f32 v206, v206, v231, v214
v_mfma_f32_16x16x32_bf16 v[90:93], a[16:19], v[138:141], v[90:93]
s_waitcnt lgkmcnt(2)
v_fma_f32 v207, v207, v231, v215
v_fma_f32 v208, v208, v231, v216
v_mfma_f32_16x16x32_bf16 v[90:93], a[24:27], v[142:145], v[90:93]
s_waitcnt lgkmcnt(1)
v_mul_f32 v158, v78, v86
v_mul_f32 v159, v79, v87
v_mfma_f32_16x16x32_bf16 v[90:93], a[32:35], v[146:149], v[90:93]
s_waitcnt lgkmcnt(0)
v_mul_f32 v160, v80, v88
v_mul_f32 v161, v81, v89
v_mfma_f32_16x16x32_bf16 v[90:93], a[40:43], v[150:153], v[90:93]
ds_read_b128 v[106:109], v8 offset:24576
v_cvt_pk_bf16_f32 v164, v158, v159
v_mfma_f32_16x16x32_bf16 v[98:101], a[144:147], v[130:133], 0
ds_read_b128 v[110:113], v8 offset:25600
v_cvt_pk_bf16_f32 v165, v160, v161
v_mfma_f32_16x16x32_bf16 v[98:101], a[152:155], v[134:137], v[98:101]
v_mfma_f32_16x16x32_bf16 v[98:101], a[160:163], v[138:141], v[98:101]
s_waitcnt vmcnt(9)
s_barrier
v_mfma_f32_16x16x32_bf16 v[98:101], a[168:171], v[142:145], v[98:101]
s_waitcnt vmcnt(7)
s_mov_b64 exec, 1
global_store_dword v252, v228, s[38:39] sc1
s_mov_b64 exec, -1
v_mfma_f32_16x16x32_bf16 v[98:101], a[176:179], v[146:149], v[98:101]
v_fma_f32 v209, v209, v231, v217
v_cvt_pk_bf16_f32 v222, v206, v207
v_mfma_f32_16x16x32_bf16 v[98:101], a[184:187], v[150:153], v[98:101]
ds_read_b128 v[114:117], v8 offset:26624
v_cvt_pk_bf16_f32 v223, v208, v209
v_mfma_f32_16x16x32_bf16 v[94:97], a[4:7], v[130:133], 0
ds_write_b64 v14, v[222:223]
v_mfma_f32_16x16x32_bf16 v[94:97], a[12:15], v[134:137], v[94:97]
global_store_dwordx4 v3, v[162:165], s[38:39] sc1
v_mfma_f32_16x16x32_bf16 v[94:97], a[20:23], v[138:141], v[94:97]
ds_read_b128 v[118:121], v8 offset:27648
v_add_u32 v3, 147456, v3
v_mfma_f32_16x16x32_bf16 v[94:97], a[28:31], v[142:145], v[94:97]
ds_read_b64 v[224:225], v15
v_mfma_f32_16x16x32_bf16 v[94:97], a[36:39], v[146:149], v[94:97]
v_mfma_f32_16x16x32_bf16 v[94:97], a[44:47], v[150:153], v[94:97]
ds_read_b128 v[122:125], v8 offset:28672
v_mfma_f32_16x16x32_bf16 v[102:105], a[148:151], v[130:133], 0
v_mfma_f32_16x16x32_bf16 v[102:105], a[156:159], v[134:137], v[102:105]
v_mfma_f32_16x16x32_bf16 v[102:105], a[164:167], v[138:141], v[102:105]
ds_read_b128 v[126:129], v8 offset:29696
v_mfma_f32_16x16x32_bf16 v[102:105], a[172:175], v[142:145], v[102:105]
v_mfma_f32_16x16x32_bf16 v[102:105], a[180:183], v[146:149], v[102:105]
s_waitcnt lgkmcnt(2)
global_store_dwordx2 v13, v[224:225], s[60:61] sc1
v_mov_b32 v229, v227
v_mfma_f32_16x16x32_bf16 v[102:105], a[188:191], v[150:153], v[102:105]
v_mfma_f32_16x16x32_bf16 v[90:93], a[48:51], v[106:109], v[90:93]
v_mfma_f32_16x16x32_bf16 v[90:93], a[56:59], v[110:113], v[90:93]
v_mfma_f32_16x16x32_bf16 v[90:93], a[64:67], v[114:117], v[90:93]
v_mfma_f32_16x16x32_bf16 v[90:93], a[72:75], v[118:121], v[90:93]
s_waitcnt vmcnt(8)
v_add_f32 v182, v182, v184
s_waitcnt vmcnt(7)
v_lshlrev_b32 v206, 16, v170
v_add_f32 v183, v183, v185
s_waitcnt lgkmcnt(1)
v_mfma_f32_16x16x32_bf16 v[90:93], a[80:83], v[122:125], v[90:93]
s_waitcnt lgkmcnt(0)
v_and_b32 v207, 0xffff0000, v170
v_mul_f32 v182, 0x3ae38e39, v182
v_lshlrev_b32 v208, 16, v171
v_mfma_f32_16x16x32_bf16 v[90:93], a[88:91], v[126:129], v[90:93]
ds_read_b128 v[130:133], v8 offset:30720
v_mul_f32 v183, 0x3ae38e39, v183
v_and_b32 v209, 0xffff0000, v171
v_mfma_f32_16x16x32_bf16 v[98:101], a[192:195], v[106:109], v[98:101]
v_fma_f32 v232, -v182, v182, v183
v_lshlrev_b32 v210, 16, v172
v_max_f32 v232, 0, v232
v_mfma_f32_16x16x32_bf16 v[98:101], a[200:203], v[110:113], v[98:101]
v_and_b32 v211, 0xffff0000, v172
v_add_f32 v232, 0x3727c5ac, v232
v_mfma_f32_16x16x32_bf16 v[98:101], a[208:211], v[114:117], v[98:101]
ds_read_b128 v[134:137], v8 offset:31744
v_lshlrev_b32 v212, 16, v173
.Lgu_in24:
v_mfma_f32_16x16x32_bf16 v[98:101], a[216:219], v[118:121], v[98:101]
s_waitcnt vmcnt(3)
v_sub_u32 v230, v230, v17
v_cmp_lt_u32 vcc, 0x7f, v230
v_mfma_f32_16x16x32_bf16 v[98:101], a[224:227], v[122:125], v[98:101]
v_rcp_f32 v231, v232
v_mfma_f32_16x16x32_bf16 v[98:101], a[232:235], v[126:129], v[98:101]
ds_read_b128 v[138:141], v8 offset:32768
v_and_b32 v213, 0xffff0000, v173
v_mfma_f32_16x16x32_bf16 v[94:97], a[52:55], v[106:109], v[94:97]
s_and_b64 s[88:89], vcc, s[76:77]
s_cbranch_scc1 .Lgu_sp22
.Lgu_rt23:
v_mfma_f32_16x16x32_bf16 v[94:97], a[60:63], v[110:113], v[94:97]
s_cmp_eq_u64 vcc, 0
v_lshlrev_b32 v214, 16, v174
v_and_b32 v215, 0xffff0000, v174
v_mfma_f32_16x16x32_bf16 v[94:97], a[68:71], v[114:117], v[94:97]
s_cselect_b64 s[54:55], s[84:85], s[54:55]
ds_read_b128 v[142:145], v8 offset:33792
v_lshlrev_b32 v216, 16, v175
v_mfma_f32_16x16x32_bf16 v[94:97], a[76:79], v[118:121], v[94:97]
s_cselect_b64 s[52:53], s[86:87], s[52:53]
global_load_dwordx2 v[200:201], v12, s[54:55] offset:0 sc1
v_and_b32 v217, 0xffff0000, v175
v_mfma_f32_16x16x32_bf16 v[94:97], a[84:87], v[122:125], v[94:97]
s_cselect_b64 s[60:61], s[56:57], s[50:51]
global_load_dwordx2 v[202:203], v12, s[54:55] offset:128 sc1
v_lshlrev_b32 v218, 16, v176
v_mfma_f32_16x16x32_bf16 v[94:97], a[92:95], v[126:129], v[94:97]
s_cselect_b32 s82, 1, 0
ds_read_b128 v[146:149], v8 offset:34816
global_load_dwordx4 v[188:191], v10, s[52:53] offset:0 sc1
v_mfma_f32_16x16x32_bf16 v[102:105], a[196:199], v[106:109], v[102:105]
s_add_u32 s62, s62, s82
global_load_dwordx4 v[192:195], v10, s[52:53] offset:1024 sc1
v_and_b32 v219, 0xffff0000, v176
v_mfma_f32_16x16x32_bf16 v[102:105], a[204:207], v[110:113], v[102:105]
s_add_u32 s83, s62, s37
global_load_dwordx4 v[196:199], v10, s[52:53] offset:2048 sc1
v_add_u32 v17, s82, v17
v_add_u32 v227, -1, s83
v_mfma_f32_16x16x32_bf16 v[102:105], a[212:215], v[114:117], v[102:105]
s_cmp_lt_i32 s62, 32
ds_read_b128 v[150:153], v8 offset:35840
global_load_dwordx2 v[204:205], v11, s[54:55] sc1
v_add_lshl_u32 v253, v17, 0, 7
v_mfma_f32_16x16x32_bf16 v[102:105], a[220:223], v[118:121], v[102:105]
s_cselect_b64 s[56:57], s[46:47], s[48:49]
v_bfi_b32 v253, s69, v253, v16
v_lshlrev_b32 v220, 16, v177
v_mfma_f32_16x16x32_bf16 v[102:105], a[228:231], v[122:125], v[102:105]
s_mul_i32 s80, s62, 18432
v_and_b32 v221, 0xffff0000, v177
v_add_f32 v206, v206, v214
v_mfma_f32_16x16x32_bf16 v[102:105], a[236:239], v[126:129], v[102:105]
s_waitcnt lgkmcnt(5)
v_mfma_f32_16x16x32_bf16 v[90:93], a[96:99], v[130:133], v[90:93]
s_barrier
s_waitcnt lgkmcnt(4)
v_mfma_f32_16x16x32_bf16 v[90:93], a[104:107], v[134:137], v[90:93]
s_waitcnt lgkmcnt(3)
v_add_f32 v207, v207, v215
v_add_f32 v208, v208, v216
v_mfma_f32_16x16x32_bf16 v[90:93], a[112:115], v[138:141], v[90:93]
s_waitcnt lgkmcnt(2)
v_add_f32 v209, v209, v217
v_add_f32 v210, v210, v218
v_mfma_f32_16x16x32_bf16 v[90:93], a[120:123], v[142:145], v[90:93]
s_waitcnt lgkmcnt(1)
v_add_f32 v211, v211, v219
v_add_f32 v212, v212, v220
v_mfma_f32_16x16x32_bf16 v[90:93], a[128:131], v[146:149], v[90:93]
s_waitcnt lgkmcnt(0)
v_add_f32 v213, v213, v221
v_lshlrev_b32 v214, 16, v178
v_mfma_f32_16x16x32_bf16 v[90:93], a[136:139], v[150:153], v[90:93]
s_sub_u32 s56, s56, s80
v_and_b32 v215, 0xffff0000, v178
v_lshlrev_b32 v216, 16, v179
v_mfma_f32_16x16x32_bf16 v[98:101], v[26:29], v[130:133], v[98:101]
s_subb_u32 s57, s57, 0
v_and_b32 v217, 0xffff0000, v179
v_lshlrev_b32 v218, 16, v180
v_mfma_f32_16x16x32_bf16 v[98:101], v[34:37], v[134:137], v[98:101]
s_add_u32 s84, s54, 18688
v_and_b32 v219, 0xffff0000, v180
v_lshlrev_b32 v220, 16, v181
v_mfma_f32_16x16x32_bf16 v[98:101], v[42:45], v[138:141], v[98:101]
s_addc_u32 s85, s55, 0
v_and_b32 v221, 0xffff0000, v181
v_add_f32 v206, v206, v214
v_mfma_f32_16x16x32_bf16 v[98:101], v[50:53], v[142:145], v[98:101]
s_add_u32 s86, s52, 110592
v_xor_b32 v8, 0x10000, v8
v_add_f32 v207, v207, v215
v_mfma_f32_16x16x32_bf16 v[98:101], v[58:61], v[146:149], v[98:101]
s_addc_u32 s87, s53, 0
v_add_f32 v208, v208, v216
v_add_f32 v209, v209, v217
v_mfma_f32_16x16x32_bf16 v[98:101], v[66:69], v[150:153], v[98:101]
v_mfma_f32_16x16x32_bf16 v[94:97], a[100:103], v[130:133], v[94:97]
s_barrier
v_add_lshl_u32 v252, v229, 0, 7
v_mfma_f32_16x16x32_bf16 v[94:97], a[108:111], v[134:137], v[94:97]
s_add_u32 s72, s72, 1
v_add_f32 v210, v210, v218
v_add_f32 v211, v211, v219
v_bfi_b32 v252, s69, v252, v18
v_mfma_f32_16x16x32_bf16 v[94:97], a[116:119], v[138:141], v[94:97]
s_cmp_le_i32 s62, s72
v_add_f32 v212, v212, v220
v_add_f32 v213, v213, v221
v_mfma_f32_16x16x32_bf16 v[94:97], a[124:127], v[142:145], v[94:97]
s_cselect_b64 s[76:77], -1, 0
ds_read_b128 v[106:109], v8 offset:0
v_lshlrev_b32 v214, 16, v186
v_mfma_f32_16x16x32_bf16 v[94:97], a[132:135], v[146:149], v[94:97]
ds_read_b128 v[110:113], v8 offset:1024
v_mul_f32 v154, v90, v98
v_mfma_f32_16x16x32_bf16 v[94:97], a[140:143], v[150:153], v[94:97]
ds_read_b128 v[114:117], v8 offset:2048
v_mul_f32 v155, v91, v99
v_mfma_f32_16x16x32_bf16 v[102:105], v[30:33], v[130:133], v[102:105]
ds_read_b128 v[118:121], v8 offset:3072
v_mul_f32 v156, v92, v100
v_mfma_f32_16x16x32_bf16 v[102:105], v[38:41], v[134:137], v[102:105]
ds_read_b128 v[122:125], v8 offset:4096
v_mul_f32 v157, v93, v101
v_mfma_f32_16x16x32_bf16 v[102:105], v[46:49], v[138:141], v[102:105]
ds_read_b128 v[126:129], v8 offset:5120
v_cvt_pk_bf16_f32 v162, v154, v155
v_mfma_f32_16x16x32_bf16 v[102:105], v[54:57], v[142:145], v[102:105]
v_cvt_pk_bf16_f32 v163, v156, v157
v_and_b32 v215, 0xffff0000, v186
v_mfma_f32_16x16x32_bf16 v[102:105], v[62:65], v[146:149], v[102:105]
v_mfma_f32_16x16x32_bf16 v[102:105], v[70:73], v[150:153], v[102:105]
s_sub_u32 s70, s70, 1
s_cbranch_scc0 .Lgu_top25
v_mov_b32 v232, s62
ds_write_b32 v1, v232 offset:36864
v_subrev_u32 v17, s62, v17
s_waitcnt vmcnt(7) lgkmcnt(0)
s_barrier
v_mul_f32 v154, v90, v98
v_mul_f32 v155, v91, v99
v_mul_f32 v156, v92, v100
v_mul_f32 v157, v93, v101
v_cvt_pk_bf16_f32 v162, v154, v155
v_cvt_pk_bf16_f32 v163, v156, v157
v_mul_f32 v158, v94, v102
v_mul_f32 v159, v95, v103
v_mul_f32 v160, v96, v104
v_mul_f32 v161, v97, v105
v_cvt_pk_bf16_f32 v164, v158, v159
v_cvt_pk_bf16_f32 v165, v160, v161
global_store_dwordx4 v3, v[162:165], s[38:39] sc1
v_mov_b32 v231, 192
ds_read_b32 v231, v231 offset:36864
s_waitcnt vmcnt(0)
s_barrier
s_waitcnt lgkmcnt(0)
v_readfirstlane_b32 s62, v231
s_nop 3
s_max_i32 s62, s62, 2
s_min_i32 s62, s62, 66
s_add_u32 s62, s62, 1
s_branch .Lgu_fin
.Lgu_fin:
s_cmp_gt_i32 s62, 63
s_cbranch_scc1 .Lgu_end
s_cmp_gt_i32 s62, 61
s_cselect_b32 s82, -2, 0
s_add_u32 s82, s82, s62
v_add_u32 v233, s82, v17
v_add_lshl_u32 v253, v233, 0, 7
v_bfi_b32 v253, s69, v253, v16
global_load_dword v230, v253, s[38:39] sc1
s_cmp_lt_i32 s62, 32
s_cselect_b64 s[56:57], s[46:47], s[48:49]
s_mul_i32 s80, s62, 18432
s_sub_u32 s56, s56, s80
s_subb_u32 s57, s57, 0
s_mov_b64 s[58:59], s[56:57]
s_mul_i32 s80, s62, 110592
s_mul_i32 s81, s62, 18688
s_mov_b64 s[52:53], s[38:39]
s_mov_b64 s[54:55], s[38:39]
s_add_u32 s52, s52, s80
s_addc_u32 s53, s53, 0
s_add_u32 s54, s54, s81
s_addc_u32 s55, s55, 0
s_waitcnt vmcnt(0)
v_sub_u32 v230, v230, v233
v_cmp_lt_u32 vcc, 0x7f, v230
s_cbranch_vccnz .Lgu_sp27
.Lgu_rt28:
.Lgu_re:
global_load_dwordx2 v[182:183], v12, s[54:55] offset:0 sc1
global_load_dwordx2 v[184:185], v12, s[54:55] offset:128 sc1
global_load_dwordx4 v[170:173], v10, s[52:53] offset:0 sc1
global_load_dwordx4 v[174:177], v10, s[52:53] offset:1024 sc1
global_load_dwordx4 v[178:181], v10, s[52:53] offset:2048 sc1
global_load_dwordx2 v[186:187], v11, s[54:55] sc1
s_waitcnt vmcnt(3)
v_max3_u32 v232, v170, v171, v172
s_waitcnt vmcnt(2)
v_max3_u32 v232, v232, v173, v174
v_max3_u32 v232, v232, v175, v176
s_waitcnt vmcnt(1)
v_max3_u32 v232, v232, v177, v178
v_max3_u32 v232, v232, v179, v180
v_max3_u32 v232, v232, v181, v181
v_cmp_eq_u32 vcc, -1, v232
s_waitcnt vmcnt(0)
s_cbranch_vccz .Lgu_ok
s_cmp_lt_i32 s62, 62
s_cbranch_scc1 .Lgu_ok
s_add_u32 s71, s71, 1
s_cmp_lt_u32 s71, 3000000
s_cbranch_scc1 .Lgu_re
.Lgu_ok:
v_add_f32 v182, v182, v184
v_lshlrev_b32 v206, 16, v170
v_add_f32 v183, v183, v185
v_and_b32 v207, 0xffff0000, v170
v_mul_f32 v182, 0x3ae38e39, v182
v_lshlrev_b32 v208, 16, v171
v_mul_f32 v183, 0x3ae38e39, v183
v_and_b32 v209, 0xffff0000, v171
v_fma_f32 v232, -v182, v182, v183
v_lshlrev_b32 v210, 16, v172
v_max_f32 v232, 0, v232
v_and_b32 v211, 0xffff0000, v172
v_add_f32 v232, 0x3727c5ac, v232
v_lshlrev_b32 v212, 16, v173
v_rcp_f32 v231, v232
v_and_b32 v213, 0xffff0000, v173
v_lshlrev_b32 v214, 16, v174
v_and_b32 v215, 0xffff0000, v174
v_lshlrev_b32 v216, 16, v175
v_and_b32 v217, 0xffff0000, v175
v_lshlrev_b32 v218, 16, v176
v_and_b32 v219, 0xffff0000, v176
v_lshlrev_b32 v220, 16, v177
v_and_b32 v221, 0xffff0000, v177
v_add_f32 v206, v206, v214
v_add_f32 v207, v207, v215
v_add_f32 v208, v208, v216
v_add_f32 v209, v209, v217
v_add_f32 v210, v210, v218
v_add_f32 v211, v211, v219
v_add_f32 v212, v212, v220
v_add_f32 v213, v213, v221
v_lshlrev_b32 v214, 16, v178
v_and_b32 v215, 0xffff0000, v178
v_lshlrev_b32 v216, 16, v179
v_and_b32 v217, 0xffff0000, v179
v_lshlrev_b32 v218, 16, v180
v_and_b32 v219, 0xffff0000, v180
v_lshlrev_b32 v220, 16, v181
v_and_b32 v221, 0xffff0000, v181
v_add_f32 v206, v206, v214
v_add_f32 v207, v207, v215
v_add_f32 v208, v208, v216
v_add_f32 v209, v209, v217
v_add_f32 v210, v210, v218
v_add_f32 v211, v211, v219
v_add_f32 v212, v212, v220
v_add_f32 v213, v213, v221
v_lshlrev_b32 v214, 16, v186
v_and_b32 v215, 0xffff0000, v186
v_lshlrev_b32 v216, 16, v187
v_and_b32 v217, 0xffff0000, v187
v_add_f32_dpp v206, v210, v206 row_ror:8 row_mask:0xf bank_mask:0xf
v_add_f32_dpp v207, v211, v207 row_ror:8 row_mask:0xf bank_mask:0xf
v_add_f32_dpp v208, v212, v208 row_ror:8 row_mask:0xf bank_mask:0xf
v_add_f32_dpp v209, v213, v209 row_ror:8 row_mask:0xf bank_mask:0xf
v_fma_f32 v206, v206, v231, v214
v_fma_f32 v207, v207, v231, v215
v_fma_f32 v208, v208, v231, v216
v_fma_f32 v209, v209, v231, v217
v_cvt_pk_bf16_f32 v222, v206, v207
v_cvt_pk_bf16_f32 v223, v208, v209
ds_write_b64 v14, v[222:223]
ds_read_b64 v[224:225], v15
s_waitcnt lgkmcnt(0)
global_store_dwordx2 v13, v[224:225], s[58:59] sc1
s_add_u32 s62, s62, 4
s_branch .Lgu_fin
.Lgu_end:
s_endpgm
)"
R"(.Lgu_sp11:
s_sleep 1
v_add_lshl_u32 v253, v5, 0, 7
v_bfi_b32 v253, s69, v253, v4
global_load_dword v254, v253, s[38:39] sc1
s_add_u32 s71, s71, 1
s_waitcnt vmcnt(0)
v_sub_u32 v254, v254, v5
v_cmp_lt_u32 vcc, 0x7f, v254
s_cbranch_vccz .Lgu_rt12
s_cmp_lt_u32 s71, 3000000
s_cbranch_scc1 .Lgu_sp11
s_branch .Lgu_rt12
.Lgu_sp13:
s_sleep 1
v_add_lshl_u32 v253, v5, 0, 7
v_bfi_b32 v253, s69, v253, v4
global_load_dword v255, v253, s[38:39] sc1
s_add_u32 s71, s71, 1
s_waitcnt vmcnt(0)
v_sub_u32 v255, v255, v5
v_cmp_lt_u32 vcc, 0x7f, v255
s_cbranch_vccz .Lgu_rt14
s_cmp_lt_u32 s71, 3000000
s_cbranch_scc1 .Lgu_sp13
s_branch .Lgu_rt14
.Lgu_sp16:
s_sleep 1
v_add_lshl_u32 v253, v5, 0, 7
v_bfi_b32 v253, s69, v253, v4
global_load_dword v254, v253, s[38:39] sc1
s_add_u32 s71, s71, 1
s_waitcnt vmcnt(0)
v_sub_u32 v254, v254, v5
v_cmp_lt_u32 vcc, 0x7f, v254
s_cbranch_vccz .Lgu_rt17
s_cmp_lt_u32 s71, 3000000
s_cbranch_scc1 .Lgu_sp16
s_branch .Lgu_rt17
.Lgu_sp20:
s_sleep 1
v_add_lshl_u32 v253, v17, 0, 7
v_bfi_b32 v253, s69, v253, v16
global_load_dword v230, v253, s[38:39] sc1
s_add_u32 s71, s71, 1
s_waitcnt vmcnt(0)
v_sub_u32 v230, v230, v17
v_cmp_lt_u32 vcc, 0x7f, v230
s_and_b64 vcc, vcc, s[76:77]
s_cbranch_vccz .Lgu_rt21
s_cmp_lt_u32 s71, 3000000
s_cbranch_scc1 .Lgu_sp20
s_branch .Lgu_rt21
.Lgu_sp22:
s_sleep 1
v_add_lshl_u32 v253, v17, 0, 7
v_bfi_b32 v253, s69, v253, v16
global_load_dword v230, v253, s[38:39] sc1
s_add_u32 s71, s71, 1
s_waitcnt vmcnt(0)
v_sub_u32 v230, v230, v17
v_cmp_lt_u32 vcc, 0x7f, v230
s_and_b64 vcc, vcc, s[76:77]
s_cbranch_vccz .Lgu_rt23
s_cmp_lt_u32 s71, 3000000
s_cbranch_scc1 .Lgu_sp22
s_branch .Lgu_rt23
.Lgu_sp27:
s_sleep 1
v_add_lshl_u32 v253, v233, 0, 7
v_bfi_b32 v253, s69, v253, v16
global_load_dword v230, v253, s[38:39] sc1
s_add_u32 s71, s71, 1
s_waitcnt vmcnt(0)
v_sub_u32 v230, v230, v233
v_cmp_lt_u32 vcc, 0x7f, v230
s_cbranch_vccz .Lgu_rt28
s_cmp_lt_u32 s71, 3000000
s_cbranch_scc1 .Lgu_sp27
s_branch .Lgu_rt28
.Le3:
)"
    :: "{v[0:31]}"(va), "{s[36:51]}"(sa), "{s[52:67]}"(sb) : "v255", "a255", "s99", "vcc", "memory");
  }
}

extern "C" void kernel_launch(void* const* d_in, const int* in_sizes, int n_in,
                              void* d_out, int out_size, void* d_ws, size_t ws_size,
                              hipStream_t stream) {
  k_main<<<256, 256, 0, stream>>>((const char*)d_in[0], (const char*)d_in[1], (const char*)d_in[2], (const char*)d_in[4],
                                  (const char*)d_in[5], (const char*)d_in[6], (char*)d_ws, (char*)d_out);
}
```
